```python
import torch

def _softmax(x, axis=-1):
    return torch.softmax(x, dim=axis)
def _gelu(x):
    return torch.nn.functional.gelu(x, approximate="tanh")
def _silu(x):
    return torch.nn.functional.silu(x)
def _sigmoid(x):
    return torch.sigmoid(x)
def _layernorm(x, axis=-1, eps=1e-5):
    m = x.mean(dim=axis, keepdim=True)
    v = x.var(dim=axis, keepdim=True, unbiased=False)
    return (x - m) / torch.sqrt(v + eps)
def _scatter_add(tgt, idx, src, axis=0):
    return tgt.clone().index_add_(axis, idx, src)
def _segment_sum(src, seg, n):
    out = torch.zeros((n,) + src.shape[1:], dtype=src.dtype)
    return out.index_add_(0, seg, src)
def _pad(x, pw):
    flat = tuple(v for pair in reversed(pw) for v in pair)
    return torch.nn.functional.pad(x, flat)

kernel_name = 'standalone__window_attn_swin_v2__vision'

def setup_inputs():

    _w = (lambda a: (lambda t: t.to(torch.bfloat16) if t.is_floating_point() else t)(torch.as_tensor(a)))
    win = 7
    H = W = (512 // win) * win
    C = 192
    nh = 4
    shift = win // 2
    coords = torch.stack(torch.meshgrid(torch.arange(win), torch.arange(win), indexing="ij"), -1).reshape(-1, 2)
    rel = coords[:, None, :] - coords[None, :, :]
    rel_idx = ((rel[..., 0] + win - 1) * (2 * win - 1) + (rel[..., 1] + win - 1)).to(torch.int64)
    tab = torch.stack(torch.meshgrid(torch.arange(1 - win, win), torch.arange(1 - win, win), indexing="ij"), -1).reshape(-1, 2).to(float)
    tab = (lambda _x: torch.sign(torch.as_tensor(_x)))(tab) * (lambda _x: torch.log1p(torch.as_tensor(_x)))(torch.abs(tab))
    img = torch.zeros((H, W), dtype=torch.int64)
    cnt = 0
    for hs in (slice(0, H - win), slice(H - win, H - shift), slice(H - shift, H)):
        for ws in (slice(0, W - win), slice(W - win, W - shift), slice(W - shift, W)):
            img[hs, ws] = cnt; cnt += 1
    mw = img.reshape(H // win, win, W // win, win).permute(0, 2, 1, 3).reshape(-1, win * win)
    mask = (mw[:, :, None] != mw[:, None, :]).to(float) * (-100.0)
    return {
        "x": _w((lambda *s: torch.randn(*s, dtype=torch.bfloat16) * 0.05)(1, H, W, C)),
        "wqkv": _w((lambda *s: torch.randn(*s, dtype=torch.bfloat16) * 1.6)(C, 3 * C)),
        "wo": _w((lambda *s: torch.randn(*s, dtype=torch.bfloat16) * 0.05)(C, C)),
        "logit_scale": _w((lambda *s: torch.randn(*s, dtype=torch.bfloat16) * 1.6)(nh)),
        "cpb_w1": _w((lambda *s: torch.randn(*s, dtype=torch.bfloat16) * 1.6)(2, 4 * nh)),
        "cpb_w2": _w((lambda *s: torch.randn(*s, dtype=torch.bfloat16) * 1.6)(4 * nh, nh)),
        "rel_coords": _w(tab),
        "rel_idx": _w(rel_idx),
        "attn_mask": _w(mask),
    }

def reference(x, wqkv, wo, logit_scale, cpb_w1, cpb_w2, rel_coords, rel_idx, attn_mask):
    B, H, W, C = x.shape
    win = 7
    nh = 4
    hd = C // nh
    shift = win // 2
    ww = win * win
    nH, nW = H // win, W // win
    xs = torch.roll(x, (-shift, -shift), (1, 2))
    xw = (lambda x, *d: x.permute(*d))(xs.reshape(B, nH, win, nW, win, C), 0, 1, 3, 2, 4, 5).reshape(B * nH * nW, ww, C)
    qkv = (lambda x, *d: x.permute(*d))((xw @ wqkv).reshape(B * nH * nW, ww, 3, nh, hd), 2, 0, 3, 1, 4)
    q, k, v = qkv[0], qkv[1], qkv[2]
    qn = q * (torch.einsum("bhqd,bhqd->bhq", q, q)[..., None] + 1e-12) ** -0.5
    kn = k * (torch.einsum("bhkd,bhkd->bhk", k, k)[..., None] + 1e-12) ** -0.5
    logits = torch.einsum("bhqd,bhkd->bhqk", qn, kn)
    tau = torch.exp(torch.clip(logit_scale, -100.0, 4.6052))
    logits = logits * tau[None, :, None, None]
    hcpb = rel_coords @ cpb_w1
    hcpb = torch.where(hcpb > 0, hcpb, hcpb * 0)
    bias_tab = 16.0 * _sigmoid(hcpb @ cpb_w2)
    bias = (lambda x, *d: x.permute(*d))(bias_tab[rel_idx], 2, 0, 1)
    logits = logits.reshape(B, nH * nW, nh, ww, ww) + bias[None, None, :, :, :] + attn_mask[None, :, None, :, :]
    p = _softmax(logits.reshape(B * nH * nW, nh, ww, ww), axis=-1)
    o = (lambda x, *d: x.permute(*d))(torch.einsum("bhqk,bhkd->bhqd", p, v), 0, 2, 1, 3).reshape(B * nH * nW, ww, C) @ wo
    o = (lambda x, *d: x.permute(*d))(o.reshape(B, nH, nW, win, win, C), 0, 1, 3, 2, 4, 5).reshape(B, H, W, C)
    o = torch.roll(o, (shift, shift), (1, 2))
    return _layernorm(o, axis=-1)
```

```cpp
#include <hip/hip_runtime.h>
#include <hip/hip_bf16.h>
#include <stdint.h>

namespace swin {

constexpr unsigned CT_BYTES = 15360u;
constexpr unsigned LDS_BYTES = 163840u;

__device__ __attribute__((aligned(256))) const unsigned CT_TAB[CT_BYTES / 4] = {0x13800u,0x100000u,0x100000u,0x100000u,0x13800u,0x100000u,0x100000u,0x100000u,0x13800u,0x100000u,0x100000u,0x100000u,0x13800u,0x100000u,0x100000u,0x100000u,0x13810u,0x100000u,0x100000u,0x100000u,0x13810u,0x100000u,0x100000u,0x100000u,0x13810u,0x100000u,0x100000u,0x100000u,0x13810u,0x100000u,0x100000u,0x100000u,0x13820u,0x100000u,0x100000u,0x100000u,0x13820u,0x100000u,0x100000u,0x100000u,0x13820u,0x100000u,0x100000u,0x100000u,0x13820u,0x100000u,0x100000u,0x100000u,0x13830u,0x100000u,0x100000u,0x100000u,0x13830u,0x100000u,0x100000u,0x100000u,0x13830u,0x100000u,0x100000u,0x100000u,0x13830u,0x100000u,0x100000u,0x100000u,0x23000u,0x100000u,0x100000u,0x100000u,0x23000u,0x100000u,0x100000u,0x100000u,0x23000u,0x100000u,0x100000u,0x100000u,0x23000u,0x100000u,0x100000u,0x100000u,0x23030u,0x100000u,0x100000u,0x100000u,0x23030u,0x100000u,0x100000u,0x100000u,0x23030u,0x100000u,0x100000u,0x100000u,0x23030u,0x100000u,0x100000u,0x100000u,0x23060u,0x100000u,0x100000u,0x100000u,0x23060u,0x100000u,0x100000u,0x100000u,0x23060u,0x100000u,0x100000u,0x100000u,0x23060u,0x100000u,0x100000u,0x100000u,0x23090u,0x100000u,0x100000u,0x100000u,0x23090u,0x100000u,0x100000u,0x100000u,0x23090u,0x100000u,0x100000u,0x100000u,0x23090u,0x100000u,0x100000u,0x100000u,0x13800u,0x13802u,0x13804u,0x13806u,0x13810u,0x13812u,0x13814u,0x13816u,0x13820u,0x13822u,0x13824u,0x13826u,0x13830u,0x13832u,0x13834u,0x13836u,0x13808u,0x1380au,0x1380cu,0x1380eu,0x13818u,0x1381au,0x1381cu,0x1381eu,0x13828u,0x1382au,0x1382cu,0x1382eu,0x13838u,0x1383au,0x1383cu,0x1383eu,0x13840u,0x13842u,0x13844u,0x13846u,0x13850u,0x13852u,0x13854u,0x13856u,0x13860u,0x13862u,0x13864u,0x13866u,0x13870u,0x13872u,0x13874u,0x13876u,0x13848u,0x1384au,0x1384cu,0x1384eu,0x13858u,0x1385au,0x1385cu,0x1385eu,0x13868u,0x1386au,0x1386cu,0x1386eu,0x13878u,0x1387au,0x1387cu,0x1387eu,0x0u,0x0u,0x0u,0x0u,0x0u,0x0u,0x0u,0x0u,0x0u,0x0u,0x0u,0x0u,0x0u,0x0u,0x0u,0x0u,0x0u,0x0u,0x0u,0x0u,0x0u,0x0u,0x0u,0x0u,0x0u,0x0u,0x0u,0x0u,0x0u,0x0u,0x0u,0x0u,0x0u,0x0u,0x0u,0x0u,0x0u,0x0u,0x0u,0x0u,0x0u,0x0u,0x0u,0x0u,0x0u,0x0u,0x0u,0x0u,0x0u,0x0u,0x0u,0x0u,0x0u,0x0u,0x0u,0x0u,0x0u,0x0u,0x0u,0x0u,0x0u,0x0u,0x0u,0x0u,0x18180u,0x100000u,0x100000u,0x100000u,0x18180u,0x100000u,0x100000u,0x100000u,0x18180u,0x100000u,0x100000u,0x100000u,0x18180u,0x100000u,0x100000u,0x100000u,0x18190u,0x100000u,0x100000u,0x100000u,0x18190u,0x100000u,0x100000u,0x100000u,0x18190u,0x100000u,0x100000u,0x100000u,0x18190u,0x100000u,0x100000u,0x100000u,0x181a0u,0x100000u,0x100000u,0x100000u,0x181a0u,0x100000u,0x100000u,0x100000u,0x181a0u,0x100000u,0x100000u,0x100000u,0x181a0u,0x100000u,0x100000u,0x100000u,0x181b0u,0x100000u,0x100000u,0x100000u,0x181b0u,0x100000u,0x100000u,0x100000u,0x181b0u,0x100000u,0x100000u,0x100000u,0x181b0u,0x100000u,0x100000u,0x100000u,0x230c0u,0x100000u,0x100000u,0x100000u,0x230c0u,0x100000u,0x100000u,0x100000u,0x230c0u,0x100000u,0x100000u,0x100000u,0x230c0u,0x100000u,0x100000u,0x100000u,0x230f0u,0x100000u,0x100000u,0x100000u,0x230f0u,0x100000u,0x100000u,0x100000u,0x230f0u,0x100000u,0x100000u,0x100000u,0x230f0u,0x100000u,0x100000u,0x100000u,0x23120u,0x100000u,0x100000u,0x100000u,0x23120u,0x100000u,0x100000u,0x100000u,0x23120u,0x100000u,0x100000u,0x100000u,0x23120u,0x100000u,0x100000u,0x100000u,0x23150u,0x100000u,0x100000u,0x100000u,0x23150u,0x100000u,0x100000u,0x100000u,0x23150u,0x100000u,0x100000u,0x100000u,0x23150u,0x100000u,0x100000u,0x100000u,0x15080u,0x15082u,0x15084u,0x15086u,0x15090u,0x15092u,0x15094u,0x15096u,0x150a0u,0x150a2u,0x150a4u,0x150a6u,0x150b0u,0x150b2u,0x150b4u,0x150b6u,0x15088u,0x1508au,0x1508cu,0x1508eu,0x15098u,0x1509au,0x1509cu,0x1509eu,0x150a8u,0x150aau,0x150acu,0x150aeu,0x150b8u,0x150bau,0x150bcu,0x150beu,0x150c0u,0x150c2u,0x150c4u,0x150c6u,0x150d0u,0x150d2u,0x150d4u,0x150d6u,0x150e0u,0x150e2u,0x150e4u,0x150e6u,0x150f0u,0x150f2u,0x150f4u,0x150f6u,0x150c8u,0x150cau,0x150ccu,0x150ceu,0x150d8u,0x150dau,0x150dcu,0x150deu,0x150e8u,0x150eau,0x150ecu,0x150eeu,0x150f8u,0x150fau,0x150fcu,0x150feu,0x0u,0x0u,0x0u,0x0u,0x0u,0x0u,0x0u,0x0u,0x0u,0x0u,0x0u,0x0u,0x0u,0x0u,0x0u,0x0u,0x0u,0x0u,0x0u,0x0u,0x0u,0x0u,0x0u,0x0u,0x0u,0x0u,0x0u,0x0u,0x0u,0x0u,0x0u,0x0u,0x0u,0x0u,0x0u,0x0u,0x0u,0x0u,0x0u,0x0u,0x0u,0x0u,0x0u,0x0u,0x0u,0x0u,0x0u,0x0u,0x0u,0x0u,0x0u,0x0u,0x0u,0x0u,0x0u,0x0u,0x0u,0x0u,0x0u,0x0u,0x0u,0x0u,0x0u,0x0u,0x15080u,0x100000u,0x100000u,0x100000u,0x15080u,0x100000u,0x100000u,0x100000u,0x15080u,0x100000u,0x100000u,0x100000u,0x15080u,0x100000u,0x100000u,0x100000u,0x15090u,0x100000u,0x100000u,0x100000u,0x15090u,0x100000u,0x100000u,0x100000u,0x15090u,0x100000u,0x100000u,0x100000u,0x15090u,0x100000u,0x100000u,0x100000u,0x150a0u,0x100000u,0x100000u,0x100000u,0x150a0u,0x100000u,0x100000u,0x100000u,0x150a0u,0x100000u,0x100000u,0x100000u,0x150a0u,0x100000u,0x100000u,0x100000u,0x150b0u,0x100000u,0x100000u,0x100000u,0x150b0u,0x100000u,0x100000u,0x100000u,0x150b0u,0x100000u,0x100000u,0x100000u,0x150b0u,0x100000u,0x100000u,0x100000u,0x23180u,0x100000u,0x100000u,0x100000u,0x23180u,0x100000u,0x100000u,0x100000u,0x23180u,0x100000u,0x100000u,0x100000u,0x23180u,0x100000u,0x100000u,0x100000u,0x231b0u,0x100000u,0x100000u,0x100000u,0x231b0u,0x100000u,0x100000u,0x100000u,0x231b0u,0x100000u,0x100000u,0x100000u,0x231b0u,0x100000u,0x100000u,0x100000u,0x231e0u,0x100000u,0x100000u,0x100000u,0x231e0u,0x100000u,0x100000u,0x100000u,0x231e0u,0x100000u,0x100000u,0x100000u,0x231e0u,0x100000u,0x100000u,0x100000u,0x23210u,0x100000u,0x100000u,0x100000u,0x23210u,0x100000u,0x100000u,0x100000u,0x23210u,0x100000u,0x100000u,0x100000u,0x23210u,0x100000u,0x100000u,0x100000u,0x16900u,0x16902u,0x16904u,0x16906u,0x16910u,0x16912u,0x16914u,0x16916u,0x16920u,0x16922u,0x16924u,0x16926u,0x16930u,0x16932u,0x16934u,0x16936u,0x16908u,0x1690au,0x1690cu,0x1690eu,0x16918u,0x1691au,0x1691cu,0x1691eu,0x16928u,0x1692au,0x1692cu,0x1692eu,0x16938u,0x1693au,0x1693cu,0x1693eu,0x16940u,0x16942u,0x16944u,0x16946u,0x16950u,0x16952u,0x16954u,0x16956u,0x16960u,0x16962u,0x16964u,0x16966u,0x16970u,0x16972u,0x16974u,0x16976u,0x16948u,0x1694au,0x1694cu,0x1694eu,0x16958u,0x1695au,0x1695cu,0x1695eu,0x16968u,0x1696au,0x1696cu,0x1696eu,0x16978u,0x1697au,0x1697cu,0x1697eu,0x0u,0x0u,0x0u,0x0u,0x0u,0x0u,0x0u,0x0u,0x0u,0x0u,0x0u,0x0u,0x0u,0x0u,0x0u,0x0u,0x0u,0x0u,0x0u,0x0u,0x0u,0x0u,0x0u,0x0u,0x0u,0x0u,0x0u,0x0u,0x0u,0x0u,0x0u,0x0u,0x0u,0x0u,0x0u,0x0u,0x0u,0x0u,0x0u,0x0u,0x0u,0x0u,0x0u,0x0u,0x0u,0x0u,0x0u,0x0u,0x0u,0x0u,0x0u,0x0u,0x0u,0x0u,0x0u,0x0u,0x0u,0x0u,0x0u,0x0u,0x0u,0x0u,0x0u,0x0u,0x16900u,0x100000u,0x100000u,0x100000u,0x16900u,0x100000u,0x100000u,0x100000u,0x16900u,0x100000u,0x100000u,0x100000u,0x16900u,0x100000u,0x100000u,0x100000u,0x16910u,0x100000u,0x100000u,0x100000u,0x16910u,0x100000u,0x100000u,0x100000u,0x16910u,0x100000u,0x100000u,0x100000u,0x16910u,0x100000u,0x100000u,0x100000u,0x16920u,0x100000u,0x100000u,0x100000u,0x16920u,0x100000u,0x100000u,0x100000u,0x16920u,0x100000u,0x100000u,0x100000u,0x16920u,0x100000u,0x100000u,0x100000u,0x16930u,0x100000u,0x100000u,0x100000u,0x16930u,0x100000u,0x100000u,0x100000u,0x16930u,0x100000u,0x100000u,0x100000u,0x16930u,0x100000u,0x100000u,0x100000u,0x23240u,0x100000u,0x100000u,0x100000u,0x23240u,0x100000u,0x100000u,0x100000u,0x23240u,0x100000u,0x100000u,0x100000u,0x23240u,0x100000u,0x100000u,0x100000u,0x23270u,0x100000u,0x100000u,0x100000u,0x23270u,0x100000u,0x100000u,0x100000u,0x23270u,0x100000u,0x100000u,0x100000u,0x23270u,0x100000u,0x100000u,0x100000u,0x232a0u,0x100000u,0x100000u,0x100000u,0x232a0u,0x100000u,0x100000u,0x100000u,0x232a0u,0x100000u,0x100000u,0x100000u,0x232a0u,0x100000u,0x100000u,0x100000u,0x232d0u,0x100000u,0x100000u,0x100000u,0x232d0u,0x100000u,0x100000u,0x100000u,0x232d0u,0x100000u,0x100000u,0x100000u,0x232d0u,0x100000u,0x100000u,0x100000u,0x18180u,0x18182u,0x18184u,0x18186u,0x18190u,0x18192u,0x18194u,0x18196u,0x181a0u,0x181a2u,0x181a4u,0x181a6u,0x181b0u,0x181b2u,0x181b4u,0x181b6u,0x18188u,0x1818au,0x1818cu,0x1818eu,0x18198u,0x1819au,0x1819cu,0x1819eu,0x181a8u,0x181aau,0x181acu,0x181aeu,0x181b8u,0x181bau,0x181bcu,0x181beu,0x181c0u,0x181c2u,0x181c4u,0x181c6u,0x181d0u,0x181d2u,0x181d4u,0x181d6u,0x181e0u,0x181e2u,0x181e4u,0x181e6u,0x181f0u,0x181f2u,0x181f4u,0x181f6u,0x181c8u,0x181cau,0x181ccu,0x181ceu,0x181d8u,0x181dau,0x181dcu,0x181deu,0x181e8u,0x181eau,0x181ecu,0x181eeu,0x181f8u,0x181fau,0x181fcu,0x181feu,0x0u,0x0u,0x0u,0x0u,0x0u,0x0u,0x0u,0x0u,0x0u,0x0u,0x0u,0x0u,0x0u,0x0u,0x0u,0x0u,0x0u,0x0u,0x0u,0x0u,0x0u,0x0u,0x0u,0x0u,0x0u,0x0u,0x0u,0x0u,0x0u,0x0u,0x0u,0x0u,0x0u,0x0u,0x0u,0x0u,0x0u,0x0u,0x0u,0x0u,0x0u,0x0u,0x0u,0x0u,0x0u,0x0u,0x0u,0x0u,0x0u,0x0u,0x0u,0x0u,0x0u,0x0u,0x0u,0x0u,0x0u,0x0u,0x0u,0x0u,0x0u,0x0u,0x0u,0x0u,0x9000u,0x9040u,0x9090u,0x90d0u,0x9120u,0x9160u,0x91b0u,0x91f0u,0x9200u,0x9240u,0x9290u,0x92d0u,0x9320u,0x9360u,0x93b0u,0x93f0u,0x9010u,0x9050u,0x9080u,0x90c0u,0x9130u,0x9170u,0x91a0u,0x91e0u,0x9210u,0x9250u,0x9280u,0x92c0u,0x9330u,0x9370u,0x93a0u,0x93e0u,0x9020u,0x9060u,0x90b0u,0x90f0u,0x9100u,0x9140u,0x9190u,0x91d0u,0x9220u,0x9260u,0x92b0u,0x92f0u,0x9300u,0x9340u,0x9390u,0x93d0u,0x9030u,0x9070u,0x90a0u,0x90e0u,0x9110u,0x9150u,0x9180u,0x91c0u,0x9230u,0x9270u,0x92a0u,0x92e0u,0x9310u,0x9350u,0x9380u,0x93c0u,0x0u,0x80u,0x110u,0x190u,0x440u,0x4c0u,0x550u,0x5d0u,0x220u,0x2a0u,0x330u,0x3b0u,0x660u,0x6e0u,0x770u,0x7f0u,0x800u,0x880u,0x910u,0x990u,0xc40u,0xcc0u,0xd50u,0xdd0u,0xa20u,0xaa0u,0xb30u,0xbb0u,0xe60u,0xee0u,0xf70u,0xff0u,0x10u,0x90u,0x100u,0x180u,0x450u,0x4d0u,0x540u,0x5c0u,0x230u,0x2b0u,0x320u,0x3a0u,0x670u,0x6f0u,0x760u,0x7e0u,0x810u,0x890u,0x900u,0x980u,0xc50u,0xcd0u,0xd40u,0xdc0u,0xa30u,0xab0u,0xb20u,0xba0u,0xe70u,0xef0u,0xf60u,0xfe0u,0x9000u,0x9010u,0x9020u,0x9030u,0x9040u,0x9050u,0x9060u,0x9070u,0x9080u,0x9090u,0x90a0u,0x90b0u,0x90c0u,0x90d0u,0x90e0u,0x90f0u,0x9008u,0x9018u,0x9028u,0x9038u,0x9048u,0x9058u,0x9068u,0x9078u,0x9088u,0x9098u,0x90a8u,0x90b8u,0x90c8u,0x90d8u,0x90e8u,0x90f8u,0x9200u,0x9210u,0x9220u,0x9230u,0x9240u,0x9250u,0x9260u,0x9270u,0x9280u,0x9290u,0x92a0u,0x92b0u,0x92c0u,0x92d0u,0x92e0u,0x92f0u,0x9208u,0x9218u,0x9228u,0x9238u,0x9248u,0x9258u,0x9268u,0x9278u,0x9288u,0x9298u,0x92a8u,0x92b8u,0x92c8u,0x92d8u,0x92e8u,0x92f8u,0x23300u,0x23302u,0x23304u,0x23306u,0x23308u,0x2330au,0x2330cu,0x2330eu,0x23310u,0x23312u,0x23314u,0x23316u,0x23318u,0x2331au,0x2331cu,0x2331eu,0x23300u,0x23302u,0x23304u,0x23306u,0x23308u,0x2330au,0x2330cu,0x2330eu,0x23310u,0x23312u,0x23314u,0x23316u,0x23318u,0x2331au,0x2331cu,0x2331eu,0x23300u,0x23302u,0x23304u,0x23306u,0x23308u,0x2330au,0x2330cu,0x2330eu,0x23310u,0x23312u,0x23314u,0x23316u,0x23318u,0x2331au,0x2331cu,0x2331eu,0x23300u,0x23302u,0x23304u,0x23306u,0x23308u,0x2330au,0x2330cu,0x2330eu,0x23310u,0x23312u,0x23314u,0x23316u,0x23318u,0x2331au,0x2331cu,0x2331eu,0x27d00u,0x27d02u,0x27d04u,0x27d06u,0x27d08u,0x27d0au,0x27d0cu,0x27d0eu,0x27d10u,0x27d12u,0x27d14u,0x27d16u,0x27d18u,0x27d1au,0x27d1cu,0x27d1eu,0x27d20u,0x27d22u,0x27d24u,0x27d26u,0x27d28u,0x27d2au,0x27d2cu,0x27d2eu,0x27d30u,0x27d32u,0x27d34u,0x27d36u,0x27d38u,0x27d3au,0x27d3cu,0x27d3eu,0x27d40u,0x27d42u,0x27d44u,0x27d46u,0x27d48u,0x27d4au,0x27d4cu,0x27d4eu,0x27d50u,0x27d52u,0x27d54u,0x27d56u,0x27d58u,0x27d5au,0x27d5cu,0x27d5eu,0x100000u,0x100000u,0x100000u,0x100000u,0x100000u,0x100000u,0x100000u,0x100000u,0x100000u,0x100000u,0x100000u,0x100000u,0x100000u,0x100000u,0x100000u,0x100000u,0x23900u,0x23910u,0x23920u,0x23930u,0x23940u,0x23950u,0x23960u,0x23970u,0x23980u,0x23990u,0x239a0u,0x239b0u,0x239c0u,0x239d0u,0x239e0u,0x239f0u,0x23904u,0x23914u,0x23924u,0x23934u,0x23944u,0x23954u,0x23964u,0x23974u,0x23984u,0x23994u,0x239a4u,0x239b4u,0x239c4u,0x239d4u,0x239e4u,0x239f4u,0x23a00u,0x23a10u,0x23a20u,0x23a30u,0x23a40u,0x23a50u,0x23a60u,0x23a70u,0x23a80u,0x23a90u,0x23aa0u,0x23ab0u,0x23ac0u,0x23ad0u,0x23ae0u,0x23af0u,0x23a04u,0x23a14u,0x23a24u,0x23a34u,0x23a44u,0x23a54u,0x23a64u,0x23a74u,0x23a84u,0x23a94u,0x23aa4u,0x23ab4u,0x23ac4u,0x23ad4u,0x23ae4u,0x23af4u,0x23900u,0x23910u,0x23920u,0x23930u,0x23940u,0x23950u,0x23960u,0x23970u,0x23980u,0x23990u,0x239a0u,0x239b0u,0x239c0u,0x239d0u,0x239e0u,0x239f0u,0x23a00u,0x23a10u,0x23a20u,0x23a30u,0x23a40u,0x23a50u,0x23a60u,0x23a70u,0x23a80u,0x23a90u,0x23aa0u,0x23ab0u,0x23ac0u,0x23ad0u,0x23ae0u,0x23af0u,0x23900u,0x23910u,0x23920u,0x23930u,0x23940u,0x23950u,0x23960u,0x23970u,0x23980u,0x23990u,0x239a0u,0x239b0u,0x239c0u,0x239d0u,0x239e0u,0x239f0u,0x23a00u,0x23a10u,0x23a20u,0x23a30u,0x23a40u,0x23a50u,0x23a60u,0x23a70u,0x23a80u,0x23a90u,0x23aa0u,0x23ab0u,0x23ac0u,0x23ad0u,0x23ae0u,0x23af0u,0x0u,0x10u,0x20u,0x30u,0x180u,0x190u,0x1a0u,0x1b0u,0x310u,0x300u,0x330u,0x320u,0x490u,0x480u,0x4b0u,0x4a0u,0x620u,0x630u,0x600u,0x610u,0x7a0u,0x7b0u,0x780u,0x790u,0x930u,0x920u,0x910u,0x900u,0x2feb0u,0x2fea0u,0x2fe90u,0x2fe80u,0x30000u,0x30010u,0x30020u,0x30030u,0x30180u,0x30190u,0x301a0u,0x301b0u,0x30310u,0x30300u,0x30330u,0x30320u,0x30490u,0x30480u,0x304b0u,0x304a0u,0x30620u,0x30630u,0x30600u,0x30610u,0x307a0u,0x307b0u,0x30780u,0x30790u,0x5fd30u,0x5fd20u,0x5fd10u,0x5fd00u,0x5feb0u,0x5fea0u,0x5fe90u,0x5fe80u,0x60000u,0x60010u,0x60020u,0x60030u,0x60180u,0x60190u,0x601a0u,0x601b0u,0x60310u,0x60300u,0x60330u,0x60320u,0x60490u,0x60480u,0x604b0u,0x604a0u,0x60620u,0x60630u,0x60600u,0x60610u,0x8fba0u,0x8fbb0u,0x8fb80u,0x8fb90u,0x8fd30u,0x8fd20u,0x8fd10u,0x8fd00u,0x8feb0u,0x8fea0u,0x8fe90u,0x8fe80u,0x90000u,0x90010u,0x90020u,0x90030u,0x90180u,0x90190u,0x901a0u,0x901b0u,0x90310u,0x90300u,0x90330u,0x90320u,0x90490u,0x90480u,0x904b0u,0x904a0u,0xbfa20u,0xbfa30u,0xbfa00u,0xbfa10u,0xbfba0u,0xbfbb0u,0xbfb80u,0xbfb90u,0xbfd30u,0xbfd20u,0xbfd10u,0xbfd00u,0xbfeb0u,0xbfea0u,0xbfe90u,0xbfe80u,0x1e400u,0x1e462u,0x1e4c4u,0x1e526u,0x1e588u,0x1e5eau,0x1e64cu,0x1e6aeu,0x1e710u,0x1e772u,0x1e7d4u,0x1e836u,0x1e898u,0x1e8fau,0x1e95cu,0x1e9beu,0x1e408u,0x1e46au,0x1e4ccu,0x1e52eu,0x1e590u,0x1e5f2u,0x1e654u,0x1e6b6u,0x1e718u,0x1e77au,0x1e7dcu,0x1e83eu,0x1e8a0u,0x1e902u,0x1e964u,0x1e9c6u,0x1e410u,0x1e472u,0x1e4d4u,0x1e536u,0x1e598u,0x1e5fau,0x1e65cu,0x1e6beu,0x1e720u,0x1e782u,0x1e7e4u,0x1e846u,0x1e8a8u,0x1e90au,0x1e96cu,0x1e9ceu,0x1e418u,0x1e47au,0x1e4dcu,0x1e53eu,0x1e5a0u,0x1e602u,0x1e664u,0x1e6c6u,0x1e728u,0x1e78au,0x1e7ecu,0x1e84eu,0x1e8b0u,0x1e912u,0x1e974u,0x1e9d6u,0xc0000u,0xc0010u,0xc0020u,0xc0030u,0xc0040u,0xc0050u,0xc0060u,0xc0070u,0xc0180u,0xc0190u,0xc01a0u,0xc01b0u,0xc01c0u,0xc01d0u,0xc01e0u,0xc01f0u,0xc0310u,0xc0300u,0xc0330u,0xc0320u,0xc0350u,0xc0340u,0xc0370u,0xc0360u,0xef890u,0xef880u,0xef8b0u,0xef8a0u,0xef8d0u,0xef8c0u,0xef8f0u,0xef8e0u,0xefa20u,0xefa30u,0xefa00u,0xefa10u,0xefa60u,0xefa70u,0xefa40u,0xefa50u,0xefba0u,0xefbb0u,0xefb80u,0xefb90u,0xefbe0u,0xefbf0u,0xefbc0u,0xefbd0u,0xefd30u,0xefd20u,0xefd10u,0xefd00u,0xefd70u,0xefd60u,0xefd50u,0xefd40u,0xefeb0u,0xefea0u,0xefe90u,0xefe80u,0xefef0u,0xefee0u,0xefed0u,0xefec0u,0x9800u,0x9840u,0x9890u,0x98d0u,0x9920u,0x9960u,0x99b0u,0x99f0u,0x9a00u,0x9a40u,0x9a90u,0x9ad0u,0x9b20u,0x9b60u,0x9bb0u,0x9bf0u,0x9810u,0x9850u,0x9880u,0x98c0u,0x9930u,0x9970u,0x99a0u,0x99e0u,0x9a10u,0x9a50u,0x9a80u,0x9ac0u,0x9b30u,0x9b70u,0x9ba0u,0x9be0u,0x9820u,0x9860u,0x98b0u,0x98f0u,0x9900u,0x9940u,0x9990u,0x99d0u,0x9a20u,0x9a60u,0x9ab0u,0x9af0u,0x9b00u,0x9b40u,0x9b90u,0x9bd0u,0x9830u,0x9870u,0x98a0u,0x98e0u,0x9910u,0x9950u,0x9980u,0x99c0u,0x9a30u,0x9a70u,0x9aa0u,0x9ae0u,0x9b10u,0x9b50u,0x9b80u,0x9bc0u,0x0u,0x80u,0x110u,0x190u,0x440u,0x4c0u,0x550u,0x5d0u,0x220u,0x2a0u,0x330u,0x3b0u,0x660u,0x6e0u,0x770u,0x7f0u,0x800u,0x880u,0x910u,0x990u,0xc40u,0xcc0u,0xd50u,0xdd0u,0xa20u,0xaa0u,0xb30u,0xbb0u,0xe60u,0xee0u,0xf70u,0xff0u,0x10u,0x90u,0x100u,0x180u,0x450u,0x4d0u,0x540u,0x5c0u,0x230u,0x2b0u,0x320u,0x3a0u,0x670u,0x6f0u,0x760u,0x7e0u,0x810u,0x890u,0x900u,0x980u,0xc50u,0xcd0u,0xd40u,0xdc0u,0xa30u,0xab0u,0xb20u,0xba0u,0xe70u,0xef0u,0xf60u,0xfe0u,0xb400u,0xb410u,0xb420u,0xb430u,0xb440u,0xb450u,0xb460u,0xb470u,0xb480u,0xb490u,0xb4a0u,0xb4b0u,0xb4c0u,0xb4d0u,0xb4e0u,0xb4f0u,0xb408u,0xb418u,0xb428u,0xb438u,0xb448u,0xb458u,0xb468u,0xb478u,0xb488u,0xb498u,0xb4a8u,0xb4b8u,0xb4c8u,0xb4d8u,0xb4e8u,0xb4f8u,0xb600u,0xb610u,0xb620u,0xb630u,0xb640u,0xb650u,0xb660u,0xb670u,0xb680u,0xb690u,0xb6a0u,0xb6b0u,0xb6c0u,0xb6d0u,0xb6e0u,0xb6f0u,0xb608u,0xb618u,0xb628u,0xb638u,0xb648u,0xb658u,0xb668u,0xb678u,0xb688u,0xb698u,0xb6a8u,0xb6b8u,0xb6c8u,0xb6d8u,0xb6e8u,0xb6f8u,0x23380u,0x23382u,0x23384u,0x23386u,0x23388u,0x2338au,0x2338cu,0x2338eu,0x23390u,0x23392u,0x23394u,0x23396u,0x23398u,0x2339au,0x2339cu,0x2339eu,0x23380u,0x23382u,0x23384u,0x23386u,0x23388u,0x2338au,0x2338cu,0x2338eu,0x23390u,0x23392u,0x23394u,0x23396u,0x23398u,0x2339au,0x2339cu,0x2339eu,0x23380u,0x23382u,0x23384u,0x23386u,0x23388u,0x2338au,0x2338cu,0x2338eu,0x23390u,0x23392u,0x23394u,0x23396u,0x23398u,0x2339au,0x2339cu,0x2339eu,0x23380u,0x23382u,0x23384u,0x23386u,0x23388u,0x2338au,0x2338cu,0x2338eu,0x23390u,0x23392u,0x23394u,0x23396u,0x23398u,0x2339au,0x2339cu,0x2339eu,0x27e20u,0x27e22u,0x27e24u,0x27e26u,0x27e28u,0x27e2au,0x27e2cu,0x27e2eu,0x27e30u,0x27e32u,0x27e34u,0x27e36u,0x27e38u,0x27e3au,0x27e3cu,0x27e3eu,0x27e40u,0x27e42u,0x27e44u,0x27e46u,0x27e48u,0x27e4au,0x27e4cu,0x27e4eu,0x27e50u,0x27e52u,0x27e54u,0x27e56u,0x27e58u,0x27e5au,0x27e5cu,0x27e5eu,0x27e60u,0x27e62u,0x27e64u,0x27e66u,0x27e68u,0x27e6au,0x27e6cu,0x27e6eu,0x27e70u,0x27e72u,0x27e74u,0x27e76u,0x27e78u,0x27e7au,0x27e7cu,0x27e7eu,0x100000u,0x100000u,0x100000u,0x100000u,0x100000u,0x100000u,0x100000u,0x100000u,0x100000u,0x100000u,0x100000u,0x100000u,0x100000u,0x100000u,0x100000u,0x100000u,0x23908u,0x23918u,0x23928u,0x23938u,0x23948u,0x23958u,0x23968u,0x23978u,0x23988u,0x23998u,0x239a8u,0x239b8u,0x239c8u,0x239d8u,0x239e8u,0x239f8u,0x2390cu,0x2391cu,0x2392cu,0x2393cu,0x2394cu,0x2395cu,0x2396cu,0x2397cu,0x2398cu,0x2399cu,0x239acu,0x239bcu,0x239ccu,0x239dcu,0x239ecu,0x239fcu,0x23a08u,0x23a18u,0x23a28u,0x23a38u,0x23a48u,0x23a58u,0x23a68u,0x23a78u,0x23a88u,0x23a98u,0x23aa8u,0x23ab8u,0x23ac8u,0x23ad8u,0x23ae8u,0x23af8u,0x23a0cu,0x23a1cu,0x23a2cu,0x23a3cu,0x23a4cu,0x23a5cu,0x23a6cu,0x23a7cu,0x23a8cu,0x23a9cu,0x23aacu,0x23abcu,0x23accu,0x23adcu,0x23aecu,0x23afcu,0x23900u,0x23910u,0x23920u,0x23930u,0x23940u,0x23950u,0x23960u,0x23970u,0x23980u,0x23990u,0x239a0u,0x239b0u,0x239c0u,0x239d0u,0x239e0u,0x239f0u,0x23a00u,0x23a10u,0x23a20u,0x23a30u,0x23a40u,0x23a50u,0x23a60u,0x23a70u,0x23a80u,0x23a90u,0x23aa0u,0x23ab0u,0x23ac0u,0x23ad0u,0x23ae0u,0x23af0u,0x23900u,0x23910u,0x23920u,0x23930u,0x23940u,0x23950u,0x23960u,0x23970u,0x23980u,0x23990u,0x239a0u,0x239b0u,0x239c0u,0x239d0u,0x239e0u,0x239f0u,0x23a00u,0x23a10u,0x23a20u,0x23a30u,0x23a40u,0x23a50u,0x23a60u,0x23a70u,0x23a80u,0x23a90u,0x23aa0u,0x23ab0u,0x23ac0u,0x23ad0u,0x23ae0u,0x23af0u,0xc0000u,0xc0010u,0xc0020u,0xc0030u,0xc0180u,0xc0190u,0xc01a0u,0xc01b0u,0xc0310u,0xc0300u,0xc0330u,0xc0320u,0xef890u,0xef880u,0xef8b0u,0xef8a0u,0xefa20u,0xefa30u,0xefa00u,0xefa10u,0xefba0u,0xefbb0u,0xefb80u,0xefb90u,0xefd30u,0xefd20u,0xefd10u,0xefd00u,0xefeb0u,0xefea0u,0xefe90u,0xefe80u,0xf0000u,0xf0010u,0xf0020u,0xf0030u,0xf0180u,0xf0190u,0xf01a0u,0xf01b0u,0x11f710u,0x11f700u,0x11f730u,0x11f720u,0x11f890u,0x11f880u,0x11f8b0u,0x11f8a0u,0x11fa20u,0x11fa30u,0x11fa00u,0x11fa10u,0x11fba0u,0x11fbb0u,0x11fb80u,0x11fb90u,0x11fd30u,0x11fd20u,0x11fd10u,0x11fd00u,0x11feb0u,0x11fea0u,0x11fe90u,0x11fe80u,0xc0040u,0xc01c0u,0xc0340u,0xef8c0u,0xefa40u,0xefbc0u,0xefd40u,0xefec0u,0xf0040u,0xf01c0u,0x11f740u,0x11f8c0u,0x11fa40u,0x11fbc0u,0x11fd40u,0x11fec0u,0xc0040u,0xc01c0u,0xc0340u,0xef8c0u,0xefa40u,0xefbc0u,0xefd40u,0xefec0u,0xf0040u,0xf01c0u,0x11f740u,0x11f8c0u,0x11fa40u,0x11fbc0u,0x11fd40u,0x11fec0u,0xc0050u,0xc01d0u,0xc0350u,0xef8d0u,0xefa50u,0xefbd0u,0xefd50u,0xefed0u,0xf0050u,0xf01d0u,0x11f750u,0x11f8d0u,0x11fa50u,0x11fbd0u,0x11fd50u,0x11fed0u,0xc0050u,0xc01d0u,0xc0350u,0xef8d0u,0xefa50u,0xefbd0u,0xefd50u,0xefed0u,0xf0050u,0xf01d0u,0x11f750u,0x11f8d0u,0x11fa50u,0x11fbd0u,0x11fd50u,0x11fed0u,0x1f040u,0x1f0a2u,0x1f104u,0x1f166u,0x1f1c8u,0x1f22au,0x1f28cu,0x1f2eeu,0x1f350u,0x1f3b2u,0x1f414u,0x1f476u,0x1f4d8u,0x1f53au,0x1f59cu,0x1f5feu,0x1f048u,0x1f0aau,0x1f10cu,0x1f16eu,0x1f1d0u,0x1f232u,0x1f294u,0x1f2f6u,0x1f358u,0x1f3bau,0x1f41cu,0x1f47eu,0x1f4e0u,0x1f542u,0x1f5a4u,0x1f606u,0x1f050u,0x1f0b2u,0x1f114u,0x1f176u,0x1f1d8u,0x1f23au,0x1f29cu,0x1f2feu,0x1f360u,0x1f3c2u,0x1f424u,0x1f486u,0x1f4e8u,0x1f54au,0x1f5acu,0x1f60eu,0x1f058u,0x1f0bau,0x1f11cu,0x1f17eu,0x1f1e0u,0x1f242u,0x1f2a4u,0x1f306u,0x1f368u,0x1f3cau,0x1f42cu,0x1f48eu,0x1f4f0u,0x1f552u,0x1f5b4u,0x1f616u,0xf0040u,0xf0050u,0xf0060u,0xf0070u,0xf0000u,0xf0010u,0xf0020u,0xf0030u,0xf01c0u,0xf01d0u,0xf01e0u,0xf01f0u,0xf0180u,0xf0190u,0xf01a0u,0xf01b0u,0x11f750u,0x11f740u,0x11f770u,0x11f760u,0x11f710u,0x11f700u,0x11f730u,0x11f720u,0x11f8d0u,0x11f8c0u,0x11f8f0u,0x11f8e0u,0x11f890u,0x11f880u,0x11f8b0u,0x11f8a0u,0x11fa60u,0x11fa70u,0x11fa40u,0x11fa50u,0x11fa20u,0x11fa30u,0x11fa00u,0x11fa10u,0x11fbe0u,0x11fbf0u,0x11fbc0u,0x11fbd0u,0x11fba0u,0x11fbb0u,0x11fb80u,0x11fb90u,0x11fd70u,0x11fd60u,0x11fd50u,0x11fd40u,0x11fd30u,0x11fd20u,0x11fd10u,0x11fd00u,0x11fef0u,0x11fee0u,0x11fed0u,0x11fec0u,0x11feb0u,0x11fea0u,0x11fe90u,0x11fe80u,0xa000u,0xa080u,0xa110u,0xa190u,0xa220u,0xa2a0u,0xa330u,0xa3b0u,0xa440u,0xa4c0u,0xa550u,0xa5d0u,0xa660u,0xa6e0u,0xa770u,0xa7f0u,0xa010u,0xa090u,0xa100u,0xa180u,0xa230u,0xa2b0u,0xa320u,0xa3a0u,0xa450u,0xa4d0u,0xa540u,0xa5c0u,0xa670u,0xa6f0u,0xa760u,0xa7e0u,0xa020u,0xa0a0u,0xa130u,0xa1b0u,0xa200u,0xa280u,0xa310u,0xa390u,0xa460u,0xa4e0u,0xa570u,0xa5f0u,0xa640u,0xa6c0u,0xa750u,0xa7d0u,0xa030u,0xa0b0u,0xa120u,0xa1a0u,0xa210u,0xa290u,0xa300u,0xa380u,0xa470u,0xa4f0u,0xa560u,0xa5e0u,0xa650u,0xa6d0u,0xa740u,0xa7c0u,0x0u,0x80u,0x110u,0x190u,0x440u,0x4c0u,0x550u,0x5d0u,0x220u,0x2a0u,0x330u,0x3b0u,0x660u,0x6e0u,0x770u,0x7f0u,0x800u,0x880u,0x910u,0x990u,0xc40u,0xcc0u,0xd50u,0xdd0u,0xa20u,0xaa0u,0xb30u,0xbb0u,0xe60u,0xee0u,0xf70u,0xff0u,0x10u,0x90u,0x100u,0x180u,0x450u,0x4d0u,0x540u,0x5c0u,0x230u,0x2b0u,0x320u,0x3a0u,0x670u,0x6f0u,0x760u,0x7e0u,0x810u,0x890u,0x900u,0x980u,0xc50u,0xcd0u,0xd40u,0xdc0u,0xa30u,0xab0u,0xb20u,0xba0u,0xe70u,0xef0u,0xf60u,0xfe0u,0xa000u,0xa010u,0xa020u,0xa030u,0xa040u,0xa050u,0xa060u,0xa070u,0xa080u,0xa090u,0xa0a0u,0xa0b0u,0xa0c0u,0xa0d0u,0xa0e0u,0xa0f0u,0xa008u,0xa018u,0xa028u,0xa038u,0xa048u,0xa058u,0xa068u,0xa078u,0xa088u,0xa098u,0xa0a8u,0xa0b8u,0xa0c8u,0xa0d8u,0xa0e8u,0xa0f8u,0xa200u,0xa210u,0xa220u,0xa230u,0xa240u,0xa250u,0xa260u,0xa270u,0xa280u,0xa290u,0xa2a0u,0xa2b0u,0xa2c0u,0xa2d0u,0xa2e0u,0xa2f0u,0xa208u,0xa218u,0xa228u,0xa238u,0xa248u,0xa258u,0xa268u,0xa278u,0xa288u,0xa298u,0xa2a8u,0xa2b8u,0xa2c8u,0xa2d8u,0xa2e8u,0xa2f8u,0x23400u,0x23402u,0x23404u,0x23406u,0x23408u,0x2340au,0x2340cu,0x2340eu,0x23410u,0x23412u,0x23414u,0x23416u,0x23418u,0x2341au,0x2341cu,0x2341eu,0x23400u,0x23402u,0x23404u,0x23406u,0x23408u,0x2340au,0x2340cu,0x2340eu,0x23410u,0x23412u,0x23414u,0x23416u,0x23418u,0x2341au,0x2341cu,0x2341eu,0x23400u,0x23402u,0x23404u,0x23406u,0x23408u,0x2340au,0x2340cu,0x2340eu,0x23410u,0x23412u,0x23414u,0x23416u,0x23418u,0x2341au,0x2341cu,0x2341eu,0x23400u,0x23402u,0x23404u,0x23406u,0x23408u,0x2340au,0x2340cu,0x2340eu,0x23410u,0x23412u,0x23414u,0x23416u,0x23418u,0x2341au,0x2341cu,0x2341eu,0x27d80u,0x27d82u,0x27d84u,0x27d86u,0x27d88u,0x27d8au,0x27d8cu,0x27d8eu,0x27d90u,0x27d92u,0x27d94u,0x27d96u,0x27d98u,0x27d9au,0x27d9cu,0x27d9eu,0x27da0u,0x27da2u,0x27da4u,0x27da6u,0x27da8u,0x27daau,0x27dacu,0x27daeu,0x27db0u,0x27db2u,0x27db4u,0x27db6u,0x27db8u,0x27dbau,0x27dbcu,0x27dbeu,0x27d60u,0x27d62u,0x27d64u,0x27d66u,0x27d68u,0x27d6au,0x27d6cu,0x27d6eu,0x27d70u,0x27d72u,0x27d74u,0x27d76u,0x27d78u,0x27d7au,0x27d7cu,0x27d7eu,0x100000u,0x100000u,0x100000u,0x100000u,0x100000u,0x100000u,0x100000u,0x100000u,0x100000u,0x100000u,0x100000u,0x100000u,0x100000u,0x100000u,0x100000u,0x100000u,0x23d00u,0x23d10u,0x23d20u,0x23d30u,0x23d40u,0x23d50u,0x23d60u,0x23d70u,0x23d80u,0x23d90u,0x23da0u,0x23db0u,0x23dc0u,0x23dd0u,0x23de0u,0x23df0u,0x23d04u,0x23d14u,0x23d24u,0x23d34u,0x23d44u,0x23d54u,0x23d64u,0x23d74u,0x23d84u,0x23d94u,0x23da4u,0x23db4u,0x23dc4u,0x23dd4u,0x23de4u,0x23df4u,0x23e00u,0x23e10u,0x23e20u,0x23e30u,0x23e40u,0x23e50u,0x23e60u,0x23e70u,0x23e80u,0x23e90u,0x23ea0u,0x23eb0u,0x23ec0u,0x23ed0u,0x23ee0u,0x23ef0u,0x23e04u,0x23e14u,0x23e24u,0x23e34u,0x23e44u,0x23e54u,0x23e64u,0x23e74u,0x23e84u,0x23e94u,0x23ea4u,0x23eb4u,0x23ec4u,0x23ed4u,0x23ee4u,0x23ef4u,0x23900u,0x23910u,0x23920u,0x23930u,0x23940u,0x23950u,0x23960u,0x23970u,0x23980u,0x23990u,0x239a0u,0x239b0u,0x239c0u,0x239d0u,0x239e0u,0x239f0u,0x23a00u,0x23a10u,0x23a20u,0x23a30u,0x23a40u,0x23a50u,0x23a60u,0x23a70u,0x23a80u,0x23a90u,0x23aa0u,0x23ab0u,0x23ac0u,0x23ad0u,0x23ae0u,0x23af0u,0x23900u,0x23910u,0x23920u,0x23930u,0x23940u,0x23950u,0x23960u,0x23970u,0x23980u,0x23990u,0x239a0u,0x239b0u,0x239c0u,0x239d0u,0x239e0u,0x239f0u,0x23a00u,0x23a10u,0x23a20u,0x23a30u,0x23a40u,0x23a50u,0x23a60u,0x23a70u,0x23a80u,0x23a90u,0x23aa0u,0x23ab0u,0x23ac0u,0x23ad0u,0x23ae0u,0x23af0u,0x0u,0x10u,0x20u,0x30u,0x40u,0x50u,0x60u,0x70u,0x180u,0x190u,0x1a0u,0x1b0u,0x1c0u,0x1d0u,0x1e0u,0x1f0u,0x310u,0x300u,0x330u,0x320u,0x350u,0x340u,0x370u,0x360u,0x490u,0x480u,0x4b0u,0x4a0u,0x4d0u,0x4c0u,0x4f0u,0x4e0u,0x620u,0x630u,0x600u,0x610u,0x660u,0x670u,0x640u,0x650u,0x7a0u,0x7b0u,0x780u,0x790u,0x7e0u,0x7f0u,0x7c0u,0x7d0u,0x930u,0x920u,0x910u,0x900u,0x970u,0x960u,0x950u,0x940u,0x2feb0u,0x2fea0u,0x2fe90u,0x2fe80u,0x2fef0u,0x2fee0u,0x2fed0u,0x2fec0u,0x30040u,0x30050u,0x30060u,0x30070u,0x30000u,0x30010u,0x30020u,0x30030u,0x301c0u,0x301d0u,0x301e0u,0x301f0u,0x30180u,0x30190u,0x301a0u,0x301b0u,0x30350u,0x30340u,0x30370u,0x30360u,0x30310u,0x30300u,0x30330u,0x30320u,0x304d0u,0x304c0u,0x304f0u,0x304e0u,0x30490u,0x30480u,0x304b0u,0x304a0u,0x30660u,0x30670u,0x30640u,0x30650u,0x30620u,0x30630u,0x30600u,0x30610u,0x307e0u,0x307f0u,0x307c0u,0x307d0u,0x307a0u,0x307b0u,0x30780u,0x30790u,0x5fd70u,0x5fd60u,0x5fd50u,0x5fd40u,0x5fd30u,0x5fd20u,0x5fd10u,0x5fd00u,0x5fef0u,0x5fee0u,0x5fed0u,0x5fec0u,0x5feb0u,0x5fea0u,0x5fe90u,0x5fe80u,0x1ea20u,0x1ea82u,0x1eae4u,0x1eb46u,0x1eba8u,0x1ec0au,0x1ec6cu,0x1ecceu,0x1ed30u,0x1ed92u,0x1edf4u,0x1ee56u,0x1eeb8u,0x1ef1au,0x1ef7cu,0x1efdeu,0x1ea28u,0x1ea8au,0x1eaecu,0x1eb4eu,0x1ebb0u,0x1ec12u,0x1ec74u,0x1ecd6u,0x1ed38u,0x1ed9au,0x1edfcu,0x1ee5eu,0x1eec0u,0x1ef22u,0x1ef84u,0x1efe6u,0x1ea30u,0x1ea92u,0x1eaf4u,0x1eb56u,0x1ebb8u,0x1ec1au,0x1ec7cu,0x1ecdeu,0x1ed40u,0x1eda2u,0x1ee04u,0x1ee66u,0x1eec8u,0x1ef2au,0x1ef8cu,0x1efeeu,0x1ea38u,0x1ea9au,0x1eafcu,0x1eb5eu,0x1ebc0u,0x1ec22u,0x1ec84u,0x1ece6u,0x1ed48u,0x1edaau,0x1ee0cu,0x1ee6eu,0x1eed0u,0x1ef32u,0x1ef94u,0x1eff6u,0xc0000u,0xc0010u,0xc0020u,0xc0030u,0xc0040u,0xc0050u,0xc0060u,0xc0070u,0xc0180u,0xc0190u,0xc01a0u,0xc01b0u,0xc01c0u,0xc01d0u,0xc01e0u,0xc01f0u,0xc0310u,0xc0300u,0xc0330u,0xc0320u,0xc0350u,0xc0340u,0xc0370u,0xc0360u,0xef890u,0xef880u,0xef8b0u,0xef8a0u,0xef8d0u,0xef8c0u,0xef8f0u,0xef8e0u,0xefa20u,0xefa30u,0xefa00u,0xefa10u,0xefa60u,0xefa70u,0xefa40u,0xefa50u,0xefba0u,0xefbb0u,0xefb80u,0xefb90u,0xefbe0u,0xefbf0u,0xefbc0u,0xefbd0u,0xefd30u,0xefd20u,0xefd10u,0xefd00u,0xefd70u,0xefd60u,0xefd50u,0xefd40u,0xefeb0u,0xefea0u,0xefe90u,0xefe80u,0xefef0u,0xefee0u,0xefed0u,0xefec0u,0xb000u,0xb080u,0xb110u,0xb190u,0xb220u,0xb2a0u,0xb330u,0xb3b0u,0xb440u,0xb4c0u,0xb550u,0xb5d0u,0xb660u,0xb6e0u,0xb770u,0xb7f0u,0xb010u,0xb090u,0xb100u,0xb180u,0xb230u,0xb2b0u,0xb320u,0xb3a0u,0xb450u,0xb4d0u,0xb540u,0xb5c0u,0xb670u,0xb6f0u,0xb760u,0xb7e0u,0xb020u,0xb0a0u,0xb130u,0xb1b0u,0xb200u,0xb280u,0xb310u,0xb390u,0xb460u,0xb4e0u,0xb570u,0xb5f0u,0xb640u,0xb6c0u,0xb750u,0xb7d0u,0xb030u,0xb0b0u,0xb120u,0xb1a0u,0xb210u,0xb290u,0xb300u,0xb380u,0xb470u,0xb4f0u,0xb560u,0xb5e0u,0xb650u,0xb6d0u,0xb740u,0xb7c0u,0x0u,0x80u,0x110u,0x190u,0x440u,0x4c0u,0x550u,0x5d0u,0x220u,0x2a0u,0x330u,0x3b0u,0x660u,0x6e0u,0x770u,0x7f0u,0x800u,0x880u,0x910u,0x990u,0xc40u,0xcc0u,0xd50u,0xdd0u,0xa20u,0xaa0u,0xb30u,0xbb0u,0xe60u,0xee0u,0xf70u,0xff0u,0x10u,0x90u,0x100u,0x180u,0x450u,0x4d0u,0x540u,0x5c0u,0x230u,0x2b0u,0x320u,0x3a0u,0x670u,0x6f0u,0x760u,0x7e0u,0x810u,0x890u,0x900u,0x980u,0xc50u,0xcd0u,0xd40u,0xdc0u,0xa30u,0xab0u,0xb20u,0xba0u,0xe70u,0xef0u,0xf60u,0xfe0u,0xa800u,0xa810u,0xa820u,0xa830u,0xa840u,0xa850u,0xa860u,0xa870u,0xa880u,0xa890u,0xa8a0u,0xa8b0u,0xa8c0u,0xa8d0u,0xa8e0u,0xa8f0u,0xa808u,0xa818u,0xa828u,0xa838u,0xa848u,0xa858u,0xa868u,0xa878u,0xa888u,0xa898u,0xa8a8u,0xa8b8u,0xa8c8u,0xa8d8u,0xa8e8u,0xa8f8u,0xaa00u,0xaa10u,0xaa20u,0xaa30u,0xaa40u,0xaa50u,0xaa60u,0xaa70u,0xaa80u,0xaa90u,0xaaa0u,0xaab0u,0xaac0u,0xaad0u,0xaae0u,0xaaf0u,0xaa08u,0xaa18u,0xaa28u,0xaa38u,0xaa48u,0xaa58u,0xaa68u,0xaa78u,0xaa88u,0xaa98u,0xaaa8u,0xaab8u,0xaac8u,0xaad8u,0xaae8u,0xaaf8u,0x23440u,0x23442u,0x23444u,0x23446u,0x23448u,0x2344au,0x2344cu,0x2344eu,0x23450u,0x23452u,0x23454u,0x23456u,0x23458u,0x2345au,0x2345cu,0x2345eu,0x23440u,0x23442u,0x23444u,0x23446u,0x23448u,0x2344au,0x2344cu,0x2344eu,0x23450u,0x23452u,0x23454u,0x23456u,0x23458u,0x2345au,0x2345cu,0x2345eu,0x23440u,0x23442u,0x23444u,0x23446u,0x23448u,0x2344au,0x2344cu,0x2344eu,0x23450u,0x23452u,0x23454u,0x23456u,0x23458u,0x2345au,0x2345cu,0x2345eu,0x23440u,0x23442u,0x23444u,0x23446u,0x23448u,0x2344au,0x2344cu,0x2344eu,0x23450u,0x23452u,0x23454u,0x23456u,0x23458u,0x2345au,0x2345cu,0x2345eu,0x27dc0u,0x27dc2u,0x27dc4u,0x27dc6u,0x27dc8u,0x27dcau,0x27dccu,0x27dceu,0x27dd0u,0x27dd2u,0x27dd4u,0x27dd6u,0x27dd8u,0x27ddau,0x27ddcu,0x27ddeu,0x27de0u,0x27de2u,0x27de4u,0x27de6u,0x27de8u,0x27deau,0x27decu,0x27deeu,0x27df0u,0x27df2u,0x27df4u,0x27df6u,0x27df8u,0x27dfau,0x27dfcu,0x27dfeu,0x27e00u,0x27e02u,0x27e04u,0x27e06u,0x27e08u,0x27e0au,0x27e0cu,0x27e0eu,0x27e10u,0x27e12u,0x27e14u,0x27e16u,0x27e18u,0x27e1au,0x27e1cu,0x27e1eu,0x100000u,0x100000u,0x100000u,0x100000u,0x100000u,0x100000u,0x100000u,0x100000u,0x100000u,0x100000u,0x100000u,0x100000u,0x100000u,0x100000u,0x100000u,0x100000u,0x23d08u,0x23d18u,0x23d28u,0x23d38u,0x23d48u,0x23d58u,0x23d68u,0x23d78u,0x23d88u,0x23d98u,0x23da8u,0x23db8u,0x23dc8u,0x23dd8u,0x23de8u,0x23df8u,0x23d0cu,0x23d1cu,0x23d2cu,0x23d3cu,0x23d4cu,0x23d5cu,0x23d6cu,0x23d7cu,0x23d8cu,0x23d9cu,0x23dacu,0x23dbcu,0x23dccu,0x23ddcu,0x23decu,0x23dfcu,0x23e08u,0x23e18u,0x23e28u,0x23e38u,0x23e48u,0x23e58u,0x23e68u,0x23e78u,0x23e88u,0x23e98u,0x23ea8u,0x23eb8u,0x23ec8u,0x23ed8u,0x23ee8u,0x23ef8u,0x23e0cu,0x23e1cu,0x23e2cu,0x23e3cu,0x23e4cu,0x23e5cu,0x23e6cu,0x23e7cu,0x23e8cu,0x23e9cu,0x23eacu,0x23ebcu,0x23eccu,0x23edcu,0x23eecu,0x23efcu,0x23900u,0x23910u,0x23920u,0x23930u,0x23940u,0x23950u,0x23960u,0x23970u,0x23980u,0x23990u,0x239a0u,0x239b0u,0x239c0u,0x239d0u,0x239e0u,0x239f0u,0x23a00u,0x23a10u,0x23a20u,0x23a30u,0x23a40u,0x23a50u,0x23a60u,0x23a70u,0x23a80u,0x23a90u,0x23aa0u,0x23ab0u,0x23ac0u,0x23ad0u,0x23ae0u,0x23af0u,0x23900u,0x23910u,0x23920u,0x23930u,0x23940u,0x23950u,0x23960u,0x23970u,0x23980u,0x23990u,0x239a0u,0x239b0u,0x239c0u,0x239d0u,0x239e0u,0x239f0u,0x23a00u,0x23a10u,0x23a20u,0x23a30u,0x23a40u,0x23a50u,0x23a60u,0x23a70u,0x23a80u,0x23a90u,0x23aa0u,0x23ab0u,0x23ac0u,0x23ad0u,0x23ae0u,0x23af0u,0x60000u,0x60010u,0x60020u,0x60030u,0x60040u,0x60050u,0x60060u,0x60070u,0x60180u,0x60190u,0x601a0u,0x601b0u,0x601c0u,0x601d0u,0x601e0u,0x601f0u,0x60310u,0x60300u,0x60330u,0x60320u,0x60350u,0x60340u,0x60370u,0x60360u,0x60490u,0x60480u,0x604b0u,0x604a0u,0x604d0u,0x604c0u,0x604f0u,0x604e0u,0x60620u,0x60630u,0x60600u,0x60610u,0x60660u,0x60670u,0x60640u,0x60650u,0x8fba0u,0x8fbb0u,0x8fb80u,0x8fb90u,0x8fbe0u,0x8fbf0u,0x8fbc0u,0x8fbd0u,0x8fd30u,0x8fd20u,0x8fd10u,0x8fd00u,0x8fd70u,0x8fd60u,0x8fd50u,0x8fd40u,0x8feb0u,0x8fea0u,0x8fe90u,0x8fe80u,0x8fef0u,0x8fee0u,0x8fed0u,0x8fec0u,0x90040u,0x90050u,0x90060u,0x90070u,0x90000u,0x90010u,0x90020u,0x90030u,0x901c0u,0x901d0u,0x901e0u,0x901f0u,0x90180u,0x90190u,0x901a0u,0x901b0u,0x90350u,0x90340u,0x90370u,0x90360u,0x90310u,0x90300u,0x90330u,0x90320u,0x904d0u,0x904c0u,0x904f0u,0x904e0u,0x90490u,0x90480u,0x904b0u,0x904a0u,0xbfa60u,0xbfa70u,0xbfa40u,0xbfa50u,0xbfa20u,0xbfa30u,0xbfa00u,0xbfa10u,0xbfbe0u,0xbfbf0u,0xbfbc0u,0xbfbd0u,0xbfba0u,0xbfbb0u,0xbfb80u,0xbfb90u,0xbfd70u,0xbfd60u,0xbfd50u,0xbfd40u,0xbfd30u,0xbfd20u,0xbfd10u,0xbfd00u,0xbfef0u,0xbfee0u,0xbfed0u,0xbfec0u,0xbfeb0u,0xbfea0u,0xbfe90u,0xbfe80u,0x1f600u,0x1f602u,0x1f604u,0x1f606u,0x1f608u,0x1f60au,0x1f60cu,0x1f60eu,0x1f610u,0x1f612u,0x1f614u,0x1f616u,0x1f618u,0x1f61au,0x1f61cu,0x1f61eu,0x1f620u,0x1f622u,0x1f624u,0x1f626u,0x1f628u,0x1f62au,0x1f62cu,0x1f62eu,0x1f630u,0x1f632u,0x1f634u,0x1f636u,0x1f638u,0x1f63au,0x1f63cu,0x1f63eu,0x1f640u,0x1f642u,0x1f644u,0x1f646u,0x1f648u,0x1f64au,0x1f64cu,0x1f64eu,0x1f650u,0x1f652u,0x1f654u,0x1f656u,0x1f658u,0x1f65au,0x1f65cu,0x1f65eu,0x1f660u,0x1f660u,0x1f660u,0x1f660u,0x1f660u,0x1f660u,0x1f660u,0x1f660u,0x1f660u,0x1f660u,0x1f660u,0x1f660u,0x1f660u,0x1f660u,0x1f660u,0x1f660u,0xf0040u,0xf0050u,0xf0060u,0xf0070u,0xf0000u,0xf0010u,0xf0020u,0xf0030u,0xf01c0u,0xf01d0u,0xf01e0u,0xf01f0u,0xf0180u,0xf0190u,0xf01a0u,0xf01b0u,0x11f750u,0x11f740u,0x11f770u,0x11f760u,0x11f710u,0x11f700u,0x11f730u,0x11f720u,0x11f8d0u,0x11f8c0u,0x11f8f0u,0x11f8e0u,0x11f890u,0x11f880u,0x11f8b0u,0x11f8a0u,0x11fa60u,0x11fa70u,0x11fa40u,0x11fa50u,0x11fa20u,0x11fa30u,0x11fa00u,0x11fa10u,0x11fbe0u,0x11fbf0u,0x11fbc0u,0x11fbd0u,0x11fba0u,0x11fbb0u,0x11fb80u,0x11fb90u,0x11fd70u,0x11fd60u,0x11fd50u,0x11fd40u,0x11fd30u,0x11fd20u,0x11fd10u,0x11fd00u,0x11fef0u,0x11fee0u,0x11fed0u,0x11fec0u,0x11feb0u,0x11fea0u,0x11fe90u,0x11fe80u};

__global__ __launch_bounds__(512, 2) void swin_main(const uint16_t* x, const uint16_t* mask, char* ws, uint16_t* out,
                                                    const uint16_t* wqkv, const uint16_t* wo, const uint16_t* ls,
                                                    const uint16_t* w1, const uint16_t* w2, const uint16_t* rc,
                                                    const int* ridx) {
  __shared__ __attribute__((aligned(16))) char smem[LDS_BYTES];
  const int wave = __builtin_amdgcn_readfirstlane(threadIdx.x >> 6);
  const unsigned lds0 = (unsigned)(size_t)smem;
  asm volatile(
      ".p2align 5\n"
      "s_mov_b64 s[60:61], %0\n"
      "s_mov_b32 s24, %1\n"
      "s_mov_b32 s25, %2\n"
      "s_mov_b64 s[62:63], %3\n"
      "s_load_dwordx8 s[16:23], s[60:61], 0\n"
      "s_load_dwordx4 s[4:7], s[60:61], 32\n"
      "s_load_dwordx8 s[80:87], s[60:61], 48\n"
      "s_load_dwordx2 s[90:91], s[60:61], 80\n"
      "s_getpc_b64 s[58:59]\n"
      "L_ipf0_%=:\n"
      "s_mov_b32 s98, 0x1ffff\n"
      "s_mov_b32 s99, 0x1ffff\n"
      "s_cmp_ge_u32 s24, 6\n"
      "s_cbranch_scc1 L_B1_%=\n"
      "s_cmp_ge_u32 s24, 4\n"
      "s_cbranch_scc1 L_B0_%=\n"
      "v_mbcnt_lo_u32_b32 v20, -1, 0\n"
      "v_mbcnt_hi_u32_b32 v20, -1, v20\n"
      "v_lshlrev_b32 v20, 4, v20\n"
      "s_cmp_lt_u32 s25, 209\n"
      "s_cselect_b32 s26, 11, 10\n"
      "s_mov_b32 s27, 0\n"
      "s_mov_b32 s28, s25\n"
      "v_lshrrev_b32 v21, 2, v20\n"
      "v_lshrrev_b32 v22, 6, v20\n"
      "v_mul_u32_u24 v22, 1152, v22\n"
      "v_and_b32 v23, 48, v20\n"
      "v_add_u32_e64 v22, v22, v23\n"
      "s_mul_i32 s29, s24, 96\n"
      "v_add_u32 v22, s29, v22\n"
      "v_bfe_u32 v23, v20, 5, 1\n"
      "v_mul_u32_u24 v23, 0x160, v23\n"
      "v_add_u32_e64 v23, v23, v22\n"
      "s_waitcnt lgkmcnt(0)\n"
      "s_load_dwordx2 s[52:53], s[80:81], 0\n"
      "s_setprio 3\n"
      "s_lshr_b32 s29, s25, 3\n"
      "s_mov_b64 s[34:35], s[4:5]\n"
      "s_movk_i32 s30, 54\n"
      "s_mov_b32 s31, 221056\n"
      "s_cmp_eq_u32 s24, 1\n"
      "s_cselect_b64 s[34:35], s[6:7], s[34:35]\n"
      "s_cselect_b32 s30, 18, s30\n"
      "s_cselect_b32 s31, 73600, s31\n"
      "s_cmp_eq_u32 s24, 2\n"
      "s_cselect_b64 s[34:35], s[58:59], s[34:35]\n"
      "s_cselect_b32 s30, 18, s30\n"
      "s_cselect_b32 s31, L_cend_%=-L_ipf0_%=-128, s31\n"
      "s_cmp_eq_u32 s24, 3\n"
      "s_cselect_b64 s[34:35], s[62:63], s[34:35]\n"
      "s_cselect_b32 s30, 4, s30\n"
      "s_cselect_b32 s31, 15232, s31\n"
      "s_mul_i32 s29, s29, s30\n"
      "s_lshl_b32 s29, s29, 7\n"
      "s_sub_u32 s30, s30, 1\n"
      "s_lshl_b32 s30, s30, 7\n"
      "v_lshlrev_b32 v24, 3, v20\n"
      "v_min_u32_e64 v24, s30, v24\n"
      "v_add_u32_e64 v24, s29, v24\n"
      "v_min_u32_e64 v24, s31, v24\n"
      "global_load_dword a0, v24, s[34:35]\n"
      "s_mul_i32 s54, s24, 6144\n"
      "s_add_u32 s54, s54, 49152\n"
      "s_add_u32 s34, s4, 0\n"
      "s_addc_u32 s35, s5, 0\n"
      "s_add_u32 m0, s54, 0\n"
      "s_nop 0\n"
      "global_load_lds_dwordx4 v22, s[34:35]\n"
      "s_add_u32 m0, s54, 1984\n"
      "s_nop 0\n"
      "global_load_lds_dwordx4 v23, s[34:35] offset:64\n"
      "s_add_u32 m0, s54, 3712\n"
      "s_nop 0\n"
      "global_load_lds_dwordx4 v22, s[34:35] offset:384\n"
      "s_add_u32 s34, s4, 18432\n"
      "s_addc_u32 s35, s5, 0\n"
      "s_add_u32 m0, s54, 1024\n"
      "s_nop 0\n"
      "global_load_lds_dwordx4 v22, s[34:35]\n"
      "s_add_u32 m0, s54, 3008\n"
      "s_nop 0\n"
      "global_load_lds_dwordx4 v23, s[34:35] offset:64\n"
      "s_add_u32 m0, s54, 4736\n"
      "s_nop 0\n"
      "global_load_lds_dwordx4 v22, s[34:35] offset:384\n"
      "s_add_u32 s34, s4, 36864\n"
      "s_addc_u32 s35, s5, 0\n"
      "s_add_u32 m0, s54, 24576\n"
      "s_nop 0\n"
      "global_load_lds_dwordx4 v22, s[34:35]\n"
      "s_add_u32 m0, s54, 26560\n"
      "s_nop 0\n"
      "global_load_lds_dwordx4 v23, s[34:35] offset:64\n"
      "s_add_u32 m0, s54, 28288\n"
      "s_nop 0\n"
      "global_load_lds_dwordx4 v22, s[34:35] offset:384\n"
      "s_add_u32 s34, s4, 55296\n"
      "s_addc_u32 s35, s5, 0\n"
      "s_add_u32 m0, s54, 25600\n"
      "s_nop 0\n"
      "global_load_lds_dwordx4 v22, s[34:35]\n"
      "s_add_u32 m0, s54, 27584\n"
      "s_nop 0\n"
      "global_load_lds_dwordx4 v23, s[34:35] offset:64\n"
      "s_add_u32 m0, s54, 29312\n"
      "s_nop 0\n"
      "global_load_lds_dwordx4 v22, s[34:35] offset:384\n"
      "s_add_u32 s34, s4, 73728\n"
      "s_addc_u32 s35, s5, 0\n"
      "s_add_u32 m0, s54, 49664\n"
      "s_nop 0\n"
      "global_load_lds_dwordx4 v22, s[34:35]\n"
      "s_add_u32 m0, s54, 51648\n"
      "s_nop 0\n"
      "global_load_lds_dwordx4 v23, s[34:35] offset:64\n"
      "s_add_u32 m0, s54, 53376\n"
      "s_nop 0\n"
      "global_load_lds_dwordx4 v22, s[34:35] offset:384\n"
      "s_add_u32 s34, s4, 92160\n"
      "s_addc_u32 s35, s5, 0\n"
      "s_add_u32 m0, s54, 50688\n"
      "s_nop 0\n"
      "global_load_lds_dwordx4 v22, s[34:35]\n"
      "s_add_u32 m0, s54, 52672\n"
      "s_nop 0\n"
      "global_load_lds_dwordx4 v23, s[34:35] offset:64\n"
      "s_add_u32 m0, s54, 54400\n"
      "s_nop 0\n"
      "global_load_lds_dwordx4 v22, s[34:35] offset:384\n"
      "v_bfe_u32 v24, v20, 5, 3\n"
      "v_lshrrev_b32 v25, 8, v20\n"
      "v_xor_b32 v24, v24, v25\n"
      "v_and_b32 v0, 0xf0, v20\n"
      "v_lshlrev_b32 v0, 3, v0\n"
      "v_lshl_add_u32 v0, v24, 4, v0\n"
      "v_xor_b32 v1, 64, v0\n"
      "v_add_u32 v17, 154112, v21\n"
      "s_mul_i32 s29, s24, 6144\n"
      "s_add_u32 s30, s29, 98816\n"
      "v_lshrrev_b32 v3, 1, v20\n"
      "v_add_u32 v3, s30, v3\n"
      "s_lshl_b32 s30, s24, 10\n"
      "s_add_u32 s30, s30, 147712\n"
      "v_add_u32 v16, s30, v21\n"
      "v_bfe_u32 v24, v20, 4, 4\n"
      "v_lshrrev_b32 v25, 8, v20\n"
      "v_lshlrev_b32 v6, 2, v25\n"
      "v_sub_u32 v24, v24, v6\n"
      "v_and_b32 v25, 1, v24\n"
      "v_lshlrev_b32 v25, 4, v25\n"
      "s_movk_i32 s29, 0x3fb9\n"
      "v_lshlrev_b32 v25, v25, s29\n"
      "v_lshrrev_b32 v24, 1, v24\n"
      "v_cmp_eq_u32 vcc, 0, v24\n"
      "s_nop 1\n"
      "v_cndmask_b32 v6, 0, v25, vcc\n"
      "v_cmp_eq_u32 vcc, 1, v24\n"
      "s_nop 1\n"
      "v_cndmask_b32 v7, 0, v25, vcc\n"
      "v_mov_b32 v10, v6\n"
      "v_mov_b32_e64 v11, v7\n"
      "v_mov_b32_e64 v14, v6\n"
      "v_mov_b32_e64 v15, v7\n"
      "s_mul_i32 s29, s24, 6272\n"
      "s_add_u32 s29, s29, 73728\n"
      "v_add_u32_e64 v2, s29, v20\n"
      "v_and_b32 v21, 0x300, v20\n"
      "v_and_b32 v25, 0xc0, v20\n"
      "v_lshl_or_b32 v21, v21, 1, v25\n"
      "v_bfe_u32 v25, v20, 4, 2\n"
      "v_lshl_or_b32 v21, v25, 3, v21\n"
      "v_add_u32 v21, s54, v21\n"
      "s_mov_b32 s41, 0x3fb8aa3b\n"
      "s_mov_b32 s51, 0x2a8cbccc\n"
      "s_mov_b32 s42, 0xffff0000\n"
      "s_mov_b32 s43, 0x0\n"
      "s_mov_b32 s44, 0x0\n"
      "s_mov_b32 s45, 0xffff\n"
      "s_mov_b32 s46, 0x0\n"
      "s_mov_b32 s47, 0xffff0000\n"
      "s_mov_b32 s56, 0x30003\n"
      "s_mov_b32 s57, 0x30003\n"
      "s_waitcnt lgkmcnt(0)\n"
      "s_lshl_b32 s29, s24, 4\n"
      "s_lshr_b64 s[52:53], s[52:53], s29\n"
      "s_lshl_b32 s52, s52, 16\n"
      "v_mov_b32_e64 v20, s52\n"
      "v_max_f32 v20, 0xc2c80000, v20\n"
      "v_min_f32 v20, 0x40930000, v20\n"
      "v_mul_f32 v20, s41, v20\n"
      "v_exp_f32_e64 v20, v20\n"
      "s_nop 0\n"
      "v_cvt_pk_bf16_f32 v20, v20, v20\n"
      "v_lshlrev_b32 v20, 16, v20\n"
      "v_cmp_nge_f32 vcc, 0x41f00000, v20\n"
      "v_mul_f32 v20, s41, v20\n"
      "s_mov_b64 s[76:77], vcc\n"
      "v_readfirstlane_b32 s40, v20\n"
      "v_mov_b32 v24, 0\n"
      "v_mov_b32 v25, 0\n"
      "v_mov_b32 v26, 0\n"
      "v_mov_b32 v27, 0\n"
      "v_mov_b32 v20, 147200\n"
      "ds_write_b128 v20, v[24:27]\n"
      "ds_write_b128 v20, v[24:27] offset:16\n"
      "ds_write_b128 v20, v[24:27] offset:32\n"
      "s_waitcnt vmcnt(12)\n"
      "ds_read_b64_tr_b16 a[0:1], v21\n"
      "ds_read_b64_tr_b16 a[2:3], v21 offset:256\n"
      "ds_read_b64_tr_b16 a[4:5], v21 offset:32\n"
      "ds_read_b64_tr_b16 a[6:7], v21 offset:288\n"
      "ds_read_b64_tr_b16 a[48:49], v21 offset:2048\n"
      "ds_read_b64_tr_b16 a[50:51], v21 offset:2304\n"
      "ds_read_b64_tr_b16 a[52:53], v21 offset:2080\n"
      "ds_read_b64_tr_b16 a[54:55], v21 offset:2336\n"
      "ds_read_b64_tr_b16 a[96:97], v21 offset:4096\n"
      "ds_read_b64_tr_b16 a[98:99], v21 offset:4352\n"
      "ds_read_b64_tr_b16 a[100:101], v21 offset:4128\n"
      "ds_read_b64_tr_b16 a[102:103], v21 offset:4384\n"
      "s_waitcnt lgkmcnt(0)\n"
      "s_add_u32 s34, s4, 110592\n"
      "s_addc_u32 s35, s5, 0\n"
      "s_add_u32 m0, s54, 0\n"
      "s_nop 0\n"
      "global_load_lds_dwordx4 v22, s[34:35]\n"
      "s_add_u32 m0, s54, 1984\n"
      "s_nop 0\n"
      "global_load_lds_dwordx4 v23, s[34:35] offset:64\n"
      "s_add_u32 m0, s54, 3712\n"
      "s_nop 0\n"
      "global_load_lds_dwordx4 v22, s[34:35] offset:384\n"
      "s_add_u32 s34, s4, 129024\n"
      "s_addc_u32 s35, s5, 0\n"
      "s_add_u32 m0, s54, 1024\n"
      "s_nop 0\n"
      "global_load_lds_dwordx4 v22, s[34:35]\n"
      "s_add_u32 m0, s54, 3008\n"
      "s_nop 0\n"
      "global_load_lds_dwordx4 v23, s[34:35] offset:64\n"
      "s_add_u32 m0, s54, 4736\n"
      "s_nop 0\n"
      "global_load_lds_dwordx4 v22, s[34:35] offset:384\n"
      "s_waitcnt vmcnt(12)\n"
      "ds_read_b64_tr_b16 a[8:9], v21 offset:24576\n"
      "ds_read_b64_tr_b16 a[10:11], v21 offset:24832\n"
      "ds_read_b64_tr_b16 a[12:13], v21 offset:24608\n"
      "ds_read_b64_tr_b16 a[14:15], v21 offset:24864\n"
      "ds_read_b64_tr_b16 a[56:57], v21 offset:26624\n"
      "ds_read_b64_tr_b16 a[58:59], v21 offset:26880\n"
      "ds_read_b64_tr_b16 a[60:61], v21 offset:26656\n"
      "ds_read_b64_tr_b16 a[62:63], v21 offset:26912\n"
      "ds_read_b64_tr_b16 a[104:105], v21 offset:28672\n"
      "ds_read_b64_tr_b16 a[106:107], v21 offset:28928\n"
      "ds_read_b64_tr_b16 a[108:109], v21 offset:28704\n"
      "ds_read_b64_tr_b16 a[110:111], v21 offset:28960\n"
      "s_waitcnt lgkmcnt(0)\n"
      "s_add_u32 s34, s4, 147456\n"
      "s_addc_u32 s35, s5, 0\n"
      "s_add_u32 m0, s54, 24576\n"
      "s_nop 0\n"
      "global_load_lds_dwordx4 v22, s[34:35]\n"
      "s_add_u32 m0, s54, 26560\n"
      "s_nop 0\n"
      "global_load_lds_dwordx4 v23, s[34:35] offset:64\n"
      "s_add_u32 m0, s54, 28288\n"
      "s_nop 0\n"
      "global_load_lds_dwordx4 v22, s[34:35] offset:384\n"
      "s_add_u32 s34, s4, 165888\n"
      "s_addc_u32 s35, s5, 0\n"
      "s_add_u32 m0, s54, 25600\n"
      "s_nop 0\n"
      "global_load_lds_dwordx4 v22, s[34:35]\n"
      "s_add_u32 m0, s54, 27584\n"
      "s_nop 0\n"
      "global_load_lds_dwordx4 v23, s[34:35] offset:64\n"
      "s_add_u32 m0, s54, 29312\n"
      "s_nop 0\n"
      "global_load_lds_dwordx4 v22, s[34:35] offset:384\n"
      "s_waitcnt vmcnt(12)\n"
      "ds_read_b64_tr_b16 a[16:17], v21 offset:49664\n"
      "ds_read_b64_tr_b16 a[18:19], v21 offset:49920\n"
      "ds_read_b64_tr_b16 a[20:21], v21 offset:49696\n"
      "ds_read_b64_tr_b16 a[22:23], v21 offset:49952\n"
      "ds_read_b64_tr_b16 a[64:65], v21 offset:51712\n"
      "ds_read_b64_tr_b16 a[66:67], v21 offset:51968\n"
      "ds_read_b64_tr_b16 a[68:69], v21 offset:51744\n"
      "ds_read_b64_tr_b16 a[70:71], v21 offset:52000\n"
      "ds_read_b64_tr_b16 a[112:113], v21 offset:53760\n"
      "ds_read_b64_tr_b16 a[114:115], v21 offset:54016\n"
      "ds_read_b64_tr_b16 a[116:117], v21 offset:53792\n"
      "ds_read_b64_tr_b16 a[118:119], v21 offset:54048\n"
      "s_waitcnt lgkmcnt(0)\n"
      "s_add_u32 s34, s4, 184320\n"
      "s_addc_u32 s35, s5, 0\n"
      "s_add_u32 m0, s54, 49664\n"
      "s_nop 0\n"
      "global_load_lds_dwordx4 v22, s[34:35]\n"
      "s_add_u32 m0, s54, 51648\n"
      "s_nop 0\n"
      "global_load_lds_dwordx4 v23, s[34:35] offset:64\n"
      "s_add_u32 m0, s54, 53376\n"
      "s_nop 0\n"
      "global_load_lds_dwordx4 v22, s[34:35] offset:384\n"
      "s_add_u32 s34, s4, 202752\n"
      "s_addc_u32 s35, s5, 0\n"
      "s_add_u32 m0, s54, 50688\n"
      "s_nop 0\n"
      "global_load_lds_dwordx4 v22, s[34:35]\n"
      "s_add_u32 m0, s54, 52672\n"
      "s_nop 0\n"
      "global_load_lds_dwordx4 v23, s[34:35] offset:64\n"
      "s_add_u32 m0, s54, 54400\n"
      "s_nop 0\n"
      "global_load_lds_dwordx4 v22, s[34:35] offset:384\n"
      "s_barrier\n"
      "s_mul_i32 s53, s24, 192\n"
      "s_add_u32 s53, s53, 143360\n"
      "ds_read_b128 v[28:31], v0 offset:36864\n"
      "ds_read_b128 v[32:35], v1 offset:36864\n"
      "s_waitcnt lgkmcnt(1)\n"
      "v_mfma_f32_16x16x32_bf16 v[36:39], a[0:3], v[28:31], 0\n"
      "ds_read_b128 v[40:43], v0 offset:40960\n"
      "v_mfma_f32_16x16x32_bf16 v[44:47], a[4:7], v[28:31], 0\n"
      "v_mfma_f32_16x16x32_bf16 v[48:51], a[48:51], v[28:31], 0\n"
      "v_mfma_f32_16x16x32_bf16 v[52:55], a[96:99], v[28:31], 0\n"
      "v_mfma_f32_16x16x32_bf16 v[56:59], a[100:103], v[28:31], 0\n"
      "v_mfma_f32_16x16x32_bf16 v[60:63], a[52:55], v[28:31], 0\n"
      "s_waitcnt lgkmcnt(0)\n"
      "v_mfma_f32_16x16x32_bf16 v[36:39], a[8:11], v[32:35], v[36:39]\n"
      "ds_read_b128 v[28:31], v1 offset:40960\n"
      "v_mfma_f32_16x16x32_bf16 v[44:47], a[12:15], v[32:35], v[44:47]\n"
      "v_mfma_f32_16x16x32_bf16 v[48:51], a[56:59], v[32:35], v[48:51]\n"
      "v_mfma_f32_16x16x32_bf16 v[52:55], a[104:107], v[32:35], v[52:55]\n"
      "v_mfma_f32_16x16x32_bf16 v[56:59], a[108:111], v[32:35], v[56:59]\n"
      "v_mfma_f32_16x16x32_bf16 v[60:63], a[60:63], v[32:35], v[60:63]\n"
      "s_waitcnt vmcnt(12)\n"
      "ds_read_b64_tr_b16 a[24:25], v21\n"
      "ds_read_b64_tr_b16 a[26:27], v21 offset:256\n"
      "ds_read_b64_tr_b16 a[28:29], v21 offset:32\n"
      "ds_read_b64_tr_b16 a[30:31], v21 offset:288\n"
      "ds_read_b64_tr_b16 a[72:73], v21 offset:2048\n"
      "ds_read_b64_tr_b16 a[74:75], v21 offset:2304\n"
      "ds_read_b64_tr_b16 a[76:77], v21 offset:2080\n"
      "ds_read_b64_tr_b16 a[78:79], v21 offset:2336\n"
      "ds_read_b64_tr_b16 a[120:121], v21 offset:4096\n"
      "ds_read_b64_tr_b16 a[122:123], v21 offset:4352\n"
      "ds_read_b64_tr_b16 a[124:125], v21 offset:4128\n"
      "ds_read_b64_tr_b16 a[126:127], v21 offset:4384\n"
      "v_mfma_f32_16x16x32_bf16 v[36:39], a[16:19], v[40:43], v[36:39]\n"
      "ds_read_b128 v[32:35], v0 offset:45056\n"
      "v_mfma_f32_16x16x32_bf16 v[44:47], a[20:23], v[40:43], v[44:47]\n"
      "v_mfma_f32_16x16x32_bf16 v[48:51], a[64:67], v[40:43], v[48:51]\n"
      "v_mfma_f32_16x16x32_bf16 v[52:55], a[112:115], v[40:43], v[52:55]\n"
      "v_mfma_f32_16x16x32_bf16 v[56:59], a[116:119], v[40:43], v[56:59]\n"
      "v_mfma_f32_16x16x32_bf16 v[60:63], a[68:71], v[40:43], v[60:63]\n"
      "s_waitcnt vmcnt(6)\n"
      "ds_read_b64_tr_b16 a[32:33], v21 offset:24576\n"
      "ds_read_b64_tr_b16 a[34:35], v21 offset:24832\n"
      "ds_read_b64_tr_b16 a[36:37], v21 offset:24608\n"
      "ds_read_b64_tr_b16 a[38:39], v21 offset:24864\n"
      "ds_read_b64_tr_b16 a[80:81], v21 offset:26624\n"
      "ds_read_b64_tr_b16 a[82:83], v21 offset:26880\n"
      "ds_read_b64_tr_b16 a[84:85], v21 offset:26656\n"
      "ds_read_b64_tr_b16 a[86:87], v21 offset:26912\n"
      "ds_read_b64_tr_b16 a[128:129], v21 offset:28672\n"
      "ds_read_b64_tr_b16 a[130:131], v21 offset:28928\n"
      "ds_read_b64_tr_b16 a[132:133], v21 offset:28704\n"
      "ds_read_b64_tr_b16 a[134:135], v21 offset:28960\n"
      "s_waitcnt lgkmcnt(12)\n"
      "v_mfma_f32_16x16x32_bf16 v[36:39], a[24:27], v[28:31], v[36:39]\n"
      "ds_read_b128 v[40:43], v1 offset:45056\n"
      "v_mfma_f32_16x16x32_bf16 v[44:47], a[28:31], v[28:31], v[44:47]\n"
      "v_mfma_f32_16x16x32_bf16 v[48:51], a[72:75], v[28:31], v[48:51]\n"
      "v_mfma_f32_16x16x32_bf16 v[52:55], a[120:123], v[28:31], v[52:55]\n"
      "v_mfma_f32_16x16x32_bf16 v[56:59], a[124:127], v[28:31], v[56:59]\n"
      "v_mfma_f32_16x16x32_bf16 v[60:63], a[76:79], v[28:31], v[60:63]\n"
      "s_waitcnt vmcnt(0)\n"
      "ds_read_b64_tr_b16 a[40:41], v21 offset:49664\n"
      "ds_read_b64_tr_b16 a[42:43], v21 offset:49920\n"
      "ds_read_b64_tr_b16 a[44:45], v21 offset:49696\n"
      "ds_read_b64_tr_b16 a[46:47], v21 offset:49952\n"
      "ds_read_b64_tr_b16 a[88:89], v21 offset:51712\n"
      "ds_read_b64_tr_b16 a[90:91], v21 offset:51968\n"
      "ds_read_b64_tr_b16 a[92:93], v21 offset:51744\n"
      "ds_read_b64_tr_b16 a[94:95], v21 offset:52000\n"
      "ds_read_b64_tr_b16 a[136:137], v21 offset:53760\n"
      "ds_read_b64_tr_b16 a[138:139], v21 offset:54016\n"
      "ds_read_b64_tr_b16 a[140:141], v21 offset:53792\n"
      "ds_read_b64_tr_b16 a[142:143], v21 offset:54048\n"
      "s_waitcnt lgkmcnt(12)\n"
      "v_mfma_f32_16x16x32_bf16 v[36:39], a[32:35], v[32:35], v[36:39]\n"
      "ds_read_b128 v[28:31], v0 offset:38912\n"
      "v_mfma_f32_16x16x32_bf16 v[44:47], a[36:39], v[32:35], v[44:47]\n"
      "v_mfma_f32_16x16x32_bf16 v[48:51], a[80:83], v[32:35], v[48:51]\n"
      "v_mfma_f32_16x16x32_bf16 v[52:55], a[128:131], v[32:35], v[52:55]\n"
      "v_mfma_f32_16x16x32_bf16 v[56:59], a[132:135], v[32:35], v[56:59]\n"
      "v_mfma_f32_16x16x32_bf16 v[60:63], a[84:87], v[32:35], v[60:63]\n"
      "s_waitcnt lgkmcnt(0)\n"
      "v_mfma_f32_16x16x32_bf16 v[36:39], a[40:43], v[40:43], v[36:39]\n"
      "ds_read_b128 v[32:35], v1 offset:38912\n"
      "v_mfma_f32_16x16x32_bf16 v[44:47], a[44:47], v[40:43], v[44:47]\n"
      "v_mfma_f32_16x16x32_bf16 v[48:51], a[88:91], v[40:43], v[48:51]\n"
      "v_mfma_f32_16x16x32_bf16 v[52:55], a[136:139], v[40:43], v[52:55]\n"
      "v_mfma_f32_16x16x32_bf16 v[56:59], a[140:143], v[40:43], v[56:59]\n"
      "v_mfma_f32_16x16x32_bf16 v[60:63], a[92:95], v[40:43], v[60:63]\n"
      "s_and_b32 s29, s24, 3\n"
      "s_lshl_b32 s29, s29, 2\n"
      "v_add_u32 v21, s29, v20\n"
      "v_mov_b32 v22, 1\n"
      "ds_write_b32 v21, v22 offset:16\n"
      "ds_write_b128 v2, v[24:27] offset:1024\n"
      "v_mfma_f32_16x16x32_bf16 v[24:27], a[0:3], v[28:31], 0\n"
      "ds_read_b128 v[40:43], v0 offset:43008\n"
      "v_mfma_f32_16x16x32_bf16 v[64:67], a[4:7], v[28:31], 0\n"
      "v_mfma_f32_16x16x32_bf16 v[68:71], a[48:51], v[28:31], 0\n"
      "v_mfma_f32_16x16x32_bf16 v[72:75], a[96:99], v[28:31], 0\n"
      "v_mfma_f32_16x16x32_bf16 v[76:79], a[100:103], v[28:31], 0\n"
      "v_fma_f32 v21, v36, v36, s51\n"
      "v_fma_f32 v22, v52, v52, s51\n"
      "v_fmac_f32 v21, v37, v37\n"
      "v_mfma_f32_16x16x32_bf16 v[80:83], a[52:55], v[28:31], 0\n"
      "v_fmac_f32 v22, v53, v53\n"
      "v_fmac_f32 v21, v38, v38\n"
      "s_waitcnt lgkmcnt(0)\n"
      "v_mfma_f32_16x16x32_bf16 v[24:27], a[8:11], v[32:35], v[24:27]\n"
      "ds_read_b128 v[28:31], v1 offset:43008\n"
      "v_fmac_f32 v22, v54, v54\n"
      "v_fmac_f32 v21, v39, v39\n"
      "v_mfma_f32_16x16x32_bf16 v[64:67], a[12:15], v[32:35], v[64:67]\n"
      "v_fmac_f32 v22, v55, v55\n"
      "v_fmac_f32 v21, v44, v44\n"
      "v_mfma_f32_16x16x32_bf16 v[68:71], a[56:59], v[32:35], v[68:71]\n"
      "v_fmac_f32 v22, v56, v56\n"
      "v_fmac_f32 v21, v45, v45\n"
      "v_mfma_f32_16x16x32_bf16 v[72:75], a[104:107], v[32:35], v[72:75]\n"
      "v_fmac_f32 v22, v57, v57\n"
      "v_fmac_f32 v21, v46, v46\n"
      "v_fmac_f32 v22, v58, v58\n"
      "v_mfma_f32_16x16x32_bf16 v[76:79], a[108:111], v[32:35], v[76:79]\n"
      "v_fmac_f32 v21, v47, v47\n"
      "v_fmac_f32_e64 v22, v59, v59\n"
      "v_mfma_f32_16x16x32_bf16 v[80:83], a[60:63], v[32:35], v[80:83]\n"
      "v_fmac_f32 v21, v48, v48\n"
      "v_fmac_f32 v22, v60, v60\n"
      "v_mfma_f32_16x16x32_bf16 v[24:27], a[16:19], v[40:43], v[24:27]\n"
      "ds_read_b128 v[32:35], v0 offset:47104\n"
      "v_fmac_f32 v21, v49, v49\n"
      "v_fmac_f32 v22, v61, v61\n"
      "v_mfma_f32_16x16x32_bf16 v[64:67], a[20:23], v[40:43], v[64:67]\n"
      "v_fmac_f32 v21, v50, v50\n"
      "v_fmac_f32 v22, v62, v62\n"
      "v_mfma_f32_16x16x32_bf16 v[68:71], a[64:67], v[40:43], v[68:71]\n"
      "v_fmac_f32 v21, v51, v51\n"
      "v_fmac_f32 v22, v63, v63\n"
      "s_nop 1\n"
      "v_permlane16_swap_b32 v21, v22\n"
      "v_mfma_f32_16x16x32_bf16 v[72:75], a[112:115], v[40:43], v[72:75]\n"
      "v_add_f32 v21, v21, v22\n"
      "v_mov_b32 v22, v21\n"
      "v_mfma_f32_16x16x32_bf16 v[76:79], a[116:119], v[40:43], v[76:79]\n"
      "s_nop 0\n"
      "v_permlane32_swap_b32 v21, v22\n"
      "v_add_f32 v21, v21, v22\n"
      "v_mfma_f32_16x16x32_bf16 v[80:83], a[68:71], v[40:43], v[80:83]\n"
      "v_rsq_f32 v21, v21\n"
      "s_nop 0\n"
      "v_mov_b32 v22, v21\n"
      "s_waitcnt lgkmcnt(0)\n"
      "v_mfma_f32_16x16x32_bf16 v[24:27], a[24:27], v[28:31], v[24:27]\n"
      "ds_read_b128 v[40:43], v1 offset:47104\n"
      "v_permlane16_swap_b32 v21, v22\n"
      "v_mul_f32 v52, v22, v52\n"
      "v_mfma_f32_16x16x32_bf16 v[64:67], a[28:31], v[28:31], v[64:67]\n"
      "v_mul_f32 v53, v22, v53\n"
      "v_mul_f32 v54, v22, v54\n"
      "v_mfma_f32_16x16x32_bf16 v[68:71], a[72:75], v[28:31], v[68:71]\n"
      "v_mul_f32 v55, v22, v55\n"
      "v_mul_f32 v56, v22, v56\n"
      "v_mul_f32 v57, v22, v57\n"
      "v_mfma_f32_16x16x32_bf16 v[72:75], a[120:123], v[28:31], v[72:75]\n"
      "v_mul_f32 v58, v22, v58\n"
      "v_mul_f32 v59, v22, v59\n"
      "v_mfma_f32_16x16x32_bf16 v[76:79], a[124:127], v[28:31], v[76:79]\n"
      "v_mul_f32 v60, v22, v60\n"
      "v_mul_f32 v61, v22, v61\n"
      "v_mfma_f32_16x16x32_bf16 v[80:83], a[76:79], v[28:31], v[80:83]\n"
      "v_mul_f32 v62, v22, v62\n"
      "v_mul_f32 v63, v22, v63\n"
      "v_mfma_f32_16x16x32_bf16 v[24:27], a[32:35], v[32:35], v[24:27]\n"
      "v_cvt_pk_bf16_f32 v52, v52, v53\n"
      "v_cvt_pk_bf16_f32 v53, v54, v55\n"
      "v_mfma_f32_16x16x32_bf16 v[64:67], a[36:39], v[32:35], v[64:67]\n"
      "v_cvt_pk_bf16_f32 v54, v56, v57\n"
      "v_cvt_pk_bf16_f32 v55, v58, v59\n"
      "v_cvt_pk_bf16_f32 v60, v60, v61\n"
      "v_mfma_f32_16x16x32_bf16 v[68:71], a[80:83], v[32:35], v[68:71]\n"
      "v_cvt_pk_bf16_f32 v61, v62, v63\n"
      "v_mfma_f32_16x16x32_bf16 v[72:75], a[128:131], v[32:35], v[72:75]\n"
      "v_mul_f32 v21, s40, v21\n"
      "v_mul_f32 v36, v21, v36\n"
      "v_mfma_f32_16x16x32_bf16 v[76:79], a[132:135], v[32:35], v[76:79]\n"
      "v_mul_f32 v37, v21, v37\n"
      "v_mul_f32 v38, v21, v38\n"
      "v_mfma_f32_16x16x32_bf16 v[80:83], a[84:87], v[32:35], v[80:83]\n"
      "v_mul_f32 v39, v21, v39\n"
      "v_mul_f32 v44, v21, v44\n"
      "s_waitcnt lgkmcnt(0)\n"
      "v_mfma_f32_16x16x32_bf16 v[24:27], a[40:43], v[40:43], v[24:27]\n"
      "v_mul_f32 v45, v21, v45\n"
      "v_mul_f32 v46, v21, v46\n"
      "v_mul_f32 v47, v21, v47\n"
      "v_mfma_f32_16x16x32_bf16 v[64:67], a[44:47], v[40:43], v[64:67]\n"
      "v_mul_f32 v48, v21, v48\n"
      "v_mul_f32 v49, v21, v49\n"
      "v_mfma_f32_16x16x32_bf16 v[68:71], a[88:91], v[40:43], v[68:71]\n"
      "v_mul_f32 v50, v21, v50\n"
      "v_mul_f32 v51, v21, v51\n"
      "v_mfma_f32_16x16x32_bf16 v[72:75], a[136:139], v[40:43], v[72:75]\n"
      "v_cvt_pk_bf16_f32 v36, v36, v37\n"
      "v_cvt_pk_bf16_f32 v37, v38, v39\n"
      "v_mfma_f32_16x16x32_bf16 v[76:79], a[140:143], v[40:43], v[76:79]\n"
      "v_cvt_pk_bf16_f32 v38, v44, v45\n"
      "v_cvt_pk_bf16_f32 v39, v46, v47\n"
      "v_mfma_f32_16x16x32_bf16 v[80:83], a[92:95], v[40:43], v[80:83]\n"
      "v_cvt_pk_bf16_f32 v48, v48, v49\n"
      "v_cvt_pk_bf16_f32 v49, v50, v51\n"
      "v_fma_f32 v21, v24, v24, s51\n"
      "v_fma_f32 v22, v72, v72, s51\n"
      "v_fmac_f32 v21, v25, v25\n"
      "v_fmac_f32 v22, v73, v73\n"
      "v_fmac_f32 v21, v26, v26\n"
      "v_fmac_f32 v22, v74, v74\n"
      "v_fmac_f32 v21, v27, v27\n"
      "v_fmac_f32 v22, v75, v75\n"
      "v_fmac_f32 v21, v64, v64\n"
      "v_fmac_f32 v22, v76, v76\n"
      "v_fmac_f32 v21, v65, v65\n"
      "v_fmac_f32 v22, v77, v77\n"
      "v_fmac_f32 v21, v66, v66\n"
      "v_fmac_f32 v22, v78, v78\n"
      "v_fmac_f32 v21, v67, v67\n"
      "v_fmac_f32 v22, v79, v79\n"
      "v_fmac_f32 v21, v68, v68\n"
      "v_fmac_f32 v22, v80, v80\n"
      "v_fmac_f32 v21, v69, v69\n"
      "v_fmac_f32 v22, v81, v81\n"
      "v_fmac_f32 v21, v70, v70\n"
      "v_fmac_f32 v22, v82, v82\n"
      "v_fmac_f32 v21, v71, v71\n"
      "v_fmac_f32 v22, v83, v83\n"
      "s_nop 1\n"
      "v_permlane16_swap_b32 v21, v22\n"
      "v_add_f32 v21, v21, v22\n"
      "v_mov_b32 v22, v21\n"
      "s_nop 1\n"
      "v_permlane32_swap_b32 v21, v22\n"
      "v_add_f32 v21, v21, v22\n"
      "v_rsq_f32 v21, v21\n"
      "s_nop 0\n"
      "v_mov_b32 v22, v21\n"
      "s_nop 1\n"
      "v_permlane16_swap_b32 v21, v22\n"
      "v_mul_f32 v72, v22, v72\n"
      "v_mul_f32 v73, v22, v73\n"
      "v_mul_f32 v74, v22, v74\n"
      "v_mul_f32 v75, v22, v75\n"
      "v_mul_f32 v76, v22, v76\n"
      "v_mul_f32 v77, v22, v77\n"
      "v_mul_f32 v78, v22, v78\n"
      "v_mul_f32 v79, v22, v79\n"
      "v_mul_f32 v80, v22, v80\n"
      "v_mul_f32 v81, v22, v81\n"
      "v_mul_f32 v82, v22, v82\n"
      "v_mul_f32 v83, v22, v83\n"
      "v_cvt_pk_bf16_f32 v72, v72, v73\n"
      "v_cvt_pk_bf16_f32 v73, v74, v75\n"
      "v_cvt_pk_bf16_f32 v74, v76, v77\n"
      "v_cvt_pk_bf16_f32 v75, v78, v79\n"
      "v_cvt_pk_bf16_f32 v80, v80, v81\n"
      "v_cvt_pk_bf16_f32 v81, v82, v83\n"
      "v_mul_f32 v21, s40, v21\n"
      "v_mul_f32 v24, v21, v24\n"
      "v_mul_f32 v25, v21, v25\n"
      "v_mul_f32 v26, v21, v26\n"
      "v_mul_f32 v27, v21, v27\n"
      "v_mul_f32 v64, v21, v64\n"
      "v_mul_f32 v65, v21, v65\n"
      "v_mul_f32 v66, v21, v66\n"
      "v_mul_f32 v67, v21, v67\n"
      "v_mul_f32 v68, v21, v68\n"
      "v_mul_f32 v69, v21, v69\n"
      "v_mul_f32 v70, v21, v70\n"
      "v_mul_f32_e64 v71, v21, v71\n"
      "v_cvt_pk_bf16_f32 v24, v24, v25\n"
      "v_cvt_pk_bf16_f32 v25, v26, v27\n"
      "v_cvt_pk_bf16_f32 v26, v64, v65\n"
      "v_cvt_pk_bf16_f32 v27, v66, v67\n"
      "v_cvt_pk_bf16_f32 v68, v68, v69\n"
      "v_cvt_pk_bf16_f32 v69, v70, v71\n"
      "v_bfe_u32 v21, v3, 3, 4\n"
      "v_bfe_u32 v22, v3, 7, 2\n"
      "v_mul_u32_u24_e64 v22, 48, v22\n"
      "v_mul_u32_u24 v23, 1152, v21\n"
      "v_mul_u32_u24 v28, 0xc0, v21\n"
      "v_add3_u32 v28, v28, v22, s54\n"
      "v_add3_u32 v23, v23, v22, s53\n"
      "s_mov_b64 exec, s[56:57]\n"
      "ds_write_b128 v23, v[36:39]\n"
      "ds_write_b64 v23, v[48:49] offset:16\n"
      "ds_write_b64 v23, v[60:61] offset:24\n"
      "ds_write_b128 v23, v[52:55] offset:32\n"
      "s_mov_b64 exec, -1\n"
      "ds_write_b128 v28, v[36:39]\n"
      "ds_write_b64 v28, v[48:49] offset:16\n"
      "ds_write_b64 v28, v[60:61] offset:24\n"
      "ds_write_b128 v28, v[52:55] offset:32\n"
      "ds_write_b128 v28, v[24:27] offset:3072\n"
      "ds_write_b64 v28, v[68:69] offset:3088\n"
      "ds_write_b64 v28, v[80:81] offset:3096\n"
      "ds_write_b128 v28, v[72:75] offset:3104\n"
      "s_waitcnt lgkmcnt(0)\n"
      "L_poll_1_%=:\n"
      "ds_read_b128 v[24:27], v20\n"
      "s_waitcnt lgkmcnt(0)\n"
      "v_and_b32 v24, v24, v25\n"
      "v_and_b32 v26, v26, v27\n"
      "v_and_b32_e64 v24, v24, v26\n"
      "s_nop 0\n"
      "v_readfirstlane_b32 s29, v24\n"
      "s_nop 3\n"
      "s_cmp_eq_u32 s29, 0\n"
      "s_cbranch_scc1 L_poll_1_%=\n"
      "s_nop 7\n"
      "s_nop 7\n"
      "L_Aloop_%=:\n"
      "s_setprio 0\n"
      "ds_read_b128 v[24:27], v0\n"
      "ds_read_b128 v[28:31], v1\n"
      "s_waitcnt lgkmcnt(0)\n"
      "v_mfma_f32_16x16x32_bf16 v[32:35], a[0:3], v[24:27], 0\n"
      "v_mfma_f32_16x16x32_bf16 v[32:35], a[8:11], v[28:31], v[32:35]\n"
      "ds_read_b128 v[36:39], v0 offset:4096\n"
      "v_mfma_f32_16x16x32_bf16 v[40:43], a[4:7], v[24:27], 0\n"
      "v_mfma_f32_16x16x32_bf16 v[40:43], a[12:15], v[28:31], v[40:43]\n"
      "ds_read_b128 v[44:47], v1 offset:4096\n"
      "v_mfma_f32_16x16x32_bf16 v[48:51], a[48:51], v[24:27], 0\n"
      "v_mfma_f32_16x16x32_bf16 v[48:51], a[56:59], v[28:31], v[48:51]\n"
      "v_mfma_f32_16x16x32_bf16 v[52:55], a[96:99], v[24:27], 0\n"
      "v_mfma_f32_16x16x32_bf16 v[52:55], a[104:107], v[28:31], v[52:55]\n"
      "v_mfma_f32_16x16x32_bf16 v[56:59], a[100:103], v[24:27], 0\n"
      "v_mfma_f32_16x16x32_bf16 v[56:59], a[108:111], v[28:31], v[56:59]\n"
      "v_mfma_f32_16x16x32_bf16 v[20:23], a[52:55], v[24:27], 0\n"
      "v_mfma_f32_16x16x32_bf16 v[20:23], a[60:63], v[28:31], v[20:23]\n"
      "s_waitcnt lgkmcnt(0)\n"
      "v_mfma_f32_16x16x32_bf16 v[32:35], a[16:19], v[36:39], v[32:35]\n"
      "v_mfma_f32_16x16x32_bf16 v[32:35], a[24:27], v[44:47], v[32:35]\n"
      "ds_read_b128 v[24:27], v0 offset:8192\n"
      "v_mfma_f32_16x16x32_bf16 v[40:43], a[20:23], v[36:39], v[40:43]\n"
      "v_mfma_f32_16x16x32_bf16 v[40:43], a[28:31], v[44:47], v[40:43]\n"
      "ds_read_b128 v[28:31], v1 offset:8192\n"
      "v_mfma_f32_16x16x32_bf16 v[48:51], a[64:67], v[36:39], v[48:51]\n"
      "v_mfma_f32_16x16x32_bf16 v[48:51], a[72:75], v[44:47], v[48:51]\n"
      "v_mfma_f32_16x16x32_bf16 v[52:55], a[112:115], v[36:39], v[52:55]\n"
      "v_mfma_f32_16x16x32_bf16 v[52:55], a[120:123], v[44:47], v[52:55]\n"
      "v_mfma_f32_16x16x32_bf16 v[56:59], a[116:119], v[36:39], v[56:59]\n"
      "v_mfma_f32_16x16x32_bf16 v[56:59], a[124:127], v[44:47], v[56:59]\n"
      "v_mfma_f32_16x16x32_bf16 v[20:23], a[68:71], v[36:39], v[20:23]\n"
      "v_mfma_f32_16x16x32_bf16 v[20:23], a[76:79], v[44:47], v[20:23]\n"
      "s_waitcnt lgkmcnt(0)\n"
      "v_mfma_f32_16x16x32_bf16 v[32:35], a[32:35], v[24:27], v[32:35]\n"
      "v_mfma_f32_16x16x32_bf16 v[32:35], a[40:43], v[28:31], v[32:35]\n"
      "ds_read_b128 v[44:47], v0 offset:2048\n"
      "v_mfma_f32_16x16x32_bf16 v[40:43], a[36:39], v[24:27], v[40:43]\n"
      "v_mfma_f32_16x16x32_bf16 v[40:43], a[44:47], v[28:31], v[40:43]\n"
      "ds_read_b128 v[36:39], v1 offset:2048\n"
      "v_mfma_f32_16x16x32_bf16 v[48:51], a[80:83], v[24:27], v[48:51]\n"
      "v_mfma_f32_16x16x32_bf16 v[48:51], a[88:91], v[28:31], v[48:51]\n"
      "v_mfma_f32_16x16x32_bf16 v[52:55], a[128:131], v[24:27], v[52:55]\n"
      "v_mfma_f32_16x16x32_bf16 v[52:55], a[136:139], v[28:31], v[52:55]\n"
      "v_mfma_f32_16x16x32_bf16 v[56:59], a[132:135], v[24:27], v[56:59]\n"
      "v_mfma_f32_16x16x32_bf16 v[56:59], a[140:143], v[28:31], v[56:59]\n"
      "v_mfma_f32_16x16x32_bf16 v[20:23], a[84:87], v[24:27], v[20:23]\n"
      "v_mfma_f32_16x16x32_bf16 v[20:23], a[92:95], v[28:31], v[20:23]\n"
      "s_waitcnt lgkmcnt(0)\n"
      "v_mfma_f32_16x16x32_bf16 v[60:63], a[0:3], v[44:47], 0\n"
      "v_mfma_f32_16x16x32_bf16 v[60:63], a[8:11], v[36:39], v[60:63]\n"
      "ds_read_b128 v[28:31], v0 offset:6144\n"
      "v_mfma_f32_16x16x32_bf16 v[64:67], a[4:7], v[44:47], 0\n"
      "v_mfma_f32_16x16x32_bf16 v[64:67], a[12:15], v[36:39], v[64:67]\n"
      "ds_read_b128 v[68:71], v1 offset:6144\n"
      "v_mfma_f32_16x16x32_bf16 v[72:75], a[48:51], v[44:47], 0\n"
      "v_fma_f32 v18, v32, v32, s51\n"
      "v_fma_f32 v19, v52, v52, s51\n"
      "v_fmac_f32 v18, v33, v33\n"
      "v_mfma_f32_16x16x32_bf16 v[72:75], a[56:59], v[36:39], v[72:75]\n"
      "v_fmac_f32 v19, v53, v53\n"
      "v_fmac_f32_e64 v18, v34, v34\n"
      "v_mfma_f32_16x16x32_bf16 v[76:79], a[96:99], v[44:47], 0\n"
      "v_fmac_f32 v19, v54, v54\n"
      "v_fmac_f32 v18, v35, v35\n"
      "v_mfma_f32_16x16x32_bf16 v[76:79], a[104:107], v[36:39], v[76:79]\n"
      "v_fmac_f32 v19, v55, v55\n"
      "v_fmac_f32 v18, v40, v40\n"
      "v_mfma_f32_16x16x32_bf16 v[80:83], a[100:103], v[44:47], 0\n"
      "v_fmac_f32 v19, v56, v56\n"
      "v_fmac_f32 v18, v41, v41\n"
      "v_mfma_f32_16x16x32_bf16 v[80:83], a[108:111], v[36:39], v[80:83]\n"
      "v_fmac_f32 v19, v57, v57\n"
      "v_fmac_f32 v18, v42, v42\n"
      "v_fmac_f32 v19, v58, v58\n"
      "v_mfma_f32_16x16x32_bf16 v[24:27], a[52:55], v[44:47], 0\n"
      "v_fmac_f32 v18, v43, v43\n"
      "v_fmac_f32 v19, v59, v59\n"
      "v_mfma_f32_16x16x32_bf16 v[24:27], a[60:63], v[36:39], v[24:27]\n"
      "v_fmac_f32 v18, v48, v48\n"
      "v_fmac_f32 v19, v20, v20\n"
      "s_waitcnt lgkmcnt(0)\n"
      "v_mfma_f32_16x16x32_bf16 v[60:63], a[16:19], v[28:31], v[60:63]\n"
      "v_fmac_f32 v18, v49, v49\n"
      "v_fmac_f32 v19, v21, v21\n"
      "v_mfma_f32_16x16x32_bf16 v[60:63], a[24:27], v[68:71], v[60:63]\n"
      "ds_read_b128 v[36:39], v0 offset:10240\n"
      "v_fmac_f32 v18, v50, v50\n"
      "v_fmac_f32 v19, v22, v22\n"
      "v_mfma_f32_16x16x32_bf16 v[64:67], a[20:23], v[28:31], v[64:67]\n"
      "v_fmac_f32 v18, v51, v51\n"
      "v_fmac_f32 v19, v23, v23\n"
      "s_nop 1\n"
      "v_permlane16_swap_b32 v18, v19\n"
      "v_mfma_f32_16x16x32_bf16 v[64:67], a[28:31], v[68:71], v[64:67]\n"
      "ds_read_b128 v[44:47], v1 offset:10240\n"
      "v_add_f32 v18, v18, v19\n"
      "v_mov_b32 v19, v18\n"
      "v_mfma_f32_16x16x32_bf16 v[72:75], a[64:67], v[28:31], v[72:75]\n"
      "s_nop 0\n"
      "v_permlane32_swap_b32 v18, v19\n"
      "v_add_f32 v18, v18, v19\n"
      "v_mfma_f32_16x16x32_bf16 v[72:75], a[72:75], v[68:71], v[72:75]\n"
      "v_rsq_f32 v18, v18\n"
      "s_nop 0\n"
      "v_mov_b32 v19, v18\n"
      "v_mfma_f32_16x16x32_bf16 v[76:79], a[112:115], v[28:31], v[76:79]\n"
      "s_nop 0\n"
      "v_permlane16_swap_b32 v18, v19\n"
      "v_mul_f32_e64 v52, v19, v52\n"
      "v_mfma_f32_16x16x32_bf16 v[76:79], a[120:123], v[68:71], v[76:79]\n"
      "v_mul_f32 v53, v19, v53\n"
      "v_mul_f32 v54, v19, v54\n"
      "v_mfma_f32_16x16x32_bf16 v[80:83], a[116:119], v[28:31], v[80:83]\n"
      "v_mul_f32 v55, v19, v55\n"
      "v_mul_f32 v56, v19, v56\n"
      "v_mul_f32 v57, v19, v57\n"
      "v_mfma_f32_16x16x32_bf16 v[80:83], a[124:127], v[68:71], v[80:83]\n"
      "v_mul_f32 v58, v19, v58\n"
      "v_mul_f32 v59, v19, v59\n"
      "v_mfma_f32_16x16x32_bf16 v[24:27], a[68:71], v[28:31], v[24:27]\n"
      "v_mul_f32 v20, v19, v20\n"
      "v_mul_f32 v21, v19, v21\n"
      "v_mfma_f32_16x16x32_bf16 v[24:27], a[76:79], v[68:71], v[24:27]\n"
      "v_mul_f32 v22, v19, v22\n"
      "v_mul_f32 v23, v19, v23\n"
      "s_waitcnt lgkmcnt(0)\n"
      "v_mfma_f32_16x16x32_bf16 v[60:63], a[32:35], v[36:39], v[60:63]\n"
      "v_cvt_pk_bf16_f32 v52, v52, v53\n"
      "v_cvt_pk_bf16_f32 v53, v54, v55\n"
      "v_mfma_f32_16x16x32_bf16 v[60:63], a[40:43], v[44:47], v[60:63]\n"
      "ds_read_b128 v[68:71], v0 offset:12288\n"
      "v_cvt_pk_bf16_f32 v54, v56, v57\n"
      "v_cvt_pk_bf16_f32 v55, v58, v59\n"
      "v_cvt_pk_bf16_f32 v20, v20, v21\n"
      "v_mfma_f32_16x16x32_bf16 v[64:67], a[36:39], v[36:39], v[64:67]\n"
      "v_cvt_pk_bf16_f32 v21, v22, v23\n"
      "v_mfma_f32_16x16x32_bf16 v[64:67], a[44:47], v[44:47], v[64:67]\n"
      "ds_read_b128 v[56:59], v1 offset:12288\n"
      "v_mul_f32 v18, s40, v18\n"
      "v_mul_f32 v32, v18, v32\n"
      "v_mfma_f32_16x16x32_bf16 v[72:75], a[80:83], v[36:39], v[72:75]\n"
      "v_mul_f32 v33, v18, v33\n"
      "v_mul_f32 v34, v18, v34\n"
      "v_mfma_f32_16x16x32_bf16 v[72:75], a[88:91], v[44:47], v[72:75]\n"
      "v_mul_f32 v35, v18, v35\n"
      "v_mul_f32 v40, v18, v40\n"
      "v_mfma_f32_16x16x32_bf16 v[76:79], a[128:131], v[36:39], v[76:79]\n"
      "v_mul_f32 v41, v18, v41\n"
      "v_mul_f32 v42, v18, v42\n"
      "v_mul_f32 v43, v18, v43\n"
      "v_mfma_f32_16x16x32_bf16 v[76:79], a[136:139], v[44:47], v[76:79]\n"
      "v_mul_f32 v48, v18, v48\n"
      "v_mul_f32 v49, v18, v49\n"
      "v_mfma_f32_16x16x32_bf16 v[80:83], a[132:135], v[36:39], v[80:83]\n"
      "v_mul_f32 v50, v18, v50\n"
      "v_mul_f32 v51, v18, v51\n"
      "v_mfma_f32_16x16x32_bf16 v[80:83], a[140:143], v[44:47], v[80:83]\n"
      "v_cvt_pk_bf16_f32 v32, v32, v33\n"
      "v_cvt_pk_bf16_f32 v33, v34, v35\n"
      "v_mfma_f32_16x16x32_bf16 v[24:27], a[84:87], v[36:39], v[24:27]\n"
      "v_cvt_pk_bf16_f32 v34, v40, v41\n"
      "v_cvt_pk_bf16_f32 v35, v42, v43\n"
      "v_mfma_f32_16x16x32_bf16 v[24:27], a[92:95], v[44:47], v[24:27]\n"
      "v_cvt_pk_bf16_f32 v4, v48, v49\n"
      "v_cvt_pk_bf16_f32 v5, v50, v51\n"
      "s_waitcnt lgkmcnt(0)\n"
      "v_mfma_f32_16x16x32_bf16 v[48:51], a[0:3], v[68:71], 0\n"
      "v_mfma_f32_16x16x32_bf16 v[48:51], a[8:11], v[56:59], v[48:51]\n"
      "ds_read_b128 v[44:47], v0 offset:16384\n"
      "v_mfma_f32_16x16x32_bf16 v[40:43], a[4:7], v[68:71], 0\n"
      "v_mfma_f32_16x16x32_bf16 v[40:43], a[12:15], v[56:59], v[40:43]\n"
      "ds_read_b128 v[36:39], v1 offset:16384\n"
      "v_mfma_f32_16x16x32_bf16 v[84:87], a[48:51], v[68:71], 0\n"
      "v_fma_f32 v22, v60, v60, s51\n"
      "v_fma_f32 v23, v76, v76, s51\n"
      "v_fmac_f32 v22, v61, v61\n"
      "v_mfma_f32_16x16x32_bf16 v[84:87], a[56:59], v[56:59], v[84:87]\n"
      "v_fmac_f32 v23, v77, v77\n"
      "v_fmac_f32_e64 v22, v62, v62\n"
      "v_mfma_f32_16x16x32_bf16 v[88:91], a[96:99], v[68:71], 0\n"
      "v_fmac_f32 v23, v78, v78\n"
      "v_fmac_f32 v22, v63, v63\n"
      "v_mfma_f32_16x16x32_bf16 v[88:91], a[104:107], v[56:59], v[88:91]\n"
      "v_fmac_f32 v23, v79, v79\n"
      "v_fmac_f32 v22, v64, v64\n"
      "v_mfma_f32_16x16x32_bf16 v[92:95], a[100:103], v[68:71], 0\n"
      "v_fmac_f32 v23, v80, v80\n"
      "v_fmac_f32 v22, v65, v65\n"
      "v_mfma_f32_16x16x32_bf16 v[92:95], a[108:111], v[56:59], v[92:95]\n"
      "v_fmac_f32 v23, v81, v81\n"
      "v_fmac_f32 v22, v66, v66\n"
      "v_fmac_f32 v23, v82, v82\n"
      "v_mfma_f32_16x16x32_bf16 v[28:31], a[52:55], v[68:71], 0\n"
      "v_fmac_f32 v22, v67, v67\n"
      "v_fmac_f32 v23, v83, v83\n"
      "v_mfma_f32_16x16x32_bf16 v[28:31], a[60:63], v[56:59], v[28:31]\n"
      "v_fmac_f32 v22, v72, v72\n"
      "v_fmac_f32 v23, v24, v24\n"
      "s_waitcnt lgkmcnt(0)\n"
      "v_mfma_f32_16x16x32_bf16 v[48:51], a[16:19], v[44:47], v[48:51]\n"
      "v_fmac_f32 v22, v73, v73\n"
      "v_fmac_f32 v23, v25, v25\n"
      "v_mfma_f32_16x16x32_bf16 v[48:51], a[24:27], v[36:39], v[48:51]\n"
      "ds_read_b128 v[56:59], v0 offset:20480\n"
      "v_fmac_f32 v22, v74, v74\n"
      "v_fmac_f32 v23, v26, v26\n"
      "v_mfma_f32_16x16x32_bf16 v[40:43], a[20:23], v[44:47], v[40:43]\n"
      "v_fmac_f32 v22, v75, v75\n"
      "v_fmac_f32 v23, v27, v27\n"
      "s_nop 1\n"
      "v_permlane16_swap_b32 v22, v23\n"
      "v_mfma_f32_16x16x32_bf16 v[40:43], a[28:31], v[36:39], v[40:43]\n"
      "ds_read_b128 v[68:71], v1 offset:20480\n"
      "v_add_f32 v22, v22, v23\n"
      "v_mov_b32 v23, v22\n"
      "v_mfma_f32_16x16x32_bf16 v[84:87], a[64:67], v[44:47], v[84:87]\n"
      "s_nop 0\n"
      "v_permlane32_swap_b32 v22, v23\n"
      "v_add_f32 v22, v22, v23\n"
      "v_mfma_f32_16x16x32_bf16 v[84:87], a[72:75], v[36:39], v[84:87]\n"
      "v_rsq_f32 v22, v22\n"
      "s_nop 0\n"
      "v_mov_b32 v23, v22\n"
      "v_mfma_f32_16x16x32_bf16 v[88:91], a[112:115], v[44:47], v[88:91]\n"
      "s_nop 0\n"
      "v_permlane16_swap_b32 v22, v23\n"
      "v_mul_f32_e64 v76, v23, v76\n"
      "v_mfma_f32_16x16x32_bf16 v[88:91], a[120:123], v[36:39], v[88:91]\n"
      "v_mul_f32 v77, v23, v77\n"
      "v_mul_f32 v78, v23, v78\n"
      "v_mfma_f32_16x16x32_bf16 v[92:95], a[116:119], v[44:47], v[92:95]\n"
      "v_mul_f32 v79, v23, v79\n"
      "v_mul_f32 v80, v23, v80\n"
      "v_mul_f32 v81, v23, v81\n"
      "v_mfma_f32_16x16x32_bf16 v[92:95], a[124:127], v[36:39], v[92:95]\n"
      "v_mul_f32 v82, v23, v82\n"
      "v_mul_f32 v83, v23, v83\n"
      "v_mfma_f32_16x16x32_bf16 v[28:31], a[68:71], v[44:47], v[28:31]\n"
      "v_mul_f32 v24, v23, v24\n"
      "v_mul_f32 v25, v23, v25\n"
      "v_mfma_f32_16x16x32_bf16 v[28:31], a[76:79], v[36:39], v[28:31]\n"
      "v_mul_f32 v26, v23, v26\n"
      "v_mul_f32 v27, v23, v27\n"
      "s_waitcnt lgkmcnt(0)\n"
      "v_mfma_f32_16x16x32_bf16 v[48:51], a[32:35], v[56:59], v[48:51]\n"
      "v_cvt_pk_bf16_f32 v76, v76, v77\n"
      "v_cvt_pk_bf16_f32 v77, v78, v79\n"
      "v_mfma_f32_16x16x32_bf16 v[48:51], a[40:43], v[68:71], v[48:51]\n"
      "v_cvt_pk_bf16_f32 v78, v80, v81\n"
      "v_cvt_pk_bf16_f32 v79, v82, v83\n"
      "v_cvt_pk_bf16_f32 v24, v24, v25\n"
      "v_mfma_f32_16x16x32_bf16 v[40:43], a[36:39], v[56:59], v[40:43]\n"
      "v_cvt_pk_bf16_f32 v25, v26, v27\n"
      "v_mfma_f32_16x16x32_bf16 v[40:43], a[44:47], v[68:71], v[40:43]\n"
      "v_mul_f32 v22, s40, v22\n"
      "v_mul_f32 v60, v22, v60\n"
      "v_mfma_f32_16x16x32_bf16 v[84:87], a[80:83], v[56:59], v[84:87]\n"
      "v_mul_f32 v61, v22, v61\n"
      "v_mul_f32 v62, v22, v62\n"
      "v_mfma_f32_16x16x32_bf16 v[84:87], a[88:91], v[68:71], v[84:87]\n"
      "v_mul_f32 v63, v22, v63\n"
      "v_mul_f32 v64, v22, v64\n"
      "v_mfma_f32_16x16x32_bf16 v[88:91], a[128:131], v[56:59], v[88:91]\n"
      "v_mul_f32 v65, v22, v65\n"
      "v_mul_f32 v66, v22, v66\n"
      "v_mul_f32 v67, v22, v67\n"
      "v_mfma_f32_16x16x32_bf16 v[88:91], a[136:139], v[68:71], v[88:91]\n"
      "v_mul_f32 v72, v22, v72\n"
      "v_mul_f32 v73, v22, v73\n"
      "v_mfma_f32_16x16x32_bf16 v[92:95], a[132:135], v[56:59], v[92:95]\n"
      "v_mul_f32 v74, v22, v74\n"
      "v_mul_f32_e64 v75, v22, v75\n"
      "v_mfma_f32_16x16x32_bf16 v[92:95], a[140:143], v[68:71], v[92:95]\n"
      "v_cvt_pk_bf16_f32 v60, v60, v61\n"
      "v_cvt_pk_bf16_f32 v61, v62, v63\n"
      "v_mfma_f32_16x16x32_bf16 v[28:31], a[84:87], v[56:59], v[28:31]\n"
      "v_cvt_pk_bf16_f32 v62, v64, v65\n"
      "v_cvt_pk_bf16_f32 v63, v66, v67\n"
      "v_mfma_f32_16x16x32_bf16 v[28:31], a[92:95], v[68:71], v[28:31]\n"
      "v_cvt_pk_bf16_f32 v8, v72, v73\n"
      "v_cvt_pk_bf16_f32 v9, v74, v75\n"
      "s_waitcnt lgkmcnt(0)\n"
      "s_barrier\n"
      "ds_read_b32 v74, v16 offset:256\n"
      "ds_read_b64 v[22:23], v3\n"
      "ds_read_b64 v[26:27], v3 offset:512\n"
      "ds_read_b64 v[70:71], v3 offset:1536\n"
      "ds_read2st64_b32 v[72:73], v17 offset0:0 offset1:11\n"
      "s_setprio 0\n"
      "v_fma_f32 v75, v48, v48, s51\n"
      "v_fma_f32 v66, v88, v88, s51\n"
      "v_fmac_f32 v75, v49, v49\n"
      "v_fmac_f32 v66, v89, v89\n"
      "v_fmac_f32 v75, v50, v50\n"
      "v_fmac_f32 v66, v90, v90\n"
      "v_fmac_f32 v75, v51, v51\n"
      "v_fmac_f32 v66, v91, v91\n"
      "v_fmac_f32 v75, v40, v40\n"
      "v_fmac_f32 v66, v92, v92\n"
      "v_fmac_f32 v75, v41, v41\n"
      "v_fmac_f32 v66, v93, v93\n"
      "s_setprio 2\n"
      "v_fmac_f32 v75, v42, v42\n"
      "v_fmac_f32 v66, v94, v94\n"
      "v_fmac_f32 v75, v43, v43\n"
      "v_fmac_f32 v66, v95, v95\n"
      "v_fmac_f32 v75, v84, v84\n"
      "v_fmac_f32 v66, v28, v28\n"
      "v_fmac_f32 v75, v85, v85\n"
      "v_fmac_f32 v66, v29, v29\n"
      "v_fmac_f32 v75, v86, v86\n"
      "v_fmac_f32 v66, v30, v30\n"
      "v_fmac_f32 v75, v87, v87\n"
      "v_fmac_f32 v66, v31, v31\n"
      "s_nop 1\n"
      "v_permlane16_swap_b32 v75, v66\n"
      "v_add_f32 v75, v75, v66\n"
      "s_waitcnt lgkmcnt(0)\n"
      "ds_read_b128 v[56:59], v74 offset:32\n"
      "ds_read_b64 v[68:69], v74 offset:24\n"
      "v_mfma_f32_16x16x32_bf16 v[80:83], v[52:55], v[32:35], 0\n"
      "v_mov_b32 v66, v75\n"
      "v_mfma_f32_16x16x32_bf16 v[80:83], v[20:23], v[4:7], v[80:83]\n"
      "v_cmp_ne_u64 s[74:75], 0, v[72:73]\n"
      "v_permlane32_swap_b32 v75, v66\n"
      "v_mfma_f32_16x16x32_bf16 v[36:39], v[76:79], v[32:35], 0\n"
      "v_add_f32 v75, v75, v66\n"
      "v_rsq_f32 v75, v75\n"
      "v_mfma_f32_16x16x32_bf16 v[36:39], v[24:27], v[4:7], v[36:39]\n"
      "v_mov_b32 v66, v75\n"
      "s_nop 1\n"
      "v_permlane16_swap_b32 v75, v66\n"
      "v_mul_f32 v88, v66, v88\n"
      "v_mul_f32 v89, v66, v89\n"
      "v_mul_f32 v90, v66, v90\n"
      "v_mul_f32 v91, v66, v91\n"
      "v_mul_f32 v92, v66, v92\n"
      "v_mul_f32 v93, v66, v93\n"
      "v_mul_f32 v94, v66, v94\n"
      "v_mul_f32 v95, v66, v95\n"
      "v_mul_f32 v28, v66, v28\n"
      "v_mul_f32 v29, v66, v29\n"
      "v_mul_f32 v30, v66, v30\n"
      "v_mul_f32 v31, v66, v31\n"
      "v_cvt_pk_bf16_f32 v88, v88, v89\n"
      "v_cvt_pk_bf16_f32 v89, v90, v91\n"
      "v_cvt_pk_bf16_f32 v90, v92, v93\n"
      "s_waitcnt lgkmcnt(0)\n"
      "v_mfma_f32_16x16x32_bf16 v[64:67], v[56:59], v[32:35], 0\n"
      "v_cvt_pk_bf16_f32 v91, v94, v95\n"
      "v_cvt_pk_bf16_f32 v28, v28, v29\n"
      "v_mfma_f32_16x16x32_bf16 v[64:67], v[68:71], v[4:7], v[64:67]\n"
      "s_setprio 0\n"
      "v_cvt_pk_bf16_f32 v29, v30, v31\n"
      "v_mul_f32 v75, s40, v75\n"
      "ds_read_b64 v[30:31], v3 offset:1024\n"
      "v_mul_f32 v48, v75, v48\n"
      "v_mul_f32 v49, v75, v49\n"
      "v_mul_f32 v50, v75, v50\n"
      "v_mul_f32 v51, v75, v51\n"
      "v_mul_f32 v40, v75, v40\n"
      "v_mul_f32 v41, v75, v41\n"
      "v_mul_f32 v42, v75, v42\n"
      "v_mul_f32 v43, v75, v43\n"
      "v_mul_f32 v84, v75, v84\n"
      "v_mul_f32 v85, v75, v85\n"
      "v_mul_f32 v86, v75, v86\n"
      "v_mul_f32 v87, v75, v87\n"
      "v_cvt_pk_bf16_f32 v48, v48, v49\n"
      "v_cvt_pk_bf16_f32 v49, v50, v51\n"
      "v_cvt_pk_bf16_f32 v50, v40, v41\n"
      "v_cvt_pk_bf16_f32 v51, v42, v43\n"
      "v_cvt_pk_bf16_f32 v12, v84, v85\n"
      "v_cvt_pk_bf16_f32 v13, v86, v87\n"
      "s_setprio 2\n"
      "s_waitcnt lgkmcnt(0)\n"
      "v_mfma_f32_16x16x32_bf16 v[84:87], v[88:91], v[32:35], 0\n"
      "v_mfma_f32_16x16x32_bf16 v[84:87], v[28:31], v[4:7], v[84:87]\n"
      "s_setprio 0\n"
      "s_or_b64 s[34:35], s[74:75], s[76:77]\n"
      "s_cbranch_scc1 L_gen_2_%=\n"
      "ds_read_b64 v[22:23], v3 offset:2048\n"
      "ds_read_b64 v[26:27], v3 offset:2560\n"
      "ds_read_b64 v[30:31], v3 offset:3072\n"
      "ds_read_b64 v[70:71], v3 offset:3584\n"
      "v_exp_f32 v80, v80\n"
      "v_exp_f32 v81, v81\n"
      "v_exp_f32 v82, v82\n"
      "v_exp_f32 v83, v83\n"
      "s_setprio 2\n"
      "v_exp_f32 v36, v36\n"
      "v_exp_f32 v37, v37\n"
      "v_exp_f32 v38, v38\n"
      "v_exp_f32 v39, v39\n"
      "v_exp_f32 v84, v84\n"
      "v_exp_f32 v85, v85\n"
      "v_exp_f32 v86, v86\n"
      "v_exp_f32 v87, v87\n"
      "v_exp_f32 v64, v64\n"
      "v_cvt_pk_bf16_f32 v80, v80, v81\n"
      "v_cvt_pk_bf16_f32 v81, v82, v83\n"
      "v_cvt_pk_bf16_f32 v82, v36, v37\n"
      "v_cvt_pk_bf16_f32 v83, v38, v39\n"
      "v_cvt_pk_bf16_f32 v36, v84, v85\n"
      "s_waitcnt lgkmcnt(0)\n"
      "v_mfma_f32_16x16x32_bf16 v[32:35], v[52:55], v[60:63], 0\n"
      "ds_read_b128 v[40:43], v74\n"
      "ds_read_b64 v[84:85], v74 offset:16\n"
      "ds_read_b32 v72, v16 offset:768\n"
      "v_mfma_f32_16x16x32_bf16 v[32:35], v[20:23], v[8:11], v[32:35]\n"
      "ds_read_b64 v[22:23], v3 offset:4096\n"
      "ds_read_b32 v39, v16 offset:512\n"
      "v_mfma_f32_16x16x32_bf16 v[92:95], v[76:79], v[60:63], 0\n"
      "v_cvt_pk_bf16_f32 v37, v86, v87\n"
      "v_cvt_pk_bf16_f32 v38, v64, 0\n"
      "v_mfma_f32_16x16x32_bf16 v[92:95], v[24:27], v[8:11], v[92:95]\n"
      "ds_read_b64 v[26:27], v3 offset:4608\n"
      "ds_write_b128 v2, v[80:83]\n"
      "v_mfma_f32_16x16x32_bf16 v[80:83], v[88:91], v[60:63], 0\n"
      "ds_write_b96 v2, v[36:38] offset:1024\n"
      "v_mfma_f32_16x16x32_bf16 v[80:83], v[28:31], v[8:11], v[80:83]\n"
      "ds_read_b64 v[30:31], v3 offset:5120\n"
      "v_exp_f32_e64 v32, v32\n"
      "v_mfma_f32_16x16x32_bf16 v[64:67], v[56:59], v[60:63], 0\n"
      "v_mfma_f32_16x16x32_bf16 v[64:67], v[68:71], v[8:11], v[64:67]\n"
      "ds_read_b64 v[70:71], v3 offset:5632\n"
      "s_setprio 0\n"
      "v_exp_f32 v33, v33\n"
      "v_exp_f32 v34, v34\n"
      "v_exp_f32 v35, v35\n"
      "s_setprio 2\n"
      "s_waitcnt lgkmcnt(1)\n"
      "v_mfma_f32_16x16x32_bf16 v[60:63], v[52:55], v[48:51], 0\n"
      "v_exp_f32 v92, v92\n"
      "v_mfma_f32_16x16x32_bf16 v[60:63], v[20:23], v[12:15], v[60:63]\n"
      "v_exp_f32 v93, v93\n"
      "v_mfma_f32_16x16x32_bf16 v[44:47], v[76:79], v[48:51], 0\n"
      "v_exp_f32 v94, v94\n"
      "v_mfma_f32_16x16x32_bf16 v[44:47], v[24:27], v[12:15], v[44:47]\n"
      "v_exp_f32 v95, v95\n"
      "v_exp_f32 v80, v80\n"
      "v_exp_f32 v81, v81\n"
      "v_mfma_f32_16x16x32_bf16 v[96:99], v[88:91], v[48:51], 0\n"
      "v_exp_f32 v82, v82\n"
      "v_mfma_f32_16x16x32_bf16 v[96:99], v[28:31], v[12:15], v[96:99]\n"
      "v_exp_f32_e64 v83, v83\n"
      "s_waitcnt lgkmcnt(0)\n"
      "v_mfma_f32_16x16x32_bf16 v[100:103], v[56:59], v[48:51], 0\n"
      "v_mfma_f32_16x16x32_bf16 v[100:103], v[68:71], v[12:15], v[100:103]\n"
      "v_exp_f32 v64, v64\n"
      "s_setprio 0\n"
      "v_cvt_pk_bf16_f32 v32, v32, v33\n"
      "v_cvt_pk_bf16_f32 v33, v34, v35\n"
      "v_cvt_pk_bf16_f32 v34, v92, v93\n"
      "v_cvt_pk_bf16_f32 v35, v94, v95\n"
      "v_cvt_pk_bf16_f32 v92, v80, v81\n"
      "v_cvt_pk_bf16_f32 v93, v82, v83\n"
      "v_cvt_pk_bf16_f32 v94, v64, 0\n"
      "ds_write_b128 v2, v[32:35] offset:2048\n"
      "ds_write_b96 v2, v[92:94] offset:3072\n"
      "v_exp_f32 v60, v60\n"
      "v_exp_f32 v61, v61\n"
      "v_exp_f32 v62, v62\n"
      "v_exp_f32 v63, v63\n"
      "s_setprio 2\n"
      "v_mfma_f32_16x16x32_bf16 v[32:35], v[40:43], v[52:55], v[72:75]\n"
      "v_exp_f32 v44, v44\n"
      "v_mfma_f32_16x16x32_bf16 v[52:55], v[40:43], v[76:79], v[72:75]\n"
      "v_exp_f32 v45, v45\n"
      "v_mfma_f32_16x16x32_bf16 v[76:79], v[40:43], v[88:91], v[72:75]\n"
      "v_exp_f32 v46, v46\n"
      "v_mfma_f32_16x16x32_bf16 v[88:91], v[40:43], v[56:59], v[72:75]\n"
      "v_exp_f32 v47, v47\n"
      "v_exp_f32 v96, v96\n"
      "v_mfma_f32_16x16x16_bf16 v[32:35], v[84:85], v[20:21], v[32:35]\n"
      "v_exp_f32 v97, v97\n"
      "v_mfma_f32_16x16x16_bf16 v[52:55], v[84:85], v[24:25], v[52:55]\n"
      "v_exp_f32 v98, v98\n"
      "v_mfma_f32_16x16x16_bf16 v[76:79], v[84:85], v[28:29], v[76:79]\n"
      "v_exp_f32 v99, v99\n"
      "v_mfma_f32_16x16x16_bf16 v[88:91], v[84:85], v[68:69], v[88:91]\n"
      "v_exp_f32_e64 v100, v100\n"
      "s_setprio 0\n"
      "v_cvt_pk_bf16_f32 v60, v60, v61\n"
      "v_cvt_pk_bf16_f32 v61, v62, v63\n"
      "v_cvt_pk_bf16_f32 v62, v44, v45\n"
      "v_cvt_pk_bf16_f32 v63, v46, v47\n"
      "v_cvt_pk_bf16_f32 v44, v96, v97\n"
      "v_cvt_pk_bf16_f32 v45, v98, v99\n"
      "v_cvt_pk_bf16_f32 v46, v100, 0\n"
      "ds_write_b128 v2, v[60:63] offset:4096\n"
      "ds_write_b96 v2, v[44:46] offset:5120\n"
      "v_cndmask_b32 v32, v32, v52, s[42:43]\n"
      "v_cndmask_b32 v32, v32, v76, s[44:45]\n"
      "v_cndmask_b32 v32, v32, v88, s[46:47]\n"
      "v_exp_f32 v32, v32\n"
      "s_nop 0\n"
      "v_cvt_pk_bf16_f32 v32, v32, v32\n"
      "ds_write_b16 v39, v32\n"
      "L_join_3_%=:\n"
      "s_waitcnt lgkmcnt(0)\n"
      "s_barrier\n"
      "s_cmp_eq_u32 s27, 10\n"
      "s_cbranch_scc1 L_Aend_%=\n"
      "s_setprio 0\n"
      "ds_read_b128 v[24:27], v0 offset:24576\n"
      "ds_read_b128 v[28:31], v1 offset:24576\n"
      "s_waitcnt lgkmcnt(0)\n"
      "v_mfma_f32_16x16x32_bf16 v[32:35], a[0:3], v[24:27], 0\n"
      "v_mfma_f32_16x16x32_bf16 v[32:35], a[8:11], v[28:31], v[32:35]\n"
      "ds_read_b128 v[36:39], v0 offset:28672\n"
      "v_mfma_f32_16x16x32_bf16 v[40:43], a[4:7], v[24:27], 0\n"
      "v_mfma_f32_16x16x32_bf16 v[40:43], a[12:15], v[28:31], v[40:43]\n"
      "ds_read_b128 v[44:47], v1 offset:28672\n"
      "v_mfma_f32_16x16x32_bf16 v[48:51], a[48:51], v[24:27], 0\n"
      "v_mfma_f32_16x16x32_bf16 v[48:51], a[56:59], v[28:31], v[48:51]\n"
      "v_mfma_f32_16x16x32_bf16 v[52:55], a[96:99], v[24:27], 0\n"
      "v_mfma_f32_16x16x32_bf16 v[52:55], a[104:107], v[28:31], v[52:55]\n"
      "v_mfma_f32_16x16x32_bf16 v[56:59], a[100:103], v[24:27], 0\n"
      "v_mfma_f32_16x16x32_bf16 v[56:59], a[108:111], v[28:31], v[56:59]\n"
      "v_mfma_f32_16x16x32_bf16 v[20:23], a[52:55], v[24:27], 0\n"
      "v_mfma_f32_16x16x32_bf16 v[20:23], a[60:63], v[28:31], v[20:23]\n"
      "s_waitcnt lgkmcnt(0)\n"
      "v_mfma_f32_16x16x32_bf16 v[32:35], a[16:19], v[36:39], v[32:35]\n"
      "v_mfma_f32_16x16x32_bf16 v[32:35], a[24:27], v[44:47], v[32:35]\n"
      "ds_read_b128 v[24:27], v0 offset:32768\n"
      "v_mfma_f32_16x16x32_bf16 v[40:43], a[20:23], v[36:39], v[40:43]\n"
      "v_mfma_f32_16x16x32_bf16 v[40:43], a[28:31], v[44:47], v[40:43]\n"
      "ds_read_b128 v[28:31], v1 offset:32768\n"
      "v_mfma_f32_16x16x32_bf16 v[48:51], a[64:67], v[36:39], v[48:51]\n"
      "v_mfma_f32_16x16x32_bf16 v[48:51], a[72:75], v[44:47], v[48:51]\n"
      "v_mfma_f32_16x16x32_bf16 v[52:55], a[112:115], v[36:39], v[52:55]\n"
      "v_mfma_f32_16x16x32_bf16 v[52:55], a[120:123], v[44:47], v[52:55]\n"
      "v_mfma_f32_16x16x32_bf16 v[56:59], a[116:119], v[36:39], v[56:59]\n"
      "v_mfma_f32_16x16x32_bf16 v[56:59], a[124:127], v[44:47], v[56:59]\n"
      "v_mfma_f32_16x16x32_bf16 v[20:23], a[68:71], v[36:39], v[20:23]\n"
      "v_mfma_f32_16x16x32_bf16 v[20:23], a[76:79], v[44:47], v[20:23]\n"
      "s_waitcnt lgkmcnt(0)\n"
      "v_mfma_f32_16x16x32_bf16 v[32:35], a[32:35], v[24:27], v[32:35]\n"
      "v_mfma_f32_16x16x32_bf16 v[32:35], a[40:43], v[28:31], v[32:35]\n"
      "ds_read_b128 v[44:47], v0 offset:26624\n"
      "v_mfma_f32_16x16x32_bf16 v[40:43], a[36:39], v[24:27], v[40:43]\n"
      "v_mfma_f32_16x16x32_bf16 v[40:43], a[44:47], v[28:31], v[40:43]\n"
      "ds_read_b128 v[36:39], v1 offset:26624\n"
      "v_mfma_f32_16x16x32_bf16 v[48:51], a[80:83], v[24:27], v[48:51]\n"
      "v_mfma_f32_16x16x32_bf16 v[48:51], a[88:91], v[28:31], v[48:51]\n"
      "v_mfma_f32_16x16x32_bf16 v[52:55], a[128:131], v[24:27], v[52:55]\n"
      "v_mfma_f32_16x16x32_bf16 v[52:55], a[136:139], v[28:31], v[52:55]\n"
      "v_mfma_f32_16x16x32_bf16 v[56:59], a[132:135], v[24:27], v[56:59]\n"
      "v_mfma_f32_16x16x32_bf16 v[56:59], a[140:143], v[28:31], v[56:59]\n"
      "v_mfma_f32_16x16x32_bf16 v[20:23], a[84:87], v[24:27], v[20:23]\n"
      "v_mfma_f32_16x16x32_bf16 v[20:23], a[92:95], v[28:31], v[20:23]\n"
      "s_waitcnt lgkmcnt(0)\n"
      "v_mfma_f32_16x16x32_bf16 v[60:63], a[0:3], v[44:47], 0\n"
      "v_mfma_f32_16x16x32_bf16 v[60:63], a[8:11], v[36:39], v[60:63]\n"
      "ds_read_b128 v[28:31], v0 offset:30720\n"
      "v_mfma_f32_16x16x32_bf16 v[64:67], a[4:7], v[44:47], 0\n"
      "v_mfma_f32_16x16x32_bf16 v[64:67], a[12:15], v[36:39], v[64:67]\n"
      "ds_read_b128 v[68:71], v1 offset:30720\n"
      "v_mfma_f32_16x16x32_bf16 v[72:75], a[48:51], v[44:47], 0\n"
      "v_fma_f32 v18, v32, v32, s51\n"
      "v_fma_f32 v19, v52, v52, s51\n"
      "v_fmac_f32 v18, v33, v33\n"
      "v_mfma_f32_16x16x32_bf16 v[72:75], a[56:59], v[36:39], v[72:75]\n"
      "v_fmac_f32 v19, v53, v53\n"
      "v_fmac_f32 v18, v34, v34\n"
      "v_mfma_f32_16x16x32_bf16 v[76:79], a[96:99], v[44:47], 0\n"
      "v_fmac_f32 v19, v54, v54\n"
      "v_fmac_f32 v18, v35, v35\n"
      "v_mfma_f32_16x16x32_bf16 v[76:79], a[104:107], v[36:39], v[76:79]\n"
      "v_fmac_f32 v19, v55, v55\n"
      "v_fmac_f32 v18, v40, v40\n"
      "v_mfma_f32_16x16x32_bf16 v[80:83], a[100:103], v[44:47], 0\n"
      "v_fmac_f32 v19, v56, v56\n"
      "v_fmac_f32 v18, v41, v41\n"
      "v_mfma_f32_16x16x32_bf16 v[80:83], a[108:111], v[36:39], v[80:83]\n"
      "v_fmac_f32 v19, v57, v57\n"
      "v_fmac_f32 v18, v42, v42\n"
      "v_fmac_f32 v19, v58, v58\n"
      "v_mfma_f32_16x16x32_bf16 v[24:27], a[52:55], v[44:47], 0\n"
      "v_fmac_f32 v18, v43, v43\n"
      "v_fmac_f32 v19, v59, v59\n"
      "v_mfma_f32_16x16x32_bf16 v[24:27], a[60:63], v[36:39], v[24:27]\n"
      "v_fmac_f32 v18, v48, v48\n"
      "v_fmac_f32 v19, v20, v20\n"
      "s_waitcnt lgkmcnt(0)\n"
      "v_mfma_f32_16x16x32_bf16 v[60:63], a[16:19], v[28:31], v[60:63]\n"
      "v_fmac_f32 v18, v49, v49\n"
      "v_fmac_f32 v19, v21, v21\n"
      "v_mfma_f32_16x16x32_bf16 v[60:63], a[24:27], v[68:71], v[60:63]\n"
      "ds_read_b128 v[36:39], v0 offset:34816\n"
      "v_fmac_f32 v18, v50, v50\n"
      "v_fmac_f32 v19, v22, v22\n"
      "v_mfma_f32_16x16x32_bf16 v[64:67], a[20:23], v[28:31], v[64:67]\n"
      "v_fmac_f32 v18, v51, v51\n"
      "v_fmac_f32 v19, v23, v23\n"
      "s_nop 1\n"
      "v_permlane16_swap_b32 v18, v19\n"
      "v_mfma_f32_16x16x32_bf16 v[64:67], a[28:31], v[68:71], v[64:67]\n"
      "ds_read_b128 v[44:47], v1 offset:34816\n"
      "v_add_f32 v18, v18, v19\n"
      "v_mov_b32 v19, v18\n"
      "v_mfma_f32_16x16x32_bf16 v[72:75], a[64:67], v[28:31], v[72:75]\n"
      "s_nop 0\n"
      "v_permlane32_swap_b32 v18, v19\n"
      "v_add_f32 v18, v18, v19\n"
      "v_mfma_f32_16x16x32_bf16 v[72:75], a[72:75], v[68:71], v[72:75]\n"
      "v_rsq_f32 v18, v18\n"
      "s_nop 0\n"
      "v_mov_b32 v19, v18\n"
      "v_mfma_f32_16x16x32_bf16 v[76:79], a[112:115], v[28:31], v[76:79]\n"
      "s_nop 0\n"
      "v_permlane16_swap_b32 v18, v19\n"
      "v_mul_f32 v52, v19, v52\n"
      "v_mfma_f32_16x16x32_bf16 v[76:79], a[120:123], v[68:71], v[76:79]\n"
      "v_mul_f32 v53, v19, v53\n"
      "v_mul_f32_e64 v54, v19, v54\n"
      "v_mfma_f32_16x16x32_bf16 v[80:83], a[116:119], v[28:31], v[80:83]\n"
      "v_mul_f32 v55, v19, v55\n"
      "v_mul_f32 v56, v19, v56\n"
      "v_mul_f32 v57, v19, v57\n"
      "v_mfma_f32_16x16x32_bf16 v[80:83], a[124:127], v[68:71], v[80:83]\n"
      "v_mul_f32 v58, v19, v58\n"
      "v_mul_f32 v59, v19, v59\n"
      "v_mfma_f32_16x16x32_bf16 v[24:27], a[68:71], v[28:31], v[24:27]\n"
      "v_mul_f32 v20, v19, v20\n"
      "v_mul_f32 v21, v19, v21\n"
      "v_mfma_f32_16x16x32_bf16 v[24:27], a[76:79], v[68:71], v[24:27]\n"
      "v_mul_f32 v22, v19, v22\n"
      "v_mul_f32 v23, v19, v23\n"
      "s_waitcnt lgkmcnt(0)\n"
      "v_mfma_f32_16x16x32_bf16 v[60:63], a[32:35], v[36:39], v[60:63]\n"
      "v_cvt_pk_bf16_f32 v52, v52, v53\n"
      "v_cvt_pk_bf16_f32 v53, v54, v55\n"
      "v_mfma_f32_16x16x32_bf16 v[60:63], a[40:43], v[44:47], v[60:63]\n"
      "ds_read_b128 v[68:71], v0 offset:14336\n"
      "v_cvt_pk_bf16_f32 v54, v56, v57\n"
      "v_cvt_pk_bf16_f32 v55, v58, v59\n"
      "v_cvt_pk_bf16_f32 v20, v20, v21\n"
      "v_mfma_f32_16x16x32_bf16 v[64:67], a[36:39], v[36:39], v[64:67]\n"
      "v_cvt_pk_bf16_f32 v21, v22, v23\n"
      "v_mfma_f32_16x16x32_bf16 v[64:67], a[44:47], v[44:47], v[64:67]\n"
      "ds_read_b128 v[56:59], v1 offset:14336\n"
      "v_mul_f32 v18, s40, v18\n"
      "v_mul_f32 v32, v18, v32\n"
      "v_mfma_f32_16x16x32_bf16 v[72:75], a[80:83], v[36:39], v[72:75]\n"
      "v_mul_f32 v33, v18, v33\n"
      "v_mul_f32 v34, v18, v34\n"
      "v_mfma_f32_16x16x32_bf16 v[72:75], a[88:91], v[44:47], v[72:75]\n"
      "v_mul_f32 v35, v18, v35\n"
      "v_mul_f32 v40, v18, v40\n"
      "v_mfma_f32_16x16x32_bf16 v[76:79], a[128:131], v[36:39], v[76:79]\n"
      "v_mul_f32 v41, v18, v41\n"
      "v_mul_f32 v42, v18, v42\n"
      "v_mul_f32 v43, v18, v43\n"
      "v_mfma_f32_16x16x32_bf16 v[76:79], a[136:139], v[44:47], v[76:79]\n"
      "v_mul_f32 v48, v18, v48\n"
      "v_mul_f32 v49, v18, v49\n"
      "v_mfma_f32_16x16x32_bf16 v[80:83], a[132:135], v[36:39], v[80:83]\n"
      "v_mul_f32 v50, v18, v50\n"
      "v_mul_f32 v51, v18, v51\n"
      "v_mfma_f32_16x16x32_bf16 v[80:83], a[140:143], v[44:47], v[80:83]\n"
      "v_cvt_pk_bf16_f32 v32, v32, v33\n"
      "v_cvt_pk_bf16_f32 v33, v34, v35\n"
      "v_mfma_f32_16x16x32_bf16 v[24:27], a[84:87], v[36:39], v[24:27]\n"
      "v_cvt_pk_bf16_f32 v34, v40, v41\n"
      "v_cvt_pk_bf16_f32 v35, v42, v43\n"
      "v_mfma_f32_16x16x32_bf16 v[24:27], a[92:95], v[44:47], v[24:27]\n"
      "v_cvt_pk_bf16_f32 v4, v48, v49\n"
      "v_cvt_pk_bf16_f32 v5, v50, v51\n"
      "s_waitcnt lgkmcnt(0)\n"
      "v_mfma_f32_16x16x32_bf16 v[48:51], a[0:3], v[68:71], 0\n"
      "v_mfma_f32_16x16x32_bf16 v[48:51], a[8:11], v[56:59], v[48:51]\n"
      "ds_read_b128 v[44:47], v0 offset:18432\n"
      "v_mfma_f32_16x16x32_bf16 v[40:43], a[4:7], v[68:71], 0\n"
      "v_mfma_f32_16x16x32_bf16 v[40:43], a[12:15], v[56:59], v[40:43]\n"
      "ds_read_b128 v[36:39], v1 offset:18432\n"
      "v_mfma_f32_16x16x32_bf16 v[84:87], a[48:51], v[68:71], 0\n"
      "v_fma_f32 v22, v60, v60, s51\n"
      "v_fma_f32 v23, v76, v76, s51\n"
      "v_fmac_f32_e64 v22, v61, v61\n"
      "v_mfma_f32_16x16x32_bf16 v[84:87], a[56:59], v[56:59], v[84:87]\n"
      "v_fmac_f32 v23, v77, v77\n"
      "v_fmac_f32 v22, v62, v62\n"
      "v_mfma_f32_16x16x32_bf16 v[88:91], a[96:99], v[68:71], 0\n"
      "v_fmac_f32 v23, v78, v78\n"
      "v_fmac_f32 v22, v63, v63\n"
      "v_mfma_f32_16x16x32_bf16 v[88:91], a[104:107], v[56:59], v[88:91]\n"
      "v_fmac_f32 v23, v79, v79\n"
      "v_fmac_f32 v22, v64, v64\n"
      "v_mfma_f32_16x16x32_bf16 v[92:95], a[100:103], v[68:71], 0\n"
      "v_fmac_f32 v23, v80, v80\n"
      "v_fmac_f32 v22, v65, v65\n"
      "v_mfma_f32_16x16x32_bf16 v[92:95], a[108:111], v[56:59], v[92:95]\n"
      "v_fmac_f32 v23, v81, v81\n"
      "v_fmac_f32 v22, v66, v66\n"
      "v_fmac_f32 v23, v82, v82\n"
      "v_mfma_f32_16x16x32_bf16 v[28:31], a[52:55], v[68:71], 0\n"
      "v_fmac_f32 v22, v67, v67\n"
      "v_fmac_f32 v23, v83, v83\n"
      "v_mfma_f32_16x16x32_bf16 v[28:31], a[60:63], v[56:59], v[28:31]\n"
      "v_fmac_f32 v22, v72, v72\n"
      "v_fmac_f32 v23, v24, v24\n"
      "s_waitcnt lgkmcnt(0)\n"
      "v_mfma_f32_16x16x32_bf16 v[48:51], a[16:19], v[44:47], v[48:51]\n"
      "v_fmac_f32 v22, v73, v73\n"
      "v_fmac_f32 v23, v25, v25\n"
      "v_mfma_f32_16x16x32_bf16 v[48:51], a[24:27], v[36:39], v[48:51]\n"
      "ds_read_b128 v[56:59], v0 offset:22528\n"
      "v_fmac_f32 v22, v74, v74\n"
      "v_fmac_f32 v23, v26, v26\n"
      "v_mfma_f32_16x16x32_bf16 v[40:43], a[20:23], v[44:47], v[40:43]\n"
      "v_fmac_f32 v22, v75, v75\n"
      "v_fmac_f32 v23, v27, v27\n"
      "s_nop 1\n"
      "v_permlane16_swap_b32 v22, v23\n"
      "v_mfma_f32_16x16x32_bf16 v[40:43], a[28:31], v[36:39], v[40:43]\n"
      "ds_read_b128 v[68:71], v1 offset:22528\n"
      "v_add_f32 v22, v22, v23\n"
      "v_mov_b32 v23, v22\n"
      "v_mfma_f32_16x16x32_bf16 v[84:87], a[64:67], v[44:47], v[84:87]\n"
      "s_nop 0\n"
      "v_permlane32_swap_b32 v22, v23\n"
      "v_add_f32 v22, v22, v23\n"
      "v_mfma_f32_16x16x32_bf16 v[84:87], a[72:75], v[36:39], v[84:87]\n"
      "v_rsq_f32 v22, v22\n"
      "s_nop 0\n"
      "v_mov_b32 v23, v22\n"
      "v_mfma_f32_16x16x32_bf16 v[88:91], a[112:115], v[44:47], v[88:91]\n"
      "s_nop 0\n"
      "v_permlane16_swap_b32 v22, v23\n"
      "v_mul_f32 v76, v23, v76\n"
      "v_mfma_f32_16x16x32_bf16 v[88:91], a[120:123], v[36:39], v[88:91]\n"
      "v_mul_f32 v77, v23, v77\n"
      "v_mul_f32_e64 v78, v23, v78\n"
      "v_mfma_f32_16x16x32_bf16 v[92:95], a[116:119], v[44:47], v[92:95]\n"
      "v_mul_f32 v79, v23, v79\n"
      "v_mul_f32 v80, v23, v80\n"
      "v_mul_f32 v81, v23, v81\n"
      "v_mfma_f32_16x16x32_bf16 v[92:95], a[124:127], v[36:39], v[92:95]\n"
      "v_mul_f32 v82, v23, v82\n"
      "v_mul_f32 v83, v23, v83\n"
      "v_mfma_f32_16x16x32_bf16 v[28:31], a[68:71], v[44:47], v[28:31]\n"
      "v_mul_f32 v24, v23, v24\n"
      "v_mul_f32 v25, v23, v25\n"
      "v_mfma_f32_16x16x32_bf16 v[28:31], a[76:79], v[36:39], v[28:31]\n"
      "v_mul_f32 v26, v23, v26\n"
      "v_mul_f32 v27, v23, v27\n"
      "s_waitcnt lgkmcnt(0)\n"
      "v_mfma_f32_16x16x32_bf16 v[48:51], a[32:35], v[56:59], v[48:51]\n"
      "v_cvt_pk_bf16_f32 v76, v76, v77\n"
      "v_cvt_pk_bf16_f32 v77, v78, v79\n"
      "v_mfma_f32_16x16x32_bf16 v[48:51], a[40:43], v[68:71], v[48:51]\n"
      "v_cvt_pk_bf16_f32 v78, v80, v81\n"
      "v_cvt_pk_bf16_f32 v79, v82, v83\n"
      "v_cvt_pk_bf16_f32 v24, v24, v25\n"
      "v_mfma_f32_16x16x32_bf16 v[40:43], a[36:39], v[56:59], v[40:43]\n"
      "v_cvt_pk_bf16_f32 v25, v26, v27\n"
      "v_mfma_f32_16x16x32_bf16 v[40:43], a[44:47], v[68:71], v[40:43]\n"
      "v_mul_f32 v22, s40, v22\n"
      "v_mul_f32 v60, v22, v60\n"
      "v_mfma_f32_16x16x32_bf16 v[84:87], a[80:83], v[56:59], v[84:87]\n"
      "v_mul_f32 v61, v22, v61\n"
      "v_mul_f32 v62, v22, v62\n"
      "v_mfma_f32_16x16x32_bf16 v[84:87], a[88:91], v[68:71], v[84:87]\n"
      "v_mul_f32 v63, v22, v63\n"
      "v_mul_f32 v64, v22, v64\n"
      "v_mfma_f32_16x16x32_bf16 v[88:91], a[128:131], v[56:59], v[88:91]\n"
      "v_mul_f32 v65, v22, v65\n"
      "v_mul_f32 v66, v22, v66\n"
      "v_mul_f32 v67, v22, v67\n"
      "v_mfma_f32_16x16x32_bf16 v[88:91], a[136:139], v[68:71], v[88:91]\n"
      "v_mul_f32 v72, v22, v72\n"
      "v_mul_f32 v73, v22, v73\n"
      "v_mfma_f32_16x16x32_bf16 v[92:95], a[132:135], v[56:59], v[92:95]\n"
      "v_mul_f32 v74, v22, v74\n"
      "v_mul_f32_e64 v75, v22, v75\n"
      "v_mfma_f32_16x16x32_bf16 v[92:95], a[140:143], v[68:71], v[92:95]\n"
      "v_cvt_pk_bf16_f32 v60, v60, v61\n"
      "v_cvt_pk_bf16_f32 v61, v62, v63\n"
      "v_mfma_f32_16x16x32_bf16 v[28:31], a[84:87], v[56:59], v[28:31]\n"
      "v_cvt_pk_bf16_f32 v62, v64, v65\n"
      "v_cvt_pk_bf16_f32 v63, v66, v67\n"
      "v_mfma_f32_16x16x32_bf16 v[28:31], a[92:95], v[68:71], v[28:31]\n"
      "v_cvt_pk_bf16_f32 v8, v72, v73\n"
      "v_cvt_pk_bf16_f32 v9, v74, v75\n"
      "s_waitcnt lgkmcnt(0)\n"
      "s_barrier\n"
      "ds_read_b32 v74, v16 offset:256\n"
      "ds_read_b64 v[22:23], v3\n"
      "ds_read_b64 v[26:27], v3 offset:512\n"
      "ds_read_b64 v[70:71], v3 offset:1536\n"
      "ds_read2st64_b32 v[72:73], v17 offset0:0 offset1:11\n"
      "s_setprio 0\n"
      "v_fma_f32 v75, v48, v48, s51\n"
      "v_fma_f32 v66, v88, v88, s51\n"
      "v_fmac_f32 v75, v49, v49\n"
      "v_fmac_f32 v66, v89, v89\n"
      "v_fmac_f32 v75, v50, v50\n"
      "v_fmac_f32 v66, v90, v90\n"
      "v_fmac_f32 v75, v51, v51\n"
      "v_fmac_f32 v66, v91, v91\n"
      "v_fmac_f32 v75, v40, v40\n"
      "v_fmac_f32 v66, v92, v92\n"
      "v_fmac_f32 v75, v41, v41\n"
      "v_fmac_f32 v66, v93, v93\n"
      "s_setprio 2\n"
      "v_fmac_f32 v75, v42, v42\n"
      "v_fmac_f32 v66, v94, v94\n"
      "v_fmac_f32 v75, v43, v43\n"
      "v_fmac_f32 v66, v95, v95\n"
      "v_fmac_f32 v75, v84, v84\n"
      "v_fmac_f32 v66, v28, v28\n"
      "v_fmac_f32 v75, v85, v85\n"
      "v_fmac_f32 v66, v29, v29\n"
      "v_fmac_f32 v75, v86, v86\n"
      "v_fmac_f32 v66, v30, v30\n"
      "v_fmac_f32 v75, v87, v87\n"
      "v_fmac_f32 v66, v31, v31\n"
      "s_nop 1\n"
      "v_permlane16_swap_b32 v75, v66\n"
      "v_add_f32 v75, v75, v66\n"
      "s_waitcnt lgkmcnt(0)\n"
      "ds_read_b128 v[56:59], v74 offset:1184\n"
      "ds_read_b64 v[68:69], v74 offset:1176\n"
      "v_mfma_f32_16x16x32_bf16 v[80:83], v[52:55], v[32:35], 0\n"
      "v_mov_b32 v66, v75\n"
      "v_mfma_f32_16x16x32_bf16 v[80:83], v[20:23], v[4:7], v[80:83]\n"
      "v_cmp_ne_u64 s[74:75], 0, v[72:73]\n"
      "v_permlane32_swap_b32 v75, v66\n"
      "v_mfma_f32_16x16x32_bf16 v[36:39], v[76:79], v[32:35], 0\n"
      "v_add_f32 v75, v75, v66\n"
      "v_rsq_f32 v75, v75\n"
      "v_mfma_f32_16x16x32_bf16 v[36:39], v[24:27], v[4:7], v[36:39]\n"
      "v_mov_b32 v66, v75\n"
      "s_nop 1\n"
      "v_permlane16_swap_b32 v75, v66\n"
      "v_mul_f32 v88, v66, v88\n"
      "v_mul_f32 v89, v66, v89\n"
      "v_mul_f32 v90, v66, v90\n"
      "v_mul_f32 v91, v66, v91\n"
      "v_mul_f32 v92, v66, v92\n"
      "v_mul_f32 v93, v66, v93\n"
      "v_mul_f32 v94, v66, v94\n"
      "v_mul_f32 v95, v66, v95\n"
      "v_mul_f32 v28, v66, v28\n"
      "v_mul_f32 v29, v66, v29\n"
      "v_mul_f32 v30, v66, v30\n"
      "v_mul_f32 v31, v66, v31\n"
      "v_cvt_pk_bf16_f32 v88, v88, v89\n"
      "v_cvt_pk_bf16_f32 v89, v90, v91\n"
      "v_cvt_pk_bf16_f32 v90, v92, v93\n"
      "s_waitcnt lgkmcnt(0)\n"
      "v_mfma_f32_16x16x32_bf16 v[64:67], v[56:59], v[32:35], 0\n"
      "v_cvt_pk_bf16_f32 v91, v94, v95\n"
      "v_cvt_pk_bf16_f32 v28, v28, v29\n"
      "v_mfma_f32_16x16x32_bf16 v[64:67], v[68:71], v[4:7], v[64:67]\n"
      "s_setprio 0\n"
      "v_cvt_pk_bf16_f32 v29, v30, v31\n"
      "v_mul_f32 v75, s40, v75\n"
      "ds_read_b64 v[30:31], v3 offset:1024\n"
      "v_mul_f32 v48, v75, v48\n"
      "v_mul_f32 v49, v75, v49\n"
      "v_mul_f32 v50, v75, v50\n"
      "v_mul_f32 v51, v75, v51\n"
      "v_mul_f32 v40, v75, v40\n"
      "v_mul_f32 v41, v75, v41\n"
      "v_mul_f32 v42, v75, v42\n"
      "v_mul_f32 v43, v75, v43\n"
      "v_mul_f32 v84, v75, v84\n"
      "v_mul_f32 v85, v75, v85\n"
      "v_mul_f32 v86, v75, v86\n"
      "v_mul_f32 v87, v75, v87\n"
      "v_cvt_pk_bf16_f32 v48, v48, v49\n"
      "v_cvt_pk_bf16_f32 v49, v50, v51\n"
      "v_cvt_pk_bf16_f32 v50, v40, v41\n"
      "v_cvt_pk_bf16_f32 v51, v42, v43\n"
      "v_cvt_pk_bf16_f32 v12, v84, v85\n"
      "v_cvt_pk_bf16_f32 v13, v86, v87\n"
      "s_setprio 2\n"
      "s_waitcnt lgkmcnt(0)\n"
      "v_mfma_f32_16x16x32_bf16 v[84:87], v[88:91], v[32:35], 0\n"
      "v_mfma_f32_16x16x32_bf16 v[84:87], v[28:31], v[4:7], v[84:87]\n"
      "s_setprio 0\n"
      "s_or_b64 s[34:35], s[74:75], s[76:77]\n"
      "s_cbranch_scc1 L_gen_5_%=\n"
      "ds_read_b64 v[22:23], v3 offset:2048\n"
      "ds_read_b64 v[26:27], v3 offset:2560\n"
      "ds_read_b64 v[30:31], v3 offset:3072\n"
      "ds_read_b64 v[70:71], v3 offset:3584\n"
      "v_exp_f32 v80, v80\n"
      "v_exp_f32 v81, v81\n"
      "v_exp_f32 v82, v82\n"
      "v_exp_f32 v83, v83\n"
      "s_setprio 2\n"
      "v_exp_f32 v36, v36\n"
      "v_exp_f32 v37, v37\n"
      "v_exp_f32 v38, v38\n"
      "v_exp_f32 v39, v39\n"
      "v_exp_f32 v84, v84\n"
      "v_exp_f32 v85, v85\n"
      "v_exp_f32 v86, v86\n"
      "v_exp_f32 v87, v87\n"
      "v_exp_f32 v64, v64\n"
      "v_cvt_pk_bf16_f32 v80, v80, v81\n"
      "v_cvt_pk_bf16_f32 v81, v82, v83\n"
      "v_cvt_pk_bf16_f32 v82, v36, v37\n"
      "v_cvt_pk_bf16_f32 v83, v38, v39\n"
      "v_cvt_pk_bf16_f32 v36, v84, v85\n"
      "s_waitcnt lgkmcnt(0)\n"
      "v_mfma_f32_16x16x32_bf16 v[32:35], v[52:55], v[60:63], 0\n"
      "ds_read_b128 v[40:43], v74 offset:1152\n"
      "ds_read_b64 v[84:85], v74 offset:1168\n"
      "ds_read_b32 v72, v16 offset:768\n"
      "v_mfma_f32_16x16x32_bf16 v[32:35], v[20:23], v[8:11], v[32:35]\n"
      "ds_read_b64 v[22:23], v3 offset:4096\n"
      "ds_read_b32 v39, v16 offset:512\n"
      "v_mfma_f32_16x16x32_bf16 v[92:95], v[76:79], v[60:63], 0\n"
      "v_cvt_pk_bf16_f32 v37, v86, v87\n"
      "v_cvt_pk_bf16_f32 v38, v64, 0\n"
      "v_mfma_f32_16x16x32_bf16 v[92:95], v[24:27], v[8:11], v[92:95]\n"
      "ds_read_b64 v[26:27], v3 offset:4608\n"
      "ds_write_b128 v2, v[80:83]\n"
      "v_mfma_f32_16x16x32_bf16 v[80:83], v[88:91], v[60:63], 0\n"
      "ds_write_b96 v2, v[36:38] offset:1024\n"
      "v_mfma_f32_16x16x32_bf16 v[80:83], v[28:31], v[8:11], v[80:83]\n"
      "ds_read_b64 v[30:31], v3 offset:5120\n"
      "v_exp_f32_e64 v32, v32\n"
      "v_mfma_f32_16x16x32_bf16 v[64:67], v[56:59], v[60:63], 0\n"
      "v_mfma_f32_16x16x32_bf16 v[64:67], v[68:71], v[8:11], v[64:67]\n"
      "ds_read_b64 v[70:71], v3 offset:5632\n"
      "s_setprio 0\n"
      "v_exp_f32 v33, v33\n"
      "v_exp_f32 v34, v34\n"
      "v_exp_f32 v35, v35\n"
      "s_setprio 2\n"
      "s_waitcnt lgkmcnt(1)\n"
      "v_mfma_f32_16x16x32_bf16 v[60:63], v[52:55], v[48:51], 0\n"
      "v_exp_f32_e64 v92, v92\n"
      "v_mfma_f32_16x16x32_bf16 v[60:63], v[20:23], v[12:15], v[60:63]\n"
      "v_exp_f32 v93, v93\n"
      "v_mfma_f32_16x16x32_bf16 v[44:47], v[76:79], v[48:51], 0\n"
      "v_exp_f32 v94, v94\n"
      "v_mfma_f32_16x16x32_bf16 v[44:47], v[24:27], v[12:15], v[44:47]\n"
      "v_exp_f32 v95, v95\n"
      "v_exp_f32 v80, v80\n"
      "v_exp_f32 v81, v81\n"
      "v_mfma_f32_16x16x32_bf16 v[96:99], v[88:91], v[48:51], 0\n"
      "v_exp_f32 v82, v82\n"
      "v_mfma_f32_16x16x32_bf16 v[96:99], v[28:31], v[12:15], v[96:99]\n"
      "v_exp_f32 v83, v83\n"
      "s_waitcnt lgkmcnt(0)\n"
      "v_mfma_f32_16x16x32_bf16 v[100:103], v[56:59], v[48:51], 0\n"
      "v_mfma_f32_16x16x32_bf16 v[100:103], v[68:71], v[12:15], v[100:103]\n"
      "v_exp_f32 v64, v64\n"
      "s_setprio 0\n"
      "v_cvt_pk_bf16_f32 v32, v32, v33\n"
      "v_cvt_pk_bf16_f32 v33, v34, v35\n"
      "v_cvt_pk_bf16_f32 v34, v92, v93\n"
      "v_cvt_pk_bf16_f32 v35, v94, v95\n"
      "v_cvt_pk_bf16_f32 v92, v80, v81\n"
      "v_cvt_pk_bf16_f32 v93, v82, v83\n"
      "v_cvt_pk_bf16_f32 v94, v64, 0\n"
      "ds_write_b128 v2, v[32:35] offset:2048\n"
      "ds_write_b96 v2, v[92:94] offset:3072\n"
      "v_exp_f32 v60, v60\n"
      "v_exp_f32 v61, v61\n"
      "v_exp_f32 v62, v62\n"
      "v_exp_f32_e64 v63, v63\n"
      "s_setprio 2\n"
      "v_mfma_f32_16x16x32_bf16 v[32:35], v[40:43], v[52:55], v[72:75]\n"
      "v_exp_f32 v44, v44\n"
      "v_mfma_f32_16x16x32_bf16 v[52:55], v[40:43], v[76:79], v[72:75]\n"
      "v_exp_f32 v45, v45\n"
      "v_mfma_f32_16x16x32_bf16 v[76:79], v[40:43], v[88:91], v[72:75]\n"
      "v_exp_f32 v46, v46\n"
      "v_mfma_f32_16x16x32_bf16 v[88:91], v[40:43], v[56:59], v[72:75]\n"
      "v_exp_f32 v47, v47\n"
      "v_exp_f32 v96, v96\n"
      "v_mfma_f32_16x16x16_bf16 v[32:35], v[84:85], v[20:21], v[32:35]\n"
      "v_exp_f32 v97, v97\n"
      "v_mfma_f32_16x16x16_bf16 v[52:55], v[84:85], v[24:25], v[52:55]\n"
      "v_exp_f32 v98, v98\n"
      "v_mfma_f32_16x16x16_bf16 v[76:79], v[84:85], v[28:29], v[76:79]\n"
      "v_exp_f32 v99, v99\n"
      "v_mfma_f32_16x16x16_bf16 v[88:91], v[84:85], v[68:69], v[88:91]\n"
      "v_exp_f32 v100, v100\n"
      "s_setprio 0\n"
      "v_cvt_pk_bf16_f32 v60, v60, v61\n"
      "v_cvt_pk_bf16_f32 v61, v62, v63\n"
      "v_cvt_pk_bf16_f32 v62, v44, v45\n"
      "v_cvt_pk_bf16_f32 v63, v46, v47\n"
      "v_cvt_pk_bf16_f32 v44, v96, v97\n"
      "v_cvt_pk_bf16_f32 v45, v98, v99\n"
      "v_cvt_pk_bf16_f32 v46, v100, 0\n"
      "ds_write_b128 v2, v[60:63] offset:4096\n"
      "ds_write_b96 v2, v[44:46] offset:5120\n"
      "v_cndmask_b32 v32, v32, v52, s[42:43]\n"
      "v_cndmask_b32 v32, v32, v76, s[44:45]\n"
      "v_cndmask_b32 v32, v32, v88, s[46:47]\n"
      "v_exp_f32 v32, v32\n"
      "s_nop 0\n"
      "v_cvt_pk_bf16_f32 v32, v32, v32\n"
      "ds_write_b16 v39, v32\n"
      "L_join_6_%=:\n"
      "s_waitcnt lgkmcnt(0)\n"
      "s_barrier\n"
      "s_add_u32 s27, s27, 1\n"
      "s_cmp_lt_u32 s27, s26\n"
      "s_cbranch_scc1 L_Aloop_%=\n"
      "L_Aend_%=:\n"
      "s_nop 7\n"
      "s_nop 7\n"
      "s_waitcnt lgkmcnt(0)\n"
      "s_endpgm\n"
      "L_gen_2_%=:\n"
      "s_waitcnt lgkmcnt(0)\n"
      "s_nop 7\n"
      "s_nop 7\n"
      "s_cmp_lg_u64 s[74:75], 0\n"
      "s_cbranch_scc1 L_gen_4_%=\n"
      "v_max3_f32 v65, v80, v81, v82\n"
      "v_max3_f32 v66, v83, v36, v37\n"
      "v_max3_f32 v65, v65, v38, v39\n"
      "v_max3_f32 v66, v66, v84, v85\n"
      "v_max3_f32 v65, v65, v86, v87\n"
      "v_max3_f32 v65, v65, v66, v64\n"
      "v_mov_b32 v66, v65\n"
      "s_nop 1\n"
      "v_permlane16_swap_b32 v65, v66\n"
      "v_max_f32 v65, v65, v66\n"
      "v_mov_b32_e64 v66, v65\n"
      "ds_read_b64 v[22:23], v3 offset:2048\n"
      "ds_read_b64 v[26:27], v3 offset:2560\n"
      "ds_read_b64 v[30:31], v3 offset:3072\n"
      "ds_read_b64 v[70:71], v3 offset:3584\n"
      "v_permlane32_swap_b32 v65, v66\n"
      "v_max_f32 v65, v65, v66\n"
      "v_sub_f32 v80, v80, v65\n"
      "v_sub_f32 v81, v81, v65\n"
      "v_exp_f32 v80, v80\n"
      "v_sub_f32 v82, v82, v65\n"
      "v_exp_f32 v81, v81\n"
      "v_sub_f32 v83, v83, v65\n"
      "v_exp_f32 v82, v82\n"
      "v_sub_f32 v36, v36, v65\n"
      "v_exp_f32 v83, v83\n"
      "v_sub_f32 v37, v37, v65\n"
      "s_setprio 2\n"
      "s_waitcnt lgkmcnt(0)\n"
      "v_mfma_f32_16x16x32_bf16 v[32:35], v[52:55], v[60:63], 0\n"
      "v_exp_f32 v36, v36\n"
      "v_sub_f32 v38, v38, v65\n"
      "v_mfma_f32_16x16x32_bf16 v[32:35], v[20:23], v[8:11], v[32:35]\n"
      "v_exp_f32 v37, v37\n"
      "v_sub_f32 v39, v39, v65\n"
      "v_mfma_f32_16x16x32_bf16 v[40:43], v[76:79], v[60:63], 0\n"
      "v_exp_f32 v38, v38\n"
      "v_sub_f32 v84, v84, v65\n"
      "v_mfma_f32_16x16x32_bf16 v[40:43], v[24:27], v[8:11], v[40:43]\n"
      "v_exp_f32 v39, v39\n"
      "v_sub_f32 v85, v85, v65\n"
      "v_mfma_f32_16x16x32_bf16 v[44:47], v[88:91], v[60:63], 0\n"
      "v_exp_f32 v84, v84\n"
      "v_sub_f32 v86, v86, v65\n"
      "v_mfma_f32_16x16x32_bf16 v[44:47], v[28:31], v[8:11], v[44:47]\n"
      "v_exp_f32 v85, v85\n"
      "v_sub_f32 v87, v87, v65\n"
      "v_mfma_f32_16x16x32_bf16 v[92:95], v[56:59], v[60:63], 0\n"
      "v_exp_f32 v86, v86\n"
      "v_sub_f32 v64, v64, v65\n"
      "v_mfma_f32_16x16x32_bf16 v[92:95], v[68:71], v[8:11], v[92:95]\n"
      "v_exp_f32 v87, v87\n"
      "v_exp_f32 v64, v64\n"
      "s_setprio 0\n"
      "v_cvt_pk_bf16_f32 v80, v80, v81\n"
      "v_cvt_pk_bf16_f32 v81, v82, v83\n"
      "v_cvt_pk_bf16_f32 v82, v36, v37\n"
      "s_nop 1\n"
      "v_max3_f32 v93, v32, v33, v34\n"
      "v_max3_f32 v94, v35, v40, v41\n"
      "v_max3_f32 v93, v93, v42, v43\n"
      "v_max3_f32 v94, v94, v44, v45\n"
      "v_max3_f32 v93, v93, v46, v47\n"
      "v_max3_f32 v93, v93, v94, v92\n"
      "v_mov_b32 v94, v93\n"
      "v_cvt_pk_bf16_f32 v83, v38, v39\n"
      "v_cvt_pk_bf16_f32 v36, v84, v85\n"
      "v_permlane16_swap_b32 v93, v94\n"
      "v_max_f32 v93, v93, v94\n"
      "v_mov_b32 v94, v93\n"
      "v_cvt_pk_bf16_f32 v37, v86, v87\n"
      "v_cvt_pk_bf16_f32 v38, v64, 0\n"
      "ds_write_b128 v2, v[80:83]\n"
      "ds_write_b96 v2, v[36:38] offset:1024\n"
      "ds_read_b64 v[22:23], v3 offset:4096\n"
      "ds_read_b64 v[26:27], v3 offset:4608\n"
      "ds_read_b64 v[30:31], v3 offset:5120\n"
      "ds_read_b64 v[70:71], v3 offset:5632\n"
      "v_permlane32_swap_b32 v93, v94\n"
      "v_max_f32 v93, v93, v94\n"
      "v_sub_f32 v32, v32, v93\n"
      "v_sub_f32 v33, v33, v93\n"
      "v_exp_f32 v32, v32\n"
      "v_sub_f32 v34, v34, v93\n"
      "v_exp_f32 v33, v33\n"
      "v_sub_f32 v35, v35, v93\n"
      "v_exp_f32 v34, v34\n"
      "v_sub_f32 v40, v40, v93\n"
      "v_exp_f32 v35, v35\n"
      "v_sub_f32 v41, v41, v93\n"
      "s_setprio 2\n"
      "s_waitcnt lgkmcnt(0)\n"
      "v_mfma_f32_16x16x32_bf16 v[80:83], v[52:55], v[48:51], 0\n"
      "v_exp_f32 v40, v40\n"
      "v_sub_f32 v42, v42, v93\n"
      "v_mfma_f32_16x16x32_bf16 v[80:83], v[20:23], v[12:15], v[80:83]\n"
      "v_exp_f32 v41, v41\n"
      "v_sub_f32 v43, v43, v93\n"
      "v_mfma_f32_16x16x32_bf16 v[36:39], v[76:79], v[48:51], 0\n"
      "v_exp_f32 v42, v42\n"
      "v_sub_f32 v44, v44, v93\n"
      "v_mfma_f32_16x16x32_bf16 v[36:39], v[24:27], v[12:15], v[36:39]\n"
      "v_exp_f32 v43, v43\n"
      "v_sub_f32 v45, v45, v93\n"
      "v_mfma_f32_16x16x32_bf16 v[84:87], v[88:91], v[48:51], 0\n"
      "v_exp_f32 v44, v44\n"
      "v_sub_f32 v46, v46, v93\n"
      "v_mfma_f32_16x16x32_bf16 v[84:87], v[28:31], v[12:15], v[84:87]\n"
      "v_exp_f32 v45, v45\n"
      "v_sub_f32 v47, v47, v93\n"
      "v_mfma_f32_16x16x32_bf16 v[60:63], v[56:59], v[48:51], 0\n"
      "v_exp_f32 v46, v46\n"
      "v_sub_f32 v92, v92, v93\n"
      "v_mfma_f32_16x16x32_bf16 v[60:63], v[68:71], v[12:15], v[60:63]\n"
      "v_exp_f32 v47, v47\n"
      "v_exp_f32 v92, v92\n"
      "s_setprio 0\n"
      "v_cvt_pk_bf16_f32 v32, v32, v33\n"
      "v_cvt_pk_bf16_f32 v33, v34, v35\n"
      "v_cvt_pk_bf16_f32 v34, v40, v41\n"
      "s_nop 1\n"
      "v_max3_f32 v61, v80, v81, v82\n"
      "v_max3_f32 v62, v83, v36, v37\n"
      "v_max3_f32 v61, v61, v38, v39\n"
      "v_max3_f32 v62, v62, v84, v85\n"
      "v_max3_f32 v61, v61, v86, v87\n"
      "v_max3_f32 v61, v61, v62, v60\n"
      "v_mov_b32_e64 v62, v61\n"
      "v_cvt_pk_bf16_f32 v35, v42, v43\n"
      "v_cvt_pk_bf16_f32 v40, v44, v45\n"
      "v_permlane16_swap_b32 v61, v62\n"
      "v_max_f32 v61, v61, v62\n"
      "v_mov_b32_e64 v62, v61\n"
      "v_cvt_pk_bf16_f32 v41, v46, v47\n"
      "v_cvt_pk_bf16_f32 v42, v92, 0\n"
      "ds_write_b128 v2, v[32:35] offset:2048\n"
      "ds_write_b96 v2, v[40:42] offset:3072\n"
      "ds_read_b128 v[32:35], v74\n"
      "ds_read_b64 v[40:41], v74 offset:16\n"
      "ds_read_b32 v44, v16 offset:768\n"
      "ds_read_b32 v74, v16 offset:512\n"
      "v_permlane32_swap_b32 v61, v62\n"
      "v_max_f32 v61, v61, v62\n"
      "v_sub_f32 v80, v80, v61\n"
      "v_sub_f32 v81, v81, v61\n"
      "v_exp_f32 v80, v80\n"
      "v_sub_f32 v82, v82, v61\n"
      "v_exp_f32 v81, v81\n"
      "v_sub_f32 v83, v83, v61\n"
      "v_exp_f32 v82, v82\n"
      "v_sub_f32 v36, v36, v61\n"
      "v_exp_f32 v83, v83\n"
      "v_sub_f32 v37, v37, v61\n"
      "s_setprio 2\n"
      "s_waitcnt lgkmcnt(0)\n"
      "v_mfma_f32_16x16x32_bf16 v[48:51], v[32:35], v[52:55], v[44:47]\n"
      "v_exp_f32 v36, v36\n"
      "v_sub_f32 v38, v38, v61\n"
      "v_mfma_f32_16x16x32_bf16 v[52:55], v[32:35], v[76:79], v[44:47]\n"
      "v_exp_f32 v37, v37\n"
      "v_sub_f32 v39, v39, v61\n"
      "v_mfma_f32_16x16x32_bf16 v[76:79], v[32:35], v[88:91], v[44:47]\n"
      "v_exp_f32 v38, v38\n"
      "v_sub_f32 v84, v84, v61\n"
      "v_mfma_f32_16x16x32_bf16 v[88:91], v[32:35], v[56:59], v[44:47]\n"
      "v_exp_f32 v39, v39\n"
      "v_sub_f32 v85, v85, v61\n"
      "v_mfma_f32_16x16x16_bf16 v[48:51], v[40:41], v[20:21], v[48:51]\n"
      "v_exp_f32 v84, v84\n"
      "v_sub_f32 v86, v86, v61\n"
      "v_mfma_f32_16x16x16_bf16 v[52:55], v[40:41], v[24:25], v[52:55]\n"
      "v_exp_f32 v85, v85\n"
      "v_sub_f32 v87, v87, v61\n"
      "v_mfma_f32_16x16x16_bf16 v[76:79], v[40:41], v[28:29], v[76:79]\n"
      "v_exp_f32 v86, v86\n"
      "v_sub_f32 v60, v60, v61\n"
      "v_mfma_f32_16x16x16_bf16 v[88:91], v[40:41], v[68:69], v[88:91]\n"
      "v_exp_f32 v87, v87\n"
      "v_exp_f32_e64 v60, v60\n"
      "s_setprio 0\n"
      "v_cndmask_b32 v48, v48, v52, s[42:43]\n"
      "s_nop 0\n"
      "v_cndmask_b32 v48, v48, v76, s[44:45]\n"
      "s_nop 1\n"
      "v_cndmask_b32 v48, v48, v88, s[46:47]\n"
      "v_cvt_pk_bf16_f32 v80, v80, v81\n"
      "v_cvt_pk_bf16_f32 v81, v82, v83\n"
      "v_max_f32_dpp v88, v48, v48 row_ror:8 row_mask:0xf bank_mask:0xf\n"
      "v_cvt_pk_bf16_f32 v82, v36, v37\n"
      "v_cvt_pk_bf16_f32 v83, v38, v39\n"
      "v_max_f32_dpp v88, v88, v88 row_ror:4 row_mask:0xf bank_mask:0xf\n"
      "v_cvt_pk_bf16_f32 v36, v84, v85\n"
      "v_cvt_pk_bf16_f32 v37, v86, v87\n"
      "v_max_f32_dpp v88, v88, v88 row_ror:2 row_mask:0xf bank_mask:0xf\n"
      "v_cvt_pk_bf16_f32 v38, v60, 0\n"
      "s_nop 0\n"
      "v_max_f32_dpp v88, v88, v88 row_ror:1 row_mask:0xf bank_mask:0xf\n"
      "s_nop 1\n"
      "v_max_f32_dpp v88, v88, v88 row_bcast:15 row_mask:0xa bank_mask:0xf\n"
      "s_nop 1\n"
      "v_max_f32_dpp v88, v88, v88 row_bcast:31 row_mask:0xc bank_mask:0xf\n"
      "s_nop 0\n"
      "v_readlane_b32 s30, v88, 63\n"
      "s_nop 1\n"
      "v_subrev_f32 v48, s30, v48\n"
      "v_exp_f32 v48, v48\n"
      "s_nop 0\n"
      "v_cvt_pk_bf16_f32 v48, v48, v48\n"
      "ds_write_b128 v2, v[80:83] offset:4096\n"
      "ds_write_b96 v2, v[36:38] offset:5120\n"
      "ds_write_b16 v74, v48\n"
      "s_branch L_join_3_%=\n"
      "L_gen_4_%=:\n"
      "s_waitcnt lgkmcnt(0)\n"
      "s_nop 7\n"
      "s_nop 7\n"
      "s_lshl_b32 s29, s27, 8\n"
      "s_add_u32 s29, s29, s25\n"
      "s_lshl_b32 s29, s29, 1\n"
      "s_add_u32 s29, s29, 0\n"
      "s_add_u32 s30, s25, 5120\n"
      "s_cmp_eq_u32 s27, 10\n"
      "s_cselect_b32 s29, s30, s29\n"
      "s_mul_i32 s29, s29, 4802\n"
      "s_add_u32 s29, s29, s18\n"
      "s_and_b32 s29, s29, 15\n"
      "s_and_b32 s30, s27, 1\n"
      "s_mul_i32 s30, s30, 9728\n"
      "s_add_u32 s29, s29, s30\n"
      "s_add_u32 s29, s29, 123904\n"
      "v_bfe_u32 v65, v3, 3, 4\n"
      "v_mul_u32_u24 v65, 0x62, v65\n"
      "v_add_u32_e64 v65, s29, v65\n"
      "v_bfe_u32 v66, v3, 7, 2\n"
      "v_lshl_add_u32 v66, v66, 3, v65\n"
      "ds_read_u16 v67, v66\n"
      "ds_read_u16 v18, v66 offset:2\n"
      "ds_read_u16 v19, v66 offset:4\n"
      "ds_read_u16 v32, v66 offset:6\n"
      "ds_read_u16 v33, v66 offset:32\n"
      "ds_read_u16 v34, v66 offset:34\n"
      "ds_read_u16 v35, v66 offset:36\n"
      "ds_read_u16 v40, v66 offset:38\n"
      "ds_read_u16 v41, v66 offset:64\n"
      "ds_read_u16 v42, v66 offset:66\n"
      "ds_read_u16 v43, v66 offset:68\n"
      "ds_read_u16 v44, v66 offset:70\n"
      "ds_read_u16 v45, v65 offset:96\n"
      "s_waitcnt lgkmcnt(12)\n"
      "v_lshlrev_b32 v67, 16, v67\n"
      "v_fmac_f32 v80, s41, v67\n"
      "s_waitcnt lgkmcnt(11)\n"
      "v_lshlrev_b32 v18, 16, v18\n"
      "v_fmac_f32 v81, s41, v18\n"
      "s_waitcnt lgkmcnt(10)\n"
      "v_lshlrev_b32 v19, 16, v19\n"
      "v_fmac_f32 v82, s41, v19\n"
      "s_waitcnt lgkmcnt(9)\n"
      "v_lshlrev_b32 v32, 16, v32\n"
      "v_fmac_f32 v83, s41, v32\n"
      "s_waitcnt lgkmcnt(7)\n"
      "v_lshlrev_b32 v33, 16, v33\n"
      "v_fmac_f32 v36, s41, v33\n"
      "v_lshlrev_b32 v34, 16, v34\n"
      "v_fmac_f32 v37, s41, v34\n"
      "s_waitcnt lgkmcnt(3)\n"
      "v_lshlrev_b32 v35, 16, v35\n"
      "v_fmac_f32 v38, s41, v35\n"
      "v_lshlrev_b32 v40, 16, v40\n"
      "v_fmac_f32 v39, s41, v40\n"
      "v_lshlrev_b32 v41, 16, v41\n"
      "v_fmac_f32 v84, s41, v41\n"
      "v_lshlrev_b32 v42, 16, v42\n"
      "v_fmac_f32 v85, s41, v42\n"
      "s_waitcnt lgkmcnt(0)\n"
      "v_lshlrev_b32 v43, 16, v43\n"
      "v_fmac_f32 v86, s41, v43\n"
      "v_lshlrev_b32 v44, 16, v44\n"
      "v_fmac_f32 v87, s41, v44\n"
      "v_lshlrev_b32 v45, 16, v45\n"
      "v_fmac_f32_e64 v64, s41, v45\n"
      "v_max3_f32 v45, v80, v81, v82\n"
      "v_max3_f32 v44, v83, v36, v37\n"
      "v_max3_f32 v45, v45, v38, v39\n"
      "v_max3_f32 v44, v44, v84, v85\n"
      "v_max3_f32 v45, v45, v86, v87\n"
      "v_max3_f32 v45, v45, v44, v64\n"
      "v_mov_b32 v44, v45\n"
      "s_nop 1\n"
      "v_permlane16_swap_b32 v45, v44\n"
      "v_max_f32 v45, v45, v44\n"
      "v_mov_b32_e64 v44, v45\n"
      "ds_read_b64 v[22:23], v3 offset:2048\n"
      "ds_read_b64 v[26:27], v3 offset:2560\n"
      "ds_read_b64 v[30:31], v3 offset:3072\n"
      "ds_read_b64 v[70:71], v3 offset:3584\n"
      "v_permlane32_swap_b32 v45, v44\n"
      "v_max_f32 v45, v45, v44\n"
      "v_sub_f32 v80, v80, v45\n"
      "v_sub_f32 v81, v81, v45\n"
      "v_exp_f32 v80, v80\n"
      "v_sub_f32 v82, v82, v45\n"
      "v_exp_f32 v81, v81\n"
      "v_sub_f32 v83, v83, v45\n"
      "v_exp_f32 v82, v82\n"
      "v_sub_f32 v36, v36, v45\n"
      "v_exp_f32 v83, v83\n"
      "v_sub_f32 v37, v37, v45\n"
      "s_setprio 2\n"
      "s_waitcnt lgkmcnt(0)\n"
      "v_mfma_f32_16x16x32_bf16 v[40:43], v[52:55], v[60:63], 0\n"
      "v_exp_f32 v36, v36\n"
      "v_sub_f32 v38, v38, v45\n"
      "v_mfma_f32_16x16x32_bf16 v[40:43], v[20:23], v[8:11], v[40:43]\n"
      "v_exp_f32 v37, v37\n"
      "v_sub_f32 v39, v39, v45\n"
      "v_mfma_f32_16x16x32_bf16 v[32:35], v[76:79], v[60:63], 0\n"
      "v_exp_f32 v38, v38\n"
      "v_sub_f32 v84, v84, v45\n"
      "v_mfma_f32_16x16x32_bf16 v[32:35], v[24:27], v[8:11], v[32:35]\n"
      "v_exp_f32 v39, v39\n"
      "v_sub_f32 v85, v85, v45\n"
      "v_mfma_f32_16x16x32_bf16 v[92:95], v[88:91], v[60:63], 0\n"
      "v_exp_f32 v84, v84\n"
      "v_sub_f32 v86, v86, v45\n"
      "v_mfma_f32_16x16x32_bf16 v[92:95], v[28:31], v[8:11], v[92:95]\n"
      "v_exp_f32 v85, v85\n"
      "v_sub_f32 v87, v87, v45\n"
      "v_mfma_f32_16x16x32_bf16 v[96:99], v[56:59], v[60:63], 0\n"
      "v_exp_f32 v86, v86\n"
      "v_sub_f32 v64, v64, v45\n"
      "v_mfma_f32_16x16x32_bf16 v[96:99], v[68:71], v[8:11], v[96:99]\n"
      "v_exp_f32 v87, v87\n"
      "v_exp_f32 v64, v64\n"
      "s_setprio 0\n"
      "s_nop 4\n"
      "ds_read_u16 v97, v66 offset:1568\n"
      "ds_read_u16 v98, v66 offset:1570\n"
      "ds_read_u16 v99, v66 offset:1572\n"
      "ds_read_u16 v45, v66 offset:1574\n"
      "ds_read_u16 v60, v66 offset:1600\n"
      "ds_read_u16 v61, v66 offset:1602\n"
      "ds_read_u16 v62, v66 offset:1604\n"
      "ds_read_u16 v63, v66 offset:1606\n"
      "ds_read_u16 v44, v66 offset:1632\n"
      "ds_read_u16 v19, v66 offset:1634\n"
      "ds_read_u16 v18, v66 offset:1636\n"
      "ds_read_u16 v67, v66 offset:1638\n"
      "ds_read_u16 v46, v65 offset:1664\n"
      "s_waitcnt lgkmcnt(12)\n"
      "v_lshlrev_b32 v97, 16, v97\n"
      "v_fmac_f32 v40, s41, v97\n"
      "s_waitcnt lgkmcnt(11)\n"
      "v_lshlrev_b32 v98, 16, v98\n"
      "v_fmac_f32 v41, s41, v98\n"
      "s_waitcnt lgkmcnt(10)\n"
      "v_lshlrev_b32 v99, 16, v99\n"
      "v_fmac_f32 v42, s41, v99\n"
      "s_waitcnt lgkmcnt(9)\n"
      "v_lshlrev_b32 v45, 16, v45\n"
      "v_fmac_f32 v43, s41, v45\n"
      "s_waitcnt lgkmcnt(7)\n"
      "v_lshlrev_b32 v60, 16, v60\n"
      "v_fmac_f32 v32, s41, v60\n"
      "v_lshlrev_b32 v61, 16, v61\n"
      "v_fmac_f32 v33, s41, v61\n"
      "s_waitcnt lgkmcnt(3)\n"
      "v_lshlrev_b32 v62, 16, v62\n"
      "v_fmac_f32 v34, s41, v62\n"
      "v_lshlrev_b32 v63, 16, v63\n"
      "v_fmac_f32 v35, s41, v63\n"
      "v_lshlrev_b32 v44, 16, v44\n"
      "v_fmac_f32 v92, s41, v44\n"
      "v_lshlrev_b32 v19, 16, v19\n"
      "v_fmac_f32 v93, s41, v19\n"
      "s_waitcnt lgkmcnt(0)\n"
      "v_lshlrev_b32 v18, 16, v18\n"
      "v_fmac_f32 v94, s41, v18\n"
      "v_lshlrev_b32 v67, 16, v67\n"
      "v_fmac_f32 v95, s41, v67\n"
      "v_lshlrev_b32 v46, 16, v46\n"
      "v_fmac_f32_e64 v96, s41, v46\n"
      "v_cvt_pk_bf16_f32 v80, v80, v81\n"
      "v_cvt_pk_bf16_f32 v81, v82, v83\n"
      "v_cvt_pk_bf16_f32 v82, v36, v37\n"
      "v_max3_f32 v46, v40, v41, v42\n"
      "v_max3_f32 v67, v43, v32, v33\n"
      "v_max3_f32 v46, v46, v34, v35\n"
      "v_max3_f32 v67, v67, v92, v93\n"
      "v_max3_f32 v46, v46, v94, v95\n"
      "v_max3_f32 v46, v46, v67, v96\n"
      "v_mov_b32_e64 v67, v46\n"
      "v_cvt_pk_bf16_f32 v83, v38, v39\n"
      "v_cvt_pk_bf16_f32 v36, v84, v85\n"
      "v_permlane16_swap_b32 v46, v67\n"
      "v_max_f32 v46, v46, v67\n"
      "v_mov_b32_e64 v67, v46\n"
      "v_cvt_pk_bf16_f32 v37, v86, v87\n"
      "v_cvt_pk_bf16_f32 v38, v64, 0\n"
      "ds_write_b128 v2, v[80:83]\n"
      "ds_write_b96 v2, v[36:38] offset:1024\n"
      "ds_read_b64 v[22:23], v3 offset:4096\n"
      "ds_read_b64 v[26:27], v3 offset:4608\n"
      "ds_read_b64 v[30:31], v3 offset:5120\n"
      "ds_read_b64 v[70:71], v3 offset:5632\n"
      "v_permlane32_swap_b32 v46, v67\n"
      "v_max_f32 v46, v46, v67\n"
      "v_sub_f32 v40, v40, v46\n"
      "v_sub_f32 v41, v41, v46\n"
      "v_exp_f32 v40, v40\n"
      "v_sub_f32 v42, v42, v46\n"
      "v_exp_f32 v41, v41\n"
      "v_sub_f32 v43, v43, v46\n"
      "v_exp_f32 v42, v42\n"
      "v_sub_f32 v32, v32, v46\n"
      "v_exp_f32 v43, v43\n"
      "v_sub_f32 v33, v33, v46\n"
      "s_setprio 2\n"
      "s_waitcnt lgkmcnt(0)\n"
      "v_mfma_f32_16x16x32_bf16 v[80:83], v[52:55], v[48:51], 0\n"
      "v_exp_f32 v32, v32\n"
      "v_sub_f32 v34, v34, v46\n"
      "v_mfma_f32_16x16x32_bf16 v[80:83], v[20:23], v[12:15], v[80:83]\n"
      "v_exp_f32 v33, v33\n"
      "v_sub_f32 v35, v35, v46\n"
      "v_mfma_f32_16x16x32_bf16 v[36:39], v[76:79], v[48:51], 0\n"
      "v_exp_f32 v34, v34\n"
      "v_sub_f32 v92, v92, v46\n"
      "v_mfma_f32_16x16x32_bf16 v[36:39], v[24:27], v[12:15], v[36:39]\n"
      "v_exp_f32 v35, v35\n"
      "v_sub_f32 v93, v93, v46\n"
      "v_mfma_f32_16x16x32_bf16 v[84:87], v[88:91], v[48:51], 0\n"
      "v_exp_f32 v92, v92\n"
      "v_sub_f32 v94, v94, v46\n"
      "v_mfma_f32_16x16x32_bf16 v[84:87], v[28:31], v[12:15], v[84:87]\n"
      "v_exp_f32 v93, v93\n"
      "v_sub_f32 v95, v95, v46\n"
      "v_mfma_f32_16x16x32_bf16 v[60:63], v[56:59], v[48:51], 0\n"
      "v_exp_f32 v94, v94\n"
      "v_sub_f32 v96, v96, v46\n"
      "v_mfma_f32_16x16x32_bf16 v[60:63], v[68:71], v[12:15], v[60:63]\n"
      "v_exp_f32 v95, v95\n"
      "v_exp_f32 v96, v96\n"
      "s_setprio 0\n"
      "s_nop 4\n"
      "ds_read_u16 v61, v66 offset:3136\n"
      "ds_read_u16 v62, v66 offset:3138\n"
      "ds_read_u16 v63, v66 offset:3140\n"
      "ds_read_u16 v70, v66 offset:3142\n"
      "ds_read_u16 v71, v66 offset:3168\n"
      "ds_read_u16 v46, v66 offset:3170\n"
      "ds_read_u16 v48, v66 offset:3172\n"
      "ds_read_u16 v49, v66 offset:3174\n"
      "ds_read_u16 v50, v66 offset:3200\n"
      "ds_read_u16 v51, v66 offset:3202\n"
      "ds_read_u16 v30, v66 offset:3204\n"
      "ds_read_u16 v31, v66 offset:3206\n"
      "ds_read_u16 v26, v65 offset:3232\n"
      "s_waitcnt lgkmcnt(12)\n"
      "v_lshlrev_b32 v61, 16, v61\n"
      "v_fmac_f32 v80, s41, v61\n"
      "s_waitcnt lgkmcnt(11)\n"
      "v_lshlrev_b32 v62, 16, v62\n"
      "v_fmac_f32 v81, s41, v62\n"
      "s_waitcnt lgkmcnt(10)\n"
      "v_lshlrev_b32 v63, 16, v63\n"
      "v_fmac_f32 v82, s41, v63\n"
      "s_waitcnt lgkmcnt(9)\n"
      "v_lshlrev_b32 v70, 16, v70\n"
      "v_fmac_f32 v83, s41, v70\n"
      "s_waitcnt lgkmcnt(7)\n"
      "v_lshlrev_b32 v71, 16, v71\n"
      "v_fmac_f32 v36, s41, v71\n"
      "v_lshlrev_b32 v46, 16, v46\n"
      "v_fmac_f32 v37, s41, v46\n"
      "s_waitcnt lgkmcnt(3)\n"
      "v_lshlrev_b32 v48, 16, v48\n"
      "v_fmac_f32 v38, s41, v48\n"
      "v_lshlrev_b32 v49, 16, v49\n"
      "v_fmac_f32 v39, s41, v49\n"
      "v_lshlrev_b32 v50, 16, v50\n"
      "v_fmac_f32 v84, s41, v50\n"
      "v_lshlrev_b32 v51, 16, v51\n"
      "v_fmac_f32 v85, s41, v51\n"
      "s_waitcnt lgkmcnt(0)\n"
      "v_lshlrev_b32 v30, 16, v30\n"
      "v_fmac_f32 v86, s41, v30\n"
      "v_lshlrev_b32 v31, 16, v31\n"
      "v_fmac_f32 v87, s41, v31\n"
      "v_lshlrev_b32 v26, 16, v26\n"
      "v_fmac_f32_e64 v60, s41, v26\n"
      "v_cvt_pk_bf16_f32 v40, v40, v41\n"
      "v_cvt_pk_bf16_f32 v41, v42, v43\n"
      "v_cvt_pk_bf16_f32 v42, v32, v33\n"
      "v_max3_f32 v26, v80, v81, v82\n"
      "v_max3_f32 v31, v83, v36, v37\n"
      "v_max3_f32 v26, v26, v38, v39\n"
      "v_max3_f32 v31, v31, v84, v85\n"
      "v_max3_f32 v26, v26, v86, v87\n"
      "v_max3_f32 v26, v26, v31, v60\n"
      "v_mov_b32_e64 v31, v26\n"
      "v_cvt_pk_bf16_f32 v43, v34, v35\n"
      "v_cvt_pk_bf16_f32 v32, v92, v93\n"
      "v_permlane16_swap_b32 v26, v31\n"
      "v_max_f32 v26, v26, v31\n"
      "v_mov_b32_e64 v31, v26\n"
      "v_cvt_pk_bf16_f32 v33, v94, v95\n"
      "v_cvt_pk_bf16_f32 v34, v96, 0\n"
      "ds_write_b128 v2, v[40:43] offset:2048\n"
      "ds_write_b96 v2, v[32:34] offset:3072\n"
      "ds_read_b128 v[40:43], v74\n"
      "ds_read_b64 v[32:33], v74 offset:16\n"
      "ds_read_b32 v92, v16 offset:768\n"
      "ds_read_b32 v74, v16 offset:512\n"
      "v_permlane32_swap_b32 v26, v31\n"
      "v_max_f32 v26, v26, v31\n"
      "v_sub_f32 v80, v80, v26\n"
      "v_sub_f32 v81, v81, v26\n"
      "v_exp_f32 v80, v80\n"
      "v_sub_f32 v82, v82, v26\n"
      "v_exp_f32 v81, v81\n"
      "v_sub_f32 v83, v83, v26\n"
      "v_exp_f32 v82, v82\n"
      "v_sub_f32 v36, v36, v26\n"
      "v_exp_f32 v83, v83\n"
      "v_sub_f32 v37, v37, v26\n"
      "s_setprio 2\n"
      "s_waitcnt lgkmcnt(0)\n"
      "v_mfma_f32_16x16x32_bf16 v[48:51], v[40:43], v[52:55], v[92:95]\n"
      "v_exp_f32 v36, v36\n"
      "v_sub_f32 v38, v38, v26\n"
      "v_mfma_f32_16x16x32_bf16 v[52:55], v[40:43], v[76:79], v[92:95]\n"
      "v_exp_f32 v37, v37\n"
      "v_sub_f32 v39, v39, v26\n"
      "v_mfma_f32_16x16x32_bf16 v[76:79], v[40:43], v[88:91], v[92:95]\n"
      "v_exp_f32 v38, v38\n"
      "v_sub_f32 v84, v84, v26\n"
      "v_mfma_f32_16x16x32_bf16 v[88:91], v[40:43], v[56:59], v[92:95]\n"
      "v_exp_f32 v39, v39\n"
      "v_sub_f32 v85, v85, v26\n"
      "v_mfma_f32_16x16x16_bf16 v[48:51], v[32:33], v[20:21], v[48:51]\n"
      "v_exp_f32 v84, v84\n"
      "v_sub_f32 v86, v86, v26\n"
      "v_mfma_f32_16x16x16_bf16 v[52:55], v[32:33], v[24:25], v[52:55]\n"
      "v_exp_f32 v85, v85\n"
      "v_sub_f32 v87, v87, v26\n"
      "v_mfma_f32_16x16x16_bf16 v[76:79], v[32:33], v[28:29], v[76:79]\n"
      "v_exp_f32 v86, v86\n"
      "v_sub_f32 v60, v60, v26\n"
      "v_mfma_f32_16x16x16_bf16 v[88:91], v[32:33], v[68:69], v[88:91]\n"
      "v_exp_f32 v87, v87\n"
      "v_exp_f32_e64 v60, v60\n"
      "s_setprio 0\n"
      "v_cndmask_b32 v48, v48, v52, s[42:43]\n"
      "s_nop 0\n"
      "v_cndmask_b32 v48, v48, v76, s[44:45]\n"
      "s_nop 1\n"
      "v_cndmask_b32 v48, v48, v88, s[46:47]\n"
      "v_bfe_u32 v66, v3, 3, 6\n"
      "v_min_u32_e64 v66, 48, v66\n"
      "v_lshlrev_b32_e64 v66, 1, v66\n"
      "v_add_u32_e64 v66, s29, v66\n"
      "ds_read_u16 v88, v66 offset:4704\n"
      "s_waitcnt lgkmcnt(0)\n"
      "v_lshlrev_b32 v88, 16, v88\n"
      "v_fmac_f32_e64 v48, s41, v88\n"
      "v_cvt_pk_bf16_f32 v80, v80, v81\n"
      "v_cvt_pk_bf16_f32 v81, v82, v83\n"
      "v_max_f32_dpp v88, v48, v48 row_ror:8 row_mask:0xf bank_mask:0xf\n"
      "v_cvt_pk_bf16_f32 v82, v36, v37\n"
      "v_cvt_pk_bf16_f32 v83, v38, v39\n"
      "v_max_f32_dpp v88, v88, v88 row_ror:4 row_mask:0xf bank_mask:0xf\n"
      "v_cvt_pk_bf16_f32 v36, v84, v85\n"
      "v_cvt_pk_bf16_f32 v37, v86, v87\n"
      "v_max_f32_dpp v88, v88, v88 row_ror:2 row_mask:0xf bank_mask:0xf\n"
      "v_cvt_pk_bf16_f32 v38, v60, 0\n"
      "s_nop 0\n"
      "v_max_f32_dpp v88, v88, v88 row_ror:1 row_mask:0xf bank_mask:0xf\n"
      "s_nop 1\n"
      "v_max_f32_dpp v88, v88, v88 row_bcast:15 row_mask:0xa bank_mask:0xf\n"
      "s_nop 1\n"
      "v_max_f32_dpp v88, v88, v88 row_bcast:31 row_mask:0xc bank_mask:0xf\n"
      "s_nop 0\n"
      "v_readlane_b32 s30, v88, 63\n"
      "s_nop 1\n"
      "v_subrev_f32 v48, s30, v48\n"
      "v_exp_f32 v48, v48\n"
      "s_nop 0\n"
      "v_cvt_pk_bf16_f32 v48, v48, v48\n"
      "ds_write_b128 v2, v[80:83] offset:4096\n"
      "ds_write_b96 v2, v[36:38] offset:5120\n"
      "ds_write_b16 v74, v48\n"
      "s_branch L_join_3_%=\n"
      "L_gen_5_%=:\n"
      "s_waitcnt lgkmcnt(0)\n"
      "s_nop 7\n"
      "s_nop 7\n"
      "s_cmp_lg_u64 s[74:75], 0\n"
      "s_cbranch_scc1 L_gen_7_%=\n"
      "v_max3_f32 v65, v80, v81, v82\n"
      "v_max3_f32 v66, v83, v36, v37\n"
      "v_max3_f32 v65, v65, v38, v39\n"
      "v_max3_f32 v66, v66, v84, v85\n"
      "v_max3_f32 v65, v65, v86, v87\n"
      "v_max3_f32 v65, v65, v66, v64\n"
      "v_mov_b32 v66, v65\n"
      "s_nop 1\n"
      "v_permlane16_swap_b32 v65, v66\n"
      "v_max_f32 v65, v65, v66\n"
      "v_mov_b32_e64 v66, v65\n"
      "ds_read_b64 v[22:23], v3 offset:2048\n"
      "ds_read_b64 v[26:27], v3 offset:2560\n"
      "ds_read_b64 v[30:31], v3 offset:3072\n"
      "ds_read_b64 v[70:71], v3 offset:3584\n"
      "v_permlane32_swap_b32 v65, v66\n"
      "v_max_f32 v65, v65, v66\n"
      "v_sub_f32 v80, v80, v65\n"
      "v_sub_f32 v81, v81, v65\n"
      "v_exp_f32 v80, v80\n"
      "v_sub_f32 v82, v82, v65\n"
      "v_exp_f32 v81, v81\n"
      "v_sub_f32 v83, v83, v65\n"
      "v_exp_f32 v82, v82\n"
      "v_sub_f32 v36, v36, v65\n"
      "v_exp_f32 v83, v83\n"
      "v_sub_f32 v37, v37, v65\n"
      "s_setprio 2\n"
      "s_waitcnt lgkmcnt(0)\n"
      "v_mfma_f32_16x16x32_bf16 v[32:35], v[52:55], v[60:63], 0\n"
      "v_exp_f32 v36, v36\n"
      "v_sub_f32 v38, v38, v65\n"
      "v_mfma_f32_16x16x32_bf16 v[32:35], v[20:23], v[8:11], v[32:35]\n"
      "v_exp_f32 v37, v37\n"
      "v_sub_f32 v39, v39, v65\n"
      "v_mfma_f32_16x16x32_bf16 v[40:43], v[76:79], v[60:63], 0\n"
      "v_exp_f32 v38, v38\n"
      "v_sub_f32 v84, v84, v65\n"
      "v_mfma_f32_16x16x32_bf16 v[40:43], v[24:27], v[8:11], v[40:43]\n"
      "v_exp_f32 v39, v39\n"
      "v_sub_f32 v85, v85, v65\n"
      "v_mfma_f32_16x16x32_bf16 v[44:47], v[88:91], v[60:63], 0\n"
      "v_exp_f32 v84, v84\n"
      "v_sub_f32 v86, v86, v65\n"
      "v_mfma_f32_16x16x32_bf16 v[44:47], v[28:31], v[8:11], v[44:47]\n"
      "v_exp_f32 v85, v85\n"
      "v_sub_f32 v87, v87, v65\n"
      "v_mfma_f32_16x16x32_bf16 v[92:95], v[56:59], v[60:63], 0\n"
      "v_exp_f32 v86, v86\n"
      "v_sub_f32 v64, v64, v65\n"
      "v_mfma_f32_16x16x32_bf16 v[92:95], v[68:71], v[8:11], v[92:95]\n"
      "v_exp_f32 v87, v87\n"
      "v_exp_f32 v64, v64\n"
      "s_setprio 0\n"
      "v_cvt_pk_bf16_f32 v80, v80, v81\n"
      "v_cvt_pk_bf16_f32 v81, v82, v83\n"
      "v_cvt_pk_bf16_f32 v82, v36, v37\n"
      "s_nop 1\n"
      "v_max3_f32 v93, v32, v33, v34\n"
      "v_max3_f32 v94, v35, v40, v41\n"
      "v_max3_f32 v93, v93, v42, v43\n"
      "v_max3_f32 v94, v94, v44, v45\n"
      "v_max3_f32 v93, v93, v46, v47\n"
      "v_max3_f32 v93, v93, v94, v92\n"
      "v_mov_b32_e64 v94, v93\n"
      "v_cvt_pk_bf16_f32 v83, v38, v39\n"
      "v_cvt_pk_bf16_f32 v36, v84, v85\n"
      "v_permlane16_swap_b32 v93, v94\n"
      "v_max_f32 v93, v93, v94\n"
      "v_mov_b32_e64 v94, v93\n"
      "v_cvt_pk_bf16_f32 v37, v86, v87\n"
      "v_cvt_pk_bf16_f32 v38, v64, 0\n"
      "ds_write_b128 v2, v[80:83]\n"
      "ds_write_b96 v2, v[36:38] offset:1024\n"
      "ds_read_b64 v[22:23], v3 offset:4096\n"
      "ds_read_b64 v[26:27], v3 offset:4608\n"
      "ds_read_b64 v[30:31], v3 offset:5120\n"
      "ds_read_b64 v[70:71], v3 offset:5632\n"
      "v_permlane32_swap_b32 v93, v94\n"
      "v_max_f32 v93, v93, v94\n"
      "v_sub_f32 v32, v32, v93\n"
      "v_sub_f32 v33, v33, v93\n"
      "v_exp_f32 v32, v32\n"
      "v_sub_f32 v34, v34, v93\n"
      "v_exp_f32 v33, v33\n"
      "v_sub_f32 v35, v35, v93\n"
      "v_exp_f32 v34, v34\n"
      "v_sub_f32 v40, v40, v93\n"
      "v_exp_f32 v35, v35\n"
      "v_sub_f32 v41, v41, v93\n"
      "s_setprio 2\n"
      "s_waitcnt lgkmcnt(0)\n"
      "v_mfma_f32_16x16x32_bf16 v[80:83], v[52:55], v[48:51], 0\n"
      "v_exp_f32 v40, v40\n"
      "v_sub_f32 v42, v42, v93\n"
      "v_mfma_f32_16x16x32_bf16 v[80:83], v[20:23], v[12:15], v[80:83]\n"
      "v_exp_f32 v41, v41\n"
      "v_sub_f32 v43, v43, v93\n"
      "v_mfma_f32_16x16x32_bf16 v[36:39], v[76:79], v[48:51], 0\n"
      "v_exp_f32 v42, v42\n"
      "v_sub_f32 v44, v44, v93\n"
      "v_mfma_f32_16x16x32_bf16 v[36:39], v[24:27], v[12:15], v[36:39]\n"
      "v_exp_f32 v43, v43\n"
      "v_sub_f32 v45, v45, v93\n"
      "v_mfma_f32_16x16x32_bf16 v[84:87], v[88:91], v[48:51], 0\n"
      "v_exp_f32 v44, v44\n"
      "v_sub_f32 v46, v46, v93\n"
      "v_mfma_f32_16x16x32_bf16 v[84:87], v[28:31], v[12:15], v[84:87]\n"
      "v_exp_f32 v45, v45\n"
      "v_sub_f32 v47, v47, v93\n"
      "v_mfma_f32_16x16x32_bf16 v[60:63], v[56:59], v[48:51], 0\n"
      "v_exp_f32 v46, v46\n"
      "v_sub_f32 v92, v92, v93\n"
      "v_mfma_f32_16x16x32_bf16 v[60:63], v[68:71], v[12:15], v[60:63]\n"
      "v_exp_f32 v47, v47\n"
      "v_exp_f32 v92, v92\n"
      "s_setprio 0\n"
      "v_cvt_pk_bf16_f32 v32, v32, v33\n"
      "v_cvt_pk_bf16_f32 v33, v34, v35\n"
      "v_cvt_pk_bf16_f32 v34, v40, v41\n"
      "s_nop 1\n"
      "v_max3_f32 v61, v80, v81, v82\n"
      "v_max3_f32 v62, v83, v36, v37\n"
      "v_max3_f32 v61, v61, v38, v39\n"
      "v_max3_f32 v62, v62, v84, v85\n"
      "v_max3_f32 v61, v61, v86, v87\n"
      "v_max3_f32 v61, v61, v62, v60\n"
      "v_mov_b32 v62, v61\n"
      "v_cvt_pk_bf16_f32 v35, v42, v43\n"
      "v_cvt_pk_bf16_f32 v40, v44, v45\n"
      "v_permlane16_swap_b32 v61, v62\n"
      "v_max_f32 v61, v61, v62\n"
      "v_mov_b32 v62, v61\n"
      "v_cvt_pk_bf16_f32 v41, v46, v47\n"
      "v_cvt_pk_bf16_f32 v42, v92, 0\n"
      "ds_write_b128 v2, v[32:35] offset:2048\n"
      "ds_write_b96 v2, v[40:42] offset:3072\n"
      "ds_read_b128 v[32:35], v74 offset:1152\n"
      "ds_read_b64 v[40:41], v74 offset:1168\n"
      "ds_read_b32 v44, v16 offset:768\n"
      "ds_read_b32 v74, v16 offset:512\n"
      "v_permlane32_swap_b32 v61, v62\n"
      "v_max_f32 v61, v61, v62\n"
      "v_sub_f32 v80, v80, v61\n"
      "v_sub_f32 v81, v81, v61\n"
      "v_exp_f32 v80, v80\n"
      "v_sub_f32 v82, v82, v61\n"
      "v_exp_f32 v81, v81\n"
      "v_sub_f32 v83, v83, v61\n"
      "v_exp_f32 v82, v82\n"
      "v_sub_f32 v36, v36, v61\n"
      "v_exp_f32 v83, v83\n"
      "v_sub_f32 v37, v37, v61\n"
      "s_setprio 2\n"
      "s_waitcnt lgkmcnt(0)\n"
      "v_mfma_f32_16x16x32_bf16 v[48:51], v[32:35], v[52:55], v[44:47]\n"
      "v_exp_f32 v36, v36\n"
      "v_sub_f32 v38, v38, v61\n"
      "v_mfma_f32_16x16x32_bf16 v[52:55], v[32:35], v[76:79], v[44:47]\n"
      "v_exp_f32 v37, v37\n"
      "v_sub_f32 v39, v39, v61\n"
      "v_mfma_f32_16x16x32_bf16 v[76:79], v[32:35], v[88:91], v[44:47]\n"
      "v_exp_f32 v38, v38\n"
      "v_sub_f32 v84, v84, v61\n"
      "v_mfma_f32_16x16x32_bf16 v[88:91], v[32:35], v[56:59], v[44:47]\n"
      "v_exp_f32 v39, v39\n"
      "v_sub_f32 v85, v85, v61\n"
      "v_mfma_f32_16x16x16_bf16 v[48:51], v[40:41], v[20:21], v[48:51]\n"
      "v_exp_f32 v84, v84\n"
      "v_sub_f32 v86, v86, v61\n"
      "v_mfma_f32_16x16x16_bf16 v[52:55], v[40:41], v[24:25], v[52:55]\n"
      "v_exp_f32 v85, v85\n"
      "v_sub_f32 v87, v87, v61\n"
      "v_mfma_f32_16x16x16_bf16 v[76:79], v[40:41], v[28:29], v[76:79]\n"
      "v_exp_f32 v86, v86\n"
      "v_sub_f32 v60, v60, v61\n"
      "v_mfma_f32_16x16x16_bf16 v[88:91], v[40:41], v[68:69], v[88:91]\n"
      "v_exp_f32 v87, v87\n"
      "v_exp_f32_e64 v60, v60\n"
      "s_setprio 0\n"
      "v_cndmask_b32 v48, v48, v52, s[42:43]\n"
      "s_nop 0\n"
      "v_cndmask_b32 v48, v48, v76, s[44:45]\n"
      "s_nop 1\n"
      "v_cndmask_b32 v48, v48, v88, s[46:47]\n"
      "v_cvt_pk_bf16_f32 v80, v80, v81\n"
      "v_cvt_pk_bf16_f32 v81, v82, v83\n"
      "v_max_f32_dpp v88, v48, v48 row_ror:8 row_mask:0xf bank_mask:0xf\n"
      "v_cvt_pk_bf16_f32 v82, v36, v37\n"
      "v_cvt_pk_bf16_f32 v83, v38, v39\n"
      "v_max_f32_dpp v88, v88, v88 row_ror:4 row_mask:0xf bank_mask:0xf\n"
      "v_cvt_pk_bf16_f32 v36, v84, v85\n"
      "v_cvt_pk_bf16_f32 v37, v86, v87\n"
      "v_max_f32_dpp v88, v88, v88 row_ror:2 row_mask:0xf bank_mask:0xf\n"
      "v_cvt_pk_bf16_f32 v38, v60, 0\n"
      "s_nop 0\n"
      "v_max_f32_dpp v88, v88, v88 row_ror:1 row_mask:0xf bank_mask:0xf\n"
      "s_nop 1\n"
      "v_max_f32_dpp v88, v88, v88 row_bcast:15 row_mask:0xa bank_mask:0xf\n"
      "s_nop 1\n"
      "v_max_f32_dpp v88, v88, v88 row_bcast:31 row_mask:0xc bank_mask:0xf\n"
      "s_nop 0\n"
      "v_readlane_b32 s30, v88, 63\n"
      "s_nop 1\n"
      "v_subrev_f32 v48, s30, v48\n"
      "v_exp_f32 v48, v48\n"
      "s_nop 0\n"
      "v_cvt_pk_bf16_f32 v48, v48, v48\n"
      "ds_write_b128 v2, v[80:83] offset:4096\n"
      "ds_write_b96 v2, v[36:38] offset:5120\n"
      "ds_write_b16 v74, v48\n"
      "s_branch L_join_6_%=\n"
      "L_gen_7_%=:\n"
      "s_waitcnt lgkmcnt(0)\n"
      "s_nop 7\n"
      "s_nop 7\n"
      "s_lshl_b32 s29, s27, 8\n"
      "s_add_u32 s29, s29, s25\n"
      "s_lshl_b32 s29, s29, 1\n"
      "s_add_u32 s29, s29, 1\n"
      "s_add_u32 s30, s25, 5120\n"
      "s_cmp_eq_u32 s27, 10\n"
      "s_cselect_b32 s29, s30, s29\n"
      "s_mul_i32 s29, s29, 4802\n"
      "s_add_u32 s29, s29, s18\n"
      "s_and_b32 s29, s29, 15\n"
      "s_and_b32 s30, s27, 1\n"
      "s_mul_i32 s30, s30, 9728\n"
      "s_add_u32 s29, s29, s30\n"
      "s_add_u32 s29, s29, 128768\n"
      "v_bfe_u32 v65, v3, 3, 4\n"
      "v_mul_u32_u24 v65, 0x62, v65\n"
      "v_add_u32_e64 v65, s29, v65\n"
      "v_bfe_u32 v66, v3, 7, 2\n"
      "v_lshl_add_u32 v66, v66, 3, v65\n"
      "ds_read_u16 v67, v66\n"
      "ds_read_u16 v18, v66 offset:2\n"
      "ds_read_u16 v19, v66 offset:4\n"
      "ds_read_u16 v32, v66 offset:6\n"
      "ds_read_u16 v33, v66 offset:32\n"
      "ds_read_u16 v34, v66 offset:34\n"
      "ds_read_u16 v35, v66 offset:36\n"
      "ds_read_u16 v40, v66 offset:38\n"
      "ds_read_u16 v41, v66 offset:64\n"
      "ds_read_u16 v42, v66 offset:66\n"
      "ds_read_u16 v43, v66 offset:68\n"
      "ds_read_u16 v44, v66 offset:70\n"
      "ds_read_u16 v45, v65 offset:96\n"
      "s_waitcnt lgkmcnt(12)\n"
      "v_lshlrev_b32 v67, 16, v67\n"
      "v_fmac_f32 v80, s41, v67\n"
      "s_waitcnt lgkmcnt(11)\n"
      "v_lshlrev_b32 v18, 16, v18\n"
      "v_fmac_f32 v81, s41, v18\n"
      "s_waitcnt lgkmcnt(10)\n"
      "v_lshlrev_b32 v19, 16, v19\n"
      "v_fmac_f32 v82, s41, v19\n"
      "s_waitcnt lgkmcnt(9)\n"
      "v_lshlrev_b32 v32, 16, v32\n"
      "v_fmac_f32 v83, s41, v32\n"
      "s_waitcnt lgkmcnt(7)\n"
      "v_lshlrev_b32 v33, 16, v33\n"
      "v_fmac_f32 v36, s41, v33\n"
      "v_lshlrev_b32 v34, 16, v34\n"
      "v_fmac_f32 v37, s41, v34\n"
      "s_waitcnt lgkmcnt(3)\n"
      "v_lshlrev_b32 v35, 16, v35\n"
      "v_fmac_f32 v38, s41, v35\n"
      "v_lshlrev_b32 v40, 16, v40\n"
      "v_fmac_f32 v39, s41, v40\n"
      "v_lshlrev_b32 v41, 16, v41\n"
      "v_fmac_f32 v84, s41, v41\n"
      "v_lshlrev_b32 v42, 16, v42\n"
      "v_fmac_f32 v85, s41, v42\n"
      "s_waitcnt lgkmcnt(0)\n"
      "v_lshlrev_b32 v43, 16, v43\n"
      "v_fmac_f32 v86, s41, v43\n"
      "v_lshlrev_b32 v44, 16, v44\n"
      "v_fmac_f32 v87, s41, v44\n"
      "v_lshlrev_b32 v45, 16, v45\n"
      "v_fmac_f32_e64 v64, s41, v45\n"
      "v_max3_f32 v45, v80, v81, v82\n"
      "v_max3_f32 v44, v83, v36, v37\n"
      "v_max3_f32 v45, v45, v38, v39\n"
      "v_max3_f32 v44, v44, v84, v85\n"
      "v_max3_f32 v45, v45, v86, v87\n"
      "v_max3_f32 v45, v45, v44, v64\n"
      "v_mov_b32 v44, v45\n"
      "s_nop 1\n"
      "v_permlane16_swap_b32 v45, v44\n"
      "v_max_f32 v45, v45, v44\n"
      "v_mov_b32_e64 v44, v45\n"
      "ds_read_b64 v[22:23], v3 offset:2048\n"
      "ds_read_b64 v[26:27], v3 offset:2560\n"
      "ds_read_b64 v[30:31], v3 offset:3072\n"
      "ds_read_b64 v[70:71], v3 offset:3584\n"
      "v_permlane32_swap_b32 v45, v44\n"
      "v_max_f32 v45, v45, v44\n"
      "v_sub_f32 v80, v80, v45\n"
      "v_sub_f32 v81, v81, v45\n"
      "v_exp_f32 v80, v80\n"
      "v_sub_f32 v82, v82, v45\n"
      "v_exp_f32 v81, v81\n"
      "v_sub_f32 v83, v83, v45\n"
      "v_exp_f32 v82, v82\n"
      "v_sub_f32 v36, v36, v45\n"
      "v_exp_f32 v83, v83\n"
      "v_sub_f32 v37, v37, v45\n"
      "s_setprio 2\n"
      "s_waitcnt lgkmcnt(0)\n"
      "v_mfma_f32_16x16x32_bf16 v[40:43], v[52:55], v[60:63], 0\n"
      "v_exp_f32 v36, v36\n"
      "v_sub_f32 v38, v38, v45\n"
      "v_mfma_f32_16x16x32_bf16 v[40:43], v[20:23], v[8:11], v[40:43]\n"
      "v_exp_f32 v37, v37\n"
      "v_sub_f32 v39, v39, v45\n"
      "v_mfma_f32_16x16x32_bf16 v[32:35], v[76:79], v[60:63], 0\n"
      "v_exp_f32 v38, v38\n"
      "v_sub_f32 v84, v84, v45\n"
      "v_mfma_f32_16x16x32_bf16 v[32:35], v[24:27], v[8:11], v[32:35]\n"
      "v_exp_f32 v39, v39\n"
      "v_sub_f32 v85, v85, v45\n"
      "v_mfma_f32_16x16x32_bf16 v[92:95], v[88:91], v[60:63], 0\n"
      "v_exp_f32 v84, v84\n"
      "v_sub_f32 v86, v86, v45\n"
      "v_mfma_f32_16x16x32_bf16 v[92:95], v[28:31], v[8:11], v[92:95]\n"
      "v_exp_f32 v85, v85\n"
      "v_sub_f32 v87, v87, v45\n"
      "v_mfma_f32_16x16x32_bf16 v[96:99], v[56:59], v[60:63], 0\n"
      "v_exp_f32 v86, v86\n"
      "v_sub_f32 v64, v64, v45\n"
      "v_mfma_f32_16x16x32_bf16 v[96:99], v[68:71], v[8:11], v[96:99]\n"
      "v_exp_f32 v87, v87\n"
      "v_exp_f32 v64, v64\n"
      "s_setprio 0\n"
      "s_nop 4\n"
      "ds_read_u16 v97, v66 offset:1568\n"
      "ds_read_u16 v98, v66 offset:1570\n"
      "ds_read_u16 v99, v66 offset:1572\n"
      "ds_read_u16 v45, v66 offset:1574\n"
      "ds_read_u16 v60, v66 offset:1600\n"
      "ds_read_u16 v61, v66 offset:1602\n"
      "ds_read_u16 v62, v66 offset:1604\n"
      "ds_read_u16 v63, v66 offset:1606\n"
      "ds_read_u16 v44, v66 offset:1632\n"
      "ds_read_u16 v19, v66 offset:1634\n"
      "ds_read_u16 v18, v66 offset:1636\n"
      "ds_read_u16 v67, v66 offset:1638\n"
      "ds_read_u16 v46, v65 offset:1664\n"
      "s_waitcnt lgkmcnt(12)\n"
      "v_lshlrev_b32 v97, 16, v97\n"
      "v_fmac_f32 v40, s41, v97\n"
      "s_waitcnt lgkmcnt(11)\n"
      "v_lshlrev_b32 v98, 16, v98\n"
      "v_fmac_f32 v41, s41, v98\n"
      "s_waitcnt lgkmcnt(10)\n"
      "v_lshlrev_b32 v99, 16, v99\n"
      "v_fmac_f32 v42, s41, v99\n"
      "s_waitcnt lgkmcnt(9)\n"
      "v_lshlrev_b32 v45, 16, v45\n"
      "v_fmac_f32 v43, s41, v45\n"
      "s_waitcnt lgkmcnt(7)\n"
      "v_lshlrev_b32 v60, 16, v60\n"
      "v_fmac_f32 v32, s41, v60\n"
      "v_lshlrev_b32 v61, 16, v61\n"
      "v_fmac_f32 v33, s41, v61\n"
      "s_waitcnt lgkmcnt(3)\n"
      "v_lshlrev_b32 v62, 16, v62\n"
      "v_fmac_f32 v34, s41, v62\n"
      "v_lshlrev_b32 v63, 16, v63\n"
      "v_fmac_f32 v35, s41, v63\n"
      "v_lshlrev_b32 v44, 16, v44\n"
      "v_fmac_f32 v92, s41, v44\n"
      "v_lshlrev_b32 v19, 16, v19\n"
      "v_fmac_f32 v93, s41, v19\n"
      "s_waitcnt lgkmcnt(0)\n"
      "v_lshlrev_b32 v18, 16, v18\n"
      "v_fmac_f32 v94, s41, v18\n"
      "v_lshlrev_b32 v67, 16, v67\n"
      "v_fmac_f32 v95, s41, v67\n"
      "v_lshlrev_b32 v46, 16, v46\n"
      "v_fmac_f32_e64 v96, s41, v46\n"
      "v_cvt_pk_bf16_f32 v80, v80, v81\n"
      "v_cvt_pk_bf16_f32 v81, v82, v83\n"
      "v_cvt_pk_bf16_f32 v82, v36, v37\n"
      "v_max3_f32 v46, v40, v41, v42\n"
      "v_max3_f32 v67, v43, v32, v33\n"
      "v_max3_f32 v46, v46, v34, v35\n"
      "v_max3_f32 v67, v67, v92, v93\n"
      "v_max3_f32 v46, v46, v94, v95\n"
      "v_max3_f32 v46, v46, v67, v96\n"
      "v_mov_b32 v67, v46\n"
      "v_cvt_pk_bf16_f32 v83, v38, v39\n"
      "v_cvt_pk_bf16_f32 v36, v84, v85\n"
      "v_permlane16_swap_b32 v46, v67\n"
      "v_max_f32 v46, v46, v67\n"
      "v_mov_b32 v67, v46\n"
      "v_cvt_pk_bf16_f32 v37, v86, v87\n"
      "v_cvt_pk_bf16_f32 v38, v64, 0\n"
      "ds_write_b128 v2, v[80:83]\n"
      "ds_write_b96 v2, v[36:38] offset:1024\n"
      "ds_read_b64 v[22:23], v3 offset:4096\n"
      "ds_read_b64 v[26:27], v3 offset:4608\n"
      "ds_read_b64 v[30:31], v3 offset:5120\n"
      "ds_read_b64 v[70:71], v3 offset:5632\n"
      "v_permlane32_swap_b32 v46, v67\n"
      "v_max_f32 v46, v46, v67\n"
      "v_sub_f32 v40, v40, v46\n"
      "v_sub_f32 v41, v41, v46\n"
      "v_exp_f32 v40, v40\n"
      "v_sub_f32 v42, v42, v46\n"
      "v_exp_f32 v41, v41\n"
      "v_sub_f32 v43, v43, v46\n"
      "v_exp_f32 v42, v42\n"
      "v_sub_f32 v32, v32, v46\n"
      "v_exp_f32 v43, v43\n"
      "v_sub_f32 v33, v33, v46\n"
      "s_setprio 2\n"
      "s_waitcnt lgkmcnt(0)\n"
      "v_mfma_f32_16x16x32_bf16 v[80:83], v[52:55], v[48:51], 0\n"
      "v_exp_f32 v32, v32\n"
      "v_sub_f32 v34, v34, v46\n"
      "v_mfma_f32_16x16x32_bf16 v[80:83], v[20:23], v[12:15], v[80:83]\n"
      "v_exp_f32 v33, v33\n"
      "v_sub_f32 v35, v35, v46\n"
      "v_mfma_f32_16x16x32_bf16 v[36:39], v[76:79], v[48:51], 0\n"
      "v_exp_f32 v34, v34\n"
      "v_sub_f32 v92, v92, v46\n"
      "v_mfma_f32_16x16x32_bf16 v[36:39], v[24:27], v[12:15], v[36:39]\n"
      "v_exp_f32 v35, v35\n"
      "v_sub_f32 v93, v93, v46\n"
      "v_mfma_f32_16x16x32_bf16 v[84:87], v[88:91], v[48:51], 0\n"
      "v_exp_f32 v92, v92\n"
      "v_sub_f32 v94, v94, v46\n"
      "v_mfma_f32_16x16x32_bf16 v[84:87], v[28:31], v[12:15], v[84:87]\n"
      "v_exp_f32 v93, v93\n"
      "v_sub_f32 v95, v95, v46\n"
      "v_mfma_f32_16x16x32_bf16 v[60:63], v[56:59], v[48:51], 0\n"
      "v_exp_f32 v94, v94\n"
      "v_sub_f32 v96, v96, v46\n"
      "v_mfma_f32_16x16x32_bf16 v[60:63], v[68:71], v[12:15], v[60:63]\n"
      "v_exp_f32 v95, v95\n"
      "v_exp_f32 v96, v96\n"
      "s_setprio 0\n"
      "s_nop 4\n"
      "ds_read_u16 v61, v66 offset:3136\n"
      "ds_read_u16 v62, v66 offset:3138\n"
      "ds_read_u16 v63, v66 offset:3140\n"
      "ds_read_u16 v70, v66 offset:3142\n"
      "ds_read_u16 v71, v66 offset:3168\n"
      "ds_read_u16 v46, v66 offset:3170\n"
      "ds_read_u16 v48, v66 offset:3172\n"
      "ds_read_u16 v49, v66 offset:3174\n"
      "ds_read_u16 v50, v66 offset:3200\n"
      "ds_read_u16 v51, v66 offset:3202\n"
      "ds_read_u16 v30, v66 offset:3204\n"
      "ds_read_u16 v31, v66 offset:3206\n"
      "ds_read_u16 v26, v65 offset:3232\n"
      "s_waitcnt lgkmcnt(12)\n"
      "v_lshlrev_b32 v61, 16, v61\n"
      "v_fmac_f32 v80, s41, v61\n"
      "s_waitcnt lgkmcnt(11)\n"
      "v_lshlrev_b32 v62, 16, v62\n"
      "v_fmac_f32 v81, s41, v62\n"
      "s_waitcnt lgkmcnt(10)\n"
      "v_lshlrev_b32 v63, 16, v63\n"
      "v_fmac_f32 v82, s41, v63\n"
      "s_waitcnt lgkmcnt(9)\n"
      "v_lshlrev_b32 v70, 16, v70\n"
      "v_fmac_f32 v83, s41, v70\n"
      "s_waitcnt lgkmcnt(7)\n"
      "v_lshlrev_b32 v71, 16, v71\n"
      "v_fmac_f32 v36, s41, v71\n"
      "v_lshlrev_b32 v46, 16, v46\n"
      "v_fmac_f32 v37, s41, v46\n"
      "s_waitcnt lgkmcnt(3)\n"
      "v_lshlrev_b32 v48, 16, v48\n"
      "v_fmac_f32 v38, s41, v48\n"
      "v_lshlrev_b32 v49, 16, v49\n"
      "v_fmac_f32 v39, s41, v49\n"
      "v_lshlrev_b32 v50, 16, v50\n"
      "v_fmac_f32 v84, s41, v50\n"
      "v_lshlrev_b32 v51, 16, v51\n"
      "v_fmac_f32 v85, s41, v51\n"
      "s_waitcnt lgkmcnt(0)\n"
      "v_lshlrev_b32 v30, 16, v30\n"
      "v_fmac_f32 v86, s41, v30\n"
      "v_lshlrev_b32 v31, 16, v31\n"
      "v_fmac_f32 v87, s41, v31\n"
      "v_lshlrev_b32 v26, 16, v26\n"
      "v_fmac_f32_e64 v60, s41, v26\n"
      "v_cvt_pk_bf16_f32 v40, v40, v41\n"
      "v_cvt_pk_bf16_f32 v41, v42, v43\n"
      "v_cvt_pk_bf16_f32 v42, v32, v33\n"
      "v_max3_f32 v26, v80, v81, v82\n"
      "v_max3_f32 v31, v83, v36, v37\n"
      "v_max3_f32 v26, v26, v38, v39\n"
      "v_max3_f32 v31, v31, v84, v85\n"
      "v_max3_f32 v26, v26, v86, v87\n"
      "v_max3_f32 v26, v26, v31, v60\n"
      "v_mov_b32_e64 v31, v26\n"
      "v_cvt_pk_bf16_f32 v43, v34, v35\n"
      "v_cvt_pk_bf16_f32 v32, v92, v93\n"
      "v_permlane16_swap_b32 v26, v31\n"
      "v_max_f32 v26, v26, v31\n"
      "v_mov_b32_e64 v31, v26\n"
      "v_cvt_pk_bf16_f32 v33, v94, v95\n"
      "v_cvt_pk_bf16_f32 v34, v96, 0\n"
      "ds_write_b128 v2, v[40:43] offset:2048\n"
      "ds_write_b96 v2, v[32:34] offset:3072\n"
      "ds_read_b128 v[40:43], v74 offset:1152\n"
      "ds_read_b64 v[32:33], v74 offset:1168\n"
      "ds_read_b32 v92, v16 offset:768\n"
      "ds_read_b32 v74, v16 offset:512\n"
      "v_permlane32_swap_b32 v26, v31\n"
      "v_max_f32 v26, v26, v31\n"
      "v_sub_f32 v80, v80, v26\n"
      "v_sub_f32 v81, v81, v26\n"
      "v_exp_f32 v80, v80\n"
      "v_sub_f32 v82, v82, v26\n"
      "v_exp_f32 v81, v81\n"
      "v_sub_f32 v83, v83, v26\n"
      "v_exp_f32 v82, v82\n"
      "v_sub_f32 v36, v36, v26\n"
      "v_exp_f32 v83, v83\n"
      "v_sub_f32 v37, v37, v26\n"
      "s_setprio 2\n"
      "s_waitcnt lgkmcnt(0)\n"
      "v_mfma_f32_16x16x32_bf16 v[48:51], v[40:43], v[52:55], v[92:95]\n"
      "v_exp_f32 v36, v36\n"
      "v_sub_f32 v38, v38, v26\n"
      "v_mfma_f32_16x16x32_bf16 v[52:55], v[40:43], v[76:79], v[92:95]\n"
      "v_exp_f32 v37, v37\n"
      "v_sub_f32 v39, v39, v26\n"
      "v_mfma_f32_16x16x32_bf16 v[76:79], v[40:43], v[88:91], v[92:95]\n"
      "v_exp_f32 v38, v38\n"
      "v_sub_f32 v84, v84, v26\n"
      "v_mfma_f32_16x16x32_bf16 v[88:91], v[40:43], v[56:59], v[92:95]\n"
      "v_exp_f32 v39, v39\n"
      "v_sub_f32 v85, v85, v26\n"
      "v_mfma_f32_16x16x16_bf16 v[48:51], v[32:33], v[20:21], v[48:51]\n"
      "v_exp_f32 v84, v84\n"
      "v_sub_f32 v86, v86, v26\n"
      "v_mfma_f32_16x16x16_bf16 v[52:55], v[32:33], v[24:25], v[52:55]\n"
      "v_exp_f32 v85, v85\n"
      "v_sub_f32 v87, v87, v26\n"
      "v_mfma_f32_16x16x16_bf16 v[76:79], v[32:33], v[28:29], v[76:79]\n"
      "v_exp_f32 v86, v86\n"
      "v_sub_f32 v60, v60, v26\n"
      "v_mfma_f32_16x16x16_bf16 v[88:91], v[32:33], v[68:69], v[88:91]\n"
      "v_exp_f32 v87, v87\n"
      "v_exp_f32_e64 v60, v60\n"
      "s_setprio 0\n"
      "v_cndmask_b32 v48, v48, v52, s[42:43]\n"
      "s_nop 0\n"
      "v_cndmask_b32 v48, v48, v76, s[44:45]\n"
      "s_nop 1\n"
      "v_cndmask_b32 v48, v48, v88, s[46:47]\n"
      "v_bfe_u32 v66, v3, 3, 6\n"
      "v_min_u32_e64 v66, 48, v66\n"
      "v_lshlrev_b32_e64 v66, 1, v66\n"
      "v_add_u32_e64 v66, s29, v66\n"
      "ds_read_u16 v88, v66 offset:4704\n"
      "s_waitcnt lgkmcnt(0)\n"
      "v_lshlrev_b32 v88, 16, v88\n"
      "v_fmac_f32_e64 v48, s41, v88\n"
      "v_cvt_pk_bf16_f32 v80, v80, v81\n"
      "v_cvt_pk_bf16_f32 v81, v82, v83\n"
      "v_max_f32_dpp v88, v48, v48 row_ror:8 row_mask:0xf bank_mask:0xf\n"
      "v_cvt_pk_bf16_f32 v82, v36, v37\n"
      "v_cvt_pk_bf16_f32 v83, v38, v39\n"
      "v_max_f32_dpp v88, v88, v88 row_ror:4 row_mask:0xf bank_mask:0xf\n"
      "v_cvt_pk_bf16_f32 v36, v84, v85\n"
      "v_cvt_pk_bf16_f32 v37, v86, v87\n"
      "v_max_f32_dpp v88, v88, v88 row_ror:2 row_mask:0xf bank_mask:0xf\n"
      "v_cvt_pk_bf16_f32 v38, v60, 0\n"
      "s_nop 0\n"
      "v_max_f32_dpp v88, v88, v88 row_ror:1 row_mask:0xf bank_mask:0xf\n"
      "s_nop 1\n"
      "v_max_f32_dpp v88, v88, v88 row_bcast:15 row_mask:0xa bank_mask:0xf\n"
      "s_nop 1\n"
      "v_max_f32_dpp v88, v88, v88 row_bcast:31 row_mask:0xc bank_mask:0xf\n"
      "s_nop 0\n"
      "v_readlane_b32 s30, v88, 63\n"
      "s_nop 1\n"
      "v_subrev_f32 v48, s30, v48\n"
      "v_exp_f32 v48, v48\n"
      "s_nop 0\n"
      "v_cvt_pk_bf16_f32 v48, v48, v48\n"
      "ds_write_b128 v2, v[80:83] offset:4096\n"
      "ds_write_b96 v2, v[36:38] offset:5120\n"
      "ds_write_b16 v74, v48\n"
      "s_branch L_join_6_%=\n"
      "L_B0_%=:\n"
      "v_mbcnt_lo_u32_b32 v12, -1, 0\n"
      "v_mbcnt_hi_u32_b32 v12, -1, v12\n"
      "v_lshlrev_b32 v12, 4, v12\n"
      "s_cmp_lt_u32 s25, 209\n"
      "s_cselect_b32 s26, 11, 10\n"
      "s_mov_b32 s27, 0\n"
      "s_mov_b32 s28, s25\n"
      "v_lshrrev_b32 v13, 2, v12\n"
      "s_sub_u32 s33, s24, 4\n"
      "v_lshrrev_b32 v14, 7, v12\n"
      "v_bfe_u32 v15, v12, 4, 3\n"
      "v_add_u32 v2, 36864, v12\n"
      "s_lshl_b32 s29, s33, 3\n"
      "v_add_u32 v16, s29, v14\n"
      "v_lshrrev_b32 v17, 1, v16\n"
      "v_lshlrev_b32 v17, 9, v17\n"
      "v_and_b32 v18, 1, v16\n"
      "v_add_u32 v17, v17, v18\n"
      "s_lshl_b32 s29, s25, 1\n"
      "v_add_u32 v17, s29, v17\n"
      "s_add_u32 s29, s25, 5120\n"
      "s_min_u32 s29, s29, 5328\n"
      "v_mov_b32 v18, s29\n"
      "v_cmp_gt_u32 vcc, 20, v16\n"
      "s_nop 1\n"
      "v_cndmask_b32 v17, v18, v17, vcc\n"
      "s_mov_b32 s29, 58835169\n"
      "v_mul_hi_u32 v18, v17, s29\n"
      "v_mul_u32_u24 v19, 73, v18\n"
      "v_sub_u32 v19, v17, v19\n"
      "v_mul_u32_u24 v18, 7, v18\n"
      "v_add_u32 v18, 9, v18\n"
      "v_subrev_u32 v17, 0x1ff, v18\n"
      "v_min_u32 v18, v18, v17\n"
      "v_mul_u32_u24 v19, 7, v19\n"
      "v_add_u32 v19, 9, v19\n"
      "v_subrev_u32 v17, 0x1ff, v19\n"
      "v_min_u32 v19, v19, v17\n"
      "v_mul_u32_u24 v18, 0x1ff, v18\n"
      "v_add_u32 v18, v18, v19\n"
      "v_mul_u32_u24 v18, 0x180, v18\n"
      "v_bfe_u32 v19, v16, 1, 3\n"
      "v_xor_b32 v19, v19, v15\n"
      "v_lshl_add_u32 v17, v19, 4, v18\n"
      "s_lshl_b32 s33, s33, 10\n"
      "s_waitcnt lgkmcnt(0)\n"
      "s_add_u32 m0, s33, 36864\n"
      "s_nop 0\n"
      "global_load_lds_dwordx4 v17, s[16:17]\n"
      "s_add_u32 m0, s33, 40832\n"
      "s_nop 0\n"
      "global_load_lds_dwordx4 v17, s[16:17] offset:128\n"
      "s_add_u32 m0, s33, 44800\n"
      "s_nop 0\n"
      "global_load_lds_dwordx4 v17, s[16:17] offset:256\n"
      "s_load_dwordx16 s[64:79], s[82:83], 0\n"
      "s_mov_b64 s[92:93], s[62:63]\n"
      "s_load_dwordx16 s[48:63], s[84:85], 0\n"
      "s_lshr_b32 s29, s33, 2\n"
      "v_add_u32_e64 v17, s29, v13\n"
      "v_min_u32 v17, 672, v17\n"
      "v_lshlrev_b32 v18, 1, v17\n"
      "v_add_u32 v18, 145664, v18\n"
      "global_load_dword v17, v17, s[86:87]\n"
      "s_add_u32 s29, s33, 0\n"
      "v_add_u32 v19, s29, v12\n"
      "v_min_u32 v19, 9588, v19\n"
      "s_add_u32 m0, s33, 24576\n"
      "s_nop 0\n"
      "global_load_lds_dwordx4 v19, s[90:91]\n"
      "s_add_u32 s29, s33, 4096\n"
      "v_add_u32 v19, s29, v12\n"
      "v_min_u32 v19, 9588, v19\n"
      "s_add_u32 m0, s33, 28672\n"
      "s_nop 0\n"
      "global_load_lds_dwordx4 v19, s[90:91]\n"
      "s_cmp_ge_u32 s24, 6\n"
      "s_cbranch_scc1 L_ix_10_%=\n"
      "s_add_u32 s29, s33, 8192\n"
      "v_add_u32_e64 v19, s29, v12\n"
      "v_min_u32 v19, 9588, v19\n"
      "s_add_u32 m0, s33, 32768\n"
      "s_nop 0\n"
      "global_load_lds_dwordx4 v19, s[90:91]\n"
      "L_ix_10_%=:\n"
      "v_mul_u32_u24 v19, 37, v16\n"
      "v_lshrrev_b32 v19, 8, v19\n"
      "v_mul_u32_u24 v20, 7, v19\n"
      "v_sub_u32_e64 v20, v16, v20\n"
      "v_cmp_le_u32 s[36:37], 4, v19\n"
      "v_cmp_le_u32 s[38:39], 4, v20\n"
      "v_mul_u32_u24 v19, 0x1ff, v19\n"
      "v_add_u32 v19, v19, v20\n"
      "v_mul_u32_u24 v19, 0x180, v19\n"
      "v_bfe_u32 v20, v16, 1, 3\n"
      "v_xor_b32 v20, v20, v15\n"
      "v_lshl_add_u32 v0, v20, 4, v19\n"
      "s_lshl_b32 s10, s28, 1\n"
      "s_add_u32 s11, s10, 1\n"
      "s_mul_hi_u32 s29, s10, 58835169\n"
      "s_mul_i32 s30, s29, 73\n"
      "s_sub_u32 s30, s10, s30\n"
      "s_mul_i32 s40, s29, 1373568\n"
      "s_mul_i32 s31, s30, 2688\n"
      "s_add_u32 s40, s40, s31\n"
      "s_add_u32 s40, s40, 589824\n"
      "s_cmp_eq_u32 s29, 72\n"
      "s_cselect_b32 s41, 100270464, 0\n"
      "s_cmp_eq_u32 s30, 72\n"
      "s_cselect_b32 s42, 196224, 0\n"
      "s_mul_hi_u32 s29, s11, 58835169\n"
      "s_mul_i32 s30, s29, 73\n"
      "s_sub_u32 s30, s11, s30\n"
      "s_mul_i32 s43, s29, 1373568\n"
      "s_mul_i32 s31, s30, 2688\n"
      "s_add_u32 s43, s43, s31\n"
      "s_add_u32 s43, s43, 589824\n"
      "s_cmp_eq_u32 s29, 72\n"
      "s_cselect_b32 s44, 100270464, 0\n"
      "s_cmp_eq_u32 s30, 72\n"
      "s_cselect_b32 s45, 196224, 0\n"
      "s_lshr_b32 s29, s33, 10\n"
      "s_and_b32 s29, s29, 1\n"
      "s_lshl_b32 s29, s29, 3\n"
      "v_add_u32 v19, s29, v14\n"
      "v_add_u32 v14, 32, v19\n"
      "v_mul_u32_u24 v16, 37, v14\n"
      "v_lshrrev_b32 v16, 8, v16\n"
      "v_mul_u32_u24 v20, 7, v16\n"
      "v_sub_u32_e64 v20, v14, v20\n"
      "v_cmp_le_u32 s[88:89], 4, v20\n"
      "v_mul_u32_u24 v16, 0x1ff, v16\n"
      "v_add_u32 v16, v16, v20\n"
      "v_mul_u32_u24 v16, 0x180, v16\n"
      "v_bfe_u32 v14, v19, 1, 3\n"
      "v_xor_b32 v14, v14, v15\n"
      "v_lshl_add_u32 v15, v14, 4, v16\n"
      "s_add_u32 s32, s18, 25589857\n"
      "s_and_b32 s32, s32, -16\n"
      "s_and_b32 s95, s24, 1\n"
      "s_lshl_b32 s95, s95, 10\n"
      "s_cmp_eq_u32 s95, 0\n"
      "s_cselect_b32 s90, -1, 0\n"
      "s_cselect_b32 s91, 0xffff, 0\n"
      "s_waitcnt lgkmcnt(0)\n"
      "s_pack_ll_b32_b16 s46, s64, s72\n"
      "s_waitcnt vmcnt(2)\n"
      "v_dot2_f32_bf16 v14, s46, v17, 0\n"
      "s_pack_hh_b32_b16 s47, s64, s72\n"
      "v_dot2_f32_bf16 v16, s47, v17, 0\n"
      "s_pack_ll_b32_b16 s8, s65, s73\n"
      "v_dot2_f32_bf16 v19, s8, v17, 0\n"
      "s_pack_hh_b32_b16 s9, s65, s73\n"
      "v_dot2_f32_bf16 v20, s9, v17, 0\n"
      "s_pack_ll_b32_b16 s46, s66, s74\n"
      "v_dot2_f32_bf16 v21, s46, v17, 0\n"
      "s_pack_hh_b32_b16 s47, s66, s74\n"
      "v_dot2_f32_bf16 v22, s47, v17, 0\n"
      "s_pack_ll_b32_b16 s8, s67, s75\n"
      "v_dot2_f32_bf16 v23, s8, v17, 0\n"
      "s_pack_hh_b32_b16 s9, s67, s75\n"
      "v_dot2_f32_bf16 v24, s9, v17, 0\n"
      "s_pack_ll_b32_b16 s46, s68, s76\n"
      "v_dot2_f32_bf16 v25, s46, v17, 0\n"
      "s_pack_hh_b32_b16 s47, s68, s76\n"
      "v_dot2_f32_bf16 v26, s47, v17, 0\n"
      "s_pack_ll_b32_b16 s8, s69, s77\n"
      "v_dot2_f32_bf16 v27, s8, v17, 0\n"
      "s_pack_hh_b32_b16 s9, s69, s77\n"
      "v_dot2_f32_bf16 v28, s9, v17, 0\n"
      "s_pack_ll_b32_b16 s46, s70, s78\n"
      "v_dot2_f32_bf16 v29, s46, v17, 0\n"
      "s_pack_hh_b32_b16 s47, s70, s78\n"
      "v_dot2_f32_bf16 v30, s47, v17, 0\n"
      "s_pack_ll_b32_b16 s8, s71, s79\n"
      "v_dot2_f32_bf16 v31, s8, v17, 0\n"
      "s_pack_hh_b32_b16 s9, s71, s79\n"
      "v_dot2_f32_bf16 v32, s9, v17, 0\n"
      "s_load_dwordx16 s[64:79], s[84:85], 64\n"
      "v_max_f32 v14, 0, v14\n"
      "v_max_f32 v16, 0, v16\n"
      "v_cvt_pk_bf16_f32 v14, v14, v16\n"
      "v_max_f32 v19, 0, v19\n"
      "v_max_f32 v20, 0, v20\n"
      "v_cvt_pk_bf16_f32 v19, v19, v20\n"
      "v_max_f32 v21, 0, v21\n"
      "v_max_f32 v22, 0, v22\n"
      "v_cvt_pk_bf16_f32 v21, v21, v22\n"
      "v_max_f32 v23, 0, v23\n"
      "v_max_f32 v24, 0, v24\n"
      "v_cvt_pk_bf16_f32 v23, v23, v24\n"
      "v_max_f32 v25, 0, v25\n"
      "v_max_f32 v26, 0, v26\n"
      "v_cvt_pk_bf16_f32 v25, v25, v26\n"
      "v_max_f32 v27, 0, v27\n"
      "v_max_f32 v28, 0, v28\n"
      "v_cvt_pk_bf16_f32 v27, v27, v28\n"
      "v_max_f32 v29, 0, v29\n"
      "v_max_f32 v30, 0, v30\n"
      "v_cvt_pk_bf16_f32 v29, v29, v30\n"
      "v_max_f32 v31, 0, v31\n"
      "v_max_f32_e64 v32, 0, v32\n"
      "v_cvt_pk_bf16_f32 v31, v31, v32\n"
      "s_pack_ll_b32_b16 s46, s48, s50\n"
      "v_dot2_f32_bf16 v26, s46, v14, 0\n"
      "s_pack_hh_b32_b16 s47, s48, s50\n"
      "v_dot2_f32_bf16 v22, s47, v14, 0\n"
      "s_pack_ll_b32_b16 s8, s49, s51\n"
      "v_dot2_f32_bf16 v24, s8, v14, 0\n"
      "s_pack_hh_b32_b16 s9, s49, s51\n"
      "v_dot2_f32_bf16 v16, s9, v14, 0\n"
      "s_pack_ll_b32_b16 s46, s52, s54\n"
      "v_dot2_f32_bf16 v26, s46, v19, v26\n"
      "s_pack_hh_b32_b16 s47, s52, s54\n"
      "v_dot2_f32_bf16 v22, s47, v19, v22\n"
      "s_pack_ll_b32_b16 s8, s53, s55\n"
      "v_dot2_f32_bf16 v24, s8, v19, v24\n"
      "s_pack_hh_b32_b16 s9, s53, s55\n"
      "v_dot2_f32_bf16 v16, s9, v19, v16\n"
      "s_pack_ll_b32_b16 s46, s56, s58\n"
      "v_dot2_f32_bf16 v26, s46, v21, v26\n"
      "s_pack_hh_b32_b16 s47, s56, s58\n"
      "v_dot2_f32_bf16 v22, s47, v21, v22\n"
      "s_pack_ll_b32_b16 s8, s57, s59\n"
      "v_dot2_f32_bf16 v24, s8, v21, v24\n"
      "s_pack_hh_b32_b16 s9, s57, s59\n"
      "v_dot2_f32_bf16 v16, s9, v21, v16\n"
      "s_pack_ll_b32_b16 s46, s60, s62\n"
      "v_dot2_f32_bf16 v26, s46, v23, v26\n"
      "s_pack_hh_b32_b16 s47, s60, s62\n"
      "v_dot2_f32_bf16 v22, s47, v23, v22\n"
      "s_pack_ll_b32_b16 s8, s61, s63\n"
      "v_dot2_f32_bf16 v24, s8, v23, v24\n"
      "s_pack_hh_b32_b16 s9, s61, s63\n"
      "v_dot2_f32_bf16 v16, s9, v23, v16\n"
      "s_waitcnt lgkmcnt(0)\n"
      "s_pack_ll_b32_b16 s46, s64, s66\n"
      "v_dot2_f32_bf16 v26, s46, v25, v26\n"
      "s_pack_hh_b32_b16 s47, s64, s66\n"
      "v_dot2_f32_bf16 v22, s47, v25, v22\n"
      "s_pack_ll_b32_b16 s8, s65, s67\n"
      "v_dot2_f32_bf16 v24, s8, v25, v24\n"
      "s_pack_hh_b32_b16 s9, s65, s67\n"
      "v_dot2_f32_bf16 v16, s9, v25, v16\n"
      "s_pack_ll_b32_b16 s46, s68, s70\n"
      "v_dot2_f32_bf16 v26, s46, v27, v26\n"
      "s_pack_hh_b32_b16 s47, s68, s70\n"
      "v_dot2_f32_bf16 v22, s47, v27, v22\n"
      "s_pack_ll_b32_b16 s8, s69, s71\n"
      "v_dot2_f32_bf16 v24, s8, v27, v24\n"
      "s_pack_hh_b32_b16 s9, s69, s71\n"
      "v_dot2_f32_bf16 v16, s9, v27, v16\n"
      "s_pack_ll_b32_b16 s46, s72, s74\n"
      "v_dot2_f32_bf16 v26, s46, v29, v26\n"
      "s_pack_hh_b32_b16 s47, s72, s74\n"
      "v_dot2_f32_bf16 v22, s47, v29, v22\n"
      "s_pack_ll_b32_b16 s8, s73, s75\n"
      "v_dot2_f32_bf16 v24, s8, v29, v24\n"
      "s_pack_hh_b32_b16 s9, s73, s75\n"
      "v_dot2_f32_bf16 v16, s9, v29, v16\n"
      "s_pack_ll_b32_b16 s46, s76, s78\n"
      "v_dot2_f32_bf16 v26, s46, v31, v26\n"
      "s_pack_hh_b32_b16 s47, s76, s78\n"
      "v_dot2_f32_bf16 v22, s47, v31, v22\n"
      "s_pack_ll_b32_b16 s8, s77, s79\n"
      "v_dot2_f32_bf16 v24, s8, v31, v24\n"
      "s_pack_hh_b32_b16 s9, s77, s79\n"
      "v_dot2_f32_bf16 v16, s9, v31, v16\n"
      "v_cvt_pk_bf16_f32 v26, v26, v26\n"
      "v_lshlrev_b32 v26, 16, v26\n"
      "v_mul_f32 v26, 0xbfb8aa3b, v26\n"
      "v_exp_f32 v26, v26\n"
      "s_nop 0\n"
      "v_add_f32 v26, 1.0, v26\n"
      "v_rcp_f32 v26, v26\n"
      "s_nop 0\n"
      "v_cvt_pk_bf16_f32 v26, v26, v26\n"
      "v_lshlrev_b32 v26, 16, v26\n"
      "v_mul_f32 v26, 0x41800000, v26\n"
      "v_cvt_pk_bf16_f32 v22, v22, v22\n"
      "v_lshlrev_b32 v22, 16, v22\n"
      "v_mul_f32 v22, 0xbfb8aa3b, v22\n"
      "v_exp_f32 v22, v22\n"
      "s_nop 0\n"
      "v_add_f32 v22, 1.0, v22\n"
      "v_rcp_f32 v22, v22\n"
      "s_nop 0\n"
      "v_cvt_pk_bf16_f32 v22, v22, v22\n"
      "v_lshlrev_b32_e64 v22, 16, v22\n"
      "v_mul_f32 v22, 0x41800000, v22\n"
      "v_cvt_pk_bf16_f32 v24, v24, v24\n"
      "v_lshlrev_b32 v24, 16, v24\n"
      "v_mul_f32 v24, 0xbfb8aa3b, v24\n"
      "v_exp_f32 v24, v24\n"
      "s_nop 0\n"
      "v_add_f32 v24, 1.0, v24\n"
      "v_rcp_f32 v24, v24\n"
      "s_nop 0\n"
      "v_cvt_pk_bf16_f32 v24, v24, v24\n"
      "v_lshlrev_b32_e64 v24, 16, v24\n"
      "v_mul_f32 v24, 0x41800000, v24\n"
      "v_cvt_pk_bf16_f32 v16, v16, v16\n"
      "v_lshlrev_b32 v16, 16, v16\n"
      "v_mul_f32 v16, 0xbfb8aa3b, v16\n"
      "v_exp_f32 v16, v16\n"
      "s_nop 0\n"
      "v_add_f32 v16, 1.0, v16\n"
      "v_rcp_f32 v16, v16\n"
      "s_nop 0\n"
      "v_cvt_pk_bf16_f32 v16, v16, v16\n"
      "v_lshlrev_b32_e64 v16, 16, v16\n"
      "v_mul_f32 v16, 0x41800000, v16\n"
      "v_cvt_pk_bf16_f32 v20, v26, v22\n"
      "v_cvt_pk_bf16_f32 v21, v24, v16\n"
      "ds_write_b64 v18, v[20:21]\n"
      "s_waitcnt vmcnt(0)\n"
      "s_waitcnt lgkmcnt(0)\n"
      "s_barrier\n"
      "s_setprio 3\n"
      "s_mov_b32 s8, 40960\n"
      "v_add_u32 v18, s40, v0\n"
      "s_or_b32 s14, s41, s42\n"
      "s_cbranch_scc1 L_edge_11_%=\n"
      "L_eb_12_%=:\n"
      "s_add_u32 m0, s33, 0\n"
      "s_nop 0\n"
      "global_load_lds_dwordx4 v18, s[16:17]\n"
      "s_add_u32 m0, s33, 3968\n"
      "s_nop 0\n"
      "global_load_lds_dwordx4 v18, s[16:17] offset:128\n"
      "s_add_u32 m0, s33, 7936\n"
      "s_nop 0\n"
      "global_load_lds_dwordx4 v18, s[16:17] offset:256\n"
      "v_add_u32 v18, s40, v15\n"
      "s_or_b32 s14, s41, s42\n"
      "s_cbranch_scc1 L_edge_13_%=\n"
      "L_eb_14_%=:\n"
      "s_add_u32 m0, s33, 12288\n"
      "s_nop 0\n"
      "global_load_lds_dwordx4 v18, s[16:17]\n"
      "s_add_u32 m0, s33, 16256\n"
      "s_nop 0\n"
      "global_load_lds_dwordx4 v18, s[16:17] offset:128\n"
      "s_add_u32 m0, s33, 20224\n"
      "s_nop 0\n"
      "global_load_lds_dwordx4 v18, s[16:17] offset:256\n"
      "s_sub_u32 s31, s24, 4\n"
      "s_lshl_b32 s29, s31, 12\n"
      "s_add_u32 s34, s92, s29\n"
      "s_addc_u32 s35, s93, 0\n"
      "s_add_u32 m0, s29, 147712\n"
      "s_nop 0\n"
      "global_load_lds_dwordx4 v12, s[34:35]\n"
      "global_load_lds_dwordx4 v12, s[34:35] offset:1024\n"
      "global_load_lds_dwordx4 v12, s[34:35] offset:2048\n"
      "s_cmp_eq_u32 s24, 7\n"
      "s_cbranch_scc1 L_ct3_15_%=\n"
      "global_load_lds_dwordx4 v12, s[34:35] offset:3072\n"
      "L_ct3_15_%=:\n"
      "s_mov_b32 s12, 0x10001\n"
      "s_mov_b32 s13, 0x10001\n"
      "s_mov_b32 s70, 0\n"
      "s_cmp_eq_u32 s24, 5\n"
      "s_cselect_b32 s70, 64, s70\n"
      "s_mov_b32 s71, 0\n"
      "s_cmp_eq_u32 s24, 5\n"
      "s_cselect_b32 s71, 2048, s71\n"
      "s_mov_b32 s72, 0\n"
      "s_cmp_eq_u32 s24, 5\n"
      "s_cselect_b32 s72, -2816, s72\n"
      "s_add_u32 s73, s71, 36864\n"
      "s_mov_b32 s74, 0x3ebcbe62\n"
      "s_mov_b32 s63, 3072\n"
      "s_cmp_eq_u32 s24, 5\n"
      "s_cselect_b32 s63, 7168, s63\n"
      "s_mov_b32 s15, 0x7fff7fff\n"
      "s_mov_b32 s55, -4096\n"
      "s_cmp_eq_u32 s24, 5\n"
      "s_cselect_b32 s55, -5888, s55\n"
      "v_mov_b32 v8, 0\n"
      "s_mov_b32 exec_lo, 0x11111111\n"
      "s_mov_b32 exec_hi, 0x11111111\n"
      "v_mov_b32 v8, 0x3f803f80\n"
      "s_mov_b64 exec, -1\n"
      "v_mov_b32 v9, v8\n"
      "v_mov_b32 v10, v8\n"
      "v_mov_b32 v11, v8\n"
      "s_mov_b32 s30, 151808\n"
      "s_cmp_eq_u32 s24, 5\n"
      "s_cselect_b32 s30, 154624, s30\n"
      "v_add_u32 v1, s30, v13\n"
      "s_mov_b32 s30, 73728\n"
      "s_cmp_eq_u32 s24, 5\n"
      "s_cselect_b32 s30, 92544, s30\n"
      "v_add_u32 v3, s30, v12\n"
      "v_bfe_u32 v31, v12, 4, 4\n"
      "v_mul_u32_u24 v31, 1152, v31\n"
      "v_lshrrev_b32 v13, 8, v12\n"
      "v_lshl_add_u32 v31, v13, 3, v31\n"
      "s_mov_b32 s30, 768\n"
      "s_cmp_eq_u32 s24, 5\n"
      "s_cselect_b32 s30, 896, s30\n"
      "v_add_u32 v31, s30, v31\n"
      "v_bfe_u32 v18, v12, 5, 3\n"
      "v_xor_b32 v18, v18, v13\n"
      "v_and_b32 v6, 0xf0, v12\n"
      "v_lshlrev_b32 v4, 3, v6\n"
      "v_lshl_add_u32 v4, v18, 4, v4\n"
      "v_xor_b32 v5, 64, v4\n"
      "v_lshl_add_u32 v6, v13, 9, v6\n"
      "v_add_u32 v6, 36864, v6\n"
      "s_mul_i32 s94, s25, 12288\n"
      "s_add_u32 s94, s94, 12582912\n"
      "v_lshrrev_b32 v13, 6, v12\n"
      "v_mul_u32_u24 v13, 1152, v13\n"
      "v_and_b32 v18, 48, v12\n"
      "v_add_u32 v13, v13, v18\n"
      "s_mov_b32 s30, 768\n"
      "s_cmp_eq_u32 s24, 5\n"
      "s_cselect_b32 s30, 1056, s30\n"
      "v_add_u32_e64 v13, s30, v13\n"
      "v_bfe_u32 v18, v12, 5, 1\n"
      "s_mov_b32 s30, 0\n"
      "s_cmp_eq_u32 s24, 5\n"
      "s_cselect_b32 s30, -128, s30\n"
      "v_mul_lo_u32 v18, v18, s30\n"
      "v_add_u32 v18, v18, v13\n"
      "v_lshrrev_b32 v15, 6, v12\n"
      "v_mul_u32_u24 v15, 384, v15\n"
      "v_and_b32 v16, 48, v12\n"
      "v_add_u32 v15, v15, v16\n"
      "s_mov_b32 s30, 0\n"
      "s_cmp_eq_u32 s24, 5\n"
      "s_cselect_b32 s30, 64, s30\n"
      "v_add_u32_e64 v15, s30, v15\n"
      "s_mov_b32 s30, 76800\n"
      "s_cmp_eq_u32 s24, 5\n"
      "s_cselect_b32 s30, 83072, s30\n"
      "v_add_u32_e64 v16, s30, v12\n"
      "v_and_b32 v20, 0x300, v12\n"
      "v_and_b32 v21, 0xc0, v12\n"
      "v_lshl_or_b32 v20, v20, 1, v21\n"
      "v_bfe_u32 v21, v12, 4, 2\n"
      "v_lshl_or_b32 v20, v21, 3, v20\n"
      "v_add_u32 v20, s30, v20\n"
      "v_mov_b32 v29, 147200\n"
      "s_add_u32 s34, s4, 0\n"
      "s_addc_u32 s35, s5, 0\n"
      "global_load_dwordx4 a[0:3], v13, s[34:35]\n"
      "global_load_dwordx4 a[48:51], v18, s[34:35] offset:64\n"
      "s_add_u32 s34, s4, 18432\n"
      "s_addc_u32 s35, s5, 0\n"
      "global_load_dwordx4 a[4:7], v13, s[34:35]\n"
      "global_load_dwordx4 a[52:55], v18, s[34:35] offset:64\n"
      "s_add_u32 s34, s4, 36864\n"
      "s_addc_u32 s35, s5, 0\n"
      "global_load_dwordx4 a[8:11], v13, s[34:35]\n"
      "global_load_dwordx4 a[56:59], v18, s[34:35] offset:64\n"
      "s_add_u32 s34, s4, 55296\n"
      "s_addc_u32 s35, s5, 0\n"
      "global_load_dwordx4 a[12:15], v13, s[34:35]\n"
      "global_load_dwordx4 a[60:63], v18, s[34:35] offset:64\n"
      "s_waitcnt vmcnt(8)\n"
      "s_and_b32 s29, s24, 3\n"
      "s_lshl_b32 s29, s29, 2\n"
      "v_add_u32 v21, s29, v29\n"
      "v_mov_b32_e64 v22, 1\n"
      "ds_write_b32 v21, v22\n"
      "s_add_u32 s34, s4, 73728\n"
      "s_addc_u32 s35, s5, 0\n"
      "global_load_dwordx4 a[16:19], v13, s[34:35]\n"
      "global_load_dwordx4 a[64:67], v18, s[34:35] offset:64\n"
      "s_add_u32 s34, s4, 92160\n"
      "s_addc_u32 s35, s5, 0\n"
      "global_load_dwordx4 a[20:23], v13, s[34:35]\n"
      "global_load_dwordx4 a[68:71], v18, s[34:35] offset:64\n"
      "s_add_u32 s34, s4, 110592\n"
      "s_addc_u32 s35, s5, 0\n"
      "global_load_dwordx4 a[24:27], v13, s[34:35]\n"
      "global_load_dwordx4 a[72:75], v18, s[34:35] offset:64\n"
      "s_add_u32 s34, s4, 129024\n"
      "s_addc_u32 s35, s5, 0\n"
      "global_load_dwordx4 a[28:31], v13, s[34:35]\n"
      "global_load_dwordx4 a[76:79], v18, s[34:35] offset:64\n"
      "s_add_u32 s34, s4, 147456\n"
      "s_addc_u32 s35, s5, 0\n"
      "global_load_dwordx4 a[32:35], v13, s[34:35]\n"
      "global_load_dwordx4 a[80:83], v18, s[34:35] offset:64\n"
      "s_add_u32 s34, s4, 165888\n"
      "s_addc_u32 s35, s5, 0\n"
      "global_load_dwordx4 a[36:39], v13, s[34:35]\n"
      "global_load_dwordx4 a[84:87], v18, s[34:35] offset:64\n"
      "s_add_u32 s34, s4, 184320\n"
      "s_addc_u32 s35, s5, 0\n"
      "global_load_dwordx4 a[40:43], v13, s[34:35]\n"
      "global_load_dwordx4 a[88:91], v18, s[34:35] offset:64\n"
      "s_add_u32 s34, s4, 202752\n"
      "s_addc_u32 s35, s5, 0\n"
      "global_load_dwordx4 a[44:47], v13, s[34:35]\n"
      "global_load_dwordx4 a[92:95], v18, s[34:35] offset:64\n"
      "s_waitcnt lgkmcnt(0)\n"
      "L_poll_16_%=:\n"
      "ds_read_b128 v[24:27], v29 offset:16\n"
      "s_waitcnt lgkmcnt(0)\n"
      "v_and_b32 v24, v24, v25\n"
      "v_and_b32 v26, v26, v27\n"
      "v_and_b32 v24, v24, v26\n"
      "s_nop 0\n"
      "v_readfirstlane_b32 s29, v24\n"
      "s_nop 3\n"
      "s_cmp_eq_u32 s29, 0\n"
      "s_cbranch_scc1 L_poll_16_%=\n"
      "s_mov_b32 s48, 145664\n"
      "s_add_u32 s34, s6, 0\n"
      "s_addc_u32 s35, s7, 0\n"
      "global_load_dwordx4 a[96:99], v15, s[34:35]\n"
      "v_lshrrev_b32_e64 v13, 8, v12\n"
      "v_mul_u32_u24 v13, 784, v13\n"
      "v_bfe_u32 v18, v12, 2, 6\n"
      "v_add_u32 v13, v13, v18\n"
      "s_lshr_b32 s49, s33, 10\n"
      "s_mul_i32 s49, s49, 3136\n"
      "s_add_u32 s49, s49, 24576\n"
      "v_add_u32 v13, s49, v13\n"
      "s_lshl_b32 s49, s33, 1\n"
      "s_add_u32 s49, s49, 98816\n"
      "v_lshrrev_b32 v18, 1, v12\n"
      "v_add_u32 v18, s49, v18\n"
      "v_mov_b32 v12, 0xf149f149\n"
      "s_mov_b32 s46, 0x05040100\n"
      "s_mov_b32 s47, 0x07060302\n"
      "ds_read_b32 v24, v13\n"
      "ds_read_b32 v26, v13 offset:196\n"
      "ds_read_b32 v27, v13 offset:392\n"
      "ds_read_b32 v25, v13 offset:588\n"
      "ds_read_b32 v21, v13 offset:64\n"
      "s_add_u32 s34, s6, 6144\n"
      "s_addc_u32 s35, s7, 0\n"
      "global_load_dwordx4 a[100:103], v15, s[34:35]\n"
      "ds_read_b32 v22, v13 offset:260\n"
      "ds_read_b32 v23, v13 offset:456\n"
      "ds_read_b32 v19, v13 offset:652\n"
      "ds_read_b32 v14, v13 offset:128\n"
      "ds_read_b32 v17, v13 offset:324\n"
      "ds_read_b32 v30, v13 offset:520\n"
      "ds_read_b32 v32, v13 offset:716\n"
      "ds_read_b32 v28, v13 offset:192\n"
      "ds_read_b32 v33, v13 offset:388\n"
      "ds_read_b32 v34, v13 offset:584\n"
      "ds_read_b32 v35, v13 offset:780\n"
      "s_waitcnt lgkmcnt(12)\n"
      "v_lshl_add_u32 v24, v24, 3, s48\n"
      "ds_read_b64 v[36:37], v24\n"
      "v_lshl_add_u32 v26, v26, 3, s48\n"
      "ds_read_b64 v[38:39], v26\n"
      "v_lshl_add_u32 v27, v27, 3, s48\n"
      "ds_read_b64 v[40:41], v27\n"
      "v_lshl_add_u32 v25, v25, 3, s48\n"
      "ds_read_b64 v[26:27], v25\n"
      "s_waitcnt lgkmcnt(11)\n"
      "v_lshl_add_u32 v21, v21, 3, s48\n"
      "s_add_u32 s34, s6, 12288\n"
      "s_addc_u32 s35, s7, 0\n"
      "global_load_dwordx4 a[104:107], v15, s[34:35]\n"
      "ds_read_b64 v[24:25], v21\n"
      "v_lshl_add_u32 v22, v22, 3, s48\n"
      "ds_read_b64 v[42:43], v22\n"
      "v_lshl_add_u32 v23, v23, 3, s48\n"
      "ds_read_b64 v[44:45], v23\n"
      "v_lshl_add_u32 v19, v19, 3, s48\n"
      "ds_read_b64 v[22:23], v19\n"
      "v_lshl_add_u32 v14, v14, 3, s48\n"
      "ds_read_b64 v[46:47], v14\n"
      "s_waitcnt lgkmcnt(6)\n"
      "v_lshl_add_u32 v17, v17, 3, s48\n"
      "ds_read_b64 v[48:49], v17\n"
      "v_lshl_add_u32 v30, v30, 3, s48\n"
      "ds_read_b64 v[50:51], v30\n"
      "v_lshl_add_u32 v32, v32, 3, s48\n"
      "ds_read_b64 v[52:53], v32\n"
      "v_lshl_add_u32 v28, v28, 3, s48\n"
      "ds_read_b64 v[54:55], v28\n"
      "v_lshl_add_u32 v33, v33, 3, s48\n"
      "ds_read_b64 v[56:57], v33\n"
      "v_lshl_add_u32 v34, v34, 3, s48\n"
      "s_add_u32 s34, s6, 18432\n"
      "s_addc_u32 s35, s7, 0\n"
      "global_load_dwordx4 a[108:111], v15, s[34:35]\n"
      "ds_read_b64 v[32:33], v34\n"
      "v_lshl_add_u32 v35, v35, 3, s48\n"
      "ds_read_b64 v[58:59], v35\n"
      "v_perm_b32 v34, v38, v36, s46\n"
      "s_waitcnt lgkmcnt(6)\n"
      "v_perm_b32 v35, v26, v40, s46\n"
      "ds_write_b64 v18, v[34:35]\n"
      "v_perm_b32 v34, v38, v36, s47\n"
      "v_perm_b32 v35, v26, v40, s47\n"
      "ds_write_b64 v18, v[34:35] offset:6144\n"
      "v_perm_b32 v34, v39, v37, s46\n"
      "v_perm_b32 v35, v27, v41, s46\n"
      "ds_write_b64 v18, v[34:35] offset:12288\n"
      "v_perm_b32 v34, v39, v37, s47\n"
      "v_perm_b32 v35, v27, v41, s47\n"
      "ds_write_b64 v18, v[34:35] offset:18432\n"
      "v_perm_b32 v26, v42, v24, s46\n"
      "v_perm_b32 v27, v22, v44, s46\n"
      "ds_write_b64 v18, v[26:27] offset:512\n"
      "v_perm_b32 v26, v42, v24, s47\n"
      "v_perm_b32 v27, v22, v44, s47\n"
      "s_add_u32 s34, s6, 24576\n"
      "s_addc_u32 s35, s7, 0\n"
      "global_load_dwordx4 a[112:115], v15, s[34:35]\n"
      "ds_write_b64 v18, v[26:27] offset:6656\n"
      "v_perm_b32 v26, v43, v25, s46\n"
      "v_perm_b32 v27, v23, v45, s46\n"
      "ds_write_b64 v18, v[26:27] offset:12800\n"
      "v_perm_b32 v26, v43, v25, s47\n"
      "v_perm_b32 v27, v23, v45, s47\n"
      "ds_write_b64 v18, v[26:27] offset:18944\n"
      "v_perm_b32 v26, v48, v46, s46\n"
      "s_waitcnt lgkmcnt(4)\n"
      "v_perm_b32 v27, v52, v50, s46\n"
      "ds_write_b64 v18, v[26:27] offset:1024\n"
      "v_perm_b32 v26, v48, v46, s47\n"
      "v_perm_b32 v27, v52, v50, s47\n"
      "ds_write_b64 v18, v[26:27] offset:7168\n"
      "v_perm_b32 v26, v49, v47, s46\n"
      "v_perm_b32 v27, v53, v51, s46\n"
      "ds_write_b64 v18, v[26:27] offset:13312\n"
      "v_perm_b32 v26, v49, v47, s47\n"
      "v_perm_b32 v27, v53, v51, s47\n"
      "ds_write_b64 v18, v[26:27] offset:19456\n"
      "v_perm_b32 v26, v56, v54, s46\n"
      "s_add_u32 s34, s6, 30720\n"
      "s_addc_u32 s35, s7, 0\n"
      "global_load_dwordx4 a[116:119], v15, s[34:35]\n"
      "v_cndmask_b32 v26, v12, v26, s[12:13]\n"
      "v_perm_b32 v27, v58, v32, s46\n"
      "v_cndmask_b32 v27, v12, v27, s[12:13]\n"
      "ds_write_b64 v18, v[26:27] offset:1536\n"
      "v_perm_b32 v26, v56, v54, s47\n"
      "v_cndmask_b32 v26, v12, v26, s[12:13]\n"
      "v_perm_b32 v27, v58, v32, s47\n"
      "v_cndmask_b32 v27, v12, v27, s[12:13]\n"
      "ds_write_b64 v18, v[26:27] offset:7680\n"
      "v_perm_b32 v26, v57, v55, s46\n"
      "v_cndmask_b32 v26, v12, v26, s[12:13]\n"
      "v_perm_b32 v27, v59, v33, s46\n"
      "v_cndmask_b32 v27, v12, v27, s[12:13]\n"
      "ds_write_b64 v18, v[26:27] offset:13824\n"
      "v_perm_b32 v26, v57, v55, s47\n"
      "v_cndmask_b32 v26, v12, v26, s[12:13]\n"
      "v_perm_b32 v27, v59, v33, s47\n"
      "v_cndmask_b32 v27, v12, v27, s[12:13]\n"
      "ds_write_b64 v18, v[26:27] offset:19968\n"
      "s_waitcnt lgkmcnt(0)\n"
      "L_poll_19_%=:\n"
      "ds_read_b128 v[24:27], v29\n"
      "s_waitcnt lgkmcnt(0)\n"
      "v_and_b32 v24, v24, v25\n"
      "v_and_b32 v26, v26, v27\n"
      "v_and_b32 v24, v24, v26\n"
      "s_nop 0\n"
      "v_readfirstlane_b32 s29, v24\n"
      "s_nop 3\n"
      "s_cmp_eq_u32 s29, 0\n"
      "s_cbranch_scc1 L_poll_19_%=\n"
      "s_waitcnt lgkmcnt(0)\n"
      "s_add_u32 s31, s8, s95\n"
      "v_add_u32 v12, s31, v2\n"
      "ds_read_b128 v[24:27], v12 offset:46080\n"
      "ds_read_b128 v[32:35], v12 offset:48000\n"
      "ds_read_b128 v[36:39], v12 offset:48848\n"
      "s_waitcnt lgkmcnt(2)\n"
      "v_or3_b32 v24, v24, v25, v26\n"
      "s_waitcnt lgkmcnt(1)\n"
      "v_or3_b32 v24, v24, v27, v32\n"
      "v_or3_b32 v24, v24, v33, v34\n"
      "s_waitcnt lgkmcnt(0)\n"
      "v_or3_b32 v24, v24, v35, v36\n"
      "v_or3_b32 v24, v24, v37, v38\n"
      "v_bitop3_b32 v24, v24, v39, s15 bitop3:0xa8\n"
      "ds_write_b32 v1, v24 offset:2304\n"
      "s_waitcnt vmcnt(29)\n"
      "ds_write_b128 v16, a[0:3]\n"
      "s_waitcnt vmcnt(27)\n"
      "ds_write_b128 v16, a[4:7] offset:1024\n"
      "ds_read_b64_tr_b16 a[0:1], v20\n"
      "ds_read_b64_tr_b16 a[2:3], v20 offset:256\n"
      "ds_read_b64_tr_b16 a[4:5], v20 offset:32\n"
      "ds_read_b64_tr_b16 a[6:7], v20 offset:288\n"
      "ds_write_b128 v16, a[48:51]\n"
      "s_waitcnt vmcnt(26)\n"
      "ds_write_b128 v16, a[52:55] offset:1024\n"
      "ds_read_b64_tr_b16 a[48:49], v20\n"
      "ds_read_b64_tr_b16 a[50:51], v20 offset:256\n"
      "ds_read_b64_tr_b16 a[52:53], v20 offset:32\n"
      "ds_read_b64_tr_b16 a[54:55], v20 offset:288\n"
      "s_add_u32 s34, s6, 36864\n"
      "s_addc_u32 s35, s7, 0\n"
      "global_load_dwordx4 a[120:123], v15, s[34:35]\n"
      "s_add_u32 s34, s6, 43008\n"
      "s_addc_u32 s35, s7, 0\n"
      "global_load_dwordx4 a[124:127], v15, s[34:35]\n"
      "s_waitcnt vmcnt(27)\n"
      "ds_write_b128 v16, a[8:11]\n"
      "s_waitcnt vmcnt(25)\n"
      "ds_write_b128 v16, a[12:15] offset:1024\n"
      "ds_read_b64_tr_b16 a[8:9], v20\n"
      "ds_read_b64_tr_b16 a[10:11], v20 offset:256\n"
      "ds_read_b64_tr_b16 a[12:13], v20 offset:32\n"
      "ds_read_b64_tr_b16 a[14:15], v20 offset:288\n"
      "ds_write_b128 v16, a[56:59]\n"
      "s_waitcnt vmcnt(24)\n"
      "ds_write_b128 v16, a[60:63] offset:1024\n"
      "ds_read_b64_tr_b16 a[56:57], v20\n"
      "ds_read_b64_tr_b16 a[58:59], v20 offset:256\n"
      "ds_read_b64_tr_b16 a[60:61], v20 offset:32\n"
      "ds_read_b64_tr_b16 a[62:63], v20 offset:288\n"
      "s_add_u32 s34, s6, 49152\n"
      "s_addc_u32 s35, s7, 0\n"
      "global_load_dwordx4 a[128:131], v15, s[34:35]\n"
      "s_add_u32 s34, s6, 55296\n"
      "s_addc_u32 s35, s7, 0\n"
      "global_load_dwordx4 a[132:135], v15, s[34:35]\n"
      "s_waitcnt vmcnt(25)\n"
      "ds_write_b128 v16, a[16:19]\n"
      "s_waitcnt vmcnt(23)\n"
      "ds_write_b128 v16, a[20:23] offset:1024\n"
      "ds_read_b64_tr_b16 a[16:17], v20\n"
      "ds_read_b64_tr_b16 a[18:19], v20 offset:256\n"
      "ds_read_b64_tr_b16 a[20:21], v20 offset:32\n"
      "ds_read_b64_tr_b16 a[22:23], v20 offset:288\n"
      "ds_write_b128 v16, a[64:67]\n"
      "s_waitcnt vmcnt(22)\n"
      "ds_write_b128 v16, a[68:71] offset:1024\n"
      "ds_read_b64_tr_b16 a[64:65], v20\n"
      "ds_read_b64_tr_b16 a[66:67], v20 offset:256\n"
      "ds_read_b64_tr_b16 a[68:69], v20 offset:32\n"
      "ds_read_b64_tr_b16 a[70:71], v20 offset:288\n"
      "s_add_u32 s34, s6, 61440\n"
      "s_addc_u32 s35, s7, 0\n"
      "global_load_dwordx4 a[136:139], v15, s[34:35]\n"
      "s_add_u32 s34, s6, 67584\n"
      "s_addc_u32 s35, s7, 0\n"
      "global_load_dwordx4 a[140:143], v15, s[34:35]\n"
      "s_waitcnt vmcnt(23)\n"
      "ds_write_b128 v16, a[24:27]\n"
      "s_waitcnt vmcnt(21)\n"
      "ds_write_b128 v16, a[28:31] offset:1024\n"
      "ds_read_b64_tr_b16 a[24:25], v20\n"
      "ds_read_b64_tr_b16 a[26:27], v20 offset:256\n"
      "ds_read_b64_tr_b16 a[28:29], v20 offset:32\n"
      "ds_read_b64_tr_b16 a[30:31], v20 offset:288\n"
      "ds_write_b128 v16, a[72:75]\n"
      "s_waitcnt vmcnt(20)\n"
      "ds_write_b128 v16, a[76:79] offset:1024\n"
      "ds_read_b64_tr_b16 a[72:73], v20\n"
      "ds_read_b64_tr_b16 a[74:75], v20 offset:256\n"
      "ds_read_b64_tr_b16 a[76:77], v20 offset:32\n"
      "ds_read_b64_tr_b16 a[78:79], v20 offset:288\n"
      "s_waitcnt vmcnt(19)\n"
      "ds_write_b128 v16, a[32:35]\n"
      "s_waitcnt vmcnt(17)\n"
      "ds_write_b128 v16, a[36:39] offset:1024\n"
      "ds_read_b64_tr_b16 a[32:33], v20\n"
      "ds_read_b64_tr_b16 a[34:35], v20 offset:256\n"
      "ds_read_b64_tr_b16 a[36:37], v20 offset:32\n"
      "ds_read_b64_tr_b16 a[38:39], v20 offset:288\n"
      "ds_write_b128 v16, a[80:83]\n"
      "s_waitcnt vmcnt(16)\n"
      "ds_write_b128 v16, a[84:87] offset:1024\n"
      "ds_read_b64_tr_b16 a[80:81], v20\n"
      "ds_read_b64_tr_b16 a[82:83], v20 offset:256\n"
      "ds_read_b64_tr_b16 a[84:85], v20 offset:32\n"
      "ds_read_b64_tr_b16 a[86:87], v20 offset:288\n"
      "s_waitcnt vmcnt(15)\n"
      "ds_write_b128 v16, a[40:43]\n"
      "s_waitcnt vmcnt(13)\n"
      "ds_write_b128 v16, a[44:47] offset:1024\n"
      "ds_read_b64_tr_b16 a[40:41], v20\n"
      "ds_read_b64_tr_b16 a[42:43], v20 offset:256\n"
      "ds_read_b64_tr_b16 a[44:45], v20 offset:32\n"
      "ds_read_b64_tr_b16 a[46:47], v20 offset:288\n"
      "ds_write_b128 v16, a[88:91]\n"
      "s_waitcnt vmcnt(12)\n"
      "ds_write_b128 v16, a[92:95] offset:1024\n"
      "ds_read_b64_tr_b16 a[88:89], v20\n"
      "ds_read_b64_tr_b16 a[90:91], v20 offset:256\n"
      "ds_read_b64_tr_b16 a[92:93], v20 offset:32\n"
      "ds_read_b64_tr_b16 a[94:95], v20 offset:288\n"
      "v_mov_b32 v24, 0\n"
      "v_mov_b32 v25, 0\n"
      "v_mov_b32_e64 v26, 0\n"
      "v_mov_b32_e64 v27, 0\n"
      "ds_write_b128 v16, v[24:27]\n"
      "ds_write_b128 v16, v[24:27] offset:2048\n"
      "s_mul_i32 s31, s11, 4802\n"
      "s_add_u32 s76, s18, s31\n"
      "s_addc_u32 s77, s19, 0\n"
      "s_and_b32 s76, s76, -16\n"
      "s_sub_u32 s78, s32, s76\n"
      "s_add_u32 s31, s95, -36864\n"
      "v_add_u32_e64 v16, s31, v2\n"
      "v_min_u32_e64 v16, s78, v16\n"
      "s_add_u32 m0, s8, 87808\n"
      "s_add_u32 m0, m0, s95\n"
      "s_nop 0\n"
      "global_load_lds_dwordx4 v16, s[76:77]\n"
      "s_add_u32 s31, s95, -34816\n"
      "v_add_u32_e64 v16, s31, v2\n"
      "v_min_u32_e64 v16, s78, v16\n"
      "s_add_u32 m0, s8, 89856\n"
      "s_add_u32 m0, m0, s95\n"
      "s_nop 0\n"
      "global_load_lds_dwordx4 v16, s[76:77]\n"
      "s_add_u32 s31, s95, -32768\n"
      "v_add_u32 v16, s31, v2\n"
      "v_min_u32_e64 v16, s78, v16\n"
      "s_add_u32 m0, s8, 91904\n"
      "s_add_u32 m0, m0, s95\n"
      "s_mov_b64 exec, s[90:91]\n"
      "global_load_lds_dwordx4 v16, s[76:77]\n"
      "s_mov_b64 exec, -1\n"
      "s_mov_b32 s8, 50688\n"
      "ds_read_b128 v[12:15], v4\n"
      "ds_read_b128 v[16:19], v5\n"
      "ds_read_b128 v[20:23], v4 offset:4096\n"
      "ds_read_b128 v[24:27], v5 offset:4096\n"
      "s_waitcnt lgkmcnt(0)\n"
      "s_barrier\n"
      "s_setprio 1\n"
      "v_mfma_f32_16x16x32_bf16 v[56:59], v[12:15], a[0:3], 0\n"
      "s_add_u32 s28, s28, 256\n"
      "s_lshl_b32 s10, s28, 1\n"
      "s_add_u32 s11, s10, 1\n"
      "s_add_u32 s14, s25, 5120\n"
      "s_min_u32 s14, s14, 5328\n"
      "s_cmp_lt_u32 s27, 9\n"
      "s_cselect_b32 s10, s10, s14\n"
      "s_cselect_b32 s11, s11, s14\n"
      "s_mul_hi_u32 s29, s10, 58835169\n"
      "s_mul_i32 s30, s29, 73\n"
      "s_sub_u32 s30, s10, s30\n"
      "s_mul_i32 s52, s29, 1373568\n"
      "s_mul_i32 s31, s30, 2688\n"
      "s_add_u32 s52, s52, s31\n"
      "s_add_u32 s52, s52, 589824\n"
      "s_cmp_eq_u32 s29, 72\n"
      "s_cselect_b32 s53, 100270464, 0\n"
      "s_cmp_eq_u32 s30, 72\n"
      "s_cselect_b32 s54, 196224, 0\n"
      "s_mul_hi_u32 s29, s11, 58835169\n"
      "s_mul_i32 s30, s29, 73\n"
      "s_sub_u32 s30, s11, s30\n"
      "s_mul_i32 s0, s29, 1373568\n"
      "s_mul_i32 s31, s30, 2688\n"
      "s_add_u32 s0, s0, s31\n"
      "s_add_u32 s0, s0, 589824\n"
      "s_cmp_eq_u32 s29, 72\n"
      "s_cselect_b32 s1, 100270464, 0\n"
      "s_cmp_eq_u32 s30, 72\n"
      "s_cselect_b32 s2, 196224, 0\n"
      "v_mfma_f32_16x16x32_bf16 v[56:59], v[16:19], a[8:11], v[56:59]\n"
      "v_mfma_f32_16x16x32_bf16 v[52:55], v[12:15], a[4:7], 0\n"
      "v_mfma_f32_16x16x32_bf16 v[52:55], v[16:19], a[12:15], v[52:55]\n"
      "v_mfma_f32_16x16x32_bf16 v[48:51], v[12:15], a[48:51], 0\n"
      "v_mfma_f32_16x16x32_bf16 v[48:51], v[16:19], a[56:59], v[48:51]\n"
      "v_mfma_f32_16x16x32_bf16 v[32:35], v[12:15], a[52:55], 0\n"
      "v_add_u32 v12, s43, v0\n"
      "s_or_b32 s14, s44, s45\n"
      "s_cbranch_scc1 L_edge_20_%=\n"
      "L_eb_21_%=:\n"
      "s_add_u32 m0, s33, 24576\n"
      "s_nop 0\n"
      "global_load_lds_dwordx4 v12, s[16:17]\n"
      "v_mfma_f32_16x16x32_bf16 v[32:35], v[16:19], a[60:63], v[32:35]\n"
      "ds_read_b128 v[16:19], v4 offset:8192\n"
      "ds_read_b128 v[36:39], v5 offset:8192\n"
      "v_mfma_f32_16x16x32_bf16 v[56:59], v[20:23], a[16:19], v[56:59]\n"
      "s_add_u32 m0, s33, 28544\n"
      "s_nop 0\n"
      "global_load_lds_dwordx4 v12, s[16:17] offset:128\n"
      "v_mfma_f32_16x16x32_bf16 v[56:59], v[24:27], a[24:27], v[56:59]\n"
      "v_mfma_f32_16x16x32_bf16 v[52:55], v[20:23], a[20:23], v[52:55]\n"
      "s_add_u32 m0, s33, 32512\n"
      "s_nop 0\n"
      "global_load_lds_dwordx4 v12, s[16:17] offset:256\n"
      "v_mfma_f32_16x16x32_bf16 v[52:55], v[24:27], a[28:31], v[52:55]\n"
      "v_mfma_f32_16x16x32_bf16 v[48:51], v[20:23], a[64:67], v[48:51]\n"
      "v_mfma_f32_16x16x32_bf16 v[48:51], v[24:27], a[72:75], v[48:51]\n"
      "v_mfma_f32_16x16x32_bf16 v[32:35], v[20:23], a[68:71], v[32:35]\n"
      "v_mfma_f32_16x16x32_bf16 v[32:35], v[24:27], a[76:79], v[32:35]\n"
      "ds_read_b128 v[12:15], v4 offset:2048\n"
      "ds_read_b128 v[20:23], v5 offset:2048\n"
      "s_waitcnt lgkmcnt(2)\n"
      "v_mfma_f32_16x16x32_bf16 v[56:59], v[16:19], a[32:35], v[56:59]\n"
      "v_mfma_f32_16x16x32_bf16 v[56:59], v[36:39], a[40:43], v[56:59]\n"
      "v_mfma_f32_16x16x32_bf16 v[52:55], v[16:19], a[36:39], v[52:55]\n"
      "v_mfma_f32_16x16x32_bf16 v[52:55], v[36:39], a[44:47], v[52:55]\n"
      "v_mfma_f32_16x16x32_bf16 v[48:51], v[16:19], a[80:83], v[48:51]\n"
      "v_mfma_f32_16x16x32_bf16 v[48:51], v[36:39], a[88:91], v[48:51]\n"
      "v_mfma_f32_16x16x32_bf16 v[32:35], v[16:19], a[84:87], v[32:35]\n"
      "v_mfma_f32_16x16x32_bf16 v[32:35], v[36:39], a[92:95], v[32:35]\n"
      "ds_read_b128 v[16:19], v4 offset:6144\n"
      "ds_read_b128 v[24:27], v5 offset:6144\n"
      "s_waitcnt lgkmcnt(2)\n"
      "v_mfma_f32_16x16x32_bf16 v[36:39], v[12:15], a[0:3], 0\n"
      "v_mfma_f32_16x16x32_bf16 v[36:39], v[20:23], a[8:11], v[36:39]\n"
      "v_mfma_f32_16x16x32_bf16 v[44:47], v[12:15], a[4:7], 0\n"
      "v_mfma_f32_16x16x32_bf16 v[44:47], v[20:23], a[12:15], v[44:47]\n"
      "v_mfma_f32_16x16x32_bf16 v[40:43], v[12:15], a[48:51], 0\n"
      "v_mfma_f32_16x16x32_bf16 v[40:43], v[20:23], a[56:59], v[40:43]\n"
      "v_mfma_f32_16x16x32_bf16 v[60:63], v[12:15], a[52:55], 0\n"
      "v_mfma_f32_16x16x32_bf16 v[60:63], v[20:23], a[60:63], v[60:63]\n"
      "ds_read_b128 v[12:15], v4 offset:10240\n"
      "ds_read_b128 v[20:23], v5 offset:10240\n"
      "s_waitcnt lgkmcnt(2)\n"
      "v_mfma_f32_16x16x32_bf16 v[36:39], v[16:19], a[16:19], v[36:39]\n"
      "v_mfma_f32_16x16x32_bf16 v[36:39], v[24:27], a[24:27], v[36:39]\n"
      "v_mfma_f32_16x16x32_bf16 v[44:47], v[16:19], a[20:23], v[44:47]\n"
      "v_mfma_f32_16x16x32_bf16 v[44:47], v[24:27], a[28:31], v[44:47]\n"
      "v_mfma_f32_16x16x32_bf16 v[40:43], v[16:19], a[64:67], v[40:43]\n"
      "v_mfma_f32_16x16x32_bf16 v[40:43], v[24:27], a[72:75], v[40:43]\n"
      "v_mfma_f32_16x16x32_bf16 v[60:63], v[16:19], a[68:71], v[60:63]\n"
      "v_mfma_f32_16x16x32_bf16 v[60:63], v[24:27], a[76:79], v[60:63]\n"
      "ds_read_b128 v[16:19], v4 offset:36864\n"
      "ds_read_b128 v[24:27], v5 offset:36864\n"
      "s_waitcnt lgkmcnt(2)\n"
      "v_mfma_f32_16x16x32_bf16 v[36:39], v[12:15], a[32:35], v[36:39]\n"
      "v_mfma_f32_16x16x32_bf16 v[36:39], v[20:23], a[40:43], v[36:39]\n"
      "v_mfma_f32_16x16x32_bf16 v[44:47], v[12:15], a[36:39], v[44:47]\n"
      "v_mfma_f32_16x16x32_bf16 v[44:47], v[20:23], a[44:47], v[44:47]\n"
      "v_mfma_f32_16x16x32_bf16 v[40:43], v[12:15], a[80:83], v[40:43]\n"
      "v_mfma_f32_16x16x32_bf16 v[40:43], v[20:23], a[88:91], v[40:43]\n"
      "v_mfma_f32_16x16x32_bf16 v[60:63], v[12:15], a[84:87], v[60:63]\n"
      "v_mfma_f32_16x16x32_bf16 v[60:63], v[20:23], a[92:95], v[60:63]\n"
      "ds_read_b128 v[12:15], v4 offset:40960\n"
      "ds_read_b128 v[20:23], v5 offset:40960\n"
      "s_waitcnt lgkmcnt(2)\n"
      "v_mfma_f32_16x16x32_bf16 v[64:67], a[0:3], v[16:19], 0\n"
      "v_mfma_f32_16x16x32_bf16 v[64:67], a[8:11], v[24:27], v[64:67]\n"
      "v_mfma_f32_16x16x32_bf16 v[68:71], a[4:7], v[16:19], 0\n"
      "v_mfma_f32_16x16x32_bf16 v[68:71], a[12:15], v[24:27], v[68:71]\n"
      "v_mfma_f32_16x16x32_bf16 v[72:75], a[48:51], v[16:19], 0\n"
      "v_cvt_pk_bf16_f32 v56, v56, v57\n"
      "v_mfma_f32_16x16x32_bf16 v[72:75], a[56:59], v[24:27], v[72:75]\n"
      "v_cvt_pk_bf16_f32 v57, v58, v59\n"
      "v_mfma_f32_16x16x32_bf16 v[76:79], a[52:55], v[16:19], 0\n"
      "v_cvt_pk_bf16_f32 v58, v36, v37\n"
      "v_mfma_f32_16x16x32_bf16 v[76:79], a[60:63], v[24:27], v[76:79]\n"
      "ds_read_b128 v[16:19], v4 offset:45056\n"
      "ds_read_b128 v[24:27], v5 offset:45056\n"
      "v_cvt_pk_bf16_f32 v59, v38, v39\n"
      "s_waitcnt lgkmcnt(2)\n"
      "v_mfma_f32_16x16x32_bf16 v[64:67], a[16:19], v[12:15], v[64:67]\n"
      "v_mfma_f32_16x16x32_bf16 v[64:67], a[24:27], v[20:23], v[64:67]\n"
      "v_cvt_pk_bf16_f32 v52, v52, v53\n"
      "v_mfma_f32_16x16x32_bf16 v[68:71], a[20:23], v[12:15], v[68:71]\n"
      "v_cvt_pk_bf16_f32 v53, v54, v55\n"
      "v_mfma_f32_16x16x32_bf16 v[68:71], a[28:31], v[20:23], v[68:71]\n"
      "v_cvt_pk_bf16_f32 v54, v44, v45\n"
      "v_mfma_f32_16x16x32_bf16 v[72:75], a[64:67], v[12:15], v[72:75]\n"
      "v_cvt_pk_bf16_f32 v55, v46, v47\n"
      "v_mfma_f32_16x16x32_bf16 v[72:75], a[72:75], v[20:23], v[72:75]\n"
      "v_mfma_f32_16x16x32_bf16 v[76:79], a[68:71], v[12:15], v[76:79]\n"
      "v_cvt_pk_bf16_f32 v48, v48, v49\n"
      "v_mfma_f32_16x16x32_bf16 v[76:79], a[76:79], v[20:23], v[76:79]\n"
      "ds_read_b128 v[12:15], v4 offset:38912\n"
      "ds_read_b128 v[20:23], v5 offset:38912\n"
      "v_cvt_pk_bf16_f32 v49, v50, v51\n"
      "s_waitcnt lgkmcnt(2)\n"
      "v_mfma_f32_16x16x32_bf16 v[64:67], a[32:35], v[16:19], v[64:67]\n"
      "v_cvt_pk_bf16_f32 v50, v40, v41\n"
      "v_mfma_f32_16x16x32_bf16 v[64:67], a[40:43], v[24:27], v[64:67]\n"
      "v_cvt_pk_bf16_f32 v51, v42, v43\n"
      "v_mfma_f32_16x16x32_bf16 v[68:71], a[36:39], v[16:19], v[68:71]\n"
      "v_mfma_f32_16x16x32_bf16 v[68:71], a[44:47], v[24:27], v[68:71]\n"
      "v_cvt_pk_bf16_f32 v32, v32, v33\n"
      "v_mfma_f32_16x16x32_bf16 v[72:75], a[80:83], v[16:19], v[72:75]\n"
      "v_cvt_pk_bf16_f32 v33, v34, v35\n"
      "v_mfma_f32_16x16x32_bf16 v[72:75], a[88:91], v[24:27], v[72:75]\n"
      "v_cvt_pk_bf16_f32 v34, v60, v61\n"
      "v_mfma_f32_16x16x32_bf16 v[76:79], a[84:87], v[16:19], v[76:79]\n"
      "v_cvt_pk_bf16_f32 v35, v62, v63\n"
      "v_mfma_f32_16x16x32_bf16 v[76:79], a[92:95], v[24:27], v[76:79]\n"
      "ds_read_b128 v[16:19], v4 offset:43008\n"
      "ds_read_b128 v[24:27], v5 offset:43008\n"
      "s_waitcnt lgkmcnt(2)\n"
      "v_mfma_f32_16x16x32_bf16 v[36:39], a[0:3], v[12:15], 0\n"
      "v_mfma_f32_16x16x32_bf16 v[36:39], a[8:11], v[20:23], v[36:39]\n"
      "v_mfma_f32_16x16x32_bf16 v[60:63], a[4:7], v[12:15], 0\n"
      "v_mfma_f32_16x16x32_bf16 v[60:63], a[12:15], v[20:23], v[60:63]\n"
      "v_mfma_f32_16x16x32_bf16 v[40:43], a[48:51], v[12:15], 0\n"
      "v_mfma_f32_16x16x32_bf16 v[40:43], a[56:59], v[20:23], v[40:43]\n"
      "v_mfma_f32_16x16x32_bf16 v[44:47], a[52:55], v[12:15], 0\n"
      "v_mfma_f32_16x16x32_bf16 v[44:47], a[60:63], v[20:23], v[44:47]\n"
      "ds_read_b128 v[12:15], v4 offset:47104\n"
      "ds_read_b128 v[20:23], v5 offset:47104\n"
      "s_waitcnt lgkmcnt(2)\n"
      "v_mfma_f32_16x16x32_bf16 v[36:39], a[16:19], v[16:19], v[36:39]\n"
      "v_mfma_f32_16x16x32_bf16 v[36:39], a[24:27], v[24:27], v[36:39]\n"
      "v_mfma_f32_16x16x32_bf16 v[60:63], a[20:23], v[16:19], v[60:63]\n"
      "v_mfma_f32_16x16x32_bf16 v[60:63], a[28:31], v[24:27], v[60:63]\n"
      "v_mfma_f32_16x16x32_bf16 v[40:43], a[64:67], v[16:19], v[40:43]\n"
      "v_mfma_f32_16x16x32_bf16 v[40:43], a[72:75], v[24:27], v[40:43]\n"
      "v_mfma_f32_16x16x32_bf16 v[44:47], a[68:71], v[16:19], v[44:47]\n"
      "v_mfma_f32_16x16x32_bf16 v[44:47], a[76:79], v[24:27], v[44:47]\n"
      "ds_read_b128 v[24:27], v4 offset:12288\n"
      "ds_read_b128 v[80:83], v5 offset:12288\n"
      "s_waitcnt lgkmcnt(2)\n"
      "v_mfma_f32_16x16x32_bf16 v[36:39], a[32:35], v[12:15], v[36:39]\n"
      "v_mfma_f32_16x16x32_bf16 v[36:39], a[40:43], v[20:23], v[36:39]\n"
      "v_mfma_f32_16x16x32_bf16 v[60:63], a[36:39], v[12:15], v[60:63]\n"
      "v_mfma_f32_16x16x32_bf16 v[60:63], a[44:47], v[20:23], v[60:63]\n"
      "v_mfma_f32_16x16x32_bf16 v[40:43], a[80:83], v[12:15], v[40:43]\n"
      "v_mfma_f32_16x16x32_bf16 v[40:43], a[88:91], v[20:23], v[40:43]\n"
      "v_mfma_f32_16x16x32_bf16 v[44:47], a[84:87], v[12:15], v[44:47]\n"
      "v_mfma_f32_16x16x32_bf16 v[44:47], a[92:95], v[20:23], v[44:47]\n"
      "ds_read_b128 v[84:87], v4 offset:16384\n"
      "ds_read_b128 v[88:91], v5 offset:16384\n"
      "s_and_b32 s29, s24, 3\n"
      "s_lshl_b32 s29, s29, 2\n"
      "v_add_u32 v12, s29, v29\n"
      "v_mov_b32_e64 v13, 1\n"
      "ds_write_b32 v12, v13 offset:32\n"
      "s_waitcnt lgkmcnt(3)\n"
      "v_mfma_f32_16x16x32_bf16 v[12:15], v[24:27], a[0:3], 0\n"
      "v_mfma_f32_16x16x32_bf16 v[12:15], v[80:83], a[8:11], v[12:15]\n"
      "v_mfma_f32_16x16x32_bf16 v[16:19], v[24:27], a[4:7], 0\n"
      "v_mfma_f32_16x16x32_bf16 v[16:19], v[80:83], a[12:15], v[16:19]\n"
      "v_mfma_f32_16x16x32_bf16 v[20:23], v[24:27], a[48:51], 0\n"
      "s_mul_i32 s29, s25, 36864\n"
      "s_add_u32 s29, s29, 1048576\n"
      "v_mfma_f32_16x16x32_bf16 v[20:23], v[80:83], a[56:59], v[20:23]\n"
      "s_add_u32 s56, s20, s29\n"
      "s_addc_u32 s57, s21, 0\n"
      "v_mfma_f32_16x16x32_bf16 v[92:95], v[24:27], a[52:55], 0\n"
      "v_add_u32 v24, 143360, v31\n"
      "v_mfma_f32_16x16x32_bf16 v[92:95], v[80:83], a[60:63], v[92:95]\n"
      "ds_read_b128 v[80:83], v4 offset:20480\n"
      "ds_read_b128 v[96:99], v5 offset:20480\n"
      "v_add_u32 v25, 18432, v31\n"
      "v_cvt_pk_bf16_f32 v64, v64, v65\n"
      "s_waitcnt lgkmcnt(2)\n"
      "v_mfma_f32_16x16x32_bf16 v[12:15], v[84:87], a[16:19], v[12:15]\n"
      "v_cvt_pk_bf16_f32 v65, v66, v67\n"
      "v_mfma_f32_16x16x32_bf16 v[12:15], v[88:91], a[24:27], v[12:15]\n"
      "v_cvt_pk_bf16_f32 v68, v68, v69\n"
      "v_mfma_f32_16x16x32_bf16 v[16:19], v[84:87], a[20:23], v[16:19]\n"
      "v_cvt_pk_bf16_f32 v69, v70, v71\n"
      "v_mfma_f32_16x16x32_bf16 v[16:19], v[88:91], a[28:31], v[16:19]\n"
      "v_cvt_pk_bf16_f32 v72, v72, v73\n"
      "v_mfma_f32_16x16x32_bf16 v[20:23], v[84:87], a[64:67], v[20:23]\n"
      "v_cvt_pk_bf16_f32 v73, v74, v75\n"
      "v_mfma_f32_16x16x32_bf16 v[20:23], v[88:91], a[72:75], v[20:23]\n"
      "v_cvt_pk_bf16_f32 v76, v76, v77\n"
      "v_mfma_f32_16x16x32_bf16 v[92:95], v[84:87], a[68:71], v[92:95]\n"
      "v_cvt_pk_bf16_f32 v77, v78, v79\n"
      "v_mfma_f32_16x16x32_bf16 v[92:95], v[88:91], a[76:79], v[92:95]\n"
      "v_cvt_pk_bf16_f32 v36, v36, v37\n"
      "v_cvt_pk_bf16_f32 v37, v38, v39\n"
      "s_waitcnt lgkmcnt(0)\n"
      "v_mfma_f32_16x16x32_bf16 v[12:15], v[80:83], a[32:35], v[12:15]\n"
      "v_mfma_f32_16x16x32_bf16 v[12:15], v[96:99], a[40:43], v[12:15]\n"
      "v_cvt_pk_bf16_f32 v60, v60, v61\n"
      "v_cvt_pk_bf16_f32 v61, v62, v63\n"
      "v_mfma_f32_16x16x32_bf16 v[16:19], v[80:83], a[36:39], v[16:19]\n"
      "v_mfma_f32_16x16x32_bf16 v[16:19], v[96:99], a[44:47], v[16:19]\n"
      "v_cvt_pk_bf16_f32 v40, v40, v41\n"
      "v_cvt_pk_bf16_f32 v41, v42, v43\n"
      "v_mfma_f32_16x16x32_bf16 v[20:23], v[80:83], a[80:83], v[20:23]\n"
      "v_cvt_pk_bf16_f32 v44, v44, v45\n"
      "v_mfma_f32_16x16x32_bf16 v[20:23], v[96:99], a[88:91], v[20:23]\n"
      "v_cvt_pk_bf16_f32 v45, v46, v47\n"
      "v_mfma_f32_16x16x32_bf16 v[92:95], v[80:83], a[84:87], v[92:95]\n"
      "s_mov_b32 s34, 0xfffcfffc\n"
      "s_mov_b32 s35, 0xfffcfffc\n"
      "s_mov_b64 exec, s[34:35]\n"
      "global_store_dwordx2 v31, v[64:65], s[56:57]\n"
      "global_store_dwordx2 v31, v[68:69], s[56:57] offset:32\n"
      "global_store_dwordx2 v31, v[72:73], s[56:57] offset:64\n"
      "global_store_dwordx2 v31, v[76:77], s[56:57] offset:96\n"
      "s_mov_b32 s34, 0x30003\n"
      "s_mov_b32 s35, 0x30003\n"
      "s_mov_b64 exec, s[34:35]\n"
      "ds_write_b64 v24, v[64:65]\n"
      "ds_write_b64 v24, v[68:69] offset:32\n"
      "ds_write_b64 v24, v[72:73] offset:64\n"
      "ds_write_b64 v24, v[76:77] offset:96\n"
      "s_mov_b64 exec, -1\n"
      "v_mfma_f32_16x16x32_bf16 v[92:95], v[96:99], a[92:95], v[92:95]\n"
      "s_mov_b32 s34, 0x1f001f\n"
      "s_mov_b32 s35, 0x1f001f\n"
      "s_mov_b64 exec, s[34:35]\n"
      "global_store_dwordx2 v25, v[36:37], s[56:57]\n"
      "global_store_dwordx2 v25, v[60:61], s[56:57] offset:32\n"
      "global_store_dwordx2 v25, v[40:41], s[56:57] offset:64\n"
      "global_store_dwordx2 v25, v[44:45], s[56:57] offset:96\n"
      "s_mov_b64 exec, -1\n"
      "ds_read_b32 v25, v1 offset:512\n"
      "ds_read_b32 v24, v1 offset:768\n"
      "v_cvt_pk_bf16_f32 v12, v12, v13\n"
      "v_cvt_pk_bf16_f32 v13, v14, v15\n"
      "v_cvt_pk_bf16_f32 v16, v16, v17\n"
      "v_cvt_pk_bf16_f32 v17, v18, v19\n"
      "v_cvt_pk_bf16_f32 v20, v20, v21\n"
      "v_cvt_pk_bf16_f32 v21, v22, v23\n"
      "v_cvt_pk_bf16_f32 v92, v92, v93\n"
      "v_cvt_pk_bf16_f32 v93, v94, v95\n"
      "s_waitcnt lgkmcnt(0)\n"
      "L_poll_22_%=:\n"
      "ds_read_b128 v[36:39], v29 offset:32\n"
      "s_waitcnt lgkmcnt(0)\n"
      "v_and_b32 v36, v36, v37\n"
      "v_and_b32 v38, v38, v39\n"
      "v_and_b32 v36, v36, v38\n"
      "s_nop 0\n"
      "v_readfirstlane_b32 s29, v36\n"
      "s_nop 3\n"
      "s_cmp_eq_u32 s29, 0\n"
      "s_cbranch_scc1 L_poll_22_%=\n"
      "v_add_u32 v14, s63, v2\n"
      "ds_write_b128 v14, v[32:35]\n"
      "ds_write_b64 v14, v[92:93] offset:1024\n"
      "s_waitcnt vmcnt(8)\n"
      "s_waitcnt lgkmcnt(0)\n"
      "s_barrier\n"
      "s_mov_b32 s56, 0xffff0000\n"
      "s_mov_b32 s57, 0x0\n"
      "s_mov_b32 s58, 0x0\n"
      "s_mov_b32 s59, 0xffff\n"
      "s_setprio 1\n"
      "s_sub_u32 s3, 96512, s8\n"
      "ds_read_b128 v[32:35], v3\n"
      "ds_read_b128 v[36:39], v3 offset:1024\n"
      "ds_read_u16 v14, v24\n"
      "ds_read_u16 v18, v24 offset:32\n"
      "ds_read_u16 v22, v24 offset:64\n"
      "v_mov_b32_e64 v15, 0\n"
      "v_mov_b32_e64 v19, 0\n"
      "v_mov_b32 v23, 0\n"
      "ds_read_b32 v92, v1 offset:1024\n"
      "v_add_u32_e64 v24, s55, v1\n"
      "ds_read_b32 v24, v24\n"
      "s_add_u32 s31, s3, s95\n"
      "v_add_u32 v29, s31, v2\n"
      "ds_read_b128 v[96:99], v29 offset:46080\n"
      "ds_read_b128 v[44:47], v29 offset:48000\n"
      "ds_read_b128 v[40:43], v29 offset:48848\n"
      "s_waitcnt lgkmcnt(2)\n"
      "v_or3_b32 v96, v96, v97, v98\n"
      "v_mfma_f32_16x16x32_bf16 v[28:31], v[8:11], v[32:35], 0\n"
      "s_waitcnt lgkmcnt(1)\n"
      "v_or3_b32 v96, v96, v99, v44\n"
      "v_or3_b32 v96, v96, v45, v46\n"
      "v_mfma_f32_16x16x32_bf16 v[28:31], v[8:11], v[36:39], v[28:31]\n"
      "s_waitcnt lgkmcnt(0)\n"
      "v_or3_b32 v96, v96, v47, v40\n"
      "v_mfma_f32_16x16x32_bf16 v[44:47], v[56:59], v[32:35], 0\n"
      "v_mfma_f32_16x16x32_bf16 v[44:47], v[12:15], v[36:39], v[44:47]\n"
      "v_mfma_f32_16x16x32_bf16 v[60:63], v[52:55], v[32:35], 0\n"
      "v_mfma_f32_16x16x32_bf16 v[60:63], v[16:19], v[36:39], v[60:63]\n"
      "v_mfma_f32_16x16x32_bf16 v[80:83], v[48:51], v[32:35], 0\n"
      "v_mfma_f32_16x16x32_bf16 v[80:83], v[20:23], v[36:39], v[80:83]\n"
      "ds_read_b128 v[32:35], v3 offset:2048\n"
      "ds_read_b128 v[36:39], v3 offset:3072\n"
      "v_rcp_f32 v28, v28\n"
      "s_nop 0\n"
      "v_mul_f32 v44, v28, v44\n"
      "v_mul_f32 v45, v28, v45\n"
      "v_mul_f32 v46, v28, v46\n"
      "v_mul_f32 v47, v28, v47\n"
      "v_cvt_pk_bf16_f32 v44, v44, v45\n"
      "v_cvt_pk_bf16_f32 v45, v46, v47\n"
      "ds_write_b64 v25, v[44:45]\n"
      "v_mul_f32 v60, v28, v60\n"
      "v_mul_f32 v61, v28, v61\n"
      "v_mul_f32 v62, v28, v62\n"
      "v_mul_f32 v63, v28, v63\n"
      "v_cvt_pk_bf16_f32 v60, v60, v61\n"
      "v_cvt_pk_bf16_f32 v61, v62, v63\n"
      "ds_write_b64 v25, v[60:61] offset:1024\n"
      "v_mul_f32 v80, v28, v80\n"
      "v_mul_f32 v81, v28, v81\n"
      "v_mul_f32_e64 v82, v28, v82\n"
      "v_mul_f32 v83, v28, v83\n"
      "v_cvt_pk_bf16_f32 v80, v80, v81\n"
      "v_cvt_pk_bf16_f32 v81, v82, v83\n"
      "ds_write_b64 v25, v[80:81] offset:2048\n"
      "s_waitcnt lgkmcnt(3)\n"
      "v_mfma_f32_16x16x32_bf16 v[28:31], v[8:11], v[32:35], 0\n"
      "v_or3_b32 v96, v96, v41, v42\n"
      "v_bitop3_b32 v96, v96, v43, s15 bitop3:0xa8\n"
      "v_mfma_f32_16x16x32_bf16 v[28:31], v[8:11], v[36:39], v[28:31]\n"
      "ds_write_b32 v1, v96 offset:2304\n"
      "s_lshl_b32 s29, s33, 1\n"
      "s_nop 5\n"
      "v_add_u32_e64 v29, s29, v2\n"
      "v_subrev_u32 v30, 36864, v2\n"
      "v_and_b32 v31, 0x300, v30\n"
      "v_and_b32 v26, 0xf0, v30\n"
      "v_lshl_or_b32 v31, v31, 1, v26\n"
      "s_add_u32 s29, s29, 36864\n"
      "v_add_u32_e64 v31, s29, v31\n"
      "v_mfma_f32_16x16x32_bf16 v[80:83], v[56:59], v[32:35], 0\n"
      "v_mfma_f32_16x16x32_bf16 v[80:83], v[12:15], v[36:39], v[80:83]\n"
      "v_mfma_f32_16x16x32_bf16 v[40:43], v[52:55], v[32:35], 0\n"
      "v_mfma_f32_16x16x32_bf16 v[40:43], v[16:19], v[36:39], v[40:43]\n"
      "v_mfma_f32_16x16x32_bf16 v[60:63], v[48:51], v[32:35], 0\n"
      "v_mfma_f32_16x16x32_bf16 v[60:63], v[20:23], v[36:39], v[60:63]\n"
      "ds_read_b128 v[32:35], v3 offset:4096\n"
      "ds_read_b128 v[36:39], v3 offset:5120\n"
      "v_rcp_f32 v28, v28\n"
      "s_nop 0\n"
      "v_mul_f32 v80, v28, v80\n"
      "v_mul_f32 v81, v28, v81\n"
      "v_mul_f32 v82, v28, v82\n"
      "v_mul_f32 v83, v28, v83\n"
      "v_cvt_pk_bf16_f32 v80, v80, v81\n"
      "v_cvt_pk_bf16_f32 v81, v82, v83\n"
      "ds_write_b64 v25, v[80:81] offset:256\n"
      "v_mul_f32 v40, v28, v40\n"
      "v_mul_f32 v41, v28, v41\n"
      "v_mul_f32 v42, v28, v42\n"
      "v_mul_f32 v43, v28, v43\n"
      "v_cvt_pk_bf16_f32 v40, v40, v41\n"
      "v_cvt_pk_bf16_f32 v41, v42, v43\n"
      "ds_write_b64 v25, v[40:41] offset:1280\n"
      "v_mul_f32 v60, v28, v60\n"
      "v_mul_f32 v61, v28, v61\n"
      "v_mul_f32 v62, v28, v62\n"
      "v_mul_f32_e64 v63, v28, v63\n"
      "v_cvt_pk_bf16_f32 v60, v60, v61\n"
      "v_cvt_pk_bf16_f32 v61, v62, v63\n"
      "ds_write_b64 v25, v[60:61] offset:2304\n"
      "s_waitcnt lgkmcnt(3)\n"
      "v_mfma_f32_16x16x32_bf16 v[60:63], v[8:11], v[32:35], 0\n"
      "ds_write_b128 v29, a[96:99] offset:24576\n"
      "ds_write_b128 v29, a[100:103] offset:25600\n"
      "ds_read_b64_tr_b16 a[96:97], v31 offset:24576\n"
      "ds_read_b64_tr_b16 a[98:99], v31 offset:24832\n"
      "ds_read_b64_tr_b16 a[100:101], v31 offset:24584\n"
      "ds_read_b64_tr_b16 a[102:103], v31 offset:24840\n"
      "ds_write_b128 v29, a[104:107] offset:24576\n"
      "ds_write_b128 v29, a[108:111] offset:25600\n"
      "ds_read_b64_tr_b16 a[104:105], v31 offset:24576\n"
      "ds_read_b64_tr_b16 a[106:107], v31 offset:24832\n"
      "ds_read_b64_tr_b16 a[108:109], v31 offset:24584\n"
      "ds_read_b64_tr_b16 a[110:111], v31 offset:24840\n"
      "v_mfma_f32_16x16x32_bf16 v[60:63], v[8:11], v[36:39], v[60:63]\n"
      "ds_write_b128 v29, a[112:115] offset:24576\n"
      "ds_write_b128 v29, a[116:119] offset:25600\n"
      "ds_read_b64_tr_b16 a[112:113], v31 offset:24576\n"
      "ds_read_b64_tr_b16 a[114:115], v31 offset:24832\n"
      "ds_read_b64_tr_b16 a[116:117], v31 offset:24584\n"
      "ds_read_b64_tr_b16 a[118:119], v31 offset:24840\n"
      "v_mfma_f32_16x16x32_bf16 v[40:43], v[56:59], v[32:35], 0\n"
      "v_mfma_f32_16x16x32_bf16 v[40:43], v[12:15], v[36:39], v[40:43]\n"
      "v_mfma_f32_16x16x32_bf16 v[80:83], v[52:55], v[32:35], 0\n"
      "v_mfma_f32_16x16x32_bf16 v[80:83], v[16:19], v[36:39], v[80:83]\n"
      "v_mfma_f32_16x16x32_bf16 v[44:47], v[48:51], v[32:35], 0\n"
      "v_mfma_f32_16x16x32_bf16 v[44:47], v[20:23], v[36:39], v[44:47]\n"
      "ds_read_b128 v[96:99], v24\n"
      "ds_read_b128 v[76:79], v24 offset:64\n"
      "v_rcp_f32 v60, v60\n"
      "s_nop 0\n"
      "v_mul_f32 v40, v60, v40\n"
      "v_mul_f32 v41, v60, v41\n"
      "v_mul_f32 v42, v60, v42\n"
      "v_mul_f32 v43, v60, v43\n"
      "v_cvt_pk_bf16_f32 v40, v40, v41\n"
      "v_cvt_pk_bf16_f32 v41, v42, v43\n"
      "ds_write_b64 v25, v[40:41] offset:12288\n"
      "v_mul_f32 v80, v60, v80\n"
      "v_mul_f32 v81, v60, v81\n"
      "v_mul_f32 v82, v60, v82\n"
      "v_mul_f32 v83, v60, v83\n"
      "v_cvt_pk_bf16_f32 v80, v80, v81\n"
      "v_cvt_pk_bf16_f32 v81, v82, v83\n"
      "ds_write_b64 v25, v[80:81] offset:13312\n"
      "v_mul_f32_e64 v44, v60, v44\n"
      "v_mul_f32_e64 v45, v60, v45\n"
      "v_mul_f32_e64 v46, v60, v46\n"
      "v_mul_f32 v47, v60, v47\n"
      "v_cvt_pk_bf16_f32 v44, v44, v45\n"
      "v_cvt_pk_bf16_f32 v45, v46, v47\n"
      "ds_write_b64 v25, v[44:45] offset:14336\n"
      "s_waitcnt lgkmcnt(3)\n"
      "v_mfma_f32_16x16x32_bf16 v[44:47], v[96:99], v[8:11], 0\n"
      "ds_write_b128 v29, a[120:123] offset:24576\n"
      "ds_write_b128 v29, a[124:127] offset:25600\n"
      "ds_read_b64_tr_b16 a[120:121], v31 offset:24576\n"
      "ds_read_b64_tr_b16 a[122:123], v31 offset:24832\n"
      "ds_read_b64_tr_b16 a[124:125], v31 offset:24584\n"
      "ds_read_b64_tr_b16 a[126:127], v31 offset:24840\n"
      "ds_write_b128 v29, a[128:131] offset:24576\n"
      "ds_write_b128 v29, a[132:135] offset:25600\n"
      "ds_read_b64_tr_b16 a[128:129], v31 offset:24576\n"
      "ds_read_b64_tr_b16 a[130:131], v31 offset:24832\n"
      "ds_read_b64_tr_b16 a[132:133], v31 offset:24584\n"
      "ds_read_b64_tr_b16 a[134:135], v31 offset:24840\n"
      "v_mfma_f32_16x16x32_bf16 v[44:47], v[76:79], v[8:11], v[44:47]\n"
      "ds_write_b128 v29, a[136:139] offset:24576\n"
      "ds_write_b128 v29, a[140:143] offset:25600\n"
      "ds_read_b64_tr_b16 a[136:137], v31 offset:24576\n"
      "ds_read_b64_tr_b16 a[138:139], v31 offset:24832\n"
      "ds_read_b64_tr_b16 a[140:141], v31 offset:24584\n"
      "ds_read_b64_tr_b16 a[142:143], v31 offset:24840\n"
      "ds_read_b128 v[24:27], v4 offset:24576\n"
      "ds_read_b128 v[28:31], v5 offset:24576\n"
      "ds_read_b128 v[32:35], v4 offset:28672\n"
      "ds_read_b128 v[36:39], v5 offset:28672\n"
      "v_mfma_f32_16x16x32_bf16 v[80:83], v[96:99], v[56:59], 0\n"
      "v_mfma_f32_16x16x32_bf16 v[80:83], v[76:79], v[12:15], v[80:83]\n"
      "v_mfma_f32_16x16x32_bf16 v[12:15], v[96:99], v[52:55], 0\n"
      "v_mfma_f32_16x16x32_bf16 v[12:15], v[76:79], v[16:19], v[12:15]\n"
      "v_mfma_f32_16x16x32_bf16 v[16:19], v[96:99], v[48:51], 0\n"
      "v_mfma_f32_16x16x32_bf16 v[16:19], v[76:79], v[20:23], v[16:19]\n"
      "s_nop 5\n"
      "v_cndmask_b32 v80, v80, v12, s[56:57]\n"
      "s_nop 0\n"
      "v_cndmask_b32 v80, v80, v16, s[58:59]\n"
      "v_mov_b32_dpp v44, v44 quad_perm:[0,0,0,0] row_mask:0xf bank_mask:0xf\n"
      "v_rcp_f32 v44, v44\n"
      "s_nop 0\n"
      "v_mul_f32_e64 v80, v44, v80\n"
      "v_cvt_pk_bf16_f32 v80, v80, v80\n"
      "ds_write_b16 v92, v80\n"
      "s_waitcnt vmcnt(0)\n"
      "s_waitcnt lgkmcnt(0)\n"
      "s_barrier\n"
      "s_nop 7\n"
      "s_nop 7\n"
      "L_B0loop_%=:\n"
      "s_setprio 1\n"
      "v_mfma_f32_16x16x32_bf16 v[40:43], v[24:27], a[0:3], 0\n"
      "v_mfma_f32_16x16x32_bf16 v[40:43], v[28:31], a[8:11], v[40:43]\n"
      "v_mfma_f32_16x16x32_bf16 v[44:47], v[24:27], a[4:7], 0\n"
      "v_mfma_f32_16x16x32_bf16 v[44:47], v[28:31], a[12:15], v[44:47]\n"
      "v_mfma_f32_16x16x32_bf16 v[48:51], v[24:27], a[48:51], 0\n"
      "v_mfma_f32_16x16x32_bf16 v[48:51], v[28:31], a[56:59], v[48:51]\n"
      "v_mfma_f32_16x16x32_bf16 v[12:15], v[24:27], a[52:55], 0\n"
      "ds_read_b32 v16, v1 offset:2560\n"
      "v_add_u32_e64 v17, s52, v0\n"
      "s_or_b32 s14, s53, s54\n"
      "s_cbranch_scc1 L_edge_23_%=\n"
      "L_eb_24_%=:\n"
      "s_add_u32 m0, s33, 0\n"
      "s_nop 0\n"
      "global_load_lds_dwordx4 v17, s[16:17]\n"
      "v_mfma_f32_16x16x32_bf16 v[12:15], v[28:31], a[60:63], v[12:15]\n"
      "ds_read_b128 v[20:23], v4 offset:32768\n"
      "ds_read_b128 v[24:27], v5 offset:32768\n"
      "v_mfma_f32_16x16x32_bf16 v[40:43], v[32:35], a[16:19], v[40:43]\n"
      "v_mfma_f32_16x16x32_bf16 v[40:43], v[36:39], a[24:27], v[40:43]\n"
      "v_mfma_f32_16x16x32_bf16 v[44:47], v[32:35], a[20:23], v[44:47]\n"
      "s_add_u32 m0, s33, 3968\n"
      "s_nop 0\n"
      "global_load_lds_dwordx4 v17, s[16:17] offset:128\n"
      "v_mfma_f32_16x16x32_bf16 v[44:47], v[36:39], a[28:31], v[44:47]\n"
      "v_mfma_f32_16x16x32_bf16 v[48:51], v[32:35], a[64:67], v[48:51]\n"
      "v_mfma_f32_16x16x32_bf16 v[48:51], v[36:39], a[72:75], v[48:51]\n"
      "v_mfma_f32_16x16x32_bf16 v[12:15], v[32:35], a[68:71], v[12:15]\n"
      "s_add_u32 m0, s33, 7936\n"
      "s_nop 0\n"
      "global_load_lds_dwordx4 v17, s[16:17] offset:256\n"
      "v_mfma_f32_16x16x32_bf16 v[12:15], v[36:39], a[76:79], v[12:15]\n"
      "ds_read_b128 v[32:35], v4 offset:26624\n"
      "ds_read_b128 v[36:39], v5 offset:26624\n"
      "s_waitcnt lgkmcnt(2)\n"
      "v_mfma_f32_16x16x32_bf16 v[40:43], v[20:23], a[32:35], v[40:43]\n"
      "v_mfma_f32_16x16x32_bf16 v[40:43], v[24:27], a[40:43], v[40:43]\n"
      "v_mfma_f32_16x16x32_bf16 v[44:47], v[20:23], a[36:39], v[44:47]\n"
      "v_add_u32 v17, s52, v16\n"
      "s_or_b32 s14, s53, s54\n"
      "s_cbranch_scc1 L_edge_25_%=\n"
      "L_eb_26_%=:\n"
      "s_add_u32 m0, s33, 12288\n"
      "s_nop 0\n"
      "global_load_lds_dwordx4 v17, s[16:17]\n"
      "v_mfma_f32_16x16x32_bf16 v[44:47], v[24:27], a[44:47], v[44:47]\n"
      "v_mfma_f32_16x16x32_bf16 v[48:51], v[20:23], a[80:83], v[48:51]\n"
      "v_mfma_f32_16x16x32_bf16 v[48:51], v[24:27], a[88:91], v[48:51]\n"
      "v_mfma_f32_16x16x32_bf16 v[12:15], v[20:23], a[84:87], v[12:15]\n"
      "s_add_u32 m0, s33, 16256\n"
      "s_nop 0\n"
      "global_load_lds_dwordx4 v17, s[16:17] offset:128\n"
      "v_mfma_f32_16x16x32_bf16 v[12:15], v[24:27], a[92:95], v[12:15]\n"
      "ds_read_b128 v[20:23], v4 offset:30720\n"
      "ds_read_b128 v[24:27], v5 offset:30720\n"
      "s_waitcnt lgkmcnt(2)\n"
      "v_mfma_f32_16x16x32_bf16 v[28:31], v[32:35], a[0:3], 0\n"
      "v_mfma_f32_16x16x32_bf16 v[28:31], v[36:39], a[8:11], v[28:31]\n"
      "v_mfma_f32_16x16x32_bf16 v[52:55], v[32:35], a[4:7], 0\n"
      "s_add_u32 m0, s33, 20224\n"
      "s_nop 0\n"
      "global_load_lds_dwordx4 v17, s[16:17] offset:256\n"
      "v_mfma_f32_16x16x32_bf16 v[52:55], v[36:39], a[12:15], v[52:55]\n"
      "v_mfma_f32_16x16x32_bf16 v[16:19], v[32:35], a[48:51], 0\n"
      "v_mfma_f32_16x16x32_bf16 v[16:19], v[36:39], a[56:59], v[16:19]\n"
      "v_mfma_f32_16x16x32_bf16 v[56:59], v[32:35], a[52:55], 0\n"
      "s_mul_i32 s31, s11, 4802\n"
      "s_add_u32 s76, s18, s31\n"
      "s_addc_u32 s77, s19, 0\n"
      "s_and_b32 s76, s76, -16\n"
      "s_sub_u32 s78, s32, s76\n"
      "s_add_u32 s31, s95, -36864\n"
      "v_add_u32 v32, s31, v2\n"
      "v_min_u32 v32, s78, v32\n"
      "s_add_u32 m0, s8, 87808\n"
      "s_add_u32 m0, m0, s95\n"
      "s_nop 0\n"
      "global_load_lds_dwordx4 v32, s[76:77]\n"
      "v_mfma_f32_16x16x32_bf16 v[56:59], v[36:39], a[60:63], v[56:59]\n"
      "ds_read_b128 v[36:39], v4 offset:34816\n"
      "ds_read_b128 v[32:35], v5 offset:34816\n"
      "s_waitcnt lgkmcnt(2)\n"
      "v_mfma_f32_16x16x32_bf16 v[28:31], v[20:23], a[16:19], v[28:31]\n"
      "v_mfma_f32_16x16x32_bf16 v[28:31], v[24:27], a[24:27], v[28:31]\n"
      "v_mfma_f32_16x16x32_bf16 v[52:55], v[20:23], a[20:23], v[52:55]\n"
      "s_add_u32 s31, s95, -34816\n"
      "v_add_u32 v7, s31, v2\n"
      "v_min_u32_e64 v7, s78, v7\n"
      "s_add_u32 m0, s8, 89856\n"
      "s_add_u32 m0, m0, s95\n"
      "s_nop 0\n"
      "global_load_lds_dwordx4 v7, s[76:77]\n"
      "v_mfma_f32_16x16x32_bf16 v[52:55], v[24:27], a[28:31], v[52:55]\n"
      "v_mfma_f32_16x16x32_bf16 v[16:19], v[20:23], a[64:67], v[16:19]\n"
      "v_mfma_f32_16x16x32_bf16 v[16:19], v[24:27], a[72:75], v[16:19]\n"
      "v_mfma_f32_16x16x32_bf16 v[56:59], v[20:23], a[68:71], v[56:59]\n"
      "s_add_u32 s31, s95, -32768\n"
      "v_add_u32_e64 v20, s31, v2\n"
      "v_min_u32_e64 v20, s78, v20\n"
      "s_add_u32 m0, s8, 91904\n"
      "s_add_u32 m0, m0, s95\n"
      "s_mov_b64 exec, s[90:91]\n"
      "global_load_lds_dwordx4 v20, s[76:77]\n"
      "s_mov_b64 exec, -1\n"
      "v_mfma_f32_16x16x32_bf16 v[56:59], v[24:27], a[76:79], v[56:59]\n"
      "ds_read_b128 v[20:23], v6\n"
      "ds_read_b128 v[24:27], v6 offset:2048\n"
      "s_waitcnt lgkmcnt(2)\n"
      "v_mfma_f32_16x16x32_bf16 v[28:31], v[36:39], a[32:35], v[28:31]\n"
      "v_mfma_f32_16x16x32_bf16 v[28:31], v[32:35], a[40:43], v[28:31]\n"
      "v_mfma_f32_16x16x32_bf16 v[52:55], v[36:39], a[36:39], v[52:55]\n"
      "s_lshl_b32 s14, s27, 1\n"
      "s_add_u32 s14, s14, 2\n"
      "s_lshl_b32 s29, s25, 5\n"
      "s_add_u32 s29, s29, s14\n"
      "s_mul_i32 s29, s29, 1152\n"
      "s_add_u32 s29, s29, s95\n"
      "s_add_u32 s29, s29, 1011712\n"
      "s_add_u32 s34, s20, s29\n"
      "s_addc_u32 s35, s21, 0\n"
      "s_add_u32 m0, s95, 143360\n"
      "s_cmp_eq_u32 s95, 0\n"
      "s_cselect_b32 exec_lo, -1, 0xff\n"
      "s_cselect_b32 exec_hi, -1, 0\n"
      "global_load_lds_dwordx4 v2, s[34:35]\n"
      "s_mov_b64 exec, -1\n"
      "v_mfma_f32_16x16x32_bf16 v[52:55], v[32:35], a[44:47], v[52:55]\n"
      "v_mfma_f32_16x16x32_bf16 v[16:19], v[36:39], a[80:83], v[16:19]\n"
      "v_mfma_f32_16x16x32_bf16 v[16:19], v[32:35], a[88:91], v[16:19]\n"
      "v_mfma_f32_16x16x32_bf16 v[56:59], v[36:39], a[84:87], v[56:59]\n"
      "v_mfma_f32_16x16x32_bf16 v[56:59], v[32:35], a[92:95], v[56:59]\n"
      "ds_read_b128 v[32:35], v6 offset:4096\n"
      "ds_read_b128 v[36:39], v6 offset:6144\n"
      "s_waitcnt lgkmcnt(2)\n"
      "v_mfma_f32_16x16x32_bf16 v[60:63], a[96:99], v[20:23], 0\n"
      "v_mfma_f32_16x16x32_bf16 v[60:63], a[104:107], v[24:27], v[60:63]\n"
      "v_mfma_f32_16x16x32_bf16 v[64:67], a[100:103], v[20:23], 0\n"
      "v_mfma_f32_16x16x32_bf16 v[64:67], a[108:111], v[24:27], v[64:67]\n"
      "ds_read_b128 v[20:23], v6 offset:8192\n"
      "ds_read_b128 v[24:27], v6 offset:10240\n"
      "s_waitcnt lgkmcnt(2)\n"
      "v_mfma_f32_16x16x32_bf16 v[60:63], a[112:115], v[32:35], v[60:63]\n"
      "v_cvt_pk_bf16_f32 v40, v40, v41\n"
      "v_cvt_pk_bf16_f32 v41, v42, v43\n"
      "v_cvt_pk_bf16_f32 v42, v28, v29\n"
      "v_mfma_f32_16x16x32_bf16 v[60:63], a[120:123], v[36:39], v[60:63]\n"
      "v_cvt_pk_bf16_f32 v43, v30, v31\n"
      "v_cvt_pk_bf16_f32 v44, v44, v45\n"
      "v_mfma_f32_16x16x32_bf16 v[64:67], a[116:119], v[32:35], v[64:67]\n"
      "v_cvt_pk_bf16_f32 v45, v46, v47\n"
      "v_cvt_pk_bf16_f32 v46, v52, v53\n"
      "v_mfma_f32_16x16x32_bf16 v[64:67], a[124:127], v[36:39], v[64:67]\n"
      "ds_read_b128 v[28:31], v6 offset:256\n"
      "ds_read_b128 v[32:35], v6 offset:2304\n"
      "v_cvt_pk_bf16_f32 v47, v54, v55\n"
      "v_cvt_pk_bf16_f32 v48, v48, v49\n"
      "s_waitcnt lgkmcnt(2)\n"
      "v_mfma_f32_16x16x32_bf16 v[60:63], a[128:131], v[20:23], v[60:63]\n"
      "v_cvt_pk_bf16_f32 v49, v50, v51\n"
      "v_cvt_pk_bf16_f32 v50, v16, v17\n"
      "v_cvt_pk_bf16_f32 v51, v18, v19\n"
      "v_mfma_f32_16x16x32_bf16 v[60:63], a[136:139], v[24:27], v[60:63]\n"
      "v_cvt_pk_bf16_f32 v12, v12, v13\n"
      "v_cvt_pk_bf16_f32 v13, v14, v15\n"
      "v_mfma_f32_16x16x32_bf16 v[64:67], a[132:135], v[20:23], v[64:67]\n"
      "v_cvt_pk_bf16_f32 v14, v56, v57\n"
      "v_cvt_pk_bf16_f32 v15, v58, v59\n"
      "v_mfma_f32_16x16x32_bf16 v[64:67], a[140:143], v[24:27], v[64:67]\n"
      "ds_read_b128 v[16:19], v6 offset:4352\n"
      "ds_read_b128 v[20:23], v6 offset:6400\n"
      "v_add_u32 v24, s63, v2\n"
      "ds_write_b128 v24, v[12:15] offset:24576\n"
      "s_waitcnt lgkmcnt(3)\n"
      "v_mfma_f32_16x16x32_bf16 v[24:27], a[96:99], v[28:31], 0\n"
      "v_mfma_f32_16x16x32_bf16 v[24:27], a[104:107], v[32:35], v[24:27]\n"
      "v_mfma_f32_16x16x32_bf16 v[36:39], a[100:103], v[28:31], 0\n"
      "v_mfma_f32_16x16x32_bf16 v[36:39], a[108:111], v[32:35], v[36:39]\n"
      "ds_read_b128 v[12:15], v6 offset:8448\n"
      "ds_read_b128 v[28:31], v6 offset:10496\n"
      "s_waitcnt lgkmcnt(2)\n"
      "v_mfma_f32_16x16x32_bf16 v[24:27], a[112:115], v[16:19], v[24:27]\n"
      "v_add_f32 v32, v60, v61\n"
      "v_mul_f32 v33, v60, v60\n"
      "v_fmac_f32 v33, v61, v61\n"
      "v_mfma_f32_16x16x32_bf16 v[24:27], a[120:123], v[20:23], v[24:27]\n"
      "v_add_f32 v32, v32, v62\n"
      "v_fmac_f32 v33, v62, v62\n"
      "v_mfma_f32_16x16x32_bf16 v[36:39], a[116:119], v[16:19], v[36:39]\n"
      "v_add_f32 v32, v32, v63\n"
      "v_fmac_f32_e64 v33, v63, v63\n"
      "v_mfma_f32_16x16x32_bf16 v[36:39], a[124:127], v[20:23], v[36:39]\n"
      "ds_read_b128 v[16:19], v6 offset:12288\n"
      "ds_read_b128 v[20:23], v6 offset:14336\n"
      "v_add_f32 v32, v32, v64\n"
      "v_fmac_f32_e64 v33, v64, v64\n"
      "s_waitcnt lgkmcnt(2)\n"
      "v_mfma_f32_16x16x32_bf16 v[24:27], a[128:131], v[12:15], v[24:27]\n"
      "v_add_f32 v32, v32, v65\n"
      "v_fmac_f32 v33, v65, v65\n"
      "v_mfma_f32_16x16x32_bf16 v[24:27], a[136:139], v[28:31], v[24:27]\n"
      "v_add_f32 v32, v32, v66\n"
      "v_fmac_f32 v33, v66, v66\n"
      "v_mfma_f32_16x16x32_bf16 v[36:39], a[132:135], v[12:15], v[36:39]\n"
      "v_add_f32 v32, v32, v67\n"
      "v_fmac_f32 v33, v67, v67\n"
      "v_mfma_f32_16x16x32_bf16 v[36:39], a[140:143], v[28:31], v[36:39]\n"
      "ds_read_b128 v[12:15], v6 offset:16384\n"
      "ds_read_b128 v[28:31], v6 offset:18432\n"
      "v_permlane16_swap_b32 v32, v33\n"
      "v_add_f32_e64 v32, v32, v33\n"
      "s_waitcnt lgkmcnt(2)\n"
      "v_mfma_f32_16x16x32_bf16 v[56:59], a[96:99], v[16:19], 0\n"
      "v_mfma_f32_16x16x32_bf16 v[56:59], a[104:107], v[20:23], v[56:59]\n"
      "v_mfma_f32_16x16x32_bf16 v[52:55], a[100:103], v[16:19], 0\n"
      "v_mfma_f32_16x16x32_bf16 v[52:55], a[108:111], v[20:23], v[52:55]\n"
      "ds_read_b128 v[16:19], v6 offset:20480\n"
      "ds_read_b128 v[20:23], v6 offset:22528\n"
      "s_waitcnt lgkmcnt(2)\n"
      "v_mfma_f32_16x16x32_bf16 v[56:59], a[112:115], v[12:15], v[56:59]\n"
      "v_add_f32 v33, v24, v25\n"
      "v_mul_f32 v34, v24, v24\n"
      "v_fmac_f32 v34, v25, v25\n"
      "v_mfma_f32_16x16x32_bf16 v[56:59], a[120:123], v[28:31], v[56:59]\n"
      "v_add_f32 v33, v33, v26\n"
      "v_fmac_f32 v34, v26, v26\n"
      "v_add_f32 v33, v33, v27\n"
      "v_mfma_f32_16x16x32_bf16 v[52:55], a[116:119], v[12:15], v[52:55]\n"
      "v_fmac_f32 v34, v27, v27\n"
      "v_add_f32 v33, v33, v36\n"
      "v_mfma_f32_16x16x32_bf16 v[52:55], a[124:127], v[28:31], v[52:55]\n"
      "ds_read_b128 v[28:31], v4 offset:14336\n"
      "ds_read_b128 v[68:71], v5 offset:14336\n"
      "v_fmac_f32 v34, v36, v36\n"
      "v_add_f32 v33, v33, v37\n"
      "v_fmac_f32 v34, v37, v37\n"
      "s_waitcnt lgkmcnt(2)\n"
      "v_mfma_f32_16x16x32_bf16 v[56:59], a[128:131], v[16:19], v[56:59]\n"
      "v_add_f32 v33, v33, v38\n"
      "v_fmac_f32 v34, v38, v38\n"
      "v_add_f32 v33, v33, v39\n"
      "v_mfma_f32_16x16x32_bf16 v[56:59], a[136:139], v[20:23], v[56:59]\n"
      "v_fmac_f32 v34, v39, v39\n"
      "s_nop 1\n"
      "v_permlane16_swap_b32 v33, v34\n"
      "v_mfma_f32_16x16x32_bf16 v[52:55], a[132:135], v[16:19], v[52:55]\n"
      "v_add_f32 v33, v33, v34\n"
      "ds_read_b32 v16, v1 offset:1280\n"
      "s_nop 0\n"
      "v_permlane32_swap_b32 v32, v33\n"
      "v_mfma_f32_16x16x32_bf16 v[52:55], a[140:143], v[20:23], v[52:55]\n"
      "ds_read_b128 v[72:75], v4 offset:18432\n"
      "ds_read_b128 v[76:79], v5 offset:18432\n"
      "v_add_f32 v32, v32, v33\n"
      "s_waitcnt lgkmcnt(2)\n"
      "ds_write_b32 v16, v32\n"
      "v_mfma_f32_16x16x32_bf16 v[12:15], v[28:31], a[0:3], 0\n"
      "v_mfma_f32_16x16x32_bf16 v[12:15], v[68:71], a[8:11], v[12:15]\n"
      "v_mfma_f32_16x16x32_bf16 v[16:19], v[28:31], a[4:7], 0\n"
      "v_mfma_f32_16x16x32_bf16 v[16:19], v[68:71], a[12:15], v[16:19]\n"
      "v_mfma_f32_16x16x32_bf16 v[20:23], v[28:31], a[48:51], 0\n"
      "v_add_f32 v32, v56, v57\n"
      "v_mul_f32 v33, v56, v56\n"
      "v_mfma_f32_16x16x32_bf16 v[20:23], v[68:71], a[56:59], v[20:23]\n"
      "v_fmac_f32 v33, v57, v57\n"
      "v_mfma_f32_16x16x32_bf16 v[80:83], v[28:31], a[52:55], 0\n"
      "v_add_f32 v32, v32, v58\n"
      "v_mfma_f32_16x16x32_bf16 v[80:83], v[68:71], a[60:63], v[80:83]\n"
      "ds_read_b128 v[28:31], v4 offset:22528\n"
      "ds_read_b128 v[68:71], v5 offset:22528\n"
      "v_fmac_f32 v33, v58, v58\n"
      "s_waitcnt lgkmcnt(2)\n"
      "v_mfma_f32_16x16x32_bf16 v[12:15], v[72:75], a[16:19], v[12:15]\n"
      "v_add_f32 v32, v32, v59\n"
      "v_mfma_f32_16x16x32_bf16 v[12:15], v[76:79], a[24:27], v[12:15]\n"
      "v_fmac_f32 v33, v59, v59\n"
      "v_add_f32_e64 v32, v32, v52\n"
      "v_mfma_f32_16x16x32_bf16 v[16:19], v[72:75], a[20:23], v[16:19]\n"
      "v_fmac_f32 v33, v52, v52\n"
      "v_mfma_f32_16x16x32_bf16 v[16:19], v[76:79], a[28:31], v[16:19]\n"
      "v_add_f32 v32, v32, v53\n"
      "v_mfma_f32_16x16x32_bf16 v[20:23], v[72:75], a[64:67], v[20:23]\n"
      "v_fmac_f32 v33, v53, v53\n"
      "v_mfma_f32_16x16x32_bf16 v[20:23], v[76:79], a[72:75], v[20:23]\n"
      "v_add_f32 v32, v32, v54\n"
      "v_mfma_f32_16x16x32_bf16 v[80:83], v[72:75], a[68:71], v[80:83]\n"
      "s_setprio 0\n"
      "v_fmac_f32 v33, v54, v54\n"
      "v_add_f32 v32, v32, v55\n"
      "v_mfma_f32_16x16x32_bf16 v[80:83], v[76:79], a[76:79], v[80:83]\n"
      "v_fmac_f32 v33, v55, v55\n"
      "s_waitcnt lgkmcnt(0)\n"
      "v_mfma_f32_16x16x32_bf16 v[12:15], v[28:31], a[32:35], v[12:15]\n"
      "v_permlane16_swap_b32 v32, v33\n"
      "v_mfma_f32_16x16x32_bf16 v[12:15], v[68:71], a[40:43], v[12:15]\n"
      "v_add_f32_e64 v32, v32, v33\n"
      "v_mfma_f32_16x16x32_bf16 v[16:19], v[28:31], a[36:39], v[16:19]\n"
      "ds_read_b32 v34, v1 offset:1280\n"
      "v_mfma_f32_16x16x32_bf16 v[16:19], v[68:71], a[44:47], v[16:19]\n"
      "v_mov_b32 v33, v32\n"
      "s_nop 1\n"
      "v_permlane32_swap_b32_e64 v32, v33\n"
      "v_mfma_f32_16x16x32_bf16 v[20:23], v[28:31], a[80:83], v[20:23]\n"
      "v_add_f32 v32, v32, v33\n"
      "v_mfma_f32_16x16x32_bf16 v[20:23], v[68:71], a[88:91], v[20:23]\n"
      "s_waitcnt lgkmcnt(0)\n"
      "ds_write_b32 v34, v32 offset:512\n"
      "v_mfma_f32_16x16x32_bf16 v[80:83], v[28:31], a[84:87], v[80:83]\n"
      "ds_read_b32 v28, v1 offset:512\n"
      "v_mfma_f32_16x16x32_bf16 v[80:83], v[68:71], a[92:95], v[80:83]\n"
      "ds_read_b32 v29, v1 offset:768\n"
      "v_cvt_pk_bf16_f32 v12, v12, v13\n"
      "v_cvt_pk_bf16_f32 v13, v14, v15\n"
      "v_cvt_pk_bf16_f32 v16, v16, v17\n"
      "v_cvt_pk_bf16_f32 v17, v18, v19\n"
      "v_cvt_pk_bf16_f32 v20, v20, v21\n"
      "v_cvt_pk_bf16_f32 v21, v22, v23\n"
      "s_nop 0\n"
      "v_cvt_pk_bf16_f32 v80, v80, v81\n"
      "v_cvt_pk_bf16_f32 v81, v82, v83\n"
      "v_add_u32 v14, s63, v2\n"
      "ds_write_b64 v14, v[80:81] offset:25600\n"
      "s_waitcnt vmcnt(4)\n"
      "s_waitcnt lgkmcnt(0)\n"
      "s_barrier\n"
      "s_setprio 1\n"
      "v_add_u32 v14, 108800, v2\n"
      "ds_read_b128 v[32:35], v14\n"
      "ds_read_b128 v[80:83], v14 offset:1024\n"
      "ds_read_b32 v30, v1\n"
      "s_waitcnt lgkmcnt(2)\n"
      "v_add_f32 v32, v32, v34\n"
      "v_add_f32_e64 v33, v33, v35\n"
      "ds_read_b128 v[68:71], v3\n"
      "ds_read_b128 v[76:79], v3 offset:1024\n"
      "ds_read_u16 v14, v29 offset:1152\n"
      "ds_read_u16 v18, v29 offset:1184\n"
      "ds_read_u16 v22, v29 offset:1216\n"
      "v_mov_b32 v15, 0\n"
      "v_mov_b32 v19, 0\n"
      "v_mov_b32_e64 v23, 0\n"
      "ds_read_b32 v29, v1 offset:1024\n"
      "v_add_u32 v35, s55, v1\n"
      "ds_read_b32 v35, v35\n"
      "s_waitcnt lgkmcnt(8)\n"
      "v_add_f32 v80, v80, v82\n"
      "v_add_f32 v81, v81, v83\n"
      "v_add_f32 v32, v32, v80\n"
      "v_add_f32 v33, v33, v81\n"
      "v_fma_f32 v80, -v32, v32, s74\n"
      "v_fmac_f32 v80, 0x43400000, v33\n"
      "v_max_f32 v80, s74, v80\n"
      "v_rsq_f32 v80, v80\n"
      "s_nop 0\n"
      "v_mul_f32 v33, 0x43400000, v80\n"
      "v_mul_f32 v34, -v32, v80\n"
      "v_mov_b32 v32, v33\n"
      "v_mov_b32 v31, v34\n"
      "s_nop 0\n"
      "v_permlane32_swap_b32 v33, v32\n"
      "v_permlane32_swap_b32 v34, v31\n"
      "s_waitcnt lgkmcnt(0)\n"
      "v_mfma_f32_16x16x32_bf16 v[80:83], v[8:11], v[68:71], 0\n"
      "v_mov_b32 v72, v33\n"
      "v_mov_b32_e64 v73, v34\n"
      "v_mfma_f32_16x16x32_bf16 v[80:83], v[8:11], v[76:79], v[80:83]\n"
      "v_permlane16_swap_b32 v33, v72\n"
      "v_permlane16_swap_b32 v34, v73\n"
      "v_mfma_f32_16x16x32_bf16 v[84:87], v[40:43], v[68:71], 0\n"
      "v_fma_f32 v60, v60, v33, v34\n"
      "v_fma_f32 v61, v61, v33, v34\n"
      "v_mfma_f32_16x16x32_bf16 v[84:87], v[12:15], v[76:79], v[84:87]\n"
      "v_cvt_pk_bf16_f32 v60, v60, v61\n"
      "v_fma_f32 v62, v62, v33, v34\n"
      "v_mfma_f32_16x16x32_bf16 v[88:91], v[44:47], v[68:71], 0\n"
      "v_fma_f32 v63, v63, v33, v34\n"
      "v_cvt_pk_bf16_f32 v61, v62, v63\n"
      "v_mfma_f32_16x16x32_bf16 v[88:91], v[16:19], v[76:79], v[88:91]\n"
      "v_fma_f32 v64, v64, v33, v34\n"
      "v_fma_f32 v65, v65, v33, v34\n"
      "v_mfma_f32_16x16x32_bf16 v[92:95], v[48:51], v[68:71], 0\n"
      "v_cvt_pk_bf16_f32 v62, v64, v65\n"
      "v_fma_f32 v66, v66, v33, v34\n"
      "v_mfma_f32_16x16x32_bf16 v[92:95], v[20:23], v[76:79], v[92:95]\n"
      "v_fma_f32 v67, v67, v33, v34\n"
      "v_cvt_pk_bf16_f32 v63, v66, v67\n"
      "ds_read_b128 v[64:67], v3 offset:2048\n"
      "ds_read_b128 v[76:79], v3 offset:3072\n"
      "ds_write_b128 v30, v[60:63]\n"
      "v_rcp_f32 v80, v80\n"
      "s_nop 0\n"
      "v_mul_f32 v84, v80, v84\n"
      "v_mul_f32 v85, v80, v85\n"
      "v_mul_f32 v86, v80, v86\n"
      "v_mul_f32 v87, v80, v87\n"
      "v_cvt_pk_bf16_f32 v84, v84, v85\n"
      "v_cvt_pk_bf16_f32 v85, v86, v87\n"
      "ds_write_b64 v28, v[84:85] offset:24576\n"
      "v_mul_f32 v88, v80, v88\n"
      "v_mul_f32 v89, v80, v89\n"
      "v_mul_f32 v90, v80, v90\n"
      "v_mul_f32 v91, v80, v91\n"
      "v_cvt_pk_bf16_f32 v88, v88, v89\n"
      "v_cvt_pk_bf16_f32 v89, v90, v91\n"
      "ds_write_b64 v28, v[88:89] offset:25600\n"
      "v_mul_f32 v92, v80, v92\n"
      "v_mul_f32 v93, v80, v93\n"
      "v_mul_f32_e64 v94, v80, v94\n"
      "v_mul_f32 v95, v80, v95\n"
      "v_cvt_pk_bf16_f32 v92, v92, v93\n"
      "v_cvt_pk_bf16_f32 v93, v94, v95\n"
      "ds_write_b64 v28, v[92:93] offset:26624\n"
      "s_waitcnt lgkmcnt(3)\n"
      "v_mfma_f32_16x16x32_bf16 v[92:95], v[8:11], v[64:67], 0\n"
      "v_fma_f32 v24, v24, v72, v73\n"
      "v_fma_f32 v25, v25, v72, v73\n"
      "v_mfma_f32_16x16x32_bf16 v[92:95], v[8:11], v[76:79], v[92:95]\n"
      "v_cvt_pk_bf16_f32 v24, v24, v25\n"
      "v_fma_f32 v26, v26, v72, v73\n"
      "v_mfma_f32_16x16x32_bf16 v[88:91], v[40:43], v[64:67], 0\n"
      "v_fma_f32 v27, v27, v72, v73\n"
      "v_cvt_pk_bf16_f32 v25, v26, v27\n"
      "v_mfma_f32_16x16x32_bf16 v[88:91], v[12:15], v[76:79], v[88:91]\n"
      "v_fma_f32 v36, v36, v72, v73\n"
      "v_fma_f32 v37, v37, v72, v73\n"
      "v_mfma_f32_16x16x32_bf16 v[84:87], v[44:47], v[64:67], 0\n"
      "v_cvt_pk_bf16_f32 v26, v36, v37\n"
      "v_fma_f32 v38, v38, v72, v73\n"
      "v_mfma_f32_16x16x32_bf16 v[84:87], v[16:19], v[76:79], v[84:87]\n"
      "v_fma_f32 v39, v39, v72, v73\n"
      "v_cvt_pk_bf16_f32 v27, v38, v39\n"
      "v_mfma_f32_16x16x32_bf16 v[36:39], v[48:51], v[64:67], 0\n"
      "ds_write_b128 v30, v[24:27] offset:1024\n"
      "v_add_u32 v33, s71, v2\n"
      "v_mfma_f32_16x16x32_bf16 v[36:39], v[20:23], v[76:79], v[36:39]\n"
      "v_add_u32 v34, s72, v1\n"
      "ds_read_b32 v76, v34 offset:1792\n"
      "ds_read_b128 v[64:67], v3 offset:4096\n"
      "ds_read_b128 v[68:71], v3 offset:5120\n"
      "ds_read_b32 v77, v34 offset:2048\n"
      "ds_read_b128 v[60:63], v33\n"
      "v_rcp_f32 v92, v92\n"
      "s_nop 0\n"
      "v_mul_f32 v88, v92, v88\n"
      "v_mul_f32 v89, v92, v89\n"
      "v_mul_f32 v90, v92, v90\n"
      "v_mul_f32 v91, v92, v91\n"
      "v_cvt_pk_bf16_f32 v88, v88, v89\n"
      "v_cvt_pk_bf16_f32 v89, v90, v91\n"
      "ds_write_b64 v28, v[88:89] offset:24832\n"
      "v_mul_f32 v84, v92, v84\n"
      "v_mul_f32 v85, v92, v85\n"
      "v_mul_f32 v86, v92, v86\n"
      "v_mul_f32 v87, v92, v87\n"
      "v_cvt_pk_bf16_f32 v84, v84, v85\n"
      "v_cvt_pk_bf16_f32 v85, v86, v87\n"
      "ds_write_b64 v28, v[84:85] offset:25856\n"
      "v_mul_f32_e64 v36, v92, v36\n"
      "v_mul_f32_e64 v37, v92, v37\n"
      "v_mul_f32_e64 v38, v92, v38\n"
      "v_mul_f32_e64 v39, v92, v39\n"
      "v_cvt_pk_bf16_f32 v36, v36, v37\n"
      "v_cvt_pk_bf16_f32 v37, v38, v39\n"
      "ds_write_b64 v28, v[36:37] offset:26880\n"
      "s_waitcnt lgkmcnt(3)\n"
      "v_mfma_f32_16x16x32_bf16 v[36:39], v[8:11], v[64:67], 0\n"
      "ds_read_b128 v[24:27], v33 offset:1024\n"
      "s_add_u32 s14, s40, s70\n"
      "s_add_u32 s66, s22, s14\n"
      "s_addc_u32 s67, s23, 0\n"
      "v_mfma_f32_16x16x32_bf16 v[36:39], v[8:11], v[68:71], v[36:39]\n"
      "s_or_b32 s14, s41, s42\n"
      "s_cbranch_scc1 L_edge_27_%=\n"
      "L_eb_28_%=:\n"
      "global_store_dwordx4 v76, v[60:63], s[66:67] sc1\n"
      "s_nop 1\n"
      "v_mfma_f32_16x16x32_bf16 v[60:63], v[40:43], v[64:67], 0\n"
      "s_waitcnt lgkmcnt(0)\n"
      "global_store_dwordx4 v77, v[24:27], s[66:67] sc1\n"
      "v_fma_f32 v56, v56, v32, v31\n"
      "v_mfma_f32_16x16x32_bf16 v[60:63], v[12:15], v[68:71], v[60:63]\n"
      "v_fma_f32 v57, v57, v32, v31\n"
      "v_cvt_pk_bf16_f32 v56, v56, v57\n"
      "v_mfma_f32_16x16x32_bf16 v[24:27], v[44:47], v[64:67], 0\n"
      "v_fma_f32 v58, v58, v32, v31\n"
      "v_fma_f32 v59, v59, v32, v31\n"
      "v_mfma_f32_16x16x32_bf16 v[24:27], v[16:19], v[68:71], v[24:27]\n"
      "v_cvt_pk_bf16_f32 v57, v58, v59\n"
      "v_fma_f32 v52, v52, v32, v31\n"
      "v_mfma_f32_16x16x32_bf16 v[84:87], v[48:51], v[64:67], 0\n"
      "v_fma_f32 v53, v53, v32, v31\n"
      "v_cvt_pk_bf16_f32 v58, v52, v53\n"
      "v_mfma_f32_16x16x32_bf16 v[84:87], v[20:23], v[68:71], v[84:87]\n"
      "v_fma_f32 v54, v54, v32, v31\n"
      "v_fma_f32 v55, v55, v32, v31\n"
      "ds_read_b128 v[68:71], v35\n"
      "ds_read_b128 v[64:67], v35 offset:64\n"
      "v_cvt_pk_bf16_f32 v59, v54, v55\n"
      "v_rcp_f32 v36, v36\n"
      "s_nop 0\n"
      "v_mul_f32 v60, v36, v60\n"
      "v_mul_f32 v61, v36, v61\n"
      "v_mul_f32 v62, v36, v62\n"
      "v_mul_f32 v63, v36, v63\n"
      "v_cvt_pk_bf16_f32 v60, v60, v61\n"
      "v_cvt_pk_bf16_f32 v61, v62, v63\n"
      "ds_write_b64 v28, v[60:61] offset:12544\n"
      "v_mul_f32 v24, v36, v24\n"
      "v_mul_f32 v25, v36, v25\n"
      "v_mul_f32 v26, v36, v26\n"
      "v_mul_f32 v27, v36, v27\n"
      "v_cvt_pk_bf16_f32 v24, v24, v25\n"
      "v_cvt_pk_bf16_f32 v25, v26, v27\n"
      "ds_write_b64 v28, v[24:25] offset:13568\n"
      "v_mul_f32 v84, v36, v84\n"
      "v_mul_f32 v85, v36, v85\n"
      "v_mul_f32 v86, v36, v86\n"
      "v_mul_f32 v87, v36, v87\n"
      "v_cvt_pk_bf16_f32 v84, v84, v85\n"
      "v_cvt_pk_bf16_f32 v85, v86, v87\n"
      "ds_write_b64 v28, v[84:85] offset:14592\n"
      "s_waitcnt lgkmcnt(3)\n"
      "v_mfma_f32_16x16x32_bf16 v[36:39], v[68:71], v[8:11], 0\n"
      "ds_write_b128 v30, v[56:59]\n"
      "v_add_u32 v24, s71, v2\n"
      "v_mfma_f32_16x16x32_bf16 v[36:39], v[64:67], v[8:11], v[36:39]\n"
      "v_add_u32_e64 v25, s72, v1\n"
      "ds_read_b32 v26, v25 offset:4608\n"
      "v_mfma_f32_16x16x32_bf16 v[32:35], v[68:71], v[40:43], 0\n"
      "ds_read_b128 v[56:59], v24\n"
      "s_add_u32 s14, s40, s70\n"
      "s_add_u32 s66, s22, s14\n"
      "s_addc_u32 s67, s23, 0\n"
      "v_mfma_f32_16x16x32_bf16 v[32:35], v[64:67], v[12:15], v[32:35]\n"
      "s_or_b32 s14, s41, s42\n"
      "s_waitcnt lgkmcnt(1)\n"
      "s_cbranch_scc1 L_edge_29_%=\n"
      "L_eb_30_%=:\n"
      "s_waitcnt lgkmcnt(0)\n"
      "global_store_dwordx4 v26, v[56:59], s[66:67] sc1\n"
      "s_nop 1\n"
      "v_mfma_f32_16x16x32_bf16 v[56:59], v[68:71], v[44:47], 0\n"
      "s_add_u32 s31, s8, s95\n"
      "v_add_u32 v12, s31, v2\n"
      "ds_read_b128 v[24:27], v12 offset:46080\n"
      "ds_read_b128 v[44:47], v12 offset:48000\n"
      "ds_read_b128 v[40:43], v12 offset:48848\n"
      "s_mov_b32 s49, s43\n"
      "s_mov_b32 s50, s44\n"
      "s_mov_b32 s51, s45\n"
      "s_mov_b32 s40, s52\n"
      "s_mov_b32 s41, s53\n"
      "s_mov_b32 s42, s54\n"
      "s_mov_b32 s43, s0\n"
      "s_mov_b32 s44, s1\n"
      "s_mov_b32 s45, s2\n"
      "v_mfma_f32_16x16x32_bf16 v[56:59], v[64:67], v[16:19], v[56:59]\n"
      "s_waitcnt lgkmcnt(2)\n"
      "v_or3_b32 v24, v24, v25, v26\n"
      "s_waitcnt lgkmcnt(1)\n"
      "v_or3_b32 v24, v24, v27, v44\n"
      "v_mfma_f32_16x16x32_bf16 v[84:87], v[68:71], v[48:51], 0\n"
      "v_or3_b32 v24, v24, v45, v46\n"
      "s_waitcnt lgkmcnt(0)\n"
      "v_or3_b32 v24, v24, v47, v40\n"
      "v_mfma_f32_16x16x32_bf16 v[84:87], v[64:67], v[20:23], v[84:87]\n"
      "v_or3_b32 v24, v24, v41, v42\n"
      "v_bitop3_b32 v24, v24, v43, s15 bitop3:0xa8\n"
      "ds_write_b32 v1, v24 offset:2304\n"
      "ds_read_b128 v[12:15], v4\n"
      "ds_read_b128 v[16:19], v5\n"
      "ds_read_b128 v[20:23], v4 offset:4096\n"
      "ds_read_b128 v[24:27], v5 offset:4096\n"
      "v_cndmask_b32 v32, v32, v56, s[56:57]\n"
      "v_cndmask_b32 v32, v32, v84, s[58:59]\n"
      "v_mov_b32_dpp v36, v36 quad_perm:[0,0,0,0] row_mask:0xf bank_mask:0xf\n"
      "v_rcp_f32 v36, v36\n"
      "s_nop 0\n"
      "v_mul_f32_e64 v32, v36, v32\n"
      "v_cvt_pk_bf16_f32 v32, v32, v32\n"
      "ds_write_b16 v29, v32 offset:384\n"
      "s_waitcnt vmcnt(3)\n"
      "s_add_u32 s94, s94, 768\n"
      "s_waitcnt lgkmcnt(0)\n"
      "s_barrier\n"
      "s_add_u32 s27, s27, 1\n"
      "s_sub_u32 s8, 91648, s8\n"
      "s_cmp_ge_u32 s27, 10\n"
      "s_cbranch_scc1 L_B0tail_%=\n"
      "s_setprio 1\n"
      "v_mfma_f32_16x16x32_bf16 v[32:35], v[12:15], a[0:3], 0\n"
      "s_add_u32 s28, s28, 256\n"
      "s_lshl_b32 s10, s28, 1\n"
      "s_add_u32 s11, s10, 1\n"
      "s_add_u32 s14, s25, 5120\n"
      "s_min_u32 s14, s14, 5328\n"
      "s_cmp_lt_u32 s27, 9\n"
      "s_cselect_b32 s10, s10, s14\n"
      "s_cselect_b32 s11, s11, s14\n"
      "s_mul_hi_u32 s29, s10, 58835169\n"
      "s_mul_i32 s30, s29, 73\n"
      "s_sub_u32 s30, s10, s30\n"
      "s_mul_i32 s52, s29, 1373568\n"
      "s_mul_i32 s31, s30, 2688\n"
      "s_add_u32 s52, s52, s31\n"
      "s_add_u32 s52, s52, 589824\n"
      "s_cmp_eq_u32 s29, 72\n"
      "s_cselect_b32 s53, 100270464, 0\n"
      "s_cmp_eq_u32 s30, 72\n"
      "s_cselect_b32 s54, 196224, 0\n"
      "s_mul_hi_u32 s29, s11, 58835169\n"
      "s_mul_i32 s30, s29, 73\n"
      "s_sub_u32 s30, s11, s30\n"
      "s_mul_i32 s0, s29, 1373568\n"
      "s_mul_i32 s31, s30, 2688\n"
      "s_add_u32 s0, s0, s31\n"
      "s_add_u32 s0, s0, 589824\n"
      "s_cmp_eq_u32 s29, 72\n"
      "s_cselect_b32 s1, 100270464, 0\n"
      "s_cmp_eq_u32 s30, 72\n"
      "s_cselect_b32 s2, 196224, 0\n"
      "v_mfma_f32_16x16x32_bf16 v[32:35], v[16:19], a[8:11], v[32:35]\n"
      "v_mfma_f32_16x16x32_bf16 v[36:39], v[12:15], a[4:7], 0\n"
      "v_mfma_f32_16x16x32_bf16 v[36:39], v[16:19], a[12:15], v[36:39]\n"
      "v_mfma_f32_16x16x32_bf16 v[28:31], v[12:15], a[48:51], 0\n"
      "v_mfma_f32_16x16x32_bf16 v[28:31], v[16:19], a[56:59], v[28:31]\n"
      "v_mfma_f32_16x16x32_bf16 v[40:43], v[12:15], a[52:55], 0\n"
      "v_add_u32 v12, 126208, v2\n"
      "ds_read_b128 v[44:47], v12\n"
      "v_mfma_f32_16x16x32_bf16 v[40:43], v[16:19], a[60:63], v[40:43]\n"
      "ds_read_b128 v[12:15], v4 offset:8192\n"
      "ds_read_b128 v[16:19], v5 offset:8192\n"
      "v_mfma_f32_16x16x32_bf16 v[32:35], v[20:23], a[16:19], v[32:35]\n"
      "v_mfma_f32_16x16x32_bf16 v[32:35], v[24:27], a[24:27], v[32:35]\n"
      "v_mfma_f32_16x16x32_bf16 v[36:39], v[20:23], a[20:23], v[36:39]\n"
      "s_sub_u32 s31, s94, 37632\n"
      "s_add_u32 s34, s20, s31\n"
      "s_addc_u32 s35, s21, 0\n"
      "s_mov_b64 exec, s[90:91]\n"
      "s_waitcnt lgkmcnt(0)\n"
      "global_store_dwordx4 v2, v[44:47], s[34:35] sc1\n"
      "s_mov_b64 exec, -1\n"
      "v_mfma_f32_16x16x32_bf16 v[36:39], v[24:27], a[28:31], v[36:39]\n"
      "v_mfma_f32_16x16x32_bf16 v[28:31], v[20:23], a[64:67], v[28:31]\n"
      "v_mfma_f32_16x16x32_bf16 v[28:31], v[24:27], a[72:75], v[28:31]\n"
      "v_mfma_f32_16x16x32_bf16 v[40:43], v[20:23], a[68:71], v[40:43]\n"
      "v_add_u32 v20, s43, v0\n"
      "s_or_b32 s14, s44, s45\n"
      "s_cbranch_scc1 L_edge_31_%=\n"
      "L_eb_32_%=:\n"
      "s_add_u32 m0, s33, 24576\n"
      "s_nop 0\n"
      "global_load_lds_dwordx4 v20, s[16:17]\n"
      "v_mfma_f32_16x16x32_bf16 v[40:43], v[24:27], a[76:79], v[40:43]\n"
      "ds_read_b128 v[24:27], v4 offset:2048\n"
      "ds_read_b128 v[44:47], v5 offset:2048\n"
      "v_mfma_f32_16x16x32_bf16 v[32:35], v[12:15], a[32:35], v[32:35]\n"
      "v_mfma_f32_16x16x32_bf16 v[32:35], v[16:19], a[40:43], v[32:35]\n"
      "v_mfma_f32_16x16x32_bf16 v[36:39], v[12:15], a[36:39], v[36:39]\n"
      "s_add_u32 m0, s33, 28544\n"
      "s_nop 0\n"
      "global_load_lds_dwordx4 v20, s[16:17] offset:128\n"
      "v_mfma_f32_16x16x32_bf16 v[36:39], v[16:19], a[44:47], v[36:39]\n"
      "v_mfma_f32_16x16x32_bf16 v[28:31], v[12:15], a[80:83], v[28:31]\n"
      "v_mfma_f32_16x16x32_bf16 v[28:31], v[16:19], a[88:91], v[28:31]\n"
      "v_mfma_f32_16x16x32_bf16 v[40:43], v[12:15], a[84:87], v[40:43]\n"
      "s_add_u32 m0, s33, 32512\n"
      "s_nop 0\n"
      "global_load_lds_dwordx4 v20, s[16:17] offset:256\n"
      "v_mfma_f32_16x16x32_bf16 v[40:43], v[16:19], a[92:95], v[40:43]\n"
      "ds_read_b128 v[12:15], v4 offset:6144\n"
      "ds_read_b128 v[16:19], v5 offset:6144\n"
      "s_waitcnt lgkmcnt(2)\n"
      "v_mfma_f32_16x16x32_bf16 v[20:23], v[24:27], a[0:3], 0\n"
      "v_mfma_f32_16x16x32_bf16 v[20:23], v[44:47], a[8:11], v[20:23]\n"
      "v_mfma_f32_16x16x32_bf16 v[48:51], v[24:27], a[4:7], 0\n"
      "v_mfma_f32_16x16x32_bf16 v[48:51], v[44:47], a[12:15], v[48:51]\n"
      "v_mfma_f32_16x16x32_bf16 v[52:55], v[24:27], a[48:51], 0\n"
      "v_mfma_f32_16x16x32_bf16 v[52:55], v[44:47], a[56:59], v[52:55]\n"
      "v_mfma_f32_16x16x32_bf16 v[56:59], v[24:27], a[52:55], 0\n"
      "v_mfma_f32_16x16x32_bf16 v[56:59], v[44:47], a[60:63], v[56:59]\n"
      "ds_read_b128 v[24:27], v4 offset:10240\n"
      "ds_read_b128 v[44:47], v5 offset:10240\n"
      "s_waitcnt lgkmcnt(2)\n"
      "v_mfma_f32_16x16x32_bf16 v[20:23], v[12:15], a[16:19], v[20:23]\n"
      "v_mfma_f32_16x16x32_bf16 v[20:23], v[16:19], a[24:27], v[20:23]\n"
      "v_mfma_f32_16x16x32_bf16 v[48:51], v[12:15], a[20:23], v[48:51]\n"
      "v_mfma_f32_16x16x32_bf16 v[48:51], v[16:19], a[28:31], v[48:51]\n"
      "v_mfma_f32_16x16x32_bf16 v[52:55], v[12:15], a[64:67], v[52:55]\n"
      "v_mfma_f32_16x16x32_bf16 v[52:55], v[16:19], a[72:75], v[52:55]\n"
      "v_mfma_f32_16x16x32_bf16 v[56:59], v[12:15], a[68:71], v[56:59]\n"
      "v_mfma_f32_16x16x32_bf16 v[56:59], v[16:19], a[76:79], v[56:59]\n"
      "ds_read_b128 v[12:15], v6 offset:24576\n"
      "ds_read_b128 v[16:19], v6 offset:26624\n"
      "s_waitcnt lgkmcnt(2)\n"
      "v_mfma_f32_16x16x32_bf16 v[20:23], v[24:27], a[32:35], v[20:23]\n"
      "v_mfma_f32_16x16x32_bf16 v[20:23], v[44:47], a[40:43], v[20:23]\n"
      "v_mfma_f32_16x16x32_bf16 v[48:51], v[24:27], a[36:39], v[48:51]\n"
      "v_mfma_f32_16x16x32_bf16 v[48:51], v[44:47], a[44:47], v[48:51]\n"
      "v_mfma_f32_16x16x32_bf16 v[52:55], v[24:27], a[80:83], v[52:55]\n"
      "v_mfma_f32_16x16x32_bf16 v[52:55], v[44:47], a[88:91], v[52:55]\n"
      "v_mfma_f32_16x16x32_bf16 v[56:59], v[24:27], a[84:87], v[56:59]\n"
      "v_mfma_f32_16x16x32_bf16 v[56:59], v[44:47], a[92:95], v[56:59]\n"
      "ds_read_b128 v[24:27], v6 offset:28672\n"
      "ds_read_b128 v[44:47], v6 offset:30720\n"
      "s_waitcnt lgkmcnt(2)\n"
      "v_mfma_f32_16x16x32_bf16 v[60:63], a[96:99], v[12:15], 0\n"
      "v_mfma_f32_16x16x32_bf16 v[60:63], a[104:107], v[16:19], v[60:63]\n"
      "v_mfma_f32_16x16x32_bf16 v[64:67], a[100:103], v[12:15], 0\n"
      "v_mfma_f32_16x16x32_bf16 v[64:67], a[108:111], v[16:19], v[64:67]\n"
      "ds_read_b128 v[12:15], v6 offset:32768\n"
      "ds_read_b128 v[16:19], v6 offset:34816\n"
      "s_waitcnt lgkmcnt(2)\n"
      "v_mfma_f32_16x16x32_bf16 v[60:63], a[112:115], v[24:27], v[60:63]\n"
      "v_cvt_pk_bf16_f32 v32, v32, v33\n"
      "v_cvt_pk_bf16_f32 v33, v34, v35\n"
      "v_cvt_pk_bf16_f32 v34, v20, v21\n"
      "v_mfma_f32_16x16x32_bf16 v[60:63], a[120:123], v[44:47], v[60:63]\n"
      "v_cvt_pk_bf16_f32 v35, v22, v23\n"
      "v_cvt_pk_bf16_f32 v36, v36, v37\n"
      "v_mfma_f32_16x16x32_bf16 v[64:67], a[116:119], v[24:27], v[64:67]\n"
      "v_cvt_pk_bf16_f32 v37, v38, v39\n"
      "v_cvt_pk_bf16_f32 v38, v48, v49\n"
      "v_mfma_f32_16x16x32_bf16 v[64:67], a[124:127], v[44:47], v[64:67]\n"
      "ds_read_b128 v[20:23], v6 offset:24832\n"
      "ds_read_b128 v[24:27], v6 offset:26880\n"
      "v_cvt_pk_bf16_f32 v39, v50, v51\n"
      "v_cvt_pk_bf16_f32 v28, v28, v29\n"
      "s_waitcnt lgkmcnt(2)\n"
      "v_mfma_f32_16x16x32_bf16 v[60:63], a[128:131], v[12:15], v[60:63]\n"
      "v_cvt_pk_bf16_f32 v29, v30, v31\n"
      "v_cvt_pk_bf16_f32 v30, v52, v53\n"
      "v_cvt_pk_bf16_f32 v31, v54, v55\n"
      "v_mfma_f32_16x16x32_bf16 v[60:63], a[136:139], v[16:19], v[60:63]\n"
      "v_cvt_pk_bf16_f32 v40, v40, v41\n"
      "v_cvt_pk_bf16_f32 v41, v42, v43\n"
      "v_mfma_f32_16x16x32_bf16 v[64:67], a[132:135], v[12:15], v[64:67]\n"
      "v_cvt_pk_bf16_f32 v42, v56, v57\n"
      "v_cvt_pk_bf16_f32 v43, v58, v59\n"
      "v_mfma_f32_16x16x32_bf16 v[64:67], a[140:143], v[16:19], v[64:67]\n"
      "ds_read_b128 v[12:15], v6 offset:28928\n"
      "ds_read_b128 v[16:19], v6 offset:30976\n"
      "v_add_u32 v58, s63, v2\n"
      "ds_write_b128 v58, v[40:43]\n"
      "s_waitcnt lgkmcnt(3)\n"
      "v_mfma_f32_16x16x32_bf16 v[40:43], a[96:99], v[20:23], 0\n"
      "v_mfma_f32_16x16x32_bf16 v[40:43], a[104:107], v[24:27], v[40:43]\n"
      "v_mfma_f32_16x16x32_bf16 v[56:59], a[100:103], v[20:23], 0\n"
      "v_mfma_f32_16x16x32_bf16 v[56:59], a[108:111], v[24:27], v[56:59]\n"
      "ds_read_b128 v[20:23], v6 offset:33024\n"
      "ds_read_b128 v[24:27], v6 offset:35072\n"
      "s_waitcnt lgkmcnt(2)\n"
      "v_mfma_f32_16x16x32_bf16 v[40:43], a[112:115], v[12:15], v[40:43]\n"
      "v_add_f32 v54, v60, v61\n"
      "v_mul_f32 v55, v60, v60\n"
      "v_fmac_f32 v55, v61, v61\n"
      "v_mfma_f32_16x16x32_bf16 v[40:43], a[120:123], v[16:19], v[40:43]\n"
      "v_add_f32 v54, v54, v62\n"
      "v_fmac_f32 v55, v62, v62\n"
      "v_mfma_f32_16x16x32_bf16 v[56:59], a[116:119], v[12:15], v[56:59]\n"
      "v_add_f32 v54, v54, v63\n"
      "v_fmac_f32_e64 v55, v63, v63\n"
      "v_mfma_f32_16x16x32_bf16 v[56:59], a[124:127], v[16:19], v[56:59]\n"
      "ds_read_b128 v[12:15], v6 offset:12544\n"
      "ds_read_b128 v[16:19], v6 offset:14592\n"
      "v_add_f32 v54, v54, v64\n"
      "v_fmac_f32_e64 v55, v64, v64\n"
      "s_waitcnt lgkmcnt(2)\n"
      "v_mfma_f32_16x16x32_bf16 v[40:43], a[128:131], v[20:23], v[40:43]\n"
      "v_add_f32 v54, v54, v65\n"
      "v_fmac_f32 v55, v65, v65\n"
      "v_mfma_f32_16x16x32_bf16 v[40:43], a[136:139], v[24:27], v[40:43]\n"
      "v_add_f32 v54, v54, v66\n"
      "v_fmac_f32 v55, v66, v66\n"
      "v_mfma_f32_16x16x32_bf16 v[56:59], a[132:135], v[20:23], v[56:59]\n"
      "v_add_f32 v54, v54, v67\n"
      "v_fmac_f32 v55, v67, v67\n"
      "v_mfma_f32_16x16x32_bf16 v[56:59], a[140:143], v[24:27], v[56:59]\n"
      "ds_read_b128 v[20:23], v6 offset:16640\n"
      "ds_read_b128 v[24:27], v6 offset:18688\n"
      "v_permlane16_swap_b32 v54, v55\n"
      "v_add_f32_e64 v54, v54, v55\n"
      "s_waitcnt lgkmcnt(2)\n"
      "v_mfma_f32_16x16x32_bf16 v[48:51], a[96:99], v[12:15], 0\n"
      "v_mfma_f32_16x16x32_bf16 v[48:51], a[104:107], v[16:19], v[48:51]\n"
      "v_mfma_f32_16x16x32_bf16 v[44:47], a[100:103], v[12:15], 0\n"
      "v_mfma_f32_16x16x32_bf16 v[44:47], a[108:111], v[16:19], v[44:47]\n"
      "ds_read_b128 v[12:15], v6 offset:20736\n"
      "ds_read_b128 v[16:19], v6 offset:22784\n"
      "s_waitcnt lgkmcnt(2)\n"
      "v_mfma_f32_16x16x32_bf16 v[48:51], a[112:115], v[20:23], v[48:51]\n"
      "v_add_f32 v52, v40, v41\n"
      "v_mul_f32 v53, v40, v40\n"
      "v_fmac_f32 v53, v41, v41\n"
      "v_mfma_f32_16x16x32_bf16 v[48:51], a[120:123], v[24:27], v[48:51]\n"
      "v_add_f32 v52, v52, v42\n"
      "v_fmac_f32 v53, v42, v42\n"
      "v_add_f32 v52, v52, v43\n"
      "v_mfma_f32_16x16x32_bf16 v[44:47], a[116:119], v[20:23], v[44:47]\n"
      "v_fmac_f32 v53, v43, v43\n"
      "v_add_f32 v52, v52, v56\n"
      "v_mfma_f32_16x16x32_bf16 v[44:47], a[124:127], v[24:27], v[44:47]\n"
      "ds_read_b128 v[24:27], v4 offset:12288\n"
      "ds_read_b128 v[68:71], v5 offset:12288\n"
      "v_fmac_f32 v53, v56, v56\n"
      "v_add_f32 v52, v52, v57\n"
      "v_fmac_f32 v53, v57, v57\n"
      "s_waitcnt lgkmcnt(2)\n"
      "v_mfma_f32_16x16x32_bf16 v[48:51], a[128:131], v[12:15], v[48:51]\n"
      "v_add_f32 v52, v52, v58\n"
      "v_fmac_f32 v53, v58, v58\n"
      "v_add_f32 v52, v52, v59\n"
      "v_mfma_f32_16x16x32_bf16 v[48:51], a[136:139], v[16:19], v[48:51]\n"
      "v_fmac_f32 v53, v59, v59\n"
      "s_nop 1\n"
      "v_permlane16_swap_b32 v52, v53\n"
      "v_mfma_f32_16x16x32_bf16 v[44:47], a[132:135], v[12:15], v[44:47]\n"
      "v_add_f32 v52, v52, v53\n"
      "ds_read_b32 v12, v1 offset:1280\n"
      "s_nop 0\n"
      "v_permlane32_swap_b32 v54, v52\n"
      "v_mfma_f32_16x16x32_bf16 v[44:47], a[140:143], v[16:19], v[44:47]\n"
      "ds_read_b128 v[72:75], v4 offset:16384\n"
      "ds_read_b128 v[76:79], v5 offset:16384\n"
      "v_add_f32 v54, v54, v52\n"
      "s_waitcnt lgkmcnt(2)\n"
      "ds_write_b32 v12, v54\n"
      "v_mfma_f32_16x16x32_bf16 v[12:15], v[24:27], a[0:3], 0\n"
      "v_mfma_f32_16x16x32_bf16 v[12:15], v[68:71], a[8:11], v[12:15]\n"
      "v_mfma_f32_16x16x32_bf16 v[16:19], v[24:27], a[4:7], 0\n"
      "v_mfma_f32_16x16x32_bf16 v[16:19], v[68:71], a[12:15], v[16:19]\n"
      "v_mfma_f32_16x16x32_bf16 v[20:23], v[24:27], a[48:51], 0\n"
      "v_add_f32 v53, v48, v49\n"
      "v_mul_f32 v54, v48, v48\n"
      "v_mfma_f32_16x16x32_bf16 v[20:23], v[68:71], a[56:59], v[20:23]\n"
      "v_fmac_f32 v54, v49, v49\n"
      "v_mfma_f32_16x16x32_bf16 v[80:83], v[24:27], a[52:55], 0\n"
      "v_add_f32 v53, v53, v50\n"
      "v_mfma_f32_16x16x32_bf16 v[80:83], v[68:71], a[60:63], v[80:83]\n"
      "ds_read_b128 v[24:27], v4 offset:20480\n"
      "ds_read_b128 v[68:71], v5 offset:20480\n"
      "v_fmac_f32 v54, v50, v50\n"
      "s_waitcnt lgkmcnt(2)\n"
      "v_mfma_f32_16x16x32_bf16 v[12:15], v[72:75], a[16:19], v[12:15]\n"
      "v_add_f32 v53, v53, v51\n"
      "v_mfma_f32_16x16x32_bf16 v[12:15], v[76:79], a[24:27], v[12:15]\n"
      "v_fmac_f32 v54, v51, v51\n"
      "v_add_f32_e64 v53, v53, v44\n"
      "v_mfma_f32_16x16x32_bf16 v[16:19], v[72:75], a[20:23], v[16:19]\n"
      "v_fmac_f32 v54, v44, v44\n"
      "v_mfma_f32_16x16x32_bf16 v[16:19], v[76:79], a[28:31], v[16:19]\n"
      "v_add_f32 v53, v53, v45\n"
      "v_mfma_f32_16x16x32_bf16 v[20:23], v[72:75], a[64:67], v[20:23]\n"
      "v_fmac_f32 v54, v45, v45\n"
      "v_mfma_f32_16x16x32_bf16 v[20:23], v[76:79], a[72:75], v[20:23]\n"
      "v_add_f32 v53, v53, v46\n"
      "v_mfma_f32_16x16x32_bf16 v[80:83], v[72:75], a[68:71], v[80:83]\n"
      "v_fmac_f32 v54, v46, v46\n"
      "s_setprio 0\n"
      "v_add_f32 v53, v53, v47\n"
      "v_mfma_f32_16x16x32_bf16 v[80:83], v[76:79], a[76:79], v[80:83]\n"
      "v_fmac_f32 v54, v47, v47\n"
      "s_waitcnt lgkmcnt(0)\n"
      "v_mfma_f32_16x16x32_bf16 v[12:15], v[24:27], a[32:35], v[12:15]\n"
      "v_permlane16_swap_b32 v53, v54\n"
      "v_mfma_f32_16x16x32_bf16 v[12:15], v[68:71], a[40:43], v[12:15]\n"
      "v_add_f32_e64 v53, v53, v54\n"
      "v_mfma_f32_16x16x32_bf16 v[16:19], v[24:27], a[36:39], v[16:19]\n"
      "ds_read_b32 v76, v1 offset:1280\n"
      "v_mfma_f32_16x16x32_bf16 v[16:19], v[68:71], a[44:47], v[16:19]\n"
      "v_mov_b32 v54, v53\n"
      "s_nop 1\n"
      "v_permlane32_swap_b32_e64 v53, v54\n"
      "v_mfma_f32_16x16x32_bf16 v[20:23], v[24:27], a[80:83], v[20:23]\n"
      "v_add_f32 v53, v53, v54\n"
      "v_mfma_f32_16x16x32_bf16 v[20:23], v[68:71], a[88:91], v[20:23]\n"
      "s_waitcnt lgkmcnt(0)\n"
      "ds_write_b32 v76, v53 offset:512\n"
      "v_mfma_f32_16x16x32_bf16 v[80:83], v[24:27], a[84:87], v[80:83]\n"
      "ds_read_b32 v24, v1 offset:512\n"
      "v_mfma_f32_16x16x32_bf16 v[80:83], v[68:71], a[92:95], v[80:83]\n"
      "ds_read_b32 v25, v1 offset:768\n"
      "v_cvt_pk_bf16_f32 v12, v12, v13\n"
      "v_cvt_pk_bf16_f32 v13, v14, v15\n"
      "v_cvt_pk_bf16_f32 v16, v16, v17\n"
      "v_cvt_pk_bf16_f32 v17, v18, v19\n"
      "v_cvt_pk_bf16_f32 v20, v20, v21\n"
      "v_cvt_pk_bf16_f32 v21, v22, v23\n"
      "s_nop 0\n"
      "v_cvt_pk_bf16_f32 v80, v80, v81\n"
      "v_cvt_pk_bf16_f32 v81, v82, v83\n"
      "v_add_u32 v14, s63, v2\n"
      "ds_write_b64 v14, v[80:81] offset:1024\n"
      "s_waitcnt vmcnt(0)\n"
      "s_waitcnt lgkmcnt(0)\n"
      "s_barrier\n"
      "s_setprio 1\n"
      "s_sub_u32 s3, 96512, s8\n"
      "v_add_u32 v14, 108800, v2\n"
      "ds_read_b128 v[80:83], v14\n"
      "ds_read_b128 v[68:71], v14 offset:1024\n"
      "ds_read_b32 v26, v1\n"
      "s_waitcnt lgkmcnt(2)\n"
      "v_add_f32 v80, v80, v82\n"
      "v_add_f32_e64 v81, v81, v83\n"
      "ds_read_b128 v[76:79], v3\n"
      "ds_read_b128 v[72:75], v3 offset:1024\n"
      "ds_read_u16 v14, v25\n"
      "ds_read_u16 v18, v25 offset:32\n"
      "ds_read_u16 v22, v25 offset:64\n"
      "v_mov_b32 v15, 0\n"
      "v_mov_b32 v19, 0\n"
      "v_mov_b32 v23, 0\n"
      "ds_read_b32 v83, v1 offset:1024\n"
      "v_add_u32 v25, s55, v1\n"
      "ds_read_b32 v25, v25\n"
      "s_waitcnt lgkmcnt(8)\n"
      "v_add_f32 v68, v68, v70\n"
      "v_add_f32 v69, v69, v71\n"
      "v_add_f32 v80, v80, v68\n"
      "v_add_f32 v81, v81, v69\n"
      "v_fma_f32 v68, -v80, v80, s74\n"
      "v_fmac_f32 v68, 0x43400000, v81\n"
      "v_max_f32 v68, s74, v68\n"
      "v_rsq_f32 v68, v68\n"
      "s_nop 0\n"
      "v_mul_f32 v27, 0x43400000, v68\n"
      "v_mul_f32 v81, -v80, v68\n"
      "v_mov_b32 v68, v27\n"
      "v_mov_b32 v80, v81\n"
      "s_nop 0\n"
      "v_permlane32_swap_b32 v27, v68\n"
      "v_permlane32_swap_b32 v81, v80\n"
      "s_waitcnt lgkmcnt(0)\n"
      "v_mfma_f32_16x16x32_bf16 v[52:55], v[8:11], v[76:79], 0\n"
      "v_mov_b32 v69, v27\n"
      "v_mov_b32 v71, v81\n"
      "v_mfma_f32_16x16x32_bf16 v[52:55], v[8:11], v[72:75], v[52:55]\n"
      "v_permlane16_swap_b32 v27, v69\n"
      "v_permlane16_swap_b32 v81, v71\n"
      "v_mfma_f32_16x16x32_bf16 v[84:87], v[32:35], v[76:79], 0\n"
      "v_fma_f32 v60, v60, v27, v81\n"
      "v_fma_f32 v61, v61, v27, v81\n"
      "v_mfma_f32_16x16x32_bf16 v[84:87], v[12:15], v[72:75], v[84:87]\n"
      "v_cvt_pk_bf16_f32 v60, v60, v61\n"
      "v_fma_f32 v62, v62, v27, v81\n"
      "v_mfma_f32_16x16x32_bf16 v[88:91], v[36:39], v[76:79], 0\n"
      "v_fma_f32 v63, v63, v27, v81\n"
      "v_cvt_pk_bf16_f32 v61, v62, v63\n"
      "v_mfma_f32_16x16x32_bf16 v[88:91], v[16:19], v[72:75], v[88:91]\n"
      "v_fma_f32 v64, v64, v27, v81\n"
      "v_fma_f32 v65, v65, v27, v81\n"
      "v_mfma_f32_16x16x32_bf16 v[92:95], v[28:31], v[76:79], 0\n"
      "v_cvt_pk_bf16_f32 v62, v64, v65\n"
      "v_fma_f32 v66, v66, v27, v81\n"
      "v_mfma_f32_16x16x32_bf16 v[92:95], v[20:23], v[72:75], v[92:95]\n"
      "v_fma_f32 v67, v67, v27, v81\n"
      "v_cvt_pk_bf16_f32 v63, v66, v67\n"
      "ds_read_b128 v[64:67], v3 offset:2048\n"
      "ds_read_b128 v[72:75], v3 offset:3072\n"
      "ds_write_b128 v26, v[60:63] offset:24576\n"
      "v_rcp_f32 v52, v52\n"
      "s_nop 0\n"
      "v_mul_f32 v84, v52, v84\n"
      "v_mul_f32 v85, v52, v85\n"
      "v_mul_f32 v86, v52, v86\n"
      "v_mul_f32 v87, v52, v87\n"
      "v_cvt_pk_bf16_f32 v84, v84, v85\n"
      "v_cvt_pk_bf16_f32 v85, v86, v87\n"
      "ds_write_b64 v24, v[84:85]\n"
      "v_mul_f32 v88, v52, v88\n"
      "v_mul_f32 v89, v52, v89\n"
      "v_mul_f32 v90, v52, v90\n"
      "v_mul_f32 v91, v52, v91\n"
      "v_cvt_pk_bf16_f32 v88, v88, v89\n"
      "v_cvt_pk_bf16_f32 v89, v90, v91\n"
      "ds_write_b64 v24, v[88:89] offset:1024\n"
      "v_mul_f32 v92, v52, v92\n"
      "v_mul_f32 v93, v52, v93\n"
      "v_mul_f32_e64 v94, v52, v94\n"
      "v_mul_f32 v95, v52, v95\n"
      "v_cvt_pk_bf16_f32 v92, v92, v93\n"
      "v_cvt_pk_bf16_f32 v93, v94, v95\n"
      "ds_write_b64 v24, v[92:93] offset:2048\n"
      "s_waitcnt lgkmcnt(3)\n"
      "v_mfma_f32_16x16x32_bf16 v[92:95], v[8:11], v[64:67], 0\n"
      "v_fma_f32 v40, v40, v69, v71\n"
      "v_fma_f32 v41, v41, v69, v71\n"
      "v_mfma_f32_16x16x32_bf16 v[92:95], v[8:11], v[72:75], v[92:95]\n"
      "v_cvt_pk_bf16_f32 v40, v40, v41\n"
      "v_fma_f32 v42, v42, v69, v71\n"
      "v_mfma_f32_16x16x32_bf16 v[88:91], v[32:35], v[64:67], 0\n"
      "v_fma_f32 v43, v43, v69, v71\n"
      "v_cvt_pk_bf16_f32 v41, v42, v43\n"
      "v_mfma_f32_16x16x32_bf16 v[88:91], v[12:15], v[72:75], v[88:91]\n"
      "v_fma_f32 v56, v56, v69, v71\n"
      "v_fma_f32 v57, v57, v69, v71\n"
      "v_mfma_f32_16x16x32_bf16 v[84:87], v[36:39], v[64:67], 0\n"
      "v_cvt_pk_bf16_f32 v42, v56, v57\n"
      "v_fma_f32 v58, v58, v69, v71\n"
      "v_mfma_f32_16x16x32_bf16 v[84:87], v[16:19], v[72:75], v[84:87]\n"
      "v_fma_f32 v59, v59, v69, v71\n"
      "v_cvt_pk_bf16_f32 v43, v58, v59\n"
      "v_mfma_f32_16x16x32_bf16 v[56:59], v[28:31], v[64:67], 0\n"
      "ds_write_b128 v26, v[40:43] offset:25600\n"
      "v_add_u32 v27, s71, v2\n"
      "v_mfma_f32_16x16x32_bf16 v[56:59], v[20:23], v[72:75], v[56:59]\n"
      "v_add_u32 v72, s72, v1\n"
      "ds_read_b32 v73, v72 offset:1792\n"
      "ds_read_b128 v[64:67], v3 offset:4096\n"
      "ds_read_b128 v[76:79], v3 offset:5120\n"
      "ds_read_b32 v74, v72 offset:2048\n"
      "ds_read_b128 v[60:63], v27 offset:24576\n"
      "v_rcp_f32 v92, v92\n"
      "s_nop 0\n"
      "v_mul_f32 v88, v92, v88\n"
      "v_mul_f32 v89, v92, v89\n"
      "v_mul_f32 v90, v92, v90\n"
      "v_mul_f32 v91, v92, v91\n"
      "v_cvt_pk_bf16_f32 v88, v88, v89\n"
      "v_cvt_pk_bf16_f32 v89, v90, v91\n"
      "ds_write_b64 v24, v[88:89] offset:256\n"
      "v_mul_f32 v84, v92, v84\n"
      "v_mul_f32 v85, v92, v85\n"
      "v_mul_f32 v86, v92, v86\n"
      "v_mul_f32 v87, v92, v87\n"
      "v_cvt_pk_bf16_f32 v84, v84, v85\n"
      "v_cvt_pk_bf16_f32 v85, v86, v87\n"
      "ds_write_b64 v24, v[84:85] offset:1280\n"
      "v_mul_f32_e64 v56, v92, v56\n"
      "v_mul_f32_e64 v57, v92, v57\n"
      "v_mul_f32_e64 v58, v92, v58\n"
      "v_mul_f32_e64 v59, v92, v59\n"
      "v_cvt_pk_bf16_f32 v56, v56, v57\n"
      "v_cvt_pk_bf16_f32 v57, v58, v59\n"
      "ds_write_b64 v24, v[56:57] offset:2304\n"
      "s_waitcnt lgkmcnt(3)\n"
      "v_mfma_f32_16x16x32_bf16 v[56:59], v[8:11], v[64:67], 0\n"
      "ds_read_b128 v[40:43], v27 offset:25600\n"
      "s_add_u32 s14, s49, s70\n"
      "s_add_u32 s66, s22, s14\n"
      "s_addc_u32 s67, s23, 0\n"
      "v_mfma_f32_16x16x32_bf16 v[56:59], v[8:11], v[76:79], v[56:59]\n"
      "s_or_b32 s14, s50, s51\n"
      "s_cbranch_scc1 L_edge_33_%=\n"
      "L_eb_34_%=:\n"
      "global_store_dwordx4 v73, v[60:63], s[66:67] sc1\n"
      "s_nop 1\n"
      "v_mfma_f32_16x16x32_bf16 v[60:63], v[32:35], v[64:67], 0\n"
      "s_waitcnt lgkmcnt(0)\n"
      "global_store_dwordx4 v74, v[40:43], s[66:67] sc1\n"
      "v_fma_f32 v48, v48, v68, v80\n"
      "v_mfma_f32_16x16x32_bf16 v[60:63], v[12:15], v[76:79], v[60:63]\n"
      "v_fma_f32 v49, v49, v68, v80\n"
      "v_cvt_pk_bf16_f32 v48, v48, v49\n"
      "v_mfma_f32_16x16x32_bf16 v[40:43], v[36:39], v[64:67], 0\n"
      "v_fma_f32 v50, v50, v68, v80\n"
      "v_fma_f32 v51, v51, v68, v80\n"
      "v_mfma_f32_16x16x32_bf16 v[40:43], v[16:19], v[76:79], v[40:43]\n"
      "v_cvt_pk_bf16_f32 v49, v50, v51\n"
      "v_fma_f32 v44, v44, v68, v80\n"
      "v_mfma_f32_16x16x32_bf16 v[72:75], v[28:31], v[64:67], 0\n"
      "v_fma_f32 v45, v45, v68, v80\n"
      "v_cvt_pk_bf16_f32 v50, v44, v45\n"
      "v_mfma_f32_16x16x32_bf16 v[72:75], v[20:23], v[76:79], v[72:75]\n"
      "v_fma_f32 v46, v46, v68, v80\n"
      "v_fma_f32 v47, v47, v68, v80\n"
      "ds_read_b128 v[76:79], v25\n"
      "ds_read_b128 v[64:67], v25 offset:64\n"
      "v_cvt_pk_bf16_f32 v51, v46, v47\n"
      "v_rcp_f32 v56, v56\n"
      "s_nop 0\n"
      "v_mul_f32 v60, v56, v60\n"
      "v_mul_f32 v61, v56, v61\n"
      "v_mul_f32 v62, v56, v62\n"
      "v_mul_f32 v63, v56, v63\n"
      "v_cvt_pk_bf16_f32 v60, v60, v61\n"
      "v_cvt_pk_bf16_f32 v61, v62, v63\n"
      "ds_write_b64 v24, v[60:61] offset:12288\n"
      "v_mul_f32 v40, v56, v40\n"
      "v_mul_f32 v41, v56, v41\n"
      "v_mul_f32 v42, v56, v42\n"
      "v_mul_f32 v43, v56, v43\n"
      "v_cvt_pk_bf16_f32 v40, v40, v41\n"
      "v_cvt_pk_bf16_f32 v41, v42, v43\n"
      "ds_write_b64 v24, v[40:41] offset:13312\n"
      "v_mul_f32_e64 v72, v56, v72\n"
      "v_mul_f32_e64 v73, v56, v73\n"
      "v_mul_f32_e64 v74, v56, v74\n"
      "v_mul_f32 v75, v56, v75\n"
      "v_cvt_pk_bf16_f32 v72, v72, v73\n"
      "v_cvt_pk_bf16_f32 v73, v74, v75\n"
      "ds_write_b64 v24, v[72:73] offset:14336\n"
      "s_waitcnt lgkmcnt(3)\n"
      "v_mfma_f32_16x16x32_bf16 v[72:75], v[76:79], v[8:11], 0\n"
      "ds_write_b128 v26, v[48:51] offset:24576\n"
      "v_add_u32 v26, s71, v2\n"
      "v_mfma_f32_16x16x32_bf16 v[72:75], v[64:67], v[8:11], v[72:75]\n"
      "v_add_u32 v24, s72, v1\n"
      "ds_read_b32 v25, v24 offset:4608\n"
      "v_mfma_f32_16x16x32_bf16 v[40:43], v[76:79], v[32:35], 0\n"
      "ds_read_b128 v[48:51], v26 offset:24576\n"
      "s_add_u32 s14, s49, s70\n"
      "s_add_u32 s66, s22, s14\n"
      "s_addc_u32 s67, s23, 0\n"
      "v_mfma_f32_16x16x32_bf16 v[40:43], v[64:67], v[12:15], v[40:43]\n"
      "s_or_b32 s14, s50, s51\n"
      "s_waitcnt lgkmcnt(1)\n"
      "s_cbranch_scc1 L_edge_35_%=\n"
      "L_eb_36_%=:\n"
      "s_waitcnt lgkmcnt(0)\n"
      "global_store_dwordx4 v25, v[48:51], s[66:67] sc1\n"
      "v_mfma_f32_16x16x32_bf16 v[12:15], v[76:79], v[36:39], 0\n"
      "s_add_u32 s31, s3, s95\n"
      "v_add_u32 v25, s31, v2\n"
      "ds_read_b128 v[36:39], v25 offset:46080\n"
      "ds_read_b128 v[32:35], v25 offset:48000\n"
      "ds_read_b128 v[48:51], v25 offset:48848\n"
      "v_mfma_f32_16x16x32_bf16 v[12:15], v[64:67], v[16:19], v[12:15]\n"
      "s_waitcnt lgkmcnt(2)\n"
      "v_or3_b32 v36, v36, v37, v38\n"
      "s_waitcnt lgkmcnt(1)\n"
      "v_or3_b32 v36, v36, v39, v32\n"
      "v_mfma_f32_16x16x32_bf16 v[16:19], v[76:79], v[28:31], 0\n"
      "v_or3_b32 v36, v36, v33, v34\n"
      "s_waitcnt lgkmcnt(0)\n"
      "v_or3_b32 v36, v36, v35, v48\n"
      "v_mfma_f32_16x16x32_bf16 v[16:19], v[64:67], v[20:23], v[16:19]\n"
      "v_or3_b32 v36, v36, v49, v50\n"
      "v_bitop3_b32 v36, v36, v51, s15 bitop3:0xa8\n"
      "ds_write_b32 v1, v36 offset:2304\n"
      "ds_read_b128 v[24:27], v4 offset:24576\n"
      "ds_read_b128 v[28:31], v5 offset:24576\n"
      "ds_read_b128 v[32:35], v4 offset:28672\n"
      "ds_read_b128 v[36:39], v5 offset:28672\n"
      "v_cndmask_b32 v40, v40, v12, s[56:57]\n"
      "v_cndmask_b32 v40, v40, v16, s[58:59]\n"
      "v_mov_b32_dpp v72, v72 quad_perm:[0,0,0,0] row_mask:0xf bank_mask:0xf\n"
      "v_rcp_f32 v72, v72\n"
      "s_nop 0\n"
      "v_mul_f32 v40, v72, v40\n"
      "v_cvt_pk_bf16_f32 v40, v40, v40\n"
      "ds_write_b16 v83, v40\n"
      "s_waitcnt lgkmcnt(0)\n"
      "s_barrier\n"
      "s_branch L_B0loop_%=\n"
      "L_B0tail_%=:\n"
      "s_cmp_ge_u32 s25, 209\n"
      "s_cbranch_scc1 L_B0drain_%=\n"
      "s_nop 7\n"
      "s_nop 7\n"
      "s_setprio 1\n"
      "v_mfma_f32_16x16x32_bf16 v[28:31], v[12:15], a[0:3], 0\n"
      "v_mfma_f32_16x16x32_bf16 v[28:31], v[16:19], a[8:11], v[28:31]\n"
      "v_mfma_f32_16x16x32_bf16 v[32:35], v[12:15], a[4:7], 0\n"
      "v_mfma_f32_16x16x32_bf16 v[32:35], v[16:19], a[12:15], v[32:35]\n"
      "v_mfma_f32_16x16x32_bf16 v[36:39], v[12:15], a[48:51], 0\n"
      "v_mfma_f32_16x16x32_bf16 v[36:39], v[16:19], a[56:59], v[36:39]\n"
      "v_mfma_f32_16x16x32_bf16 v[40:43], v[12:15], a[52:55], 0\n"
      "v_add_u32 v12, 126208, v2\n"
      "ds_read_b128 v[44:47], v12\n"
      "v_subrev_u32 v12, 36864, v2\n"
      "v_cmp_le_u32 vcc, 0x180, v12\n"
      "v_lshlrev_b32_e64 v12, 5, v12\n"
      "v_subrev_u32 v13, 12272, v12\n"
      "v_cndmask_b32 v12, v12, v13, vcc\n"
      "v_mfma_f32_16x16x32_bf16 v[40:43], v[16:19], a[60:63], v[40:43]\n"
      "ds_read_b128 v[16:19], v4 offset:8192\n"
      "ds_read_b128 v[48:51], v5 offset:8192\n"
      "v_mfma_f32_16x16x32_bf16 v[28:31], v[20:23], a[16:19], v[28:31]\n"
      "v_mfma_f32_16x16x32_bf16 v[28:31], v[24:27], a[24:27], v[28:31]\n"
      "v_mfma_f32_16x16x32_bf16 v[32:35], v[20:23], a[20:23], v[32:35]\n"
      "s_mov_b64 exec, s[90:91]\n"
      "s_waitcnt lgkmcnt(2)\n"
      "ds_write_b128 v12, v[44:47] offset:24864\n"
      "s_mov_b64 exec, -1\n"
      "v_mfma_f32_16x16x32_bf16 v[32:35], v[24:27], a[28:31], v[32:35]\n"
      "v_mfma_f32_16x16x32_bf16 v[36:39], v[20:23], a[64:67], v[36:39]\n"
      "v_mfma_f32_16x16x32_bf16 v[36:39], v[24:27], a[72:75], v[36:39]\n"
      "v_mfma_f32_16x16x32_bf16 v[40:43], v[20:23], a[68:71], v[40:43]\n"
      "v_subrev_u32 v12, 36864, v2\n"
      "v_lshrrev_b32_e64 v13, 9, v12\n"
      "v_bfe_u32 v12, v12, 4, 5\n"
      "v_lshlrev_b32 v13, 4, v13\n"
      "v_mul_u32_u24 v44, 0x180, v12\n"
      "v_add_u32 v13, v13, v44\n"
      "s_mul_i32 s29, s24, 96\n"
      "s_sub_u32 s29, s29, 384\n"
      "v_add_u32 v13, s29, v13\n"
      "v_lshrrev_b32 v20, 1, v12\n"
      "v_lshlrev_b32 v20, 9, v20\n"
      "v_and_b32 v44, 1, v12\n"
      "v_add_u32 v20, v20, v44\n"
      "s_lshl_b32 s29, s25, 1\n"
      "v_add_u32 v20, s29, v20\n"
      "s_add_u32 s29, s25, 5120\n"
      "s_min_u32 s29, s29, 5328\n"
      "v_mov_b32 v44, s29\n"
      "v_cmp_gt_u32 vcc, 20, v12\n"
      "s_nop 1\n"
      "v_cndmask_b32 v20, v44, v20, vcc\n"
      "s_mov_b32 s29, 58835169\n"
      "v_mul_hi_u32 v44, v20, s29\n"
      "v_mul_u32_u24 v12, 73, v44\n"
      "v_sub_u32 v12, v20, v12\n"
      "v_mul_u32_u24 v44, 7, v44\n"
      "v_add_u32 v44, 9, v44\n"
      "v_subrev_u32 v20, 0x1ff, v44\n"
      "v_min_u32_e64 v44, v44, v20\n"
      "v_mul_u32_u24_e64 v12, 7, v12\n"
      "v_add_u32_e64 v12, 9, v12\n"
      "v_subrev_u32 v20, 0x1ff, v12\n"
      "v_min_u32_e64 v12, v12, v20\n"
      "v_mul_u32_u24 v44, 0x1ff, v44\n"
      "v_add_u32_e64 v44, v44, v12\n"
      "v_mul_u32_u24 v44, 0x180, v44\n"
      "s_mul_i32 s29, s25, 12288\n"
      "s_add_u32 s29, s29, 12582912\n"
      "s_add_u32 s34, s20, s29\n"
      "s_addc_u32 s35, s21, 0\n"
      "s_mul_i32 s30, s33, 3\n"
      "s_nop 0\n"
      "s_mov_b32 exec_lo, 0x3ffff\n"
      "s_mov_b32 exec_hi, 0x3ffff\n"
      "s_add_u32 m0, s30, 24576\n"
      "s_nop 0\n"
      "global_load_lds_dwordx4 v13, s[34:35]\n"
      "s_add_u32 m0, s30, 25568\n"
      "s_nop 0\n"
      "global_load_lds_dwordx4 v13, s[34:35] offset:32\n"
      "s_add_u32 m0, s30, 26560\n"
      "s_nop 0\n"
      "global_load_lds_dwordx4 v13, s[34:35] offset:64\n"
      "s_mov_b64 exec, -1\n"
      "v_mfma_f32_16x16x32_bf16 v[40:43], v[24:27], a[76:79], v[40:43]\n"
      "ds_read_b128 v[12:15], v4 offset:2048\n"
      "ds_read_b128 v[20:23], v5 offset:2048\n"
      "s_waitcnt lgkmcnt(2)\n"
      "v_mfma_f32_16x16x32_bf16 v[28:31], v[16:19], a[32:35], v[28:31]\n"
      "v_mfma_f32_16x16x32_bf16 v[28:31], v[48:51], a[40:43], v[28:31]\n"
      "v_mfma_f32_16x16x32_bf16 v[32:35], v[16:19], a[36:39], v[32:35]\n"
      "v_mfma_f32_16x16x32_bf16 v[32:35], v[48:51], a[44:47], v[32:35]\n"
      "v_mfma_f32_16x16x32_bf16 v[36:39], v[16:19], a[80:83], v[36:39]\n"
      "v_mfma_f32_16x16x32_bf16 v[36:39], v[48:51], a[88:91], v[36:39]\n"
      "v_mfma_f32_16x16x32_bf16 v[40:43], v[16:19], a[84:87], v[40:43]\n"
      "v_mfma_f32_16x16x32_bf16 v[40:43], v[48:51], a[92:95], v[40:43]\n"
      "ds_read_b128 v[16:19], v4 offset:6144\n"
      "ds_read_b128 v[48:51], v5 offset:6144\n"
      "s_waitcnt lgkmcnt(2)\n"
      "v_mfma_f32_16x16x32_bf16 v[24:27], v[12:15], a[0:3], 0\n"
      "v_mfma_f32_16x16x32_bf16 v[24:27], v[20:23], a[8:11], v[24:27]\n"
      "v_mfma_f32_16x16x32_bf16 v[52:55], v[12:15], a[4:7], 0\n"
      "v_mfma_f32_16x16x32_bf16 v[52:55], v[20:23], a[12:15], v[52:55]\n"
      "v_mfma_f32_16x16x32_bf16 v[56:59], v[12:15], a[48:51], 0\n"
      "v_mfma_f32_16x16x32_bf16 v[56:59], v[20:23], a[56:59], v[56:59]\n"
      "v_mfma_f32_16x16x32_bf16 v[60:63], v[12:15], a[52:55], 0\n"
      "v_mfma_f32_16x16x32_bf16 v[60:63], v[20:23], a[60:63], v[60:63]\n"
      "ds_read_b128 v[12:15], v4 offset:10240\n"
      "ds_read_b128 v[20:23], v5 offset:10240\n"
      "s_waitcnt lgkmcnt(2)\n"
      "v_mfma_f32_16x16x32_bf16 v[24:27], v[16:19], a[16:19], v[24:27]\n"
      "v_mfma_f32_16x16x32_bf16 v[24:27], v[48:51], a[24:27], v[24:27]\n"
      "v_mfma_f32_16x16x32_bf16 v[52:55], v[16:19], a[20:23], v[52:55]\n"
      "v_mfma_f32_16x16x32_bf16 v[52:55], v[48:51], a[28:31], v[52:55]\n"
      "v_mfma_f32_16x16x32_bf16 v[56:59], v[16:19], a[64:67], v[56:59]\n"
      "v_mfma_f32_16x16x32_bf16 v[56:59], v[48:51], a[72:75], v[56:59]\n"
      "v_mfma_f32_16x16x32_bf16 v[60:63], v[16:19], a[68:71], v[60:63]\n"
      "v_mfma_f32_16x16x32_bf16 v[60:63], v[48:51], a[76:79], v[60:63]\n"
      "ds_read_b128 v[16:19], v6 offset:24576\n"
      "ds_read_b128 v[48:51], v6 offset:26624\n"
      "s_waitcnt lgkmcnt(2)\n"
      "v_mfma_f32_16x16x32_bf16 v[24:27], v[12:15], a[32:35], v[24:27]\n"
      "v_mfma_f32_16x16x32_bf16 v[24:27], v[20:23], a[40:43], v[24:27]\n"
      "v_mfma_f32_16x16x32_bf16 v[52:55], v[12:15], a[36:39], v[52:55]\n"
      "v_mfma_f32_16x16x32_bf16 v[52:55], v[20:23], a[44:47], v[52:55]\n"
      "v_mfma_f32_16x16x32_bf16 v[56:59], v[12:15], a[80:83], v[56:59]\n"
      "v_mfma_f32_16x16x32_bf16 v[56:59], v[20:23], a[88:91], v[56:59]\n"
      "v_mfma_f32_16x16x32_bf16 v[60:63], v[12:15], a[84:87], v[60:63]\n"
      "v_mfma_f32_16x16x32_bf16 v[60:63], v[20:23], a[92:95], v[60:63]\n"
      "ds_read_b128 v[12:15], v6 offset:28672\n"
      "ds_read_b128 v[20:23], v6 offset:30720\n"
      "s_waitcnt lgkmcnt(2)\n"
      "v_mfma_f32_16x16x32_bf16 v[64:67], a[96:99], v[16:19], 0\n"
      "v_mfma_f32_16x16x32_bf16 v[64:67], a[104:107], v[48:51], v[64:67]\n"
      "v_mfma_f32_16x16x32_bf16 v[68:71], a[100:103], v[16:19], 0\n"
      "v_mfma_f32_16x16x32_bf16 v[68:71], a[108:111], v[48:51], v[68:71]\n"
      "ds_read_b128 v[16:19], v6 offset:32768\n"
      "ds_read_b128 v[48:51], v6 offset:34816\n"
      "s_waitcnt lgkmcnt(2)\n"
      "v_mfma_f32_16x16x32_bf16 v[64:67], a[112:115], v[12:15], v[64:67]\n"
      "v_cvt_pk_bf16_f32 v28, v28, v29\n"
      "v_cvt_pk_bf16_f32 v29, v30, v31\n"
      "v_cvt_pk_bf16_f32 v30, v24, v25\n"
      "v_mfma_f32_16x16x32_bf16 v[64:67], a[120:123], v[20:23], v[64:67]\n"
      "v_cvt_pk_bf16_f32 v31, v26, v27\n"
      "v_cvt_pk_bf16_f32 v32, v32, v33\n"
      "v_mfma_f32_16x16x32_bf16 v[68:71], a[116:119], v[12:15], v[68:71]\n"
      "v_cvt_pk_bf16_f32 v33, v34, v35\n"
      "v_cvt_pk_bf16_f32 v34, v52, v53\n"
      "v_mfma_f32_16x16x32_bf16 v[68:71], a[124:127], v[20:23], v[68:71]\n"
      "ds_read_b128 v[12:15], v6 offset:24832\n"
      "ds_read_b128 v[20:23], v6 offset:26880\n"
      "v_cvt_pk_bf16_f32 v35, v54, v55\n"
      "v_cvt_pk_bf16_f32 v36, v36, v37\n"
      "s_waitcnt lgkmcnt(2)\n"
      "v_mfma_f32_16x16x32_bf16 v[64:67], a[128:131], v[16:19], v[64:67]\n"
      "v_cvt_pk_bf16_f32 v37, v38, v39\n"
      "v_cvt_pk_bf16_f32 v38, v56, v57\n"
      "v_cvt_pk_bf16_f32 v39, v58, v59\n"
      "v_mfma_f32_16x16x32_bf16 v[64:67], a[136:139], v[48:51], v[64:67]\n"
      "v_cvt_pk_bf16_f32 v40, v40, v41\n"
      "v_cvt_pk_bf16_f32 v41, v42, v43\n"
      "v_mfma_f32_16x16x32_bf16 v[68:71], a[132:135], v[16:19], v[68:71]\n"
      "v_cvt_pk_bf16_f32 v42, v60, v61\n"
      "v_cvt_pk_bf16_f32 v43, v62, v63\n"
      "v_mfma_f32_16x16x32_bf16 v[68:71], a[140:143], v[48:51], v[68:71]\n"
      "ds_read_b128 v[16:19], v6 offset:28928\n"
      "ds_read_b128 v[48:51], v6 offset:30976\n"
      "v_add_u32_e64 v62, s63, v2\n"
      "ds_write_b128 v62, v[40:43]\n"
      "s_waitcnt lgkmcnt(3)\n"
      "v_mfma_f32_16x16x32_bf16 v[40:43], a[96:99], v[12:15], 0\n"
      "v_mfma_f32_16x16x32_bf16 v[40:43], a[104:107], v[20:23], v[40:43]\n"
      "v_mfma_f32_16x16x32_bf16 v[60:63], a[100:103], v[12:15], 0\n"
      "v_mfma_f32_16x16x32_bf16 v[60:63], a[108:111], v[20:23], v[60:63]\n"
      "ds_read_b128 v[12:15], v6 offset:33024\n"
      "ds_read_b128 v[20:23], v6 offset:35072\n"
      "s_waitcnt lgkmcnt(2)\n"
      "v_mfma_f32_16x16x32_bf16 v[40:43], a[112:115], v[16:19], v[40:43]\n"
      "v_add_f32 v58, v64, v65\n"
      "v_mul_f32 v59, v64, v64\n"
      "v_fmac_f32 v59, v65, v65\n"
      "v_mfma_f32_16x16x32_bf16 v[40:43], a[120:123], v[48:51], v[40:43]\n"
      "v_add_f32 v58, v58, v66\n"
      "v_fmac_f32 v59, v66, v66\n"
      "v_mfma_f32_16x16x32_bf16 v[60:63], a[116:119], v[16:19], v[60:63]\n"
      "v_add_f32 v58, v58, v67\n"
      "v_fmac_f32 v59, v67, v67\n"
      "v_mfma_f32_16x16x32_bf16 v[60:63], a[124:127], v[48:51], v[60:63]\n"
      "ds_read_b128 v[16:19], v6 offset:12544\n"
      "ds_read_b128 v[48:51], v6 offset:14592\n"
      "v_add_f32 v58, v58, v68\n"
      "v_fmac_f32 v59, v68, v68\n"
      "s_waitcnt lgkmcnt(2)\n"
      "v_mfma_f32_16x16x32_bf16 v[40:43], a[128:131], v[12:15], v[40:43]\n"
      "v_add_f32 v58, v58, v69\n"
      "v_fmac_f32 v59, v69, v69\n"
      "v_mfma_f32_16x16x32_bf16 v[40:43], a[136:139], v[20:23], v[40:43]\n"
      "v_add_f32 v58, v58, v70\n"
      "v_fmac_f32 v59, v70, v70\n"
      "v_mfma_f32_16x16x32_bf16 v[60:63], a[132:135], v[12:15], v[60:63]\n"
      "v_add_f32 v58, v58, v71\n"
      "v_fmac_f32_e64 v59, v71, v71\n"
      "v_mfma_f32_16x16x32_bf16 v[60:63], a[140:143], v[20:23], v[60:63]\n"
      "ds_read_b128 v[12:15], v6 offset:16640\n"
      "ds_read_b128 v[20:23], v6 offset:18688\n"
      "v_permlane16_swap_b32 v58, v59\n"
      "v_add_f32_e64 v58, v58, v59\n"
      "s_waitcnt lgkmcnt(2)\n"
      "v_mfma_f32_16x16x32_bf16 v[52:55], a[96:99], v[16:19], 0\n"
      "v_mfma_f32_16x16x32_bf16 v[52:55], a[104:107], v[48:51], v[52:55]\n"
      "v_mfma_f32_16x16x32_bf16 v[24:27], a[100:103], v[16:19], 0\n"
      "v_mfma_f32_16x16x32_bf16 v[24:27], a[108:111], v[48:51], v[24:27]\n"
      "ds_read_b128 v[16:19], v6 offset:20736\n"
      "ds_read_b128 v[48:51], v6 offset:22784\n"
      "s_waitcnt lgkmcnt(2)\n"
      "v_mfma_f32_16x16x32_bf16 v[52:55], a[112:115], v[12:15], v[52:55]\n"
      "v_add_f32 v56, v40, v41\n"
      "v_mul_f32 v57, v40, v40\n"
      "v_fmac_f32 v57, v41, v41\n"
      "v_mfma_f32_16x16x32_bf16 v[52:55], a[120:123], v[20:23], v[52:55]\n"
      "v_add_f32 v56, v56, v42\n"
      "v_fmac_f32 v57, v42, v42\n"
      "v_add_f32_e64 v56, v56, v43\n"
      "v_mfma_f32_16x16x32_bf16 v[24:27], a[116:119], v[12:15], v[24:27]\n"
      "v_fmac_f32 v57, v43, v43\n"
      "v_add_f32 v56, v56, v60\n"
      "v_mfma_f32_16x16x32_bf16 v[24:27], a[124:127], v[20:23], v[24:27]\n"
      "ds_read_b128 v[72:75], v4 offset:12288\n"
      "ds_read_b128 v[76:79], v5 offset:12288\n"
      "v_fmac_f32 v57, v60, v60\n"
      "v_add_f32 v56, v56, v61\n"
      "v_fmac_f32 v57, v61, v61\n"
      "s_waitcnt lgkmcnt(2)\n"
      "v_mfma_f32_16x16x32_bf16 v[52:55], a[128:131], v[16:19], v[52:55]\n"
      "v_add_f32 v56, v56, v62\n"
      "v_fmac_f32 v57, v62, v62\n"
      "v_add_f32 v56, v56, v63\n"
      "v_mfma_f32_16x16x32_bf16 v[52:55], a[136:139], v[48:51], v[52:55]\n"
      "v_fmac_f32 v57, v63, v63\n"
      "s_nop 1\n"
      "v_permlane16_swap_b32 v56, v57\n"
      "v_mfma_f32_16x16x32_bf16 v[24:27], a[132:135], v[16:19], v[24:27]\n"
      "v_add_f32 v56, v56, v57\n"
      "ds_read_b32 v16, v1 offset:1280\n"
      "s_nop 0\n"
      "v_permlane32_swap_b32_e64 v58, v56\n"
      "v_mfma_f32_16x16x32_bf16 v[24:27], a[140:143], v[48:51], v[24:27]\n"
      "ds_read_b128 v[48:51], v4 offset:16384\n"
      "ds_read_b128 v[80:83], v5 offset:16384\n"
      "v_add_f32 v58, v58, v56\n"
      "s_waitcnt lgkmcnt(2)\n"
      "ds_write_b32 v16, v58\n"
      "v_mfma_f32_16x16x32_bf16 v[12:15], v[72:75], a[0:3], 0\n"
      "v_mfma_f32_16x16x32_bf16 v[12:15], v[76:79], a[8:11], v[12:15]\n"
      "v_mfma_f32_16x16x32_bf16 v[16:19], v[72:75], a[4:7], 0\n"
      "v_mfma_f32_16x16x32_bf16 v[16:19], v[76:79], a[12:15], v[16:19]\n"
      "v_mfma_f32_16x16x32_bf16 v[20:23], v[72:75], a[48:51], 0\n"
      "v_add_f32 v57, v52, v53\n"
      "v_mul_f32 v58, v52, v52\n"
      "v_mfma_f32_16x16x32_bf16 v[20:23], v[76:79], a[56:59], v[20:23]\n"
      "v_fmac_f32 v58, v53, v53\n"
      "v_mfma_f32_16x16x32_bf16 v[84:87], v[72:75], a[52:55], 0\n"
      "v_add_f32 v57, v57, v54\n"
      "v_mfma_f32_16x16x32_bf16 v[84:87], v[76:79], a[60:63], v[84:87]\n"
      "ds_read_b128 v[76:79], v4 offset:20480\n"
      "ds_read_b128 v[72:75], v5 offset:20480\n"
      "v_fmac_f32 v58, v54, v54\n"
      "s_waitcnt lgkmcnt(2)\n"
      "v_mfma_f32_16x16x32_bf16 v[12:15], v[48:51], a[16:19], v[12:15]\n"
      "v_add_f32 v57, v57, v55\n"
      "v_fmac_f32 v58, v55, v55\n"
      "v_mfma_f32_16x16x32_bf16 v[12:15], v[80:83], a[24:27], v[12:15]\n"
      "v_add_f32 v57, v57, v24\n"
      "v_mfma_f32_16x16x32_bf16 v[16:19], v[48:51], a[20:23], v[16:19]\n"
      "v_fmac_f32 v58, v24, v24\n"
      "v_mfma_f32_16x16x32_bf16 v[16:19], v[80:83], a[28:31], v[16:19]\n"
      "v_add_f32 v57, v57, v25\n"
      "v_mfma_f32_16x16x32_bf16 v[20:23], v[48:51], a[64:67], v[20:23]\n"
      "v_fmac_f32 v58, v25, v25\n"
      "v_add_f32 v57, v57, v26\n"
      "v_mfma_f32_16x16x32_bf16 v[20:23], v[80:83], a[72:75], v[20:23]\n"
      "v_fmac_f32 v58, v26, v26\n"
      "v_mfma_f32_16x16x32_bf16 v[84:87], v[48:51], a[68:71], v[84:87]\n"
      "s_setprio 0\n"
      "v_add_f32 v57, v57, v27\n"
      "v_mfma_f32_16x16x32_bf16 v[84:87], v[80:83], a[76:79], v[84:87]\n"
      "v_fmac_f32 v58, v27, v27\n"
      "s_waitcnt lgkmcnt(0)\n"
      "v_mfma_f32_16x16x32_bf16 v[12:15], v[76:79], a[32:35], v[12:15]\n"
      "v_permlane16_swap_b32_e64 v57, v58\n"
      "v_add_f32_e64 v57, v57, v58\n"
      "v_mfma_f32_16x16x32_bf16 v[12:15], v[72:75], a[40:43], v[12:15]\n"
      "ds_read_b32 v80, v1 offset:1280\n"
      "v_mfma_f32_16x16x32_bf16 v[16:19], v[76:79], a[36:39], v[16:19]\n"
      "v_mov_b32 v58, v57\n"
      "v_mfma_f32_16x16x32_bf16 v[16:19], v[72:75], a[44:47], v[16:19]\n"
      "s_nop 0\n"
      "v_permlane32_swap_b32 v57, v58\n"
      "v_mfma_f32_16x16x32_bf16 v[20:23], v[76:79], a[80:83], v[20:23]\n"
      "v_add_f32_e64 v57, v57, v58\n"
      "s_waitcnt lgkmcnt(0)\n"
      "ds_write_b32 v80, v57 offset:512\n"
      "v_mfma_f32_16x16x32_bf16 v[20:23], v[72:75], a[88:91], v[20:23]\n"
      "ds_read_b32 v57, v1 offset:512\n"
      "v_mfma_f32_16x16x32_bf16 v[84:87], v[76:79], a[84:87], v[84:87]\n"
      "ds_read_b32 v76, v1 offset:768\n"
      "v_mfma_f32_16x16x32_bf16 v[84:87], v[72:75], a[92:95], v[84:87]\n"
      "ds_read_b32 v72, v1 offset:1536\n"
      "v_cvt_pk_bf16_f32 v12, v12, v13\n"
      "v_cvt_pk_bf16_f32 v13, v14, v15\n"
      "v_cvt_pk_bf16_f32 v16, v16, v17\n"
      "v_cvt_pk_bf16_f32 v17, v18, v19\n"
      "v_cvt_pk_bf16_f32 v20, v20, v21\n"
      "v_cvt_pk_bf16_f32 v21, v22, v23\n"
      "s_nop 0\n"
      "v_cvt_pk_bf16_f32 v84, v84, v85\n"
      "v_cvt_pk_bf16_f32 v85, v86, v87\n"
      "v_add_u32 v14, s63, v2\n"
      "ds_write_b64 v14, v[84:85] offset:1024\n"
      "s_waitcnt vmcnt(0)\n"
      "s_waitcnt lgkmcnt(0)\n"
      "s_barrier\n"
      "s_setprio 1\n"
      "v_subrev_u32 v14, 12288, v6\n"
      "v_add_u32 v18, 108800, v2\n"
      "ds_read_b128 v[84:87], v18\n"
      "ds_read_b128 v[80:83], v18 offset:1024\n"
      "ds_read_b32 v18, v1\n"
      "s_waitcnt lgkmcnt(2)\n"
      "v_add_f32 v84, v84, v86\n"
      "v_add_f32 v85, v85, v87\n"
      "s_waitcnt lgkmcnt(1)\n"
      "v_add_f32 v80, v80, v82\n"
      "v_add_f32 v81, v81, v83\n"
      "ds_read_b128 v[48:51], v14\n"
      "ds_read_b128 v[88:91], v14 offset:2048\n"
      "ds_read_b128 v[92:95], v14 offset:4096\n"
      "ds_read_b128 v[96:99], v14 offset:6144\n"
      "s_waitcnt lgkmcnt(2)\n"
      "v_mfma_f32_16x16x32_bf16 v[100:103], a[96:99], v[48:51], 0\n"
      "v_add_f32 v84, v84, v80\n"
      "v_add_f32 v85, v85, v81\n"
      "v_fma_f32 v80, -v84, v84, s74\n"
      "v_mfma_f32_16x16x32_bf16 v[100:103], a[104:107], v[88:91], v[100:103]\n"
      "v_fmac_f32 v80, 0x43400000, v85\n"
      "v_max_f32 v80, s74, v80\n"
      "v_rsq_f32 v80, v80\n"
      "v_mfma_f32_16x16x32_bf16 v[104:107], a[100:103], v[48:51], 0\n"
      "v_mul_f32 v22, 0x43400000, v80\n"
      "v_mul_f32 v15, -v84, v80\n"
      "v_mov_b32 v19, v22\n"
      "v_mfma_f32_16x16x32_bf16 v[104:107], a[108:111], v[88:91], v[104:107]\n"
      "ds_read_b128 v[88:91], v14 offset:8192\n"
      "ds_read_b128 v[48:51], v14 offset:10240\n"
      "v_mov_b32 v23, v15\n"
      "v_permlane32_swap_b32 v22, v19\n"
      "s_nop 0\n"
      "v_permlane32_swap_b32 v15, v23\n"
      "s_waitcnt lgkmcnt(2)\n"
      "v_mfma_f32_16x16x32_bf16 v[100:103], a[112:115], v[92:95], v[100:103]\n"
      "v_mov_b32 v80, v22\n"
      "v_mov_b32 v84, v15\n"
      "s_nop 0\n"
      "v_permlane16_swap_b32_e64 v22, v80\n"
      "v_mfma_f32_16x16x32_bf16 v[100:103], a[120:123], v[96:99], v[100:103]\n"
      "v_permlane16_swap_b32_e64 v15, v84\n"
      "v_fma_f32 v64, v64, v22, v15\n"
      "v_fma_f32 v65, v65, v22, v15\n"
      "v_mfma_f32_16x16x32_bf16 v[104:107], a[116:119], v[92:95], v[104:107]\n"
      "v_cvt_pk_bf16_f32 v64, v64, v65\n"
      "v_fma_f32 v66, v66, v22, v15\n"
      "v_fma_f32 v67, v67, v22, v15\n"
      "v_mfma_f32_16x16x32_bf16 v[104:107], a[124:127], v[96:99], v[104:107]\n"
      "ds_read_b128 v[96:99], v14 offset:256\n"
      "ds_read_b128 v[92:95], v14 offset:2304\n"
      "v_cvt_pk_bf16_f32 v65, v66, v67\n"
      "v_fma_f32 v68, v68, v22, v15\n"
      "v_fma_f32 v69, v69, v22, v15\n"
      "s_waitcnt lgkmcnt(2)\n"
      "v_mfma_f32_16x16x32_bf16 v[100:103], a[128:131], v[88:91], v[100:103]\n"
      "v_cvt_pk_bf16_f32 v66, v68, v69\n"
      "v_fma_f32 v70, v70, v22, v15\n"
      "v_fma_f32 v71, v71, v22, v15\n"
      "v_mfma_f32_16x16x32_bf16 v[100:103], a[136:139], v[48:51], v[100:103]\n"
      "v_cvt_pk_bf16_f32 v67, v70, v71\n"
      "ds_write_b128 v18, v[64:67] offset:24576\n"
      "v_fma_f32 v40, v40, v80, v84\n"
      "v_mfma_f32_16x16x32_bf16 v[104:107], a[132:135], v[88:91], v[104:107]\n"
      "v_fma_f32 v41, v41, v80, v84\n"
      "v_cvt_pk_bf16_f32 v40, v40, v41\n"
      "v_fma_f32 v42, v42, v80, v84\n"
      "v_mfma_f32_16x16x32_bf16 v[104:107], a[140:143], v[48:51], v[104:107]\n"
      "ds_read_b128 v[48:51], v14 offset:4352\n"
      "ds_read_b128 v[88:91], v14 offset:6400\n"
      "v_fma_f32 v43, v43, v80, v84\n"
      "v_cvt_pk_bf16_f32 v41, v42, v43\n"
      "v_fma_f32 v60, v60, v80, v84\n"
      "s_waitcnt lgkmcnt(2)\n"
      "v_mfma_f32_16x16x32_bf16 v[68:71], a[96:99], v[96:99], 0\n"
      "v_fma_f32 v61, v61, v80, v84\n"
      "v_cvt_pk_bf16_f32 v42, v60, v61\n"
      "v_fma_f32 v62, v62, v80, v84\n"
      "v_mfma_f32_16x16x32_bf16 v[68:71], a[104:107], v[92:95], v[68:71]\n"
      "v_fma_f32 v63, v63, v80, v84\n"
      "v_cvt_pk_bf16_f32 v43, v62, v63\n"
      "ds_write_b128 v18, v[40:43] offset:25600\n"
      "v_mfma_f32_16x16x32_bf16 v[60:63], a[100:103], v[96:99], 0\n"
      "v_add_u32_e64 v22, s71, v2\n"
      "v_add_u32 v15, s72, v1\n"
      "ds_read_b32 v96, v15 offset:1792\n"
      "v_mfma_f32_16x16x32_bf16 v[60:63], a[108:111], v[92:95], v[60:63]\n"
      "ds_read_b128 v[92:95], v14 offset:8448\n"
      "ds_read_b128 v[80:83], v14 offset:10496\n"
      "ds_read_b32 v14, v15 offset:2048\n"
      "ds_read_b128 v[64:67], v22 offset:24576\n"
      "ds_read_b128 v[40:43], v22 offset:25600\n"
      "s_waitcnt lgkmcnt(6)\n"
      "v_mfma_f32_16x16x32_bf16 v[68:71], a[112:115], v[48:51], v[68:71]\n"
      "s_add_u32 s14, s49, s70\n"
      "s_add_u32 s66, s22, s14\n"
      "s_addc_u32 s67, s23, 0\n"
      "s_or_b32 s14, s50, s51\n"
      "s_waitcnt lgkmcnt(2)\n"
      "s_cbranch_scc1 L_edge_37_%=\n"
      "L_eb_38_%=:\n"
      "s_waitcnt lgkmcnt(1)\n"
      "global_store_dwordx4 v96, v[64:67], s[66:67] sc1\n"
      "v_mfma_f32_16x16x32_bf16 v[68:71], a[120:123], v[88:91], v[68:71]\n"
      "s_waitcnt lgkmcnt(0)\n"
      "global_store_dwordx4 v14, v[40:43], s[66:67] sc1\n"
      "v_fma_f32 v52, v52, v19, v23\n"
      "v_fma_f32 v53, v53, v19, v23\n"
      "v_mfma_f32_16x16x32_bf16 v[60:63], a[116:119], v[48:51], v[60:63]\n"
      "v_cvt_pk_bf16_f32 v52, v52, v53\n"
      "v_fma_f32 v54, v54, v19, v23\n"
      "v_fma_f32 v55, v55, v19, v23\n"
      "v_mfma_f32_16x16x32_bf16 v[60:63], a[124:127], v[88:91], v[60:63]\n"
      "v_cvt_pk_bf16_f32 v53, v54, v55\n"
      "v_fma_f32 v24, v24, v19, v23\n"
      "v_fma_f32 v25, v25, v19, v23\n"
      "v_mfma_f32_16x16x32_bf16 v[68:71], a[128:131], v[92:95], v[68:71]\n"
      "v_cvt_pk_bf16_f32 v54, v24, v25\n"
      "v_fma_f32 v26, v26, v19, v23\n"
      "v_fma_f32 v27, v27, v19, v23\n"
      "v_mfma_f32_16x16x32_bf16 v[68:71], a[136:139], v[80:83], v[68:71]\n"
      "v_cvt_pk_bf16_f32 v55, v26, v27\n"
      "ds_write_b128 v18, v[52:55] offset:24576\n"
      "v_add_u32 v18, s71, v2\n"
      "v_mfma_f32_16x16x32_bf16 v[60:63], a[132:135], v[92:95], v[60:63]\n"
      "v_add_u32 v14, s72, v1\n"
      "ds_read_b32 v19, v14 offset:4608\n"
      "ds_read_b128 v[52:55], v18 offset:24576\n"
      "v_mfma_f32_16x16x32_bf16 v[60:63], a[140:143], v[80:83], v[60:63]\n"
      "s_add_u32 s14, s49, s70\n"
      "s_add_u32 s66, s22, s14\n"
      "s_addc_u32 s67, s23, 0\n"
      "s_or_b32 s14, s50, s51\n"
      "s_waitcnt lgkmcnt(1)\n"
      "s_cbranch_scc1 L_edge_39_%=\n"
      "L_eb_40_%=:\n"
      "s_waitcnt lgkmcnt(0)\n"
      "global_store_dwordx4 v19, v[52:55], s[66:67] sc1\n"
      "s_nop 1\n"
      "ds_read_b128 v[52:55], v3\n"
      "ds_read_b128 v[80:83], v3 offset:1024\n"
      "ds_read_u16 v14, v76\n"
      "ds_read_u16 v18, v76 offset:32\n"
      "ds_read_u16 v22, v76 offset:64\n"
      "v_mov_b32 v15, 0\n"
      "v_mov_b32 v19, 0\n"
      "v_mov_b32 v23, 0\n"
      "ds_read_b32 v76, v1 offset:1024\n"
      "v_add_u32 v92, s55, v1\n"
      "ds_read_b32 v92, v92\n"
      "s_waitcnt lgkmcnt(1)\n"
      "v_subrev_u32 v76, 163072, v76\n"
      "v_and_b32 v93, 14, v76\n"
      "v_and_b32_e64 v76, -16, v76\n"
      "v_lshl_add_u32 v76, v76, 5, v93\n"
      "v_add_u32 v76, 49408, v76\n"
      "v_add_f32 v93, v100, v101\n"
      "v_mul_f32 v94, v100, v100\n"
      "v_fmac_f32 v94, v101, v101\n"
      "v_add_f32 v93, v93, v102\n"
      "v_mfma_f32_16x16x32_bf16 v[24:27], v[8:11], v[52:55], 0\n"
      "v_fmac_f32 v94, v102, v102\n"
      "v_add_f32 v93, v93, v103\n"
      "v_mfma_f32_16x16x32_bf16 v[24:27], v[8:11], v[80:83], v[24:27]\n"
      "v_fmac_f32 v94, v103, v103\n"
      "v_add_f32 v93, v93, v104\n"
      "v_mfma_f32_16x16x32_bf16 v[88:91], v[28:31], v[52:55], 0\n"
      "v_fmac_f32 v94, v104, v104\n"
      "v_add_f32 v93, v93, v105\n"
      "v_mfma_f32_16x16x32_bf16 v[88:91], v[12:15], v[80:83], v[88:91]\n"
      "v_fmac_f32 v94, v105, v105\n"
      "v_add_f32 v93, v93, v106\n"
      "v_mfma_f32_16x16x32_bf16 v[48:51], v[32:35], v[52:55], 0\n"
      "v_mfma_f32_16x16x32_bf16 v[48:51], v[16:19], v[80:83], v[48:51]\n"
      "v_mfma_f32_16x16x32_bf16 v[40:43], v[36:39], v[52:55], 0\n"
      "v_mfma_f32_16x16x32_bf16 v[40:43], v[20:23], v[80:83], v[40:43]\n"
      "ds_read_b128 v[80:83], v3 offset:2048\n"
      "ds_read_b128 v[52:55], v3 offset:3072\n"
      "v_rcp_f32 v24, v24\n"
      "s_nop 0\n"
      "v_mul_f32 v88, v24, v88\n"
      "v_mul_f32 v89, v24, v89\n"
      "v_mul_f32 v90, v24, v90\n"
      "v_mul_f32 v91, v24, v91\n"
      "v_cvt_pk_bf16_f32 v88, v88, v89\n"
      "v_cvt_pk_bf16_f32 v89, v90, v91\n"
      "ds_write_b64 v57, v[88:89]\n"
      "v_mul_f32 v48, v24, v48\n"
      "v_mul_f32 v49, v24, v49\n"
      "v_mul_f32 v50, v24, v50\n"
      "v_mul_f32 v51, v24, v51\n"
      "v_cvt_pk_bf16_f32 v48, v48, v49\n"
      "v_cvt_pk_bf16_f32 v49, v50, v51\n"
      "ds_write_b64 v57, v[48:49] offset:1024\n"
      "v_mul_f32 v40, v24, v40\n"
      "v_mul_f32 v41, v24, v41\n"
      "v_mul_f32 v42, v24, v42\n"
      "v_mul_f32 v43, v24, v43\n"
      "v_cvt_pk_bf16_f32 v40, v40, v41\n"
      "v_cvt_pk_bf16_f32 v41, v42, v43\n"
      "ds_write_b64 v57, v[40:41] offset:2048\n"
      "s_waitcnt lgkmcnt(3)\n"
      "v_mfma_f32_16x16x32_bf16 v[40:43], v[8:11], v[80:83], 0\n"
      "v_fmac_f32 v94, v106, v106\n"
      "v_add_f32_e64 v93, v93, v107\n"
      "v_mfma_f32_16x16x32_bf16 v[40:43], v[8:11], v[52:55], v[40:43]\n"
      "v_fmac_f32 v94, v107, v107\n"
      "s_nop 1\n"
      "v_permlane16_swap_b32 v93, v94\n"
      "v_mfma_f32_16x16x32_bf16 v[48:51], v[28:31], v[80:83], 0\n"
      "v_add_f32 v93, v93, v94\n"
      "s_nop 1\n"
      "v_add_f32 v41, v68, v69\n"
      "v_mfma_f32_16x16x32_bf16 v[48:51], v[12:15], v[52:55], v[48:51]\n"
      "v_mul_f32 v42, v68, v68\n"
      "v_fmac_f32 v42, v69, v69\n"
      "v_mfma_f32_16x16x32_bf16 v[88:91], v[32:35], v[80:83], 0\n"
      "v_add_f32_e64 v41, v41, v70\n"
      "v_mfma_f32_16x16x32_bf16 v[88:91], v[16:19], v[52:55], v[88:91]\n"
      "v_mfma_f32_16x16x32_bf16 v[24:27], v[36:39], v[80:83], 0\n"
      "v_mfma_f32_16x16x32_bf16 v[24:27], v[20:23], v[52:55], v[24:27]\n"
      "ds_read_b128 v[52:55], v3 offset:4096\n"
      "ds_read_b128 v[80:83], v3 offset:5120\n"
      "v_rcp_f32 v40, v40\n"
      "s_nop 0\n"
      "v_mul_f32 v48, v40, v48\n"
      "v_mul_f32 v49, v40, v49\n"
      "v_mul_f32 v50, v40, v50\n"
      "v_mul_f32 v51, v40, v51\n"
      "v_cvt_pk_bf16_f32 v48, v48, v49\n"
      "v_cvt_pk_bf16_f32 v49, v50, v51\n"
      "ds_write_b64 v57, v[48:49] offset:256\n"
      "v_mul_f32 v88, v40, v88\n"
      "v_mul_f32 v89, v40, v89\n"
      "v_mul_f32 v90, v40, v90\n"
      "v_mul_f32 v91, v40, v91\n"
      "v_cvt_pk_bf16_f32 v88, v88, v89\n"
      "v_cvt_pk_bf16_f32 v89, v90, v91\n"
      "ds_write_b64 v57, v[88:89] offset:1280\n"
      "v_mul_f32 v24, v40, v24\n"
      "v_mul_f32 v25, v40, v25\n"
      "v_mul_f32 v26, v40, v26\n"
      "v_mul_f32 v27, v40, v27\n"
      "v_cvt_pk_bf16_f32 v24, v24, v25\n"
      "v_cvt_pk_bf16_f32 v25, v26, v27\n"
      "ds_write_b64 v57, v[24:25] offset:2304\n"
      "s_waitcnt lgkmcnt(3)\n"
      "v_mfma_f32_16x16x32_bf16 v[24:27], v[8:11], v[52:55], 0\n"
      "v_fmac_f32 v42, v70, v70\n"
      "v_add_f32 v41, v41, v71\n"
      "v_mfma_f32_16x16x32_bf16 v[24:27], v[8:11], v[80:83], v[24:27]\n"
      "v_fmac_f32 v42, v71, v71\n"
      "v_add_f32 v41, v41, v60\n"
      "v_mfma_f32_16x16x32_bf16 v[88:91], v[28:31], v[52:55], 0\n"
      "v_fmac_f32 v42, v60, v60\n"
      "v_add_f32 v41, v41, v61\n"
      "v_mfma_f32_16x16x32_bf16 v[88:91], v[12:15], v[80:83], v[88:91]\n"
      "v_fmac_f32 v42, v61, v61\n"
      "v_add_f32 v41, v41, v62\n"
      "v_mfma_f32_16x16x32_bf16 v[48:51], v[32:35], v[52:55], 0\n"
      "v_fmac_f32 v42, v62, v62\n"
      "v_mfma_f32_16x16x32_bf16 v[48:51], v[16:19], v[80:83], v[48:51]\n"
      "v_mfma_f32_16x16x32_bf16 v[64:67], v[36:39], v[52:55], 0\n"
      "v_mfma_f32_16x16x32_bf16 v[64:67], v[20:23], v[80:83], v[64:67]\n"
      "ds_read_b128 v[80:83], v92\n"
      "ds_read_b128 v[52:55], v92 offset:64\n"
      "v_rcp_f32 v24, v24\n"
      "s_nop 0\n"
      "v_mul_f32 v88, v24, v88\n"
      "v_mul_f32 v89, v24, v89\n"
      "v_mul_f32 v90, v24, v90\n"
      "v_mul_f32 v91, v24, v91\n"
      "v_cvt_pk_bf16_f32 v88, v88, v89\n"
      "v_cvt_pk_bf16_f32 v89, v90, v91\n"
      "ds_write_b64 v57, v[88:89] offset:12288\n"
      "v_mul_f32 v48, v24, v48\n"
      "v_mul_f32 v49, v24, v49\n"
      "v_mul_f32 v50, v24, v50\n"
      "v_mul_f32 v51, v24, v51\n"
      "v_cvt_pk_bf16_f32 v48, v48, v49\n"
      "v_cvt_pk_bf16_f32 v49, v50, v51\n"
      "ds_write_b64 v57, v[48:49] offset:13312\n"
      "v_mul_f32 v64, v24, v64\n"
      "v_mul_f32 v65, v24, v65\n"
      "v_mul_f32 v66, v24, v66\n"
      "v_mul_f32 v67, v24, v67\n"
      "v_cvt_pk_bf16_f32 v64, v64, v65\n"
      "v_cvt_pk_bf16_f32 v65, v66, v67\n"
      "ds_write_b64 v57, v[64:65] offset:14336\n"
      "s_waitcnt lgkmcnt(3)\n"
      "v_mfma_f32_16x16x32_bf16 v[64:67], v[80:83], v[8:11], 0\n"
      "v_add_f32 v41, v41, v63\n"
      "v_fmac_f32 v42, v63, v63\n"
      "v_mfma_f32_16x16x32_bf16 v[64:67], v[52:55], v[8:11], v[64:67]\n"
      "s_nop 0\n"
      "v_permlane16_swap_b32 v41, v42\n"
      "v_add_f32 v41, v41, v42\n"
      "v_mfma_f32_16x16x32_bf16 v[48:51], v[80:83], v[28:31], 0\n"
      "ds_read_b32 v28, v1 offset:1280\n"
      "s_waitcnt lgkmcnt(0)\n"
      "v_add_u32 v28, -22272, v28\n"
      "v_mfma_f32_16x16x32_bf16 v[48:51], v[52:55], v[12:15], v[48:51]\n"
      "v_permlane32_swap_b32 v93, v41\n"
      "v_add_f32_e64 v93, v93, v41\n"
      "v_mfma_f32_16x16x32_bf16 v[12:15], v[80:83], v[32:35], 0\n"
      "ds_write_b32 v28, v93\n"
      "v_mfma_f32_16x16x32_bf16 v[12:15], v[52:55], v[16:19], v[12:15]\n"
      "v_mfma_f32_16x16x32_bf16 v[16:19], v[80:83], v[36:39], 0\n"
      "v_mfma_f32_16x16x32_bf16 v[16:19], v[52:55], v[20:23], v[16:19]\n"
      "s_nop 5\n"
      "v_cndmask_b32 v48, v48, v12, s[56:57]\n"
      "s_nop 0\n"
      "v_cndmask_b32 v48, v48, v16, s[58:59]\n"
      "v_mov_b32_dpp v64, v64 quad_perm:[0,0,0,0] row_mask:0xf bank_mask:0xf\n"
      "v_rcp_f32 v64, v64\n"
      "s_nop 0\n"
      "v_mul_f32_e64 v48, v64, v48\n"
      "v_cvt_pk_bf16_f32 v48, v48, v48\n"
      "ds_write_b16 v76, v48\n"
      "v_subrev_u32 v48, 22272, v72\n"
      "s_waitcnt lgkmcnt(0)\n"
      "s_barrier\n"
      "ds_read_b128 v[16:19], v48\n"
      "ds_read_b128 v[20:23], v48 offset:1024\n"
      "ds_read_b32 v48, v1\n"
      "ds_read_b128 v[52:55], v6\n"
      "ds_read_b128 v[36:39], v6 offset:2048\n"
      "ds_read_b128 v[80:83], v6 offset:4096\n"
      "ds_read_b128 v[12:15], v6 offset:6144\n"
      "s_waitcnt lgkmcnt(2)\n"
      "v_mfma_f32_16x16x32_bf16 v[32:35], a[96:99], v[52:55], 0\n"
      "v_add_f32 v16, v16, v18\n"
      "v_add_f32 v17, v17, v19\n"
      "v_add_f32 v20, v20, v22\n"
      "v_mfma_f32_16x16x32_bf16 v[32:35], a[104:107], v[36:39], v[32:35]\n"
      "v_add_f32 v21, v21, v23\n"
      "v_add_f32 v16, v16, v20\n"
      "v_add_f32_e64 v17, v17, v21\n"
      "v_mfma_f32_16x16x32_bf16 v[28:31], a[100:103], v[52:55], 0\n"
      "v_fma_f32 v20, -v16, v16, s74\n"
      "v_fmac_f32 v20, 0x43400000, v17\n"
      "v_mfma_f32_16x16x32_bf16 v[28:31], a[108:111], v[36:39], v[28:31]\n"
      "ds_read_b128 v[36:39], v6 offset:8192\n"
      "ds_read_b128 v[52:55], v6 offset:10240\n"
      "v_max_f32 v20, s74, v20\n"
      "v_rsq_f32_e64 v20, v20\n"
      "s_nop 0\n"
      "v_mul_f32 v17, 0x43400000, v20\n"
      "s_waitcnt lgkmcnt(2)\n"
      "v_mfma_f32_16x16x32_bf16 v[32:35], a[112:115], v[80:83], v[32:35]\n"
      "v_mul_f32 v21, -v16, v20\n"
      "v_mov_b32 v16, v17\n"
      "v_mov_b32 v20, v21\n"
      "v_mfma_f32_16x16x32_bf16 v[32:35], a[120:123], v[12:15], v[32:35]\n"
      "v_permlane16_swap_b32 v17, v16\n"
      "v_permlane16_swap_b32_e64 v21, v20\n"
      "v_mfma_f32_16x16x32_bf16 v[28:31], a[116:119], v[80:83], v[28:31]\n"
      "v_fma_f32 v100, v100, v17, v21\n"
      "v_fma_f32 v101, v101, v17, v21\n"
      "v_cvt_pk_bf16_f32 v100, v100, v101\n"
      "v_mfma_f32_16x16x32_bf16 v[28:31], a[124:127], v[12:15], v[28:31]\n"
      "ds_read_b128 v[12:15], v6 offset:256\n"
      "ds_read_b128 v[80:83], v6 offset:2304\n"
      "v_fma_f32 v102, v102, v17, v21\n"
      "v_fma_f32 v103, v103, v17, v21\n"
      "v_cvt_pk_bf16_f32 v101, v102, v103\n"
      "s_waitcnt lgkmcnt(2)\n"
      "v_mfma_f32_16x16x32_bf16 v[32:35], a[128:131], v[36:39], v[32:35]\n"
      "v_fma_f32 v104, v104, v17, v21\n"
      "v_fma_f32 v105, v105, v17, v21\n"
      "v_mfma_f32_16x16x32_bf16 v[32:35], a[136:139], v[52:55], v[32:35]\n"
      "v_cvt_pk_bf16_f32 v102, v104, v105\n"
      "v_fma_f32 v106, v106, v17, v21\n"
      "v_fma_f32 v107, v107, v17, v21\n"
      "v_mfma_f32_16x16x32_bf16 v[28:31], a[132:135], v[36:39], v[28:31]\n"
      "v_cvt_pk_bf16_f32 v103, v106, v107\n"
      "ds_write_b128 v48, v[100:103] offset:24576\n"
      "v_fma_f32 v68, v68, v16, v20\n"
      "v_mfma_f32_16x16x32_bf16 v[28:31], a[140:143], v[52:55], v[28:31]\n"
      "ds_read_b128 v[52:55], v6 offset:4352\n"
      "ds_read_b128 v[36:39], v6 offset:6400\n"
      "v_fma_f32 v69, v69, v16, v20\n"
      "v_cvt_pk_bf16_f32 v68, v68, v69\n"
      "s_waitcnt lgkmcnt(3)\n"
      "v_mfma_f32_16x16x32_bf16 v[104:107], a[96:99], v[12:15], 0\n"
      "v_fma_f32 v70, v70, v16, v20\n"
      "v_fma_f32 v71, v71, v16, v20\n"
      "v_cvt_pk_bf16_f32 v69, v70, v71\n"
      "v_mfma_f32_16x16x32_bf16 v[104:107], a[104:107], v[80:83], v[104:107]\n"
      "v_fma_f32 v60, v60, v16, v20\n"
      "v_fma_f32 v61, v61, v16, v20\n"
      "v_cvt_pk_bf16_f32 v70, v60, v61\n"
      "v_mfma_f32_16x16x32_bf16 v[64:67], a[100:103], v[12:15], 0\n"
      "v_fma_f32 v62, v62, v16, v20\n"
      "v_fma_f32 v63, v63, v16, v20\n"
      "v_mfma_f32_16x16x32_bf16 v[64:67], a[108:111], v[80:83], v[64:67]\n"
      "ds_read_b128 v[80:83], v6 offset:8448\n"
      "ds_read_b128 v[12:15], v6 offset:10496\n"
      "v_cvt_pk_bf16_f32 v71, v62, v63\n"
      "ds_write_b128 v48, v[68:71] offset:25600\n"
      "v_add_u32 v62, s71, v2\n"
      "s_waitcnt lgkmcnt(3)\n"
      "v_mfma_f32_16x16x32_bf16 v[104:107], a[112:115], v[52:55], v[104:107]\n"
      "ds_read_b128 v[100:103], v62 offset:24576\n"
      "ds_read_b128 v[68:71], v62 offset:25600\n"
      "v_bfe_u32 v62, v2, 6, 4\n"
      "v_mfma_f32_16x16x32_bf16 v[104:107], a[120:123], v[36:39], v[104:107]\n"
      "v_add_u32_e64 v63, 0, v62\n"
      "v_bfe_u32 v16, v63, 1, 2\n"
      "v_mfma_f32_16x16x32_bf16 v[64:67], a[116:119], v[52:55], v[64:67]\n"
      "v_lshlrev_b32_e64 v63, 2, v63\n"
      "ds_bpermute_b32 v52, v63, v44\n"
      "v_bfe_u32 v63, v2, 4, 2\n"
      "v_mfma_f32_16x16x32_bf16 v[64:67], a[124:127], v[36:39], v[64:67]\n"
      "ds_read_b128 v[36:39], v6 offset:12288\n"
      "ds_read_b128 v[20:23], v6 offset:14336\n"
      "v_xor_b32 v63, v63, v16\n"
      "s_waitcnt lgkmcnt(2)\n"
      "v_lshl_add_u32 v52, v63, 4, v52\n"
      "v_add_u32_e64 v63, 16, v62\n"
      "v_mfma_f32_16x16x32_bf16 v[104:107], a[128:131], v[80:83], v[104:107]\n"
      "v_bfe_u32 v16, v63, 1, 2\n"
      "v_lshlrev_b32_e64 v63, 2, v63\n"
      "v_mfma_f32_16x16x32_bf16 v[104:107], a[136:139], v[12:15], v[104:107]\n"
      "ds_bpermute_b32 v62, v63, v44\n"
      "v_bfe_u32 v63, v2, 4, 2\n"
      "v_xor_b32 v63, v63, v16\n"
      "v_mfma_f32_16x16x32_bf16 v[64:67], a[132:135], v[80:83], v[64:67]\n"
      "s_waitcnt lgkmcnt(0)\n"
      "v_lshl_add_u32 v62, v63, 4, v62\n"
      "s_add_u32 s66, s22, s70\n"
      "s_addc_u32 s67, s23, 0\n"
      "v_mfma_f32_16x16x32_bf16 v[64:67], a[140:143], v[12:15], v[64:67]\n"
      "ds_read_b128 v[12:15], v6 offset:16384\n"
      "ds_read_b128 v[80:83], v6 offset:18432\n"
      "global_store_dwordx4 v52, v[100:103], s[66:67] sc1\n"
      "s_mov_b32 s34, 0xffff\n"
      "s_mov_b32 s35, 0x0\n"
      "s_mov_b64 exec, s[34:35]\n"
      "global_store_dwordx4 v62, v[68:71], s[66:67] sc1\n"
      "s_mov_b64 exec, -1\n"
      "s_nop 0\n"
      "v_mfma_f32_16x16x32_bf16 v[68:71], a[96:99], v[36:39], 0\n"
      "v_mfma_f32_16x16x32_bf16 v[68:71], a[104:107], v[20:23], v[68:71]\n"
      "v_mfma_f32_16x16x32_bf16 v[100:103], a[100:103], v[36:39], 0\n"
      "v_mfma_f32_16x16x32_bf16 v[100:103], a[108:111], v[20:23], v[100:103]\n"
      "ds_read_b128 v[20:23], v6 offset:20480\n"
      "ds_read_b128 v[36:39], v6 offset:22528\n"
      "s_waitcnt lgkmcnt(2)\n"
      "v_mfma_f32_16x16x32_bf16 v[68:71], a[112:115], v[12:15], v[68:71]\n"
      "v_add_f32 v62, v32, v33\n"
      "v_mul_f32 v63, v32, v32\n"
      "v_mfma_f32_16x16x32_bf16 v[68:71], a[120:123], v[80:83], v[68:71]\n"
      "v_fmac_f32 v63, v33, v33\n"
      "v_add_f32 v62, v62, v34\n"
      "v_mfma_f32_16x16x32_bf16 v[100:103], a[116:119], v[12:15], v[100:103]\n"
      "v_fmac_f32 v63, v34, v34\n"
      "v_add_f32_e64 v62, v62, v35\n"
      "v_mfma_f32_16x16x32_bf16 v[100:103], a[124:127], v[80:83], v[100:103]\n"
      "ds_read_b128 v[80:83], v6 offset:12544\n"
      "ds_read_b128 v[12:15], v6 offset:14592\n"
      "v_fmac_f32 v63, v35, v35\n"
      "v_add_f32_e64 v62, v62, v28\n"
      "s_waitcnt lgkmcnt(2)\n"
      "v_mfma_f32_16x16x32_bf16 v[68:71], a[128:131], v[20:23], v[68:71]\n"
      "v_fmac_f32 v63, v28, v28\n"
      "v_add_f32 v62, v62, v29\n"
      "v_mfma_f32_16x16x32_bf16 v[68:71], a[136:139], v[36:39], v[68:71]\n"
      "v_fmac_f32 v63, v29, v29\n"
      "v_add_f32 v62, v62, v30\n"
      "v_mfma_f32_16x16x32_bf16 v[100:103], a[132:135], v[20:23], v[100:103]\n"
      "v_fmac_f32 v63, v30, v30\n"
      "v_add_f32 v62, v62, v31\n"
      "v_mfma_f32_16x16x32_bf16 v[100:103], a[140:143], v[36:39], v[100:103]\n"
      "ds_read_b128 v[36:39], v6 offset:16640\n"
      "ds_read_b128 v[20:23], v6 offset:18688\n"
      "v_fmac_f32 v63, v31, v31\n"
      "s_nop 1\n"
      "v_permlane16_swap_b32 v62, v63\n"
      "s_waitcnt lgkmcnt(2)\n"
      "v_mfma_f32_16x16x32_bf16 v[52:55], a[96:99], v[80:83], 0\n"
      "v_add_f32 v62, v62, v63\n"
      "v_add_f32 v16, v104, v105\n"
      "v_mfma_f32_16x16x32_bf16 v[52:55], a[104:107], v[12:15], v[52:55]\n"
      "v_mul_f32 v17, v104, v104\n"
      "v_mfma_f32_16x16x32_bf16 v[88:91], a[100:103], v[80:83], 0\n"
      "v_fmac_f32 v17, v105, v105\n"
      "v_add_f32 v16, v16, v106\n"
      "v_mfma_f32_16x16x32_bf16 v[88:91], a[108:111], v[12:15], v[88:91]\n"
      "ds_read_b128 v[12:15], v6 offset:20736\n"
      "ds_read_b128 v[80:83], v6 offset:22784\n"
      "v_fmac_f32 v17, v106, v106\n"
      "v_add_f32 v16, v16, v107\n"
      "s_waitcnt lgkmcnt(2)\n"
      "v_mfma_f32_16x16x32_bf16 v[52:55], a[112:115], v[36:39], v[52:55]\n"
      "v_fmac_f32 v17, v107, v107\n"
      "v_add_f32 v16, v16, v64\n"
      "v_mfma_f32_16x16x32_bf16 v[52:55], a[120:123], v[20:23], v[52:55]\n"
      "v_fmac_f32 v17, v64, v64\n"
      "v_add_f32 v16, v16, v65\n"
      "v_mfma_f32_16x16x32_bf16 v[88:91], a[116:119], v[36:39], v[88:91]\n"
      "v_fmac_f32 v17, v65, v65\n"
      "v_add_f32 v16, v16, v66\n"
      "v_mfma_f32_16x16x32_bf16 v[88:91], a[124:127], v[20:23], v[88:91]\n"
      "v_fmac_f32 v17, v66, v66\n"
      "v_add_f32 v16, v16, v67\n"
      "s_waitcnt lgkmcnt(0)\n"
      "v_mfma_f32_16x16x32_bf16 v[52:55], a[128:131], v[12:15], v[52:55]\n"
      "v_fmac_f32 v17, v67, v67\n"
      "s_nop 1\n"
      "v_permlane16_swap_b32 v16, v17\n"
      "v_mfma_f32_16x16x32_bf16 v[52:55], a[136:139], v[80:83], v[52:55]\n"
      "v_add_f32 v16, v16, v17\n"
      "ds_read_b32 v17, v1 offset:1280\n"
      "v_mfma_f32_16x16x32_bf16 v[88:91], a[132:135], v[12:15], v[88:91]\n"
      "v_permlane32_swap_b32 v62, v16\n"
      "v_add_f32 v62, v62, v16\n"
      "v_mfma_f32_16x16x32_bf16 v[88:91], a[140:143], v[80:83], v[88:91]\n"
      "s_waitcnt lgkmcnt(0)\n"
      "ds_write_b32 v17, v62\n"
      "v_add_f32 v16, v68, v69\n"
      "v_mul_f32 v17, v68, v68\n"
      "v_fmac_f32 v17, v69, v69\n"
      "v_add_f32 v16, v16, v70\n"
      "v_fmac_f32 v17, v70, v70\n"
      "v_add_f32 v16, v16, v71\n"
      "v_fmac_f32 v17, v71, v71\n"
      "v_add_f32 v16, v16, v100\n"
      "v_fmac_f32 v17, v100, v100\n"
      "v_add_f32 v16, v16, v101\n"
      "v_fmac_f32 v17, v101, v101\n"
      "v_add_f32 v16, v16, v102\n"
      "v_fmac_f32 v17, v102, v102\n"
      "v_add_f32 v16, v16, v103\n"
      "v_fmac_f32 v17, v103, v103\n"
      "s_nop 1\n"
      "v_permlane16_swap_b32 v16, v17\n"
      "v_add_f32 v16, v16, v17\n"
      "v_add_f32 v80, v52, v53\n"
      "v_mul_f32 v81, v52, v52\n"
      "v_fmac_f32 v81, v53, v53\n"
      "v_add_f32 v80, v80, v54\n"
      "v_fmac_f32 v81, v54, v54\n"
      "v_add_f32 v80, v80, v55\n"
      "v_fmac_f32 v81, v55, v55\n"
      "v_add_f32 v80, v80, v88\n"
      "v_fmac_f32 v81, v88, v88\n"
      "v_add_f32 v80, v80, v89\n"
      "v_fmac_f32 v81, v89, v89\n"
      "v_add_f32 v80, v80, v90\n"
      "v_fmac_f32 v81, v90, v90\n"
      "v_add_f32 v80, v80, v91\n"
      "v_fmac_f32 v81, v91, v91\n"
      "s_nop 1\n"
      "v_permlane16_swap_b32 v80, v81\n"
      "v_add_f32 v80, v80, v81\n"
      "ds_read_b32 v81, v1 offset:1280\n"
      "s_nop 0\n"
      "v_permlane32_swap_b32 v16, v80\n"
      "v_add_f32 v16, v16, v80\n"
      "s_waitcnt lgkmcnt(0)\n"
      "ds_write_b32 v81, v16 offset:512\n"
      "s_waitcnt lgkmcnt(0)\n"
      "s_barrier\n"
      "ds_read_b128 v[80:83], v72\n"
      "ds_read_b128 v[12:15], v72 offset:1024\n"
      "ds_read_b128 v[20:23], v72 offset:512\n"
      "ds_read_b128 v[36:39], v72 offset:1536\n"
      "s_waitcnt lgkmcnt(3)\n"
      "v_add_f32 v80, v80, v82\n"
      "v_add_f32 v81, v81, v83\n"
      "s_waitcnt lgkmcnt(2)\n"
      "v_add_f32 v12, v12, v14\n"
      "v_add_f32 v13, v13, v15\n"
      "v_add_f32 v80, v80, v12\n"
      "v_add_f32 v81, v81, v13\n"
      "v_fma_f32 v12, -v80, v80, s74\n"
      "v_fmac_f32 v12, 0x43400000, v81\n"
      "v_max_f32 v12, s74, v12\n"
      "v_rsq_f32 v12, v12\n"
      "s_nop 0\n"
      "v_mul_f32 v81, 0x43400000, v12\n"
      "v_mul_f32 v13, -v80, v12\n"
      "v_mov_b32 v12, v81\n"
      "v_mov_b32 v80, v13\n"
      "s_nop 0\n"
      "v_permlane16_swap_b32 v81, v12\n"
      "v_permlane16_swap_b32 v13, v80\n"
      "v_fma_f32 v32, v32, v81, v13\n"
      "v_fma_f32 v33, v33, v81, v13\n"
      "v_cvt_pk_bf16_f32 v32, v32, v33\n"
      "v_fma_f32 v34, v34, v81, v13\n"
      "v_fma_f32 v35, v35, v81, v13\n"
      "v_cvt_pk_bf16_f32 v33, v34, v35\n"
      "v_fma_f32 v28, v28, v81, v13\n"
      "v_fma_f32 v29, v29, v81, v13\n"
      "v_cvt_pk_bf16_f32 v34, v28, v29\n"
      "v_fma_f32 v30, v30, v81, v13\n"
      "v_fma_f32 v31, v31, v81, v13\n"
      "v_cvt_pk_bf16_f32 v35, v30, v31\n"
      "ds_write_b128 v48, v[32:35] offset:24576\n"
      "v_fma_f32 v104, v104, v12, v80\n"
      "v_fma_f32 v105, v105, v12, v80\n"
      "v_cvt_pk_bf16_f32 v104, v104, v105\n"
      "v_fma_f32 v106, v106, v12, v80\n"
      "v_fma_f32 v107, v107, v12, v80\n"
      "v_cvt_pk_bf16_f32 v105, v106, v107\n"
      "v_fma_f32 v64, v64, v12, v80\n"
      "v_fma_f32 v65, v65, v12, v80\n"
      "v_cvt_pk_bf16_f32 v106, v64, v65\n"
      "v_fma_f32 v66, v66, v12, v80\n"
      "v_fma_f32 v67, v67, v12, v80\n"
      "v_cvt_pk_bf16_f32 v107, v66, v67\n"
      "ds_write_b128 v48, v[104:107] offset:25600\n"
      "v_add_u32 v66, s71, v2\n"
      "v_add_u32 v67, s72, v1\n"
      "ds_read_b32 v12, v67 offset:1792\n"
      "ds_read_b32 v80, v67 offset:2048\n"
      "ds_read_b128 v[32:35], v66 offset:24576\n"
      "ds_read_b128 v[104:107], v66 offset:25600\n"
      "s_add_u32 s14, s40, s70\n"
      "s_add_u32 s66, s22, s14\n"
      "s_addc_u32 s67, s23, 0\n"
      "s_or_b32 s14, s41, s42\n"
      "s_waitcnt lgkmcnt(2)\n"
      "s_cbranch_scc1 L_edge_41_%=\n"
      "L_eb_42_%=:\n"
      "s_waitcnt lgkmcnt(1)\n"
      "global_store_dwordx4 v12, v[32:35], s[66:67] sc1\n"
      "s_waitcnt lgkmcnt(0)\n"
      "global_store_dwordx4 v80, v[104:107], s[66:67] sc1\n"
      "v_add_f32 v20, v20, v22\n"
      "v_add_f32 v21, v21, v23\n"
      "v_add_f32 v36, v36, v38\n"
      "v_add_f32 v37, v37, v39\n"
      "v_add_f32 v20, v20, v36\n"
      "v_add_f32 v21, v21, v37\n"
      "v_fma_f32 v36, -v20, v20, s74\n"
      "v_fmac_f32 v36, 0x43400000, v21\n"
      "v_max_f32 v36, s74, v36\n"
      "v_rsq_f32 v36, v36\n"
      "s_nop 0\n"
      "v_mul_f32 v21, 0x43400000, v36\n"
      "v_mul_f32 v37, -v20, v36\n"
      "v_mov_b32 v20, v21\n"
      "v_mov_b32 v36, v37\n"
      "s_nop 0\n"
      "v_permlane16_swap_b32 v21, v20\n"
      "v_permlane16_swap_b32 v37, v36\n"
      "v_fma_f32 v68, v68, v21, v37\n"
      "v_fma_f32 v69, v69, v21, v37\n"
      "v_cvt_pk_bf16_f32 v68, v68, v69\n"
      "v_fma_f32 v70, v70, v21, v37\n"
      "v_fma_f32 v71, v71, v21, v37\n"
      "v_cvt_pk_bf16_f32 v69, v70, v71\n"
      "v_fma_f32 v100, v100, v21, v37\n"
      "v_fma_f32 v101, v101, v21, v37\n"
      "v_cvt_pk_bf16_f32 v70, v100, v101\n"
      "v_fma_f32 v102, v102, v21, v37\n"
      "v_fma_f32 v103, v103, v21, v37\n"
      "v_cvt_pk_bf16_f32 v71, v102, v103\n"
      "ds_write_b128 v48, v[68:71] offset:24576\n"
      "v_fma_f32 v52, v52, v20, v36\n"
      "v_fma_f32 v53, v53, v20, v36\n"
      "v_cvt_pk_bf16_f32 v52, v52, v53\n"
      "v_fma_f32 v54, v54, v20, v36\n"
      "v_fma_f32 v55, v55, v20, v36\n"
      "v_cvt_pk_bf16_f32 v53, v54, v55\n"
      "v_fma_f32 v88, v88, v20, v36\n"
      "v_fma_f32 v89, v89, v20, v36\n"
      "v_cvt_pk_bf16_f32 v54, v88, v89\n"
      "v_fma_f32 v90, v90, v20, v36\n"
      "v_fma_f32 v91, v91, v20, v36\n"
      "v_cvt_pk_bf16_f32 v55, v90, v91\n"
      "ds_write_b128 v48, v[52:55] offset:25600\n"
      "v_add_u32 v48, s71, v2\n"
      "v_add_u32 v90, s72, v1\n"
      "ds_read_b32 v91, v90 offset:4608\n"
      "ds_read_b32 v20, v90 offset:4608\n"
      "ds_read_b128 v[68:71], v48 offset:24576\n"
      "ds_read_b128 v[52:55], v48 offset:25600\n"
      "s_add_u32 s14, s40, s70\n"
      "s_add_u32 s66, s22, s14\n"
      "s_addc_u32 s67, s23, 0\n"
      "s_add_u32 s14, s40, s70\n"
      "s_add_u32 s68, s22, s14\n"
      "s_addc_u32 s69, s23, 0\n"
      "s_waitcnt lgkmcnt(2)\n"
      "v_add_u32 v20, 393216, v20\n"
      "s_or_b32 s14, s41, s42\n"
      "s_cbranch_scc1 L_edge_43_%=\n"
      "L_eb_44_%=:\n"
      "s_waitcnt lgkmcnt(1)\n"
      "global_store_dwordx4 v91, v[68:71], s[66:67] sc1\n"
      "s_mov_b32 s34, 0xf\n"
      "s_mov_b32 s35, 0x0\n"
      "s_mov_b64 exec, s[34:35]\n"
      "s_waitcnt lgkmcnt(0)\n"
      "global_store_dwordx4 v20, v[52:55], s[68:69] sc1\n"
      "s_mov_b64 exec, -1\n"
      "s_endpgm\n"
      "L_B0drain_%=:\n"
      "s_nop 7\n"
      "s_nop 7\n"
      "s_waitcnt lgkmcnt(0)\n"
      "v_add_u32 v20, 126208, v2\n"
      "ds_read_b128 v[16:19], v20\n"
      "s_sub_u32 s31, s94, 37632\n"
      "s_add_u32 s34, s20, s31\n"
      "s_addc_u32 s35, s21, 0\n"
      "s_mov_b64 exec, s[90:91]\n"
      "s_waitcnt lgkmcnt(0)\n"
      "global_store_dwordx4 v2, v[16:19], s[34:35] sc1\n"
      "s_mov_b64 exec, -1\n"
      "v_subrev_u32 v20, 36864, v2\n"
      "v_lshrrev_b32 v21, 9, v20\n"
      "v_bfe_u32 v20, v20, 4, 5\n"
      "v_lshlrev_b32 v21, 4, v21\n"
      "v_mul_u32_u24 v22, 0x180, v20\n"
      "v_add_u32 v21, v21, v22\n"
      "s_mul_i32 s29, s24, 96\n"
      "s_sub_u32 s29, s29, 384\n"
      "v_add_u32 v21, s29, v21\n"
      "v_lshrrev_b32 v23, 1, v20\n"
      "v_lshlrev_b32 v23, 9, v23\n"
      "v_and_b32 v22, 1, v20\n"
      "v_add_u32 v23, v23, v22\n"
      "s_lshl_b32 s29, s25, 1\n"
      "v_add_u32 v23, s29, v23\n"
      "s_add_u32 s29, s25, 5120\n"
      "s_min_u32 s29, s29, 5328\n"
      "v_mov_b32 v22, s29\n"
      "v_cmp_gt_u32 vcc, 20, v20\n"
      "s_nop 1\n"
      "v_cndmask_b32 v23, v22, v23, vcc\n"
      "s_mov_b32 s29, 58835169\n"
      "v_mul_hi_u32 v22, v23, s29\n"
      "v_mul_u32_u24 v20, 73, v22\n"
      "v_sub_u32 v20, v23, v20\n"
      "v_mul_u32_u24 v22, 7, v22\n"
      "v_add_u32 v22, 9, v22\n"
      "v_subrev_u32 v23, 0x1ff, v22\n"
      "v_min_u32 v22, v22, v23\n"
      "v_mul_u32_u24 v20, 7, v20\n"
      "v_add_u32 v20, 9, v20\n"
      "v_subrev_u32 v23, 0x1ff, v20\n"
      "v_min_u32 v20, v20, v23\n"
      "v_mul_u32_u24 v22, 0x1ff, v22\n"
      "v_add_u32 v22, v22, v20\n"
      "v_mul_u32_u24 v22, 0x180, v22\n"
      "s_waitcnt vmcnt(0)\n"
      "ds_read_b128 v[16:19], v6 offset:24576\n"
      "ds_read_b128 v[12:15], v6 offset:26624\n"
      "ds_read_b128 v[24:27], v6 offset:28672\n"
      "ds_read_b128 v[28:31], v6 offset:30720\n"
      "s_waitcnt lgkmcnt(2)\n"
      "v_mfma_f32_16x16x32_bf16 v[32:35], a[96:99], v[16:19], 0\n"
      "v_mfma_f32_16x16x32_bf16 v[32:35], a[104:107], v[12:15], v[32:35]\n"
      "v_mfma_f32_16x16x32_bf16 v[36:39], a[100:103], v[16:19], 0\n"
      "v_mfma_f32_16x16x32_bf16 v[36:39], a[108:111], v[12:15], v[36:39]\n"
      "ds_read_b128 v[12:15], v6 offset:32768\n"
      "ds_read_b128 v[16:19], v6 offset:34816\n"
      "s_waitcnt lgkmcnt(2)\n"
      "v_mfma_f32_16x16x32_bf16 v[32:35], a[112:115], v[24:27], v[32:35]\n"
      "v_mfma_f32_16x16x32_bf16 v[32:35], a[120:123], v[28:31], v[32:35]\n"
      "v_mfma_f32_16x16x32_bf16 v[36:39], a[116:119], v[24:27], v[36:39]\n"
      "v_mfma_f32_16x16x32_bf16 v[36:39], a[124:127], v[28:31], v[36:39]\n"
      "ds_read_b128 v[28:31], v6 offset:24832\n"
      "ds_read_b128 v[24:27], v6 offset:26880\n"
      "s_waitcnt lgkmcnt(2)\n"
      "v_mfma_f32_16x16x32_bf16 v[32:35], a[128:131], v[12:15], v[32:35]\n"
      "v_mfma_f32_16x16x32_bf16 v[32:35], a[136:139], v[16:19], v[32:35]\n"
      "v_mfma_f32_16x16x32_bf16 v[36:39], a[132:135], v[12:15], v[36:39]\n"
      "v_mfma_f32_16x16x32_bf16 v[36:39], a[140:143], v[16:19], v[36:39]\n"
      "ds_read_b128 v[16:19], v6 offset:28928\n"
      "ds_read_b128 v[12:15], v6 offset:30976\n"
      "s_waitcnt lgkmcnt(2)\n"
      "v_mfma_f32_16x16x32_bf16 v[40:43], a[96:99], v[28:31], 0\n"
      "v_mfma_f32_16x16x32_bf16 v[40:43], a[104:107], v[24:27], v[40:43]\n"
      "v_mfma_f32_16x16x32_bf16 v[44:47], a[100:103], v[28:31], 0\n"
      "v_mfma_f32_16x16x32_bf16 v[44:47], a[108:111], v[24:27], v[44:47]\n"
      "ds_read_b128 v[24:27], v6 offset:33024\n"
      "ds_read_b128 v[28:31], v6 offset:35072\n"
      "s_waitcnt lgkmcnt(2)\n"
      "v_mfma_f32_16x16x32_bf16 v[40:43], a[112:115], v[16:19], v[40:43]\n"
      "v_mfma_f32_16x16x32_bf16 v[40:43], a[120:123], v[12:15], v[40:43]\n"
      "v_mfma_f32_16x16x32_bf16 v[44:47], a[116:119], v[16:19], v[44:47]\n"
      "v_mfma_f32_16x16x32_bf16 v[44:47], a[124:127], v[12:15], v[44:47]\n"
      "ds_read_b128 v[12:15], v6 offset:12544\n"
      "ds_read_b128 v[16:19], v6 offset:14592\n"
      "s_waitcnt lgkmcnt(2)\n"
      "v_mfma_f32_16x16x32_bf16 v[40:43], a[128:131], v[24:27], v[40:43]\n"
      "v_mfma_f32_16x16x32_bf16 v[40:43], a[136:139], v[28:31], v[40:43]\n"
      "v_mfma_f32_16x16x32_bf16 v[44:47], a[132:135], v[24:27], v[44:47]\n"
      "v_mfma_f32_16x16x32_bf16 v[44:47], a[140:143], v[28:31], v[44:47]\n"
      "ds_read_b128 v[28:31], v6 offset:16640\n"
      "ds_read_b128 v[24:27], v6 offset:18688\n"
      "s_waitcnt lgkmcnt(2)\n"
      "v_mfma_f32_16x16x32_bf16 v[48:51], a[96:99], v[12:15], 0\n"
      "v_mfma_f32_16x16x32_bf16 v[48:51], a[104:107], v[16:19], v[48:51]\n"
      "v_mfma_f32_16x16x32_bf16 v[52:55], a[100:103], v[12:15], 0\n"
      "v_mfma_f32_16x16x32_bf16 v[52:55], a[108:111], v[16:19], v[52:55]\n"
      "ds_read_b128 v[16:19], v6 offset:20736\n"
      "ds_read_b128 v[12:15], v6 offset:22784\n"
      "s_waitcnt lgkmcnt(2)\n"
      "v_mfma_f32_16x16x32_bf16 v[48:51], a[112:115], v[28:31], v[48:51]\n"
      "v_add_f32 v56, v32, v33\n"
      "v_mul_f32 v57, v32, v32\n"
      "v_fmac_f32 v57, v33, v33\n"
      "v_add_f32 v56, v56, v34\n"
      "v_fmac_f32 v57, v34, v34\n"
      "v_mfma_f32_16x16x32_bf16 v[48:51], a[120:123], v[24:27], v[48:51]\n"
      "v_add_f32 v56, v56, v35\n"
      "v_fmac_f32 v57, v35, v35\n"
      "v_add_f32 v56, v56, v36\n"
      "v_fmac_f32 v57, v36, v36\n"
      "v_add_f32 v56, v56, v37\n"
      "v_mfma_f32_16x16x32_bf16 v[52:55], a[116:119], v[28:31], v[52:55]\n"
      "v_fmac_f32 v57, v37, v37\n"
      "v_add_f32 v56, v56, v38\n"
      "v_fmac_f32 v57, v38, v38\n"
      "v_add_f32 v56, v56, v39\n"
      "v_fmac_f32 v57, v39, v39\n"
      "v_mfma_f32_16x16x32_bf16 v[52:55], a[124:127], v[24:27], v[52:55]\n"
      "s_nop 0\n"
      "v_permlane16_swap_b32 v56, v57\n"
      "v_add_f32 v56, v56, v57\n"
      "v_add_f32 v24, v40, v41\n"
      "v_mul_f32 v25, v40, v40\n"
      "s_waitcnt lgkmcnt(0)\n"
      "v_mfma_f32_16x16x32_bf16 v[48:51], a[128:131], v[16:19], v[48:51]\n"
      "v_fmac_f32 v25, v41, v41\n"
      "v_add_f32 v24, v24, v42\n"
      "v_fmac_f32 v25, v42, v42\n"
      "v_add_f32 v24, v24, v43\n"
      "v_fmac_f32 v25, v43, v43\n"
      "v_mfma_f32_16x16x32_bf16 v[48:51], a[136:139], v[12:15], v[48:51]\n"
      "v_add_f32 v24, v24, v44\n"
      "v_fmac_f32 v25, v44, v44\n"
      "v_add_f32 v24, v24, v45\n"
      "v_fmac_f32 v25, v45, v45\n"
      "v_add_f32 v24, v24, v46\n"
      "v_mfma_f32_16x16x32_bf16 v[52:55], a[132:135], v[16:19], v[52:55]\n"
      "v_fmac_f32 v25, v46, v46\n"
      "v_add_f32 v24, v24, v47\n"
      "v_fmac_f32 v25, v47, v47\n"
      "s_nop 1\n"
      "v_permlane16_swap_b32 v24, v25\n"
      "v_add_f32 v24, v24, v25\n"
      "v_mfma_f32_16x16x32_bf16 v[52:55], a[140:143], v[12:15], v[52:55]\n"
      "ds_read_b32 v12, v1 offset:1280\n"
      "v_permlane32_swap_b32 v56, v24\n"
      "v_add_f32 v56, v56, v24\n"
      "s_waitcnt lgkmcnt(0)\n"
      "ds_write_b32 v12, v56\n"
      "v_add_f32 v12, v48, v49\n"
      "v_mul_f32 v13, v48, v48\n"
      "v_fmac_f32 v13, v49, v49\n"
      "v_add_f32 v12, v12, v50\n"
      "v_fmac_f32 v13, v50, v50\n"
      "v_add_f32 v12, v12, v51\n"
      "v_fmac_f32 v13, v51, v51\n"
      "v_add_f32 v12, v12, v52\n"
      "v_fmac_f32 v13, v52, v52\n"
      "v_add_f32 v12, v12, v53\n"
      "v_fmac_f32 v13, v53, v53\n"
      "v_add_f32 v12, v12, v54\n"
      "v_fmac_f32 v13, v54, v54\n"
      "v_add_f32 v12, v12, v55\n"
      "v_fmac_f32 v13, v55, v55\n"
      "s_nop 1\n"
      "v_permlane16_swap_b32 v12, v13\n"
      "v_add_f32 v12, v12, v13\n"
      "ds_read_b32 v56, v1 offset:1280\n"
      "v_mov_b32 v13, v12\n"
      "s_nop 1\n"
      "v_permlane32_swap_b32_e64 v12, v13\n"
      "v_add_f32_e64 v12, v12, v13\n"
      "s_waitcnt lgkmcnt(0)\n"
      "ds_write_b32 v56, v12 offset:512\n"
      "s_waitcnt lgkmcnt(0)\n"
      "s_barrier\n"
      "s_mul_i32 s29, s25, 12288\n"
      "s_add_u32 s29, s29, 12582912\n"
      "s_add_u32 s34, s20, s29\n"
      "s_addc_u32 s35, s21, 0\n"
      "s_mul_i32 s30, s33, 3\n"
      "s_nop 0\n"
      "s_mov_b32 exec_lo, 0xfffff\n"
      "s_mov_b32 exec_hi, 0xfffff\n"
      "s_add_u32 m0, s30, 36864\n"
      "s_nop 0\n"
      "global_load_lds_dwordx4 v21, s[34:35]\n"
      "s_add_u32 m0, s30, 37856\n"
      "s_nop 0\n"
      "global_load_lds_dwordx4 v21, s[34:35] offset:32\n"
      "s_add_u32 m0, s30, 38848\n"
      "s_nop 0\n"
      "global_load_lds_dwordx4 v21, s[34:35] offset:64\n"
      "s_mov_b64 exec, -1\n"
      "ds_read_b32 v21, v1 offset:1536\n"
      "v_add_u32 v12, 108800, v2\n"
      "ds_read_b128 v[16:19], v12\n"
      "ds_read_b128 v[24:27], v12 offset:1024\n"
      "ds_read_b32 v12, v1\n"
      "s_waitcnt lgkmcnt(2)\n"
      "v_add_f32 v16, v16, v18\n"
      "v_add_f32 v17, v17, v19\n"
      "s_waitcnt lgkmcnt(1)\n"
      "v_add_f32 v24, v24, v26\n"
      "v_add_f32 v25, v25, v27\n"
      "v_add_f32 v16, v16, v24\n"
      "v_add_f32 v17, v17, v25\n"
      "v_fma_f32 v24, -v16, v16, s74\n"
      "v_fmac_f32 v24, 0x43400000, v17\n"
      "v_max_f32 v24, s74, v24\n"
      "v_rsq_f32_e64 v24, v24\n"
      "s_nop 0\n"
      "v_mul_f32 v17, 0x43400000, v24\n"
      "v_mul_f32 v25, -v16, v24\n"
      "v_mov_b32 v16, v17\n"
      "v_mov_b32 v24, v25\n"
      "s_nop 0\n"
      "v_permlane32_swap_b32 v17, v16\n"
      "v_permlane32_swap_b32 v25, v24\n"
      "v_mov_b32 v27, v17\n"
      "v_mov_b32 v26, v25\n"
      "s_nop 0\n"
      "v_permlane16_swap_b32 v17, v27\n"
      "v_permlane16_swap_b32_e64 v25, v26\n"
      "v_fma_f32 v32, v32, v17, v25\n"
      "v_fma_f32 v33, v33, v17, v25\n"
      "v_cvt_pk_bf16_f32 v32, v32, v33\n"
      "v_fma_f32 v34, v34, v17, v25\n"
      "v_fma_f32 v35, v35, v17, v25\n"
      "v_cvt_pk_bf16_f32 v33, v34, v35\n"
      "v_fma_f32 v36, v36, v17, v25\n"
      "v_fma_f32 v37, v37, v17, v25\n"
      "v_cvt_pk_bf16_f32 v34, v36, v37\n"
      "v_fma_f32 v38, v38, v17, v25\n"
      "v_fma_f32 v39, v39, v17, v25\n"
      "v_cvt_pk_bf16_f32 v35, v38, v39\n"
      "s_waitcnt lgkmcnt(0)\n"
      "ds_write_b128 v12, v[32:35] offset:24576\n"
      "v_fma_f32 v40, v40, v27, v26\n"
      "v_fma_f32 v41, v41, v27, v26\n"
      "v_cvt_pk_bf16_f32 v40, v40, v41\n"
      "v_fma_f32 v42, v42, v27, v26\n"
      "v_fma_f32 v43, v43, v27, v26\n"
      "v_cvt_pk_bf16_f32 v41, v42, v43\n"
      "v_fma_f32 v44, v44, v27, v26\n"
      "v_fma_f32 v45, v45, v27, v26\n"
      "v_cvt_pk_bf16_f32 v42, v44, v45\n"
      "v_fma_f32 v46, v46, v27, v26\n"
      "v_fma_f32 v47, v47, v27, v26\n"
      "v_cvt_pk_bf16_f32 v43, v46, v47\n"
      "ds_write_b128 v12, v[40:43] offset:25600\n"
      "v_add_u32 v46, s71, v2\n"
      "v_add_u32 v47, s72, v1\n"
      "ds_read_b32 v26, v47 offset:1792\n"
      "ds_read_b32 v27, v47 offset:2048\n"
      "ds_read_b128 v[32:35], v46 offset:24576\n"
      "ds_read_b128 v[40:43], v46 offset:25600\n"
      "s_add_u32 s14, s49, s70\n"
      "s_add_u32 s66, s22, s14\n"
      "s_addc_u32 s67, s23, 0\n"
      "s_or_b32 s14, s50, s51\n"
      "s_waitcnt lgkmcnt(2)\n"
      "s_cbranch_scc1 L_edge_45_%=\n"
      "L_eb_46_%=:\n"
      "s_waitcnt lgkmcnt(1)\n"
      "global_store_dwordx4 v26, v[32:35], s[66:67] sc1\n"
      "s_waitcnt lgkmcnt(0)\n"
      "global_store_dwordx4 v27, v[40:43], s[66:67] sc1\n"
      "v_fma_f32 v48, v48, v16, v24\n"
      "v_fma_f32 v49, v49, v16, v24\n"
      "v_cvt_pk_bf16_f32 v48, v48, v49\n"
      "v_fma_f32 v50, v50, v16, v24\n"
      "v_fma_f32 v51, v51, v16, v24\n"
      "v_cvt_pk_bf16_f32 v49, v50, v51\n"
      "v_fma_f32 v52, v52, v16, v24\n"
      "v_fma_f32 v53, v53, v16, v24\n"
      "v_cvt_pk_bf16_f32 v50, v52, v53\n"
      "v_fma_f32 v54, v54, v16, v24\n"
      "v_fma_f32 v55, v55, v16, v24\n"
      "v_cvt_pk_bf16_f32 v51, v54, v55\n"
      "ds_write_b128 v12, v[48:51] offset:24576\n"
      "v_add_u32_e64 v12, s71, v2\n"
      "v_add_u32_e64 v54, s72, v1\n"
      "ds_read_b32 v55, v54 offset:4608\n"
      "ds_read_b128 v[48:51], v12 offset:24576\n"
      "s_add_u32 s14, s49, s70\n"
      "s_add_u32 s66, s22, s14\n"
      "s_addc_u32 s67, s23, 0\n"
      "s_or_b32 s14, s50, s51\n"
      "s_waitcnt lgkmcnt(1)\n"
      "s_cbranch_scc1 L_edge_47_%=\n"
      "L_eb_48_%=:\n"
      "s_waitcnt lgkmcnt(0)\n"
      "global_store_dwordx4 v55, v[48:51], s[66:67] sc1\n"
      "s_waitcnt vmcnt(3)\n"
      "s_waitcnt lgkmcnt(0)\n"
      "s_barrier\n"
      "ds_read_b128 v[48:51], v6\n"
      "ds_read_b128 v[52:55], v6 offset:2048\n"
      "ds_read_b128 v[40:43], v6 offset:4096\n"
      "ds_read_b128 v[32:35], v6 offset:6144\n"
      "s_waitcnt lgkmcnt(2)\n"
      "v_mfma_f32_16x16x32_bf16 v[24:27], a[96:99], v[48:51], 0\n"
      "v_mfma_f32_16x16x32_bf16 v[24:27], a[104:107], v[52:55], v[24:27]\n"
      "v_mfma_f32_16x16x32_bf16 v[44:47], a[100:103], v[48:51], 0\n"
      "v_mfma_f32_16x16x32_bf16 v[44:47], a[108:111], v[52:55], v[44:47]\n"
      "ds_read_b128 v[52:55], v6 offset:8192\n"
      "ds_read_b128 v[48:51], v6 offset:10240\n"
      "s_waitcnt lgkmcnt(2)\n"
      "v_mfma_f32_16x16x32_bf16 v[24:27], a[112:115], v[40:43], v[24:27]\n"
      "v_mfma_f32_16x16x32_bf16 v[24:27], a[120:123], v[32:35], v[24:27]\n"
      "v_mfma_f32_16x16x32_bf16 v[44:47], a[116:119], v[40:43], v[44:47]\n"
      "v_mfma_f32_16x16x32_bf16 v[44:47], a[124:127], v[32:35], v[44:47]\n"
      "ds_read_b128 v[32:35], v6 offset:256\n"
      "ds_read_b128 v[40:43], v6 offset:2304\n"
      "s_waitcnt lgkmcnt(2)\n"
      "v_mfma_f32_16x16x32_bf16 v[24:27], a[128:131], v[52:55], v[24:27]\n"
      "v_mfma_f32_16x16x32_bf16 v[24:27], a[136:139], v[48:51], v[24:27]\n"
      "v_mfma_f32_16x16x32_bf16 v[44:47], a[132:135], v[52:55], v[44:47]\n"
      "v_mfma_f32_16x16x32_bf16 v[44:47], a[140:143], v[48:51], v[44:47]\n"
      "ds_read_b128 v[48:51], v6 offset:4352\n"
      "ds_read_b128 v[52:55], v6 offset:6400\n"
      "s_waitcnt lgkmcnt(2)\n"
      "v_mfma_f32_16x16x32_bf16 v[36:39], a[96:99], v[32:35], 0\n"
      "v_mfma_f32_16x16x32_bf16 v[36:39], a[104:107], v[40:43], v[36:39]\n"
      "v_mfma_f32_16x16x32_bf16 v[16:19], a[100:103], v[32:35], 0\n"
      "v_mfma_f32_16x16x32_bf16 v[16:19], a[108:111], v[40:43], v[16:19]\n"
      "ds_read_b128 v[40:43], v6 offset:8448\n"
      "ds_read_b128 v[32:35], v6 offset:10496\n"
      "s_waitcnt lgkmcnt(2)\n"
      "v_mfma_f32_16x16x32_bf16 v[36:39], a[112:115], v[48:51], v[36:39]\n"
      "v_mfma_f32_16x16x32_bf16 v[36:39], a[120:123], v[52:55], v[36:39]\n"
      "v_mfma_f32_16x16x32_bf16 v[16:19], a[116:119], v[48:51], v[16:19]\n"
      "v_mfma_f32_16x16x32_bf16 v[16:19], a[124:127], v[52:55], v[16:19]\n"
      "s_waitcnt lgkmcnt(0)\n"
      "v_mfma_f32_16x16x32_bf16 v[36:39], a[128:131], v[40:43], v[36:39]\n"
      "v_mfma_f32_16x16x32_bf16 v[36:39], a[136:139], v[32:35], v[36:39]\n"
      "v_mfma_f32_16x16x32_bf16 v[16:19], a[132:135], v[40:43], v[16:19]\n"
      "v_mfma_f32_16x16x32_bf16 v[16:19], a[140:143], v[32:35], v[16:19]\n"
      "v_add_f32 v32, v24, v25\n"
      "v_mul_f32 v33, v24, v24\n"
      "v_fmac_f32 v33, v25, v25\n"
      "v_add_f32 v32, v32, v26\n"
      "v_fmac_f32 v33, v26, v26\n"
      "v_add_f32 v32, v32, v27\n"
      "v_fmac_f32 v33, v27, v27\n"
      "v_add_f32 v32, v32, v44\n"
      "v_fmac_f32 v33, v44, v44\n"
      "v_add_f32 v32, v32, v45\n"
      "v_fmac_f32 v33, v45, v45\n"
      "v_add_f32 v32, v32, v46\n"
      "v_fmac_f32 v33, v46, v46\n"
      "v_add_f32 v32, v32, v47\n"
      "v_fmac_f32 v33, v47, v47\n"
      "s_nop 1\n"
      "v_permlane16_swap_b32 v32, v33\n"
      "v_add_f32 v32, v32, v33\n"
      "v_add_f32 v33, v36, v37\n"
      "v_mul_f32 v34, v36, v36\n"
      "v_fmac_f32 v34, v37, v37\n"
      "v_add_f32 v33, v33, v38\n"
      "v_fmac_f32 v34, v38, v38\n"
      "v_add_f32 v33, v33, v39\n"
      "v_fmac_f32 v34, v39, v39\n"
      "v_add_f32 v33, v33, v16\n"
      "v_fmac_f32 v34, v16, v16\n"
      "v_add_f32 v33, v33, v17\n"
      "v_fmac_f32 v34, v17, v17\n"
      "v_add_f32 v33, v33, v18\n"
      "v_fmac_f32 v34, v18, v18\n"
      "v_add_f32 v33, v33, v19\n"
      "v_fmac_f32 v34, v19, v19\n"
      "s_nop 1\n"
      "v_permlane16_swap_b32 v33, v34\n"
      "v_add_f32 v33, v33, v34\n"
      "ds_read_b32 v34, v1 offset:1280\n"
      "s_nop 0\n"
      "v_permlane32_swap_b32 v32, v33\n"
      "v_add_f32 v32, v32, v33\n"
      "s_waitcnt lgkmcnt(0)\n"
      "ds_write_b32 v34, v32\n"
      "s_waitcnt lgkmcnt(0)\n"
      "s_barrier\n"
      "ds_read_b128 v[32:35], v21\n"
      "ds_read_b128 v[40:43], v21 offset:1024\n"
      "ds_read_b32 v21, v1\n"
      "s_waitcnt lgkmcnt(2)\n"
      "v_add_f32 v32, v32, v34\n"
      "v_add_f32 v33, v33, v35\n"
      "s_waitcnt lgkmcnt(1)\n"
      "v_add_f32 v40, v40, v42\n"
      "v_add_f32 v41, v41, v43\n"
      "v_add_f32 v32, v32, v40\n"
      "v_add_f32 v33, v33, v41\n"
      "v_fma_f32 v40, -v32, v32, s74\n"
      "v_fmac_f32 v40, 0x43400000, v33\n"
      "v_max_f32 v40, s74, v40\n"
      "v_rsq_f32 v40, v40\n"
      "s_nop 0\n"
      "v_mul_f32 v33, 0x43400000, v40\n"
      "v_mul_f32 v41, -v32, v40\n"
      "v_mov_b32 v32, v33\n"
      "v_mov_b32 v40, v41\n"
      "s_nop 0\n"
      "v_permlane16_swap_b32 v33, v32\n"
      "v_permlane16_swap_b32 v41, v40\n"
      "v_fma_f32 v24, v24, v33, v41\n"
      "v_fma_f32 v25, v25, v33, v41\n"
      "v_cvt_pk_bf16_f32 v24, v24, v25\n"
      "v_fma_f32 v26, v26, v33, v41\n"
      "v_fma_f32 v27, v27, v33, v41\n"
      "v_cvt_pk_bf16_f32 v25, v26, v27\n"
      "v_fma_f32 v44, v44, v33, v41\n"
      "v_fma_f32 v45, v45, v33, v41\n"
      "v_cvt_pk_bf16_f32 v26, v44, v45\n"
      "v_fma_f32 v46, v46, v33, v41\n"
      "v_fma_f32 v47, v47, v33, v41\n"
      "v_cvt_pk_bf16_f32 v27, v46, v47\n"
      "s_waitcnt lgkmcnt(0)\n"
      "ds_write_b128 v21, v[24:27] offset:24576\n"
      "v_fma_f32 v36, v36, v32, v40\n"
      "v_fma_f32 v37, v37, v32, v40\n"
      "v_cvt_pk_bf16_f32 v36, v36, v37\n"
      "v_fma_f32 v38, v38, v32, v40\n"
      "v_fma_f32 v39, v39, v32, v40\n"
      "v_cvt_pk_bf16_f32 v37, v38, v39\n"
      "v_fma_f32 v16, v16, v32, v40\n"
      "v_fma_f32 v17, v17, v32, v40\n"
      "v_cvt_pk_bf16_f32 v38, v16, v17\n"
      "v_fma_f32 v18, v18, v32, v40\n"
      "v_fma_f32 v19, v19, v32, v40\n"
      "v_cvt_pk_bf16_f32 v39, v18, v19\n"
      "ds_write_b128 v21, v[36:39] offset:25600\n"
      "v_add_u32 v21, s71, v2\n"
      "ds_read_b128 v[24:27], v21 offset:24576\n"
      "ds_read_b128 v[36:39], v21 offset:25600\n"
      "v_bfe_u32 v21, v2, 6, 4\n"
      "v_add_u32_e64 v18, 0, v21\n"
      "v_bfe_u32 v19, v18, 1, 2\n"
      "v_lshlrev_b32 v18, 2, v18\n"
      "ds_bpermute_b32 v32, v18, v22\n"
      "v_bfe_u32 v18, v2, 4, 2\n"
      "v_xor_b32 v18, v18, v19\n"
      "s_waitcnt lgkmcnt(0)\n"
      "v_lshl_add_u32 v32, v18, 4, v32\n"
      "v_add_u32 v18, 16, v21\n"
      "v_bfe_u32 v19, v18, 1, 2\n"
      "v_lshlrev_b32_e64 v18, 2, v18\n"
      "ds_bpermute_b32 v21, v18, v22\n"
      "v_bfe_u32 v18, v2, 4, 2\n"
      "v_xor_b32 v18, v18, v19\n"
      "s_waitcnt lgkmcnt(0)\n"
      "v_lshl_add_u32 v21, v18, 4, v21\n"
      "s_add_u32 s66, s22, s70\n"
      "s_addc_u32 s67, s23, 0\n"
      "global_store_dwordx4 v32, v[24:27], s[66:67] sc1\n"
      "s_mov_b32 s34, 0xffff\n"
      "s_mov_b32 s35, 0x0\n"
      "s_mov_b64 exec, s[34:35]\n"
      "global_store_dwordx4 v21, v[36:39], s[66:67] sc1\n"
      "s_mov_b64 exec, -1\n"
      "s_endpgm\n"
      "L_edge_11_%=:\n"
      "s_mov_b64 exec, s[36:37]\n"
      "v_subrev_u32 v18, s41, v18\n"
      "s_mov_b64 exec, s[38:39]\n"
      "v_subrev_u32 v18, s42, v18\n"
      "s_mov_b64 exec, -1\n"
      "s_nop 4\n"
      "s_branch L_eb_12_%=\n"
      "L_edge_13_%=:\n"
      "s_mov_b64 exec, -1\n"
      "v_subrev_u32 v18, s41, v18\n"
      "s_mov_b64 exec, s[88:89]\n"
      "v_subrev_u32 v18, s42, v18\n"
      "s_mov_b64 exec, -1\n"
      "s_nop 4\n"
      "s_branch L_eb_14_%=\n"
      "L_edge_20_%=:\n"
      "s_mov_b64 exec, s[36:37]\n"
      "v_subrev_u32 v12, s44, v12\n"
      "s_mov_b64 exec, s[38:39]\n"
      "v_subrev_u32 v12, s45, v12\n"
      "s_mov_b64 exec, -1\n"
      "s_nop 4\n"
      "s_branch L_eb_21_%=\n"
      "L_edge_23_%=:\n"
      "s_mov_b64 exec, s[36:37]\n"
      "v_subrev_u32 v17, s53, v17\n"
      "s_mov_b64 exec, s[38:39]\n"
      "v_subrev_u32 v17, s54, v17\n"
      "s_mov_b64 exec, -1\n"
      "s_nop 4\n"
      "s_branch L_eb_24_%=\n"
      "L_edge_25_%=:\n"
      "s_mov_b64 exec, -1\n"
      "v_subrev_u32 v17, s53, v17\n"
      "s_mov_b64 exec, s[88:89]\n"
      "v_subrev_u32 v17, s54, v17\n"
      "s_mov_b64 exec, -1\n"
      "s_nop 4\n"
      "s_branch L_eb_26_%=\n"
      "L_edge_27_%=:\n"
      "s_sub_u32 s66, s66, s41\n"
      "s_subb_u32 s67, s67, 0\n"
      "s_sub_u32 s66, s66, s42\n"
      "s_subb_u32 s67, s67, 0\n"
      "s_mov_b32 s34, 0xffffffff\n"
      "s_mov_b32 s35, 0xffffffff\n"
      "s_mov_b64 exec, s[34:35]\n"
      "v_add_u32_e64 v76, s41, v76\n"
      "s_mov_b32 s34, 0xf000ffff\n"
      "s_mov_b32 s35, 0xff000fff\n"
      "s_mov_b64 exec, s[34:35]\n"
      "v_add_u32_e64 v76, s42, v76\n"
      "s_mov_b32 s34, 0xffffffff\n"
      "s_mov_b32 s35, 0xffff\n"
      "s_mov_b64 exec, s[34:35]\n"
      "v_add_u32 v77, s41, v77\n"
      "s_mov_b32 s34, 0xfff000ff\n"
      "s_mov_b32 s35, 0xffff000f\n"
      "s_mov_b64 exec, s[34:35]\n"
      "v_add_u32 v77, s42, v77\n"
      "s_mov_b64 exec, -1\n"
      "s_nop 4\n"
      "s_branch L_eb_28_%=\n"
      "L_edge_29_%=:\n"
      "s_sub_u32 s66, s66, s41\n"
      "s_subb_u32 s67, s67, 0\n"
      "s_sub_u32 s66, s66, s42\n"
      "s_subb_u32 s67, s67, 0\n"
      "s_mov_b32 s34, 0xffff000\n"
      "s_mov_b32 s35, 0xffff00\n"
      "s_mov_b64 exec, s[34:35]\n"
      "v_add_u32 v26, s42, v26\n"
      "s_mov_b64 exec, -1\n"
      "s_nop 4\n"
      "s_branch L_eb_30_%=\n"
      "L_edge_31_%=:\n"
      "s_mov_b64 exec, s[36:37]\n"
      "v_subrev_u32 v20, s44, v20\n"
      "s_mov_b64 exec, s[38:39]\n"
      "v_subrev_u32 v20, s45, v20\n"
      "s_mov_b64 exec, -1\n"
      "s_nop 4\n"
      "s_branch L_eb_32_%=\n"
      "L_edge_33_%=:\n"
      "s_sub_u32 s66, s66, s50\n"
      "s_subb_u32 s67, s67, 0\n"
      "s_sub_u32 s66, s66, s51\n"
      "s_subb_u32 s67, s67, 0\n"
      "s_mov_b32 s34, 0xffffffff\n"
      "s_mov_b32 s35, 0xffffffff\n"
      "s_mov_b64 exec, s[34:35]\n"
      "v_add_u32 v73, s50, v73\n"
      "s_mov_b32 s34, 0xf000ffff\n"
      "s_mov_b32 s35, 0xff000fff\n"
      "s_mov_b64 exec, s[34:35]\n"
      "v_add_u32 v73, s51, v73\n"
      "s_mov_b32 s34, 0xffffffff\n"
      "s_mov_b32 s35, 0xffff\n"
      "s_mov_b64 exec, s[34:35]\n"
      "v_add_u32 v74, s50, v74\n"
      "s_mov_b32 s34, 0xfff000ff\n"
      "s_mov_b32 s35, 0xffff000f\n"
      "s_mov_b64 exec, s[34:35]\n"
      "v_add_u32 v74, s51, v74\n"
      "s_mov_b64 exec, -1\n"
      "s_nop 4\n"
      "s_branch L_eb_34_%=\n"
      "L_edge_35_%=:\n"
      "s_sub_u32 s66, s66, s50\n"
      "s_subb_u32 s67, s67, 0\n"
      "s_sub_u32 s66, s66, s51\n"
      "s_subb_u32 s67, s67, 0\n"
      "s_mov_b32 s34, 0xffff000\n"
      "s_mov_b32 s35, 0xffff00\n"
      "s_mov_b64 exec, s[34:35]\n"
      "v_add_u32 v25, s51, v25\n"
      "s_mov_b64 exec, -1\n"
      "s_nop 4\n"
      "s_branch L_eb_36_%=\n"
      "L_edge_37_%=:\n"
      "s_sub_u32 s66, s66, s50\n"
      "s_subb_u32 s67, s67, 0\n"
      "s_sub_u32 s66, s66, s51\n"
      "s_subb_u32 s67, s67, 0\n"
      "s_mov_b32 s34, 0xffffffff\n"
      "s_mov_b32 s35, 0xffffffff\n"
      "s_mov_b64 exec, s[34:35]\n"
      "v_add_u32 v96, s50, v96\n"
      "s_mov_b32 s34, 0xf000ffff\n"
      "s_mov_b32 s35, 0xff000fff\n"
      "s_mov_b64 exec, s[34:35]\n"
      "v_add_u32 v96, s51, v96\n"
      "s_mov_b32 s34, 0xffffffff\n"
      "s_mov_b32 s35, 0xffff\n"
      "s_mov_b64 exec, s[34:35]\n"
      "v_add_u32_e64 v14, s50, v14\n"
      "s_mov_b32 s34, 0xfff000ff\n"
      "s_mov_b32 s35, 0xffff000f\n"
      "s_mov_b64 exec, s[34:35]\n"
      "v_add_u32 v14, s51, v14\n"
      "s_mov_b64 exec, -1\n"
      "s_nop 4\n"
      "s_branch L_eb_38_%=\n"
      "L_edge_39_%=:\n"
      "s_sub_u32 s66, s66, s50\n"
      "s_subb_u32 s67, s67, 0\n"
      "s_sub_u32 s66, s66, s51\n"
      "s_subb_u32 s67, s67, 0\n"
      "s_mov_b32 s34, 0xffff000\n"
      "s_mov_b32 s35, 0xffff00\n"
      "s_mov_b64 exec, s[34:35]\n"
      "v_add_u32 v19, s51, v19\n"
      "s_mov_b64 exec, -1\n"
      "s_nop 4\n"
      "s_branch L_eb_40_%=\n"
      "L_edge_41_%=:\n"
      "s_sub_u32 s66, s66, s41\n"
      "s_subb_u32 s67, s67, 0\n"
      "s_sub_u32 s66, s66, s42\n"
      "s_subb_u32 s67, s67, 0\n"
      "s_mov_b32 s34, 0xffffffff\n"
      "s_mov_b32 s35, 0xffffffff\n"
      "s_mov_b64 exec, s[34:35]\n"
      "v_add_u32 v12, s41, v12\n"
      "s_mov_b32 s34, 0xf000ffff\n"
      "s_mov_b32 s35, 0xff000fff\n"
      "s_mov_b64 exec, s[34:35]\n"
      "v_add_u32 v12, s42, v12\n"
      "s_mov_b32 s34, 0xffffffff\n"
      "s_mov_b32 s35, 0xffff\n"
      "s_mov_b64 exec, s[34:35]\n"
      "v_add_u32_e64 v80, s41, v80\n"
      "s_mov_b32 s34, 0xfff000ff\n"
      "s_mov_b32 s35, 0xffff000f\n"
      "s_mov_b64 exec, s[34:35]\n"
      "v_add_u32 v80, s42, v80\n"
      "s_mov_b64 exec, -1\n"
      "s_nop 4\n"
      "s_branch L_eb_42_%=\n"
      "L_edge_43_%=:\n"
      "s_sub_u32 s66, s66, s41\n"
      "s_subb_u32 s67, s67, 0\n"
      "s_sub_u32 s66, s66, s42\n"
      "s_subb_u32 s67, s67, 0\n"
      "s_sub_u32 s68, s68, s41\n"
      "s_subb_u32 s69, s69, 0\n"
      "s_sub_u32 s68, s68, s42\n"
      "s_subb_u32 s69, s69, 0\n"
      "s_mov_b32 s34, 0xffff000\n"
      "s_mov_b32 s35, 0xffff00\n"
      "s_mov_b64 exec, s[34:35]\n"
      "v_add_u32_e64 v91, s42, v91\n"
      "s_mov_b32 s34, 0xffff000\n"
      "s_mov_b32 s35, 0xffff00\n"
      "s_mov_b64 exec, s[34:35]\n"
      "v_add_u32 v20, s42, v20\n"
      "s_mov_b64 exec, -1\n"
      "s_nop 4\n"
      "s_branch L_eb_44_%=\n"
      "L_edge_45_%=:\n"
      "s_sub_u32 s66, s66, s50\n"
      "s_subb_u32 s67, s67, 0\n"
      "s_sub_u32 s66, s66, s51\n"
      "s_subb_u32 s67, s67, 0\n"
      "s_mov_b32 s34, 0xffffffff\n"
      "s_mov_b32 s35, 0xffffffff\n"
      "s_mov_b64 exec, s[34:35]\n"
      "v_add_u32 v26, s50, v26\n"
      "s_mov_b32 s34, 0xf000ffff\n"
      "s_mov_b32 s35, 0xff000fff\n"
      "s_mov_b64 exec, s[34:35]\n"
      "v_add_u32 v26, s51, v26\n"
      "s_mov_b32 s34, 0xffffffff\n"
      "s_mov_b32 s35, 0xffff\n"
      "s_mov_b64 exec, s[34:35]\n"
      "v_add_u32 v27, s50, v27\n"
      "s_mov_b32 s34, 0xfff000ff\n"
      "s_mov_b32 s35, 0xffff000f\n"
      "s_mov_b64 exec, s[34:35]\n"
      "v_add_u32 v27, s51, v27\n"
      "s_mov_b64 exec, -1\n"
      "s_nop 4\n"
      "s_branch L_eb_46_%=\n"
      "L_edge_47_%=:\n"
      "s_sub_u32 s66, s66, s50\n"
      "s_subb_u32 s67, s67, 0\n"
      "s_sub_u32 s66, s66, s51\n"
      "s_subb_u32 s67, s67, 0\n"
      "s_mov_b32 s34, 0xffff000\n"
      "s_mov_b32 s35, 0xffff00\n"
      "s_mov_b64 exec, s[34:35]\n"
      "v_add_u32 v55, s51, v55\n"
      "s_mov_b64 exec, -1\n"
      "s_nop 4\n"
      "s_branch L_eb_48_%=\n"
      "L_B1_%=:\n"
      "v_mbcnt_lo_u32_b32 v12, -1, 0\n"
      "v_mbcnt_hi_u32_b32 v12, -1, v12\n"
      "v_lshlrev_b32_e64 v12, 4, v12\n"
      "s_cmp_lt_u32 s25, 209\n"
      "s_cselect_b32 s26, 11, 10\n"
      "s_mov_b32 s27, 0\n"
      "s_mov_b32 s28, s25\n"
      "v_lshrrev_b32 v13, 2, v12\n"
      "s_sub_u32 s33, s24, 4\n"
      "v_lshrrev_b32 v14, 7, v12\n"
      "v_bfe_u32 v15, v12, 4, 3\n"
      "v_add_u32 v2, 36864, v12\n"
      "s_lshl_b32 s29, s33, 3\n"
      "v_add_u32 v16, s29, v14\n"
      "v_lshrrev_b32 v17, 1, v16\n"
      "v_lshlrev_b32 v17, 9, v17\n"
      "v_and_b32 v18, 1, v16\n"
      "v_add_u32 v17, v17, v18\n"
      "s_lshl_b32 s29, s25, 1\n"
      "v_add_u32 v17, s29, v17\n"
      "s_add_u32 s29, s25, 5120\n"
      "s_min_u32 s29, s29, 5328\n"
      "v_mov_b32 v18, s29\n"
      "v_cmp_gt_u32 vcc, 20, v16\n"
      "s_nop 1\n"
      "v_cndmask_b32 v17, v18, v17, vcc\n"
      "s_mov_b32 s29, 58835169\n"
      "v_mul_hi_u32 v18, v17, s29\n"
      "v_mul_u32_u24 v19, 73, v18\n"
      "v_sub_u32 v19, v17, v19\n"
      "v_mul_u32_u24 v18, 7, v18\n"
      "v_add_u32 v18, 9, v18\n"
      "v_subrev_u32 v17, 0x1ff, v18\n"
      "v_min_u32 v18, v18, v17\n"
      "v_mul_u32_u24 v19, 7, v19\n"
      "v_add_u32 v19, 9, v19\n"
      "v_subrev_u32 v17, 0x1ff, v19\n"
      "v_min_u32 v19, v19, v17\n"
      "v_mul_u32_u24 v18, 0x1ff, v18\n"
      "v_add_u32 v18, v18, v19\n"
      "v_mul_u32_u24 v18, 0x180, v18\n"
      "v_bfe_u32 v19, v16, 1, 3\n"
      "v_xor_b32 v19, v19, v15\n"
      "v_lshl_add_u32 v17, v19, 4, v18\n"
      "s_lshl_b32 s33, s33, 10\n"
      "s_waitcnt lgkmcnt(0)\n"
      "s_add_u32 m0, s33, 36864\n"
      "s_nop 0\n"
      "global_load_lds_dwordx4 v17, s[16:17]\n"
      "s_add_u32 m0, s33, 40832\n"
      "s_nop 0\n"
      "global_load_lds_dwordx4 v17, s[16:17] offset:128\n"
      "s_add_u32 m0, s33, 44800\n"
      "s_nop 0\n"
      "global_load_lds_dwordx4 v17, s[16:17] offset:256\n"
      "s_load_dwordx16 s[64:79], s[82:83], 0\n"
      "s_mov_b64 s[92:93], s[62:63]\n"
      "s_load_dwordx16 s[48:63], s[84:85], 0\n"
      "s_lshr_b32 s29, s33, 2\n"
      "v_add_u32_e64 v17, s29, v13\n"
      "v_min_u32 v17, 672, v17\n"
      "v_lshlrev_b32 v18, 1, v17\n"
      "v_add_u32 v18, 145664, v18\n"
      "global_load_dword v17, v17, s[86:87]\n"
      "s_add_u32 s29, s33, 0\n"
      "v_add_u32 v19, s29, v12\n"
      "v_min_u32 v19, 9588, v19\n"
      "s_add_u32 m0, s33, 24576\n"
      "s_nop 0\n"
      "global_load_lds_dwordx4 v19, s[90:91]\n"
      "s_add_u32 s29, s33, 4096\n"
      "v_add_u32 v19, s29, v12\n"
      "v_min_u32 v19, 9588, v19\n"
      "s_add_u32 m0, s33, 28672\n"
      "s_nop 0\n"
      "global_load_lds_dwordx4 v19, s[90:91]\n"
      "s_cmp_ge_u32 s24, 6\n"
      "s_cbranch_scc1 L_ix_51_%=\n"
      "s_add_u32 s29, s33, 8192\n"
      "v_add_u32_e64 v19, s29, v12\n"
      "v_min_u32 v19, 9588, v19\n"
      "s_add_u32 m0, s33, 32768\n"
      "s_nop 0\n"
      "global_load_lds_dwordx4 v19, s[90:91]\n"
      "L_ix_51_%=:\n"
      "v_mul_u32_u24 v19, 37, v16\n"
      "v_lshrrev_b32 v19, 8, v19\n"
      "v_mul_u32_u24 v20, 7, v19\n"
      "v_sub_u32_e64 v20, v16, v20\n"
      "v_cmp_le_u32 s[36:37], 4, v19\n"
      "v_cmp_le_u32 s[38:39], 4, v20\n"
      "v_mul_u32_u24 v19, 0x1ff, v19\n"
      "v_add_u32 v19, v19, v20\n"
      "v_mul_u32_u24 v19, 0x180, v19\n"
      "v_bfe_u32 v20, v16, 1, 3\n"
      "v_xor_b32 v20, v20, v15\n"
      "v_lshl_add_u32 v0, v20, 4, v19\n"
      "s_lshl_b32 s10, s28, 1\n"
      "s_add_u32 s11, s10, 1\n"
      "s_mul_hi_u32 s29, s10, 58835169\n"
      "s_mul_i32 s30, s29, 73\n"
      "s_sub_u32 s30, s10, s30\n"
      "s_mul_i32 s40, s29, 1373568\n"
      "s_mul_i32 s31, s30, 2688\n"
      "s_add_u32 s40, s40, s31\n"
      "s_add_u32 s40, s40, 589824\n"
      "s_cmp_eq_u32 s29, 72\n"
      "s_cselect_b32 s41, 100270464, 0\n"
      "s_cmp_eq_u32 s30, 72\n"
      "s_cselect_b32 s42, 196224, 0\n"
      "s_mul_hi_u32 s29, s11, 58835169\n"
      "s_mul_i32 s30, s29, 73\n"
      "s_sub_u32 s30, s11, s30\n"
      "s_mul_i32 s43, s29, 1373568\n"
      "s_mul_i32 s31, s30, 2688\n"
      "s_add_u32 s43, s43, s31\n"
      "s_add_u32 s43, s43, 589824\n"
      "s_cmp_eq_u32 s29, 72\n"
      "s_cselect_b32 s44, 100270464, 0\n"
      "s_cmp_eq_u32 s30, 72\n"
      "s_cselect_b32 s45, 196224, 0\n"
      "s_lshr_b32 s29, s33, 10\n"
      "s_and_b32 s29, s29, 1\n"
      "s_lshl_b32 s29, s29, 3\n"
      "v_add_u32 v19, s29, v14\n"
      "v_add_u32 v14, 32, v19\n"
      "v_mul_u32_u24 v16, 37, v14\n"
      "v_lshrrev_b32 v16, 8, v16\n"
      "v_mul_u32_u24 v20, 7, v16\n"
      "v_sub_u32_e64 v20, v14, v20\n"
      "v_cmp_le_u32 s[88:89], 4, v20\n"
      "v_mul_u32_u24 v16, 0x1ff, v16\n"
      "v_add_u32 v16, v16, v20\n"
      "v_mul_u32_u24 v16, 0x180, v16\n"
      "v_bfe_u32 v14, v19, 1, 3\n"
      "v_xor_b32 v14, v14, v15\n"
      "v_lshl_add_u32 v15, v14, 4, v16\n"
      "s_add_u32 s32, s18, 25589857\n"
      "s_and_b32 s32, s32, -16\n"
      "s_and_b32 s95, s24, 1\n"
      "s_lshl_b32 s95, s95, 10\n"
      "s_cmp_eq_u32 s95, 0\n"
      "s_cselect_b32 s90, -1, 0\n"
      "s_cselect_b32 s91, 0xffff, 0\n"
      "s_waitcnt lgkmcnt(0)\n"
      "s_pack_ll_b32_b16 s46, s64, s72\n"
      "s_waitcnt vmcnt(2)\n"
      "v_dot2_f32_bf16 v14, s46, v17, 0\n"
      "s_pack_hh_b32_b16 s47, s64, s72\n"
      "v_dot2_f32_bf16 v16, s47, v17, 0\n"
      "s_pack_ll_b32_b16 s8, s65, s73\n"
      "v_dot2_f32_bf16 v19, s8, v17, 0\n"
      "s_pack_hh_b32_b16 s9, s65, s73\n"
      "v_dot2_f32_bf16 v20, s9, v17, 0\n"
      "s_pack_ll_b32_b16 s46, s66, s74\n"
      "v_dot2_f32_bf16 v21, s46, v17, 0\n"
      "s_pack_hh_b32_b16 s47, s66, s74\n"
      "v_dot2_f32_bf16 v22, s47, v17, 0\n"
      "s_pack_ll_b32_b16 s8, s67, s75\n"
      "v_dot2_f32_bf16 v23, s8, v17, 0\n"
      "s_pack_hh_b32_b16 s9, s67, s75\n"
      "v_dot2_f32_bf16 v24, s9, v17, 0\n"
      "s_pack_ll_b32_b16 s46, s68, s76\n"
      "v_dot2_f32_bf16 v25, s46, v17, 0\n"
      "s_pack_hh_b32_b16 s47, s68, s76\n"
      "v_dot2_f32_bf16 v26, s47, v17, 0\n"
      "s_pack_ll_b32_b16 s8, s69, s77\n"
      "v_dot2_f32_bf16 v27, s8, v17, 0\n"
      "s_pack_hh_b32_b16 s9, s69, s77\n"
      "v_dot2_f32_bf16 v28, s9, v17, 0\n"
      "s_pack_ll_b32_b16 s46, s70, s78\n"
      "v_dot2_f32_bf16 v29, s46, v17, 0\n"
      "s_pack_hh_b32_b16 s47, s70, s78\n"
      "v_dot2_f32_bf16 v30, s47, v17, 0\n"
      "s_pack_ll_b32_b16 s8, s71, s79\n"
      "v_dot2_f32_bf16 v31, s8, v17, 0\n"
      "s_pack_hh_b32_b16 s9, s71, s79\n"
      "v_dot2_f32_bf16 v32, s9, v17, 0\n"
      "s_load_dwordx16 s[64:79], s[84:85], 64\n"
      "v_max_f32 v14, 0, v14\n"
      "v_max_f32 v16, 0, v16\n"
      "v_cvt_pk_bf16_f32 v14, v14, v16\n"
      "v_max_f32 v19, 0, v19\n"
      "v_max_f32 v20, 0, v20\n"
      "v_cvt_pk_bf16_f32 v19, v19, v20\n"
      "v_max_f32 v21, 0, v21\n"
      "v_max_f32 v22, 0, v22\n"
      "v_cvt_pk_bf16_f32 v21, v21, v22\n"
      "v_max_f32 v23, 0, v23\n"
      "v_max_f32 v24, 0, v24\n"
      "v_cvt_pk_bf16_f32 v23, v23, v24\n"
      "v_max_f32 v25, 0, v25\n"
      "v_max_f32 v26, 0, v26\n"
      "v_cvt_pk_bf16_f32 v25, v25, v26\n"
      "v_max_f32 v27, 0, v27\n"
      "v_max_f32 v28, 0, v28\n"
      "v_cvt_pk_bf16_f32 v27, v27, v28\n"
      "v_max_f32 v29, 0, v29\n"
      "v_max_f32 v30, 0, v30\n"
      "v_cvt_pk_bf16_f32 v29, v29, v30\n"
      "v_max_f32 v31, 0, v31\n"
      "v_max_f32 v32, 0, v32\n"
      "v_cvt_pk_bf16_f32 v31, v31, v32\n"
      "s_pack_ll_b32_b16 s46, s48, s50\n"
      "v_dot2_f32_bf16 v26, s46, v14, 0\n"
      "s_pack_hh_b32_b16 s47, s48, s50\n"
      "v_dot2_f32_bf16 v22, s47, v14, 0\n"
      "s_pack_ll_b32_b16 s8, s49, s51\n"
      "v_dot2_f32_bf16 v24, s8, v14, 0\n"
      "s_pack_hh_b32_b16 s9, s49, s51\n"
      "v_dot2_f32_bf16 v16, s9, v14, 0\n"
      "s_pack_ll_b32_b16 s46, s52, s54\n"
      "v_dot2_f32_bf16 v26, s46, v19, v26\n"
      "s_pack_hh_b32_b16 s47, s52, s54\n"
      "v_dot2_f32_bf16 v22, s47, v19, v22\n"
      "s_pack_ll_b32_b16 s8, s53, s55\n"
      "v_dot2_f32_bf16 v24, s8, v19, v24\n"
      "s_pack_hh_b32_b16 s9, s53, s55\n"
      "v_dot2_f32_bf16 v16, s9, v19, v16\n"
      "s_pack_ll_b32_b16 s46, s56, s58\n"
      "v_dot2_f32_bf16 v26, s46, v21, v26\n"
      "s_pack_hh_b32_b16 s47, s56, s58\n"
      "v_dot2_f32_bf16 v22, s47, v21, v22\n"
      "s_pack_ll_b32_b16 s8, s57, s59\n"
      "v_dot2_f32_bf16 v24, s8, v21, v24\n"
      "s_pack_hh_b32_b16 s9, s57, s59\n"
      "v_dot2_f32_bf16 v16, s9, v21, v16\n"
      "s_pack_ll_b32_b16 s46, s60, s62\n"
      "v_dot2_f32_bf16 v26, s46, v23, v26\n"
      "s_pack_hh_b32_b16 s47, s60, s62\n"
      "v_dot2_f32_bf16 v22, s47, v23, v22\n"
      "s_pack_ll_b32_b16 s8, s61, s63\n"
      "v_dot2_f32_bf16 v24, s8, v23, v24\n"
      "s_pack_hh_b32_b16 s9, s61, s63\n"
      "v_dot2_f32_bf16 v16, s9, v23, v16\n"
      "s_waitcnt lgkmcnt(0)\n"
      "s_pack_ll_b32_b16 s46, s64, s66\n"
      "v_dot2_f32_bf16 v26, s46, v25, v26\n"
      "s_pack_hh_b32_b16 s47, s64, s66\n"
      "v_dot2_f32_bf16 v22, s47, v25, v22\n"
      "s_pack_ll_b32_b16 s8, s65, s67\n"
      "v_dot2_f32_bf16 v24, s8, v25, v24\n"
      "s_pack_hh_b32_b16 s9, s65, s67\n"
      "v_dot2_f32_bf16 v16, s9, v25, v16\n"
      "s_pack_ll_b32_b16 s46, s68, s70\n"
      "v_dot2_f32_bf16 v26, s46, v27, v26\n"
      "s_pack_hh_b32_b16 s47, s68, s70\n"
      "v_dot2_f32_bf16 v22, s47, v27, v22\n"
      "s_pack_ll_b32_b16 s8, s69, s71\n"
      "v_dot2_f32_bf16 v24, s8, v27, v24\n"
      "s_pack_hh_b32_b16 s9, s69, s71\n"
      "v_dot2_f32_bf16 v16, s9, v27, v16\n"
      "s_pack_ll_b32_b16 s46, s72, s74\n"
      "v_dot2_f32_bf16 v26, s46, v29, v26\n"
      "s_pack_hh_b32_b16 s47, s72, s74\n"
      "v_dot2_f32_bf16 v22, s47, v29, v22\n"
      "s_pack_ll_b32_b16 s8, s73, s75\n"
      "v_dot2_f32_bf16 v24, s8, v29, v24\n"
      "s_pack_hh_b32_b16 s9, s73, s75\n"
      "v_dot2_f32_bf16 v16, s9, v29, v16\n"
      "s_pack_ll_b32_b16 s46, s76, s78\n"
      "v_dot2_f32_bf16 v26, s46, v31, v26\n"
      "s_pack_hh_b32_b16 s47, s76, s78\n"
      "v_dot2_f32_bf16 v22, s47, v31, v22\n"
      "s_pack_ll_b32_b16 s8, s77, s79\n"
      "v_dot2_f32_bf16 v24, s8, v31, v24\n"
      "s_pack_hh_b32_b16 s9, s77, s79\n"
      "v_dot2_f32_bf16 v16, s9, v31, v16\n"
      "v_cvt_pk_bf16_f32 v26, v26, v26\n"
      "v_lshlrev_b32 v26, 16, v26\n"
      "v_mul_f32 v26, 0xbfb8aa3b, v26\n"
      "v_exp_f32 v26, v26\n"
      "s_nop 0\n"
      "v_add_f32 v26, 1.0, v26\n"
      "v_rcp_f32 v26, v26\n"
      "s_nop 0\n"
      "v_cvt_pk_bf16_f32 v26, v26, v26\n"
      "v_lshlrev_b32_e64 v26, 16, v26\n"
      "v_mul_f32 v26, 0x41800000, v26\n"
      "v_cvt_pk_bf16_f32 v22, v22, v22\n"
      "v_lshlrev_b32 v22, 16, v22\n"
      "v_mul_f32 v22, 0xbfb8aa3b, v22\n"
      "v_exp_f32 v22, v22\n"
      "s_nop 0\n"
      "v_add_f32 v22, 1.0, v22\n"
      "v_rcp_f32 v22, v22\n"
      "s_nop 0\n"
      "v_cvt_pk_bf16_f32 v22, v22, v22\n"
      "v_lshlrev_b32_e64 v22, 16, v22\n"
      "v_mul_f32 v22, 0x41800000, v22\n"
      "v_cvt_pk_bf16_f32 v24, v24, v24\n"
      "v_lshlrev_b32 v24, 16, v24\n"
      "v_mul_f32 v24, 0xbfb8aa3b, v24\n"
      "v_exp_f32 v24, v24\n"
      "s_nop 0\n"
      "v_add_f32 v24, 1.0, v24\n"
      "v_rcp_f32 v24, v24\n"
      "s_nop 0\n"
      "v_cvt_pk_bf16_f32 v24, v24, v24\n"
      "v_lshlrev_b32_e64 v24, 16, v24\n"
      "v_mul_f32 v24, 0x41800000, v24\n"
      "v_cvt_pk_bf16_f32 v16, v16, v16\n"
      "v_lshlrev_b32 v16, 16, v16\n"
      "v_mul_f32 v16, 0xbfb8aa3b, v16\n"
      "v_exp_f32 v16, v16\n"
      "s_nop 0\n"
      "v_add_f32 v16, 1.0, v16\n"
      "v_rcp_f32 v16, v16\n"
      "s_nop 0\n"
      "v_cvt_pk_bf16_f32 v16, v16, v16\n"
      "v_lshlrev_b32_e64 v16, 16, v16\n"
      "v_mul_f32 v16, 0x41800000, v16\n"
      "v_cvt_pk_bf16_f32 v20, v26, v22\n"
      "v_cvt_pk_bf16_f32 v21, v24, v16\n"
      "ds_write_b64 v18, v[20:21]\n"
      "s_waitcnt vmcnt(0)\n"
      "s_waitcnt lgkmcnt(0)\n"
      "s_barrier\n"
      "s_setprio 3\n"
      "s_mov_b32 s8, 40960\n"
      "v_add_u32 v18, s40, v0\n"
      "s_or_b32 s14, s41, s42\n"
      "s_cbranch_scc1 L_edge_52_%=\n"
      "L_eb_53_%=:\n"
      "s_add_u32 m0, s33, 0\n"
      "s_nop 0\n"
      "global_load_lds_dwordx4 v18, s[16:17]\n"
      "s_add_u32 m0, s33, 3968\n"
      "s_nop 0\n"
      "global_load_lds_dwordx4 v18, s[16:17] offset:128\n"
      "s_add_u32 m0, s33, 7936\n"
      "s_nop 0\n"
      "global_load_lds_dwordx4 v18, s[16:17] offset:256\n"
      "v_add_u32_e64 v18, s43, v15\n"
      "s_or_b32 s14, s44, s45\n"
      "s_cbranch_scc1 L_edge_54_%=\n"
      "L_eb_55_%=:\n"
      "s_add_u32 m0, s33, 12288\n"
      "s_nop 0\n"
      "global_load_lds_dwordx4 v18, s[16:17]\n"
      "s_add_u32 m0, s33, 16256\n"
      "s_nop 0\n"
      "global_load_lds_dwordx4 v18, s[16:17] offset:128\n"
      "s_add_u32 m0, s33, 20224\n"
      "s_nop 0\n"
      "global_load_lds_dwordx4 v18, s[16:17] offset:256\n"
      "s_mul_i32 s31, s10, 4802\n"
      "s_add_u32 s76, s18, s31\n"
      "s_addc_u32 s77, s19, 0\n"
      "s_and_b32 s76, s76, -16\n"
      "s_sub_u32 s78, s32, s76\n"
      "s_add_u32 s31, s95, -36864\n"
      "v_add_u32 v18, s31, v2\n"
      "v_min_u32 v18, s78, v18\n"
      "s_add_u32 m0, s8, 82944\n"
      "s_add_u32 m0, m0, s95\n"
      "s_nop 0\n"
      "global_load_lds_dwordx4 v18, s[76:77]\n"
      "s_add_u32 s31, s95, -34816\n"
      "v_add_u32_e64 v18, s31, v2\n"
      "v_min_u32_e64 v18, s78, v18\n"
      "s_add_u32 m0, s8, 84992\n"
      "s_add_u32 m0, m0, s95\n"
      "s_nop 0\n"
      "global_load_lds_dwordx4 v18, s[76:77]\n"
      "s_add_u32 s31, s95, -32768\n"
      "v_add_u32_e64 v18, s31, v2\n"
      "v_min_u32_e64 v18, s78, v18\n"
      "s_add_u32 m0, s8, 87040\n"
      "s_add_u32 m0, m0, s95\n"
      "s_mov_b64 exec, s[90:91]\n"
      "global_load_lds_dwordx4 v18, s[76:77]\n"
      "s_mov_b64 exec, -1\n"
      "s_sub_u32 s31, s24, 4\n"
      "s_lshl_b32 s29, s31, 12\n"
      "s_add_u32 s34, s92, s29\n"
      "s_addc_u32 s35, s93, 0\n"
      "s_add_u32 m0, s29, 147712\n"
      "s_nop 0\n"
      "global_load_lds_dwordx4 v12, s[34:35]\n"
      "global_load_lds_dwordx4 v12, s[34:35] offset:1024\n"
      "global_load_lds_dwordx4 v12, s[34:35] offset:2048\n"
      "s_cmp_eq_u32 s24, 7\n"
      "s_cbranch_scc1 L_ct3_56_%=\n"
      "global_load_lds_dwordx4 v12, s[34:35] offset:3072\n"
      "L_ct3_56_%=:\n"
      "s_mov_b32 s12, 0x10001\n"
      "s_mov_b32 s13, 0x10001\n"
      "s_mov_b32 s70, 128\n"
      "s_cmp_eq_u32 s24, 7\n"
      "s_cselect_b32 s70, 256, s70\n"
      "s_mov_b32 s71, 4096\n"
      "s_cmp_eq_u32 s24, 7\n"
      "s_cselect_b32 s71, 8192, s71\n"
      "s_mov_b32 s72, 0\n"
      "s_cmp_eq_u32 s24, 7\n"
      "s_cselect_b32 s72, -2816, s72\n"
      "s_add_u32 s73, s71, 36864\n"
      "s_mov_b32 s74, 0x3ebcbe62\n"
      "s_mov_b32 s63, 3072\n"
      "s_cmp_eq_u32 s24, 7\n"
      "s_cselect_b32 s63, 7168, s63\n"
      "s_mov_b32 s92, -160\n"
      "s_cmp_eq_u32 s24, 7\n"
      "s_cselect_b32 s92, -96, s92\n"
      "s_mov_b32 s62, -1024\n"
      "s_cmp_eq_u32 s24, 7\n"
      "s_cselect_b32 s62, 2048, s62\n"
      "s_mov_b32 s15, 0x7fff7fff\n"
      "s_mov_b32 s55, -7680\n"
      "s_cmp_eq_u32 s24, 7\n"
      "s_cselect_b32 s55, -9472, s55\n"
      "v_mov_b32_e64 v8, 0\n"
      "s_mov_b32 exec_lo, 0x11111111\n"
      "s_mov_b32 exec_hi, 0x11111111\n"
      "v_mov_b32 v8, 0x3f803f80\n"
      "s_mov_b64 exec, -1\n"
      "v_mov_b32 v9, v8\n"
      "v_mov_b32 v10, v8\n"
      "v_mov_b32 v11, v8\n"
      "s_mov_b32 s30, 157440\n"
      "s_cmp_eq_u32 s24, 7\n"
      "s_cselect_b32 s30, 160256, s30\n"
      "v_add_u32 v1, s30, v13\n"
      "s_mov_b32 s30, 80000\n"
      "s_cmp_eq_u32 s24, 7\n"
      "s_cselect_b32 s30, 86272, s30\n"
      "v_add_u32 v3, s30, v12\n"
      "v_bfe_u32 v31, v12, 4, 4\n"
      "v_mul_u32_u24 v31, 1152, v31\n"
      "v_lshrrev_b32 v18, 8, v12\n"
      "v_lshl_add_u32 v31, v18, 3, v31\n"
      "s_mov_b32 s30, 1024\n"
      "s_cmp_eq_u32 s24, 7\n"
      "s_cselect_b32 s30, 1088, s30\n"
      "v_add_u32 v31, s30, v31\n"
      "v_bfe_u32 v15, v12, 5, 3\n"
      "v_xor_b32 v15, v15, v18\n"
      "v_and_b32 v6, 0xf0, v12\n"
      "v_lshlrev_b32 v4, 3, v6\n"
      "v_lshl_add_u32 v4, v15, 4, v4\n"
      "v_xor_b32 v5, 64, v4\n"
      "v_lshl_add_u32 v6, v18, 9, v6\n"
      "v_add_u32 v6, 36864, v6\n"
      "s_mul_i32 s94, s25, 12288\n"
      "s_add_u32 s94, s94, 12582912\n"
      "v_lshrrev_b32 v15, 6, v12\n"
      "v_mul_u32_u24 v15, 1152, v15\n"
      "v_and_b32 v18, 48, v12\n"
      "v_add_u32 v15, v15, v18\n"
      "s_mov_b32 s30, 896\n"
      "s_cmp_eq_u32 s24, 7\n"
      "s_cselect_b32 s30, 960, s30\n"
      "v_add_u32 v15, s30, v15\n"
      "v_lshrrev_b32 v18, 6, v12\n"
      "v_mul_u32_u24 v18, 384, v18\n"
      "v_and_b32 v16, 48, v12\n"
      "v_add_u32 v18, v18, v16\n"
      "s_mov_b32 s30, 128\n"
      "s_cmp_eq_u32 s24, 7\n"
      "s_cselect_b32 s30, 256, s30\n"
      "v_add_u32 v18, s30, v18\n"
      "s_mov_b32 s30, 89344\n"
      "s_cmp_eq_u32 s24, 7\n"
      "s_cselect_b32 s30, 95616, s30\n"
      "v_add_u32 v16, s30, v12\n"
      "v_and_b32 v20, 0x300, v12\n"
      "v_and_b32 v21, 0xc0, v12\n"
      "v_lshl_or_b32 v20, v20, 1, v21\n"
      "v_bfe_u32 v21, v12, 4, 2\n"
      "v_lshl_or_b32 v20, v21, 3, v20\n"
      "v_add_u32 v20, s30, v20\n"
      "v_mov_b32 v29, 147200\n"
      "s_add_u32 s34, s4, 0\n"
      "s_addc_u32 s35, s5, 0\n"
      "global_load_dwordx4 a[0:3], v15, s[34:35]\n"
      "s_add_u32 s34, s4, 18432\n"
      "s_addc_u32 s35, s5, 0\n"
      "global_load_dwordx4 a[4:7], v15, s[34:35]\n"
      "s_add_u32 s34, s4, 36864\n"
      "s_addc_u32 s35, s5, 0\n"
      "global_load_dwordx4 a[8:11], v15, s[34:35]\n"
      "s_add_u32 s34, s4, 55296\n"
      "s_addc_u32 s35, s5, 0\n"
      "global_load_dwordx4 a[12:15], v15, s[34:35]\n"
      "s_add_u32 s34, s4, 73728\n"
      "s_addc_u32 s35, s5, 0\n"
      "global_load_dwordx4 a[16:19], v15, s[34:35]\n"
      "s_add_u32 s34, s4, 92160\n"
      "s_addc_u32 s35, s5, 0\n"
      "global_load_dwordx4 a[20:23], v15, s[34:35]\n"
      "s_add_u32 s34, s4, 110592\n"
      "s_addc_u32 s35, s5, 0\n"
      "global_load_dwordx4 a[24:27], v15, s[34:35]\n"
      "s_waitcnt vmcnt(7)\n"
      "s_and_b32 s29, s24, 3\n"
      "s_lshl_b32 s29, s29, 2\n"
      "v_add_u32 v21, s29, v29\n"
      "v_mov_b32 v22, 1\n"
      "ds_write_b32 v21, v22\n"
      "s_add_u32 s34, s4, 129024\n"
      "s_addc_u32 s35, s5, 0\n"
      "global_load_dwordx4 a[28:31], v15, s[34:35]\n"
      "s_add_u32 s34, s4, 147456\n"
      "s_addc_u32 s35, s5, 0\n"
      "global_load_dwordx4 a[32:35], v15, s[34:35]\n"
      "s_add_u32 s34, s4, 165888\n"
      "s_addc_u32 s35, s5, 0\n"
      "global_load_dwordx4 a[36:39], v15, s[34:35]\n"
      "s_add_u32 s34, s4, 184320\n"
      "s_addc_u32 s35, s5, 0\n"
      "global_load_dwordx4 a[40:43], v15, s[34:35]\n"
      "s_add_u32 s34, s4, 202752\n"
      "s_addc_u32 s35, s5, 0\n"
      "global_load_dwordx4 a[44:47], v15, s[34:35]\n"
      "s_waitcnt lgkmcnt(0)\n"
      "L_poll_57_%=:\n"
      "ds_read_b128 v[24:27], v29 offset:16\n"
      "s_waitcnt lgkmcnt(0)\n"
      "v_and_b32 v24, v24, v25\n"
      "v_and_b32 v26, v26, v27\n"
      "v_and_b32 v24, v24, v26\n"
      "s_nop 0\n"
      "v_readfirstlane_b32 s29, v24\n"
      "s_nop 3\n"
      "s_cmp_eq_u32 s29, 0\n"
      "s_cbranch_scc1 L_poll_57_%=\n"
      "s_mov_b32 s48, 145664\n"
      "s_cmp_eq_u32 s24, 7\n"
      "s_cbranch_scc1 L_b48_58_%=\n"
      "s_add_u32 s34, s6, 0\n"
      "s_addc_u32 s35, s7, 0\n"
      "global_load_dwordx4 a[48:51], v18, s[34:35]\n"
      "global_load_dwordx4 a[96:99], v18, s[34:35] offset:64\n"
      "v_lshrrev_b32 v15, 8, v12\n"
      "v_mul_u32_u24 v15, 784, v15\n"
      "v_bfe_u32 v24, v12, 2, 6\n"
      "v_add_u32 v15, v15, v24\n"
      "s_lshr_b32 s49, s33, 10\n"
      "s_mul_i32 s49, s49, 3136\n"
      "s_add_u32 s49, s49, 24576\n"
      "v_add_u32 v15, s49, v15\n"
      "s_lshl_b32 s49, s33, 1\n"
      "s_add_u32 s49, s49, 98816\n"
      "v_lshrrev_b32 v24, 1, v12\n"
      "v_add_u32 v24, s49, v24\n"
      "v_mov_b32 v12, 0xf149f149\n"
      "s_mov_b32 s46, 0x05040100\n"
      "s_mov_b32 s47, 0x07060302\n"
      "ds_read_b32 v26, v15\n"
      "ds_read_b32 v27, v15 offset:196\n"
      "ds_read_b32 v25, v15 offset:392\n"
      "ds_read_b32 v21, v15 offset:588\n"
      "ds_read_b32 v22, v15 offset:64\n"
      "s_add_u32 s34, s6, 6144\n"
      "s_addc_u32 s35, s7, 0\n"
      "global_load_dwordx4 a[52:55], v18, s[34:35]\n"
      "global_load_dwordx4 a[100:103], v18, s[34:35] offset:64\n"
      "ds_read_b32 v23, v15 offset:260\n"
      "ds_read_b32 v19, v15 offset:456\n"
      "ds_read_b32 v14, v15 offset:652\n"
      "ds_read_b32 v17, v15 offset:128\n"
      "ds_read_b32 v30, v15 offset:324\n"
      "ds_read_b32 v32, v15 offset:520\n"
      "ds_read_b32 v28, v15 offset:716\n"
      "ds_read_b32 v33, v15 offset:192\n"
      "ds_read_b32 v34, v15 offset:388\n"
      "ds_read_b32 v35, v15 offset:584\n"
      "ds_read_b32 v7, v15 offset:780\n"
      "s_waitcnt lgkmcnt(11)\n"
      "v_lshl_add_u32 v26, v26, 3, s48\n"
      "ds_read_b64 v[36:37], v26\n"
      "v_lshl_add_u32 v27, v27, 3, s48\n"
      "ds_read_b64 v[38:39], v27\n"
      "v_lshl_add_u32 v25, v25, 3, s48\n"
      "ds_read_b64 v[26:27], v25\n"
      "v_lshl_add_u32 v21, v21, 3, s48\n"
      "ds_read_b64 v[40:41], v21\n"
      "v_lshl_add_u32 v22, v22, 3, s48\n"
      "s_add_u32 s34, s6, 12288\n"
      "s_addc_u32 s35, s7, 0\n"
      "global_load_dwordx4 a[56:59], v18, s[34:35]\n"
      "global_load_dwordx4 a[104:107], v18, s[34:35] offset:64\n"
      "ds_read_b64 v[42:43], v22\n"
      "s_waitcnt lgkmcnt(6)\n"
      "v_lshl_add_u32 v23, v23, 3, s48\n"
      "ds_read_b64 v[44:45], v23\n"
      "v_lshl_add_u32 v19, v19, 3, s48\n"
      "ds_read_b64 v[22:23], v19\n"
      "v_lshl_add_u32 v14, v14, 3, s48\n"
      "ds_read_b64 v[46:47], v14\n"
      "v_lshl_add_u32 v17, v17, 3, s48\n"
      "ds_read_b64 v[14:15], v17\n"
      "v_lshl_add_u32 v30, v30, 3, s48\n"
      "ds_read_b64 v[48:49], v30\n"
      "v_lshl_add_u32 v32, v32, 3, s48\n"
      "ds_read_b64 v[50:51], v32\n"
      "v_lshl_add_u32 v28, v28, 3, s48\n"
      "ds_read_b64 v[52:53], v28\n"
      "v_lshl_add_u32 v33, v33, 3, s48\n"
      "ds_read_b64 v[54:55], v33\n"
      "v_lshl_add_u32 v34, v34, 3, s48\n"
      "ds_read_b64 v[32:33], v34\n"
      "v_lshl_add_u32 v35, v35, 3, s48\n"
      "s_add_u32 s34, s6, 18432\n"
      "s_addc_u32 s35, s7, 0\n"
      "global_load_dwordx4 a[60:63], v18, s[34:35]\n"
      "global_load_dwordx4 a[108:111], v18, s[34:35] offset:64\n"
      "ds_read_b64 v[56:57], v35\n"
      "s_waitcnt lgkmcnt(6)\n"
      "v_lshl_add_u32 v7, v7, 3, s48\n"
      "ds_read_b64 v[34:35], v7\n"
      "v_perm_b32 v58, v38, v36, s46\n"
      "v_perm_b32 v59, v40, v26, s46\n"
      "ds_write_b64 v24, v[58:59]\n"
      "v_perm_b32 v58, v38, v36, s47\n"
      "v_perm_b32 v59, v40, v26, s47\n"
      "ds_write_b64 v24, v[58:59] offset:6144\n"
      "v_perm_b32 v58, v39, v37, s46\n"
      "v_perm_b32 v59, v41, v27, s46\n"
      "ds_write_b64 v24, v[58:59] offset:12288\n"
      "v_perm_b32 v58, v39, v37, s47\n"
      "v_perm_b32 v59, v41, v27, s47\n"
      "ds_write_b64 v24, v[58:59] offset:18432\n"
      "v_perm_b32 v26, v44, v42, s46\n"
      "v_perm_b32 v27, v46, v22, s46\n"
      "ds_write_b64 v24, v[26:27] offset:512\n"
      "v_perm_b32 v26, v44, v42, s47\n"
      "v_perm_b32 v27, v46, v22, s47\n"
      "s_add_u32 s34, s6, 24576\n"
      "s_addc_u32 s35, s7, 0\n"
      "global_load_dwordx4 a[64:67], v18, s[34:35]\n"
      "global_load_dwordx4 a[112:115], v18, s[34:35] offset:64\n"
      "ds_write_b64 v24, v[26:27] offset:6656\n"
      "v_perm_b32 v26, v45, v43, s46\n"
      "v_perm_b32 v27, v47, v23, s46\n"
      "ds_write_b64 v24, v[26:27] offset:12800\n"
      "v_perm_b32 v26, v45, v43, s47\n"
      "v_perm_b32 v27, v47, v23, s47\n"
      "ds_write_b64 v24, v[26:27] offset:18944\n"
      "s_waitcnt lgkmcnt(4)\n"
      "v_perm_b32 v26, v48, v14, s46\n"
      "v_perm_b32 v27, v52, v50, s46\n"
      "ds_write_b64 v24, v[26:27] offset:1024\n"
      "v_perm_b32 v26, v48, v14, s47\n"
      "v_perm_b32 v27, v52, v50, s47\n"
      "ds_write_b64 v24, v[26:27] offset:7168\n"
      "v_perm_b32 v26, v49, v15, s46\n"
      "v_perm_b32 v27, v53, v51, s46\n"
      "ds_write_b64 v24, v[26:27] offset:13312\n"
      "v_perm_b32 v14, v49, v15, s47\n"
      "v_perm_b32 v15, v53, v51, s47\n"
      "ds_write_b64 v24, v[14:15] offset:19456\n"
      "v_perm_b32 v14, v32, v54, s46\n"
      "s_add_u32 s34, s6, 30720\n"
      "s_addc_u32 s35, s7, 0\n"
      "global_load_dwordx4 a[68:71], v18, s[34:35]\n"
      "global_load_dwordx4 a[116:119], v18, s[34:35] offset:64\n"
      "v_cndmask_b32 v14, v12, v14, s[12:13]\n"
      "v_perm_b32 v15, v34, v56, s46\n"
      "v_cndmask_b32 v15, v12, v15, s[12:13]\n"
      "ds_write_b64 v24, v[14:15] offset:1536\n"
      "v_perm_b32 v14, v32, v54, s47\n"
      "v_cndmask_b32 v14, v12, v14, s[12:13]\n"
      "v_perm_b32 v15, v34, v56, s47\n"
      "v_cndmask_b32 v15, v12, v15, s[12:13]\n"
      "ds_write_b64 v24, v[14:15] offset:7680\n"
      "v_perm_b32 v14, v33, v55, s46\n"
      "v_cndmask_b32 v14, v12, v14, s[12:13]\n"
      "v_perm_b32 v15, v35, v57, s46\n"
      "v_cndmask_b32 v15, v12, v15, s[12:13]\n"
      "ds_write_b64 v24, v[14:15] offset:13824\n"
      "v_perm_b32 v14, v33, v55, s47\n"
      "v_cndmask_b32 v14, v12, v14, s[12:13]\n"
      "v_perm_b32 v15, v35, v57, s47\n"
      "v_cndmask_b32 v15, v12, v15, s[12:13]\n"
      "ds_write_b64 v24, v[14:15] offset:19968\n"
      "s_waitcnt lgkmcnt(0)\n"
      "L_poll_60_%=:\n"
      "ds_read_b128 v[24:27], v29\n"
      "s_waitcnt lgkmcnt(0)\n"
      "v_and_b32 v24, v24, v25\n"
      "v_and_b32 v26, v26, v27\n"
      "v_and_b32_e64 v24, v24, v26\n"
      "s_nop 0\n"
      "v_readfirstlane_b32 s29, v24\n"
      "s_nop 3\n"
      "s_cmp_eq_u32 s29, 0\n"
      "s_cbranch_scc1 L_poll_60_%=\n"
      "s_branch L_bj_59_%=\n"
      "L_b48_58_%=:\n"
      "s_nop 7\n"
      "s_nop 7\n"
      "s_add_u32 s34, s6, 0\n"
      "s_addc_u32 s35, s7, 0\n"
      "global_load_dwordx4 a[48:51], v18, s[34:35]\n"
      "global_load_dwordx4 a[96:99], v18, s[34:35] offset:64\n"
      "s_add_u32 s34, s6, 6144\n"
      "s_addc_u32 s35, s7, 0\n"
      "global_load_dwordx4 a[52:55], v18, s[34:35]\n"
      "global_load_dwordx4 a[100:103], v18, s[34:35] offset:64\n"
      "s_add_u32 s34, s6, 12288\n"
      "s_addc_u32 s35, s7, 0\n"
      "global_load_dwordx4 a[56:59], v18, s[34:35]\n"
      "global_load_dwordx4 a[104:107], v18, s[34:35] offset:64\n"
      "s_add_u32 s34, s6, 18432\n"
      "s_addc_u32 s35, s7, 0\n"
      "global_load_dwordx4 a[60:63], v18, s[34:35]\n"
      "global_load_dwordx4 a[108:111], v18, s[34:35] offset:64\n"
      "s_add_u32 s34, s6, 24576\n"
      "s_addc_u32 s35, s7, 0\n"
      "global_load_dwordx4 a[64:67], v18, s[34:35]\n"
      "global_load_dwordx4 a[112:115], v18, s[34:35] offset:64\n"
      "s_add_u32 s34, s6, 30720\n"
      "s_addc_u32 s35, s7, 0\n"
      "global_load_dwordx4 a[68:71], v18, s[34:35]\n"
      "global_load_dwordx4 a[116:119], v18, s[34:35] offset:64\n"
      "v_min_u32 v14, 188, v13\n"
      "v_add_u32 v14, 33984, v14\n"
      "ds_read_b32 v14, v14\n"
      "v_mov_b32 v15, 34176\n"
      "ds_read_b32 v15, v15 offset:12\n"
      "s_mov_b32 s46, 0x0\n"
      "s_mov_b32 s47, 0x10000\n"
      "s_waitcnt lgkmcnt(0)\n"
      "v_cndmask_b32 v14, v14, v15, s[46:47]\n"
      "v_lshl_add_u32 v14, v14, 3, s48\n"
      "ds_read_b64 v[24:25], v14\n"
      "v_mov_b32 v12, 0xf149f2ca\n"
      "s_mov_b32 s46, 0xffffffff\n"
      "s_mov_b32 s47, 0x1ffff\n"
      "s_waitcnt lgkmcnt(0)\n"
      "v_lshlrev_b32_e64 v15, 16, v24\n"
      "v_mul_f32 v15, 0x3fb8aa3b, v15\n"
      "v_cndmask_b32 v15, v12, v15, s[46:47]\n"
      "v_and_b32 v26, 0xffff0000, v24\n"
      "v_mul_f32 v26, 0x3fb8aa3b, v26\n"
      "v_cndmask_b32 v26, v12, v26, s[46:47]\n"
      "v_lshlrev_b32_e64 v24, 16, v25\n"
      "v_mul_f32 v24, 0x3fb8aa3b, v24\n"
      "v_cndmask_b32 v24, v12, v24, s[46:47]\n"
      "v_and_b32 v27, 0xffff0000, v25\n"
      "v_mul_f32 v27, 0x3fb8aa3b, v27\n"
      "v_cndmask_b32 v27, v12, v27, s[46:47]\n"
      "v_add_u32 v14, 147712, v13\n"
      "s_waitcnt lgkmcnt(0)\n"
      "L_poll_61_%=:\n"
      "ds_read_b128 v[32:35], v29\n"
      "s_waitcnt lgkmcnt(0)\n"
      "v_and_b32 v32, v32, v33\n"
      "v_and_b32 v34, v34, v35\n"
      "v_and_b32_e64 v32, v32, v34\n"
      "s_nop 0\n"
      "v_readfirstlane_b32 s29, v32\n"
      "s_nop 3\n"
      "s_cmp_eq_u32 s29, 0\n"
      "s_cbranch_scc1 L_poll_61_%=\n"
      "ds_write_b32 v14, v15 offset:768\n"
      "ds_write_b32 v14, v26 offset:1792\n"
      "ds_write_b32 v14, v24 offset:2816\n"
      "ds_write_b32 v14, v27 offset:3840\n"
      "L_bj_59_%=:\n"
      "s_nop 7\n"
      "s_nop 7\n"
      "s_waitcnt lgkmcnt(0)\n"
      "s_waitcnt vmcnt(23)\n"
      "ds_write_b128 v16, a[0:3]\n"
      "s_waitcnt vmcnt(22)\n"
      "ds_write_b128 v16, a[4:7] offset:1024\n"
      "ds_read_b64_tr_b16 a[0:1], v20\n"
      "ds_read_b64_tr_b16 a[2:3], v20 offset:256\n"
      "ds_read_b64_tr_b16 a[4:5], v20 offset:32\n"
      "ds_read_b64_tr_b16 a[6:7], v20 offset:288\n"
      "s_add_u32 s34, s6, 36864\n"
      "s_addc_u32 s35, s7, 0\n"
      "global_load_dwordx4 a[72:75], v18, s[34:35]\n"
      "global_load_dwordx4 a[120:123], v18, s[34:35] offset:64\n"
      "s_add_u32 s34, s6, 43008\n"
      "s_addc_u32 s35, s7, 0\n"
      "global_load_dwordx4 a[76:79], v18, s[34:35]\n"
      "global_load_dwordx4 a[124:127], v18, s[34:35] offset:64\n"
      "s_waitcnt vmcnt(25)\n"
      "ds_write_b128 v16, a[8:11]\n"
      "s_waitcnt vmcnt(24)\n"
      "ds_write_b128 v16, a[12:15] offset:1024\n"
      "ds_read_b64_tr_b16 a[8:9], v20\n"
      "ds_read_b64_tr_b16 a[10:11], v20 offset:256\n"
      "ds_read_b64_tr_b16 a[12:13], v20 offset:32\n"
      "ds_read_b64_tr_b16 a[14:15], v20 offset:288\n"
      "s_add_u32 s34, s6, 49152\n"
      "s_addc_u32 s35, s7, 0\n"
      "global_load_dwordx4 a[80:83], v18, s[34:35]\n"
      "global_load_dwordx4 a[128:131], v18, s[34:35] offset:64\n"
      "s_add_u32 s34, s6, 55296\n"
      "s_addc_u32 s35, s7, 0\n"
      "global_load_dwordx4 a[84:87], v18, s[34:35]\n"
      "global_load_dwordx4 a[132:135], v18, s[34:35] offset:64\n"
      "s_waitcnt vmcnt(27)\n"
      "ds_write_b128 v16, a[16:19]\n"
      "s_waitcnt vmcnt(26)\n"
      "ds_write_b128 v16, a[20:23] offset:1024\n"
      "ds_read_b64_tr_b16 a[16:17], v20\n"
      "ds_read_b64_tr_b16 a[18:19], v20 offset:256\n"
      "ds_read_b64_tr_b16 a[20:21], v20 offset:32\n"
      "ds_read_b64_tr_b16 a[22:23], v20 offset:288\n"
      "s_add_u32 s34, s6, 61440\n"
      "s_addc_u32 s35, s7, 0\n"
      "global_load_dwordx4 a[88:91], v18, s[34:35]\n"
      "global_load_dwordx4 a[136:139], v18, s[34:35] offset:64\n"
      "s_add_u32 s34, s6, 67584\n"
      "s_addc_u32 s35, s7, 0\n"
      "global_load_dwordx4 a[92:95], v18, s[34:35]\n"
      "global_load_dwordx4 a[140:143], v18, s[34:35] offset:64\n"
      "s_waitcnt vmcnt(29)\n"
      "ds_write_b128 v16, a[24:27]\n"
      "s_waitcnt vmcnt(28)\n"
      "ds_write_b128 v16, a[28:31] offset:1024\n"
      "ds_read_b64_tr_b16 a[24:25], v20\n"
      "ds_read_b64_tr_b16 a[26:27], v20 offset:256\n"
      "ds_read_b64_tr_b16 a[28:29], v20 offset:32\n"
      "ds_read_b64_tr_b16 a[30:31], v20 offset:288\n"
      "s_waitcnt vmcnt(27)\n"
      "ds_write_b128 v16, a[32:35]\n"
      "s_waitcnt vmcnt(26)\n"
      "ds_write_b128 v16, a[36:39] offset:1024\n"
      "ds_read_b64_tr_b16 a[32:33], v20\n"
      "ds_read_b64_tr_b16 a[34:35], v20 offset:256\n"
      "ds_read_b64_tr_b16 a[36:37], v20 offset:32\n"
      "ds_read_b64_tr_b16 a[38:39], v20 offset:288\n"
      "s_waitcnt vmcnt(25)\n"
      "ds_write_b128 v16, a[40:43]\n"
      "s_waitcnt vmcnt(24)\n"
      "ds_write_b128 v16, a[44:47] offset:1024\n"
      "ds_read_b64_tr_b16 a[40:41], v20\n"
      "ds_read_b64_tr_b16 a[42:43], v20 offset:256\n"
      "ds_read_b64_tr_b16 a[44:45], v20 offset:32\n"
      "ds_read_b64_tr_b16 a[46:47], v20 offset:288\n"
      "v_mov_b32 v12, 0\n"
      "v_mov_b32 v13, 0\n"
      "v_mov_b32 v14, 0\n"
      "v_mov_b32 v15, 0\n"
      "ds_write_b128 v16, v[12:15]\n"
      "ds_write_b128 v16, v[12:15] offset:2048\n"
      "s_mov_b32 s8, 50688\n"
      "ds_read_b128 v[12:15], v4\n"
      "ds_read_b128 v[16:19], v5\n"
      "ds_read_b128 v[20:23], v4 offset:4096\n"
      "ds_read_b128 v[24:27], v5 offset:4096\n"
      "s_waitcnt lgkmcnt(0)\n"
      "s_barrier\n"
      "s_setprio 1\n"
      "v_mfma_f32_16x16x32_bf16 v[52:55], v[12:15], a[0:3], 0\n"
      "s_add_u32 s28, s28, 256\n"
      "s_lshl_b32 s10, s28, 1\n"
      "s_add_u32 s11, s10, 1\n"
      "s_add_u32 s14, s25, 5120\n"
      "s_min_u32 s14, s14, 5328\n"
      "s_cmp_lt_u32 s27, 9\n"
      "s_cselect_b32 s10, s10, s14\n"
      "s_cselect_b32 s11, s11, s14\n"
      "s_mul_hi_u32 s29, s10, 58835169\n"
      "s_mul_i32 s30, s29, 73\n"
      "s_sub_u32 s30, s10, s30\n"
      "s_mul_i32 s52, s29, 1373568\n"
      "s_mul_i32 s31, s30, 2688\n"
      "s_add_u32 s52, s52, s31\n"
      "s_add_u32 s52, s52, 589824\n"
      "s_cmp_eq_u32 s29, 72\n"
      "s_cselect_b32 s53, 100270464, 0\n"
      "s_cmp_eq_u32 s30, 72\n"
      "s_cselect_b32 s54, 196224, 0\n"
      "s_mul_hi_u32 s29, s11, 58835169\n"
      "s_mul_i32 s30, s29, 73\n"
      "s_sub_u32 s30, s11, s30\n"
      "s_mul_i32 s0, s29, 1373568\n"
      "s_mul_i32 s31, s30, 2688\n"
      "s_add_u32 s0, s0, s31\n"
      "s_add_u32 s0, s0, 589824\n"
      "s_cmp_eq_u32 s29, 72\n"
      "s_cselect_b32 s1, 100270464, 0\n"
      "s_cmp_eq_u32 s30, 72\n"
      "s_cselect_b32 s2, 196224, 0\n"
      "v_mfma_f32_16x16x32_bf16 v[52:55], v[16:19], a[8:11], v[52:55]\n"
      "v_mfma_f32_16x16x32_bf16 v[48:51], v[12:15], a[4:7], 0\n"
      "v_mfma_f32_16x16x32_bf16 v[48:51], v[16:19], a[12:15], v[48:51]\n"
      "ds_read_b128 v[12:15], v4 offset:8192\n"
      "ds_read_b128 v[16:19], v5 offset:8192\n"
      "v_mfma_f32_16x16x32_bf16 v[52:55], v[20:23], a[16:19], v[52:55]\n"
      "v_mfma_f32_16x16x32_bf16 v[52:55], v[24:27], a[24:27], v[52:55]\n"
      "v_mfma_f32_16x16x32_bf16 v[48:51], v[20:23], a[20:23], v[48:51]\n"
      "v_add_u32 v20, s43, v0\n"
      "s_or_b32 s14, s44, s45\n"
      "s_cbranch_scc1 L_edge_62_%=\n"
      "L_eb_63_%=:\n"
      "s_add_u32 m0, s33, 24576\n"
      "s_nop 0\n"
      "global_load_lds_dwordx4 v20, s[16:17]\n"
      "v_mfma_f32_16x16x32_bf16 v[48:51], v[24:27], a[28:31], v[48:51]\n"
      "ds_read_b128 v[24:27], v4 offset:2048\n"
      "ds_read_b128 v[32:35], v5 offset:2048\n"
      "s_waitcnt lgkmcnt(2)\n"
      "v_mfma_f32_16x16x32_bf16 v[52:55], v[12:15], a[32:35], v[52:55]\n"
      "s_add_u32 m0, s33, 28544\n"
      "s_nop 0\n"
      "global_load_lds_dwordx4 v20, s[16:17] offset:128\n"
      "v_mfma_f32_16x16x32_bf16 v[52:55], v[16:19], a[40:43], v[52:55]\n"
      "v_mfma_f32_16x16x32_bf16 v[48:51], v[12:15], a[36:39], v[48:51]\n"
      "s_add_u32 m0, s33, 32512\n"
      "s_nop 0\n"
      "global_load_lds_dwordx4 v20, s[16:17] offset:256\n"
      "v_mfma_f32_16x16x32_bf16 v[48:51], v[16:19], a[44:47], v[48:51]\n"
      "ds_read_b128 v[12:15], v4 offset:6144\n"
      "ds_read_b128 v[16:19], v5 offset:6144\n"
      "s_waitcnt lgkmcnt(2)\n"
      "v_mfma_f32_16x16x32_bf16 v[20:23], v[24:27], a[0:3], 0\n"
      "s_mul_i32 s31, s10, 4802\n"
      "s_add_u32 s76, s18, s31\n"
      "s_addc_u32 s77, s19, 0\n"
      "s_and_b32 s76, s76, -16\n"
      "s_sub_u32 s78, s32, s76\n"
      "s_add_u32 s31, s95, -36864\n"
      "v_add_u32_e64 v28, s31, v2\n"
      "v_min_u32_e64 v28, s78, v28\n"
      "s_add_u32 m0, s8, 82944\n"
      "s_add_u32 m0, m0, s95\n"
      "s_nop 0\n"
      "global_load_lds_dwordx4 v28, s[76:77]\n"
      "v_mfma_f32_16x16x32_bf16 v[20:23], v[32:35], a[8:11], v[20:23]\n"
      "v_mfma_f32_16x16x32_bf16 v[56:59], v[24:27], a[4:7], 0\n"
      "s_add_u32 s31, s95, -34816\n"
      "v_add_u32_e64 v24, s31, v2\n"
      "v_min_u32_e64 v24, s78, v24\n"
      "s_add_u32 m0, s8, 84992\n"
      "s_add_u32 m0, m0, s95\n"
      "s_nop 0\n"
      "global_load_lds_dwordx4 v24, s[76:77]\n"
      "v_mfma_f32_16x16x32_bf16 v[56:59], v[32:35], a[12:15], v[56:59]\n"
      "ds_read_b128 v[24:27], v4 offset:10240\n"
      "ds_read_b128 v[32:35], v5 offset:10240\n"
      "s_waitcnt lgkmcnt(2)\n"
      "v_mfma_f32_16x16x32_bf16 v[20:23], v[12:15], a[16:19], v[20:23]\n"
      "s_add_u32 s31, s95, -32768\n"
      "v_add_u32 v28, s31, v2\n"
      "v_min_u32_e64 v28, s78, v28\n"
      "s_add_u32 m0, s8, 87040\n"
      "s_add_u32 m0, m0, s95\n"
      "s_mov_b64 exec, s[90:91]\n"
      "global_load_lds_dwordx4 v28, s[76:77]\n"
      "s_mov_b64 exec, -1\n"
      "v_mfma_f32_16x16x32_bf16 v[20:23], v[16:19], a[24:27], v[20:23]\n"
      "v_mfma_f32_16x16x32_bf16 v[56:59], v[12:15], a[20:23], v[56:59]\n"
      "v_mfma_f32_16x16x32_bf16 v[56:59], v[16:19], a[28:31], v[56:59]\n"
      "ds_read_b128 v[12:15], v4 offset:36864\n"
      "ds_read_b128 v[16:19], v5 offset:36864\n"
      "s_waitcnt lgkmcnt(2)\n"
      "v_mfma_f32_16x16x32_bf16 v[20:23], v[24:27], a[32:35], v[20:23]\n"
      "v_mfma_f32_16x16x32_bf16 v[20:23], v[32:35], a[40:43], v[20:23]\n"
      "v_mfma_f32_16x16x32_bf16 v[56:59], v[24:27], a[36:39], v[56:59]\n"
      "v_mfma_f32_16x16x32_bf16 v[56:59], v[32:35], a[44:47], v[56:59]\n"
      "ds_read_b128 v[24:27], v4 offset:40960\n"
      "ds_read_b128 v[32:35], v5 offset:40960\n"
      "s_waitcnt lgkmcnt(2)\n"
      "v_mfma_f32_16x16x32_bf16 v[44:47], a[0:3], v[12:15], 0\n"
      "v_mfma_f32_16x16x32_bf16 v[44:47], a[8:11], v[16:19], v[44:47]\n"
      "v_mfma_f32_16x16x32_bf16 v[40:43], a[4:7], v[12:15], 0\n"
      "v_mfma_f32_16x16x32_bf16 v[40:43], a[12:15], v[16:19], v[40:43]\n"
      "ds_read_b128 v[12:15], v4 offset:45056\n"
      "ds_read_b128 v[16:19], v5 offset:45056\n"
      "s_waitcnt lgkmcnt(2)\n"
      "v_mfma_f32_16x16x32_bf16 v[44:47], a[16:19], v[24:27], v[44:47]\n"
      "v_cvt_pk_bf16_f32 v52, v52, v53\n"
      "v_mfma_f32_16x16x32_bf16 v[44:47], a[24:27], v[32:35], v[44:47]\n"
      "v_cvt_pk_bf16_f32 v53, v54, v55\n"
      "v_mfma_f32_16x16x32_bf16 v[40:43], a[20:23], v[24:27], v[40:43]\n"
      "v_cvt_pk_bf16_f32 v54, v20, v21\n"
      "v_mfma_f32_16x16x32_bf16 v[40:43], a[28:31], v[32:35], v[40:43]\n"
      "ds_read_b128 v[24:27], v4 offset:38912\n"
      "ds_read_b128 v[32:35], v5 offset:38912\n"
      "v_cvt_pk_bf16_f32 v55, v22, v23\n"
      "s_waitcnt lgkmcnt(2)\n"
      "v_mfma_f32_16x16x32_bf16 v[44:47], a[32:35], v[12:15], v[44:47]\n"
      "v_cvt_pk_bf16_f32 v48, v48, v49\n"
      "v_mfma_f32_16x16x32_bf16 v[44:47], a[40:43], v[16:19], v[44:47]\n"
      "v_cvt_pk_bf16_f32 v49, v50, v51\n"
      "v_mfma_f32_16x16x32_bf16 v[40:43], a[36:39], v[12:15], v[40:43]\n"
      "v_cvt_pk_bf16_f32 v50, v56, v57\n"
      "v_mfma_f32_16x16x32_bf16 v[40:43], a[44:47], v[16:19], v[40:43]\n"
      "ds_read_b128 v[12:15], v4 offset:43008\n"
      "ds_read_b128 v[16:19], v5 offset:43008\n"
      "v_cvt_pk_bf16_f32 v51, v58, v59\n"
      "s_waitcnt lgkmcnt(2)\n"
      "v_mfma_f32_16x16x32_bf16 v[20:23], a[0:3], v[24:27], 0\n"
      "v_mfma_f32_16x16x32_bf16 v[20:23], a[8:11], v[32:35], v[20:23]\n"
      "v_mfma_f32_16x16x32_bf16 v[56:59], a[4:7], v[24:27], 0\n"
      "v_mfma_f32_16x16x32_bf16 v[56:59], a[12:15], v[32:35], v[56:59]\n"
      "ds_read_b128 v[24:27], v4 offset:47104\n"
      "ds_read_b128 v[32:35], v5 offset:47104\n"
      "s_waitcnt lgkmcnt(2)\n"
      "v_mfma_f32_16x16x32_bf16 v[20:23], a[16:19], v[12:15], v[20:23]\n"
      "v_mfma_f32_16x16x32_bf16 v[20:23], a[24:27], v[16:19], v[20:23]\n"
      "v_mfma_f32_16x16x32_bf16 v[56:59], a[20:23], v[12:15], v[56:59]\n"
      "v_mfma_f32_16x16x32_bf16 v[56:59], a[28:31], v[16:19], v[56:59]\n"
      "ds_read_b128 v[36:39], v4 offset:12288\n"
      "ds_read_b128 v[60:63], v5 offset:12288\n"
      "s_waitcnt lgkmcnt(2)\n"
      "v_mfma_f32_16x16x32_bf16 v[20:23], a[32:35], v[24:27], v[20:23]\n"
      "v_mfma_f32_16x16x32_bf16 v[20:23], a[40:43], v[32:35], v[20:23]\n"
      "v_mfma_f32_16x16x32_bf16 v[56:59], a[36:39], v[24:27], v[56:59]\n"
      "v_mfma_f32_16x16x32_bf16 v[56:59], a[44:47], v[32:35], v[56:59]\n"
      "ds_read_b128 v[24:27], v4 offset:16384\n"
      "ds_read_b128 v[32:35], v5 offset:16384\n"
      "s_and_b32 s29, s24, 3\n"
      "s_lshl_b32 s29, s29, 2\n"
      "v_add_u32 v12, s29, v29\n"
      "v_mov_b32 v13, 1\n"
      "ds_write_b32 v12, v13 offset:32\n"
      "s_waitcnt lgkmcnt(3)\n"
      "v_mfma_f32_16x16x32_bf16 v[12:15], v[36:39], a[0:3], 0\n"
      "v_mfma_f32_16x16x32_bf16 v[12:15], v[60:63], a[8:11], v[12:15]\n"
      "v_mfma_f32_16x16x32_bf16 v[16:19], v[36:39], a[4:7], 0\n"
      "v_mfma_f32_16x16x32_bf16 v[16:19], v[60:63], a[12:15], v[16:19]\n"
      "ds_read_b128 v[60:63], v4 offset:20480\n"
      "ds_read_b128 v[36:39], v5 offset:20480\n"
      "s_waitcnt lgkmcnt(2)\n"
      "v_mfma_f32_16x16x32_bf16 v[12:15], v[24:27], a[16:19], v[12:15]\n"
      "s_mul_i32 s29, s25, 36864\n"
      "s_add_u32 s29, s29, 1048576\n"
      "s_add_u32 s56, s20, s29\n"
      "v_mfma_f32_16x16x32_bf16 v[12:15], v[32:35], a[24:27], v[12:15]\n"
      "s_addc_u32 s57, s21, 0\n"
      "v_add_u32 v28, 143360, v31\n"
      "v_mfma_f32_16x16x32_bf16 v[16:19], v[24:27], a[20:23], v[16:19]\n"
      "v_add_u32 v24, 18432, v31\n"
      "v_cvt_pk_bf16_f32 v44, v44, v45\n"
      "v_cvt_pk_bf16_f32 v45, v46, v47\n"
      "v_mfma_f32_16x16x32_bf16 v[16:19], v[32:35], a[28:31], v[16:19]\n"
      "v_cvt_pk_bf16_f32 v40, v40, v41\n"
      "s_waitcnt lgkmcnt(0)\n"
      "v_mfma_f32_16x16x32_bf16 v[12:15], v[60:63], a[32:35], v[12:15]\n"
      "v_cvt_pk_bf16_f32 v41, v42, v43\n"
      "v_cvt_pk_bf16_f32 v20, v20, v21\n"
      "v_mfma_f32_16x16x32_bf16 v[12:15], v[36:39], a[40:43], v[12:15]\n"
      "v_cvt_pk_bf16_f32 v21, v22, v23\n"
      "v_mfma_f32_16x16x32_bf16 v[16:19], v[60:63], a[36:39], v[16:19]\n"
      "v_cvt_pk_bf16_f32 v56, v56, v57\n"
      "v_cvt_pk_bf16_f32 v57, v58, v59\n"
      "v_mfma_f32_16x16x32_bf16 v[16:19], v[36:39], a[44:47], v[16:19]\n"
      "s_mov_b32 s34, 0xfffcfffc\n"
      "s_mov_b32 s35, 0xfffcfffc\n"
      "s_mov_b64 exec, s[34:35]\n"
      "global_store_dwordx2 v31, v[44:45], s[56:57]\n"
      "global_store_dwordx2 v31, v[40:41], s[56:57] offset:32\n"
      "s_mov_b32 s34, 0x30003\n"
      "s_mov_b32 s35, 0x30003\n"
      "s_mov_b64 exec, s[34:35]\n"
      "ds_write_b64 v28, v[44:45]\n"
      "ds_write_b64 v28, v[40:41] offset:32\n"
      "s_mov_b64 exec, -1\n"
      "s_mov_b32 s34, 0x1f001f\n"
      "s_mov_b32 s35, 0x1f001f\n"
      "s_mov_b64 exec, s[34:35]\n"
      "global_store_dwordx2 v24, v[20:21], s[56:57]\n"
      "global_store_dwordx2 v24, v[56:57], s[56:57] offset:32\n"
      "s_mov_b64 exec, -1\n"
      "ds_read_b32 v20, v1 offset:512\n"
      "ds_read_b32 v21, v1 offset:768\n"
      "v_cvt_pk_bf16_f32 v12, v12, v13\n"
      "v_cvt_pk_bf16_f32 v13, v14, v15\n"
      "v_cvt_pk_bf16_f32 v16, v16, v17\n"
      "v_cvt_pk_bf16_f32 v17, v18, v19\n"
      "v_subrev_u32 v14, 36864, v2\n"
      "v_lshrrev_b32_e64 v18, 4, v14\n"
      "v_mul_u32_u24 v18, 43, v18\n"
      "v_lshrrev_b32_e64 v18, 9, v18\n"
      "v_mul_u32_u24 v15, 960, v18\n"
      "v_add_u32_e64 v15, v15, v14\n"
      "s_mul_i32 s29, s95, 12\n"
      "v_add_u32 v14, s29, v2\n"
      "s_mul_i32 s29, s25, 36864\n"
      "s_add_u32 s29, s29, 1049728\n"
      "s_mul_i32 s30, s95, 3\n"
      "s_lshr_b32 s30, s30, 3\n"
      "s_add_u32 s58, s29, s30\n"
      "ds_read_b128 v[24:27], v14 offset:12480\n"
      "ds_read_b128 v[32:35], v14 offset:18624\n"
      "s_add_u32 s29, s58, 0\n"
      "s_add_u32 s34, s20, s29\n"
      "s_addc_u32 s35, s21, 0\n"
      "s_waitcnt lgkmcnt(0)\n"
      "s_nop 0\n"
      "s_mov_b32 exec_hi, 0x0fffffff\n"
      "global_store_dwordx4 v15, v[24:27], s[34:35] sc1\n"
      "global_store_dwordx4 v15, v[32:35], s[34:35] offset:192 sc1\n"
      "s_mov_b32 exec_hi, -1\n"
      "ds_read_b128 v[24:27], v14 offset:13440\n"
      "ds_read_b128 v[32:35], v14 offset:19584\n"
      "s_add_u32 s29, s58, 5760\n"
      "s_add_u32 s34, s20, s29\n"
      "s_addc_u32 s35, s21, 0\n"
      "s_waitcnt lgkmcnt(0)\n"
      "s_nop 0\n"
      "s_mov_b32 exec_hi, 0x0fffffff\n"
      "global_store_dwordx4 v15, v[24:27], s[34:35] sc1\n"
      "global_store_dwordx4 v15, v[32:35], s[34:35] offset:192 sc1\n"
      "s_mov_b32 exec_hi, -1\n"
      "ds_read_b128 v[24:27], v14 offset:14400\n"
      "ds_read_b128 v[32:35], v14 offset:20544\n"
      "s_add_u32 s29, s58, 11520\n"
      "s_add_u32 s34, s20, s29\n"
      "s_addc_u32 s35, s21, 0\n"
      "s_waitcnt lgkmcnt(0)\n"
      "s_nop 0\n"
      "s_mov_b32 exec_hi, 0x0fffffff\n"
      "global_store_dwordx4 v15, v[24:27], s[34:35] sc1\n"
      "global_store_dwordx4 v15, v[32:35], s[34:35] offset:192 sc1\n"
      "s_mov_b32 exec_hi, -1\n"
      "ds_read_b128 v[24:27], v14 offset:15360\n"
      "ds_read_b128 v[32:35], v14 offset:21504\n"
      "s_add_u32 s29, s58, 17280\n"
      "s_add_u32 s34, s20, s29\n"
      "s_addc_u32 s35, s21, 0\n"
      "s_waitcnt lgkmcnt(0)\n"
      "s_nop 0\n"
      "s_mov_b32 exec_hi, 0x0fffffff\n"
      "global_store_dwordx4 v15, v[24:27], s[34:35] sc1\n"
      "global_store_dwordx4 v15, v[32:35], s[34:35] offset:192 sc1\n"
      "s_mov_b32 exec_hi, -1\n"
      "s_mul_i32 s30, s95, 10\n"
      "v_add_u32_e64 v14, s30, v2\n"
      "v_subrev_u32 v15, 36864, v2\n"
      "v_and_b32 v18, 0x300, v15\n"
      "v_and_b32 v19, 0xf0, v15\n"
      "v_lshl_or_b32 v18, v18, 1, v19\n"
      "s_add_u32 s30, s30, 36864\n"
      "v_add_u32 v18, s30, v18\n"
      "s_waitcnt lgkmcnt(0)\n"
      "L_poll_64_%=:\n"
      "ds_read_b128 v[24:27], v29 offset:32\n"
      "s_waitcnt lgkmcnt(0)\n"
      "v_and_b32 v24, v24, v25\n"
      "v_and_b32 v26, v26, v27\n"
      "v_and_b32 v24, v24, v26\n"
      "s_nop 0\n"
      "v_readfirstlane_b32 s29, v24\n"
      "s_nop 3\n"
      "s_cmp_eq_u32 s29, 0\n"
      "s_cbranch_scc1 L_poll_64_%=\n"
      "s_waitcnt vmcnt(41)\n"
      "ds_write_b128 v14, a[48:51]\n"
      "s_waitcnt vmcnt(39)\n"
      "ds_write_b128 v14, a[52:55] offset:1024\n"
      "ds_read_b64_tr_b16 a[48:49], v18\n"
      "ds_read_b64_tr_b16 a[50:51], v18 offset:256\n"
      "ds_read_b64_tr_b16 a[52:53], v18 offset:8\n"
      "ds_read_b64_tr_b16 a[54:55], v18 offset:264\n"
      "ds_write_b128 v14, a[96:99]\n"
      "s_waitcnt vmcnt(38)\n"
      "ds_write_b128 v14, a[100:103] offset:1024\n"
      "ds_read_b64_tr_b16 a[96:97], v18\n"
      "ds_read_b64_tr_b16 a[98:99], v18 offset:256\n"
      "ds_read_b64_tr_b16 a[100:101], v18 offset:8\n"
      "ds_read_b64_tr_b16 a[102:103], v18 offset:264\n"
      "s_waitcnt vmcnt(37)\n"
      "ds_write_b128 v14, a[56:59]\n"
      "s_waitcnt vmcnt(35)\n"
      "ds_write_b128 v14, a[60:63] offset:1024\n"
      "ds_read_b64_tr_b16 a[56:57], v18\n"
      "ds_read_b64_tr_b16 a[58:59], v18 offset:256\n"
      "ds_read_b64_tr_b16 a[60:61], v18 offset:8\n"
      "ds_read_b64_tr_b16 a[62:63], v18 offset:264\n"
      "ds_write_b128 v14, a[104:107]\n"
      "s_waitcnt vmcnt(34)\n"
      "ds_write_b128 v14, a[108:111] offset:1024\n"
      "ds_read_b64_tr_b16 a[104:105], v18\n"
      "ds_read_b64_tr_b16 a[106:107], v18 offset:256\n"
      "ds_read_b64_tr_b16 a[108:109], v18 offset:8\n"
      "ds_read_b64_tr_b16 a[110:111], v18 offset:264\n"
      "s_waitcnt vmcnt(33)\n"
      "ds_write_b128 v14, a[64:67]\n"
      "s_waitcnt vmcnt(31)\n"
      "ds_write_b128 v14, a[68:71] offset:1024\n"
      "ds_read_b64_tr_b16 a[64:65], v18\n"
      "ds_read_b64_tr_b16 a[66:67], v18 offset:256\n"
      "ds_read_b64_tr_b16 a[68:69], v18 offset:8\n"
      "ds_read_b64_tr_b16 a[70:71], v18 offset:264\n"
      "ds_write_b128 v14, a[112:115]\n"
      "s_waitcnt vmcnt(30)\n"
      "ds_write_b128 v14, a[116:119] offset:1024\n"
      "ds_read_b64_tr_b16 a[112:113], v18\n"
      "ds_read_b64_tr_b16 a[114:115], v18 offset:256\n"
      "ds_read_b64_tr_b16 a[116:117], v18 offset:8\n"
      "ds_read_b64_tr_b16 a[118:119], v18 offset:264\n"
      "s_waitcnt vmcnt(15)\n"
      "s_waitcnt lgkmcnt(0)\n"
      "s_barrier\n"
      "s_mov_b32 s56, 0xffff0000\n"
      "s_mov_b32 s57, 0x0\n"
      "s_mov_b32 s58, 0x0\n"
      "s_mov_b32 s59, 0xffff\n"
      "s_setprio 1\n"
      "ds_read_b128 v[24:27], v3\n"
      "ds_read_b128 v[32:35], v3 offset:1024\n"
      "v_add_u32_e64 v14, s63, v2\n"
      "ds_read_b128 v[56:59], v14\n"
      "ds_read_b64 v[36:37], v14 offset:1024\n"
      "v_add_u32 v14, s92, v21\n"
      "ds_read_u16 v38, v14\n"
      "v_add_u32 v29, s62, v20\n"
      "ds_read_u16 v14, v21\n"
      "ds_read_u16 v18, v21 offset:32\n"
      "v_mov_b32 v15, 0\n"
      "v_mov_b32 v19, 0\n"
      "v_mov_b32_e64 v39, 0\n"
      "ds_read_b32 v40, v1 offset:1024\n"
      "v_add_u32 v21, s55, v1\n"
      "ds_read_b32 v21, v21\n"
      "s_lshl_b32 s29, s33, 1\n"
      "v_add_u32_e64 v28, s29, v2\n"
      "v_subrev_u32 v31, 36864, v2\n"
      "v_and_b32 v22, 0x300, v31\n"
      "v_and_b32 v23, 0xf0, v31\n"
      "v_lshl_or_b32 v22, v22, 1, v23\n"
      "s_add_u32 s29, s29, 36864\n"
      "v_add_u32 v22, s29, v22\n"
      "ds_write_b128 v28, a[72:75] offset:24576\n"
      "ds_write_b128 v28, a[76:79] offset:25600\n"
      "ds_read_b64_tr_b16 a[72:73], v22 offset:24576\n"
      "ds_read_b64_tr_b16 a[74:75], v22 offset:24832\n"
      "ds_read_b64_tr_b16 a[76:77], v22 offset:24584\n"
      "ds_read_b64_tr_b16 a[78:79], v22 offset:24840\n"
      "s_waitcnt lgkmcnt(7)\n"
      "v_mfma_f32_16x16x32_bf16 v[44:47], v[8:11], v[24:27], 0\n"
      "ds_write_b128 v28, a[120:123] offset:24576\n"
      "ds_write_b128 v28, a[124:127] offset:25600\n"
      "ds_read_b64_tr_b16 a[120:121], v22 offset:24576\n"
      "ds_read_b64_tr_b16 a[122:123], v22 offset:24832\n"
      "ds_read_b64_tr_b16 a[124:125], v22 offset:24584\n"
      "ds_read_b64_tr_b16 a[126:127], v22 offset:24840\n"
      "v_mfma_f32_16x16x32_bf16 v[44:47], v[8:11], v[32:35], v[44:47]\n"
      "v_mfma_f32_16x16x32_bf16 v[60:63], v[52:55], v[24:27], 0\n"
      "v_mfma_f32_16x16x32_bf16 v[60:63], v[12:15], v[32:35], v[60:63]\n"
      "v_mfma_f32_16x16x32_bf16 v[64:67], v[48:51], v[24:27], 0\n"
      "v_mfma_f32_16x16x32_bf16 v[64:67], v[16:19], v[32:35], v[64:67]\n"
      "v_mfma_f32_16x16x32_bf16 v[68:71], v[56:59], v[24:27], 0\n"
      "v_mfma_f32_16x16x32_bf16 v[68:71], v[36:39], v[32:35], v[68:71]\n"
      "ds_read_b128 v[24:27], v3 offset:2048\n"
      "ds_read_b128 v[32:35], v3 offset:3072\n"
      "v_rcp_f32 v44, v44\n"
      "s_nop 0\n"
      "v_mul_f32 v60, v44, v60\n"
      "v_mul_f32 v61, v44, v61\n"
      "v_mul_f32 v62, v44, v62\n"
      "v_mul_f32 v63, v44, v63\n"
      "v_cvt_pk_bf16_f32 v60, v60, v61\n"
      "v_cvt_pk_bf16_f32 v61, v62, v63\n"
      "ds_write_b64 v20, v[60:61]\n"
      "v_mul_f32 v64, v44, v64\n"
      "v_mul_f32 v65, v44, v65\n"
      "v_mul_f32 v66, v44, v66\n"
      "v_mul_f32 v67, v44, v67\n"
      "v_cvt_pk_bf16_f32 v64, v64, v65\n"
      "v_cvt_pk_bf16_f32 v65, v66, v67\n"
      "ds_write_b64 v20, v[64:65] offset:1024\n"
      "v_mul_f32 v68, v44, v68\n"
      "v_mul_f32 v69, v44, v69\n"
      "v_mul_f32_e64 v70, v44, v70\n"
      "v_mul_f32 v71, v44, v71\n"
      "v_cvt_pk_bf16_f32 v68, v68, v69\n"
      "v_cvt_pk_bf16_f32 v69, v70, v71\n"
      "ds_write_b64 v29, v[68:69]\n"
      "s_waitcnt lgkmcnt(3)\n"
      "v_mfma_f32_16x16x32_bf16 v[68:71], v[8:11], v[24:27], 0\n"
      "ds_write_b128 v28, a[80:83] offset:24576\n"
      "ds_write_b128 v28, a[84:87] offset:25600\n"
      "ds_read_b64_tr_b16 a[80:81], v22 offset:24576\n"
      "ds_read_b64_tr_b16 a[82:83], v22 offset:24832\n"
      "ds_read_b64_tr_b16 a[84:85], v22 offset:24584\n"
      "ds_read_b64_tr_b16 a[86:87], v22 offset:24840\n"
      "ds_write_b128 v28, a[128:131] offset:24576\n"
      "ds_write_b128 v28, a[132:135] offset:25600\n"
      "ds_read_b64_tr_b16 a[128:129], v22 offset:24576\n"
      "ds_read_b64_tr_b16 a[130:131], v22 offset:24832\n"
      "ds_read_b64_tr_b16 a[132:133], v22 offset:24584\n"
      "ds_read_b64_tr_b16 a[134:135], v22 offset:24840\n"
      "v_mfma_f32_16x16x32_bf16 v[68:71], v[8:11], v[32:35], v[68:71]\n"
      "v_mfma_f32_16x16x32_bf16 v[64:67], v[52:55], v[24:27], 0\n"
      "v_mfma_f32_16x16x32_bf16 v[64:67], v[12:15], v[32:35], v[64:67]\n"
      "v_mfma_f32_16x16x32_bf16 v[60:63], v[48:51], v[24:27], 0\n"
      "v_mfma_f32_16x16x32_bf16 v[60:63], v[16:19], v[32:35], v[60:63]\n"
      "v_mfma_f32_16x16x32_bf16 v[44:47], v[56:59], v[24:27], 0\n"
      "v_mfma_f32_16x16x32_bf16 v[44:47], v[36:39], v[32:35], v[44:47]\n"
      "ds_read_b128 v[24:27], v3 offset:4096\n"
      "ds_read_b128 v[32:35], v3 offset:5120\n"
      "v_rcp_f32 v68, v68\n"
      "s_nop 0\n"
      "v_mul_f32 v64, v68, v64\n"
      "v_mul_f32 v65, v68, v65\n"
      "v_mul_f32 v66, v68, v66\n"
      "v_mul_f32 v67, v68, v67\n"
      "v_cvt_pk_bf16_f32 v64, v64, v65\n"
      "v_cvt_pk_bf16_f32 v65, v66, v67\n"
      "ds_write_b64 v20, v[64:65] offset:256\n"
      "v_mul_f32 v60, v68, v60\n"
      "v_mul_f32 v61, v68, v61\n"
      "v_mul_f32 v62, v68, v62\n"
      "v_mul_f32 v63, v68, v63\n"
      "v_cvt_pk_bf16_f32 v60, v60, v61\n"
      "v_cvt_pk_bf16_f32 v61, v62, v63\n"
      "ds_write_b64 v20, v[60:61] offset:1280\n"
      "v_mul_f32 v44, v68, v44\n"
      "v_mul_f32 v45, v68, v45\n"
      "v_mul_f32 v46, v68, v46\n"
      "v_mul_f32_e64 v47, v68, v47\n"
      "v_cvt_pk_bf16_f32 v44, v44, v45\n"
      "v_cvt_pk_bf16_f32 v45, v46, v47\n"
      "ds_write_b64 v29, v[44:45] offset:256\n"
      "s_waitcnt lgkmcnt(3)\n"
      "v_mfma_f32_16x16x32_bf16 v[44:47], v[8:11], v[24:27], 0\n"
      "ds_write_b128 v28, a[88:91] offset:24576\n"
      "ds_write_b128 v28, a[92:95] offset:25600\n"
      "ds_read_b64_tr_b16 a[88:89], v22 offset:24576\n"
      "ds_read_b64_tr_b16 a[90:91], v22 offset:24832\n"
      "ds_read_b64_tr_b16 a[92:93], v22 offset:24584\n"
      "ds_read_b64_tr_b16 a[94:95], v22 offset:24840\n"
      "v_mfma_f32_16x16x32_bf16 v[44:47], v[8:11], v[32:35], v[44:47]\n"
      "v_mfma_f32_16x16x32_bf16 v[60:63], v[52:55], v[24:27], 0\n"
      "v_mfma_f32_16x16x32_bf16 v[60:63], v[12:15], v[32:35], v[60:63]\n"
      "v_mfma_f32_16x16x32_bf16 v[64:67], v[48:51], v[24:27], 0\n"
      "v_mfma_f32_16x16x32_bf16 v[64:67], v[16:19], v[32:35], v[64:67]\n"
      "v_mfma_f32_16x16x32_bf16 v[68:71], v[56:59], v[24:27], 0\n"
      "v_mfma_f32_16x16x32_bf16 v[68:71], v[36:39], v[32:35], v[68:71]\n"
      "ds_read_b128 v[72:75], v21\n"
      "ds_read_b128 v[76:79], v21 offset:64\n"
      "v_rcp_f32 v44, v44\n"
      "s_nop 0\n"
      "v_mul_f32 v60, v44, v60\n"
      "v_mul_f32 v61, v44, v61\n"
      "v_mul_f32 v62, v44, v62\n"
      "v_mul_f32 v63, v44, v63\n"
      "v_cvt_pk_bf16_f32 v60, v60, v61\n"
      "v_cvt_pk_bf16_f32 v61, v62, v63\n"
      "ds_write_b64 v20, v[60:61] offset:12288\n"
      "v_mul_f32 v64, v44, v64\n"
      "v_mul_f32 v65, v44, v65\n"
      "v_mul_f32 v66, v44, v66\n"
      "v_mul_f32 v67, v44, v67\n"
      "v_cvt_pk_bf16_f32 v64, v64, v65\n"
      "v_cvt_pk_bf16_f32 v65, v66, v67\n"
      "ds_write_b64 v20, v[64:65] offset:13312\n"
      "v_mul_f32 v68, v44, v68\n"
      "v_mul_f32 v69, v44, v69\n"
      "v_mul_f32_e64 v70, v44, v70\n"
      "v_mul_f32 v71, v44, v71\n"
      "v_cvt_pk_bf16_f32 v68, v68, v69\n"
      "v_cvt_pk_bf16_f32 v69, v70, v71\n"
      "ds_write_b64 v29, v[68:69] offset:12288\n"
      "s_waitcnt lgkmcnt(3)\n"
      "v_mfma_f32_16x16x32_bf16 v[68:71], v[72:75], v[8:11], 0\n"
      "ds_write_b128 v28, a[136:139] offset:24576\n"
      "ds_write_b128 v28, a[140:143] offset:25600\n"
      "ds_read_b64_tr_b16 a[136:137], v22 offset:24576\n"
      "ds_read_b64_tr_b16 a[138:139], v22 offset:24832\n"
      "ds_read_b64_tr_b16 a[140:141], v22 offset:24584\n"
      "ds_read_b64_tr_b16 a[142:143], v22 offset:24840\n"
      "ds_read_b128 v[20:23], v4 offset:24576\n"
      "ds_read_b128 v[24:27], v5 offset:24576\n"
      "ds_read_b128 v[28:31], v4 offset:28672\n"
      "ds_read_b128 v[32:35], v5 offset:28672\n"
      "v_mfma_f32_16x16x32_bf16 v[68:71], v[76:79], v[8:11], v[68:71]\n"
      "v_mfma_f32_16x16x32_bf16 v[64:67], v[72:75], v[52:55], 0\n"
      "v_mfma_f32_16x16x32_bf16 v[64:67], v[76:79], v[12:15], v[64:67]\n"
      "v_mfma_f32_16x16x32_bf16 v[12:15], v[72:75], v[48:51], 0\n"
      "v_mfma_f32_16x16x32_bf16 v[12:15], v[76:79], v[16:19], v[12:15]\n"
      "v_mfma_f32_16x16x32_bf16 v[16:19], v[72:75], v[56:59], 0\n"
      "v_mfma_f32_16x16x32_bf16 v[16:19], v[76:79], v[36:39], v[16:19]\n"
      "s_nop 5\n"
      "v_cndmask_b32 v64, v64, v12, s[56:57]\n"
      "s_nop 0\n"
      "v_cndmask_b32 v64, v64, v16, s[58:59]\n"
      "v_mov_b32_dpp v68, v68 quad_perm:[0,0,0,0] row_mask:0xf bank_mask:0xf\n"
      "v_rcp_f32 v68, v68\n"
      "s_nop 0\n"
      "v_mul_f32 v64, v68, v64\n"
      "v_cvt_pk_bf16_f32 v64, v64, v64\n"
      "ds_write_b16 v40, v64\n"
      "s_waitcnt vmcnt(0)\n"
      "v_add_u32 v12, s52, v0\n"
      "s_or_b32 s14, s53, s54\n"
      "s_cbranch_scc1 L_edge_65_%=\n"
      "L_eb_66_%=:\n"
      "s_add_u32 m0, s33, 0\n"
      "s_nop 0\n"
      "global_load_lds_dwordx4 v12, s[16:17]\n"
      "s_add_u32 m0, s33, 3968\n"
      "s_nop 0\n"
      "global_load_lds_dwordx4 v12, s[16:17] offset:128\n"
      "s_add_u32 m0, s33, 7936\n"
      "s_nop 0\n"
      "global_load_lds_dwordx4 v12, s[16:17] offset:256\n"
      "s_waitcnt lgkmcnt(0)\n"
      "s_barrier\n"
      "s_nop 7\n"
      "s_nop 7\n"
      "L_B1loop_%=:\n"
      "s_setprio 1\n"
      "v_mfma_f32_16x16x32_bf16 v[40:43], v[20:23], a[0:3], 0\n"
      "v_mfma_f32_16x16x32_bf16 v[40:43], v[24:27], a[8:11], v[40:43]\n"
      "v_mfma_f32_16x16x32_bf16 v[36:39], v[20:23], a[4:7], 0\n"
      "v_mfma_f32_16x16x32_bf16 v[36:39], v[24:27], a[12:15], v[36:39]\n"
      "ds_read_b128 v[24:27], v4 offset:32768\n"
      "ds_read_b128 v[12:15], v5 offset:32768\n"
      "v_mfma_f32_16x16x32_bf16 v[40:43], v[28:31], a[16:19], v[40:43]\n"
      "v_mfma_f32_16x16x32_bf16 v[40:43], v[32:35], a[24:27], v[40:43]\n"
      "v_mfma_f32_16x16x32_bf16 v[36:39], v[28:31], a[20:23], v[36:39]\n"
      "v_mfma_f32_16x16x32_bf16 v[36:39], v[32:35], a[28:31], v[36:39]\n"
      "ds_read_b128 v[20:23], v4 offset:26624\n"
      "ds_read_b128 v[16:19], v5 offset:26624\n"
      "s_waitcnt lgkmcnt(2)\n"
      "v_mfma_f32_16x16x32_bf16 v[40:43], v[24:27], a[32:35], v[40:43]\n"
      "v_mfma_f32_16x16x32_bf16 v[40:43], v[12:15], a[40:43], v[40:43]\n"
      "v_mfma_f32_16x16x32_bf16 v[36:39], v[24:27], a[36:39], v[36:39]\n"
      "v_mfma_f32_16x16x32_bf16 v[36:39], v[12:15], a[44:47], v[36:39]\n"
      "ds_read_b128 v[24:27], v4 offset:30720\n"
      "ds_read_b128 v[32:35], v5 offset:30720\n"
      "s_waitcnt lgkmcnt(2)\n"
      "v_mfma_f32_16x16x32_bf16 v[28:31], v[20:23], a[0:3], 0\n"
      "v_mfma_f32_16x16x32_bf16 v[28:31], v[16:19], a[8:11], v[28:31]\n"
      "v_mfma_f32_16x16x32_bf16 v[12:15], v[20:23], a[4:7], 0\n"
      "v_mfma_f32_16x16x32_bf16 v[12:15], v[16:19], a[12:15], v[12:15]\n"
      "ds_read_b128 v[20:23], v4 offset:34816\n"
      "ds_read_b128 v[16:19], v5 offset:34816\n"
      "s_waitcnt lgkmcnt(2)\n"
      "v_mfma_f32_16x16x32_bf16 v[28:31], v[24:27], a[16:19], v[28:31]\n"
      "v_mfma_f32_16x16x32_bf16 v[28:31], v[32:35], a[24:27], v[28:31]\n"
      "v_mfma_f32_16x16x32_bf16 v[12:15], v[24:27], a[20:23], v[12:15]\n"
      "v_mfma_f32_16x16x32_bf16 v[12:15], v[32:35], a[28:31], v[12:15]\n"
      "ds_read_b128 v[44:47], v6\n"
      "ds_read_b128 v[92:95], v6 offset:2048\n"
      "s_waitcnt lgkmcnt(2)\n"
      "v_mfma_f32_16x16x32_bf16 v[28:31], v[20:23], a[32:35], v[28:31]\n"
      "v_mfma_f32_16x16x32_bf16 v[28:31], v[16:19], a[40:43], v[28:31]\n"
      "v_mfma_f32_16x16x32_bf16 v[12:15], v[20:23], a[36:39], v[12:15]\n"
      "v_mfma_f32_16x16x32_bf16 v[12:15], v[16:19], a[44:47], v[12:15]\n"
      "ds_read_b128 v[88:91], v6 offset:4096\n"
      "ds_read_b128 v[32:35], v6 offset:6144\n"
      "s_waitcnt lgkmcnt(2)\n"
      "v_mfma_f32_16x16x32_bf16 v[24:27], a[48:51], v[44:47], 0\n"
      "v_mfma_f32_16x16x32_bf16 v[24:27], a[56:59], v[92:95], v[24:27]\n"
      "v_mfma_f32_16x16x32_bf16 v[16:19], a[52:55], v[44:47], 0\n"
      "v_mfma_f32_16x16x32_bf16 v[16:19], a[60:63], v[92:95], v[16:19]\n"
      "v_mfma_f32_16x16x32_bf16 v[20:23], a[96:99], v[44:47], 0\n"
      "v_cvt_pk_bf16_f32 v40, v40, v41\n"
      "v_mfma_f32_16x16x32_bf16 v[20:23], a[104:107], v[92:95], v[20:23]\n"
      "v_mfma_f32_16x16x32_bf16 v[72:75], a[100:103], v[44:47], 0\n"
      "v_cvt_pk_bf16_f32 v41, v42, v43\n"
      "v_mfma_f32_16x16x32_bf16 v[72:75], a[108:111], v[92:95], v[72:75]\n"
      "ds_read_b128 v[44:47], v6 offset:8192\n"
      "ds_read_b128 v[92:95], v6 offset:10240\n"
      "s_waitcnt lgkmcnt(2)\n"
      "v_mfma_f32_16x16x32_bf16 v[24:27], a[64:67], v[88:91], v[24:27]\n"
      "v_mfma_f32_16x16x32_bf16 v[24:27], a[72:75], v[32:35], v[24:27]\n"
      "v_cvt_pk_bf16_f32 v42, v28, v29\n"
      "v_mfma_f32_16x16x32_bf16 v[16:19], a[68:71], v[88:91], v[16:19]\n"
      "v_mfma_f32_16x16x32_bf16 v[16:19], a[76:79], v[32:35], v[16:19]\n"
      "v_cvt_pk_bf16_f32 v43, v30, v31\n"
      "v_mfma_f32_16x16x32_bf16 v[20:23], a[112:115], v[88:91], v[20:23]\n"
      "v_mfma_f32_16x16x32_bf16 v[20:23], a[120:123], v[32:35], v[20:23]\n"
      "v_mfma_f32_16x16x32_bf16 v[72:75], a[116:119], v[88:91], v[72:75]\n"
      "v_cvt_pk_bf16_f32 v36, v36, v37\n"
      "v_mfma_f32_16x16x32_bf16 v[72:75], a[124:127], v[32:35], v[72:75]\n"
      "ds_read_b128 v[28:31], v6 offset:256\n"
      "ds_read_b128 v[88:91], v6 offset:2304\n"
      "s_waitcnt lgkmcnt(2)\n"
      "v_mfma_f32_16x16x32_bf16 v[24:27], a[80:83], v[44:47], v[24:27]\n"
      "v_cvt_pk_bf16_f32 v37, v38, v39\n"
      "v_mfma_f32_16x16x32_bf16 v[24:27], a[88:91], v[92:95], v[24:27]\n"
      "v_mfma_f32_16x16x32_bf16 v[16:19], a[84:87], v[44:47], v[16:19]\n"
      "v_mfma_f32_16x16x32_bf16 v[16:19], a[92:95], v[92:95], v[16:19]\n"
      "v_cvt_pk_bf16_f32 v38, v12, v13\n"
      "v_mfma_f32_16x16x32_bf16 v[20:23], a[128:131], v[44:47], v[20:23]\n"
      "v_mfma_f32_16x16x32_bf16 v[20:23], a[136:139], v[92:95], v[20:23]\n"
      "v_cvt_pk_bf16_f32 v39, v14, v15\n"
      "v_mfma_f32_16x16x32_bf16 v[72:75], a[132:135], v[44:47], v[72:75]\n"
      "v_mfma_f32_16x16x32_bf16 v[72:75], a[140:143], v[92:95], v[72:75]\n"
      "ds_read_b128 v[92:95], v6 offset:4352\n"
      "ds_read_b128 v[44:47], v6 offset:6400\n"
      "s_waitcnt lgkmcnt(2)\n"
      "v_mfma_f32_16x16x32_bf16 v[60:63], a[48:51], v[28:31], 0\n"
      "v_mfma_f32_16x16x32_bf16 v[60:63], a[56:59], v[88:91], v[60:63]\n"
      "v_mfma_f32_16x16x32_bf16 v[68:71], a[52:55], v[28:31], 0\n"
      "v_mfma_f32_16x16x32_bf16 v[68:71], a[60:63], v[88:91], v[68:71]\n"
      "v_mfma_f32_16x16x32_bf16 v[12:15], a[96:99], v[28:31], 0\n"
      "v_add_f32 v32, v24, v25\n"
      "v_mul_f32 v33, v24, v24\n"
      "v_mfma_f32_16x16x32_bf16 v[12:15], a[104:107], v[88:91], v[12:15]\n"
      "v_fmac_f32 v33, v25, v25\n"
      "v_add_f32 v32, v32, v26\n"
      "v_mfma_f32_16x16x32_bf16 v[64:67], a[100:103], v[28:31], 0\n"
      "v_fmac_f32 v33, v26, v26\n"
      "v_mfma_f32_16x16x32_bf16 v[64:67], a[108:111], v[88:91], v[64:67]\n"
      "ds_read_b128 v[88:91], v6 offset:8448\n"
      "ds_read_b128 v[28:31], v6 offset:10496\n"
      "v_add_f32 v32, v32, v27\n"
      "v_fmac_f32 v33, v27, v27\n"
      "s_waitcnt lgkmcnt(2)\n"
      "v_mfma_f32_16x16x32_bf16 v[60:63], a[64:67], v[92:95], v[60:63]\n"
      "v_add_f32 v32, v32, v16\n"
      "v_fmac_f32 v33, v16, v16\n"
      "v_mfma_f32_16x16x32_bf16 v[60:63], a[72:75], v[44:47], v[60:63]\n"
      "v_add_f32 v32, v32, v17\n"
      "v_mfma_f32_16x16x32_bf16 v[68:71], a[68:71], v[92:95], v[68:71]\n"
      "v_fmac_f32 v33, v17, v17\n"
      "v_add_f32 v32, v32, v18\n"
      "v_mfma_f32_16x16x32_bf16 v[68:71], a[76:79], v[44:47], v[68:71]\n"
      "v_fmac_f32 v33, v18, v18\n"
      "v_add_f32 v32, v32, v19\n"
      "v_mfma_f32_16x16x32_bf16 v[12:15], a[112:115], v[92:95], v[12:15]\n"
      "v_fmac_f32 v33, v19, v19\n"
      "v_mfma_f32_16x16x32_bf16 v[12:15], a[120:123], v[44:47], v[12:15]\n"
      "v_add_f32 v32, v32, v20\n"
      "v_fmac_f32 v33, v20, v20\n"
      "v_mfma_f32_16x16x32_bf16 v[64:67], a[116:119], v[92:95], v[64:67]\n"
      "v_add_f32 v32, v32, v21\n"
      "v_fmac_f32 v33, v21, v21\n"
      "v_mfma_f32_16x16x32_bf16 v[64:67], a[124:127], v[44:47], v[64:67]\n"
      "ds_read_b128 v[92:95], v6 offset:12288\n"
      "ds_read_b128 v[84:87], v6 offset:14336\n"
      "v_add_f32 v32, v32, v22\n"
      "s_waitcnt lgkmcnt(2)\n"
      "v_mfma_f32_16x16x32_bf16 v[60:63], a[80:83], v[88:91], v[60:63]\n"
      "v_fmac_f32 v33, v22, v22\n"
      "v_add_f32 v32, v32, v23\n"
      "v_mfma_f32_16x16x32_bf16 v[60:63], a[88:91], v[28:31], v[60:63]\n"
      "v_fmac_f32 v33, v23, v23\n"
      "v_add_f32 v32, v32, v72\n"
      "v_mfma_f32_16x16x32_bf16 v[68:71], a[84:87], v[88:91], v[68:71]\n"
      "v_fmac_f32_e64 v33, v72, v72\n"
      "v_mfma_f32_16x16x32_bf16 v[68:71], a[92:95], v[28:31], v[68:71]\n"
      "v_add_f32 v32, v32, v73\n"
      "v_fmac_f32 v33, v73, v73\n"
      "v_mfma_f32_16x16x32_bf16 v[12:15], a[128:131], v[88:91], v[12:15]\n"
      "v_add_f32 v32, v32, v74\n"
      "v_fmac_f32 v33, v74, v74\n"
      "v_mfma_f32_16x16x32_bf16 v[12:15], a[136:139], v[28:31], v[12:15]\n"
      "v_add_f32 v32, v32, v75\n"
      "v_mfma_f32_16x16x32_bf16 v[64:67], a[132:135], v[88:91], v[64:67]\n"
      "v_fmac_f32 v33, v75, v75\n"
      "s_nop 1\n"
      "v_permlane16_swap_b32 v32, v33\n"
      "v_mfma_f32_16x16x32_bf16 v[64:67], a[140:143], v[28:31], v[64:67]\n"
      "ds_read_b128 v[76:79], v6 offset:16384\n"
      "ds_read_b128 v[88:91], v6 offset:18432\n"
      "v_add_f32 v32, v32, v33\n"
      "s_waitcnt lgkmcnt(2)\n"
      "v_mfma_f32_16x16x32_bf16 v[44:47], a[48:51], v[92:95], 0\n"
      "v_mfma_f32_16x16x32_bf16 v[44:47], a[56:59], v[84:87], v[44:47]\n"
      "v_mfma_f32_16x16x32_bf16 v[56:59], a[52:55], v[92:95], 0\n"
      "v_mfma_f32_16x16x32_bf16 v[56:59], a[60:63], v[84:87], v[56:59]\n"
      "v_mfma_f32_16x16x32_bf16 v[52:55], a[96:99], v[92:95], 0\n"
      "v_add_f32 v33, v60, v61\n"
      "v_mul_f32 v34, v60, v60\n"
      "v_mfma_f32_16x16x32_bf16 v[52:55], a[104:107], v[84:87], v[52:55]\n"
      "v_fmac_f32 v34, v61, v61\n"
      "v_add_f32 v33, v33, v62\n"
      "v_mfma_f32_16x16x32_bf16 v[48:51], a[100:103], v[92:95], 0\n"
      "v_fmac_f32 v34, v62, v62\n"
      "v_add_f32 v33, v33, v63\n"
      "v_mfma_f32_16x16x32_bf16 v[48:51], a[108:111], v[84:87], v[48:51]\n"
      "ds_read_b128 v[84:87], v6 offset:20480\n"
      "ds_read_b128 v[28:31], v6 offset:22528\n"
      "v_fmac_f32 v34, v63, v63\n"
      "v_add_f32 v33, v33, v68\n"
      "s_waitcnt lgkmcnt(2)\n"
      "v_mfma_f32_16x16x32_bf16 v[44:47], a[64:67], v[76:79], v[44:47]\n"
      "v_fmac_f32 v34, v68, v68\n"
      "v_add_f32 v33, v33, v69\n"
      "v_mfma_f32_16x16x32_bf16 v[44:47], a[72:75], v[88:91], v[44:47]\n"
      "v_fmac_f32 v34, v69, v69\n"
      "v_add_f32 v33, v33, v70\n"
      "v_mfma_f32_16x16x32_bf16 v[56:59], a[68:71], v[76:79], v[56:59]\n"
      "v_fmac_f32 v34, v70, v70\n"
      "v_mfma_f32_16x16x32_bf16 v[56:59], a[76:79], v[88:91], v[56:59]\n"
      "v_add_f32 v33, v33, v71\n"
      "v_fmac_f32 v34, v71, v71\n"
      "v_mfma_f32_16x16x32_bf16 v[52:55], a[112:115], v[76:79], v[52:55]\n"
      "v_add_f32 v33, v33, v12\n"
      "v_fmac_f32 v34, v12, v12\n"
      "v_mfma_f32_16x16x32_bf16 v[52:55], a[120:123], v[88:91], v[52:55]\n"
      "v_add_f32 v33, v33, v13\n"
      "v_fmac_f32 v34, v13, v13\n"
      "v_mfma_f32_16x16x32_bf16 v[48:51], a[116:119], v[76:79], v[48:51]\n"
      "v_add_f32 v33, v33, v14\n"
      "v_fmac_f32 v34, v14, v14\n"
      "v_mfma_f32_16x16x32_bf16 v[48:51], a[124:127], v[88:91], v[48:51]\n"
      "ds_read_b128 v[88:91], v4 offset:14336\n"
      "ds_read_b128 v[76:79], v5 offset:14336\n"
      "v_add_f32 v33, v33, v15\n"
      "v_fmac_f32 v34, v15, v15\n"
      "s_waitcnt lgkmcnt(2)\n"
      "v_mfma_f32_16x16x32_bf16 v[44:47], a[80:83], v[84:87], v[44:47]\n"
      "v_add_f32 v33, v33, v64\n"
      "v_fmac_f32 v34, v64, v64\n"
      "v_mfma_f32_16x16x32_bf16 v[44:47], a[88:91], v[28:31], v[44:47]\n"
      "v_add_f32 v33, v33, v65\n"
      "v_mfma_f32_16x16x32_bf16 v[56:59], a[84:87], v[84:87], v[56:59]\n"
      "v_fmac_f32 v34, v65, v65\n"
      "v_add_f32 v33, v33, v66\n"
      "v_mfma_f32_16x16x32_bf16 v[56:59], a[92:95], v[28:31], v[56:59]\n"
      "v_fmac_f32 v34, v66, v66\n"
      "v_add_f32 v33, v33, v67\n"
      "v_mfma_f32_16x16x32_bf16 v[52:55], a[128:131], v[84:87], v[52:55]\n"
      "v_fmac_f32 v34, v67, v67\n"
      "s_nop 1\n"
      "v_permlane16_swap_b32 v33, v34\n"
      "v_mfma_f32_16x16x32_bf16 v[52:55], a[136:139], v[28:31], v[52:55]\n"
      "v_add_f32 v33, v33, v34\n"
      "ds_read_b32 v34, v1 offset:1280\n"
      "v_mfma_f32_16x16x32_bf16 v[48:51], a[132:135], v[84:87], v[48:51]\n"
      "v_permlane32_swap_b32 v32, v33\n"
      "v_add_f32_e64 v32, v32, v33\n"
      "v_mfma_f32_16x16x32_bf16 v[48:51], a[140:143], v[28:31], v[48:51]\n"
      "ds_read_b128 v[80:83], v4 offset:18432\n"
      "ds_read_b128 v[92:95], v5 offset:18432\n"
      "s_waitcnt lgkmcnt(2)\n"
      "ds_write_b32 v34, v32\n"
      "v_mfma_f32_16x16x32_bf16 v[28:31], v[88:91], a[0:3], 0\n"
      "v_mfma_f32_16x16x32_bf16 v[28:31], v[76:79], a[8:11], v[28:31]\n"
      "v_mfma_f32_16x16x32_bf16 v[32:35], v[88:91], a[4:7], 0\n"
      "v_mfma_f32_16x16x32_bf16 v[32:35], v[76:79], a[12:15], v[32:35]\n"
      "ds_read_b128 v[84:87], v4 offset:22528\n"
      "ds_read_b128 v[88:91], v5 offset:22528\n"
      "s_waitcnt lgkmcnt(2)\n"
      "v_mfma_f32_16x16x32_bf16 v[28:31], v[80:83], a[16:19], v[28:31]\n"
      "v_add_f32 v76, v44, v45\n"
      "v_mul_f32 v77, v44, v44\n"
      "v_fmac_f32 v77, v45, v45\n"
      "v_add_f32 v76, v76, v46\n"
      "v_fmac_f32 v77, v46, v46\n"
      "v_mfma_f32_16x16x32_bf16 v[28:31], v[92:95], a[24:27], v[28:31]\n"
      "v_add_f32 v76, v76, v47\n"
      "v_fmac_f32 v77, v47, v47\n"
      "v_add_f32 v76, v76, v56\n"
      "v_fmac_f32 v77, v56, v56\n"
      "v_add_f32 v76, v76, v57\n"
      "v_mfma_f32_16x16x32_bf16 v[32:35], v[80:83], a[20:23], v[32:35]\n"
      "v_fmac_f32 v77, v57, v57\n"
      "v_add_f32 v76, v76, v58\n"
      "v_fmac_f32 v77, v58, v58\n"
      "v_add_f32 v76, v76, v59\n"
      "v_fmac_f32 v77, v59, v59\n"
      "v_mfma_f32_16x16x32_bf16 v[32:35], v[92:95], a[28:31], v[32:35]\n"
      "v_add_f32 v76, v76, v52\n"
      "v_fmac_f32 v77, v52, v52\n"
      "v_add_f32 v76, v76, v53\n"
      "v_fmac_f32 v77, v53, v53\n"
      "s_setprio 0\n"
      "v_add_f32 v76, v76, v54\n"
      "s_waitcnt lgkmcnt(0)\n"
      "v_mfma_f32_16x16x32_bf16 v[28:31], v[84:87], a[32:35], v[28:31]\n"
      "v_fmac_f32 v77, v54, v54\n"
      "v_add_f32 v76, v76, v55\n"
      "v_fmac_f32 v77, v55, v55\n"
      "v_add_f32 v76, v76, v48\n"
      "v_fmac_f32 v77, v48, v48\n"
      "v_mfma_f32_16x16x32_bf16 v[28:31], v[88:91], a[40:43], v[28:31]\n"
      "v_add_f32 v76, v76, v49\n"
      "v_fmac_f32 v77, v49, v49\n"
      "v_add_f32 v76, v76, v50\n"
      "v_fmac_f32 v77, v50, v50\n"
      "v_add_f32 v76, v76, v51\n"
      "v_mfma_f32_16x16x32_bf16 v[32:35], v[84:87], a[36:39], v[32:35]\n"
      "v_fmac_f32 v77, v51, v51\n"
      "s_nop 1\n"
      "v_permlane16_swap_b32 v76, v77\n"
      "v_add_f32_e64 v76, v76, v77\n"
      "ds_read_b32 v92, v1 offset:1280\n"
      "v_mov_b32 v77, v76\n"
      "v_mfma_f32_16x16x32_bf16 v[32:35], v[88:91], a[44:47], v[32:35]\n"
      "s_nop 0\n"
      "v_permlane32_swap_b32_e64 v76, v77\n"
      "v_add_f32 v76, v76, v77\n"
      "s_waitcnt lgkmcnt(0)\n"
      "ds_write_b32 v92, v76 offset:512\n"
      "ds_read_b32 v92, v1 offset:512\n"
      "ds_read_b32 v93, v1 offset:768\n"
      "v_cvt_pk_bf16_f32 v28, v28, v29\n"
      "v_cvt_pk_bf16_f32 v29, v30, v31\n"
      "v_cvt_pk_bf16_f32 v32, v32, v33\n"
      "v_cvt_pk_bf16_f32 v33, v34, v35\n"
      "s_waitcnt vmcnt(0)\n"
      "s_waitcnt lgkmcnt(0)\n"
      "s_barrier\n"
      "ds_read_b32 v95, v1\n"
      "ds_read_b128 v[88:91], v3\n"
      "ds_read_b128 v[84:87], v3 offset:1024\n"
      "ds_read_u16 v30, v93 offset:1152\n"
      "ds_read_u16 v34, v93 offset:1184\n"
      "ds_read_b32 v7, v1 offset:1024\n"
      "s_setprio 1\n"
      "v_add_u32 v76, 108800, v2\n"
      "ds_read_b128 v[104:107], v76\n"
      "ds_read_b128 v[100:103], v76 offset:1024\n"
      "v_add_u32_e64 v82, s63, v2\n"
      "ds_read_b128 v[76:79], v82 offset:24576\n"
      "ds_read_b64 v[80:81], v82 offset:25600\n"
      "v_add_u32 v35, s92, v93\n"
      "ds_read_u16 v82, v35 offset:1152\n"
      "v_add_u32 v93, s62, v92\n"
      "s_waitcnt lgkmcnt(4)\n"
      "v_add_f32 v104, v104, v106\n"
      "v_add_f32 v105, v105, v107\n"
      "s_waitcnt lgkmcnt(3)\n"
      "v_add_f32 v100, v100, v102\n"
      "v_add_f32 v101, v101, v103\n"
      "v_add_f32 v104, v104, v100\n"
      "v_add_f32 v105, v105, v101\n"
      "v_fma_f32 v100, -v104, v104, s74\n"
      "v_fmac_f32 v100, 0x43400000, v105\n"
      "v_max_f32 v100, s74, v100\n"
      "v_rsq_f32 v100, v100\n"
      "v_xor_b32_e64 v97, 64, v95\n"
      "v_mul_f32 v35, 0x43400000, v100\n"
      "v_mul_f32 v83, -v104, v100\n"
      "v_mov_b32 v94, v35\n"
      "v_mov_b32 v96, v83\n"
      "v_mov_b32 v31, 0\n"
      "v_permlane32_swap_b32 v35, v94\n"
      "v_permlane32_swap_b32 v83, v96\n"
      "v_mov_b32 v105, v35\n"
      "v_mov_b32 v104, v83\n"
      "s_nop 0\n"
      "v_permlane16_swap_b32 v35, v105\n"
      "v_permlane16_swap_b32 v83, v104\n"
      "v_fma_f32 v24, v24, v35, v83\n"
      "v_fma_f32 v25, v25, v35, v83\n"
      "v_cvt_pk_bf16_f32 v24, v24, v25\n"
      "v_fma_f32 v26, v26, v35, v83\n"
      "v_fma_f32 v27, v27, v35, v83\n"
      "v_cvt_pk_bf16_f32 v25, v26, v27\n"
      "v_fma_f32 v16, v16, v35, v83\n"
      "v_fma_f32 v17, v17, v35, v83\n"
      "v_cvt_pk_bf16_f32 v26, v16, v17\n"
      "v_fma_f32 v18, v18, v35, v83\n"
      "v_fma_f32 v19, v19, v35, v83\n"
      "v_cvt_pk_bf16_f32 v27, v18, v19\n"
      "v_mfma_f32_16x16x32_bf16 v[100:103], v[8:11], v[88:91], 0\n"
      "ds_write_b128 v95, v[24:27]\n"
      "v_mfma_f32_16x16x32_bf16 v[100:103], v[8:11], v[84:87], v[100:103]\n"
      "v_fma_f32 v20, v20, v35, v83\n"
      "v_fma_f32 v21, v21, v35, v83\n"
      "v_cvt_pk_bf16_f32 v20, v20, v21\n"
      "v_fma_f32 v22, v22, v35, v83\n"
      "v_fma_f32 v23, v23, v35, v83\n"
      "v_cvt_pk_bf16_f32 v21, v22, v23\n"
      "v_fma_f32 v72, v72, v35, v83\n"
      "v_fma_f32 v73, v73, v35, v83\n"
      "v_cvt_pk_bf16_f32 v22, v72, v73\n"
      "v_mfma_f32_16x16x32_bf16 v[16:19], v[40:43], v[88:91], 0\n"
      "v_fma_f32 v74, v74, v35, v83\n"
      "v_fma_f32 v75, v75, v35, v83\n"
      "v_mfma_f32_16x16x32_bf16 v[16:19], v[28:31], v[84:87], v[16:19]\n"
      "v_cvt_pk_bf16_f32 v23, v74, v75\n"
      "v_mov_b32 v35, 0\n"
      "v_mov_b32 v83, 0\n"
      "v_add_u32_e64 v98, s55, v1\n"
      "ds_read_b32 v98, v98\n"
      "ds_write_b128 v97, v[20:23]\n"
      "v_fma_f32 v60, v60, v105, v104\n"
      "v_fma_f32 v61, v61, v105, v104\n"
      "v_cvt_pk_bf16_f32 v60, v60, v61\n"
      "v_fma_f32 v62, v62, v105, v104\n"
      "v_fma_f32 v63, v63, v105, v104\n"
      "v_cvt_pk_bf16_f32 v61, v62, v63\n"
      "v_fma_f32 v68, v68, v105, v104\n"
      "v_fma_f32 v69, v69, v105, v104\n"
      "v_cvt_pk_bf16_f32 v62, v68, v69\n"
      "v_mfma_f32_16x16x32_bf16 v[72:75], v[36:39], v[88:91], 0\n"
      "v_fma_f32 v70, v70, v105, v104\n"
      "v_fma_f32 v71, v71, v105, v104\n"
      "v_mfma_f32_16x16x32_bf16 v[72:75], v[32:35], v[84:87], v[72:75]\n"
      "v_cvt_pk_bf16_f32 v63, v70, v71\n"
      "ds_write_b128 v95, v[60:63] offset:2048\n"
      "v_fma_f32 v12, v12, v105, v104\n"
      "v_fma_f32 v13, v13, v105, v104\n"
      "v_cvt_pk_bf16_f32 v12, v12, v13\n"
      "v_fma_f32 v14, v14, v105, v104\n"
      "v_fma_f32 v15, v15, v105, v104\n"
      "v_cvt_pk_bf16_f32 v13, v14, v15\n"
      "v_fma_f32 v64, v64, v105, v104\n"
      "v_fma_f32 v65, v65, v105, v104\n"
      "v_cvt_pk_bf16_f32 v14, v64, v65\n"
      "ds_read_b128 v[68:71], v3 offset:2048\n"
      "v_fma_f32 v66, v66, v105, v104\n"
      "v_fma_f32 v67, v67, v105, v104\n"
      "v_cvt_pk_bf16_f32 v15, v66, v67\n"
      "s_waitcnt lgkmcnt(2)\n"
      "v_mfma_f32_16x16x32_bf16 v[64:67], v[76:79], v[88:91], 0\n"
      "ds_read_b128 v[88:91], v3 offset:3072\n"
      "ds_write_b128 v97, v[12:15] offset:2048\n"
      "v_mfma_f32_16x16x32_bf16 v[64:67], v[80:83], v[84:87], v[64:67]\n"
      "v_add_u32 v85, s71, v2\n"
      "ds_read_b128 v[24:27], v85\n"
      "ds_read_b128 v[20:23], v85 offset:1024\n"
      "ds_read_b128 v[60:63], v85 offset:2048\n"
      "ds_read_b128 v[12:15], v85 offset:3072\n"
      "v_add_u32_e64 v104, s72, v1\n"
      "ds_read_b32 v85, v104 offset:1792\n"
      "ds_read_b32 v84, v104 offset:2048\n"
      "ds_read_b32 v99, v104 offset:4608\n"
      "ds_read_b32 v108, v104 offset:4864\n"
      "v_rcp_f32 v100, v100\n"
      "s_add_u32 s14, s40, s70\n"
      "v_mul_f32 v16, v100, v16\n"
      "v_mul_f32 v17, v100, v17\n"
      "v_mul_f32 v18, v100, v18\n"
      "v_mul_f32 v19, v100, v19\n"
      "v_cvt_pk_bf16_f32 v16, v16, v17\n"
      "v_cvt_pk_bf16_f32 v17, v18, v19\n"
      "ds_write_b64 v92, v[16:17] offset:24576\n"
      "ds_read_b128 v[16:19], v3 offset:4096\n"
      "v_mul_f32 v72, v100, v72\n"
      "v_mul_f32 v73, v100, v73\n"
      "v_mul_f32 v74, v100, v74\n"
      "v_mul_f32 v75, v100, v75\n"
      "v_cvt_pk_bf16_f32 v72, v72, v73\n"
      "v_cvt_pk_bf16_f32 v73, v74, v75\n"
      "ds_write_b64 v92, v[72:73] offset:25600\n"
      "s_waitcnt lgkmcnt(3)\n"
      "v_mfma_f32_16x16x32_bf16 v[104:107], v[8:11], v[68:71], 0\n"
      "v_mul_f32 v64, v100, v64\n"
      "v_mul_f32 v65, v100, v65\n"
      "v_mfma_f32_16x16x32_bf16 v[104:107], v[8:11], v[88:91], v[104:107]\n"
      "v_mul_f32 v66, v100, v66\n"
      "v_mul_f32_e64 v67, v100, v67\n"
      "v_mfma_f32_16x16x32_bf16 v[100:103], v[40:43], v[68:71], 0\n"
      "v_cvt_pk_bf16_f32 v64, v64, v65\n"
      "v_cvt_pk_bf16_f32 v65, v66, v67\n"
      "v_mfma_f32_16x16x32_bf16 v[100:103], v[28:31], v[88:91], v[100:103]\n"
      "ds_write_b64 v93, v[64:65] offset:24576\n"
      "v_mfma_f32_16x16x32_bf16 v[72:75], v[36:39], v[68:71], 0\n"
      "s_add_u32 s66, s22, s14\n"
      "s_addc_u32 s67, s23, 0\n"
      "v_mfma_f32_16x16x32_bf16 v[72:75], v[32:35], v[88:91], v[72:75]\n"
      "s_or_b32 s14, s41, s42\n"
      "s_cbranch_scc1 L_edge_67_%=\n"
      "L_eb_68_%=:\n"
      "v_xor_b32_e64 v97, 64, v95\n"
      "global_store_dwordx4 v85, v[24:27], s[66:67] sc1\n"
      "s_nop 1\n"
      "ds_read_b128 v[24:27], v3 offset:5120\n"
      "global_store_dwordx4 v84, v[20:23], s[66:67] sc1\n"
      "ds_read_b128 v[84:87], v98\n"
      "global_store_dwordx4 v99, v[60:63], s[66:67] sc1\n"
      "s_nop 1\n"
      "ds_read_b128 v[60:63], v98 offset:64\n"
      "v_mfma_f32_16x16x32_bf16 v[20:23], v[76:79], v[68:71], 0\n"
      "global_store_dwordx4 v108, v[12:15], s[66:67] sc1\n"
      "v_mfma_f32_16x16x32_bf16 v[20:23], v[80:83], v[88:91], v[20:23]\n"
      "v_fma_f32 v44, v44, v94, v96\n"
      "v_fma_f32 v45, v45, v94, v96\n"
      "v_cvt_pk_bf16_f32 v44, v44, v45\n"
      "v_fma_f32 v46, v46, v94, v96\n"
      "v_rcp_f32_e64 v104, v104\n"
      "v_fma_f32 v47, v47, v94, v96\n"
      "v_mul_f32 v100, v104, v100\n"
      "v_mul_f32 v101, v104, v101\n"
      "v_mul_f32 v102, v104, v102\n"
      "v_mul_f32 v103, v104, v103\n"
      "v_cvt_pk_bf16_f32 v100, v100, v101\n"
      "v_cvt_pk_bf16_f32 v101, v102, v103\n"
      "ds_write_b64 v92, v[100:101] offset:24832\n"
      "v_mul_f32 v72, v104, v72\n"
      "v_mul_f32_e64 v73, v104, v73\n"
      "s_waitcnt lgkmcnt(1)\n"
      "v_mfma_f32_16x16x32_bf16 v[100:103], v[8:11], v[16:19], 0\n"
      "v_mul_f32 v74, v104, v74\n"
      "v_mul_f32 v75, v104, v75\n"
      "v_mfma_f32_16x16x32_bf16 v[100:103], v[8:11], v[24:27], v[100:103]\n"
      "v_cvt_pk_bf16_f32 v72, v72, v73\n"
      "v_cvt_pk_bf16_f32 v73, v74, v75\n"
      "v_mfma_f32_16x16x32_bf16 v[64:67], v[40:43], v[16:19], 0\n"
      "ds_write_b64 v92, v[72:73] offset:25856\n"
      "v_mfma_f32_16x16x32_bf16 v[64:67], v[28:31], v[24:27], v[64:67]\n"
      "v_mul_f32 v20, v104, v20\n"
      "v_mul_f32 v21, v104, v21\n"
      "v_mfma_f32_16x16x32_bf16 v[88:91], v[36:39], v[16:19], 0\n"
      "v_mul_f32 v22, v104, v22\n"
      "v_mul_f32 v23, v104, v23\n"
      "v_mfma_f32_16x16x32_bf16 v[88:91], v[32:35], v[24:27], v[88:91]\n"
      "v_cvt_pk_bf16_f32 v20, v20, v21\n"
      "v_cvt_pk_bf16_f32 v21, v22, v23\n"
      "v_mfma_f32_16x16x32_bf16 v[68:71], v[76:79], v[16:19], 0\n"
      "ds_write_b64 v93, v[20:21] offset:24832\n"
      "v_mfma_f32_16x16x32_bf16 v[68:71], v[80:83], v[24:27], v[68:71]\n"
      "v_cvt_pk_bf16_f32 v45, v46, v47\n"
      "ds_read_b128 v[12:15], v4\n"
      "v_mfma_f32_16x16x32_bf16 v[104:107], v[84:87], v[8:11], 0\n"
      "v_fma_f32 v56, v56, v94, v96\n"
      "ds_read_b128 v[16:19], v5\n"
      "v_mfma_f32_16x16x32_bf16 v[104:107], v[60:63], v[8:11], v[104:107]\n"
      "v_fma_f32 v57, v57, v94, v96\n"
      "ds_read_b128 v[20:23], v4 offset:4096\n"
      "ds_read_b128 v[24:27], v5 offset:4096\n"
      "v_mfma_f32_16x16x32_bf16 v[72:75], v[84:87], v[40:43], 0\n"
      "v_cvt_pk_bf16_f32 v46, v56, v57\n"
      "v_fma_f32 v58, v58, v94, v96\n"
      "v_mfma_f32_16x16x32_bf16 v[72:75], v[60:63], v[28:31], v[72:75]\n"
      "v_fma_f32 v59, v59, v94, v96\n"
      "v_cvt_pk_bf16_f32 v47, v58, v59\n"
      "v_mfma_f32_16x16x32_bf16 v[56:59], v[84:87], v[36:39], 0\n"
      "ds_write_b128 v95, v[44:47]\n"
      "v_mfma_f32_16x16x32_bf16 v[56:59], v[60:63], v[32:35], v[56:59]\n"
      "v_fma_f32 v52, v52, v94, v96\n"
      "ds_read_b32 v28, v1 offset:2560\n"
      "v_mfma_f32_16x16x32_bf16 v[32:35], v[84:87], v[76:79], 0\n"
      "v_fma_f32 v53, v53, v94, v96\n"
      "v_cvt_pk_bf16_f32 v52, v52, v53\n"
      "v_mfma_f32_16x16x32_bf16 v[32:35], v[60:63], v[80:83], v[32:35]\n"
      "v_fma_f32 v54, v54, v94, v96\n"
      "v_fma_f32 v55, v55, v94, v96\n"
      "v_cvt_pk_bf16_f32 v53, v54, v55\n"
      "v_fma_f32 v48, v48, v94, v96\n"
      "v_fma_f32 v49, v49, v94, v96\n"
      "v_rcp_f32 v100, v100\n"
      "v_cvt_pk_bf16_f32 v54, v48, v49\n"
      "v_mul_f32 v64, v100, v64\n"
      "v_mul_f32 v65, v100, v65\n"
      "v_mul_f32 v66, v100, v66\n"
      "v_mul_f32_e64 v67, v100, v67\n"
      "v_cvt_pk_bf16_f32 v64, v64, v65\n"
      "v_cvt_pk_bf16_f32 v65, v66, v67\n"
      "ds_write_b64 v92, v[64:65] offset:12544\n"
      "v_mul_f32 v88, v100, v88\n"
      "v_mul_f32 v89, v100, v89\n"
      "v_mul_f32 v90, v100, v90\n"
      "v_mul_f32 v91, v100, v91\n"
      "v_cvt_pk_bf16_f32 v88, v88, v89\n"
      "v_cvt_pk_bf16_f32 v89, v90, v91\n"
      "ds_write_b64 v92, v[88:89] offset:13568\n"
      "v_mul_f32 v68, v100, v68\n"
      "v_mul_f32 v69, v100, v69\n"
      "v_mul_f32_e64 v70, v100, v70\n"
      "v_mul_f32_e64 v71, v100, v71\n"
      "v_cvt_pk_bf16_f32 v68, v68, v69\n"
      "v_cvt_pk_bf16_f32 v69, v70, v71\n"
      "ds_write_b64 v93, v[68:69] offset:12544\n"
      "v_fma_f32 v50, v50, v94, v96\n"
      "v_fma_f32 v51, v51, v94, v96\n"
      "v_cvt_pk_bf16_f32 v55, v50, v51\n"
      "ds_write_b128 v97, v[52:55]\n"
      "v_add_u32_e64 v30, s71, v2\n"
      "ds_read_b128 v[44:47], v30\n"
      "ds_read_b128 v[52:55], v30 offset:1024\n"
      "v_add_u32_e64 v31, s72, v1\n"
      "ds_read_b32 v30, v31 offset:2560\n"
      "ds_read_b32 v29, v31 offset:5376\n"
      "s_add_u32 s14, s40, s70\n"
      "s_add_u32 s66, s22, s14\n"
      "s_addc_u32 s67, s23, 0\n"
      "s_or_b32 s14, s41, s42\n"
      "v_cndmask_b32 v72, v72, v56, s[56:57]\n"
      "v_cndmask_b32 v72, v72, v32, s[58:59]\n"
      "v_mov_b32_dpp v104, v104 quad_perm:[0,0,0,0] row_mask:0xf bank_mask:0xf\n"
      "v_rcp_f32 v104, v104\n"
      "s_nop 0\n"
      "v_mul_f32 v72, v104, v72\n"
      "v_cvt_pk_bf16_f32 v72, v72, v72\n"
      "ds_write_b16 v7, v72 offset:384\n"
      "s_waitcnt lgkmcnt(1)\n"
      "s_cbranch_scc1 L_edge_69_%=\n"
      "L_eb_70_%=:\n"
      "s_mov_b32 s49, s43\n"
      "global_store_dwordx4 v30, v[44:47], s[66:67] sc1\n"
      "global_store_dwordx4 v29, v[52:55], s[66:67] sc1\n"
      "s_mov_b32 s50, s44\n"
      "s_mov_b32 s51, s45\n"
      "s_mov_b32 s40, s52\n"
      "s_mov_b32 s41, s53\n"
      "s_mov_b32 s42, s54\n"
      "s_mov_b32 s43, s0\n"
      "s_mov_b32 s44, s1\n"
      "s_mov_b32 s45, s2\n"
      "s_add_u32 s94, s94, 768\n"
      "v_add_u32 v29, s43, v0\n"
      "s_or_b32 s14, s44, s45\n"
      "s_cbranch_scc1 L_edge_71_%=\n"
      "L_eb_72_%=:\n"
      "s_cmp_lt_u32 s27, 9\n"
      "s_cselect_b64 exec, -1, 0\n"
      "s_add_u32 m0, s33, 24576\n"
      "s_nop 0\n"
      "global_load_lds_dwordx4 v29, s[16:17]\n"
      "s_add_u32 m0, s33, 28544\n"
      "s_nop 0\n"
      "global_load_lds_dwordx4 v29, s[16:17] offset:128\n"
      "s_add_u32 m0, s33, 32512\n"
      "s_nop 0\n"
      "global_load_lds_dwordx4 v29, s[16:17] offset:256\n"
      "s_mov_b64 exec, -1\n"
      "v_add_u32_e64 v29, s43, v28\n"
      "s_or_b32 s14, s44, s45\n"
      "s_cbranch_scc1 L_edge_73_%=\n"
      "L_eb_74_%=:\n"
      "s_cmp_lt_u32 s27, 9\n"
      "s_cselect_b64 exec, -1, 0\n"
      "s_add_u32 m0, s33, 12288\n"
      "s_nop 0\n"
      "global_load_lds_dwordx4 v29, s[16:17]\n"
      "s_add_u32 m0, s33, 16256\n"
      "s_nop 0\n"
      "global_load_lds_dwordx4 v29, s[16:17] offset:128\n"
      "s_add_u32 m0, s33, 20224\n"
      "s_nop 0\n"
      "global_load_lds_dwordx4 v29, s[16:17] offset:256\n"
      "s_mov_b64 exec, -1\n"
      "s_waitcnt lgkmcnt(0)\n"
      "s_barrier\n"
      "s_add_u32 s27, s27, 1\n"
      "s_sub_u32 s8, 91648, s8\n"
      "s_cmp_ge_u32 s27, 10\n"
      "s_cbranch_scc1 L_B1tail_%=\n"
      "s_setprio 1\n"
      "v_mfma_f32_16x16x32_bf16 v[36:39], v[12:15], a[0:3], 0\n"
      "s_add_u32 s28, s28, 256\n"
      "s_lshl_b32 s10, s28, 1\n"
      "s_add_u32 s11, s10, 1\n"
      "s_add_u32 s14, s25, 5120\n"
      "s_min_u32 s14, s14, 5328\n"
      "s_cmp_lt_u32 s27, 9\n"
      "s_cselect_b32 s10, s10, s14\n"
      "s_cselect_b32 s11, s11, s14\n"
      "s_mul_hi_u32 s29, s10, 58835169\n"
      "s_mul_i32 s30, s29, 73\n"
      "s_sub_u32 s30, s10, s30\n"
      "s_mul_i32 s52, s29, 1373568\n"
      "s_mul_i32 s31, s30, 2688\n"
      "s_add_u32 s52, s52, s31\n"
      "s_add_u32 s52, s52, 589824\n"
      "s_cmp_eq_u32 s29, 72\n"
      "s_cselect_b32 s53, 100270464, 0\n"
      "s_cmp_eq_u32 s30, 72\n"
      "s_cselect_b32 s54, 196224, 0\n"
      "s_mul_hi_u32 s29, s11, 58835169\n"
      "s_mul_i32 s30, s29, 73\n"
      "s_sub_u32 s30, s11, s30\n"
      "s_mul_i32 s0, s29, 1373568\n"
      "s_mul_i32 s31, s30, 2688\n"
      "s_add_u32 s0, s0, s31\n"
      "s_add_u32 s0, s0, 589824\n"
      "s_cmp_eq_u32 s29, 72\n"
      "s_cselect_b32 s1, 100270464, 0\n"
      "s_cmp_eq_u32 s30, 72\n"
      "s_cselect_b32 s2, 196224, 0\n"
      "v_mfma_f32_16x16x32_bf16 v[36:39], v[16:19], a[8:11], v[36:39]\n"
      "v_mfma_f32_16x16x32_bf16 v[40:43], v[12:15], a[4:7], 0\n"
      "v_mfma_f32_16x16x32_bf16 v[40:43], v[16:19], a[12:15], v[40:43]\n"
      "ds_read_b128 v[12:15], v4 offset:8192\n"
      "ds_read_b128 v[16:19], v5 offset:8192\n"
      "v_mfma_f32_16x16x32_bf16 v[36:39], v[20:23], a[16:19], v[36:39]\n"
      "v_mfma_f32_16x16x32_bf16 v[36:39], v[24:27], a[24:27], v[36:39]\n"
      "v_mfma_f32_16x16x32_bf16 v[40:43], v[20:23], a[20:23], v[40:43]\n"
      "s_lshl_b32 s14, s27, 1\n"
      "s_add_u32 s14, s14, 1\n"
      "s_lshl_b32 s29, s25, 5\n"
      "s_add_u32 s29, s29, s14\n"
      "s_mul_i32 s29, s29, 1152\n"
      "s_add_u32 s29, s29, s95\n"
      "s_add_u32 s29, s29, 1011712\n"
      "s_add_u32 s34, s20, s29\n"
      "s_addc_u32 s35, s21, 0\n"
      "s_add_u32 m0, s95, 144512\n"
      "s_cmp_eq_u32 s95, 0\n"
      "s_cselect_b32 exec_lo, -1, 0xff\n"
      "s_cselect_b32 exec_hi, -1, 0\n"
      "global_load_lds_dwordx4 v2, s[34:35]\n"
      "s_mov_b64 exec, -1\n"
      "v_mfma_f32_16x16x32_bf16 v[40:43], v[24:27], a[28:31], v[40:43]\n"
      "ds_read_b128 v[20:23], v4 offset:2048\n"
      "ds_read_b128 v[24:27], v5 offset:2048\n"
      "s_waitcnt lgkmcnt(2)\n"
      "v_mfma_f32_16x16x32_bf16 v[36:39], v[12:15], a[32:35], v[36:39]\n"
      "v_mfma_f32_16x16x32_bf16 v[36:39], v[16:19], a[40:43], v[36:39]\n"
      "v_mfma_f32_16x16x32_bf16 v[40:43], v[12:15], a[36:39], v[40:43]\n"
      "s_mul_i32 s31, s10, 4802\n"
      "s_add_u32 s76, s18, s31\n"
      "s_addc_u32 s77, s19, 0\n"
      "s_and_b32 s76, s76, -16\n"
      "s_sub_u32 s78, s32, s76\n"
      "s_add_u32 s31, s95, -36864\n"
      "v_add_u32 v12, s31, v2\n"
      "v_min_u32 v12, s78, v12\n"
      "s_add_u32 m0, s8, 82944\n"
      "s_add_u32 m0, m0, s95\n"
      "s_nop 0\n"
      "global_load_lds_dwordx4 v12, s[76:77]\n"
      "v_mfma_f32_16x16x32_bf16 v[40:43], v[16:19], a[44:47], v[40:43]\n"
      "ds_read_b128 v[16:19], v4 offset:6144\n"
      "ds_read_b128 v[12:15], v5 offset:6144\n"
      "s_waitcnt lgkmcnt(2)\n"
      "v_mfma_f32_16x16x32_bf16 v[28:31], v[20:23], a[0:3], 0\n"
      "v_mfma_f32_16x16x32_bf16 v[28:31], v[24:27], a[8:11], v[28:31]\n"
      "v_mfma_f32_16x16x32_bf16 v[32:35], v[20:23], a[4:7], 0\n"
      "s_add_u32 s31, s95, -34816\n"
      "v_add_u32_e64 v20, s31, v2\n"
      "v_min_u32 v20, s78, v20\n"
      "s_add_u32 m0, s8, 84992\n"
      "s_add_u32 m0, m0, s95\n"
      "s_nop 0\n"
      "global_load_lds_dwordx4 v20, s[76:77]\n"
      "v_mfma_f32_16x16x32_bf16 v[32:35], v[24:27], a[12:15], v[32:35]\n"
      "ds_read_b128 v[24:27], v4 offset:10240\n"
      "ds_read_b128 v[20:23], v5 offset:10240\n"
      "s_waitcnt lgkmcnt(2)\n"
      "v_mfma_f32_16x16x32_bf16 v[28:31], v[16:19], a[16:19], v[28:31]\n"
      "v_mfma_f32_16x16x32_bf16 v[28:31], v[12:15], a[24:27], v[28:31]\n"
      "v_mfma_f32_16x16x32_bf16 v[32:35], v[16:19], a[20:23], v[32:35]\n"
      "s_add_u32 s31, s95, -32768\n"
      "v_add_u32_e64 v16, s31, v2\n"
      "v_min_u32 v16, s78, v16\n"
      "s_add_u32 m0, s8, 87040\n"
      "s_add_u32 m0, m0, s95\n"
      "s_mov_b64 exec, s[90:91]\n"
      "global_load_lds_dwordx4 v16, s[76:77]\n"
      "s_mov_b64 exec, -1\n"
      "v_mfma_f32_16x16x32_bf16 v[32:35], v[12:15], a[28:31], v[32:35]\n"
      "ds_read_b128 v[56:59], v6 offset:24576\n"
      "ds_read_b128 v[48:51], v6 offset:26624\n"
      "s_waitcnt lgkmcnt(2)\n"
      "v_mfma_f32_16x16x32_bf16 v[28:31], v[24:27], a[32:35], v[28:31]\n"
      "v_mfma_f32_16x16x32_bf16 v[28:31], v[20:23], a[40:43], v[28:31]\n"
      "v_mfma_f32_16x16x32_bf16 v[32:35], v[24:27], a[36:39], v[32:35]\n"
      "v_mfma_f32_16x16x32_bf16 v[32:35], v[20:23], a[44:47], v[32:35]\n"
      "ds_read_b128 v[16:19], v6 offset:28672\n"
      "ds_read_b128 v[12:15], v6 offset:30720\n"
      "s_waitcnt lgkmcnt(2)\n"
      "v_mfma_f32_16x16x32_bf16 v[44:47], a[48:51], v[56:59], 0\n"
      "v_mfma_f32_16x16x32_bf16 v[44:47], a[56:59], v[48:51], v[44:47]\n"
      "v_mfma_f32_16x16x32_bf16 v[20:23], a[52:55], v[56:59], 0\n"
      "v_mfma_f32_16x16x32_bf16 v[20:23], a[60:63], v[48:51], v[20:23]\n"
      "v_mfma_f32_16x16x32_bf16 v[24:27], a[96:99], v[56:59], 0\n"
      "v_cvt_pk_bf16_f32 v36, v36, v37\n"
      "v_mfma_f32_16x16x32_bf16 v[24:27], a[104:107], v[48:51], v[24:27]\n"
      "v_mfma_f32_16x16x32_bf16 v[52:55], a[100:103], v[56:59], 0\n"
      "v_cvt_pk_bf16_f32 v37, v38, v39\n"
      "v_mfma_f32_16x16x32_bf16 v[52:55], a[108:111], v[48:51], v[52:55]\n"
      "ds_read_b128 v[48:51], v6 offset:32768\n"
      "ds_read_b128 v[56:59], v6 offset:34816\n"
      "s_waitcnt lgkmcnt(2)\n"
      "v_mfma_f32_16x16x32_bf16 v[44:47], a[64:67], v[16:19], v[44:47]\n"
      "v_mfma_f32_16x16x32_bf16 v[44:47], a[72:75], v[12:15], v[44:47]\n"
      "v_cvt_pk_bf16_f32 v38, v28, v29\n"
      "v_mfma_f32_16x16x32_bf16 v[20:23], a[68:71], v[16:19], v[20:23]\n"
      "v_mfma_f32_16x16x32_bf16 v[20:23], a[76:79], v[12:15], v[20:23]\n"
      "v_cvt_pk_bf16_f32 v39, v30, v31\n"
      "v_mfma_f32_16x16x32_bf16 v[24:27], a[112:115], v[16:19], v[24:27]\n"
      "v_mfma_f32_16x16x32_bf16 v[24:27], a[120:123], v[12:15], v[24:27]\n"
      "v_mfma_f32_16x16x32_bf16 v[52:55], a[116:119], v[16:19], v[52:55]\n"
      "v_cvt_pk_bf16_f32 v40, v40, v41\n"
      "v_mfma_f32_16x16x32_bf16 v[52:55], a[124:127], v[12:15], v[52:55]\n"
      "ds_read_b128 v[60:63], v6 offset:24832\n"
      "ds_read_b128 v[12:15], v6 offset:26880\n"
      "s_waitcnt lgkmcnt(2)\n"
      "v_mfma_f32_16x16x32_bf16 v[44:47], a[80:83], v[48:51], v[44:47]\n"
      "v_cvt_pk_bf16_f32 v41, v42, v43\n"
      "v_mfma_f32_16x16x32_bf16 v[44:47], a[88:91], v[56:59], v[44:47]\n"
      "v_mfma_f32_16x16x32_bf16 v[20:23], a[84:87], v[48:51], v[20:23]\n"
      "v_mfma_f32_16x16x32_bf16 v[20:23], a[92:95], v[56:59], v[20:23]\n"
      "v_cvt_pk_bf16_f32 v42, v32, v33\n"
      "v_mfma_f32_16x16x32_bf16 v[24:27], a[128:131], v[48:51], v[24:27]\n"
      "v_mfma_f32_16x16x32_bf16 v[24:27], a[136:139], v[56:59], v[24:27]\n"
      "v_cvt_pk_bf16_f32 v43, v34, v35\n"
      "v_mfma_f32_16x16x32_bf16 v[52:55], a[132:135], v[48:51], v[52:55]\n"
      "v_mfma_f32_16x16x32_bf16 v[52:55], a[140:143], v[56:59], v[52:55]\n"
      "ds_read_b128 v[48:51], v6 offset:28928\n"
      "ds_read_b128 v[16:19], v6 offset:30976\n"
      "s_waitcnt lgkmcnt(2)\n"
      "v_mfma_f32_16x16x32_bf16 v[28:31], a[48:51], v[60:63], 0\n"
      "v_mfma_f32_16x16x32_bf16 v[28:31], a[56:59], v[12:15], v[28:31]\n"
      "v_mfma_f32_16x16x32_bf16 v[56:59], a[52:55], v[60:63], 0\n"
      "v_mfma_f32_16x16x32_bf16 v[56:59], a[60:63], v[12:15], v[56:59]\n"
      "v_mfma_f32_16x16x32_bf16 v[32:35], a[96:99], v[60:63], 0\n"
      "v_add_f32 v92, v44, v45\n"
      "v_mul_f32_e64 v93, v44, v44\n"
      "v_mfma_f32_16x16x32_bf16 v[32:35], a[104:107], v[12:15], v[32:35]\n"
      "v_fmac_f32 v93, v45, v45\n"
      "v_add_f32 v92, v92, v46\n"
      "v_mfma_f32_16x16x32_bf16 v[64:67], a[100:103], v[60:63], 0\n"
      "v_fmac_f32_e64 v93, v46, v46\n"
      "v_mfma_f32_16x16x32_bf16 v[64:67], a[108:111], v[12:15], v[64:67]\n"
      "ds_read_b128 v[60:63], v6 offset:33024\n"
      "ds_read_b128 v[12:15], v6 offset:35072\n"
      "v_add_f32 v92, v92, v47\n"
      "v_fmac_f32 v93, v47, v47\n"
      "s_waitcnt lgkmcnt(2)\n"
      "v_mfma_f32_16x16x32_bf16 v[28:31], a[64:67], v[48:51], v[28:31]\n"
      "v_add_f32 v92, v92, v20\n"
      "v_fmac_f32 v93, v20, v20\n"
      "v_mfma_f32_16x16x32_bf16 v[28:31], a[72:75], v[16:19], v[28:31]\n"
      "v_add_f32 v92, v92, v21\n"
      "v_mfma_f32_16x16x32_bf16 v[56:59], a[68:71], v[48:51], v[56:59]\n"
      "v_fmac_f32 v93, v21, v21\n"
      "v_add_f32 v92, v92, v22\n"
      "v_mfma_f32_16x16x32_bf16 v[56:59], a[76:79], v[16:19], v[56:59]\n"
      "v_fmac_f32 v93, v22, v22\n"
      "v_add_f32 v92, v92, v23\n"
      "v_mfma_f32_16x16x32_bf16 v[32:35], a[112:115], v[48:51], v[32:35]\n"
      "v_fmac_f32 v93, v23, v23\n"
      "v_mfma_f32_16x16x32_bf16 v[32:35], a[120:123], v[16:19], v[32:35]\n"
      "v_add_f32 v92, v92, v24\n"
      "v_fmac_f32_e64 v93, v24, v24\n"
      "v_mfma_f32_16x16x32_bf16 v[64:67], a[116:119], v[48:51], v[64:67]\n"
      "v_add_f32 v92, v92, v25\n"
      "v_fmac_f32 v93, v25, v25\n"
      "v_mfma_f32_16x16x32_bf16 v[64:67], a[124:127], v[16:19], v[64:67]\n"
      "ds_read_b128 v[48:51], v6 offset:12544\n"
      "ds_read_b128 v[16:19], v6 offset:14592\n"
      "v_add_f32 v92, v92, v26\n"
      "s_waitcnt lgkmcnt(2)\n"
      "v_mfma_f32_16x16x32_bf16 v[28:31], a[80:83], v[60:63], v[28:31]\n"
      "v_fmac_f32 v93, v26, v26\n"
      "v_add_f32 v92, v92, v27\n"
      "v_mfma_f32_16x16x32_bf16 v[28:31], a[88:91], v[12:15], v[28:31]\n"
      "v_fmac_f32 v93, v27, v27\n"
      "v_add_f32 v92, v92, v52\n"
      "v_mfma_f32_16x16x32_bf16 v[56:59], a[84:87], v[60:63], v[56:59]\n"
      "v_fmac_f32_e64 v93, v52, v52\n"
      "v_mfma_f32_16x16x32_bf16 v[56:59], a[92:95], v[12:15], v[56:59]\n"
      "v_add_f32 v92, v92, v53\n"
      "v_fmac_f32 v93, v53, v53\n"
      "v_mfma_f32_16x16x32_bf16 v[32:35], a[128:131], v[60:63], v[32:35]\n"
      "v_add_f32 v92, v92, v54\n"
      "v_fmac_f32 v93, v54, v54\n"
      "v_mfma_f32_16x16x32_bf16 v[32:35], a[136:139], v[12:15], v[32:35]\n"
      "v_add_f32 v92, v92, v55\n"
      "v_mfma_f32_16x16x32_bf16 v[64:67], a[132:135], v[60:63], v[64:67]\n"
      "v_fmac_f32 v93, v55, v55\n"
      "s_nop 1\n"
      "v_permlane16_swap_b32 v92, v93\n"
      "v_mfma_f32_16x16x32_bf16 v[64:67], a[140:143], v[12:15], v[64:67]\n"
      "ds_read_b128 v[12:15], v6 offset:16640\n"
      "ds_read_b128 v[72:75], v6 offset:18688\n"
      "v_add_f32 v92, v92, v93\n"
      "s_waitcnt lgkmcnt(2)\n"
      "v_mfma_f32_16x16x32_bf16 v[68:71], a[48:51], v[48:51], 0\n"
      "v_mfma_f32_16x16x32_bf16 v[68:71], a[56:59], v[16:19], v[68:71]\n"
      "v_mfma_f32_16x16x32_bf16 v[76:79], a[52:55], v[48:51], 0\n"
      "v_mfma_f32_16x16x32_bf16 v[76:79], a[60:63], v[16:19], v[76:79]\n"
      "v_mfma_f32_16x16x32_bf16 v[60:63], a[96:99], v[48:51], 0\n"
      "v_add_f32 v93, v28, v29\n"
      "v_mul_f32 v94, v28, v28\n"
      "v_mfma_f32_16x16x32_bf16 v[60:63], a[104:107], v[16:19], v[60:63]\n"
      "v_fmac_f32 v94, v29, v29\n"
      "v_add_f32 v93, v93, v30\n"
      "v_mfma_f32_16x16x32_bf16 v[80:83], a[100:103], v[48:51], 0\n"
      "v_fmac_f32 v94, v30, v30\n"
      "v_add_f32 v93, v93, v31\n"
      "v_mfma_f32_16x16x32_bf16 v[80:83], a[108:111], v[16:19], v[80:83]\n"
      "ds_read_b128 v[16:19], v6 offset:20736\n"
      "ds_read_b128 v[48:51], v6 offset:22784\n"
      "v_fmac_f32 v94, v31, v31\n"
      "v_add_f32 v93, v93, v56\n"
      "s_waitcnt lgkmcnt(2)\n"
      "v_mfma_f32_16x16x32_bf16 v[68:71], a[64:67], v[12:15], v[68:71]\n"
      "v_fmac_f32 v94, v56, v56\n"
      "v_add_f32 v93, v93, v57\n"
      "v_mfma_f32_16x16x32_bf16 v[68:71], a[72:75], v[72:75], v[68:71]\n"
      "v_fmac_f32 v94, v57, v57\n"
      "v_add_f32 v93, v93, v58\n"
      "v_mfma_f32_16x16x32_bf16 v[76:79], a[68:71], v[12:15], v[76:79]\n"
      "v_fmac_f32 v94, v58, v58\n"
      "v_mfma_f32_16x16x32_bf16 v[76:79], a[76:79], v[72:75], v[76:79]\n"
      "v_add_f32 v93, v93, v59\n"
      "v_fmac_f32 v94, v59, v59\n"
      "v_mfma_f32_16x16x32_bf16 v[60:63], a[112:115], v[12:15], v[60:63]\n"
      "v_add_f32 v93, v93, v32\n"
      "v_fmac_f32 v94, v32, v32\n"
      "v_mfma_f32_16x16x32_bf16 v[60:63], a[120:123], v[72:75], v[60:63]\n"
      "v_add_f32 v93, v93, v33\n"
      "v_fmac_f32 v94, v33, v33\n"
      "v_mfma_f32_16x16x32_bf16 v[80:83], a[116:119], v[12:15], v[80:83]\n"
      "v_add_f32 v93, v93, v34\n"
      "v_fmac_f32 v94, v34, v34\n"
      "v_mfma_f32_16x16x32_bf16 v[80:83], a[124:127], v[72:75], v[80:83]\n"
      "ds_read_b128 v[84:87], v4 offset:12288\n"
      "ds_read_b128 v[72:75], v5 offset:12288\n"
      "v_add_f32 v93, v93, v35\n"
      "v_fmac_f32 v94, v35, v35\n"
      "s_waitcnt lgkmcnt(2)\n"
      "v_mfma_f32_16x16x32_bf16 v[68:71], a[80:83], v[16:19], v[68:71]\n"
      "v_add_f32 v93, v93, v64\n"
      "v_fmac_f32 v94, v64, v64\n"
      "v_mfma_f32_16x16x32_bf16 v[68:71], a[88:91], v[48:51], v[68:71]\n"
      "v_add_f32 v93, v93, v65\n"
      "v_mfma_f32_16x16x32_bf16 v[76:79], a[84:87], v[16:19], v[76:79]\n"
      "v_fmac_f32 v94, v65, v65\n"
      "v_add_f32 v93, v93, v66\n"
      "v_mfma_f32_16x16x32_bf16 v[76:79], a[92:95], v[48:51], v[76:79]\n"
      "v_fmac_f32 v94, v66, v66\n"
      "v_add_f32 v93, v93, v67\n"
      "v_mfma_f32_16x16x32_bf16 v[60:63], a[128:131], v[16:19], v[60:63]\n"
      "v_fmac_f32 v94, v67, v67\n"
      "s_nop 1\n"
      "v_permlane16_swap_b32 v93, v94\n"
      "v_mfma_f32_16x16x32_bf16 v[60:63], a[136:139], v[48:51], v[60:63]\n"
      "v_add_f32 v93, v93, v94\n"
      "ds_read_b32 v12, v1 offset:1280\n"
      "v_mfma_f32_16x16x32_bf16 v[80:83], a[132:135], v[16:19], v[80:83]\n"
      "v_permlane32_swap_b32 v92, v93\n"
      "v_add_f32_e64 v92, v92, v93\n"
      "v_mfma_f32_16x16x32_bf16 v[80:83], a[140:143], v[48:51], v[80:83]\n"
      "ds_read_b128 v[48:51], v4 offset:16384\n"
      "ds_read_b128 v[88:91], v5 offset:16384\n"
      "s_waitcnt lgkmcnt(2)\n"
      "ds_write_b32 v12, v92\n"
      "v_mfma_f32_16x16x32_bf16 v[12:15], v[84:87], a[0:3], 0\n"
      "v_mfma_f32_16x16x32_bf16 v[12:15], v[72:75], a[8:11], v[12:15]\n"
      "v_mfma_f32_16x16x32_bf16 v[16:19], v[84:87], a[4:7], 0\n"
      "v_mfma_f32_16x16x32_bf16 v[16:19], v[72:75], a[12:15], v[16:19]\n"
      "ds_read_b128 v[72:75], v4 offset:20480\n"
      "ds_read_b128 v[84:87], v5 offset:20480\n"
      "s_waitcnt lgkmcnt(2)\n"
      "v_mfma_f32_16x16x32_bf16 v[12:15], v[48:51], a[16:19], v[12:15]\n"
      "v_add_f32 v92, v68, v69\n"
      "v_mul_f32 v93, v68, v68\n"
      "v_fmac_f32 v93, v69, v69\n"
      "v_add_f32 v92, v92, v70\n"
      "v_fmac_f32 v93, v70, v70\n"
      "v_mfma_f32_16x16x32_bf16 v[12:15], v[88:91], a[24:27], v[12:15]\n"
      "v_add_f32 v92, v92, v71\n"
      "v_fmac_f32 v93, v71, v71\n"
      "v_add_f32 v92, v92, v76\n"
      "v_fmac_f32 v93, v76, v76\n"
      "v_add_f32 v92, v92, v77\n"
      "v_mfma_f32_16x16x32_bf16 v[16:19], v[48:51], a[20:23], v[16:19]\n"
      "v_fmac_f32 v93, v77, v77\n"
      "s_setprio 0\n"
      "v_add_f32 v92, v92, v78\n"
      "v_fmac_f32 v93, v78, v78\n"
      "v_add_f32 v92, v92, v79\n"
      "v_fmac_f32 v93, v79, v79\n"
      "v_mfma_f32_16x16x32_bf16 v[16:19], v[88:91], a[28:31], v[16:19]\n"
      "v_add_f32 v92, v92, v60\n"
      "v_fmac_f32 v93, v60, v60\n"
      "v_add_f32 v92, v92, v61\n"
      "v_fmac_f32 v93, v61, v61\n"
      "v_add_f32 v92, v92, v62\n"
      "s_waitcnt lgkmcnt(0)\n"
      "v_mfma_f32_16x16x32_bf16 v[12:15], v[72:75], a[32:35], v[12:15]\n"
      "v_fmac_f32 v93, v62, v62\n"
      "v_add_f32 v92, v92, v63\n"
      "v_fmac_f32 v93, v63, v63\n"
      "v_add_f32 v92, v92, v80\n"
      "v_fmac_f32 v93, v80, v80\n"
      "v_mfma_f32_16x16x32_bf16 v[12:15], v[84:87], a[40:43], v[12:15]\n"
      "v_add_f32 v92, v92, v81\n"
      "v_fmac_f32 v93, v81, v81\n"
      "v_add_f32 v92, v92, v82\n"
      "v_fmac_f32 v93, v82, v82\n"
      "v_add_f32 v92, v92, v83\n"
      "v_mfma_f32_16x16x32_bf16 v[16:19], v[72:75], a[36:39], v[16:19]\n"
      "v_fmac_f32 v93, v83, v83\n"
      "s_nop 1\n"
      "v_permlane16_swap_b32 v92, v93\n"
      "v_add_f32_e64 v92, v92, v93\n"
      "ds_read_b32 v74, v1 offset:1280\n"
      "v_mov_b32 v93, v92\n"
      "v_mfma_f32_16x16x32_bf16 v[16:19], v[84:87], a[44:47], v[16:19]\n"
      "s_nop 0\n"
      "v_permlane32_swap_b32_e64 v92, v93\n"
      "v_add_f32 v92, v92, v93\n"
      "s_waitcnt lgkmcnt(0)\n"
      "ds_write_b32 v74, v92 offset:512\n"
      "ds_read_b32 v7, v1 offset:512\n"
      "ds_read_b32 v74, v1 offset:768\n"
      "v_cvt_pk_bf16_f32 v12, v12, v13\n"
      "v_cvt_pk_bf16_f32 v13, v14, v15\n"
      "v_cvt_pk_bf16_f32 v16, v16, v17\n"
      "v_cvt_pk_bf16_f32 v17, v18, v19\n"
      "s_waitcnt vmcnt(4)\n"
      "s_waitcnt lgkmcnt(0)\n"
      "s_barrier\n"
      "ds_read_b32 v104, v1\n"
      "ds_read_b128 v[48:51], v3\n"
      "ds_read_b128 v[88:91], v3 offset:1024\n"
      "ds_read_u16 v14, v74\n"
      "ds_read_u16 v18, v74 offset:32\n"
      "ds_read_b32 v105, v1 offset:1024\n"
      "s_setprio 1\n"
      "v_add_u32 v72, 108800, v2\n"
      "ds_read_b128 v[84:87], v72\n"
      "ds_read_b128 v[92:95], v72 offset:1024\n"
      "v_add_u32_e64 v15, s63, v2\n"
      "ds_read_b128 v[96:99], v15\n"
      "ds_read_b64 v[72:73], v15 offset:1024\n"
      "v_add_u32 v15, s92, v74\n"
      "ds_read_u16 v74, v15\n"
      "v_add_u32 v106, s62, v7\n"
      "s_waitcnt lgkmcnt(4)\n"
      "v_add_f32 v84, v84, v86\n"
      "v_add_f32 v85, v85, v87\n"
      "s_waitcnt lgkmcnt(3)\n"
      "v_add_f32 v92, v92, v94\n"
      "v_add_f32 v93, v93, v95\n"
      "v_add_f32 v84, v84, v92\n"
      "v_add_f32 v85, v85, v93\n"
      "v_fma_f32 v92, -v84, v84, s74\n"
      "v_fmac_f32 v92, 0x43400000, v85\n"
      "v_max_f32 v92, s74, v92\n"
      "v_rsq_f32 v92, v92\n"
      "v_xor_b32_e64 v87, 64, v104\n"
      "v_mul_f32 v75, 0x43400000, v92\n"
      "v_mul_f32 v19, -v84, v92\n"
      "v_mov_b32 v107, v75\n"
      "v_mov_b32 v108, v19\n"
      "v_mov_b32 v15, 0\n"
      "v_permlane32_swap_b32 v75, v107\n"
      "v_permlane32_swap_b32 v19, v108\n"
      "v_mov_b32 v85, v75\n"
      "v_mov_b32 v84, v19\n"
      "s_nop 0\n"
      "v_permlane16_swap_b32 v75, v85\n"
      "v_permlane16_swap_b32 v19, v84\n"
      "v_fma_f32 v44, v44, v75, v19\n"
      "v_fma_f32 v45, v45, v75, v19\n"
      "v_cvt_pk_bf16_f32 v44, v44, v45\n"
      "v_fma_f32 v46, v46, v75, v19\n"
      "v_fma_f32 v47, v47, v75, v19\n"
      "v_cvt_pk_bf16_f32 v45, v46, v47\n"
      "v_fma_f32 v20, v20, v75, v19\n"
      "v_fma_f32 v21, v21, v75, v19\n"
      "v_cvt_pk_bf16_f32 v46, v20, v21\n"
      "v_fma_f32 v22, v22, v75, v19\n"
      "v_fma_f32 v23, v23, v75, v19\n"
      "v_cvt_pk_bf16_f32 v47, v22, v23\n"
      "v_mfma_f32_16x16x32_bf16 v[20:23], v[8:11], v[48:51], 0\n"
      "ds_write_b128 v104, v[44:47] offset:24576\n"
      "v_mfma_f32_16x16x32_bf16 v[20:23], v[8:11], v[88:91], v[20:23]\n"
      "v_fma_f32 v24, v24, v75, v19\n"
      "v_fma_f32 v25, v25, v75, v19\n"
      "v_cvt_pk_bf16_f32 v24, v24, v25\n"
      "v_fma_f32 v26, v26, v75, v19\n"
      "v_fma_f32 v27, v27, v75, v19\n"
      "v_cvt_pk_bf16_f32 v25, v26, v27\n"
      "v_fma_f32 v52, v52, v75, v19\n"
      "v_fma_f32 v53, v53, v75, v19\n"
      "v_cvt_pk_bf16_f32 v26, v52, v53\n"
      "v_mfma_f32_16x16x32_bf16 v[92:95], v[36:39], v[48:51], 0\n"
      "v_fma_f32 v54, v54, v75, v19\n"
      "v_fma_f32 v55, v55, v75, v19\n"
      "v_mfma_f32_16x16x32_bf16 v[92:95], v[12:15], v[88:91], v[92:95]\n"
      "v_cvt_pk_bf16_f32 v27, v54, v55\n"
      "v_mov_b32 v19, 0\n"
      "v_mov_b32 v75, 0\n"
      "v_add_u32_e64 v86, s55, v1\n"
      "ds_read_b32 v86, v86\n"
      "ds_write_b128 v87, v[24:27] offset:24576\n"
      "v_fma_f32 v28, v28, v85, v84\n"
      "v_fma_f32 v29, v29, v85, v84\n"
      "v_cvt_pk_bf16_f32 v28, v28, v29\n"
      "v_fma_f32 v30, v30, v85, v84\n"
      "v_fma_f32 v31, v31, v85, v84\n"
      "v_cvt_pk_bf16_f32 v29, v30, v31\n"
      "v_fma_f32 v56, v56, v85, v84\n"
      "v_fma_f32 v57, v57, v85, v84\n"
      "v_cvt_pk_bf16_f32 v30, v56, v57\n"
      "v_mfma_f32_16x16x32_bf16 v[52:55], v[40:43], v[48:51], 0\n"
      "v_fma_f32 v58, v58, v85, v84\n"
      "v_fma_f32 v59, v59, v85, v84\n"
      "v_mfma_f32_16x16x32_bf16 v[52:55], v[16:19], v[88:91], v[52:55]\n"
      "v_cvt_pk_bf16_f32 v31, v58, v59\n"
      "ds_write_b128 v104, v[28:31] offset:26624\n"
      "v_fma_f32 v32, v32, v85, v84\n"
      "v_fma_f32 v33, v33, v85, v84\n"
      "v_cvt_pk_bf16_f32 v32, v32, v33\n"
      "v_fma_f32 v34, v34, v85, v84\n"
      "v_fma_f32 v35, v35, v85, v84\n"
      "v_cvt_pk_bf16_f32 v33, v34, v35\n"
      "v_fma_f32 v64, v64, v85, v84\n"
      "v_fma_f32 v65, v65, v85, v84\n"
      "v_cvt_pk_bf16_f32 v34, v64, v65\n"
      "ds_read_b128 v[56:59], v3 offset:2048\n"
      "v_fma_f32 v66, v66, v85, v84\n"
      "v_fma_f32 v67, v67, v85, v84\n"
      "v_cvt_pk_bf16_f32 v35, v66, v67\n"
      "s_waitcnt lgkmcnt(2)\n"
      "v_mfma_f32_16x16x32_bf16 v[64:67], v[96:99], v[48:51], 0\n"
      "ds_read_b128 v[48:51], v3 offset:3072\n"
      "ds_write_b128 v87, v[32:35] offset:26624\n"
      "v_mfma_f32_16x16x32_bf16 v[64:67], v[72:75], v[88:91], v[64:67]\n"
      "v_add_u32 v88, s71, v2\n"
      "ds_read_b128 v[44:47], v88 offset:24576\n"
      "ds_read_b128 v[24:27], v88 offset:25600\n"
      "ds_read_b128 v[28:31], v88 offset:26624\n"
      "ds_read_b128 v[32:35], v88 offset:27648\n"
      "v_add_u32_e64 v21, s72, v1\n"
      "ds_read_b32 v89, v21 offset:1792\n"
      "ds_read_b32 v88, v21 offset:2048\n"
      "ds_read_b32 v87, v21 offset:4608\n"
      "ds_read_b32 v109, v21 offset:4864\n"
      "v_rcp_f32 v20, v20\n"
      "s_add_u32 s14, s49, s70\n"
      "v_mul_f32 v92, v20, v92\n"
      "v_mul_f32 v93, v20, v93\n"
      "v_mul_f32 v94, v20, v94\n"
      "v_mul_f32 v95, v20, v95\n"
      "v_cvt_pk_bf16_f32 v92, v92, v93\n"
      "v_cvt_pk_bf16_f32 v93, v94, v95\n"
      "ds_write_b64 v7, v[92:93]\n"
      "ds_read_b128 v[92:95], v3 offset:4096\n"
      "v_mul_f32 v52, v20, v52\n"
      "v_mul_f32 v53, v20, v53\n"
      "v_mul_f32 v54, v20, v54\n"
      "v_mul_f32 v55, v20, v55\n"
      "v_cvt_pk_bf16_f32 v52, v52, v53\n"
      "v_cvt_pk_bf16_f32 v53, v54, v55\n"
      "ds_write_b64 v7, v[52:53] offset:1024\n"
      "s_waitcnt lgkmcnt(3)\n"
      "v_mfma_f32_16x16x32_bf16 v[52:55], v[8:11], v[56:59], 0\n"
      "v_mul_f32 v64, v20, v64\n"
      "v_mul_f32 v65, v20, v65\n"
      "v_mfma_f32_16x16x32_bf16 v[52:55], v[8:11], v[48:51], v[52:55]\n"
      "v_mul_f32 v66, v20, v66\n"
      "v_mul_f32_e64 v67, v20, v67\n"
      "v_mfma_f32_16x16x32_bf16 v[20:23], v[36:39], v[56:59], 0\n"
      "v_cvt_pk_bf16_f32 v64, v64, v65\n"
      "v_cvt_pk_bf16_f32 v65, v66, v67\n"
      "v_mfma_f32_16x16x32_bf16 v[20:23], v[12:15], v[48:51], v[20:23]\n"
      "ds_write_b64 v106, v[64:65]\n"
      "v_mfma_f32_16x16x32_bf16 v[64:67], v[40:43], v[56:59], 0\n"
      "s_add_u32 s66, s22, s14\n"
      "s_addc_u32 s67, s23, 0\n"
      "v_mfma_f32_16x16x32_bf16 v[64:67], v[16:19], v[48:51], v[64:67]\n"
      "s_or_b32 s14, s50, s51\n"
      "s_cbranch_scc1 L_edge_75_%=\n"
      "L_eb_76_%=:\n"
      "v_xor_b32 v53, 64, v104\n"
      "global_store_dwordx4 v89, v[44:47], s[66:67] sc1\n"
      "s_nop 1\n"
      "ds_read_b128 v[44:47], v3 offset:5120\n"
      "global_store_dwordx4 v88, v[24:27], s[66:67] sc1\n"
      "ds_read_b128 v[88:91], v86\n"
      "global_store_dwordx4 v87, v[28:31], s[66:67] sc1\n"
      "ds_read_b128 v[100:103], v86 offset:64\n"
      "v_mfma_f32_16x16x32_bf16 v[84:87], v[96:99], v[56:59], 0\n"
      "global_store_dwordx4 v109, v[32:35], s[66:67] sc1\n"
      "v_mfma_f32_16x16x32_bf16 v[84:87], v[72:75], v[48:51], v[84:87]\n"
      "v_fma_f32 v68, v68, v107, v108\n"
      "v_fma_f32 v69, v69, v107, v108\n"
      "v_cvt_pk_bf16_f32 v68, v68, v69\n"
      "v_fma_f32 v70, v70, v107, v108\n"
      "v_rcp_f32 v52, v52\n"
      "v_fma_f32 v71, v71, v107, v108\n"
      "v_mul_f32 v20, v52, v20\n"
      "v_mul_f32 v21, v52, v21\n"
      "v_mul_f32 v22, v52, v22\n"
      "v_mul_f32 v23, v52, v23\n"
      "v_cvt_pk_bf16_f32 v20, v20, v21\n"
      "v_cvt_pk_bf16_f32 v21, v22, v23\n"
      "ds_write_b64 v7, v[20:21] offset:256\n"
      "v_mul_f32 v64, v52, v64\n"
      "v_mul_f32 v65, v52, v65\n"
      "s_waitcnt lgkmcnt(1)\n"
      "v_mfma_f32_16x16x32_bf16 v[56:59], v[8:11], v[92:95], 0\n"
      "v_mul_f32 v66, v52, v66\n"
      "v_mul_f32 v67, v52, v67\n"
      "v_mfma_f32_16x16x32_bf16 v[56:59], v[8:11], v[44:47], v[56:59]\n"
      "v_cvt_pk_bf16_f32 v64, v64, v65\n"
      "v_cvt_pk_bf16_f32 v65, v66, v67\n"
      "v_mfma_f32_16x16x32_bf16 v[48:51], v[36:39], v[92:95], 0\n"
      "ds_write_b64 v7, v[64:65] offset:1280\n"
      "v_mfma_f32_16x16x32_bf16 v[48:51], v[12:15], v[44:47], v[48:51]\n"
      "v_mul_f32 v84, v52, v84\n"
      "v_mul_f32 v85, v52, v85\n"
      "ds_read_b128 v[20:23], v4 offset:24576\n"
      "v_mfma_f32_16x16x32_bf16 v[64:67], v[40:43], v[92:95], 0\n"
      "v_mul_f32 v86, v52, v86\n"
      "ds_read_b128 v[24:27], v5 offset:24576\n"
      "v_mfma_f32_16x16x32_bf16 v[64:67], v[16:19], v[44:47], v[64:67]\n"
      "v_mul_f32 v87, v52, v87\n"
      "ds_read_b128 v[28:31], v4 offset:28672\n"
      "v_cvt_pk_bf16_f32 v84, v84, v85\n"
      "v_cvt_pk_bf16_f32 v85, v86, v87\n"
      "ds_write_b64 v106, v[84:85] offset:256\n"
      "ds_read_b128 v[32:35], v5 offset:28672\n"
      "v_cvt_pk_bf16_f32 v69, v70, v71\n"
      "v_fma_f32 v76, v76, v107, v108\n"
      "v_fma_f32 v77, v77, v107, v108\n"
      "v_cvt_pk_bf16_f32 v70, v76, v77\n"
      "v_fma_f32 v78, v78, v107, v108\n"
      "v_fma_f32 v79, v79, v107, v108\n"
      "v_cvt_pk_bf16_f32 v71, v78, v79\n"
      "v_mfma_f32_16x16x32_bf16 v[76:79], v[96:99], v[92:95], 0\n"
      "ds_write_b128 v104, v[68:71] offset:24576\n"
      "v_mfma_f32_16x16x32_bf16 v[76:79], v[72:75], v[44:47], v[76:79]\n"
      "v_fma_f32 v60, v60, v107, v108\n"
      "v_fma_f32 v61, v61, v107, v108\n"
      "v_mfma_f32_16x16x32_bf16 v[92:95], v[88:91], v[8:11], 0\n"
      "v_cvt_pk_bf16_f32 v60, v60, v61\n"
      "v_fma_f32 v62, v62, v107, v108\n"
      "v_mfma_f32_16x16x32_bf16 v[92:95], v[100:103], v[8:11], v[92:95]\n"
      "v_fma_f32 v63, v63, v107, v108\n"
      "v_cvt_pk_bf16_f32 v61, v62, v63\n"
      "v_mfma_f32_16x16x32_bf16 v[44:47], v[88:91], v[36:39], 0\n"
      "v_fma_f32 v80, v80, v107, v108\n"
      "v_fma_f32 v81, v81, v107, v108\n"
      "v_mfma_f32_16x16x32_bf16 v[44:47], v[100:103], v[12:15], v[44:47]\n"
      "v_rcp_f32_e64 v56, v56\n"
      "v_mfma_f32_16x16x32_bf16 v[12:15], v[88:91], v[40:43], 0\n"
      "v_mul_f32 v48, v56, v48\n"
      "v_mul_f32 v49, v56, v49\n"
      "v_mfma_f32_16x16x32_bf16 v[12:15], v[100:103], v[16:19], v[12:15]\n"
      "v_mul_f32 v50, v56, v50\n"
      "v_mul_f32 v51, v56, v51\n"
      "v_mfma_f32_16x16x32_bf16 v[16:19], v[88:91], v[96:99], 0\n"
      "v_cvt_pk_bf16_f32 v48, v48, v49\n"
      "v_cvt_pk_bf16_f32 v49, v50, v51\n"
      "v_mfma_f32_16x16x32_bf16 v[16:19], v[100:103], v[72:75], v[16:19]\n"
      "ds_write_b64 v7, v[48:49] offset:12288\n"
      "v_mul_f32 v64, v56, v64\n"
      "v_mul_f32 v65, v56, v65\n"
      "v_mul_f32 v66, v56, v66\n"
      "v_mul_f32 v67, v56, v67\n"
      "v_cvt_pk_bf16_f32 v64, v64, v65\n"
      "v_cvt_pk_bf16_f32 v65, v66, v67\n"
      "ds_write_b64 v7, v[64:65] offset:13312\n"
      "v_mul_f32 v76, v56, v76\n"
      "v_mul_f32 v77, v56, v77\n"
      "v_mul_f32 v78, v56, v78\n"
      "v_mul_f32 v79, v56, v79\n"
      "v_cvt_pk_bf16_f32 v76, v76, v77\n"
      "v_cvt_pk_bf16_f32 v77, v78, v79\n"
      "ds_write_b64 v106, v[76:77] offset:12288\n"
      "v_cvt_pk_bf16_f32 v62, v80, v81\n"
      "v_fma_f32 v82, v82, v107, v108\n"
      "v_fma_f32 v83, v83, v107, v108\n"
      "v_cvt_pk_bf16_f32 v63, v82, v83\n"
      "ds_write_b128 v53, v[60:63] offset:24576\n"
      "v_add_u32 v17, s71, v2\n"
      "ds_read_b128 v[68:71], v17 offset:24576\n"
      "ds_read_b128 v[60:63], v17 offset:25600\n"
      "v_add_u32 v17, s72, v1\n"
      "ds_read_b32 v18, v17 offset:2560\n"
      "ds_read_b32 v19, v17 offset:5376\n"
      "s_add_u32 s14, s49, s70\n"
      "s_add_u32 s66, s22, s14\n"
      "s_addc_u32 s67, s23, 0\n"
      "s_or_b32 s14, s50, s51\n"
      "v_cndmask_b32 v44, v44, v12, s[56:57]\n"
      "v_cndmask_b32 v44, v44, v16, s[58:59]\n"
      "v_mov_b32_dpp v92, v92 quad_perm:[0,0,0,0] row_mask:0xf bank_mask:0xf\n"
      "v_rcp_f32 v92, v92\n"
      "s_nop 0\n"
      "v_mul_f32_e64 v44, v92, v44\n"
      "v_cvt_pk_bf16_f32 v44, v44, v44\n"
      "ds_write_b16 v105, v44\n"
      "s_waitcnt lgkmcnt(1)\n"
      "s_cbranch_scc1 L_edge_77_%=\n"
      "L_eb_78_%=:\n"
      "global_store_dwordx4 v18, v[68:71], s[66:67] sc1\n"
      "global_store_dwordx4 v19, v[60:63], s[66:67] sc1\n"
      "s_waitcnt vmcnt(9)\n"
      "v_add_u32_e64 v16, s52, v0\n"
      "s_or_b32 s14, s53, s54\n"
      "s_cbranch_scc1 L_edge_79_%=\n"
      "L_eb_80_%=:\n"
      "s_add_u32 m0, s33, 0\n"
      "s_nop 0\n"
      "global_load_lds_dwordx4 v16, s[16:17]\n"
      "s_add_u32 m0, s33, 3968\n"
      "s_nop 0\n"
      "global_load_lds_dwordx4 v16, s[16:17] offset:128\n"
      "s_add_u32 m0, s33, 7936\n"
      "s_nop 0\n"
      "global_load_lds_dwordx4 v16, s[16:17] offset:256\n"
      "s_waitcnt lgkmcnt(0)\n"
      "s_barrier\n"
      "s_branch L_B1loop_%=\n"
      "L_B1tail_%=:\n"
      "s_cmp_ge_u32 s25, 209\n"
      "s_cbranch_scc1 L_B1drain_%=\n"
      "s_nop 7\n"
      "s_nop 7\n"
      "s_setprio 1\n"
      "v_mfma_f32_16x16x32_bf16 v[32:35], v[12:15], a[0:3], 0\n"
      "v_mfma_f32_16x16x32_bf16 v[32:35], v[16:19], a[8:11], v[32:35]\n"
      "v_mfma_f32_16x16x32_bf16 v[28:31], v[12:15], a[4:7], 0\n"
      "v_mfma_f32_16x16x32_bf16 v[28:31], v[16:19], a[12:15], v[28:31]\n"
      "ds_read_b128 v[12:15], v4 offset:8192\n"
      "ds_read_b128 v[16:19], v5 offset:8192\n"
      "v_mfma_f32_16x16x32_bf16 v[32:35], v[20:23], a[16:19], v[32:35]\n"
      "v_mfma_f32_16x16x32_bf16 v[32:35], v[24:27], a[24:27], v[32:35]\n"
      "v_mfma_f32_16x16x32_bf16 v[28:31], v[20:23], a[20:23], v[28:31]\n"
      "v_subrev_u32 v21, 36864, v2\n"
      "v_lshrrev_b32 v20, 9, v21\n"
      "v_bfe_u32 v21, v21, 4, 5\n"
      "v_lshlrev_b32 v20, 4, v20\n"
      "v_mul_u32_u24 v7, 0x180, v21\n"
      "v_add_u32 v20, v20, v7\n"
      "s_mul_i32 s29, s24, 96\n"
      "s_sub_u32 s29, s29, 384\n"
      "v_add_u32 v20, s29, v20\n"
      "v_lshrrev_b32 v22, 1, v21\n"
      "v_lshlrev_b32 v22, 9, v22\n"
      "v_and_b32 v7, 1, v21\n"
      "v_add_u32 v22, v22, v7\n"
      "s_lshl_b32 s29, s25, 1\n"
      "v_add_u32 v22, s29, v22\n"
      "s_add_u32 s29, s25, 5120\n"
      "s_min_u32 s29, s29, 5328\n"
      "v_mov_b32 v7, s29\n"
      "v_cmp_gt_u32 vcc, 20, v21\n"
      "s_nop 1\n"
      "v_cndmask_b32 v22, v7, v22, vcc\n"
      "s_mov_b32 s29, 58835169\n"
      "v_mul_hi_u32 v7, v22, s29\n"
      "v_mul_u32_u24 v21, 73, v7\n"
      "v_sub_u32 v21, v22, v21\n"
      "v_mul_u32_u24 v7, 7, v7\n"
      "v_add_u32 v7, 9, v7\n"
      "v_subrev_u32 v22, 0x1ff, v7\n"
      "v_min_u32 v7, v7, v22\n"
      "v_mul_u32_u24 v21, 7, v21\n"
      "v_add_u32 v21, 9, v21\n"
      "v_subrev_u32 v22, 0x1ff, v21\n"
      "v_min_u32 v21, v21, v22\n"
      "v_mul_u32_u24 v7, 0x1ff, v7\n"
      "v_add_u32 v7, v7, v21\n"
      "v_mul_u32_u24 v7, 0x180, v7\n"
      "s_mul_i32 s29, s25, 12288\n"
      "s_add_u32 s29, s29, 12582912\n"
      "s_add_u32 s34, s20, s29\n"
      "s_addc_u32 s35, s21, 0\n"
      "s_mul_i32 s30, s33, 3\n"
      "s_nop 0\n"
      "s_mov_b32 exec_lo, 0x3ffff\n"
      "s_mov_b32 exec_hi, 0x3ffff\n"
      "s_add_u32 m0, s30, 24576\n"
      "s_nop 0\n"
      "global_load_lds_dwordx4 v20, s[34:35]\n"
      "s_add_u32 m0, s30, 25568\n"
      "s_nop 0\n"
      "global_load_lds_dwordx4 v20, s[34:35] offset:32\n"
      "s_add_u32 m0, s30, 26560\n"
      "s_nop 0\n"
      "global_load_lds_dwordx4 v20, s[34:35] offset:64\n"
      "s_mov_b64 exec, -1\n"
      "v_mfma_f32_16x16x32_bf16 v[28:31], v[24:27], a[28:31], v[28:31]\n"
      "ds_read_b128 v[20:23], v4 offset:2048\n"
      "ds_read_b128 v[24:27], v5 offset:2048\n"
      "s_waitcnt lgkmcnt(2)\n"
      "v_mfma_f32_16x16x32_bf16 v[32:35], v[12:15], a[32:35], v[32:35]\n"
      "v_mfma_f32_16x16x32_bf16 v[32:35], v[16:19], a[40:43], v[32:35]\n"
      "v_mfma_f32_16x16x32_bf16 v[28:31], v[12:15], a[36:39], v[28:31]\n"
      "v_mfma_f32_16x16x32_bf16 v[28:31], v[16:19], a[44:47], v[28:31]\n"
      "ds_read_b128 v[48:51], v4 offset:6144\n"
      "ds_read_b128 v[92:95], v5 offset:6144\n"
      "s_waitcnt lgkmcnt(2)\n"
      "v_mfma_f32_16x16x32_bf16 v[12:15], v[20:23], a[0:3], 0\n"
      "v_mfma_f32_16x16x32_bf16 v[12:15], v[24:27], a[8:11], v[12:15]\n"
      "v_mfma_f32_16x16x32_bf16 v[16:19], v[20:23], a[4:7], 0\n"
      "v_mfma_f32_16x16x32_bf16 v[16:19], v[24:27], a[12:15], v[16:19]\n"
      "ds_read_b128 v[24:27], v4 offset:10240\n"
      "ds_read_b128 v[20:23], v5 offset:10240\n"
      "s_waitcnt lgkmcnt(2)\n"
      "v_mfma_f32_16x16x32_bf16 v[12:15], v[48:51], a[16:19], v[12:15]\n"
      "v_mfma_f32_16x16x32_bf16 v[12:15], v[92:95], a[24:27], v[12:15]\n"
      "v_mfma_f32_16x16x32_bf16 v[16:19], v[48:51], a[20:23], v[16:19]\n"
      "v_mfma_f32_16x16x32_bf16 v[16:19], v[92:95], a[28:31], v[16:19]\n"
      "ds_read_b128 v[92:95], v6 offset:24576\n"
      "ds_read_b128 v[48:51], v6 offset:26624\n"
      "s_waitcnt lgkmcnt(2)\n"
      "v_mfma_f32_16x16x32_bf16 v[12:15], v[24:27], a[32:35], v[12:15]\n"
      "v_mfma_f32_16x16x32_bf16 v[12:15], v[20:23], a[40:43], v[12:15]\n"
      "v_mfma_f32_16x16x32_bf16 v[16:19], v[24:27], a[36:39], v[16:19]\n"
      "v_mfma_f32_16x16x32_bf16 v[16:19], v[20:23], a[44:47], v[16:19]\n"
      "ds_read_b128 v[64:67], v6 offset:28672\n"
      "ds_read_b128 v[44:47], v6 offset:30720\n"
      "s_waitcnt lgkmcnt(2)\n"
      "v_mfma_f32_16x16x32_bf16 v[36:39], a[48:51], v[92:95], 0\n"
      "v_mfma_f32_16x16x32_bf16 v[36:39], a[56:59], v[48:51], v[36:39]\n"
      "v_mfma_f32_16x16x32_bf16 v[24:27], a[52:55], v[92:95], 0\n"
      "v_mfma_f32_16x16x32_bf16 v[24:27], a[60:63], v[48:51], v[24:27]\n"
      "v_mfma_f32_16x16x32_bf16 v[60:63], a[96:99], v[92:95], 0\n"
      "v_cvt_pk_bf16_f32 v32, v32, v33\n"
      "v_mfma_f32_16x16x32_bf16 v[60:63], a[104:107], v[48:51], v[60:63]\n"
      "v_mfma_f32_16x16x32_bf16 v[20:23], a[100:103], v[92:95], 0\n"
      "v_cvt_pk_bf16_f32 v33, v34, v35\n"
      "v_mfma_f32_16x16x32_bf16 v[20:23], a[108:111], v[48:51], v[20:23]\n"
      "ds_read_b128 v[48:51], v6 offset:32768\n"
      "ds_read_b128 v[92:95], v6 offset:34816\n"
      "s_waitcnt lgkmcnt(2)\n"
      "v_mfma_f32_16x16x32_bf16 v[36:39], a[64:67], v[64:67], v[36:39]\n"
      "v_mfma_f32_16x16x32_bf16 v[36:39], a[72:75], v[44:47], v[36:39]\n"
      "v_cvt_pk_bf16_f32 v34, v12, v13\n"
      "v_mfma_f32_16x16x32_bf16 v[24:27], a[68:71], v[64:67], v[24:27]\n"
      "v_mfma_f32_16x16x32_bf16 v[24:27], a[76:79], v[44:47], v[24:27]\n"
      "v_cvt_pk_bf16_f32 v35, v14, v15\n"
      "v_mfma_f32_16x16x32_bf16 v[60:63], a[112:115], v[64:67], v[60:63]\n"
      "v_mfma_f32_16x16x32_bf16 v[60:63], a[120:123], v[44:47], v[60:63]\n"
      "v_mfma_f32_16x16x32_bf16 v[20:23], a[116:119], v[64:67], v[20:23]\n"
      "v_cvt_pk_bf16_f32 v28, v28, v29\n"
      "v_mfma_f32_16x16x32_bf16 v[20:23], a[124:127], v[44:47], v[20:23]\n"
      "ds_read_b128 v[64:67], v6 offset:24832\n"
      "ds_read_b128 v[40:43], v6 offset:26880\n"
      "s_waitcnt lgkmcnt(2)\n"
      "v_mfma_f32_16x16x32_bf16 v[36:39], a[80:83], v[48:51], v[36:39]\n"
      "v_cvt_pk_bf16_f32 v29, v30, v31\n"
      "v_mfma_f32_16x16x32_bf16 v[36:39], a[88:91], v[92:95], v[36:39]\n"
      "v_mfma_f32_16x16x32_bf16 v[24:27], a[84:87], v[48:51], v[24:27]\n"
      "v_mfma_f32_16x16x32_bf16 v[24:27], a[92:95], v[92:95], v[24:27]\n"
      "v_cvt_pk_bf16_f32 v30, v16, v17\n"
      "v_mfma_f32_16x16x32_bf16 v[60:63], a[128:131], v[48:51], v[60:63]\n"
      "v_mfma_f32_16x16x32_bf16 v[60:63], a[136:139], v[92:95], v[60:63]\n"
      "v_cvt_pk_bf16_f32 v31, v18, v19\n"
      "v_mfma_f32_16x16x32_bf16 v[20:23], a[132:135], v[48:51], v[20:23]\n"
      "v_mfma_f32_16x16x32_bf16 v[20:23], a[140:143], v[92:95], v[20:23]\n"
      "ds_read_b128 v[44:47], v6 offset:28928\n"
      "ds_read_b128 v[92:95], v6 offset:30976\n"
      "s_waitcnt lgkmcnt(2)\n"
      "v_mfma_f32_16x16x32_bf16 v[48:51], a[48:51], v[64:67], 0\n"
      "v_mfma_f32_16x16x32_bf16 v[48:51], a[56:59], v[40:43], v[48:51]\n"
      "v_mfma_f32_16x16x32_bf16 v[80:83], a[52:55], v[64:67], 0\n"
      "v_mfma_f32_16x16x32_bf16 v[80:83], a[60:63], v[40:43], v[80:83]\n"
      "v_mfma_f32_16x16x32_bf16 v[76:79], a[96:99], v[64:67], 0\n"
      "v_add_f32 v12, v36, v37\n"
      "v_mul_f32 v13, v36, v36\n"
      "v_mfma_f32_16x16x32_bf16 v[76:79], a[104:107], v[40:43], v[76:79]\n"
      "v_fmac_f32 v13, v37, v37\n"
      "v_add_f32 v12, v12, v38\n"
      "v_mfma_f32_16x16x32_bf16 v[52:55], a[100:103], v[64:67], 0\n"
      "v_fmac_f32_e64 v13, v38, v38\n"
      "v_mfma_f32_16x16x32_bf16 v[52:55], a[108:111], v[40:43], v[52:55]\n"
      "ds_read_b128 v[16:19], v6 offset:33024\n"
      "ds_read_b128 v[64:67], v6 offset:35072\n"
      "v_add_f32 v12, v12, v39\n"
      "v_fmac_f32 v13, v39, v39\n"
      "s_waitcnt lgkmcnt(2)\n"
      "v_mfma_f32_16x16x32_bf16 v[48:51], a[64:67], v[44:47], v[48:51]\n"
      "v_add_f32 v12, v12, v24\n"
      "v_fmac_f32 v13, v24, v24\n"
      "v_mfma_f32_16x16x32_bf16 v[48:51], a[72:75], v[92:95], v[48:51]\n"
      "v_add_f32 v12, v12, v25\n"
      "v_mfma_f32_16x16x32_bf16 v[80:83], a[68:71], v[44:47], v[80:83]\n"
      "v_fmac_f32 v13, v25, v25\n"
      "v_add_f32 v12, v12, v26\n"
      "v_mfma_f32_16x16x32_bf16 v[80:83], a[76:79], v[92:95], v[80:83]\n"
      "v_fmac_f32 v13, v26, v26\n"
      "v_add_f32 v12, v12, v27\n"
      "v_mfma_f32_16x16x32_bf16 v[76:79], a[112:115], v[44:47], v[76:79]\n"
      "v_fmac_f32_e64 v13, v27, v27\n"
      "v_mfma_f32_16x16x32_bf16 v[76:79], a[120:123], v[92:95], v[76:79]\n"
      "v_add_f32 v12, v12, v60\n"
      "v_fmac_f32 v13, v60, v60\n"
      "v_mfma_f32_16x16x32_bf16 v[52:55], a[116:119], v[44:47], v[52:55]\n"
      "v_add_f32 v12, v12, v61\n"
      "v_fmac_f32 v13, v61, v61\n"
      "v_mfma_f32_16x16x32_bf16 v[52:55], a[124:127], v[92:95], v[52:55]\n"
      "ds_read_b128 v[92:95], v6 offset:12544\n"
      "ds_read_b128 v[44:47], v6 offset:14592\n"
      "v_add_f32 v12, v12, v62\n"
      "s_waitcnt lgkmcnt(2)\n"
      "v_mfma_f32_16x16x32_bf16 v[48:51], a[80:83], v[16:19], v[48:51]\n"
      "v_fmac_f32 v13, v62, v62\n"
      "v_add_f32 v12, v12, v63\n"
      "v_mfma_f32_16x16x32_bf16 v[48:51], a[88:91], v[64:67], v[48:51]\n"
      "v_fmac_f32 v13, v63, v63\n"
      "v_add_f32 v12, v12, v20\n"
      "v_mfma_f32_16x16x32_bf16 v[80:83], a[84:87], v[16:19], v[80:83]\n"
      "v_fmac_f32 v13, v20, v20\n"
      "v_mfma_f32_16x16x32_bf16 v[80:83], a[92:95], v[64:67], v[80:83]\n"
      "v_add_f32 v12, v12, v21\n"
      "v_fmac_f32 v13, v21, v21\n"
      "v_mfma_f32_16x16x32_bf16 v[76:79], a[128:131], v[16:19], v[76:79]\n"
      "v_add_f32 v12, v12, v22\n"
      "v_fmac_f32 v13, v22, v22\n"
      "v_mfma_f32_16x16x32_bf16 v[76:79], a[136:139], v[64:67], v[76:79]\n"
      "v_add_f32 v12, v12, v23\n"
      "v_mfma_f32_16x16x32_bf16 v[52:55], a[132:135], v[16:19], v[52:55]\n"
      "v_fmac_f32 v13, v23, v23\n"
      "s_nop 1\n"
      "v_permlane16_swap_b32_e64 v12, v13\n"
      "v_mfma_f32_16x16x32_bf16 v[52:55], a[140:143], v[64:67], v[52:55]\n"
      "ds_read_b128 v[40:43], v6 offset:16640\n"
      "ds_read_b128 v[104:107], v6 offset:18688\n"
      "v_add_f32 v12, v12, v13\n"
      "s_waitcnt lgkmcnt(2)\n"
      "v_mfma_f32_16x16x32_bf16 v[64:67], a[48:51], v[92:95], 0\n"
      "v_mfma_f32_16x16x32_bf16 v[64:67], a[56:59], v[44:47], v[64:67]\n"
      "v_mfma_f32_16x16x32_bf16 v[72:75], a[52:55], v[92:95], 0\n"
      "v_mfma_f32_16x16x32_bf16 v[72:75], a[60:63], v[44:47], v[72:75]\n"
      "v_mfma_f32_16x16x32_bf16 v[56:59], a[96:99], v[92:95], 0\n"
      "v_add_f32 v13, v48, v49\n"
      "v_mul_f32 v14, v48, v48\n"
      "v_mfma_f32_16x16x32_bf16 v[56:59], a[104:107], v[44:47], v[56:59]\n"
      "v_fmac_f32 v14, v49, v49\n"
      "v_add_f32 v13, v13, v50\n"
      "v_mfma_f32_16x16x32_bf16 v[68:71], a[100:103], v[92:95], 0\n"
      "v_fmac_f32 v14, v50, v50\n"
      "v_add_f32 v13, v13, v51\n"
      "v_mfma_f32_16x16x32_bf16 v[68:71], a[108:111], v[44:47], v[68:71]\n"
      "ds_read_b128 v[16:19], v6 offset:20736\n"
      "ds_read_b128 v[44:47], v6 offset:22784\n"
      "v_fmac_f32 v14, v51, v51\n"
      "v_add_f32 v13, v13, v80\n"
      "s_waitcnt lgkmcnt(2)\n"
      "v_mfma_f32_16x16x32_bf16 v[64:67], a[64:67], v[40:43], v[64:67]\n"
      "v_fmac_f32 v14, v80, v80\n"
      "v_add_f32 v13, v13, v81\n"
      "v_mfma_f32_16x16x32_bf16 v[64:67], a[72:75], v[104:107], v[64:67]\n"
      "v_fmac_f32 v14, v81, v81\n"
      "v_add_f32 v13, v13, v82\n"
      "v_mfma_f32_16x16x32_bf16 v[72:75], a[68:71], v[40:43], v[72:75]\n"
      "v_fmac_f32 v14, v82, v82\n"
      "v_mfma_f32_16x16x32_bf16 v[72:75], a[76:79], v[104:107], v[72:75]\n"
      "v_add_f32 v13, v13, v83\n"
      "v_fmac_f32 v14, v83, v83\n"
      "v_mfma_f32_16x16x32_bf16 v[56:59], a[112:115], v[40:43], v[56:59]\n"
      "v_add_f32 v13, v13, v76\n"
      "v_fmac_f32 v14, v76, v76\n"
      "v_mfma_f32_16x16x32_bf16 v[56:59], a[120:123], v[104:107], v[56:59]\n"
      "v_add_f32 v13, v13, v77\n"
      "v_fmac_f32 v14, v77, v77\n"
      "v_mfma_f32_16x16x32_bf16 v[68:71], a[116:119], v[40:43], v[68:71]\n"
      "v_add_f32 v13, v13, v78\n"
      "v_fmac_f32 v14, v78, v78\n"
      "v_mfma_f32_16x16x32_bf16 v[68:71], a[124:127], v[104:107], v[68:71]\n"
      "ds_read_b128 v[92:95], v4 offset:12288\n"
      "ds_read_b128 v[104:107], v5 offset:12288\n"
      "v_add_f32 v13, v13, v79\n"
      "v_fmac_f32 v14, v79, v79\n"
      "s_waitcnt lgkmcnt(2)\n"
      "v_mfma_f32_16x16x32_bf16 v[64:67], a[80:83], v[16:19], v[64:67]\n"
      "v_add_f32 v13, v13, v52\n"
      "v_fmac_f32 v14, v52, v52\n"
      "v_mfma_f32_16x16x32_bf16 v[64:67], a[88:91], v[44:47], v[64:67]\n"
      "v_add_f32 v13, v13, v53\n"
      "v_mfma_f32_16x16x32_bf16 v[72:75], a[84:87], v[16:19], v[72:75]\n"
      "v_fmac_f32 v14, v53, v53\n"
      "v_add_f32 v13, v13, v54\n"
      "v_mfma_f32_16x16x32_bf16 v[72:75], a[92:95], v[44:47], v[72:75]\n"
      "v_fmac_f32 v14, v54, v54\n"
      "v_add_f32 v13, v13, v55\n"
      "v_mfma_f32_16x16x32_bf16 v[56:59], a[128:131], v[16:19], v[56:59]\n"
      "v_fmac_f32 v14, v55, v55\n"
      "s_nop 1\n"
      "v_permlane16_swap_b32 v13, v14\n"
      "v_mfma_f32_16x16x32_bf16 v[56:59], a[136:139], v[44:47], v[56:59]\n"
      "v_add_f32 v13, v13, v14\n"
      "ds_read_b32 v14, v1 offset:1280\n"
      "v_mfma_f32_16x16x32_bf16 v[68:71], a[132:135], v[16:19], v[68:71]\n"
      "v_permlane32_swap_b32 v12, v13\n"
      "v_add_f32 v12, v12, v13\n"
      "v_mfma_f32_16x16x32_bf16 v[68:71], a[140:143], v[44:47], v[68:71]\n"
      "ds_read_b128 v[100:103], v4 offset:16384\n"
      "ds_read_b128 v[40:43], v5 offset:16384\n"
      "s_waitcnt lgkmcnt(2)\n"
      "ds_write_b32 v14, v12\n"
      "v_mfma_f32_16x16x32_bf16 v[12:15], v[92:95], a[0:3], 0\n"
      "v_mfma_f32_16x16x32_bf16 v[12:15], v[104:107], a[8:11], v[12:15]\n"
      "v_mfma_f32_16x16x32_bf16 v[16:19], v[92:95], a[4:7], 0\n"
      "v_mfma_f32_16x16x32_bf16 v[16:19], v[104:107], a[12:15], v[16:19]\n"
      "ds_read_b128 v[104:107], v4 offset:20480\n"
      "ds_read_b128 v[44:47], v5 offset:20480\n"
      "s_waitcnt lgkmcnt(2)\n"
      "v_mfma_f32_16x16x32_bf16 v[12:15], v[100:103], a[16:19], v[12:15]\n"
      "v_add_f32 v92, v64, v65\n"
      "v_mul_f32 v93, v64, v64\n"
      "v_fmac_f32 v93, v65, v65\n"
      "v_add_f32 v92, v92, v66\n"
      "v_fmac_f32 v93, v66, v66\n"
      "v_add_f32 v92, v92, v67\n"
      "v_mfma_f32_16x16x32_bf16 v[12:15], v[40:43], a[24:27], v[12:15]\n"
      "v_fmac_f32 v93, v67, v67\n"
      "v_add_f32 v92, v92, v72\n"
      "v_fmac_f32 v93, v72, v72\n"
      "v_add_f32 v92, v92, v73\n"
      "v_fmac_f32_e64 v93, v73, v73\n"
      "v_mfma_f32_16x16x32_bf16 v[16:19], v[100:103], a[20:23], v[16:19]\n"
      "v_add_f32 v92, v92, v74\n"
      "v_fmac_f32 v93, v74, v74\n"
      "v_add_f32 v92, v92, v75\n"
      "s_setprio 0\n"
      "v_fmac_f32 v93, v75, v75\n"
      "v_add_f32 v92, v92, v56\n"
      "v_mfma_f32_16x16x32_bf16 v[16:19], v[40:43], a[28:31], v[16:19]\n"
      "v_fmac_f32 v93, v56, v56\n"
      "v_add_f32 v92, v92, v57\n"
      "v_fmac_f32 v93, v57, v57\n"
      "v_add_f32 v92, v92, v58\n"
      "v_fmac_f32 v93, v58, v58\n"
      "s_waitcnt lgkmcnt(0)\n"
      "v_mfma_f32_16x16x32_bf16 v[12:15], v[104:107], a[32:35], v[12:15]\n"
      "v_add_f32 v92, v92, v59\n"
      "v_fmac_f32 v93, v59, v59\n"
      "v_add_f32 v92, v92, v68\n"
      "v_fmac_f32 v93, v68, v68\n"
      "v_add_f32_e64 v92, v92, v69\n"
      "v_mfma_f32_16x16x32_bf16 v[12:15], v[44:47], a[40:43], v[12:15]\n"
      "v_fmac_f32 v93, v69, v69\n"
      "v_add_f32 v92, v92, v70\n"
      "v_fmac_f32 v93, v70, v70\n"
      "v_add_f32 v92, v92, v71\n"
      "v_fmac_f32_e64 v93, v71, v71\n"
      "v_mfma_f32_16x16x32_bf16 v[16:19], v[104:107], a[36:39], v[16:19]\n"
      "s_nop 0\n"
      "v_permlane16_swap_b32 v92, v93\n"
      "v_add_f32 v92, v92, v93\n"
      "ds_read_b32 v104, v1 offset:1280\n"
      "v_mov_b32 v93, v92\n"
      "s_nop 1\n"
      "v_permlane32_swap_b32 v92, v93\n"
      "v_mfma_f32_16x16x32_bf16 v[16:19], v[44:47], a[44:47], v[16:19]\n"
      "v_add_f32 v92, v92, v93\n"
      "s_waitcnt lgkmcnt(0)\n"
      "ds_write_b32 v104, v92 offset:512\n"
      "ds_read_b32 v88, v1 offset:512\n"
      "ds_read_b32 v89, v1 offset:768\n"
      "ds_read_b32 v84, v1 offset:1536\n"
      "v_cvt_pk_bf16_f32 v12, v12, v13\n"
      "v_cvt_pk_bf16_f32 v13, v14, v15\n"
      "v_cvt_pk_bf16_f32 v16, v16, v17\n"
      "v_cvt_pk_bf16_f32 v17, v18, v19\n"
      "s_waitcnt vmcnt(0)\n"
      "s_waitcnt lgkmcnt(0)\n"
      "s_barrier\n"
      "s_setprio 1\n"
      "v_subrev_u32 v15, 12288, v6\n"
      "v_add_u32 v14, 108800, v2\n"
      "ds_read_b128 v[104:107], v14\n"
      "ds_read_b128 v[100:103], v14 offset:1024\n"
      "ds_read_b32 v14, v1\n"
      "s_waitcnt lgkmcnt(2)\n"
      "v_add_f32 v104, v104, v106\n"
      "v_add_f32 v105, v105, v107\n"
      "s_waitcnt lgkmcnt(1)\n"
      "v_add_f32 v100, v100, v102\n"
      "v_add_f32 v101, v101, v103\n"
      "v_add_f32 v104, v104, v100\n"
      "v_add_f32 v105, v105, v101\n"
      "v_fma_f32 v100, -v104, v104, s74\n"
      "v_fmac_f32 v100, 0x43400000, v105\n"
      "v_max_f32 v100, s74, v100\n"
      "v_rsq_f32 v100, v100\n"
      "s_nop 0\n"
      "v_mul_f32 v92, 0x43400000, v100\n"
      "v_mul_f32 v93, -v104, v100\n"
      "v_mov_b32 v18, v92\n"
      "v_mov_b32 v19, v93\n"
      "s_nop 0\n"
      "v_permlane32_swap_b32 v92, v18\n"
      "v_permlane32_swap_b32 v93, v19\n"
      "v_mov_b32 v85, v92\n"
      "v_mov_b32_e64 v91, v93\n"
      "s_nop 0\n"
      "v_permlane16_swap_b32 v92, v85\n"
      "v_permlane16_swap_b32_e64 v93, v91\n"
      "s_waitcnt lgkmcnt(0)\n"
      "v_xor_b32_e64 v90, 64, v14\n"
      "v_fma_f32 v36, v36, v92, v93\n"
      "v_fma_f32 v37, v37, v92, v93\n"
      "v_cvt_pk_bf16_f32 v36, v36, v37\n"
      "v_fma_f32 v38, v38, v92, v93\n"
      "v_fma_f32 v39, v39, v92, v93\n"
      "v_cvt_pk_bf16_f32 v37, v38, v39\n"
      "v_fma_f32 v24, v24, v92, v93\n"
      "v_fma_f32 v25, v25, v92, v93\n"
      "v_cvt_pk_bf16_f32 v38, v24, v25\n"
      "v_fma_f32 v26, v26, v92, v93\n"
      "v_fma_f32 v27, v27, v92, v93\n"
      "v_cvt_pk_bf16_f32 v39, v26, v27\n"
      "ds_write_b128 v14, v[36:39] offset:24576\n"
      "v_fma_f32 v60, v60, v92, v93\n"
      "v_fma_f32 v61, v61, v92, v93\n"
      "v_cvt_pk_bf16_f32 v60, v60, v61\n"
      "v_fma_f32 v62, v62, v92, v93\n"
      "v_fma_f32 v63, v63, v92, v93\n"
      "v_cvt_pk_bf16_f32 v61, v62, v63\n"
      "v_fma_f32 v20, v20, v92, v93\n"
      "v_fma_f32 v21, v21, v92, v93\n"
      "v_cvt_pk_bf16_f32 v62, v20, v21\n"
      "v_fma_f32 v22, v22, v92, v93\n"
      "v_fma_f32 v23, v23, v92, v93\n"
      "v_cvt_pk_bf16_f32 v63, v22, v23\n"
      "ds_read_b128 v[96:99], v15\n"
      "ds_read_b128 v[100:103], v15 offset:2048\n"
      "ds_read_b128 v[104:107], v15 offset:4096\n"
      "ds_read_b128 v[92:95], v15 offset:6144\n"
      "s_waitcnt lgkmcnt(2)\n"
      "v_mfma_f32_16x16x32_bf16 v[24:27], a[48:51], v[96:99], 0\n"
      "ds_write_b128 v90, v[60:63] offset:24576\n"
      "v_fma_f32 v48, v48, v85, v91\n"
      "v_mfma_f32_16x16x32_bf16 v[24:27], a[56:59], v[100:103], v[24:27]\n"
      "v_fma_f32 v49, v49, v85, v91\n"
      "v_cvt_pk_bf16_f32 v48, v48, v49\n"
      "v_mfma_f32_16x16x32_bf16 v[44:47], a[52:55], v[96:99], 0\n"
      "v_fma_f32 v50, v50, v85, v91\n"
      "v_mfma_f32_16x16x32_bf16 v[44:47], a[60:63], v[100:103], v[44:47]\n"
      "v_fma_f32 v51, v51, v85, v91\n"
      "v_cvt_pk_bf16_f32 v49, v50, v51\n"
      "v_mfma_f32_16x16x32_bf16 v[20:23], a[96:99], v[96:99], 0\n"
      "v_fma_f32 v80, v80, v85, v91\n"
      "v_fma_f32 v81, v81, v85, v91\n"
      "v_mfma_f32_16x16x32_bf16 v[20:23], a[104:107], v[100:103], v[20:23]\n"
      "v_cvt_pk_bf16_f32 v50, v80, v81\n"
      "v_mfma_f32_16x16x32_bf16 v[40:43], a[100:103], v[96:99], 0\n"
      "v_fma_f32 v82, v82, v85, v91\n"
      "v_fma_f32 v83, v83, v85, v91\n"
      "v_mfma_f32_16x16x32_bf16 v[40:43], a[108:111], v[100:103], v[40:43]\n"
      "ds_read_b128 v[100:103], v15 offset:8192\n"
      "ds_read_b128 v[96:99], v15 offset:10240\n"
      "v_cvt_pk_bf16_f32 v51, v82, v83\n"
      "ds_write_b128 v14, v[48:51] offset:26624\n"
      "s_waitcnt lgkmcnt(3)\n"
      "v_mfma_f32_16x16x32_bf16 v[24:27], a[64:67], v[104:107], v[24:27]\n"
      "v_fma_f32 v76, v76, v85, v91\n"
      "v_mfma_f32_16x16x32_bf16 v[24:27], a[72:75], v[92:95], v[24:27]\n"
      "v_fma_f32 v77, v77, v85, v91\n"
      "v_cvt_pk_bf16_f32 v76, v76, v77\n"
      "v_mfma_f32_16x16x32_bf16 v[44:47], a[68:71], v[104:107], v[44:47]\n"
      "v_fma_f32 v78, v78, v85, v91\n"
      "v_fma_f32 v79, v79, v85, v91\n"
      "v_mfma_f32_16x16x32_bf16 v[44:47], a[76:79], v[92:95], v[44:47]\n"
      "v_cvt_pk_bf16_f32 v77, v78, v79\n"
      "v_mfma_f32_16x16x32_bf16 v[20:23], a[112:115], v[104:107], v[20:23]\n"
      "v_fma_f32 v52, v52, v85, v91\n"
      "v_fma_f32 v53, v53, v85, v91\n"
      "v_mfma_f32_16x16x32_bf16 v[20:23], a[120:123], v[92:95], v[20:23]\n"
      "v_cvt_pk_bf16_f32 v78, v52, v53\n"
      "v_fma_f32 v54, v54, v85, v91\n"
      "v_mfma_f32_16x16x32_bf16 v[40:43], a[116:119], v[104:107], v[40:43]\n"
      "v_fma_f32 v55, v55, v85, v91\n"
      "v_mfma_f32_16x16x32_bf16 v[40:43], a[124:127], v[92:95], v[40:43]\n"
      "ds_read_b128 v[80:83], v15 offset:256\n"
      "ds_read_b128 v[92:95], v15 offset:2304\n"
      "v_cvt_pk_bf16_f32 v79, v54, v55\n"
      "ds_write_b128 v90, v[76:79] offset:26624\n"
      "s_waitcnt lgkmcnt(3)\n"
      "v_mfma_f32_16x16x32_bf16 v[24:27], a[80:83], v[100:103], v[24:27]\n"
      "v_add_u32 v104, s71, v2\n"
      "v_add_u32_e64 v105, s72, v1\n"
      "v_mfma_f32_16x16x32_bf16 v[24:27], a[88:91], v[96:99], v[24:27]\n"
      "ds_read_b32 v52, v105 offset:1792\n"
      "v_mfma_f32_16x16x32_bf16 v[44:47], a[84:87], v[100:103], v[44:47]\n"
      "ds_read_b32 v54, v105 offset:2048\n"
      "ds_read_b32 v53, v105 offset:4608\n"
      "v_mfma_f32_16x16x32_bf16 v[44:47], a[92:95], v[96:99], v[44:47]\n"
      "ds_read_b32 v55, v105 offset:4864\n"
      "ds_read_b128 v[36:39], v104 offset:24576\n"
      "v_mfma_f32_16x16x32_bf16 v[20:23], a[128:131], v[100:103], v[20:23]\n"
      "ds_read_b128 v[60:63], v104 offset:25600\n"
      "v_mfma_f32_16x16x32_bf16 v[20:23], a[136:139], v[96:99], v[20:23]\n"
      "ds_read_b128 v[48:51], v104 offset:26624\n"
      "ds_read_b128 v[76:79], v104 offset:27648\n"
      "v_mfma_f32_16x16x32_bf16 v[40:43], a[132:135], v[100:103], v[40:43]\n"
      "s_add_u32 s14, s49, s70\n"
      "s_add_u32 s66, s22, s14\n"
      "s_addc_u32 s67, s23, 0\n"
      "s_or_b32 s14, s50, s51\n"
      "s_waitcnt lgkmcnt(3)\n"
      "s_cbranch_scc1 L_edge_81_%=\n"
      "L_eb_82_%=:\n"
      "v_mfma_f32_16x16x32_bf16 v[40:43], a[140:143], v[96:99], v[40:43]\n"
      "ds_read_b128 v[100:103], v15 offset:4352\n"
      "ds_read_b128 v[96:99], v15 offset:6400\n"
      "global_store_dwordx4 v52, v[36:39], s[66:67] sc1\n"
      "s_nop 1\n"
      "v_mfma_f32_16x16x32_bf16 v[36:39], a[48:51], v[80:83], 0\n"
      "s_waitcnt lgkmcnt(2)\n"
      "global_store_dwordx4 v54, v[60:63], s[66:67] sc1\n"
      "global_store_dwordx4 v53, v[48:51], s[66:67] sc1\n"
      "v_mfma_f32_16x16x32_bf16 v[36:39], a[56:59], v[92:95], v[36:39]\n"
      "global_store_dwordx4 v55, v[76:79], s[66:67] sc1\n"
      "s_nop 1\n"
      "v_xor_b32 v76, 64, v14\n"
      "v_mfma_f32_16x16x32_bf16 v[60:63], a[52:55], v[80:83], 0\n"
      "v_fma_f32 v64, v64, v18, v19\n"
      "v_mfma_f32_16x16x32_bf16 v[60:63], a[60:63], v[92:95], v[60:63]\n"
      "v_fma_f32 v65, v65, v18, v19\n"
      "v_cvt_pk_bf16_f32 v64, v64, v65\n"
      "v_mfma_f32_16x16x32_bf16 v[48:51], a[96:99], v[80:83], 0\n"
      "v_fma_f32 v66, v66, v18, v19\n"
      "v_fma_f32 v67, v67, v18, v19\n"
      "v_mfma_f32_16x16x32_bf16 v[48:51], a[104:107], v[92:95], v[48:51]\n"
      "v_cvt_pk_bf16_f32 v65, v66, v67\n"
      "v_mfma_f32_16x16x32_bf16 v[52:55], a[100:103], v[80:83], 0\n"
      "v_fma_f32 v72, v72, v18, v19\n"
      "v_fma_f32 v73, v73, v18, v19\n"
      "v_mfma_f32_16x16x32_bf16 v[52:55], a[108:111], v[92:95], v[52:55]\n"
      "ds_read_b128 v[92:95], v15 offset:8448\n"
      "ds_read_b128 v[80:83], v15 offset:10496\n"
      "v_cvt_pk_bf16_f32 v66, v72, v73\n"
      "v_fma_f32 v74, v74, v18, v19\n"
      "s_waitcnt lgkmcnt(2)\n"
      "v_mfma_f32_16x16x32_bf16 v[36:39], a[64:67], v[100:103], v[36:39]\n"
      "v_fma_f32 v75, v75, v18, v19\n"
      "v_mfma_f32_16x16x32_bf16 v[36:39], a[72:75], v[96:99], v[36:39]\n"
      "v_cvt_pk_bf16_f32 v67, v74, v75\n"
      "ds_write_b128 v14, v[64:67] offset:24576\n"
      "v_mfma_f32_16x16x32_bf16 v[60:63], a[68:71], v[100:103], v[60:63]\n"
      "v_fma_f32 v56, v56, v18, v19\n"
      "v_fma_f32 v57, v57, v18, v19\n"
      "v_mfma_f32_16x16x32_bf16 v[60:63], a[76:79], v[96:99], v[60:63]\n"
      "v_cvt_pk_bf16_f32 v56, v56, v57\n"
      "v_mfma_f32_16x16x32_bf16 v[48:51], a[112:115], v[100:103], v[48:51]\n"
      "v_fma_f32 v58, v58, v18, v19\n"
      "v_fma_f32 v59, v59, v18, v19\n"
      "v_mfma_f32_16x16x32_bf16 v[48:51], a[120:123], v[96:99], v[48:51]\n"
      "v_cvt_pk_bf16_f32 v57, v58, v59\n"
      "v_fma_f32 v68, v68, v18, v19\n"
      "v_mfma_f32_16x16x32_bf16 v[52:55], a[116:119], v[100:103], v[52:55]\n"
      "v_fma_f32 v69, v69, v18, v19\n"
      "v_mfma_f32_16x16x32_bf16 v[52:55], a[124:127], v[96:99], v[52:55]\n"
      "v_cvt_pk_bf16_f32 v58, v68, v69\n"
      "v_fma_f32 v70, v70, v18, v19\n"
      "s_waitcnt lgkmcnt(0)\n"
      "v_mfma_f32_16x16x32_bf16 v[36:39], a[80:83], v[92:95], v[36:39]\n"
      "v_fma_f32 v71, v71, v18, v19\n"
      "v_cvt_pk_bf16_f32 v59, v70, v71\n"
      "v_mfma_f32_16x16x32_bf16 v[36:39], a[88:91], v[80:83], v[36:39]\n"
      "ds_write_b128 v76, v[56:59] offset:24576\n"
      "v_mfma_f32_16x16x32_bf16 v[60:63], a[84:87], v[92:95], v[60:63]\n"
      "v_add_u32 v68, s71, v2\n"
      "v_add_u32 v69, s72, v1\n"
      "v_mfma_f32_16x16x32_bf16 v[60:63], a[92:95], v[80:83], v[60:63]\n"
      "ds_read_b32 v71, v69 offset:2560\n"
      "ds_read_b32 v70, v69 offset:5376\n"
      "v_mfma_f32_16x16x32_bf16 v[48:51], a[128:131], v[92:95], v[48:51]\n"
      "ds_read_b128 v[64:67], v68 offset:24576\n"
      "v_mfma_f32_16x16x32_bf16 v[48:51], a[136:139], v[80:83], v[48:51]\n"
      "ds_read_b128 v[56:59], v68 offset:25600\n"
      "s_add_u32 s14, s49, s70\n"
      "s_add_u32 s66, s22, s14\n"
      "s_addc_u32 s67, s23, 0\n"
      "v_mfma_f32_16x16x32_bf16 v[52:55], a[132:135], v[92:95], v[52:55]\n"
      "s_or_b32 s14, s50, s51\n"
      "s_waitcnt lgkmcnt(2)\n"
      "s_cbranch_scc1 L_edge_83_%=\n"
      "L_eb_84_%=:\n"
      "s_waitcnt lgkmcnt(1)\n"
      "global_store_dwordx4 v71, v[64:67], s[66:67] sc1\n"
      "v_mfma_f32_16x16x32_bf16 v[52:55], a[140:143], v[80:83], v[52:55]\n"
      "s_waitcnt lgkmcnt(0)\n"
      "global_store_dwordx4 v70, v[56:59], s[66:67] sc1\n"
      "ds_read_b128 v[68:71], v3\n"
      "ds_read_b128 v[80:83], v3 offset:1024\n"
      "v_add_u32_e64 v66, s63, v2\n"
      "ds_read_b128 v[56:59], v66\n"
      "ds_read_b64 v[64:65], v66 offset:1024\n"
      "v_add_u32 v67, s92, v89\n"
      "ds_read_u16 v66, v67\n"
      "v_add_u32 v72, s62, v88\n"
      "ds_read_u16 v14, v89\n"
      "ds_read_u16 v18, v89 offset:32\n"
      "v_mov_b32 v15, 0\n"
      "v_mov_b32 v19, 0\n"
      "v_mov_b32_e64 v67, 0\n"
      "ds_read_b32 v74, v1 offset:1024\n"
      "v_add_u32 v73, s55, v1\n"
      "ds_read_b32 v73, v73\n"
      "s_waitcnt lgkmcnt(1)\n"
      "v_subrev_u32 v74, 163072, v74\n"
      "v_and_b32 v76, 14, v74\n"
      "v_and_b32 v74, -16, v74\n"
      "v_lshl_add_u32 v74, v74, 5, v76\n"
      "v_add_u32 v74, 49408, v74\n"
      "v_add_f32 v75, v24, v25\n"
      "v_mul_f32 v76, v24, v24\n"
      "v_fmac_f32 v76, v25, v25\n"
      "v_add_f32 v75, v75, v26\n"
      "v_mfma_f32_16x16x32_bf16 v[104:107], v[8:11], v[68:71], 0\n"
      "v_fmac_f32 v76, v26, v26\n"
      "v_add_f32 v75, v75, v27\n"
      "v_mfma_f32_16x16x32_bf16 v[104:107], v[8:11], v[80:83], v[104:107]\n"
      "v_fmac_f32 v76, v27, v27\n"
      "v_add_f32 v75, v75, v44\n"
      "v_mfma_f32_16x16x32_bf16 v[100:103], v[32:35], v[68:71], 0\n"
      "v_fmac_f32 v76, v44, v44\n"
      "v_add_f32 v75, v75, v45\n"
      "v_mfma_f32_16x16x32_bf16 v[100:103], v[12:15], v[80:83], v[100:103]\n"
      "v_fmac_f32 v76, v45, v45\n"
      "v_add_f32 v75, v75, v46\n"
      "v_mfma_f32_16x16x32_bf16 v[96:99], v[28:31], v[68:71], 0\n"
      "v_fmac_f32 v76, v46, v46\n"
      "v_add_f32 v75, v75, v47\n"
      "v_mfma_f32_16x16x32_bf16 v[96:99], v[16:19], v[80:83], v[96:99]\n"
      "v_fmac_f32 v76, v47, v47\n"
      "v_add_f32 v75, v75, v20\n"
      "v_mfma_f32_16x16x32_bf16 v[92:95], v[56:59], v[68:71], 0\n"
      "v_fmac_f32 v76, v20, v20\n"
      "v_add_f32 v75, v75, v21\n"
      "v_mfma_f32_16x16x32_bf16 v[92:95], v[64:67], v[80:83], v[92:95]\n"
      "v_fmac_f32 v76, v21, v21\n"
      "v_add_f32 v75, v75, v22\n"
      "ds_read_b128 v[68:71], v3 offset:2048\n"
      "ds_read_b128 v[80:83], v3 offset:3072\n"
      "v_rcp_f32 v104, v104\n"
      "s_nop 0\n"
      "v_mul_f32 v100, v104, v100\n"
      "v_mul_f32 v101, v104, v101\n"
      "v_mul_f32 v102, v104, v102\n"
      "v_mul_f32 v103, v104, v103\n"
      "v_cvt_pk_bf16_f32 v100, v100, v101\n"
      "v_cvt_pk_bf16_f32 v101, v102, v103\n"
      "ds_write_b64 v88, v[100:101]\n"
      "v_mul_f32 v96, v104, v96\n"
      "v_mul_f32 v97, v104, v97\n"
      "v_mul_f32 v98, v104, v98\n"
      "v_mul_f32 v99, v104, v99\n"
      "v_cvt_pk_bf16_f32 v96, v96, v97\n"
      "v_cvt_pk_bf16_f32 v97, v98, v99\n"
      "ds_write_b64 v88, v[96:97] offset:1024\n"
      "v_mul_f32 v92, v104, v92\n"
      "v_mul_f32 v93, v104, v93\n"
      "v_mul_f32_e64 v94, v104, v94\n"
      "v_mul_f32 v95, v104, v95\n"
      "v_cvt_pk_bf16_f32 v92, v92, v93\n"
      "v_cvt_pk_bf16_f32 v93, v94, v95\n"
      "ds_write_b64 v72, v[92:93]\n"
      "s_waitcnt lgkmcnt(3)\n"
      "v_mfma_f32_16x16x32_bf16 v[108:111], v[8:11], v[68:71], 0\n"
      "v_fmac_f32 v76, v22, v22\n"
      "v_add_f32 v75, v75, v23\n"
      "v_mfma_f32_16x16x32_bf16 v[108:111], v[8:11], v[80:83], v[108:111]\n"
      "v_fmac_f32 v76, v23, v23\n"
      "v_add_f32 v75, v75, v40\n"
      "v_mfma_f32_16x16x32_bf16 v[104:107], v[32:35], v[68:71], 0\n"
      "v_fmac_f32 v76, v40, v40\n"
      "v_add_f32 v75, v75, v41\n"
      "v_mfma_f32_16x16x32_bf16 v[104:107], v[12:15], v[80:83], v[104:107]\n"
      "v_fmac_f32 v76, v41, v41\n"
      "v_add_f32 v75, v75, v42\n"
      "v_mfma_f32_16x16x32_bf16 v[92:95], v[28:31], v[68:71], 0\n"
      "v_fmac_f32 v76, v42, v42\n"
      "v_add_f32 v75, v75, v43\n"
      "v_mfma_f32_16x16x32_bf16 v[92:95], v[16:19], v[80:83], v[92:95]\n"
      "v_fmac_f32 v76, v43, v43\n"
      "s_nop 1\n"
      "v_permlane16_swap_b32 v75, v76\n"
      "v_mfma_f32_16x16x32_bf16 v[96:99], v[56:59], v[68:71], 0\n"
      "v_add_f32 v75, v75, v76\n"
      "v_add_f32 v68, v36, v37\n"
      "v_mfma_f32_16x16x32_bf16 v[96:99], v[64:67], v[80:83], v[96:99]\n"
      "v_mul_f32 v69, v36, v36\n"
      "v_fmac_f32 v69, v37, v37\n"
      "ds_read_b128 v[100:103], v3 offset:4096\n"
      "ds_read_b128 v[80:83], v3 offset:5120\n"
      "v_add_f32 v68, v68, v38\n"
      "v_rcp_f32 v108, v108\n"
      "s_nop 0\n"
      "v_mul_f32 v104, v108, v104\n"
      "v_mul_f32 v105, v108, v105\n"
      "v_mul_f32 v106, v108, v106\n"
      "v_mul_f32 v107, v108, v107\n"
      "v_cvt_pk_bf16_f32 v104, v104, v105\n"
      "v_cvt_pk_bf16_f32 v105, v106, v107\n"
      "ds_write_b64 v88, v[104:105] offset:256\n"
      "v_mul_f32 v92, v108, v92\n"
      "v_mul_f32 v93, v108, v93\n"
      "v_mul_f32 v94, v108, v94\n"
      "v_mul_f32 v95, v108, v95\n"
      "v_cvt_pk_bf16_f32 v92, v92, v93\n"
      "v_cvt_pk_bf16_f32 v93, v94, v95\n"
      "ds_write_b64 v88, v[92:93] offset:1280\n"
      "v_mul_f32 v96, v108, v96\n"
      "v_mul_f32 v97, v108, v97\n"
      "v_mul_f32_e64 v98, v108, v98\n"
      "v_mul_f32 v99, v108, v99\n"
      "v_cvt_pk_bf16_f32 v96, v96, v97\n"
      "v_cvt_pk_bf16_f32 v97, v98, v99\n"
      "ds_write_b64 v72, v[96:97] offset:256\n"
      "s_waitcnt lgkmcnt(3)\n"
      "v_mfma_f32_16x16x32_bf16 v[104:107], v[8:11], v[100:103], 0\n"
      "v_fmac_f32 v69, v38, v38\n"
      "v_add_f32 v68, v68, v39\n"
      "v_mfma_f32_16x16x32_bf16 v[104:107], v[8:11], v[80:83], v[104:107]\n"
      "v_fmac_f32 v69, v39, v39\n"
      "v_add_f32 v68, v68, v60\n"
      "v_mfma_f32_16x16x32_bf16 v[76:79], v[32:35], v[100:103], 0\n"
      "v_fmac_f32 v69, v60, v60\n"
      "v_add_f32 v68, v68, v61\n"
      "v_mfma_f32_16x16x32_bf16 v[76:79], v[12:15], v[80:83], v[76:79]\n"
      "v_fmac_f32 v69, v61, v61\n"
      "v_add_f32 v68, v68, v62\n"
      "v_mfma_f32_16x16x32_bf16 v[92:95], v[28:31], v[100:103], 0\n"
      "v_fmac_f32 v69, v62, v62\n"
      "v_add_f32 v68, v68, v63\n"
      "v_mfma_f32_16x16x32_bf16 v[92:95], v[16:19], v[80:83], v[92:95]\n"
      "v_fmac_f32 v69, v63, v63\n"
      "v_add_f32 v68, v68, v48\n"
      "v_mfma_f32_16x16x32_bf16 v[96:99], v[56:59], v[100:103], 0\n"
      "v_fmac_f32 v69, v48, v48\n"
      "v_add_f32 v68, v68, v49\n"
      "v_mfma_f32_16x16x32_bf16 v[96:99], v[64:67], v[80:83], v[96:99]\n"
      "v_fmac_f32 v69, v49, v49\n"
      "v_add_f32 v68, v68, v50\n"
      "ds_read_b128 v[100:103], v73\n"
      "ds_read_b128 v[80:83], v73 offset:64\n"
      "v_fmac_f32 v69, v50, v50\n"
      "v_rcp_f32 v104, v104\n"
      "s_nop 0\n"
      "v_mul_f32 v76, v104, v76\n"
      "v_mul_f32 v77, v104, v77\n"
      "v_mul_f32 v78, v104, v78\n"
      "v_mul_f32_e64 v79, v104, v79\n"
      "v_cvt_pk_bf16_f32 v76, v76, v77\n"
      "v_cvt_pk_bf16_f32 v77, v78, v79\n"
      "ds_write_b64 v88, v[76:77] offset:12288\n"
      "v_mul_f32 v92, v104, v92\n"
      "v_mul_f32 v93, v104, v93\n"
      "v_mul_f32 v94, v104, v94\n"
      "v_mul_f32 v95, v104, v95\n"
      "v_cvt_pk_bf16_f32 v92, v92, v93\n"
      "v_cvt_pk_bf16_f32 v93, v94, v95\n"
      "ds_write_b64 v88, v[92:93] offset:13312\n"
      "v_mul_f32 v96, v104, v96\n"
      "v_mul_f32 v97, v104, v97\n"
      "v_mul_f32_e64 v98, v104, v98\n"
      "v_mul_f32 v99, v104, v99\n"
      "v_cvt_pk_bf16_f32 v96, v96, v97\n"
      "v_cvt_pk_bf16_f32 v97, v98, v99\n"
      "ds_write_b64 v72, v[96:97] offset:12288\n"
      "s_waitcnt lgkmcnt(3)\n"
      "v_mfma_f32_16x16x32_bf16 v[96:99], v[100:103], v[8:11], 0\n"
      "v_add_f32 v68, v68, v51\n"
      "v_fmac_f32 v69, v51, v51\n"
      "v_mfma_f32_16x16x32_bf16 v[96:99], v[80:83], v[8:11], v[96:99]\n"
      "v_add_f32 v68, v68, v52\n"
      "v_fmac_f32 v69, v52, v52\n"
      "v_mfma_f32_16x16x32_bf16 v[92:95], v[100:103], v[32:35], 0\n"
      "v_add_f32 v68, v68, v53\n"
      "v_fmac_f32 v69, v53, v53\n"
      "v_mfma_f32_16x16x32_bf16 v[92:95], v[80:83], v[12:15], v[92:95]\n"
      "v_add_f32 v68, v68, v54\n"
      "v_fmac_f32 v69, v54, v54\n"
      "v_mfma_f32_16x16x32_bf16 v[32:35], v[100:103], v[28:31], 0\n"
      "v_add_f32 v68, v68, v55\n"
      "v_fmac_f32 v69, v55, v55\n"
      "v_mfma_f32_16x16x32_bf16 v[32:35], v[80:83], v[16:19], v[32:35]\n"
      "s_nop 0\n"
      "v_permlane16_swap_b32 v68, v69\n"
      "v_add_f32_e64 v68, v68, v69\n"
      "v_mfma_f32_16x16x32_bf16 v[28:31], v[100:103], v[56:59], 0\n"
      "s_nop 3\n"
      "ds_read_b32 v33, v1 offset:1280\n"
      "s_waitcnt lgkmcnt(0)\n"
      "v_add_u32 v33, -22272, v33\n"
      "v_mfma_f32_16x16x32_bf16 v[28:31], v[80:83], v[64:67], v[28:31]\n"
      "v_permlane32_swap_b32 v75, v68\n"
      "v_add_f32 v75, v75, v68\n"
      "ds_write_b32 v33, v75\n"
      "v_cndmask_b32 v92, v92, v32, s[56:57]\n"
      "s_nop 3\n"
      "v_cndmask_b32 v92, v92, v28, s[58:59]\n"
      "v_mov_b32_dpp v96, v96 quad_perm:[0,0,0,0] row_mask:0xf bank_mask:0xf\n"
      "v_rcp_f32 v96, v96\n"
      "s_nop 0\n"
      "v_mul_f32 v92, v96, v92\n"
      "v_cvt_pk_bf16_f32 v92, v92, v92\n"
      "ds_write_b16 v74, v92\n"
      "v_subrev_u32 v32, 22272, v84\n"
      "s_waitcnt lgkmcnt(0)\n"
      "s_barrier\n"
      "ds_read_b128 v[28:31], v32\n"
      "ds_read_b128 v[72:75], v32 offset:1024\n"
      "ds_read_b32 v76, v1\n"
      "ds_read_b128 v[80:83], v6\n"
      "ds_read_b128 v[32:35], v6 offset:2048\n"
      "ds_read_b128 v[68:71], v6 offset:4096\n"
      "ds_read_b128 v[64:67], v6 offset:6144\n"
      "s_waitcnt lgkmcnt(2)\n"
      "v_mfma_f32_16x16x32_bf16 v[16:19], a[48:51], v[80:83], 0\n"
      "v_add_f32 v28, v28, v30\n"
      "v_add_f32 v29, v29, v31\n"
      "v_add_f32 v72, v72, v74\n"
      "v_mfma_f32_16x16x32_bf16 v[16:19], a[56:59], v[32:35], v[16:19]\n"
      "v_add_f32 v73, v73, v75\n"
      "v_add_f32 v28, v28, v72\n"
      "v_mfma_f32_16x16x32_bf16 v[56:59], a[52:55], v[80:83], 0\n"
      "v_add_f32_e64 v29, v29, v73\n"
      "v_fma_f32 v72, -v28, v28, s74\n"
      "v_mfma_f32_16x16x32_bf16 v[56:59], a[60:63], v[32:35], v[56:59]\n"
      "v_fmac_f32 v72, 0x43400000, v29\n"
      "v_max_f32 v72, s74, v72\n"
      "v_mfma_f32_16x16x32_bf16 v[12:15], a[96:99], v[80:83], 0\n"
      "v_rsq_f32 v72, v72\n"
      "s_nop 0\n"
      "v_mul_f32 v103, 0x43400000, v72\n"
      "v_mfma_f32_16x16x32_bf16 v[12:15], a[104:107], v[32:35], v[12:15]\n"
      "v_mul_f32 v102, -v28, v72\n"
      "v_mov_b32 v101, v103\n"
      "v_mfma_f32_16x16x32_bf16 v[28:31], a[100:103], v[80:83], 0\n"
      "v_mov_b32 v100, v102\n"
      "v_permlane16_swap_b32 v103, v101\n"
      "v_mfma_f32_16x16x32_bf16 v[28:31], a[108:111], v[32:35], v[28:31]\n"
      "ds_read_b128 v[32:35], v6 offset:8192\n"
      "ds_read_b128 v[80:83], v6 offset:10240\n"
      "v_permlane16_swap_b32 v102, v100\n"
      "v_xor_b32_e64 v88, 64, v76\n"
      "s_waitcnt lgkmcnt(2)\n"
      "v_mfma_f32_16x16x32_bf16 v[16:19], a[64:67], v[68:71], v[16:19]\n"
      "v_fma_f32 v24, v24, v103, v102\n"
      "v_fma_f32 v25, v25, v103, v102\n"
      "v_mfma_f32_16x16x32_bf16 v[16:19], a[72:75], v[64:67], v[16:19]\n"
      "v_cvt_pk_bf16_f32 v24, v24, v25\n"
      "v_fma_f32 v26, v26, v103, v102\n"
      "v_mfma_f32_16x16x32_bf16 v[56:59], a[68:71], v[68:71], v[56:59]\n"
      "v_fma_f32 v27, v27, v103, v102\n"
      "v_cvt_pk_bf16_f32 v25, v26, v27\n"
      "v_mfma_f32_16x16x32_bf16 v[56:59], a[76:79], v[64:67], v[56:59]\n"
      "v_fma_f32 v44, v44, v103, v102\n"
      "v_fma_f32 v45, v45, v103, v102\n"
      "v_mfma_f32_16x16x32_bf16 v[12:15], a[112:115], v[68:71], v[12:15]\n"
      "v_cvt_pk_bf16_f32 v26, v44, v45\n"
      "v_fma_f32 v46, v46, v103, v102\n"
      "v_fma_f32 v47, v47, v103, v102\n"
      "v_mfma_f32_16x16x32_bf16 v[12:15], a[120:123], v[64:67], v[12:15]\n"
      "v_cvt_pk_bf16_f32 v27, v46, v47\n"
      "ds_write_b128 v76, v[24:27] offset:24576\n"
      "v_mfma_f32_16x16x32_bf16 v[28:31], a[116:119], v[68:71], v[28:31]\n"
      "v_fma_f32 v20, v20, v103, v102\n"
      "v_fma_f32 v21, v21, v103, v102\n"
      "v_mfma_f32_16x16x32_bf16 v[28:31], a[124:127], v[64:67], v[28:31]\n"
      "ds_read_b128 v[72:75], v6 offset:256\n"
      "ds_read_b128 v[68:71], v6 offset:2304\n"
      "v_cvt_pk_bf16_f32 v20, v20, v21\n"
      "v_fma_f32 v22, v22, v103, v102\n"
      "s_waitcnt lgkmcnt(3)\n"
      "v_mfma_f32_16x16x32_bf16 v[16:19], a[80:83], v[32:35], v[16:19]\n"
      "v_fma_f32 v23, v23, v103, v102\n"
      "v_cvt_pk_bf16_f32 v21, v22, v23\n"
      "v_mfma_f32_16x16x32_bf16 v[16:19], a[88:91], v[80:83], v[16:19]\n"
      "v_fma_f32 v40, v40, v103, v102\n"
      "v_fma_f32 v41, v41, v103, v102\n"
      "v_mfma_f32_16x16x32_bf16 v[56:59], a[84:87], v[32:35], v[56:59]\n"
      "v_cvt_pk_bf16_f32 v22, v40, v41\n"
      "v_fma_f32 v42, v42, v103, v102\n"
      "v_mfma_f32_16x16x32_bf16 v[56:59], a[92:95], v[80:83], v[56:59]\n"
      "v_fma_f32 v43, v43, v103, v102\n"
      "v_cvt_pk_bf16_f32 v23, v42, v43\n"
      "v_mfma_f32_16x16x32_bf16 v[12:15], a[128:131], v[32:35], v[12:15]\n"
      "ds_write_b128 v88, v[20:23] offset:24576\n"
      "v_fma_f32 v36, v36, v101, v100\n"
      "v_mfma_f32_16x16x32_bf16 v[12:15], a[136:139], v[80:83], v[12:15]\n"
      "v_fma_f32 v37, v37, v101, v100\n"
      "v_cvt_pk_bf16_f32 v36, v36, v37\n"
      "v_mfma_f32_16x16x32_bf16 v[28:31], a[132:135], v[32:35], v[28:31]\n"
      "v_fma_f32 v38, v38, v101, v100\n"
      "v_fma_f32 v39, v39, v101, v100\n"
      "v_mfma_f32_16x16x32_bf16 v[28:31], a[140:143], v[80:83], v[28:31]\n"
      "ds_read_b128 v[80:83], v6 offset:4352\n"
      "ds_read_b128 v[64:67], v6 offset:6400\n"
      "v_cvt_pk_bf16_f32 v37, v38, v39\n"
      "v_fma_f32 v60, v60, v101, v100\n"
      "s_waitcnt lgkmcnt(2)\n"
      "v_mfma_f32_16x16x32_bf16 v[40:43], a[48:51], v[72:75], 0\n"
      "v_fma_f32 v61, v61, v101, v100\n"
      "v_cvt_pk_bf16_f32 v38, v60, v61\n"
      "v_fma_f32 v62, v62, v101, v100\n"
      "v_mfma_f32_16x16x32_bf16 v[40:43], a[56:59], v[68:71], v[40:43]\n"
      "v_fma_f32 v63, v63, v101, v100\n"
      "v_cvt_pk_bf16_f32 v39, v62, v63\n"
      "v_mfma_f32_16x16x32_bf16 v[32:35], a[52:55], v[72:75], 0\n"
      "ds_write_b128 v76, v[36:39] offset:26624\n"
      "v_fma_f32 v48, v48, v101, v100\n"
      "v_mfma_f32_16x16x32_bf16 v[32:35], a[60:63], v[68:71], v[32:35]\n"
      "v_fma_f32 v49, v49, v101, v100\n"
      "v_cvt_pk_bf16_f32 v48, v48, v49\n"
      "v_mfma_f32_16x16x32_bf16 v[60:63], a[96:99], v[72:75], 0\n"
      "v_fma_f32 v50, v50, v101, v100\n"
      "v_fma_f32 v51, v51, v101, v100\n"
      "v_mfma_f32_16x16x32_bf16 v[60:63], a[104:107], v[68:71], v[60:63]\n"
      "v_cvt_pk_bf16_f32 v49, v50, v51\n"
      "v_fma_f32 v52, v52, v101, v100\n"
      "v_mfma_f32_16x16x32_bf16 v[44:47], a[100:103], v[72:75], 0\n"
      "v_fma_f32 v53, v53, v101, v100\n"
      "v_cvt_pk_bf16_f32 v50, v52, v53\n"
      "v_mfma_f32_16x16x32_bf16 v[44:47], a[108:111], v[68:71], v[44:47]\n"
      "ds_read_b128 v[68:71], v6 offset:8448\n"
      "ds_read_b128 v[72:75], v6 offset:10496\n"
      "v_fma_f32 v54, v54, v101, v100\n"
      "v_fma_f32 v55, v55, v101, v100\n"
      "s_waitcnt lgkmcnt(2)\n"
      "v_mfma_f32_16x16x32_bf16 v[40:43], a[64:67], v[80:83], v[40:43]\n"
      "v_cvt_pk_bf16_f32 v51, v54, v55\n"
      "ds_write_b128 v88, v[48:51] offset:26624\n"
      "v_mfma_f32_16x16x32_bf16 v[40:43], a[72:75], v[64:67], v[40:43]\n"
      "v_add_u32 v50, s71, v2\n"
      "ds_read_b128 v[24:27], v50 offset:24576\n"
      "v_mfma_f32_16x16x32_bf16 v[32:35], a[68:71], v[80:83], v[32:35]\n"
      "ds_read_b128 v[20:23], v50 offset:25600\n"
      "ds_read_b128 v[36:39], v50 offset:26624\n"
      "v_mfma_f32_16x16x32_bf16 v[32:35], a[76:79], v[64:67], v[32:35]\n"
      "v_bfe_u32 v50, v2, 7, 3\n"
      "v_add_u32_e64 v88, 0, v50\n"
      "v_mfma_f32_16x16x32_bf16 v[60:63], a[112:115], v[80:83], v[60:63]\n"
      "v_bfe_u32 v48, v88, 1, 3\n"
      "v_lshlrev_b32_e64 v88, 2, v88\n"
      "ds_bpermute_b32 v49, v88, v7\n"
      "v_mfma_f32_16x16x32_bf16 v[60:63], a[120:123], v[64:67], v[60:63]\n"
      "v_bfe_u32 v88, v2, 4, 3\n"
      "v_xor_b32 v88, v88, v48\n"
      "v_mfma_f32_16x16x32_bf16 v[44:47], a[116:119], v[80:83], v[44:47]\n"
      "s_waitcnt lgkmcnt(0)\n"
      "v_lshl_add_u32 v49, v88, 4, v49\n"
      "v_add_u32_e64 v88, 8, v50\n"
      "v_mfma_f32_16x16x32_bf16 v[44:47], a[124:127], v[64:67], v[44:47]\n"
      "ds_read_b128 v[52:55], v6 offset:12288\n"
      "ds_read_b128 v[80:83], v6 offset:14336\n"
      "v_bfe_u32 v48, v88, 1, 3\n"
      "v_lshlrev_b32_e64 v88, 2, v88\n"
      "v_mfma_f32_16x16x32_bf16 v[40:43], a[80:83], v[68:71], v[40:43]\n"
      "ds_bpermute_b32 v51, v88, v7\n"
      "v_bfe_u32 v88, v2, 4, 3\n"
      "v_mfma_f32_16x16x32_bf16 v[40:43], a[88:91], v[72:75], v[40:43]\n"
      "v_xor_b32 v88, v88, v48\n"
      "s_waitcnt lgkmcnt(0)\n"
      "v_lshl_add_u32 v51, v88, 4, v51\n"
      "v_mfma_f32_16x16x32_bf16 v[32:35], a[84:87], v[68:71], v[32:35]\n"
      "v_add_u32_e64 v88, 16, v50\n"
      "v_bfe_u32 v48, v88, 1, 3\n"
      "v_mfma_f32_16x16x32_bf16 v[32:35], a[92:95], v[72:75], v[32:35]\n"
      "v_lshlrev_b32_e64 v88, 2, v88\n"
      "ds_bpermute_b32 v50, v88, v7\n"
      "v_mfma_f32_16x16x32_bf16 v[60:63], a[128:131], v[68:71], v[60:63]\n"
      "v_bfe_u32 v88, v2, 4, 3\n"
      "v_xor_b32 v88, v88, v48\n"
      "v_mfma_f32_16x16x32_bf16 v[60:63], a[136:139], v[72:75], v[60:63]\n"
      "s_waitcnt lgkmcnt(0)\n"
      "v_lshl_add_u32 v50, v88, 4, v50\n"
      "s_add_u32 s66, s22, s70\n"
      "v_mfma_f32_16x16x32_bf16 v[44:47], a[132:135], v[68:71], v[44:47]\n"
      "s_addc_u32 s67, s23, 0\n"
      "global_store_dwordx4 v49, v[24:27], s[66:67] sc1\n"
      "v_mfma_f32_16x16x32_bf16 v[44:47], a[140:143], v[72:75], v[44:47]\n"
      "ds_read_b128 v[68:71], v6 offset:16384\n"
      "ds_read_b128 v[72:75], v6 offset:18432\n"
      "global_store_dwordx4 v51, v[20:23], s[66:67] sc1\n"
      "s_mov_b32 s34, 0xffffffff\n"
      "s_mov_b32 s35, 0x0\n"
      "s_mov_b64 exec, s[34:35]\n"
      "global_store_dwordx4 v50, v[36:39], s[66:67] sc1\n"
      "s_mov_b64 exec, -1\n"
      "s_nop 0\n"
      "v_mfma_f32_16x16x32_bf16 v[36:39], a[48:51], v[52:55], 0\n"
      "v_mfma_f32_16x16x32_bf16 v[36:39], a[56:59], v[80:83], v[36:39]\n"
      "v_mfma_f32_16x16x32_bf16 v[48:51], a[52:55], v[52:55], 0\n"
      "v_mfma_f32_16x16x32_bf16 v[48:51], a[60:63], v[80:83], v[48:51]\n"
      "v_mfma_f32_16x16x32_bf16 v[24:27], a[96:99], v[52:55], 0\n"
      "v_add_f32 v88, v16, v17\n"
      "v_mul_f32 v89, v16, v16\n"
      "v_mfma_f32_16x16x32_bf16 v[24:27], a[104:107], v[80:83], v[24:27]\n"
      "v_fmac_f32 v89, v17, v17\n"
      "v_add_f32 v88, v88, v18\n"
      "v_mfma_f32_16x16x32_bf16 v[20:23], a[100:103], v[52:55], 0\n"
      "v_fmac_f32 v89, v18, v18\n"
      "v_mfma_f32_16x16x32_bf16 v[20:23], a[108:111], v[80:83], v[20:23]\n"
      "ds_read_b128 v[52:55], v6 offset:20480\n"
      "ds_read_b128 v[80:83], v6 offset:22528\n"
      "v_add_f32 v88, v88, v19\n"
      "v_fmac_f32 v89, v19, v19\n"
      "s_waitcnt lgkmcnt(2)\n"
      "v_mfma_f32_16x16x32_bf16 v[36:39], a[64:67], v[68:71], v[36:39]\n"
      "v_add_f32 v88, v88, v56\n"
      "v_mfma_f32_16x16x32_bf16 v[36:39], a[72:75], v[72:75], v[36:39]\n"
      "v_fmac_f32 v89, v56, v56\n"
      "v_add_f32 v88, v88, v57\n"
      "v_mfma_f32_16x16x32_bf16 v[48:51], a[68:71], v[68:71], v[48:51]\n"
      "v_fmac_f32 v89, v57, v57\n"
      "v_add_f32 v88, v88, v58\n"
      "v_mfma_f32_16x16x32_bf16 v[48:51], a[76:79], v[72:75], v[48:51]\n"
      "v_fmac_f32_e64 v89, v58, v58\n"
      "v_mfma_f32_16x16x32_bf16 v[24:27], a[112:115], v[68:71], v[24:27]\n"
      "v_add_f32 v88, v88, v59\n"
      "v_fmac_f32 v89, v59, v59\n"
      "v_mfma_f32_16x16x32_bf16 v[24:27], a[120:123], v[72:75], v[24:27]\n"
      "v_add_f32_e64 v88, v88, v12\n"
      "v_mfma_f32_16x16x32_bf16 v[20:23], a[116:119], v[68:71], v[20:23]\n"
      "v_fmac_f32 v89, v12, v12\n"
      "v_add_f32 v88, v88, v13\n"
      "v_mfma_f32_16x16x32_bf16 v[20:23], a[124:127], v[72:75], v[20:23]\n"
      "ds_read_b128 v[100:103], v6 offset:12544\n"
      "ds_read_b128 v[96:99], v6 offset:14592\n"
      "v_fmac_f32 v89, v13, v13\n"
      "v_add_f32 v88, v88, v14\n"
      "s_waitcnt lgkmcnt(2)\n"
      "v_mfma_f32_16x16x32_bf16 v[36:39], a[80:83], v[52:55], v[36:39]\n"
      "v_fmac_f32 v89, v14, v14\n"
      "v_mfma_f32_16x16x32_bf16 v[36:39], a[88:91], v[80:83], v[36:39]\n"
      "v_add_f32 v88, v88, v15\n"
      "v_fmac_f32 v89, v15, v15\n"
      "v_mfma_f32_16x16x32_bf16 v[48:51], a[84:87], v[52:55], v[48:51]\n"
      "v_add_f32_e64 v88, v88, v28\n"
      "v_mfma_f32_16x16x32_bf16 v[48:51], a[92:95], v[80:83], v[48:51]\n"
      "v_fmac_f32 v89, v28, v28\n"
      "v_add_f32 v88, v88, v29\n"
      "v_mfma_f32_16x16x32_bf16 v[24:27], a[128:131], v[52:55], v[24:27]\n"
      "v_fmac_f32 v89, v29, v29\n"
      "v_add_f32 v88, v88, v30\n"
      "v_mfma_f32_16x16x32_bf16 v[24:27], a[136:139], v[80:83], v[24:27]\n"
      "v_fmac_f32 v89, v30, v30\n"
      "v_mfma_f32_16x16x32_bf16 v[20:23], a[132:135], v[52:55], v[20:23]\n"
      "v_add_f32 v88, v88, v31\n"
      "v_fmac_f32_e64 v89, v31, v31\n"
      "v_mfma_f32_16x16x32_bf16 v[20:23], a[140:143], v[80:83], v[20:23]\n"
      "ds_read_b128 v[92:95], v6 offset:16640\n"
      "ds_read_b128 v[80:83], v6 offset:18688\n"
      "v_permlane16_swap_b32 v88, v89\n"
      "s_waitcnt lgkmcnt(2)\n"
      "v_mfma_f32_16x16x32_bf16 v[52:55], a[48:51], v[100:103], 0\n"
      "v_add_f32 v88, v88, v89\n"
      "v_add_f32 v89, v40, v41\n"
      "v_mfma_f32_16x16x32_bf16 v[52:55], a[56:59], v[96:99], v[52:55]\n"
      "v_mul_f32_e64 v90, v40, v40\n"
      "v_mfma_f32_16x16x32_bf16 v[72:75], a[52:55], v[100:103], 0\n"
      "v_fmac_f32 v90, v41, v41\n"
      "v_add_f32 v89, v89, v42\n"
      "v_mfma_f32_16x16x32_bf16 v[72:75], a[60:63], v[96:99], v[72:75]\n"
      "v_fmac_f32 v90, v42, v42\n"
      "v_add_f32 v89, v89, v43\n"
      "v_mfma_f32_16x16x32_bf16 v[64:67], a[96:99], v[100:103], 0\n"
      "v_fmac_f32 v90, v43, v43\n"
      "v_mfma_f32_16x16x32_bf16 v[64:67], a[104:107], v[96:99], v[64:67]\n"
      "v_add_f32 v89, v89, v32\n"
      "v_fmac_f32_e64 v90, v32, v32\n"
      "v_mfma_f32_16x16x32_bf16 v[68:71], a[100:103], v[100:103], 0\n"
      "v_add_f32_e64 v89, v89, v33\n"
      "v_mfma_f32_16x16x32_bf16 v[68:71], a[108:111], v[96:99], v[68:71]\n"
      "ds_read_b128 v[100:103], v6 offset:20736\n"
      "ds_read_b128 v[96:99], v6 offset:22784\n"
      "v_fmac_f32 v90, v33, v33\n"
      "v_add_f32 v89, v89, v34\n"
      "s_waitcnt lgkmcnt(2)\n"
      "v_mfma_f32_16x16x32_bf16 v[52:55], a[64:67], v[92:95], v[52:55]\n"
      "v_fmac_f32 v90, v34, v34\n"
      "v_add_f32 v89, v89, v35\n"
      "v_mfma_f32_16x16x32_bf16 v[52:55], a[72:75], v[80:83], v[52:55]\n"
      "v_fmac_f32 v90, v35, v35\n"
      "v_mfma_f32_16x16x32_bf16 v[72:75], a[68:71], v[92:95], v[72:75]\n"
      "v_add_f32 v89, v89, v60\n"
      "v_fmac_f32 v90, v60, v60\n"
      "v_mfma_f32_16x16x32_bf16 v[72:75], a[76:79], v[80:83], v[72:75]\n"
      "v_add_f32 v89, v89, v61\n"
      "v_mfma_f32_16x16x32_bf16 v[64:67], a[112:115], v[92:95], v[64:67]\n"
      "v_fmac_f32 v90, v61, v61\n"
      "v_add_f32_e64 v89, v89, v62\n"
      "v_mfma_f32_16x16x32_bf16 v[64:67], a[120:123], v[80:83], v[64:67]\n"
      "v_fmac_f32 v90, v62, v62\n"
      "v_add_f32 v89, v89, v63\n"
      "v_mfma_f32_16x16x32_bf16 v[68:71], a[116:119], v[92:95], v[68:71]\n"
      "v_fmac_f32_e64 v90, v63, v63\n"
      "v_mfma_f32_16x16x32_bf16 v[68:71], a[124:127], v[80:83], v[68:71]\n"
      "v_add_f32 v89, v89, v44\n"
      "v_fmac_f32 v90, v44, v44\n"
      "s_waitcnt lgkmcnt(0)\n"
      "v_mfma_f32_16x16x32_bf16 v[52:55], a[80:83], v[100:103], v[52:55]\n"
      "v_add_f32 v89, v89, v45\n"
      "v_mfma_f32_16x16x32_bf16 v[52:55], a[88:91], v[96:99], v[52:55]\n"
      "v_fmac_f32 v90, v45, v45\n"
      "v_add_f32 v89, v89, v46\n"
      "v_mfma_f32_16x16x32_bf16 v[72:75], a[84:87], v[100:103], v[72:75]\n"
      "v_fmac_f32 v90, v46, v46\n"
      "v_add_f32 v89, v89, v47\n"
      "v_mfma_f32_16x16x32_bf16 v[72:75], a[92:95], v[96:99], v[72:75]\n"
      "v_fmac_f32 v90, v47, v47\n"
      "v_mfma_f32_16x16x32_bf16 v[64:67], a[128:131], v[100:103], v[64:67]\n"
      "s_nop 0\n"
      "v_permlane16_swap_b32 v89, v90\n"
      "v_add_f32 v89, v89, v90\n"
      "v_mfma_f32_16x16x32_bf16 v[64:67], a[136:139], v[96:99], v[64:67]\n"
      "ds_read_b32 v80, v1 offset:1280\n"
      "v_mfma_f32_16x16x32_bf16 v[68:71], a[132:135], v[100:103], v[68:71]\n"
      "v_permlane32_swap_b32 v88, v89\n"
      "v_add_f32 v88, v88, v89\n"
      "v_mfma_f32_16x16x32_bf16 v[68:71], a[140:143], v[96:99], v[68:71]\n"
      "s_waitcnt lgkmcnt(0)\n"
      "ds_write_b32 v80, v88\n"
      "v_add_f32 v80, v36, v37\n"
      "v_mul_f32 v81, v36, v36\n"
      "v_fmac_f32 v81, v37, v37\n"
      "v_add_f32 v80, v80, v38\n"
      "v_fmac_f32 v81, v38, v38\n"
      "v_add_f32 v80, v80, v39\n"
      "v_fmac_f32 v81, v39, v39\n"
      "v_add_f32 v80, v80, v48\n"
      "v_fmac_f32 v81, v48, v48\n"
      "v_add_f32 v80, v80, v49\n"
      "v_fmac_f32 v81, v49, v49\n"
      "v_add_f32 v80, v80, v50\n"
      "v_fmac_f32 v81, v50, v50\n"
      "v_add_f32 v80, v80, v51\n"
      "v_fmac_f32 v81, v51, v51\n"
      "v_add_f32 v80, v80, v24\n"
      "v_fmac_f32 v81, v24, v24\n"
      "v_add_f32 v80, v80, v25\n"
      "v_fmac_f32 v81, v25, v25\n"
      "v_add_f32 v80, v80, v26\n"
      "v_fmac_f32 v81, v26, v26\n"
      "v_add_f32 v80, v80, v27\n"
      "v_fmac_f32 v81, v27, v27\n"
      "v_add_f32 v80, v80, v20\n"
      "v_fmac_f32 v81, v20, v20\n"
      "v_add_f32 v80, v80, v21\n"
      "v_fmac_f32 v81, v21, v21\n"
      "v_add_f32 v80, v80, v22\n"
      "v_fmac_f32 v81, v22, v22\n"
      "v_add_f32 v80, v80, v23\n"
      "v_fmac_f32 v81, v23, v23\n"
      "s_nop 1\n"
      "v_permlane16_swap_b32 v80, v81\n"
      "v_add_f32 v80, v80, v81\n"
      "v_add_f32 v81, v52, v53\n"
      "v_mul_f32 v82, v52, v52\n"
      "v_fmac_f32 v82, v53, v53\n"
      "v_add_f32 v81, v81, v54\n"
      "v_fmac_f32 v82, v54, v54\n"
      "v_add_f32 v81, v81, v55\n"
      "v_fmac_f32 v82, v55, v55\n"
      "v_add_f32 v81, v81, v72\n"
      "v_fmac_f32 v82, v72, v72\n"
      "v_add_f32 v81, v81, v73\n"
      "v_fmac_f32 v82, v73, v73\n"
      "v_add_f32 v81, v81, v74\n"
      "v_fmac_f32 v82, v74, v74\n"
      "v_add_f32 v81, v81, v75\n"
      "v_fmac_f32 v82, v75, v75\n"
      "v_add_f32 v81, v81, v64\n"
      "v_fmac_f32 v82, v64, v64\n"
      "v_add_f32 v81, v81, v65\n"
      "v_fmac_f32 v82, v65, v65\n"
      "v_add_f32 v81, v81, v66\n"
      "v_fmac_f32 v82, v66, v66\n"
      "v_add_f32 v81, v81, v67\n"
      "v_fmac_f32 v82, v67, v67\n"
      "v_add_f32 v81, v81, v68\n"
      "v_fmac_f32 v82, v68, v68\n"
      "v_add_f32 v81, v81, v69\n"
      "v_fmac_f32 v82, v69, v69\n"
      "v_add_f32 v81, v81, v70\n"
      "v_fmac_f32 v82, v70, v70\n"
      "v_add_f32 v81, v81, v71\n"
      "v_fmac_f32 v82, v71, v71\n"
      "s_nop 1\n"
      "v_permlane16_swap_b32 v81, v82\n"
      "v_add_f32 v81, v81, v82\n"
      "ds_read_b32 v82, v1 offset:1280\n"
      "s_nop 0\n"
      "v_permlane32_swap_b32 v80, v81\n"
      "v_add_f32_e64 v80, v80, v81\n"
      "s_waitcnt lgkmcnt(0)\n"
      "ds_write_b32 v82, v80 offset:512\n"
      "s_waitcnt lgkmcnt(0)\n"
      "s_barrier\n"
      "ds_read_b128 v[100:103], v84\n"
      "ds_read_b128 v[96:99], v84 offset:1024\n"
      "ds_read_b128 v[80:83], v84 offset:512\n"
      "ds_read_b128 v[88:91], v84 offset:1536\n"
      "s_waitcnt lgkmcnt(3)\n"
      "v_add_f32 v100, v100, v102\n"
      "v_add_f32 v101, v101, v103\n"
      "s_waitcnt lgkmcnt(2)\n"
      "v_add_f32 v96, v96, v98\n"
      "v_add_f32 v97, v97, v99\n"
      "v_add_f32 v100, v100, v96\n"
      "v_add_f32 v101, v101, v97\n"
      "v_fma_f32 v96, -v100, v100, s74\n"
      "v_fmac_f32 v96, 0x43400000, v101\n"
      "v_max_f32 v96, s74, v96\n"
      "v_rsq_f32 v96, v96\n"
      "s_nop 0\n"
      "v_mul_f32 v99, 0x43400000, v96\n"
      "v_mul_f32 v101, -v100, v96\n"
      "v_mov_b32 v96, v99\n"
      "v_mov_b32 v84, v101\n"
      "s_nop 0\n"
      "v_permlane16_swap_b32_e64 v99, v96\n"
      "v_permlane16_swap_b32_e64 v101, v84\n"
      "v_xor_b32_e64 v97, 64, v76\n"
      "v_fma_f32 v16, v16, v99, v101\n"
      "v_fma_f32 v17, v17, v99, v101\n"
      "v_cvt_pk_bf16_f32 v16, v16, v17\n"
      "v_fma_f32 v18, v18, v99, v101\n"
      "v_fma_f32 v19, v19, v99, v101\n"
      "v_cvt_pk_bf16_f32 v17, v18, v19\n"
      "v_fma_f32 v56, v56, v99, v101\n"
      "v_fma_f32 v57, v57, v99, v101\n"
      "v_cvt_pk_bf16_f32 v18, v56, v57\n"
      "v_fma_f32 v58, v58, v99, v101\n"
      "v_fma_f32 v59, v59, v99, v101\n"
      "v_cvt_pk_bf16_f32 v19, v58, v59\n"
      "ds_write_b128 v76, v[16:19] offset:24576\n"
      "v_fma_f32 v12, v12, v99, v101\n"
      "v_fma_f32 v13, v13, v99, v101\n"
      "v_cvt_pk_bf16_f32 v12, v12, v13\n"
      "v_fma_f32 v14, v14, v99, v101\n"
      "v_fma_f32 v15, v15, v99, v101\n"
      "v_cvt_pk_bf16_f32 v13, v14, v15\n"
      "v_fma_f32 v28, v28, v99, v101\n"
      "v_fma_f32 v29, v29, v99, v101\n"
      "v_cvt_pk_bf16_f32 v14, v28, v29\n"
      "v_fma_f32 v30, v30, v99, v101\n"
      "v_fma_f32 v31, v31, v99, v101\n"
      "v_cvt_pk_bf16_f32 v15, v30, v31\n"
      "ds_write_b128 v97, v[12:15] offset:24576\n"
      "v_fma_f32 v40, v40, v96, v84\n"
      "v_fma_f32 v41, v41, v96, v84\n"
      "v_cvt_pk_bf16_f32 v40, v40, v41\n"
      "v_fma_f32 v42, v42, v96, v84\n"
      "v_fma_f32 v43, v43, v96, v84\n"
      "v_cvt_pk_bf16_f32 v41, v42, v43\n"
      "v_fma_f32 v32, v32, v96, v84\n"
      "v_fma_f32 v33, v33, v96, v84\n"
      "v_cvt_pk_bf16_f32 v42, v32, v33\n"
      "v_fma_f32 v34, v34, v96, v84\n"
      "v_fma_f32 v35, v35, v96, v84\n"
      "v_cvt_pk_bf16_f32 v43, v34, v35\n"
      "ds_write_b128 v76, v[40:43] offset:26624\n"
      "v_fma_f32 v60, v60, v96, v84\n"
      "v_fma_f32 v61, v61, v96, v84\n"
      "v_cvt_pk_bf16_f32 v60, v60, v61\n"
      "v_fma_f32 v62, v62, v96, v84\n"
      "v_fma_f32 v63, v63, v96, v84\n"
      "v_cvt_pk_bf16_f32 v61, v62, v63\n"
      "v_fma_f32 v44, v44, v96, v84\n"
      "v_fma_f32 v45, v45, v96, v84\n"
      "v_cvt_pk_bf16_f32 v62, v44, v45\n"
      "v_fma_f32 v46, v46, v96, v84\n"
      "v_fma_f32 v47, v47, v96, v84\n"
      "v_cvt_pk_bf16_f32 v63, v46, v47\n"
      "ds_write_b128 v97, v[60:63] offset:26624\n"
      "v_add_u32_e64 v96, s71, v2\n"
      "v_add_u32_e64 v44, s72, v1\n"
      "ds_read_b32 v47, v44 offset:1792\n"
      "ds_read_b32 v97, v44 offset:2048\n"
      "ds_read_b32 v84, v44 offset:4608\n"
      "ds_read_b32 v46, v44 offset:4864\n"
      "ds_read_b128 v[16:19], v96 offset:24576\n"
      "ds_read_b128 v[12:15], v96 offset:25600\n"
      "ds_read_b128 v[40:43], v96 offset:26624\n"
      "ds_read_b128 v[60:63], v96 offset:27648\n"
      "s_add_u32 s14, s40, s70\n"
      "s_add_u32 s66, s22, s14\n"
      "s_addc_u32 s67, s23, 0\n"
      "s_or_b32 s14, s41, s42\n"
      "s_waitcnt lgkmcnt(4)\n"
      "s_cbranch_scc1 L_edge_85_%=\n"
      "L_eb_86_%=:\n"
      "s_waitcnt lgkmcnt(3)\n"
      "global_store_dwordx4 v47, v[16:19], s[66:67] sc1\n"
      "s_waitcnt lgkmcnt(2)\n"
      "global_store_dwordx4 v97, v[12:15], s[66:67] sc1\n"
      "s_waitcnt lgkmcnt(1)\n"
      "global_store_dwordx4 v84, v[40:43], s[66:67] sc1\n"
      "s_waitcnt lgkmcnt(0)\n"
      "global_store_dwordx4 v46, v[60:63], s[66:67] sc1\n"
      "v_add_f32 v80, v80, v82\n"
      "v_add_f32 v81, v81, v83\n"
      "v_add_f32 v88, v88, v90\n"
      "v_add_f32 v89, v89, v91\n"
      "v_add_f32 v80, v80, v88\n"
      "v_add_f32 v81, v81, v89\n"
      "v_fma_f32 v88, -v80, v80, s74\n"
      "v_fmac_f32 v88, 0x43400000, v81\n"
      "v_max_f32 v88, s74, v88\n"
      "v_rsq_f32 v88, v88\n"
      "s_nop 0\n"
      "v_mul_f32 v89, 0x43400000, v88\n"
      "v_mul_f32 v91, -v80, v88\n"
      "v_mov_b32 v80, v89\n"
      "v_mov_b32 v81, v91\n"
      "s_nop 0\n"
      "v_permlane16_swap_b32 v89, v80\n"
      "v_permlane16_swap_b32 v91, v81\n"
      "v_xor_b32_e64 v88, 64, v76\n"
      "v_fma_f32 v36, v36, v89, v91\n"
      "v_fma_f32 v37, v37, v89, v91\n"
      "v_cvt_pk_bf16_f32 v36, v36, v37\n"
      "v_fma_f32 v38, v38, v89, v91\n"
      "v_fma_f32 v39, v39, v89, v91\n"
      "v_cvt_pk_bf16_f32 v37, v38, v39\n"
      "v_fma_f32 v48, v48, v89, v91\n"
      "v_fma_f32 v49, v49, v89, v91\n"
      "v_cvt_pk_bf16_f32 v38, v48, v49\n"
      "v_fma_f32 v50, v50, v89, v91\n"
      "v_fma_f32 v51, v51, v89, v91\n"
      "v_cvt_pk_bf16_f32 v39, v50, v51\n"
      "ds_write_b128 v76, v[36:39] offset:24576\n"
      "v_fma_f32 v24, v24, v89, v91\n"
      "v_fma_f32 v25, v25, v89, v91\n"
      "v_cvt_pk_bf16_f32 v24, v24, v25\n"
      "v_fma_f32 v26, v26, v89, v91\n"
      "v_fma_f32 v27, v27, v89, v91\n"
      "v_cvt_pk_bf16_f32 v25, v26, v27\n"
      "v_fma_f32 v20, v20, v89, v91\n"
      "v_fma_f32 v21, v21, v89, v91\n"
      "v_cvt_pk_bf16_f32 v26, v20, v21\n"
      "v_fma_f32 v22, v22, v89, v91\n"
      "v_fma_f32 v23, v23, v89, v91\n"
      "v_cvt_pk_bf16_f32 v27, v22, v23\n"
      "ds_write_b128 v88, v[24:27] offset:24576\n"
      "v_fma_f32 v52, v52, v80, v81\n"
      "v_fma_f32 v53, v53, v80, v81\n"
      "v_cvt_pk_bf16_f32 v52, v52, v53\n"
      "v_fma_f32 v54, v54, v80, v81\n"
      "v_fma_f32 v55, v55, v80, v81\n"
      "v_cvt_pk_bf16_f32 v53, v54, v55\n"
      "v_fma_f32 v72, v72, v80, v81\n"
      "v_fma_f32 v73, v73, v80, v81\n"
      "v_cvt_pk_bf16_f32 v54, v72, v73\n"
      "v_fma_f32 v74, v74, v80, v81\n"
      "v_fma_f32 v75, v75, v80, v81\n"
      "v_cvt_pk_bf16_f32 v55, v74, v75\n"
      "ds_write_b128 v76, v[52:55] offset:26624\n"
      "v_fma_f32 v64, v64, v80, v81\n"
      "v_fma_f32 v65, v65, v80, v81\n"
      "v_cvt_pk_bf16_f32 v64, v64, v65\n"
      "v_fma_f32 v66, v66, v80, v81\n"
      "v_fma_f32 v67, v67, v80, v81\n"
      "v_cvt_pk_bf16_f32 v65, v66, v67\n"
      "v_fma_f32 v68, v68, v80, v81\n"
      "v_fma_f32 v69, v69, v80, v81\n"
      "v_cvt_pk_bf16_f32 v66, v68, v69\n"
      "v_fma_f32 v70, v70, v80, v81\n"
      "v_fma_f32 v71, v71, v80, v81\n"
      "v_cvt_pk_bf16_f32 v67, v70, v71\n"
      "ds_write_b128 v88, v[64:67] offset:26624\n"
      "v_add_u32 v67, s71, v2\n"
      "v_add_u32 v88, s72, v1\n"
      "ds_read_b32 v65, v88 offset:2560\n"
      "ds_read_b32 v66, v88 offset:5376\n"
      "ds_read_b32 v64, v88 offset:2560\n"
      "ds_read_b128 v[36:39], v67 offset:24576\n"
      "ds_read_b128 v[24:27], v67 offset:25600\n"
      "ds_read_b128 v[52:55], v67 offset:26624\n"
      "s_add_u32 s14, s40, s70\n"
      "s_add_u32 s66, s22, s14\n"
      "s_addc_u32 s67, s23, 0\n"
      "s_add_u32 s14, s40, s70\n"
      "s_add_u32 s68, s22, s14\n"
      "s_addc_u32 s69, s23, 0\n"
      "s_waitcnt lgkmcnt(3)\n"
      "v_add_u32 v64, 393216, v64\n"
      "s_or_b32 s14, s41, s42\n"
      "s_cbranch_scc1 L_edge_87_%=\n"
      "L_eb_88_%=:\n"
      "s_waitcnt lgkmcnt(2)\n"
      "global_store_dwordx4 v65, v[36:39], s[66:67] sc1\n"
      "s_waitcnt lgkmcnt(1)\n"
      "global_store_dwordx4 v66, v[24:27], s[66:67] sc1\n"
      "s_mov_b32 s34, 0xff\n"
      "s_mov_b32 s35, 0x0\n"
      "s_mov_b64 exec, s[34:35]\n"
      "s_waitcnt lgkmcnt(0)\n"
      "global_store_dwordx4 v64, v[52:55], s[68:69] sc1\n"
      "s_mov_b64 exec, -1\n"
      "s_endpgm\n"
      "L_B1drain_%=:\n"
      "s_nop 7\n"
      "s_nop 7\n"
      "s_waitcnt lgkmcnt(0)\n"
      "v_subrev_u32 v16, 36864, v2\n"
      "v_lshrrev_b32 v17, 9, v16\n"
      "v_bfe_u32 v16, v16, 4, 5\n"
      "v_lshlrev_b32_e64 v17, 4, v17\n"
      "v_mul_u32_u24 v18, 0x180, v16\n"
      "v_add_u32 v17, v17, v18\n"
      "s_mul_i32 s29, s24, 96\n"
      "s_sub_u32 s29, s29, 384\n"
      "v_add_u32 v17, s29, v17\n"
      "v_lshrrev_b32 v19, 1, v16\n"
      "v_lshlrev_b32 v19, 9, v19\n"
      "v_and_b32 v18, 1, v16\n"
      "v_add_u32 v19, v19, v18\n"
      "s_lshl_b32 s29, s25, 1\n"
      "v_add_u32 v19, s29, v19\n"
      "s_add_u32 s29, s25, 5120\n"
      "s_min_u32 s29, s29, 5328\n"
      "v_mov_b32 v18, s29\n"
      "v_cmp_gt_u32 vcc, 20, v16\n"
      "s_nop 1\n"
      "v_cndmask_b32 v19, v18, v19, vcc\n"
      "s_mov_b32 s29, 58835169\n"
      "v_mul_hi_u32 v18, v19, s29\n"
      "v_mul_u32_u24 v16, 73, v18\n"
      "v_sub_u32 v16, v19, v16\n"
      "v_mul_u32_u24 v18, 7, v18\n"
      "v_add_u32_e64 v18, 9, v18\n"
      "v_subrev_u32 v19, 0x1ff, v18\n"
      "v_min_u32 v18, v18, v19\n"
      "v_mul_u32_u24 v16, 7, v16\n"
      "v_add_u32 v16, 9, v16\n"
      "v_subrev_u32 v19, 0x1ff, v16\n"
      "v_min_u32 v16, v16, v19\n"
      "v_mul_u32_u24 v18, 0x1ff, v18\n"
      "v_add_u32 v18, v18, v16\n"
      "v_mul_u32_u24 v18, 0x180, v18\n"
      "s_waitcnt vmcnt(0)\n"
      "ds_read_b128 v[12:15], v6 offset:24576\n"
      "ds_read_b128 v[24:27], v6 offset:26624\n"
      "ds_read_b128 v[20:23], v6 offset:28672\n"
      "ds_read_b128 v[28:31], v6 offset:30720\n"
      "s_waitcnt lgkmcnt(2)\n"
      "v_mfma_f32_16x16x32_bf16 v[32:35], a[48:51], v[12:15], 0\n"
      "v_mfma_f32_16x16x32_bf16 v[32:35], a[56:59], v[24:27], v[32:35]\n"
      "v_mfma_f32_16x16x32_bf16 v[36:39], a[52:55], v[12:15], 0\n"
      "v_mfma_f32_16x16x32_bf16 v[36:39], a[60:63], v[24:27], v[36:39]\n"
      "v_mfma_f32_16x16x32_bf16 v[40:43], a[96:99], v[12:15], 0\n"
      "v_mfma_f32_16x16x32_bf16 v[40:43], a[104:107], v[24:27], v[40:43]\n"
      "v_mfma_f32_16x16x32_bf16 v[44:47], a[100:103], v[12:15], 0\n"
      "v_mfma_f32_16x16x32_bf16 v[44:47], a[108:111], v[24:27], v[44:47]\n"
      "ds_read_b128 v[24:27], v6 offset:32768\n"
      "ds_read_b128 v[12:15], v6 offset:34816\n"
      "s_waitcnt lgkmcnt(2)\n"
      "v_mfma_f32_16x16x32_bf16 v[32:35], a[64:67], v[20:23], v[32:35]\n"
      "v_mfma_f32_16x16x32_bf16 v[32:35], a[72:75], v[28:31], v[32:35]\n"
      "v_mfma_f32_16x16x32_bf16 v[36:39], a[68:71], v[20:23], v[36:39]\n"
      "v_mfma_f32_16x16x32_bf16 v[36:39], a[76:79], v[28:31], v[36:39]\n"
      "v_mfma_f32_16x16x32_bf16 v[40:43], a[112:115], v[20:23], v[40:43]\n"
      "v_mfma_f32_16x16x32_bf16 v[40:43], a[120:123], v[28:31], v[40:43]\n"
      "v_mfma_f32_16x16x32_bf16 v[44:47], a[116:119], v[20:23], v[44:47]\n"
      "v_mfma_f32_16x16x32_bf16 v[44:47], a[124:127], v[28:31], v[44:47]\n"
      "ds_read_b128 v[28:31], v6 offset:24832\n"
      "ds_read_b128 v[20:23], v6 offset:26880\n"
      "s_waitcnt lgkmcnt(2)\n"
      "v_mfma_f32_16x16x32_bf16 v[32:35], a[80:83], v[24:27], v[32:35]\n"
      "v_mfma_f32_16x16x32_bf16 v[32:35], a[88:91], v[12:15], v[32:35]\n"
      "v_mfma_f32_16x16x32_bf16 v[36:39], a[84:87], v[24:27], v[36:39]\n"
      "v_mfma_f32_16x16x32_bf16 v[36:39], a[92:95], v[12:15], v[36:39]\n"
      "v_mfma_f32_16x16x32_bf16 v[40:43], a[128:131], v[24:27], v[40:43]\n"
      "v_mfma_f32_16x16x32_bf16 v[40:43], a[136:139], v[12:15], v[40:43]\n"
      "v_mfma_f32_16x16x32_bf16 v[44:47], a[132:135], v[24:27], v[44:47]\n"
      "v_mfma_f32_16x16x32_bf16 v[44:47], a[140:143], v[12:15], v[44:47]\n"
      "ds_read_b128 v[12:15], v6 offset:28928\n"
      "ds_read_b128 v[24:27], v6 offset:30976\n"
      "s_waitcnt lgkmcnt(2)\n"
      "v_mfma_f32_16x16x32_bf16 v[48:51], a[48:51], v[28:31], 0\n"
      "v_mfma_f32_16x16x32_bf16 v[48:51], a[56:59], v[20:23], v[48:51]\n"
      "v_mfma_f32_16x16x32_bf16 v[52:55], a[52:55], v[28:31], 0\n"
      "v_mfma_f32_16x16x32_bf16 v[52:55], a[60:63], v[20:23], v[52:55]\n"
      "v_mfma_f32_16x16x32_bf16 v[56:59], a[96:99], v[28:31], 0\n"
      "v_mfma_f32_16x16x32_bf16 v[56:59], a[104:107], v[20:23], v[56:59]\n"
      "v_mfma_f32_16x16x32_bf16 v[60:63], a[100:103], v[28:31], 0\n"
      "v_mfma_f32_16x16x32_bf16 v[60:63], a[108:111], v[20:23], v[60:63]\n"
      "ds_read_b128 v[20:23], v6 offset:33024\n"
      "ds_read_b128 v[28:31], v6 offset:35072\n"
      "s_waitcnt lgkmcnt(2)\n"
      "v_mfma_f32_16x16x32_bf16 v[48:51], a[64:67], v[12:15], v[48:51]\n"
      "v_mfma_f32_16x16x32_bf16 v[48:51], a[72:75], v[24:27], v[48:51]\n"
      "v_mfma_f32_16x16x32_bf16 v[52:55], a[68:71], v[12:15], v[52:55]\n"
      "v_mfma_f32_16x16x32_bf16 v[52:55], a[76:79], v[24:27], v[52:55]\n"
      "v_mfma_f32_16x16x32_bf16 v[56:59], a[112:115], v[12:15], v[56:59]\n"
      "v_mfma_f32_16x16x32_bf16 v[56:59], a[120:123], v[24:27], v[56:59]\n"
      "v_mfma_f32_16x16x32_bf16 v[60:63], a[116:119], v[12:15], v[60:63]\n"
      "v_mfma_f32_16x16x32_bf16 v[60:63], a[124:127], v[24:27], v[60:63]\n"
      "ds_read_b128 v[24:27], v6 offset:12544\n"
      "ds_read_b128 v[12:15], v6 offset:14592\n"
      "s_waitcnt lgkmcnt(2)\n"
      "v_mfma_f32_16x16x32_bf16 v[48:51], a[80:83], v[20:23], v[48:51]\n"
      "v_mfma_f32_16x16x32_bf16 v[48:51], a[88:91], v[28:31], v[48:51]\n"
      "v_mfma_f32_16x16x32_bf16 v[52:55], a[84:87], v[20:23], v[52:55]\n"
      "v_mfma_f32_16x16x32_bf16 v[52:55], a[92:95], v[28:31], v[52:55]\n"
      "v_mfma_f32_16x16x32_bf16 v[56:59], a[128:131], v[20:23], v[56:59]\n"
      "v_mfma_f32_16x16x32_bf16 v[56:59], a[136:139], v[28:31], v[56:59]\n"
      "v_mfma_f32_16x16x32_bf16 v[60:63], a[132:135], v[20:23], v[60:63]\n"
      "v_mfma_f32_16x16x32_bf16 v[60:63], a[140:143], v[28:31], v[60:63]\n"
      "ds_read_b128 v[28:31], v6 offset:16640\n"
      "ds_read_b128 v[20:23], v6 offset:18688\n"
      "s_waitcnt lgkmcnt(2)\n"
      "v_mfma_f32_16x16x32_bf16 v[64:67], a[48:51], v[24:27], 0\n"
      "v_mfma_f32_16x16x32_bf16 v[64:67], a[56:59], v[12:15], v[64:67]\n"
      "v_mfma_f32_16x16x32_bf16 v[68:71], a[52:55], v[24:27], 0\n"
      "v_mfma_f32_16x16x32_bf16 v[68:71], a[60:63], v[12:15], v[68:71]\n"
      "v_mfma_f32_16x16x32_bf16 v[72:75], a[96:99], v[24:27], 0\n"
      "v_add_f32 v76, v32, v33\n"
      "v_mul_f32 v77, v32, v32\n"
      "v_fmac_f32 v77, v33, v33\n"
      "v_add_f32 v76, v76, v34\n"
      "v_mfma_f32_16x16x32_bf16 v[72:75], a[104:107], v[12:15], v[72:75]\n"
      "v_fmac_f32 v77, v34, v34\n"
      "v_add_f32 v76, v76, v35\n"
      "v_fmac_f32 v77, v35, v35\n"
      "v_mfma_f32_16x16x32_bf16 v[80:83], a[100:103], v[24:27], 0\n"
      "v_add_f32 v76, v76, v36\n"
      "v_fmac_f32 v77, v36, v36\n"
      "v_add_f32 v76, v76, v37\n"
      "v_fmac_f32 v77, v37, v37\n"
      "v_mfma_f32_16x16x32_bf16 v[80:83], a[108:111], v[12:15], v[80:83]\n"
      "ds_read_b128 v[12:15], v6 offset:20736\n"
      "ds_read_b128 v[24:27], v6 offset:22784\n"
      "v_add_f32 v76, v76, v38\n"
      "v_fmac_f32 v77, v38, v38\n"
      "v_add_f32 v76, v76, v39\n"
      "s_waitcnt lgkmcnt(2)\n"
      "v_mfma_f32_16x16x32_bf16 v[64:67], a[64:67], v[28:31], v[64:67]\n"
      "v_fmac_f32 v77, v39, v39\n"
      "v_add_f32 v76, v76, v40\n"
      "v_fmac_f32 v77, v40, v40\n"
      "v_add_f32 v76, v76, v41\n"
      "v_mfma_f32_16x16x32_bf16 v[64:67], a[72:75], v[20:23], v[64:67]\n"
      "v_fmac_f32 v77, v41, v41\n"
      "v_add_f32 v76, v76, v42\n"
      "v_fmac_f32 v77, v42, v42\n"
      "v_mfma_f32_16x16x32_bf16 v[68:71], a[68:71], v[28:31], v[68:71]\n"
      "v_add_f32 v76, v76, v43\n"
      "v_fmac_f32 v77, v43, v43\n"
      "v_add_f32 v76, v76, v44\n"
      "v_fmac_f32 v77, v44, v44\n"
      "v_mfma_f32_16x16x32_bf16 v[68:71], a[76:79], v[20:23], v[68:71]\n"
      "v_add_f32 v76, v76, v45\n"
      "v_fmac_f32 v77, v45, v45\n"
      "v_add_f32 v76, v76, v46\n"
      "v_mfma_f32_16x16x32_bf16 v[72:75], a[112:115], v[28:31], v[72:75]\n"
      "v_fmac_f32 v77, v46, v46\n"
      "v_add_f32 v76, v76, v47\n"
      "v_fmac_f32 v77, v47, v47\n"
      "s_nop 1\n"
      "v_permlane16_swap_b32 v76, v77\n"
      "v_mfma_f32_16x16x32_bf16 v[72:75], a[120:123], v[20:23], v[72:75]\n"
      "v_add_f32 v76, v76, v77\n"
      "v_add_f32 v77, v48, v49\n"
      "v_mul_f32 v78, v48, v48\n"
      "v_mfma_f32_16x16x32_bf16 v[80:83], a[116:119], v[28:31], v[80:83]\n"
      "v_fmac_f32 v78, v49, v49\n"
      "v_add_f32 v77, v77, v50\n"
      "v_fmac_f32 v78, v50, v50\n"
      "v_add_f32 v77, v77, v51\n"
      "v_mfma_f32_16x16x32_bf16 v[80:83], a[124:127], v[20:23], v[80:83]\n"
      "v_fmac_f32 v78, v51, v51\n"
      "v_add_f32 v77, v77, v52\n"
      "v_fmac_f32 v78, v52, v52\n"
      "s_waitcnt lgkmcnt(0)\n"
      "v_mfma_f32_16x16x32_bf16 v[64:67], a[80:83], v[12:15], v[64:67]\n"
      "v_add_f32 v77, v77, v53\n"
      "v_fmac_f32 v78, v53, v53\n"
      "v_add_f32 v77, v77, v54\n"
      "v_fmac_f32 v78, v54, v54\n"
      "v_mfma_f32_16x16x32_bf16 v[64:67], a[88:91], v[24:27], v[64:67]\n"
      "v_add_f32 v77, v77, v55\n"
      "v_fmac_f32 v78, v55, v55\n"
      "v_add_f32 v77, v77, v56\n"
      "v_mfma_f32_16x16x32_bf16 v[68:71], a[84:87], v[12:15], v[68:71]\n"
      "v_fmac_f32 v78, v56, v56\n"
      "v_add_f32 v77, v77, v57\n"
      "v_fmac_f32 v78, v57, v57\n"
      "v_add_f32_e64 v77, v77, v58\n"
      "v_mfma_f32_16x16x32_bf16 v[68:71], a[92:95], v[24:27], v[68:71]\n"
      "v_fmac_f32 v78, v58, v58\n"
      "v_add_f32 v77, v77, v59\n"
      "v_fmac_f32 v78, v59, v59\n"
      "v_mfma_f32_16x16x32_bf16 v[72:75], a[128:131], v[12:15], v[72:75]\n"
      "v_add_f32 v77, v77, v60\n"
      "v_fmac_f32 v78, v60, v60\n"
      "v_add_f32 v77, v77, v61\n"
      "v_fmac_f32 v78, v61, v61\n"
      "v_mfma_f32_16x16x32_bf16 v[72:75], a[136:139], v[24:27], v[72:75]\n"
      "v_add_f32 v77, v77, v62\n"
      "v_fmac_f32 v78, v62, v62\n"
      "v_add_f32 v77, v77, v63\n"
      "v_mfma_f32_16x16x32_bf16 v[80:83], a[132:135], v[12:15], v[80:83]\n"
      "v_fmac_f32 v78, v63, v63\n"
      "s_nop 1\n"
      "v_permlane16_swap_b32 v77, v78\n"
      "v_add_f32 v77, v77, v78\n"
      "ds_read_b32 v78, v1 offset:1280\n"
      "v_mfma_f32_16x16x32_bf16 v[80:83], a[140:143], v[24:27], v[80:83]\n"
      "v_permlane32_swap_b32 v76, v77\n"
      "v_add_f32 v76, v76, v77\n"
      "s_waitcnt lgkmcnt(0)\n"
      "ds_write_b32 v78, v76\n"
      "v_add_f32 v76, v64, v65\n"
      "v_mul_f32 v77, v64, v64\n"
      "v_fmac_f32 v77, v65, v65\n"
      "v_add_f32 v76, v76, v66\n"
      "v_fmac_f32 v77, v66, v66\n"
      "v_add_f32 v76, v76, v67\n"
      "v_fmac_f32 v77, v67, v67\n"
      "v_add_f32 v76, v76, v68\n"
      "v_fmac_f32 v77, v68, v68\n"
      "v_add_f32 v76, v76, v69\n"
      "v_fmac_f32 v77, v69, v69\n"
      "v_add_f32 v76, v76, v70\n"
      "v_fmac_f32 v77, v70, v70\n"
      "v_add_f32 v76, v76, v71\n"
      "v_fmac_f32 v77, v71, v71\n"
      "v_add_f32 v76, v76, v72\n"
      "v_fmac_f32 v77, v72, v72\n"
      "v_add_f32 v76, v76, v73\n"
      "v_fmac_f32 v77, v73, v73\n"
      "v_add_f32 v76, v76, v74\n"
      "v_fmac_f32 v77, v74, v74\n"
      "v_add_f32 v76, v76, v75\n"
      "v_fmac_f32 v77, v75, v75\n"
      "v_add_f32 v76, v76, v80\n"
      "v_fmac_f32 v77, v80, v80\n"
      "v_add_f32 v76, v76, v81\n"
      "v_fmac_f32 v77, v81, v81\n"
      "v_add_f32 v76, v76, v82\n"
      "v_fmac_f32 v77, v82, v82\n"
      "v_add_f32 v76, v76, v83\n"
      "v_fmac_f32 v77, v83, v83\n"
      "s_nop 1\n"
      "v_permlane16_swap_b32 v76, v77\n"
      "v_add_f32 v76, v76, v77\n"
      "ds_read_b32 v78, v1 offset:1280\n"
      "v_mov_b32 v77, v76\n"
      "s_nop 1\n"
      "v_permlane32_swap_b32 v76, v77\n"
      "v_add_f32 v76, v76, v77\n"
      "s_waitcnt lgkmcnt(0)\n"
      "ds_write_b32 v78, v76 offset:512\n"
      "s_waitcnt lgkmcnt(0)\n"
      "s_barrier\n"
      "s_mul_i32 s29, s25, 12288\n"
      "s_add_u32 s29, s29, 12582912\n"
      "s_add_u32 s34, s20, s29\n"
      "s_addc_u32 s35, s21, 0\n"
      "s_mul_i32 s30, s33, 3\n"
      "s_nop 0\n"
      "s_mov_b32 exec_lo, 0xfffff\n"
      "s_mov_b32 exec_hi, 0xfffff\n"
      "s_add_u32 m0, s30, 36864\n"
      "s_nop 0\n"
      "global_load_lds_dwordx4 v17, s[34:35]\n"
      "s_add_u32 m0, s30, 37856\n"
      "s_nop 0\n"
      "global_load_lds_dwordx4 v17, s[34:35] offset:32\n"
      "s_add_u32 m0, s30, 38848\n"
      "s_nop 0\n"
      "global_load_lds_dwordx4 v17, s[34:35] offset:64\n"
      "s_mov_b64 exec, -1\n"
      "ds_read_b32 v17, v1 offset:1536\n"
      "v_add_u32 v76, 108800, v2\n"
      "ds_read_b128 v[24:27], v76\n"
      "ds_read_b128 v[12:15], v76 offset:1024\n"
      "ds_read_b32 v76, v1\n"
      "s_waitcnt lgkmcnt(2)\n"
      "v_add_f32 v24, v24, v26\n"
      "v_add_f32 v25, v25, v27\n"
      "s_waitcnt lgkmcnt(1)\n"
      "v_add_f32 v12, v12, v14\n"
      "v_add_f32 v13, v13, v15\n"
      "v_add_f32 v24, v24, v12\n"
      "v_add_f32 v25, v25, v13\n"
      "v_fma_f32 v12, -v24, v24, s74\n"
      "v_fmac_f32 v12, 0x43400000, v25\n"
      "v_max_f32 v12, s74, v12\n"
      "v_rsq_f32 v12, v12\n"
      "s_nop 0\n"
      "v_mul_f32 v25, 0x43400000, v12\n"
      "v_mul_f32 v13, -v24, v12\n"
      "v_mov_b32 v12, v25\n"
      "v_mov_b32 v24, v13\n"
      "s_nop 0\n"
      "v_permlane32_swap_b32 v25, v12\n"
      "v_permlane32_swap_b32 v13, v24\n"
      "v_mov_b32 v15, v25\n"
      "v_mov_b32 v14, v13\n"
      "s_nop 0\n"
      "v_permlane16_swap_b32 v25, v15\n"
      "v_permlane16_swap_b32 v13, v14\n"
      "s_waitcnt lgkmcnt(0)\n"
      "v_xor_b32_e64 v27, 64, v76\n"
      "v_fma_f32 v32, v32, v25, v13\n"
      "v_fma_f32 v33, v33, v25, v13\n"
      "v_cvt_pk_bf16_f32 v32, v32, v33\n"
      "v_fma_f32 v34, v34, v25, v13\n"
      "v_fma_f32 v35, v35, v25, v13\n"
      "v_cvt_pk_bf16_f32 v33, v34, v35\n"
      "v_fma_f32 v36, v36, v25, v13\n"
      "v_fma_f32 v37, v37, v25, v13\n"
      "v_cvt_pk_bf16_f32 v34, v36, v37\n"
      "v_fma_f32 v38, v38, v25, v13\n"
      "v_fma_f32 v39, v39, v25, v13\n"
      "v_cvt_pk_bf16_f32 v35, v38, v39\n"
      "ds_write_b128 v76, v[32:35] offset:24576\n"
      "v_fma_f32 v40, v40, v25, v13\n"
      "v_fma_f32 v41, v41, v25, v13\n"
      "v_cvt_pk_bf16_f32 v40, v40, v41\n"
      "v_fma_f32 v42, v42, v25, v13\n"
      "v_fma_f32 v43, v43, v25, v13\n"
      "v_cvt_pk_bf16_f32 v41, v42, v43\n"
      "v_fma_f32 v44, v44, v25, v13\n"
      "v_fma_f32 v45, v45, v25, v13\n"
      "v_cvt_pk_bf16_f32 v42, v44, v45\n"
      "v_fma_f32 v46, v46, v25, v13\n"
      "v_fma_f32 v47, v47, v25, v13\n"
      "v_cvt_pk_bf16_f32 v43, v46, v47\n"
      "ds_write_b128 v27, v[40:43] offset:24576\n"
      "v_fma_f32 v48, v48, v15, v14\n"
      "v_fma_f32 v49, v49, v15, v14\n"
      "v_cvt_pk_bf16_f32 v48, v48, v49\n"
      "v_fma_f32 v50, v50, v15, v14\n"
      "v_fma_f32 v51, v51, v15, v14\n"
      "v_cvt_pk_bf16_f32 v49, v50, v51\n"
      "v_fma_f32 v52, v52, v15, v14\n"
      "v_fma_f32 v53, v53, v15, v14\n"
      "v_cvt_pk_bf16_f32 v50, v52, v53\n"
      "v_fma_f32 v54, v54, v15, v14\n"
      "v_fma_f32 v55, v55, v15, v14\n"
      "v_cvt_pk_bf16_f32 v51, v54, v55\n"
      "ds_write_b128 v76, v[48:51] offset:26624\n"
      "v_fma_f32 v56, v56, v15, v14\n"
      "v_fma_f32 v57, v57, v15, v14\n"
      "v_cvt_pk_bf16_f32 v56, v56, v57\n"
      "v_fma_f32 v58, v58, v15, v14\n"
      "v_fma_f32 v59, v59, v15, v14\n"
      "v_cvt_pk_bf16_f32 v57, v58, v59\n"
      "v_fma_f32 v60, v60, v15, v14\n"
      "v_fma_f32 v61, v61, v15, v14\n"
      "v_cvt_pk_bf16_f32 v58, v60, v61\n"
      "v_fma_f32 v62, v62, v15, v14\n"
      "v_fma_f32 v63, v63, v15, v14\n"
      "v_cvt_pk_bf16_f32 v59, v62, v63\n"
      "ds_write_b128 v27, v[56:59] offset:26624\n"
      "v_add_u32_e64 v27, s71, v2\n"
      "v_add_u32_e64 v62, s72, v1\n"
      "ds_read_b32 v63, v62 offset:1792\n"
      "ds_read_b32 v14, v62 offset:2048\n"
      "ds_read_b32 v15, v62 offset:4608\n"
      "ds_read_b32 v60, v62 offset:4864\n"
      "ds_read_b128 v[32:35], v27 offset:24576\n"
      "ds_read_b128 v[40:43], v27 offset:25600\n"
      "ds_read_b128 v[48:51], v27 offset:26624\n"
      "ds_read_b128 v[56:59], v27 offset:27648\n"
      "s_add_u32 s14, s49, s70\n"
      "s_add_u32 s66, s22, s14\n"
      "s_addc_u32 s67, s23, 0\n"
      "s_or_b32 s14, s50, s51\n"
      "s_waitcnt lgkmcnt(4)\n"
      "s_cbranch_scc1 L_edge_89_%=\n"
      "L_eb_90_%=:\n"
      "s_waitcnt lgkmcnt(3)\n"
      "global_store_dwordx4 v63, v[32:35], s[66:67] sc1\n"
      "s_waitcnt lgkmcnt(2)\n"
      "global_store_dwordx4 v14, v[40:43], s[66:67] sc1\n"
      "s_waitcnt lgkmcnt(1)\n"
      "global_store_dwordx4 v15, v[48:51], s[66:67] sc1\n"
      "s_waitcnt lgkmcnt(0)\n"
      "global_store_dwordx4 v60, v[56:59], s[66:67] sc1\n"
      "s_nop 1\n"
      "v_xor_b32 v56, 64, v76\n"
      "v_fma_f32 v64, v64, v12, v24\n"
      "v_fma_f32 v65, v65, v12, v24\n"
      "v_cvt_pk_bf16_f32 v64, v64, v65\n"
      "v_fma_f32 v66, v66, v12, v24\n"
      "v_fma_f32 v67, v67, v12, v24\n"
      "v_cvt_pk_bf16_f32 v65, v66, v67\n"
      "v_fma_f32 v68, v68, v12, v24\n"
      "v_fma_f32 v69, v69, v12, v24\n"
      "v_cvt_pk_bf16_f32 v66, v68, v69\n"
      "v_fma_f32 v70, v70, v12, v24\n"
      "v_fma_f32 v71, v71, v12, v24\n"
      "v_cvt_pk_bf16_f32 v67, v70, v71\n"
      "ds_write_b128 v76, v[64:67] offset:24576\n"
      "v_fma_f32 v72, v72, v12, v24\n"
      "v_fma_f32 v73, v73, v12, v24\n"
      "v_cvt_pk_bf16_f32 v72, v72, v73\n"
      "v_fma_f32 v74, v74, v12, v24\n"
      "v_fma_f32 v75, v75, v12, v24\n"
      "v_cvt_pk_bf16_f32 v73, v74, v75\n"
      "v_fma_f32 v80, v80, v12, v24\n"
      "v_fma_f32 v81, v81, v12, v24\n"
      "v_cvt_pk_bf16_f32 v74, v80, v81\n"
      "v_fma_f32 v82, v82, v12, v24\n"
      "v_fma_f32 v83, v83, v12, v24\n"
      "v_cvt_pk_bf16_f32 v75, v82, v83\n"
      "ds_write_b128 v56, v[72:75] offset:24576\n"
      "v_add_u32_e64 v56, s71, v2\n"
      "v_add_u32 v82, s72, v1\n"
      "ds_read_b32 v83, v82 offset:2560\n"
      "ds_read_b32 v12, v82 offset:5376\n"
      "ds_read_b128 v[64:67], v56 offset:24576\n"
      "ds_read_b128 v[72:75], v56 offset:25600\n"
      "s_add_u32 s14, s49, s70\n"
      "s_add_u32 s66, s22, s14\n"
      "s_addc_u32 s67, s23, 0\n"
      "s_or_b32 s14, s50, s51\n"
      "s_waitcnt lgkmcnt(2)\n"
      "s_cbranch_scc1 L_edge_91_%=\n"
      "L_eb_92_%=:\n"
      "s_waitcnt lgkmcnt(1)\n"
      "global_store_dwordx4 v83, v[64:67], s[66:67] sc1\n"
      "s_waitcnt lgkmcnt(0)\n"
      "global_store_dwordx4 v12, v[72:75], s[66:67] sc1\n"
      "s_waitcnt vmcnt(6)\n"
      "s_waitcnt lgkmcnt(0)\n"
      "s_barrier\n"
      "ds_read_b128 v[72:75], v6\n"
      "ds_read_b128 v[64:67], v6 offset:2048\n"
      "ds_read_b128 v[80:83], v6 offset:4096\n"
      "ds_read_b128 v[68:71], v6 offset:6144\n"
      "s_waitcnt lgkmcnt(2)\n"
      "v_mfma_f32_16x16x32_bf16 v[56:59], a[48:51], v[72:75], 0\n"
      "v_mfma_f32_16x16x32_bf16 v[56:59], a[56:59], v[64:67], v[56:59]\n"
      "v_mfma_f32_16x16x32_bf16 v[48:51], a[52:55], v[72:75], 0\n"
      "v_mfma_f32_16x16x32_bf16 v[48:51], a[60:63], v[64:67], v[48:51]\n"
      "v_mfma_f32_16x16x32_bf16 v[40:43], a[96:99], v[72:75], 0\n"
      "v_mfma_f32_16x16x32_bf16 v[40:43], a[104:107], v[64:67], v[40:43]\n"
      "v_mfma_f32_16x16x32_bf16 v[12:15], a[100:103], v[72:75], 0\n"
      "v_mfma_f32_16x16x32_bf16 v[12:15], a[108:111], v[64:67], v[12:15]\n"
      "ds_read_b128 v[64:67], v6 offset:8192\n"
      "ds_read_b128 v[72:75], v6 offset:10240\n"
      "s_waitcnt lgkmcnt(2)\n"
      "v_mfma_f32_16x16x32_bf16 v[56:59], a[64:67], v[80:83], v[56:59]\n"
      "v_mfma_f32_16x16x32_bf16 v[56:59], a[72:75], v[68:71], v[56:59]\n"
      "v_mfma_f32_16x16x32_bf16 v[48:51], a[68:71], v[80:83], v[48:51]\n"
      "v_mfma_f32_16x16x32_bf16 v[48:51], a[76:79], v[68:71], v[48:51]\n"
      "v_mfma_f32_16x16x32_bf16 v[40:43], a[112:115], v[80:83], v[40:43]\n"
      "v_mfma_f32_16x16x32_bf16 v[40:43], a[120:123], v[68:71], v[40:43]\n"
      "v_mfma_f32_16x16x32_bf16 v[12:15], a[116:119], v[80:83], v[12:15]\n"
      "v_mfma_f32_16x16x32_bf16 v[12:15], a[124:127], v[68:71], v[12:15]\n"
      "ds_read_b128 v[68:71], v6 offset:256\n"
      "ds_read_b128 v[80:83], v6 offset:2304\n"
      "s_waitcnt lgkmcnt(2)\n"
      "v_mfma_f32_16x16x32_bf16 v[56:59], a[80:83], v[64:67], v[56:59]\n"
      "v_mfma_f32_16x16x32_bf16 v[56:59], a[88:91], v[72:75], v[56:59]\n"
      "v_mfma_f32_16x16x32_bf16 v[48:51], a[84:87], v[64:67], v[48:51]\n"
      "v_mfma_f32_16x16x32_bf16 v[48:51], a[92:95], v[72:75], v[48:51]\n"
      "v_mfma_f32_16x16x32_bf16 v[40:43], a[128:131], v[64:67], v[40:43]\n"
      "v_mfma_f32_16x16x32_bf16 v[40:43], a[136:139], v[72:75], v[40:43]\n"
      "v_mfma_f32_16x16x32_bf16 v[12:15], a[132:135], v[64:67], v[12:15]\n"
      "v_mfma_f32_16x16x32_bf16 v[12:15], a[140:143], v[72:75], v[12:15]\n"
      "ds_read_b128 v[72:75], v6 offset:4352\n"
      "ds_read_b128 v[64:67], v6 offset:6400\n"
      "s_waitcnt lgkmcnt(2)\n"
      "v_mfma_f32_16x16x32_bf16 v[32:35], a[48:51], v[68:71], 0\n"
      "v_mfma_f32_16x16x32_bf16 v[32:35], a[56:59], v[80:83], v[32:35]\n"
      "v_mfma_f32_16x16x32_bf16 v[60:63], a[52:55], v[68:71], 0\n"
      "v_mfma_f32_16x16x32_bf16 v[60:63], a[60:63], v[80:83], v[60:63]\n"
      "v_mfma_f32_16x16x32_bf16 v[24:27], a[96:99], v[68:71], 0\n"
      "v_mfma_f32_16x16x32_bf16 v[24:27], a[104:107], v[80:83], v[24:27]\n"
      "v_mfma_f32_16x16x32_bf16 v[52:55], a[100:103], v[68:71], 0\n"
      "v_mfma_f32_16x16x32_bf16 v[52:55], a[108:111], v[80:83], v[52:55]\n"
      "ds_read_b128 v[80:83], v6 offset:8448\n"
      "ds_read_b128 v[68:71], v6 offset:10496\n"
      "s_waitcnt lgkmcnt(2)\n"
      "v_mfma_f32_16x16x32_bf16 v[32:35], a[64:67], v[72:75], v[32:35]\n"
      "v_mfma_f32_16x16x32_bf16 v[32:35], a[72:75], v[64:67], v[32:35]\n"
      "v_mfma_f32_16x16x32_bf16 v[60:63], a[68:71], v[72:75], v[60:63]\n"
      "v_mfma_f32_16x16x32_bf16 v[60:63], a[76:79], v[64:67], v[60:63]\n"
      "v_mfma_f32_16x16x32_bf16 v[24:27], a[112:115], v[72:75], v[24:27]\n"
      "v_mfma_f32_16x16x32_bf16 v[24:27], a[120:123], v[64:67], v[24:27]\n"
      "v_mfma_f32_16x16x32_bf16 v[52:55], a[116:119], v[72:75], v[52:55]\n"
      "v_mfma_f32_16x16x32_bf16 v[52:55], a[124:127], v[64:67], v[52:55]\n"
      "s_waitcnt lgkmcnt(0)\n"
      "v_mfma_f32_16x16x32_bf16 v[32:35], a[80:83], v[80:83], v[32:35]\n"
      "v_mfma_f32_16x16x32_bf16 v[32:35], a[88:91], v[68:71], v[32:35]\n"
      "v_mfma_f32_16x16x32_bf16 v[60:63], a[84:87], v[80:83], v[60:63]\n"
      "v_mfma_f32_16x16x32_bf16 v[60:63], a[92:95], v[68:71], v[60:63]\n"
      "v_mfma_f32_16x16x32_bf16 v[24:27], a[128:131], v[80:83], v[24:27]\n"
      "v_mfma_f32_16x16x32_bf16 v[24:27], a[136:139], v[68:71], v[24:27]\n"
      "v_mfma_f32_16x16x32_bf16 v[52:55], a[132:135], v[80:83], v[52:55]\n"
      "v_mfma_f32_16x16x32_bf16 v[52:55], a[140:143], v[68:71], v[52:55]\n"
      "v_add_f32 v68, v56, v57\n"
      "v_mul_f32 v69, v56, v56\n"
      "v_fmac_f32 v69, v57, v57\n"
      "v_add_f32 v68, v68, v58\n"
      "v_fmac_f32 v69, v58, v58\n"
      "v_add_f32 v68, v68, v59\n"
      "v_fmac_f32 v69, v59, v59\n"
      "v_add_f32 v68, v68, v48\n"
      "v_fmac_f32 v69, v48, v48\n"
      "v_add_f32 v68, v68, v49\n"
      "v_fmac_f32 v69, v49, v49\n"
      "v_add_f32 v68, v68, v50\n"
      "v_fmac_f32 v69, v50, v50\n"
      "v_add_f32 v68, v68, v51\n"
      "v_fmac_f32 v69, v51, v51\n"
      "v_add_f32 v68, v68, v40\n"
      "v_fmac_f32 v69, v40, v40\n"
      "v_add_f32 v68, v68, v41\n"
      "v_fmac_f32 v69, v41, v41\n"
      "v_add_f32 v68, v68, v42\n"
      "v_fmac_f32 v69, v42, v42\n"
      "v_add_f32 v68, v68, v43\n"
      "v_fmac_f32 v69, v43, v43\n"
      "v_add_f32 v68, v68, v12\n"
      "v_fmac_f32 v69, v12, v12\n"
      "v_add_f32 v68, v68, v13\n"
      "v_fmac_f32 v69, v13, v13\n"
      "v_add_f32 v68, v68, v14\n"
      "v_fmac_f32 v69, v14, v14\n"
      "v_add_f32 v68, v68, v15\n"
      "v_fmac_f32 v69, v15, v15\n"
      "s_nop 1\n"
      "v_permlane16_swap_b32 v68, v69\n"
      "v_add_f32 v68, v68, v69\n"
      "v_add_f32 v69, v32, v33\n"
      "v_mul_f32 v70, v32, v32\n"
      "v_fmac_f32 v70, v33, v33\n"
      "v_add_f32 v69, v69, v34\n"
      "v_fmac_f32 v70, v34, v34\n"
      "v_add_f32 v69, v69, v35\n"
      "v_fmac_f32 v70, v35, v35\n"
      "v_add_f32 v69, v69, v60\n"
      "v_fmac_f32 v70, v60, v60\n"
      "v_add_f32 v69, v69, v61\n"
      "v_fmac_f32 v70, v61, v61\n"
      "v_add_f32 v69, v69, v62\n"
      "v_fmac_f32 v70, v62, v62\n"
      "v_add_f32 v69, v69, v63\n"
      "v_fmac_f32 v70, v63, v63\n"
      "v_add_f32 v69, v69, v24\n"
      "v_fmac_f32 v70, v24, v24\n"
      "v_add_f32 v69, v69, v25\n"
      "v_fmac_f32 v70, v25, v25\n"
      "v_add_f32 v69, v69, v26\n"
      "v_fmac_f32 v70, v26, v26\n"
      "v_add_f32 v69, v69, v27\n"
      "v_fmac_f32 v70, v27, v27\n"
      "v_add_f32 v69, v69, v52\n"
      "v_fmac_f32 v70, v52, v52\n"
      "v_add_f32 v69, v69, v53\n"
      "v_fmac_f32 v70, v53, v53\n"
      "v_add_f32 v69, v69, v54\n"
      "v_fmac_f32 v70, v54, v54\n"
      "v_add_f32 v69, v69, v55\n"
      "v_fmac_f32 v70, v55, v55\n"
      "s_nop 1\n"
      "v_permlane16_swap_b32 v69, v70\n"
      "v_add_f32 v69, v69, v70\n"
      "ds_read_b32 v70, v1 offset:1280\n"
      "s_nop 0\n"
      "v_permlane32_swap_b32 v68, v69\n"
      "v_add_f32 v68, v68, v69\n"
      "s_waitcnt lgkmcnt(0)\n"
      "ds_write_b32 v70, v68\n"
      "s_waitcnt lgkmcnt(0)\n"
      "s_barrier\n"
      "ds_read_b128 v[68:71], v17\n"
      "ds_read_b128 v[80:83], v17 offset:1024\n"
      "ds_read_b32 v17, v1\n"
      "s_waitcnt lgkmcnt(2)\n"
      "v_add_f32 v68, v68, v70\n"
      "v_add_f32 v69, v69, v71\n"
      "s_waitcnt lgkmcnt(1)\n"
      "v_add_f32 v80, v80, v82\n"
      "v_add_f32 v81, v81, v83\n"
      "v_add_f32 v68, v68, v80\n"
      "v_add_f32 v69, v69, v81\n"
      "v_fma_f32 v80, -v68, v68, s74\n"
      "v_fmac_f32 v80, 0x43400000, v69\n"
      "v_max_f32 v80, s74, v80\n"
      "v_rsq_f32 v80, v80\n"
      "s_nop 0\n"
      "v_mul_f32 v69, 0x43400000, v80\n"
      "v_mul_f32 v81, -v68, v80\n"
      "v_mov_b32 v68, v69\n"
      "v_mov_b32 v80, v81\n"
      "s_nop 0\n"
      "v_permlane16_swap_b32 v69, v68\n"
      "v_permlane16_swap_b32 v81, v80\n"
      "s_waitcnt lgkmcnt(0)\n"
      "v_xor_b32 v83, 64, v17\n"
      "v_fma_f32 v56, v56, v69, v81\n"
      "v_fma_f32 v57, v57, v69, v81\n"
      "v_cvt_pk_bf16_f32 v56, v56, v57\n"
      "v_fma_f32 v58, v58, v69, v81\n"
      "v_fma_f32 v59, v59, v69, v81\n"
      "v_cvt_pk_bf16_f32 v57, v58, v59\n"
      "v_fma_f32 v48, v48, v69, v81\n"
      "v_fma_f32 v49, v49, v69, v81\n"
      "v_cvt_pk_bf16_f32 v58, v48, v49\n"
      "v_fma_f32 v50, v50, v69, v81\n"
      "v_fma_f32 v51, v51, v69, v81\n"
      "v_cvt_pk_bf16_f32 v59, v50, v51\n"
      "ds_write_b128 v17, v[56:59] offset:24576\n"
      "v_fma_f32 v40, v40, v69, v81\n"
      "v_fma_f32 v41, v41, v69, v81\n"
      "v_cvt_pk_bf16_f32 v40, v40, v41\n"
      "v_fma_f32 v42, v42, v69, v81\n"
      "v_fma_f32 v43, v43, v69, v81\n"
      "v_cvt_pk_bf16_f32 v41, v42, v43\n"
      "v_fma_f32 v12, v12, v69, v81\n"
      "v_fma_f32 v13, v13, v69, v81\n"
      "v_cvt_pk_bf16_f32 v42, v12, v13\n"
      "v_fma_f32 v14, v14, v69, v81\n"
      "v_fma_f32 v15, v15, v69, v81\n"
      "v_cvt_pk_bf16_f32 v43, v14, v15\n"
      "ds_write_b128 v83, v[40:43] offset:24576\n"
      "v_fma_f32 v32, v32, v68, v80\n"
      "v_fma_f32 v33, v33, v68, v80\n"
      "v_cvt_pk_bf16_f32 v32, v32, v33\n"
      "v_fma_f32 v34, v34, v68, v80\n"
      "v_fma_f32 v35, v35, v68, v80\n"
      "v_cvt_pk_bf16_f32 v33, v34, v35\n"
      "v_fma_f32 v60, v60, v68, v80\n"
      "v_fma_f32 v61, v61, v68, v80\n"
      "v_cvt_pk_bf16_f32 v34, v60, v61\n"
      "v_fma_f32 v62, v62, v68, v80\n"
      "v_fma_f32 v63, v63, v68, v80\n"
      "v_cvt_pk_bf16_f32 v35, v62, v63\n"
      "ds_write_b128 v17, v[32:35] offset:26624\n"
      "v_fma_f32 v24, v24, v68, v80\n"
      "v_fma_f32 v25, v25, v68, v80\n"
      "v_cvt_pk_bf16_f32 v24, v24, v25\n"
      "v_fma_f32 v26, v26, v68, v80\n"
      "v_fma_f32 v27, v27, v68, v80\n"
      "v_cvt_pk_bf16_f32 v25, v26, v27\n"
      "v_fma_f32 v52, v52, v68, v80\n"
      "v_fma_f32 v53, v53, v68, v80\n"
      "v_cvt_pk_bf16_f32 v26, v52, v53\n"
      "v_fma_f32 v54, v54, v68, v80\n"
      "v_fma_f32 v55, v55, v68, v80\n"
      "v_cvt_pk_bf16_f32 v27, v54, v55\n"
      "ds_write_b128 v83, v[24:27] offset:26624\n"
      "v_add_u32_e64 v24, s71, v2\n"
      "ds_read_b128 v[56:59], v24 offset:24576\n"
      "ds_read_b128 v[40:43], v24 offset:25600\n"
      "ds_read_b128 v[32:35], v24 offset:26624\n"
      "v_bfe_u32 v24, v2, 7, 3\n"
      "v_add_u32_e64 v25, 0, v24\n"
      "v_bfe_u32 v26, v25, 1, 3\n"
      "v_lshlrev_b32 v25, 2, v25\n"
      "ds_bpermute_b32 v27, v25, v18\n"
      "v_bfe_u32 v25, v2, 4, 3\n"
      "v_xor_b32 v25, v25, v26\n"
      "s_waitcnt lgkmcnt(0)\n"
      "v_lshl_add_u32 v27, v25, 4, v27\n"
      "v_add_u32 v25, 8, v24\n"
      "v_bfe_u32 v26, v25, 1, 3\n"
      "v_lshlrev_b32_e64 v25, 2, v25\n"
      "ds_bpermute_b32 v83, v25, v18\n"
      "v_bfe_u32 v25, v2, 4, 3\n"
      "v_xor_b32 v25, v25, v26\n"
      "s_waitcnt lgkmcnt(0)\n"
      "v_lshl_add_u32 v83, v25, 4, v83\n"
      "v_add_u32 v25, 16, v24\n"
      "v_bfe_u32 v26, v25, 1, 3\n"
      "v_lshlrev_b32 v25, 2, v25\n"
      "ds_bpermute_b32 v24, v25, v18\n"
      "v_bfe_u32 v25, v2, 4, 3\n"
      "v_xor_b32 v25, v25, v26\n"
      "s_waitcnt lgkmcnt(0)\n"
      "v_lshl_add_u32 v24, v25, 4, v24\n"
      "s_add_u32 s66, s22, s70\n"
      "s_addc_u32 s67, s23, 0\n"
      "global_store_dwordx4 v27, v[56:59], s[66:67] sc1\n"
      "global_store_dwordx4 v83, v[40:43], s[66:67] sc1\n"
      "s_mov_b32 s34, 0xffffffff\n"
      "s_mov_b32 s35, 0x0\n"
      "s_mov_b64 exec, s[34:35]\n"
      "global_store_dwordx4 v24, v[32:35], s[66:67] sc1\n"
      "s_mov_b64 exec, -1\n"
      "s_endpgm\n"
      "L_edge_52_%=:\n"
      "s_mov_b64 exec, s[36:37]\n"
      "v_subrev_u32 v18, s41, v18\n"
      "s_mov_b64 exec, s[38:39]\n"
      "v_subrev_u32 v18, s42, v18\n"
      "s_mov_b64 exec, -1\n"
      "s_nop 4\n"
      "s_branch L_eb_53_%=\n"
      "L_edge_54_%=:\n"
      "s_mov_b64 exec, -1\n"
      "v_subrev_u32 v18, s44, v18\n"
      "s_mov_b64 exec, s[88:89]\n"
      "v_subrev_u32 v18, s45, v18\n"
      "s_mov_b64 exec, -1\n"
      "s_nop 4\n"
      "s_branch L_eb_55_%=\n"
      "L_edge_62_%=:\n"
      "s_mov_b64 exec, s[36:37]\n"
      "v_subrev_u32 v20, s44, v20\n"
      "s_mov_b64 exec, s[38:39]\n"
      "v_subrev_u32 v20, s45, v20\n"
      "s_mov_b64 exec, -1\n"
      "s_nop 4\n"
      "s_branch L_eb_63_%=\n"
      "L_edge_65_%=:\n"
      "s_mov_b64 exec, s[36:37]\n"
      "v_subrev_u32 v12, s53, v12\n"
      "s_mov_b64 exec, s[38:39]\n"
      "v_subrev_u32 v12, s54, v12\n"
      "s_mov_b64 exec, -1\n"
      "s_nop 4\n"
      "s_branch L_eb_66_%=\n"
      "L_edge_67_%=:\n"
      "s_sub_u32 s66, s66, s41\n"
      "s_subb_u32 s67, s67, 0\n"
      "s_sub_u32 s66, s66, s42\n"
      "s_subb_u32 s67, s67, 0\n"
      "s_mov_b32 s34, 0xffffffff\n"
      "s_mov_b32 s35, 0xffffffff\n"
      "s_mov_b64 exec, s[34:35]\n"
      "v_add_u32 v85, s41, v85\n"
      "s_mov_b32 s34, 0xffffffff\n"
      "s_mov_b32 s35, 0xff000000\n"
      "s_mov_b64 exec, s[34:35]\n"
      "v_add_u32 v85, s42, v85\n"
      "s_mov_b32 s34, 0xffffffff\n"
      "s_mov_b32 s35, 0xffffffff\n"
      "s_mov_b64 exec, s[34:35]\n"
      "v_add_u32 v84, s41, v84\n"
      "s_mov_b32 s34, 0xffffff\n"
      "s_mov_b32 s35, 0xffff0000\n"
      "s_mov_b64 exec, s[34:35]\n"
      "v_add_u32 v84, s42, v84\n"
      "s_mov_b32 s34, 0xffffffff\n"
      "s_mov_b32 s35, 0xffffffff\n"
      "s_mov_b64 exec, s[34:35]\n"
      "v_add_u32 v99, s41, v99\n"
      "s_mov_b32 s34, 0xffff\n"
      "s_mov_b32 s35, 0xffffff00\n"
      "s_mov_b64 exec, s[34:35]\n"
      "v_add_u32 v99, s42, v99\n"
      "s_mov_b32 s34, 0xffffffff\n"
      "s_mov_b32 s35, 0x0\n"
      "s_mov_b64 exec, s[34:35]\n"
      "v_add_u32 v108, s41, v108\n"
      "s_mov_b32 s34, 0xff\n"
      "s_mov_b32 s35, 0xffffffff\n"
      "s_mov_b64 exec, s[34:35]\n"
      "v_add_u32 v108, s42, v108\n"
      "s_mov_b64 exec, -1\n"
      "s_nop 4\n"
      "s_branch L_eb_68_%=\n"
      "L_edge_69_%=:\n"
      "s_sub_u32 s66, s66, s41\n"
      "s_subb_u32 s67, s67, 0\n"
      "s_sub_u32 s66, s66, s42\n"
      "s_subb_u32 s67, s67, 0\n"
      "s_mov_b32 s34, 0xff000000\n"
      "s_mov_b32 s35, 0xffffff\n"
      "s_mov_b64 exec, s[34:35]\n"
      "v_add_u32 v30, s42, v30\n"
      "s_mov_b32 s34, 0xffff0000\n"
      "s_mov_b32 s35, 0xffff\n"
      "s_mov_b64 exec, s[34:35]\n"
      "v_add_u32 v29, s42, v29\n"
      "s_mov_b64 exec, -1\n"
      "s_nop 4\n"
      "s_branch L_eb_70_%=\n"
      "L_edge_71_%=:\n"
      "s_mov_b64 exec, s[36:37]\n"
      "v_subrev_u32 v29, s44, v29\n"
      "s_mov_b64 exec, s[38:39]\n"
      "v_subrev_u32 v29, s45, v29\n"
      "s_mov_b64 exec, -1\n"
      "s_nop 4\n"
      "s_branch L_eb_72_%=\n"
      "L_edge_73_%=:\n"
      "s_mov_b64 exec, -1\n"
      "v_subrev_u32 v29, s44, v29\n"
      "s_mov_b64 exec, s[88:89]\n"
      "v_subrev_u32 v29, s45, v29\n"
      "s_mov_b64 exec, -1\n"
      "s_nop 4\n"
      "s_branch L_eb_74_%=\n"
      "L_edge_75_%=:\n"
      "s_sub_u32 s66, s66, s50\n"
      "s_subb_u32 s67, s67, 0\n"
      "s_sub_u32 s66, s66, s51\n"
      "s_subb_u32 s67, s67, 0\n"
      "s_mov_b32 s34, 0xffffffff\n"
      "s_mov_b32 s35, 0xffffffff\n"
      "s_mov_b64 exec, s[34:35]\n"
      "v_add_u32 v89, s50, v89\n"
      "s_mov_b32 s34, 0xffffffff\n"
      "s_mov_b32 s35, 0xff000000\n"
      "s_mov_b64 exec, s[34:35]\n"
      "v_add_u32 v89, s51, v89\n"
      "s_mov_b32 s34, 0xffffffff\n"
      "s_mov_b32 s35, 0xffffffff\n"
      "s_mov_b64 exec, s[34:35]\n"
      "v_add_u32 v88, s50, v88\n"
      "s_mov_b32 s34, 0xffffff\n"
      "s_mov_b32 s35, 0xffff0000\n"
      "s_mov_b64 exec, s[34:35]\n"
      "v_add_u32 v88, s51, v88\n"
      "s_mov_b32 s34, 0xffffffff\n"
      "s_mov_b32 s35, 0xffffffff\n"
      "s_mov_b64 exec, s[34:35]\n"
      "v_add_u32 v87, s50, v87\n"
      "s_mov_b32 s34, 0xffff\n"
      "s_mov_b32 s35, 0xffffff00\n"
      "s_mov_b64 exec, s[34:35]\n"
      "v_add_u32 v87, s51, v87\n"
      "s_mov_b32 s34, 0xffffffff\n"
      "s_mov_b32 s35, 0x0\n"
      "s_mov_b64 exec, s[34:35]\n"
      "v_add_u32 v109, s50, v109\n"
      "s_mov_b32 s34, 0xff\n"
      "s_mov_b32 s35, 0xffffffff\n"
      "s_mov_b64 exec, s[34:35]\n"
      "v_add_u32 v109, s51, v109\n"
      "s_mov_b64 exec, -1\n"
      "s_nop 4\n"
      "s_branch L_eb_76_%=\n"
      "L_edge_77_%=:\n"
      "s_sub_u32 s66, s66, s50\n"
      "s_subb_u32 s67, s67, 0\n"
      "s_sub_u32 s66, s66, s51\n"
      "s_subb_u32 s67, s67, 0\n"
      "s_mov_b32 s34, 0xff000000\n"
      "s_mov_b32 s35, 0xffffff\n"
      "s_mov_b64 exec, s[34:35]\n"
      "v_add_u32 v18, s51, v18\n"
      "s_mov_b32 s34, 0xffff0000\n"
      "s_mov_b32 s35, 0xffff\n"
      "s_mov_b64 exec, s[34:35]\n"
      "v_add_u32 v19, s51, v19\n"
      "s_mov_b64 exec, -1\n"
      "s_nop 4\n"
      "s_branch L_eb_78_%=\n"
      "L_edge_79_%=:\n"
      "s_mov_b64 exec, s[36:37]\n"
      "v_subrev_u32 v16, s53, v16\n"
      "s_mov_b64 exec, s[38:39]\n"
      "v_subrev_u32 v16, s54, v16\n"
      "s_mov_b64 exec, -1\n"
      "s_nop 4\n"
      "s_branch L_eb_80_%=\n"
      "L_edge_81_%=:\n"
      "s_sub_u32 s66, s66, s50\n"
      "s_subb_u32 s67, s67, 0\n"
      "s_sub_u32 s66, s66, s51\n"
      "s_subb_u32 s67, s67, 0\n"
      "s_mov_b32 s34, 0xffffffff\n"
      "s_mov_b32 s35, 0xffffffff\n"
      "s_mov_b64 exec, s[34:35]\n"
      "v_add_u32 v52, s50, v52\n"
      "s_mov_b32 s34, 0xffffffff\n"
      "s_mov_b32 s35, 0xff000000\n"
      "s_mov_b64 exec, s[34:35]\n"
      "v_add_u32 v52, s51, v52\n"
      "s_mov_b32 s34, 0xffffffff\n"
      "s_mov_b32 s35, 0xffffffff\n"
      "s_mov_b64 exec, s[34:35]\n"
      "v_add_u32 v54, s50, v54\n"
      "s_mov_b32 s34, 0xffffff\n"
      "s_mov_b32 s35, 0xffff0000\n"
      "s_mov_b64 exec, s[34:35]\n"
      "v_add_u32 v54, s51, v54\n"
      "s_mov_b32 s34, 0xffffffff\n"
      "s_mov_b32 s35, 0xffffffff\n"
      "s_mov_b64 exec, s[34:35]\n"
      "v_add_u32_e64 v53, s50, v53\n"
      "s_mov_b32 s34, 0xffff\n"
      "s_mov_b32 s35, 0xffffff00\n"
      "s_mov_b64 exec, s[34:35]\n"
      "v_add_u32 v53, s51, v53\n"
      "s_mov_b32 s34, 0xffffffff\n"
      "s_mov_b32 s35, 0x0\n"
      "s_mov_b64 exec, s[34:35]\n"
      "v_add_u32 v55, s50, v55\n"
      "s_mov_b32 s34, 0xff\n"
      "s_mov_b32 s35, 0xffffffff\n"
      "s_mov_b64 exec, s[34:35]\n"
      "v_add_u32 v55, s51, v55\n"
      "s_mov_b64 exec, -1\n"
      "s_nop 4\n"
      "s_branch L_eb_82_%=\n"
      "L_edge_83_%=:\n"
      "s_sub_u32 s66, s66, s50\n"
      "s_subb_u32 s67, s67, 0\n"
      "s_sub_u32 s66, s66, s51\n"
      "s_subb_u32 s67, s67, 0\n"
      "s_mov_b32 s34, 0xff000000\n"
      "s_mov_b32 s35, 0xffffff\n"
      "s_mov_b64 exec, s[34:35]\n"
      "v_add_u32 v71, s51, v71\n"
      "s_mov_b32 s34, 0xffff0000\n"
      "s_mov_b32 s35, 0xffff\n"
      "s_mov_b64 exec, s[34:35]\n"
      "v_add_u32 v70, s51, v70\n"
      "s_mov_b64 exec, -1\n"
      "s_nop 4\n"
      "s_branch L_eb_84_%=\n"
      "L_edge_85_%=:\n"
      "s_sub_u32 s66, s66, s41\n"
      "s_subb_u32 s67, s67, 0\n"
      "s_sub_u32 s66, s66, s42\n"
      "s_subb_u32 s67, s67, 0\n"
      "s_mov_b32 s34, 0xffffffff\n"
      "s_mov_b32 s35, 0xffffffff\n"
      "s_mov_b64 exec, s[34:35]\n"
      "v_add_u32 v47, s41, v47\n"
      "s_mov_b32 s34, 0xffffffff\n"
      "s_mov_b32 s35, 0xff000000\n"
      "s_mov_b64 exec, s[34:35]\n"
      "v_add_u32 v47, s42, v47\n"
      "s_mov_b32 s34, 0xffffffff\n"
      "s_mov_b32 s35, 0xffffffff\n"
      "s_mov_b64 exec, s[34:35]\n"
      "v_add_u32 v97, s41, v97\n"
      "s_mov_b32 s34, 0xffffff\n"
      "s_mov_b32 s35, 0xffff0000\n"
      "s_mov_b64 exec, s[34:35]\n"
      "v_add_u32 v97, s42, v97\n"
      "s_mov_b32 s34, 0xffffffff\n"
      "s_mov_b32 s35, 0xffffffff\n"
      "s_mov_b64 exec, s[34:35]\n"
      "v_add_u32 v84, s41, v84\n"
      "s_mov_b32 s34, 0xffff\n"
      "s_mov_b32 s35, 0xffffff00\n"
      "s_mov_b64 exec, s[34:35]\n"
      "v_add_u32 v84, s42, v84\n"
      "s_mov_b32 s34, 0xffffffff\n"
      "s_mov_b32 s35, 0x0\n"
      "s_mov_b64 exec, s[34:35]\n"
      "v_add_u32 v46, s41, v46\n"
      "s_mov_b32 s34, 0xff\n"
      "s_mov_b32 s35, 0xffffffff\n"
      "s_mov_b64 exec, s[34:35]\n"
      "v_add_u32 v46, s42, v46\n"
      "s_mov_b64 exec, -1\n"
      "s_nop 4\n"
      "s_branch L_eb_86_%=\n"
      "L_edge_87_%=:\n"
      "s_sub_u32 s66, s66, s41\n"
      "s_subb_u32 s67, s67, 0\n"
      "s_sub_u32 s66, s66, s42\n"
      "s_subb_u32 s67, s67, 0\n"
      "s_sub_u32 s68, s68, s41\n"
      "s_subb_u32 s69, s69, 0\n"
      "s_sub_u32 s68, s68, s42\n"
      "s_subb_u32 s69, s69, 0\n"
      "s_mov_b32 s34, 0xff000000\n"
      "s_mov_b32 s35, 0xffffff\n"
      "s_mov_b64 exec, s[34:35]\n"
      "v_add_u32 v65, s42, v65\n"
      "s_mov_b32 s34, 0xffff0000\n"
      "s_mov_b32 s35, 0xffff\n"
      "s_mov_b64 exec, s[34:35]\n"
      "v_add_u32 v66, s42, v66\n"
      "s_mov_b32 s34, 0xff000000\n"
      "s_mov_b32 s35, 0xffffff\n"
      "s_mov_b64 exec, s[34:35]\n"
      "v_add_u32 v64, s42, v64\n"
      "s_mov_b64 exec, -1\n"
      "s_nop 4\n"
      "s_branch L_eb_88_%=\n"
      "L_edge_89_%=:\n"
      "s_sub_u32 s66, s66, s50\n"
      "s_subb_u32 s67, s67, 0\n"
      "s_sub_u32 s66, s66, s51\n"
      "s_subb_u32 s67, s67, 0\n"
      "s_mov_b32 s34, 0xffffffff\n"
      "s_mov_b32 s35, 0xffffffff\n"
      "s_mov_b64 exec, s[34:35]\n"
      "v_add_u32 v63, s50, v63\n"
      "s_mov_b32 s34, 0xffffffff\n"
      "s_mov_b32 s35, 0xff000000\n"
      "s_mov_b64 exec, s[34:35]\n"
      "v_add_u32 v63, s51, v63\n"
      "s_mov_b32 s34, 0xffffffff\n"
      "s_mov_b32 s35, 0xffffffff\n"
      "s_mov_b64 exec, s[34:35]\n"
      "v_add_u32 v14, s50, v14\n"
      "s_mov_b32 s34, 0xffffff\n"
      "s_mov_b32 s35, 0xffff0000\n"
      "s_mov_b64 exec, s[34:35]\n"
      "v_add_u32 v14, s51, v14\n"
      "s_mov_b32 s34, 0xffffffff\n"
      "s_mov_b32 s35, 0xffffffff\n"
      "s_mov_b64 exec, s[34:35]\n"
      "v_add_u32 v15, s50, v15\n"
      "s_mov_b32 s34, 0xffff\n"
      "s_mov_b32 s35, 0xffffff00\n"
      "s_mov_b64 exec, s[34:35]\n"
      "v_add_u32 v15, s51, v15\n"
      "s_mov_b32 s34, 0xffffffff\n"
      "s_mov_b32 s35, 0x0\n"
      "s_mov_b64 exec, s[34:35]\n"
      "v_add_u32 v60, s50, v60\n"
      "s_mov_b32 s34, 0xff\n"
      "s_mov_b32 s35, 0xffffffff\n"
      "s_mov_b64 exec, s[34:35]\n"
      "v_add_u32 v60, s51, v60\n"
      "s_mov_b64 exec, -1\n"
      "s_nop 4\n"
      "s_branch L_eb_90_%=\n"
      "L_edge_91_%=:\n"
      "s_sub_u32 s66, s66, s50\n"
      "s_subb_u32 s67, s67, 0\n"
      "s_sub_u32 s66, s66, s51\n"
      "s_subb_u32 s67, s67, 0\n"
      "s_mov_b32 s34, 0xff000000\n"
      "s_mov_b32 s35, 0xffffff\n"
      "s_mov_b64 exec, s[34:35]\n"
      "v_add_u32 v83, s51, v83\n"
      "s_mov_b32 s34, 0xffff0000\n"
      "s_mov_b32 s35, 0xffff\n"
      "s_mov_b64 exec, s[34:35]\n"
      "v_add_u32 v12, s51, v12\n"
      "s_mov_b64 exec, -1\n"
      "s_nop 4\n"
      "s_branch L_eb_92_%=\n"
      "L_cend_%=:\n"
      :
      : "s"(__builtin_amdgcn_kernarg_segment_ptr()), "s"(wave), "s"(blockIdx.x), "s"(CT_TAB), "s"(lds0)
      : "v0","v1","v2","v3","v4","v5","v6","v7","v8","v9","v10","v11","v12","v13","v14","v15","v16","v17","v18","v19","v20","v21","v22","v23","v24","v25","v26","v27","v28","v29","v30","v31","v32","v33","v34","v35","v36","v37","v38","v39","v40","v41","v42","v43","v44","v45","v46","v47","v48","v49","v50","v51","v52","v53","v54","v55","v56","v57","v58","v59","v60","v61","v62","v63","v64","v65","v66","v67","v68","v69","v70","v71","v72","v73","v74","v75","v76","v77","v78","v79","v80","v81","v82","v83","v84","v85","v86","v87","v88","v89","v90","v91","v92","v93","v94","v95","v96","v97","v98","v99","v100","v101","v102","v103","v104","v105","v106","v107","v108","v109","v110","v111","a0","a1","a2","a3","a4","a5","a6","a7","a8","a9","a10","a11","a12","a13","a14","a15","a16","a17","a18","a19","a20","a21","a22","a23","a24","a25","a26","a27","a28","a29","a30","a31","a32","a33","a34","a35","a36","a37","a38","a39","a40","a41","a42","a43","a44","a45","a46","a47","a48","a49","a50","a51","a52","a53","a54","a55","a56","a57","a58","a59","a60","a61","a62","a63","a64","a65","a66","a67","a68","a69","a70","a71","a72","a73","a74","a75","a76","a77","a78","a79","a80","a81","a82","a83","a84","a85","a86","a87","a88","a89","a90","a91","a92","a93","a94","a95","a96","a97","a98","a99","a100","a101","a102","a103","a104","a105","a106","a107","a108","a109","a110","a111","a112","a113","a114","a115","a116","a117","a118","a119","a120","a121","a122","a123","a124","a125","a126","a127","a128","a129","a130","a131","a132","a133","a134","a135","a136","a137","a138","a139","a140","a141","a142","a143","s16","s17","s18","s19","s20","s21","s22","s23","s24","s25","s26","s27","s28","s29","s30","s31","s33","s34","s35","s36","s37","s38","s39","s40","s41","s42","s43","s44","s45","s46","s47","s48","s49","s50","s51","s52","s53","s54","s55","s56","s57","s58","s59","s60","s61","s62","s63","s64","s65","s66","s67","s68","s69","s70","s71","s72","s73","s74","s75","s76","s77","s78","s79","s80","s81","s82","s83","s84","s85","s86","s87","s88","s89","s90","s91","s92","s93","s94","s95","s96","s97","s98","s99","s100","s101", "vcc", "scc", "memory");
}

}

extern "C" void kernel_launch(void* const* d_in, const int* in_sizes, int n_in, void* d_out, int out_size, void* d_ws,
                              size_t ws_size, hipStream_t stream) {
  const uint16_t* x = (const uint16_t*)d_in[0];
  const uint16_t* wqkv = (const uint16_t*)d_in[1];
  const uint16_t* wo = (const uint16_t*)d_in[2];
  const uint16_t* ls = (const uint16_t*)d_in[3];
  const uint16_t* w1 = (const uint16_t*)d_in[4];
  const uint16_t* w2 = (const uint16_t*)d_in[5];
  const uint16_t* rc = (const uint16_t*)d_in[6];
  const int* ridx = (const int*)d_in[7];
  const uint16_t* mask = (const uint16_t*)d_in[8];
  swin::swin_main<<<256, 512, 0, stream>>>(x, mask, (char*)d_ws, (uint16_t*)d_out, wqkv, wo, ls, w1, w2, rc, ridx);
}
```

```cpp
#include <hip/hip_runtime.h>
#include <hip/hip_bf16.h>
#include <stdint.h>

namespace swin {

constexpr unsigned LDS_BYTES = 163840u;

__global__ __launch_bounds__(256, 1) void swin_main(const uint16_t* x, const uint16_t* mask, char* ws, uint16_t* out,
                                                    const uint16_t* wqkv, const uint16_t* wo, const uint16_t* ls,
                                                    const uint16_t* w1, const uint16_t* w2, const uint16_t* rc,
                                                    const int* ridx) {
  __shared__ __attribute__((aligned(16))) char smem[LDS_BYTES];
  const int wave = __builtin_amdgcn_readfirstlane(threadIdx.x >> 6);
  const unsigned lds0 = (unsigned)(size_t)smem;
  asm volatile(
      ".p2align 5\n"
      "s_mov_b64 s[60:61], %0\n"
      "s_mov_b32 s24, %1\n"
      "s_mov_b32 s25, %2\n"
      "s_load_dwordx8 s[16:23], s[60:61], 0\n"
      "s_load_dwordx4 s[4:7], s[60:61], 32\n"
      "s_load_dwordx8 s[80:87], s[60:61], 48\n"
      "s_load_dwordx2 s[90:91], s[60:61], 80\n"
      "s_getpc_b64 s[12:13]\n"
      "L_ipf0_%=:\n"
      "v_mbcnt_lo_u32_b32 v234, -1, 0\n"
      "v_mbcnt_hi_u32_b32 v234, -1, v234\n"
      "s_lshr_b32 s28, s25, 3\n"
      "s_movk_i32 s29, 54\n"
      "s_mov_b32 s30, 221056\n"
      "s_cmp_eq_u32 s24, 1\n"
      "s_cselect_b32 s29, 18, s29\n"
      "s_cselect_b32 s30, 73600, s30\n"
      "s_cmp_eq_u32 s24, 2\n"
      "s_cselect_b32 s29, 16, s29\n"
      "s_cselect_b32 s30, L_cend_%=-L_ipf0_%=-128, s30\n"
      "s_cmp_eq_u32 s24, 3\n"
      "s_cselect_b32 s29, 3, s29\n"
      "s_cselect_b32 s30, 9476, s30\n"
      "s_mul_i32 s28, s28, s29\n"
      "s_sub_u32 s29, s29, 1\n"
      "v_min_u32 v136, s29, v234\n"
      "v_add_lshl_u32 v136, v136, s28, 7\n"
      "v_min_u32_e64 v136, s30, v136\n"
      "s_waitcnt lgkmcnt(0)\n"
      "s_cmp_eq_u32 s24, 0\n"
      "s_cselect_b64 s[12:13], s[4:5], s[12:13]\n"
      "s_cmp_eq_u32 s24, 1\n"
      "s_cselect_b64 s[12:13], s[6:7], s[12:13]\n"
      "s_cmp_eq_u32 s24, 3\n"
      "s_cselect_b64 s[12:13], s[90:91], s[12:13]\n"
      "global_load_dword v31, v136, s[12:13]\n"
      "s_load_dwordx16 s[36:51], s[82:83], 0\n"
      "s_load_dwordx2 s[88:89], s[80:81], 0\n"
      "s_mul_i32 s30, s24, 256\n"
      "v_lshl_add_u32 v136, v234, 2, s30\n"
      "v_min_u32 v136, 672, v136\n"
      "v_add_u32 v146, 149136, v136\n"
      "global_load_dword v136, v136, s[86:87]\n"
      "s_cmp_lt_u32 s25, 209\n"
      "s_cselect_b32 s26, 21, 20\n"
      "s_sub_u32 s3, s26, 1\n"
      "v_min_u32 v137, s3, v234\n"
      "s_cmp_lt_u32 s25, 209\n"
      "s_cselect_b32 s28, 17, 15\n"
      "s_cselect_b32 s29, 81, 0\n"
      "s_cselect_b32 s30, 0, 993\n"
      "s_cselect_b64 s[34:35], 0, 0xa8000\n"
      "s_addk_i32 s29, 128\n"
      "s_addk_i32 s30, 799\n"
      "s_add_u32 s30, s30, s25\n"
      "v_lshl_add_u32 v139, v137, 8, s25\n"
      "v_mul_u32_u24 v138, s29, v137\n"
      "v_add_u32 v138, s30, v138\n"
      "v_cmp_gt_u32 vcc, s28, v137\n"
      "s_nop 1\n"
      "v_cndmask_b32 v139, v138, v139, vcc\n"
      "v_add_u32 v138, 141, v139\n"
      "v_cmp_gt_u32 vcc, 5184, v139\n"
      "s_nop 1\n"
      "v_cndmask_b32 v139, v138, v139, vcc\n"
      "v_mul_u32_u24 v138, 47, v137\n"
      "v_lshrrev_b32 v138, 1, v138\n"
      "s_add_u32 s30, s25, 4623\n"
      "v_add_u32 v138, s30, v138\n"
      "s_or_b32 s35, s34, s34\n"
      "s_cselect_b32 s35, -1, 0\n"
      "s_and_b32 s28, s35, 0xfff00000\n"
      "s_or_b32 s34, s34, s28\n"
      "v_cndmask_b32 v139, v139, v138, s[34:35]\n"
      "v_mul_u32_u24 v226, 58255, v139\n"
      "v_lshrrev_b32 v226, 22, v226\n"
      "v_mul_u32_u24 v138, 184, v226\n"
      "v_add_u32 v226, v139, v138\n"
      "v_add_u32 v138, 13248, v139\n"
      "v_cmp_gt_u32 vcc, 5184, v139\n"
      "s_nop 1\n"
      "v_cndmask_b32 v226, v138, v226, vcc\n"
      "v_lshlrev_b32_e64 v138, 8, v139\n"
      "v_subrev_u32 v138, 1345720, v138\n"
      "v_cmp_gt_u32 vcc, 5257, v139\n"
      "s_nop 1\n"
      "v_cndmask_b32 v226, v138, v226, vcc\n"
      "s_mul_i32 s30, s24, 32\n"
      "v_lshrrev_b32 v227, 3, v234\n"
      "v_lshl_add_u32 v235, v227, 2, s30\n"
      "ds_bpermute_b32 v235, v235, v226\n"
      "s_lshl_b32 s76, s24, 10\n"
      "v_xor_b32 v228, v227, v234\n"
      "v_and_b32 v228, 7, v228\n"
      "s_waitcnt lgkmcnt(0)\n"
      "v_and_b32 v138, 0xff, v235\n"
      "v_lshrrev_b32_e64 v235, 8, v235\n"
      "v_mad_u32_u24 v235, v235, 7, 9\n"
      "v_subrev_u32 v139, 0x1ff, v235\n"
      "v_min_u32_e64 v235, v235, v139\n"
      "v_mad_u32_u24 v138, v138, 7, 9\n"
      "v_subrev_u32 v139, 0x1ff, v138\n"
      "v_min_u32_e64 v138, v138, v139\n"
      "v_mul_u32_u24 v235, 0x1ff, v235\n"
      "v_add_u32_e64 v235, v235, v138\n"
      "v_mul_u32_u24 v235, 0x180, v235\n"
      "v_lshl_add_u32 v235, v228, 4, v235\n"
      "s_add_u32 m0, s76, 18432\n"
      "s_nop 0\n"
      "global_load_lds_dwordx4 v235, s[16:17]\n"
      "s_add_u32 m0, s76, 24448\n"
      "s_nop 0\n"
      "global_load_lds_dwordx4 v235, s[16:17] offset:128\n"
      "s_add_u32 m0, s76, 30464\n"
      "s_nop 0\n"
      "global_load_lds_dwordx4 v235, s[16:17] offset:256\n"
      "s_mul_i32 s30, s24, 1024\n"
      "v_lshl_add_u32 v138, v234, 4, s30\n"
      "v_min_u32 v138, 9600, v138\n"
      "s_add_u32 m0, s76, 124928\n"
      "s_nop 0\n"
      "global_load_lds_dwordx4 v138, s[90:91]\n"
      "s_mul_i32 s30, s24, 1024\n"
      "s_add_u32 s30, s30, 4096\n"
      "v_lshl_add_u32 v138, v234, 4, s30\n"
      "v_min_u32 v138, 9600, v138\n"
      "s_add_u32 m0, s76, 129024\n"
      "s_nop 0\n"
      "global_load_lds_dwordx4 v138, s[90:91]\n"
      "s_mul_i32 s30, s24, 1024\n"
      "s_add_u32 s30, s30, 8192\n"
      "v_lshl_add_u32 v138, v234, 4, s30\n"
      "v_min_u32 v138, 9600, v138\n"
      "s_add_u32 m0, s76, 133120\n"
      "s_nop 0\n"
      "global_load_lds_dwordx4 v138, s[90:91]\n"
      "s_mul_i32 s2, s24, 9216\n"
      "s_add_u32 s2, s2, 36864\n"
      "s_lshl_b32 s29, s24, 2\n"
      "s_and_b32 s29, s29, 4\n"
      "v_xor_b32 v150, s29, v234\n"
      "s_mul_i32 s30, s24, 9216\n"
      "v_lshl_add_u32 v150, v150, 4, s30\n"
      "v_xor_b32 v151, 32, v150\n"
      "s_mov_b32 s34, 0xffff0000\n"
      "s_mov_b32 s35, 0xffffffff\n"
      "v_cndmask_b32 v148, v150, v151, s[34:35]\n"
      "s_mov_b32 s34, 0xffffffff\n"
      "s_mov_b32 s35, 0x0\n"
      "v_cndmask_b32 v149, v150, v151, s[34:35]\n"
      "s_mov_b32 s34, 0x0\n"
      "s_mov_b32 s35, 0xffff0000\n"
      "v_cndmask_b32 v147, v150, v151, s[34:35]\n"
      "s_add_u32 m0, s2, 0\n"
      "s_nop 0\n"
      "global_load_lds_dwordx4 v150, s[4:5]\n"
      "global_load_lds_dwordx4 v150, s[4:5] offset:1024\n"
      "global_load_lds_dwordx4 v148, s[4:5] offset:2048\n"
      "global_load_lds_dwordx4 v151, s[4:5] offset:3072\n"
      "s_add_u32 s4, s4, 4096\n"
      "s_addc_u32 s5, s5, 0\n"
      "s_add_u32 m0, s2, 4096\n"
      "s_nop 0\n"
      "global_load_lds_dwordx4 v149, s[4:5]\n"
      "global_load_lds_dwordx4 v150, s[4:5] offset:1024\n"
      "global_load_lds_dwordx4 v147, s[4:5] offset:2048\n"
      "global_load_lds_dwordx4 v151, s[4:5] offset:3072\n"
      "s_add_u32 s4, s4, 4096\n"
      "s_addc_u32 s5, s5, 0\n"
      "s_add_u32 m0, s2, 8192\n"
      "s_nop 0\n"
      "global_load_lds_dwordx4 v151, s[4:5]\n"
      "s_add_u32 s4, s4, 28672\n"
      "s_addc_u32 s5, s5, 0\n"
      "s_add_u32 m0, s2, 36864\n"
      "s_nop 0\n"
      "global_load_lds_dwordx4 v150, s[4:5]\n"
      "global_load_lds_dwordx4 v150, s[4:5] offset:1024\n"
      "global_load_lds_dwordx4 v148, s[4:5] offset:2048\n"
      "global_load_lds_dwordx4 v151, s[4:5] offset:3072\n"
      "s_add_u32 s4, s4, 4096\n"
      "s_addc_u32 s5, s5, 0\n"
      "s_add_u32 m0, s2, 40960\n"
      "s_nop 0\n"
      "global_load_lds_dwordx4 v149, s[4:5]\n"
      "global_load_lds_dwordx4 v150, s[4:5] offset:1024\n"
      "global_load_lds_dwordx4 v147, s[4:5] offset:2048\n"
      "global_load_lds_dwordx4 v151, s[4:5] offset:3072\n"
      "s_add_u32 s4, s4, 4096\n"
      "s_addc_u32 s5, s5, 0\n"
      "s_add_u32 m0, s2, 45056\n"
      "s_nop 0\n"
      "global_load_lds_dwordx4 v151, s[4:5]\n"
      "s_add_u32 s4, s4, 32768\n"
      "s_addc_u32 s5, s5, 0\n"
      "v_add_u32 v152, 4096, v151\n"
      "s_mul_i32 s30, s24, 9216\n"
      "s_add_u32 s30, s30, 36864\n"
      "v_lshl_add_u32 v236, v234, 4, s30\n"
      "v_bfe_u32 v238, v234, 3, 1\n"
      "v_lshl_add_u32 v237, v238, 1, 0\n"
      "v_bfe_u32 v238, v234, 4, 1\n"
      "global_load_dwordx4 a[72:75], v150, s[4:5] offset:-4096\n"
      "v_lshl_add_u32 v237, v238, 2, v237\n"
      "s_mov_b32 s30, 36864\n"
      "v_and_b32 v244, 1, v234\n"
      "v_lshl_add_u32 v241, v244, 3, s30\n"
      "v_lshrrev_b32 v243, 4, v234\n"
      "s_movk_i32 s31, 9216\n"
      "v_mad_u32_u24 v241, v243, s31, v241\n"
      "v_bfe_u32 v240, v234, 2, 2\n"
      "s_movk_i32 s31, 1152\n"
      "v_mad_u32_u24 v241, v240, s31, v241\n"
      "s_mul_i32 s30, s24, 6\n"
      "v_bfe_u32 v239, v234, 1, 1\n"
      "global_load_dwordx4 a[76:79], v150, s[4:5] offset:-3072\n"
      "v_lshl_add_u32 v138, v239, 0, s30\n"
      "v_xor_b32 v138, v138, v237\n"
      "v_lshl_add_u32 v242, v138, 4, v241\n"
      "s_mul_i32 s30, s24, 6\n"
      "s_add_u32 s30, s30, 2\n"
      "v_lshl_add_u32 v138, v239, 0, s30\n"
      "v_xor_b32 v138, v138, v237\n"
      "v_lshl_add_u32 v245, v138, 4, v241\n"
      "s_mul_i32 s30, s24, 6\n"
      "s_add_u32 s30, s30, 4\n"
      "v_lshl_add_u32 v153, v239, 0, s30\n"
      "v_xor_b32 v153, v153, v237\n"
      "global_load_dwordx4 a[80:83], v148, s[4:5] offset:-2048\n"
      "v_lshl_add_u32 v241, v153, 4, v241\n"
      "s_waitcnt lgkmcnt(0)\n"
      "s_pack_ll_b32_b16 s28, s36, s44\n"
      "s_waitcnt vmcnt(27)\n"
      "v_dot2_f32_bf16 v153, s28, v136, 0\n"
      "s_pack_hh_b32_b16 s29, s36, s44\n"
      "v_dot2_f32_bf16 v137, s29, v136, 0\n"
      "s_pack_ll_b32_b16 s30, s37, s45\n"
      "v_dot2_f32_bf16 v143, s30, v136, 0\n"
      "s_pack_hh_b32_b16 s31, s37, s45\n"
      "v_dot2_f32_bf16 v139, s31, v136, 0\n"
      "s_pack_ll_b32_b16 s28, s38, s46\n"
      "v_dot2_f32_bf16 v145, s28, v136, 0\n"
      "global_load_dwordx4 a[84:87], v151, s[4:5] offset:-1024\n"
      "s_pack_hh_b32_b16 s29, s38, s46\n"
      "v_dot2_f32_bf16 v144, s29, v136, 0\n"
      "s_pack_ll_b32_b16 s30, s39, s47\n"
      "v_dot2_f32_bf16 v166, s30, v136, 0\n"
      "s_pack_hh_b32_b16 s31, s39, s47\n"
      "v_dot2_f32_bf16 v138, s31, v136, 0\n"
      "s_pack_ll_b32_b16 s28, s40, s48\n"
      "v_dot2_f32_bf16 v169, s28, v136, 0\n"
      "s_pack_hh_b32_b16 s29, s40, s48\n"
      "v_dot2_f32_bf16 v161, s29, v136, 0\n"
      "s_pack_ll_b32_b16 s30, s41, s49\n"
      "v_dot2_f32_bf16 v142, s30, v136, 0\n"
      "global_load_dwordx4 a[88:91], v149, s[4:5]\n"
      "s_pack_hh_b32_b16 s31, s41, s49\n"
      "v_dot2_f32_bf16 v158, s31, v136, 0\n"
      "s_pack_ll_b32_b16 s28, s42, s50\n"
      "v_dot2_f32_bf16 v160, s28, v136, 0\n"
      "s_pack_hh_b32_b16 s29, s42, s50\n"
      "v_dot2_f32_bf16 v167, s29, v136, 0\n"
      "s_pack_ll_b32_b16 s30, s43, s51\n"
      "v_dot2_f32_bf16 v168, s30, v136, 0\n"
      "s_pack_hh_b32_b16 s31, s43, s51\n"
      "v_dot2_f32_bf16 v159, s31, v136, 0\n"
      "s_load_dwordx16 s[36:51], s[84:85], 0\n"
      "v_max_f32 v153, 0, v153\n"
      "global_load_dwordx4 a[92:95], v150, s[4:5] offset:1024\n"
      "v_max_f32 v137, 0, v137\n"
      "v_cvt_pk_bf16_f32 v153, v153, v137\n"
      "v_max_f32 v143, 0, v143\n"
      "v_max_f32 v139, 0, v139\n"
      "v_cvt_pk_bf16_f32 v143, v143, v139\n"
      "v_max_f32 v145, 0, v145\n"
      "v_max_f32 v144, 0, v144\n"
      "v_cvt_pk_bf16_f32 v145, v145, v144\n"
      "v_max_f32 v166, 0, v166\n"
      "v_max_f32 v138, 0, v138\n"
      "v_cvt_pk_bf16_f32 v166, v166, v138\n"
      "v_max_f32 v169, 0, v169\n"
      "global_load_dwordx4 a[96:99], v147, s[4:5] offset:2048\n"
      "v_max_f32 v161, 0, v161\n"
      "v_cvt_pk_bf16_f32 v169, v169, v161\n"
      "v_max_f32 v142, 0, v142\n"
      "v_max_f32_e64 v158, 0, v158\n"
      "v_cvt_pk_bf16_f32 v142, v142, v158\n"
      "v_max_f32_e64 v160, 0, v160\n"
      "v_max_f32_e64 v167, 0, v167\n"
      "v_cvt_pk_bf16_f32 v160, v160, v167\n"
      "v_max_f32 v168, 0, v168\n"
      "v_max_f32_e64 v159, 0, v159\n"
      "v_cvt_pk_bf16_f32 v168, v168, v159\n"
      "s_waitcnt lgkmcnt(0)\n"
      "global_load_dwordx4 a[100:103], v151, s[4:5] offset:3072\n"
      "s_pack_ll_b32_b16 s28, s36, s38\n"
      "v_dot2_f32_bf16 v136, s28, v153, 0\n"
      "s_pack_hh_b32_b16 s29, s36, s38\n"
      "v_dot2_f32_bf16 v137, s29, v153, 0\n"
      "s_pack_ll_b32_b16 s30, s37, s39\n"
      "v_dot2_f32_bf16 v138, s30, v153, 0\n"
      "s_pack_hh_b32_b16 s31, s37, s39\n"
      "v_dot2_f32_bf16 v139, s31, v153, 0\n"
      "s_pack_ll_b32_b16 s28, s40, s42\n"
      "v_dot2_f32_bf16 v136, s28, v143, v136\n"
      "s_pack_hh_b32_b16 s29, s40, s42\n"
      "v_dot2_f32_bf16 v137, s29, v143, v137\n"
      "global_load_dwordx4 a[104:107], v152, s[4:5]\n"
      "s_pack_ll_b32_b16 s30, s41, s43\n"
      "v_dot2_f32_bf16 v138, s30, v143, v138\n"
      "s_pack_hh_b32_b16 s31, s41, s43\n"
      "v_dot2_f32_bf16 v139, s31, v143, v139\n"
      "s_pack_ll_b32_b16 s28, s44, s46\n"
      "v_dot2_f32_bf16 v136, s28, v145, v136\n"
      "s_pack_hh_b32_b16 s29, s44, s46\n"
      "v_dot2_f32_bf16 v137, s29, v145, v137\n"
      "s_pack_ll_b32_b16 s30, s45, s47\n"
      "v_dot2_f32_bf16 v138, s30, v145, v138\n"
      "s_pack_hh_b32_b16 s31, s45, s47\n"
      "v_dot2_f32_bf16 v139, s31, v145, v139\n"
      "v_add_u32 v150, 36864, v150\n"
      "v_add_u32 v151, 36864, v151\n"
      "v_add_u32 v148, 36864, v148\n"
      "v_add_u32 v149, 36864, v149\n"
      "v_add_u32 v147, 36864, v147\n"
      "v_add_u32 v152, 36864, v152\n"
      "global_load_dwordx4 a[108:111], v150, s[4:5] offset:-4096\n"
      "s_pack_ll_b32_b16 s28, s48, s50\n"
      "v_dot2_f32_bf16 v136, s28, v166, v136\n"
      "s_pack_hh_b32_b16 s29, s48, s50\n"
      "v_dot2_f32_bf16 v137, s29, v166, v137\n"
      "s_pack_ll_b32_b16 s30, s49, s51\n"
      "v_dot2_f32_bf16 v138, s30, v166, v138\n"
      "s_pack_hh_b32_b16 s31, s49, s51\n"
      "v_dot2_f32_bf16 v139, s31, v166, v139\n"
      "s_load_dwordx16 s[36:51], s[84:85], 64\n"
      "s_waitcnt lgkmcnt(0)\n"
      "s_pack_ll_b32_b16 s28, s36, s38\n"
      "v_dot2_f32_bf16 v136, s28, v169, v136\n"
      "global_load_dwordx4 a[112:115], v150, s[4:5] offset:-3072\n"
      "s_pack_hh_b32_b16 s29, s36, s38\n"
      "v_dot2_f32_bf16 v137, s29, v169, v137\n"
      "s_pack_ll_b32_b16 s30, s37, s39\n"
      "v_dot2_f32_bf16 v138, s30, v169, v138\n"
      "s_pack_hh_b32_b16 s31, s37, s39\n"
      "v_dot2_f32_bf16 v139, s31, v169, v139\n"
      "s_pack_ll_b32_b16 s28, s40, s42\n"
      "v_dot2_f32_bf16 v136, s28, v142, v136\n"
      "s_pack_hh_b32_b16 s29, s40, s42\n"
      "v_dot2_f32_bf16 v137, s29, v142, v137\n"
      "s_pack_ll_b32_b16 s30, s41, s43\n"
      "v_dot2_f32_bf16 v138, s30, v142, v138\n"
      "global_load_dwordx4 a[116:119], v148, s[4:5] offset:-2048\n"
      "s_pack_hh_b32_b16 s31, s41, s43\n"
      "v_dot2_f32_bf16 v139, s31, v142, v139\n"
      "s_pack_ll_b32_b16 s28, s44, s46\n"
      "v_dot2_f32_bf16 v136, s28, v160, v136\n"
      "s_pack_hh_b32_b16 s29, s44, s46\n"
      "v_dot2_f32_bf16 v137, s29, v160, v137\n"
      "s_pack_ll_b32_b16 s30, s45, s47\n"
      "v_dot2_f32_bf16 v138, s30, v160, v138\n"
      "s_pack_hh_b32_b16 s31, s45, s47\n"
      "v_dot2_f32_bf16 v139, s31, v160, v139\n"
      "s_pack_ll_b32_b16 s28, s48, s50\n"
      "v_dot2_f32_bf16 v136, s28, v168, v136\n"
      "global_load_dwordx4 a[120:123], v151, s[4:5] offset:-1024\n"
      "s_pack_hh_b32_b16 s29, s48, s50\n"
      "v_dot2_f32_bf16 v137, s29, v168, v137\n"
      "s_pack_ll_b32_b16 s30, s49, s51\n"
      "v_dot2_f32_bf16 v138, s30, v168, v138\n"
      "s_pack_hh_b32_b16 s31, s49, s51\n"
      "v_dot2_f32_bf16 v139, s31, v168, v139\n"
      "s_nop 2\n"
      "v_cvt_pk_bf16_f32 v136, v136, v136\n"
      "v_lshlrev_b32 v136, 16, v136\n"
      "v_mul_f32 v136, 0xbfb8aa3b, v136\n"
      "v_exp_f32 v136, v136\n"
      "s_nop 0\n"
      "v_add_f32 v136, 1.0, v136\n"
      "global_load_dwordx4 a[124:127], v149, s[4:5]\n"
      "v_rcp_f32 v136, v136\n"
      "s_nop 0\n"
      "v_cvt_pk_bf16_f32 v136, v136, v136\n"
      "v_lshlrev_b32 v136, 16, v136\n"
      "v_mul_f32 v136, 0x41800000, v136\n"
      "v_mul_f32 v136, 0x3fb8aa3b, v136\n"
      "v_cvt_pk_bf16_f32 v137, v137, v137\n"
      "v_lshlrev_b32 v137, 16, v137\n"
      "v_mul_f32 v137, 0xbfb8aa3b, v137\n"
      "v_exp_f32 v137, v137\n"
      "s_nop 0\n"
      "v_add_f32 v137, 1.0, v137\n"
      "v_rcp_f32_e64 v137, v137\n"
      "s_nop 0\n"
      "v_cvt_pk_bf16_f32 v137, v137, v137\n"
      "global_load_dwordx4 a[128:131], v150, s[4:5] offset:1024\n"
      "v_lshlrev_b32_e64 v137, 16, v137\n"
      "v_mul_f32 v137, 0x41800000, v137\n"
      "v_mul_f32 v137, 0x3fb8aa3b, v137\n"
      "v_cvt_pk_bf16_f32 v138, v138, v138\n"
      "v_lshlrev_b32 v138, 16, v138\n"
      "v_mul_f32 v138, 0xbfb8aa3b, v138\n"
      "v_exp_f32 v138, v138\n"
      "s_nop 0\n"
      "v_add_f32 v138, 1.0, v138\n"
      "v_rcp_f32 v138, v138\n"
      "s_nop 0\n"
      "v_cvt_pk_bf16_f32 v138, v138, v138\n"
      "v_lshlrev_b32_e64 v138, 16, v138\n"
      "v_mul_f32 v138, 0x41800000, v138\n"
      "global_load_dwordx4 a[132:135], v147, s[4:5] offset:2048\n"
      "v_mul_f32 v138, 0x3fb8aa3b, v138\n"
      "v_cvt_pk_bf16_f32 v139, v139, v139\n"
      "v_lshlrev_b32 v139, 16, v139\n"
      "v_mul_f32 v139, 0xbfb8aa3b, v139\n"
      "v_exp_f32 v139, v139\n"
      "s_nop 0\n"
      "v_add_f32 v139, 1.0, v139\n"
      "v_rcp_f32 v139, v139\n"
      "s_nop 0\n"
      "v_cvt_pk_bf16_f32 v139, v139, v139\n"
      "v_lshlrev_b32_e64 v139, 16, v139\n"
      "v_mul_f32 v139, 0x41800000, v139\n"
      "v_mul_f32 v139, 0x3fb8aa3b, v139\n"
      "ds_write_b32 v146, v136\n"
      "global_load_dwordx4 a[136:139], v151, s[4:5] offset:3072\n"
      "ds_write_b32 v146, v137 offset:768\n"
      "ds_write_b32 v146, v138 offset:1536\n"
      "ds_write_b32 v146, v139 offset:2304\n"
      "v_xor_b32 v146, v243, v234\n"
      "v_and_b32 v146, 7, v146\n"
      "v_and_b32_e64 v247, 15, v234\n"
      "v_lshl_add_u32 v114, v247, 7, 0\n"
      "v_lshl_add_u32 v114, v146, 4, v114\n"
      "v_xor_b32 v115, 64, v114\n"
      "v_and_b32 v246, 3, v234\n"
      "v_lshrrev_b32 v249, 5, v234\n"
      "v_and_b32 v146, 7, v234\n"
      "global_load_dwordx4 a[140:143], v152, s[4:5]\n"
      "v_bfe_u32 v229, v234, 3, 2\n"
      "s_mul_i32 s30, s24, 18432\n"
      "v_lshl_add_u32 v248, v146, 4, s30\n"
      "s_movk_i32 s31, 384\n"
      "v_mad_u32_u24 v248, v227, s31, v248\n"
      "s_waitcnt vmcnt(18)\n"
      "s_waitcnt lgkmcnt(0)\n"
      "s_barrier\n"
      "L_sched_%=:\n"
      "ds_read_b64_tr_b16 a[0:1], v242\n"
      "ds_read_b64_tr_b16 a[2:3], v242 offset:4608\n"
      "ds_read_b64_tr_b16 a[4:5], v245\n"
      "ds_read_b64_tr_b16 a[6:7], v245 offset:4608\n"
      "ds_read_b64_tr_b16 a[8:9], v241\n"
      "ds_read_b64_tr_b16 a[10:11], v241 offset:4608\n"
      "ds_read_b64_tr_b16 a[12:13], v242 offset:384\n"
      "ds_read_b64_tr_b16 a[14:15], v242 offset:4992\n"
      "ds_read_b64_tr_b16 a[16:17], v245 offset:384\n"
      "ds_read_b64_tr_b16 a[18:19], v245 offset:4992\n"
      "ds_read_b64_tr_b16 a[20:21], v241 offset:384\n"
      "ds_read_b64_tr_b16 a[22:23], v241 offset:4992\n"
      "ds_read_b64_tr_b16 a[24:25], v242 offset:768\n"
      "ds_read_b64_tr_b16 a[26:27], v242 offset:5376\n"
      "ds_read_b64_tr_b16 a[28:29], v245 offset:768\n"
      "ds_read_b64_tr_b16 a[30:31], v245 offset:5376\n"
      "ds_read_b64_tr_b16 a[32:33], v241 offset:768\n"
      "ds_read_b64_tr_b16 a[34:35], v241 offset:5376\n"
      "ds_read_b64_tr_b16 a[36:37], v242 offset:36864\n"
      "ds_read_b64_tr_b16 a[38:39], v242 offset:41472\n"
      "ds_read_b64_tr_b16 a[40:41], v245 offset:36864\n"
      "ds_read_b64_tr_b16 a[42:43], v245 offset:41472\n"
      "ds_read_b64_tr_b16 a[44:45], v241 offset:36864\n"
      "ds_read_b64_tr_b16 a[46:47], v241 offset:41472\n"
      "ds_read_b64_tr_b16 a[48:49], v242 offset:37248\n"
      "ds_read_b64_tr_b16 a[50:51], v242 offset:41856\n"
      "ds_read_b64_tr_b16 a[52:53], v245 offset:37248\n"
      "ds_read_b64_tr_b16 a[54:55], v245 offset:41856\n"
      "ds_read_b64_tr_b16 a[56:57], v241 offset:37248\n"
      "ds_read_b64_tr_b16 a[58:59], v241 offset:41856\n"
      "ds_read_b64_tr_b16 a[60:61], v242 offset:37632\n"
      "ds_read_b64_tr_b16 a[62:63], v242 offset:42240\n"
      "ds_read_b64_tr_b16 a[64:65], v245 offset:37632\n"
      "ds_read_b64_tr_b16 a[66:67], v245 offset:42240\n"
      "ds_read_b64_tr_b16 a[68:69], v241 offset:37632\n"
      "ds_read_b64_tr_b16 a[70:71], v241 offset:42240\n"
      "v_mov_b32 v146, 134528\n"
      "v_add_u32 v150, 36864, v150\n"
      "global_load_dwordx4 a[144:147], v150, s[4:5] offset:-4096\n"
      "global_load_dwordx4 a[148:151], v150, s[4:5] offset:-3072\n"
      "v_add_u32 v151, 36864, v151\n"
      "v_add_u32 v148, 36864, v148\n"
      "global_load_dwordx4 a[152:155], v148, s[4:5] offset:-2048\n"
      "global_load_dwordx4 a[156:159], v151, s[4:5] offset:-1024\n"
      "v_add_u32 v149, 36864, v149\n"
      "v_add_u32 v147, 36864, v147\n"
      "v_add_u32 v152, 36864, v152\n"
      "s_mov_b32 s30, 124928\n"
      "v_mov_b32 v137, s30\n"
      "s_movk_i32 s31, 196\n"
      "v_mad_u32_u24 v137, v247, s31, v137\n"
      "v_add_u32_e64 v139, 0, v137\n"
      "v_lshl_add_u32 v139, v243, 4, v139\n"
      "ds_read2_b32 v[78:79], v139 offset0:0 offset1:1\n"
      "ds_read2_b32 v[80:81], v139 offset0:2 offset1:3\n"
      "ds_read2_b32 v[82:83], v139 offset0:16 offset1:17\n"
      "ds_read2_b32 v[84:85], v139 offset0:18 offset1:19\n"
      "ds_read2_b32 v[86:87], v139 offset0:32 offset1:33\n"
      "ds_read2_b32 v[88:89], v139 offset0:34 offset1:35\n"
      "global_load_dwordx4 a[160:163], v149, s[4:5]\n"
      "v_add_u32 v138, 3136, v139\n"
      "ds_read2_b32 v[90:91], v138 offset0:0 offset1:1\n"
      "ds_read2_b32 v[92:93], v138 offset0:2 offset1:3\n"
      "ds_read2_b32 v[94:95], v138 offset0:16 offset1:17\n"
      "ds_read2_b32 v[96:97], v138 offset0:18 offset1:19\n"
      "ds_read2_b32 v[98:99], v138 offset0:32 offset1:33\n"
      "s_mov_b32 s30, 134336\n"
      "v_lshl_add_u32 v136, v247, 2, s30\n"
      "ds_read2_b32 v[100:101], v138 offset0:34 offset1:35\n"
      "global_load_dwordx4 a[164:167], v150, s[4:5] offset:1024\n"
      "ds_read_b32 v70, v137 offset:192\n"
      "ds_read_b32 v72, v137 offset:3328\n"
      "ds_read_b32 v74, v137 offset:6464\n"
      "ds_read_b32 v71, v136\n"
      "global_load_dwordx4 a[168:171], v147, s[4:5] offset:2048\n"
      "ds_read_b32 v73, v136 offset:64\n"
      "ds_read_b32 v75, v136 offset:128\n"
      "ds_read_b32 v77, v146\n"
      "global_load_dwordx4 a[172:175], v151, s[4:5] offset:3072\n"
      "global_load_dwordx4 a[176:179], v152, s[4:5]\n"
      "v_add_u32 v146, 6272, v139\n"
      "ds_read2_b32 v[102:103], v146 offset0:0 offset1:1\n"
      "ds_read2_b32 v[104:105], v146 offset0:2 offset1:3\n"
      "ds_read2_b32 v[106:107], v146 offset0:16 offset1:17\n"
      "ds_read2_b32 v[108:109], v146 offset0:18 offset1:19\n"
      "ds_read2_b32 v[110:111], v146 offset0:32 offset1:33\n"
      "ds_read2_b32 v[112:113], v146 offset0:34 offset1:35\n"
      "v_add_u32 v150, 36864, v150\n"
      "global_load_dwordx4 a[180:183], v150, s[4:5] offset:-4096\n"
      "v_add_u32 v151, 36864, v151\n"
      "v_add_u32 v148, 36864, v148\n"
      "v_add_u32 v149, 36864, v149\n"
      "v_add_u32 v147, 36864, v147\n"
      "v_add_u32 v152, 36864, v152\n"
      "s_mul_i32 s2, s24, 768\n"
      "s_add_u32 s2, s2, 149136\n"
      "s_waitcnt lgkmcnt(6)\n"
      "global_load_dwordx4 a[184:187], v150, s[4:5] offset:-3072\n"
      "v_lshl_add_u32 v78, v78, 2, s2\n"
      "ds_read_b32 v78, v78\n"
      "v_lshl_add_u32 v79, v79, 2, s2\n"
      "ds_read_b32 v79, v79\n"
      "v_lshl_add_u32 v80, v80, 2, s2\n"
      "ds_read_b32 v80, v80\n"
      "v_lshl_add_u32 v81, v81, 2, s2\n"
      "ds_read_b32 v81, v81\n"
      "global_load_dwordx4 a[188:191], v148, s[4:5] offset:-2048\n"
      "v_lshl_add_u32 v82, v82, 2, s2\n"
      "ds_read_b32 v82, v82\n"
      "v_lshl_add_u32 v83, v83, 2, s2\n"
      "ds_read_b32 v83, v83\n"
      "v_lshl_add_u32 v84, v84, 2, s2\n"
      "ds_read_b32 v84, v84\n"
      "global_load_dwordx4 a[192:195], v151, s[4:5] offset:-1024\n"
      "v_lshl_add_u32 v85, v85, 2, s2\n"
      "ds_read_b32 v85, v85\n"
      "v_lshl_add_u32 v86, v86, 2, s2\n"
      "ds_read_b32 v86, v86\n"
      "v_lshl_add_u32 v87, v87, 2, s2\n"
      "ds_read_b32 v87, v87\n"
      "global_load_dwordx4 a[196:199], v149, s[4:5]\n"
      "v_lshl_add_u32 v88, v88, 2, s2\n"
      "ds_read_b32 v88, v88\n"
      "v_lshl_add_u32 v89, v89, 2, s2\n"
      "ds_read_b32 v89, v89\n"
      "v_lshl_add_u32 v90, v90, 2, s2\n"
      "ds_read_b32 v90, v90\n"
      "global_load_dwordx4 a[200:203], v150, s[4:5] offset:1024\n"
      "v_lshl_add_u32 v91, v91, 2, s2\n"
      "ds_read_b32 v91, v91\n"
      "v_lshl_add_u32 v92, v92, 2, s2\n"
      "ds_read_b32 v92, v92\n"
      "global_load_dwordx4 a[204:207], v147, s[4:5] offset:2048\n"
      "v_lshl_add_u32 v93, v93, 2, s2\n"
      "ds_read_b32 v93, v93\n"
      "v_lshl_add_u32 v94, v94, 2, s2\n"
      "ds_read_b32 v94, v94\n"
      "v_lshl_add_u32 v95, v95, 2, s2\n"
      "ds_read_b32 v95, v95\n"
      "v_lshl_add_u32 v96, v96, 2, s2\n"
      "ds_read_b32 v96, v96\n"
      "global_load_dwordx4 a[208:211], v151, s[4:5] offset:3072\n"
      "v_lshl_add_u32 v97, v97, 2, s2\n"
      "ds_read_b32 v97, v97\n"
      "v_lshl_add_u32 v98, v98, 2, s2\n"
      "ds_read_b32 v98, v98\n"
      "v_lshl_add_u32 v99, v99, 2, s2\n"
      "ds_read_b32 v99, v99\n"
      "global_load_dwordx4 a[212:215], v152, s[4:5]\n"
      "v_lshl_add_u32 v100, v100, 2, s2\n"
      "ds_read_b32 v100, v100\n"
      "v_lshl_add_u32 v101, v101, 2, s2\n"
      "ds_read_b32 v101, v101\n"
      "s_waitcnt lgkmcnt(6)\n"
      "v_lshl_add_u32 v102, v102, 2, s2\n"
      "ds_read_b32 v102, v102\n"
      "v_lshl_add_u32 v103, v103, 2, s2\n"
      "ds_read_b32 v103, v103\n"
      "v_lshl_add_u32 v104, v104, 2, s2\n"
      "ds_read_b32 v104, v104\n"
      "v_lshl_add_u32 v105, v105, 2, s2\n"
      "ds_read_b32 v105, v105\n"
      "v_lshl_add_u32 v106, v106, 2, s2\n"
      "ds_read_b32 v106, v106\n"
      "v_lshl_add_u32 v107, v107, 2, s2\n"
      "ds_read_b32 v107, v107\n"
      "v_lshl_add_u32 v108, v108, 2, s2\n"
      "ds_read_b32 v108, v108\n"
      "v_lshl_add_u32 v109, v109, 2, s2\n"
      "ds_read_b32 v109, v109\n"
      "v_lshl_add_u32 v110, v110, 2, s2\n"
      "ds_read_b32 v110, v110\n"
      "v_lshl_add_u32 v111, v111, 2, s2\n"
      "ds_read_b32 v111, v111\n"
      "v_lshl_add_u32 v112, v112, 2, s2\n"
      "ds_read_b32 v112, v112\n"
      "v_lshl_add_u32 v113, v113, 2, s2\n"
      "ds_read_b32 v113, v113\n"
      "v_lshl_add_u32 v70, v70, 2, s2\n"
      "ds_read_b32 v70, v70\n"
      "v_lshl_add_u32 v71, v71, 2, s2\n"
      "ds_read_b32 v71, v71\n"
      "v_lshl_add_u32 v72, v72, 2, s2\n"
      "ds_read_b32 v72, v72\n"
      "v_lshl_add_u32 v73, v73, 2, s2\n"
      "ds_read_b32 v73, v73\n"
      "v_lshl_add_u32 v74, v74, 2, s2\n"
      "ds_read_b32 v74, v74\n"
      "v_lshl_add_u32 v75, v75, 2, s2\n"
      "ds_read_b32 v75, v75\n"
      "v_lshl_add_u32 v77, v77, 2, s2\n"
      "ds_read_b32 v77, v77\n"
      "s_waitcnt lgkmcnt(6)\n"
      "s_waitcnt lgkmcnt(4)\n"
      "s_waitcnt lgkmcnt(2)\n"
      "s_mov_b32 exec_lo, 0xffff0000\n"
      "s_mov_b32 exec_hi, 0xffffffff\n"
      "v_mov_b32 v70, 0xf149f2ca\n"
      "v_mov_b32 v72, 0xf149f2ca\n"
      "v_mov_b32 v74, 0xf149f2ca\n"
      "s_mov_b64 exec, -1\n"
      "s_waitcnt lgkmcnt(0)\n"
      "s_mov_b32 exec_lo, 0x0\n"
      "s_mov_b32 exec_hi, 0xfffe0000\n"
      "v_mov_b32 v77, 0xf149f2ca\n"
      "s_mov_b64 exec, -1\n"
      "s_waitcnt lgkmcnt(0)\n"
      "s_barrier\n"
      "v_add_u32 v222, 3072, v248\n"
      "s_mov_b32 s37, 0x3fb8aa3b\n"
      "ds_read_b128 v[146:149], v114 offset:18432\n"
      "ds_read_b128 v[136:139], v115 offset:18432\n"
      "ds_read_b128 v[150:153], v114 offset:20480\n"
      "ds_read_b128 v[142:145], v115 offset:20480\n"
      "s_waitcnt vmcnt(35)\n"
      "ds_write_b128 v236, a[72:75]\n"
      "s_waitcnt vmcnt(34)\n"
      "ds_write_b128 v236, a[76:79] offset:1024\n"
      "s_waitcnt vmcnt(33)\n"
      "ds_write_b128 v236, a[80:83] offset:2048\n"
      "s_waitcnt vmcnt(32)\n"
      "ds_write_b128 v236, a[84:87] offset:3072\n"
      "global_load_dwordx4 a[216:219], v222, s[6:7] offset:-3072\n"
      "s_waitcnt vmcnt(32)\n"
      "ds_write_b128 v236, a[88:91] offset:4096\n"
      "s_waitcnt lgkmcnt(5)\n"
      "v_mfma_f32_16x16x32_bf16 v[166:169], a[0:3], v[146:149], 0\n"
      "v_readlane_b32 s80, v226, 0\n"
      "v_mfma_f32_16x16x32_bf16 v[166:169], a[36:39], v[136:139], v[166:169]\n"
      "s_waitcnt vmcnt(31)\n"
      "ds_write_b128 v236, a[92:95] offset:5120\n"
      "v_mfma_f32_16x16x32_bf16 v[158:161], a[4:7], v[146:149], 0\n"
      "global_load_dwordx4 a[220:223], v222, s[6:7] offset:-2944\n"
      "v_mfma_f32_16x16x32_bf16 v[158:161], a[40:43], v[136:139], v[158:161]\n"
      "s_waitcnt vmcnt(31)\n"
      "ds_write_b128 v236, a[96:99] offset:6144\n"
      "v_mfma_f32_16x16x32_bf16 v[154:157], a[8:11], v[146:149], 0\n"
      "global_load_dwordx4 a[224:227], v222, s[6:7] offset:-2816\n"
      "v_mfma_f32_16x16x32_bf16 v[154:157], a[44:47], v[136:139], v[154:157]\n"
      "s_waitcnt vmcnt(31)\n"
      "ds_write_b128 v236, a[100:103] offset:7168\n"
      "v_mfma_f32_16x16x32_bf16 v[162:165], a[12:15], v[146:149], 0\n"
      "global_load_dwordx4 a[228:231], v222, s[6:7]\n"
      "v_mfma_f32_16x16x32_bf16 v[162:165], a[48:51], v[136:139], v[162:165]\n"
      "s_waitcnt vmcnt(31)\n"
      "ds_write_b128 v236, a[104:107] offset:8192\n"
      "v_mfma_f32_16x16x32_bf16 v[174:177], a[16:19], v[146:149], 0\n"
      "global_load_dwordx4 a[232:235], v222, s[6:7] offset:128\n"
      "v_mfma_f32_16x16x32_bf16 v[174:177], a[52:55], v[136:139], v[174:177]\n"
      "s_waitcnt vmcnt(31)\n"
      "ds_write_b128 v236, a[108:111] offset:36864\n"
      "v_mfma_f32_16x16x32_bf16 v[182:185], a[20:23], v[146:149], 0\n"
      "global_load_dwordx4 a[236:239], v222, s[6:7] offset:256\n"
      "v_mfma_f32_16x16x32_bf16 v[182:185], a[56:59], v[136:139], v[182:185]\n"
      "s_waitcnt vmcnt(31)\n"
      "ds_write_b128 v236, a[112:115] offset:37888\n"
      "v_mfma_f32_16x16x32_bf16 v[170:173], a[24:27], v[146:149], 0\n"
      "s_mov_b32 s38, 0x2a8cbccc\n"
      "v_add_u32 v222, 9216, v248\n"
      "v_readlane_b32 s81, v226, 1\n"
      "v_mfma_f32_16x16x32_bf16 v[170:173], a[60:63], v[136:139], v[170:173]\n"
      "s_waitcnt vmcnt(30)\n"
      "ds_write_b128 v236, a[116:119] offset:38912\n"
      "v_mfma_f32_16x16x32_bf16 v[178:181], a[28:31], v[146:149], 0\n"
      "global_load_dwordx4 a[240:243], v222, s[6:7] offset:-3072\n"
      "v_mfma_f32_16x16x32_bf16 v[178:181], a[64:67], v[136:139], v[178:181]\n"
      "s_waitcnt vmcnt(30)\n"
      "ds_write_b128 v236, a[120:123] offset:39936\n"
      "v_mfma_f32_16x16x32_bf16 v[186:189], a[32:35], v[146:149], 0\n"
      "global_load_dwordx4 a[244:247], v222, s[6:7] offset:-2944\n"
      "v_mfma_f32_16x16x32_bf16 v[186:189], a[68:71], v[136:139], v[186:189]\n"
      "s_waitcnt vmcnt(30)\n"
      "ds_write_b128 v236, a[124:127] offset:40960\n"
      "v_mfma_f32_16x16x32_bf16 v[194:197], a[0:3], v[150:153], 0\n"
      "global_load_dwordx4 a[248:251], v222, s[6:7] offset:-2816\n"
      "v_mfma_f32_16x16x32_bf16 v[194:197], a[36:39], v[142:145], v[194:197]\n"
      "s_waitcnt vmcnt(30)\n"
      "ds_write_b128 v236, a[128:131] offset:41984\n"
      "v_mfma_f32_16x16x32_bf16 v[190:193], a[4:7], v[150:153], 0\n"
      "global_load_dwordx4 a[252:255], v222, s[6:7]\n"
      "v_mfma_f32_16x16x32_bf16 v[190:193], a[40:43], v[142:145], v[190:193]\n"
      "s_waitcnt vmcnt(30)\n"
      "ds_write_b128 v236, a[132:135] offset:43008\n"
      "v_mfma_f32_16x16x32_bf16 v[146:149], a[8:11], v[150:153], 0\n"
      "global_load_dwordx4 v[0:3], v222, s[6:7] offset:128\n"
      "v_mfma_f32_16x16x32_bf16 v[146:149], a[44:47], v[142:145], v[146:149]\n"
      "s_waitcnt vmcnt(30)\n"
      "ds_write_b128 v236, a[136:139] offset:44032\n"
      "v_mfma_f32_16x16x32_bf16 v[198:201], a[12:15], v[150:153], 0\n"
      "global_load_dwordx4 v[4:7], v222, s[6:7] offset:256\n"
      "v_mfma_f32_16x16x32_bf16 v[198:201], a[48:51], v[142:145], v[198:201]\n"
      "s_waitcnt vmcnt(30)\n"
      "ds_write_b128 v236, a[140:143] offset:45056\n"
      "v_mfma_f32_16x16x32_bf16 v[202:205], a[16:19], v[150:153], 0\n"
      "s_mov_b32 s39, 0x38fba882\n"
      "v_readlane_b32 s82, v226, 2\n"
      "v_readlane_b32 s83, v226, 3\n"
      "v_mfma_f32_16x16x32_bf16 v[202:205], a[52:55], v[142:145], v[202:205]\n"
      "s_mov_b32 s40, 0x7fff7fff\n"
      "v_readlane_b32 s84, v226, 4\n"
      "v_readlane_b32 s85, v226, 5\n"
      "v_mfma_f32_16x16x32_bf16 v[206:209], a[20:23], v[150:153], 0\n"
      "s_mov_b32 s41, 0x415db3d7\n"
      "v_readlane_b32 s86, v226, 6\n"
      "v_readlane_b32 s87, v226, 7\n"
      "v_mfma_f32_16x16x32_bf16 v[206:209], a[56:59], v[142:145], v[206:209]\n"
      "s_mov_b32 s42, 0xffff0000\n"
      "v_readlane_b32 s90, v226, 10\n"
      "v_readlane_b32 s91, v226, 11\n"
      "v_readlane_b32 s92, v226, 12\n"
      "v_readlane_b32 s93, v226, 13\n"
      "v_readlane_b32 s94, v226, 14\n"
      "v_readlane_b32 s95, v226, 15\n"
      "v_readlane_b32 s96, v226, 16\n"
      "v_readlane_b32 s97, v226, 17\n"
      "v_readlane_b32 s98, v226, 18\n"
      "v_readlane_b32 s99, v226, 19\n"
      "v_readlane_b32 s100, v226, 20\n"
      "v_mov_b32 v36, 0\n"
      "v_mov_b32 v37, 0\n"
      "v_mov_b32 v42, 0\n"
      "v_add_u32 v222, 15360, v248\n"
      "v_mov_b32 v43, 0\n"
      "v_mov_b32 v48, 0\n"
      "v_mov_b32 v49, 0\n"
      "v_mov_b32 v53, 0\n"
      "v_mov_b32 v57, 0\n"
      "v_mov_b32 v61, 0\n"
      "v_mov_b32 v65, 0\n"
      "s_mov_b32 s43, 0x0\n"
      "s_mov_b32 s44, 0x0\n"
      "s_mov_b32 s45, 0xffff\n"
      "s_mov_b32 s46, 0x0\n"
      "s_mov_b32 s47, 0xffff0000\n"
      "s_lshl_b32 s28, s24, 4\n"
      "s_lshr_b64 s[88:89], s[88:89], s28\n"
      "v_readlane_b32 s89, v226, 9\n"
      "s_lshl_b32 s88, s88, 16\n"
      "v_mov_b32 v137, s88\n"
      "v_mfma_f32_16x16x32_bf16 v[214:217], a[24:27], v[150:153], 0\n"
      "global_load_dwordx4 v[8:11], v222, s[6:7] offset:-3072\n"
      "v_mfma_f32_16x16x32_bf16 v[214:217], a[60:63], v[142:145], v[214:217]\n"
      "global_load_dwordx4 v[12:15], v222, s[6:7] offset:-2944\n"
      "v_max_f32 v137, 0xc2c80000, v137\n"
      "v_min_f32 v137, 0x40930000, v137\n"
      "v_mul_f32 v137, s37, v137\n"
      "v_exp_f32 v137, v137\n"
      "v_readlane_b32 s88, v226, 8\n"
      "v_cvt_pk_bf16_f32 v137, v137, v137\n"
      "v_lshlrev_b32_e64 v137, 16, v137\n"
      "v_cmp_nge_f32 vcc, 0x41f00000, v137\n"
      "v_mfma_f32_16x16x32_bf16 v[210:213], a[28:31], v[150:153], 0\n"
      "global_load_dwordx4 v[16:19], v222, s[6:7] offset:-2816\n"
      "v_mfma_f32_16x16x32_bf16 v[210:213], a[64:67], v[142:145], v[210:213]\n"
      "s_mov_b64 s[50:51], vcc\n"
      "v_mul_f32 v137, s37, v137\n"
      "v_mov_b32 v136, 0\n"
      "v_readfirstlane_b32 s36, v137\n"
      "v_mov_b32 v137, 0\n"
      "v_mov_b32 v138, 0\n"
      "v_mov_b32 v139, 0\n"
      "v_mov_b32 v140, 0\n"
      "v_mov_b32 v141, 0\n"
      "v_mov_b32 v66, 0\n"
      "v_mov_b32_e64 v218, 0\n"
      "global_load_dwordx4 v[20:23], v222, s[6:7]\n"
      "global_load_dwordx4 v[24:27], v222, s[6:7] offset:128\n"
      "global_load_dwordx4 v[28:31], v222, s[6:7] offset:256\n"
      "s_mov_b32 exec_lo, 0x11111111\n"
      "s_mov_b32 exec_hi, 0x11111111\n"
      "v_mov_b32 v66, 0x3f803f80\n"
      "s_mov_b64 exec, -1\n"
      "v_mov_b32 v67, v66\n"
      "v_mov_b32_e64 v68, v66\n"
      "v_mov_b32_e64 v69, v66\n"
      "s_mov_b32 exec_lo, 0xf0000f\n"
      "s_mov_b32 exec_hi, 0xf0000f00\n"
      "v_mov_b32 v218, 0x3f803f80\n"
      "s_mov_b64 exec, -1\n"
      "s_mov_b32 exec_lo, 0x100001\n"
      "s_mov_b32 exec_hi, 0x10000100\n"
      "v_mov_b32 v138, 0x3f80\n"
      "s_mov_b64 exec, -1\n"
      "s_mov_b32 exec_lo, 0x200002\n"
      "s_mov_b32 exec_hi, 0x20000200\n"
      "v_mov_b32 v138, 0x3f800000\n"
      "s_mov_b64 exec, -1\n"
      "s_mov_b32 exec_lo, 0x400004\n"
      "s_mov_b32 exec_hi, 0x40000400\n"
      "v_mov_b32 v139, 0x3f80\n"
      "s_mov_b64 exec, -1\n"
      "v_mfma_f32_16x16x32_bf16 v[222:225], a[32:35], v[150:153], 0\n"
      "v_mov_b32 v219, v218\n"
      "v_mfma_f32_16x16x32_bf16 v[222:225], a[68:71], v[142:145], v[222:225]\n"
      "s_mov_b32 exec_lo, 0x800008\n"
      "s_mov_b32 exec_hi, 0x80000800\n"
      "v_mov_b32 v139, 0x3f800000\n"
      "s_mov_b64 exec, -1\n"
      "v_mov_b32 v220, v218\n"
      "v_mov_b32 v221, v218\n"
      "s_waitcnt lgkmcnt(0)\n"
      "s_barrier\n"
      "ds_read_b64_tr_b16 a[72:73], v242\n"
      "ds_read_b64_tr_b16 a[74:75], v242 offset:4608\n"
      "ds_read_b64_tr_b16 a[76:77], v245\n"
      "ds_read_b64_tr_b16 a[78:79], v245 offset:4608\n"
      "ds_read_b64_tr_b16 a[80:81], v241\n"
      "ds_read_b64_tr_b16 a[82:83], v241 offset:4608\n"
      "ds_read_b64_tr_b16 a[84:85], v242 offset:384\n"
      "ds_read_b64_tr_b16 a[86:87], v242 offset:4992\n"
      "ds_read_b64_tr_b16 a[88:89], v245 offset:384\n"
      "ds_read_b64_tr_b16 a[90:91], v245 offset:4992\n"
      "ds_read_b64_tr_b16 a[92:93], v241 offset:384\n"
      "ds_read_b64_tr_b16 a[94:95], v241 offset:4992\n"
      "ds_read_b64_tr_b16 a[96:97], v242 offset:768\n"
      "ds_read_b64_tr_b16 a[98:99], v242 offset:5376\n"
      "ds_read_b64_tr_b16 a[100:101], v245 offset:768\n"
      "ds_read_b64_tr_b16 a[102:103], v245 offset:5376\n"
      "ds_read_b64_tr_b16 a[104:105], v241 offset:768\n"
      "ds_read_b64_tr_b16 a[106:107], v241 offset:5376\n"
      "ds_read_b64_tr_b16 a[108:109], v242 offset:36864\n"
      "ds_read_b64_tr_b16 a[110:111], v242 offset:41472\n"
      "ds_read_b64_tr_b16 a[112:113], v245 offset:36864\n"
      "ds_read_b64_tr_b16 a[114:115], v245 offset:41472\n"
      "ds_read_b64_tr_b16 a[116:117], v241 offset:36864\n"
      "ds_read_b64_tr_b16 a[118:119], v241 offset:41472\n"
      "ds_read_b64_tr_b16 a[120:121], v242 offset:37248\n"
      "ds_read_b64_tr_b16 a[122:123], v242 offset:41856\n"
      "ds_read_b64_tr_b16 a[124:125], v245 offset:37248\n"
      "s_and_b32 s31, s24, 1\n"
      "s_mul_i32 s31, s31, 1024\n"
      "s_lshr_b32 s77, s24, 1\n"
      "s_mul_i32 s77, s77, 6144\n"
      "s_add_u32 s77, s77, s31\n"
      "s_add_u32 s77, s77, 4096\n"
      "s_mul_i32 s78, s24, 512\n"
      "s_add_u32 s78, s78, 16384\n"
      "s_mul_i32 s79, s24, 176\n"
      "s_add_u32 s79, s79, 114688\n"
      "s_mul_i32 s30, s24, 8\n"
      "v_lshl_add_u32 v152, v227, 0, s30\n"
      "ds_read_b64_tr_b16 a[126:127], v245 offset:41856\n"
      "v_mul_u32_u24 v150, 37, v152\n"
      "ds_read_b64_tr_b16 a[128:129], v241 offset:37248\n"
      "v_lshrrev_b32 v150, 8, v150\n"
      "v_mul_u32_u24 v151, 7, v150\n"
      "ds_read_b64_tr_b16 a[130:131], v241 offset:41856\n"
      "v_sub_u32 v151, v152, v151\n"
      "ds_read_b64_tr_b16 a[132:133], v242 offset:37632\n"
      "v_cmp_gt_u32 s[64:65], 4, v150\n"
      "ds_read_b64_tr_b16 a[134:135], v242 offset:42240\n"
      "v_cmp_gt_u32 s[70:71], 4, v151\n"
      "ds_read_b64_tr_b16 a[136:137], v245 offset:37632\n"
      "ds_read_b64_tr_b16 a[138:139], v245 offset:42240\n"
      "v_mul_u32_u24 v150, 0x1ff, v150\n"
      "ds_read_b64_tr_b16 a[140:141], v241 offset:37632\n"
      "ds_read_b64_tr_b16 a[142:143], v241 offset:42240\n"
      "v_add_u32 v150, v150, v151\n"
      "v_mul_u32_u24 v150, 0x180, v150\n"
      "v_lshl_add_u32 v150, v228, 4, v150\n"
      "v_add_u32 v116, 0, v150\n"
      "s_and_b32 s31, s24, 1\n"
      "s_mul_i32 s31, s31, 8\n"
      "s_lshr_b32 s30, s24, 1\n"
      "s_mul_i32 s30, s30, 0\n"
      "s_add_u32 s30, s30, s31\n"
      "s_add_u32 s30, s30, 32\n"
      "v_lshl_add_u32 v151, v227, 0, s30\n"
      "v_mul_u32_u24 v227, 37, v151\n"
      "v_lshrrev_b32 v227, 8, v227\n"
      "v_mul_u32_u24 v150, 7, v227\n"
      "v_sub_u32 v150, v151, v150\n"
      "v_cmp_gt_u32 s[66:67], 4, v227\n"
      "v_cmp_gt_u32 s[72:73], 4, v150\n"
      "v_mul_u32_u24 v227, 0x1ff, v227\n"
      "v_add_u32 v227, v227, v150\n"
      "v_mul_u32_u24 v227, 0x180, v227\n"
      "v_lshl_add_u32 v227, v228, 4, v227\n"
      "s_and_b32 s31, s24, 1\n"
      "s_mul_i32 s31, s31, 0\n"
      "s_lshr_b32 s30, s24, 1\n"
      "s_mul_i32 s30, s30, 128\n"
      "s_add_u32 s30, s30, s31\n"
      "v_add_u32 v117, s30, v227\n"
      "v_readfirstlane_b32 s31, v226\n"
      "s_mul_i32 s30, s24, 4\n"
      "s_add_u32 s30, s30, 32\n"
      "v_lshl_add_u32 v227, v229, 0, s30\n"
      "v_mul_u32_u24 v226, 37, v227\n"
      "v_lshrrev_b32_e64 v226, 8, v226\n"
      "v_mul_u32_u24_e64 v229, 7, v226\n"
      "v_sub_u32_e64 v229, v227, v229\n"
      "v_cmp_gt_u32 s[68:69], 4, v226\n"
      "v_cmp_gt_u32 s[74:75], 4, v229\n"
      "v_mul_u32_u24 v226, 0x1ff, v226\n"
      "v_add_u32_e64 v226, v226, v229\n"
      "v_mul_u32_u24 v226, 0x180, v226\n"
      "v_xor_b32_e64 v229, v227, v234\n"
      "v_and_b32_e64 v229, 7, v229\n"
      "v_lshl_add_u32 v226, v229, 4, v226\n"
      "s_mov_b32 s30, 256\n"
      "v_add_u32 v118, s30, v226\n"
      "s_mul_i32 s30, s24, 1024\n"
      "s_add_u32 s30, s30, 73728\n"
      "v_lshl_add_u32 v119, v234, 4, s30\n"
      "s_mul_i32 s30, s24, 176\n"
      "s_add_u32 s30, s30, 114688\n"
      "v_lshl_add_u32 v122, v234, 4, s30\n"
      "s_mov_b32 s60, 100270464\n"
      "s_mov_b32 s61, 196224\n"
      "s_mov_b32 s11, 100466688\n"
      "s_sub_u32 s16, s16, 99876864\n"
      "s_subb_u32 s17, s17, 0\n"
      "s_sub_u32 s22, s22, 99876864\n"
      "s_subb_u32 s23, s23, 0\n"
      "s_add_u32 s12, s20, 16777216\n"
      "s_addc_u32 s13, s21, 0\n"
      "s_lshr_b32 s8, s31, 8\n"
      "s_and_b32 s9, s31, 0xff\n"
      "s_mul_i32 s10, s8, 73\n"
      "s_add_u32 s10, s10, s9\n"
      "s_mul_i32 s28, s10, 4802\n"
      "s_add_u32 s56, s18, s28\n"
      "s_addc_u32 s57, s19, 0\n"
      "s_and_b32 s56, s56, -16\n"
      "s_sub_u32 s58, s56, 110592\n"
      "s_subb_u32 s59, s57, 0\n"
      "s_sub_u32 s56, s56, 73728\n"
      "s_subb_u32 s57, s57, 0\n"
      "s_mul_i32 s28, s8, 1373568\n"
      "s_mul_i32 s29, s9, 2688\n"
      "s_add_u32 s28, s28, s29\n"
      "s_cmp_eq_u32 s8, 72\n"
      "s_cselect_b32 s8, 0, 100270464\n"
      "s_cmp_eq_u32 s9, 72\n"
      "s_cselect_b32 s9, 0, 196224\n"
      "s_add_u32 s29, s8, s9\n"
      "s_add_u32 s28, s28, s29\n"
      "s_add_u32 s52, s16, s28\n"
      "s_addc_u32 s53, s17, 0\n"
      "s_cmp_lg_u32 s29, s11\n"
      "s_cbranch_scc1 L_edge_1_%=\n"
      "L_eb_2_%=:\n"
      "s_add_u32 m0, s76, 0\n"
      "s_nop 0\n"
      "global_load_lds_dwordx4 v116, s[52:53]\n"
      "s_add_u32 m0, s76, 6016\n"
      "s_nop 0\n"
      "global_load_lds_dwordx4 v116, s[52:53] offset:128\n"
      "s_add_u32 m0, s76, 12032\n"
      "s_nop 0\n"
      "global_load_lds_dwordx4 v116, s[52:53] offset:256\n"
      "s_add_u32 m0, s77, 0\n"
      "s_nop 0\n"
      "global_load_lds_dwordx4 v117, s[52:53]\n"
      "s_add_u32 m0, s78, 0\n"
      "s_mov_b32 exec_hi, 0\n"
      "global_load_lds_dwordx4 v118, s[52:53]\n"
      "s_mov_b32 exec_hi, -1\n"
      "s_add_u32 m0, s76, 110592\n"
      "s_nop 0\n"
      "global_load_lds_dwordx4 v119, s[56:57]\n"
      "s_add_u32 m0, s79, 0\n"
      "s_mov_b64 exec, 0xfff\n"
      "global_load_lds_dwordx4 v122, s[58:59]\n"
      "s_mov_b64 exec, -1\n"
      "s_waitcnt lgkmcnt(0)\n"
      "s_barrier\n"
      "ds_read_b128 v[226:229], v114 offset:24576\n"
      "ds_read_b128 v[150:153], v115 offset:24576\n"
      "ds_read_b128 v[230:233], v114 offset:26624\n"
      "ds_read_b128 v[142:145], v115 offset:26624\n"
      "s_and_b32 s31, s24, 1\n"
      "s_mul_i32 s31, s31, 1024\n"
      "s_lshr_b32 s30, s24, 1\n"
      "s_mul_i32 s30, s30, 6144\n"
      "s_waitcnt vmcnt(42)\n"
      "ds_write_b128 v236, a[144:147]\n"
      "s_add_u32 s30, s30, s31\n"
      "s_add_u32 s30, s30, 77824\n"
      "v_lshl_add_u32 v120, v234, 4, s30\n"
      "s_mul_i32 s30, s24, 512\n"
      "s_add_u32 s30, s30, 90112\n"
      "v_lshl_add_u32 v121, v234, 4, s30\n"
      "s_mov_b32 s30, 36864\n"
      "v_lshl_add_u32 v123, v247, 4, s30\n"
      "s_movk_i32 s31, 768\n"
      "v_mad_u32_u24 v123, v243, s31, v123\n"
      "s_mul_i32 s30, s24, 3072\n"
      "v_add_u32_e64 v124, s30, v123\n"
      "s_and_b32 s31, s24, 1\n"
      "s_waitcnt vmcnt(41)\n"
      "ds_write_b128 v236, a[148:151] offset:1024\n"
      "s_mul_i32 s31, s31, 8\n"
      "s_waitcnt vmcnt(40)\n"
      "ds_write_b128 v236, a[152:155] offset:2048\n"
      "s_waitcnt lgkmcnt(3)\n"
      "v_mfma_f32_16x16x32_bf16 v[166:169], a[72:75], v[226:229], v[166:169]\n"
      "s_lshr_b32 s30, s24, 1\n"
      "s_mul_i32 s30, s30, 3072\n"
      "v_mfma_f32_16x16x32_bf16 v[166:169], a[108:111], v[150:153], v[166:169]\n"
      "s_waitcnt vmcnt(39)\n"
      "ds_write_b128 v236, a[156:159] offset:3072\n"
      "v_mfma_f32_16x16x32_bf16 v[158:161], a[76:79], v[226:229], v[158:161]\n"
      "s_add_u32 s30, s30, s31\n"
      "s_add_u32 s30, s30, 12288\n"
      "v_add_u32 v125, s30, v123\n"
      "v_mfma_f32_16x16x32_bf16 v[158:161], a[112:115], v[150:153], v[158:161]\n"
      "s_waitcnt vmcnt(38)\n"
      "ds_write_b128 v236, a[160:163] offset:4096\n"
      "v_mfma_f32_16x16x32_bf16 v[154:157], a[80:83], v[226:229], v[154:157]\n"
      "s_mul_i32 s30, s24, 6\n"
      "v_mfma_f32_16x16x32_bf16 v[154:157], a[116:119], v[150:153], v[154:157]\n"
      "s_waitcnt vmcnt(37)\n"
      "ds_write_b128 v236, a[164:167] offset:5120\n"
      "v_mfma_f32_16x16x32_bf16 v[162:165], a[84:87], v[226:229], v[162:165]\n"
      "v_lshl_add_u32 v251, v243, 0, s30\n"
      "s_mov_b32 s30, 73728\n"
      "v_mfma_f32_16x16x32_bf16 v[162:165], a[120:123], v[150:153], v[162:165]\n"
      "s_waitcnt vmcnt(36)\n"
      "ds_write_b128 v236, a[168:171] offset:6144\n"
      "v_mfma_f32_16x16x32_bf16 v[174:177], a[88:91], v[226:229], v[174:177]\n"
      "v_lshl_add_u32 v126, v247, 7, s30\n"
      "v_lshrrev_b32 v250, 3, v251\n"
      "v_mfma_f32_16x16x32_bf16 v[174:177], a[124:127], v[150:153], v[174:177]\n"
      "s_waitcnt vmcnt(35)\n"
      "ds_write_b128 v236, a[172:175] offset:7168\n"
      "v_mfma_f32_16x16x32_bf16 v[182:185], a[92:95], v[226:229], v[182:185]\n"
      "s_mul_i32 s30, s24, 6\n"
      "v_mul_u32_u24 v250, 6144, v250\n"
      "v_add_u32 v126, v126, v250\n"
      "v_mfma_f32_16x16x32_bf16 v[182:185], a[128:131], v[150:153], v[182:185]\n"
      "s_waitcnt vmcnt(34)\n"
      "ds_write_b128 v236, a[176:179] offset:8192\n"
      "v_mfma_f32_16x16x32_bf16 v[170:173], a[96:99], v[226:229], v[170:173]\n"
      "s_add_u32 s30, s30, 4\n"
      "v_xor_b32 v251, v251, v234\n"
      "v_and_b32 v251, 7, v251\n"
      "v_mfma_f32_16x16x32_bf16 v[170:173], a[132:135], v[150:153], v[170:173]\n"
      "s_waitcnt vmcnt(33)\n"
      "ds_write_b128 v236, a[180:183] offset:36864\n"
      "v_mfma_f32_16x16x32_bf16 v[178:181], a[100:103], v[226:229], v[178:181]\n"
      "v_lshl_add_u32 v126, v251, 4, v126\n"
      "v_lshl_add_u32 v251, v249, 0, s30\n"
      "v_mfma_f32_16x16x32_bf16 v[178:181], a[136:139], v[150:153], v[178:181]\n"
      "s_waitcnt vmcnt(32)\n"
      "ds_write_b128 v236, a[184:187] offset:37888\n"
      "v_mfma_f32_16x16x32_bf16 v[186:189], a[104:107], v[226:229], v[186:189]\n"
      "s_mov_b32 s30, 73728\n"
      "v_lshl_add_u32 v127, v247, 7, s30\n"
      "v_lshl_add_u32 v127, v238, 3, v127\n"
      "v_mfma_f32_16x16x32_bf16 v[186:189], a[140:143], v[150:153], v[186:189]\n"
      "s_waitcnt vmcnt(31)\n"
      "ds_write_b128 v236, a[188:191] offset:38912\n"
      "v_mfma_f32_16x16x32_bf16 v[194:197], a[72:75], v[230:233], v[194:197]\n"
      "s_mov_b32 s30, 120320\n"
      "v_lshrrev_b32 v150, 3, v251\n"
      "v_mul_u32_u24 v150, 6144, v150\n"
      "v_mfma_f32_16x16x32_bf16 v[194:197], a[108:111], v[142:145], v[194:197]\n"
      "s_waitcnt vmcnt(30)\n"
      "ds_write_b128 v236, a[192:195] offset:39936\n"
      "v_mfma_f32_16x16x32_bf16 v[190:193], a[76:79], v[230:233], v[190:193]\n"
      "v_add_u32 v127, v127, v150\n"
      "v_xor_b32_e64 v251, v251, v234\n"
      "v_mfma_f32_16x16x32_bf16 v[190:193], a[112:115], v[142:145], v[190:193]\n"
      "s_waitcnt vmcnt(29)\n"
      "ds_write_b128 v236, a[196:199] offset:40960\n"
      "v_mfma_f32_16x16x32_bf16 v[146:149], a[80:83], v[230:233], v[146:149]\n"
      "v_and_b32 v251, 7, v251\n"
      "v_lshl_add_u32 v127, v251, 4, v127\n"
      "v_mfma_f32_16x16x32_bf16 v[146:149], a[116:119], v[142:145], v[146:149]\n"
      "s_waitcnt vmcnt(28)\n"
      "ds_write_b128 v236, a[200:203] offset:41984\n"
      "v_mfma_f32_16x16x32_bf16 v[198:201], a[84:87], v[230:233], v[198:201]\n"
      "v_lshl_add_u32 v128, v234, 4, s30\n"
      "v_add_u32 v235, 99876864, v235\n"
      "v_mfma_f32_16x16x32_bf16 v[198:201], a[120:123], v[142:145], v[198:201]\n"
      "s_waitcnt vmcnt(27)\n"
      "ds_write_b128 v236, a[204:207] offset:43008\n"
      "v_mfma_f32_16x16x32_bf16 v[202:205], a[88:91], v[230:233], v[202:205]\n"
      "s_mul_i32 s30, s24, 4\n"
      "v_add_u32 v129, s30, v128\n"
      "s_mul_i32 s30, s24, 128\n"
      "v_mfma_f32_16x16x32_bf16 v[202:205], a[124:127], v[142:145], v[202:205]\n"
      "s_add_u32 s30, s30, 124416\n"
      "v_lshl_add_u32 v130, v249, 6, s30\n"
      "v_lshl_add_u32 v130, v240, 4, v130\n"
      "v_mfma_f32_16x16x32_bf16 v[206:209], a[92:95], v[230:233], v[206:209]\n"
      "s_mul_i32 s30, s24, 128\n"
      "v_lshl_add_u32 v130, v238, 3, v130\n"
      "v_lshl_add_u32 v130, v246, 1, v130\n"
      "v_mfma_f32_16x16x32_bf16 v[206:209], a[128:131], v[142:145], v[206:209]\n"
      "s_waitcnt vmcnt(26)\n"
      "ds_write_b128 v236, a[208:211] offset:44032\n"
      "v_mfma_f32_16x16x32_bf16 v[214:217], a[96:99], v[230:233], v[214:217]\n"
      "s_add_u32 s30, s30, 124416\n"
      "v_lshl_add_u32 v131, v243, 4, s30\n"
      "v_mfma_f32_16x16x32_bf16 v[214:217], a[132:135], v[142:145], v[214:217]\n"
      "s_mov_b32 exec_lo, 0xeeeeeeee\n"
      "s_mov_b32 exec_hi, 0xeeeeeeee\n"
      "v_mov_b32 v131, 1048576\n"
      "s_mov_b64 exec, -1\n"
      "v_mfma_f32_16x16x32_bf16 v[210:213], a[100:103], v[230:233], v[210:213]\n"
      "s_mul_i32 s30, s24, 192\n"
      "s_add_u32 s30, s30, 124928\n"
      "v_mad_u32_u24 v132, v243, 48, s30\n"
      "v_mfma_f32_16x16x32_bf16 v[210:213], a[136:139], v[142:145], v[210:213]\n"
      "s_waitcnt vmcnt(25)\n"
      "ds_write_b128 v236, a[212:215] offset:45056\n"
      "v_mfma_f32_16x16x32_bf16 v[222:225], a[104:107], v[230:233], v[222:225]\n"
      "v_lshl_add_u32 v236, v243, 5, 0\n"
      "v_xor_b32 v236, v236, v248\n"
      "v_mfma_f32_16x16x32_bf16 v[222:225], a[140:143], v[142:145], v[222:225]\n"
      "s_mov_b32 exec_lo, 0xfffefffe\n"
      "s_mov_b32 exec_hi, 0xfffefffe\n"
      "v_mov_b32 v132, 1048576\n"
      "s_mov_b64 exec, -1\n"
      "v_add_u32 v248, 36864, v236\n"
      "s_mul_i32 s30, s24, 192\n"
      "s_add_u32 s30, s30, 124928\n"
      "v_mad_u32_u24 v133, v243, 48, s30\n"
      "s_mov_b32 exec_lo, 0xdddddddd\n"
      "s_mov_b32 exec_hi, 0xdddddddd\n"
      "v_mov_b32 v133, 1048576\n"
      "s_mov_b64 exec, -1\n"
      "s_mul_i32 s30, s24, 96\n"
      "s_add_u32 s30, s30, 125696\n"
      "v_lshl_add_u32 v134, v247, 1, s30\n"
      "s_mul_i32 s30, s24, 1344\n"
      "s_add_u32 s30, s30, 149104\n"
      "v_lshl_add_u32 v135, v238, 3, s30\n"
      "v_lshl_add_u32 v135, v246, 1, v135\n"
      "s_movk_i32 s31, 336\n"
      "v_mad_u32_u24 v135, v240, s31, v135\n"
      "s_mov_b32 exec_lo, 0x0\n"
      "s_mov_b32 exec_hi, 0xffff0000\n"
      "v_mov_b32 v135, 1048576\n"
      "s_mov_b64 exec, -1\n"
      "s_and_b32 s31, s24, 1\n"
      "s_mul_i32 s31, s31, 8\n"
      "s_lshr_b32 s30, s24, 1\n"
      "s_mul_i32 s30, s30, 1344\n"
      "s_add_u32 s30, s30, s31\n"
      "s_add_u32 s30, s30, 154480\n"
      "v_lshl_add_u32 v236, v246, 1, s30\n"
      "s_movk_i32 s31, 336\n"
      "v_mad_u32_u24 v236, v240, s31, v236\n"
      "v_cndmask_b32 v135, v135, v236, s[44:45]\n"
      "s_mul_i32 s30, s24, 256\n"
      "s_add_u32 s30, s30, 157200\n"
      "v_lshl_add_u32 v236, v234, 2, s30\n"
      "ds_write_b32 v236, v235\n"
      "s_mul_i32 s30, s24, 192\n"
      "s_add_u32 s30, s30, 124928\n"
      "v_mad_u32_u24 v251, v243, 48, s30\n"
      "s_movk_i32 s31, 1152\n"
      "v_mad_u32_u24 v251, v247, s31, v251\n"
      "s_mul_i32 s30, s24, 96\n"
      "s_add_u32 s30, s30, 125696\n"
      "v_lshl_add_u32 v249, v243, 3, s30\n"
      "s_movk_i32 s31, 1152\n"
      "v_mad_u32_u24 v249, v247, s31, v249\n"
      "s_mul_i32 s30, s24, 6\n"
      "v_lshl_add_u32 v235, v246, 0, s30\n"
      "v_xor_b32 v235, v235, v237\n"
      "s_mov_b32 s30, 36864\n"
      "v_mov_b32 v250, s30\n"
      "s_movk_i32 s31, 1536\n"
      "v_mad_u32_u24 v250, v243, s31, v250\n"
      "s_movk_i32 s31, 384\n"
      "v_mad_u32_u24 v250, v240, s31, v250\n"
      "v_lshl_add_u32 v250, v235, 4, v250\n"
      "v_add_u32 v246, 36864, v250\n"
      "s_mul_i32 s30, s24, 6\n"
      "s_add_u32 s30, s30, 4\n"
      "v_lshl_add_u32 v235, v239, 0, s30\n"
      "v_xor_b32 v235, v235, v237\n"
      "s_mov_b32 s30, 36864\n"
      "v_lshl_add_u32 v247, v244, 3, s30\n"
      "s_movk_i32 s31, 1536\n"
      "v_mad_u32_u24 v247, v243, s31, v247\n"
      "s_movk_i32 s31, 384\n"
      "v_mad_u32_u24 v247, v240, s31, v247\n"
      "v_lshl_add_u32 v247, v235, 4, v247\n"
      "v_add_u32 v252, 36864, v247\n"
      "s_waitcnt lgkmcnt(0)\n"
      "s_barrier\n"
      "ds_read_b64_tr_b16 a[144:145], v242\n"
      "ds_read_b64_tr_b16 a[146:147], v242 offset:4608\n"
      "ds_read_b64_tr_b16 a[148:149], v245\n"
      "ds_read_b64_tr_b16 a[150:151], v245 offset:4608\n"
      "ds_read_b64_tr_b16 a[152:153], v241\n"
      "ds_read_b64_tr_b16 a[154:155], v241 offset:4608\n"
      "ds_read_b64_tr_b16 a[156:157], v242 offset:384\n"
      "ds_read_b64_tr_b16 a[158:159], v242 offset:4992\n"
      "ds_read_b64_tr_b16 a[160:161], v245 offset:384\n"
      "ds_read_b64_tr_b16 a[162:163], v245 offset:4992\n"
      "ds_read_b64_tr_b16 a[164:165], v241 offset:384\n"
      "ds_read_b64_tr_b16 a[166:167], v241 offset:4992\n"
      "ds_read_b64_tr_b16 a[168:169], v242 offset:768\n"
      "ds_read_b64_tr_b16 a[170:171], v242 offset:5376\n"
      "ds_read_b64_tr_b16 a[172:173], v245 offset:768\n"
      "ds_read_b64_tr_b16 a[174:175], v245 offset:5376\n"
      "ds_read_b64_tr_b16 a[176:177], v241 offset:768\n"
      "ds_read_b64_tr_b16 a[178:179], v241 offset:5376\n"
      "ds_read_b64_tr_b16 a[180:181], v242 offset:36864\n"
      "ds_read_b64_tr_b16 a[182:183], v242 offset:41472\n"
      "ds_read_b64_tr_b16 a[184:185], v245 offset:36864\n"
      "ds_read_b64_tr_b16 a[186:187], v245 offset:41472\n"
      "ds_read_b64_tr_b16 a[188:189], v241 offset:36864\n"
      "ds_read_b64_tr_b16 a[190:191], v241 offset:41472\n"
      "ds_read_b64_tr_b16 a[192:193], v242 offset:37248\n"
      "ds_read_b64_tr_b16 a[194:195], v242 offset:41856\n"
      "ds_read_b64_tr_b16 a[196:197], v245 offset:37248\n"
      "ds_read_b64_tr_b16 a[198:199], v245 offset:41856\n"
      "ds_read_b64_tr_b16 a[200:201], v241 offset:37248\n"
      "ds_read_b64_tr_b16 a[202:203], v241 offset:41856\n"
      "ds_read_b64_tr_b16 a[204:205], v242 offset:37632\n"
      "ds_read_b64_tr_b16 a[206:207], v242 offset:42240\n"
      "ds_read_b64_tr_b16 a[208:209], v245 offset:37632\n"
      "ds_read_b64_tr_b16 a[210:211], v245 offset:42240\n"
      "ds_read_b64_tr_b16 a[212:213], v241 offset:37632\n"
      "ds_read_b64_tr_b16 a[214:215], v241 offset:42240\n"
      "ds_read_b128 v[234:237], v114 offset:30720\n"
      "ds_read_b128 v[150:153], v115 offset:30720\n"
      "ds_read_b128 v[230:233], v114 offset:32768\n"
      "ds_read_b128 v[142:145], v115 offset:32768\n"
      "s_waitcnt lgkmcnt(0)\n"
      "s_barrier\n"
      "v_mfma_f32_16x16x32_bf16 v[166:169], a[144:147], v[234:237], v[166:169]\n"
      "v_mfma_f32_16x16x32_bf16 v[166:169], a[180:183], v[150:153], v[166:169]\n"
      "s_waitcnt vmcnt(24)\n"
      "v_mfma_f32_16x16x32_bf16 v[226:229], v[218:221], a[216:219], 0\n"
      "s_waitcnt vmcnt(23)\n"
      "v_mfma_f32_16x16x32_bf16 v[226:229], v[218:221], a[220:223], v[226:229]\n"
      "s_waitcnt vmcnt(22)\n"
      "v_mfma_f32_16x16x32_bf16 v[226:229], v[218:221], a[224:227], v[226:229]\n"
      "v_mfma_f32_16x16x32_bf16 v[158:161], a[148:151], v[234:237], v[158:161]\n"
      "v_mfma_f32_16x16x32_bf16 v[158:161], a[184:187], v[150:153], v[158:161]\n"
      "v_mfma_f32_16x16x32_bf16 v[154:157], a[152:155], v[234:237], v[154:157]\n"
      "v_mfma_f32_16x16x32_bf16 v[154:157], a[188:191], v[150:153], v[154:157]\n"
      "v_mfma_f32_16x16x32_bf16 v[162:165], a[156:159], v[234:237], v[162:165]\n"
      "v_mfma_f32_16x16x32_bf16 v[162:165], a[192:195], v[150:153], v[162:165]\n"
      "s_nop 1\n"
      "v_add_f32_dpp v226, v226, v226 quad_perm:[1,0,3,2] row_mask:0xf bank_mask:0xf\n"
      "v_mfma_f32_16x16x32_bf16 v[174:177], a[160:163], v[234:237], v[174:177]\n"
      "s_nop 0\n"
      "v_add_f32_dpp v226, v226, v226 quad_perm:[2,3,0,1] row_mask:0xf bank_mask:0xf\n"
      "v_mfma_f32_16x16x32_bf16 v[174:177], a[196:199], v[150:153], v[174:177]\n"
      "s_nop 0\n"
      "v_add_f32_dpp v226, v226, v226 row_half_mirror row_mask:0xf bank_mask:0xf\n"
      "v_mul_f32 v229, 0xbbaaaaab, v226\n"
      "v_mfma_f32_16x16x32_bf16 v[182:185], a[164:167], v[234:237], v[182:185]\n"
      "v_mul_f32 v228, 0xbbaaaaab, v226\n"
      "v_mul_f32 v227, 0xbbaaaaab, v226\n"
      "v_mfma_f32_16x16x32_bf16 v[182:185], a[200:203], v[150:153], v[182:185]\n"
      "v_mul_f32 v226, 0xbbaaaaab, v226\n"
      "v_mfma_f32_16x16x32_bf16 v[170:173], a[168:171], v[234:237], v[170:173]\n"
      "v_mfma_f32_16x16x32_bf16 v[170:173], a[204:207], v[150:153], v[170:173]\n"
      "v_mfma_f32_16x16x32_bf16 v[238:241], v[138:141], a[216:219], v[226:229]\n"
      "v_mfma_f32_16x16x32_bf16 v[242:245], v[136:139], a[216:219], v[226:229]\n"
      "v_mfma_f32_16x16x32_bf16 v[178:181], a[172:175], v[234:237], v[178:181]\n"
      "v_mfma_f32_16x16x32_bf16 v[178:181], a[208:211], v[150:153], v[178:181]\n"
      "v_mfma_f32_16x16x32_bf16 v[186:189], a[176:179], v[234:237], v[186:189]\n"
      "v_mfma_f32_16x16x32_bf16 v[186:189], a[212:215], v[150:153], v[186:189]\n"
      "v_mfma_f32_16x16x32_bf16 v[194:197], a[144:147], v[230:233], v[194:197]\n"
      "s_nop 1\n"
      "v_cvt_pk_bf16_f32 v238, v238, v239\n"
      "v_mfma_f32_16x16x32_bf16 v[194:197], a[180:183], v[142:145], v[194:197]\n"
      "v_cvt_pk_bf16_f32 v239, v240, v241\n"
      "v_cvt_pk_bf16_f32 v240, v242, v243\n"
      "v_mfma_f32_16x16x32_bf16 v[190:193], a[148:151], v[230:233], v[190:193]\n"
      "v_cvt_pk_bf16_f32 v241, v244, v245\n"
      "v_mfma_f32_16x16x32_bf16 v[190:193], a[184:187], v[142:145], v[190:193]\n"
      "ds_write_b128 v248, v[238:241]\n"
      "v_mfma_f32_16x16x32_bf16 v[238:241], v[138:141], a[220:223], v[226:229]\n"
      "v_mfma_f32_16x16x32_bf16 v[150:153], v[136:139], a[220:223], v[226:229]\n"
      "v_mfma_f32_16x16x32_bf16 v[146:149], a[152:155], v[230:233], v[146:149]\n"
      "v_mfma_f32_16x16x32_bf16 v[146:149], a[188:191], v[142:145], v[146:149]\n"
      "v_mfma_f32_16x16x32_bf16 v[198:201], a[156:159], v[230:233], v[198:201]\n"
      "v_mfma_f32_16x16x32_bf16 v[198:201], a[192:195], v[142:145], v[198:201]\n"
      "v_mfma_f32_16x16x32_bf16 v[202:205], a[160:163], v[230:233], v[202:205]\n"
      "s_nop 1\n"
      "v_cvt_pk_bf16_f32 v238, v238, v239\n"
      "v_mfma_f32_16x16x32_bf16 v[202:205], a[196:199], v[142:145], v[202:205]\n"
      "v_cvt_pk_bf16_f32 v239, v240, v241\n"
      "v_cvt_pk_bf16_f32 v240, v150, v151\n"
      "v_mfma_f32_16x16x32_bf16 v[206:209], a[164:167], v[230:233], v[206:209]\n"
      "v_cvt_pk_bf16_f32 v241, v152, v153\n"
      "v_mfma_f32_16x16x32_bf16 v[206:209], a[200:203], v[142:145], v[206:209]\n"
      "ds_write_b128 v248, v[238:241] offset:128\n"
      "v_mfma_f32_16x16x32_bf16 v[238:241], v[138:141], a[224:227], v[226:229]\n"
      "v_mfma_f32_16x16x32_bf16 v[150:153], v[136:139], a[224:227], v[226:229]\n"
      "v_mfma_f32_16x16x32_bf16 v[214:217], a[168:171], v[230:233], v[214:217]\n"
      "v_mfma_f32_16x16x32_bf16 v[214:217], a[204:207], v[142:145], v[214:217]\n"
      "v_mfma_f32_16x16x32_bf16 v[210:213], a[172:175], v[230:233], v[210:213]\n"
      "v_mfma_f32_16x16x32_bf16 v[210:213], a[208:211], v[142:145], v[210:213]\n"
      "v_mfma_f32_16x16x32_bf16 v[222:225], a[176:179], v[230:233], v[222:225]\n"
      "s_nop 1\n"
      "v_cvt_pk_bf16_f32 v238, v238, v239\n"
      "v_mfma_f32_16x16x32_bf16 v[222:225], a[212:215], v[142:145], v[222:225]\n"
      "v_cvt_pk_bf16_f32 v239, v240, v241\n"
      "v_cvt_pk_bf16_f32 v240, v150, v151\n"
      "s_waitcnt vmcnt(21)\n"
      "v_mfma_f32_16x16x32_bf16 v[142:145], v[218:221], a[228:231], 0\n"
      "v_cvt_pk_bf16_f32 v241, v152, v153\n"
      "v_fma_f32 v226, v166, v166, s38\n"
      "s_waitcnt vmcnt(20)\n"
      "v_mfma_f32_16x16x32_bf16 v[142:145], v[218:221], a[232:235], v[142:145]\n"
      "ds_write_b128 v248, v[238:241] offset:256\n"
      "s_waitcnt vmcnt(19)\n"
      "v_mfma_f32_16x16x32_bf16 v[142:145], v[218:221], a[236:239], v[142:145]\n"
      "v_fma_f32 v227, v162, v162, s38\n"
      "v_fmac_f32 v226, v167, v167\n"
      "v_fmac_f32 v227, v163, v163\n"
      "v_fmac_f32 v226, v168, v168\n"
      "v_fmac_f32 v227, v164, v164\n"
      "v_fmac_f32 v226, v169, v169\n"
      "v_fmac_f32 v227, v165, v165\n"
      "v_fmac_f32 v226, v158, v158\n"
      "v_add_f32_dpp v142, v142, v142 quad_perm:[1,0,3,2] row_mask:0xf bank_mask:0xf\n"
      "v_fmac_f32 v227, v174, v174\n"
      "s_nop 0\n"
      "v_add_f32_dpp v142, v142, v142 quad_perm:[2,3,0,1] row_mask:0xf bank_mask:0xf\n"
      "s_nop 1\n"
      "v_add_f32_dpp v142, v142, v142 row_half_mirror row_mask:0xf bank_mask:0xf\n"
      "v_mul_f32 v145, 0xbbaaaaab, v142\n"
      "v_mul_f32 v144, 0xbbaaaaab, v142\n"
      "v_mul_f32 v143, 0xbbaaaaab, v142\n"
      "v_mul_f32 v142, 0xbbaaaaab, v142\n"
      "v_fmac_f32 v226, v159, v159\n"
      "v_fmac_f32 v227, v175, v175\n"
      "v_mfma_f32_16x16x32_bf16 v[238:241], v[138:141], a[228:231], v[142:145]\n"
      "v_fmac_f32 v226, v160, v160\n"
      "v_fmac_f32 v227, v176, v176\n"
      "v_mfma_f32_16x16x32_bf16 v[150:153], v[136:139], a[228:231], v[142:145]\n"
      "v_fmac_f32 v226, v161, v161\n"
      "s_nop 3\n"
      "v_cvt_pk_bf16_f32 v238, v238, v239\n"
      "v_cvt_pk_bf16_f32 v239, v240, v241\n"
      "v_fmac_f32_e64 v227, v177, v177\n"
      "v_cvt_pk_bf16_f32 v240, v150, v151\n"
      "v_cvt_pk_bf16_f32 v241, v152, v153\n"
      "v_mfma_f32_16x16x32_bf16 v[230:233], v[138:141], a[232:235], v[142:145]\n"
      "ds_write_b128 v248, v[238:241] offset:3072\n"
      "v_mfma_f32_16x16x32_bf16 v[238:241], v[136:139], a[232:235], v[142:145]\n"
      "v_fmac_f32 v226, v154, v154\n"
      "v_fmac_f32 v227, v182, v182\n"
      "v_fmac_f32 v226, v155, v155\n"
      "v_fmac_f32 v227, v183, v183\n"
      "v_fmac_f32 v226, v156, v156\n"
      "v_fmac_f32 v227, v184, v184\n"
      "v_cvt_pk_bf16_f32 v230, v230, v231\n"
      "v_cvt_pk_bf16_f32 v231, v232, v233\n"
      "v_cvt_pk_bf16_f32 v232, v238, v239\n"
      "v_cvt_pk_bf16_f32 v233, v240, v241\n"
      "v_mfma_f32_16x16x32_bf16 v[150:153], v[138:141], a[236:239], v[142:145]\n"
      "ds_write_b128 v248, v[230:233] offset:3200\n"
      "v_mfma_f32_16x16x32_bf16 v[238:241], v[136:139], a[236:239], v[142:145]\n"
      "v_fmac_f32 v226, v157, v157\n"
      "v_fmac_f32 v227, v185, v185\n"
      "s_waitcnt vmcnt(18)\n"
      "v_mfma_f32_16x16x32_bf16 v[230:233], v[218:221], a[240:243], 0\n"
      "v_permlane16_swap_b32 v226, v227\n"
      "s_waitcnt vmcnt(17)\n"
      "v_mfma_f32_16x16x32_bf16 v[230:233], v[218:221], a[244:247], v[230:233]\n"
      "v_cvt_pk_bf16_f32 v150, v150, v151\n"
      "v_cvt_pk_bf16_f32 v151, v152, v153\n"
      "s_waitcnt vmcnt(16)\n"
      "v_mfma_f32_16x16x32_bf16 v[230:233], v[218:221], a[248:251], v[230:233]\n"
      "v_cvt_pk_bf16_f32 v152, v238, v239\n"
      "v_cvt_pk_bf16_f32 v153, v240, v241\n"
      "v_add_f32 v226, v226, v227\n"
      "v_mov_b32 v227, v226\n"
      "s_nop 1\n"
      "v_permlane32_swap_b32 v226, v227\n"
      "v_add_f32 v226, v226, v227\n"
      "v_add_f32_dpp v230, v230, v230 quad_perm:[1,0,3,2] row_mask:0xf bank_mask:0xf\n"
      "v_rsq_f32_e64 v226, v226\n"
      "s_nop 0\n"
      "v_add_f32_dpp v230, v230, v230 quad_perm:[2,3,0,1] row_mask:0xf bank_mask:0xf\n"
      "v_mov_b32 v227, v226\n"
      "s_nop 0\n"
      "v_add_f32_dpp v230, v230, v230 row_half_mirror row_mask:0xf bank_mask:0xf\n"
      "v_mul_f32 v233, 0xbbaaaaab, v230\n"
      "v_mul_f32 v232, 0xbbaaaaab, v230\n"
      "v_mul_f32 v231, 0xbbaaaaab, v230\n"
      "v_mul_f32 v230, 0xbbaaaaab, v230\n"
      "v_permlane16_swap_b32 v226, v227\n"
      "v_mul_f32 v162, v227, v162\n"
      "v_mfma_f32_16x16x32_bf16 v[142:145], v[138:141], a[240:243], v[230:233]\n"
      "ds_write_b128 v248, v[150:153] offset:3328\n"
      "v_mfma_f32_16x16x32_bf16 v[150:153], v[136:139], a[240:243], v[230:233]\n"
      "v_mul_f32 v163, v227, v163\n"
      "v_cvt_pk_bf16_f32 v162, v162, v163\n"
      "v_mul_f32 v164, v227, v164\n"
      "v_mul_f32 v165, v227, v165\n"
      "v_cvt_pk_bf16_f32 v163, v164, v165\n"
      "v_mul_f32 v174, v227, v174\n"
      "v_cvt_pk_bf16_f32 v142, v142, v143\n"
      "v_cvt_pk_bf16_f32 v143, v144, v145\n"
      "v_cvt_pk_bf16_f32 v144, v150, v151\n"
      "v_cvt_pk_bf16_f32 v145, v152, v153\n"
      "v_mfma_f32_16x16x32_bf16 v[238:241], v[138:141], a[244:247], v[230:233]\n"
      "ds_write_b128 v248, v[142:145] offset:6144\n"
      "v_mfma_f32_16x16x32_bf16 v[142:145], v[136:139], a[244:247], v[230:233]\n"
      "v_mul_f32 v175, v227, v175\n"
      "v_cvt_pk_bf16_f32 v164, v174, v175\n"
      "v_mul_f32 v176, v227, v176\n"
      "v_mul_f32_e64 v177, v227, v177\n"
      "v_cvt_pk_bf16_f32 v165, v176, v177\n"
      "v_mfma_f32_16x16x32_bf16 v[150:153], v[138:141], a[248:251], v[230:233]\n"
      "v_cvt_pk_bf16_f32 v238, v238, v239\n"
      "v_cvt_pk_bf16_f32 v239, v240, v241\n"
      "v_cvt_pk_bf16_f32 v240, v142, v143\n"
      "v_cvt_pk_bf16_f32 v241, v144, v145\n"
      "v_mfma_f32_16x16x32_bf16 v[174:177], v[136:139], a[248:251], v[230:233]\n"
      "ds_write_b128 v248, v[238:241] offset:6272\n"
      "s_waitcnt vmcnt(15)\n"
      "v_mfma_f32_16x16x32_bf16 v[142:145], v[218:221], a[252:255], 0\n"
      "v_cvt_pk_bf16_f32 v150, v150, v151\n"
      "v_cvt_pk_bf16_f32 v151, v152, v153\n"
      "s_waitcnt vmcnt(14)\n"
      "v_mfma_f32_16x16x32_bf16 v[142:145], v[218:221], v[0:3], v[142:145]\n"
      "v_mul_f32 v182, v227, v182\n"
      "v_mul_f32_e64 v183, v227, v183\n"
      "s_waitcnt vmcnt(13)\n"
      "v_mfma_f32_16x16x32_bf16 v[142:145], v[218:221], v[4:7], v[142:145]\n"
      "v_cvt_pk_bf16_f32 v152, v174, v175\n"
      "v_cvt_pk_bf16_f32 v153, v176, v177\n"
      "v_cvt_pk_bf16_f32 v34, v182, v183\n"
      "v_mul_f32 v184, v227, v184\n"
      "v_mul_f32 v185, v227, v185\n"
      "v_cvt_pk_bf16_f32 v35, v184, v185\n"
      "v_mul_f32 v226, s36, v226\n"
      "v_mul_f32 v166, v226, v166\n"
      "v_add_f32_dpp v142, v142, v142 quad_perm:[1,0,3,2] row_mask:0xf bank_mask:0xf\n"
      "v_mul_f32_e64 v167, v226, v167\n"
      "v_cvt_pk_bf16_f32 v166, v166, v167\n"
      "v_add_f32_dpp v142, v142, v142 quad_perm:[2,3,0,1] row_mask:0xf bank_mask:0xf\n"
      "v_mul_f32 v168, v226, v168\n"
      "v_mul_f32 v169, v226, v169\n"
      "v_add_f32_dpp v142, v142, v142 row_half_mirror row_mask:0xf bank_mask:0xf\n"
      "v_mul_f32 v145, 0xbbaaaaab, v142\n"
      "v_mul_f32 v144, 0xbbaaaaab, v142\n"
      "v_mul_f32 v143, 0xbbaaaaab, v142\n"
      "v_mul_f32 v142, 0xbbaaaaab, v142\n"
      "v_cvt_pk_bf16_f32 v167, v168, v169\n"
      "v_mul_f32_e64 v158, v226, v158\n"
      "v_mfma_f32_16x16x32_bf16 v[238:241], v[138:141], a[252:255], v[142:145]\n"
      "ds_write_b128 v248, v[150:153] offset:6400\n"
      "v_mfma_f32_16x16x32_bf16 v[150:153], v[136:139], a[252:255], v[142:145]\n"
      "v_mul_f32 v159, v226, v159\n"
      "v_cvt_pk_bf16_f32 v168, v158, v159\n"
      "v_mfma_f32_16x16x32_bf16 v[182:185], v[138:141], v[0:3], v[142:145]\n"
      "v_mul_f32 v160, v226, v160\n"
      "v_mul_f32_e64 v161, v226, v161\n"
      "v_mfma_f32_16x16x32_bf16 v[174:177], v[136:139], v[0:3], v[142:145]\n"
      "v_cvt_pk_bf16_f32 v238, v238, v239\n"
      "v_cvt_pk_bf16_f32 v239, v240, v241\n"
      "v_mfma_f32_16x16x32_bf16 v[230:233], v[138:141], v[4:7], v[142:145]\n"
      "v_cvt_pk_bf16_f32 v240, v150, v151\n"
      "v_cvt_pk_bf16_f32 v241, v152, v153\n"
      "v_mfma_f32_16x16x32_bf16 v[150:153], v[136:139], v[4:7], v[142:145]\n"
      "ds_write_b128 v248, v[238:241] offset:9216\n"
      "s_waitcnt vmcnt(12)\n"
      "v_mfma_f32_16x16x32_bf16 v[238:241], v[218:221], v[8:11], 0\n"
      "v_cvt_pk_bf16_f32 v182, v182, v183\n"
      "v_cvt_pk_bf16_f32 v183, v184, v185\n"
      "s_waitcnt vmcnt(11)\n"
      "v_mfma_f32_16x16x32_bf16 v[238:241], v[218:221], v[12:15], v[238:241]\n"
      "v_cvt_pk_bf16_f32 v184, v174, v175\n"
      "v_cvt_pk_bf16_f32 v169, v160, v161\n"
      "s_waitcnt vmcnt(10)\n"
      "v_mfma_f32_16x16x32_bf16 v[238:241], v[218:221], v[16:19], v[238:241]\n"
      "ds_write_b128 v251, v[166:169]\n"
      "v_cvt_pk_bf16_f32 v185, v176, v177\n"
      "v_mul_f32 v154, v226, v154\n"
      "v_mul_f32 v155, v226, v155\n"
      "v_cvt_pk_bf16_f32 v32, v154, v155\n"
      "v_mul_f32 v156, v226, v156\n"
      "v_mul_f32_e64 v157, v226, v157\n"
      "v_cvt_pk_bf16_f32 v33, v156, v157\n"
      "v_cvt_pk_bf16_f32 v170, v170, v171\n"
      "v_cvt_pk_bf16_f32 v230, v230, v231\n"
      "v_cvt_pk_bf16_f32 v171, v172, v173\n"
      "v_cvt_pk_bf16_f32 v231, v232, v233\n"
      "v_cvt_pk_bf16_f32 v178, v178, v179\n"
      "v_cvt_pk_bf16_f32 v232, v150, v151\n"
      "v_cvt_pk_bf16_f32 v233, v152, v153\n"
      "v_cvt_pk_bf16_f32 v179, v180, v181\n"
      "v_cvt_pk_bf16_f32 v186, v186, v187\n"
      "v_cvt_pk_bf16_f32 v187, v188, v189\n"
      "v_fma_f32 v176, v194, v194, s38\n"
      "v_fma_f32 v188, v198, v198, s38\n"
      "v_fmac_f32 v176, v195, v195\n"
      "v_fmac_f32 v188, v199, v199\n"
      "v_fmac_f32 v176, v196, v196\n"
      "v_add_f32_dpp v238, v238, v238 quad_perm:[1,0,3,2] row_mask:0xf bank_mask:0xf\n"
      "v_fmac_f32 v188, v200, v200\n"
      "v_fmac_f32_e64 v176, v197, v197\n"
      "v_add_f32_dpp v238, v238, v238 quad_perm:[2,3,0,1] row_mask:0xf bank_mask:0xf\n"
      "v_fmac_f32 v188, v201, v201\n"
      "v_fmac_f32 v176, v190, v190\n"
      "v_add_f32_dpp v238, v238, v238 row_half_mirror row_mask:0xf bank_mask:0xf\n"
      "v_mul_f32 v241, 0xbbaaaaab, v238\n"
      "v_mul_f32 v240, 0xbbaaaaab, v238\n"
      "v_mul_f32 v239, 0xbbaaaaab, v238\n"
      "v_fmac_f32 v188, v202, v202\n"
      "v_mul_f32 v238, 0xbbaaaaab, v238\n"
      "v_fmac_f32 v176, v191, v191\n"
      "v_fmac_f32_e64 v188, v203, v203\n"
      "v_mfma_f32_16x16x32_bf16 v[154:157], v[138:141], v[8:11], v[238:241]\n"
      "ds_write_b128 v248, v[182:185] offset:9344\n"
      "v_mfma_f32_16x16x32_bf16 v[166:169], v[136:139], v[8:11], v[238:241]\n"
      "ds_write_b128 v248, v[230:233] offset:9472\n"
      "v_mfma_f32_16x16x32_bf16 v[182:185], v[138:141], v[12:15], v[238:241]\n"
      "ds_write_b128 v251, v[32:35] offset:16\n"
      "v_mfma_f32_16x16x32_bf16 v[150:153], v[136:139], v[12:15], v[238:241]\n"
      "ds_write_b128 v251, v[162:165] offset:32\n"
      "v_mfma_f32_16x16x32_bf16 v[162:165], v[138:141], v[16:19], v[238:241]\n"
      "ds_write_b64 v249, v[170:171]\n"
      "v_mfma_f32_16x16x32_bf16 v[170:173], v[136:139], v[16:19], v[238:241]\n"
      "ds_write_b64 v249, v[178:179] offset:32\n"
      "v_fmac_f32 v176, v192, v192\n"
      "v_cvt_pk_bf16_f32 v154, v154, v155\n"
      "v_fmac_f32 v188, v204, v204\n"
      "v_cvt_pk_bf16_f32 v155, v156, v157\n"
      "v_fmac_f32 v176, v193, v193\n"
      "v_cvt_pk_bf16_f32 v156, v166, v167\n"
      "v_cvt_pk_bf16_f32 v157, v168, v169\n"
      "v_fmac_f32 v188, v205, v205\n"
      "v_fmac_f32 v176, v146, v146\n"
      "v_fmac_f32 v188, v206, v206\n"
      "v_fmac_f32 v176, v147, v147\n"
      "v_fmac_f32 v188, v207, v207\n"
      "v_fmac_f32 v176, v148, v148\n"
      "v_fmac_f32 v188, v208, v208\n"
      "v_fmac_f32 v176, v149, v149\n"
      "v_cvt_pk_bf16_f32 v182, v182, v183\n"
      "v_fmac_f32 v188, v209, v209\n"
      "v_cvt_pk_bf16_f32 v183, v184, v185\n"
      "v_cvt_pk_bf16_f32 v184, v150, v151\n"
      "v_permlane16_swap_b32 v176, v188\n"
      "v_cvt_pk_bf16_f32 v185, v152, v153\n"
      "v_add_f32 v176, v176, v188\n"
      "v_mov_b32 v188, v176\n"
      "v_cvt_pk_bf16_f32 v162, v162, v163\n"
      "s_nop 0\n"
      "v_permlane32_swap_b32 v176, v188\n"
      "v_add_f32 v176, v176, v188\n"
      "v_rsq_f32 v176, v176\n"
      "s_nop 0\n"
      "v_mov_b32 v188, v176\n"
      "s_nop 1\n"
      "v_permlane16_swap_b32_e64 v176, v188\n"
      "v_mul_f32_e64 v198, v188, v198\n"
      "ds_write_b64 v249, v[186:187] offset:64\n"
      "s_waitcnt vmcnt(9)\n"
      "v_mfma_f32_16x16x32_bf16 v[150:153], v[218:221], v[20:23], 0\n"
      "ds_write_b128 v248, v[154:157] offset:12288\n"
      "s_waitcnt vmcnt(8)\n"
      "v_mfma_f32_16x16x32_bf16 v[150:153], v[218:221], v[24:27], v[150:153]\n"
      "ds_write_b128 v248, v[182:185] offset:12416\n"
      "s_waitcnt vmcnt(7)\n"
      "v_mfma_f32_16x16x32_bf16 v[150:153], v[218:221], v[28:31], v[150:153]\n"
      "v_cvt_pk_bf16_f32 v163, v164, v165\n"
      "v_mul_f32 v199, v188, v199\n"
      "v_cvt_pk_bf16_f32 v164, v170, v171\n"
      "v_cvt_pk_bf16_f32 v165, v172, v173\n"
      "v_cvt_pk_bf16_f32 v198, v198, v199\n"
      "v_mul_f32 v200, v188, v200\n"
      "v_mul_f32 v201, v188, v201\n"
      "v_cvt_pk_bf16_f32 v199, v200, v201\n"
      "v_mul_f32 v202, v188, v202\n"
      "v_mul_f32 v203, v188, v203\n"
      "v_cvt_pk_bf16_f32 v200, v202, v203\n"
      "v_mul_f32 v204, v188, v204\n"
      "v_mul_f32 v205, v188, v205\n"
      "v_add_f32_dpp v150, v150, v150 quad_perm:[1,0,3,2] row_mask:0xf bank_mask:0xf\n"
      "v_cvt_pk_bf16_f32 v201, v204, v205\n"
      "v_mul_f32 v206, v188, v206\n"
      "v_add_f32_dpp v150, v150, v150 quad_perm:[2,3,0,1] row_mask:0xf bank_mask:0xf\n"
      "v_mul_f32 v207, v188, v207\n"
      "v_cvt_pk_bf16_f32 v40, v206, v207\n"
      "v_add_f32_dpp v150, v150, v150 row_half_mirror row_mask:0xf bank_mask:0xf\n"
      "v_mul_f32 v153, 0xbbaaaaab, v150\n"
      "v_mul_f32 v152, 0xbbaaaaab, v150\n"
      "v_mul_f32 v151, 0xbbaaaaab, v150\n"
      "v_mul_f32 v208, v188, v208\n"
      "v_mul_f32 v150, 0xbbaaaaab, v150\n"
      "v_mul_f32 v209, v188, v209\n"
      "v_cvt_pk_bf16_f32 v41, v208, v209\n"
      "v_mfma_f32_16x16x32_bf16 v[186:189], v[138:141], v[20:23], v[150:153]\n"
      "ds_write_b128 v248, v[162:165] offset:12544\n"
      "v_mfma_f32_16x16x32_bf16 v[166:169], v[136:139], v[20:23], v[150:153]\n"
      "s_waitcnt vmcnt(0)\n"
      "v_mul_f32 v176, s36, v176\n"
      "v_mul_f32 v194, v176, v194\n"
      "v_mfma_f32_16x16x32_bf16 v[182:185], v[138:141], v[24:27], v[150:153]\n"
      "ds_read_b128 v[154:157], v119 offset:36864\n"
      "v_mul_f32 v195, v176, v195\n"
      "v_mfma_f32_16x16x32_bf16 v[178:181], v[136:139], v[24:27], v[150:153]\n"
      "ds_read_b128 v[162:165], v122\n"
      "v_cvt_pk_bf16_f32 v186, v186, v187\n"
      "v_mfma_f32_16x16x32_bf16 v[170:173], v[138:141], v[28:31], v[150:153]\n"
      "v_cvt_pk_bf16_f32 v187, v188, v189\n"
      "v_cvt_pk_bf16_f32 v188, v166, v167\n"
      "v_mfma_f32_16x16x32_bf16 v[140:143], v[136:139], v[28:31], v[150:153]\n"
      "v_cvt_pk_bf16_f32 v189, v168, v169\n"
      "ds_write_b128 v248, v[186:189] offset:15360\n"
      "v_cvt_pk_bf16_f32 v194, v194, v195\n"
      "v_mul_f32 v196, v176, v196\n"
      "v_mul_f32 v197, v176, v197\n"
      "v_cvt_pk_bf16_f32 v195, v196, v197\n"
      "v_mul_f32_e64 v190, v176, v190\n"
      "v_mul_f32_e64 v191, v176, v191\n"
      "v_cvt_pk_bf16_f32 v196, v190, v191\n"
      "v_mul_f32_e64 v192, v176, v192\n"
      "v_cvt_pk_bf16_f32 v182, v182, v183\n"
      "v_mul_f32_e64 v193, v176, v193\n"
      "v_cvt_pk_bf16_f32 v183, v184, v185\n"
      "v_cvt_pk_bf16_f32 v184, v178, v179\n"
      "v_cvt_pk_bf16_f32 v197, v192, v193\n"
      "v_cvt_pk_bf16_f32 v185, v180, v181\n"
      "ds_write_b128 v248, v[182:185] offset:15488\n"
      "v_mul_f32_e64 v146, v176, v146\n"
      "v_mul_f32_e64 v147, v176, v147\n"
      "v_cvt_pk_bf16_f32 v38, v146, v147\n"
      "v_mul_f32 v148, v176, v148\n"
      "v_mul_f32_e64 v149, v176, v149\n"
      "v_cvt_pk_bf16_f32 v39, v148, v149\n"
      "v_cvt_pk_bf16_f32 v214, v214, v215\n"
      "v_cvt_pk_bf16_f32 v170, v170, v171\n"
      "v_cvt_pk_bf16_f32 v215, v216, v217\n"
      "v_cvt_pk_bf16_f32 v171, v172, v173\n"
      "v_cvt_pk_bf16_f32 v210, v210, v211\n"
      "v_cvt_pk_bf16_f32 v172, v140, v141\n"
      "v_cvt_pk_bf16_f32 v173, v142, v143\n"
      "ds_write_b128 v248, v[170:173] offset:15616\n"
      "v_cvt_pk_bf16_f32 v211, v212, v213\n"
      "v_cvt_pk_bf16_f32 v222, v222, v223\n"
      "v_cvt_pk_bf16_f32 v223, v224, v225\n"
      "s_waitcnt lgkmcnt(2)\n"
      "v_or3_b32 v154, v154, v155, v156\n"
      "s_mov_b32 exec_lo, 0x001f001f\n"
      "s_mov_b32 exec_hi, 0x001f001f\n"
      "ds_write_b128 v251, v[194:197] offset:18432\n"
      "ds_write_b128 v251, v[38:41] offset:18448\n"
      "ds_write_b128 v251, v[198:201] offset:18464\n"
      "ds_write_b64 v249, v[214:215] offset:18432\n"
      "ds_write_b64 v249, v[210:211] offset:18464\n"
      "ds_write_b64 v249, v[222:223] offset:18496\n"
      "s_mov_b64 exec, -1\n"
      "s_mov_b64 exec, 0xfff\n"
      "v_or3_b32 v154, v154, v162, v163\n"
      "v_or3_b32 v154, v154, v164, v165\n"
      "s_mov_b64 exec, -1\n"
      "v_bitop3_b32 v154, v154, v157, s40 bitop3:0xa8\n"
      "ds_write_b32 v129, v154 offset:2048\n"
      "s_waitcnt lgkmcnt(0)\n"
      "s_barrier\n"
      "ds_read_b64_tr_b16 a[216:217], v250\n"
      "ds_read_b64_tr_b16 a[220:221], v250 offset:8\n"
      "ds_read_b64_tr_b16 a[224:225], v247\n"
      "ds_read_b64_tr_b16 a[218:219], v250 offset:6144\n"
      "ds_read_b64_tr_b16 a[222:223], v250 offset:6152\n"
      "ds_read_b64_tr_b16 a[226:227], v247 offset:6144\n"
      "ds_read_b64_tr_b16 a[228:229], v250 offset:18432\n"
      "ds_read_b64_tr_b16 a[232:233], v250 offset:18440\n"
      "ds_read_b64_tr_b16 a[236:237], v247 offset:18432\n"
      "ds_read_b64_tr_b16 a[230:231], v250 offset:24576\n"
      "ds_read_b64_tr_b16 a[234:235], v250 offset:24584\n"
      "ds_read_b64_tr_b16 a[238:239], v247 offset:24576\n"
      "ds_read_b64_tr_b16 a[240:241], v246\n"
      "ds_read_b64_tr_b16 a[244:245], v246 offset:8\n"
      "ds_read_b64_tr_b16 a[248:249], v252\n"
      "ds_read_b64_tr_b16 a[242:243], v246 offset:6144\n"
      "ds_read_b64_tr_b16 a[246:247], v246 offset:6152\n"
      "ds_read_b64_tr_b16 a[250:251], v252 offset:6144\n"
      "ds_read_b64_tr_b16 a[252:253], v246 offset:18432\n"
      "ds_read_b64_tr_b16 v[0:1], v246 offset:18440\n"
      "ds_read_b64_tr_b16 v[4:5], v252 offset:18432\n"
      "ds_read_b64_tr_b16 a[254:255], v246 offset:24576\n"
      "ds_read_b64_tr_b16 v[2:3], v246 offset:24584\n"
      "ds_read_b64_tr_b16 v[6:7], v252 offset:24576\n"
      "ds_read_b64_tr_b16 v[8:9], v250 offset:12288\n"
      "ds_read_b64_tr_b16 v[12:13], v250 offset:12296\n"
      "ds_read_b64_tr_b16 v[16:17], v247 offset:12288\n"
      "ds_read_b64_tr_b16 v[10:11], v250 offset:30720\n"
      "ds_read_b64_tr_b16 v[14:15], v250 offset:30728\n"
      "ds_read_b64_tr_b16 v[18:19], v247 offset:30720\n"
      "ds_read_b64_tr_b16 v[20:21], v246 offset:12288\n"
      "ds_read_b64_tr_b16 v[24:25], v246 offset:12296\n"
      "ds_read_b64_tr_b16 v[28:29], v252 offset:12288\n"
      "ds_read_b64_tr_b16 v[22:23], v246 offset:30720\n"
      "ds_read_b64_tr_b16 v[26:27], v246 offset:30728\n"
      "ds_read_b64_tr_b16 v[30:31], v252 offset:30720\n"
      "s_mov_b32 s27, 0\n"
      "s_add_u32 s28, s27, 1\n"
      "s_min_u32 m0, s28, s3\n"
      "s_nop 0\n"
      "s_movrels_b32 s9, s80\n"
      "s_lshr_b32 s8, s9, 8\n"
      "s_and_b32 s9, s9, 0xff\n"
      "s_mul_i32 s10, s8, 73\n"
      "s_add_u32 s10, s10, s9\n"
      "s_mul_i32 s28, s10, 4802\n"
      "s_add_u32 s56, s18, s28\n"
      "s_addc_u32 s57, s19, 0\n"
      "s_and_b32 s56, s56, -16\n"
      "s_sub_u32 s58, s56, 110592\n"
      "s_subb_u32 s59, s57, 0\n"
      "s_sub_u32 s56, s56, 73728\n"
      "s_subb_u32 s57, s57, 0\n"
      "s_mul_i32 s28, s8, 1373568\n"
      "s_mul_i32 s29, s9, 2688\n"
      "s_add_u32 s28, s28, s29\n"
      "s_cmp_eq_u32 s8, 72\n"
      "s_cselect_b32 s8, 0, 100270464\n"
      "s_cmp_eq_u32 s9, 72\n"
      "s_cselect_b32 s9, 0, 196224\n"
      "s_add_u32 s29, s8, s9\n"
      "s_add_u32 s28, s28, s29\n"
      "s_add_u32 s52, s16, s28\n"
      "s_addc_u32 s53, s17, 0\n"
      "s_cmp_lg_u32 s29, s11\n"
      "s_cbranch_scc1 L_edge_3_%=\n"
      "L_eb_4_%=:\n"
      "s_waitcnt lgkmcnt(0)\n"
      "s_barrier\n"
      ".p2align 6\n"
      "L_loop_%=:\n"
      "v_mfma_f32_16x16x32_bf16 v[200:203], v[172:175], v[66:69], 0\n"
      "ds_read_b128 v[204:207], v114\n"
      "s_add_u32 m0, s76, 115456\n"
      "v_mfma_f32_16x16x32_bf16 v[200:203], v[176:179], v[66:69], v[200:203]\n"
      "ds_read_b128 v[192:195], v115\n"
      "v_mfma_f32_16x16x32_bf16 v[196:199], v[172:175], v[180:183], 0\n"
      "ds_read_b128 v[212:215], v128 offset:1024\n"
      "v_mfma_f32_16x16x32_bf16 v[196:199], v[176:179], v[50:53], v[196:199]\n"
      "ds_read_b128 v[208:211], v128 offset:2048\n"
      "v_mfma_f32_16x16x32_bf16 v[180:183], v[172:175], v[184:187], 0\n"
      "global_load_lds_dwordx4 v119, s[56:57]\n"
      "v_mfma_f32_16x16x32_bf16 v[180:183], v[176:179], v[54:57], v[180:183]\n"
      "ds_read_b128 v[184:187], v114 offset:6144\n"
      "v_mov_b32_dpp v200, v200 quad_perm:[0,0,0,0] row_mask:0xf bank_mask:0xf\n"
      "v_mfma_f32_16x16x32_bf16 v[216:219], v[172:175], v[188:191], 0\n"
      "ds_read_b128 v[188:191], v115 offset:6144\n"
      "s_add_u32 m0, s79, 4864\n"
      "v_mfma_f32_16x16x32_bf16 v[216:219], v[176:179], v[58:61], v[216:219]\n"
      "s_mov_b64 exec, 0xfff\n"
      "global_load_lds_dwordx4 v122, s[58:59]\n"
      "s_mov_b64 exec, -1\n"
      "v_cndmask_b32 v196, v196, v180, s[42:43]\n"
      "v_rcp_f32_e64 v200, v200\n"
      "s_add_u32 m0, s76, 18432\n"
      "s_nop 1\n"
      "v_cndmask_b32 v196, v196, v216, s[44:45]\n"
      "v_mul_f32 v196, v200, v196\n"
      "v_cvt_pk_bf16_f32 v196, v196, v196\n"
      "s_waitcnt lgkmcnt(3)\n"
      "v_mfma_f32_16x16x32_bf16 v[198:201], a[0:3], v[204:207], 0\n"
      "v_mfma_f32_16x16x32_bf16 v[198:201], a[36:39], v[192:195], v[198:201]\n"
      "ds_write_b16 v135, v196 offset:16\n"
      "v_mfma_f32_16x16x32_bf16 v[216:219], a[4:7], v[204:207], 0\n"
      "s_waitcnt lgkmcnt(3)\n"
      "v_add_f32 v212, v212, v213\n"
      "v_add_f32 v214, v214, v215\n"
      "v_mfma_f32_16x16x32_bf16 v[216:219], a[40:43], v[192:195], v[216:219]\n"
      "v_add_f32 v212, v212, v214\n"
      "v_or3_b32 v208, v208, v209, v210\n"
      "v_mfma_f32_16x16x32_bf16 v[224:227], a[8:11], v[204:207], 0\n"
      "ds_read_b128 v[220:223], v114 offset:12288\n"
      "s_waitcnt lgkmcnt(2)\n"
      "v_mfma_f32_16x16x32_bf16 v[224:227], a[44:47], v[192:195], v[224:227]\n"
      "v_rsq_f32 v212, v212\n"
      "v_mfma_f32_16x16x32_bf16 v[228:231], a[12:15], v[204:207], 0\n"
      "ds_read_b128 v[236:239], v115 offset:12288\n"
      "v_mul_f32 v212, s41, v212\n"
      "v_mfma_f32_16x16x32_bf16 v[228:231], a[48:51], v[192:195], v[228:231]\n"
      "v_mov_b32 v215, v212\n"
      "v_or_b32_e64 v208, v208, v211\n"
      "v_mfma_f32_16x16x32_bf16 v[232:235], a[16:19], v[204:207], 0\n"
      "v_permlane32_swap_b32 v212, v215\n"
      "v_mfma_f32_16x16x32_bf16 v[232:235], a[52:55], v[192:195], v[232:235]\n"
      "v_mov_b32 v214, v212\n"
      "v_cmp_ne_u32 s[48:49], 0, v208\n"
      "v_mfma_f32_16x16x32_bf16 v[208:211], a[20:23], v[204:207], 0\n"
      "v_permlane16_swap_b32 v212, v214\n"
      "v_mfma_f32_16x16x32_bf16 v[208:211], a[56:59], v[192:195], v[208:211]\n"
      "v_mul_f32 v136, v212, v136\n"
      "v_mul_f32_e64 v137, v212, v137\n"
      "v_mfma_f32_16x16x32_bf16 v[172:175], v[204:207], a[24:27], 0\n"
      "global_load_lds_dwordx4 v116, s[52:53]\n"
      "v_mfma_f32_16x16x32_bf16 v[172:175], v[192:195], a[60:63], v[172:175]\n"
      "s_add_u32 m0, s76, 24448\n"
      "v_cvt_pk_bf16_f32 v136, v136, v137\n"
      "v_mul_f32 v138, v212, v138\n"
      "v_mfma_f32_16x16x32_bf16 v[176:179], v[204:207], a[28:31], 0\n"
      "v_mul_f32 v139, v212, v139\n"
      "v_cvt_pk_bf16_f32 v137, v138, v139\n"
      "v_mfma_f32_16x16x32_bf16 v[176:179], v[192:195], a[64:67], v[176:179]\n"
      "v_mul_f32 v140, v212, v140\n"
      "v_mul_f32_e64 v141, v212, v141\n"
      "v_mfma_f32_16x16x32_bf16 v[180:183], v[204:207], a[32:35], 0\n"
      "s_waitcnt lgkmcnt(0)\n"
      "v_cvt_pk_bf16_f32 v138, v140, v141\n"
      "v_mul_f32 v142, v212, v142\n"
      "v_mfma_f32_16x16x32_bf16 v[180:183], v[192:195], a[68:71], v[180:183]\n"
      "v_mul_f32 v143, v212, v143\n"
      "v_cvt_pk_bf16_f32 v139, v142, v143\n"
      "v_mfma_f32_16x16x32_bf16 v[198:201], a[72:75], v[184:187], v[198:201]\n"
      "v_mul_f32 v144, v212, v144\n"
      "v_mul_f32 v145, v212, v145\n"
      "v_mfma_f32_16x16x32_bf16 v[198:201], a[108:111], v[188:191], v[198:201]\n"
      "ds_write_b128 v126, v[136:139]\n"
      "v_mfma_f32_16x16x32_bf16 v[216:219], a[76:79], v[184:187], v[216:219]\n"
      "ds_read_b128 v[136:139], v114 offset:2048\n"
      "v_cvt_pk_bf16_f32 v144, v144, v145\n"
      "v_mfma_f32_16x16x32_bf16 v[216:219], a[112:115], v[188:191], v[216:219]\n"
      "v_mul_f32 v146, v212, v146\n"
      "v_mul_f32 v147, v212, v147\n"
      "v_mfma_f32_16x16x32_bf16 v[224:227], a[80:83], v[184:187], v[224:227]\n"
      "ds_read_b128 v[140:143], v115 offset:2048\n"
      "v_cvt_pk_bf16_f32 v145, v146, v147\n"
      "v_mfma_f32_16x16x32_bf16 v[224:227], a[116:119], v[188:191], v[224:227]\n"
      "ds_write_b64 v127, v[144:145]\n"
      "v_mfma_f32_16x16x32_bf16 v[228:231], a[84:87], v[184:187], v[228:231]\n"
      "v_mul_f32 v148, v214, v148\n"
      "v_mul_f32 v149, v214, v149\n"
      "v_mfma_f32_16x16x32_bf16 v[228:231], a[120:123], v[188:191], v[228:231]\n"
      "global_load_lds_dwordx4 v116, s[52:53] offset:128\n"
      "v_mfma_f32_16x16x32_bf16 v[232:235], a[88:91], v[184:187], v[232:235]\n"
      "s_add_u32 m0, s76, 30464\n"
      "v_cvt_pk_bf16_f32 v148, v148, v149\n"
      "v_mul_f32 v150, v214, v150\n"
      "v_mfma_f32_16x16x32_bf16 v[232:235], a[124:127], v[188:191], v[232:235]\n"
      "v_mul_f32 v151, v214, v151\n"
      "v_cvt_pk_bf16_f32 v149, v150, v151\n"
      "v_mfma_f32_16x16x32_bf16 v[208:211], a[92:95], v[184:187], v[208:211]\n"
      "v_mul_f32 v152, v214, v152\n"
      "v_mul_f32 v153, v214, v153\n"
      "v_mfma_f32_16x16x32_bf16 v[208:211], a[128:131], v[188:191], v[208:211]\n"
      "v_cvt_pk_bf16_f32 v150, v152, v153\n"
      "v_mul_f32 v154, v214, v154\n"
      "v_mfma_f32_16x16x32_bf16 v[172:175], v[184:187], a[96:99], v[172:175]\n"
      "v_mul_f32 v155, v214, v155\n"
      "v_cvt_pk_bf16_f32 v151, v154, v155\n"
      "v_mfma_f32_16x16x32_bf16 v[172:175], v[188:191], a[132:135], v[172:175]\n"
      "s_waitcnt lgkmcnt(0)\n"
      "v_mul_f32 v156, v214, v156\n"
      "v_mul_f32 v157, v214, v157\n"
      "v_mfma_f32_16x16x32_bf16 v[176:179], v[184:187], a[100:103], v[176:179]\n"
      "v_cvt_pk_bf16_f32 v156, v156, v157\n"
      "v_mul_f32 v158, v214, v158\n"
      "v_mfma_f32_16x16x32_bf16 v[176:179], v[188:191], a[136:139], v[176:179]\n"
      "ds_write_b128 v126, v[148:151] offset:2048\n"
      "v_mfma_f32_16x16x32_bf16 v[180:183], v[184:187], a[104:107], v[180:183]\n"
      "v_mul_f32_e64 v159, v214, v159\n"
      "v_cvt_pk_bf16_f32 v157, v158, v159\n"
      "v_mfma_f32_16x16x32_bf16 v[180:183], v[188:191], a[140:143], v[180:183]\n"
      "ds_write_b64 v127, v[156:157] offset:2048\n"
      "v_mfma_f32_16x16x32_bf16 v[198:201], a[144:147], v[220:223], v[198:201]\n"
      "v_mul_f32 v160, v215, v160\n"
      "v_mul_f32 v161, v215, v161\n"
      "v_mfma_f32_16x16x32_bf16 v[198:201], a[180:183], v[236:239], v[198:201]\n"
      "ds_read_b128 v[144:147], v114 offset:8192\n"
      "v_cvt_pk_bf16_f32 v160, v160, v161\n"
      "v_mfma_f32_16x16x32_bf16 v[216:219], a[148:151], v[220:223], v[216:219]\n"
      "global_load_lds_dwordx4 v116, s[52:53] offset:256\n"
      "v_mfma_f32_16x16x32_bf16 v[216:219], a[184:187], v[236:239], v[216:219]\n"
      "ds_read_b128 v[148:151], v115 offset:8192\n"
      "v_mul_f32 v162, v215, v162\n"
      "v_mfma_f32_16x16x32_bf16 v[224:227], a[152:155], v[220:223], v[224:227]\n"
      "s_add_u32 m0, s77, 18432\n"
      "v_mul_f32 v163, v215, v163\n"
      "v_cvt_pk_bf16_f32 v161, v162, v163\n"
      "v_mfma_f32_16x16x32_bf16 v[224:227], a[188:191], v[236:239], v[224:227]\n"
      "v_mul_f32 v164, v215, v164\n"
      "v_mul_f32 v165, v215, v165\n"
      "v_mfma_f32_16x16x32_bf16 v[228:231], a[156:159], v[220:223], v[228:231]\n"
      "v_cvt_pk_bf16_f32 v162, v164, v165\n"
      "v_mul_f32 v166, v215, v166\n"
      "v_mfma_f32_16x16x32_bf16 v[228:231], a[192:195], v[236:239], v[228:231]\n"
      "v_mul_f32 v167, v215, v167\n"
      "v_cvt_pk_bf16_f32 v163, v166, v167\n"
      "v_mfma_f32_16x16x32_bf16 v[232:235], a[160:163], v[220:223], v[232:235]\n"
      "v_mul_f32_e64 v168, v215, v168\n"
      "v_mul_f32_e64 v169, v215, v169\n"
      "v_mfma_f32_16x16x32_bf16 v[232:235], a[196:199], v[236:239], v[232:235]\n"
      "ds_write_b128 v126, v[160:163] offset:4096\n"
      "v_mfma_f32_16x16x32_bf16 v[208:211], a[164:167], v[220:223], v[208:211]\n"
      "v_cvt_pk_bf16_f32 v168, v168, v169\n"
      "v_mul_f32_e64 v170, v215, v170\n"
      "v_mfma_f32_16x16x32_bf16 v[208:211], a[200:203], v[236:239], v[208:211]\n"
      "v_mul_f32 v171, v215, v171\n"
      "v_cvt_pk_bf16_f32 v169, v170, v171\n"
      "v_mfma_f32_16x16x32_bf16 v[172:175], v[220:223], a[168:171], v[172:175]\n"
      "s_waitcnt lgkmcnt(1)\n"
      "v_fma_f32 v215, v198, v198, s38\n"
      "v_fma_f32 v214, v228, v228, s38\n"
      "v_mfma_f32_16x16x32_bf16 v[172:175], v[236:239], a[204:207], v[172:175]\n"
      "ds_write_b64 v127, v[168:169] offset:4096\n"
      "v_mfma_f32_16x16x32_bf16 v[176:179], v[220:223], a[172:175], v[176:179]\n"
      "v_fmac_f32 v215, v199, v199\n"
      "v_fmac_f32 v214, v229, v229\n"
      "v_mfma_f32_16x16x32_bf16 v[176:179], v[236:239], a[208:211], v[176:179]\n"
      "v_fmac_f32 v215, v200, v200\n"
      "v_fmac_f32 v214, v230, v230\n"
      "v_mfma_f32_16x16x32_bf16 v[180:183], v[220:223], a[176:179], v[180:183]\n"
      "v_fmac_f32 v215, v201, v201\n"
      "v_fmac_f32 v214, v231, v231\n"
      "v_mfma_f32_16x16x32_bf16 v[180:183], v[236:239], a[212:215], v[180:183]\n"
      "v_fmac_f32 v215, v216, v216\n"
      "v_fmac_f32 v214, v232, v232\n"
      "v_mfma_f32_16x16x32_bf16 v[152:155], a[0:3], v[136:139], 0\n"
      "global_load_lds_dwordx4 v117, s[52:53]\n"
      "v_mfma_f32_16x16x32_bf16 v[152:155], a[36:39], v[140:143], v[152:155]\n"
      "s_add_u32 m0, s78, 18432\n"
      "v_fmac_f32 v215, v217, v217\n"
      "v_fmac_f32 v214, v233, v233\n"
      "v_mfma_f32_16x16x32_bf16 v[164:167], a[4:7], v[136:139], 0\n"
      "v_fmac_f32 v215, v218, v218\n"
      "v_fmac_f32 v214, v234, v234\n"
      "v_mfma_f32_16x16x32_bf16 v[164:167], a[40:43], v[140:143], v[164:167]\n"
      "v_fmac_f32 v215, v219, v219\n"
      "v_fmac_f32 v214, v235, v235\n"
      "v_mfma_f32_16x16x32_bf16 v[156:159], a[8:11], v[136:139], 0\n"
      "v_fmac_f32 v215, v224, v224\n"
      "v_fmac_f32 v214, v208, v208\n"
      "v_mfma_f32_16x16x32_bf16 v[156:159], a[44:47], v[140:143], v[156:159]\n"
      "v_fmac_f32 v215, v225, v225\n"
      "v_fmac_f32 v214, v209, v209\n"
      "v_mfma_f32_16x16x32_bf16 v[160:163], a[12:15], v[136:139], 0\n"
      "ds_read_b128 v[184:187], v114 offset:14336\n"
      "v_fmac_f32 v215, v226, v226\n"
      "v_mfma_f32_16x16x32_bf16 v[160:163], a[48:51], v[140:143], v[160:163]\n"
      "v_fmac_f32 v214, v210, v210\n"
      "v_fmac_f32 v215, v227, v227\n"
      "v_mfma_f32_16x16x32_bf16 v[168:171], a[16:19], v[136:139], 0\n"
      "ds_read_b128 v[188:191], v115 offset:14336\n"
      "v_fmac_f32 v214, v211, v211\n"
      "v_mfma_f32_16x16x32_bf16 v[168:171], a[52:55], v[140:143], v[168:171]\n"
      "s_nop 0\n"
      "v_permlane16_swap_b32 v215, v214\n"
      "v_mfma_f32_16x16x32_bf16 v[192:195], a[20:23], v[136:139], 0\n"
      "v_add_f32 v215, v215, v214\n"
      "v_mov_b32 v214, v215\n"
      "v_mfma_f32_16x16x32_bf16 v[192:195], a[56:59], v[140:143], v[192:195]\n"
      "s_nop 0\n"
      "v_permlane32_swap_b32 v215, v214\n"
      "v_mfma_f32_16x16x32_bf16 v[220:223], v[136:139], a[24:27], 0\n"
      "v_add_f32 v215, v215, v214\n"
      "v_mfma_f32_16x16x32_bf16 v[220:223], v[140:143], a[60:63], v[220:223]\n"
      "v_rsq_f32 v215, v215\n"
      "v_mfma_f32_16x16x32_bf16 v[236:239], v[136:139], a[28:31], 0\n"
      "v_mov_b32 v214, v215\n"
      "v_mfma_f32_16x16x32_bf16 v[236:239], v[140:143], a[64:67], v[236:239]\n"
      "s_nop 0\n"
      "v_permlane16_swap_b32 v215, v214\n"
      "v_mfma_f32_16x16x32_bf16 v[204:207], v[136:139], a[32:35], 0\n"
      "v_mul_f32 v228, v214, v228\n"
      "v_mul_f32 v229, v214, v229\n"
      "v_mfma_f32_16x16x32_bf16 v[204:207], v[140:143], a[68:71], v[204:207]\n"
      "s_mov_b32 exec_hi, 0\n"
      "global_load_lds_dwordx4 v118, s[52:53]\n"
      "s_mov_b32 exec_hi, -1\n"
      "v_mfma_f32_16x16x32_bf16 v[152:155], a[72:75], v[144:147], v[152:155]\n"
      "s_waitcnt lgkmcnt(0)\n"
      "v_cvt_pk_bf16_f32 v228, v228, v229\n"
      "v_mul_f32_e64 v230, v214, v230\n"
      "v_mfma_f32_16x16x32_bf16 v[152:155], a[108:111], v[148:151], v[152:155]\n"
      "s_sub_u32 s28, s27, 3\n"
      "v_mul_f32 v231, v214, v231\n"
      "v_cvt_pk_bf16_f32 v229, v230, v231\n"
      "v_mfma_f32_16x16x32_bf16 v[164:167], a[76:79], v[144:147], v[164:167]\n"
      "s_cselect_b64 s[54:55], s[12:13], s[22:23]\n"
      "v_mul_f32 v232, v214, v232\n"
      "v_mul_f32 v233, v214, v233\n"
      "v_mfma_f32_16x16x32_bf16 v[164:167], a[112:115], v[148:151], v[164:167]\n"
      "s_max_i32 m0, s28, 0\n"
      "v_cvt_pk_bf16_f32 v230, v232, v233\n"
      "v_mul_f32 v234, v214, v234\n"
      "v_mfma_f32_16x16x32_bf16 v[156:159], a[80:83], v[144:147], v[156:159]\n"
      "s_movrels_b32 s9, s80\n"
      "v_mul_f32_e64 v235, v214, v235\n"
      "v_cvt_pk_bf16_f32 v231, v234, v235\n"
      "v_mfma_f32_16x16x32_bf16 v[156:159], a[116:119], v[148:151], v[156:159]\n"
      "s_lshr_b32 s8, s9, 8\n"
      "v_mul_f32 v208, v214, v208\n"
      "v_mul_f32_e64 v209, v214, v209\n"
      "v_mfma_f32_16x16x32_bf16 v[160:163], a[84:87], v[144:147], v[160:163]\n"
      "ds_read_b128 v[136:139], v114 offset:4096\n"
      "v_cvt_pk_bf16_f32 v34, v208, v209\n"
      "v_mfma_f32_16x16x32_bf16 v[160:163], a[120:123], v[148:151], v[160:163]\n"
      "ds_read_b128 v[140:143], v115 offset:4096\n"
      "v_mul_f32 v210, v214, v210\n"
      "v_mfma_f32_16x16x32_bf16 v[168:171], a[88:91], v[144:147], v[168:171]\n"
      "s_and_b32 s9, s9, 0xff\n"
      "v_mul_f32 v211, v214, v211\n"
      "v_cvt_pk_bf16_f32 v35, v210, v211\n"
      "v_mfma_f32_16x16x32_bf16 v[168:171], a[124:127], v[148:151], v[168:171]\n"
      "s_mul_i32 s28, s8, 1373568\n"
      "v_mul_f32 v215, s36, v215\n"
      "v_mul_f32 v198, v215, v198\n"
      "v_mfma_f32_16x16x32_bf16 v[192:195], a[92:95], v[144:147], v[192:195]\n"
      "s_mul_i32 s29, s9, 2688\n"
      "v_mul_f32 v199, v215, v199\n"
      "v_cvt_pk_bf16_f32 v198, v198, v199\n"
      "v_mfma_f32_16x16x32_bf16 v[192:195], a[128:131], v[148:151], v[192:195]\n"
      "s_add_u32 s28, s28, s29\n"
      "v_mul_f32 v200, v215, v200\n"
      "v_mul_f32 v201, v215, v201\n"
      "v_mfma_f32_16x16x32_bf16 v[220:223], v[144:147], a[96:99], v[220:223]\n"
      "s_cmp_eq_u32 s8, 72\n"
      "v_cvt_pk_bf16_f32 v199, v200, v201\n"
      "v_mul_f32 v216, v215, v216\n"
      "v_mfma_f32_16x16x32_bf16 v[220:223], v[148:151], a[132:135], v[220:223]\n"
      "s_cselect_b32 s8, 0, 100270464\n"
      "v_mul_f32 v217, v215, v217\n"
      "v_cvt_pk_bf16_f32 v200, v216, v217\n"
      "v_mfma_f32_16x16x32_bf16 v[236:239], v[144:147], a[100:103], v[236:239]\n"
      "s_cmp_eq_u32 s9, 72\n"
      "v_mul_f32 v218, v215, v218\n"
      "v_mul_f32 v219, v215, v219\n"
      "v_mfma_f32_16x16x32_bf16 v[236:239], v[148:151], a[136:139], v[236:239]\n"
      "s_cselect_b32 s9, 0, 196224\n"
      "v_cvt_pk_bf16_f32 v201, v218, v219\n"
      "v_mul_f32 v224, v215, v224\n"
      "v_mfma_f32_16x16x32_bf16 v[204:207], v[144:147], a[104:107], v[204:207]\n"
      "s_add_u32 s29, s8, s9\n"
      "v_mul_f32 v225, v215, v225\n"
      "s_waitcnt lgkmcnt(0)\n"
      "v_mfma_f32_16x16x32_bf16 v[204:207], v[148:151], a[140:143], v[204:207]\n"
      "ds_read_b128 v[144:147], v119 offset:18432\n"
      "v_cvt_pk_bf16_f32 v32, v224, v225\n"
      "v_mfma_f32_16x16x32_bf16 v[152:155], a[144:147], v[184:187], v[152:155]\n"
      "s_add_u32 s28, s28, s29\n"
      "v_mul_f32 v226, v215, v226\n"
      "v_mul_f32_e64 v227, v215, v227\n"
      "v_mfma_f32_16x16x32_bf16 v[152:155], a[180:183], v[188:191], v[152:155]\n"
      "s_add_u32 s54, s54, s28\n"
      "v_cvt_pk_bf16_f32 v33, v226, v227\n"
      "v_cvt_pk_bf16_f32 v172, v172, v173\n"
      "v_mfma_f32_16x16x32_bf16 v[164:167], a[148:151], v[184:187], v[164:167]\n"
      "s_addc_u32 s55, s55, 0\n"
      "v_cvt_pk_bf16_f32 v173, v174, v175\n"
      "v_cvt_pk_bf16_f32 v176, v176, v177\n"
      "v_mfma_f32_16x16x32_bf16 v[164:167], a[184:187], v[188:191], v[164:167]\n"
      "s_cmp_lg_u32 s29, s11\n"
      "v_cvt_pk_bf16_f32 v177, v178, v179\n"
      "v_cvt_pk_bf16_f32 v180, v180, v181\n"
      "v_mfma_f32_16x16x32_bf16 v[156:159], a[152:155], v[184:187], v[156:159]\n"
      "s_cbranch_scc1 L_edge_5_%=\n"
      "L_eb_6_%=:\n"
      "v_mfma_f32_16x16x32_bf16 v[156:159], a[188:191], v[188:191], v[156:159]\n"
      "v_cvt_pk_bf16_f32 v181, v182, v183\n"
      "v_fma_f32 v196, v152, v152, s38\n"
      "v_mfma_f32_16x16x32_bf16 v[160:163], a[156:159], v[184:187], v[160:163]\n"
      "v_fmac_f32_e64 v196, v153, v153\n"
      "v_mfma_f32_16x16x32_bf16 v[160:163], a[192:195], v[188:191], v[160:163]\n"
      "v_fmac_f32 v196, v154, v154\n"
      "v_mfma_f32_16x16x32_bf16 v[168:171], a[160:163], v[184:187], v[168:171]\n"
      "ds_read_b128 v[224:227], v114 offset:10240\n"
      "s_waitcnt lgkmcnt(1)\n"
      "v_mfma_f32_16x16x32_bf16 v[168:171], a[196:199], v[188:191], v[168:171]\n"
      "global_store_dwordx4 v116, v[144:147], s[54:55] sc1\n"
      "v_mfma_f32_16x16x32_bf16 v[192:195], a[164:167], v[184:187], v[192:195]\n"
      "ds_read_b128 v[148:151], v115 offset:10240\n"
      "v_fma_f32 v197, v160, v160, s38\n"
      "v_mfma_f32_16x16x32_bf16 v[192:195], a[200:203], v[188:191], v[192:195]\n"
      "v_fmac_f32 v197, v161, v161\n"
      "v_fmac_f32 v197, v162, v162\n"
      "v_mfma_f32_16x16x32_bf16 v[220:223], v[184:187], a[168:171], v[220:223]\n"
      "v_fmac_f32 v196, v155, v155\n"
      "v_fmac_f32 v197, v163, v163\n"
      "v_mfma_f32_16x16x32_bf16 v[220:223], v[188:191], a[204:207], v[220:223]\n"
      "v_fmac_f32 v196, v164, v164\n"
      "v_fmac_f32 v197, v168, v168\n"
      "v_mfma_f32_16x16x32_bf16 v[236:239], v[184:187], a[172:175], v[236:239]\n"
      "v_fmac_f32 v196, v165, v165\n"
      "v_fmac_f32 v197, v169, v169\n"
      "v_mfma_f32_16x16x32_bf16 v[236:239], v[188:191], a[208:211], v[236:239]\n"
      "v_fmac_f32 v196, v166, v166\n"
      "v_fmac_f32 v197, v170, v170\n"
      "v_mfma_f32_16x16x32_bf16 v[204:207], v[184:187], a[176:179], v[204:207]\n"
      "v_cvt_pk_bf16_f32 v174, v220, v221\n"
      "v_cvt_pk_bf16_f32 v175, v222, v223\n"
      "v_mfma_f32_16x16x32_bf16 v[204:207], v[188:191], a[212:215], v[204:207]\n"
      "v_fmac_f32 v196, v167, v167\n"
      "v_fmac_f32 v197, v171, v171\n"
      "v_mfma_f32_16x16x32_bf16 v[188:191], a[0:3], v[136:139], 0\n"
      "v_cvt_pk_bf16_f32 v178, v236, v237\n"
      "v_cvt_pk_bf16_f32 v179, v238, v239\n"
      "v_mfma_f32_16x16x32_bf16 v[188:191], a[36:39], v[140:143], v[188:191]\n"
      "v_fmac_f32 v196, v156, v156\n"
      "s_waitcnt lgkmcnt(0)\n"
      "v_mfma_f32_16x16x32_bf16 v[184:187], a[4:7], v[136:139], 0\n"
      "v_cvt_pk_bf16_f32 v182, v204, v205\n"
      "v_cvt_pk_bf16_f32 v183, v206, v207\n"
      "v_mfma_f32_16x16x32_bf16 v[184:187], a[40:43], v[140:143], v[184:187]\n"
      "v_fmac_f32 v197, v192, v192\n"
      "v_fmac_f32 v196, v157, v157\n"
      "v_mfma_f32_16x16x32_bf16 v[236:239], a[8:11], v[136:139], 0\n"
      "ds_read_b128 v[144:147], v119 offset:24576\n"
      "v_fmac_f32 v197, v193, v193\n"
      "v_mfma_f32_16x16x32_bf16 v[236:239], a[44:47], v[140:143], v[236:239]\n"
      "v_fmac_f32 v196, v158, v158\n"
      "v_fmac_f32 v197, v194, v194\n"
      "v_mfma_f32_16x16x32_bf16 v[204:207], a[12:15], v[136:139], 0\n"
      "v_fmac_f32 v196, v159, v159\n"
      "v_fmac_f32 v197, v195, v195\n"
      "v_mfma_f32_16x16x32_bf16 v[204:207], a[48:51], v[140:143], v[204:207]\n"
      "s_nop 0\n"
      "v_permlane16_swap_b32 v196, v197\n"
      "v_mfma_f32_16x16x32_bf16 v[220:223], a[16:19], v[136:139], 0\n"
      "v_add_f32 v196, v196, v197\n"
      "v_mov_b32 v197, v196\n"
      "v_mfma_f32_16x16x32_bf16 v[220:223], a[52:55], v[140:143], v[220:223]\n"
      "s_nop 0\n"
      "v_permlane32_swap_b32 v196, v197\n"
      "v_mfma_f32_16x16x32_bf16 v[216:219], a[20:23], v[136:139], 0\n"
      "v_add_f32 v196, v196, v197\n"
      "v_mfma_f32_16x16x32_bf16 v[216:219], a[56:59], v[140:143], v[216:219]\n"
      "v_rsq_f32 v196, v196\n"
      "v_mfma_f32_16x16x32_bf16 v[232:235], v[136:139], a[24:27], 0\n"
      "v_mov_b32 v197, v196\n"
      "v_mfma_f32_16x16x32_bf16 v[232:235], v[140:143], a[60:63], v[232:235]\n"
      "s_nop 0\n"
      "v_permlane16_swap_b32 v196, v197\n"
      "v_mfma_f32_16x16x32_bf16 v[208:211], v[136:139], a[28:31], 0\n"
      "s_waitcnt lgkmcnt(0)\n"
      "v_mul_f32 v160, v197, v160\n"
      "v_mul_f32_e64 v161, v197, v161\n"
      "v_mfma_f32_16x16x32_bf16 v[208:211], v[140:143], a[64:67], v[208:211]\n"
      "global_store_dwordx4 v116, v[144:147], s[54:55] offset:128 sc1\n"
      "v_mfma_f32_16x16x32_bf16 v[212:215], v[136:139], a[32:35], 0\n"
      "ds_read_b128 v[136:139], v114 offset:16384\n"
      "v_cvt_pk_bf16_f32 v160, v160, v161\n"
      "v_mfma_f32_16x16x32_bf16 v[212:215], v[140:143], a[68:71], v[212:215]\n"
      "ds_read_b128 v[140:143], v115 offset:16384\n"
      "v_mul_f32_e64 v162, v197, v162\n"
      "v_mfma_f32_16x16x32_bf16 v[188:191], a[72:75], v[224:227], v[188:191]\n"
      "v_mul_f32 v163, v197, v163\n"
      "v_cvt_pk_bf16_f32 v161, v162, v163\n"
      "v_mfma_f32_16x16x32_bf16 v[188:191], a[108:111], v[148:151], v[188:191]\n"
      "v_mul_f32 v168, v197, v168\n"
      "v_mul_f32 v169, v197, v169\n"
      "v_mfma_f32_16x16x32_bf16 v[184:187], a[76:79], v[224:227], v[184:187]\n"
      "v_cvt_pk_bf16_f32 v162, v168, v169\n"
      "v_mul_f32 v170, v197, v170\n"
      "v_mfma_f32_16x16x32_bf16 v[184:187], a[112:115], v[148:151], v[184:187]\n"
      "v_mul_f32 v171, v197, v171\n"
      "v_cvt_pk_bf16_f32 v163, v170, v171\n"
      "v_mfma_f32_16x16x32_bf16 v[236:239], a[80:83], v[224:227], v[236:239]\n"
      "v_mul_f32 v192, v197, v192\n"
      "v_mul_f32_e64 v193, v197, v193\n"
      "v_mfma_f32_16x16x32_bf16 v[236:239], a[116:119], v[148:151], v[236:239]\n"
      "v_cvt_pk_bf16_f32 v40, v192, v193\n"
      "v_mul_f32 v194, v197, v194\n"
      "v_mfma_f32_16x16x32_bf16 v[204:207], a[84:87], v[224:227], v[204:207]\n"
      "v_mul_f32 v195, v197, v195\n"
      "v_cvt_pk_bf16_f32 v41, v194, v195\n"
      "v_mfma_f32_16x16x32_bf16 v[204:207], a[120:123], v[148:151], v[204:207]\n"
      "v_mul_f32 v196, s36, v196\n"
      "v_mul_f32 v152, v196, v152\n"
      "v_mfma_f32_16x16x32_bf16 v[220:223], a[88:91], v[224:227], v[220:223]\n"
      "s_waitcnt vmcnt(7)\n"
      "v_mul_f32_e64 v153, v196, v153\n"
      "s_waitcnt lgkmcnt(0)\n"
      "v_mfma_f32_16x16x32_bf16 v[220:223], a[124:127], v[148:151], v[220:223]\n"
      "ds_read_b128 v[192:195], v119 offset:41728\n"
      "v_cvt_pk_bf16_f32 v152, v152, v153\n"
      "v_mfma_f32_16x16x32_bf16 v[216:219], a[92:95], v[224:227], v[216:219]\n"
      "ds_read_b128 v[144:147], v122 offset:4864\n"
      "v_mul_f32_e64 v154, v196, v154\n"
      "v_mfma_f32_16x16x32_bf16 v[216:219], a[128:131], v[148:151], v[216:219]\n"
      "v_mul_f32_e64 v155, v196, v155\n"
      "v_cvt_pk_bf16_f32 v153, v154, v155\n"
      "v_mfma_f32_16x16x32_bf16 v[232:235], v[224:227], a[96:99], v[232:235]\n"
      "ds_read_b128 v[168:171], v132 offset:32\n"
      "v_mul_f32 v164, v196, v164\n"
      "v_mfma_f32_16x16x32_bf16 v[232:235], v[148:151], a[132:135], v[232:235]\n"
      "ds_read_b64 v[240:241], v132 offset:24\n"
      "v_mul_f32 v165, v196, v165\n"
      "v_mfma_f32_16x16x32_bf16 v[208:211], v[224:227], a[100:103], v[208:211]\n"
      "ds_read_u16 v52, v134\n"
      "v_cvt_pk_bf16_f32 v154, v164, v165\n"
      "v_mfma_f32_16x16x32_bf16 v[208:211], v[148:151], a[136:139], v[208:211]\n"
      "ds_read_b64 v[242:243], v133 offset:16\n"
      "v_mul_f32 v166, v196, v166\n"
      "v_mfma_f32_16x16x32_bf16 v[212:215], v[224:227], a[104:107], v[212:215]\n"
      "ds_read_b128 v[224:227], v133\n"
      "v_mul_f32 v167, v196, v167\n"
      "v_mfma_f32_16x16x32_bf16 v[212:215], v[148:151], a[140:143], v[212:215]\n"
      "ds_read_u16 v56, v134 offset:32\n"
      "v_cvt_pk_bf16_f32 v155, v166, v167\n"
      "v_mfma_f32_16x16x32_bf16 v[188:191], a[144:147], v[136:139], v[188:191]\n"
      "ds_read_u16 v60, v134 offset:64\n"
      "v_mul_f32 v156, v196, v156\n"
      "v_mfma_f32_16x16x32_bf16 v[188:191], a[180:183], v[140:143], v[188:191]\n"
      "ds_read_b128 v[164:167], v123 offset:18432\n"
      "v_mul_f32 v157, v196, v157\n"
      "v_mfma_f32_16x16x32_bf16 v[184:187], a[148:151], v[136:139], v[184:187]\n"
      "s_waitcnt lgkmcnt(8)\n"
      "v_cvt_pk_bf16_f32 v38, v156, v157\n"
      "v_mul_f32 v158, v196, v158\n"
      "v_mfma_f32_16x16x32_bf16 v[184:187], a[184:187], v[140:143], v[184:187]\n"
      "ds_read_b128 v[148:151], v123 offset:21504\n"
      "v_mul_f32 v159, v196, v159\n"
      "v_mfma_f32_16x16x32_bf16 v[236:239], a[152:155], v[136:139], v[236:239]\n"
      "v_cvt_pk_bf16_f32 v39, v158, v159\n"
      "v_add_u32 v135, 32, v135\n"
      "v_mfma_f32_16x16x32_bf16 v[236:239], a[188:191], v[140:143], v[236:239]\n"
      "ds_read_b128 v[156:159], v123 offset:24576\n"
      "v_or3_b32 v192, v192, v193, v194\n"
      "v_mfma_f32_16x16x32_bf16 v[204:207], a[156:159], v[136:139], v[204:207]\n"
      "v_fma_f32 v196, v188, v188, s38\n"
      "v_fmac_f32 v196, v189, v189\n"
      "v_mfma_f32_16x16x32_bf16 v[204:207], a[192:195], v[140:143], v[204:207]\n"
      "s_mov_b64 exec, 0xfff\n"
      "v_or3_b32 v192, v192, v144, v145\n"
      "v_or3_b32 v192, v192, v146, v147\n"
      "s_mov_b64 exec, -1\n"
      "v_mfma_f32_16x16x32_bf16 v[220:223], a[160:163], v[136:139], v[220:223]\n"
      "ds_read_b128 v[244:247], v123 offset:27648\n"
      "v_bitop3_b32 v192, v192, v195, s40 bitop3:0xa8\n"
      "v_mfma_f32_16x16x32_bf16 v[220:223], a[196:199], v[140:143], v[220:223]\n"
      "ds_write_b32 v129, v192 offset:3072\n"
      "v_mfma_f32_16x16x32_bf16 v[216:219], a[164:167], v[136:139], v[216:219]\n"
      "s_or_b64 s[34:35], s[48:49], s[50:51]\n"
      "v_fma_f32 v197, v204, v204, s38\n"
      "v_fmac_f32 v197, v205, v205\n"
      "v_mfma_f32_16x16x32_bf16 v[216:219], a[200:203], v[140:143], v[216:219]\n"
      "v_fmac_f32 v196, v190, v190\n"
      "v_fmac_f32 v197, v206, v206\n"
      "v_mfma_f32_16x16x32_bf16 v[232:235], v[136:139], a[168:171], v[232:235]\n"
      "s_waitcnt lgkmcnt(3)\n"
      "v_fmac_f32 v196, v191, v191\n"
      "v_mfma_f32_16x16x32_bf16 v[232:235], v[140:143], a[204:207], v[232:235]\n"
      "v_fmac_f32 v197, v207, v207\n"
      "v_fmac_f32 v196, v184, v184\n"
      "v_mfma_f32_16x16x32_bf16 v[208:211], v[136:139], a[172:175], v[208:211]\n"
      "v_fmac_f32 v197, v220, v220\n"
      "v_fmac_f32 v196, v185, v185\n"
      "v_mfma_f32_16x16x32_bf16 v[208:211], v[140:143], a[208:211], v[208:211]\n"
      "v_fmac_f32 v197, v221, v221\n"
      "v_fmac_f32 v196, v186, v186\n"
      "v_mfma_f32_16x16x32_bf16 v[212:215], v[136:139], a[176:179], v[212:215]\n"
      "v_fmac_f32 v197, v222, v222\n"
      "v_fmac_f32 v196, v187, v187\n"
      "v_mfma_f32_16x16x32_bf16 v[212:215], v[140:143], a[212:215], v[212:215]\n"
      "v_fmac_f32 v197, v223, v223\n"
      "v_fmac_f32 v196, v236, v236\n"
      "v_mfma_f32_16x16x32_bf16 v[136:139], a[216:219], v[164:167], 0\n"
      "s_waitcnt lgkmcnt(0)\n"
      "v_fmac_f32 v197, v216, v216\n"
      "v_fmac_f32 v196, v237, v237\n"
      "v_mfma_f32_16x16x32_bf16 v[136:139], a[228:231], v[148:151], v[136:139]\n"
      "v_fmac_f32 v197, v217, v217\n"
      "v_fmac_f32 v196, v238, v238\n"
      "v_mfma_f32_16x16x32_bf16 v[140:143], a[220:223], v[164:167], 0\n"
      "v_fmac_f32 v197, v218, v218\n"
      "v_fmac_f32 v196, v239, v239\n"
      "v_mfma_f32_16x16x32_bf16 v[140:143], a[232:235], v[148:151], v[140:143]\n"
      "v_fmac_f32 v197, v219, v219\n"
      "v_mfma_f32_16x16x32_bf16 v[144:147], a[224:227], v[164:167], 0\n"
      "s_nop 0\n"
      "v_permlane16_swap_b32 v196, v197\n"
      "v_mfma_f32_16x16x32_bf16 v[144:147], a[236:239], v[148:151], v[144:147]\n"
      "ds_read_b128 v[148:151], v123 offset:30720\n"
      "v_add_f32 v196, v196, v197\n"
      "v_mfma_f32_16x16x32_bf16 v[136:139], a[240:243], v[156:159], v[136:139]\n"
      "v_mov_b32 v197, v196\n"
      "v_mfma_f32_16x16x32_bf16 v[136:139], a[252:255], v[244:247], v[136:139]\n"
      "ds_read_b128 v[192:195], v123 offset:33792\n"
      "v_permlane32_swap_b32 v196, v197\n"
      "v_mfma_f32_16x16x32_bf16 v[140:143], a[244:247], v[156:159], v[140:143]\n"
      "v_add_f32 v196, v196, v197\n"
      "v_mfma_f32_16x16x32_bf16 v[140:143], v[0:3], v[244:247], v[140:143]\n"
      "v_rsq_f32 v196, v196\n"
      "v_mfma_f32_16x16x32_bf16 v[144:147], a[248:251], v[156:159], v[144:147]\n"
      "v_mov_b32 v197, v196\n"
      "v_mfma_f32_16x16x32_bf16 v[144:147], v[4:7], v[244:247], v[144:147]\n"
      "s_nop 0\n"
      "v_permlane16_swap_b32 v196, v197\n"
      "v_mfma_f32_16x16x32_bf16 v[164:167], v[228:231], v[198:201], v[78:81]\n"
      "v_mul_f32 v204, v197, v204\n"
      "v_mul_f32 v205, v197, v205\n"
      "v_mfma_f32_16x16x32_bf16 v[164:167], v[34:37], v[32:35], v[164:167]\n"
      "v_cvt_pk_bf16_f32 v204, v204, v205\n"
      "v_mul_f32 v206, v197, v206\n"
      "v_mul_f32 v207, v197, v207\n"
      "v_cvt_pk_bf16_f32 v205, v206, v207\n"
      "v_mul_f32 v220, v197, v220\n"
      "v_mul_f32 v221, v197, v221\n"
      "v_cvt_pk_bf16_f32 v206, v220, v221\n"
      "v_mfma_f32_16x16x32_bf16 v[156:159], v[160:163], v[198:201], v[82:85]\n"
      "v_mul_f32 v222, v197, v222\n"
      "v_mul_f32 v223, v197, v223\n"
      "v_mfma_f32_16x16x32_bf16 v[156:159], v[40:43], v[32:35], v[156:159]\n"
      "s_waitcnt lgkmcnt(0)\n"
      "v_cvt_pk_bf16_f32 v207, v222, v223\n"
      "v_mul_f32 v216, v197, v216\n"
      "v_mfma_f32_16x16x32_bf16 v[136:139], v[8:11], v[148:151], v[136:139]\n"
      "v_mul_f32 v217, v197, v217\n"
      "v_cvt_pk_bf16_f32 v46, v216, v217\n"
      "v_mfma_f32_16x16x32_bf16 v[136:139], v[20:23], v[192:195], v[136:139]\n"
      "v_mul_f32 v218, v197, v218\n"
      "v_mul_f32 v219, v197, v219\n"
      "v_mfma_f32_16x16x32_bf16 v[140:143], v[12:15], v[148:151], v[140:143]\n"
      "v_cvt_pk_bf16_f32 v47, v218, v219\n"
      "v_mul_f32 v196, s36, v196\n"
      "v_mfma_f32_16x16x32_bf16 v[140:143], v[24:27], v[192:195], v[140:143]\n"
      "v_mul_f32 v188, v196, v188\n"
      "v_mul_f32 v189, v196, v189\n"
      "v_mfma_f32_16x16x32_bf16 v[144:147], v[16:19], v[148:151], v[144:147]\n"
      "v_cvt_pk_bf16_f32 v188, v188, v189\n"
      "v_mul_f32_e64 v190, v196, v190\n"
      "v_mfma_f32_16x16x32_bf16 v[144:147], v[28:31], v[192:195], v[144:147]\n"
      "v_mul_f32 v191, v196, v191\n"
      "v_cvt_pk_bf16_f32 v189, v190, v191\n"
      "v_mfma_f32_16x16x32_bf16 v[192:195], v[168:171], v[198:201], v[70:73]\n"
      "v_mul_f32 v184, v196, v184\n"
      "v_mul_f32 v185, v196, v185\n"
      "v_mfma_f32_16x16x32_bf16 v[192:195], v[224:227], v[228:231], v[192:195]\n"
      "v_cvt_pk_bf16_f32 v190, v184, v185\n"
      "v_mul_f32 v186, v196, v186\n"
      "v_mfma_f32_16x16x32_bf16 v[192:195], v[240:243], v[32:35], v[192:195]\n"
      "v_mul_f32 v187, v196, v187\n"
      "v_cvt_pk_bf16_f32 v191, v186, v187\n"
      "v_mfma_f32_16x16x32_bf16 v[184:187], v[204:207], v[198:201], v[86:89]\n"
      "v_mul_f32 v236, v196, v236\n"
      "v_mul_f32_e64 v237, v196, v237\n"
      "v_mfma_f32_16x16x32_bf16 v[184:187], v[46:49], v[32:35], v[184:187]\n"
      "v_cvt_pk_bf16_f32 v44, v236, v237\n"
      "v_mul_f32 v238, v196, v238\n"
      "v_mul_f32 v239, v196, v239\n"
      "v_cvt_pk_bf16_f32 v45, v238, v239\n"
      "v_cvt_pk_bf16_f32 v50, v232, v233\n"
      "v_cvt_pk_bf16_f32 v51, v234, v235\n"
      "v_cvt_pk_bf16_f32 v54, v208, v209\n"
      "v_cvt_pk_bf16_f32 v55, v210, v211\n"
      "v_cvt_pk_bf16_f32 v58, v212, v213\n"
      "v_cvt_pk_bf16_f32 v59, v214, v215\n"
      "v_fma_f32 v252, v136, v136, s39\n"
      "v_fmac_f32 v252, v137, v137\n"
      "v_fmac_f32 v252, v138, v138\n"
      "v_fmac_f32 v252, v139, v139\n"
      "v_fmac_f32 v252, v140, v140\n"
      "v_fmac_f32 v252, v141, v141\n"
      "v_fmac_f32 v252, v142, v142\n"
      "v_fmac_f32 v252, v143, v143\n"
      "v_fmac_f32 v252, v144, v144\n"
      "v_fmac_f32 v252, v145, v145\n"
      "v_fmac_f32 v252, v146, v146\n"
      "v_fmac_f32 v252, v147, v147\n"
      "s_cbranch_scc1 L_gen_7_%=\n"
      "L_fast_10_%=:\n"
      "v_mfma_f32_16x16x32_bf16 v[236:239], v[228:231], v[152:155], v[90:93]\n"
      "ds_read_b128 v[232:235], v123 offset:18688\n"
      "v_mfma_f32_16x16x32_bf16 v[236:239], v[34:37], v[38:41], v[236:239]\n"
      "ds_read_b128 v[208:211], v123 offset:21760\n"
      "v_exp_f32_e64 v164, v164\n"
      "v_mfma_f32_16x16x32_bf16 v[212:215], v[160:163], v[152:155], v[94:97]\n"
      "ds_read_b128 v[216:219], v119 offset:30720\n"
      "v_mfma_f32_16x16x32_bf16 v[212:215], v[40:43], v[38:41], v[212:215]\n"
      "ds_read_b128 v[244:247], v123 offset:24832\n"
      "v_exp_f32 v165, v165\n"
      "v_mfma_f32_16x16x32_bf16 v[220:223], v[168:171], v[152:155], v[72:75]\n"
      "v_exp_f32 v166, v166\n"
      "v_mfma_f32_16x16x32_bf16 v[220:223], v[224:227], v[160:163], v[220:223]\n"
      "v_cvt_pk_bf16_f32 v164, v164, v165\n"
      "v_exp_f32 v167, v167\n"
      "v_mfma_f32_16x16x32_bf16 v[220:223], v[240:243], v[38:41], v[220:223]\n"
      "v_exp_f32 v156, v156\n"
      "v_mfma_f32_16x16x32_bf16 v[198:201], v[204:207], v[152:155], v[98:101]\n"
      "v_cvt_pk_bf16_f32 v165, v166, v167\n"
      "v_mfma_f32_16x16x32_bf16 v[198:201], v[46:49], v[38:41], v[198:201]\n"
      "v_exp_f32 v157, v157\n"
      "v_exp_f32 v158, v158\n"
      "v_cvt_pk_bf16_f32 v166, v156, v157\n"
      "v_exp_f32 v159, v159\n"
      "s_waitcnt lgkmcnt(1)\n"
      "v_mfma_f32_16x16x32_bf16 v[148:151], a[216:219], v[232:235], 0\n"
      "ds_read_b128 v[194:197], v123 offset:27904\n"
      "v_cvt_pk_bf16_f32 v167, v158, v159\n"
      "v_mfma_f32_16x16x32_bf16 v[148:151], a[228:231], v[208:211], v[148:151]\n"
      "v_exp_f32_e64 v184, v184\n"
      "v_mfma_f32_16x16x32_bf16 v[152:155], a[220:223], v[232:235], 0\n"
      "global_store_dwordx4 v116, v[216:219], s[54:55] offset:256 sc1\n"
      "v_mfma_f32_16x16x32_bf16 v[152:155], a[232:235], v[208:211], v[152:155]\n"
      "v_exp_f32 v185, v185\n"
      "v_mfma_f32_16x16x32_bf16 v[156:159], a[224:227], v[232:235], 0\n"
      "v_exp_f32 v186, v186\n"
      "v_mfma_f32_16x16x32_bf16 v[156:159], a[236:239], v[208:211], v[156:159]\n"
      "ds_read_b128 v[208:211], v123 offset:30976\n"
      "v_exp_f32 v187, v187\n"
      "v_exp_f32 v192, v192\n"
      "v_cvt_pk_bf16_f32 v62, v184, v185\n"
      "v_cvt_pk_bf16_f32 v63, v186, v187\n"
      "v_cvt_pk_bf16_f32 v64, v192, 0\n"
      "v_exp_f32 v236, v236\n"
      "v_exp_f32 v237, v237\n"
      "v_mfma_f32_16x16x32_bf16 v[232:235], v[66:69], v[164:167], 0\n"
      "ds_read_b128 v[184:187], v123 offset:34048\n"
      "v_cvt_pk_bf16_f32 v236, v236, v237\n"
      "v_mfma_f32_16x16x32_bf16 v[232:235], v[66:69], v[62:65], v[232:235]\n"
      "s_waitcnt lgkmcnt(2)\n"
      "v_exp_f32 v238, v238\n"
      "v_mfma_f32_16x16x32_bf16 v[148:151], a[240:243], v[244:247], v[148:151]\n"
      "v_exp_f32 v239, v239\n"
      "v_mfma_f32_16x16x32_bf16 v[148:151], a[252:255], v[194:197], v[148:151]\n"
      "v_exp_f32 v212, v212\n"
      "v_mfma_f32_16x16x32_bf16 v[152:155], a[244:247], v[244:247], v[152:155]\n"
      "v_cvt_pk_bf16_f32 v237, v238, v239\n"
      "v_mfma_f32_16x16x32_bf16 v[152:155], v[0:3], v[194:197], v[152:155]\n"
      "v_exp_f32_e64 v213, v213\n"
      "v_mfma_f32_16x16x32_bf16 v[156:159], a[248:251], v[244:247], v[156:159]\n"
      "ds_read_b128 v[244:247], v123 offset:18944\n"
      "v_cvt_pk_bf16_f32 v238, v212, v213\n"
      "v_mfma_f32_16x16x32_bf16 v[156:159], v[4:7], v[194:197], v[156:159]\n"
      "ds_read_b128 v[194:197], v123 offset:22016\n"
      "v_exp_f32 v214, v214\n"
      "v_mfma_f32_16x16x32_bf16 v[216:219], v[172:175], v[164:167], 0\n"
      "v_exp_f32_e64 v215, v215\n"
      "v_mfma_f32_16x16x32_bf16 v[216:219], v[50:53], v[62:65], v[216:219]\n"
      "s_waitcnt lgkmcnt(2)\n"
      "v_cvt_pk_bf16_f32 v239, v214, v215\n"
      "v_mfma_f32_16x16x32_bf16 v[148:151], v[8:11], v[208:211], v[148:151]\n"
      "v_exp_f32 v198, v198\n"
      "v_mfma_f32_16x16x32_bf16 v[148:151], v[20:23], v[184:187], v[148:151]\n"
      "v_exp_f32 v199, v199\n"
      "v_mfma_f32_16x16x32_bf16 v[152:155], v[12:15], v[208:211], v[152:155]\n"
      "v_exp_f32 v200, v200\n"
      "v_mfma_f32_16x16x32_bf16 v[152:155], v[24:27], v[184:187], v[152:155]\n"
      "v_exp_f32 v201, v201\n"
      "v_mfma_f32_16x16x32_bf16 v[156:159], v[16:19], v[208:211], v[156:159]\n"
      "v_exp_f32 v220, v220\n"
      "v_mfma_f32_16x16x32_bf16 v[156:159], v[28:31], v[184:187], v[156:159]\n"
      "v_fma_f32 v192, v148, v148, s39\n"
      "v_fmac_f32 v192, v149, v149\n"
      "v_mfma_f32_16x16x32_bf16 v[184:187], v[176:179], v[164:167], 0\n"
      "v_fmac_f32 v192, v150, v150\n"
      "v_fmac_f32 v192, v151, v151\n"
      "v_mfma_f32_16x16x32_bf16 v[184:187], v[54:57], v[62:65], v[184:187]\n"
      "v_fmac_f32 v192, v152, v152\n"
      "v_fmac_f32 v192, v153, v153\n"
      "v_mfma_f32_16x16x32_bf16 v[212:215], v[180:183], v[164:167], 0\n"
      "v_fmac_f32 v192, v154, v154\n"
      "v_fmac_f32 v192, v155, v155\n"
      "v_mfma_f32_16x16x32_bf16 v[212:215], v[58:61], v[62:65], v[212:215]\n"
      "v_fmac_f32 v192, v156, v156\n"
      "v_fmac_f32 v192, v157, v157\n"
      "v_mfma_f32_16x16x32_bf16 v[208:211], v[228:231], v[188:191], v[102:105]\n"
      "ds_read_b128 v[164:167], v120 offset:18432\n"
      "v_fmac_f32 v192, v158, v158\n"
      "v_mfma_f32_16x16x32_bf16 v[208:211], v[34:37], v[44:47], v[208:211]\n"
      "s_waitcnt lgkmcnt(1)\n"
      "v_fmac_f32_e64 v192, v159, v159\n"
      "v_cvt_pk_bf16_f32 v62, v198, v199\n"
      "v_mfma_f32_16x16x32_bf16 v[248:251], v[160:163], v[188:191], v[106:109]\n"
      "v_cvt_pk_bf16_f32 v63, v200, v201\n"
      "v_cvt_pk_bf16_f32 v64, v220, 0\n"
      "v_mfma_f32_16x16x32_bf16 v[248:251], v[40:43], v[44:47], v[248:251]\n"
      "v_rcp_f32 v232, v232\n"
      "v_mfma_f32_16x16x32_bf16 v[198:201], v[168:171], v[188:191], v[74:77]\n"
      "v_mul_f32 v216, v232, v216\n"
      "v_mul_f32_e64 v217, v232, v217\n"
      "v_mfma_f32_16x16x32_bf16 v[198:201], v[224:227], v[204:207], v[198:201]\n"
      "v_cvt_pk_bf16_f32 v216, v216, v217\n"
      "v_mul_f32 v218, v232, v218\n"
      "v_mfma_f32_16x16x32_bf16 v[198:201], v[240:243], v[44:47], v[198:201]\n"
      "v_mul_f32 v219, v232, v219\n"
      "v_cvt_pk_bf16_f32 v217, v218, v219\n"
      "v_mfma_f32_16x16x32_bf16 v[228:231], v[204:207], v[188:191], v[110:113]\n"
      "ds_read_b128 v[204:207], v123 offset:25088\n"
      "v_mul_f32 v184, v232, v184\n"
      "v_mfma_f32_16x16x32_bf16 v[228:231], v[46:49], v[44:47], v[228:231]\n"
      "ds_read_b128 v[188:191], v123 offset:28160\n"
      "v_mul_f32 v185, v232, v185\n"
      "v_cvt_pk_bf16_f32 v218, v184, v185\n"
      "v_mul_f32 v186, v232, v186\n"
      "v_mul_f32 v187, v232, v187\n"
      "v_cvt_pk_bf16_f32 v219, v186, v187\n"
      "v_mfma_f32_16x16x32_bf16 v[200:203], v[224:227], v[168:171], v[76:79]\n"
      "ds_write_b128 v124, v[216:219]\n"
      "v_mfma_f32_16x16x32_bf16 v[216:219], v[66:69], v[236:239], 0\n"
      "ds_read_b128 v[168:171], v121 offset:18432\n"
      "v_mul_f32 v212, v232, v212\n"
      "v_mfma_f32_16x16x32_bf16 v[216:219], v[66:69], v[62:65], v[216:219]\n"
      "s_waitcnt lgkmcnt(4)\n"
      "v_mul_f32_e64 v213, v232, v213\n"
      "v_cvt_pk_bf16_f32 v212, v212, v213\n"
      "v_mfma_f32_16x16x16_bf16 v[200:203], v[242:243], v[240:241], v[200:203]\n"
      "global_store_dwordx4 v117, v[164:167], s[54:55] sc1\n"
      "v_mfma_f32_16x16x32_bf16 v[240:243], v[172:175], v[236:239], 0\n"
      "ds_read_b128 v[224:227], v123 offset:31232\n"
      "v_mul_f32_e64 v214, v232, v214\n"
      "v_mfma_f32_16x16x32_bf16 v[240:243], v[50:53], v[62:65], v[240:243]\n"
      "ds_read_b128 v[184:187], v123 offset:34304\n"
      "v_mul_f32_e64 v215, v232, v215\n"
      "v_mfma_f32_16x16x32_bf16 v[232:235], v[176:179], v[236:239], 0\n"
      "v_cvt_pk_bf16_f32 v213, v214, v215\n"
      "v_cndmask_b32 v193, v193, v221, s[42:43]\n"
      "v_mfma_f32_16x16x32_bf16 v[232:235], v[54:57], v[62:65], v[232:235]\n"
      "ds_write_b64 v125, v[212:213]\n"
      "v_mfma_f32_16x16x32_bf16 v[212:215], v[180:183], v[236:239], 0\n"
      "s_waitcnt lgkmcnt(4)\n"
      "v_exp_f32 v208, v208\n"
      "v_mfma_f32_16x16x32_bf16 v[212:215], v[58:61], v[62:65], v[212:215]\n"
      "v_exp_f32 v209, v209\n"
      "v_mfma_f32_16x16x32_bf16 v[160:163], a[216:219], v[244:247], 0\n"
      "s_waitcnt lgkmcnt(3)\n"
      "v_cvt_pk_bf16_f32 v208, v208, v209\n"
      "v_mfma_f32_16x16x32_bf16 v[160:163], a[228:231], v[194:197], v[160:163]\n"
      "s_mov_b32 exec_hi, 0\n"
      "global_store_dwordx4 v118, v[168:171], s[54:55] sc1\n"
      "s_mov_b32 exec_hi, -1\n"
      "v_mfma_f32_16x16x32_bf16 v[164:167], a[220:223], v[244:247], 0\n"
      "s_add_u32 s28, s27, 2\n"
      "v_exp_f32 v210, v210\n"
      "v_mfma_f32_16x16x32_bf16 v[164:167], a[232:235], v[194:197], v[164:167]\n"
      "s_min_u32 m0, s28, s3\n"
      "v_exp_f32 v211, v211\n"
      "v_mfma_f32_16x16x32_bf16 v[168:171], a[224:227], v[244:247], 0\n"
      "s_waitcnt lgkmcnt(1)\n"
      "v_exp_f32_e64 v248, v248\n"
      "v_mfma_f32_16x16x32_bf16 v[168:171], a[236:239], v[194:197], v[168:171]\n"
      "s_movrels_b32 s9, s80\n"
      "v_cvt_pk_bf16_f32 v209, v210, v211\n"
      "v_cndmask_b32 v193, v193, v199, s[44:45]\n"
      "v_mfma_f32_16x16x32_bf16 v[160:163], a[240:243], v[204:207], v[160:163]\n"
      "s_lshr_b32 s8, s9, 8\n"
      "v_exp_f32 v249, v249\n"
      "v_mfma_f32_16x16x32_bf16 v[160:163], a[252:255], v[188:191], v[160:163]\n"
      "s_and_b32 s9, s9, 0xff\n"
      "v_exp_f32_e64 v250, v250\n"
      "v_mfma_f32_16x16x32_bf16 v[164:167], a[244:247], v[204:207], v[164:167]\n"
      "s_mul_i32 s10, s8, 73\n"
      "v_cvt_pk_bf16_f32 v210, v248, v249\n"
      "v_cndmask_b32 v193, v193, v201, s[46:47]\n"
      "v_mfma_f32_16x16x32_bf16 v[164:167], v[0:3], v[188:191], v[164:167]\n"
      "s_add_u32 s10, s10, s9\n"
      "v_exp_f32 v251, v251\n"
      "v_mfma_f32_16x16x32_bf16 v[168:171], a[248:251], v[204:207], v[168:171]\n"
      "s_mul_i32 s28, s10, 4802\n"
      "v_exp_f32 v228, v228\n"
      "v_mfma_f32_16x16x32_bf16 v[168:171], v[4:7], v[188:191], v[168:171]\n"
      "s_add_u32 s56, s18, s28\n"
      "v_cvt_pk_bf16_f32 v211, v250, v251\n"
      "v_exp_f32 v229, v229\n"
      "v_mfma_f32_16x16x32_bf16 v[160:163], v[8:11], v[224:227], v[160:163]\n"
      "s_addc_u32 s57, s19, 0\n"
      "v_exp_f32_e64 v230, v230\n"
      "v_mfma_f32_16x16x32_bf16 v[160:163], v[20:23], v[184:187], v[160:163]\n"
      "s_and_b32 s56, s56, -16\n"
      "v_exp_f32 v231, v231\n"
      "v_mfma_f32_16x16x32_bf16 v[164:167], v[12:15], v[224:227], v[164:167]\n"
      "s_sub_u32 s58, s56, 110592\n"
      "v_exp_f32 v198, v198\n"
      "v_mfma_f32_16x16x32_bf16 v[164:167], v[24:27], v[184:187], v[164:167]\n"
      "s_subb_u32 s59, s57, 0\n"
      "v_cvt_pk_bf16_f32 v62, v228, v229\n"
      "v_cvt_pk_bf16_f32 v63, v230, v231\n"
      "v_mfma_f32_16x16x32_bf16 v[168:171], v[16:19], v[224:227], v[168:171]\n"
      "s_sub_u32 s56, s56, 73728\n"
      "v_cvt_pk_bf16_f32 v64, v198, 0\n"
      "v_fma_f32 v198, v160, v160, s39\n"
      "v_mfma_f32_16x16x32_bf16 v[168:171], v[28:31], v[184:187], v[168:171]\n"
      "s_subb_u32 s57, s57, 0\n"
      "v_exp_f32 v193, v193\n"
      "v_mfma_f32_16x16x32_bf16 v[224:227], v[66:69], v[208:211], 0\n"
      "s_mul_i32 s28, s8, 1373568\n"
      "v_cvt_pk_bf16_f32 v193, v193, v193\n"
      "v_fmac_f32_e64 v198, v161, v161\n"
      "v_mfma_f32_16x16x32_bf16 v[224:227], v[66:69], v[62:65], v[224:227]\n"
      "ds_write_b16 v130, v193\n"
      "v_mfma_f32_16x16x32_bf16 v[228:231], v[172:175], v[208:211], 0\n"
      "ds_read_b128 v[184:187], v131\n"
      "v_fmac_f32_e64 v198, v162, v162\n"
      "v_mfma_f32_16x16x32_bf16 v[228:231], v[50:53], v[62:65], v[228:231]\n"
      "ds_read_b128 v[188:191], v131 offset:64\n"
      "v_fmac_f32 v198, v163, v163\n"
      "v_mfma_f32_16x16x32_bf16 v[248:251], v[176:179], v[208:211], 0\n"
      "s_mul_i32 s29, s9, 2688\n"
      "v_rcp_f32 v216, v216\n"
      "v_mfma_f32_16x16x32_bf16 v[248:251], v[54:57], v[62:65], v[248:251]\n"
      "s_add_u32 s28, s28, s29\n"
      "v_mul_f32 v240, v216, v240\n"
      "v_mul_f32_e64 v241, v216, v241\n"
      "v_mfma_f32_16x16x32_bf16 v[204:207], v[180:183], v[208:211], 0\n"
      "s_cmp_eq_u32 s8, 72\n"
      "v_cvt_pk_bf16_f32 v240, v240, v241\n"
      "v_mul_f32_e64 v242, v216, v242\n"
      "v_mfma_f32_16x16x32_bf16 v[204:207], v[58:61], v[62:65], v[204:207]\n"
      "s_cselect_b32 s8, 0, 100270464\n"
      "v_mul_f32 v243, v216, v243\n"
      "v_cvt_pk_bf16_f32 v241, v242, v243\n"
      "v_mul_f32 v232, v216, v232\n"
      "v_mul_f32_e64 v233, v216, v233\n"
      "v_cvt_pk_bf16_f32 v242, v232, v233\n"
      "v_mul_f32 v234, v216, v234\n"
      "v_mul_f32 v235, v216, v235\n"
      "v_cvt_pk_bf16_f32 v243, v234, v235\n"
      "ds_write_b128 v124, v[240:243] offset:256\n"
      "v_mul_f32 v212, v216, v212\n"
      "v_mul_f32 v213, v216, v213\n"
      "v_cvt_pk_bf16_f32 v212, v212, v213\n"
      "v_mul_f32 v214, v216, v214\n"
      "v_mul_f32 v215, v216, v215\n"
      "v_cvt_pk_bf16_f32 v213, v214, v215\n"
      "ds_write_b64 v125, v[212:213] offset:256\n"
      "v_fmac_f32 v198, v164, v164\n"
      "v_fmac_f32 v198, v165, v165\n"
      "v_fmac_f32 v198, v166, v166\n"
      "v_fmac_f32 v198, v167, v167\n"
      "v_fmac_f32 v198, v168, v168\n"
      "v_fmac_f32 v198, v169, v169\n"
      "v_fmac_f32 v198, v170, v170\n"
      "v_fmac_f32 v198, v171, v171\n"
      "v_rcp_f32 v224, v224\n"
      "v_permlane16_swap_b32 v252, v192\n"
      "v_mul_f32 v228, v224, v228\n"
      "v_mul_f32 v229, v224, v229\n"
      "v_cvt_pk_bf16_f32 v228, v228, v229\n"
      "v_mul_f32 v230, v224, v230\n"
      "v_mov_b32 v212, v198\n"
      "v_mul_f32 v231, v224, v231\n"
      "v_add_f32 v252, v252, v192\n"
      "v_cvt_pk_bf16_f32 v229, v230, v231\n"
      "v_mul_f32 v248, v224, v248\n"
      "v_mul_f32 v249, v224, v249\n"
      "v_permlane16_swap_b32 v198, v212\n"
      "v_cvt_pk_bf16_f32 v230, v248, v249\n"
      "v_mul_f32 v250, v224, v250\n"
      "v_mul_f32 v251, v224, v251\n"
      "v_add_f32 v198, v198, v212\n"
      "v_cvt_pk_bf16_f32 v231, v250, v251\n"
      "ds_write_b128 v124, v[228:231] offset:512\n"
      "v_permlane32_swap_b32 v252, v198\n"
      "v_mul_f32 v204, v224, v204\n"
      "v_mul_f32 v205, v224, v205\n"
      "v_cvt_pk_bf16_f32 v204, v204, v205\n"
      "v_add_f32 v252, v252, v198\n"
      "ds_write_b32 v129, v252\n"
      "v_mul_f32 v206, v224, v206\n"
      "v_mul_f32 v207, v224, v207\n"
      "v_cvt_pk_bf16_f32 v205, v206, v207\n"
      "ds_write_b64 v125, v[204:205] offset:512\n"
      "s_cmp_eq_u32 s9, 72\n"
      "s_cselect_b32 s9, 0, 196224\n"
      "s_add_u32 s29, s8, s9\n"
      "s_add_u32 s28, s28, s29\n"
      "s_add_u32 s52, s16, s28\n"
      "s_addc_u32 s53, s17, 0\n"
      "s_cmp_lg_u32 s29, s11\n"
      "s_cbranch_scc1 L_edge_11_%=\n"
      "L_eb_12_%=:\n"
      "L_join_9_%=:\n"
      "s_cmp_ge_u32 s27, s3\n"
      "s_cbranch_scc1 L_tail0_%=\n"
      "s_add_u32 s27, s27, 1\n"
      "s_waitcnt vmcnt(5)\n"
      "s_waitcnt lgkmcnt(0)\n"
      "s_barrier\n"
      "v_mfma_f32_16x16x32_bf16 v[200:203], v[184:187], v[66:69], 0\n"
      "ds_read_b128 v[208:211], v114 offset:18432\n"
      "s_add_u32 m0, s76, 110592\n"
      "v_mfma_f32_16x16x32_bf16 v[200:203], v[188:191], v[66:69], v[200:203]\n"
      "ds_read_b128 v[192:195], v115 offset:18432\n"
      "v_mfma_f32_16x16x32_bf16 v[196:199], v[184:187], v[172:175], 0\n"
      "ds_read_b128 v[216:219], v128\n"
      "v_mfma_f32_16x16x32_bf16 v[196:199], v[188:191], v[50:53], v[196:199]\n"
      "ds_read_b128 v[212:215], v128 offset:3072\n"
      "v_mfma_f32_16x16x32_bf16 v[204:207], v[184:187], v[176:179], 0\n"
      "global_load_lds_dwordx4 v119, s[56:57]\n"
      "v_mfma_f32_16x16x32_bf16 v[204:207], v[188:191], v[54:57], v[204:207]\n"
      "ds_read_b128 v[172:175], v114 offset:24576\n"
      "v_mov_b32_dpp v200, v200 quad_perm:[0,0,0,0] row_mask:0xf bank_mask:0xf\n"
      "v_mfma_f32_16x16x32_bf16 v[176:179], v[184:187], v[180:183], 0\n"
      "ds_read_b128 v[220:223], v115 offset:24576\n"
      "s_add_u32 m0, s79, 0\n"
      "v_mfma_f32_16x16x32_bf16 v[176:179], v[188:191], v[58:61], v[176:179]\n"
      "s_mov_b64 exec, 0xfff\n"
      "global_load_lds_dwordx4 v122, s[58:59]\n"
      "s_mov_b64 exec, -1\n"
      "v_cndmask_b32 v196, v196, v204, s[42:43]\n"
      "v_rcp_f32_e64 v200, v200\n"
      "s_add_u32 m0, s76, 0\n"
      "s_nop 1\n"
      "v_cndmask_b32 v196, v196, v176, s[44:45]\n"
      "v_mul_f32 v196, v200, v196\n"
      "v_cvt_pk_bf16_f32 v196, v196, v196\n"
      "s_waitcnt lgkmcnt(3)\n"
      "v_mfma_f32_16x16x32_bf16 v[204:207], a[0:3], v[208:211], 0\n"
      "v_mfma_f32_16x16x32_bf16 v[204:207], a[36:39], v[192:195], v[204:207]\n"
      "ds_write_b16 v135, v196\n"
      "v_mfma_f32_16x16x32_bf16 v[176:179], a[4:7], v[208:211], 0\n"
      "s_waitcnt lgkmcnt(3)\n"
      "v_add_f32 v216, v216, v217\n"
      "v_add_f32_e64 v218, v218, v219\n"
      "v_mfma_f32_16x16x32_bf16 v[176:179], a[40:43], v[192:195], v[176:179]\n"
      "v_add_f32_e64 v216, v216, v218\n"
      "v_or3_b32 v212, v212, v213, v214\n"
      "v_mfma_f32_16x16x32_bf16 v[200:203], a[8:11], v[208:211], 0\n"
      "ds_read_b128 v[196:199], v114 offset:30720\n"
      "s_waitcnt lgkmcnt(2)\n"
      "v_mfma_f32_16x16x32_bf16 v[200:203], a[44:47], v[192:195], v[200:203]\n"
      "v_rsq_f32 v216, v216\n"
      "v_mfma_f32_16x16x32_bf16 v[224:227], a[12:15], v[208:211], 0\n"
      "ds_read_b128 v[232:235], v115 offset:30720\n"
      "v_mul_f32 v216, s41, v216\n"
      "v_mfma_f32_16x16x32_bf16 v[224:227], a[48:51], v[192:195], v[224:227]\n"
      "v_mov_b32 v219, v216\n"
      "v_or_b32_e64 v212, v212, v215\n"
      "v_mfma_f32_16x16x32_bf16 v[228:231], a[16:19], v[208:211], 0\n"
      "v_permlane32_swap_b32 v216, v219\n"
      "v_mfma_f32_16x16x32_bf16 v[228:231], a[52:55], v[192:195], v[228:231]\n"
      "v_mov_b32 v218, v216\n"
      "v_cmp_ne_u32 s[48:49], 0, v212\n"
      "v_mfma_f32_16x16x32_bf16 v[212:215], a[20:23], v[208:211], 0\n"
      "v_permlane16_swap_b32 v216, v218\n"
      "v_mfma_f32_16x16x32_bf16 v[212:215], a[56:59], v[192:195], v[212:215]\n"
      "v_mul_f32 v136, v216, v136\n"
      "v_mul_f32_e64 v137, v216, v137\n"
      "v_mfma_f32_16x16x32_bf16 v[180:183], v[208:211], a[24:27], 0\n"
      "global_load_lds_dwordx4 v116, s[52:53]\n"
      "v_mfma_f32_16x16x32_bf16 v[180:183], v[192:195], a[60:63], v[180:183]\n"
      "s_add_u32 m0, s76, 6016\n"
      "v_cvt_pk_bf16_f32 v136, v136, v137\n"
      "v_mul_f32 v138, v216, v138\n"
      "v_mfma_f32_16x16x32_bf16 v[184:187], v[208:211], a[28:31], 0\n"
      "v_mul_f32 v139, v216, v139\n"
      "v_cvt_pk_bf16_f32 v137, v138, v139\n"
      "v_mfma_f32_16x16x32_bf16 v[184:187], v[192:195], a[64:67], v[184:187]\n"
      "v_mul_f32 v140, v216, v140\n"
      "v_mul_f32_e64 v141, v216, v141\n"
      "v_mfma_f32_16x16x32_bf16 v[188:191], v[208:211], a[32:35], 0\n"
      "s_waitcnt lgkmcnt(0)\n"
      "v_cvt_pk_bf16_f32 v138, v140, v141\n"
      "v_mul_f32 v142, v216, v142\n"
      "v_mfma_f32_16x16x32_bf16 v[188:191], v[192:195], a[68:71], v[188:191]\n"
      "v_mul_f32 v143, v216, v143\n"
      "v_cvt_pk_bf16_f32 v139, v142, v143\n"
      "v_mfma_f32_16x16x32_bf16 v[204:207], a[72:75], v[172:175], v[204:207]\n"
      "v_mul_f32 v144, v216, v144\n"
      "v_mul_f32 v145, v216, v145\n"
      "v_mfma_f32_16x16x32_bf16 v[204:207], a[108:111], v[220:223], v[204:207]\n"
      "ds_write_b128 v126, v[136:139] offset:18432\n"
      "v_mfma_f32_16x16x32_bf16 v[176:179], a[76:79], v[172:175], v[176:179]\n"
      "ds_read_b128 v[136:139], v114 offset:20480\n"
      "v_cvt_pk_bf16_f32 v144, v144, v145\n"
      "v_mfma_f32_16x16x32_bf16 v[176:179], a[112:115], v[220:223], v[176:179]\n"
      "v_mul_f32 v146, v216, v146\n"
      "v_mul_f32 v147, v216, v147\n"
      "v_mfma_f32_16x16x32_bf16 v[200:203], a[80:83], v[172:175], v[200:203]\n"
      "ds_read_b128 v[140:143], v115 offset:20480\n"
      "v_cvt_pk_bf16_f32 v145, v146, v147\n"
      "v_mfma_f32_16x16x32_bf16 v[200:203], a[116:119], v[220:223], v[200:203]\n"
      "ds_write_b64 v127, v[144:145] offset:18432\n"
      "v_mfma_f32_16x16x32_bf16 v[224:227], a[84:87], v[172:175], v[224:227]\n"
      "v_mul_f32 v148, v218, v148\n"
      "v_mul_f32 v149, v218, v149\n"
      "v_mfma_f32_16x16x32_bf16 v[224:227], a[120:123], v[220:223], v[224:227]\n"
      "global_load_lds_dwordx4 v116, s[52:53] offset:128\n"
      "v_mfma_f32_16x16x32_bf16 v[228:231], a[88:91], v[172:175], v[228:231]\n"
      "s_add_u32 m0, s76, 12032\n"
      "v_cvt_pk_bf16_f32 v148, v148, v149\n"
      "v_mul_f32 v150, v218, v150\n"
      "v_mfma_f32_16x16x32_bf16 v[228:231], a[124:127], v[220:223], v[228:231]\n"
      "v_mul_f32 v151, v218, v151\n"
      "v_cvt_pk_bf16_f32 v149, v150, v151\n"
      "v_mfma_f32_16x16x32_bf16 v[212:215], a[92:95], v[172:175], v[212:215]\n"
      "v_mul_f32 v152, v218, v152\n"
      "v_mul_f32 v153, v218, v153\n"
      "v_mfma_f32_16x16x32_bf16 v[212:215], a[128:131], v[220:223], v[212:215]\n"
      "v_cvt_pk_bf16_f32 v150, v152, v153\n"
      "v_mul_f32 v154, v218, v154\n"
      "v_mfma_f32_16x16x32_bf16 v[180:183], v[172:175], a[96:99], v[180:183]\n"
      "v_mul_f32 v155, v218, v155\n"
      "v_cvt_pk_bf16_f32 v151, v154, v155\n"
      "v_mfma_f32_16x16x32_bf16 v[180:183], v[220:223], a[132:135], v[180:183]\n"
      "s_waitcnt lgkmcnt(0)\n"
      "v_mul_f32 v156, v218, v156\n"
      "v_mul_f32 v157, v218, v157\n"
      "v_mfma_f32_16x16x32_bf16 v[184:187], v[172:175], a[100:103], v[184:187]\n"
      "v_cvt_pk_bf16_f32 v156, v156, v157\n"
      "v_mul_f32 v158, v218, v158\n"
      "v_mfma_f32_16x16x32_bf16 v[184:187], v[220:223], a[136:139], v[184:187]\n"
      "ds_write_b128 v126, v[148:151] offset:20480\n"
      "v_mfma_f32_16x16x32_bf16 v[188:191], v[172:175], a[104:107], v[188:191]\n"
      "v_mul_f32_e64 v159, v218, v159\n"
      "v_cvt_pk_bf16_f32 v157, v158, v159\n"
      "v_mfma_f32_16x16x32_bf16 v[188:191], v[220:223], a[140:143], v[188:191]\n"
      "ds_write_b64 v127, v[156:157] offset:20480\n"
      "v_mfma_f32_16x16x32_bf16 v[204:207], a[144:147], v[196:199], v[204:207]\n"
      "v_mul_f32 v160, v219, v160\n"
      "v_mul_f32 v161, v219, v161\n"
      "v_mfma_f32_16x16x32_bf16 v[204:207], a[180:183], v[232:235], v[204:207]\n"
      "ds_read_b128 v[144:147], v114 offset:26624\n"
      "v_cvt_pk_bf16_f32 v160, v160, v161\n"
      "v_mfma_f32_16x16x32_bf16 v[176:179], a[148:151], v[196:199], v[176:179]\n"
      "global_load_lds_dwordx4 v116, s[52:53] offset:256\n"
      "v_mfma_f32_16x16x32_bf16 v[176:179], a[184:187], v[232:235], v[176:179]\n"
      "ds_read_b128 v[148:151], v115 offset:26624\n"
      "v_mul_f32 v162, v219, v162\n"
      "v_mfma_f32_16x16x32_bf16 v[200:203], a[152:155], v[196:199], v[200:203]\n"
      "s_add_u32 m0, s77, 0\n"
      "v_mul_f32_e64 v163, v219, v163\n"
      "v_cvt_pk_bf16_f32 v161, v162, v163\n"
      "v_mfma_f32_16x16x32_bf16 v[200:203], a[188:191], v[232:235], v[200:203]\n"
      "v_mul_f32 v164, v219, v164\n"
      "v_mul_f32 v165, v219, v165\n"
      "v_mfma_f32_16x16x32_bf16 v[224:227], a[156:159], v[196:199], v[224:227]\n"
      "v_cvt_pk_bf16_f32 v162, v164, v165\n"
      "v_mul_f32 v166, v219, v166\n"
      "v_mfma_f32_16x16x32_bf16 v[224:227], a[192:195], v[232:235], v[224:227]\n"
      "v_mul_f32 v167, v219, v167\n"
      "v_cvt_pk_bf16_f32 v163, v166, v167\n"
      "v_mfma_f32_16x16x32_bf16 v[228:231], a[160:163], v[196:199], v[228:231]\n"
      "v_mul_f32_e64 v168, v219, v168\n"
      "v_mul_f32_e64 v169, v219, v169\n"
      "v_mfma_f32_16x16x32_bf16 v[228:231], a[196:199], v[232:235], v[228:231]\n"
      "ds_write_b128 v126, v[160:163] offset:22528\n"
      "v_mfma_f32_16x16x32_bf16 v[212:215], a[164:167], v[196:199], v[212:215]\n"
      "v_cvt_pk_bf16_f32 v168, v168, v169\n"
      "v_mul_f32_e64 v170, v219, v170\n"
      "v_mfma_f32_16x16x32_bf16 v[212:215], a[200:203], v[232:235], v[212:215]\n"
      "v_mul_f32 v171, v219, v171\n"
      "v_cvt_pk_bf16_f32 v169, v170, v171\n"
      "v_mfma_f32_16x16x32_bf16 v[180:183], v[196:199], a[168:171], v[180:183]\n"
      "s_waitcnt lgkmcnt(1)\n"
      "v_fma_f32 v219, v204, v204, s38\n"
      "v_fma_f32 v218, v224, v224, s38\n"
      "v_mfma_f32_16x16x32_bf16 v[180:183], v[232:235], a[204:207], v[180:183]\n"
      "ds_write_b64 v127, v[168:169] offset:22528\n"
      "v_mfma_f32_16x16x32_bf16 v[184:187], v[196:199], a[172:175], v[184:187]\n"
      "v_fmac_f32 v219, v205, v205\n"
      "v_fmac_f32 v218, v225, v225\n"
      "v_mfma_f32_16x16x32_bf16 v[184:187], v[232:235], a[208:211], v[184:187]\n"
      "v_fmac_f32 v219, v206, v206\n"
      "v_fmac_f32 v218, v226, v226\n"
      "v_mfma_f32_16x16x32_bf16 v[188:191], v[196:199], a[176:179], v[188:191]\n"
      "v_fmac_f32 v219, v207, v207\n"
      "v_fmac_f32 v218, v227, v227\n"
      "v_mfma_f32_16x16x32_bf16 v[188:191], v[232:235], a[212:215], v[188:191]\n"
      "v_fmac_f32 v219, v176, v176\n"
      "v_fmac_f32 v218, v228, v228\n"
      "v_mfma_f32_16x16x32_bf16 v[152:155], a[0:3], v[136:139], 0\n"
      "global_load_lds_dwordx4 v117, s[52:53]\n"
      "v_mfma_f32_16x16x32_bf16 v[152:155], a[36:39], v[140:143], v[152:155]\n"
      "s_add_u32 m0, s78, 0\n"
      "v_fmac_f32 v219, v177, v177\n"
      "v_fmac_f32_e64 v218, v229, v229\n"
      "v_mfma_f32_16x16x32_bf16 v[164:167], a[4:7], v[136:139], 0\n"
      "v_fmac_f32 v219, v178, v178\n"
      "v_fmac_f32 v218, v230, v230\n"
      "v_mfma_f32_16x16x32_bf16 v[164:167], a[40:43], v[140:143], v[164:167]\n"
      "v_fmac_f32 v219, v179, v179\n"
      "v_fmac_f32 v218, v231, v231\n"
      "v_mfma_f32_16x16x32_bf16 v[156:159], a[8:11], v[136:139], 0\n"
      "v_fmac_f32 v219, v200, v200\n"
      "v_fmac_f32 v218, v212, v212\n"
      "v_mfma_f32_16x16x32_bf16 v[156:159], a[44:47], v[140:143], v[156:159]\n"
      "v_fmac_f32 v219, v201, v201\n"
      "v_fmac_f32 v218, v213, v213\n"
      "v_mfma_f32_16x16x32_bf16 v[160:163], a[12:15], v[136:139], 0\n"
      "ds_read_b128 v[172:175], v114 offset:32768\n"
      "v_fmac_f32 v219, v202, v202\n"
      "v_mfma_f32_16x16x32_bf16 v[160:163], a[48:51], v[140:143], v[160:163]\n"
      "v_fmac_f32 v218, v214, v214\n"
      "v_fmac_f32 v219, v203, v203\n"
      "v_mfma_f32_16x16x32_bf16 v[168:171], a[16:19], v[136:139], 0\n"
      "ds_read_b128 v[232:235], v115 offset:32768\n"
      "v_fmac_f32 v218, v215, v215\n"
      "v_mfma_f32_16x16x32_bf16 v[168:171], a[52:55], v[140:143], v[168:171]\n"
      "s_nop 0\n"
      "v_permlane16_swap_b32 v219, v218\n"
      "v_mfma_f32_16x16x32_bf16 v[192:195], a[20:23], v[136:139], 0\n"
      "v_add_f32 v219, v219, v218\n"
      "v_mov_b32 v218, v219\n"
      "v_mfma_f32_16x16x32_bf16 v[192:195], a[56:59], v[140:143], v[192:195]\n"
      "s_nop 0\n"
      "v_permlane32_swap_b32 v219, v218\n"
      "v_mfma_f32_16x16x32_bf16 v[220:223], v[136:139], a[24:27], 0\n"
      "v_add_f32 v219, v219, v218\n"
      "v_mfma_f32_16x16x32_bf16 v[220:223], v[140:143], a[60:63], v[220:223]\n"
      "v_rsq_f32 v219, v219\n"
      "v_mfma_f32_16x16x32_bf16 v[196:199], v[136:139], a[28:31], 0\n"
      "v_mov_b32 v218, v219\n"
      "v_mfma_f32_16x16x32_bf16 v[196:199], v[140:143], a[64:67], v[196:199]\n"
      "s_nop 0\n"
      "v_permlane16_swap_b32 v219, v218\n"
      "v_mfma_f32_16x16x32_bf16 v[208:211], v[136:139], a[32:35], 0\n"
      "v_mul_f32 v224, v218, v224\n"
      "v_mul_f32 v225, v218, v225\n"
      "v_mfma_f32_16x16x32_bf16 v[208:211], v[140:143], a[68:71], v[208:211]\n"
      "s_mov_b32 exec_hi, 0\n"
      "global_load_lds_dwordx4 v118, s[52:53]\n"
      "s_mov_b32 exec_hi, -1\n"
      "v_mfma_f32_16x16x32_bf16 v[152:155], a[72:75], v[144:147], v[152:155]\n"
      "s_waitcnt lgkmcnt(0)\n"
      "v_cvt_pk_bf16_f32 v224, v224, v225\n"
      "v_mul_f32_e64 v226, v218, v226\n"
      "v_mfma_f32_16x16x32_bf16 v[152:155], a[108:111], v[148:151], v[152:155]\n"
      "s_sub_u32 s28, s27, 3\n"
      "v_mul_f32 v227, v218, v227\n"
      "v_cvt_pk_bf16_f32 v225, v226, v227\n"
      "v_mfma_f32_16x16x32_bf16 v[164:167], a[76:79], v[144:147], v[164:167]\n"
      "s_cselect_b64 s[54:55], s[12:13], s[22:23]\n"
      "v_mul_f32 v228, v218, v228\n"
      "v_mul_f32 v229, v218, v229\n"
      "v_mfma_f32_16x16x32_bf16 v[164:167], a[112:115], v[148:151], v[164:167]\n"
      "s_max_i32 m0, s28, 0\n"
      "v_cvt_pk_bf16_f32 v226, v228, v229\n"
      "v_mul_f32 v230, v218, v230\n"
      "v_mfma_f32_16x16x32_bf16 v[156:159], a[80:83], v[144:147], v[156:159]\n"
      "s_movrels_b32 s9, s80\n"
      "v_mul_f32_e64 v231, v218, v231\n"
      "v_cvt_pk_bf16_f32 v227, v230, v231\n"
      "v_mfma_f32_16x16x32_bf16 v[156:159], a[116:119], v[148:151], v[156:159]\n"
      "s_lshr_b32 s8, s9, 8\n"
      "v_mul_f32 v212, v218, v212\n"
      "v_mul_f32_e64 v213, v218, v213\n"
      "v_mfma_f32_16x16x32_bf16 v[160:163], a[84:87], v[144:147], v[160:163]\n"
      "ds_read_b128 v[136:139], v114 offset:22528\n"
      "v_cvt_pk_bf16_f32 v34, v212, v213\n"
      "v_mfma_f32_16x16x32_bf16 v[160:163], a[120:123], v[148:151], v[160:163]\n"
      "ds_read_b128 v[140:143], v115 offset:22528\n"
      "v_mul_f32 v214, v218, v214\n"
      "v_mfma_f32_16x16x32_bf16 v[168:171], a[88:91], v[144:147], v[168:171]\n"
      "s_and_b32 s9, s9, 0xff\n"
      "v_mul_f32 v215, v218, v215\n"
      "v_cvt_pk_bf16_f32 v35, v214, v215\n"
      "v_mfma_f32_16x16x32_bf16 v[168:171], a[124:127], v[148:151], v[168:171]\n"
      "s_mul_i32 s28, s8, 1373568\n"
      "v_mul_f32 v219, s36, v219\n"
      "v_mul_f32 v204, v219, v204\n"
      "v_mfma_f32_16x16x32_bf16 v[192:195], a[92:95], v[144:147], v[192:195]\n"
      "s_mul_i32 s29, s9, 2688\n"
      "v_mul_f32 v205, v219, v205\n"
      "v_cvt_pk_bf16_f32 v204, v204, v205\n"
      "v_mfma_f32_16x16x32_bf16 v[192:195], a[128:131], v[148:151], v[192:195]\n"
      "s_add_u32 s28, s28, s29\n"
      "v_mul_f32 v206, v219, v206\n"
      "v_mul_f32 v207, v219, v207\n"
      "v_mfma_f32_16x16x32_bf16 v[220:223], v[144:147], a[96:99], v[220:223]\n"
      "s_cmp_eq_u32 s8, 72\n"
      "v_cvt_pk_bf16_f32 v205, v206, v207\n"
      "v_mul_f32 v176, v219, v176\n"
      "v_mfma_f32_16x16x32_bf16 v[220:223], v[148:151], a[132:135], v[220:223]\n"
      "s_cselect_b32 s8, 0, 100270464\n"
      "v_mul_f32 v177, v219, v177\n"
      "v_cvt_pk_bf16_f32 v206, v176, v177\n"
      "v_mfma_f32_16x16x32_bf16 v[196:199], v[144:147], a[100:103], v[196:199]\n"
      "s_cmp_eq_u32 s9, 72\n"
      "v_mul_f32 v178, v219, v178\n"
      "v_mul_f32 v179, v219, v179\n"
      "v_mfma_f32_16x16x32_bf16 v[196:199], v[148:151], a[136:139], v[196:199]\n"
      "s_cselect_b32 s9, 0, 196224\n"
      "v_cvt_pk_bf16_f32 v207, v178, v179\n"
      "v_mul_f32 v200, v219, v200\n"
      "v_mfma_f32_16x16x32_bf16 v[208:211], v[144:147], a[104:107], v[208:211]\n"
      "s_add_u32 s29, s8, s9\n"
      "v_mul_f32 v201, v219, v201\n"
      "s_waitcnt lgkmcnt(0)\n"
      "v_mfma_f32_16x16x32_bf16 v[208:211], v[148:151], a[140:143], v[208:211]\n"
      "ds_read_b128 v[144:147], v119\n"
      "v_cvt_pk_bf16_f32 v32, v200, v201\n"
      "v_mfma_f32_16x16x32_bf16 v[152:155], a[144:147], v[172:175], v[152:155]\n"
      "s_add_u32 s28, s28, s29\n"
      "v_mul_f32 v202, v219, v202\n"
      "v_mul_f32_e64 v203, v219, v203\n"
      "v_mfma_f32_16x16x32_bf16 v[152:155], a[180:183], v[232:235], v[152:155]\n"
      "s_add_u32 s54, s54, s28\n"
      "v_cvt_pk_bf16_f32 v33, v202, v203\n"
      "v_cvt_pk_bf16_f32 v180, v180, v181\n"
      "v_mfma_f32_16x16x32_bf16 v[164:167], a[148:151], v[172:175], v[164:167]\n"
      "s_addc_u32 s55, s55, 0\n"
      "v_cvt_pk_bf16_f32 v181, v182, v183\n"
      "v_cvt_pk_bf16_f32 v184, v184, v185\n"
      "v_mfma_f32_16x16x32_bf16 v[164:167], a[184:187], v[232:235], v[164:167]\n"
      "s_cmp_lg_u32 s29, s11\n"
      "v_cvt_pk_bf16_f32 v185, v186, v187\n"
      "v_cvt_pk_bf16_f32 v188, v188, v189\n"
      "v_mfma_f32_16x16x32_bf16 v[156:159], a[152:155], v[172:175], v[156:159]\n"
      "s_cbranch_scc1 L_edge_18_%=\n"
      "L_eb_19_%=:\n"
      "v_mfma_f32_16x16x32_bf16 v[156:159], a[188:191], v[232:235], v[156:159]\n"
      "v_cvt_pk_bf16_f32 v189, v190, v191\n"
      "v_fma_f32 v240, v152, v152, s38\n"
      "v_mfma_f32_16x16x32_bf16 v[160:163], a[156:159], v[172:175], v[160:163]\n"
      "v_fmac_f32_e64 v240, v153, v153\n"
      "v_mfma_f32_16x16x32_bf16 v[160:163], a[192:195], v[232:235], v[160:163]\n"
      "v_fmac_f32 v240, v154, v154\n"
      "v_mfma_f32_16x16x32_bf16 v[168:171], a[160:163], v[172:175], v[168:171]\n"
      "ds_read_b128 v[176:179], v114 offset:28672\n"
      "s_waitcnt lgkmcnt(1)\n"
      "v_mfma_f32_16x16x32_bf16 v[168:171], a[196:199], v[232:235], v[168:171]\n"
      "global_store_dwordx4 v116, v[144:147], s[54:55] sc1\n"
      "v_mfma_f32_16x16x32_bf16 v[192:195], a[164:167], v[172:175], v[192:195]\n"
      "ds_read_b128 v[148:151], v115 offset:28672\n"
      "v_fma_f32 v236, v160, v160, s38\n"
      "v_mfma_f32_16x16x32_bf16 v[192:195], a[200:203], v[232:235], v[192:195]\n"
      "v_fmac_f32 v236, v161, v161\n"
      "v_fmac_f32 v236, v162, v162\n"
      "v_mfma_f32_16x16x32_bf16 v[220:223], v[172:175], a[168:171], v[220:223]\n"
      "v_fmac_f32 v240, v155, v155\n"
      "v_fmac_f32 v236, v163, v163\n"
      "v_mfma_f32_16x16x32_bf16 v[220:223], v[232:235], a[204:207], v[220:223]\n"
      "v_fmac_f32 v240, v164, v164\n"
      "v_fmac_f32 v236, v168, v168\n"
      "v_mfma_f32_16x16x32_bf16 v[196:199], v[172:175], a[172:175], v[196:199]\n"
      "v_fmac_f32 v240, v165, v165\n"
      "v_fmac_f32 v236, v169, v169\n"
      "v_mfma_f32_16x16x32_bf16 v[196:199], v[232:235], a[208:211], v[196:199]\n"
      "v_fmac_f32 v240, v166, v166\n"
      "v_fmac_f32 v236, v170, v170\n"
      "v_mfma_f32_16x16x32_bf16 v[208:211], v[172:175], a[176:179], v[208:211]\n"
      "v_cvt_pk_bf16_f32 v182, v220, v221\n"
      "v_cvt_pk_bf16_f32 v183, v222, v223\n"
      "v_mfma_f32_16x16x32_bf16 v[208:211], v[232:235], a[212:215], v[208:211]\n"
      "v_fmac_f32 v240, v167, v167\n"
      "v_fmac_f32 v236, v171, v171\n"
      "v_mfma_f32_16x16x32_bf16 v[232:235], a[0:3], v[136:139], 0\n"
      "v_cvt_pk_bf16_f32 v186, v196, v197\n"
      "v_cvt_pk_bf16_f32 v187, v198, v199\n"
      "v_mfma_f32_16x16x32_bf16 v[232:235], a[36:39], v[140:143], v[232:235]\n"
      "v_fmac_f32 v240, v156, v156\n"
      "s_waitcnt lgkmcnt(0)\n"
      "v_mfma_f32_16x16x32_bf16 v[172:175], a[4:7], v[136:139], 0\n"
      "v_cvt_pk_bf16_f32 v190, v208, v209\n"
      "v_cvt_pk_bf16_f32 v191, v210, v211\n"
      "v_mfma_f32_16x16x32_bf16 v[172:175], a[40:43], v[140:143], v[172:175]\n"
      "v_fmac_f32 v236, v192, v192\n"
      "v_fmac_f32 v240, v157, v157\n"
      "v_mfma_f32_16x16x32_bf16 v[196:199], a[8:11], v[136:139], 0\n"
      "ds_read_b128 v[144:147], v119 offset:6144\n"
      "v_fmac_f32 v236, v193, v193\n"
      "v_mfma_f32_16x16x32_bf16 v[196:199], a[44:47], v[140:143], v[196:199]\n"
      "v_fmac_f32 v240, v158, v158\n"
      "v_fmac_f32 v236, v194, v194\n"
      "v_mfma_f32_16x16x32_bf16 v[208:211], a[12:15], v[136:139], 0\n"
      "v_fmac_f32 v240, v159, v159\n"
      "v_fmac_f32 v236, v195, v195\n"
      "v_mfma_f32_16x16x32_bf16 v[208:211], a[48:51], v[140:143], v[208:211]\n"
      "s_nop 0\n"
      "v_permlane16_swap_b32 v240, v236\n"
      "v_mfma_f32_16x16x32_bf16 v[220:223], a[16:19], v[136:139], 0\n"
      "v_add_f32 v240, v240, v236\n"
      "v_mov_b32 v236, v240\n"
      "v_mfma_f32_16x16x32_bf16 v[220:223], a[52:55], v[140:143], v[220:223]\n"
      "s_nop 0\n"
      "v_permlane32_swap_b32 v240, v236\n"
      "v_mfma_f32_16x16x32_bf16 v[200:203], a[20:23], v[136:139], 0\n"
      "v_add_f32 v240, v240, v236\n"
      "v_mfma_f32_16x16x32_bf16 v[200:203], a[56:59], v[140:143], v[200:203]\n"
      "v_rsq_f32 v240, v240\n"
      "v_mfma_f32_16x16x32_bf16 v[228:231], v[136:139], a[24:27], 0\n"
      "v_mov_b32 v236, v240\n"
      "v_mfma_f32_16x16x32_bf16 v[228:231], v[140:143], a[60:63], v[228:231]\n"
      "s_nop 0\n"
      "v_permlane16_swap_b32 v240, v236\n"
      "v_mfma_f32_16x16x32_bf16 v[212:215], v[136:139], a[28:31], 0\n"
      "s_waitcnt lgkmcnt(0)\n"
      "v_mul_f32 v160, v236, v160\n"
      "v_mul_f32_e64 v161, v236, v161\n"
      "v_mfma_f32_16x16x32_bf16 v[212:215], v[140:143], a[64:67], v[212:215]\n"
      "global_store_dwordx4 v116, v[144:147], s[54:55] offset:128 sc1\n"
      "v_mfma_f32_16x16x32_bf16 v[216:219], v[136:139], a[32:35], 0\n"
      "ds_read_b128 v[136:139], v114 offset:34816\n"
      "v_cvt_pk_bf16_f32 v160, v160, v161\n"
      "v_mfma_f32_16x16x32_bf16 v[216:219], v[140:143], a[68:71], v[216:219]\n"
      "ds_read_b128 v[140:143], v115 offset:34816\n"
      "v_mul_f32 v162, v236, v162\n"
      "v_mfma_f32_16x16x32_bf16 v[232:235], a[72:75], v[176:179], v[232:235]\n"
      "v_mul_f32 v163, v236, v163\n"
      "v_cvt_pk_bf16_f32 v161, v162, v163\n"
      "v_mfma_f32_16x16x32_bf16 v[232:235], a[108:111], v[148:151], v[232:235]\n"
      "v_mul_f32 v168, v236, v168\n"
      "v_mul_f32 v169, v236, v169\n"
      "v_mfma_f32_16x16x32_bf16 v[172:175], a[76:79], v[176:179], v[172:175]\n"
      "v_cvt_pk_bf16_f32 v162, v168, v169\n"
      "v_mul_f32 v170, v236, v170\n"
      "v_mfma_f32_16x16x32_bf16 v[172:175], a[112:115], v[148:151], v[172:175]\n"
      "v_mul_f32 v171, v236, v171\n"
      "v_cvt_pk_bf16_f32 v163, v170, v171\n"
      "v_mfma_f32_16x16x32_bf16 v[196:199], a[80:83], v[176:179], v[196:199]\n"
      "v_mul_f32 v192, v236, v192\n"
      "v_mul_f32 v193, v236, v193\n"
      "v_mfma_f32_16x16x32_bf16 v[196:199], a[116:119], v[148:151], v[196:199]\n"
      "v_cvt_pk_bf16_f32 v40, v192, v193\n"
      "v_mul_f32 v194, v236, v194\n"
      "v_mfma_f32_16x16x32_bf16 v[208:211], a[84:87], v[176:179], v[208:211]\n"
      "v_mul_f32 v195, v236, v195\n"
      "v_cvt_pk_bf16_f32 v41, v194, v195\n"
      "v_mfma_f32_16x16x32_bf16 v[208:211], a[120:123], v[148:151], v[208:211]\n"
      "s_waitcnt vmcnt(7)\n"
      "v_mul_f32 v240, s36, v240\n"
      "v_mul_f32_e64 v152, v240, v152\n"
      "v_mfma_f32_16x16x32_bf16 v[220:223], a[88:91], v[176:179], v[220:223]\n"
      "ds_read_b128 v[168:171], v119 offset:36864\n"
      "s_waitcnt lgkmcnt(1)\n"
      "v_mfma_f32_16x16x32_bf16 v[220:223], a[124:127], v[148:151], v[220:223]\n"
      "ds_read_b128 v[144:147], v122\n"
      "v_mul_f32 v153, v240, v153\n"
      "v_mfma_f32_16x16x32_bf16 v[200:203], a[92:95], v[176:179], v[200:203]\n"
      "v_cvt_pk_bf16_f32 v152, v152, v153\n"
      "v_mul_f32 v154, v240, v154\n"
      "v_mfma_f32_16x16x32_bf16 v[200:203], a[128:131], v[148:151], v[200:203]\n"
      "v_mul_f32 v155, v240, v155\n"
      "v_cvt_pk_bf16_f32 v153, v154, v155\n"
      "v_mfma_f32_16x16x32_bf16 v[228:231], v[176:179], a[96:99], v[228:231]\n"
      "ds_read_b128 v[192:195], v132 offset:1184\n"
      "v_mul_f32_e64 v164, v240, v164\n"
      "v_mfma_f32_16x16x32_bf16 v[228:231], v[148:151], a[132:135], v[228:231]\n"
      "ds_read_b64 v[236:237], v132 offset:1176\n"
      "v_mul_f32_e64 v165, v240, v165\n"
      "v_mfma_f32_16x16x32_bf16 v[212:215], v[176:179], a[100:103], v[212:215]\n"
      "ds_read_u16 v52, v134 offset:1152\n"
      "v_cvt_pk_bf16_f32 v154, v164, v165\n"
      "v_mfma_f32_16x16x32_bf16 v[212:215], v[148:151], a[136:139], v[212:215]\n"
      "ds_read_b64 v[238:239], v133 offset:1168\n"
      "v_mul_f32 v166, v240, v166\n"
      "v_mfma_f32_16x16x32_bf16 v[216:219], v[176:179], a[104:107], v[216:219]\n"
      "ds_read_b128 v[176:179], v133 offset:1152\n"
      "v_mul_f32 v167, v240, v167\n"
      "v_mfma_f32_16x16x32_bf16 v[216:219], v[148:151], a[140:143], v[216:219]\n"
      "ds_read_u16 v56, v134 offset:1184\n"
      "v_cvt_pk_bf16_f32 v155, v166, v167\n"
      "v_mfma_f32_16x16x32_bf16 v[232:235], a[144:147], v[136:139], v[232:235]\n"
      "ds_read_u16 v60, v134 offset:1216\n"
      "v_mul_f32 v156, v240, v156\n"
      "v_mfma_f32_16x16x32_bf16 v[232:235], a[180:183], v[140:143], v[232:235]\n"
      "ds_read_b128 v[164:167], v123\n"
      "v_mul_f32 v157, v240, v157\n"
      "v_mfma_f32_16x16x32_bf16 v[172:175], a[148:151], v[136:139], v[172:175]\n"
      "s_waitcnt lgkmcnt(8)\n"
      "v_cvt_pk_bf16_f32 v38, v156, v157\n"
      "v_mul_f32 v158, v240, v158\n"
      "v_mfma_f32_16x16x32_bf16 v[172:175], a[184:187], v[140:143], v[172:175]\n"
      "ds_read_b128 v[148:151], v123 offset:3072\n"
      "v_mul_f32_e64 v159, v240, v159\n"
      "v_mfma_f32_16x16x32_bf16 v[196:199], a[152:155], v[136:139], v[196:199]\n"
      "v_cvt_pk_bf16_f32 v39, v158, v159\n"
      "v_or3_b32 v168, v168, v169, v170\n"
      "v_mfma_f32_16x16x32_bf16 v[196:199], a[188:191], v[140:143], v[196:199]\n"
      "ds_read_b128 v[156:159], v123 offset:6144\n"
      "v_fma_f32 v244, v232, v232, s38\n"
      "v_mfma_f32_16x16x32_bf16 v[208:211], a[156:159], v[136:139], v[208:211]\n"
      "v_fmac_f32 v244, v233, v233\n"
      "v_fmac_f32 v244, v234, v234\n"
      "v_mfma_f32_16x16x32_bf16 v[208:211], a[192:195], v[140:143], v[208:211]\n"
      "s_mov_b64 exec, 0xfff\n"
      "v_or3_b32 v168, v168, v144, v145\n"
      "v_or3_b32 v168, v168, v146, v147\n"
      "s_mov_b64 exec, -1\n"
      "v_mfma_f32_16x16x32_bf16 v[220:223], a[160:163], v[136:139], v[220:223]\n"
      "ds_read_b128 v[240:243], v123 offset:9216\n"
      "v_bitop3_b32 v168, v168, v171, s40 bitop3:0xa8\n"
      "v_mfma_f32_16x16x32_bf16 v[220:223], a[196:199], v[140:143], v[220:223]\n"
      "ds_write_b32 v129, v168 offset:2048\n"
      "v_mfma_f32_16x16x32_bf16 v[200:203], a[164:167], v[136:139], v[200:203]\n"
      "s_or_b64 s[34:35], s[48:49], s[50:51]\n"
      "v_fma_f32 v245, v208, v208, s38\n"
      "v_fmac_f32 v245, v209, v209\n"
      "v_mfma_f32_16x16x32_bf16 v[200:203], a[200:203], v[140:143], v[200:203]\n"
      "v_fmac_f32 v245, v210, v210\n"
      "v_fmac_f32_e64 v244, v235, v235\n"
      "v_mfma_f32_16x16x32_bf16 v[228:231], v[136:139], a[168:171], v[228:231]\n"
      "s_waitcnt lgkmcnt(3)\n"
      "v_fmac_f32 v245, v211, v211\n"
      "v_mfma_f32_16x16x32_bf16 v[228:231], v[140:143], a[204:207], v[228:231]\n"
      "v_fmac_f32 v244, v172, v172\n"
      "v_fmac_f32 v245, v220, v220\n"
      "v_mfma_f32_16x16x32_bf16 v[212:215], v[136:139], a[172:175], v[212:215]\n"
      "v_fmac_f32 v244, v173, v173\n"
      "v_fmac_f32 v245, v221, v221\n"
      "v_mfma_f32_16x16x32_bf16 v[212:215], v[140:143], a[208:211], v[212:215]\n"
      "v_fmac_f32 v244, v174, v174\n"
      "v_fmac_f32 v245, v222, v222\n"
      "v_mfma_f32_16x16x32_bf16 v[216:219], v[136:139], a[176:179], v[216:219]\n"
      "v_fmac_f32 v244, v175, v175\n"
      "v_fmac_f32 v245, v223, v223\n"
      "v_mfma_f32_16x16x32_bf16 v[216:219], v[140:143], a[212:215], v[216:219]\n"
      "v_fmac_f32 v244, v196, v196\n"
      "v_fmac_f32 v245, v200, v200\n"
      "v_mfma_f32_16x16x32_bf16 v[136:139], a[216:219], v[164:167], 0\n"
      "s_waitcnt lgkmcnt(0)\n"
      "v_fmac_f32 v244, v197, v197\n"
      "v_fmac_f32 v245, v201, v201\n"
      "v_mfma_f32_16x16x32_bf16 v[136:139], a[228:231], v[148:151], v[136:139]\n"
      "v_fmac_f32 v244, v198, v198\n"
      "v_fmac_f32 v245, v202, v202\n"
      "v_mfma_f32_16x16x32_bf16 v[140:143], a[220:223], v[164:167], 0\n"
      "v_fmac_f32 v244, v199, v199\n"
      "v_fmac_f32 v245, v203, v203\n"
      "v_mfma_f32_16x16x32_bf16 v[140:143], a[232:235], v[148:151], v[140:143]\n"
      "s_nop 0\n"
      "v_permlane16_swap_b32 v244, v245\n"
      "v_mfma_f32_16x16x32_bf16 v[144:147], a[224:227], v[164:167], 0\n"
      "v_add_f32 v244, v244, v245\n"
      "v_mov_b32_e64 v245, v244\n"
      "v_mfma_f32_16x16x32_bf16 v[144:147], a[236:239], v[148:151], v[144:147]\n"
      "ds_read_b128 v[148:151], v123 offset:12288\n"
      "v_add_u32 v132, 2304, v132\n"
      "v_mfma_f32_16x16x32_bf16 v[136:139], a[240:243], v[156:159], v[136:139]\n"
      "v_permlane32_swap_b32_e64 v244, v245\n"
      "v_mfma_f32_16x16x32_bf16 v[136:139], a[252:255], v[240:243], v[136:139]\n"
      "ds_read_b128 v[168:171], v123 offset:15360\n"
      "v_add_f32 v244, v244, v245\n"
      "v_mfma_f32_16x16x32_bf16 v[140:143], a[244:247], v[156:159], v[140:143]\n"
      "v_rsq_f32 v244, v244\n"
      "v_mfma_f32_16x16x32_bf16 v[140:143], v[0:3], v[240:243], v[140:143]\n"
      "v_mov_b32 v245, v244\n"
      "v_add_u32 v133, 2304, v133\n"
      "v_mfma_f32_16x16x32_bf16 v[144:147], a[248:251], v[156:159], v[144:147]\n"
      "v_permlane16_swap_b32 v244, v245\n"
      "v_mfma_f32_16x16x32_bf16 v[144:147], v[4:7], v[240:243], v[144:147]\n"
      "v_mul_f32 v208, v245, v208\n"
      "v_mul_f32 v209, v245, v209\n"
      "v_mfma_f32_16x16x32_bf16 v[164:167], v[224:227], v[204:207], v[78:81]\n"
      "v_cvt_pk_bf16_f32 v208, v208, v209\n"
      "v_mul_f32 v210, v245, v210\n"
      "v_mfma_f32_16x16x32_bf16 v[164:167], v[34:37], v[32:35], v[164:167]\n"
      "v_mul_f32 v211, v245, v211\n"
      "v_cvt_pk_bf16_f32 v209, v210, v211\n"
      "v_mul_f32 v220, v245, v220\n"
      "v_mul_f32 v221, v245, v221\n"
      "v_cvt_pk_bf16_f32 v210, v220, v221\n"
      "v_mfma_f32_16x16x32_bf16 v[156:159], v[160:163], v[204:207], v[82:85]\n"
      "v_mul_f32 v222, v245, v222\n"
      "v_mul_f32 v223, v245, v223\n"
      "v_mfma_f32_16x16x32_bf16 v[156:159], v[40:43], v[32:35], v[156:159]\n"
      "v_cvt_pk_bf16_f32 v211, v222, v223\n"
      "v_mul_f32 v200, v245, v200\n"
      "s_waitcnt lgkmcnt(0)\n"
      "v_mfma_f32_16x16x32_bf16 v[136:139], v[8:11], v[148:151], v[136:139]\n"
      "v_mul_f32_e64 v201, v245, v201\n"
      "v_cvt_pk_bf16_f32 v46, v200, v201\n"
      "v_mfma_f32_16x16x32_bf16 v[136:139], v[20:23], v[168:171], v[136:139]\n"
      "v_mul_f32 v202, v245, v202\n"
      "v_mul_f32 v203, v245, v203\n"
      "v_mfma_f32_16x16x32_bf16 v[140:143], v[12:15], v[148:151], v[140:143]\n"
      "v_cvt_pk_bf16_f32 v47, v202, v203\n"
      "v_add_u32 v134, 2304, v134\n"
      "v_mfma_f32_16x16x32_bf16 v[140:143], v[24:27], v[168:171], v[140:143]\n"
      "v_mul_f32 v244, s36, v244\n"
      "v_mul_f32 v232, v244, v232\n"
      "v_mfma_f32_16x16x32_bf16 v[144:147], v[16:19], v[148:151], v[144:147]\n"
      "v_mul_f32 v233, v244, v233\n"
      "v_cvt_pk_bf16_f32 v232, v232, v233\n"
      "v_mfma_f32_16x16x32_bf16 v[144:147], v[28:31], v[168:171], v[144:147]\n"
      "v_mul_f32 v234, v244, v234\n"
      "v_mul_f32 v235, v244, v235\n"
      "v_mfma_f32_16x16x32_bf16 v[200:203], v[192:195], v[204:207], v[70:73]\n"
      "v_cvt_pk_bf16_f32 v233, v234, v235\n"
      "v_mul_f32 v172, v244, v172\n"
      "v_mfma_f32_16x16x32_bf16 v[200:203], v[176:179], v[224:227], v[200:203]\n"
      "v_mul_f32 v173, v244, v173\n"
      "v_cvt_pk_bf16_f32 v234, v172, v173\n"
      "v_mfma_f32_16x16x32_bf16 v[200:203], v[236:239], v[32:35], v[200:203]\n"
      "v_mul_f32 v174, v244, v174\n"
      "v_mul_f32_e64 v175, v244, v175\n"
      "v_mfma_f32_16x16x32_bf16 v[168:171], v[208:211], v[204:207], v[86:89]\n"
      "v_cvt_pk_bf16_f32 v235, v174, v175\n"
      "v_mul_f32 v196, v244, v196\n"
      "v_mfma_f32_16x16x32_bf16 v[168:171], v[46:49], v[32:35], v[168:171]\n"
      "v_mul_f32 v197, v244, v197\n"
      "v_cvt_pk_bf16_f32 v44, v196, v197\n"
      "v_mul_f32 v198, v244, v198\n"
      "v_mul_f32 v199, v244, v199\n"
      "v_cvt_pk_bf16_f32 v45, v198, v199\n"
      "v_cvt_pk_bf16_f32 v50, v228, v229\n"
      "v_cvt_pk_bf16_f32 v51, v230, v231\n"
      "v_cvt_pk_bf16_f32 v54, v212, v213\n"
      "v_cvt_pk_bf16_f32 v55, v214, v215\n"
      "v_cvt_pk_bf16_f32 v58, v216, v217\n"
      "v_cvt_pk_bf16_f32 v59, v218, v219\n"
      "v_fma_f32 v206, v136, v136, s39\n"
      "v_fmac_f32 v206, v137, v137\n"
      "v_fmac_f32 v206, v138, v138\n"
      "v_fmac_f32 v206, v139, v139\n"
      "v_fmac_f32 v206, v140, v140\n"
      "v_fmac_f32 v206, v141, v141\n"
      "v_fmac_f32 v206, v142, v142\n"
      "v_fmac_f32 v206, v143, v143\n"
      "v_fmac_f32 v206, v144, v144\n"
      "v_fmac_f32 v206, v145, v145\n"
      "v_fmac_f32 v206, v146, v146\n"
      "v_fmac_f32 v206, v147, v147\n"
      "s_cbranch_scc1 L_gen_20_%=\n"
      "L_fast_23_%=:\n"
      "v_mfma_f32_16x16x32_bf16 v[228:231], v[224:227], v[152:155], v[90:93]\n"
      "ds_read_b128 v[212:215], v123 offset:256\n"
      "v_mfma_f32_16x16x32_bf16 v[228:231], v[34:37], v[38:41], v[228:231]\n"
      "ds_read_b128 v[216:219], v123 offset:3328\n"
      "v_exp_f32_e64 v164, v164\n"
      "v_mfma_f32_16x16x32_bf16 v[172:175], v[160:163], v[152:155], v[94:97]\n"
      "ds_read_b128 v[202:205], v119 offset:12288\n"
      "v_mfma_f32_16x16x32_bf16 v[172:175], v[40:43], v[38:41], v[172:175]\n"
      "ds_read_b128 v[240:243], v123 offset:6400\n"
      "v_exp_f32 v165, v165\n"
      "v_mfma_f32_16x16x32_bf16 v[220:223], v[192:195], v[152:155], v[72:75]\n"
      "v_exp_f32 v166, v166\n"
      "v_mfma_f32_16x16x32_bf16 v[220:223], v[176:179], v[160:163], v[220:223]\n"
      "v_cvt_pk_bf16_f32 v164, v164, v165\n"
      "v_exp_f32 v167, v167\n"
      "v_mfma_f32_16x16x32_bf16 v[220:223], v[236:239], v[38:41], v[220:223]\n"
      "v_exp_f32 v156, v156\n"
      "v_mfma_f32_16x16x32_bf16 v[196:199], v[208:211], v[152:155], v[98:101]\n"
      "v_cvt_pk_bf16_f32 v165, v166, v167\n"
      "v_mfma_f32_16x16x32_bf16 v[196:199], v[46:49], v[38:41], v[196:199]\n"
      "v_exp_f32 v157, v157\n"
      "v_exp_f32 v158, v158\n"
      "v_cvt_pk_bf16_f32 v166, v156, v157\n"
      "v_exp_f32 v159, v159\n"
      "s_waitcnt lgkmcnt(1)\n"
      "v_mfma_f32_16x16x32_bf16 v[148:151], a[216:219], v[212:215], 0\n"
      "ds_read_b128 v[244:247], v123 offset:9472\n"
      "v_cvt_pk_bf16_f32 v167, v158, v159\n"
      "v_mfma_f32_16x16x32_bf16 v[148:151], a[228:231], v[216:219], v[148:151]\n"
      "v_exp_f32_e64 v168, v168\n"
      "v_mfma_f32_16x16x32_bf16 v[152:155], a[220:223], v[212:215], 0\n"
      "global_store_dwordx4 v116, v[202:205], s[54:55] offset:256 sc1\n"
      "v_mfma_f32_16x16x32_bf16 v[152:155], a[232:235], v[216:219], v[152:155]\n"
      "v_exp_f32 v169, v169\n"
      "v_mfma_f32_16x16x32_bf16 v[156:159], a[224:227], v[212:215], 0\n"
      "v_exp_f32 v170, v170\n"
      "v_mfma_f32_16x16x32_bf16 v[156:159], a[236:239], v[216:219], v[156:159]\n"
      "ds_read_b128 v[216:219], v123 offset:12544\n"
      "v_exp_f32 v171, v171\n"
      "v_exp_f32 v200, v200\n"
      "v_cvt_pk_bf16_f32 v62, v168, v169\n"
      "v_cvt_pk_bf16_f32 v63, v170, v171\n"
      "v_cvt_pk_bf16_f32 v64, v200, 0\n"
      "v_exp_f32 v228, v228\n"
      "v_exp_f32 v229, v229\n"
      "v_mfma_f32_16x16x32_bf16 v[212:215], v[66:69], v[164:167], 0\n"
      "ds_read_b128 v[168:171], v123 offset:15616\n"
      "v_cvt_pk_bf16_f32 v228, v228, v229\n"
      "v_mfma_f32_16x16x32_bf16 v[212:215], v[66:69], v[62:65], v[212:215]\n"
      "s_waitcnt lgkmcnt(2)\n"
      "v_exp_f32 v230, v230\n"
      "v_mfma_f32_16x16x32_bf16 v[148:151], a[240:243], v[240:243], v[148:151]\n"
      "v_exp_f32 v231, v231\n"
      "v_mfma_f32_16x16x32_bf16 v[148:151], a[252:255], v[244:247], v[148:151]\n"
      "v_exp_f32 v172, v172\n"
      "v_mfma_f32_16x16x32_bf16 v[152:155], a[244:247], v[240:243], v[152:155]\n"
      "v_cvt_pk_bf16_f32 v229, v230, v231\n"
      "v_mfma_f32_16x16x32_bf16 v[152:155], v[0:3], v[244:247], v[152:155]\n"
      "v_exp_f32_e64 v173, v173\n"
      "v_mfma_f32_16x16x32_bf16 v[156:159], a[248:251], v[240:243], v[156:159]\n"
      "ds_read_b128 v[240:243], v123 offset:512\n"
      "v_cvt_pk_bf16_f32 v230, v172, v173\n"
      "v_mfma_f32_16x16x32_bf16 v[156:159], v[4:7], v[244:247], v[156:159]\n"
      "ds_read_b128 v[244:247], v123 offset:3584\n"
      "v_exp_f32_e64 v174, v174\n"
      "v_mfma_f32_16x16x32_bf16 v[202:205], v[180:183], v[164:167], 0\n"
      "v_exp_f32 v175, v175\n"
      "v_mfma_f32_16x16x32_bf16 v[202:205], v[50:53], v[62:65], v[202:205]\n"
      "s_waitcnt lgkmcnt(2)\n"
      "v_cvt_pk_bf16_f32 v231, v174, v175\n"
      "v_mfma_f32_16x16x32_bf16 v[148:151], v[8:11], v[216:219], v[148:151]\n"
      "v_exp_f32 v196, v196\n"
      "v_mfma_f32_16x16x32_bf16 v[148:151], v[20:23], v[168:171], v[148:151]\n"
      "v_exp_f32 v197, v197\n"
      "v_mfma_f32_16x16x32_bf16 v[152:155], v[12:15], v[216:219], v[152:155]\n"
      "v_exp_f32 v198, v198\n"
      "v_mfma_f32_16x16x32_bf16 v[152:155], v[24:27], v[168:171], v[152:155]\n"
      "v_exp_f32 v199, v199\n"
      "v_mfma_f32_16x16x32_bf16 v[156:159], v[16:19], v[216:219], v[156:159]\n"
      "v_exp_f32_e64 v220, v220\n"
      "v_mfma_f32_16x16x32_bf16 v[156:159], v[28:31], v[168:171], v[156:159]\n"
      "v_fma_f32 v200, v148, v148, s39\n"
      "v_fmac_f32 v200, v149, v149\n"
      "v_mfma_f32_16x16x32_bf16 v[168:171], v[184:187], v[164:167], 0\n"
      "v_fmac_f32 v200, v150, v150\n"
      "v_fmac_f32 v200, v151, v151\n"
      "v_mfma_f32_16x16x32_bf16 v[168:171], v[54:57], v[62:65], v[168:171]\n"
      "v_fmac_f32 v200, v152, v152\n"
      "v_fmac_f32 v200, v153, v153\n"
      "v_mfma_f32_16x16x32_bf16 v[248:251], v[188:191], v[164:167], 0\n"
      "v_fmac_f32 v200, v154, v154\n"
      "v_fmac_f32 v200, v155, v155\n"
      "v_mfma_f32_16x16x32_bf16 v[248:251], v[58:61], v[62:65], v[248:251]\n"
      "v_fmac_f32 v200, v156, v156\n"
      "v_fmac_f32_e64 v200, v157, v157\n"
      "v_mfma_f32_16x16x32_bf16 v[216:219], v[224:227], v[232:235], v[102:105]\n"
      "ds_read_b128 v[164:167], v120\n"
      "v_fmac_f32 v200, v158, v158\n"
      "v_mfma_f32_16x16x32_bf16 v[216:219], v[34:37], v[44:47], v[216:219]\n"
      "s_waitcnt lgkmcnt(1)\n"
      "v_fmac_f32_e64 v200, v159, v159\n"
      "v_cvt_pk_bf16_f32 v62, v196, v197\n"
      "v_mfma_f32_16x16x32_bf16 v[224:227], v[160:163], v[232:235], v[106:109]\n"
      "v_cvt_pk_bf16_f32 v63, v198, v199\n"
      "v_cvt_pk_bf16_f32 v64, v220, 0\n"
      "v_mfma_f32_16x16x32_bf16 v[224:227], v[40:43], v[44:47], v[224:227]\n"
      "v_rcp_f32 v212, v212\n"
      "v_mfma_f32_16x16x32_bf16 v[172:175], v[192:195], v[232:235], v[74:77]\n"
      "v_mul_f32 v202, v212, v202\n"
      "v_mul_f32_e64 v203, v212, v203\n"
      "v_mfma_f32_16x16x32_bf16 v[172:175], v[176:179], v[208:211], v[172:175]\n"
      "v_cvt_pk_bf16_f32 v202, v202, v203\n"
      "v_mul_f32 v204, v212, v204\n"
      "v_mfma_f32_16x16x32_bf16 v[172:175], v[236:239], v[44:47], v[172:175]\n"
      "v_mul_f32 v205, v212, v205\n"
      "v_cvt_pk_bf16_f32 v203, v204, v205\n"
      "v_mfma_f32_16x16x32_bf16 v[196:199], v[208:211], v[232:235], v[110:113]\n"
      "ds_read_b128 v[232:235], v123 offset:6656\n"
      "v_mul_f32_e64 v168, v212, v168\n"
      "v_mfma_f32_16x16x32_bf16 v[196:199], v[46:49], v[44:47], v[196:199]\n"
      "ds_read_b128 v[208:211], v123 offset:9728\n"
      "v_mul_f32 v169, v212, v169\n"
      "v_cvt_pk_bf16_f32 v204, v168, v169\n"
      "v_mul_f32 v170, v212, v170\n"
      "v_mul_f32_e64 v171, v212, v171\n"
      "v_cvt_pk_bf16_f32 v205, v170, v171\n"
      "v_mfma_f32_16x16x32_bf16 v[252:255], v[176:179], v[192:195], v[76:79]\n"
      "ds_write_b128 v124, v[202:205] offset:18432\n"
      "v_mfma_f32_16x16x32_bf16 v[202:205], v[66:69], v[228:231], 0\n"
      "ds_read_b128 v[168:171], v121\n"
      "v_mul_f32 v248, v212, v248\n"
      "v_mfma_f32_16x16x32_bf16 v[202:205], v[66:69], v[62:65], v[202:205]\n"
      "s_waitcnt lgkmcnt(4)\n"
      "v_mul_f32_e64 v249, v212, v249\n"
      "v_cvt_pk_bf16_f32 v248, v248, v249\n"
      "v_mfma_f32_16x16x16_bf16 v[252:255], v[238:239], v[236:237], v[252:255]\n"
      "global_store_dwordx4 v117, v[164:167], s[54:55] sc1\n"
      "v_mfma_f32_16x16x32_bf16 v[236:239], v[180:183], v[228:231], 0\n"
      "ds_read_b128 v[192:195], v123 offset:12800\n"
      "v_mul_f32_e64 v250, v212, v250\n"
      "v_mfma_f32_16x16x32_bf16 v[236:239], v[50:53], v[62:65], v[236:239]\n"
      "ds_read_b128 v[174:177], v123 offset:15872\n"
      "v_mul_f32_e64 v251, v212, v251\n"
      "v_mfma_f32_16x16x32_bf16 v[212:215], v[184:187], v[228:231], 0\n"
      "v_cvt_pk_bf16_f32 v249, v250, v251\n"
      "v_cndmask_b32 v201, v201, v221, s[42:43]\n"
      "v_mfma_f32_16x16x32_bf16 v[212:215], v[54:57], v[62:65], v[212:215]\n"
      "ds_write_b64 v125, v[248:249] offset:18432\n"
      "v_mfma_f32_16x16x32_bf16 v[248:251], v[188:191], v[228:231], 0\n"
      "s_waitcnt lgkmcnt(4)\n"
      "v_exp_f32 v216, v216\n"
      "v_mfma_f32_16x16x32_bf16 v[248:251], v[58:61], v[62:65], v[248:251]\n"
      "v_exp_f32 v217, v217\n"
      "v_mfma_f32_16x16x32_bf16 v[160:163], a[216:219], v[240:243], 0\n"
      "s_waitcnt lgkmcnt(3)\n"
      "v_cvt_pk_bf16_f32 v216, v216, v217\n"
      "v_mfma_f32_16x16x32_bf16 v[160:163], a[228:231], v[244:247], v[160:163]\n"
      "s_mov_b32 exec_hi, 0\n"
      "global_store_dwordx4 v118, v[168:171], s[54:55] sc1\n"
      "s_mov_b32 exec_hi, -1\n"
      "v_mfma_f32_16x16x32_bf16 v[164:167], a[220:223], v[240:243], 0\n"
      "s_add_u32 s28, s27, 2\n"
      "v_exp_f32 v218, v218\n"
      "v_mfma_f32_16x16x32_bf16 v[164:167], a[232:235], v[244:247], v[164:167]\n"
      "s_min_u32 m0, s28, s3\n"
      "v_exp_f32 v219, v219\n"
      "v_mfma_f32_16x16x32_bf16 v[168:171], a[224:227], v[240:243], 0\n"
      "s_waitcnt lgkmcnt(1)\n"
      "v_exp_f32_e64 v224, v224\n"
      "v_mfma_f32_16x16x32_bf16 v[168:171], a[236:239], v[244:247], v[168:171]\n"
      "s_movrels_b32 s9, s80\n"
      "v_cvt_pk_bf16_f32 v217, v218, v219\n"
      "v_cndmask_b32 v201, v201, v173, s[44:45]\n"
      "v_mfma_f32_16x16x32_bf16 v[160:163], a[240:243], v[232:235], v[160:163]\n"
      "s_lshr_b32 s8, s9, 8\n"
      "v_exp_f32 v225, v225\n"
      "v_mfma_f32_16x16x32_bf16 v[160:163], a[252:255], v[208:211], v[160:163]\n"
      "s_and_b32 s9, s9, 0xff\n"
      "v_exp_f32_e64 v226, v226\n"
      "v_mfma_f32_16x16x32_bf16 v[164:167], a[244:247], v[232:235], v[164:167]\n"
      "s_mul_i32 s10, s8, 73\n"
      "v_cvt_pk_bf16_f32 v218, v224, v225\n"
      "v_cndmask_b32 v201, v201, v253, s[46:47]\n"
      "v_mfma_f32_16x16x32_bf16 v[164:167], v[0:3], v[208:211], v[164:167]\n"
      "s_add_u32 s10, s10, s9\n"
      "v_exp_f32 v227, v227\n"
      "v_mfma_f32_16x16x32_bf16 v[168:171], a[248:251], v[232:235], v[168:171]\n"
      "s_mul_i32 s28, s10, 4802\n"
      "v_exp_f32_e64 v196, v196\n"
      "v_mfma_f32_16x16x32_bf16 v[168:171], v[4:7], v[208:211], v[168:171]\n"
      "s_add_u32 s56, s18, s28\n"
      "v_cvt_pk_bf16_f32 v219, v226, v227\n"
      "v_exp_f32 v197, v197\n"
      "v_mfma_f32_16x16x32_bf16 v[160:163], v[8:11], v[192:195], v[160:163]\n"
      "s_addc_u32 s57, s19, 0\n"
      "v_exp_f32 v198, v198\n"
      "v_mfma_f32_16x16x32_bf16 v[160:163], v[20:23], v[174:177], v[160:163]\n"
      "s_and_b32 s56, s56, -16\n"
      "v_exp_f32 v199, v199\n"
      "v_mfma_f32_16x16x32_bf16 v[164:167], v[12:15], v[192:195], v[164:167]\n"
      "s_sub_u32 s58, s56, 110592\n"
      "v_exp_f32 v172, v172\n"
      "v_mfma_f32_16x16x32_bf16 v[164:167], v[24:27], v[174:177], v[164:167]\n"
      "s_subb_u32 s59, s57, 0\n"
      "v_cvt_pk_bf16_f32 v62, v196, v197\n"
      "v_cvt_pk_bf16_f32 v63, v198, v199\n"
      "v_mfma_f32_16x16x32_bf16 v[168:171], v[16:19], v[192:195], v[168:171]\n"
      "s_sub_u32 s56, s56, 73728\n"
      "v_cvt_pk_bf16_f32 v64, v172, 0\n"
      "v_fma_f32 v232, v160, v160, s39\n"
      "v_mfma_f32_16x16x32_bf16 v[168:171], v[28:31], v[174:177], v[168:171]\n"
      "s_subb_u32 s57, s57, 0\n"
      "v_exp_f32 v201, v201\n"
      "v_mfma_f32_16x16x32_bf16 v[192:195], v[66:69], v[216:219], 0\n"
      "s_mul_i32 s28, s8, 1373568\n"
      "v_cvt_pk_bf16_f32 v201, v201, v201\n"
      "v_fmac_f32_e64 v232, v161, v161\n"
      "v_mfma_f32_16x16x32_bf16 v[192:195], v[66:69], v[62:65], v[192:195]\n"
      "ds_write_b16 v130, v201\n"
      "v_mfma_f32_16x16x32_bf16 v[196:199], v[180:183], v[216:219], 0\n"
      "ds_read_b128 v[172:175], v131\n"
      "v_fmac_f32_e64 v232, v162, v162\n"
      "v_mfma_f32_16x16x32_bf16 v[196:199], v[50:53], v[62:65], v[196:199]\n"
      "ds_read_b128 v[176:179], v131 offset:64\n"
      "v_fmac_f32_e64 v232, v163, v163\n"
      "v_mfma_f32_16x16x32_bf16 v[224:227], v[184:187], v[216:219], 0\n"
      "s_mul_i32 s29, s9, 2688\n"
      "v_rcp_f32 v202, v202\n"
      "v_mfma_f32_16x16x32_bf16 v[224:227], v[54:57], v[62:65], v[224:227]\n"
      "s_add_u32 s28, s28, s29\n"
      "v_mul_f32 v236, v202, v236\n"
      "v_mul_f32 v237, v202, v237\n"
      "v_mfma_f32_16x16x32_bf16 v[208:211], v[188:191], v[216:219], 0\n"
      "s_cmp_eq_u32 s8, 72\n"
      "v_cvt_pk_bf16_f32 v236, v236, v237\n"
      "v_mul_f32_e64 v238, v202, v238\n"
      "v_mfma_f32_16x16x32_bf16 v[208:211], v[58:61], v[62:65], v[208:211]\n"
      "s_cselect_b32 s8, 0, 100270464\n"
      "v_mul_f32 v239, v202, v239\n"
      "v_cvt_pk_bf16_f32 v237, v238, v239\n"
      "v_mul_f32 v212, v202, v212\n"
      "v_mul_f32 v213, v202, v213\n"
      "v_cvt_pk_bf16_f32 v238, v212, v213\n"
      "v_mul_f32 v214, v202, v214\n"
      "v_mul_f32_e64 v215, v202, v215\n"
      "v_cvt_pk_bf16_f32 v239, v214, v215\n"
      "ds_write_b128 v124, v[236:239] offset:18688\n"
      "v_mul_f32 v248, v202, v248\n"
      "v_mul_f32 v249, v202, v249\n"
      "v_cvt_pk_bf16_f32 v248, v248, v249\n"
      "v_mul_f32 v250, v202, v250\n"
      "v_mul_f32 v251, v202, v251\n"
      "v_cvt_pk_bf16_f32 v249, v250, v251\n"
      "ds_write_b64 v125, v[248:249] offset:18688\n"
      "v_fmac_f32 v232, v164, v164\n"
      "v_fmac_f32 v232, v165, v165\n"
      "v_fmac_f32 v232, v166, v166\n"
      "v_fmac_f32 v232, v167, v167\n"
      "v_fmac_f32 v232, v168, v168\n"
      "v_fmac_f32 v232, v169, v169\n"
      "v_fmac_f32 v232, v170, v170\n"
      "v_fmac_f32 v232, v171, v171\n"
      "v_rcp_f32 v192, v192\n"
      "v_permlane16_swap_b32 v206, v200\n"
      "v_mul_f32 v196, v192, v196\n"
      "v_mul_f32 v197, v192, v197\n"
      "v_cvt_pk_bf16_f32 v196, v196, v197\n"
      "v_mul_f32 v198, v192, v198\n"
      "v_mov_b32 v248, v232\n"
      "v_mul_f32 v199, v192, v199\n"
      "v_add_f32 v206, v206, v200\n"
      "v_cvt_pk_bf16_f32 v197, v198, v199\n"
      "v_mul_f32 v224, v192, v224\n"
      "v_mul_f32 v225, v192, v225\n"
      "v_permlane16_swap_b32 v232, v248\n"
      "v_cvt_pk_bf16_f32 v198, v224, v225\n"
      "v_mul_f32 v226, v192, v226\n"
      "v_mul_f32 v227, v192, v227\n"
      "v_add_f32 v232, v232, v248\n"
      "v_cvt_pk_bf16_f32 v199, v226, v227\n"
      "ds_write_b128 v124, v[196:199] offset:18944\n"
      "v_permlane32_swap_b32 v206, v232\n"
      "v_mul_f32 v208, v192, v208\n"
      "v_mul_f32_e64 v209, v192, v209\n"
      "v_cvt_pk_bf16_f32 v208, v208, v209\n"
      "v_add_f32_e64 v206, v206, v232\n"
      "ds_write_b32 v129, v206 offset:1024\n"
      "v_mul_f32_e64 v210, v192, v210\n"
      "v_mul_f32 v211, v192, v211\n"
      "v_cvt_pk_bf16_f32 v209, v210, v211\n"
      "ds_write_b64 v125, v[208:209] offset:18944\n"
      "s_cmp_eq_u32 s9, 72\n"
      "s_cselect_b32 s9, 0, 196224\n"
      "s_add_u32 s29, s8, s9\n"
      "s_add_u32 s28, s28, s29\n"
      "s_add_u32 s52, s16, s28\n"
      "s_addc_u32 s53, s17, 0\n"
      "s_cmp_lg_u32 s29, s11\n"
      "s_cbranch_scc1 L_edge_24_%=\n"
      "L_eb_25_%=:\n"
      "L_join_22_%=:\n"
      "s_cmp_ge_u32 s27, s3\n"
      "s_cbranch_scc1 L_tail1_%=\n"
      "s_add_u32 s27, s27, 1\n"
      "s_waitcnt vmcnt(5)\n"
      "s_waitcnt lgkmcnt(0)\n"
      "s_barrier\n"
      "s_branch L_loop_%=\n"
      "L_tail0_%=:\n"
      "s_nop 7\n"
      "s_nop 7\n"
      "v_mfma_f32_16x16x32_bf16 v[192:195], v[184:187], v[66:69], 0\n"
      "v_mfma_f32_16x16x32_bf16 v[192:195], v[188:191], v[66:69], v[192:195]\n"
      "v_mfma_f32_16x16x32_bf16 v[196:199], v[184:187], v[172:175], 0\n"
      "v_mfma_f32_16x16x32_bf16 v[196:199], v[188:191], v[50:53], v[196:199]\n"
      "v_mfma_f32_16x16x32_bf16 v[172:175], v[184:187], v[176:179], 0\n"
      "v_mfma_f32_16x16x32_bf16 v[172:175], v[188:191], v[54:57], v[172:175]\n"
      "v_mfma_f32_16x16x32_bf16 v[176:179], v[184:187], v[180:183], 0\n"
      "v_mfma_f32_16x16x32_bf16 v[176:179], v[188:191], v[58:61], v[176:179]\n"
      "s_nop 1\n"
      "v_mov_b32_dpp v192, v192 quad_perm:[0,0,0,0] row_mask:0xf bank_mask:0xf\n"
      "v_rcp_f32 v192, v192\n"
      "s_nop 1\n"
      "v_cndmask_b32 v196, v196, v172, s[42:43]\n"
      "s_nop 0\n"
      "v_cndmask_b32 v196, v196, v176, s[44:45]\n"
      "v_mul_f32_e64 v196, v192, v196\n"
      "v_cvt_pk_bf16_f32 v196, v196, v196\n"
      "ds_write_b16 v135, v196\n"
      "s_waitcnt lgkmcnt(0)\n"
      "s_barrier\n"
      "v_mbcnt_lo_u32_b32 v172, -1, 0\n"
      "v_mbcnt_hi_u32_b32 v172, -1, v172\n"
      "v_lshrrev_b32_e64 v176, 4, v172\n"
      "v_mul_u32_u24 v176, 336, v176\n"
      "v_and_b32_e64 v177, 15, v172\n"
      "v_lshl_add_u32 v176, v177, 4, v176\n"
      "v_add_u32 v176, 149136, v176\n"
      "s_add_u32 s27, s27, 1\n"
      "ds_read_b128 v[178:181], v128\n"
      "ds_read_b128 v[188:191], v123\n"
      "ds_read_b128 v[182:185], v123 offset:3072\n"
      "s_sub_u32 s28, s27, 3\n"
      "s_cselect_b64 s[54:55], s[12:13], s[22:23]\n"
      "s_max_i32 m0, s28, 0\n"
      "ds_read_b128 v[196:199], v119\n"
      "s_movrels_b32 s9, s80\n"
      "s_lshr_b32 s8, s9, 8\n"
      "s_and_b32 s9, s9, 0xff\n"
      "s_mul_i32 s28, s8, 1373568\n"
      "s_mul_i32 s29, s9, 2688\n"
      "s_add_u32 s28, s28, s29\n"
      "s_cmp_eq_u32 s8, 72\n"
      "s_cselect_b32 s8, 0, 100270464\n"
      "s_cmp_eq_u32 s9, 72\n"
      "s_cselect_b32 s9, 0, 196224\n"
      "s_add_u32 s29, s8, s9\n"
      "s_add_u32 s28, s28, s29\n"
      "s_add_u32 s54, s54, s28\n"
      "s_addc_u32 s55, s55, 0\n"
      "s_cmp_lg_u32 s29, s11\n"
      "ds_read_b128 v[192:195], v119 offset:6144\n"
      "ds_read_b128 v[200:203], v119 offset:12288\n"
      "ds_read_b128 v[204:207], v120\n"
      "ds_read_b128 v[208:211], v121\n"
      "ds_read_b128 v[212:215], v123 offset:6144\n"
      "ds_read_b128 v[216:219], v123 offset:9216\n"
      "s_waitcnt lgkmcnt(7)\n"
      "v_add_f32 v178, v178, v179\n"
      "v_add_f32 v180, v180, v181\n"
      "v_add_f32 v178, v178, v180\n"
      "v_mfma_f32_16x16x32_bf16 v[220:223], a[216:219], v[188:191], 0\n"
      "ds_read_b128 v[224:227], v123 offset:12288\n"
      "v_mfma_f32_16x16x32_bf16 v[220:223], a[228:231], v[182:185], v[220:223]\n"
      "ds_read_b128 v[228:231], v123 offset:15360\n"
      "v_rsq_f32 v178, v178\n"
      "v_mfma_f32_16x16x32_bf16 v[232:235], a[220:223], v[188:191], 0\n"
      "ds_read_b128 v[236:239], v123 offset:256\n"
      "v_mul_f32 v178, s41, v178\n"
      "v_mfma_f32_16x16x32_bf16 v[232:235], a[232:235], v[182:185], v[232:235]\n"
      "ds_read_b128 v[240:243], v123 offset:3328\n"
      "v_mov_b32 v180, v178\n"
      "v_mfma_f32_16x16x32_bf16 v[244:247], a[224:227], v[188:191], 0\n"
      "s_cbranch_scc1 L_edge_31_%=\n"
      "L_eb_32_%=:\n"
      "v_mfma_f32_16x16x32_bf16 v[244:247], a[236:239], v[182:185], v[244:247]\n"
      "ds_read_b128 v[182:185], v123 offset:6400\n"
      "v_permlane32_swap_b32 v178, v180\n"
      "v_mov_b32 v188, v178\n"
      "s_waitcnt lgkmcnt(5)\n"
      "s_nop 0\n"
      "v_permlane16_swap_b32_e64 v178, v188\n"
      "v_mfma_f32_16x16x32_bf16 v[220:223], a[240:243], v[212:215], v[220:223]\n"
      "global_store_dwordx4 v116, v[196:199], s[54:55] sc1\n"
      "v_mfma_f32_16x16x32_bf16 v[220:223], a[252:255], v[216:219], v[220:223]\n"
      "ds_read_b128 v[248:251], v123 offset:9472\n"
      "v_mul_f32 v136, v178, v136\n"
      "v_mfma_f32_16x16x32_bf16 v[232:235], a[244:247], v[212:215], v[232:235]\n"
      "v_mul_f32_e64 v137, v178, v137\n"
      "s_waitcnt lgkmcnt(4)\n"
      "v_mfma_f32_16x16x32_bf16 v[232:235], v[0:3], v[216:219], v[232:235]\n"
      "v_cvt_pk_bf16_f32 v136, v136, v137\n"
      "v_mul_f32_e64 v138, v178, v138\n"
      "v_mfma_f32_16x16x32_bf16 v[244:247], a[248:251], v[212:215], v[244:247]\n"
      "ds_read_b128 v[212:215], v123 offset:12544\n"
      "v_mul_f32 v139, v178, v139\n"
      "v_mfma_f32_16x16x32_bf16 v[244:247], v[4:7], v[216:219], v[244:247]\n"
      "s_waitcnt lgkmcnt(3)\n"
      "v_cvt_pk_bf16_f32 v137, v138, v139\n"
      "v_mul_f32 v140, v178, v140\n"
      "v_mfma_f32_16x16x32_bf16 v[220:223], v[8:11], v[224:227], v[220:223]\n"
      "ds_read_b128 v[216:219], v123 offset:15616\n"
      "v_mul_f32 v141, v178, v141\n"
      "v_mfma_f32_16x16x32_bf16 v[220:223], v[20:23], v[228:231], v[220:223]\n"
      "v_cvt_pk_bf16_f32 v138, v140, v141\n"
      "v_mul_f32 v142, v178, v142\n"
      "v_mfma_f32_16x16x32_bf16 v[232:235], v[12:15], v[224:227], v[232:235]\n"
      "v_mul_f32 v143, v178, v143\n"
      "v_cvt_pk_bf16_f32 v139, v142, v143\n"
      "v_mfma_f32_16x16x32_bf16 v[232:235], v[24:27], v[228:231], v[232:235]\n"
      "ds_write_b128 v126, v[136:139] offset:18432\n"
      "v_mfma_f32_16x16x32_bf16 v[244:247], v[16:19], v[224:227], v[244:247]\n"
      "ds_read_b128 v[136:139], v123 offset:512\n"
      "v_mul_f32 v144, v178, v144\n"
      "v_mfma_f32_16x16x32_bf16 v[244:247], v[28:31], v[228:231], v[244:247]\n"
      "ds_read_b128 v[140:143], v123 offset:3584\n"
      "v_mul_f32 v145, v178, v145\n"
      "v_mfma_f32_16x16x32_bf16 v[228:231], a[216:219], v[236:239], 0\n"
      "s_waitcnt lgkmcnt(4)\n"
      "v_cvt_pk_bf16_f32 v144, v144, v145\n"
      "v_mul_f32 v146, v178, v146\n"
      "v_mfma_f32_16x16x32_bf16 v[228:231], a[228:231], v[240:243], v[228:231]\n"
      "v_mul_f32_e64 v147, v178, v147\n"
      "v_cvt_pk_bf16_f32 v145, v146, v147\n"
      "v_mfma_f32_16x16x32_bf16 v[224:227], a[220:223], v[236:239], 0\n"
      "global_store_dwordx4 v116, v[192:195], s[54:55] offset:128 sc1\n"
      "v_mfma_f32_16x16x32_bf16 v[224:227], a[232:235], v[240:243], v[224:227]\n"
      "ds_write_b64 v127, v[144:145] offset:18432\n"
      "v_mfma_f32_16x16x32_bf16 v[144:147], a[224:227], v[236:239], 0\n"
      "ds_read_b128 v[190:193], v123 offset:6656\n"
      "v_mul_f32_e64 v148, v188, v148\n"
      "v_mfma_f32_16x16x32_bf16 v[144:147], a[236:239], v[240:243], v[144:147]\n"
      "s_waitcnt lgkmcnt(4)\n"
      "v_mul_f32 v149, v188, v149\n"
      "v_cvt_pk_bf16_f32 v148, v148, v149\n"
      "v_mfma_f32_16x16x32_bf16 v[228:231], a[240:243], v[182:185], v[228:231]\n"
      "ds_read_b128 v[240:243], v123 offset:9728\n"
      "v_mul_f32 v150, v188, v150\n"
      "v_mfma_f32_16x16x32_bf16 v[228:231], a[252:255], v[248:251], v[228:231]\n"
      "s_lshl_b32 s29, s24, 8\n"
      "v_mul_f32_e64 v151, v188, v151\n"
      "v_cvt_pk_bf16_f32 v149, v150, v151\n"
      "v_mfma_f32_16x16x32_bf16 v[224:227], a[244:247], v[182:185], v[224:227]\n"
      "s_waitcnt lgkmcnt(3)\n"
      "v_mul_f32 v152, v188, v152\n"
      "v_mfma_f32_16x16x32_bf16 v[224:227], v[0:3], v[248:251], v[224:227]\n"
      "s_add_u32 s29, s29, 157200\n"
      "v_mul_f32 v153, v188, v153\n"
      "v_cvt_pk_bf16_f32 v150, v152, v153\n"
      "v_mfma_f32_16x16x32_bf16 v[144:147], a[248:251], v[182:185], v[144:147]\n"
      "v_mul_f32 v154, v188, v154\n"
      "v_mul_f32 v155, v188, v155\n"
      "v_mfma_f32_16x16x32_bf16 v[144:147], v[4:7], v[248:251], v[144:147]\n"
      "v_cvt_pk_bf16_f32 v151, v154, v155\n"
      "v_mul_f32 v156, v188, v156\n"
      "v_mfma_f32_16x16x32_bf16 v[228:231], v[8:11], v[212:215], v[228:231]\n"
      "ds_read_b128 v[152:155], v123 offset:12800\n"
      "v_mul_f32_e64 v157, v188, v157\n"
      "v_mfma_f32_16x16x32_bf16 v[228:231], v[20:23], v[216:219], v[228:231]\n"
      "ds_write_b128 v126, v[148:151] offset:20480\n"
      "v_mfma_f32_16x16x32_bf16 v[224:227], v[12:15], v[212:215], v[224:227]\n"
      "ds_read_b128 v[148:151], v123 offset:15872\n"
      "v_cvt_pk_bf16_f32 v156, v156, v157\n"
      "v_mfma_f32_16x16x32_bf16 v[224:227], v[24:27], v[216:219], v[224:227]\n"
      "s_waitcnt lgkmcnt(3)\n"
      "v_mul_f32 v158, v188, v158\n"
      "v_mul_f32_e64 v159, v188, v159\n"
      "v_mfma_f32_16x16x32_bf16 v[144:147], v[16:19], v[212:215], v[144:147]\n"
      "v_cvt_pk_bf16_f32 v157, v158, v159\n"
      "v_fma_f32 v158, v220, v220, s39\n"
      "v_mfma_f32_16x16x32_bf16 v[144:147], v[28:31], v[216:219], v[144:147]\n"
      "ds_write_b64 v127, v[156:157] offset:20480\n"
      "v_mfma_f32_16x16x32_bf16 v[182:185], a[216:219], v[136:139], 0\n"
      "ds_read_b128 v[186:189], v176\n"
      "v_fmac_f32 v158, v221, v221\n"
      "v_mfma_f32_16x16x32_bf16 v[182:185], a[228:231], v[140:143], v[182:185]\n"
      "ds_read_b128 v[216:219], v176 offset:1344\n"
      "v_fmac_f32 v158, v222, v222\n"
      "v_mfma_f32_16x16x32_bf16 v[212:215], a[220:223], v[136:139], 0\n"
      "v_fmac_f32 v158, v223, v223\n"
      "v_fmac_f32 v158, v232, v232\n"
      "v_mfma_f32_16x16x32_bf16 v[212:215], a[232:235], v[140:143], v[212:215]\n"
      "global_store_dwordx4 v116, v[200:203], s[54:55] offset:256 sc1\n"
      "v_mfma_f32_16x16x32_bf16 v[248:251], a[224:227], v[136:139], 0\n"
      "v_fmac_f32 v158, v233, v233\n"
      "v_fmac_f32 v158, v234, v234\n"
      "v_mfma_f32_16x16x32_bf16 v[248:251], a[236:239], v[140:143], v[248:251]\n"
      "s_waitcnt lgkmcnt(3)\n"
      "v_fmac_f32 v158, v235, v235\n"
      "v_fmac_f32 v158, v244, v244\n"
      "v_mfma_f32_16x16x32_bf16 v[182:185], a[240:243], v[190:193], v[182:185]\n"
      "v_fmac_f32 v158, v245, v245\n"
      "v_fmac_f32 v158, v246, v246\n"
      "v_mfma_f32_16x16x32_bf16 v[182:185], a[252:255], v[240:243], v[182:185]\n"
      "v_fmac_f32 v158, v247, v247\n"
      "v_mul_f32 v160, v180, v160\n"
      "v_mfma_f32_16x16x32_bf16 v[212:215], a[244:247], v[190:193], v[212:215]\n"
      "ds_read_b128 v[140:143], v176 offset:2688\n"
      "v_mul_f32 v161, v180, v161\n"
      "v_mfma_f32_16x16x32_bf16 v[212:215], v[0:3], v[240:243], v[212:215]\n"
      "ds_read_b128 v[136:139], v176 offset:4032\n"
      "v_cvt_pk_bf16_f32 v160, v160, v161\n"
      "v_mfma_f32_16x16x32_bf16 v[248:251], a[248:251], v[190:193], v[248:251]\n"
      "v_mul_f32 v162, v180, v162\n"
      "s_waitcnt lgkmcnt(2)\n"
      "v_mfma_f32_16x16x32_bf16 v[248:251], v[4:7], v[240:243], v[248:251]\n"
      "global_store_dwordx4 v117, v[204:207], s[54:55] sc1\n"
      "v_mfma_f32_16x16x32_bf16 v[182:185], v[8:11], v[152:155], v[182:185]\n"
      "v_mul_f32_e64 v163, v180, v163\n"
      "v_cvt_pk_bf16_f32 v161, v162, v163\n"
      "v_mfma_f32_16x16x32_bf16 v[182:185], v[20:23], v[148:151], v[182:185]\n"
      "v_mul_f32 v164, v180, v164\n"
      "v_mul_f32 v165, v180, v165\n"
      "v_mfma_f32_16x16x32_bf16 v[212:215], v[12:15], v[152:155], v[212:215]\n"
      "v_cvt_pk_bf16_f32 v162, v164, v165\n"
      "v_mul_f32 v166, v180, v166\n"
      "v_mfma_f32_16x16x32_bf16 v[212:215], v[24:27], v[148:151], v[212:215]\n"
      "ds_read_b128 v[190:193], v176 offset:5376\n"
      "v_mul_f32 v167, v180, v167\n"
      "v_mfma_f32_16x16x32_bf16 v[248:251], v[16:19], v[152:155], v[248:251]\n"
      "ds_read_b128 v[152:155], v176 offset:6720\n"
      "v_cvt_pk_bf16_f32 v163, v166, v167\n"
      "v_mfma_f32_16x16x32_bf16 v[248:251], v[28:31], v[148:151], v[248:251]\n"
      "ds_write_b128 v126, v[160:163] offset:22528\n"
      "v_mfma_f32_16x16x32_bf16 v[160:163], a[216:219], v[186:189], 0\n"
      "s_waitcnt lgkmcnt(3)\n"
      "v_mul_f32 v168, v180, v168\n"
      "v_mul_f32_e64 v169, v180, v169\n"
      "v_mfma_f32_16x16x32_bf16 v[160:163], a[228:231], v[216:219], v[160:163]\n"
      "v_cvt_pk_bf16_f32 v168, v168, v169\n"
      "v_mul_f32_e64 v170, v180, v170\n"
      "v_mfma_f32_16x16x32_bf16 v[148:151], a[220:223], v[186:189], 0\n"
      "ds_read_b128 v[164:167], v176 offset:256\n"
      "v_mul_f32_e64 v171, v180, v171\n"
      "v_mfma_f32_16x16x32_bf16 v[148:151], a[232:235], v[216:219], v[148:151]\n"
      "v_cvt_pk_bf16_f32 v169, v170, v171\n"
      "v_fma_f32 v170, v228, v228, s39\n"
      "v_mfma_f32_16x16x32_bf16 v[178:181], a[224:227], v[186:189], 0\n"
      "ds_read_b128 v[186:189], v176 offset:1600\n"
      "v_fmac_f32_e64 v170, v229, v229\n"
      "v_mfma_f32_16x16x32_bf16 v[178:181], a[236:239], v[216:219], v[178:181]\n"
      "ds_write_b64 v127, v[168:169] offset:22528\n"
      "v_mfma_f32_16x16x32_bf16 v[160:163], a[240:243], v[140:143], v[160:163]\n"
      "v_fmac_f32 v170, v230, v230\n"
      "v_fmac_f32 v170, v231, v231\n"
      "v_mfma_f32_16x16x32_bf16 v[160:163], a[252:255], v[136:139], v[160:163]\n"
      "s_waitcnt lgkmcnt(3)\n"
      "v_fmac_f32 v170, v224, v224\n"
      "v_fmac_f32_e64 v170, v225, v225\n"
      "v_mfma_f32_16x16x32_bf16 v[148:151], a[244:247], v[140:143], v[148:151]\n"
      "ds_read_b128 v[216:219], v176 offset:2944\n"
      "v_fmac_f32 v170, v226, v226\n"
      "v_mfma_f32_16x16x32_bf16 v[148:151], v[0:3], v[136:139], v[148:151]\n"
      "ds_read_b128 v[204:207], v176 offset:4288\n"
      "v_fmac_f32 v170, v227, v227\n"
      "v_mfma_f32_16x16x32_bf16 v[178:181], a[248:251], v[140:143], v[178:181]\n"
      "v_fmac_f32 v170, v144, v144\n"
      "v_fmac_f32 v170, v145, v145\n"
      "v_mfma_f32_16x16x32_bf16 v[178:181], v[4:7], v[136:139], v[178:181]\n"
      "v_fmac_f32 v170, v146, v146\n"
      "v_fmac_f32 v170, v147, v147\n"
      "v_mfma_f32_16x16x32_bf16 v[160:163], v[8:11], v[190:193], v[160:163]\n"
      "ds_read_b128 v[136:139], v176 offset:5632\n"
      "v_lshl_add_u32 v140, v172, 2, s29\n"
      "v_mfma_f32_16x16x32_bf16 v[160:163], v[20:23], v[152:155], v[160:163]\n"
      "s_waitcnt lgkmcnt(3)\n"
      "v_lshrrev_b32 v141, 3, v172\n"
      "v_fma_f32 v172, v182, v182, s39\n"
      "v_mfma_f32_16x16x32_bf16 v[148:151], v[12:15], v[190:193], v[148:151]\n"
      "ds_read_b32 v140, v140\n"
      "v_fmac_f32 v172, v183, v183\n"
      "v_mfma_f32_16x16x32_bf16 v[148:151], v[24:27], v[152:155], v[148:151]\n"
      "ds_read_b128 v[240:243], v176 offset:6976\n"
      "v_fmac_f32 v172, v184, v184\n"
      "v_mfma_f32_16x16x32_bf16 v[178:181], v[16:19], v[190:193], v[178:181]\n"
      "s_lshl_b32 s29, s24, 3\n"
      "v_add_u32 v141, s29, v141\n"
      "v_cmp_gt_u32 s[48:49], s26, v141\n"
      "v_mfma_f32_16x16x32_bf16 v[178:181], v[28:31], v[152:155], v[178:181]\n"
      "v_fmac_f32 v172, v185, v185\n"
      "v_fmac_f32 v172, v212, v212\n"
      "v_mfma_f32_16x16x32_bf16 v[152:155], a[216:219], v[164:167], 0\n"
      "v_fmac_f32 v172, v213, v213\n"
      "v_fmac_f32 v172, v214, v214\n"
      "v_mfma_f32_16x16x32_bf16 v[152:155], a[228:231], v[186:189], v[152:155]\n"
      "s_waitcnt lgkmcnt(3)\n"
      "v_fmac_f32 v172, v215, v215\n"
      "v_fmac_f32 v172, v248, v248\n"
      "v_mfma_f32_16x16x32_bf16 v[174:177], a[220:223], v[164:167], 0\n"
      "v_fmac_f32 v172, v249, v249\n"
      "v_fmac_f32 v172, v250, v250\n"
      "v_mfma_f32_16x16x32_bf16 v[174:177], a[232:235], v[186:189], v[174:177]\n"
      "s_mov_b32 exec_hi, 0\n"
      "global_store_dwordx4 v118, v[208:211], s[54:55] sc1\n"
      "s_mov_b32 exec_hi, -1\n"
      "v_mfma_f32_16x16x32_bf16 v[190:193], a[224:227], v[164:167], 0\n"
      "v_fmac_f32 v172, v251, v251\n"
      "v_mov_b32_e64 v164, v172\n"
      "v_mfma_f32_16x16x32_bf16 v[190:193], a[236:239], v[186:189], v[190:193]\n"
      "v_permlane16_swap_b32 v158, v170\n"
      "v_mfma_f32_16x16x32_bf16 v[152:155], a[240:243], v[216:219], v[152:155]\n"
      "s_waitcnt lgkmcnt(0)\n"
      "v_add_f32_e64 v158, v158, v170\n"
      "v_fma_f32 v170, v160, v160, s39\n"
      "v_mfma_f32_16x16x32_bf16 v[152:155], a[252:255], v[204:207], v[152:155]\n"
      "v_permlane16_swap_b32 v172, v164\n"
      "v_mfma_f32_16x16x32_bf16 v[174:177], a[244:247], v[216:219], v[174:177]\n"
      "v_add_f32 v172, v172, v164\n"
      "v_fmac_f32 v170, v161, v161\n"
      "v_mfma_f32_16x16x32_bf16 v[174:177], v[0:3], v[204:207], v[174:177]\n"
      "v_permlane32_swap_b32 v158, v172\n"
      "v_mfma_f32_16x16x32_bf16 v[190:193], a[248:251], v[216:219], v[190:193]\n"
      "v_add_f32 v158, v158, v172\n"
      "v_fmac_f32 v170, v162, v162\n"
      "v_mfma_f32_16x16x32_bf16 v[190:193], v[4:7], v[204:207], v[190:193]\n"
      "ds_write_b32 v129, v158 offset:1024\n"
      "v_mfma_f32_16x16x32_bf16 v[152:155], v[8:11], v[136:139], v[152:155]\n"
      "v_fmac_f32 v170, v163, v163\n"
      "v_fmac_f32 v170, v148, v148\n"
      "v_mfma_f32_16x16x32_bf16 v[152:155], v[20:23], v[240:243], v[152:155]\n"
      "v_fmac_f32 v170, v149, v149\n"
      "v_fmac_f32 v170, v150, v150\n"
      "v_mfma_f32_16x16x32_bf16 v[174:177], v[12:15], v[136:139], v[174:177]\n"
      "v_fmac_f32 v170, v151, v151\n"
      "v_fmac_f32 v170, v178, v178\n"
      "v_mfma_f32_16x16x32_bf16 v[174:177], v[24:27], v[240:243], v[174:177]\n"
      "v_fmac_f32 v170, v179, v179\n"
      "v_fmac_f32 v170, v180, v180\n"
      "v_mfma_f32_16x16x32_bf16 v[190:193], v[16:19], v[136:139], v[190:193]\n"
      "v_fmac_f32_e64 v170, v181, v181\n"
      "v_fma_f32 v136, v152, v152, s39\n"
      "v_mfma_f32_16x16x32_bf16 v[190:193], v[28:31], v[240:243], v[190:193]\n"
      "v_fmac_f32 v136, v153, v153\n"
      "v_fmac_f32 v136, v154, v154\n"
      "v_fmac_f32 v136, v155, v155\n"
      "v_fmac_f32 v136, v174, v174\n"
      "v_fmac_f32 v136, v175, v175\n"
      "v_fmac_f32 v136, v176, v176\n"
      "v_fmac_f32 v136, v177, v177\n"
      "s_nop 0\n"
      "v_fmac_f32 v136, v190, v190\n"
      "v_fmac_f32 v136, v191, v191\n"
      "v_fmac_f32 v136, v192, v192\n"
      "v_fmac_f32 v136, v193, v193\n"
      "v_mov_b32 v137, v136\n"
      "v_mov_b32 v138, v137\n"
      "v_permlane16_swap_b32 v170, v136\n"
      "v_add_f32 v170, v170, v136\n"
      "v_permlane16_swap_b32 v137, v138\n"
      "v_add_f32_e64 v137, v137, v138\n"
      "s_nop 1\n"
      "v_permlane32_swap_b32_e64 v170, v137\n"
      "v_add_f32_e64 v170, v170, v137\n"
      "ds_write_b32 v129, v170 offset:2048\n"
      "s_waitcnt lgkmcnt(0)\n"
      "s_barrier\n"
      "ds_read_b128 v[136:139], v128 offset:1024\n"
      "ds_read_b128 v[164:167], v128 offset:2048\n"
      "ds_read_b128 v[186:189], v119 offset:18432\n"
      "ds_read_b128 v[168:171], v119 offset:24576\n"
      "ds_read_b128 v[156:159], v119 offset:30720\n"
      "ds_read_b128 v[240:243], v120 offset:18432\n"
      "ds_read_b128 v[204:207], v121 offset:18432\n"
      "v_subrev_u32 v172, 73728, v126\n"
      "v_subrev_u32 v141, 73728, v127\n"
      "s_add_u32 s27, s27, 1\n"
      "s_sub_u32 s28, s27, 3\n"
      "s_cselect_b64 s[54:55], s[12:13], s[22:23]\n"
      "s_max_i32 m0, s28, 0\n"
      "s_nop 0\n"
      "s_movrels_b32 s9, s80\n"
      "s_lshr_b32 s8, s9, 8\n"
      "s_and_b32 s9, s9, 0xff\n"
      "s_mul_i32 s28, s8, 1373568\n"
      "s_mul_i32 s29, s9, 2688\n"
      "s_add_u32 s28, s28, s29\n"
      "s_cmp_eq_u32 s8, 72\n"
      "s_cselect_b32 s8, 0, 100270464\n"
      "s_cmp_eq_u32 s9, 72\n"
      "s_cselect_b32 s9, 0, 196224\n"
      "s_add_u32 s29, s8, s9\n"
      "s_add_u32 s28, s28, s29\n"
      "s_add_u32 s54, s54, s28\n"
      "s_addc_u32 s55, s55, 0\n"
      "s_cmp_lg_u32 s29, s11\n"
      "s_waitcnt lgkmcnt(4)\n"
      "v_add_f32 v136, v136, v137\n"
      "v_add_f32 v138, v138, v139\n"
      "v_add_f32 v136, v136, v138\n"
      "v_rsq_f32 v136, v136\n"
      "s_nop 0\n"
      "v_mul_f32 v136, s41, v136\n"
      "v_mov_b32 v138, v136\n"
      "v_add_f32 v164, v164, v165\n"
      "s_nop 0\n"
      "v_permlane32_swap_b32 v136, v138\n"
      "v_mov_b32 v165, v136\n"
      "v_add_f32 v166, v166, v167\n"
      "v_add_f32 v164, v164, v166\n"
      "v_permlane16_swap_b32 v136, v165\n"
      "v_rsq_f32 v164, v164\n"
      "v_mul_f32 v220, v136, v220\n"
      "v_mul_f32 v164, s41, v164\n"
      "v_mov_b32 v166, v164\n"
      "v_mul_f32 v221, v136, v221\n"
      "v_cvt_pk_bf16_f32 v220, v220, v221\n"
      "v_permlane32_swap_b32 v164, v166\n"
      "v_mov_b32 v166, v164\n"
      "v_mul_f32 v222, v136, v222\n"
      "v_mul_f32 v223, v136, v223\n"
      "v_permlane16_swap_b32 v164, v166\n"
      "v_cvt_pk_bf16_f32 v221, v222, v223\n"
      "v_mul_f32 v232, v136, v232\n"
      "v_mul_f32 v233, v136, v233\n"
      "v_cvt_pk_bf16_f32 v222, v232, v233\n"
      "v_mul_f32 v234, v136, v234\n"
      "v_mul_f32 v235, v136, v235\n"
      "v_cvt_pk_bf16_f32 v223, v234, v235\n"
      "ds_write_b128 v126, v[220:223]\n"
      "v_mul_f32 v244, v136, v244\n"
      "v_mul_f32 v245, v136, v245\n"
      "v_cvt_pk_bf16_f32 v244, v244, v245\n"
      "v_mul_f32 v246, v136, v246\n"
      "v_mul_f32 v247, v136, v247\n"
      "v_cvt_pk_bf16_f32 v245, v246, v247\n"
      "ds_write_b64 v127, v[244:245]\n"
      "v_mul_f32 v228, v165, v228\n"
      "v_mul_f32 v229, v165, v229\n"
      "v_cvt_pk_bf16_f32 v228, v228, v229\n"
      "v_mul_f32 v230, v165, v230\n"
      "v_mul_f32 v231, v165, v231\n"
      "v_cvt_pk_bf16_f32 v229, v230, v231\n"
      "v_mul_f32 v224, v165, v224\n"
      "v_mul_f32 v225, v165, v225\n"
      "v_cvt_pk_bf16_f32 v230, v224, v225\n"
      "v_mul_f32 v226, v165, v226\n"
      "v_mul_f32 v227, v165, v227\n"
      "v_cvt_pk_bf16_f32 v231, v226, v227\n"
      "ds_write_b128 v126, v[228:231] offset:2048\n"
      "v_mul_f32 v144, v165, v144\n"
      "v_mul_f32 v145, v165, v145\n"
      "v_cvt_pk_bf16_f32 v144, v144, v145\n"
      "v_mul_f32 v146, v165, v146\n"
      "v_mul_f32 v147, v165, v147\n"
      "v_cvt_pk_bf16_f32 v145, v146, v147\n"
      "ds_write_b64 v127, v[144:145] offset:2048\n"
      "v_mul_f32 v182, v138, v182\n"
      "v_mul_f32 v183, v138, v183\n"
      "v_cvt_pk_bf16_f32 v182, v182, v183\n"
      "v_mul_f32 v184, v138, v184\n"
      "v_mul_f32 v185, v138, v185\n"
      "v_cvt_pk_bf16_f32 v183, v184, v185\n"
      "v_mul_f32 v212, v138, v212\n"
      "v_mul_f32 v213, v138, v213\n"
      "v_cvt_pk_bf16_f32 v184, v212, v213\n"
      "v_mul_f32 v214, v138, v214\n"
      "v_mul_f32 v215, v138, v215\n"
      "v_cvt_pk_bf16_f32 v185, v214, v215\n"
      "ds_write_b128 v126, v[182:185] offset:4096\n"
      "v_mul_f32 v248, v138, v248\n"
      "v_mul_f32 v249, v138, v249\n"
      "v_cvt_pk_bf16_f32 v248, v248, v249\n"
      "v_mul_f32 v250, v138, v250\n"
      "v_mul_f32 v251, v138, v251\n"
      "v_cvt_pk_bf16_f32 v249, v250, v251\n"
      "ds_write_b64 v127, v[248:249] offset:4096\n"
      "v_mul_f32 v160, v164, v160\n"
      "v_mul_f32 v161, v164, v161\n"
      "v_cvt_pk_bf16_f32 v160, v160, v161\n"
      "v_mul_f32 v162, v164, v162\n"
      "v_mul_f32 v163, v164, v163\n"
      "v_cvt_pk_bf16_f32 v161, v162, v163\n"
      "v_mul_f32 v148, v164, v148\n"
      "v_mul_f32 v149, v164, v149\n"
      "v_cvt_pk_bf16_f32 v162, v148, v149\n"
      "v_mul_f32 v150, v164, v150\n"
      "v_mul_f32 v151, v164, v151\n"
      "v_cvt_pk_bf16_f32 v163, v150, v151\n"
      "ds_write_b128 v172, v[160:163]\n"
      "v_mul_f32 v178, v164, v178\n"
      "v_mul_f32 v179, v164, v179\n"
      "v_cvt_pk_bf16_f32 v178, v178, v179\n"
      "s_waitcnt lgkmcnt(2)\n"
      "s_cbranch_scc1 L_edge_33_%=\n"
      "L_eb_34_%=:\n"
      "global_store_dwordx4 v116, v[186:189], s[54:55] sc1\n"
      "global_store_dwordx4 v116, v[168:171], s[54:55] offset:128 sc1\n"
      "global_store_dwordx4 v116, v[156:159], s[54:55] offset:256 sc1\n"
      "v_mul_f32 v180, v164, v180\n"
      "v_mul_f32 v181, v164, v181\n"
      "v_cvt_pk_bf16_f32 v179, v180, v181\n"
      "ds_write_b64 v141, v[178:179]\n"
      "v_mul_f32 v152, v166, v152\n"
      "v_mul_f32 v153, v166, v153\n"
      "v_cvt_pk_bf16_f32 v152, v152, v153\n"
      "v_mul_f32 v154, v166, v154\n"
      "v_mul_f32 v155, v166, v155\n"
      "v_cvt_pk_bf16_f32 v153, v154, v155\n"
      "v_mul_f32 v174, v166, v174\n"
      "v_mul_f32 v175, v166, v175\n"
      "v_cvt_pk_bf16_f32 v154, v174, v175\n"
      "v_mul_f32 v176, v166, v176\n"
      "v_mul_f32 v177, v166, v177\n"
      "v_cvt_pk_bf16_f32 v155, v176, v177\n"
      "ds_write_b128 v172, v[152:155] offset:2048\n"
      "v_mul_f32 v190, v166, v190\n"
      "v_mul_f32 v191, v166, v191\n"
      "v_cvt_pk_bf16_f32 v190, v190, v191\n"
      "v_mul_f32_e64 v192, v166, v192\n"
      "v_mul_f32_e64 v193, v166, v193\n"
      "v_cvt_pk_bf16_f32 v191, v192, v193\n"
      "ds_write_b64 v141, v[190:191] offset:2048\n"
      "global_store_dwordx4 v117, v[240:243], s[54:55] sc1\n"
      "s_mov_b32 exec_hi, 0\n"
      "global_store_dwordx4 v118, v[204:207], s[54:55] sc1\n"
      "s_mov_b32 exec_hi, -1\n"
      "s_waitcnt lgkmcnt(0)\n"
      "s_barrier\n"
      "ds_read_b128 v[152:155], v119\n"
      "ds_read_b128 v[174:177], v119 offset:6144\n"
      "ds_read_b128 v[188:191], v119 offset:12288\n"
      "ds_read_b128 v[178:181], v120\n"
      "ds_read_b128 v[156:159], v121\n"
      "v_subrev_u32 v141, 73728, v119\n"
      "ds_read_b128 v[168:171], v141\n"
      "ds_read_b128 v[160:163], v141 offset:6144\n"
      "ds_read_b128 v[148:151], v141 offset:12288\n"
      "s_add_u32 s27, s27, 1\n"
      "s_sub_u32 s28, s27, 3\n"
      "s_cselect_b64 s[54:55], s[12:13], s[22:23]\n"
      "s_max_i32 m0, s28, 0\n"
      "s_nop 0\n"
      "s_movrels_b32 s9, s80\n"
      "s_lshr_b32 s8, s9, 8\n"
      "s_and_b32 s9, s9, 0xff\n"
      "s_mul_i32 s28, s8, 1373568\n"
      "s_mul_i32 s29, s9, 2688\n"
      "s_add_u32 s28, s28, s29\n"
      "s_cmp_eq_u32 s8, 72\n"
      "s_cselect_b32 s8, 0, 100270464\n"
      "s_cmp_eq_u32 s9, 72\n"
      "s_cselect_b32 s9, 0, 196224\n"
      "s_add_u32 s29, s8, s9\n"
      "s_add_u32 s28, s28, s29\n"
      "s_add_u32 s54, s54, s28\n"
      "s_addc_u32 s55, s55, 0\n"
      "s_cmp_lg_u32 s29, s11\n"
      "s_waitcnt lgkmcnt(5)\n"
      "s_waitcnt lgkmcnt(3)\n"
      "s_cbranch_scc1 L_edge_35_%=\n"
      "L_eb_36_%=:\n"
      "global_store_dwordx4 v116, v[152:155], s[54:55] sc1\n"
      "global_store_dwordx4 v116, v[174:177], s[54:55] offset:128 sc1\n"
      "global_store_dwordx4 v116, v[188:191], s[54:55] offset:256 sc1\n"
      "global_store_dwordx4 v117, v[178:181], s[54:55] sc1\n"
      "s_waitcnt lgkmcnt(0)\n"
      "s_nop 0\n"
      "s_mov_b32 exec_hi, 0\n"
      "global_store_dwordx4 v118, v[156:159], s[54:55] sc1\n"
      "s_mov_b32 exec_hi, -1\n"
      "s_mov_b64 exec, s[48:49]\n"
      "s_cbranch_execz L_np_37_%=\n"
      "global_store_dwordx4 v140, v[168:171], s[22:23] sc1\n"
      "global_store_dwordx4 v140, v[160:163], s[22:23] offset:128 sc1\n"
      "global_store_dwordx4 v140, v[148:151], s[22:23] offset:256 sc1\n"
      "L_np_37_%=:\n"
      "s_mov_b64 exec, -1\n"
      "s_endpgm\n"
      "L_tail1_%=:\n"
      "s_nop 7\n"
      "s_nop 7\n"
      "v_mfma_f32_16x16x32_bf16 v[192:195], v[172:175], v[66:69], 0\n"
      "v_mfma_f32_16x16x32_bf16 v[192:195], v[176:179], v[66:69], v[192:195]\n"
      "v_mfma_f32_16x16x32_bf16 v[196:199], v[172:175], v[180:183], 0\n"
      "v_mfma_f32_16x16x32_bf16 v[196:199], v[176:179], v[50:53], v[196:199]\n"
      "v_mfma_f32_16x16x32_bf16 v[180:183], v[172:175], v[184:187], 0\n"
      "v_mfma_f32_16x16x32_bf16 v[180:183], v[176:179], v[54:57], v[180:183]\n"
      "v_mfma_f32_16x16x32_bf16 v[184:187], v[172:175], v[188:191], 0\n"
      "v_mfma_f32_16x16x32_bf16 v[184:187], v[176:179], v[58:61], v[184:187]\n"
      "s_nop 1\n"
      "v_mov_b32_dpp v192, v192 quad_perm:[0,0,0,0] row_mask:0xf bank_mask:0xf\n"
      "v_rcp_f32_e64 v192, v192\n"
      "s_nop 1\n"
      "v_cndmask_b32 v196, v196, v180, s[42:43]\n"
      "s_nop 0\n"
      "v_cndmask_b32 v196, v196, v184, s[44:45]\n"
      "v_mul_f32 v196, v192, v196\n"
      "v_cvt_pk_bf16_f32 v196, v196, v196\n"
      "ds_write_b16 v135, v196 offset:16\n"
      "s_waitcnt lgkmcnt(0)\n"
      "s_barrier\n"
      "v_mbcnt_lo_u32_b32 v180, -1, 0\n"
      "v_mbcnt_hi_u32_b32 v180, -1, v180\n"
      "v_lshrrev_b32 v184, 4, v180\n"
      "v_mul_u32_u24 v184, 336, v184\n"
      "v_and_b32 v185, 15, v180\n"
      "v_lshl_add_u32 v184, v185, 4, v184\n"
      "v_add_u32 v184, 149136, v184\n"
      "s_add_u32 s27, s27, 1\n"
      "ds_read_b128 v[196:199], v128 offset:1024\n"
      "ds_read_b128 v[186:189], v123 offset:18432\n"
      "ds_read_b128 v[192:195], v123 offset:21504\n"
      "s_sub_u32 s28, s27, 3\n"
      "s_cselect_b64 s[54:55], s[12:13], s[22:23]\n"
      "s_max_i32 m0, s28, 0\n"
      "ds_read_b128 v[176:179], v119 offset:18432\n"
      "s_movrels_b32 s9, s80\n"
      "s_lshr_b32 s8, s9, 8\n"
      "s_and_b32 s9, s9, 0xff\n"
      "s_mul_i32 s28, s8, 1373568\n"
      "s_mul_i32 s29, s9, 2688\n"
      "s_add_u32 s28, s28, s29\n"
      "s_cmp_eq_u32 s8, 72\n"
      "s_cselect_b32 s8, 0, 100270464\n"
      "s_cmp_eq_u32 s9, 72\n"
      "s_cselect_b32 s9, 0, 196224\n"
      "s_add_u32 s29, s8, s9\n"
      "s_add_u32 s28, s28, s29\n"
      "s_add_u32 s54, s54, s28\n"
      "s_addc_u32 s55, s55, 0\n"
      "s_cmp_lg_u32 s29, s11\n"
      "ds_read_b128 v[172:175], v119 offset:24576\n"
      "ds_read_b128 v[200:203], v119 offset:30720\n"
      "ds_read_b128 v[204:207], v120 offset:18432\n"
      "ds_read_b128 v[208:211], v121 offset:18432\n"
      "ds_read_b128 v[212:215], v123 offset:24576\n"
      "ds_read_b128 v[216:219], v123 offset:27648\n"
      "s_waitcnt lgkmcnt(7)\n"
      "v_add_f32 v196, v196, v197\n"
      "v_add_f32 v198, v198, v199\n"
      "v_add_f32 v196, v196, v198\n"
      "v_mfma_f32_16x16x32_bf16 v[220:223], a[216:219], v[186:189], 0\n"
      "ds_read_b128 v[224:227], v123 offset:30720\n"
      "v_mfma_f32_16x16x32_bf16 v[220:223], a[228:231], v[192:195], v[220:223]\n"
      "ds_read_b128 v[228:231], v123 offset:33792\n"
      "v_rsq_f32 v196, v196\n"
      "v_mfma_f32_16x16x32_bf16 v[232:235], a[220:223], v[186:189], 0\n"
      "ds_read_b128 v[236:239], v123 offset:18688\n"
      "v_mul_f32 v196, s41, v196\n"
      "v_mfma_f32_16x16x32_bf16 v[232:235], a[232:235], v[192:195], v[232:235]\n"
      "ds_read_b128 v[240:243], v123 offset:21760\n"
      "v_mov_b32 v198, v196\n"
      "v_mfma_f32_16x16x32_bf16 v[244:247], a[224:227], v[186:189], 0\n"
      "s_cbranch_scc1 L_edge_38_%=\n"
      "L_eb_39_%=:\n"
      "v_mfma_f32_16x16x32_bf16 v[244:247], a[236:239], v[192:195], v[244:247]\n"
      "ds_read_b128 v[192:195], v123 offset:24832\n"
      "v_permlane32_swap_b32 v196, v198\n"
      "v_mov_b32 v186, v196\n"
      "s_waitcnt lgkmcnt(5)\n"
      "s_nop 0\n"
      "v_permlane16_swap_b32_e64 v196, v186\n"
      "v_mfma_f32_16x16x32_bf16 v[220:223], a[240:243], v[212:215], v[220:223]\n"
      "global_store_dwordx4 v116, v[176:179], s[54:55] sc1\n"
      "v_mfma_f32_16x16x32_bf16 v[220:223], a[252:255], v[216:219], v[220:223]\n"
      "ds_read_b128 v[188:191], v123 offset:27904\n"
      "v_mul_f32 v136, v196, v136\n"
      "v_mfma_f32_16x16x32_bf16 v[232:235], a[244:247], v[212:215], v[232:235]\n"
      "v_mul_f32_e64 v137, v196, v137\n"
      "s_waitcnt lgkmcnt(4)\n"
      "v_mfma_f32_16x16x32_bf16 v[232:235], v[0:3], v[216:219], v[232:235]\n"
      "v_cvt_pk_bf16_f32 v136, v136, v137\n"
      "v_mul_f32_e64 v138, v196, v138\n"
      "v_mfma_f32_16x16x32_bf16 v[244:247], a[248:251], v[212:215], v[244:247]\n"
      "ds_read_b128 v[212:215], v123 offset:30976\n"
      "v_mul_f32 v139, v196, v139\n"
      "v_mfma_f32_16x16x32_bf16 v[244:247], v[4:7], v[216:219], v[244:247]\n"
      "s_waitcnt lgkmcnt(3)\n"
      "v_cvt_pk_bf16_f32 v137, v138, v139\n"
      "v_mul_f32 v140, v196, v140\n"
      "v_mfma_f32_16x16x32_bf16 v[220:223], v[8:11], v[224:227], v[220:223]\n"
      "ds_read_b128 v[216:219], v123 offset:34048\n"
      "v_mul_f32 v141, v196, v141\n"
      "v_mfma_f32_16x16x32_bf16 v[220:223], v[20:23], v[228:231], v[220:223]\n"
      "v_cvt_pk_bf16_f32 v138, v140, v141\n"
      "v_mul_f32 v142, v196, v142\n"
      "v_mfma_f32_16x16x32_bf16 v[232:235], v[12:15], v[224:227], v[232:235]\n"
      "v_mul_f32 v143, v196, v143\n"
      "v_cvt_pk_bf16_f32 v139, v142, v143\n"
      "v_mfma_f32_16x16x32_bf16 v[232:235], v[24:27], v[228:231], v[232:235]\n"
      "ds_write_b128 v126, v[136:139]\n"
      "v_mfma_f32_16x16x32_bf16 v[244:247], v[16:19], v[224:227], v[244:247]\n"
      "ds_read_b128 v[224:227], v123 offset:18944\n"
      "v_mul_f32 v144, v196, v144\n"
      "v_mfma_f32_16x16x32_bf16 v[244:247], v[28:31], v[228:231], v[244:247]\n"
      "ds_read_b128 v[228:231], v123 offset:22016\n"
      "v_mul_f32 v145, v196, v145\n"
      "v_mfma_f32_16x16x32_bf16 v[136:139], a[216:219], v[236:239], 0\n"
      "s_waitcnt lgkmcnt(4)\n"
      "v_cvt_pk_bf16_f32 v144, v144, v145\n"
      "v_mul_f32 v146, v196, v146\n"
      "v_mfma_f32_16x16x32_bf16 v[136:139], a[228:231], v[240:243], v[136:139]\n"
      "v_mul_f32_e64 v147, v196, v147\n"
      "v_cvt_pk_bf16_f32 v145, v146, v147\n"
      "v_mfma_f32_16x16x32_bf16 v[140:143], a[220:223], v[236:239], 0\n"
      "global_store_dwordx4 v116, v[172:175], s[54:55] offset:128 sc1\n"
      "v_mfma_f32_16x16x32_bf16 v[140:143], a[232:235], v[240:243], v[140:143]\n"
      "ds_write_b64 v127, v[144:145]\n"
      "v_mfma_f32_16x16x32_bf16 v[144:147], a[224:227], v[236:239], 0\n"
      "ds_read_b128 v[172:175], v123 offset:25088\n"
      "v_mul_f32_e64 v148, v186, v148\n"
      "v_mfma_f32_16x16x32_bf16 v[144:147], a[236:239], v[240:243], v[144:147]\n"
      "s_waitcnt lgkmcnt(4)\n"
      "v_mul_f32 v149, v186, v149\n"
      "v_cvt_pk_bf16_f32 v148, v148, v149\n"
      "v_mfma_f32_16x16x32_bf16 v[136:139], a[240:243], v[192:195], v[136:139]\n"
      "ds_read_b128 v[240:243], v123 offset:28160\n"
      "v_mul_f32 v150, v186, v150\n"
      "v_mfma_f32_16x16x32_bf16 v[136:139], a[252:255], v[188:191], v[136:139]\n"
      "s_lshl_b32 s29, s24, 8\n"
      "v_mul_f32_e64 v151, v186, v151\n"
      "v_cvt_pk_bf16_f32 v149, v150, v151\n"
      "v_mfma_f32_16x16x32_bf16 v[140:143], a[244:247], v[192:195], v[140:143]\n"
      "s_waitcnt lgkmcnt(3)\n"
      "v_mul_f32 v152, v186, v152\n"
      "v_mfma_f32_16x16x32_bf16 v[140:143], v[0:3], v[188:191], v[140:143]\n"
      "s_add_u32 s29, s29, 157200\n"
      "v_mul_f32 v153, v186, v153\n"
      "v_cvt_pk_bf16_f32 v150, v152, v153\n"
      "v_mfma_f32_16x16x32_bf16 v[144:147], a[248:251], v[192:195], v[144:147]\n"
      "v_mul_f32 v154, v186, v154\n"
      "v_mul_f32 v155, v186, v155\n"
      "v_mfma_f32_16x16x32_bf16 v[144:147], v[4:7], v[188:191], v[144:147]\n"
      "v_cvt_pk_bf16_f32 v151, v154, v155\n"
      "v_mul_f32 v156, v186, v156\n"
      "v_mfma_f32_16x16x32_bf16 v[136:139], v[8:11], v[212:215], v[136:139]\n"
      "ds_read_b128 v[188:191], v123 offset:31232\n"
      "v_mul_f32_e64 v157, v186, v157\n"
      "v_mfma_f32_16x16x32_bf16 v[136:139], v[20:23], v[216:219], v[136:139]\n"
      "ds_write_b128 v126, v[148:151] offset:2048\n"
      "v_mfma_f32_16x16x32_bf16 v[140:143], v[12:15], v[212:215], v[140:143]\n"
      "ds_read_b128 v[148:151], v123 offset:34304\n"
      "v_cvt_pk_bf16_f32 v156, v156, v157\n"
      "v_mfma_f32_16x16x32_bf16 v[140:143], v[24:27], v[216:219], v[140:143]\n"
      "s_waitcnt lgkmcnt(3)\n"
      "v_mul_f32 v158, v186, v158\n"
      "v_mul_f32_e64 v159, v186, v159\n"
      "v_mfma_f32_16x16x32_bf16 v[144:147], v[16:19], v[212:215], v[144:147]\n"
      "v_cvt_pk_bf16_f32 v157, v158, v159\n"
      "v_fma_f32 v158, v220, v220, s39\n"
      "v_mfma_f32_16x16x32_bf16 v[144:147], v[28:31], v[216:219], v[144:147]\n"
      "ds_write_b64 v127, v[156:157] offset:2048\n"
      "v_mfma_f32_16x16x32_bf16 v[216:219], a[216:219], v[224:227], 0\n"
      "ds_read_b128 v[212:215], v184\n"
      "v_fmac_f32 v158, v221, v221\n"
      "v_mfma_f32_16x16x32_bf16 v[216:219], a[228:231], v[228:231], v[216:219]\n"
      "ds_read_b128 v[154:157], v184 offset:1344\n"
      "v_fmac_f32 v158, v222, v222\n"
      "v_mfma_f32_16x16x32_bf16 v[192:195], a[220:223], v[224:227], 0\n"
      "v_fmac_f32 v158, v223, v223\n"
      "v_fmac_f32 v158, v232, v232\n"
      "v_mfma_f32_16x16x32_bf16 v[192:195], a[232:235], v[228:231], v[192:195]\n"
      "global_store_dwordx4 v116, v[200:203], s[54:55] offset:256 sc1\n"
      "v_mfma_f32_16x16x32_bf16 v[236:239], a[224:227], v[224:227], 0\n"
      "v_fmac_f32 v158, v233, v233\n"
      "v_fmac_f32 v158, v234, v234\n"
      "v_mfma_f32_16x16x32_bf16 v[236:239], a[236:239], v[228:231], v[236:239]\n"
      "s_waitcnt lgkmcnt(3)\n"
      "v_fmac_f32 v158, v235, v235\n"
      "v_fmac_f32 v158, v244, v244\n"
      "v_mfma_f32_16x16x32_bf16 v[216:219], a[240:243], v[172:175], v[216:219]\n"
      "v_fmac_f32 v158, v245, v245\n"
      "v_fmac_f32 v158, v246, v246\n"
      "v_mfma_f32_16x16x32_bf16 v[216:219], a[252:255], v[240:243], v[216:219]\n"
      "v_fmac_f32 v158, v247, v247\n"
      "v_mul_f32 v160, v198, v160\n"
      "v_mfma_f32_16x16x32_bf16 v[192:195], a[244:247], v[172:175], v[192:195]\n"
      "ds_read_b128 v[228:231], v184 offset:2688\n"
      "v_mul_f32 v161, v198, v161\n"
      "v_mfma_f32_16x16x32_bf16 v[192:195], v[0:3], v[240:243], v[192:195]\n"
      "ds_read_b128 v[200:203], v184 offset:4032\n"
      "v_cvt_pk_bf16_f32 v160, v160, v161\n"
      "v_mfma_f32_16x16x32_bf16 v[236:239], a[248:251], v[172:175], v[236:239]\n"
      "v_mul_f32 v162, v198, v162\n"
      "s_waitcnt lgkmcnt(2)\n"
      "v_mfma_f32_16x16x32_bf16 v[236:239], v[4:7], v[240:243], v[236:239]\n"
      "global_store_dwordx4 v117, v[204:207], s[54:55] sc1\n"
      "v_mfma_f32_16x16x32_bf16 v[216:219], v[8:11], v[188:191], v[216:219]\n"
      "v_mul_f32_e64 v163, v198, v163\n"
      "v_cvt_pk_bf16_f32 v161, v162, v163\n"
      "v_mfma_f32_16x16x32_bf16 v[216:219], v[20:23], v[148:151], v[216:219]\n"
      "v_mul_f32 v164, v198, v164\n"
      "v_mul_f32 v165, v198, v165\n"
      "v_mfma_f32_16x16x32_bf16 v[192:195], v[12:15], v[188:191], v[192:195]\n"
      "v_cvt_pk_bf16_f32 v162, v164, v165\n"
      "v_mul_f32 v166, v198, v166\n"
      "v_mfma_f32_16x16x32_bf16 v[192:195], v[24:27], v[148:151], v[192:195]\n"
      "ds_read_b128 v[204:207], v184 offset:5376\n"
      "v_mul_f32 v167, v198, v167\n"
      "v_mfma_f32_16x16x32_bf16 v[236:239], v[16:19], v[188:191], v[236:239]\n"
      "ds_read_b128 v[188:191], v184 offset:6720\n"
      "v_cvt_pk_bf16_f32 v163, v166, v167\n"
      "v_mfma_f32_16x16x32_bf16 v[236:239], v[28:31], v[148:151], v[236:239]\n"
      "ds_write_b128 v126, v[160:163] offset:4096\n"
      "v_mfma_f32_16x16x32_bf16 v[160:163], a[216:219], v[212:215], 0\n"
      "s_waitcnt lgkmcnt(3)\n"
      "v_mul_f32 v168, v198, v168\n"
      "v_mul_f32_e64 v169, v198, v169\n"
      "v_mfma_f32_16x16x32_bf16 v[160:163], a[228:231], v[154:157], v[160:163]\n"
      "v_cvt_pk_bf16_f32 v168, v168, v169\n"
      "v_mul_f32_e64 v170, v198, v170\n"
      "v_mfma_f32_16x16x32_bf16 v[148:151], a[220:223], v[212:215], 0\n"
      "ds_read_b128 v[164:167], v184 offset:256\n"
      "v_mul_f32_e64 v171, v198, v171\n"
      "v_mfma_f32_16x16x32_bf16 v[148:151], a[232:235], v[154:157], v[148:151]\n"
      "v_cvt_pk_bf16_f32 v169, v170, v171\n"
      "v_fma_f32 v170, v136, v136, s39\n"
      "v_mfma_f32_16x16x32_bf16 v[240:243], a[224:227], v[212:215], 0\n"
      "ds_read_b128 v[212:215], v184 offset:1600\n"
      "v_fmac_f32_e64 v170, v137, v137\n"
      "v_mfma_f32_16x16x32_bf16 v[240:243], a[236:239], v[154:157], v[240:243]\n"
      "ds_write_b64 v127, v[168:169] offset:4096\n"
      "v_mfma_f32_16x16x32_bf16 v[160:163], a[240:243], v[228:231], v[160:163]\n"
      "v_fmac_f32 v170, v138, v138\n"
      "v_fmac_f32 v170, v139, v139\n"
      "v_mfma_f32_16x16x32_bf16 v[160:163], a[252:255], v[200:203], v[160:163]\n"
      "s_waitcnt lgkmcnt(3)\n"
      "v_fmac_f32 v170, v140, v140\n"
      "v_fmac_f32_e64 v170, v141, v141\n"
      "v_mfma_f32_16x16x32_bf16 v[148:151], a[244:247], v[228:231], v[148:151]\n"
      "ds_read_b128 v[154:157], v184 offset:2944\n"
      "v_fmac_f32 v170, v142, v142\n"
      "v_mfma_f32_16x16x32_bf16 v[148:151], v[0:3], v[200:203], v[148:151]\n"
      "ds_read_b128 v[172:175], v184 offset:4288\n"
      "v_fmac_f32 v170, v143, v143\n"
      "v_mfma_f32_16x16x32_bf16 v[240:243], a[248:251], v[228:231], v[240:243]\n"
      "v_fmac_f32 v170, v144, v144\n"
      "v_fmac_f32 v170, v145, v145\n"
      "v_mfma_f32_16x16x32_bf16 v[240:243], v[4:7], v[200:203], v[240:243]\n"
      "v_fmac_f32 v170, v146, v146\n"
      "v_fmac_f32 v170, v147, v147\n"
      "v_mfma_f32_16x16x32_bf16 v[160:163], v[8:11], v[204:207], v[160:163]\n"
      "ds_read_b128 v[200:203], v184 offset:5632\n"
      "v_lshl_add_u32 v228, v180, 2, s29\n"
      "v_mfma_f32_16x16x32_bf16 v[160:163], v[20:23], v[188:191], v[160:163]\n"
      "s_waitcnt lgkmcnt(3)\n"
      "v_lshrrev_b32 v229, 3, v180\n"
      "v_fma_f32 v180, v216, v216, s39\n"
      "v_mfma_f32_16x16x32_bf16 v[148:151], v[12:15], v[204:207], v[148:151]\n"
      "ds_read_b32 v228, v228\n"
      "v_fmac_f32 v180, v217, v217\n"
      "v_mfma_f32_16x16x32_bf16 v[148:151], v[24:27], v[188:191], v[148:151]\n"
      "ds_read_b128 v[224:227], v184 offset:6976\n"
      "v_fmac_f32 v180, v218, v218\n"
      "v_mfma_f32_16x16x32_bf16 v[240:243], v[16:19], v[204:207], v[240:243]\n"
      "s_lshl_b32 s29, s24, 3\n"
      "v_add_u32 v229, s29, v229\n"
      "v_cmp_gt_u32 s[48:49], s26, v229\n"
      "v_mfma_f32_16x16x32_bf16 v[240:243], v[28:31], v[188:191], v[240:243]\n"
      "v_fmac_f32 v180, v219, v219\n"
      "v_fmac_f32 v180, v192, v192\n"
      "v_mfma_f32_16x16x32_bf16 v[188:191], a[216:219], v[164:167], 0\n"
      "v_fmac_f32 v180, v193, v193\n"
      "v_fmac_f32 v180, v194, v194\n"
      "v_mfma_f32_16x16x32_bf16 v[188:191], a[228:231], v[212:215], v[188:191]\n"
      "s_waitcnt lgkmcnt(3)\n"
      "v_fmac_f32 v180, v195, v195\n"
      "v_fmac_f32 v180, v236, v236\n"
      "v_mfma_f32_16x16x32_bf16 v[204:207], a[220:223], v[164:167], 0\n"
      "v_fmac_f32 v180, v237, v237\n"
      "v_fmac_f32 v180, v238, v238\n"
      "v_mfma_f32_16x16x32_bf16 v[204:207], a[232:235], v[212:215], v[204:207]\n"
      "s_mov_b32 exec_hi, 0\n"
      "global_store_dwordx4 v118, v[208:211], s[54:55] sc1\n"
      "s_mov_b32 exec_hi, -1\n"
      "v_mfma_f32_16x16x32_bf16 v[184:187], a[224:227], v[164:167], 0\n"
      "v_fmac_f32 v180, v239, v239\n"
      "v_mov_b32_e64 v208, v180\n"
      "v_mfma_f32_16x16x32_bf16 v[184:187], a[236:239], v[212:215], v[184:187]\n"
      "v_permlane16_swap_b32 v158, v170\n"
      "v_mfma_f32_16x16x32_bf16 v[188:191], a[240:243], v[154:157], v[188:191]\n"
      "s_waitcnt lgkmcnt(0)\n"
      "v_add_f32_e64 v158, v158, v170\n"
      "v_fma_f32 v170, v160, v160, s39\n"
      "v_mfma_f32_16x16x32_bf16 v[188:191], a[252:255], v[172:175], v[188:191]\n"
      "v_permlane16_swap_b32 v180, v208\n"
      "v_mfma_f32_16x16x32_bf16 v[204:207], a[244:247], v[154:157], v[204:207]\n"
      "v_add_f32 v180, v180, v208\n"
      "v_fmac_f32 v170, v161, v161\n"
      "v_mfma_f32_16x16x32_bf16 v[204:207], v[0:3], v[172:175], v[204:207]\n"
      "v_permlane32_swap_b32 v158, v180\n"
      "v_mfma_f32_16x16x32_bf16 v[184:187], a[248:251], v[154:157], v[184:187]\n"
      "v_add_f32 v158, v158, v180\n"
      "v_fmac_f32 v170, v162, v162\n"
      "v_mfma_f32_16x16x32_bf16 v[184:187], v[4:7], v[172:175], v[184:187]\n"
      "ds_write_b32 v129, v158\n"
      "v_mfma_f32_16x16x32_bf16 v[188:191], v[8:11], v[200:203], v[188:191]\n"
      "v_fmac_f32 v170, v163, v163\n"
      "v_fmac_f32 v170, v148, v148\n"
      "v_mfma_f32_16x16x32_bf16 v[188:191], v[20:23], v[224:227], v[188:191]\n"
      "v_fmac_f32 v170, v149, v149\n"
      "v_fmac_f32 v170, v150, v150\n"
      "v_mfma_f32_16x16x32_bf16 v[204:207], v[12:15], v[200:203], v[204:207]\n"
      "v_fmac_f32 v170, v151, v151\n"
      "v_fmac_f32 v170, v240, v240\n"
      "v_mfma_f32_16x16x32_bf16 v[204:207], v[24:27], v[224:227], v[204:207]\n"
      "v_fmac_f32 v170, v241, v241\n"
      "v_fmac_f32 v170, v242, v242\n"
      "v_mfma_f32_16x16x32_bf16 v[184:187], v[16:19], v[200:203], v[184:187]\n"
      "v_fmac_f32_e64 v170, v243, v243\n"
      "v_fma_f32 v200, v188, v188, s39\n"
      "v_mfma_f32_16x16x32_bf16 v[184:187], v[28:31], v[224:227], v[184:187]\n"
      "v_fmac_f32 v200, v189, v189\n"
      "v_fmac_f32 v200, v190, v190\n"
      "v_fmac_f32 v200, v191, v191\n"
      "v_fmac_f32 v200, v204, v204\n"
      "v_fmac_f32 v200, v205, v205\n"
      "v_fmac_f32 v200, v206, v206\n"
      "v_fmac_f32 v200, v207, v207\n"
      "s_nop 0\n"
      "v_fmac_f32 v200, v184, v184\n"
      "v_fmac_f32 v200, v185, v185\n"
      "v_fmac_f32 v200, v186, v186\n"
      "v_fmac_f32 v200, v187, v187\n"
      "v_mov_b32 v224, v200\n"
      "v_mov_b32 v225, v224\n"
      "v_permlane16_swap_b32 v170, v200\n"
      "v_add_f32 v170, v170, v200\n"
      "v_permlane16_swap_b32 v224, v225\n"
      "v_add_f32_e64 v224, v224, v225\n"
      "s_nop 1\n"
      "v_permlane32_swap_b32_e64 v170, v224\n"
      "v_add_f32_e64 v170, v170, v224\n"
      "ds_write_b32 v129, v170 offset:2048\n"
      "s_waitcnt lgkmcnt(0)\n"
      "s_barrier\n"
      "ds_read_b128 v[224:227], v128\n"
      "ds_read_b128 v[200:203], v128 offset:2048\n"
      "ds_read_b128 v[172:175], v119\n"
      "ds_read_b128 v[154:157], v119 offset:6144\n"
      "ds_read_b128 v[208:211], v119 offset:12288\n"
      "ds_read_b128 v[212:215], v120\n"
      "ds_read_b128 v[164:167], v121\n"
      "v_subrev_u32 v170, 73728, v126\n"
      "v_subrev_u32 v158, 73728, v127\n"
      "s_add_u32 s27, s27, 1\n"
      "s_sub_u32 s28, s27, 3\n"
      "s_cselect_b64 s[54:55], s[12:13], s[22:23]\n"
      "s_max_i32 m0, s28, 0\n"
      "s_nop 0\n"
      "s_movrels_b32 s9, s80\n"
      "s_lshr_b32 s8, s9, 8\n"
      "s_and_b32 s9, s9, 0xff\n"
      "s_mul_i32 s28, s8, 1373568\n"
      "s_mul_i32 s29, s9, 2688\n"
      "s_add_u32 s28, s28, s29\n"
      "s_cmp_eq_u32 s8, 72\n"
      "s_cselect_b32 s8, 0, 100270464\n"
      "s_cmp_eq_u32 s9, 72\n"
      "s_cselect_b32 s9, 0, 196224\n"
      "s_add_u32 s29, s8, s9\n"
      "s_add_u32 s28, s28, s29\n"
      "s_add_u32 s54, s54, s28\n"
      "s_addc_u32 s55, s55, 0\n"
      "s_cmp_lg_u32 s29, s11\n"
      "s_waitcnt lgkmcnt(4)\n"
      "v_add_f32 v224, v224, v225\n"
      "v_add_f32 v226, v226, v227\n"
      "v_add_f32 v224, v224, v226\n"
      "v_rsq_f32 v224, v224\n"
      "s_nop 0\n"
      "v_mul_f32 v224, s41, v224\n"
      "v_mov_b32 v226, v224\n"
      "v_add_f32 v200, v200, v201\n"
      "s_nop 0\n"
      "v_permlane32_swap_b32 v224, v226\n"
      "v_mov_b32 v201, v224\n"
      "v_add_f32 v202, v202, v203\n"
      "v_add_f32 v200, v200, v202\n"
      "v_permlane16_swap_b32 v224, v201\n"
      "v_rsq_f32 v200, v200\n"
      "v_mul_f32 v220, v224, v220\n"
      "v_mul_f32 v200, s41, v200\n"
      "v_mov_b32 v202, v200\n"
      "v_mul_f32 v221, v224, v221\n"
      "v_cvt_pk_bf16_f32 v220, v220, v221\n"
      "v_permlane32_swap_b32 v200, v202\n"
      "v_mov_b32 v202, v200\n"
      "v_mul_f32 v222, v224, v222\n"
      "v_mul_f32 v223, v224, v223\n"
      "v_permlane16_swap_b32 v200, v202\n"
      "v_cvt_pk_bf16_f32 v221, v222, v223\n"
      "v_mul_f32 v232, v224, v232\n"
      "v_mul_f32 v233, v224, v233\n"
      "v_cvt_pk_bf16_f32 v222, v232, v233\n"
      "v_mul_f32 v234, v224, v234\n"
      "v_mul_f32 v235, v224, v235\n"
      "v_cvt_pk_bf16_f32 v223, v234, v235\n"
      "ds_write_b128 v126, v[220:223] offset:18432\n"
      "v_mul_f32 v244, v224, v244\n"
      "v_mul_f32 v245, v224, v245\n"
      "v_cvt_pk_bf16_f32 v244, v244, v245\n"
      "v_mul_f32 v246, v224, v246\n"
      "v_mul_f32 v247, v224, v247\n"
      "v_cvt_pk_bf16_f32 v245, v246, v247\n"
      "ds_write_b64 v127, v[244:245] offset:18432\n"
      "v_mul_f32 v136, v201, v136\n"
      "v_mul_f32 v137, v201, v137\n"
      "v_cvt_pk_bf16_f32 v136, v136, v137\n"
      "v_mul_f32 v138, v201, v138\n"
      "v_mul_f32 v139, v201, v139\n"
      "v_cvt_pk_bf16_f32 v137, v138, v139\n"
      "v_mul_f32 v140, v201, v140\n"
      "v_mul_f32 v141, v201, v141\n"
      "v_cvt_pk_bf16_f32 v138, v140, v141\n"
      "v_mul_f32 v142, v201, v142\n"
      "v_mul_f32 v143, v201, v143\n"
      "v_cvt_pk_bf16_f32 v139, v142, v143\n"
      "ds_write_b128 v126, v[136:139] offset:20480\n"
      "v_mul_f32 v144, v201, v144\n"
      "v_mul_f32 v145, v201, v145\n"
      "v_cvt_pk_bf16_f32 v144, v144, v145\n"
      "v_mul_f32 v146, v201, v146\n"
      "v_mul_f32 v147, v201, v147\n"
      "v_cvt_pk_bf16_f32 v145, v146, v147\n"
      "ds_write_b64 v127, v[144:145] offset:20480\n"
      "v_mul_f32 v216, v226, v216\n"
      "v_mul_f32 v217, v226, v217\n"
      "v_cvt_pk_bf16_f32 v216, v216, v217\n"
      "v_mul_f32 v218, v226, v218\n"
      "v_mul_f32 v219, v226, v219\n"
      "v_cvt_pk_bf16_f32 v217, v218, v219\n"
      "v_mul_f32 v192, v226, v192\n"
      "v_mul_f32 v193, v226, v193\n"
      "v_cvt_pk_bf16_f32 v218, v192, v193\n"
      "v_mul_f32 v194, v226, v194\n"
      "v_mul_f32 v195, v226, v195\n"
      "v_cvt_pk_bf16_f32 v219, v194, v195\n"
      "ds_write_b128 v126, v[216:219] offset:22528\n"
      "v_mul_f32 v236, v226, v236\n"
      "v_mul_f32 v237, v226, v237\n"
      "v_cvt_pk_bf16_f32 v236, v236, v237\n"
      "v_mul_f32 v238, v226, v238\n"
      "v_mul_f32 v239, v226, v239\n"
      "v_cvt_pk_bf16_f32 v237, v238, v239\n"
      "ds_write_b64 v127, v[236:237] offset:22528\n"
      "v_mul_f32 v160, v200, v160\n"
      "v_mul_f32 v161, v200, v161\n"
      "v_cvt_pk_bf16_f32 v160, v160, v161\n"
      "v_mul_f32 v162, v200, v162\n"
      "v_mul_f32 v163, v200, v163\n"
      "v_cvt_pk_bf16_f32 v161, v162, v163\n"
      "v_mul_f32 v148, v200, v148\n"
      "v_mul_f32 v149, v200, v149\n"
      "v_cvt_pk_bf16_f32 v162, v148, v149\n"
      "v_mul_f32 v150, v200, v150\n"
      "v_mul_f32 v151, v200, v151\n"
      "v_cvt_pk_bf16_f32 v163, v150, v151\n"
      "ds_write_b128 v170, v[160:163] offset:18432\n"
      "v_mul_f32 v240, v200, v240\n"
      "v_mul_f32 v241, v200, v241\n"
      "v_cvt_pk_bf16_f32 v240, v240, v241\n"
      "s_waitcnt lgkmcnt(2)\n"
      "s_cbranch_scc1 L_edge_40_%=\n"
      "L_eb_41_%=:\n"
      "global_store_dwordx4 v116, v[172:175], s[54:55] sc1\n"
      "global_store_dwordx4 v116, v[154:157], s[54:55] offset:128 sc1\n"
      "global_store_dwordx4 v116, v[208:211], s[54:55] offset:256 sc1\n"
      "v_mul_f32 v242, v200, v242\n"
      "v_mul_f32 v243, v200, v243\n"
      "v_cvt_pk_bf16_f32 v241, v242, v243\n"
      "ds_write_b64 v158, v[240:241] offset:18432\n"
      "v_mul_f32 v188, v202, v188\n"
      "v_mul_f32 v189, v202, v189\n"
      "v_cvt_pk_bf16_f32 v188, v188, v189\n"
      "v_mul_f32 v190, v202, v190\n"
      "v_mul_f32 v191, v202, v191\n"
      "v_cvt_pk_bf16_f32 v189, v190, v191\n"
      "v_mul_f32 v204, v202, v204\n"
      "v_mul_f32 v205, v202, v205\n"
      "v_cvt_pk_bf16_f32 v190, v204, v205\n"
      "v_mul_f32 v206, v202, v206\n"
      "v_mul_f32 v207, v202, v207\n"
      "v_cvt_pk_bf16_f32 v191, v206, v207\n"
      "ds_write_b128 v170, v[188:191] offset:20480\n"
      "v_mul_f32 v184, v202, v184\n"
      "v_mul_f32 v185, v202, v185\n"
      "v_cvt_pk_bf16_f32 v184, v184, v185\n"
      "v_mul_f32 v186, v202, v186\n"
      "v_mul_f32 v187, v202, v187\n"
      "v_cvt_pk_bf16_f32 v185, v186, v187\n"
      "ds_write_b64 v158, v[184:185] offset:20480\n"
      "global_store_dwordx4 v117, v[212:215], s[54:55] sc1\n"
      "s_mov_b32 exec_hi, 0\n"
      "global_store_dwordx4 v118, v[164:167], s[54:55] sc1\n"
      "s_mov_b32 exec_hi, -1\n"
      "s_waitcnt lgkmcnt(0)\n"
      "s_barrier\n"
      "ds_read_b128 v[164:167], v119 offset:18432\n"
      "ds_read_b128 v[212:215], v119 offset:24576\n"
      "ds_read_b128 v[184:187], v119 offset:30720\n"
      "ds_read_b128 v[188:191], v120 offset:18432\n"
      "ds_read_b128 v[204:207], v121 offset:18432\n"
      "v_subrev_u32 v158, 73728, v119\n"
      "ds_read_b128 v[240:243], v158 offset:18432\n"
      "ds_read_b128 v[208:211], v158 offset:24576\n"
      "ds_read_b128 v[154:157], v158 offset:30720\n"
      "s_add_u32 s27, s27, 1\n"
      "s_sub_u32 s28, s27, 3\n"
      "s_cselect_b64 s[54:55], s[12:13], s[22:23]\n"
      "s_max_i32 m0, s28, 0\n"
      "s_nop 0\n"
      "s_movrels_b32 s9, s80\n"
      "s_lshr_b32 s8, s9, 8\n"
      "s_and_b32 s9, s9, 0xff\n"
      "s_mul_i32 s28, s8, 1373568\n"
      "s_mul_i32 s29, s9, 2688\n"
      "s_add_u32 s28, s28, s29\n"
      "s_cmp_eq_u32 s8, 72\n"
      "s_cselect_b32 s8, 0, 100270464\n"
      "s_cmp_eq_u32 s9, 72\n"
      "s_cselect_b32 s9, 0, 196224\n"
      "s_add_u32 s29, s8, s9\n"
      "s_add_u32 s28, s28, s29\n"
      "s_add_u32 s54, s54, s28\n"
      "s_addc_u32 s55, s55, 0\n"
      "s_cmp_lg_u32 s29, s11\n"
      "s_waitcnt lgkmcnt(5)\n"
      "s_waitcnt lgkmcnt(3)\n"
      "s_cbranch_scc1 L_edge_42_%=\n"
      "L_eb_43_%=:\n"
      "global_store_dwordx4 v116, v[164:167], s[54:55] sc1\n"
      "global_store_dwordx4 v116, v[212:215], s[54:55] offset:128 sc1\n"
      "global_store_dwordx4 v116, v[184:187], s[54:55] offset:256 sc1\n"
      "global_store_dwordx4 v117, v[188:191], s[54:55] sc1\n"
      "s_waitcnt lgkmcnt(0)\n"
      "s_nop 0\n"
      "s_mov_b32 exec_hi, 0\n"
      "global_store_dwordx4 v118, v[204:207], s[54:55] sc1\n"
      "s_mov_b32 exec_hi, -1\n"
      "s_mov_b64 exec, s[48:49]\n"
      "s_cbranch_execz L_np_44_%=\n"
      "global_store_dwordx4 v228, v[240:243], s[22:23] sc1\n"
      "global_store_dwordx4 v228, v[208:211], s[22:23] offset:128 sc1\n"
      "global_store_dwordx4 v228, v[154:157], s[22:23] offset:256 sc1\n"
      "L_np_44_%=:\n"
      "s_mov_b64 exec, -1\n"
      "s_endpgm\n"
      "L_gen_7_%=:\n"
      "s_waitcnt lgkmcnt(0)\n"
      "s_nop 7\n"
      "s_nop 7\n"
      "s_cmp_eq_u64 s[48:49], 0\n"
      "s_cbranch_scc1 L_go_13_%=\n"
      "v_mbcnt_lo_u32_b32 v148, -1, 0\n"
      "v_mbcnt_hi_u32_b32 v148, -1, v148\n"
      "v_lshlrev_b32 v149, 1, v148\n"
      "v_add_u32 v150, 4802, v149\n"
      "s_mov_b32 m0, s27\n"
      "s_nop 0\n"
      "s_movrels_b32 s9, s80\n"
      "s_lshr_b32 s8, s9, 8\n"
      "s_and_b32 s9, s9, 0xff\n"
      "s_mul_i32 s10, s8, 73\n"
      "s_add_u32 s10, s10, s9\n"
      "s_mul_i32 s30, s10, 4802\n"
      "s_add_u32 s30, s30, s18\n"
      "s_and_b32 s29, s30, 15\n"
      "v_cmp_gt_u32 vcc, s29, v149\n"
      "s_add_u32 s30, s29, 110592\n"
      "s_nop 0\n"
      "v_cndmask_b32 v149, v150, v149, vcc\n"
      "v_add_u32 v149, 110592, v149\n"
      "v_mov_b32_e64 v150, 0\n"
      "s_mov_b64 exec, 0x7f\n"
      "ds_write_b16 v149, v150\n"
      "s_mov_b64 exec, -1\n"
      "v_lshlrev_b32 v149, 4, v148\n"
      "v_add_u32 v149, 110592, v149\n"
      "ds_read_b128 v[194:197], v149\n"
      "ds_read_b128 v[198:201], v149 offset:1024\n"
      "ds_read_b128 v[208:211], v149 offset:2048\n"
      "ds_read_b128 v[212:215], v149 offset:3072\n"
      "v_min_u32 v148, 111296, v149\n"
      "ds_read_b128 v[216:219], v148 offset:4096\n"
      "s_waitcnt lgkmcnt(2)\n"
      "v_or3_b32 v194, v194, v195, v196\n"
      "v_or3_b32 v194, v194, v197, v198\n"
      "v_or3_b32 v194, v194, v199, v200\n"
      "v_or3_b32 v194, v194, v201, v208\n"
      "v_or3_b32 v194, v194, v209, v210\n"
      "s_waitcnt lgkmcnt(0)\n"
      "v_or3_b32 v194, v194, v211, v212\n"
      "v_or3_b32 v194, v194, v213, v214\n"
      "v_or3_b32 v194, v194, v215, v216\n"
      "v_or3_b32 v194, v194, v217, v218\n"
      "v_bitop3_b32 v194, v194, v219, s40 bitop3:0xa8\n"
      "v_cmp_ne_u32 s[48:49], 0, v194\n"
      "s_cmp_lg_u64 s[48:49], 0\n"
      "s_cbranch_scc1 L_gen_8_%=\n"
      "s_cmp_eq_u64 s[50:51], 0\n"
      "s_cbranch_scc1 L_fast_10_%=\n"
      "L_go_13_%=:\n"
      "v_mfma_f32_16x16x32_bf16 v[148:151], v[228:231], v[152:155], v[90:93]\n"
      "ds_read_b128 v[194:197], v119 offset:30720\n"
      "v_max3_f32 v198, v164, v165, v166\n"
      "v_mfma_f32_16x16x32_bf16 v[148:151], v[34:37], v[38:41], v[148:151]\n"
      "ds_read_b128 v[200:203], v120 offset:18432\n"
      "v_max3_f32 v199, v167, v156, v157\n"
      "v_mfma_f32_16x16x32_bf16 v[208:211], v[160:163], v[152:155], v[94:97]\n"
      "ds_read_b128 v[212:215], v121 offset:18432\n"
      "v_max3_f32 v198, v198, v158, v159\n"
      "v_mfma_f32_16x16x32_bf16 v[208:211], v[40:43], v[38:41], v[208:211]\n"
      "v_max3_f32 v199, v199, v184, v185\n"
      "v_max3_f32 v198, v198, v186, v187\n"
      "v_max3_f32 v198, v198, v199, v192\n"
      "v_mov_b32 v199, v198\n"
      "s_nop 1\n"
      "v_permlane16_swap_b32 v198, v199\n"
      "v_max_f32 v198, v198, v199\n"
      "v_mov_b32 v199, v198\n"
      "s_nop 1\n"
      "v_permlane32_swap_b32 v198, v199\n"
      "v_max_f32 v198, v198, v199\n"
      "v_sub_f32 v164, v164, v198\n"
      "v_sub_f32 v165, v165, v198\n"
      "s_waitcnt lgkmcnt(1)\n"
      "v_sub_f32 v166, v166, v198\n"
      "global_store_dwordx4 v116, v[194:197], s[54:55] offset:256 sc1\n"
      "v_sub_f32 v167, v167, v198\n"
      "v_sub_f32 v156, v156, v198\n"
      "global_store_dwordx4 v117, v[200:203], s[54:55] sc1\n"
      "v_sub_f32 v157, v157, v198\n"
      "v_sub_f32 v158, v158, v198\n"
      "s_waitcnt lgkmcnt(0)\n"
      "v_sub_f32 v159, v159, v198\n"
      "s_mov_b32 exec_hi, 0\n"
      "global_store_dwordx4 v118, v[212:215], s[54:55] sc1\n"
      "s_mov_b32 exec_hi, -1\n"
      "s_nop 0\n"
      "v_mfma_f32_16x16x32_bf16 v[212:215], v[168:171], v[152:155], v[72:75]\n"
      "v_sub_f32 v184, v184, v198\n"
      "v_sub_f32 v185, v185, v198\n"
      "v_mfma_f32_16x16x32_bf16 v[212:215], v[224:227], v[160:163], v[212:215]\n"
      "v_sub_f32 v186, v186, v198\n"
      "v_sub_f32 v187, v187, v198\n"
      "v_mfma_f32_16x16x32_bf16 v[212:215], v[240:243], v[38:41], v[212:215]\n"
      "v_sub_f32 v192, v192, v198\n"
      "v_exp_f32 v164, v164\n"
      "v_mfma_f32_16x16x32_bf16 v[198:201], v[204:207], v[152:155], v[98:101]\n"
      "v_exp_f32 v165, v165\n"
      "v_mfma_f32_16x16x32_bf16 v[198:201], v[46:49], v[38:41], v[198:201]\n"
      "v_exp_f32_e64 v166, v166\n"
      "v_cvt_pk_bf16_f32 v164, v164, v165\n"
      "v_exp_f32 v167, v167\n"
      "v_exp_f32 v156, v156\n"
      "v_cvt_pk_bf16_f32 v165, v166, v167\n"
      "v_exp_f32 v157, v157\n"
      "v_exp_f32 v158, v158\n"
      "v_cvt_pk_bf16_f32 v166, v156, v157\n"
      "v_exp_f32 v159, v159\n"
      "v_exp_f32 v184, v184\n"
      "v_cvt_pk_bf16_f32 v167, v158, v159\n"
      "v_exp_f32 v185, v185\n"
      "v_exp_f32 v186, v186\n"
      "v_exp_f32 v187, v187\n"
      "v_exp_f32 v192, v192\n"
      "v_cvt_pk_bf16_f32 v62, v184, v185\n"
      "v_cvt_pk_bf16_f32 v63, v186, v187\n"
      "v_cvt_pk_bf16_f32 v64, v192, 0\n"
      "v_max3_f32 v152, v148, v149, v150\n"
      "v_max3_f32 v153, v151, v208, v209\n"
      "v_mfma_f32_16x16x32_bf16 v[154:157], v[66:69], v[164:167], 0\n"
      "v_max3_f32 v152, v152, v210, v211\n"
      "v_max3_f32 v153, v153, v198, v199\n"
      "v_mfma_f32_16x16x32_bf16 v[154:157], v[66:69], v[62:65], v[154:157]\n"
      "v_max3_f32 v152, v152, v200, v201\n"
      "v_max3_f32 v152, v152, v153, v212\n"
      "s_nop 5\n"
      "v_mfma_f32_16x16x32_bf16 v[156:159], v[172:175], v[164:167], 0\n"
      "v_mov_b32 v153, v152\n"
      "v_mfma_f32_16x16x32_bf16 v[156:159], v[50:53], v[62:65], v[156:159]\n"
      "s_nop 0\n"
      "v_permlane16_swap_b32 v152, v153\n"
      "v_mfma_f32_16x16x32_bf16 v[184:187], v[176:179], v[164:167], 0\n"
      "v_max_f32 v152, v152, v153\n"
      "v_mov_b32 v153, v152\n"
      "v_mfma_f32_16x16x32_bf16 v[184:187], v[54:57], v[62:65], v[184:187]\n"
      "s_nop 0\n"
      "v_permlane32_swap_b32 v152, v153\n"
      "v_mfma_f32_16x16x32_bf16 v[194:197], v[180:183], v[164:167], 0\n"
      "v_max_f32 v152, v152, v153\n"
      "v_sub_f32 v148, v148, v152\n"
      "v_mfma_f32_16x16x32_bf16 v[194:197], v[58:61], v[62:65], v[194:197]\n"
      "v_sub_f32 v149, v149, v152\n"
      "v_sub_f32 v150, v150, v152\n"
      "v_mfma_f32_16x16x32_bf16 v[164:167], v[228:231], v[188:191], v[102:105]\n"
      "v_sub_f32 v151, v151, v152\n"
      "v_sub_f32 v208, v208, v152\n"
      "v_mfma_f32_16x16x32_bf16 v[164:167], v[34:37], v[44:47], v[164:167]\n"
      "v_sub_f32 v209, v209, v152\n"
      "v_sub_f32 v210, v210, v152\n"
      "v_mfma_f32_16x16x32_bf16 v[228:231], v[160:163], v[188:191], v[106:109]\n"
      "v_sub_f32 v211, v211, v152\n"
      "v_sub_f32 v198, v198, v152\n"
      "v_mfma_f32_16x16x32_bf16 v[228:231], v[40:43], v[44:47], v[228:231]\n"
      "v_sub_f32 v199, v199, v152\n"
      "v_sub_f32 v200, v200, v152\n"
      "v_sub_f32 v201, v201, v152\n"
      "v_sub_f32 v212, v212, v152\n"
      "v_exp_f32 v148, v148\n"
      "v_exp_f32 v149, v149\n"
      "v_exp_f32 v150, v150\n"
      "v_cvt_pk_bf16_f32 v148, v148, v149\n"
      "v_exp_f32 v151, v151\n"
      "v_mfma_f32_16x16x32_bf16 v[160:163], v[168:171], v[188:191], v[74:77]\n"
      "v_exp_f32 v208, v208\n"
      "v_mfma_f32_16x16x32_bf16 v[160:163], v[224:227], v[204:207], v[160:163]\n"
      "v_cvt_pk_bf16_f32 v149, v150, v151\n"
      "v_exp_f32 v209, v209\n"
      "v_mfma_f32_16x16x32_bf16 v[160:163], v[240:243], v[44:47], v[160:163]\n"
      "v_exp_f32 v210, v210\n"
      "v_cvt_pk_bf16_f32 v150, v208, v209\n"
      "v_exp_f32 v211, v211\n"
      "v_mfma_f32_16x16x32_bf16 v[214:217], v[204:207], v[188:191], v[110:113]\n"
      "v_exp_f32 v198, v198\n"
      "v_mfma_f32_16x16x32_bf16 v[214:217], v[46:49], v[44:47], v[214:217]\n"
      "v_cvt_pk_bf16_f32 v151, v210, v211\n"
      "v_exp_f32 v199, v199\n"
      "v_mfma_f32_16x16x32_bf16 v[188:191], v[224:227], v[168:171], v[76:79]\n"
      "v_exp_f32 v200, v200\n"
      "v_exp_f32 v201, v201\n"
      "v_exp_f32 v212, v212\n"
      "v_cvt_pk_bf16_f32 v62, v198, v199\n"
      "v_cvt_pk_bf16_f32 v63, v200, v201\n"
      "v_cvt_pk_bf16_f32 v64, v212, 0\n"
      "v_mfma_f32_16x16x16_bf16 v[188:191], v[242:243], v[240:241], v[188:191]\n"
      "v_rcp_f32 v154, v154\n"
      "v_mfma_f32_16x16x32_bf16 v[168:171], v[66:69], v[148:151], 0\n"
      "v_mul_f32 v156, v154, v156\n"
      "v_mul_f32 v157, v154, v157\n"
      "v_mfma_f32_16x16x32_bf16 v[168:171], v[66:69], v[62:65], v[168:171]\n"
      "v_cvt_pk_bf16_f32 v156, v156, v157\n"
      "v_mul_f32 v158, v154, v158\n"
      "v_mfma_f32_16x16x32_bf16 v[240:243], v[172:175], v[148:151], 0\n"
      "v_mul_f32 v159, v154, v159\n"
      "v_cvt_pk_bf16_f32 v157, v158, v159\n"
      "v_mfma_f32_16x16x32_bf16 v[240:243], v[50:53], v[62:65], v[240:243]\n"
      "v_mul_f32 v184, v154, v184\n"
      "v_mul_f32 v185, v154, v185\n"
      "v_mfma_f32_16x16x32_bf16 v[198:201], v[176:179], v[148:151], 0\n"
      "v_cvt_pk_bf16_f32 v158, v184, v185\n"
      "v_mul_f32 v186, v154, v186\n"
      "v_mfma_f32_16x16x32_bf16 v[198:201], v[54:57], v[62:65], v[198:201]\n"
      "v_mul_f32 v187, v154, v187\n"
      "v_cvt_pk_bf16_f32 v159, v186, v187\n"
      "v_mfma_f32_16x16x32_bf16 v[184:187], v[180:183], v[148:151], 0\n"
      "v_mul_f32 v194, v154, v194\n"
      "v_mul_f32 v195, v154, v195\n"
      "v_mfma_f32_16x16x32_bf16 v[184:187], v[58:61], v[62:65], v[184:187]\n"
      "ds_write_b128 v124, v[156:159]\n"
      "v_cvt_pk_bf16_f32 v194, v194, v195\n"
      "v_mul_f32 v196, v154, v196\n"
      "v_mul_f32 v197, v154, v197\n"
      "v_cvt_pk_bf16_f32 v195, v196, v197\n"
      "v_max3_f32 v148, v164, v165, v166\n"
      "v_max3_f32 v149, v167, v228, v229\n"
      "v_max3_f32 v148, v148, v230, v231\n"
      "v_max3_f32 v149, v149, v214, v215\n"
      "v_max3_f32 v148, v148, v216, v217\n"
      "v_max3_f32 v148, v148, v149, v160\n"
      "v_mov_b32 v149, v148\n"
      "s_nop 1\n"
      "v_permlane16_swap_b32 v148, v149\n"
      "v_max_f32 v148, v148, v149\n"
      "v_mov_b32 v149, v148\n"
      "s_nop 1\n"
      "v_permlane32_swap_b32 v148, v149\n"
      "v_max_f32 v148, v148, v149\n"
      "v_sub_f32 v164, v164, v148\n"
      "v_sub_f32 v165, v165, v148\n"
      "v_sub_f32 v166, v166, v148\n"
      "v_sub_f32 v167, v167, v148\n"
      "v_sub_f32 v228, v228, v148\n"
      "v_sub_f32 v229, v229, v148\n"
      "v_sub_f32 v230, v230, v148\n"
      "v_sub_f32 v231, v231, v148\n"
      "v_sub_f32 v214, v214, v148\n"
      "v_sub_f32 v215, v215, v148\n"
      "v_sub_f32 v216, v216, v148\n"
      "v_sub_f32 v217, v217, v148\n"
      "ds_write_b64 v125, v[194:195]\n"
      "v_sub_f32 v160, v160, v148\n"
      "v_exp_f32 v164, v164\n"
      "v_exp_f32 v165, v165\n"
      "v_exp_f32 v166, v166\n"
      "v_cvt_pk_bf16_f32 v164, v164, v165\n"
      "v_exp_f32 v167, v167\n"
      "v_exp_f32 v228, v228\n"
      "v_cvt_pk_bf16_f32 v165, v166, v167\n"
      "v_exp_f32 v229, v229\n"
      "v_exp_f32 v230, v230\n"
      "v_cvt_pk_bf16_f32 v166, v228, v229\n"
      "v_exp_f32 v231, v231\n"
      "v_exp_f32 v214, v214\n"
      "v_cvt_pk_bf16_f32 v167, v230, v231\n"
      "v_exp_f32 v215, v215\n"
      "v_exp_f32 v216, v216\n"
      "v_exp_f32 v217, v217\n"
      "v_exp_f32 v160, v160\n"
      "v_cvt_pk_bf16_f32 v62, v214, v215\n"
      "v_cvt_pk_bf16_f32 v63, v216, v217\n"
      "v_cvt_pk_bf16_f32 v64, v160, 0\n"
      "v_rcp_f32_e64 v168, v168\n"
      "v_mfma_f32_16x16x32_bf16 v[214:217], v[66:69], v[164:167], 0\n"
      "ds_read_b128 v[228:231], v123 offset:18688\n"
      "v_mul_f32 v240, v168, v240\n"
      "v_mfma_f32_16x16x32_bf16 v[214:217], v[66:69], v[62:65], v[214:217]\n"
      "ds_read_b128 v[194:197], v123 offset:21760\n"
      "v_mul_f32 v241, v168, v241\n"
      "v_mfma_f32_16x16x32_bf16 v[224:227], v[172:175], v[164:167], 0\n"
      "s_add_u32 s28, s27, 2\n"
      "v_cvt_pk_bf16_f32 v240, v240, v241\n"
      "v_mul_f32 v242, v168, v242\n"
      "v_mfma_f32_16x16x32_bf16 v[224:227], v[50:53], v[62:65], v[224:227]\n"
      "ds_read_b128 v[208:211], v123 offset:24832\n"
      "v_mul_f32 v243, v168, v243\n"
      "v_mfma_f32_16x16x32_bf16 v[204:207], v[176:179], v[164:167], 0\n"
      "s_min_u32 m0, s28, s3\n"
      "v_cvt_pk_bf16_f32 v241, v242, v243\n"
      "v_mul_f32 v198, v168, v198\n"
      "v_mfma_f32_16x16x32_bf16 v[204:207], v[54:57], v[62:65], v[204:207]\n"
      "s_movrels_b32 s9, s80\n"
      "v_mul_f32_e64 v199, v168, v199\n"
      "v_cvt_pk_bf16_f32 v242, v198, v199\n"
      "v_mfma_f32_16x16x32_bf16 v[216:219], v[180:183], v[164:167], 0\n"
      "ds_read_b128 v[164:167], v123 offset:27904\n"
      "v_mul_f32 v200, v168, v200\n"
      "v_mfma_f32_16x16x32_bf16 v[216:219], v[58:61], v[62:65], v[216:219]\n"
      "s_lshr_b32 s8, s9, 8\n"
      "v_mul_f32 v201, v168, v201\n"
      "v_cvt_pk_bf16_f32 v243, v200, v201\n"
      "v_mul_f32 v184, v168, v184\n"
      "v_mul_f32 v185, v168, v185\n"
      "v_cvt_pk_bf16_f32 v184, v184, v185\n"
      "v_mul_f32 v186, v168, v186\n"
      "v_mul_f32 v187, v168, v187\n"
      "v_cvt_pk_bf16_f32 v185, v186, v187\n"
      "v_cndmask_b32 v193, v193, v213, s[42:43]\n"
      "v_cndmask_b32 v193, v193, v161, s[44:45]\n"
      "v_cndmask_b32 v193, v193, v189, s[46:47]\n"
      "s_and_b32 s9, s9, 0xff\n"
      "s_nop 0\n"
      "v_max_f32_dpp v186, v193, v193 row_ror:8 row_mask:0xf bank_mask:0xf\n"
      "s_waitcnt lgkmcnt(2)\n"
      "v_mfma_f32_16x16x32_bf16 v[148:151], a[216:219], v[228:231], 0\n"
      "s_mul_i32 s10, s8, 73\n"
      "v_max_f32_dpp v186, v186, v186 row_ror:4 row_mask:0xf bank_mask:0xf\n"
      "s_add_u32 s10, s10, s9\n"
      "v_mfma_f32_16x16x32_bf16 v[148:151], a[228:231], v[194:197], v[148:151]\n"
      "ds_write_b128 v124, v[240:243] offset:256\n"
      "v_mfma_f32_16x16x32_bf16 v[152:155], a[220:223], v[228:231], 0\n"
      "s_mul_i32 s28, s10, 4802\n"
      "v_max_f32_dpp v186, v186, v186 row_ror:2 row_mask:0xf bank_mask:0xf\n"
      "s_add_u32 s56, s18, s28\n"
      "v_mfma_f32_16x16x32_bf16 v[152:155], a[232:235], v[194:197], v[152:155]\n"
      "ds_write_b64 v125, v[184:185] offset:256\n"
      "s_waitcnt lgkmcnt(2)\n"
      "v_mfma_f32_16x16x32_bf16 v[156:159], a[224:227], v[228:231], 0\n"
      "ds_read_b128 v[160:163], v123 offset:30976\n"
      "v_max_f32_dpp v186, v186, v186 row_ror:1 row_mask:0xf bank_mask:0xf\n"
      "v_mfma_f32_16x16x32_bf16 v[156:159], a[236:239], v[194:197], v[156:159]\n"
      "ds_read_b128 v[168:171], v123 offset:34048\n"
      "v_max_f32_dpp v186, v186, v186 row_bcast:15 row_mask:0xa bank_mask:0xf\n"
      "v_mfma_f32_16x16x32_bf16 v[148:151], a[240:243], v[208:211], v[148:151]\n"
      "s_addc_u32 s57, s19, 0\n"
      "v_max_f32_dpp v186, v186, v186 row_bcast:31 row_mask:0xc bank_mask:0xf\n"
      "s_and_b32 s56, s56, -16\n"
      "v_mfma_f32_16x16x32_bf16 v[148:151], a[252:255], v[164:167], v[148:151]\n"
      "s_sub_u32 s58, s56, 110592\n"
      "v_readlane_b32 s31, v186, 63\n"
      "v_rcp_f32_e64 v214, v214\n"
      "v_mfma_f32_16x16x32_bf16 v[152:155], a[244:247], v[208:211], v[152:155]\n"
      "s_subb_u32 s59, s57, 0\n"
      "v_subrev_f32 v193, s31, v193\n"
      "v_mul_f32_e64 v224, v214, v224\n"
      "v_mfma_f32_16x16x32_bf16 v[152:155], v[0:3], v[164:167], v[152:155]\n"
      "s_sub_u32 s56, s56, 73728\n"
      "v_exp_f32 v193, v193\n"
      "v_mfma_f32_16x16x32_bf16 v[156:159], a[248:251], v[208:211], v[156:159]\n"
      "s_subb_u32 s57, s57, 0\n"
      "v_cvt_pk_bf16_f32 v193, v193, v193\n"
      "v_mul_f32 v225, v214, v225\n"
      "v_mfma_f32_16x16x32_bf16 v[156:159], v[4:7], v[164:167], v[156:159]\n"
      "ds_write_b16 v130, v193\n"
      "v_cvt_pk_bf16_f32 v224, v224, v225\n"
      "v_mul_f32 v226, v214, v226\n"
      "v_mul_f32 v227, v214, v227\n"
      "v_cvt_pk_bf16_f32 v225, v226, v227\n"
      "v_mul_f32 v204, v214, v204\n"
      "v_mul_f32 v205, v214, v205\n"
      "v_cvt_pk_bf16_f32 v226, v204, v205\n"
      "v_mul_f32 v206, v214, v206\n"
      "v_mul_f32 v207, v214, v207\n"
      "v_cvt_pk_bf16_f32 v227, v206, v207\n"
      "s_waitcnt lgkmcnt(1)\n"
      "v_mfma_f32_16x16x32_bf16 v[148:151], v[8:11], v[160:163], v[148:151]\n"
      "ds_read_b128 v[184:187], v131\n"
      "s_mul_i32 s28, s8, 1373568\n"
      "v_mfma_f32_16x16x32_bf16 v[148:151], v[20:23], v[168:171], v[148:151]\n"
      "ds_read_b128 v[188:191], v131 offset:64\n"
      "v_mul_f32_e64 v216, v214, v216\n"
      "v_mfma_f32_16x16x32_bf16 v[152:155], v[12:15], v[160:163], v[152:155]\n"
      "ds_read_b128 v[204:207], v123 offset:18944\n"
      "v_mul_f32_e64 v217, v214, v217\n"
      "v_mfma_f32_16x16x32_bf16 v[152:155], v[24:27], v[168:171], v[152:155]\n"
      "ds_read_b128 v[208:211], v123 offset:22016\n"
      "v_cvt_pk_bf16_f32 v216, v216, v217\n"
      "v_mfma_f32_16x16x32_bf16 v[156:159], v[16:19], v[160:163], v[156:159]\n"
      "ds_read_b128 v[194:197], v123 offset:25088\n"
      "v_mul_f32 v218, v214, v218\n"
      "v_mfma_f32_16x16x32_bf16 v[156:159], v[28:31], v[168:171], v[156:159]\n"
      "ds_write_b128 v124, v[224:227] offset:512\n"
      "v_mul_f32 v219, v214, v219\n"
      "v_cvt_pk_bf16_f32 v217, v218, v219\n"
      "v_fma_f32 v218, v148, v148, s39\n"
      "v_fmac_f32 v218, v149, v149\n"
      "v_fmac_f32 v218, v150, v150\n"
      "v_fmac_f32 v218, v151, v151\n"
      "v_fmac_f32 v218, v152, v152\n"
      "v_fmac_f32_e64 v218, v153, v153\n"
      "s_mul_i32 s29, s9, 2688\n"
      "ds_write_b64 v125, v[216:217] offset:512\n"
      "v_fmac_f32 v218, v154, v154\n"
      "s_add_u32 s28, s28, s29\n"
      "ds_read_b128 v[224:227], v123 offset:28160\n"
      "ds_read_b128 v[214:217], v123 offset:31232\n"
      "ds_read_b128 v[228:231], v123 offset:34304\n"
      "s_cmp_eq_u32 s8, 72\n"
      "s_cselect_b32 s8, 0, 100270464\n"
      "s_cmp_eq_u32 s9, 72\n"
      "s_waitcnt lgkmcnt(5)\n"
      "v_mfma_f32_16x16x32_bf16 v[160:163], a[216:219], v[204:207], 0\n"
      "s_cselect_b32 s9, 0, 196224\n"
      "v_fmac_f32 v218, v155, v155\n"
      "s_add_u32 s29, s8, s9\n"
      "v_mfma_f32_16x16x32_bf16 v[160:163], a[228:231], v[208:211], v[160:163]\n"
      "s_add_u32 s28, s28, s29\n"
      "v_fmac_f32 v218, v156, v156\n"
      "v_mfma_f32_16x16x32_bf16 v[164:167], a[220:223], v[204:207], 0\n"
      "s_add_u32 s52, s16, s28\n"
      "s_addc_u32 s53, s17, 0\n"
      "s_cmp_lg_u32 s29, s11\n"
      "v_mfma_f32_16x16x32_bf16 v[164:167], a[232:235], v[208:211], v[164:167]\n"
      "s_cbranch_scc1 L_edge_14_%=\n"
      "L_eb_15_%=:\n"
      "v_mfma_f32_16x16x32_bf16 v[168:171], a[224:227], v[204:207], 0\n"
      "v_fmac_f32 v218, v157, v157\n"
      "v_mfma_f32_16x16x32_bf16 v[168:171], a[236:239], v[208:211], v[168:171]\n"
      "v_fmac_f32 v218, v158, v158\n"
      "s_waitcnt lgkmcnt(0)\n"
      "v_mfma_f32_16x16x32_bf16 v[160:163], a[240:243], v[194:197], v[160:163]\n"
      "v_fmac_f32_e64 v218, v159, v159\n"
      "v_mfma_f32_16x16x32_bf16 v[160:163], a[252:255], v[224:227], v[160:163]\n"
      "v_mfma_f32_16x16x32_bf16 v[164:167], a[244:247], v[194:197], v[164:167]\n"
      "v_mfma_f32_16x16x32_bf16 v[164:167], v[0:3], v[224:227], v[164:167]\n"
      "v_mfma_f32_16x16x32_bf16 v[168:171], a[248:251], v[194:197], v[168:171]\n"
      "v_mfma_f32_16x16x32_bf16 v[168:171], v[4:7], v[224:227], v[168:171]\n"
      "v_permlane16_swap_b32 v252, v218\n"
      "v_mfma_f32_16x16x32_bf16 v[160:163], v[8:11], v[214:217], v[160:163]\n"
      "v_add_f32 v252, v252, v218\n"
      "v_mfma_f32_16x16x32_bf16 v[160:163], v[20:23], v[228:231], v[160:163]\n"
      "v_mfma_f32_16x16x32_bf16 v[164:167], v[12:15], v[214:217], v[164:167]\n"
      "v_mfma_f32_16x16x32_bf16 v[164:167], v[24:27], v[228:231], v[164:167]\n"
      "v_mfma_f32_16x16x32_bf16 v[168:171], v[16:19], v[214:217], v[168:171]\n"
      "v_mfma_f32_16x16x32_bf16 v[168:171], v[28:31], v[228:231], v[168:171]\n"
      "s_nop 3\n"
      "v_fma_f32 v193, v160, v160, s39\n"
      "v_fmac_f32 v193, v161, v161\n"
      "v_fmac_f32 v193, v162, v162\n"
      "v_fmac_f32 v193, v163, v163\n"
      "v_fmac_f32 v193, v164, v164\n"
      "v_fmac_f32 v193, v165, v165\n"
      "v_fmac_f32 v193, v166, v166\n"
      "v_fmac_f32 v193, v167, v167\n"
      "v_fmac_f32 v193, v168, v168\n"
      "v_fmac_f32 v193, v169, v169\n"
      "v_fmac_f32 v193, v170, v170\n"
      "v_fmac_f32 v193, v171, v171\n"
      "v_mov_b32 v240, v193\n"
      "s_nop 1\n"
      "v_permlane16_swap_b32 v193, v240\n"
      "v_add_f32 v193, v193, v240\n"
      "s_nop 1\n"
      "v_permlane32_swap_b32 v252, v193\n"
      "v_add_f32_e64 v252, v252, v193\n"
      "ds_write_b32 v129, v252\n"
      "s_branch L_join_9_%=\n"
      "L_gen_8_%=:\n"
      "s_waitcnt lgkmcnt(0)\n"
      "s_nop 7\n"
      "s_nop 7\n"
      "v_mfma_f32_16x16x32_bf16 v[148:151], v[228:231], v[152:155], v[90:93]\n"
      "ds_read_b128 v[194:197], v119 offset:30720\n"
      "v_mbcnt_lo_u32_b32 v198, -1, 0\n"
      "v_mfma_f32_16x16x32_bf16 v[148:151], v[34:37], v[38:41], v[148:151]\n"
      "ds_read_b128 v[200:203], v120 offset:18432\n"
      "v_mbcnt_hi_u32_b32 v198, -1, v198\n"
      "v_and_b32 v199, 15, v198\n"
      "v_mul_u32_u24 v199, 0x62, v199\n"
      "v_add_u32 v199, s30, v199\n"
      "v_lshrrev_b32_e64 v198, 4, v198\n"
      "v_lshl_add_u32 v198, v198, 3, v199\n"
      "ds_read_b128 v[208:211], v121 offset:18432\n"
      "ds_read_u16 v212, v198\n"
      "ds_read_u16 v213, v198 offset:2\n"
      "ds_read_u16 v214, v198 offset:4\n"
      "ds_read_u16 v215, v198 offset:6\n"
      "ds_read_u16 v216, v198 offset:32\n"
      "ds_read_u16 v217, v198 offset:34\n"
      "ds_read_u16 v218, v198 offset:36\n"
      "ds_read_u16 v219, v198 offset:38\n"
      "ds_read_u16 v220, v198 offset:64\n"
      "ds_read_u16 v221, v198 offset:66\n"
      "ds_read_u16 v222, v198 offset:68\n"
      "ds_read_u16 v223, v198 offset:70\n"
      "s_waitcnt lgkmcnt(13)\n"
      "global_store_dwordx4 v116, v[194:197], s[54:55] offset:256 sc1\n"
      "global_store_dwordx4 v117, v[200:203], s[54:55] sc1\n"
      "s_nop 1\n"
      "ds_read_u16 v200, v199 offset:96\n"
      "s_waitcnt lgkmcnt(11)\n"
      "v_lshlrev_b32 v212, 16, v212\n"
      "v_fmac_f32 v164, s37, v212\n"
      "v_lshlrev_b32 v213, 16, v213\n"
      "v_fmac_f32 v165, s37, v213\n"
      "s_waitcnt lgkmcnt(7)\n"
      "v_lshlrev_b32 v214, 16, v214\n"
      "v_fmac_f32_e64 v166, s37, v214\n"
      "s_mov_b32 exec_hi, 0\n"
      "global_store_dwordx4 v118, v[208:211], s[54:55] sc1\n"
      "s_mov_b32 exec_hi, -1\n"
      "s_nop 0\n"
      "v_mfma_f32_16x16x32_bf16 v[208:211], v[160:163], v[152:155], v[94:97]\n"
      "v_lshlrev_b32 v215, 16, v215\n"
      "v_fmac_f32 v167, s37, v215\n"
      "v_mfma_f32_16x16x32_bf16 v[208:211], v[40:43], v[38:41], v[208:211]\n"
      "v_lshlrev_b32 v216, 16, v216\n"
      "v_fmac_f32 v156, s37, v216\n"
      "v_lshlrev_b32 v217, 16, v217\n"
      "v_fmac_f32 v157, s37, v217\n"
      "v_max3_f32 v217, v164, v165, v166\n"
      "v_max3_f32 v216, v167, v156, v157\n"
      "s_waitcnt lgkmcnt(1)\n"
      "v_lshlrev_b32 v218, 16, v218\n"
      "v_mfma_f32_16x16x32_bf16 v[212:215], v[168:171], v[152:155], v[72:75]\n"
      "v_fmac_f32 v158, s37, v218\n"
      "v_lshlrev_b32 v219, 16, v219\n"
      "v_mfma_f32_16x16x32_bf16 v[212:215], v[224:227], v[160:163], v[212:215]\n"
      "v_fmac_f32 v159, s37, v219\n"
      "v_lshlrev_b32 v220, 16, v220\n"
      "v_fmac_f32 v184, s37, v220\n"
      "v_max3_f32 v217, v217, v158, v159\n"
      "v_lshlrev_b32 v221, 16, v221\n"
      "v_fmac_f32 v185, s37, v221\n"
      "v_max3_f32 v216, v216, v184, v185\n"
      "v_lshlrev_b32 v222, 16, v222\n"
      "v_fmac_f32 v186, s37, v222\n"
      "v_lshlrev_b32 v223, 16, v223\n"
      "v_fmac_f32 v187, s37, v223\n"
      "v_max3_f32 v217, v217, v186, v187\n"
      "s_waitcnt lgkmcnt(0)\n"
      "v_lshlrev_b32 v200, 16, v200\n"
      "v_fmac_f32 v192, s37, v200\n"
      "v_mfma_f32_16x16x32_bf16 v[212:215], v[240:243], v[38:41], v[212:215]\n"
      "v_max3_f32 v217, v217, v216, v192\n"
      "v_mov_b32 v216, v217\n"
      "s_nop 1\n"
      "v_permlane16_swap_b32_e64 v217, v216\n"
      "v_mfma_f32_16x16x32_bf16 v[220:223], v[204:207], v[152:155], v[98:101]\n"
      "v_max_f32 v217, v217, v216\n"
      "v_mov_b32 v152, v217\n"
      "v_mfma_f32_16x16x32_bf16 v[220:223], v[46:49], v[38:41], v[220:223]\n"
      "s_nop 0\n"
      "v_permlane32_swap_b32 v217, v152\n"
      "v_max_f32 v217, v217, v152\n"
      "v_sub_f32 v164, v164, v217\n"
      "v_sub_f32 v165, v165, v217\n"
      "v_sub_f32 v166, v166, v217\n"
      "v_sub_f32 v167, v167, v217\n"
      "v_sub_f32 v156, v156, v217\n"
      "v_sub_f32 v157, v157, v217\n"
      "v_sub_f32 v158, v158, v217\n"
      "v_sub_f32 v159, v159, v217\n"
      "v_sub_f32 v184, v184, v217\n"
      "v_sub_f32 v185, v185, v217\n"
      "v_sub_f32 v186, v186, v217\n"
      "v_sub_f32 v187, v187, v217\n"
      "v_sub_f32 v192, v192, v217\n"
      "v_exp_f32 v164, v164\n"
      "v_exp_f32 v165, v165\n"
      "v_exp_f32 v166, v166\n"
      "v_cvt_pk_bf16_f32 v164, v164, v165\n"
      "v_exp_f32 v167, v167\n"
      "v_exp_f32 v156, v156\n"
      "v_cvt_pk_bf16_f32 v165, v166, v167\n"
      "v_exp_f32 v157, v157\n"
      "v_exp_f32 v158, v158\n"
      "v_cvt_pk_bf16_f32 v166, v156, v157\n"
      "v_exp_f32 v159, v159\n"
      "v_exp_f32 v184, v184\n"
      "v_cvt_pk_bf16_f32 v167, v158, v159\n"
      "v_exp_f32 v185, v185\n"
      "v_exp_f32 v186, v186\n"
      "v_exp_f32 v187, v187\n"
      "v_exp_f32 v192, v192\n"
      "v_cvt_pk_bf16_f32 v62, v184, v185\n"
      "v_cvt_pk_bf16_f32 v63, v186, v187\n"
      "v_cvt_pk_bf16_f32 v64, v192, 0\n"
      "v_mfma_f32_16x16x32_bf16 v[156:159], v[66:69], v[164:167], 0\n"
      "ds_read_u16 v152, v198 offset:1568\n"
      "v_mfma_f32_16x16x32_bf16 v[156:159], v[66:69], v[62:65], v[156:159]\n"
      "s_nop 7\n"
      "ds_read_u16 v157, v198 offset:1570\n"
      "v_mfma_f32_16x16x32_bf16 v[184:187], v[172:175], v[164:167], 0\n"
      "ds_read_u16 v158, v198 offset:1572\n"
      "v_mfma_f32_16x16x32_bf16 v[184:187], v[50:53], v[62:65], v[184:187]\n"
      "ds_read_u16 v159, v198 offset:1574\n"
      "ds_read_u16 v153, v198 offset:1600\n"
      "ds_read_u16 v154, v198 offset:1602\n"
      "ds_read_u16 v155, v198 offset:1604\n"
      "ds_read_u16 v192, v198 offset:1606\n"
      "ds_read_u16 v217, v198 offset:1632\n"
      "ds_read_u16 v216, v198 offset:1634\n"
      "ds_read_u16 v214, v198 offset:1636\n"
      "ds_read_u16 v215, v198 offset:1638\n"
      "ds_read_u16 v200, v199 offset:1664\n"
      "s_waitcnt lgkmcnt(11)\n"
      "v_lshlrev_b32 v152, 16, v152\n"
      "v_fmac_f32 v148, s37, v152\n"
      "v_lshlrev_b32 v157, 16, v157\n"
      "v_fmac_f32 v149, s37, v157\n"
      "s_waitcnt lgkmcnt(9)\n"
      "v_lshlrev_b32 v158, 16, v158\n"
      "v_fmac_f32 v150, s37, v158\n"
      "v_lshlrev_b32 v159, 16, v159\n"
      "v_mfma_f32_16x16x32_bf16 v[194:197], v[176:179], v[164:167], 0\n"
      "s_waitcnt lgkmcnt(6)\n"
      "v_fmac_f32 v151, s37, v159\n"
      "v_lshlrev_b32 v153, 16, v153\n"
      "v_mfma_f32_16x16x32_bf16 v[194:197], v[54:57], v[62:65], v[194:197]\n"
      "v_fmac_f32 v208, s37, v153\n"
      "v_lshlrev_b32 v154, 16, v154\n"
      "v_fmac_f32 v209, s37, v154\n"
      "v_lshlrev_b32 v155, 16, v155\n"
      "v_fmac_f32 v210, s37, v155\n"
      "s_waitcnt lgkmcnt(0)\n"
      "v_lshlrev_b32 v192, 16, v192\n"
      "v_fmac_f32 v211, s37, v192\n"
      "v_mfma_f32_16x16x32_bf16 v[152:155], v[180:183], v[164:167], 0\n"
      "v_lshlrev_b32 v217, 16, v217\n"
      "v_fmac_f32 v220, s37, v217\n"
      "v_mfma_f32_16x16x32_bf16 v[152:155], v[58:61], v[62:65], v[152:155]\n"
      "v_lshlrev_b32 v216, 16, v216\n"
      "v_fmac_f32 v221, s37, v216\n"
      "v_max3_f32 v159, v148, v149, v150\n"
      "v_max3_f32 v159, v159, v210, v211\n"
      "v_lshlrev_b32 v214, 16, v214\n"
      "v_fmac_f32 v222, s37, v214\n"
      "v_lshlrev_b32 v215, 16, v215\n"
      "v_fmac_f32 v223, s37, v215\n"
      "v_max3_f32 v159, v159, v222, v223\n"
      "v_lshlrev_b32 v200, 16, v200\n"
      "v_fmac_f32 v212, s37, v200\n"
      "v_mfma_f32_16x16x32_bf16 v[164:167], v[228:231], v[188:191], v[102:105]\n"
      "v_max3_f32 v158, v151, v208, v209\n"
      "v_max3_f32 v158, v158, v220, v221\n"
      "v_mfma_f32_16x16x32_bf16 v[164:167], v[34:37], v[44:47], v[164:167]\n"
      "v_max3_f32 v159, v159, v158, v212\n"
      "v_mov_b32 v158, v159\n"
      "s_nop 1\n"
      "v_permlane16_swap_b32 v159, v158\n"
      "v_max_f32 v159, v159, v158\n"
      "v_mov_b32_e64 v158, v159\n"
      "v_mfma_f32_16x16x32_bf16 v[228:231], v[160:163], v[188:191], v[106:109]\n"
      "s_nop 0\n"
      "v_permlane32_swap_b32 v159, v158\n"
      "v_mfma_f32_16x16x32_bf16 v[228:231], v[40:43], v[44:47], v[228:231]\n"
      "v_max_f32 v159, v159, v158\n"
      "v_sub_f32 v148, v148, v159\n"
      "v_sub_f32 v149, v149, v159\n"
      "v_sub_f32 v150, v150, v159\n"
      "v_sub_f32 v151, v151, v159\n"
      "v_sub_f32 v208, v208, v159\n"
      "v_sub_f32 v209, v209, v159\n"
      "v_sub_f32 v210, v210, v159\n"
      "v_sub_f32_e64 v211, v211, v159\n"
      "v_mfma_f32_16x16x32_bf16 v[160:163], v[168:171], v[188:191], v[74:77]\n"
      "v_sub_f32 v220, v220, v159\n"
      "v_sub_f32 v221, v221, v159\n"
      "v_mfma_f32_16x16x32_bf16 v[160:163], v[224:227], v[204:207], v[160:163]\n"
      "v_sub_f32 v222, v222, v159\n"
      "v_sub_f32 v223, v223, v159\n"
      "v_mfma_f32_16x16x32_bf16 v[160:163], v[240:243], v[44:47], v[160:163]\n"
      "v_sub_f32 v212, v212, v159\n"
      "v_exp_f32 v148, v148\n"
      "v_exp_f32 v149, v149\n"
      "v_exp_f32 v150, v150\n"
      "v_cvt_pk_bf16_f32 v148, v148, v149\n"
      "v_exp_f32 v151, v151\n"
      "v_mfma_f32_16x16x32_bf16 v[214:217], v[204:207], v[188:191], v[110:113]\n"
      "v_exp_f32 v208, v208\n"
      "v_mfma_f32_16x16x32_bf16 v[214:217], v[46:49], v[44:47], v[214:217]\n"
      "v_cvt_pk_bf16_f32 v149, v150, v151\n"
      "v_exp_f32 v209, v209\n"
      "v_exp_f32 v210, v210\n"
      "v_cvt_pk_bf16_f32 v150, v208, v209\n"
      "v_exp_f32 v211, v211\n"
      "v_mfma_f32_16x16x32_bf16 v[188:191], v[224:227], v[168:171], v[76:79]\n"
      "v_exp_f32 v220, v220\n"
      "v_cvt_pk_bf16_f32 v151, v210, v211\n"
      "v_exp_f32 v221, v221\n"
      "v_exp_f32 v222, v222\n"
      "v_exp_f32_e64 v223, v223\n"
      "v_cvt_pk_bf16_f32 v62, v220, v221\n"
      "v_mfma_f32_16x16x16_bf16 v[188:191], v[242:243], v[240:241], v[188:191]\n"
      "v_exp_f32 v212, v212\n"
      "v_cvt_pk_bf16_f32 v63, v222, v223\n"
      "v_cvt_pk_bf16_f32 v64, v212, 0\n"
      "v_rcp_f32 v156, v156\n"
      "v_mfma_f32_16x16x32_bf16 v[168:171], v[66:69], v[148:151], 0\n"
      "v_mul_f32 v184, v156, v184\n"
      "v_mul_f32 v185, v156, v185\n"
      "v_mfma_f32_16x16x32_bf16 v[168:171], v[66:69], v[62:65], v[168:171]\n"
      "ds_read_u16 v159, v198 offset:3136\n"
      "v_cvt_pk_bf16_f32 v184, v184, v185\n"
      "v_mfma_f32_16x16x32_bf16 v[220:223], v[172:175], v[148:151], 0\n"
      "ds_read_u16 v158, v198 offset:3138\n"
      "v_mul_f32_e64 v186, v156, v186\n"
      "v_mfma_f32_16x16x32_bf16 v[220:223], v[50:53], v[62:65], v[220:223]\n"
      "ds_read_u16 v157, v198 offset:3140\n"
      "v_mul_f32_e64 v187, v156, v187\n"
      "v_mfma_f32_16x16x32_bf16 v[240:243], v[176:179], v[148:151], 0\n"
      "ds_read_u16 v188, v198 offset:3142\n"
      "v_cvt_pk_bf16_f32 v185, v186, v187\n"
      "v_mfma_f32_16x16x32_bf16 v[240:243], v[54:57], v[62:65], v[240:243]\n"
      "ds_read_u16 v190, v198 offset:3168\n"
      "v_mul_f32 v194, v156, v194\n"
      "v_mul_f32 v195, v156, v195\n"
      "v_cvt_pk_bf16_f32 v186, v194, v195\n"
      "v_mul_f32 v196, v156, v196\n"
      "v_mul_f32 v197, v156, v197\n"
      "v_cvt_pk_bf16_f32 v187, v196, v197\n"
      "v_mfma_f32_16x16x32_bf16 v[194:197], v[180:183], v[148:151], 0\n"
      "ds_read_u16 v148, v198 offset:3170\n"
      "v_mul_f32_e64 v152, v156, v152\n"
      "v_mfma_f32_16x16x32_bf16 v[194:197], v[58:61], v[62:65], v[194:197]\n"
      "ds_write_b128 v124, v[184:187]\n"
      "v_mul_f32 v153, v156, v153\n"
      "v_cvt_pk_bf16_f32 v152, v152, v153\n"
      "v_mul_f32 v154, v156, v154\n"
      "v_mul_f32_e64 v155, v156, v155\n"
      "v_cvt_pk_bf16_f32 v153, v154, v155\n"
      "s_waitcnt lgkmcnt(5)\n"
      "v_lshlrev_b32 v159, 16, v159\n"
      "v_fmac_f32 v164, s37, v159\n"
      "v_lshlrev_b32 v158, 16, v158\n"
      "v_fmac_f32 v165, s37, v158\n"
      "s_waitcnt lgkmcnt(3)\n"
      "v_lshlrev_b32 v157, 16, v157\n"
      "v_fmac_f32 v166, s37, v157\n"
      "v_lshlrev_b32 v188, 16, v188\n"
      "ds_write_b64 v125, v[152:153]\n"
      "v_fmac_f32 v167, s37, v188\n"
      "s_waitcnt lgkmcnt(3)\n"
      "v_lshlrev_b32 v190, 16, v190\n"
      "v_fmac_f32_e64 v228, s37, v190\n"
      "ds_read_u16 v149, v198 offset:3172\n"
      "ds_read_u16 v150, v198 offset:3174\n"
      "ds_read_u16 v151, v198 offset:3200\n"
      "ds_read_u16 v152, v198 offset:3202\n"
      "ds_read_u16 v153, v198 offset:3204\n"
      "ds_read_u16 v154, v198 offset:3206\n"
      "ds_read_u16 v155, v199 offset:3232\n"
      "v_max3_f32 v157, v164, v165, v166\n"
      "s_waitcnt lgkmcnt(8)\n"
      "v_lshlrev_b32 v148, 16, v148\n"
      "v_fmac_f32 v229, s37, v148\n"
      "v_max3_f32 v148, v167, v228, v229\n"
      "s_waitcnt lgkmcnt(4)\n"
      "v_lshlrev_b32 v149, 16, v149\n"
      "v_fmac_f32 v230, s37, v149\n"
      "v_lshlrev_b32 v150, 16, v150\n"
      "v_fmac_f32 v231, s37, v150\n"
      "v_max3_f32 v157, v157, v230, v231\n"
      "v_lshlrev_b32 v151, 16, v151\n"
      "v_fmac_f32 v214, s37, v151\n"
      "s_waitcnt lgkmcnt(0)\n"
      "v_lshlrev_b32 v152, 16, v152\n"
      "v_fmac_f32 v215, s37, v152\n"
      "v_max3_f32 v148, v148, v214, v215\n"
      "v_lshlrev_b32 v153, 16, v153\n"
      "v_fmac_f32 v216, s37, v153\n"
      "v_lshlrev_b32 v154, 16, v154\n"
      "v_fmac_f32 v217, s37, v154\n"
      "v_max3_f32 v157, v157, v216, v217\n"
      "v_lshlrev_b32 v155, 16, v155\n"
      "v_fmac_f32 v160, s37, v155\n"
      "v_max3_f32 v157, v157, v148, v160\n"
      "v_mov_b32 v148, v157\n"
      "s_nop 1\n"
      "v_permlane16_swap_b32 v157, v148\n"
      "v_max_f32 v157, v157, v148\n"
      "v_mov_b32 v148, v157\n"
      "s_nop 1\n"
      "v_permlane32_swap_b32 v157, v148\n"
      "v_max_f32 v157, v157, v148\n"
      "v_sub_f32 v164, v164, v157\n"
      "v_sub_f32 v165, v165, v157\n"
      "v_sub_f32 v166, v166, v157\n"
      "v_sub_f32 v167, v167, v157\n"
      "v_sub_f32 v228, v228, v157\n"
      "v_sub_f32 v229, v229, v157\n"
      "v_sub_f32 v230, v230, v157\n"
      "v_sub_f32 v231, v231, v157\n"
      "v_sub_f32 v214, v214, v157\n"
      "v_sub_f32 v215, v215, v157\n"
      "v_sub_f32 v216, v216, v157\n"
      "v_sub_f32 v217, v217, v157\n"
      "v_sub_f32 v160, v160, v157\n"
      "v_exp_f32 v164, v164\n"
      "v_exp_f32 v165, v165\n"
      "v_exp_f32_e64 v166, v166\n"
      "v_cvt_pk_bf16_f32 v164, v164, v165\n"
      "v_exp_f32 v167, v167\n"
      "v_exp_f32 v228, v228\n"
      "v_cvt_pk_bf16_f32 v165, v166, v167\n"
      "v_exp_f32 v229, v229\n"
      "v_exp_f32 v230, v230\n"
      "v_cvt_pk_bf16_f32 v166, v228, v229\n"
      "v_exp_f32 v231, v231\n"
      "v_exp_f32 v214, v214\n"
      "v_cvt_pk_bf16_f32 v167, v230, v231\n"
      "v_exp_f32 v215, v215\n"
      "v_exp_f32 v216, v216\n"
      "v_exp_f32 v217, v217\n"
      "v_exp_f32 v160, v160\n"
      "v_cvt_pk_bf16_f32 v62, v214, v215\n"
      "v_cvt_pk_bf16_f32 v63, v216, v217\n"
      "v_cvt_pk_bf16_f32 v64, v160, 0\n"
      "v_rcp_f32 v168, v168\n"
      "v_mfma_f32_16x16x32_bf16 v[214:217], v[66:69], v[164:167], 0\n"
      "ds_read_b128 v[184:187], v123 offset:18688\n"
      "v_mul_f32 v220, v168, v220\n"
      "v_mfma_f32_16x16x32_bf16 v[214:217], v[66:69], v[62:65], v[214:217]\n"
      "ds_read_b128 v[228:231], v123 offset:21760\n"
      "v_mul_f32 v221, v168, v221\n"
      "v_mfma_f32_16x16x32_bf16 v[208:211], v[172:175], v[164:167], 0\n"
      "v_cvt_pk_bf16_f32 v220, v220, v221\n"
      "v_mul_f32 v222, v168, v222\n"
      "v_mfma_f32_16x16x32_bf16 v[208:211], v[50:53], v[62:65], v[208:211]\n"
      "ds_read_b128 v[224:227], v123 offset:24832\n"
      "v_mul_f32 v223, v168, v223\n"
      "v_mfma_f32_16x16x32_bf16 v[204:207], v[176:179], v[164:167], 0\n"
      "s_add_u32 s28, s27, 2\n"
      "v_cvt_pk_bf16_f32 v221, v222, v223\n"
      "v_mul_f32 v240, v168, v240\n"
      "v_mfma_f32_16x16x32_bf16 v[204:207], v[54:57], v[62:65], v[204:207]\n"
      "s_min_u32 m0, s28, s3\n"
      "v_mul_f32_e64 v241, v168, v241\n"
      "v_cvt_pk_bf16_f32 v222, v240, v241\n"
      "v_mfma_f32_16x16x32_bf16 v[216:219], v[180:183], v[164:167], 0\n"
      "ds_read_b128 v[164:167], v123 offset:27904\n"
      "v_mul_f32_e64 v242, v168, v242\n"
      "v_mfma_f32_16x16x32_bf16 v[216:219], v[58:61], v[62:65], v[216:219]\n"
      "s_movrels_b32 s9, s80\n"
      "v_mul_f32 v243, v168, v243\n"
      "v_cvt_pk_bf16_f32 v223, v242, v243\n"
      "v_mul_f32 v194, v168, v194\n"
      "v_mul_f32 v195, v168, v195\n"
      "v_cvt_pk_bf16_f32 v194, v194, v195\n"
      "v_mul_f32 v196, v168, v196\n"
      "v_mul_f32 v197, v168, v197\n"
      "v_cvt_pk_bf16_f32 v195, v196, v197\n"
      "v_cndmask_b32 v193, v193, v213, s[42:43]\n"
      "v_cndmask_b32 v193, v193, v161, s[44:45]\n"
      "v_cndmask_b32 v193, v193, v189, s[46:47]\n"
      "v_mbcnt_lo_u32_b32 v198, -1, 0\n"
      "v_mbcnt_hi_u32_b32 v198, -1, v198\n"
      "s_waitcnt lgkmcnt(2)\n"
      "v_mfma_f32_16x16x32_bf16 v[148:151], a[216:219], v[184:187], 0\n"
      "s_lshr_b32 s8, s9, 8\n"
      "v_min_u32_e64 v198, 48, v198\n"
      "v_lshlrev_b32_e64 v198, 1, v198\n"
      "v_mfma_f32_16x16x32_bf16 v[148:151], a[228:231], v[228:231], v[148:151]\n"
      "ds_write_b128 v124, v[220:223] offset:256\n"
      "v_mfma_f32_16x16x32_bf16 v[152:155], a[220:223], v[184:187], 0\n"
      "s_and_b32 s9, s9, 0xff\n"
      "v_add_u32_e64 v198, s30, v198\n"
      "s_mul_i32 s10, s8, 73\n"
      "v_mfma_f32_16x16x32_bf16 v[152:155], a[232:235], v[228:231], v[152:155]\n"
      "ds_write_b64 v125, v[194:195] offset:256\n"
      "s_waitcnt lgkmcnt(2)\n"
      "v_mfma_f32_16x16x32_bf16 v[156:159], a[224:227], v[184:187], 0\n"
      "ds_read_u16 v184, v198 offset:4704\n"
      "s_add_u32 s10, s10, s9\n"
      "v_mfma_f32_16x16x32_bf16 v[156:159], a[236:239], v[228:231], v[156:159]\n"
      "ds_read_b128 v[160:163], v123 offset:30976\n"
      "v_rcp_f32_e64 v214, v214\n"
      "v_mfma_f32_16x16x32_bf16 v[148:151], a[240:243], v[224:227], v[148:151]\n"
      "ds_read_b128 v[168:171], v123 offset:34048\n"
      "s_mul_i32 s28, s10, 4802\n"
      "v_mfma_f32_16x16x32_bf16 v[148:151], a[252:255], v[164:167], v[148:151]\n"
      "s_add_u32 s56, s18, s28\n"
      "s_addc_u32 s57, s19, 0\n"
      "v_mfma_f32_16x16x32_bf16 v[152:155], a[244:247], v[224:227], v[152:155]\n"
      "s_and_b32 s56, s56, -16\n"
      "s_sub_u32 s58, s56, 110592\n"
      "s_subb_u32 s59, s57, 0\n"
      "v_mfma_f32_16x16x32_bf16 v[152:155], v[0:3], v[164:167], v[152:155]\n"
      "s_sub_u32 s56, s56, 73728\n"
      "s_subb_u32 s57, s57, 0\n"
      "s_mul_i32 s28, s8, 1373568\n"
      "s_mul_i32 s29, s9, 2688\n"
      "s_add_u32 s28, s28, s29\n"
      "s_cmp_eq_u32 s8, 72\n"
      "s_cselect_b32 s8, 0, 100270464\n"
      "s_cmp_eq_u32 s9, 72\n"
      "s_cselect_b32 s9, 0, 196224\n"
      "s_add_u32 s29, s8, s9\n"
      "s_add_u32 s28, s28, s29\n"
      "s_add_u32 s52, s16, s28\n"
      "s_addc_u32 s53, s17, 0\n"
      "s_cmp_lg_u32 s29, s11\n"
      "s_cbranch_scc1 L_edge_16_%=\n"
      "L_eb_17_%=:\n"
      "s_waitcnt lgkmcnt(1)\n"
      "v_lshlrev_b32 v184, 16, v184\n"
      "v_fmac_f32 v193, s37, v184\n"
      "v_mul_f32 v208, v214, v208\n"
      "v_mul_f32 v209, v214, v209\n"
      "v_max_f32_dpp v184, v193, v193 row_ror:8 row_mask:0xf bank_mask:0xf\n"
      "v_mfma_f32_16x16x32_bf16 v[156:159], a[248:251], v[224:227], v[156:159]\n"
      "s_nop 0\n"
      "v_max_f32_dpp v184, v184, v184 row_ror:4 row_mask:0xf bank_mask:0xf\n"
      "v_cvt_pk_bf16_f32 v208, v208, v209\n"
      "v_mfma_f32_16x16x32_bf16 v[156:159], v[4:7], v[164:167], v[156:159]\n"
      "v_max_f32_dpp v184, v184, v184 row_ror:2 row_mask:0xf bank_mask:0xf\n"
      "v_mul_f32 v210, v214, v210\n"
      "v_mul_f32 v211, v214, v211\n"
      "v_max_f32_dpp v184, v184, v184 row_ror:1 row_mask:0xf bank_mask:0xf\n"
      "v_cvt_pk_bf16_f32 v209, v210, v211\n"
      "s_nop 0\n"
      "v_max_f32_dpp v184, v184, v184 row_bcast:15 row_mask:0xa bank_mask:0xf\n"
      "v_mul_f32 v204, v214, v204\n"
      "s_nop 0\n"
      "v_max_f32_dpp v184, v184, v184 row_bcast:31 row_mask:0xc bank_mask:0xf\n"
      "v_mul_f32_e64 v205, v214, v205\n"
      "v_readlane_b32 s31, v184, 63\n"
      "s_waitcnt lgkmcnt(0)\n"
      "v_mfma_f32_16x16x32_bf16 v[148:151], v[8:11], v[160:163], v[148:151]\n"
      "ds_read_b128 v[224:227], v123 offset:18944\n"
      "v_subrev_f32 v193, s31, v193\n"
      "v_mfma_f32_16x16x32_bf16 v[148:151], v[20:23], v[168:171], v[148:151]\n"
      "v_exp_f32 v193, v193\n"
      "v_cvt_pk_bf16_f32 v210, v204, v205\n"
      "v_cvt_pk_bf16_f32 v193, v193, v193\n"
      "v_mul_f32 v206, v214, v206\n"
      "v_mul_f32 v207, v214, v207\n"
      "v_cvt_pk_bf16_f32 v211, v206, v207\n"
      "ds_write_b16 v130, v193\n"
      "v_mfma_f32_16x16x32_bf16 v[152:155], v[12:15], v[160:163], v[152:155]\n"
      "ds_read_b128 v[184:187], v131\n"
      "v_mfma_f32_16x16x32_bf16 v[152:155], v[24:27], v[168:171], v[152:155]\n"
      "ds_read_b128 v[188:191], v131 offset:64\n"
      "v_mul_f32 v216, v214, v216\n"
      "v_mfma_f32_16x16x32_bf16 v[156:159], v[16:19], v[160:163], v[156:159]\n"
      "ds_read_b128 v[204:207], v123 offset:22016\n"
      "v_mul_f32 v217, v214, v217\n"
      "v_mfma_f32_16x16x32_bf16 v[156:159], v[28:31], v[168:171], v[156:159]\n"
      "ds_write_b128 v124, v[208:211] offset:512\n"
      "v_cvt_pk_bf16_f32 v216, v216, v217\n"
      "v_mul_f32 v218, v214, v218\n"
      "v_mul_f32 v219, v214, v219\n"
      "v_cvt_pk_bf16_f32 v217, v218, v219\n"
      "v_fma_f32 v218, v148, v148, s39\n"
      "v_fmac_f32 v218, v149, v149\n"
      "v_fmac_f32 v218, v150, v150\n"
      "v_fmac_f32 v218, v151, v151\n"
      "v_fmac_f32 v218, v152, v152\n"
      "v_fmac_f32 v218, v153, v153\n"
      "ds_write_b64 v125, v[216:217] offset:512\n"
      "v_fmac_f32 v218, v154, v154\n"
      "ds_read_b128 v[208:211], v123 offset:25088\n"
      "ds_read_b128 v[214:217], v123 offset:28160\n"
      "ds_read_b128 v[228:231], v123 offset:31232\n"
      "ds_read_b128 v[220:223], v123 offset:34304\n"
      "s_waitcnt lgkmcnt(5)\n"
      "v_mfma_f32_16x16x32_bf16 v[160:163], a[216:219], v[224:227], 0\n"
      "v_fmac_f32 v218, v155, v155\n"
      "v_mfma_f32_16x16x32_bf16 v[160:163], a[228:231], v[204:207], v[160:163]\n"
      "v_fmac_f32 v218, v156, v156\n"
      "v_mfma_f32_16x16x32_bf16 v[164:167], a[220:223], v[224:227], 0\n"
      "v_mfma_f32_16x16x32_bf16 v[164:167], a[232:235], v[204:207], v[164:167]\n"
      "v_mfma_f32_16x16x32_bf16 v[168:171], a[224:227], v[224:227], 0\n"
      "v_fmac_f32 v218, v157, v157\n"
      "v_mfma_f32_16x16x32_bf16 v[168:171], a[236:239], v[204:207], v[168:171]\n"
      "v_fmac_f32 v218, v158, v158\n"
      "s_waitcnt lgkmcnt(0)\n"
      "v_mfma_f32_16x16x32_bf16 v[160:163], a[240:243], v[208:211], v[160:163]\n"
      "v_fmac_f32_e64 v218, v159, v159\n"
      "v_mfma_f32_16x16x32_bf16 v[160:163], a[252:255], v[214:217], v[160:163]\n"
      "v_mfma_f32_16x16x32_bf16 v[164:167], a[244:247], v[208:211], v[164:167]\n"
      "v_mfma_f32_16x16x32_bf16 v[164:167], v[0:3], v[214:217], v[164:167]\n"
      "v_mfma_f32_16x16x32_bf16 v[168:171], a[248:251], v[208:211], v[168:171]\n"
      "v_mfma_f32_16x16x32_bf16 v[168:171], v[4:7], v[214:217], v[168:171]\n"
      "v_permlane16_swap_b32 v252, v218\n"
      "v_mfma_f32_16x16x32_bf16 v[160:163], v[8:11], v[228:231], v[160:163]\n"
      "v_add_f32 v252, v252, v218\n"
      "v_mfma_f32_16x16x32_bf16 v[160:163], v[20:23], v[220:223], v[160:163]\n"
      "v_mfma_f32_16x16x32_bf16 v[164:167], v[12:15], v[228:231], v[164:167]\n"
      "v_mfma_f32_16x16x32_bf16 v[164:167], v[24:27], v[220:223], v[164:167]\n"
      "v_mfma_f32_16x16x32_bf16 v[168:171], v[16:19], v[228:231], v[168:171]\n"
      "v_mfma_f32_16x16x32_bf16 v[168:171], v[28:31], v[220:223], v[168:171]\n"
      "s_nop 3\n"
      "v_fma_f32 v193, v160, v160, s39\n"
      "v_fmac_f32 v193, v161, v161\n"
      "v_fmac_f32 v193, v162, v162\n"
      "v_fmac_f32 v193, v163, v163\n"
      "v_fmac_f32 v193, v164, v164\n"
      "v_fmac_f32 v193, v165, v165\n"
      "v_fmac_f32 v193, v166, v166\n"
      "v_fmac_f32 v193, v167, v167\n"
      "v_fmac_f32 v193, v168, v168\n"
      "v_fmac_f32 v193, v169, v169\n"
      "v_fmac_f32 v193, v170, v170\n"
      "v_fmac_f32 v193, v171, v171\n"
      "v_mov_b32 v240, v193\n"
      "s_nop 1\n"
      "v_permlane16_swap_b32 v193, v240\n"
      "v_add_f32 v193, v193, v240\n"
      "s_nop 1\n"
      "v_permlane32_swap_b32 v252, v193\n"
      "v_add_f32_e64 v252, v252, v193\n"
      "ds_write_b32 v129, v252\n"
      "s_branch L_join_9_%=\n"
      "L_gen_20_%=:\n"
      "s_waitcnt lgkmcnt(0)\n"
      "s_nop 7\n"
      "s_nop 7\n"
      "s_cmp_eq_u64 s[48:49], 0\n"
      "s_cbranch_scc1 L_go_26_%=\n"
      "v_mbcnt_lo_u32_b32 v148, -1, 0\n"
      "v_mbcnt_hi_u32_b32 v148, -1, v148\n"
      "v_lshlrev_b32 v149, 1, v148\n"
      "v_add_u32 v150, 4802, v149\n"
      "s_mov_b32 m0, s27\n"
      "s_nop 0\n"
      "s_movrels_b32 s9, s80\n"
      "s_lshr_b32 s8, s9, 8\n"
      "s_and_b32 s9, s9, 0xff\n"
      "s_mul_i32 s10, s8, 73\n"
      "s_add_u32 s10, s10, s9\n"
      "s_mul_i32 s30, s10, 4802\n"
      "s_add_u32 s30, s30, s18\n"
      "s_and_b32 s29, s30, 15\n"
      "v_cmp_gt_u32 vcc, s29, v149\n"
      "s_add_u32 s30, s29, 115456\n"
      "s_nop 0\n"
      "v_cndmask_b32 v149, v150, v149, vcc\n"
      "v_add_u32 v149, 115456, v149\n"
      "v_mov_b32 v150, 0\n"
      "s_mov_b64 exec, 0x7f\n"
      "ds_write_b16 v149, v150\n"
      "s_mov_b64 exec, -1\n"
      "v_lshlrev_b32 v149, 4, v148\n"
      "v_add_u32 v149, 115456, v149\n"
      "ds_read_b128 v[172:175], v149\n"
      "ds_read_b128 v[196:199], v149 offset:1024\n"
      "ds_read_b128 v[202:205], v149 offset:2048\n"
      "ds_read_b128 v[212:215], v149 offset:3072\n"
      "v_min_u32 v148, 116160, v149\n"
      "ds_read_b128 v[216:219], v148 offset:4096\n"
      "s_waitcnt lgkmcnt(2)\n"
      "v_or3_b32 v172, v172, v173, v174\n"
      "v_or3_b32 v172, v172, v175, v196\n"
      "v_or3_b32 v172, v172, v197, v198\n"
      "v_or3_b32 v172, v172, v199, v202\n"
      "v_or3_b32 v172, v172, v203, v204\n"
      "s_waitcnt lgkmcnt(0)\n"
      "v_or3_b32 v172, v172, v205, v212\n"
      "v_or3_b32 v172, v172, v213, v214\n"
      "v_or3_b32 v172, v172, v215, v216\n"
      "v_or3_b32 v172, v172, v217, v218\n"
      "v_bitop3_b32 v172, v172, v219, s40 bitop3:0xa8\n"
      "v_cmp_ne_u32 s[48:49], 0, v172\n"
      "s_cmp_lg_u64 s[48:49], 0\n"
      "s_cbranch_scc1 L_gen_21_%=\n"
      "s_cmp_eq_u64 s[50:51], 0\n"
      "s_cbranch_scc1 L_fast_23_%=\n"
      "L_go_26_%=:\n"
      "v_mfma_f32_16x16x32_bf16 v[148:151], v[224:227], v[152:155], v[90:93]\n"
      "ds_read_b128 v[172:175], v119 offset:12288\n"
      "v_max3_f32 v196, v164, v165, v166\n"
      "v_mfma_f32_16x16x32_bf16 v[148:151], v[34:37], v[38:41], v[148:151]\n"
      "ds_read_b128 v[202:205], v120\n"
      "v_max3_f32 v197, v167, v156, v157\n"
      "v_mfma_f32_16x16x32_bf16 v[212:215], v[160:163], v[152:155], v[94:97]\n"
      "ds_read_b128 v[216:219], v121\n"
      "v_max3_f32 v196, v196, v158, v159\n"
      "v_mfma_f32_16x16x32_bf16 v[212:215], v[40:43], v[38:41], v[212:215]\n"
      "v_max3_f32 v197, v197, v168, v169\n"
      "v_max3_f32 v196, v196, v170, v171\n"
      "v_max3_f32 v196, v196, v197, v200\n"
      "v_mov_b32 v197, v196\n"
      "s_nop 1\n"
      "v_permlane16_swap_b32 v196, v197\n"
      "v_max_f32 v196, v196, v197\n"
      "v_mov_b32 v197, v196\n"
      "s_nop 1\n"
      "v_permlane32_swap_b32 v196, v197\n"
      "v_max_f32 v196, v196, v197\n"
      "v_sub_f32 v164, v164, v196\n"
      "v_sub_f32 v165, v165, v196\n"
      "s_waitcnt lgkmcnt(1)\n"
      "v_sub_f32 v166, v166, v196\n"
      "global_store_dwordx4 v116, v[172:175], s[54:55] offset:256 sc1\n"
      "v_sub_f32 v167, v167, v196\n"
      "v_sub_f32 v156, v156, v196\n"
      "global_store_dwordx4 v117, v[202:205], s[54:55] sc1\n"
      "v_sub_f32 v157, v157, v196\n"
      "v_sub_f32 v158, v158, v196\n"
      "s_waitcnt lgkmcnt(0)\n"
      "v_sub_f32 v159, v159, v196\n"
      "s_mov_b32 exec_hi, 0\n"
      "global_store_dwordx4 v118, v[216:219], s[54:55] sc1\n"
      "s_mov_b32 exec_hi, -1\n"
      "v_mfma_f32_16x16x32_bf16 v[172:175], v[192:195], v[152:155], v[72:75]\n"
      "v_sub_f32 v168, v168, v196\n"
      "v_sub_f32 v169, v169, v196\n"
      "v_mfma_f32_16x16x32_bf16 v[172:175], v[176:179], v[160:163], v[172:175]\n"
      "v_sub_f32 v170, v170, v196\n"
      "v_sub_f32 v171, v171, v196\n"
      "v_mfma_f32_16x16x32_bf16 v[172:175], v[236:239], v[38:41], v[172:175]\n"
      "v_sub_f32 v200, v200, v196\n"
      "v_exp_f32 v164, v164\n"
      "v_mfma_f32_16x16x32_bf16 v[216:219], v[208:211], v[152:155], v[98:101]\n"
      "v_exp_f32_e64 v165, v165\n"
      "v_mfma_f32_16x16x32_bf16 v[216:219], v[46:49], v[38:41], v[216:219]\n"
      "v_exp_f32 v166, v166\n"
      "v_cvt_pk_bf16_f32 v164, v164, v165\n"
      "v_exp_f32 v167, v167\n"
      "v_exp_f32_e64 v156, v156\n"
      "v_cvt_pk_bf16_f32 v165, v166, v167\n"
      "v_exp_f32 v157, v157\n"
      "v_exp_f32 v158, v158\n"
      "v_cvt_pk_bf16_f32 v166, v156, v157\n"
      "v_exp_f32 v159, v159\n"
      "v_exp_f32 v168, v168\n"
      "v_cvt_pk_bf16_f32 v167, v158, v159\n"
      "v_exp_f32 v169, v169\n"
      "v_exp_f32 v170, v170\n"
      "v_exp_f32 v171, v171\n"
      "v_exp_f32 v200, v200\n"
      "v_cvt_pk_bf16_f32 v62, v168, v169\n"
      "v_cvt_pk_bf16_f32 v63, v170, v171\n"
      "v_cvt_pk_bf16_f32 v64, v200, 0\n"
      "v_max3_f32 v152, v148, v149, v150\n"
      "v_max3_f32 v153, v151, v212, v213\n"
      "v_mfma_f32_16x16x32_bf16 v[154:157], v[66:69], v[164:167], 0\n"
      "v_max3_f32 v152, v152, v214, v215\n"
      "v_max3_f32 v153, v153, v216, v217\n"
      "v_mfma_f32_16x16x32_bf16 v[154:157], v[66:69], v[62:65], v[154:157]\n"
      "v_max3_f32 v152, v152, v218, v219\n"
      "v_max3_f32 v152, v152, v153, v172\n"
      "s_nop 5\n"
      "v_mfma_f32_16x16x32_bf16 v[156:159], v[180:183], v[164:167], 0\n"
      "v_mov_b32 v153, v152\n"
      "v_mfma_f32_16x16x32_bf16 v[156:159], v[50:53], v[62:65], v[156:159]\n"
      "s_nop 0\n"
      "v_permlane16_swap_b32 v152, v153\n"
      "v_mfma_f32_16x16x32_bf16 v[168:171], v[184:187], v[164:167], 0\n"
      "v_max_f32 v152, v152, v153\n"
      "v_mov_b32 v153, v152\n"
      "v_mfma_f32_16x16x32_bf16 v[168:171], v[54:57], v[62:65], v[168:171]\n"
      "s_nop 0\n"
      "v_permlane32_swap_b32 v152, v153\n"
      "v_mfma_f32_16x16x32_bf16 v[202:205], v[188:191], v[164:167], 0\n"
      "v_max_f32 v152, v152, v153\n"
      "v_sub_f32 v148, v148, v152\n"
      "v_mfma_f32_16x16x32_bf16 v[202:205], v[58:61], v[62:65], v[202:205]\n"
      "v_sub_f32 v149, v149, v152\n"
      "v_sub_f32 v150, v150, v152\n"
      "v_mfma_f32_16x16x32_bf16 v[164:167], v[224:227], v[232:235], v[102:105]\n"
      "v_sub_f32 v151, v151, v152\n"
      "v_sub_f32 v212, v212, v152\n"
      "v_mfma_f32_16x16x32_bf16 v[164:167], v[34:37], v[44:47], v[164:167]\n"
      "v_sub_f32 v213, v213, v152\n"
      "v_sub_f32 v214, v214, v152\n"
      "v_mfma_f32_16x16x32_bf16 v[224:227], v[160:163], v[232:235], v[106:109]\n"
      "v_sub_f32 v215, v215, v152\n"
      "v_sub_f32 v216, v216, v152\n"
      "v_mfma_f32_16x16x32_bf16 v[224:227], v[40:43], v[44:47], v[224:227]\n"
      "v_sub_f32 v217, v217, v152\n"
      "v_sub_f32 v218, v218, v152\n"
      "v_sub_f32 v219, v219, v152\n"
      "v_sub_f32 v172, v172, v152\n"
      "v_exp_f32 v148, v148\n"
      "v_exp_f32 v149, v149\n"
      "v_exp_f32 v150, v150\n"
      "v_cvt_pk_bf16_f32 v148, v148, v149\n"
      "v_exp_f32 v151, v151\n"
      "v_mfma_f32_16x16x32_bf16 v[160:163], v[192:195], v[232:235], v[74:77]\n"
      "v_exp_f32_e64 v212, v212\n"
      "v_mfma_f32_16x16x32_bf16 v[160:163], v[176:179], v[208:211], v[160:163]\n"
      "v_cvt_pk_bf16_f32 v149, v150, v151\n"
      "v_exp_f32 v213, v213\n"
      "v_mfma_f32_16x16x32_bf16 v[160:163], v[236:239], v[44:47], v[160:163]\n"
      "v_exp_f32 v214, v214\n"
      "v_cvt_pk_bf16_f32 v150, v212, v213\n"
      "v_exp_f32 v215, v215\n"
      "v_mfma_f32_16x16x32_bf16 v[196:199], v[208:211], v[232:235], v[110:113]\n"
      "v_exp_f32 v216, v216\n"
      "v_mfma_f32_16x16x32_bf16 v[196:199], v[46:49], v[44:47], v[196:199]\n"
      "v_cvt_pk_bf16_f32 v151, v214, v215\n"
      "v_exp_f32 v217, v217\n"
      "v_mfma_f32_16x16x32_bf16 v[212:215], v[176:179], v[192:195], v[76:79]\n"
      "v_exp_f32 v218, v218\n"
      "v_exp_f32 v219, v219\n"
      "v_exp_f32 v172, v172\n"
      "v_cvt_pk_bf16_f32 v62, v216, v217\n"
      "v_cvt_pk_bf16_f32 v63, v218, v219\n"
      "v_cvt_pk_bf16_f32 v64, v172, 0\n"
      "v_mfma_f32_16x16x16_bf16 v[212:215], v[238:239], v[236:237], v[212:215]\n"
      "v_rcp_f32 v154, v154\n"
      "v_mfma_f32_16x16x32_bf16 v[176:179], v[66:69], v[148:151], 0\n"
      "v_mul_f32 v156, v154, v156\n"
      "v_mul_f32_e64 v157, v154, v157\n"
      "v_mfma_f32_16x16x32_bf16 v[176:179], v[66:69], v[62:65], v[176:179]\n"
      "v_cvt_pk_bf16_f32 v156, v156, v157\n"
      "v_mul_f32 v158, v154, v158\n"
      "v_mfma_f32_16x16x32_bf16 v[236:239], v[180:183], v[148:151], 0\n"
      "v_mul_f32 v159, v154, v159\n"
      "v_cvt_pk_bf16_f32 v157, v158, v159\n"
      "v_mfma_f32_16x16x32_bf16 v[236:239], v[50:53], v[62:65], v[236:239]\n"
      "v_mul_f32 v168, v154, v168\n"
      "v_mul_f32 v169, v154, v169\n"
      "v_mfma_f32_16x16x32_bf16 v[214:217], v[184:187], v[148:151], 0\n"
      "v_cvt_pk_bf16_f32 v158, v168, v169\n"
      "v_mul_f32 v170, v154, v170\n"
      "v_mfma_f32_16x16x32_bf16 v[214:217], v[54:57], v[62:65], v[214:217]\n"
      "v_mul_f32 v171, v154, v171\n"
      "v_cvt_pk_bf16_f32 v159, v170, v171\n"
      "v_mfma_f32_16x16x32_bf16 v[168:171], v[188:191], v[148:151], 0\n"
      "v_mul_f32 v202, v154, v202\n"
      "v_mul_f32 v203, v154, v203\n"
      "v_mfma_f32_16x16x32_bf16 v[168:171], v[58:61], v[62:65], v[168:171]\n"
      "ds_write_b128 v124, v[156:159] offset:18432\n"
      "v_cvt_pk_bf16_f32 v202, v202, v203\n"
      "v_mul_f32 v204, v154, v204\n"
      "v_mul_f32 v205, v154, v205\n"
      "v_cvt_pk_bf16_f32 v203, v204, v205\n"
      "v_max3_f32 v148, v164, v165, v166\n"
      "v_max3_f32 v149, v167, v224, v225\n"
      "v_max3_f32 v148, v148, v226, v227\n"
      "v_max3_f32 v149, v149, v196, v197\n"
      "v_max3_f32 v148, v148, v198, v199\n"
      "v_max3_f32 v148, v148, v149, v160\n"
      "v_mov_b32 v149, v148\n"
      "s_nop 1\n"
      "v_permlane16_swap_b32 v148, v149\n"
      "v_max_f32 v148, v148, v149\n"
      "v_mov_b32 v149, v148\n"
      "s_nop 1\n"
      "v_permlane32_swap_b32 v148, v149\n"
      "v_max_f32 v148, v148, v149\n"
      "v_sub_f32 v164, v164, v148\n"
      "v_sub_f32 v165, v165, v148\n"
      "v_sub_f32 v166, v166, v148\n"
      "v_sub_f32 v167, v167, v148\n"
      "v_sub_f32 v224, v224, v148\n"
      "v_sub_f32 v225, v225, v148\n"
      "v_sub_f32 v226, v226, v148\n"
      "v_sub_f32 v227, v227, v148\n"
      "v_sub_f32 v196, v196, v148\n"
      "v_sub_f32 v197, v197, v148\n"
      "v_sub_f32 v198, v198, v148\n"
      "v_sub_f32 v199, v199, v148\n"
      "ds_write_b64 v125, v[202:203] offset:18432\n"
      "v_sub_f32 v160, v160, v148\n"
      "v_exp_f32 v164, v164\n"
      "v_exp_f32 v165, v165\n"
      "v_exp_f32 v166, v166\n"
      "v_cvt_pk_bf16_f32 v164, v164, v165\n"
      "v_exp_f32 v167, v167\n"
      "v_exp_f32 v224, v224\n"
      "v_cvt_pk_bf16_f32 v165, v166, v167\n"
      "v_exp_f32 v225, v225\n"
      "v_exp_f32 v226, v226\n"
      "v_cvt_pk_bf16_f32 v166, v224, v225\n"
      "v_exp_f32 v227, v227\n"
      "v_exp_f32 v196, v196\n"
      "v_cvt_pk_bf16_f32 v167, v226, v227\n"
      "v_exp_f32 v197, v197\n"
      "v_exp_f32 v198, v198\n"
      "v_exp_f32 v199, v199\n"
      "v_exp_f32 v160, v160\n"
      "v_cvt_pk_bf16_f32 v62, v196, v197\n"
      "v_cvt_pk_bf16_f32 v63, v198, v199\n"
      "v_cvt_pk_bf16_f32 v64, v160, 0\n"
      "v_rcp_f32 v176, v176\n"
      "v_mfma_f32_16x16x32_bf16 v[196:199], v[66:69], v[164:167], 0\n"
      "ds_read_b128 v[224:227], v123 offset:256\n"
      "v_mul_f32 v236, v176, v236\n"
      "v_mfma_f32_16x16x32_bf16 v[196:199], v[66:69], v[62:65], v[196:199]\n"
      "ds_read_b128 v[202:205], v123 offset:3328\n"
      "v_mul_f32 v237, v176, v237\n"
      "v_mfma_f32_16x16x32_bf16 v[192:195], v[180:183], v[164:167], 0\n"
      "s_add_u32 s28, s27, 2\n"
      "v_cvt_pk_bf16_f32 v236, v236, v237\n"
      "v_mul_f32 v238, v176, v238\n"
      "v_mfma_f32_16x16x32_bf16 v[192:195], v[50:53], v[62:65], v[192:195]\n"
      "ds_read_b128 v[208:211], v123 offset:6400\n"
      "v_mul_f32 v239, v176, v239\n"
      "v_mfma_f32_16x16x32_bf16 v[232:235], v[184:187], v[164:167], 0\n"
      "s_min_u32 m0, s28, s3\n"
      "v_cvt_pk_bf16_f32 v237, v238, v239\n"
      "v_mul_f32 v214, v176, v214\n"
      "v_mfma_f32_16x16x32_bf16 v[232:235], v[54:57], v[62:65], v[232:235]\n"
      "s_movrels_b32 s9, s80\n"
      "v_mul_f32 v215, v176, v215\n"
      "v_cvt_pk_bf16_f32 v238, v214, v215\n"
      "v_mfma_f32_16x16x32_bf16 v[218:221], v[188:191], v[164:167], 0\n"
      "ds_read_b128 v[164:167], v123 offset:9472\n"
      "v_mul_f32 v216, v176, v216\n"
      "v_mfma_f32_16x16x32_bf16 v[218:221], v[58:61], v[62:65], v[218:221]\n"
      "s_lshr_b32 s8, s9, 8\n"
      "v_mul_f32_e64 v217, v176, v217\n"
      "v_cvt_pk_bf16_f32 v239, v216, v217\n"
      "v_mul_f32_e64 v168, v176, v168\n"
      "v_mul_f32_e64 v169, v176, v169\n"
      "v_cvt_pk_bf16_f32 v168, v168, v169\n"
      "v_mul_f32_e64 v170, v176, v170\n"
      "v_mul_f32 v171, v176, v171\n"
      "v_cvt_pk_bf16_f32 v169, v170, v171\n"
      "v_cndmask_b32 v201, v201, v173, s[42:43]\n"
      "v_cndmask_b32 v201, v201, v161, s[44:45]\n"
      "v_cndmask_b32 v201, v201, v213, s[46:47]\n"
      "s_and_b32 s9, s9, 0xff\n"
      "s_nop 0\n"
      "v_max_f32_dpp v173, v201, v201 row_ror:8 row_mask:0xf bank_mask:0xf\n"
      "s_waitcnt lgkmcnt(2)\n"
      "v_mfma_f32_16x16x32_bf16 v[148:151], a[216:219], v[224:227], 0\n"
      "s_mul_i32 s10, s8, 73\n"
      "v_max_f32_dpp v173, v173, v173 row_ror:4 row_mask:0xf bank_mask:0xf\n"
      "s_add_u32 s10, s10, s9\n"
      "v_mfma_f32_16x16x32_bf16 v[148:151], a[228:231], v[202:205], v[148:151]\n"
      "ds_write_b128 v124, v[236:239] offset:18688\n"
      "v_mfma_f32_16x16x32_bf16 v[152:155], a[220:223], v[224:227], 0\n"
      "s_mul_i32 s28, s10, 4802\n"
      "v_max_f32_dpp v173, v173, v173 row_ror:2 row_mask:0xf bank_mask:0xf\n"
      "s_add_u32 s56, s18, s28\n"
      "v_mfma_f32_16x16x32_bf16 v[152:155], a[232:235], v[202:205], v[152:155]\n"
      "ds_write_b64 v125, v[168:169] offset:18688\n"
      "s_waitcnt lgkmcnt(2)\n"
      "v_mfma_f32_16x16x32_bf16 v[156:159], a[224:227], v[224:227], 0\n"
      "ds_read_b128 v[168:171], v123 offset:12544\n"
      "v_max_f32_dpp v173, v173, v173 row_ror:1 row_mask:0xf bank_mask:0xf\n"
      "v_mfma_f32_16x16x32_bf16 v[156:159], a[236:239], v[202:205], v[156:159]\n"
      "ds_read_b128 v[160:163], v123 offset:15616\n"
      "v_max_f32_dpp v173, v173, v173 row_bcast:15 row_mask:0xa bank_mask:0xf\n"
      "v_mfma_f32_16x16x32_bf16 v[148:151], a[240:243], v[208:211], v[148:151]\n"
      "s_addc_u32 s57, s19, 0\n"
      "v_max_f32_dpp v173, v173, v173 row_bcast:31 row_mask:0xc bank_mask:0xf\n"
      "s_and_b32 s56, s56, -16\n"
      "v_mfma_f32_16x16x32_bf16 v[148:151], a[252:255], v[164:167], v[148:151]\n"
      "s_sub_u32 s58, s56, 110592\n"
      "v_readlane_b32 s31, v173, 63\n"
      "v_rcp_f32_e64 v196, v196\n"
      "v_mfma_f32_16x16x32_bf16 v[152:155], a[244:247], v[208:211], v[152:155]\n"
      "s_subb_u32 s59, s57, 0\n"
      "v_subrev_f32 v201, s31, v201\n"
      "v_mul_f32_e64 v192, v196, v192\n"
      "v_mfma_f32_16x16x32_bf16 v[152:155], v[0:3], v[164:167], v[152:155]\n"
      "s_sub_u32 s56, s56, 73728\n"
      "v_exp_f32 v201, v201\n"
      "v_mfma_f32_16x16x32_bf16 v[156:159], a[248:251], v[208:211], v[156:159]\n"
      "s_subb_u32 s57, s57, 0\n"
      "v_cvt_pk_bf16_f32 v201, v201, v201\n"
      "v_mul_f32 v193, v196, v193\n"
      "v_mfma_f32_16x16x32_bf16 v[156:159], v[4:7], v[164:167], v[156:159]\n"
      "ds_write_b16 v130, v201\n"
      "v_cvt_pk_bf16_f32 v192, v192, v193\n"
      "v_mul_f32 v194, v196, v194\n"
      "v_mul_f32 v195, v196, v195\n"
      "v_cvt_pk_bf16_f32 v193, v194, v195\n"
      "v_mul_f32 v232, v196, v232\n"
      "v_mul_f32 v233, v196, v233\n"
      "v_cvt_pk_bf16_f32 v194, v232, v233\n"
      "v_mul_f32 v234, v196, v234\n"
      "v_mul_f32 v235, v196, v235\n"
      "v_cvt_pk_bf16_f32 v195, v234, v235\n"
      "s_waitcnt lgkmcnt(1)\n"
      "v_mfma_f32_16x16x32_bf16 v[148:151], v[8:11], v[168:171], v[148:151]\n"
      "ds_read_b128 v[172:175], v131\n"
      "s_mul_i32 s28, s8, 1373568\n"
      "v_mfma_f32_16x16x32_bf16 v[148:151], v[20:23], v[160:163], v[148:151]\n"
      "ds_read_b128 v[176:179], v131 offset:64\n"
      "v_mul_f32_e64 v218, v196, v218\n"
      "v_mfma_f32_16x16x32_bf16 v[152:155], v[12:15], v[168:171], v[152:155]\n"
      "ds_read_b128 v[232:235], v123 offset:512\n"
      "v_mul_f32_e64 v219, v196, v219\n"
      "v_mfma_f32_16x16x32_bf16 v[152:155], v[24:27], v[160:163], v[152:155]\n"
      "ds_read_b128 v[208:211], v123 offset:3584\n"
      "v_cvt_pk_bf16_f32 v218, v218, v219\n"
      "v_mfma_f32_16x16x32_bf16 v[156:159], v[16:19], v[168:171], v[156:159]\n"
      "ds_read_b128 v[202:205], v123 offset:6656\n"
      "v_mul_f32 v220, v196, v220\n"
      "v_mfma_f32_16x16x32_bf16 v[156:159], v[28:31], v[160:163], v[156:159]\n"
      "ds_write_b128 v124, v[192:195] offset:18944\n"
      "v_mul_f32 v221, v196, v221\n"
      "v_cvt_pk_bf16_f32 v219, v220, v221\n"
      "v_fma_f32 v220, v148, v148, s39\n"
      "v_fmac_f32 v220, v149, v149\n"
      "v_fmac_f32 v220, v150, v150\n"
      "v_fmac_f32 v220, v151, v151\n"
      "v_fmac_f32 v220, v152, v152\n"
      "v_fmac_f32_e64 v220, v153, v153\n"
      "s_mul_i32 s29, s9, 2688\n"
      "ds_write_b64 v125, v[218:219] offset:18944\n"
      "v_fmac_f32 v220, v154, v154\n"
      "s_add_u32 s28, s28, s29\n"
      "ds_read_b128 v[192:195], v123 offset:9728\n"
      "ds_read_b128 v[216:219], v123 offset:12800\n"
      "ds_read_b128 v[224:227], v123 offset:15872\n"
      "s_cmp_eq_u32 s8, 72\n"
      "s_cselect_b32 s8, 0, 100270464\n"
      "s_cmp_eq_u32 s9, 72\n"
      "s_waitcnt lgkmcnt(5)\n"
      "v_mfma_f32_16x16x32_bf16 v[160:163], a[216:219], v[232:235], 0\n"
      "s_cselect_b32 s9, 0, 196224\n"
      "v_fmac_f32 v220, v155, v155\n"
      "s_add_u32 s29, s8, s9\n"
      "v_mfma_f32_16x16x32_bf16 v[160:163], a[228:231], v[208:211], v[160:163]\n"
      "s_add_u32 s28, s28, s29\n"
      "v_fmac_f32 v220, v156, v156\n"
      "v_mfma_f32_16x16x32_bf16 v[164:167], a[220:223], v[232:235], 0\n"
      "s_add_u32 s52, s16, s28\n"
      "s_addc_u32 s53, s17, 0\n"
      "s_cmp_lg_u32 s29, s11\n"
      "v_mfma_f32_16x16x32_bf16 v[164:167], a[232:235], v[208:211], v[164:167]\n"
      "s_cbranch_scc1 L_edge_27_%=\n"
      "L_eb_28_%=:\n"
      "v_mfma_f32_16x16x32_bf16 v[168:171], a[224:227], v[232:235], 0\n"
      "v_fmac_f32 v220, v157, v157\n"
      "v_mfma_f32_16x16x32_bf16 v[168:171], a[236:239], v[208:211], v[168:171]\n"
      "v_fmac_f32 v220, v158, v158\n"
      "s_waitcnt lgkmcnt(0)\n"
      "v_mfma_f32_16x16x32_bf16 v[160:163], a[240:243], v[202:205], v[160:163]\n"
      "v_fmac_f32_e64 v220, v159, v159\n"
      "v_mfma_f32_16x16x32_bf16 v[160:163], a[252:255], v[192:195], v[160:163]\n"
      "v_mfma_f32_16x16x32_bf16 v[164:167], a[244:247], v[202:205], v[164:167]\n"
      "v_mfma_f32_16x16x32_bf16 v[164:167], v[0:3], v[192:195], v[164:167]\n"
      "v_mfma_f32_16x16x32_bf16 v[168:171], a[248:251], v[202:205], v[168:171]\n"
      "v_mfma_f32_16x16x32_bf16 v[168:171], v[4:7], v[192:195], v[168:171]\n"
      "v_permlane16_swap_b32 v206, v220\n"
      "v_mfma_f32_16x16x32_bf16 v[160:163], v[8:11], v[216:219], v[160:163]\n"
      "v_add_f32 v206, v206, v220\n"
      "v_mfma_f32_16x16x32_bf16 v[160:163], v[20:23], v[224:227], v[160:163]\n"
      "v_mfma_f32_16x16x32_bf16 v[164:167], v[12:15], v[216:219], v[164:167]\n"
      "v_mfma_f32_16x16x32_bf16 v[164:167], v[24:27], v[224:227], v[164:167]\n"
      "v_mfma_f32_16x16x32_bf16 v[168:171], v[16:19], v[216:219], v[168:171]\n"
      "v_mfma_f32_16x16x32_bf16 v[168:171], v[28:31], v[224:227], v[168:171]\n"
      "s_nop 3\n"
      "v_fma_f32 v201, v160, v160, s39\n"
      "v_fmac_f32 v201, v161, v161\n"
      "v_fmac_f32 v201, v162, v162\n"
      "v_fmac_f32 v201, v163, v163\n"
      "v_fmac_f32 v201, v164, v164\n"
      "v_fmac_f32 v201, v165, v165\n"
      "v_fmac_f32 v201, v166, v166\n"
      "v_fmac_f32 v201, v167, v167\n"
      "v_fmac_f32 v201, v168, v168\n"
      "v_fmac_f32 v201, v169, v169\n"
      "v_fmac_f32 v201, v170, v170\n"
      "v_fmac_f32 v201, v171, v171\n"
      "v_mov_b32 v236, v201\n"
      "s_nop 1\n"
      "v_permlane16_swap_b32 v201, v236\n"
      "v_add_f32 v201, v201, v236\n"
      "s_nop 1\n"
      "v_permlane32_swap_b32 v206, v201\n"
      "v_add_f32_e64 v206, v206, v201\n"
      "ds_write_b32 v129, v206 offset:1024\n"
      "s_branch L_join_22_%=\n"
      "L_gen_21_%=:\n"
      "s_waitcnt lgkmcnt(0)\n"
      "s_nop 7\n"
      "s_nop 7\n"
      "v_mfma_f32_16x16x32_bf16 v[148:151], v[224:227], v[152:155], v[90:93]\n"
      "ds_read_b128 v[172:175], v119 offset:12288\n"
      "v_mbcnt_lo_u32_b32 v196, -1, 0\n"
      "v_mfma_f32_16x16x32_bf16 v[148:151], v[34:37], v[38:41], v[148:151]\n"
      "ds_read_b128 v[202:205], v120\n"
      "v_mbcnt_hi_u32_b32 v196, -1, v196\n"
      "v_and_b32 v197, 15, v196\n"
      "v_mul_u32_u24 v197, 0x62, v197\n"
      "v_add_u32 v197, s30, v197\n"
      "v_lshrrev_b32_e64 v196, 4, v196\n"
      "v_lshl_add_u32 v196, v196, 3, v197\n"
      "ds_read_b128 v[212:215], v121\n"
      "ds_read_u16 v198, v196\n"
      "ds_read_u16 v199, v196 offset:2\n"
      "ds_read_u16 v207, v196 offset:4\n"
      "ds_read_u16 v216, v196 offset:6\n"
      "ds_read_u16 v217, v196 offset:32\n"
      "ds_read_u16 v218, v196 offset:34\n"
      "ds_read_u16 v219, v196 offset:36\n"
      "ds_read_u16 v220, v196 offset:38\n"
      "ds_read_u16 v221, v196 offset:64\n"
      "ds_read_u16 v222, v196 offset:66\n"
      "ds_read_u16 v223, v196 offset:68\n"
      "ds_read_u16 v228, v196 offset:70\n"
      "s_waitcnt lgkmcnt(13)\n"
      "global_store_dwordx4 v116, v[172:175], s[54:55] offset:256 sc1\n"
      "global_store_dwordx4 v117, v[202:205], s[54:55] sc1\n"
      "s_nop 0\n"
      "ds_read_u16 v172, v197 offset:96\n"
      "s_waitcnt lgkmcnt(11)\n"
      "v_lshlrev_b32 v198, 16, v198\n"
      "v_fmac_f32 v164, s37, v198\n"
      "v_lshlrev_b32 v199, 16, v199\n"
      "v_fmac_f32 v165, s37, v199\n"
      "s_waitcnt lgkmcnt(7)\n"
      "v_lshlrev_b32 v207, 16, v207\n"
      "v_fmac_f32_e64 v166, s37, v207\n"
      "s_mov_b32 exec_hi, 0\n"
      "global_store_dwordx4 v118, v[212:215], s[54:55] sc1\n"
      "s_mov_b32 exec_hi, -1\n"
      "s_nop 0\n"
      "v_mfma_f32_16x16x32_bf16 v[212:215], v[160:163], v[152:155], v[94:97]\n"
      "v_lshlrev_b32 v216, 16, v216\n"
      "v_fmac_f32 v167, s37, v216\n"
      "v_mfma_f32_16x16x32_bf16 v[212:215], v[40:43], v[38:41], v[212:215]\n"
      "v_lshlrev_b32 v217, 16, v217\n"
      "v_fmac_f32 v156, s37, v217\n"
      "v_lshlrev_b32 v218, 16, v218\n"
      "v_fmac_f32 v157, s37, v218\n"
      "v_max3_f32 v173, v164, v165, v166\n"
      "v_max3_f32 v174, v167, v156, v157\n"
      "s_waitcnt lgkmcnt(1)\n"
      "v_lshlrev_b32 v219, 16, v219\n"
      "v_mfma_f32_16x16x32_bf16 v[202:205], v[192:195], v[152:155], v[72:75]\n"
      "v_fmac_f32 v158, s37, v219\n"
      "v_lshlrev_b32 v220, 16, v220\n"
      "v_mfma_f32_16x16x32_bf16 v[202:205], v[176:179], v[160:163], v[202:205]\n"
      "v_fmac_f32 v159, s37, v220\n"
      "v_lshlrev_b32 v221, 16, v221\n"
      "v_fmac_f32 v168, s37, v221\n"
      "v_max3_f32 v173, v173, v158, v159\n"
      "v_lshlrev_b32 v222, 16, v222\n"
      "v_fmac_f32 v169, s37, v222\n"
      "v_max3_f32 v174, v174, v168, v169\n"
      "v_lshlrev_b32 v223, 16, v223\n"
      "v_fmac_f32 v170, s37, v223\n"
      "v_lshlrev_b32 v228, 16, v228\n"
      "v_fmac_f32 v171, s37, v228\n"
      "v_max3_f32 v173, v173, v170, v171\n"
      "s_waitcnt lgkmcnt(0)\n"
      "v_lshlrev_b32 v172, 16, v172\n"
      "v_fmac_f32 v200, s37, v172\n"
      "v_mfma_f32_16x16x32_bf16 v[202:205], v[236:239], v[38:41], v[202:205]\n"
      "v_max3_f32 v173, v173, v174, v200\n"
      "v_mov_b32 v174, v173\n"
      "s_nop 1\n"
      "v_permlane16_swap_b32_e64 v173, v174\n"
      "v_mfma_f32_16x16x32_bf16 v[220:223], v[208:211], v[152:155], v[98:101]\n"
      "v_max_f32 v173, v173, v174\n"
      "v_mov_b32 v152, v173\n"
      "v_mfma_f32_16x16x32_bf16 v[220:223], v[46:49], v[38:41], v[220:223]\n"
      "s_nop 0\n"
      "v_permlane32_swap_b32 v173, v152\n"
      "v_max_f32 v173, v173, v152\n"
      "v_sub_f32 v164, v164, v173\n"
      "v_sub_f32 v165, v165, v173\n"
      "v_sub_f32 v166, v166, v173\n"
      "v_sub_f32 v167, v167, v173\n"
      "v_sub_f32 v156, v156, v173\n"
      "v_sub_f32 v157, v157, v173\n"
      "v_sub_f32 v158, v158, v173\n"
      "v_sub_f32 v159, v159, v173\n"
      "v_sub_f32 v168, v168, v173\n"
      "v_sub_f32 v169, v169, v173\n"
      "v_sub_f32 v170, v170, v173\n"
      "v_sub_f32 v171, v171, v173\n"
      "v_sub_f32 v200, v200, v173\n"
      "v_exp_f32 v164, v164\n"
      "v_exp_f32 v165, v165\n"
      "v_exp_f32 v166, v166\n"
      "v_cvt_pk_bf16_f32 v164, v164, v165\n"
      "v_exp_f32 v167, v167\n"
      "v_exp_f32 v156, v156\n"
      "v_cvt_pk_bf16_f32 v165, v166, v167\n"
      "v_exp_f32 v157, v157\n"
      "v_exp_f32 v158, v158\n"
      "v_cvt_pk_bf16_f32 v166, v156, v157\n"
      "v_exp_f32 v159, v159\n"
      "v_exp_f32 v168, v168\n"
      "v_cvt_pk_bf16_f32 v167, v158, v159\n"
      "v_exp_f32 v169, v169\n"
      "v_exp_f32 v170, v170\n"
      "v_exp_f32 v171, v171\n"
      "v_exp_f32 v200, v200\n"
      "v_cvt_pk_bf16_f32 v62, v168, v169\n"
      "v_cvt_pk_bf16_f32 v63, v170, v171\n"
      "v_cvt_pk_bf16_f32 v64, v200, 0\n"
      "v_mfma_f32_16x16x32_bf16 v[156:159], v[66:69], v[164:167], 0\n"
      "ds_read_u16 v152, v196 offset:1568\n"
      "v_mfma_f32_16x16x32_bf16 v[156:159], v[66:69], v[62:65], v[156:159]\n"
      "s_nop 7\n"
      "ds_read_u16 v157, v196 offset:1570\n"
      "v_mfma_f32_16x16x32_bf16 v[172:175], v[180:183], v[164:167], 0\n"
      "ds_read_u16 v158, v196 offset:1572\n"
      "v_mfma_f32_16x16x32_bf16 v[172:175], v[50:53], v[62:65], v[172:175]\n"
      "ds_read_u16 v159, v196 offset:1574\n"
      "ds_read_u16 v153, v196 offset:1600\n"
      "ds_read_u16 v154, v196 offset:1602\n"
      "ds_read_u16 v155, v196 offset:1604\n"
      "ds_read_u16 v170, v196 offset:1606\n"
      "ds_read_u16 v171, v196 offset:1632\n"
      "ds_read_u16 v168, v196 offset:1634\n"
      "ds_read_u16 v169, v196 offset:1636\n"
      "ds_read_u16 v200, v196 offset:1638\n"
      "ds_read_u16 v204, v197 offset:1664\n"
      "s_waitcnt lgkmcnt(11)\n"
      "v_lshlrev_b32 v152, 16, v152\n"
      "v_fmac_f32 v148, s37, v152\n"
      "v_lshlrev_b32 v157, 16, v157\n"
      "v_fmac_f32 v149, s37, v157\n"
      "s_waitcnt lgkmcnt(9)\n"
      "v_lshlrev_b32 v158, 16, v158\n"
      "v_fmac_f32 v150, s37, v158\n"
      "v_lshlrev_b32 v159, 16, v159\n"
      "v_mfma_f32_16x16x32_bf16 v[216:219], v[184:187], v[164:167], 0\n"
      "s_waitcnt lgkmcnt(6)\n"
      "v_fmac_f32 v151, s37, v159\n"
      "v_lshlrev_b32 v153, 16, v153\n"
      "v_mfma_f32_16x16x32_bf16 v[216:219], v[54:57], v[62:65], v[216:219]\n"
      "v_fmac_f32 v212, s37, v153\n"
      "v_lshlrev_b32 v154, 16, v154\n"
      "v_fmac_f32 v213, s37, v154\n"
      "v_lshlrev_b32 v155, 16, v155\n"
      "v_fmac_f32 v214, s37, v155\n"
      "s_waitcnt lgkmcnt(0)\n"
      "v_lshlrev_b32 v170, 16, v170\n"
      "v_fmac_f32 v215, s37, v170\n"
      "v_mfma_f32_16x16x32_bf16 v[152:155], v[188:191], v[164:167], 0\n"
      "v_lshlrev_b32 v171, 16, v171\n"
      "v_fmac_f32 v220, s37, v171\n"
      "v_mfma_f32_16x16x32_bf16 v[152:155], v[58:61], v[62:65], v[152:155]\n"
      "v_lshlrev_b32 v168, 16, v168\n"
      "v_fmac_f32 v221, s37, v168\n"
      "v_max3_f32 v159, v148, v149, v150\n"
      "v_max3_f32 v159, v159, v214, v215\n"
      "v_lshlrev_b32 v169, 16, v169\n"
      "v_fmac_f32 v222, s37, v169\n"
      "v_lshlrev_b32 v200, 16, v200\n"
      "v_fmac_f32 v223, s37, v200\n"
      "v_max3_f32 v159, v159, v222, v223\n"
      "v_lshlrev_b32 v204, 16, v204\n"
      "v_fmac_f32 v202, s37, v204\n"
      "v_mfma_f32_16x16x32_bf16 v[166:169], v[224:227], v[232:235], v[102:105]\n"
      "v_max3_f32 v158, v151, v212, v213\n"
      "v_max3_f32 v158, v158, v220, v221\n"
      "v_mfma_f32_16x16x32_bf16 v[166:169], v[34:37], v[44:47], v[166:169]\n"
      "v_max3_f32 v159, v159, v158, v202\n"
      "v_mov_b32 v158, v159\n"
      "s_nop 1\n"
      "v_permlane16_swap_b32 v159, v158\n"
      "v_max_f32 v159, v159, v158\n"
      "v_mov_b32_e64 v158, v159\n"
      "v_mfma_f32_16x16x32_bf16 v[224:227], v[160:163], v[232:235], v[106:109]\n"
      "s_nop 0\n"
      "v_permlane32_swap_b32 v159, v158\n"
      "v_mfma_f32_16x16x32_bf16 v[224:227], v[40:43], v[44:47], v[224:227]\n"
      "v_max_f32 v159, v159, v158\n"
      "v_sub_f32 v148, v148, v159\n"
      "v_sub_f32 v149, v149, v159\n"
      "v_sub_f32 v150, v150, v159\n"
      "v_sub_f32 v151, v151, v159\n"
      "v_sub_f32 v212, v212, v159\n"
      "v_sub_f32 v213, v213, v159\n"
      "v_sub_f32 v214, v214, v159\n"
      "v_sub_f32_e64 v215, v215, v159\n"
      "v_mfma_f32_16x16x32_bf16 v[160:163], v[192:195], v[232:235], v[74:77]\n"
      "v_sub_f32 v220, v220, v159\n"
      "v_sub_f32 v221, v221, v159\n"
      "v_mfma_f32_16x16x32_bf16 v[160:163], v[176:179], v[208:211], v[160:163]\n"
      "v_sub_f32 v222, v222, v159\n"
      "v_sub_f32 v223, v223, v159\n"
      "v_mfma_f32_16x16x32_bf16 v[160:163], v[236:239], v[44:47], v[160:163]\n"
      "v_sub_f32 v202, v202, v159\n"
      "v_exp_f32 v148, v148\n"
      "v_exp_f32 v149, v149\n"
      "v_exp_f32 v150, v150\n"
      "v_cvt_pk_bf16_f32 v148, v148, v149\n"
      "v_exp_f32 v151, v151\n"
      "s_nop 1\n"
      "v_mfma_f32_16x16x32_bf16 v[162:165], v[208:211], v[232:235], v[110:113]\n"
      "v_exp_f32_e64 v212, v212\n"
      "v_mfma_f32_16x16x32_bf16 v[162:165], v[46:49], v[44:47], v[162:165]\n"
      "v_cvt_pk_bf16_f32 v149, v150, v151\n"
      "v_exp_f32 v213, v213\n"
      "v_exp_f32 v214, v214\n"
      "v_cvt_pk_bf16_f32 v150, v212, v213\n"
      "v_mfma_f32_16x16x32_bf16 v[210:213], v[176:179], v[192:195], v[76:79]\n"
      "v_exp_f32 v215, v215\n"
      "v_exp_f32 v220, v220\n"
      "v_cvt_pk_bf16_f32 v151, v214, v215\n"
      "v_exp_f32 v221, v221\n"
      "v_exp_f32 v222, v222\n"
      "v_cvt_pk_bf16_f32 v62, v220, v221\n"
      "v_mfma_f32_16x16x16_bf16 v[210:213], v[238:239], v[236:237], v[210:213]\n"
      "v_exp_f32 v223, v223\n"
      "v_exp_f32 v202, v202\n"
      "v_cvt_pk_bf16_f32 v63, v222, v223\n"
      "v_cvt_pk_bf16_f32 v64, v202, 0\n"
      "v_rcp_f32 v156, v156\n"
      "v_mfma_f32_16x16x32_bf16 v[176:179], v[66:69], v[148:151], 0\n"
      "v_mul_f32 v172, v156, v172\n"
      "v_mul_f32_e64 v173, v156, v173\n"
      "v_mfma_f32_16x16x32_bf16 v[176:179], v[66:69], v[62:65], v[176:179]\n"
      "ds_read_u16 v159, v196 offset:3136\n"
      "v_cvt_pk_bf16_f32 v172, v172, v173\n"
      "v_mfma_f32_16x16x32_bf16 v[220:223], v[180:183], v[148:151], 0\n"
      "ds_read_u16 v158, v196 offset:3138\n"
      "v_mul_f32 v174, v156, v174\n"
      "v_mfma_f32_16x16x32_bf16 v[220:223], v[50:53], v[62:65], v[220:223]\n"
      "ds_read_u16 v157, v196 offset:3140\n"
      "v_mul_f32 v175, v156, v175\n"
      "v_mfma_f32_16x16x32_bf16 v[236:239], v[184:187], v[148:151], 0\n"
      "ds_read_u16 v177, v196 offset:3142\n"
      "v_cvt_pk_bf16_f32 v173, v174, v175\n"
      "v_mfma_f32_16x16x32_bf16 v[236:239], v[54:57], v[62:65], v[236:239]\n"
      "ds_read_u16 v178, v196 offset:3168\n"
      "v_mul_f32 v216, v156, v216\n"
      "v_mfma_f32_16x16x32_bf16 v[212:215], v[188:191], v[148:151], 0\n"
      "ds_read_u16 v148, v196 offset:3170\n"
      "v_mul_f32 v217, v156, v217\n"
      "v_mfma_f32_16x16x32_bf16 v[212:215], v[58:61], v[62:65], v[212:215]\n"
      "ds_read_u16 v149, v196 offset:3172\n"
      "v_cvt_pk_bf16_f32 v174, v216, v217\n"
      "v_mul_f32 v218, v156, v218\n"
      "v_mul_f32 v219, v156, v219\n"
      "v_cvt_pk_bf16_f32 v175, v218, v219\n"
      "v_mul_f32 v152, v156, v152\n"
      "v_mul_f32 v153, v156, v153\n"
      "v_cvt_pk_bf16_f32 v152, v152, v153\n"
      "v_mul_f32 v154, v156, v154\n"
      "v_mul_f32 v155, v156, v155\n"
      "v_cvt_pk_bf16_f32 v153, v154, v155\n"
      "ds_write_b128 v124, v[172:175] offset:18432\n"
      "ds_write_b64 v125, v[152:153] offset:18432\n"
      "ds_read_u16 v150, v196 offset:3174\n"
      "ds_read_u16 v151, v196 offset:3200\n"
      "ds_read_u16 v152, v196 offset:3202\n"
      "ds_read_u16 v153, v196 offset:3204\n"
      "ds_read_u16 v154, v196 offset:3206\n"
      "ds_read_u16 v155, v197 offset:3232\n"
      "s_waitcnt lgkmcnt(8)\n"
      "v_lshlrev_b32 v159, 16, v159\n"
      "v_fmac_f32 v166, s37, v159\n"
      "v_lshlrev_b32 v158, 16, v158\n"
      "v_fmac_f32 v167, s37, v158\n"
      "v_lshlrev_b32 v157, 16, v157\n"
      "v_fmac_f32 v168, s37, v157\n"
      "v_lshlrev_b32 v177, 16, v177\n"
      "v_fmac_f32 v169, s37, v177\n"
      "v_lshlrev_b32 v178, 16, v178\n"
      "v_fmac_f32 v224, s37, v178\n"
      "v_max3_f32 v157, v166, v167, v168\n"
      "v_lshlrev_b32 v148, 16, v148\n"
      "v_fmac_f32 v225, s37, v148\n"
      "v_max3_f32 v148, v169, v224, v225\n"
      "v_lshlrev_b32 v149, 16, v149\n"
      "v_fmac_f32 v226, s37, v149\n"
      "s_waitcnt lgkmcnt(3)\n"
      "v_lshlrev_b32 v150, 16, v150\n"
      "v_fmac_f32 v227, s37, v150\n"
      "v_max3_f32 v157, v157, v226, v227\n"
      "v_lshlrev_b32 v151, 16, v151\n"
      "v_fmac_f32 v162, s37, v151\n"
      "v_lshlrev_b32 v152, 16, v152\n"
      "v_fmac_f32 v163, s37, v152\n"
      "v_max3_f32 v148, v148, v162, v163\n"
      "s_waitcnt lgkmcnt(0)\n"
      "v_lshlrev_b32 v153, 16, v153\n"
      "v_fmac_f32 v164, s37, v153\n"
      "v_lshlrev_b32 v154, 16, v154\n"
      "v_fmac_f32 v165, s37, v154\n"
      "v_max3_f32 v157, v157, v164, v165\n"
      "v_lshlrev_b32 v155, 16, v155\n"
      "v_fmac_f32_e64 v160, s37, v155\n"
      "v_max3_f32 v157, v157, v148, v160\n"
      "v_mov_b32 v148, v157\n"
      "s_nop 1\n"
      "v_permlane16_swap_b32 v157, v148\n"
      "v_max_f32 v157, v157, v148\n"
      "v_mov_b32 v148, v157\n"
      "s_nop 1\n"
      "v_permlane32_swap_b32 v157, v148\n"
      "v_max_f32 v157, v157, v148\n"
      "v_sub_f32 v166, v166, v157\n"
      "v_sub_f32 v167, v167, v157\n"
      "v_sub_f32 v168, v168, v157\n"
      "v_sub_f32 v169, v169, v157\n"
      "v_sub_f32 v224, v224, v157\n"
      "v_sub_f32 v225, v225, v157\n"
      "v_sub_f32 v226, v226, v157\n"
      "v_sub_f32 v227, v227, v157\n"
      "v_sub_f32 v162, v162, v157\n"
      "v_sub_f32 v163, v163, v157\n"
      "v_sub_f32 v164, v164, v157\n"
      "v_sub_f32 v165, v165, v157\n"
      "v_sub_f32 v160, v160, v157\n"
      "v_exp_f32 v166, v166\n"
      "v_exp_f32 v167, v167\n"
      "v_exp_f32 v168, v168\n"
      "v_cvt_pk_bf16_f32 v166, v166, v167\n"
      "v_exp_f32 v169, v169\n"
      "v_exp_f32 v224, v224\n"
      "v_cvt_pk_bf16_f32 v167, v168, v169\n"
      "v_exp_f32 v225, v225\n"
      "v_exp_f32 v226, v226\n"
      "v_cvt_pk_bf16_f32 v168, v224, v225\n"
      "v_exp_f32 v227, v227\n"
      "v_exp_f32 v162, v162\n"
      "v_cvt_pk_bf16_f32 v169, v226, v227\n"
      "v_exp_f32 v163, v163\n"
      "v_exp_f32 v164, v164\n"
      "v_exp_f32 v165, v165\n"
      "v_exp_f32 v160, v160\n"
      "v_cvt_pk_bf16_f32 v62, v162, v163\n"
      "v_cvt_pk_bf16_f32 v63, v164, v165\n"
      "v_cvt_pk_bf16_f32 v64, v160, 0\n"
      "v_rcp_f32 v176, v176\n"
      "v_mfma_f32_16x16x32_bf16 v[162:165], v[66:69], v[166:169], 0\n"
      "ds_read_b128 v[172:175], v123 offset:256\n"
      "v_mul_f32 v220, v176, v220\n"
      "v_mfma_f32_16x16x32_bf16 v[162:165], v[66:69], v[62:65], v[162:165]\n"
      "ds_read_b128 v[224:227], v123 offset:3328\n"
      "v_mul_f32_e64 v221, v176, v221\n"
      "v_mfma_f32_16x16x32_bf16 v[216:219], v[180:183], v[166:169], 0\n"
      "v_cvt_pk_bf16_f32 v220, v220, v221\n"
      "v_mul_f32 v222, v176, v222\n"
      "v_mfma_f32_16x16x32_bf16 v[216:219], v[50:53], v[62:65], v[216:219]\n"
      "ds_read_b128 v[192:195], v123 offset:6400\n"
      "v_mul_f32 v223, v176, v223\n"
      "v_mfma_f32_16x16x32_bf16 v[232:235], v[184:187], v[166:169], 0\n"
      "s_add_u32 s28, s27, 2\n"
      "v_cvt_pk_bf16_f32 v221, v222, v223\n"
      "v_mul_f32 v236, v176, v236\n"
      "v_mfma_f32_16x16x32_bf16 v[232:235], v[54:57], v[62:65], v[232:235]\n"
      "s_min_u32 m0, s28, s3\n"
      "v_mul_f32 v237, v176, v237\n"
      "v_cvt_pk_bf16_f32 v222, v236, v237\n"
      "v_mfma_f32_16x16x32_bf16 v[228:231], v[188:191], v[166:169], 0\n"
      "ds_read_b128 v[166:169], v123 offset:9472\n"
      "v_mul_f32 v238, v176, v238\n"
      "v_mfma_f32_16x16x32_bf16 v[228:231], v[58:61], v[62:65], v[228:231]\n"
      "s_movrels_b32 s9, s80\n"
      "v_mul_f32 v239, v176, v239\n"
      "v_cvt_pk_bf16_f32 v223, v238, v239\n"
      "v_mul_f32 v212, v176, v212\n"
      "v_mul_f32 v213, v176, v213\n"
      "v_cvt_pk_bf16_f32 v212, v212, v213\n"
      "v_mul_f32 v214, v176, v214\n"
      "v_mul_f32_e64 v215, v176, v215\n"
      "v_cvt_pk_bf16_f32 v213, v214, v215\n"
      "v_cndmask_b32 v201, v201, v203, s[42:43]\n"
      "v_cndmask_b32 v201, v201, v161, s[44:45]\n"
      "v_cndmask_b32 v201, v201, v211, s[46:47]\n"
      "v_mbcnt_lo_u32_b32 v196, -1, 0\n"
      "v_mbcnt_hi_u32_b32 v196, -1, v196\n"
      "s_waitcnt lgkmcnt(2)\n"
      "v_mfma_f32_16x16x32_bf16 v[148:151], a[216:219], v[172:175], 0\n"
      "s_lshr_b32 s8, s9, 8\n"
      "v_min_u32 v196, 48, v196\n"
      "v_lshlrev_b32 v196, 1, v196\n"
      "v_mfma_f32_16x16x32_bf16 v[148:151], a[228:231], v[224:227], v[148:151]\n"
      "ds_write_b128 v124, v[220:223] offset:18688\n"
      "v_mfma_f32_16x16x32_bf16 v[152:155], a[220:223], v[172:175], 0\n"
      "s_and_b32 s9, s9, 0xff\n"
      "v_add_u32_e64 v196, s30, v196\n"
      "s_mul_i32 s10, s8, 73\n"
      "v_mfma_f32_16x16x32_bf16 v[152:155], a[232:235], v[224:227], v[152:155]\n"
      "ds_write_b64 v125, v[212:213] offset:18688\n"
      "s_waitcnt lgkmcnt(2)\n"
      "v_mfma_f32_16x16x32_bf16 v[156:159], a[224:227], v[172:175], 0\n"
      "ds_read_u16 v172, v196 offset:4704\n"
      "s_add_u32 s10, s10, s9\n"
      "v_mfma_f32_16x16x32_bf16 v[156:159], a[236:239], v[224:227], v[156:159]\n"
      "ds_read_b128 v[224:227], v123 offset:12544\n"
      "v_rcp_f32_e64 v162, v162\n"
      "v_mfma_f32_16x16x32_bf16 v[148:151], a[240:243], v[192:195], v[148:151]\n"
      "ds_read_b128 v[220:223], v123 offset:15616\n"
      "s_mul_i32 s28, s10, 4802\n"
      "v_mfma_f32_16x16x32_bf16 v[148:151], a[252:255], v[166:169], v[148:151]\n"
      "s_add_u32 s56, s18, s28\n"
      "s_addc_u32 s57, s19, 0\n"
      "v_mfma_f32_16x16x32_bf16 v[152:155], a[244:247], v[192:195], v[152:155]\n"
      "s_and_b32 s56, s56, -16\n"
      "s_sub_u32 s58, s56, 110592\n"
      "s_subb_u32 s59, s57, 0\n"
      "v_mfma_f32_16x16x32_bf16 v[152:155], v[0:3], v[166:169], v[152:155]\n"
      "s_sub_u32 s56, s56, 73728\n"
      "s_subb_u32 s57, s57, 0\n"
      "s_mul_i32 s28, s8, 1373568\n"
      "s_mul_i32 s29, s9, 2688\n"
      "s_add_u32 s28, s28, s29\n"
      "s_cmp_eq_u32 s8, 72\n"
      "s_cselect_b32 s8, 0, 100270464\n"
      "s_cmp_eq_u32 s9, 72\n"
      "s_cselect_b32 s9, 0, 196224\n"
      "s_add_u32 s29, s8, s9\n"
      "s_add_u32 s28, s28, s29\n"
      "s_add_u32 s52, s16, s28\n"
      "s_addc_u32 s53, s17, 0\n"
      "s_cmp_lg_u32 s29, s11\n"
      "s_cbranch_scc1 L_edge_29_%=\n"
      "L_eb_30_%=:\n"
      "s_waitcnt lgkmcnt(1)\n"
      "v_lshlrev_b32 v172, 16, v172\n"
      "v_fmac_f32 v201, s37, v172\n"
      "v_mul_f32 v216, v162, v216\n"
      "v_mul_f32 v217, v162, v217\n"
      "v_max_f32_dpp v172, v201, v201 row_ror:8 row_mask:0xf bank_mask:0xf\n"
      "v_mfma_f32_16x16x32_bf16 v[156:159], a[248:251], v[192:195], v[156:159]\n"
      "s_nop 0\n"
      "v_max_f32_dpp v172, v172, v172 row_ror:4 row_mask:0xf bank_mask:0xf\n"
      "v_cvt_pk_bf16_f32 v216, v216, v217\n"
      "v_mfma_f32_16x16x32_bf16 v[156:159], v[4:7], v[166:169], v[156:159]\n"
      "v_max_f32_dpp v172, v172, v172 row_ror:2 row_mask:0xf bank_mask:0xf\n"
      "v_mul_f32 v218, v162, v218\n"
      "v_mul_f32 v219, v162, v219\n"
      "v_max_f32_dpp v172, v172, v172 row_ror:1 row_mask:0xf bank_mask:0xf\n"
      "v_cvt_pk_bf16_f32 v217, v218, v219\n"
      "s_nop 0\n"
      "v_max_f32_dpp v172, v172, v172 row_bcast:15 row_mask:0xa bank_mask:0xf\n"
      "v_mul_f32 v232, v162, v232\n"
      "s_nop 0\n"
      "v_max_f32_dpp v172, v172, v172 row_bcast:31 row_mask:0xc bank_mask:0xf\n"
      "v_mul_f32_e64 v233, v162, v233\n"
      "v_readlane_b32 s31, v172, 63\n"
      "s_waitcnt lgkmcnt(0)\n"
      "v_mfma_f32_16x16x32_bf16 v[148:151], v[8:11], v[224:227], v[148:151]\n"
      "ds_read_b128 v[192:195], v123 offset:512\n"
      "v_subrev_f32 v201, s31, v201\n"
      "v_mfma_f32_16x16x32_bf16 v[148:151], v[20:23], v[220:223], v[148:151]\n"
      "v_exp_f32 v201, v201\n"
      "v_cvt_pk_bf16_f32 v218, v232, v233\n"
      "v_cvt_pk_bf16_f32 v201, v201, v201\n"
      "v_mul_f32 v234, v162, v234\n"
      "v_mul_f32 v235, v162, v235\n"
      "v_cvt_pk_bf16_f32 v219, v234, v235\n"
      "ds_write_b16 v130, v201\n"
      "v_mfma_f32_16x16x32_bf16 v[152:155], v[12:15], v[224:227], v[152:155]\n"
      "ds_read_b128 v[172:175], v131\n"
      "v_mfma_f32_16x16x32_bf16 v[152:155], v[24:27], v[220:223], v[152:155]\n"
      "ds_read_b128 v[176:179], v131 offset:64\n"
      "v_mul_f32 v228, v162, v228\n"
      "v_mfma_f32_16x16x32_bf16 v[156:159], v[16:19], v[224:227], v[156:159]\n"
      "ds_read_b128 v[224:227], v123 offset:3584\n"
      "v_mul_f32 v229, v162, v229\n"
      "v_mfma_f32_16x16x32_bf16 v[156:159], v[28:31], v[220:223], v[156:159]\n"
      "ds_write_b128 v124, v[216:219] offset:18944\n"
      "v_cvt_pk_bf16_f32 v228, v228, v229\n"
      "v_mul_f32 v230, v162, v230\n"
      "v_mul_f32 v231, v162, v231\n"
      "v_cvt_pk_bf16_f32 v229, v230, v231\n"
      "v_fma_f32 v230, v148, v148, s39\n"
      "v_fmac_f32 v230, v149, v149\n"
      "v_fmac_f32 v230, v150, v150\n"
      "v_fmac_f32_e64 v230, v151, v151\n"
      "v_fmac_f32_e64 v230, v152, v152\n"
      "v_fmac_f32_e64 v230, v153, v153\n"
      "ds_write_b64 v125, v[228:229] offset:18944\n"
      "v_fmac_f32_e64 v230, v154, v154\n"
      "ds_read_b128 v[216:219], v123 offset:6656\n"
      "ds_read_b128 v[220:223], v123 offset:9728\n"
      "ds_read_b128 v[232:235], v123 offset:12800\n"
      "ds_read_b128 v[212:215], v123 offset:15872\n"
      "s_waitcnt lgkmcnt(5)\n"
      "v_mfma_f32_16x16x32_bf16 v[160:163], a[216:219], v[192:195], 0\n"
      "v_fmac_f32 v230, v155, v155\n"
      "v_mfma_f32_16x16x32_bf16 v[160:163], a[228:231], v[224:227], v[160:163]\n"
      "v_fmac_f32_e64 v230, v156, v156\n"
      "v_mfma_f32_16x16x32_bf16 v[164:167], a[220:223], v[192:195], 0\n"
      "v_mfma_f32_16x16x32_bf16 v[164:167], a[232:235], v[224:227], v[164:167]\n"
      "v_mfma_f32_16x16x32_bf16 v[168:171], a[224:227], v[192:195], 0\n"
      "v_fmac_f32_e64 v230, v157, v157\n"
      "v_mfma_f32_16x16x32_bf16 v[168:171], a[236:239], v[224:227], v[168:171]\n"
      "v_fmac_f32 v230, v158, v158\n"
      "s_waitcnt lgkmcnt(0)\n"
      "v_mfma_f32_16x16x32_bf16 v[160:163], a[240:243], v[216:219], v[160:163]\n"
      "v_fmac_f32_e64 v230, v159, v159\n"
      "v_mfma_f32_16x16x32_bf16 v[160:163], a[252:255], v[220:223], v[160:163]\n"
      "v_mfma_f32_16x16x32_bf16 v[164:167], a[244:247], v[216:219], v[164:167]\n"
      "v_mfma_f32_16x16x32_bf16 v[164:167], v[0:3], v[220:223], v[164:167]\n"
      "v_mfma_f32_16x16x32_bf16 v[168:171], a[248:251], v[216:219], v[168:171]\n"
      "v_mfma_f32_16x16x32_bf16 v[168:171], v[4:7], v[220:223], v[168:171]\n"
      "v_permlane16_swap_b32 v206, v230\n"
      "v_mfma_f32_16x16x32_bf16 v[160:163], v[8:11], v[232:235], v[160:163]\n"
      "v_add_f32 v206, v206, v230\n"
      "v_mfma_f32_16x16x32_bf16 v[160:163], v[20:23], v[212:215], v[160:163]\n"
      "v_mfma_f32_16x16x32_bf16 v[164:167], v[12:15], v[232:235], v[164:167]\n"
      "v_mfma_f32_16x16x32_bf16 v[164:167], v[24:27], v[212:215], v[164:167]\n"
      "v_mfma_f32_16x16x32_bf16 v[168:171], v[16:19], v[232:235], v[168:171]\n"
      "v_mfma_f32_16x16x32_bf16 v[168:171], v[28:31], v[212:215], v[168:171]\n"
      "s_nop 3\n"
      "v_fma_f32 v201, v160, v160, s39\n"
      "v_fmac_f32 v201, v161, v161\n"
      "v_fmac_f32 v201, v162, v162\n"
      "v_fmac_f32 v201, v163, v163\n"
      "v_fmac_f32 v201, v164, v164\n"
      "v_fmac_f32 v201, v165, v165\n"
      "v_fmac_f32 v201, v166, v166\n"
      "v_fmac_f32 v201, v167, v167\n"
      "v_fmac_f32 v201, v168, v168\n"
      "v_fmac_f32 v201, v169, v169\n"
      "v_fmac_f32 v201, v170, v170\n"
      "v_fmac_f32 v201, v171, v171\n"
      "v_mov_b32 v236, v201\n"
      "s_nop 1\n"
      "v_permlane16_swap_b32 v201, v236\n"
      "v_add_f32 v201, v201, v236\n"
      "s_nop 1\n"
      "v_permlane32_swap_b32 v206, v201\n"
      "v_add_f32 v206, v206, v201\n"
      "ds_write_b32 v129, v206 offset:1024\n"
      "s_branch L_join_22_%=\n"
      "L_edge_1_%=:\n"
      "s_mov_b32 s11, s29\n"
      "s_sub_u32 s28, s60, s8\n"
      "s_sub_u32 s29, s61, s9\n"
      "s_mov_b32 s60, s8\n"
      "s_mov_b32 s61, s9\n"
      "s_mov_b64 exec, s[64:65]\n"
      "v_add_u32 v116, s28, v116\n"
      "s_mov_b64 exec, s[70:71]\n"
      "v_add_u32 v116, s29, v116\n"
      "s_mov_b64 exec, s[66:67]\n"
      "v_add_u32 v117, s28, v117\n"
      "s_mov_b64 exec, s[72:73]\n"
      "v_add_u32 v117, s29, v117\n"
      "s_mov_b64 exec, s[68:69]\n"
      "v_add_u32 v118, s28, v118\n"
      "s_mov_b64 exec, s[74:75]\n"
      "v_add_u32 v118, s29, v118\n"
      "s_mov_b64 exec, -1\n"
      "s_nop 4\n"
      "s_branch L_eb_2_%=\n"
      "L_edge_3_%=:\n"
      "s_mov_b32 s11, s29\n"
      "s_sub_u32 s28, s60, s8\n"
      "s_sub_u32 s29, s61, s9\n"
      "s_mov_b32 s60, s8\n"
      "s_mov_b32 s61, s9\n"
      "s_mov_b64 exec, s[64:65]\n"
      "v_add_u32 v116, s28, v116\n"
      "s_mov_b64 exec, s[70:71]\n"
      "v_add_u32 v116, s29, v116\n"
      "s_mov_b64 exec, s[66:67]\n"
      "v_add_u32 v117, s28, v117\n"
      "s_mov_b64 exec, s[72:73]\n"
      "v_add_u32 v117, s29, v117\n"
      "s_mov_b64 exec, s[68:69]\n"
      "v_add_u32 v118, s28, v118\n"
      "s_mov_b64 exec, s[74:75]\n"
      "v_add_u32 v118, s29, v118\n"
      "s_mov_b64 exec, -1\n"
      "s_nop 4\n"
      "s_branch L_eb_4_%=\n"
      "L_edge_5_%=:\n"
      "s_mov_b32 s11, s29\n"
      "s_sub_u32 s28, s60, s8\n"
      "s_sub_u32 s29, s61, s9\n"
      "s_mov_b32 s60, s8\n"
      "s_mov_b32 s61, s9\n"
      "s_mov_b64 exec, s[64:65]\n"
      "v_add_u32 v116, s28, v116\n"
      "s_mov_b64 exec, s[70:71]\n"
      "v_add_u32 v116, s29, v116\n"
      "s_mov_b64 exec, s[66:67]\n"
      "v_add_u32 v117, s28, v117\n"
      "s_mov_b64 exec, s[72:73]\n"
      "v_add_u32 v117, s29, v117\n"
      "s_mov_b64 exec, s[68:69]\n"
      "v_add_u32 v118, s28, v118\n"
      "s_mov_b64 exec, s[74:75]\n"
      "v_add_u32 v118, s29, v118\n"
      "s_mov_b64 exec, -1\n"
      "s_nop 4\n"
      "s_branch L_eb_6_%=\n"
      "L_edge_11_%=:\n"
      "s_mov_b32 s11, s29\n"
      "s_sub_u32 s28, s60, s8\n"
      "s_sub_u32 s29, s61, s9\n"
      "s_mov_b32 s60, s8\n"
      "s_mov_b32 s61, s9\n"
      "s_mov_b64 exec, s[64:65]\n"
      "v_add_u32 v116, s28, v116\n"
      "s_mov_b64 exec, s[70:71]\n"
      "v_add_u32 v116, s29, v116\n"
      "s_mov_b64 exec, s[66:67]\n"
      "v_add_u32 v117, s28, v117\n"
      "s_mov_b64 exec, s[72:73]\n"
      "v_add_u32 v117, s29, v117\n"
      "s_mov_b64 exec, s[68:69]\n"
      "v_add_u32 v118, s28, v118\n"
      "s_mov_b64 exec, s[74:75]\n"
      "v_add_u32 v118, s29, v118\n"
      "s_mov_b64 exec, -1\n"
      "s_nop 4\n"
      "s_branch L_eb_12_%=\n"
      "L_edge_14_%=:\n"
      "s_mov_b32 s11, s29\n"
      "s_sub_u32 s28, s60, s8\n"
      "s_sub_u32 s29, s61, s9\n"
      "s_mov_b32 s60, s8\n"
      "s_mov_b32 s61, s9\n"
      "s_mov_b64 exec, s[64:65]\n"
      "v_add_u32 v116, s28, v116\n"
      "s_mov_b64 exec, s[70:71]\n"
      "v_add_u32 v116, s29, v116\n"
      "s_mov_b64 exec, s[66:67]\n"
      "v_add_u32 v117, s28, v117\n"
      "s_mov_b64 exec, s[72:73]\n"
      "v_add_u32 v117, s29, v117\n"
      "s_mov_b64 exec, s[68:69]\n"
      "v_add_u32 v118, s28, v118\n"
      "s_mov_b64 exec, s[74:75]\n"
      "v_add_u32 v118, s29, v118\n"
      "s_mov_b64 exec, -1\n"
      "s_nop 4\n"
      "s_branch L_eb_15_%=\n"
      "L_edge_16_%=:\n"
      "s_mov_b32 s11, s29\n"
      "s_sub_u32 s28, s60, s8\n"
      "s_sub_u32 s29, s61, s9\n"
      "s_mov_b32 s60, s8\n"
      "s_mov_b32 s61, s9\n"
      "s_mov_b64 exec, s[64:65]\n"
      "v_add_u32 v116, s28, v116\n"
      "s_mov_b64 exec, s[70:71]\n"
      "v_add_u32 v116, s29, v116\n"
      "s_mov_b64 exec, s[66:67]\n"
      "v_add_u32 v117, s28, v117\n"
      "s_mov_b64 exec, s[72:73]\n"
      "v_add_u32 v117, s29, v117\n"
      "s_mov_b64 exec, s[68:69]\n"
      "v_add_u32 v118, s28, v118\n"
      "s_mov_b64 exec, s[74:75]\n"
      "v_add_u32 v118, s29, v118\n"
      "s_mov_b64 exec, -1\n"
      "s_nop 4\n"
      "s_branch L_eb_17_%=\n"
      "L_edge_18_%=:\n"
      "s_mov_b32 s11, s29\n"
      "s_sub_u32 s28, s60, s8\n"
      "s_sub_u32 s29, s61, s9\n"
      "s_mov_b32 s60, s8\n"
      "s_mov_b32 s61, s9\n"
      "s_mov_b64 exec, s[64:65]\n"
      "v_add_u32 v116, s28, v116\n"
      "s_mov_b64 exec, s[70:71]\n"
      "v_add_u32 v116, s29, v116\n"
      "s_mov_b64 exec, s[66:67]\n"
      "v_add_u32 v117, s28, v117\n"
      "s_mov_b64 exec, s[72:73]\n"
      "v_add_u32 v117, s29, v117\n"
      "s_mov_b64 exec, s[68:69]\n"
      "v_add_u32 v118, s28, v118\n"
      "s_mov_b64 exec, s[74:75]\n"
      "v_add_u32 v118, s29, v118\n"
      "s_mov_b64 exec, -1\n"
      "s_nop 4\n"
      "s_branch L_eb_19_%=\n"
      "L_edge_24_%=:\n"
      "s_mov_b32 s11, s29\n"
      "s_sub_u32 s28, s60, s8\n"
      "s_sub_u32 s29, s61, s9\n"
      "s_mov_b32 s60, s8\n"
      "s_mov_b32 s61, s9\n"
      "s_mov_b64 exec, s[64:65]\n"
      "v_add_u32 v116, s28, v116\n"
      "s_mov_b64 exec, s[70:71]\n"
      "v_add_u32 v116, s29, v116\n"
      "s_mov_b64 exec, s[66:67]\n"
      "v_add_u32 v117, s28, v117\n"
      "s_mov_b64 exec, s[72:73]\n"
      "v_add_u32 v117, s29, v117\n"
      "s_mov_b64 exec, s[68:69]\n"
      "v_add_u32 v118, s28, v118\n"
      "s_mov_b64 exec, s[74:75]\n"
      "v_add_u32 v118, s29, v118\n"
      "s_mov_b64 exec, -1\n"
      "s_nop 4\n"
      "s_branch L_eb_25_%=\n"
      "L_edge_27_%=:\n"
      "s_mov_b32 s11, s29\n"
      "s_sub_u32 s28, s60, s8\n"
      "s_sub_u32 s29, s61, s9\n"
      "s_mov_b32 s60, s8\n"
      "s_mov_b32 s61, s9\n"
      "s_mov_b64 exec, s[64:65]\n"
      "v_add_u32 v116, s28, v116\n"
      "s_mov_b64 exec, s[70:71]\n"
      "v_add_u32 v116, s29, v116\n"
      "s_mov_b64 exec, s[66:67]\n"
      "v_add_u32 v117, s28, v117\n"
      "s_mov_b64 exec, s[72:73]\n"
      "v_add_u32 v117, s29, v117\n"
      "s_mov_b64 exec, s[68:69]\n"
      "v_add_u32 v118, s28, v118\n"
      "s_mov_b64 exec, s[74:75]\n"
      "v_add_u32 v118, s29, v118\n"
      "s_mov_b64 exec, -1\n"
      "s_nop 4\n"
      "s_branch L_eb_28_%=\n"
      "L_edge_29_%=:\n"
      "s_mov_b32 s11, s29\n"
      "s_sub_u32 s28, s60, s8\n"
      "s_sub_u32 s29, s61, s9\n"
      "s_mov_b32 s60, s8\n"
      "s_mov_b32 s61, s9\n"
      "s_mov_b64 exec, s[64:65]\n"
      "v_add_u32 v116, s28, v116\n"
      "s_mov_b64 exec, s[70:71]\n"
      "v_add_u32 v116, s29, v116\n"
      "s_mov_b64 exec, s[66:67]\n"
      "v_add_u32 v117, s28, v117\n"
      "s_mov_b64 exec, s[72:73]\n"
      "v_add_u32 v117, s29, v117\n"
      "s_mov_b64 exec, s[68:69]\n"
      "v_add_u32 v118, s28, v118\n"
      "s_mov_b64 exec, s[74:75]\n"
      "v_add_u32 v118, s29, v118\n"
      "s_mov_b64 exec, -1\n"
      "s_nop 4\n"
      "s_branch L_eb_30_%=\n"
      "L_edge_31_%=:\n"
      "s_mov_b32 s11, s29\n"
      "s_sub_u32 s28, s60, s8\n"
      "s_sub_u32 s29, s61, s9\n"
      "s_mov_b32 s60, s8\n"
      "s_mov_b32 s61, s9\n"
      "s_mov_b64 exec, s[64:65]\n"
      "v_add_u32 v116, s28, v116\n"
      "s_mov_b64 exec, s[70:71]\n"
      "v_add_u32 v116, s29, v116\n"
      "s_mov_b64 exec, s[66:67]\n"
      "v_add_u32 v117, s28, v117\n"
      "s_mov_b64 exec, s[72:73]\n"
      "v_add_u32 v117, s29, v117\n"
      "s_mov_b64 exec, s[68:69]\n"
      "v_add_u32 v118, s28, v118\n"
      "s_mov_b64 exec, s[74:75]\n"
      "v_add_u32 v118, s29, v118\n"
      "s_mov_b64 exec, -1\n"
      "s_nop 4\n"
      "s_branch L_eb_32_%=\n"
      "L_edge_33_%=:\n"
      "s_mov_b32 s11, s29\n"
      "s_sub_u32 s28, s60, s8\n"
      "s_sub_u32 s29, s61, s9\n"
      "s_mov_b32 s60, s8\n"
      "s_mov_b32 s61, s9\n"
      "s_mov_b64 exec, s[64:65]\n"
      "v_add_u32 v116, s28, v116\n"
      "s_mov_b64 exec, s[70:71]\n"
      "v_add_u32 v116, s29, v116\n"
      "s_mov_b64 exec, s[66:67]\n"
      "v_add_u32 v117, s28, v117\n"
      "s_mov_b64 exec, s[72:73]\n"
      "v_add_u32 v117, s29, v117\n"
      "s_mov_b64 exec, s[68:69]\n"
      "v_add_u32 v118, s28, v118\n"
      "s_mov_b64 exec, s[74:75]\n"
      "v_add_u32 v118, s29, v118\n"
      "s_mov_b64 exec, -1\n"
      "s_nop 4\n"
      "s_branch L_eb_34_%=\n"
      "L_edge_35_%=:\n"
      "s_mov_b32 s11, s29\n"
      "s_sub_u32 s28, s60, s8\n"
      "s_sub_u32 s29, s61, s9\n"
      "s_mov_b32 s60, s8\n"
      "s_mov_b32 s61, s9\n"
      "s_mov_b64 exec, s[64:65]\n"
      "v_add_u32 v116, s28, v116\n"
      "s_mov_b64 exec, s[70:71]\n"
      "v_add_u32 v116, s29, v116\n"
      "s_mov_b64 exec, s[66:67]\n"
      "v_add_u32 v117, s28, v117\n"
      "s_mov_b64 exec, s[72:73]\n"
      "v_add_u32 v117, s29, v117\n"
      "s_mov_b64 exec, s[68:69]\n"
      "v_add_u32 v118, s28, v118\n"
      "s_mov_b64 exec, s[74:75]\n"
      "v_add_u32 v118, s29, v118\n"
      "s_mov_b64 exec, -1\n"
      "s_nop 4\n"
      "s_branch L_eb_36_%=\n"
      "L_edge_38_%=:\n"
      "s_mov_b32 s11, s29\n"
      "s_sub_u32 s28, s60, s8\n"
      "s_sub_u32 s29, s61, s9\n"
      "s_mov_b32 s60, s8\n"
      "s_mov_b32 s61, s9\n"
      "s_mov_b64 exec, s[64:65]\n"
      "v_add_u32 v116, s28, v116\n"
      "s_mov_b64 exec, s[70:71]\n"
      "v_add_u32 v116, s29, v116\n"
      "s_mov_b64 exec, s[66:67]\n"
      "v_add_u32 v117, s28, v117\n"
      "s_mov_b64 exec, s[72:73]\n"
      "v_add_u32 v117, s29, v117\n"
      "s_mov_b64 exec, s[68:69]\n"
      "v_add_u32 v118, s28, v118\n"
      "s_mov_b64 exec, s[74:75]\n"
      "v_add_u32 v118, s29, v118\n"
      "s_mov_b64 exec, -1\n"
      "s_nop 4\n"
      "s_branch L_eb_39_%=\n"
      "L_edge_40_%=:\n"
      "s_mov_b32 s11, s29\n"
      "s_sub_u32 s28, s60, s8\n"
      "s_sub_u32 s29, s61, s9\n"
      "s_mov_b32 s60, s8\n"
      "s_mov_b32 s61, s9\n"
      "s_mov_b64 exec, s[64:65]\n"
      "v_add_u32 v116, s28, v116\n"
      "s_mov_b64 exec, s[70:71]\n"
      "v_add_u32 v116, s29, v116\n"
      "s_mov_b64 exec, s[66:67]\n"
      "v_add_u32 v117, s28, v117\n"
      "s_mov_b64 exec, s[72:73]\n"
      "v_add_u32 v117, s29, v117\n"
      "s_mov_b64 exec, s[68:69]\n"
      "v_add_u32 v118, s28, v118\n"
      "s_mov_b64 exec, s[74:75]\n"
      "v_add_u32 v118, s29, v118\n"
      "s_mov_b64 exec, -1\n"
      "s_nop 4\n"
      "s_branch L_eb_41_%=\n"
      "L_edge_42_%=:\n"
      "s_mov_b32 s11, s29\n"
      "s_sub_u32 s28, s60, s8\n"
      "s_sub_u32 s29, s61, s9\n"
      "s_mov_b32 s60, s8\n"
      "s_mov_b32 s61, s9\n"
      "s_mov_b64 exec, s[64:65]\n"
      "v_add_u32 v116, s28, v116\n"
      "s_mov_b64 exec, s[70:71]\n"
      "v_add_u32 v116, s29, v116\n"
      "s_mov_b64 exec, s[66:67]\n"
      "v_add_u32 v117, s28, v117\n"
      "s_mov_b64 exec, s[72:73]\n"
      "v_add_u32 v117, s29, v117\n"
      "s_mov_b64 exec, s[68:69]\n"
      "v_add_u32 v118, s28, v118\n"
      "s_mov_b64 exec, s[74:75]\n"
      "v_add_u32 v118, s29, v118\n"
      "s_mov_b64 exec, -1\n"
      "s_nop 4\n"
      "s_branch L_eb_43_%=\n"
      "L_cend_%=:\n"
      :
      : "s"(__builtin_amdgcn_kernarg_segment_ptr()), "s"(wave), "s"(blockIdx.x), "s"(lds0)
      : "v0","v1","v2","v3","v4","v5","v6","v7","v8","v9","v10","v11","v12","v13","v14","v15","v16","v17","v18","v19","v20","v21","v22","v23","v24","v25","v26","v27","v28","v29","v30","v31","v32","v33","v34","v35","v36","v37","v38","v39","v40","v41","v42","v43","v44","v45","v46","v47","v48","v49","v50","v51","v52","v53","v54","v55","v56","v57","v58","v59","v60","v61","v62","v63","v64","v65","v66","v67","v68","v69","v70","v71","v72","v73","v74","v75","v76","v77","v78","v79","v80","v81","v82","v83","v84","v85","v86","v87","v88","v89","v90","v91","v92","v93","v94","v95","v96","v97","v98","v99","v100","v101","v102","v103","v104","v105","v106","v107","v108","v109","v110","v111","v112","v113","v114","v115","v116","v117","v118","v119","v120","v121","v122","v123","v124","v125","v126","v127","v128","v129","v130","v131","v132","v133","v134","v135","v136","v137","v138","v139","v140","v141","v142","v143","v144","v145","v146","v147","v148","v149","v150","v151","v152","v153","v154","v155","v156","v157","v158","v159","v160","v161","v162","v163","v164","v165","v166","v167","v168","v169","v170","v171","v172","v173","v174","v175","v176","v177","v178","v179","v180","v181","v182","v183","v184","v185","v186","v187","v188","v189","v190","v191","v192","v193","v194","v195","v196","v197","v198","v199","v200","v201","v202","v203","v204","v205","v206","v207","v208","v209","v210","v211","v212","v213","v214","v215","v216","v217","v218","v219","v220","v221","v222","v223","v224","v225","v226","v227","v228","v229","v230","v231","v232","v233","v234","v235","v236","v237","v238","v239","v240","v241","v242","v243","v244","v245","v246","v247","v248","v249","v250","v251","v252","v253","v254","v255","a0","a1","a2","a3","a4","a5","a6","a7","a8","a9","a10","a11","a12","a13","a14","a15","a16","a17","a18","a19","a20","a21","a22","a23","a24","a25","a26","a27","a28","a29","a30","a31","a32","a33","a34","a35","a36","a37","a38","a39","a40","a41","a42","a43","a44","a45","a46","a47","a48","a49","a50","a51","a52","a53","a54","a55","a56","a57","a58","a59","a60","a61","a62","a63","a64","a65","a66","a67","a68","a69","a70","a71","a72","a73","a74","a75","a76","a77","a78","a79","a80","a81","a82","a83","a84","a85","a86","a87","a88","a89","a90","a91","a92","a93","a94","a95","a96","a97","a98","a99","a100","a101","a102","a103","a104","a105","a106","a107","a108","a109","a110","a111","a112","a113","a114","a115","a116","a117","a118","a119","a120","a121","a122","a123","a124","a125","a126","a127","a128","a129","a130","a131","a132","a133","a134","a135","a136","a137","a138","a139","a140","a141","a142","a143","a144","a145","a146","a147","a148","a149","a150","a151","a152","a153","a154","a155","a156","a157","a158","a159","a160","a161","a162","a163","a164","a165","a166","a167","a168","a169","a170","a171","a172","a173","a174","a175","a176","a177","a178","a179","a180","a181","a182","a183","a184","a185","a186","a187","a188","a189","a190","a191","a192","a193","a194","a195","a196","a197","a198","a199","a200","a201","a202","a203","a204","a205","a206","a207","a208","a209","a210","a211","a212","a213","a214","a215","a216","a217","a218","a219","a220","a221","a222","a223","a224","a225","a226","a227","a228","a229","a230","a231","a232","a233","a234","a235","a236","a237","a238","a239","a240","a241","a242","a243","a244","a245","a246","a247","a248","a249","a250","a251","a252","a253","a254","a255","s2","s3","s4","s5","s6","s7","s8","s9","s10","s11","s12","s13","s16","s17","s18","s19","s20","s21","s22","s23","s24","s25","s26","s27","s28","s29","s30","s31","s34","s35","s36","s37","s38","s39","s40","s41","s42","s43","s44","s45","s46","s47","s48","s49","s50","s51","s52","s53","s54","s55","s56","s57","s58","s59","s60","s61","s62","s63","s64","s65","s66","s67","s68","s69","s70","s71","s72","s73","s74","s75","s76","s77","s78","s79","s80","s81","s82","s83","s84","s85","s86","s87","s88","s89","s90","s91","s92","s93","s94","s95","s96","s97","s98","s99","s100", "vcc", "scc", "memory");
}

}

extern "C" void kernel_launch(void* const* d_in, const int* in_sizes, int n_in, void* d_out, int out_size, void* d_ws,
                              size_t ws_size, hipStream_t stream) {
  const uint16_t* x = (const uint16_t*)d_in[0];
  const uint16_t* wqkv = (const uint16_t*)d_in[1];
  const uint16_t* wo = (const uint16_t*)d_in[2];
  const uint16_t* ls = (const uint16_t*)d_in[3];
  const uint16_t* w1 = (const uint16_t*)d_in[4];
  const uint16_t* w2 = (const uint16_t*)d_in[5];
  const uint16_t* rc = (const uint16_t*)d_in[6];
  const int* ridx = (const int*)d_in[7];
  const uint16_t* mask = (const uint16_t*)d_in[8];
  swin::swin_main<<<256, 256, 0, stream>>>(x, mask, (char*)d_ws, (uint16_t*)d_out, wqkv, wo, ls, w1, w2, rc, ridx);
}
```

```cpp
#include <hip/hip_runtime.h>
#include <hip/hip_bf16.h>
#include <stdint.h>

namespace swin {

constexpr unsigned LDS_BYTES = 163840u;

__device__ __attribute__((aligned(256))) const unsigned CT_TAB[26704] = {0,64,0,0xc0000,144,208,16,0xc0010,288,352,32,0xc0020,432,496,48,0xc0030,576,512,64,0xc0040,720,656,80,0xc0050,864,800,96,0xc0060,1008,944,112,0xc0070,1024,1088,400,0xc0190,1168,1232,384,0xc0180,1312,1376,432,0xc01b0,1456,1520,416,0xc01a0,1600,1536,464,0xc01d0,1744,1680,448,0xc01c0,1888,1824,496,0xc01f0,2032,1968,480,0xc01e0,16,80,800,0xc0320,128,192,816,0xc0330,304,368,768,0xc0300,416,480,784,0xc0310,592,528,864,0xc0360,704,640,880,0xc0370,880,816,832,0xc0340,992,928,848,0xc0350,1040,1104,1200,0xef8b0,1152,1216,1184,0xef8a0,1328,1392,1168,0xef890,1440,1504,1152,0xef880,1616,1552,1264,0xef8f0,1728,1664,1248,0xef8e0,1904,1840,1232,0xef8d0,2016,1952,1216,0xef8c0,32,96,1600,0xefa40,176,240,1616,0xefa50,256,320,1632,0xefa60,400,464,1648,0xefa70,608,544,1536,0xefa00,752,688,1552,0xefa10,832,768,1568,0xefa20,976,912,1584,0xefa30,1056,1120,2000,0xefbd0,1200,1264,1984,0xefbc0,1280,1344,2032,0xefbf0,1424,1488,2016,0xefbe0,1632,1568,1936,0xefb90,1776,1712,1920,0xefb80,1856,1792,1968,0xefbb0,2000,1936,1952,0xefba0,48,112,2400,0xefd60,160,224,2416,0xefd70,272,336,2368,0xefd40,384,448,2384,0xefd50,624,560,2336,0xefd20,736,672,2352,0xefd30,848,784,2304,0xefd00,960,896,2320,0xefd10,1072,1136,0x2fef0,0xefef0,1184,1248,0x2fee0,0xefee0,1296,1360,0x2fed0,0xefed0,1408,1472,0x2fec0,0xefec0,1648,1584,0x2feb0,0xefeb0,1760,1696,0x2fea0,0xefea0,1872,1808,0x2fe90,0xefe90,1984,1920,0x2fe80,0xefe80,0xc0100,73728,77824,90112,0xc0110,73744,77840,90128,0xc0120,73760,77856,90144,0xc0130,73776,77872,90160,0xc0140,73792,77888,90176,0xc0150,73808,77904,90192,0xc0160,73824,77920,90208,0xc0170,73840,77936,90224,0xc0290,73856,77952,90240,0xc0280,73872,77968,90256,0xc02b0,73888,77984,90272,0xc02a0,73904,78000,90288,0xc02d0,73920,78016,90304,0xc02c0,73936,78032,90320,0xc02f0,73952,78048,90336,0xc02e0,73968,78064,90352,0xc0420,73984,78080,90368,0xc0430,74000,78096,90384,0xc0400,74016,78112,90400,0xc0410,74032,78128,90416,0xc0460,74048,78144,90432,0xc0470,74064,78160,90448,0xc0440,74080,78176,90464,0xc0450,74096,78192,90480,0xef9b0,74112,78208,90496,0xef9a0,74128,78224,90512,0xef990,74144,78240,90528,0xef980,74160,78256,90544,0xef9f0,74176,78272,90560,0xef9e0,74192,78288,90576,0xef9d0,74208,78304,90592,0xef9c0,74224,78320,90608,0xc0100,74240,78336,90624,0xc0110,74256,78352,90640,0xc0120,74272,78368,90656,0xc0130,74288,78384,90672,0xc0140,74304,78400,90688,0xc0150,74320,78416,90704,0xc0160,74336,78432,90720,0xc0170,74352,78448,90736,0xc0290,74368,78464,90752,0xc0280,74384,78480,90768,0xc02b0,74400,78496,90784,0xc02a0,74416,78512,90800,0xc02d0,74432,78528,90816,0xc02c0,74448,78544,90832,0xc02f0,74464,78560,90848,0xc02e0,74480,78576,90864,0xc0420,74496,78592,90880,0xc0430,74512,78608,90896,0xc0400,74528,78624,90912,0xc0410,74544,78640,90928,0xc0460,74560,78656,90944,0xc0470,74576,78672,90960,0xc0440,74592,78688,90976,0xc0450,74608,78704,90992,0xef9b0,74624,78720,91008,0xef9a0,74640,78736,91024,0xef990,74656,78752,91040,0xef980,74672,78768,91056,0xef9f0,74688,78784,91072,0xef9e0,74704,78800,91088,0xef9d0,74720,78816,91104,0xef9c0,74736,78832,91120,0x1c000,36864,36864,49152,0x1c010,36880,36880,49168,0x1c020,36896,36896,49184,0x1c030,36912,36912,49200,0x1c040,36928,36928,49216,0x1c050,36944,36944,49232,0x1c060,36960,36960,49248,0x1c070,36976,36976,49264,0x1c080,36992,36992,49280,0x1c090,37008,37008,49296,0x1c0a0,37024,37024,49312,0x1c0b0,37040,37040,49328,0x1c0c0,37056,37056,49344,0x1c0d0,37072,37072,49360,0x1c0e0,37088,37088,49376,0x1c0f0,37104,37104,49392,0x1c100,37632,37632,49920,0x1c110,37648,37648,49936,0x1c120,37664,37664,49952,0x1c130,37680,37680,49968,0x1c140,37696,37696,49984,0x1c150,37712,37712,50000,0x1c160,37728,37728,50016,0x1c170,37744,37744,50032,0x1c180,37760,37760,50048,0x1c190,37776,37776,50064,0x1c1a0,37792,37792,50080,0x1c1b0,37808,37808,50096,0x1c1c0,37824,37824,50112,0x1c1d0,37840,37840,50128,0x1c1e0,37856,37856,50144,0x1c1f0,37872,37872,50160,0x1c200,38400,38400,50688,0x1c210,38416,38416,50704,0x1c220,38432,38432,50720,0x1c230,38448,38448,50736,0x1c240,38464,38464,50752,0x1c250,38480,38480,50768,0x1c260,38496,38496,50784,0x1c270,38512,38512,50800,0x1c280,38528,38528,50816,0x1c290,38544,38544,50832,0x1c2a0,38560,38560,50848,0x1c2b0,38576,38576,50864,0x1c2c0,38592,38592,50880,0x1c2d0,38608,38608,50896,0x1c2e0,38624,38624,50912,0x1c2f0,38640,38640,50928,0x1c300,39168,39168,51456,0x1c310,39184,39184,51472,0x1c320,39200,39200,51488,0x1c330,39216,39216,51504,0x1c340,39232,39232,51520,0x1c350,39248,39248,51536,0x1c360,39264,39264,51552,0x1c370,39280,39280,51568,0x1c380,39296,39296,51584,0x1c390,39312,39312,51600,0x1c3a0,39328,39328,51616,0x1c3b0,39344,39344,51632,0x1c3c0,39360,39360,51648,0x1c3d0,39376,39376,51664,0x1c3e0,39392,39392,51680,0x1c3f0,39408,39408,51696,73728,73792,0x1d600,0x1d600,73872,73936,0x1d610,0x1d610,74016,74080,0x1d620,0x1d620,74160,74224,0x1d630,0x1d630,74304,74240,0x1d640,0x1d640,74448,74384,0x1d650,0x1d650,74592,74528,0x1d660,0x1d660,74736,74672,0x1d670,0x1d670,74752,74816,0x1d680,0x1d680,74896,74960,0x1d690,0x1d690,75040,75104,0x1d6a0,0x1d6a0,75184,75248,0x1d6b0,0x1d6b0,75328,75264,0x1d6c0,0x1d6c0,75472,75408,0x1d6d0,0x1d6d0,75616,75552,0x1d6e0,0x1d6e0,75760,75696,0x1d6f0,0x1d6f0,73744,73800,0x1d700,0x1d700,73856,73944,0x1d710,0x1d710,74032,74088,0x1d720,0x1d720,74144,74232,0x1d730,0x1d730,74320,74248,0x1d740,0x1d740,74432,74392,0x1d750,0x1d750,74608,74536,0x1d760,0x1d760,74720,74680,0x1d770,0x1d770,74768,74824,0x1d780,0x1d780,74880,74968,0x1d790,0x1d790,75056,75112,0x1d7a0,0x1d7a0,75168,75256,0x1d7b0,0x1d7b0,75344,75272,0x1d7c0,0x1d7c0,75456,75416,0x1d7d0,0x1d7d0,75632,75560,0x1d7e0,0x1d7e0,75744,75704,0x1d7f0,0x1d7f0,73760,73808,0x1d800,0x1d800,73904,73920,0x1d810,0x1d810,73984,74096,0x1d820,0x1d820,74128,74208,0x1d830,0x1d830,74336,74256,0x1d840,0x1d840,74480,74368,0x1d850,0x1d850,74560,74544,0x1d860,0x1d860,74704,74656,0x1d870,0x1d870,74784,74832,0x1d880,0x1d880,74928,74944,0x1d890,0x1d890,75008,75120,0x1d8a0,0x1d8a0,75152,75232,0x1d8b0,0x1d8b0,75360,75280,0x1d8c0,0x1d8c0,75504,75392,0x1d8d0,0x1d8d0,75584,75568,0x1d8e0,0x1d8e0,75728,75680,0x1d8f0,0x1d8f0,73776,73816,0x1d900,0x1d900,73888,73928,0x1d910,0x1d910,74000,74104,0x1d920,0x1d920,74112,74216,0x1d930,0x1d930,74352,74264,0x1d940,0x1d940,74464,74376,0x1d950,0x1d950,74576,74552,0x1d960,0x1d960,74688,74664,0x1d970,0x1d970,74800,74840,0x1d980,0x1d980,74912,74952,0x1d990,0x1d990,75024,75128,0x1d9a0,0x1d9a0,75136,75240,0x1d9b0,0x1d9b0,75376,75288,0x1d9c0,0x1d9c0,75488,75400,0x1d9d0,0x1d9d0,75600,75576,0x1d9e0,0x1d9e0,75712,75688,0x1d9f0,0x1d9f0,0x1e600,0x1e600,0x1e800,0x100000,0x1e602,0x100000,0x100000,0x1e800,0x1e604,0x100000,0x100000,0x100000,0x1e606,0x100000,0x100000,0x100000,0x1e610,0x1e600,0x100000,0x100000,0x1e612,0x100000,0x100000,0x1e800,0x1e614,0x100000,0x100000,0x100000,0x1e616,0x100000,0x100000,0x100000,0x1e620,0x1e600,0x100000,0x100000,0x1e622,0x100000,0x100000,0x1e800,0x1e624,0x100000,0x100000,0x100000,0x1e626,0x100000,0x100000,0x100000,0x1e630,0x1e600,0x100000,0x100000,0x1e632,0x100000,0x100000,0x1e800,0x1e634,0x100000,0x100000,0x100000,0x1e636,0x100000,0x100000,0x100000,0x1e608,0x1e610,0x1e830,0x100000,0x1e60a,0x100000,0x100000,0x1e830,0x1e60c,0x100000,0x100000,0x100000,0x1e60e,0x100000,0x100000,0x100000,0x1e618,0x1e610,0x100000,0x100000,0x1e61a,0x100000,0x100000,0x1e830,0x1e61c,0x100000,0x100000,0x100000,0x1e61e,0x100000,0x100000,0x100000,0x1e628,0x1e610,0x100000,0x100000,0x1e62a,0x100000,0x100000,0x1e830,0x1e62c,0x100000,0x100000,0x100000,0x1e62e,0x100000,0x100000,0x100000,0x1e638,0x1e610,0x100000,0x100000,0x1e63a,0x100000,0x100000,0x1e830,0x1e63c,0x100000,0x100000,0x100000,0x1e63e,0x100000,0x100000,0x100000,0x1e640,0x1e620,0x1e860,0x100000,0x1e642,0x100000,0x100000,0x1e860,0x1e644,0x100000,0x100000,0x100000,0x1e646,0x100000,0x100000,0x100000,0x1e650,0x1e620,0x100000,0x100000,0x1e652,0x100000,0x100000,0x1e860,0x1e654,0x100000,0x100000,0x100000,0x1e656,0x100000,0x100000,0x100000,0x1e660,0x1e620,0x100000,0x100000,0x1e662,0x100000,0x100000,0x1e860,0x1e664,0x100000,0x100000,0x100000,0x1e666,0x100000,0x100000,0x100000,0x1e670,0x1e620,0x100000,0x100000,0x1e672,0x100000,0x100000,0x1e860,0x1e674,0x100000,0x100000,0x100000,0x1e676,0x100000,0x100000,0x100000,0x1e648,0x1e630,0x1e890,0x100000,0x1e64a,0x100000,0x100000,0x1e890,0x1e64c,0x100000,0x100000,0x100000,0x1e64e,0x100000,0x100000,0x100000,0x1e658,0x1e630,0x100000,0x100000,0x1e65a,0x100000,0x100000,0x1e890,0x1e65c,0x100000,0x100000,0x100000,0x1e65e,0x100000,0x100000,0x100000,0x1e668,0x1e630,0x100000,0x100000,0x1e66a,0x100000,0x100000,0x1e890,0x1e66c,0x100000,0x100000,0x100000,0x1e66e,0x100000,0x100000,0x100000,0x1e678,0x1e630,0x100000,0x100000,0x1e67a,0x100000,0x100000,0x1e890,0x1e67c,0x100000,0x100000,0x100000,0x1e67e,0x100000,0x100000,0x100000,0x1eb00,0x247e0,36864,36928,0x1eb02,0x247e2,36880,36936,0x1eb04,0x247e4,36896,36944,0x1eb06,0x247e6,36912,36952,0x1eb08,0x24930,37248,37312,0x1eb0a,0x24932,37264,37320,0x1eb0c,0x24934,37280,37328,0x1eb0e,0x24936,37296,37336,0x1eb10,0x24a80,37664,37728,0x1eb12,0x24a82,37680,37736,0x1eb14,0x24a84,37632,37744,0x1eb16,0x24a86,37648,37752,0x1eb18,0x24bd0,38048,38112,0x1eb1a,0x24bd2,38064,38120,0x1eb1c,0x24bd4,38016,38128,0x1eb1e,0x24bd6,38032,38136,0x1eb00,0x247e8,38464,38400,0x1eb02,0x247ea,38480,38408,0x1eb04,0x247ec,38496,38416,0x1eb06,0x247ee,38512,38424,0x1eb08,0x24938,38848,38784,0x1eb0a,0x2493a,38864,38792,0x1eb0c,0x2493c,38880,38800,0x1eb0e,0x2493e,38896,38808,0x1eb10,0x24a88,39264,39200,0x1eb12,0x24a8a,39280,39208,0x1eb14,0x24a8c,39232,39216,0x1eb16,0x24a8e,39248,39224,0x1eb18,0x24bd8,39648,39584,0x1eb1a,0x24bda,39664,39592,0x1eb1c,0x24bdc,39616,39600,0x1eb1e,0x24bde,39632,39608,0x1eb00,0x25ce0,39936,40000,0x1eb02,0x25ce2,39952,40008,0x1eb04,0x25ce4,39968,40016,0x1eb06,0x25ce6,39984,40024,0x1eb08,0x25e30,40320,40384,0x1eb0a,0x25e32,40336,40392,0x1eb0c,0x25e34,40352,40400,0x1eb0e,0x25e36,40368,40408,0x1eb10,0x25f80,40736,40800,0x1eb12,0x25f82,40752,40808,0x1eb14,0x25f84,40704,40816,0x1eb16,0x25f86,40720,40824,0x1eb18,0x260d0,41120,41184,0x1eb1a,0x260d2,41136,41192,0x1eb1c,0x260d4,41088,41200,0x1eb1e,0x260d6,41104,41208,0x1eb00,0x100000,41536,41472,0x1eb02,0x100000,41552,41480,0x1eb04,0x100000,41568,41488,0x1eb06,0x100000,41584,41496,0x1eb08,0x100000,41920,41856,0x1eb0a,0x100000,41936,41864,0x1eb0c,0x100000,41952,41872,0x1eb0e,0x100000,41968,41880,0x1eb10,0x100000,42336,42272,0x1eb12,0x100000,42352,42280,0x1eb14,0x100000,42304,42288,0x1eb16,0x100000,42320,42296,0x1eb18,0x100000,42720,42656,0x1eb1a,0x100000,42736,42664,0x1eb1c,0x100000,42688,42672,0x1eb1e,0x100000,42704,42680,36864,36896,36928,36864,36872,36904,36936,36880,36880,36912,36944,36896,36888,36920,36952,36912,38016,38048,38080,36928,38024,38056,38088,36944,38032,38064,38096,36960,38040,38072,38104,36976,39200,39168,39264,36992,39208,39176,39272,37008,39216,39184,39280,37024,39224,39192,39288,37040,40352,40320,40416,37056,40360,40328,40424,37072,40368,40336,40432,37088,40376,40344,40440,37104,46144,46176,46080,37120,46152,46184,46088,37136,46160,46192,46096,37152,46168,46200,46104,37168,47296,47328,47232,37184,47304,47336,47240,37200,47312,47344,47248,37216,47320,47352,47256,37232,48480,48448,48416,37248,48488,48456,48424,37264,48496,48464,48432,37280,48504,48472,48440,37296,49632,49600,49568,37312,49640,49608,49576,37328,49648,49616,49584,37344,49656,49624,49592,37360,55296,55328,55360,37376,55304,55336,55368,37392,55312,55344,55376,37408,55320,55352,55384,37424,56448,56480,56512,37440,56456,56488,56520,37456,56464,56496,56528,37472,56472,56504,56536,37488,57632,57600,57696,37504,57640,57608,57704,37520,57648,57616,57712,37536,57656,57624,57720,37552,58784,58752,58848,37568,58792,58760,58856,37584,58800,58768,58864,37600,58808,58776,58872,37616,64576,64608,64512,37632,64584,64616,64520,37648,64592,64624,64528,37664,64600,64632,64536,37680,65728,65760,65664,37696,65736,65768,65672,37712,65744,65776,65680,37728,65752,65784,65688,37744,66912,66880,66848,37760,66920,66888,66856,37776,66928,66896,66864,37792,66936,66904,66872,37808,68064,68032,68000,37824,68072,68040,68008,37840,68080,68048,68016,37856,68088,68056,68024,37872,0x1e800,0x1e800,0x20cc0,0,0x1e8c4,0x1e8c4,0x20cc4,16,0x1e988,0x1e988,0x20cc8,32,0x1ea4c,0x1ea4c,0x20ccc,48,0x1eb10,0x1eb10,0x20cd0,64,0x1ebd4,0x1ebd4,0x20cd4,80,0x1ec98,0x1ec98,0x20cd8,96,0x1ed5c,0x1ed5c,0x20cdc,112,0x1ee20,0x1ee20,0x20ce0,384,0x1eee4,0x1eee4,0x20ce4,400,0x1efa8,0x1efa8,0x20ce8,416,0x1f06c,0x1f06c,0x20cec,432,0x1f130,0x1f130,0x20cf0,448,0x1f1f4,0x1f1f4,0x20cf4,464,0x1f2b8,0x1f2b8,0x20cf8,480,0x1f37c,0x1f37c,0x20cfc,496,0x1e810,0x1e800,0x20cc0,768,0x1e8d4,0x1e8c4,0x20cc4,784,0x1e998,0x1e988,0x20cc8,800,0x1ea5c,0x1ea4c,0x20ccc,816,0x1eb20,0x1eb10,0x20cd0,832,0x1ebe4,0x1ebd4,0x20cd4,848,0x1eca8,0x1ec98,0x20cd8,864,0x1ed6c,0x1ed5c,0x20cdc,880,0x1ee30,0x1ee20,0x20ce0,1152,0x1eef4,0x1eee4,0x20ce4,1168,0x1efb8,0x1efa8,0x20ce8,1184,0x1f07c,0x1f06c,0x20cec,1200,0x1f140,0x1f130,0x20cf0,1216,0x1f204,0x1f1f4,0x20cf4,1232,0x1f2c8,0x1f2b8,0x20cf8,1248,0x1f38c,0x1f37c,0x20cfc,1264,0x1e820,0x1e800,0x20cc0,1536,0x1e8e4,0x1e8c4,0x20cc4,1552,0x1e9a8,0x1e988,0x20cc8,1568,0x1ea6c,0x1ea4c,0x20ccc,1584,0x1eb30,0x1eb10,0x20cd0,1600,0x1ebf4,0x1ebd4,0x20cd4,1616,0x1ecb8,0x1ec98,0x20cd8,1632,0x1ed7c,0x1ed5c,0x20cdc,1648,0x1ee40,0x1ee20,0x20ce0,1920,0x1ef04,0x1eee4,0x20ce4,1936,0x1efc8,0x1efa8,0x20ce8,1952,0x1f08c,0x1f06c,0x20cec,1968,0x1f150,0x1f130,0x20cf0,1984,0x1f214,0x1f1f4,0x20cf4,2000,0x1f2d8,0x1f2b8,0x20cf8,2016,0x1f39c,0x1f37c,0x20cfc,2032,0x1e830,0x1e800,0x20cc0,2304,0x1e8f4,0x1e8c4,0x20cc4,2320,0x1e9b8,0x1e988,0x20cc8,2336,0x1ea7c,0x1ea4c,0x20ccc,2352,0x1eb40,0x1eb10,0x20cd0,2368,0x1ec04,0x1ebd4,0x20cd4,2384,0x1ecc8,0x1ec98,0x20cd8,2400,0x1ed8c,0x1ed5c,0x20cdc,2416,0x1ee50,0x1ee20,0x20ce0,2688,0x1ef14,0x1eee4,0x20ce4,2704,0x1efd8,0x1efa8,0x20ce8,2720,0x1f09c,0x1f06c,0x20cec,2736,0x1f160,0x1f130,0x20cf0,2752,0x1f224,0x1f1f4,0x20cf4,2768,0x1f2e8,0x1f2b8,0x20cf8,2784,0x1f3ac,0x1f37c,0x20cfc,2800,36864,0x26760,0x1e800,0x1eb00,36880,0x26764,0x1ec90,0x1ef90,36896,0x26768,0x1f120,0x1f420,36912,0x2676c,0x1f5b0,0x1f8b0,36928,0x26770,0x1fa40,0x1fd40,36944,0x26774,0x1fed0,0x201d0,36960,0x26778,0x20360,0x20660,36976,0x2677c,0x207f0,0x20af0,37248,0x26780,0x20c80,0x20f80,37264,0x26784,0x21110,0x21410,37280,0x26788,0x215a0,0x218a0,37296,0x2678c,0x21a30,0x21d30,37312,0x26790,0x21ec0,0x221c0,37328,0x26794,0x22350,0x22650,37344,0x26798,0x227e0,0x22ae0,37360,0x2679c,0x22c70,0x22f70,37664,0x267a0,0x1e830,0x1eb08,37680,0x267a4,0x1ecc0,0x1ef98,37632,0x267a8,0x1f150,0x1f428,37648,0x267ac,0x1f5e0,0x1f8b8,37728,0x267b0,0x1fa70,0x1fd48,37744,0x267b4,0x1ff00,0x201d8,37696,0x267b8,0x20390,0x20668,37712,0x267bc,0x20820,0x20af8,38048,0x267c0,0x20cb0,0x20f88,38064,0x267c4,0x21140,0x21418,38016,0x267c8,0x215d0,0x218a8,38032,0x267cc,0x21a60,0x21d38,38112,0x267d0,0x21ef0,0x221c8,38128,0x267d4,0x22380,0x22658,38080,0x267d8,0x22810,0x22ae8,38096,0x267dc,0x22ca0,0x22f78,38464,0x267e0,0x1e860,0x1eb10,38480,0x267e4,0x1ecf0,0x1efa0,38496,0x267e8,0x1f180,0x1f430,38512,0x267ec,0x1f610,0x1f8c0,38400,0x267f0,0x1faa0,0x1fd50,38416,0x267f4,0x1ff30,0x201e0,38432,0x267f8,0x203c0,0x20670,38448,0x267fc,0x20850,0x20b00,38848,0x26800,0x20ce0,0x20f90,38864,0x26804,0x21170,0x21420,38880,0x26808,0x21600,0x218b0,38896,0x2680c,0x21a90,0x21d40,38784,0x26810,0x21f20,0x221d0,38800,0x26814,0x223b0,0x22660,38816,0x26818,0x22840,0x22af0,38832,0x2681c,0x22cd0,0x22f80,39264,0x26820,0x1e890,0x1eb18,39280,0x26824,0x1ed20,0x1efa8,39232,0x26828,0x1f1b0,0x1f438,39248,0x2682c,0x1f640,0x1f8c8,39200,0x26830,0x1fad0,0x1fd58,39216,0x26834,0x1ff60,0x201e8,39168,0x26838,0x203f0,0x20678,39184,0x2683c,0x20880,0x20b08,39648,0x26840,0x20d10,0x20f98,39664,0x26844,0x211a0,0x21428,39616,0x26848,0x21630,0x218b8,39632,0x2684c,0x21ac0,0x21d48,39584,0x26850,0x21f50,0x221d8,39600,0x26854,0x223e0,0x22668,39552,0x26858,0x22870,0x22af8,39568,0x2685c,0x22d00,0x22f88,0x3f803f80,0x3f803f80,16256,0,0,0x3f803f80,0x3f800000,0,0,0x3f803f80,0,16256,0,0x3f803f80,0,0x3f800000,0x3f803f80,0,0,0,0,0,0,0,0,0,0,0,0,0,0,0,0x3f803f80,0,0,0,0,0,0,0,0,0,0,0,0,0,0,0,0x3f803f80,0,0,0,0,0,0,0,0,0,0,0,0,0,0,0,0x3f803f80,0,0,0,0,0,0,0,0,0,0,0,0,0,0,0,0x3f803f80,0x3f803f80,16256,0,0,0x3f803f80,0x3f800000,0,0,0x3f803f80,0,16256,0,0x3f803f80,0,0x3f800000,0x3f803f80,0,0,0,0,0,0,0,0,0,0,0,0,0,0,0,0x3f803f80,0,0,0,0,0,0,0,0,0,0,0,0,0,0,0,0x3f803f80,0,0,0,0,0,0,0,0,0,0,0,0,0,0,0,0x3f803f80,0,0,0,0,0,0,0,0,0,0,0,0,0,0,0,0x3f803f80,0x3f803f80,16256,0,0,0x3f803f80,0x3f800000,0,0,0x3f803f80,0,16256,0,0x3f803f80,0,0x3f800000,0x3f803f80,0,0,0,0,0,0,0,0,0,0,0,0,0,0,0,0x3f803f80,0,0,0,0,0,0,0,0,0,0,0,0,0,0,0,0x3f803f80,0,0,0,0,0,0,0,0,0,0,0,0,0,0,0,0x3f803f80,0,0,0,0,0,0,0,0,0,0,0,0,0,0,0,0x3f803f80,0x3f803f80,16256,0,0,0x3f803f80,0x3f800000,0,0,0x3f803f80,0,16256,0,0x3f803f80,0,0x3f800000,0,64,0x30000,0xf0000,144,208,0x30010,0xf0010,288,352,0x30020,0xf0020,432,496,0x30030,0xf0030,576,512,0x30040,0xf0040,720,656,0x30050,0xf0050,864,800,0x30060,0xf0060,1008,944,0x30070,0xf0070,1024,1088,0x30190,0xf0190,1168,1232,0x30180,0xf0180,1312,1376,0x301b0,0xf01b0,1456,1520,0x301a0,0xf01a0,1600,1536,0x301d0,0xf01d0,1744,1680,0x301c0,0xf01c0,1888,1824,0x301f0,0xf01f0,2032,1968,0x301e0,0xf01e0,16,80,0x30320,0x11f720,128,192,0x30330,0x11f730,304,368,0x30300,0x11f700,416,480,0x30310,0x11f710,592,528,0x30360,0x11f760,704,640,0x30370,0x11f770,880,816,0x30340,0x11f740,992,928,0x30350,0x11f750,1040,1104,0x304b0,0x11f8b0,1152,1216,0x304a0,0x11f8a0,1328,1392,0x30490,0x11f890,1440,1504,0x30480,0x11f880,1616,1552,0x304f0,0x11f8f0,1728,1664,0x304e0,0x11f8e0,1904,1840,0x304d0,0x11f8d0,2016,1952,0x304c0,0x11f8c0,32,96,0x30640,0x11fa40,176,240,0x30650,0x11fa50,256,320,0x30660,0x11fa60,400,464,0x30670,0x11fa70,608,544,0x30600,0x11fa00,752,688,0x30610,0x11fa10,832,768,0x30620,0x11fa20,976,912,0x30630,0x11fa30,1056,1120,0x307d0,0x11fbd0,1200,1264,0x307c0,0x11fbc0,1280,1344,0x307f0,0x11fbf0,1424,1488,0x307e0,0x11fbe0,1632,1568,0x30790,0x11fb90,1776,1712,0x30780,0x11fb80,1856,1792,0x307b0,0x11fbb0,2000,1936,0x307a0,0x11fba0,48,112,0x5fd60,0x11fd60,160,224,0x5fd70,0x11fd70,272,336,0x5fd40,0x11fd40,384,448,0x5fd50,0x11fd50,624,560,0x5fd20,0x11fd20,736,672,0x5fd30,0x11fd30,848,784,0x5fd00,0x11fd00,960,896,0x5fd10,0x11fd10,1072,1136,0x5fef0,0x11fef0,1184,1248,0x5fee0,0x11fee0,1296,1360,0x5fed0,0x11fed0,1408,1472,0x5fec0,0x11fec0,1648,1584,0x5feb0,0x11feb0,1760,1696,0x5fea0,0x11fea0,1872,1808,0x5fe90,0x11fe90,1984,1920,0x5fe80,0x11fe80,0xefb40,74752,78848,90624,0xefb50,74768,78864,90640,0xefb60,74784,78880,90656,0xefb70,74800,78896,90672,0xefb00,74816,78912,90688,0xefb10,74832,78928,90704,0xefb20,74848,78944,90720,0xefb30,74864,78960,90736,0xefcd0,74880,78976,90752,0xefcc0,74896,78992,90768,0xefcf0,74912,79008,90784,0xefce0,74928,79024,90800,0xefc90,74944,79040,90816,0xefc80,74960,79056,90832,0xefcb0,74976,79072,90848,0xefca0,74992,79088,90864,0xefe60,75008,79104,90880,0xefe70,75024,79120,90896,0xefe40,75040,79136,90912,0xefe50,75056,79152,90928,0xefe20,75072,79168,90944,0xefe30,75088,79184,90960,0xefe00,75104,79200,90976,0xefe10,75120,79216,90992,0xefff0,75136,79232,91008,0xeffe0,75152,79248,91024,0xeffd0,75168,79264,91040,0xeffc0,75184,79280,91056,0xeffb0,75200,79296,91072,0xeffa0,75216,79312,91088,0xeff90,75232,79328,91104,0xeff80,75248,79344,91120,0xefb40,75264,79360,91136,0xefb50,75280,79376,91152,0xefb60,75296,79392,91168,0xefb70,75312,79408,91184,0xefb00,75328,79424,91200,0xefb10,75344,79440,91216,0xefb20,75360,79456,91232,0xefb30,75376,79472,91248,0xefcd0,75392,79488,91264,0xefcc0,75408,79504,91280,0xefcf0,75424,79520,91296,0xefce0,75440,79536,91312,0xefc90,75456,79552,91328,0xefc80,75472,79568,91344,0xefcb0,75488,79584,91360,0xefca0,75504,79600,91376,0xefe60,75520,79616,91392,0xefe70,75536,79632,91408,0xefe40,75552,79648,91424,0xefe50,75568,79664,91440,0xefe20,75584,79680,91456,0xefe30,75600,79696,91472,0xefe00,75616,79712,91488,0xefe10,75632,79728,91504,0xefff0,75648,79744,91520,0xeffe0,75664,79760,91536,0xeffd0,75680,79776,91552,0xeffc0,75696,79792,91568,0xeffb0,75712,79808,91584,0xeffa0,75728,79824,91600,0xeff90,75744,79840,91616,0xeff80,75760,79856,91632,0x1c0b0,36864,39936,49160,0x1c0c0,36880,39952,49176,0x1c0d0,36896,39968,49192,0x1c0e0,36912,39984,49208,0x1c0f0,36928,40000,49224,0x1c100,36944,40016,49240,0x1c110,36960,40032,49256,0x1c120,36976,40048,49272,0x1c130,36992,40064,49288,0x1c140,37008,40080,49304,0x1c150,37024,40096,49320,0x1c160,37040,40112,49336,0x1c170,37056,40128,49352,0x1c180,37072,40144,49368,0x1c190,37088,40160,49384,0x1c1a0,37104,40176,49400,0x1c1b0,37632,40704,49928,0x1c1c0,37648,40720,49944,0x1c1d0,37664,40736,49960,0x1c1e0,37680,40752,49976,0x1c1f0,37696,40768,49992,0x1c200,37712,40784,50008,0x1c210,37728,40800,50024,0x1c220,37744,40816,50040,0x1c230,37760,40832,50056,0x1c240,37776,40848,50072,0x1c250,37792,40864,50088,0x1c260,37808,40880,50104,0x1c270,37824,40896,50120,0x1c280,37840,40912,50136,0x1c290,37856,40928,50152,0x1c2a0,37872,40944,50168,0x1c2b0,38400,41472,50696,0x1c2c0,38416,41488,50712,0x1c2d0,38432,41504,50728,0x1c2e0,38448,41520,50744,0x1c2f0,38464,41536,50760,0x1c300,38480,41552,50776,0x1c310,38496,41568,50792,0x1c320,38512,41584,50808,0x1c330,38528,41600,50824,0x1c340,38544,41616,50840,0x1c350,38560,41632,50856,0x1c360,38576,41648,50872,0x1c370,38592,41664,50888,0x1c380,38608,41680,50904,0x1c390,38624,41696,50920,0x1c3a0,38640,41712,50936,0x1c3b0,39168,42240,51464,0x1c3c0,39184,42256,51480,0x1c3d0,39200,42272,51496,0x1c3e0,39216,42288,51512,0x1c3f0,39232,42304,51528,0x1c400,39248,42320,51544,0x1c410,39264,42336,51560,0x1c420,39280,42352,51576,0x1c430,39296,42368,51592,0x1c440,39312,42384,51608,0x1c450,39328,42400,51624,0x1c460,39344,42416,51640,0x1c470,39360,42432,51656,0x1c480,39376,42448,51672,0x1c490,39392,42464,51688,0x1c4a0,39408,42480,51704,73824,79904,0x1d600,0x1d604,73968,80048,0x1d610,0x1d614,74048,80128,0x1d620,0x1d624,74192,80272,0x1d630,0x1d634,74272,80480,0x1d640,0x1d644,74416,80624,0x1d650,0x1d654,74496,80704,0x1d660,0x1d664,74640,80848,0x1d670,0x1d674,74848,80928,0x1d680,0x1d684,74992,81072,0x1d690,0x1d694,75072,81152,0x1d6a0,0x1d6a4,75216,81296,0x1d6b0,0x1d6b4,75296,81504,0x1d6c0,0x1d6c4,75440,81648,0x1d6d0,0x1d6d4,75520,81728,0x1d6e0,0x1d6e4,75664,81872,0x1d6f0,0x1d6f4,73840,79912,0x1d700,0x1d704,73952,80056,0x1d710,0x1d714,74064,80136,0x1d720,0x1d724,74176,80280,0x1d730,0x1d734,74288,80488,0x1d740,0x1d744,74400,80632,0x1d750,0x1d754,74512,80712,0x1d760,0x1d764,74624,80856,0x1d770,0x1d774,74864,80936,0x1d780,0x1d784,74976,81080,0x1d790,0x1d794,75088,81160,0x1d7a0,0x1d7a4,75200,81304,0x1d7b0,0x1d7b4,75312,81512,0x1d7c0,0x1d7c4,75424,81656,0x1d7d0,0x1d7d4,75536,81736,0x1d7e0,0x1d7e4,75648,81880,0x1d7f0,0x1d7f4,79872,79920,0x1d800,0x1d804,80016,80032,0x1d810,0x1d814,80160,80144,0x1d820,0x1d824,80304,80256,0x1d830,0x1d834,80448,80496,0x1d840,0x1d844,80592,80608,0x1d850,0x1d854,80736,80720,0x1d860,0x1d864,80880,80832,0x1d870,0x1d874,80896,80944,0x1d880,0x1d884,81040,81056,0x1d890,0x1d894,81184,81168,0x1d8a0,0x1d8a4,81328,81280,0x1d8b0,0x1d8b4,81472,81520,0x1d8c0,0x1d8c4,81616,81632,0x1d8d0,0x1d8d4,81760,81744,0x1d8e0,0x1d8e4,81904,81856,0x1d8f0,0x1d8f4,79888,79928,0x1d900,0x1d904,80000,80040,0x1d910,0x1d914,80176,80152,0x1d920,0x1d924,80288,80264,0x1d930,0x1d934,80464,80504,0x1d940,0x1d944,80576,80616,0x1d950,0x1d954,80752,80728,0x1d960,0x1d964,80864,80840,0x1d970,0x1d974,80912,80952,0x1d980,0x1d984,81024,81064,0x1d990,0x1d994,81200,81176,0x1d9a0,0x1d9a4,81312,81288,0x1d9b0,0x1d9b4,81488,81528,0x1d9c0,0x1d9c4,81600,81640,0x1d9d0,0x1d9d4,81776,81752,0x1d9e0,0x1d9e4,81888,81864,0x1d9f0,0x1d9f4,0x1e680,0x1e680,0x1e8c0,0x100000,0x1e682,0x100000,0x100000,0x1e8c0,0x1e684,0x100000,0x100000,0x100000,0x1e686,0x100000,0x100000,0x100000,0x1e690,0x1e680,0x100000,0x100000,0x1e692,0x100000,0x100000,0x1e8c0,0x1e694,0x100000,0x100000,0x100000,0x1e696,0x100000,0x100000,0x100000,0x1e6a0,0x1e680,0x100000,0x100000,0x1e6a2,0x100000,0x100000,0x1e8c0,0x1e6a4,0x100000,0x100000,0x100000,0x1e6a6,0x100000,0x100000,0x100000,0x1e6b0,0x1e680,0x100000,0x100000,0x1e6b2,0x100000,0x100000,0x1e8c0,0x1e6b4,0x100000,0x100000,0x100000,0x1e6b6,0x100000,0x100000,0x100000,0x1e688,0x1e690,0x1e8f0,0x100000,0x1e68a,0x100000,0x100000,0x1e8f0,0x1e68c,0x100000,0x100000,0x100000,0x1e68e,0x100000,0x100000,0x100000,0x1e698,0x1e690,0x100000,0x100000,0x1e69a,0x100000,0x100000,0x1e8f0,0x1e69c,0x100000,0x100000,0x100000,0x1e69e,0x100000,0x100000,0x100000,0x1e6a8,0x1e690,0x100000,0x100000,0x1e6aa,0x100000,0x100000,0x1e8f0,0x1e6ac,0x100000,0x100000,0x100000,0x1e6ae,0x100000,0x100000,0x100000,0x1e6b8,0x1e690,0x100000,0x100000,0x1e6ba,0x100000,0x100000,0x1e8f0,0x1e6bc,0x100000,0x100000,0x100000,0x1e6be,0x100000,0x100000,0x100000,0x1e6c0,0x1e6a0,0x1e920,0x100000,0x1e6c2,0x100000,0x100000,0x1e920,0x1e6c4,0x100000,0x100000,0x100000,0x1e6c6,0x100000,0x100000,0x100000,0x1e6d0,0x1e6a0,0x100000,0x100000,0x1e6d2,0x100000,0x100000,0x1e920,0x1e6d4,0x100000,0x100000,0x100000,0x1e6d6,0x100000,0x100000,0x100000,0x1e6e0,0x1e6a0,0x100000,0x100000,0x1e6e2,0x100000,0x100000,0x1e920,0x1e6e4,0x100000,0x100000,0x100000,0x1e6e6,0x100000,0x100000,0x100000,0x1e6f0,0x1e6a0,0x100000,0x100000,0x1e6f2,0x100000,0x100000,0x1e920,0x1e6f4,0x100000,0x100000,0x100000,0x1e6f6,0x100000,0x100000,0x100000,0x1e6c8,0x1e6b0,0x1e950,0x100000,0x1e6ca,0x100000,0x100000,0x1e950,0x1e6cc,0x100000,0x100000,0x100000,0x1e6ce,0x100000,0x100000,0x100000,0x1e6d8,0x1e6b0,0x100000,0x100000,0x1e6da,0x100000,0x100000,0x1e950,0x1e6dc,0x100000,0x100000,0x100000,0x1e6de,0x100000,0x100000,0x100000,0x1e6e8,0x1e6b0,0x100000,0x100000,0x1e6ea,0x100000,0x100000,0x1e950,0x1e6ec,0x100000,0x100000,0x100000,0x1e6ee,0x100000,0x100000,0x100000,0x1e6f8,0x1e6b0,0x100000,0x100000,0x1e6fa,0x100000,0x100000,0x1e950,0x1e6fc,0x100000,0x100000,0x100000,0x1e6fe,0x100000,0x100000,0x100000,0x1eb60,0x24d20,36960,37024,0x1eb62,0x24d22,36976,37032,0x1eb64,0x24d24,36992,37040,0x1eb66,0x24d26,37008,37048,0x1eb68,0x24e70,37344,37408,0x1eb6a,0x24e72,37360,37416,0x1eb6c,0x24e74,37376,37424,0x1eb6e,0x24e76,37392,37432,0x1eb70,0x24fc0,37696,37760,0x1eb72,0x24fc2,37712,37768,0x1eb74,0x24fc4,37792,37776,0x1eb76,0x24fc6,37808,37784,0x1eb78,0x25110,38080,38144,0x1eb7a,0x25112,38096,38152,0x1eb7c,0x25114,38176,38160,0x1eb7e,0x25116,38192,38168,0x1eb60,0x24d28,38432,38624,0x1eb62,0x24d2a,38448,38632,0x1eb64,0x24d2c,38592,38640,0x1eb66,0x24d2e,38608,38648,0x1eb68,0x24e78,38816,39008,0x1eb6a,0x24e7a,38832,39016,0x1eb6c,0x24e7c,38976,39024,0x1eb6e,0x24e7e,38992,39032,0x1eb70,0x24fc8,39168,39360,0x1eb72,0x24fca,39184,39368,0x1eb74,0x24fcc,39392,39376,0x1eb76,0x24fce,39408,39384,0x1eb78,0x25118,39552,39744,0x1eb7a,0x2511a,39568,39752,0x1eb7c,0x2511c,39776,39760,0x1eb7e,0x2511e,39792,39768,0x1eb60,0x25ce8,40032,40096,0x1eb62,0x25cea,40048,40104,0x1eb64,0x25cec,40064,40112,0x1eb66,0x25cee,40080,40120,0x1eb68,0x25e38,40416,40480,0x1eb6a,0x25e3a,40432,40488,0x1eb6c,0x25e3c,40448,40496,0x1eb6e,0x25e3e,40464,40504,0x1eb70,0x25f88,40768,40832,0x1eb72,0x25f8a,40784,40840,0x1eb74,0x25f8c,40864,40848,0x1eb76,0x25f8e,40880,40856,0x1eb78,0x260d8,41152,41216,0x1eb7a,0x260da,41168,41224,0x1eb7c,0x260dc,41248,41232,0x1eb7e,0x260de,41264,41240,0x1eb60,0x100000,41504,41696,0x1eb62,0x100000,41520,41704,0x1eb64,0x100000,41664,41712,0x1eb66,0x100000,41680,41720,0x1eb68,0x100000,41888,42080,0x1eb6a,0x100000,41904,42088,0x1eb6c,0x100000,42048,42096,0x1eb6e,0x100000,42064,42104,0x1eb70,0x100000,42240,42432,0x1eb72,0x100000,42256,42440,0x1eb74,0x100000,42464,42448,0x1eb76,0x100000,42480,42456,0x1eb78,0x100000,42624,42816,0x1eb7a,0x100000,42640,42824,0x1eb7c,0x100000,42848,42832,0x1eb7e,0x100000,42864,42840,36960,36992,37024,46080,36968,37000,37032,46096,36976,37008,37040,46112,36984,37016,37048,46128,38112,38144,38176,46144,38120,38152,38184,46160,38128,38160,38192,46176,38136,38168,38200,46192,39232,39328,39296,46208,39240,39336,39304,46224,39248,39344,39312,46240,39256,39352,39320,46256,40384,40480,40448,46272,40392,40488,40456,46288,40400,40496,40464,46304,40408,40504,40472,46320,46112,46272,46304,46336,46120,46280,46312,46352,46128,46288,46320,46368,46136,46296,46328,46384,47264,47424,47456,46400,47272,47432,47464,46416,47280,47440,47472,46432,47288,47448,47480,46448,48384,48608,48576,46464,48392,48616,48584,46480,48400,48624,48592,46496,48408,48632,48600,46512,49536,49760,49728,46528,49544,49768,49736,46544,49552,49776,49744,46560,49560,49784,49752,46576,55392,55424,55456,46592,55400,55432,55464,46608,55408,55440,55472,46624,55416,55448,55480,46640,56544,56576,56608,46656,56552,56584,56616,46672,56560,56592,56624,46688,56568,56600,56632,46704,57664,57760,57728,46720,57672,57768,57736,46736,57680,57776,57744,46752,57688,57784,57752,46768,58816,58912,58880,46784,58824,58920,58888,46800,58832,58928,58896,46816,58840,58936,58904,46832,64544,64704,64736,46848,64552,64712,64744,46864,64560,64720,64752,46880,64568,64728,64760,46896,65696,65856,65888,46912,65704,65864,65896,46928,65712,65872,65904,46944,65720,65880,65912,46960,66816,67040,67008,46976,66824,67048,67016,46992,66832,67056,67024,47008,66840,67064,67032,47024,67968,68192,68160,47040,67976,68200,68168,47056,67984,68208,68176,47072,67992,68216,68184,47088,0x1e800,0x1e800,0x20cc0,18432,0x1e8c4,0x1e8c4,0x20cc4,18448,0x1e988,0x1e988,0x20cc8,18464,0x1ea4c,0x1ea4c,0x20ccc,18480,0x1eb10,0x1eb10,0x20cd0,18496,0x1ebd4,0x1ebd4,0x20cd4,18512,0x1ec98,0x1ec98,0x20cd8,18528,0x1ed5c,0x1ed5c,0x20cdc,18544,0x1ee20,0x1ee20,0x20ce0,18816,0x1eee4,0x1eee4,0x20ce4,18832,0x1efa8,0x1efa8,0x20ce8,18848,0x1f06c,0x1f06c,0x20cec,18864,0x1f130,0x1f130,0x20cf0,18880,0x1f1f4,0x1f1f4,0x20cf4,18896,0x1f2b8,0x1f2b8,0x20cf8,18912,0x1f37c,0x1f37c,0x20cfc,18928,0x1e810,0x1e800,0x20cc0,19200,0x1e8d4,0x1e8c4,0x20cc4,19216,0x1e998,0x1e988,0x20cc8,19232,0x1ea5c,0x1ea4c,0x20ccc,19248,0x1eb20,0x1eb10,0x20cd0,19264,0x1ebe4,0x1ebd4,0x20cd4,19280,0x1eca8,0x1ec98,0x20cd8,19296,0x1ed6c,0x1ed5c,0x20cdc,19312,0x1ee30,0x1ee20,0x20ce0,19584,0x1eef4,0x1eee4,0x20ce4,19600,0x1efb8,0x1efa8,0x20ce8,19616,0x1f07c,0x1f06c,0x20cec,19632,0x1f140,0x1f130,0x20cf0,19648,0x1f204,0x1f1f4,0x20cf4,19664,0x1f2c8,0x1f2b8,0x20cf8,19680,0x1f38c,0x1f37c,0x20cfc,19696,0x1e820,0x1e800,0x20cc0,19968,0x1e8e4,0x1e8c4,0x20cc4,19984,0x1e9a8,0x1e988,0x20cc8,20000,0x1ea6c,0x1ea4c,0x20ccc,20016,0x1eb30,0x1eb10,0x20cd0,20032,0x1ebf4,0x1ebd4,0x20cd4,20048,0x1ecb8,0x1ec98,0x20cd8,20064,0x1ed7c,0x1ed5c,0x20cdc,20080,0x1ee40,0x1ee20,0x20ce0,20352,0x1ef04,0x1eee4,0x20ce4,20368,0x1efc8,0x1efa8,0x20ce8,20384,0x1f08c,0x1f06c,0x20cec,20400,0x1f150,0x1f130,0x20cf0,20416,0x1f214,0x1f1f4,0x20cf4,20432,0x1f2d8,0x1f2b8,0x20cf8,20448,0x1f39c,0x1f37c,0x20cfc,20464,0x1e830,0x1e800,0x20cc0,20736,0x1e8f4,0x1e8c4,0x20cc4,20752,0x1e9b8,0x1e988,0x20cc8,20768,0x1ea7c,0x1ea4c,0x20ccc,20784,0x1eb40,0x1eb10,0x20cd0,20800,0x1ec04,0x1ebd4,0x20cd4,20816,0x1ecc8,0x1ec98,0x20cd8,20832,0x1ed8c,0x1ed5c,0x20cdc,20848,0x1ee50,0x1ee20,0x20ce0,21120,0x1ef14,0x1eee4,0x20ce4,21136,0x1efd8,0x1efa8,0x20ce8,21152,0x1f09c,0x1f06c,0x20cec,21168,0x1f160,0x1f130,0x20cf0,21184,0x1f224,0x1f1f4,0x20cf4,21200,0x1f2e8,0x1f2b8,0x20cf8,21216,0x1f3ac,0x1f37c,0x20cfc,21232,55296,0x26860,0x1e8c0,0x1eb60,55312,0x26864,0x1ed50,0x1eff0,55328,0x26868,0x1f1e0,0x1f480,55344,0x2686c,0x1f670,0x1f910,55360,0x26870,0x1fb00,0x1fda0,55376,0x26874,0x1ff90,0x20230,55392,0x26878,0x20420,0x206c0,55408,0x2687c,0x208b0,0x20b50,55680,0x26880,0x20d40,0x20fe0,55696,0x26884,0x211d0,0x21470,55712,0x26888,0x21660,0x21900,55728,0x2688c,0x21af0,0x21d90,55744,0x26890,0x21f80,0x22220,55760,0x26894,0x22410,0x226b0,55776,0x26898,0x228a0,0x22b40,55792,0x2689c,0x22d30,0x22fd0,56096,0x268a0,0x1e8f0,0x1eb68,56112,0x268a4,0x1ed80,0x1eff8,56064,0x268a8,0x1f210,0x1f488,56080,0x268ac,0x1f6a0,0x1f918,56160,0x268b0,0x1fb30,0x1fda8,56176,0x268b4,0x1ffc0,0x20238,56128,0x268b8,0x20450,0x206c8,56144,0x268bc,0x208e0,0x20b58,56480,0x268c0,0x20d70,0x20fe8,56496,0x268c4,0x21200,0x21478,56448,0x268c8,0x21690,0x21908,56464,0x268cc,0x21b20,0x21d98,56544,0x268d0,0x21fb0,0x22228,56560,0x268d4,0x22440,0x226b8,56512,0x268d8,0x228d0,0x22b48,56528,0x268dc,0x22d60,0x22fd8,56896,0x268e0,0x1e920,0x1eb70,56912,0x268e4,0x1edb0,0x1f000,56928,0x268e8,0x1f240,0x1f490,56944,0x268ec,0x1f6d0,0x1f920,56832,0x268f0,0x1fb60,0x1fdb0,56848,0x268f4,0x1fff0,0x20240,56864,0x268f8,0x20480,0x206d0,56880,0x268fc,0x20910,0x20b60,57280,0x26900,0x20da0,0x20ff0,57296,0x26904,0x21230,0x21480,57312,0x26908,0x216c0,0x21910,57328,0x2690c,0x21b50,0x21da0,57216,0x26910,0x21fe0,0x22230,57232,0x26914,0x22470,0x226c0,57248,0x26918,0x22900,0x22b50,57264,0x2691c,0x22d90,0x22fe0,57696,0x26920,0x1e950,0x1eb78,57712,0x26924,0x1ede0,0x1f008,57664,0x26928,0x1f270,0x1f498,57680,0x2692c,0x1f700,0x1f928,57632,0x26930,0x1fb90,0x1fdb8,57648,0x26934,0x20020,0x20248,57600,0x26938,0x204b0,0x206d8,57616,0x2693c,0x20940,0x20b68,58080,0x26940,0x20dd0,0x20ff8,58096,0x26944,0x21260,0x21488,58048,0x26948,0x216f0,0x21918,58064,0x2694c,0x21b80,0x21da8,58016,0x26950,0x22010,0x22238,58032,0x26954,0x224a0,0x226c8,57984,0x26958,0x22930,0x22b58,58000,0x2695c,0x22dc0,0x22fe8,0x3f803f80,0x3f803f80,16256,0,0,0x3f803f80,0x3f800000,0,0,0x3f803f80,0,16256,0,0x3f803f80,0,0x3f800000,0x3f803f80,0,0,0,0,0,0,0,0,0,0,0,0,0,0,0,0x3f803f80,0,0,0,0,0,0,0,0,0,0,0,0,0,0,0,0x3f803f80,0,0,0,0,0,0,0,0,0,0,0,0,0,0,0,0x3f803f80,0,0,0,0,0,0,0,0,0,0,0,0,0,0,0,0x3f803f80,0x3f803f80,16256,0,0,0x3f803f80,0x3f800000,0,0,0x3f803f80,0,16256,0,0x3f803f80,0,0x3f800000,0x3f803f80,0,0,0,0,0,0,0,0,0,0,0,0,0,0,0,0x3f803f80,0,0,0,0,0,0,0,0,0,0,0,0,0,0,0,0x3f803f80,0,0,0,0,0,0,0,0,0,0,0,0,0,0,0,0x3f803f80,0,0,0,0,0,0,0,0,0,0,0,0,0,0,0,0x3f803f80,0x3f803f80,16256,0,0,0x3f803f80,0x3f800000,0,0,0x3f803f80,0,16256,0,0x3f803f80,0,0x3f800000,0x3f803f80,0,0,0,0,0,0,0,0,0,0,0,0,0,0,0,0x3f803f80,0,0,0,0,0,0,0,0,0,0,0,0,0,0,0,0x3f803f80,0,0,0,0,0,0,0,0,0,0,0,0,0,0,0,0x3f803f80,0,0,0,0,0,0,0,0,0,0,0,0,0,0,0,0x3f803f80,0x3f803f80,16256,0,0,0x3f803f80,0x3f800000,0,0,0x3f803f80,0,16256,0,0x3f803f80,0,0x3f800000,0,64,0x60000,0xc0080,144,208,0x60010,0xc0090,288,352,0x60020,0xc00a0,432,496,0x60030,0xc00b0,576,512,0x60040,0xc00c0,720,656,0x60050,0xc00d0,864,800,0x60060,0xc00e0,1008,944,0x60070,0xc00f0,1024,1088,0x60190,0xc0210,1168,1232,0x60180,0xc0200,1312,1376,0x601b0,0xc0230,1456,1520,0x601a0,0xc0220,1600,1536,0x601d0,0xc0250,1744,1680,0x601c0,0xc0240,1888,1824,0x601f0,0xc0270,2032,1968,0x601e0,0xc0260,16,80,0x60320,0xc03a0,128,192,0x60330,0xc03b0,304,368,0x60300,0xc0380,416,480,0x60310,0xc0390,592,528,0x60360,0xc03e0,704,640,0x60370,0xc03f0,880,816,0x60340,0xc03c0,992,928,0x60350,0xc03d0,1040,1104,0x604b0,0xef930,1152,1216,0x604a0,0xef920,1328,1392,0x60490,0xef910,1440,1504,0x60480,0xef900,1616,1552,0x604f0,0xef970,1728,1664,0x604e0,0xef960,1904,1840,0x604d0,0xef950,2016,1952,0x604c0,0xef940,32,96,0x60640,0xefac0,176,240,0x60650,0xefad0,256,320,0x60660,0xefae0,400,464,0x60670,0xefaf0,608,544,0x60600,0xefa80,752,688,0x60610,0xefa90,832,768,0x60620,0xefaa0,976,912,0x60630,0xefab0,1056,1120,0x8fbd0,0xefc50,1200,1264,0x8fbc0,0xefc40,1280,1344,0x8fbf0,0xefc70,1424,1488,0x8fbe0,0xefc60,1632,1568,0x8fb90,0xefc10,1776,1712,0x8fb80,0xefc00,1856,1792,0x8fbb0,0xefc30,2000,1936,0x8fba0,0xefc20,48,112,0x8fd60,0xefde0,160,224,0x8fd70,0xefdf0,272,336,0x8fd40,0xefdc0,384,448,0x8fd50,0xefdd0,624,560,0x8fd20,0xefda0,736,672,0x8fd30,0xefdb0,848,784,0x8fd00,0xefd80,960,896,0x8fd10,0xefd90,1072,1136,0x8fef0,0xeff70,1184,1248,0x8fee0,0xeff60,1296,1360,0x8fed0,0xeff50,1408,1472,0x8fec0,0xeff40,1648,1584,0x8feb0,0xeff30,1760,1696,0x8fea0,0xeff20,1872,1808,0x8fe90,0xeff10,1984,1920,0x8fe80,0xeff00,0xf0100,75776,83968,91136,0xf0110,75792,83984,91152,0xf0120,75808,84000,91168,0xf0130,75824,84016,91184,0xf0140,75840,84032,91200,0xf0150,75856,84048,91216,0xf0160,75872,84064,91232,0xf0170,75888,84080,91248,0xf0290,75904,84096,91264,0xf0280,75920,84112,91280,0xf02b0,75936,84128,91296,0xf02a0,75952,84144,91312,0xf02d0,75968,84160,91328,0xf02c0,75984,84176,91344,0xf02f0,76000,84192,91360,0xf02e0,76016,84208,91376,0x11f820,76032,84224,91392,0x11f830,76048,84240,91408,0x11f800,76064,84256,91424,0x11f810,76080,84272,91440,0x11f860,76096,84288,91456,0x11f870,76112,84304,91472,0x11f840,76128,84320,91488,0x11f850,76144,84336,91504,0x11f9b0,76160,84352,91520,0x11f9a0,76176,84368,91536,0x11f990,76192,84384,91552,0x11f980,76208,84400,91568,0x11f9f0,76224,84416,91584,0x11f9e0,76240,84432,91600,0x11f9d0,76256,84448,91616,0x11f9c0,76272,84464,91632,0xf0100,76288,84480,91648,0xf0110,76304,84496,91664,0xf0120,76320,84512,91680,0xf0130,76336,84528,91696,0xf0140,76352,84544,91712,0xf0150,76368,84560,91728,0xf0160,76384,84576,91744,0xf0170,76400,84592,91760,0xf0290,76416,84608,91776,0xf0280,76432,84624,91792,0xf02b0,76448,84640,91808,0xf02a0,76464,84656,91824,0xf02d0,76480,84672,91840,0xf02c0,76496,84688,91856,0xf02f0,76512,84704,91872,0xf02e0,76528,84720,91888,0x11f820,76544,84736,91904,0x11f830,76560,84752,91920,0x11f800,76576,84768,91936,0x11f810,76592,84784,91952,0x11f860,76608,84800,91968,0x11f870,76624,84816,91984,0x11f840,76640,84832,92000,0x11f850,76656,84848,92016,0x11f9b0,76672,84864,92032,0x11f9a0,76688,84880,92048,0x11f990,76704,84896,92064,0x11f980,76720,84912,92080,0x11f9f0,76736,84928,92096,0x11f9e0,76752,84944,92112,0x11f9d0,76768,84960,92128,0x11f9c0,76784,84976,92144,0x1c160,36864,43008,52224,0x1c170,36880,43024,52240,0x1c180,36896,43040,52256,0x1c190,36912,43056,52272,0x1c1a0,36928,43072,52288,0x1c1b0,36944,43088,52304,0x1c1c0,36960,43104,52320,0x1c1d0,36976,43120,52336,0x1c1e0,36992,43136,52352,0x1c1f0,37008,43152,52368,0x1c200,37024,43168,52384,0x1c210,37040,43184,52400,0x1c220,37056,43200,52416,0x1c230,37072,43216,52432,0x1c240,37088,43232,52448,0x1c250,37104,43248,52464,0x1c260,37632,43776,52992,0x1c270,37648,43792,53008,0x1c280,37664,43808,53024,0x1c290,37680,43824,53040,0x1c2a0,37696,43840,53056,0x1c2b0,37712,43856,53072,0x1c2c0,37728,43872,53088,0x1c2d0,37744,43888,53104,0x1c2e0,37760,43904,53120,0x1c2f0,37776,43920,53136,0x1c300,37792,43936,53152,0x1c310,37808,43952,53168,0x1c320,37824,43968,53184,0x1c330,37840,43984,53200,0x1c340,37856,44000,53216,0x1c350,37872,44016,53232,0x1c360,38400,44544,53760,0x1c370,38416,44560,53776,0x1c380,38432,44576,53792,0x1c390,38448,44592,53808,0x1c3a0,38464,44608,53824,0x1c3b0,38480,44624,53840,0x1c3c0,38496,44640,53856,0x1c3d0,38512,44656,53872,0x1c3e0,38528,44672,53888,0x1c3f0,38544,44688,53904,0x1c400,38560,44704,53920,0x1c410,38576,44720,53936,0x1c420,38592,44736,53952,0x1c430,38608,44752,53968,0x1c440,38624,44768,53984,0x1c450,38640,44784,54000,0x1c460,39168,45312,54528,0x1c470,39184,45328,54544,0x1c480,39200,45344,54560,0x1c490,39216,45360,54576,0x1c4a0,39232,45376,54592,0x1c4b0,39248,45392,54608,0x1c4c0,39264,45408,54624,0x1c4d0,39280,45424,54640,0x1c4e0,39296,45440,54656,0x1c4f0,39312,45456,54672,0x1c500,39328,45472,54688,0x1c510,39344,45488,54704,0x1c520,39360,45504,54720,0x1c530,39376,45520,54736,0x1c540,39392,45536,54752,0x1c550,39408,45552,54768,79936,86016,0x1d600,0x1d608,80080,86160,0x1d610,0x1d618,80224,86304,0x1d620,0x1d628,80368,86448,0x1d630,0x1d638,80384,86592,0x1d640,0x1d648,80528,86736,0x1d650,0x1d658,80672,86880,0x1d660,0x1d668,80816,87024,0x1d670,0x1d678,80960,87040,0x1d680,0x1d688,81104,87184,0x1d690,0x1d698,81248,87328,0x1d6a0,0x1d6a8,81392,87472,0x1d6b0,0x1d6b8,81408,87616,0x1d6c0,0x1d6c8,81552,87760,0x1d6d0,0x1d6d8,81696,87904,0x1d6e0,0x1d6e8,81840,88048,0x1d6f0,0x1d6f8,79952,86024,0x1d700,0x1d708,80064,86168,0x1d710,0x1d718,80240,86312,0x1d720,0x1d728,80352,86456,0x1d730,0x1d738,80400,86600,0x1d740,0x1d748,80512,86744,0x1d750,0x1d758,80688,86888,0x1d760,0x1d768,80800,87032,0x1d770,0x1d778,80976,87048,0x1d780,0x1d788,81088,87192,0x1d790,0x1d798,81264,87336,0x1d7a0,0x1d7a8,81376,87480,0x1d7b0,0x1d7b8,81424,87624,0x1d7c0,0x1d7c8,81536,87768,0x1d7d0,0x1d7d8,81712,87912,0x1d7e0,0x1d7e8,81824,88056,0x1d7f0,0x1d7f8,79968,86032,0x1d800,0x1d808,80112,86144,0x1d810,0x1d818,80192,86320,0x1d820,0x1d828,80336,86432,0x1d830,0x1d838,80416,86608,0x1d840,0x1d848,80560,86720,0x1d850,0x1d858,80640,86896,0x1d860,0x1d868,80784,87008,0x1d870,0x1d878,80992,87056,0x1d880,0x1d888,81136,87168,0x1d890,0x1d898,81216,87344,0x1d8a0,0x1d8a8,81360,87456,0x1d8b0,0x1d8b8,81440,87632,0x1d8c0,0x1d8c8,81584,87744,0x1d8d0,0x1d8d8,81664,87920,0x1d8e0,0x1d8e8,81808,88032,0x1d8f0,0x1d8f8,79984,86040,0x1d900,0x1d908,80096,86152,0x1d910,0x1d918,80208,86328,0x1d920,0x1d928,80320,86440,0x1d930,0x1d938,80432,86616,0x1d940,0x1d948,80544,86728,0x1d950,0x1d958,80656,86904,0x1d960,0x1d968,80768,87016,0x1d970,0x1d978,81008,87064,0x1d980,0x1d988,81120,87176,0x1d990,0x1d998,81232,87352,0x1d9a0,0x1d9a8,81344,87464,0x1d9b0,0x1d9b8,81456,87640,0x1d9c0,0x1d9c8,81568,87752,0x1d9d0,0x1d9d8,81680,87928,0x1d9e0,0x1d9e8,81792,88040,0x1d9f0,0x1d9f8,0x1e700,0x1e700,0x1e980,0x100000,0x1e702,0x100000,0x100000,0x1e980,0x1e704,0x100000,0x100000,0x100000,0x1e706,0x100000,0x100000,0x100000,0x1e710,0x1e700,0x100000,0x100000,0x1e712,0x100000,0x100000,0x1e980,0x1e714,0x100000,0x100000,0x100000,0x1e716,0x100000,0x100000,0x100000,0x1e720,0x1e700,0x100000,0x100000,0x1e722,0x100000,0x100000,0x1e980,0x1e724,0x100000,0x100000,0x100000,0x1e726,0x100000,0x100000,0x100000,0x1e730,0x1e700,0x100000,0x100000,0x1e732,0x100000,0x100000,0x1e980,0x1e734,0x100000,0x100000,0x100000,0x1e736,0x100000,0x100000,0x100000,0x1e708,0x1e710,0x1e9b0,0x100000,0x1e70a,0x100000,0x100000,0x1e9b0,0x1e70c,0x100000,0x100000,0x100000,0x1e70e,0x100000,0x100000,0x100000,0x1e718,0x1e710,0x100000,0x100000,0x1e71a,0x100000,0x100000,0x1e9b0,0x1e71c,0x100000,0x100000,0x100000,0x1e71e,0x100000,0x100000,0x100000,0x1e728,0x1e710,0x100000,0x100000,0x1e72a,0x100000,0x100000,0x1e9b0,0x1e72c,0x100000,0x100000,0x100000,0x1e72e,0x100000,0x100000,0x100000,0x1e738,0x1e710,0x100000,0x100000,0x1e73a,0x100000,0x100000,0x1e9b0,0x1e73c,0x100000,0x100000,0x100000,0x1e73e,0x100000,0x100000,0x100000,0x1e740,0x1e720,0x1e9e0,0x100000,0x1e742,0x100000,0x100000,0x1e9e0,0x1e744,0x100000,0x100000,0x100000,0x1e746,0x100000,0x100000,0x100000,0x1e750,0x1e720,0x100000,0x100000,0x1e752,0x100000,0x100000,0x1e9e0,0x1e754,0x100000,0x100000,0x100000,0x1e756,0x100000,0x100000,0x100000,0x1e760,0x1e720,0x100000,0x100000,0x1e762,0x100000,0x100000,0x1e9e0,0x1e764,0x100000,0x100000,0x100000,0x1e766,0x100000,0x100000,0x100000,0x1e770,0x1e720,0x100000,0x100000,0x1e772,0x100000,0x100000,0x1e9e0,0x1e774,0x100000,0x100000,0x100000,0x1e776,0x100000,0x100000,0x100000,0x1e748,0x1e730,0x1ea10,0x100000,0x1e74a,0x100000,0x100000,0x1ea10,0x1e74c,0x100000,0x100000,0x100000,0x1e74e,0x100000,0x100000,0x100000,0x1e758,0x1e730,0x100000,0x100000,0x1e75a,0x100000,0x100000,0x1ea10,0x1e75c,0x100000,0x100000,0x100000,0x1e75e,0x100000,0x100000,0x100000,0x1e768,0x1e730,0x100000,0x100000,0x1e76a,0x100000,0x100000,0x1ea10,0x1e76c,0x100000,0x100000,0x100000,0x1e76e,0x100000,0x100000,0x100000,0x1e778,0x1e730,0x100000,0x100000,0x1e77a,0x100000,0x100000,0x1ea10,0x1e77c,0x100000,0x100000,0x100000,0x1e77e,0x100000,0x100000,0x100000,0x1ebc0,0x25260,37056,37120,0x1ebc2,0x25262,37072,37128,0x1ebc4,0x25264,37088,37136,0x1ebc6,0x25266,37104,37144,0x1ebc8,0x253b0,37440,37504,0x1ebca,0x253b2,37456,37512,0x1ebcc,0x253b4,37472,37520,0x1ebce,0x253b6,37488,37528,0x1ebd0,0x25500,37856,37920,0x1ebd2,0x25502,37872,37928,0x1ebd4,0x25504,37824,37936,0x1ebd6,0x25506,37840,37944,0x1ebd8,0x25650,38240,38304,0x1ebda,0x25652,38256,38312,0x1ebdc,0x25654,38208,38320,0x1ebde,0x25656,38224,38328,0x1ebc0,0x25268,38528,38720,0x1ebc2,0x2526a,38544,38728,0x1ebc4,0x2526c,38560,38736,0x1ebc6,0x2526e,38576,38744,0x1ebc8,0x253b8,38912,39104,0x1ebca,0x253ba,38928,39112,0x1ebcc,0x253bc,38944,39120,0x1ebce,0x253be,38960,39128,0x1ebd0,0x25508,39328,39520,0x1ebd2,0x2550a,39344,39528,0x1ebd4,0x2550c,39296,39536,0x1ebd6,0x2550e,39312,39544,0x1ebd8,0x25658,39712,39904,0x1ebda,0x2565a,39728,39912,0x1ebdc,0x2565c,39680,39920,0x1ebde,0x2565e,39696,39928,0x1ebc0,0x26220,40128,40192,0x1ebc2,0x26222,40144,40200,0x1ebc4,0x26224,40160,40208,0x1ebc6,0x26226,40176,40216,0x1ebc8,0x26370,40512,40576,0x1ebca,0x26372,40528,40584,0x1ebcc,0x26374,40544,40592,0x1ebce,0x26376,40560,40600,0x1ebd0,0x264c0,40928,40992,0x1ebd2,0x264c2,40944,41000,0x1ebd4,0x264c4,40896,41008,0x1ebd6,0x264c6,40912,41016,0x1ebd8,0x26610,41312,41376,0x1ebda,0x26612,41328,41384,0x1ebdc,0x26614,41280,41392,0x1ebde,0x26616,41296,41400,0x1ebc0,0x100000,41600,41792,0x1ebc2,0x100000,41616,41800,0x1ebc4,0x100000,41632,41808,0x1ebc6,0x100000,41648,41816,0x1ebc8,0x100000,41984,42176,0x1ebca,0x100000,42000,42184,0x1ebcc,0x100000,42016,42192,0x1ebce,0x100000,42032,42200,0x1ebd0,0x100000,42400,42592,0x1ebd2,0x100000,42416,42600,0x1ebd4,0x100000,42368,42608,0x1ebd6,0x100000,42384,42616,0x1ebd8,0x100000,42784,42976,0x1ebda,0x100000,42800,42984,0x1ebdc,0x100000,42752,42992,0x1ebde,0x100000,42768,43000,37056,37088,37120,55296,37064,37096,37128,55312,37072,37104,37136,55328,37080,37112,37144,55344,38208,38240,38272,55360,38216,38248,38280,55376,38224,38256,38288,55392,38232,38264,38296,55408,39392,39360,39456,55424,39400,39368,39464,55440,39408,39376,39472,55456,39416,39384,39480,55472,40544,40512,40608,55488,40552,40520,40616,55504,40560,40528,40624,55520,40568,40536,40632,55536,46208,46240,46400,55552,46216,46248,46408,55568,46224,46256,46416,55584,46232,46264,46424,55600,47360,47392,47552,55616,47368,47400,47560,55632,47376,47408,47568,55648,47384,47416,47576,55664,48544,48512,48736,55680,48552,48520,48744,55696,48560,48528,48752,55712,48568,48536,48760,55728,49696,49664,49888,55744,49704,49672,49896,55760,49712,49680,49904,55776,49720,49688,49912,55792,55488,55520,55552,55808,55496,55528,55560,55824,55504,55536,55568,55840,55512,55544,55576,55856,56640,56672,56704,55872,56648,56680,56712,55888,56656,56688,56720,55904,56664,56696,56728,55920,57824,57792,57888,55936,57832,57800,57896,55952,57840,57808,57904,55968,57848,57816,57912,55984,58976,58944,59040,56000,58984,58952,59048,56016,58992,58960,59056,56032,59000,58968,59064,56048,64640,64672,64832,56064,64648,64680,64840,56080,64656,64688,64848,56096,64664,64696,64856,56112,65792,65824,65984,56128,65800,65832,65992,56144,65808,65840,66000,56160,65816,65848,66008,56176,66976,66944,67168,56192,66984,66952,67176,56208,66992,66960,67184,56224,67000,66968,67192,56240,68128,68096,68320,56256,68136,68104,68328,56272,68144,68112,68336,56288,68152,68120,68344,56304,0x1e800,0x1e800,0x20cc0,36864,0x1e8c4,0x1e8c4,0x20cc4,36880,0x1e988,0x1e988,0x20cc8,36896,0x1ea4c,0x1ea4c,0x20ccc,36912,0x1eb10,0x1eb10,0x20cd0,36928,0x1ebd4,0x1ebd4,0x20cd4,36944,0x1ec98,0x1ec98,0x20cd8,36960,0x1ed5c,0x1ed5c,0x20cdc,36976,0x1ee20,0x1ee20,0x20ce0,37248,0x1eee4,0x1eee4,0x20ce4,37264,0x1efa8,0x1efa8,0x20ce8,37280,0x1f06c,0x1f06c,0x20cec,37296,0x1f130,0x1f130,0x20cf0,37312,0x1f1f4,0x1f1f4,0x20cf4,37328,0x1f2b8,0x1f2b8,0x20cf8,37344,0x1f37c,0x1f37c,0x20cfc,37360,0x1e810,0x1e800,0x20cc0,37632,0x1e8d4,0x1e8c4,0x20cc4,37648,0x1e998,0x1e988,0x20cc8,37664,0x1ea5c,0x1ea4c,0x20ccc,37680,0x1eb20,0x1eb10,0x20cd0,37696,0x1ebe4,0x1ebd4,0x20cd4,37712,0x1eca8,0x1ec98,0x20cd8,37728,0x1ed6c,0x1ed5c,0x20cdc,37744,0x1ee30,0x1ee20,0x20ce0,38016,0x1eef4,0x1eee4,0x20ce4,38032,0x1efb8,0x1efa8,0x20ce8,38048,0x1f07c,0x1f06c,0x20cec,38064,0x1f140,0x1f130,0x20cf0,38080,0x1f204,0x1f1f4,0x20cf4,38096,0x1f2c8,0x1f2b8,0x20cf8,38112,0x1f38c,0x1f37c,0x20cfc,38128,0x1e820,0x1e800,0x20cc0,38400,0x1e8e4,0x1e8c4,0x20cc4,38416,0x1e9a8,0x1e988,0x20cc8,38432,0x1ea6c,0x1ea4c,0x20ccc,38448,0x1eb30,0x1eb10,0x20cd0,38464,0x1ebf4,0x1ebd4,0x20cd4,38480,0x1ecb8,0x1ec98,0x20cd8,38496,0x1ed7c,0x1ed5c,0x20cdc,38512,0x1ee40,0x1ee20,0x20ce0,38784,0x1ef04,0x1eee4,0x20ce4,38800,0x1efc8,0x1efa8,0x20ce8,38816,0x1f08c,0x1f06c,0x20cec,38832,0x1f150,0x1f130,0x20cf0,38848,0x1f214,0x1f1f4,0x20cf4,38864,0x1f2d8,0x1f2b8,0x20cf8,38880,0x1f39c,0x1f37c,0x20cfc,38896,0x1e830,0x1e800,0x20cc0,39168,0x1e8f4,0x1e8c4,0x20cc4,39184,0x1e9b8,0x1e988,0x20cc8,39200,0x1ea7c,0x1ea4c,0x20ccc,39216,0x1eb40,0x1eb10,0x20cd0,39232,0x1ec04,0x1ebd4,0x20cd4,39248,0x1ecc8,0x1ec98,0x20cd8,39264,0x1ed8c,0x1ed5c,0x20cdc,39280,0x1ee50,0x1ee20,0x20ce0,39552,0x1ef14,0x1eee4,0x20ce4,39568,0x1efd8,0x1efa8,0x20ce8,39584,0x1f09c,0x1f06c,0x20cec,39600,0x1f160,0x1f130,0x20cf0,39616,0x1f224,0x1f1f4,0x20cf4,39632,0x1f2e8,0x1f2b8,0x20cf8,39648,0x1f3ac,0x1f37c,0x20cfc,39664,73728,0x26960,0x1e980,0x1ebc0,73744,0x26964,0x1ee10,0x1f050,73760,0x26968,0x1f2a0,0x1f4e0,73776,0x2696c,0x1f730,0x1f970,73792,0x26970,0x1fbc0,0x1fe00,73808,0x26974,0x20050,0x20290,73824,0x26978,0x204e0,0x20720,73840,0x2697c,0x20970,0x20bb0,74112,0x26980,0x20e00,0x21040,74128,0x26984,0x21290,0x214d0,74144,0x26988,0x21720,0x21960,74160,0x2698c,0x21bb0,0x21df0,74176,0x26990,0x22040,0x22280,74192,0x26994,0x224d0,0x22710,74208,0x26998,0x22960,0x22ba0,74224,0x2699c,0x22df0,0x23030,74528,0x269a0,0x1e9b0,0x1ebc8,74544,0x269a4,0x1ee40,0x1f058,74496,0x269a8,0x1f2d0,0x1f4e8,74512,0x269ac,0x1f760,0x1f978,74592,0x269b0,0x1fbf0,0x1fe08,74608,0x269b4,0x20080,0x20298,74560,0x269b8,0x20510,0x20728,74576,0x269bc,0x209a0,0x20bb8,74912,0x269c0,0x20e30,0x21048,74928,0x269c4,0x212c0,0x214d8,74880,0x269c8,0x21750,0x21968,74896,0x269cc,0x21be0,0x21df8,74976,0x269d0,0x22070,0x22288,74992,0x269d4,0x22500,0x22718,74944,0x269d8,0x22990,0x22ba8,74960,0x269dc,0x22e20,0x23038,75328,0x269e0,0x1e9e0,0x1ebd0,75344,0x269e4,0x1ee70,0x1f060,75360,0x269e8,0x1f300,0x1f4f0,75376,0x269ec,0x1f790,0x1f980,75264,0x269f0,0x1fc20,0x1fe10,75280,0x269f4,0x200b0,0x202a0,75296,0x269f8,0x20540,0x20730,75312,0x269fc,0x209d0,0x20bc0,75712,0x26a00,0x20e60,0x21050,75728,0x26a04,0x212f0,0x214e0,75744,0x26a08,0x21780,0x21970,75760,0x26a0c,0x21c10,0x21e00,75648,0x26a10,0x220a0,0x22290,75664,0x26a14,0x22530,0x22720,75680,0x26a18,0x229c0,0x22bb0,75696,0x26a1c,0x22e50,0x23040,76128,0x26a20,0x1ea10,0x1ebd8,76144,0x26a24,0x1eea0,0x1f068,76096,0x26a28,0x1f330,0x1f4f8,76112,0x26a2c,0x1f7c0,0x1f988,76064,0x26a30,0x1fc50,0x1fe18,76080,0x26a34,0x200e0,0x202a8,76032,0x26a38,0x20570,0x20738,76048,0x26a3c,0x20a00,0x20bc8,76512,0x26a40,0x20e90,0x21058,76528,0x26a44,0x21320,0x214e8,76480,0x26a48,0x217b0,0x21978,76496,0x26a4c,0x21c40,0x21e08,76448,0x26a50,0x220d0,0x22298,76464,0x26a54,0x22560,0x22728,76416,0x26a58,0x229f0,0x22bb8,76432,0x26a5c,0x22e80,0x23048,0x3f803f80,0x3f803f80,16256,0,0,0x3f803f80,0x3f800000,0,0,0x3f803f80,0,16256,0,0x3f803f80,0,0x3f800000,0x3f803f80,0,0,0,0,0,0,0,0,0,0,0,0,0,0,0,0x3f803f80,0,0,0,0,0,0,0,0,0,0,0,0,0,0,0,0x3f803f80,0,0,0,0,0,0,0,0,0,0,0,0,0,0,0,0x3f803f80,0,0,0,0,0,0,0,0,0,0,0,0,0,0,0,0x3f803f80,0x3f803f80,16256,0,0,0x3f803f80,0x3f800000,0,0,0x3f803f80,0,16256,0,0x3f803f80,0,0x3f800000,0x3f803f80,0,0,0,0,0,0,0,0,0,0,0,0,0,0,0,0x3f803f80,0,0,0,0,0,0,0,0,0,0,0,0,0,0,0,0x3f803f80,0,0,0,0,0,0,0,0,0,0,0,0,0,0,0,0x3f803f80,0,0,0,0,0,0,0,0,0,0,0,0,0,0,0,0x3f803f80,0x3f803f80,16256,0,0,0x3f803f80,0x3f800000,0,0,0x3f803f80,0,16256,0,0x3f803f80,0,0x3f800000,0x3f803f80,0,0,0,0,0,0,0,0,0,0,0,0,0,0,0,0x3f803f80,0,0,0,0,0,0,0,0,0,0,0,0,0,0,0,0x3f803f80,0,0,0,0,0,0,0,0,0,0,0,0,0,0,0,0x3f803f80,0,0,0,0,0,0,0,0,0,0,0,0,0,0,0,0x3f803f80,0x3f803f80,16256,0,0,0x3f803f80,0x3f800000,0,0,0x3f803f80,0,16256,0,0x3f803f80,0,0x3f800000,0,64,0x90000,0xf0080,144,208,0x90010,0xf0090,288,352,0x90020,0xf00a0,432,496,0x90030,0xf00b0,576,512,0x90040,0xf00c0,720,656,0x90050,0xf00d0,864,800,0x90060,0xf00e0,1008,944,0x90070,0xf00f0,1024,1088,0x90190,0xf0210,1168,1232,0x90180,0xf0200,1312,1376,0x901b0,0xf0230,1456,1520,0x901a0,0xf0220,1600,1536,0x901d0,0xf0250,1744,1680,0x901c0,0xf0240,1888,1824,0x901f0,0xf0270,2032,1968,0x901e0,0xf0260,16,80,0x90320,0x11f7a0,128,192,0x90330,0x11f7b0,304,368,0x90300,0x11f780,416,480,0x90310,0x11f790,592,528,0x90360,0x11f7e0,704,640,0x90370,0x11f7f0,880,816,0x90340,0x11f7c0,992,928,0x90350,0x11f7d0,1040,1104,0x904b0,0x11f930,1152,1216,0x904a0,0x11f920,1328,1392,0x90490,0x11f910,1440,1504,0x90480,0x11f900,1616,1552,0x904f0,0x11f970,1728,1664,0x904e0,0x11f960,1904,1840,0x904d0,0x11f950,2016,1952,0x904c0,0x11f940,32,96,0xbfa40,0x11fac0,176,240,0xbfa50,0x11fad0,256,320,0xbfa60,0x11fae0,400,464,0xbfa70,0x11faf0,608,544,0xbfa00,0x11fa80,752,688,0xbfa10,0x11fa90,832,768,0xbfa20,0x11faa0,976,912,0xbfa30,0x11fab0,1056,1120,0xbfbd0,0x11fc50,1200,1264,0xbfbc0,0x11fc40,1280,1344,0xbfbf0,0x11fc70,1424,1488,0xbfbe0,0x11fc60,1632,1568,0xbfb90,0x11fc10,1776,1712,0xbfb80,0x11fc00,1856,1792,0xbfbb0,0x11fc30,2000,1936,0xbfba0,0x11fc20,48,112,0xbfd60,0x11fde0,160,224,0xbfd70,0x11fdf0,272,336,0xbfd40,0x11fdc0,384,448,0xbfd50,0x11fdd0,624,560,0xbfd20,0x11fda0,736,672,0xbfd30,0x11fdb0,848,784,0xbfd00,0x11fd80,960,896,0xbfd10,0x11fd90,1072,1136,0xbfef0,0x11ff70,1184,1248,0xbfee0,0x11ff60,1296,1360,0xbfed0,0x11ff50,1408,1472,0xbfec0,0x11ff40,1648,1584,0xbfeb0,0x11ff30,1760,1696,0xbfea0,0x11ff20,1872,1808,0xbfe90,0x11ff10,1984,1920,0xbfe80,0x11ff00,0x11fb40,76800,84992,91648,0x11fb50,76816,85008,91664,0x11fb60,76832,85024,91680,0x11fb70,76848,85040,91696,0x11fb00,76864,85056,91712,0x11fb10,76880,85072,91728,0x11fb20,76896,85088,91744,0x11fb30,76912,85104,91760,0x11fcd0,76928,85120,91776,0x11fcc0,76944,85136,91792,0x11fcf0,76960,85152,91808,0x11fce0,76976,85168,91824,0x11fc90,76992,85184,91840,0x11fc80,77008,85200,91856,0x11fcb0,77024,85216,91872,0x11fca0,77040,85232,91888,0x11fe60,77056,85248,91904,0x11fe70,77072,85264,91920,0x11fe40,77088,85280,91936,0x11fe50,77104,85296,91952,0x11fe20,77120,85312,91968,0x11fe30,77136,85328,91984,0x11fe00,77152,85344,92000,0x11fe10,77168,85360,92016,0x11fff0,77184,85376,92032,0x11ffe0,77200,85392,92048,0x11ffd0,77216,85408,92064,0x11ffc0,77232,85424,92080,0x11ffb0,77248,85440,92096,0x11ffa0,77264,85456,92112,0x11ff90,77280,85472,92128,0x11ff80,77296,85488,92144,0x11fb40,77312,85504,92160,0x11fb50,77328,85520,92176,0x11fb60,77344,85536,92192,0x11fb70,77360,85552,92208,0x11fb00,77376,85568,92224,0x11fb10,77392,85584,92240,0x11fb20,77408,85600,92256,0x11fb30,77424,85616,92272,0x11fcd0,77440,85632,92288,0x11fcc0,77456,85648,92304,0x11fcf0,77472,85664,92320,0x11fce0,77488,85680,92336,0x11fc90,77504,85696,92352,0x11fc80,77520,85712,92368,0x11fcb0,77536,85728,92384,0x11fca0,77552,85744,92400,0x11fe60,77568,85760,92416,0x11fe70,77584,85776,92432,0x11fe40,77600,85792,92448,0x11fe50,77616,85808,92464,0x11fe20,77632,85824,92480,0x11fe30,77648,85840,92496,0x11fe00,77664,85856,92512,0x11fe10,77680,85872,92528,0x11fff0,77696,85888,92544,0x11ffe0,77712,85904,92560,0x11ffd0,77728,85920,92576,0x11ffc0,77744,85936,92592,0x11ffb0,77760,85952,92608,0x11ffa0,77776,85968,92624,0x11ff90,77792,85984,92640,0x11ff80,77808,86000,92656,0x1c210,36864,46080,52232,0x1c220,36880,46096,52248,0x1c230,36896,46112,52264,0x1c240,36912,46128,52280,0x1c250,36928,46144,52296,0x1c260,36944,46160,52312,0x1c270,36960,46176,52328,0x1c280,36976,46192,52344,0x1c290,36992,46208,52360,0x1c2a0,37008,46224,52376,0x1c2b0,37024,46240,52392,0x1c2c0,37040,46256,52408,0x1c2d0,37056,46272,52424,0x1c2e0,37072,46288,52440,0x1c2f0,37088,46304,52456,0x1c300,37104,46320,52472,0x1c310,37632,46848,53000,0x1c320,37648,46864,53016,0x1c330,37664,46880,53032,0x1c340,37680,46896,53048,0x1c350,37696,46912,53064,0x1c360,37712,46928,53080,0x1c370,37728,46944,53096,0x1c380,37744,46960,53112,0x1c390,37760,46976,53128,0x1c3a0,37776,46992,53144,0x1c3b0,37792,47008,53160,0x1c3c0,37808,47024,53176,0x1c3d0,37824,47040,53192,0x1c3e0,37840,47056,53208,0x1c3f0,37856,47072,53224,0x1c400,37872,47088,53240,0x1c410,38400,47616,53768,0x1c420,38416,47632,53784,0x1c430,38432,47648,53800,0x1c440,38448,47664,53816,0x1c450,38464,47680,53832,0x1c460,38480,47696,53848,0x1c470,38496,47712,53864,0x1c480,38512,47728,53880,0x1c490,38528,47744,53896,0x1c4a0,38544,47760,53912,0x1c4b0,38560,47776,53928,0x1c4c0,38576,47792,53944,0x1c4d0,38592,47808,53960,0x1c4e0,38608,47824,53976,0x1c4f0,38624,47840,53992,0x1c500,38640,47856,54008,0x1c510,39168,48384,54536,0x1c520,39184,48400,54552,0x1c530,39200,48416,54568,0x1c540,39216,48432,54584,0x1c550,39232,48448,54600,0x1c560,39248,48464,54616,0x1c570,39264,48480,54632,0x1c580,39280,48496,54648,0x1c590,39296,48512,54664,0x1c5a0,39312,48528,54680,0x1c5b0,39328,48544,54696,0x1c5c0,39344,48560,54712,0x1c5d0,39360,48576,54728,0x1c5e0,39376,48592,54744,0x1c5f0,39392,48608,54760,0x1c600,39408,48624,54776,86048,86112,0x1d600,0x1d60c,86192,86256,0x1d610,0x1d61c,86272,86336,0x1d620,0x1d62c,86416,86480,0x1d630,0x1d63c,86624,86560,0x1d640,0x1d64c,86768,86704,0x1d650,0x1d65c,86848,86784,0x1d660,0x1d66c,86992,86928,0x1d670,0x1d67c,87072,87136,0x1d680,0x1d68c,87216,87280,0x1d690,0x1d69c,87296,87360,0x1d6a0,0x1d6ac,87440,87504,0x1d6b0,0x1d6bc,87648,87584,0x1d6c0,0x1d6cc,87792,87728,0x1d6d0,0x1d6dc,87872,87808,0x1d6e0,0x1d6ec,88016,87952,0x1d6f0,0x1d6fc,86064,86120,0x1d700,0x1d70c,86176,86264,0x1d710,0x1d71c,86288,86344,0x1d720,0x1d72c,86400,86488,0x1d730,0x1d73c,86640,86568,0x1d740,0x1d74c,86752,86712,0x1d750,0x1d75c,86864,86792,0x1d760,0x1d76c,86976,86936,0x1d770,0x1d77c,87088,87144,0x1d780,0x1d78c,87200,87288,0x1d790,0x1d79c,87312,87368,0x1d7a0,0x1d7ac,87424,87512,0x1d7b0,0x1d7bc,87664,87592,0x1d7c0,0x1d7cc,87776,87736,0x1d7d0,0x1d7dc,87888,87816,0x1d7e0,0x1d7ec,88000,87960,0x1d7f0,0x1d7fc,86080,86128,0x1d800,0x1d80c,86224,86240,0x1d810,0x1d81c,86368,86352,0x1d820,0x1d82c,86512,86464,0x1d830,0x1d83c,86528,86576,0x1d840,0x1d84c,86672,86688,0x1d850,0x1d85c,86816,86800,0x1d860,0x1d86c,86960,86912,0x1d870,0x1d87c,87104,87152,0x1d880,0x1d88c,87248,87264,0x1d890,0x1d89c,87392,87376,0x1d8a0,0x1d8ac,87536,87488,0x1d8b0,0x1d8bc,87552,87600,0x1d8c0,0x1d8cc,87696,87712,0x1d8d0,0x1d8dc,87840,87824,0x1d8e0,0x1d8ec,87984,87936,0x1d8f0,0x1d8fc,86096,86136,0x1d900,0x1d90c,86208,86248,0x1d910,0x1d91c,86384,86360,0x1d920,0x1d92c,86496,86472,0x1d930,0x1d93c,86544,86584,0x1d940,0x1d94c,86656,86696,0x1d950,0x1d95c,86832,86808,0x1d960,0x1d96c,86944,86920,0x1d970,0x1d97c,87120,87160,0x1d980,0x1d98c,87232,87272,0x1d990,0x1d99c,87408,87384,0x1d9a0,0x1d9ac,87520,87496,0x1d9b0,0x1d9bc,87568,87608,0x1d9c0,0x1d9cc,87680,87720,0x1d9d0,0x1d9dc,87856,87832,0x1d9e0,0x1d9ec,87968,87944,0x1d9f0,0x1d9fc,0x1e780,0x1e780,0x1ea40,0x100000,0x1e782,0x100000,0x100000,0x1ea40,0x1e784,0x100000,0x100000,0x100000,0x1e786,0x100000,0x100000,0x100000,0x1e790,0x1e780,0x100000,0x100000,0x1e792,0x100000,0x100000,0x1ea40,0x1e794,0x100000,0x100000,0x100000,0x1e796,0x100000,0x100000,0x100000,0x1e7a0,0x1e780,0x100000,0x100000,0x1e7a2,0x100000,0x100000,0x1ea40,0x1e7a4,0x100000,0x100000,0x100000,0x1e7a6,0x100000,0x100000,0x100000,0x1e7b0,0x1e780,0x100000,0x100000,0x1e7b2,0x100000,0x100000,0x1ea40,0x1e7b4,0x100000,0x100000,0x100000,0x1e7b6,0x100000,0x100000,0x100000,0x1e788,0x1e790,0x1ea70,0x100000,0x1e78a,0x100000,0x100000,0x1ea70,0x1e78c,0x100000,0x100000,0x100000,0x1e78e,0x100000,0x100000,0x100000,0x1e798,0x1e790,0x100000,0x100000,0x1e79a,0x100000,0x100000,0x1ea70,0x1e79c,0x100000,0x100000,0x100000,0x1e79e,0x100000,0x100000,0x100000,0x1e7a8,0x1e790,0x100000,0x100000,0x1e7aa,0x100000,0x100000,0x1ea70,0x1e7ac,0x100000,0x100000,0x100000,0x1e7ae,0x100000,0x100000,0x100000,0x1e7b8,0x1e790,0x100000,0x100000,0x1e7ba,0x100000,0x100000,0x1ea70,0x1e7bc,0x100000,0x100000,0x100000,0x1e7be,0x100000,0x100000,0x100000,0x1e7c0,0x1e7a0,0x1eaa0,0x100000,0x1e7c2,0x100000,0x100000,0x1eaa0,0x1e7c4,0x100000,0x100000,0x100000,0x1e7c6,0x100000,0x100000,0x100000,0x1e7d0,0x1e7a0,0x100000,0x100000,0x1e7d2,0x100000,0x100000,0x1eaa0,0x1e7d4,0x100000,0x100000,0x100000,0x1e7d6,0x100000,0x100000,0x100000,0x1e7e0,0x1e7a0,0x100000,0x100000,0x1e7e2,0x100000,0x100000,0x1eaa0,0x1e7e4,0x100000,0x100000,0x100000,0x1e7e6,0x100000,0x100000,0x100000,0x1e7f0,0x1e7a0,0x100000,0x100000,0x1e7f2,0x100000,0x100000,0x1eaa0,0x1e7f4,0x100000,0x100000,0x100000,0x1e7f6,0x100000,0x100000,0x100000,0x1e7c8,0x1e7b0,0x1ead0,0x100000,0x1e7ca,0x100000,0x100000,0x1ead0,0x1e7cc,0x100000,0x100000,0x100000,0x1e7ce,0x100000,0x100000,0x100000,0x1e7d8,0x1e7b0,0x100000,0x100000,0x1e7da,0x100000,0x100000,0x1ead0,0x1e7dc,0x100000,0x100000,0x100000,0x1e7de,0x100000,0x100000,0x100000,0x1e7e8,0x1e7b0,0x100000,0x100000,0x1e7ea,0x100000,0x100000,0x1ead0,0x1e7ec,0x100000,0x100000,0x100000,0x1e7ee,0x100000,0x100000,0x100000,0x1e7f8,0x1e7b0,0x100000,0x100000,0x1e7fa,0x100000,0x100000,0x1ead0,0x1e7fc,0x100000,0x100000,0x100000,0x1e7fe,0x100000,0x100000,0x100000,0x1ec20,0x257a0,37152,37216,0x1ec22,0x257a2,37168,37224,0x1ec24,0x257a4,37184,37232,0x1ec26,0x257a6,37200,37240,0x1ec28,0x258f0,37536,37600,0x1ec2a,0x258f2,37552,37608,0x1ec2c,0x258f4,37568,37616,0x1ec2e,0x258f6,37584,37624,0x1ec30,0x25a40,37888,37952,0x1ec32,0x25a42,37904,37960,0x1ec34,0x25a44,37984,37968,0x1ec36,0x25a46,38000,37976,0x1ec38,0x25b90,38272,38336,0x1ec3a,0x25b92,38288,38344,0x1ec3c,0x25b94,38368,38352,0x1ec3e,0x25b96,38384,38360,0x1ec20,0x257a8,38752,38688,0x1ec22,0x257aa,38768,38696,0x1ec24,0x257ac,38656,38704,0x1ec26,0x257ae,38672,38712,0x1ec28,0x258f8,39136,39072,0x1ec2a,0x258fa,39152,39080,0x1ec2c,0x258fc,39040,39088,0x1ec2e,0x258fe,39056,39096,0x1ec30,0x25a48,39488,39424,0x1ec32,0x25a4a,39504,39432,0x1ec34,0x25a4c,39456,39440,0x1ec36,0x25a4e,39472,39448,0x1ec38,0x25b98,39872,39808,0x1ec3a,0x25b9a,39888,39816,0x1ec3c,0x25b9c,39840,39824,0x1ec3e,0x25b9e,39856,39832,0x1ec20,0x26228,40224,40288,0x1ec22,0x2622a,40240,40296,0x1ec24,0x2622c,40256,40304,0x1ec26,0x2622e,40272,40312,0x1ec28,0x26378,40608,40672,0x1ec2a,0x2637a,40624,40680,0x1ec2c,0x2637c,40640,40688,0x1ec2e,0x2637e,40656,40696,0x1ec30,0x264c8,40960,41024,0x1ec32,0x264ca,40976,41032,0x1ec34,0x264cc,41056,41040,0x1ec36,0x264ce,41072,41048,0x1ec38,0x26618,41344,41408,0x1ec3a,0x2661a,41360,41416,0x1ec3c,0x2661c,41440,41424,0x1ec3e,0x2661e,41456,41432,0x1ec20,0x100000,41824,41760,0x1ec22,0x100000,41840,41768,0x1ec24,0x100000,41728,41776,0x1ec26,0x100000,41744,41784,0x1ec28,0x100000,42208,42144,0x1ec2a,0x100000,42224,42152,0x1ec2c,0x100000,42112,42160,0x1ec2e,0x100000,42128,42168,0x1ec30,0x100000,42560,42496,0x1ec32,0x100000,42576,42504,0x1ec34,0x100000,42528,42512,0x1ec36,0x100000,42544,42520,0x1ec38,0x100000,42944,42880,0x1ec3a,0x100000,42960,42888,0x1ec3c,0x100000,42912,42896,0x1ec3e,0x100000,42928,42904,37152,37184,37216,64512,37160,37192,37224,64528,37168,37200,37232,64544,37176,37208,37240,64560,38304,38336,38368,64576,38312,38344,38376,64592,38320,38352,38384,64608,38328,38360,38392,64624,39424,39520,39488,64640,39432,39528,39496,64656,39440,39536,39504,64672,39448,39544,39512,64688,40576,40672,40640,64704,40584,40680,40648,64720,40592,40688,40656,64736,40600,40696,40664,64752,46432,46336,46368,64768,46440,46344,46376,64784,46448,46352,46384,64800,46456,46360,46392,64816,47584,47488,47520,64832,47592,47496,47528,64848,47600,47504,47536,64864,47608,47512,47544,64880,48704,48672,48640,64896,48712,48680,48648,64912,48720,48688,48656,64928,48728,48696,48664,64944,49856,49824,49792,64960,49864,49832,49800,64976,49872,49840,49808,64992,49880,49848,49816,65008,55584,55616,55648,65024,55592,55624,55656,65040,55600,55632,55664,65056,55608,55640,55672,65072,56736,56768,56800,65088,56744,56776,56808,65104,56752,56784,56816,65120,56760,56792,56824,65136,57856,57952,57920,65152,57864,57960,57928,65168,57872,57968,57936,65184,57880,57976,57944,65200,59008,59104,59072,65216,59016,59112,59080,65232,59024,59120,59088,65248,59032,59128,59096,65264,64864,64768,64800,65280,64872,64776,64808,65296,64880,64784,64816,65312,64888,64792,64824,65328,66016,65920,65952,65344,66024,65928,65960,65360,66032,65936,65968,65376,66040,65944,65976,65392,67136,67104,67072,65408,67144,67112,67080,65424,67152,67120,67088,65440,67160,67128,67096,65456,68288,68256,68224,65472,68296,68264,68232,65488,68304,68272,68240,65504,68312,68280,68248,65520,0x1e800,0x1e800,0x20cc0,55296,0x1e8c4,0x1e8c4,0x20cc4,55312,0x1e988,0x1e988,0x20cc8,55328,0x1ea4c,0x1ea4c,0x20ccc,55344,0x1eb10,0x1eb10,0x20cd0,55360,0x1ebd4,0x1ebd4,0x20cd4,55376,0x1ec98,0x1ec98,0x20cd8,55392,0x1ed5c,0x1ed5c,0x20cdc,55408,0x1ee20,0x1ee20,0x20ce0,55680,0x1eee4,0x1eee4,0x20ce4,55696,0x1efa8,0x1efa8,0x20ce8,55712,0x1f06c,0x1f06c,0x20cec,55728,0x1f130,0x1f130,0x20cf0,55744,0x1f1f4,0x1f1f4,0x20cf4,55760,0x1f2b8,0x1f2b8,0x20cf8,55776,0x1f37c,0x1f37c,0x20cfc,55792,0x1e810,0x1e800,0x20cc0,56064,0x1e8d4,0x1e8c4,0x20cc4,56080,0x1e998,0x1e988,0x20cc8,56096,0x1ea5c,0x1ea4c,0x20ccc,56112,0x1eb20,0x1eb10,0x20cd0,56128,0x1ebe4,0x1ebd4,0x20cd4,56144,0x1eca8,0x1ec98,0x20cd8,56160,0x1ed6c,0x1ed5c,0x20cdc,56176,0x1ee30,0x1ee20,0x20ce0,56448,0x1eef4,0x1eee4,0x20ce4,56464,0x1efb8,0x1efa8,0x20ce8,56480,0x1f07c,0x1f06c,0x20cec,56496,0x1f140,0x1f130,0x20cf0,56512,0x1f204,0x1f1f4,0x20cf4,56528,0x1f2c8,0x1f2b8,0x20cf8,56544,0x1f38c,0x1f37c,0x20cfc,56560,0x1e820,0x1e800,0x20cc0,56832,0x1e8e4,0x1e8c4,0x20cc4,56848,0x1e9a8,0x1e988,0x20cc8,56864,0x1ea6c,0x1ea4c,0x20ccc,56880,0x1eb30,0x1eb10,0x20cd0,56896,0x1ebf4,0x1ebd4,0x20cd4,56912,0x1ecb8,0x1ec98,0x20cd8,56928,0x1ed7c,0x1ed5c,0x20cdc,56944,0x1ee40,0x1ee20,0x20ce0,57216,0x1ef04,0x1eee4,0x20ce4,57232,0x1efc8,0x1efa8,0x20ce8,57248,0x1f08c,0x1f06c,0x20cec,57264,0x1f150,0x1f130,0x20cf0,57280,0x1f214,0x1f1f4,0x20cf4,57296,0x1f2d8,0x1f2b8,0x20cf8,57312,0x1f39c,0x1f37c,0x20cfc,57328,0x1e830,0x1e800,0x20cc0,57600,0x1e8f4,0x1e8c4,0x20cc4,57616,0x1e9b8,0x1e988,0x20cc8,57632,0x1ea7c,0x1ea4c,0x20ccc,57648,0x1eb40,0x1eb10,0x20cd0,57664,0x1ec04,0x1ebd4,0x20cd4,57680,0x1ecc8,0x1ec98,0x20cd8,57696,0x1ed8c,0x1ed5c,0x20cdc,57712,0x1ee50,0x1ee20,0x20ce0,57984,0x1ef14,0x1eee4,0x20ce4,58000,0x1efd8,0x1efa8,0x20ce8,58016,0x1f09c,0x1f06c,0x20cec,58032,0x1f160,0x1f130,0x20cf0,58048,0x1f224,0x1f1f4,0x20cf4,58064,0x1f2e8,0x1f2b8,0x20cf8,58080,0x1f3ac,0x1f37c,0x20cfc,58096,92160,0x26a60,0x1ea40,0x1ec20,92176,0x26a64,0x1eed0,0x1f0b0,92192,0x26a68,0x1f360,0x1f540,92208,0x26a6c,0x1f7f0,0x1f9d0,92224,0x26a70,0x1fc80,0x1fe60,92240,0x26a74,0x20110,0x202f0,92256,0x26a78,0x205a0,0x20780,92272,0x26a7c,0x20a30,0x20c10,92544,0x26a80,0x20ec0,0x210a0,92560,0x26a84,0x21350,0x21530,92576,0x26a88,0x217e0,0x219c0,92592,0x26a8c,0x21c70,0x21e50,92608,0x26a90,0x22100,0x222e0,92624,0x26a94,0x22590,0x22770,92640,0x26a98,0x22a20,0x22c00,92656,0x26a9c,0x22eb0,0x23090,92960,0x26aa0,0x1ea70,0x1ec28,92976,0x26aa4,0x1ef00,0x1f0b8,92928,0x26aa8,0x1f390,0x1f548,92944,0x26aac,0x1f820,0x1f9d8,93024,0x26ab0,0x1fcb0,0x1fe68,93040,0x26ab4,0x20140,0x202f8,92992,0x26ab8,0x205d0,0x20788,93008,0x26abc,0x20a60,0x20c18,93344,0x26ac0,0x20ef0,0x210a8,93360,0x26ac4,0x21380,0x21538,93312,0x26ac8,0x21810,0x219c8,93328,0x26acc,0x21ca0,0x21e58,93408,0x26ad0,0x22130,0x222e8,93424,0x26ad4,0x225c0,0x22778,93376,0x26ad8,0x22a50,0x22c08,93392,0x26adc,0x22ee0,0x23098,93760,0x26ae0,0x1eaa0,0x1ec30,93776,0x26ae4,0x1ef30,0x1f0c0,93792,0x26ae8,0x1f3c0,0x1f550,93808,0x26aec,0x1f850,0x1f9e0,93696,0x26af0,0x1fce0,0x1fe70,93712,0x26af4,0x20170,0x20300,93728,0x26af8,0x20600,0x20790,93744,0x26afc,0x20a90,0x20c20,94144,0x26b00,0x20f20,0x210b0,94160,0x26b04,0x213b0,0x21540,94176,0x26b08,0x21840,0x219d0,94192,0x26b0c,0x21cd0,0x21e60,94080,0x26b10,0x22160,0x222f0,94096,0x26b14,0x225f0,0x22780,94112,0x26b18,0x22a80,0x22c10,94128,0x26b1c,0x22f10,0x230a0,94560,0x26b20,0x1ead0,0x1ec38,94576,0x26b24,0x1ef60,0x1f0c8,94528,0x26b28,0x1f3f0,0x1f558,94544,0x26b2c,0x1f880,0x1f9e8,94496,0x26b30,0x1fd10,0x1fe78,94512,0x26b34,0x201a0,0x20308,94464,0x26b38,0x20630,0x20798,94480,0x26b3c,0x20ac0,0x20c28,94944,0x26b40,0x20f50,0x210b8,94960,0x26b44,0x213e0,0x21548,94912,0x26b48,0x21870,0x219d8,94928,0x26b4c,0x21d00,0x21e68,94880,0x26b50,0x22190,0x222f8,94896,0x26b54,0x22620,0x22788,94848,0x26b58,0x22ab0,0x22c18,94864,0x26b5c,0x22f40,0x230a8,0x3f803f80,0x3f803f80,16256,0,0,0x3f803f80,0x3f800000,0,0,0x3f803f80,0,16256,0,0x3f803f80,0,0x3f800000,0x3f803f80,0,0,0,0,0,0,0,0,0,0,0,0,0,0,0,0x3f803f80,0,0,0,0,0,0,0,0,0,0,0,0,0,0,0,0x3f803f80,0,0,0,0,0,0,0,0,0,0,0,0,0,0,0,0x3f803f80,0,0,0,0,0,0,0,0,0,0,0,0,0,0,0,0x3f803f80,0x3f803f80,16256,0,0,0x3f803f80,0x3f800000,0,0,0x3f803f80,0,16256,0,0x3f803f80,0,0x3f800000,0x3f803f80,0,0,0,0,0,0,0,0,0,0,0,0,0,0,0,0x3f803f80,0,0,0,0,0,0,0,0,0,0,0,0,0,0,0,0x3f803f80,0,0,0,0,0,0,0,0,0,0,0,0,0,0,0,0x3f803f80,0,0,0,0,0,0,0,0,0,0,0,0,0,0,0,0x3f803f80,0x3f803f80,16256,0,0,0x3f803f80,0x3f800000,0,0,0x3f803f80,0,16256,0,0x3f803f80,0,0x3f800000,0x3f803f80,0,0,0,0,0,0,0,0,0,0,0,0,0,0,0,0x3f803f80,0,0,0,0,0,0,0,0,0,0,0,0,0,0,0,0x3f803f80,0,0,0,0,0,0,0,0,0,0,0,0,0,0,0,0x3f803f80,0,0,0,0,0,0,0,0,0,0,0,0,0,0,0,0x3f803f80,0x3f803f80,16256,0,0,0x3f803f80,0x3f800000,0,0,0x3f803f80,0,16256,0,0x3f803f80,0,0x3f800000,0xffffffff,0xffffffff,0,0,0,0,0xffffffff,0xff000000,0xff000000,0xffffff,0xff000000,0xff000000,0,4096,16384,0x1c000,0xffffffff,0xffffffff,0,0,0,0,0xffffff,0xffff0000,0xffff0000,65535,0xffffff,0xffffff,1024,5120,16896,0x1c0b0,0xffffffff,0xffffffff,0,0,0,0,65535,0xffffff00,0xff000000,0xffffff,0xffff0000,0xffff0000,2048,10240,17408,0x1c160,0xffffffff,0,0,0,0,0,255,0xffffffff,0xffff0000,65535,65535,65535,3072,11264,17920,0x1c210,0,0x3fb8aa3b,0x2a8cbccc,0x38fba882,0x7fff7fff,0x415db3d7,0xffff0000,0,0,65535,0,0xffff0000,0,0,0,0,0,808,1800,2608,3600,4408,5400,6208,7200,8192,9000,9992,10800,11792,12600,13592,14353,15161,15922,16914,17675,17675,17675,17675,21,20,0,0,0,0,0,0,0x1b0000,0x5b8480,0xae0a80,0xee8f00,0x1411500,0x1819980,0x1d41f80,0x214a400,0x2672a00,0x2b9b000,0x2fa3480,0x34cba80,0x38d3f00,0x3dfc500,0x4204980,0x472cf80,0x4b16680,0x4f1eb00,0x5308200,0x5830800,0x5c19f00,0x5c19f00,0x5c19f00,0x5c19f00,0x5c19f00,0x5c19f00,0x5c19f00,0x5c19f00,0x5c19f00,0x5c19f00,0x5c19f00,0x5c19f00,1,809,1801,2609,3601,4409,5401,6209,7201,8193,9001,9993,10801,11793,12601,13593,14354,15162,15923,16915,17676,17676,17676,17676,21,20,0,0,0,0,0,0,0x1b0a80,0x5b8f00,0xae1500,0xee9980,0x1411f80,0x181a400,0x1d42a00,0x214ae80,0x2673480,0x2b9ba80,0x2fa3f00,0x34cc500,0x38d4980,0x3dfcf80,0x4205400,0x472da00,0x4b17100,0x4f1f580,0x5308c80,0x5831280,0x5c1a980,0x5c1a980,0x5c1a980,0x5c1a980,0x5c1a980,0x5c1a980,0x5c1a980,0x5c1a980,0x5c1a980,0x5c1a980,0x5c1a980,0x5c1a980,2,810,1802,2610,3602,4410,5402,6210,7202,8194,9002,9994,10802,11794,12602,18432,14355,18479,15924,5448,5448,5448,5448,5448,20,19,0,0,0,0,0,0,0x1b1500,0x5b9980,0xae1f80,0xeea400,0x1412a00,0x181ae80,0x1d43480,0x214b900,0x2673f00,0x2b9c500,0x2fa4980,0x34ccf80,0x38d5400,0x3dfda00,0x4205e80,0x60a80,0x4b17b80,0x7f800,0x5309700,0x1d31900,0x1d31900,0x1d31900,0x1d31900,0x1d31900,0x1d31900,0x1d31900,0x1d31900,0x1d31900,0x1d31900,0x1d31900,0x1d31900,0x1d31900,3,811,1803,2611,3603,4411,5403,6211,7203,8195,9003,9995,10803,11795,12603,13594,14356,15163,15925,16916,17677,17677,17677,17677,21,20,0,0,0,0,0,0,0x1b1f80,0x5ba400,0xae2a00,0xeeae80,0x1413480,0x181b900,0x1d43f00,0x214c380,0x2674980,0x2b9cf80,0x2fa5400,0x34cda00,0x38d5e80,0x3dfe480,0x4206900,0x472e480,0x4b18600,0x4f20000,0x530a180,0x5831d00,0x5c1b400,0x5c1b400,0x5c1b400,0x5c1b400,0x5c1b400,0x5c1b400,0x5c1b400,0x5c1b400,0x5c1b400,0x5c1b400,0x5c1b400,0x5c1b400,4,812,1804,2612,3604,4412,5404,6212,7204,8196,9004,9996,10804,11796,12604,13595,14357,15164,15926,16917,17480,17480,17480,17480,21,20,0,0,0,0,0,0,0x1b2a00,0x5bae80,0xae3480,0xeeb900,0x1413f00,0x181c380,0x1d44980,0x214ce00,0x2675400,0x2b9da00,0x2fa5e80,0x34ce480,0x38d6900,0x3dfef00,0x4207380,0x472ef00,0x4b19080,0x4f20a80,0x530ac00,0x5832780,0x5ac2b80,0x5ac2b80,0x5ac2b80,0x5ac2b80,0x5ac2b80,0x5ac2b80,0x5ac2b80,0x5ac2b80,0x5ac2b80,0x5ac2b80,0x5ac2b80,0x5ac2b80,5,813,1805,2613,3605,4413,5405,6213,7205,8197,9005,9997,10805,11797,12605,13596,14358,15165,15927,16918,17678,17678,17678,17678,21,20,0,0,0,0,0,0,0x1b3480,0x5bb900,0xae3f00,0xeec380,0x1414980,0x181ce00,0x1d45400,0x214d880,0x2675e80,0x2b9e480,0x2fa6900,0x34cef00,0x38d7380,0x3dff980,0x4207e00,0x472f980,0x4b19b00,0x4f21500,0x530b680,0x5833200,0x5c1be80,0x5c1be80,0x5c1be80,0x5c1be80,0x5c1be80,0x5c1be80,0x5c1be80,0x5c1be80,0x5c1be80,0x5c1be80,0x5c1be80,0x5c1be80,6,814,1806,2614,3606,4414,5406,6214,7206,8198,9006,9998,10806,11798,12606,18433,14359,18480,15928,5704,5704,5704,5704,5704,20,19,0,0,0,0,0,0,0x1b3f00,0x5bc380,0xae4980,0xeece00,0x1415400,0x181d880,0x1d45e80,0x214e300,0x2676900,0x2b9ef00,0x2fa7380,0x34cf980,0x38d7e00,0x3e00400,0x4208880,0x61500,0x4b1a580,0x80280,0x530c100,0x1e80e80,0x1e80e80,0x1e80e80,0x1e80e80,0x1e80e80,0x1e80e80,0x1e80e80,0x1e80e80,0x1e80e80,0x1e80e80,0x1e80e80,0x1e80e80,0x1e80e80,7,815,1807,2615,3607,4415,5407,6215,7207,8199,9007,9999,10807,11799,12607,13597,14360,15166,15929,16919,17679,17679,17679,17679,21,20,0,0,0,0,0,0,0x1b4980,0x5bce00,0xae5400,0xeed880,0x1415e80,0x181e300,0x1d46900,0x214ed80,0x2677380,0x2b9f980,0x2fa7e00,0x34d0400,0x38d8880,0x3e00e80,0x4209300,0x4730400,0x4b1b000,0x4f21f80,0x530cb80,0x5833c80,0x5c1c900,0x5c1c900,0x5c1c900,0x5c1c900,0x5c1c900,0x5c1c900,0x5c1c900,0x5c1c900,0x5c1c900,0x5c1c900,0x5c1c900,0x5c1c900,8,816,1808,2616,3608,4416,5408,6400,7208,8200,9008,10000,10808,11800,12608,13598,14361,15167,15930,16920,17680,17680,17680,17680,21,20,0,0,0,0,0,0,0x1b5400,0x5bd880,0xae5e80,0xeee300,0x1416900,0x181ed80,0x1d47380,0x226f980,0x2677e00,0x2ba0400,0x2fa8880,0x34d0e80,0x38d9300,0x3e01900,0x4209d80,0x4730e80,0x4b1ba80,0x4f22a00,0x530d600,0x5834700,0x5c1d380,0x5c1d380,0x5c1d380,0x5c1d380,0x5c1d380,0x5c1d380,0x5c1d380,0x5c1d380,0x5c1d380,0x5c1d380,0x5c1d380,0x5c1d380,9,817,1809,2617,3609,4417,5409,6401,7209,8201,9009,10001,10809,11801,12609,13599,14362,15168,15931,16921,17681,17681,17681,17681,21,20,0,0,0,0,0,0,0x1b5e80,0x5be300,0xae6900,0xeeed80,0x1417380,0x181f800,0x1d47e00,0x2270400,0x2678880,0x2ba0e80,0x2fa9300,0x34d1900,0x38d9d80,0x3e02380,0x420a800,0x4731900,0x4b1c500,0x4f23480,0x530e080,0x5835180,0x5c1de00,0x5c1de00,0x5c1de00,0x5c1de00,0x5c1de00,0x5c1de00,0x5c1de00,0x5c1de00,0x5c1de00,0x5c1de00,0x5c1de00,0x5c1de00,10,818,1810,2618,3610,4418,5410,6402,7210,8202,9010,10002,10810,11802,12610,18434,14363,18481,15932,5960,5960,5960,5960,5960,20,19,0,0,0,0,0,0,0x1b6900,0x5bed80,0xae7380,0xeef800,0x1417e00,0x1820280,0x1d48880,0x2270e80,0x2679300,0x2ba1900,0x2fa9d80,0x34d2380,0x38da800,0x3e02e00,0x420b280,0x61f80,0x4b1cf80,0x80d00,0x530eb00,0x1fd0400,0x1fd0400,0x1fd0400,0x1fd0400,0x1fd0400,0x1fd0400,0x1fd0400,0x1fd0400,0x1fd0400,0x1fd0400,0x1fd0400,0x1fd0400,0x1fd0400,11,819,1811,2619,3611,4419,5411,6403,7211,8203,9011,10003,10811,11803,12611,13600,14364,15169,15933,16922,17682,17682,17682,17682,21,20,0,0,0,0,0,0,0x1b7380,0x5bf800,0xae7e00,0xef0280,0x1418880,0x1820d00,0x1d49300,0x2271900,0x2679d80,0x2ba2380,0x2faa800,0x34d2e00,0x38db280,0x3e03880,0x420bd00,0x4732380,0x4b1da00,0x4f23f00,0x530f580,0x5835c00,0x5c1e880,0x5c1e880,0x5c1e880,0x5c1e880,0x5c1e880,0x5c1e880,0x5c1e880,0x5c1e880,0x5c1e880,0x5c1e880,0x5c1e880,0x5c1e880,12,820,1812,2620,3612,4420,5412,6404,7212,8204,9012,10004,10812,11804,12612,13601,14365,15170,15934,16923,17736,17736,17736,17736,21,20,0,0,0,0,0,0,0x1b7e00,0x5c0280,0xae8880,0xef0d00,0x1419300,0x1821780,0x1d49d80,0x2272380,0x267a800,0x2ba2e00,0x2fab280,0x34d3880,0x38dbd00,0x3e04300,0x420c780,0x4732e00,0x4b1e480,0x4f24980,0x5310000,0x5836680,0x5c12100,0x5c12100,0x5c12100,0x5c12100,0x5c12100,0x5c12100,0x5c12100,0x5c12100,0x5c12100,0x5c12100,0x5c12100,0x5c12100,13,821,1813,2621,3613,4421,5413,6405,7213,8205,9013,10005,10813,11805,12613,13602,14366,15171,15935,16924,17683,17683,17683,17683,21,20,0,0,0,0,0,0,0x1b8880,0x5c0d00,0xae9300,0xef1780,0x1419d80,0x1822200,0x1d4a800,0x2272e00,0x267b280,0x2ba3880,0x2fabd00,0x34d4300,0x38dc780,0x3e04d80,0x420d200,0x4733880,0x4b1ef00,0x4f25400,0x5310a80,0x5837100,0x5c1f300,0x5c1f300,0x5c1f300,0x5c1f300,0x5c1f300,0x5c1f300,0x5c1f300,0x5c1f300,0x5c1f300,0x5c1f300,0x5c1f300,0x5c1f300,14,822,1814,2622,3614,4422,5414,6406,7214,8206,9014,10006,10814,11806,12614,18435,14367,18482,15936,6216,6216,6216,6216,6216,20,19,0,0,0,0,0,0,0x1b9300,0x5c1780,0xae9d80,0xef2200,0x141a800,0x1822c80,0x1d4b280,0x2273880,0x267bd00,0x2ba4300,0x2fac780,0x34d4d80,0x38dd200,0x3e05800,0x420dc80,0x62a00,0x4b1f980,0x81780,0x5311500,0x211f980,0x211f980,0x211f980,0x211f980,0x211f980,0x211f980,0x211f980,0x211f980,0x211f980,0x211f980,0x211f980,0x211f980,0x211f980,15,823,1815,2623,3615,4423,5415,6407,7215,8207,9015,10007,10815,11807,12615,13603,14368,15172,15937,16925,17684,17684,17684,17684,21,20,0,0,0,0,0,0,0x1b9d80,0x5c2200,0xaea800,0xef2c80,0x141b280,0x1823700,0x1d4bd00,0x2274300,0x267c780,0x2ba4d80,0x2fad200,0x34d5800,0x38ddc80,0x3e06280,0x420e700,0x4734300,0x4b20400,0x4f25e80,0x5311f80,0x5837b80,0x5c1fd80,0x5c1fd80,0x5c1fd80,0x5c1fd80,0x5c1fd80,0x5c1fd80,0x5c1fd80,0x5c1fd80,0x5c1fd80,0x5c1fd80,0x5c1fd80,0x5c1fd80,16,824,1816,2624,3616,4608,5416,6408,7216,8208,9016,10008,10816,11808,12800,13604,14369,15173,15938,16926,17685,17685,17685,17685,21,20,0,0,0,0,0,0,0x1ba800,0x5c2c80,0xaeb280,0xef3700,0x141bd00,0x1944300,0x1d4c780,0x2274d80,0x267d200,0x2ba5800,0x2fadc80,0x34d6280,0x38de700,0x3e06d00,0x432f300,0x4734d80,0x4b20e80,0x4f26900,0x5312a00,0x5838600,0x5c20800,0x5c20800,0x5c20800,0x5c20800,0x5c20800,0x5c20800,0x5c20800,0x5c20800,0x5c20800,0x5c20800,0x5c20800,0x5c20800,17,825,1817,2625,3617,4609,5417,6409,7217,8209,9017,10009,10817,11809,12801,13605,14370,15174,15939,16927,17686,17686,17686,17686,21,20,0,0,0,0,0,0,0x1bb280,0x5c3700,0xaebd00,0xef4180,0x141c780,0x1944d80,0x1d4d200,0x2275800,0x267dc80,0x2ba6280,0x2fae700,0x34d6d00,0x38df180,0x3e07780,0x432fd80,0x4735800,0x4b21900,0x4f27380,0x5313480,0x5839080,0x5c21280,0x5c21280,0x5c21280,0x5c21280,0x5c21280,0x5c21280,0x5c21280,0x5c21280,0x5c21280,0x5c21280,0x5c21280,0x5c21280,18,826,1818,2626,3618,4610,5418,6410,7218,8210,9018,10010,10818,11810,12802,18436,14371,18483,15940,6472,6472,6472,6472,6472,20,19,0,0,0,0,0,0,0x1bbd00,0x5c4180,0xaec780,0xef4c00,0x141d200,0x1945800,0x1d4dc80,0x2276280,0x267e700,0x2ba6d00,0x2faf180,0x34d7780,0x38dfc00,0x3e08200,0x4330800,0x63480,0x4b22380,0x82200,0x5313f00,0x226ef00,0x226ef00,0x226ef00,0x226ef00,0x226ef00,0x226ef00,0x226ef00,0x226ef00,0x226ef00,0x226ef00,0x226ef00,0x226ef00,0x226ef00,19,827,1819,2627,3619,4611,5419,6411,7219,8211,9019,10011,10819,11811,12803,13606,14372,15175,15941,16928,17687,17687,17687,17687,21,20,0,0,0,0,0,0,0x1bc780,0x5c4c00,0xaed200,0xef5680,0x141dc80,0x1946280,0x1d4e700,0x2276d00,0x267f180,0x2ba7780,0x2fafc00,0x34d8200,0x38e0680,0x3e08c80,0x4331280,0x4736280,0x4b22e00,0x4f27e00,0x5314980,0x5839b00,0x5c21d00,0x5c21d00,0x5c21d00,0x5c21d00,0x5c21d00,0x5c21d00,0x5c21d00,0x5c21d00,0x5c21d00,0x5c21d00,0x5c21d00,0x5c21d00,20,828,1820,2628,3620,4612,5420,6412,7220,8212,9020,10012,10820,11812,12804,13607,14373,15360,15942,16929,17992,17992,17992,17992,21,20,0,0,0,0,0,0,0x1bd200,0x5c5680,0xaedc80,0xef6100,0x141e700,0x1946d00,0x1d4f180,0x2277780,0x267fc00,0x2ba8200,0x2fb0680,0x34d8c80,0x38e1100,0x3e09700,0x4331d00,0x4736d00,0x4b23880,0x5048a00,0x5315400,0x583a580,0x5d61680,0x5d61680,0x5d61680,0x5d61680,0x5d61680,0x5d61680,0x5d61680,0x5d61680,0x5d61680,0x5d61680,0x5d61680,0x5d61680,21,829,1821,2629,3621,4613,5421,6413,7221,8213,9021,10013,10821,11813,12805,13608,14374,15361,15943,16930,17688,17688,17688,17688,21,20,0,0,0,0,0,0,0x1bdc80,0x5c6100,0xaee700,0xef6b80,0x141f180,0x1947780,0x1d4fc00,0x2278200,0x2680680,0x2ba8c80,0x2fb1100,0x34d9700,0x38e1b80,0x3e0a180,0x4332780,0x4737780,0x4b24300,0x5049480,0x5315e80,0x583b000,0x5c22780,0x5c22780,0x5c22780,0x5c22780,0x5c22780,0x5c22780,0x5c22780,0x5c22780,0x5c22780,0x5c22780,0x5c22780,0x5c22780,22,830,1822,2630,3622,4614,5422,6414,7222,8214,9022,10014,10822,11814,12806,18437,14375,18484,16128,6728,6728,6728,6728,6728,20,19,0,0,0,0,0,0,0x1be700,0x5c6b80,0xaef180,0xef7600,0x141fc00,0x1948200,0x1d50680,0x2278c80,0x2681100,0x2ba9700,0x2fb1b80,0x34da180,0x38e2600,0x3e0ac00,0x4333200,0x63f00,0x4b24d80,0x82c80,0x5436a80,0x23be480,0x23be480,0x23be480,0x23be480,0x23be480,0x23be480,0x23be480,0x23be480,0x23be480,0x23be480,0x23be480,0x23be480,0x23be480,23,831,1823,2631,3623,4615,5423,6415,7223,8215,9023,10015,10823,11815,12807,13609,14376,15362,16129,16931,17689,17689,17689,17689,21,20,0,0,0,0,0,0,0x1bf180,0x5c7600,0xaefc00,0xef8080,0x1420680,0x1948c80,0x1d51100,0x2279700,0x2681b80,0x2baa180,0x2fb2600,0x34dac00,0x38e3080,0x3e0b680,0x4333c80,0x4738200,0x4b25800,0x5049f00,0x5437500,0x583ba80,0x5c23200,0x5c23200,0x5c23200,0x5c23200,0x5c23200,0x5c23200,0x5c23200,0x5c23200,0x5c23200,0x5c23200,0x5c23200,0x5c23200,24,832,1824,2816,3624,4616,5424,6416,7224,8216,9024,10016,11008,11816,12808,13610,14377,15363,16130,16932,17690,17690,17690,17690,21,20,0,0,0,0,0,0,0x1bfc00,0x5c8080,0xaf0680,0x1018c80,0x1421100,0x1949700,0x1d51b80,0x227a180,0x2682600,0x2baac00,0x2fb3080,0x34db680,0x3a03c80,0x3e0c100,0x4334700,0x4738c80,0x4b26280,0x504a980,0x5437f80,0x583c500,0x5c23c80,0x5c23c80,0x5c23c80,0x5c23c80,0x5c23c80,0x5c23c80,0x5c23c80,0x5c23c80,0x5c23c80,0x5c23c80,0x5c23c80,0x5c23c80,25,833,1825,2817,3625,4617,5425,6417,7225,8217,9025,10017,11009,11817,12809,13611,14378,15364,16131,16933,17691,17691,17691,17691,21,20,0,0,0,0,0,0,0x1c0680,0x5c8b00,0xaf1100,0x1019700,0x1421b80,0x194a180,0x1d52600,0x227ac00,0x2683080,0x2bab680,0x2fb3b00,0x34dc100,0x3a04700,0x3e0cb80,0x4335180,0x4739700,0x4b26d00,0x504b400,0x5438a00,0x583cf80,0x5c24700,0x5c24700,0x5c24700,0x5c24700,0x5c24700,0x5c24700,0x5c24700,0x5c24700,0x5c24700,0x5c24700,0x5c24700,0x5c24700,26,834,1826,2818,3626,4618,5426,6418,7226,8218,9026,10018,11010,11818,12810,18438,14379,18485,16132,6984,6984,6984,6984,6984,20,19,0,0,0,0,0,0,0x1c1100,0x5c9580,0xaf1b80,0x101a180,0x1422600,0x194ac00,0x1d53080,0x227b680,0x2683b00,0x2bac100,0x2fb4580,0x34dcb80,0x3a05180,0x3e0d600,0x4335c00,0x64980,0x4b27780,0x83700,0x5439480,0x250da00,0x250da00,0x250da00,0x250da00,0x250da00,0x250da00,0x250da00,0x250da00,0x250da00,0x250da00,0x250da00,0x250da00,0x250da00,27,835,1827,2819,3627,4619,5427,6419,7227,8219,9027,10019,11011,11819,12811,13612,14380,15365,16133,16934,17692,17692,17692,17692,21,20,0,0,0,0,0,0,0x1c1b80,0x5ca000,0xaf2600,0x101ac00,0x1423080,0x194b680,0x1d53b00,0x227c100,0x2684580,0x2bacb80,0x2fb5000,0x34dd600,0x3a05c00,0x3e0e080,0x4336680,0x473a180,0x4b28200,0x504be80,0x5439f00,0x583da00,0x5c25180,0x5c25180,0x5c25180,0x5c25180,0x5c25180,0x5c25180,0x5c25180,0x5c25180,0x5c25180,0x5c25180,0x5c25180,0x5c25180,28,836,1828,2820,3628,4620,5428,6420,7228,8220,9028,10020,11012,11820,12812,13613,14381,15366,16134,16935,18248,18248,18248,18248,21,20,0,0,0,0,0,0,0x1c2600,0x5caa80,0xaf3080,0x101b680,0x1423b00,0x194c100,0x1d54580,0x227cb80,0x2685000,0x2bad600,0x2fb5a80,0x34de080,0x3a06680,0x3e0eb00,0x4337100,0x473ac00,0x4b28c80,0x504c900,0x543a980,0x583e480,0x5eb0c00,0x5eb0c00,0x5eb0c00,0x5eb0c00,0x5eb0c00,0x5eb0c00,0x5eb0c00,0x5eb0c00,0x5eb0c00,0x5eb0c00,0x5eb0c00,0x5eb0c00,29,837,1829,2821,3629,4621,5429,6421,7229,8221,9029,10021,11013,11821,12813,13614,14382,15367,16135,16936,17693,17693,17693,17693,21,20,0,0,0,0,0,0,0x1c3080,0x5cb500,0xaf3b00,0x101c100,0x1424580,0x194cb80,0x1d55000,0x227d600,0x2685a80,0x2bae080,0x2fb6500,0x34deb00,0x3a07100,0x3e0f580,0x4337b80,0x473b680,0x4b29700,0x504d380,0x543b400,0x583ef00,0x5c25c00,0x5c25c00,0x5c25c00,0x5c25c00,0x5c25c00,0x5c25c00,0x5c25c00,0x5c25c00,0x5c25c00,0x5c25c00,0x5c25c00,0x5c25c00,30,838,1830,2822,3630,4622,5430,6422,7230,8222,9030,10022,11014,11822,12814,18439,14383,18486,16136,7240,7240,7240,7240,7240,20,19,0,0,0,0,0,0,0x1c3b00,0x5cbf80,0xaf4580,0x101cb80,0x1425000,0x194d600,0x1d55a80,0x227e080,0x2686500,0x2baeb00,0x2fb6f80,0x34df580,0x3a07b80,0x3e10000,0x4338600,0x65400,0x4b2a180,0x84180,0x543be80,0x265cf80,0x265cf80,0x265cf80,0x265cf80,0x265cf80,0x265cf80,0x265cf80,0x265cf80,0x265cf80,0x265cf80,0x265cf80,0x265cf80,0x265cf80,31,839,1831,2823,3631,4623,5431,6423,7231,8223,9031,10023,11015,11823,12815,13615,14384,15368,16137,16937,17694,17694,17694,17694,21,20,0,0,0,0,0,0,0x1c4580,0x5cca00,0xaf5000,0x101d600,0x1425a80,0x194e080,0x1d56500,0x227eb00,0x2686f80,0x2baf580,0x2fb7a00,0x34e0000,0x3a08600,0x3e10a80,0x4339080,0x473c100,0x4b2ac00,0x504de00,0x543c900,0x583f980,0x5c26680,0x5c26680,0x5c26680,0x5c26680,0x5c26680,0x5c26680,0x5c26680,0x5c26680,0x5c26680,0x5c26680,0x5c26680,0x5c26680,32,1024,1832,2824,3632,4624,5432,6424,7232,8224,9216,10024,11016,11824,12816,13616,14385,15369,16138,16938,17695,17695,17695,17695,21,20,0,0,0,0,0,0,0x1c5000,0x6ed600,0xaf5a80,0x101e080,0x1426500,0x194eb00,0x1d56f80,0x227f580,0x2687a00,0x2bb0000,0x30d8600,0x34e0a80,0x3a09080,0x3e11500,0x4339b00,0x473cb80,0x4b2b680,0x504e880,0x543d380,0x5840400,0x5c27100,0x5c27100,0x5c27100,0x5c27100,0x5c27100,0x5c27100,0x5c27100,0x5c27100,0x5c27100,0x5c27100,0x5c27100,0x5c27100,33,1025,1833,2825,3633,4625,5433,6425,7233,8225,9217,10025,11017,11825,12817,13617,14386,15370,16139,16939,17696,17696,17696,17696,21,20,0,0,0,0,0,0,0x1c5a80,0x6ee080,0xaf6500,0x101eb00,0x1426f80,0x194f580,0x1d57a00,0x2280000,0x2688480,0x2bb0a80,0x30d9080,0x34e1500,0x3a09b00,0x3e11f80,0x433a580,0x473d600,0x4b2c100,0x504f300,0x543de00,0x5840e80,0x5c27b80,0x5c27b80,0x5c27b80,0x5c27b80,0x5c27b80,0x5c27b80,0x5c27b80,0x5c27b80,0x5c27b80,0x5c27b80,0x5c27b80,0x5c27b80,34,1026,1834,2826,3634,4626,5434,6426,7234,8226,9218,10026,11018,11826,12818,18440,14387,18487,16140,7496,7496,7496,7496,7496,20,19,0,0,0,0,0,0,0x1c6500,0x6eeb00,0xaf6f80,0x101f580,0x1427a00,0x1950000,0x1d58480,0x2280a80,0x2688f00,0x2bb1500,0x30d9b00,0x34e1f80,0x3a0a580,0x3e12a00,0x433b000,0x65e80,0x4b2cb80,0x84c00,0x543e880,0x27ac500,0x27ac500,0x27ac500,0x27ac500,0x27ac500,0x27ac500,0x27ac500,0x27ac500,0x27ac500,0x27ac500,0x27ac500,0x27ac500,0x27ac500,35,1027,1835,2827,3635,4627,5435,6427,7235,8227,9219,10027,11019,11827,12819,13618,14388,15371,16141,16940,17697,17697,17697,17697,21,20,0,0,0,0,0,0,0x1c6f80,0x6ef580,0xaf7a00,0x1020000,0x1428480,0x1950a80,0x1d58f00,0x2281500,0x2689980,0x2bb1f80,0x30da580,0x34e2a00,0x3a0b000,0x3e13480,0x433ba80,0x473e080,0x4b2d600,0x504fd80,0x543f300,0x5841900,0x5c28600,0x5c28600,0x5c28600,0x5c28600,0x5c28600,0x5c28600,0x5c28600,0x5c28600,0x5c28600,0x5c28600,0x5c28600,0x5c28600,36,1028,1836,2828,3636,4628,5436,6428,7236,8228,9220,10028,11020,11828,12820,13619,14389,15372,16142,16941,17698,17698,17698,17698,21,20,0,0,0,0,0,0,0x1c7a00,0x6f0000,0xaf8480,0x1020a80,0x1428f00,0x1951500,0x1d59980,0x2281f80,0x268a400,0x2bb2a00,0x30db000,0x34e3480,0x3a0ba80,0x3e13f00,0x433c500,0x473eb00,0x4b2e080,0x5050800,0x543fd80,0x5842380,0x5c29080,0x5c29080,0x5c29080,0x5c29080,0x5c29080,0x5c29080,0x5c29080,0x5c29080,0x5c29080,0x5c29080,0x5c29080,0x5c29080,37,1029,1837,2829,3637,4629,5437,6429,7237,8229,9221,10029,11021,11829,12821,13620,14390,15373,16143,16942,17699,17699,17699,17699,21,20,0,0,0,0,0,0,0x1c8480,0x6f0a80,0xaf8f00,0x1021500,0x1429980,0x1951f80,0x1d5a400,0x2282a00,0x268ae80,0x2bb3480,0x30dba80,0x34e3f00,0x3a0c500,0x3e14980,0x433cf80,0x473f580,0x4b2eb00,0x5051280,0x5440800,0x5842e00,0x5c29b00,0x5c29b00,0x5c29b00,0x5c29b00,0x5c29b00,0x5c29b00,0x5c29b00,0x5c29b00,0x5c29b00,0x5c29b00,0x5c29b00,0x5c29b00,38,1030,1838,2830,3638,4630,5438,6430,7238,8230,9222,10030,11022,11830,12822,18441,14391,18488,16144,7752,7752,7752,7752,7752,20,19,0,0,0,0,0,0,0x1c8f00,0x6f1500,0xaf9980,0x1021f80,0x142a400,0x1952a00,0x1d5ae80,0x2283480,0x268b900,0x2bb3f00,0x30dc500,0x34e4980,0x3a0cf80,0x3e15400,0x433da00,0x66900,0x4b2f580,0x85680,0x5441280,0x28fba80,0x28fba80,0x28fba80,0x28fba80,0x28fba80,0x28fba80,0x28fba80,0x28fba80,0x28fba80,0x28fba80,0x28fba80,0x28fba80,0x28fba80,39,1031,1839,2831,3639,4631,5439,6431,7239,8231,9223,10031,11023,11831,12823,13621,14392,15374,16145,16943,17700,17700,17700,17700,21,20,0,0,0,0,0,0,0x1c9980,0x6f1f80,0xafa400,0x1022a00,0x142ae80,0x1953480,0x1d5b900,0x2283f00,0x268c380,0x2bb4980,0x30dcf80,0x34e5400,0x3a0da00,0x3e15e80,0x433e480,0x4740000,0x4b30000,0x5051d00,0x5441d00,0x5843880,0x5c2a580,0x5c2a580,0x5c2a580,0x5c2a580,0x5c2a580,0x5c2a580,0x5c2a580,0x5c2a580,0x5c2a580,0x5c2a580,0x5c2a580,0x5c2a580,40,1032,1840,2832,3640,4632,5440,6432,7424,8232,9224,10032,11024,11832,12824,13622,14393,15375,16146,16944,17701,17701,17701,17701,21,20,0,0,0,0,0,0,0x1ca400,0x6f2a00,0xafae80,0x1023480,0x142b900,0x1953f00,0x1d5c380,0x2284980,0x27acf80,0x2bb5400,0x30dda00,0x34e5e80,0x3a0e480,0x3e16900,0x433ef00,0x4740a80,0x4b30a80,0x5052780,0x5442780,0x5844300,0x5c2b000,0x5c2b000,0x5c2b000,0x5c2b000,0x5c2b000,0x5c2b000,0x5c2b000,0x5c2b000,0x5c2b000,0x5c2b000,0x5c2b000,0x5c2b000,41,1033,1841,2833,3641,4633,5441,6433,7425,8233,9225,10033,11025,11833,12825,13623,14394,15376,16147,16945,17702,17702,17702,17702,21,20,0,0,0,0,0,0,0x1cae80,0x6f3480,0xafb900,0x1023f00,0x142c380,0x1954980,0x1d5ce00,0x2285400,0x27ada00,0x2bb5e80,0x30de480,0x34e6900,0x3a0ef00,0x3e17380,0x433f980,0x4741500,0x4b31500,0x5053200,0x5443200,0x5844d80,0x5c2ba80,0x5c2ba80,0x5c2ba80,0x5c2ba80,0x5c2ba80,0x5c2ba80,0x5c2ba80,0x5c2ba80,0x5c2ba80,0x5c2ba80,0x5c2ba80,0x5c2ba80,42,1034,1842,2834,3642,4634,5442,6434,7426,8234,9226,10034,11026,11834,12826,18442,14395,18489,16148,8008,8008,8008,8008,8008,20,19,0,0,0,0,0,0,0x1cb900,0x6f3f00,0xafc380,0x1024980,0x142ce00,0x1955400,0x1d5d880,0x2285e80,0x27ae480,0x2bb6900,0x30def00,0x34e7380,0x3a0f980,0x3e17e00,0x4340400,0x67380,0x4b31f80,0x86100,0x5443c80,0x2a4b000,0x2a4b000,0x2a4b000,0x2a4b000,0x2a4b000,0x2a4b000,0x2a4b000,0x2a4b000,0x2a4b000,0x2a4b000,0x2a4b000,0x2a4b000,0x2a4b000,43,1035,1843,2835,3643,4635,5443,6435,7427,8235,9227,10035,11027,11835,12827,13624,14396,15377,16149,16946,17703,17703,17703,17703,21,20,0,0,0,0,0,0,0x1cc380,0x6f4980,0xafce00,0x1025400,0x142d880,0x1955e80,0x1d5e300,0x2286900,0x27aef00,0x2bb7380,0x30df980,0x34e7e00,0x3a10400,0x3e18880,0x4340e80,0x4741f80,0x4b32a00,0x5053c80,0x5444700,0x5845800,0x5c2c500,0x5c2c500,0x5c2c500,0x5c2c500,0x5c2c500,0x5c2c500,0x5c2c500,0x5c2c500,0x5c2c500,0x5c2c500,0x5c2c500,0x5c2c500,44,1036,1844,2836,3644,4636,5444,6436,7428,8236,9228,10036,11028,11836,12828,13625,14397,15378,16150,16947,17704,17704,17704,17704,21,20,0,0,0,0,0,0,0x1cce00,0x6f5400,0xafd880,0x1025e80,0x142e300,0x1956900,0x1d5ed80,0x2287380,0x27af980,0x2bb7e00,0x30e0400,0x34e8880,0x3a10e80,0x3e19300,0x4341900,0x4742a00,0x4b33480,0x5054700,0x5445180,0x5846280,0x5c2cf80,0x5c2cf80,0x5c2cf80,0x5c2cf80,0x5c2cf80,0x5c2cf80,0x5c2cf80,0x5c2cf80,0x5c2cf80,0x5c2cf80,0x5c2cf80,0x5c2cf80,45,1037,1845,2837,3645,4637,5445,6437,7429,8237,9229,10037,11029,11837,12829,13626,14398,15379,16151,16948,17705,17705,17705,17705,21,20,0,0,0,0,0,0,0x1cd880,0x6f5e80,0xafe300,0x1026900,0x142ed80,0x1957380,0x1d5f800,0x2287e00,0x27b0400,0x2bb8880,0x30e0e80,0x34e9300,0x3a11900,0x3e19d80,0x4342380,0x4743480,0x4b33f00,0x5055180,0x5445c00,0x5846d00,0x5c2da00,0x5c2da00,0x5c2da00,0x5c2da00,0x5c2da00,0x5c2da00,0x5c2da00,0x5c2da00,0x5c2da00,0x5c2da00,0x5c2da00,0x5c2da00,46,1038,1846,2838,3646,4638,5446,6438,7430,8238,9230,10038,11030,11838,12830,18443,14399,18490,16152,8264,8264,8264,8264,8264,20,19,0,0,0,0,0,0,0x1ce300,0x6f6900,0xafed80,0x1027380,0x142f800,0x1957e00,0x1d60280,0x2288880,0x27b0e80,0x2bb9300,0x30e1900,0x34e9d80,0x3a12380,0x3e1a800,0x4342e00,0x67e00,0x4b34980,0x86b80,0x5446680,0x2b9a580,0x2b9a580,0x2b9a580,0x2b9a580,0x2b9a580,0x2b9a580,0x2b9a580,0x2b9a580,0x2b9a580,0x2b9a580,0x2b9a580,0x2b9a580,0x2b9a580,47,1039,1847,2839,3647,4639,5447,6439,7431,8239,9231,10039,11031,11839,12831,13627,14400,15380,16153,16949,17706,17706,17706,17706,21,20,0,0,0,0,0,0,0x1ced80,0x6f7380,0xaff800,0x1027e00,0x1430280,0x1958880,0x1d60d00,0x2289300,0x27b1900,0x2bb9d80,0x30e2380,0x34ea800,0x3a12e00,0x3e1b280,0x4343880,0x4743f00,0x4b35400,0x5055c00,0x5447100,0x5847780,0x5c2e480,0x5c2e480,0x5c2e480,0x5c2e480,0x5c2e480,0x5c2e480,0x5c2e480,0x5c2e480,0x5c2e480,0x5c2e480,0x5c2e480,0x5c2e480,48,1040,1848,2840,3648,4640,5632,6440,7432,8240,9232,10040,11032,11840,12832,13628,14401,15381,16154,16950,17707,17707,17707,17707,21,20,0,0,0,0,0,0,0x1cf800,0x6f7e00,0xb00280,0x1028880,0x1430d00,0x1959300,0x1e81900,0x2289d80,0x27b2380,0x2bba800,0x30e2e00,0x34eb280,0x3a13880,0x3e1bd00,0x4344300,0x4744980,0x4b35e80,0x5056680,0x5447b80,0x5848200,0x5c2ef00,0x5c2ef00,0x5c2ef00,0x5c2ef00,0x5c2ef00,0x5c2ef00,0x5c2ef00,0x5c2ef00,0x5c2ef00,0x5c2ef00,0x5c2ef00,0x5c2ef00,49,1041,1849,2841,3649,4641,5633,6441,7433,8241,9233,10041,11033,11841,12833,13629,14402,15382,16155,16951,17708,17708,17708,17708,21,20,0,0,0,0,0,0,0x1d0280,0x6f8880,0xb00d00,0x1029300,0x1431780,0x1959d80,0x1e82380,0x228a800,0x27b2e00,0x2bbb280,0x30e3880,0x34ebd00,0x3a14300,0x3e1c780,0x4344d80,0x4745400,0x4b36900,0x5057100,0x5448600,0x5848c80,0x5c2f980,0x5c2f980,0x5c2f980,0x5c2f980,0x5c2f980,0x5c2f980,0x5c2f980,0x5c2f980,0x5c2f980,0x5c2f980,0x5c2f980,0x5c2f980,50,1042,1850,2842,3650,4642,5634,6442,7434,8242,9234,10042,11034,11842,12834,18444,14403,18491,16156,8520,8520,8520,8520,8520,20,19,0,0,0,0,0,0,0x1d0d00,0x6f9300,0xb01780,0x1029d80,0x1432200,0x195a800,0x1e82e00,0x228b280,0x27b3880,0x2bbbd00,0x30e4300,0x34ec780,0x3a14d80,0x3e1d200,0x4345800,0x68880,0x4b37380,0x87600,0x5449080,0x2ce9b00,0x2ce9b00,0x2ce9b00,0x2ce9b00,0x2ce9b00,0x2ce9b00,0x2ce9b00,0x2ce9b00,0x2ce9b00,0x2ce9b00,0x2ce9b00,0x2ce9b00,0x2ce9b00,51,1043,1851,2843,3651,4643,5635,6443,7435,8243,9235,10043,11035,11843,12835,13630,14404,15383,16157,16952,17709,17709,17709,17709,21,20,0,0,0,0,0,0,0x1d1780,0x6f9d80,0xb02200,0x102a800,0x1432c80,0x195b280,0x1e83880,0x228bd00,0x27b4300,0x2bbc780,0x30e4d80,0x34ed200,0x3a15800,0x3e1dc80,0x4346280,0x4745e80,0x4b37e00,0x5057b80,0x5449b00,0x5849700,0x5c30400,0x5c30400,0x5c30400,0x5c30400,0x5c30400,0x5c30400,0x5c30400,0x5c30400,0x5c30400,0x5c30400,0x5c30400,0x5c30400,52,1044,1852,2844,3652,4644,5636,6444,7436,8244,9236,10044,11036,11844,12836,13631,14405,15384,16158,16953,17710,17710,17710,17710,21,20,0,0,0,0,0,0,0x1d2200,0x6fa800,0xb02c80,0x102b280,0x1433700,0x195bd00,0x1e84300,0x228c780,0x27b4d80,0x2bbd200,0x30e5800,0x34edc80,0x3a16280,0x3e1e700,0x4346d00,0x4746900,0x4b38880,0x5058600,0x544a580,0x584a180,0x5c30e80,0x5c30e80,0x5c30e80,0x5c30e80,0x5c30e80,0x5c30e80,0x5c30e80,0x5c30e80,0x5c30e80,0x5c30e80,0x5c30e80,0x5c30e80,53,1045,1853,2845,3653,4645,5637,6445,7437,8245,9237,10045,11037,11845,12837,13632,14406,15385,16159,16954,17711,17711,17711,17711,21,20,0,0,0,0,0,0,0x1d2c80,0x6fb280,0xb03700,0x102bd00,0x1434180,0x195c780,0x1e84d80,0x228d200,0x27b5800,0x2bbdc80,0x30e6280,0x34ee700,0x3a16d00,0x3e1f180,0x4347780,0x4747380,0x4b39300,0x5059080,0x544b000,0x584ac00,0x5c31900,0x5c31900,0x5c31900,0x5c31900,0x5c31900,0x5c31900,0x5c31900,0x5c31900,0x5c31900,0x5c31900,0x5c31900,0x5c31900,54,1046,1854,2846,3654,4646,5638,6446,7438,8246,9238,10046,11038,11846,12838,18445,14407,18492,16160,8776,8776,8776,8776,8776,20,19,0,0,0,0,0,0,0x1d3700,0x6fbd00,0xb04180,0x102c780,0x1434c00,0x195d200,0x1e85800,0x228dc80,0x27b6280,0x2bbe700,0x30e6d00,0x34ef180,0x3a17780,0x3e1fc00,0x4348200,0x69300,0x4b39d80,0x88080,0x544ba80,0x2e39080,0x2e39080,0x2e39080,0x2e39080,0x2e39080,0x2e39080,0x2e39080,0x2e39080,0x2e39080,0x2e39080,0x2e39080,0x2e39080,0x2e39080,55,1047,1855,2847,3655,4647,5639,6447,7439,8247,9239,10047,11039,11847,12839,13633,14592,15386,16161,16955,17712,17712,17712,17712,21,20,0,0,0,0,0,0,0x1d4180,0x6fc780,0xb04c00,0x102d200,0x1435680,0x195dc80,0x1e86280,0x228e700,0x27b6d00,0x2bbf180,0x30e7780,0x34efc00,0x3a18200,0x3e20680,0x4348c80,0x4747e00,0x4c5a980,0x5059b00,0x544c500,0x584b680,0x5c32380,0x5c32380,0x5c32380,0x5c32380,0x5c32380,0x5c32380,0x5c32380,0x5c32380,0x5c32380,0x5c32380,0x5c32380,0x5c32380,56,1048,1856,2848,3840,4648,5640,6448,7440,8248,9240,10048,11040,12032,12840,13634,14593,15387,16162,16956,17713,17713,17713,17713,21,20,0,0,0,0,0,0,0x1d4c00,0x6fd200,0xb05680,0x102dc80,0x1556280,0x195e700,0x1e86d00,0x228f180,0x27b7780,0x2bbfc00,0x30e8200,0x34f0680,0x3a18c80,0x3f41280,0x4349700,0x4748880,0x4c5b400,0x505a580,0x544cf80,0x584c100,0x5c32e00,0x5c32e00,0x5c32e00,0x5c32e00,0x5c32e00,0x5c32e00,0x5c32e00,0x5c32e00,0x5c32e00,0x5c32e00,0x5c32e00,0x5c32e00,57,1049,1857,2849,3841,4649,5641,6449,7441,8249,9241,10049,11041,12033,12841,13635,14594,15388,16163,16957,17714,17714,17714,17714,21,20,0,0,0,0,0,0,0x1d5680,0x6fdc80,0xb06100,0x102e700,0x1556d00,0x195f180,0x1e87780,0x228fc00,0x27b8200,0x2bc0680,0x30e8c80,0x34f1100,0x3a19700,0x3f41d00,0x434a180,0x4749300,0x4c5be80,0x505b000,0x544da00,0x584cb80,0x5c33880,0x5c33880,0x5c33880,0x5c33880,0x5c33880,0x5c33880,0x5c33880,0x5c33880,0x5c33880,0x5c33880,0x5c33880,0x5c33880,58,1050,1858,2850,3842,4650,5642,6450,7442,8250,9242,10050,11042,12034,12842,18446,14595,18493,16164,9032,9032,9032,9032,9032,20,19,0,0,0,0,0,0,0x1d6100,0x6fe700,0xb06b80,0x102f180,0x1557780,0x195fc00,0x1e88200,0x2290680,0x27b8c80,0x2bc1100,0x30e9700,0x34f1b80,0x3a1a180,0x3f42780,0x434ac00,0x69d80,0x4c5c900,0x88b00,0x544e480,0x2f88600,0x2f88600,0x2f88600,0x2f88600,0x2f88600,0x2f88600,0x2f88600,0x2f88600,0x2f88600,0x2f88600,0x2f88600,0x2f88600,0x2f88600,59,1051,1859,2851,3843,4651,5643,6451,7443,8251,9243,10051,11043,12035,12843,13636,14596,15389,16165,16958,17715,17715,17715,17715,21,20,0,0,0,0,0,0,0x1d6b80,0x6ff180,0xb07600,0x102fc00,0x1558200,0x1960680,0x1e88c80,0x2291100,0x27b9700,0x2bc1b80,0x30ea180,0x34f2600,0x3a1ac00,0x3f43200,0x434b680,0x4749d80,0x4c5d380,0x505ba80,0x544ef00,0x584d600,0x5c34300,0x5c34300,0x5c34300,0x5c34300,0x5c34300,0x5c34300,0x5c34300,0x5c34300,0x5c34300,0x5c34300,0x5c34300,0x5c34300,60,1052,1860,2852,3844,4652,5644,6452,7444,8252,9244,10052,11044,12036,12844,13637,14597,15390,16166,16959,17716,17716,17716,17716,21,20,0,0,0,0,0,0,0x1d7600,0x6ffc00,0xb08080,0x1030680,0x1558c80,0x1961100,0x1e89700,0x2291b80,0x27ba180,0x2bc2600,0x30eac00,0x34f3080,0x3a1b680,0x3f43c80,0x434c100,0x474a800,0x4c5de00,0x505c500,0x544f980,0x584e080,0x5c34d80,0x5c34d80,0x5c34d80,0x5c34d80,0x5c34d80,0x5c34d80,0x5c34d80,0x5c34d80,0x5c34d80,0x5c34d80,0x5c34d80,0x5c34d80,61,1053,1861,2853,3845,4653,5645,6453,7445,8253,9245,10053,11045,12037,12845,13638,14598,15391,16167,16960,17717,17717,17717,17717,21,20,0,0,0,0,0,0,0x1d8080,0x700680,0xb08b00,0x1031100,0x1559700,0x1961b80,0x1e8a180,0x2292600,0x27bac00,0x2bc3080,0x30eb680,0x34f3b00,0x3a1c100,0x3f44700,0x434cb80,0x474b280,0x4c5e880,0x505cf80,0x5450400,0x584eb00,0x5c35800,0x5c35800,0x5c35800,0x5c35800,0x5c35800,0x5c35800,0x5c35800,0x5c35800,0x5c35800,0x5c35800,0x5c35800,0x5c35800,62,1054,1862,2854,3846,4654,5646,6454,7446,8254,9246,10054,11046,12038,12846,18447,14599,18494,16168,9288,9288,9288,9288,9288,20,19,0,0,0,0,0,0,0x1d8b00,0x701100,0xb09580,0x1031b80,0x155a180,0x1962600,0x1e8ac00,0x2293080,0x27bb680,0x2bc3b00,0x30ec100,0x34f4580,0x3a1cb80,0x3f45180,0x434d600,0x6a800,0x4c5f300,0x89580,0x5450e80,0x30d7b80,0x30d7b80,0x30d7b80,0x30d7b80,0x30d7b80,0x30d7b80,0x30d7b80,0x30d7b80,0x30d7b80,0x30d7b80,0x30d7b80,0x30d7b80,0x30d7b80,63,1055,1863,2855,3847,4655,5647,6455,7447,8255,9247,10055,11047,12039,12847,13639,14600,15392,16169,16961,17718,17718,17718,17718,21,20,0,0,0,0,0,0,0x1d9580,0x701b80,0xb0a000,0x1032600,0x155ac00,0x1963080,0x1e8b680,0x2293b00,0x27bc100,0x2bc4580,0x30ecb80,0x34f5000,0x3a1d600,0x3f45c00,0x434e080,0x474bd00,0x4c5fd80,0x505da00,0x5451900,0x584f580,0x5c36280,0x5c36280,0x5c36280,0x5c36280,0x5c36280,0x5c36280,0x5c36280,0x5c36280,0x5c36280,0x5c36280,0x5c36280,0x5c36280,64,1056,2048,2856,3848,4656,5648,6456,7448,8256,9248,10240,11048,12040,12848,13824,14601,15393,16170,16962,17719,17719,17719,17719,21,20,0,0,0,0,0,0,0x1da000,0x702600,0xc2ac00,0x1033080,0x155b680,0x1963b00,0x1e8c100,0x2294580,0x27bcb80,0x2bc5000,0x30ed600,0x3615c00,0x3a1e080,0x3f46680,0x434eb00,0x486c900,0x4c60800,0x505e480,0x5452380,0x5850000,0x5c36d00,0x5c36d00,0x5c36d00,0x5c36d00,0x5c36d00,0x5c36d00,0x5c36d00,0x5c36d00,0x5c36d00,0x5c36d00,0x5c36d00,0x5c36d00,65,1057,2049,2857,3849,4657,5649,6457,7449,8257,9249,10241,11049,12041,12849,13825,14602,15394,16171,16963,17720,17720,17720,17720,21,20,0,0,0,0,0,0,0x1daa80,0x703080,0xc2b680,0x1033b00,0x155c100,0x1964580,0x1e8cb80,0x2295000,0x27bd600,0x2bc5a80,0x30ee080,0x3616680,0x3a1eb00,0x3f47100,0x434f580,0x486d380,0x4c61280,0x505ef00,0x5452e00,0x5850a80,0x5c37780,0x5c37780,0x5c37780,0x5c37780,0x5c37780,0x5c37780,0x5c37780,0x5c37780,0x5c37780,0x5c37780,0x5c37780,0x5c37780,66,1058,2050,2858,3850,4658,5650,6458,7450,8258,9250,10242,11050,12042,12850,18448,14603,18495,16172,9544,9544,9544,9544,9544,20,19,0,0,0,0,0,0,0x1db500,0x703b00,0xc2c100,0x1034580,0x155cb80,0x1965000,0x1e8d600,0x2295a80,0x27be080,0x2bc6500,0x30eeb00,0x3617100,0x3a1f580,0x3f47b80,0x4350000,0x6b280,0x4c61d00,0x8a000,0x5453880,0x3227100,0x3227100,0x3227100,0x3227100,0x3227100,0x3227100,0x3227100,0x3227100,0x3227100,0x3227100,0x3227100,0x3227100,0x3227100,67,1059,2051,2859,3851,4659,5651,6459,7451,8259,9251,10243,11051,12043,12851,13826,14604,15395,16173,16964,17721,17721,17721,17721,21,20,0,0,0,0,0,0,0x1dbf80,0x704580,0xc2cb80,0x1035000,0x155d600,0x1965a80,0x1e8e080,0x2296500,0x27beb00,0x2bc6f80,0x30ef580,0x3617b80,0x3a20000,0x3f48600,0x4350a80,0x486de00,0x4c62780,0x505f980,0x5454300,0x5851500,0x5c38200,0x5c38200,0x5c38200,0x5c38200,0x5c38200,0x5c38200,0x5c38200,0x5c38200,0x5c38200,0x5c38200,0x5c38200,0x5c38200,68,1060,2052,2860,3852,4660,5652,6460,7452,8260,9252,10244,11052,12044,12852,13827,14605,15396,16174,16965,17722,17722,17722,17722,21,20,0,0,0,0,0,0,0x1dca00,0x705000,0xc2d600,0x1035a80,0x155e080,0x1966500,0x1e8eb00,0x2296f80,0x27bf580,0x2bc7a00,0x30f0000,0x3618600,0x3a20a80,0x3f49080,0x4351500,0x486e880,0x4c63200,0x5060400,0x5454d80,0x5851f80,0x5c38c80,0x5c38c80,0x5c38c80,0x5c38c80,0x5c38c80,0x5c38c80,0x5c38c80,0x5c38c80,0x5c38c80,0x5c38c80,0x5c38c80,0x5c38c80,69,1061,2053,2861,3853,4661,5653,6461,7453,8261,9253,10245,11053,12045,12853,13828,14606,15397,16175,16966,17723,17723,17723,17723,21,20,0,0,0,0,0,0,0x1dd480,0x705a80,0xc2e080,0x1036500,0x155eb00,0x1966f80,0x1e8f580,0x2297a00,0x27c0000,0x2bc8480,0x30f0a80,0x3619080,0x3a21500,0x3f49b00,0x4351f80,0x486f300,0x4c63c80,0x5060e80,0x5455800,0x5852a00,0x5c39700,0x5c39700,0x5c39700,0x5c39700,0x5c39700,0x5c39700,0x5c39700,0x5c39700,0x5c39700,0x5c39700,0x5c39700,0x5c39700,70,1062,2054,2862,3854,4662,5654,6462,7454,8262,9254,10246,11054,12046,12854,18449,14607,18496,16176,9800,9800,9800,9800,9800,20,19,0,0,0,0,0,0,0x1ddf00,0x706500,0xc2eb00,0x1036f80,0x155f580,0x1967a00,0x1e90000,0x2298480,0x27c0a80,0x2bc8f00,0x30f1500,0x3619b00,0x3a21f80,0x3f4a580,0x4352a00,0x6bd00,0x4c64700,0x8aa80,0x5456280,0x3376680,0x3376680,0x3376680,0x3376680,0x3376680,0x3376680,0x3376680,0x3376680,0x3376680,0x3376680,0x3376680,0x3376680,0x3376680,71,1063,2055,2863,3855,4663,5655,6463,7455,8263,9255,10247,11055,12047,12855,13829,14608,15398,16177,16967,17724,17724,17724,17724,21,20,0,0,0,0,0,0,0x1de980,0x706f80,0xc2f580,0x1037a00,0x1560000,0x1968480,0x1e90a80,0x2298f00,0x27c1500,0x2bc9980,0x30f1f80,0x361a580,0x3a22a00,0x3f4b000,0x4353480,0x486fd80,0x4c65180,0x5061900,0x5456d00,0x5853480,0x5c3a180,0x5c3a180,0x5c3a180,0x5c3a180,0x5c3a180,0x5c3a180,0x5c3a180,0x5c3a180,0x5c3a180,0x5c3a180,0x5c3a180,0x5c3a180,256,1064,2056,2864,3856,4664,5656,6464,7456,8448,9256,10248,11056,12048,12856,13830,14609,15399,16178,17152,17725,17725,17725,17725,21,20,0,0,0,0,0,0,0x2ff580,0x707a00,0xc30000,0x1038480,0x1560a80,0x1968f00,0x1e91500,0x2299980,0x27c1f80,0x2cea580,0x30f2a00,0x361b000,0x3a23480,0x3f4ba80,0x4353f00,0x4870800,0x4c65c00,0x5062380,0x5457780,0x5974080,0x5c3ac00,0x5c3ac00,0x5c3ac00,0x5c3ac00,0x5c3ac00,0x5c3ac00,0x5c3ac00,0x5c3ac00,0x5c3ac00,0x5c3ac00,0x5c3ac00,0x5c3ac00,257,1065,2057,2865,3857,4665,5657,6465,7457,8449,9257,10249,11057,12049,12857,13831,14610,15400,16179,17153,17726,17726,17726,17726,21,20,0,0,0,0,0,0,0x300000,0x708480,0xc30a80,0x1038f00,0x1561500,0x1969980,0x1e91f80,0x229a400,0x27c2a00,0x2ceb000,0x30f3480,0x361ba80,0x3a23f00,0x3f4c500,0x4354980,0x4871280,0x4c66680,0x5062e00,0x5458200,0x5974b00,0x5c3b680,0x5c3b680,0x5c3b680,0x5c3b680,0x5c3b680,0x5c3b680,0x5c3b680,0x5c3b680,0x5c3b680,0x5c3b680,0x5c3b680,0x5c3b680,258,1066,2058,2866,3858,4666,5658,6466,7458,8450,9258,10250,11058,12050,12858,18450,14611,18497,16180,10056,10056,10056,10056,10056,20,19,0,0,0,0,0,0,0x300a80,0x708f00,0xc31500,0x1039980,0x1561f80,0x196a400,0x1e92a00,0x229ae80,0x27c3480,0x2ceba80,0x30f3f00,0x361c500,0x3a24980,0x3f4cf80,0x4355400,0x6c780,0x4c67100,0x8b500,0x5458c80,0x34c5c00,0x34c5c00,0x34c5c00,0x34c5c00,0x34c5c00,0x34c5c00,0x34c5c00,0x34c5c00,0x34c5c00,0x34c5c00,0x34c5c00,0x34c5c00,0x34c5c00,259,1067,2059,2867,3859,4667,5659,6467,7459,8451,9259,10251,11059,12051,12859,13832,14612,15401,16181,17154,17727,17727,17727,17727,21,20,0,0,0,0,0,0,0x301500,0x709980,0xc31f80,0x103a400,0x1562a00,0x196ae80,0x1e93480,0x229b900,0x27c3f00,0x2cec500,0x30f4980,0x361cf80,0x3a25400,0x3f4da00,0x4355e80,0x4871d00,0x4c67b80,0x5063880,0x5459700,0x5975580,0x5c3c100,0x5c3c100,0x5c3c100,0x5c3c100,0x5c3c100,0x5c3c100,0x5c3c100,0x5c3c100,0x5c3c100,0x5c3c100,0x5c3c100,0x5c3c100,260,1068,2060,2868,3860,4668,5660,6468,7460,8452,9260,10252,11060,12052,12860,13833,14613,15402,16182,17155,17728,17728,17728,17728,21,20,0,0,0,0,0,0,0x301f80,0x70a400,0xc32a00,0x103ae80,0x1563480,0x196b900,0x1e93f00,0x229c380,0x27c4980,0x2cecf80,0x30f5400,0x361da00,0x3a25e80,0x3f4e480,0x4356900,0x4872780,0x4c68600,0x5064300,0x545a180,0x5976000,0x5c3cb80,0x5c3cb80,0x5c3cb80,0x5c3cb80,0x5c3cb80,0x5c3cb80,0x5c3cb80,0x5c3cb80,0x5c3cb80,0x5c3cb80,0x5c3cb80,0x5c3cb80,261,1069,2061,2869,3861,4669,5661,6469,7461,8453,9261,10253,11061,12053,12861,13834,14614,15403,16183,17156,17729,17729,17729,17729,21,20,0,0,0,0,0,0,0x302a00,0x70ae80,0xc33480,0x103b900,0x1563f00,0x196c380,0x1e94980,0x229ce00,0x27c5400,0x2ceda00,0x30f5e80,0x361e480,0x3a26900,0x3f4ef00,0x4357380,0x4873200,0x4c69080,0x5064d80,0x545ac00,0x5976a80,0x5c3d600,0x5c3d600,0x5c3d600,0x5c3d600,0x5c3d600,0x5c3d600,0x5c3d600,0x5c3d600,0x5c3d600,0x5c3d600,0x5c3d600,0x5c3d600,262,1070,2062,2870,3862,4670,5662,6470,7462,8454,9262,10254,11062,12054,12862,18451,14615,18498,16184,10312,10312,10312,10312,10312,20,19,0,0,0,0,0,0,0x303480,0x70b900,0xc33f00,0x103c380,0x1564980,0x196ce00,0x1e95400,0x229d880,0x27c5e80,0x2cee480,0x30f6900,0x361ef00,0x3a27380,0x3f4f980,0x4357e00,0x6d200,0x4c69b00,0x8bf80,0x545b680,0x3615180,0x3615180,0x3615180,0x3615180,0x3615180,0x3615180,0x3615180,0x3615180,0x3615180,0x3615180,0x3615180,0x3615180,0x3615180,263,1071,2063,2871,3863,4671,5663,6471,7463,8455,9263,10255,11063,12055,12863,13835,14616,15404,16185,17157,17730,17730,17730,17730,21,20,0,0,0,0,0,0,0x303f00,0x70c380,0xc34980,0x103ce00,0x1565400,0x196d880,0x1e95e80,0x229e300,0x27c6900,0x2ceef00,0x30f7380,0x361f980,0x3a27e00,0x3f50400,0x4358880,0x4873c80,0x4c6a580,0x5065800,0x545c100,0x5977500,0x5c3e080,0x5c3e080,0x5c3e080,0x5c3e080,0x5c3e080,0x5c3e080,0x5c3e080,0x5c3e080,0x5c3e080,0x5c3e080,0x5c3e080,0x5c3e080,264,1072,2064,2872,3864,4672,5664,6656,7464,8456,9264,10256,11064,12056,12864,13836,14617,15405,16186,17158,17731,17731,17731,17731,21,20,0,0,0,0,0,0,0x304980,0x70ce00,0xc35400,0x103d880,0x1565e80,0x196e300,0x1e96900,0x23bef00,0x27c7380,0x2cef980,0x30f7e00,0x3620400,0x3a28880,0x3f50e80,0x4359300,0x4874700,0x4c6b000,0x5066280,0x545cb80,0x5977f80,0x5c3eb00,0x5c3eb00,0x5c3eb00,0x5c3eb00,0x5c3eb00,0x5c3eb00,0x5c3eb00,0x5c3eb00,0x5c3eb00,0x5c3eb00,0x5c3eb00,0x5c3eb00,265,1073,2065,2873,3865,4673,5665,6657,7465,8457,9265,10257,11065,12057,12865,13837,14618,15406,16187,17159,17732,17732,17732,17732,21,20,0,0,0,0,0,0,0x305400,0x70d880,0xc35e80,0x103e300,0x1566900,0x196ed80,0x1e97380,0x23bf980,0x27c7e00,0x2cf0400,0x30f8880,0x3620e80,0x3a29300,0x3f51900,0x4359d80,0x4875180,0x4c6ba80,0x5066d00,0x545d600,0x5978a00,0x5c3f580,0x5c3f580,0x5c3f580,0x5c3f580,0x5c3f580,0x5c3f580,0x5c3f580,0x5c3f580,0x5c3f580,0x5c3f580,0x5c3f580,0x5c3f580,266,1074,2066,2874,3866,4674,5666,6658,7466,8458,9266,10258,11066,12058,12866,18452,14619,18499,16188,10568,10568,10568,10568,10568,20,19,0,0,0,0,0,0,0x305e80,0x70e300,0xc36900,0x103ed80,0x1567380,0x196f800,0x1e97e00,0x23c0400,0x27c8880,0x2cf0e80,0x30f9300,0x3621900,0x3a29d80,0x3f52380,0x435a800,0x6dc80,0x4c6c500,0x8ca00,0x545e080,0x3764700,0x3764700,0x3764700,0x3764700,0x3764700,0x3764700,0x3764700,0x3764700,0x3764700,0x3764700,0x3764700,0x3764700,0x3764700,267,1075,2067,2875,3867,4675,5667,6659,7467,8459,9267,10259,11067,12059,12867,13838,14620,15407,16189,17160,17733,17733,17733,17733,21,20,0,0,0,0,0,0,0x306900,0x70ed80,0xc37380,0x103f800,0x1567e00,0x1970280,0x1e98880,0x23c0e80,0x27c9300,0x2cf1900,0x30f9d80,0x3622380,0x3a2a800,0x3f52e00,0x435b280,0x4875c00,0x4c6cf80,0x5067780,0x545eb00,0x5979480,0x5c40000,0x5c40000,0x5c40000,0x5c40000,0x5c40000,0x5c40000,0x5c40000,0x5c40000,0x5c40000,0x5c40000,0x5c40000,0x5c40000,268,1076,2068,2876,3868,4676,5668,6660,7468,8460,9268,10260,11068,12060,12868,13839,14621,15408,16190,17161,17734,17734,17734,17734,21,20,0,0,0,0,0,0,0x307380,0x70f800,0xc37e00,0x1040280,0x1568880,0x1970d00,0x1e99300,0x23c1900,0x27c9d80,0x2cf2380,0x30fa800,0x3622e00,0x3a2b280,0x3f53880,0x435bd00,0x4876680,0x4c6da00,0x5068200,0x545f580,0x5979f00,0x5c40a80,0x5c40a80,0x5c40a80,0x5c40a80,0x5c40a80,0x5c40a80,0x5c40a80,0x5c40a80,0x5c40a80,0x5c40a80,0x5c40a80,0x5c40a80,269,1077,2069,2877,3869,4677,5669,6661,7469,8461,9269,10261,11069,12061,12869,13840,14622,15409,16191,17162,17735,17735,17735,17735,21,20,0,0,0,0,0,0,0x307e00,0x710280,0xc38880,0x1040d00,0x1569300,0x1971780,0x1e99d80,0x23c2380,0x27ca800,0x2cf2e00,0x30fb280,0x3623880,0x3a2bd00,0x3f54300,0x435c780,0x4877100,0x4c6e480,0x5068c80,0x5460000,0x597a980,0x5c41500,0x5c41500,0x5c41500,0x5c41500,0x5c41500,0x5c41500,0x5c41500,0x5c41500,0x5c41500,0x5c41500,0x5c41500,0x5c41500,270,1078,2070,2878,3870,4678,5670,6662,7470,8462,9270,10262,11070,12062,12870,18453,14623,18500,16192,10824,10824,10824,10824,10824,20,19,0,0,0,0,0,0,0x308880,0x710d00,0xc39300,0x1041780,0x1569d80,0x1972200,0x1e9a800,0x23c2e00,0x27cb280,0x2cf3880,0x30fbd00,0x3624300,0x3a2c780,0x3f54d80,0x435d200,0x6e700,0x4c6ef00,0x8d480,0x5460a80,0x38b3c80,0x38b3c80,0x38b3c80,0x38b3c80,0x38b3c80,0x38b3c80,0x38b3c80,0x38b3c80,0x38b3c80,0x38b3c80,0x38b3c80,0x38b3c80,0x38b3c80,271,1079,2071,2879,3871,4679,5671,6663,7471,8463,9271,10263,11071,12063,12871,13841,14624,15410,16193,17163,17920,17920,17920,17920,21,20,0,0,0,0,0,0,0x309300,0x711780,0xc39d80,0x1042200,0x156a800,0x1972c80,0x1e9b280,0x23c3880,0x27cbd00,0x2cf4300,0x30fc780,0x3624d80,0x3a2d200,0x3f55800,0x435dc80,0x4877b80,0x4c6f980,0x5069700,0x5461500,0x597b400,0x5d62100,0x5d62100,0x5d62100,0x5d62100,0x5d62100,0x5d62100,0x5d62100,0x5d62100,0x5d62100,0x5d62100,0x5d62100,0x5d62100,272,1080,2072,2880,3872,4864,5672,6664,7472,8464,9272,10264,11072,12064,13056,13842,14625,15411,16194,17164,17921,17921,17921,17921,21,20,0,0,0,0,0,0,0x309d80,0x712200,0xc3a800,0x1042c80,0x156b280,0x1a93880,0x1e9bd00,0x23c4300,0x27cc780,0x2cf4d80,0x30fd200,0x3625800,0x3a2dc80,0x3f56280,0x447e880,0x4878600,0x4c70400,0x506a180,0x5461f80,0x597be80,0x5d62b80,0x5d62b80,0x5d62b80,0x5d62b80,0x5d62b80,0x5d62b80,0x5d62b80,0x5d62b80,0x5d62b80,0x5d62b80,0x5d62b80,0x5d62b80,273,1081,2073,2881,3873,4865,5673,6665,7473,8465,9273,10265,11073,12065,13057,13843,14626,15412,16195,17165,17922,17922,17922,17922,21,20,0,0,0,0,0,0,0x30a800,0x712c80,0xc3b280,0x1043700,0x156bd00,0x1a94300,0x1e9c780,0x23c4d80,0x27cd200,0x2cf5800,0x30fdc80,0x3626280,0x3a2e700,0x3f56d00,0x447f300,0x4879080,0x4c70e80,0x506ac00,0x5462a00,0x597c900,0x5d63600,0x5d63600,0x5d63600,0x5d63600,0x5d63600,0x5d63600,0x5d63600,0x5d63600,0x5d63600,0x5d63600,0x5d63600,0x5d63600,274,1082,2074,2882,3874,4866,5674,6666,7474,8466,9274,10266,11074,12066,13058,18454,14627,18501,16196,11080,11080,11080,11080,11080,20,19,0,0,0,0,0,0,0x30b280,0x713700,0xc3bd00,0x1044180,0x156c780,0x1a94d80,0x1e9d200,0x23c5800,0x27cdc80,0x2cf6280,0x30fe700,0x3626d00,0x3a2f180,0x3f57780,0x447fd80,0x6f180,0x4c71900,0x8df00,0x5463480,0x3a03200,0x3a03200,0x3a03200,0x3a03200,0x3a03200,0x3a03200,0x3a03200,0x3a03200,0x3a03200,0x3a03200,0x3a03200,0x3a03200,0x3a03200,275,1083,2075,2883,3875,4867,5675,6667,7475,8467,9275,10267,11075,12067,13059,13844,14628,15413,16197,17166,17923,17923,17923,17923,21,20,0,0,0,0,0,0,0x30bd00,0x714180,0xc3c780,0x1044c00,0x156d200,0x1a95800,0x1e9dc80,0x23c6280,0x27ce700,0x2cf6d00,0x30ff180,0x3627780,0x3a2fc00,0x3f58200,0x4480800,0x4879b00,0x4c72380,0x506b680,0x5463f00,0x597d380,0x5d64080,0x5d64080,0x5d64080,0x5d64080,0x5d64080,0x5d64080,0x5d64080,0x5d64080,0x5d64080,0x5d64080,0x5d64080,0x5d64080,276,1084,2076,2884,3876,4868,5676,6668,7476,8468,9276,10268,11076,12068,13060,13845,14629,15414,16198,17167,17924,17924,17924,17924,21,20,0,0,0,0,0,0,0x30c780,0x714c00,0xc3d200,0x1045680,0x156dc80,0x1a96280,0x1e9e700,0x23c6d00,0x27cf180,0x2cf7780,0x30ffc00,0x3628200,0x3a30680,0x3f58c80,0x4481280,0x487a580,0x4c72e00,0x506c100,0x5464980,0x597de00,0x5d64b00,0x5d64b00,0x5d64b00,0x5d64b00,0x5d64b00,0x5d64b00,0x5d64b00,0x5d64b00,0x5d64b00,0x5d64b00,0x5d64b00,0x5d64b00,277,1085,2077,2885,3877,4869,5677,6669,7477,8469,9277,10269,11077,12069,13061,13846,14630,15415,16199,17168,17925,17925,17925,17925,21,20,0,0,0,0,0,0,0x30d200,0x715680,0xc3dc80,0x1046100,0x156e700,0x1a96d00,0x1e9f180,0x23c7780,0x27cfc00,0x2cf8200,0x3100680,0x3628c80,0x3a31100,0x3f59700,0x4481d00,0x487b000,0x4c73880,0x506cb80,0x5465400,0x597e880,0x5d65580,0x5d65580,0x5d65580,0x5d65580,0x5d65580,0x5d65580,0x5d65580,0x5d65580,0x5d65580,0x5d65580,0x5d65580,0x5d65580,278,1086,2078,2886,3878,4870,5678,6670,7478,8470,9278,10270,11078,12070,13062,18455,14631,18502,16384,11336,11336,11336,11336,11336,20,19,0,0,0,0,0,0,0x30dc80,0x716100,0xc3e700,0x1046b80,0x156f180,0x1a97780,0x1e9fc00,0x23c8200,0x27d0680,0x2cf8c80,0x3101100,0x3629700,0x3a31b80,0x3f5a180,0x4482780,0x6fc00,0x4c74300,0x8e980,0x5586000,0x3b52780,0x3b52780,0x3b52780,0x3b52780,0x3b52780,0x3b52780,0x3b52780,0x3b52780,0x3b52780,0x3b52780,0x3b52780,0x3b52780,0x3b52780,279,1087,2079,2887,3879,4871,5679,6671,7479,8471,9279,10271,11079,12071,13063,13847,14632,15416,16385,17169,17926,17926,17926,17926,21,20,0,0,0,0,0,0,0x30e700,0x716b80,0xc3f180,0x1047600,0x156fc00,0x1a98200,0x1ea0680,0x23c8c80,0x27d1100,0x2cf9700,0x3101b80,0x362a180,0x3a32600,0x3f5ac00,0x4483200,0x487ba80,0x4c74d80,0x506d600,0x5586a80,0x597f300,0x5d66000,0x5d66000,0x5d66000,0x5d66000,0x5d66000,0x5d66000,0x5d66000,0x5d66000,0x5d66000,0x5d66000,0x5d66000,0x5d66000,280,1088,2080,3072,3880,4872,5680,6672,7480,8472,9280,10272,11264,12072,13064,13848,14633,15417,16386,17170,17927,17927,17927,17927,21,20,0,0,0,0,0,0,0x30f180,0x717600,0xc3fc00,0x1168200,0x1570680,0x1a98c80,0x1ea1100,0x23c9700,0x27d1b80,0x2cfa180,0x3102600,0x362ac00,0x3b53200,0x3f5b680,0x4483c80,0x487c500,0x4c75800,0x506e080,0x5587500,0x597fd80,0x5d66a80,0x5d66a80,0x5d66a80,0x5d66a80,0x5d66a80,0x5d66a80,0x5d66a80,0x5d66a80,0x5d66a80,0x5d66a80,0x5d66a80,0x5d66a80,281,1089,2081,3073,3881,4873,5681,6673,7481,8473,9281,10273,11265,12073,13065,13849,14634,15418,16387,17171,17928,17928,17928,17928,21,20,0,0,0,0,0,0,0x30fc00,0x718080,0xc40680,0x1168c80,0x1571100,0x1a99700,0x1ea1b80,0x23ca180,0x27d2600,0x2cfac00,0x3103080,0x362b680,0x3b53c80,0x3f5c100,0x4484700,0x487cf80,0x4c76280,0x506eb00,0x5587f80,0x5980800,0x5d67500,0x5d67500,0x5d67500,0x5d67500,0x5d67500,0x5d67500,0x5d67500,0x5d67500,0x5d67500,0x5d67500,0x5d67500,0x5d67500,282,1090,2082,3074,3882,4874,5682,6674,7482,8474,9282,10274,11266,12074,13066,18456,14635,18503,16388,11592,11592,11592,11592,11592,20,19,0,0,0,0,0,0,0x310680,0x718b00,0xc41100,0x1169700,0x1571b80,0x1a9a180,0x1ea2600,0x23cac00,0x27d3080,0x2cfb680,0x3103b00,0x362c100,0x3b54700,0x3f5cb80,0x4485180,0x70680,0x4c76d00,0x8f400,0x5588a00,0x3ca1d00,0x3ca1d00,0x3ca1d00,0x3ca1d00,0x3ca1d00,0x3ca1d00,0x3ca1d00,0x3ca1d00,0x3ca1d00,0x3ca1d00,0x3ca1d00,0x3ca1d00,0x3ca1d00,283,1091,2083,3075,3883,4875,5683,6675,7483,8475,9283,10275,11267,12075,13067,13850,14636,15419,16389,17172,17929,17929,17929,17929,21,20,0,0,0,0,0,0,0x311100,0x719580,0xc41b80,0x116a180,0x1572600,0x1a9ac00,0x1ea3080,0x23cb680,0x27d3b00,0x2cfc100,0x3104580,0x362cb80,0x3b55180,0x3f5d600,0x4485c00,0x487da00,0x4c77780,0x506f580,0x5589480,0x5981280,0x5d67f80,0x5d67f80,0x5d67f80,0x5d67f80,0x5d67f80,0x5d67f80,0x5d67f80,0x5d67f80,0x5d67f80,0x5d67f80,0x5d67f80,0x5d67f80,284,1092,2084,3076,3884,4876,5684,6676,7484,8476,9284,10276,11268,12076,13068,13851,14637,15420,16390,17173,17930,17930,17930,17930,21,20,0,0,0,0,0,0,0x311b80,0x71a000,0xc42600,0x116ac00,0x1573080,0x1a9b680,0x1ea3b00,0x23cc100,0x27d4580,0x2cfcb80,0x3105000,0x362d600,0x3b55c00,0x3f5e080,0x4486680,0x487e480,0x4c78200,0x5070000,0x5589f00,0x5981d00,0x5d68a00,0x5d68a00,0x5d68a00,0x5d68a00,0x5d68a00,0x5d68a00,0x5d68a00,0x5d68a00,0x5d68a00,0x5d68a00,0x5d68a00,0x5d68a00,285,1093,2085,3077,3885,4877,5685,6677,7485,8477,9285,10277,11269,12077,13069,13852,14638,15421,16391,17174,17931,17931,17931,17931,21,20,0,0,0,0,0,0,0x312600,0x71aa80,0xc43080,0x116b680,0x1573b00,0x1a9c100,0x1ea4580,0x23ccb80,0x27d5000,0x2cfd600,0x3105a80,0x362e080,0x3b56680,0x3f5eb00,0x4487100,0x487ef00,0x4c78c80,0x5070a80,0x558a980,0x5982780,0x5d69480,0x5d69480,0x5d69480,0x5d69480,0x5d69480,0x5d69480,0x5d69480,0x5d69480,0x5d69480,0x5d69480,0x5d69480,0x5d69480,286,1094,2086,3078,3886,4878,5686,6678,7486,8478,9286,10278,11270,12078,13070,18457,14639,18504,16392,11848,11848,11848,11848,11848,20,19,0,0,0,0,0,0,0x313080,0x71b500,0xc43b00,0x116c100,0x1574580,0x1a9cb80,0x1ea5000,0x23cd600,0x27d5a80,0x2cfe080,0x3106500,0x362eb00,0x3b57100,0x3f5f580,0x4487b80,0x71100,0x4c79700,0x60000,0x558b400,0x3df1280,0x3df1280,0x3df1280,0x3df1280,0x3df1280,0x3df1280,0x3df1280,0x3df1280,0x3df1280,0x3df1280,0x3df1280,0x3df1280,0x3df1280,287,1095,2087,3079,3887,4879,5687,6679,7487,8479,9287,10279,11271,12079,13071,13853,14640,15422,16393,17175,17932,17932,17932,17932,21,20,0,0,0,0,0,0,0x313b00,0x71bf80,0xc44580,0x116cb80,0x1575000,0x1a9d600,0x1ea5a80,0x23ce080,0x27d6500,0x2cfeb00,0x3106f80,0x362f580,0x3b57b80,0x3f60000,0x4488600,0x487f980,0x4c7a180,0x5071500,0x558be80,0x5983200,0x5d69f00,0x5d69f00,0x5d69f00,0x5d69f00,0x5d69f00,0x5d69f00,0x5d69f00,0x5d69f00,0x5d69f00,0x5d69f00,0x5d69f00,0x5d69f00,288,1280,2088,3080,3888,4880,5688,6680,7488,8480,9472,10280,11272,12080,13072,13854,14641,15423,16394,17176,17933,17933,17933,17933,21,20,0,0,0,0,0,0,0x314580,0x83cb80,0xc45000,0x116d600,0x1575a80,0x1a9e080,0x1ea6500,0x23ceb00,0x27d6f80,0x2cff580,0x3227b80,0x3630000,0x3b58600,0x3f60a80,0x4489080,0x4880400,0x4c7ac00,0x5071f80,0x558c900,0x5983c80,0x5d6a980,0x5d6a980,0x5d6a980,0x5d6a980,0x5d6a980,0x5d6a980,0x5d6a980,0x5d6a980,0x5d6a980,0x5d6a980,0x5d6a980,0x5d6a980,289,1281,2089,3081,3889,4881,5689,6681,7489,8481,9473,10281,11273,12081,13073,13855,14642,15424,16395,17177,17934,17934,17934,17934,21,20,0,0,0,0,0,0,0x315000,0x83d600,0xc45a80,0x116e080,0x1576500,0x1a9eb00,0x1ea6f80,0x23cf580,0x27d7a00,0x2d00000,0x3228600,0x3630a80,0x3b59080,0x3f61500,0x4489b00,0x4880e80,0x4c7b680,0x5072a00,0x558d380,0x5984700,0x5d6b400,0x5d6b400,0x5d6b400,0x5d6b400,0x5d6b400,0x5d6b400,0x5d6b400,0x5d6b400,0x5d6b400,0x5d6b400,0x5d6b400,0x5d6b400,290,1282,2090,3082,3890,4882,5690,6682,7490,8482,9474,10282,11274,12082,13074,18458,14643,72,16396,12104,12104,12104,12104,12104,20,19,0,0,0,0,0,0,0x315a80,0x83e080,0xc46500,0x116eb00,0x1576f80,0x1a9f580,0x1ea7a00,0x23d0000,0x27d8480,0x2d00a80,0x3229080,0x3631500,0x3b59b00,0x3f61f80,0x448a580,0x71b80,0x4c7c100,0x1af580,0x558de00,0x3f40800,0x3f40800,0x3f40800,0x3f40800,0x3f40800,0x3f40800,0x3f40800,0x3f40800,0x3f40800,0x3f40800,0x3f40800,0x3f40800,0x3f40800,291,1283,2091,3083,3891,4883,5691,6683,7491,8483,9475,10283,11275,12083,13075,13856,14644,15425,16397,17178,17935,17935,17935,17935,21,20,0,0,0,0,0,0,0x316500,0x83eb00,0xc46f80,0x116f580,0x1577a00,0x1aa0000,0x1ea8480,0x23d0a80,0x27d8f00,0x2d01500,0x3229b00,0x3631f80,0x3b5a580,0x3f62a00,0x448b000,0x4881900,0x4c7cb80,0x5073480,0x558e880,0x5985180,0x5d6be80,0x5d6be80,0x5d6be80,0x5d6be80,0x5d6be80,0x5d6be80,0x5d6be80,0x5d6be80,0x5d6be80,0x5d6be80,0x5d6be80,0x5d6be80,292,1284,2092,3084,3892,4884,5692,6684,7492,8484,9476,10284,11276,12084,13076,13857,14645,15426,16398,17179,17936,17936,17936,17936,21,20,0,0,0,0,0,0,0x316f80,0x83f580,0xc47a00,0x1170000,0x1578480,0x1aa0a80,0x1ea8f00,0x23d1500,0x27d9980,0x2d01f80,0x322a580,0x3632a00,0x3b5b000,0x3f63480,0x448ba80,0x4882380,0x4c7d600,0x5073f00,0x558f300,0x5985c00,0x5d6c900,0x5d6c900,0x5d6c900,0x5d6c900,0x5d6c900,0x5d6c900,0x5d6c900,0x5d6c900,0x5d6c900,0x5d6c900,0x5d6c900,0x5d6c900,293,1285,2093,3085,3893,4885,5693,6685,7493,8485,9477,10285,11277,12085,13077,13858,14646,15427,16399,17180,17937,17937,17937,17937,21,20,0,0,0,0,0,0,0x317a00,0x840000,0xc48480,0x1170a80,0x1578f00,0x1aa1500,0x1ea9980,0x23d1f80,0x27da400,0x2d02a00,0x322b000,0x3633480,0x3b5ba80,0x3f63f00,0x448c500,0x4882e00,0x4c7e080,0x5074980,0x558fd80,0x5986680,0x5d6d380,0x5d6d380,0x5d6d380,0x5d6d380,0x5d6d380,0x5d6d380,0x5d6d380,0x5d6d380,0x5d6d380,0x5d6d380,0x5d6d380,0x5d6d380,294,1286,2094,3086,3894,4886,5694,6686,7494,8486,9478,10286,11278,12086,13078,18459,14647,328,16400,12360,12360,12360,12360,12360,20,19,0,0,0,0,0,0,0x318480,0x840a80,0xc48f00,0x1171500,0x1579980,0x1aa1f80,0x1eaa400,0x23d2a00,0x27dae80,0x2d03480,0x322ba80,0x3633f00,0x3b5c500,0x3f64980,0x448cf80,0x72600,0x4c7eb00,0x2feb00,0x5590800,0x408fd80,0x408fd80,0x408fd80,0x408fd80,0x408fd80,0x408fd80,0x408fd80,0x408fd80,0x408fd80,0x408fd80,0x408fd80,0x408fd80,0x408fd80,295,1287,2095,3087,3895,4887,5695,6687,7495,8487,9479,10287,11279,12087,13079,13859,14648,15428,16401,17181,17938,17938,17938,17938,21,20,0,0,0,0,0,0,0x318f00,0x841500,0xc49980,0x1171f80,0x157a400,0x1aa2a00,0x1eaae80,0x23d3480,0x27db900,0x2d03f00,0x322c500,0x3634980,0x3b5cf80,0x3f65400,0x448da00,0x4883880,0x4c7f580,0x5075400,0x5591280,0x5987100,0x5d6de00,0x5d6de00,0x5d6de00,0x5d6de00,0x5d6de00,0x5d6de00,0x5d6de00,0x5d6de00,0x5d6de00,0x5d6de00,0x5d6de00,0x5d6de00,296,1288,2096,3088,3896,4888,5696,6688,7680,8488,9480,10288,11280,12088,13080,13860,14649,15429,16402,17182,17939,17939,17939,17939,21,20,0,0,0,0,0,0,0x319980,0x841f80,0xc4a400,0x1172a00,0x157ae80,0x1aa3480,0x1eab900,0x23d3f00,0x28fc500,0x2d04980,0x322cf80,0x3635400,0x3b5da00,0x3f65e80,0x448e480,0x4884300,0x4c80000,0x5075e80,0x5591d00,0x5987b80,0x5d6e880,0x5d6e880,0x5d6e880,0x5d6e880,0x5d6e880,0x5d6e880,0x5d6e880,0x5d6e880,0x5d6e880,0x5d6e880,0x5d6e880,0x5d6e880,297,1289,2097,3089,3897,4889,5697,6689,7681,8489,9481,10289,11281,12089,13081,13861,14650,15430,16403,17183,17940,17940,17940,17940,21,20,0,0,0,0,0,0,0x31a400,0x842a00,0xc4ae80,0x1173480,0x157b900,0x1aa3f00,0x1eac380,0x23d4980,0x28fcf80,0x2d05400,0x322da00,0x3635e80,0x3b5e480,0x3f66900,0x448ef00,0x4884d80,0x4c80a80,0x5076900,0x5592780,0x5988600,0x5d6f300,0x5d6f300,0x5d6f300,0x5d6f300,0x5d6f300,0x5d6f300,0x5d6f300,0x5d6f300,0x5d6f300,0x5d6f300,0x5d6f300,0x5d6f300,298,1290,2098,3090,3898,4890,5698,6690,7682,8490,9482,10290,11282,12090,13082,18460,14651,584,16404,12616,12616,12616,12616,12616,20,19,0,0,0,0,0,0,0x31ae80,0x843480,0xc4b900,0x1173f00,0x157c380,0x1aa4980,0x1eace00,0x23d5400,0x28fda00,0x2d05e80,0x322e480,0x3636900,0x3b5ef00,0x3f67380,0x448f980,0x73080,0x4c81500,0x44e080,0x5593200,0x41df300,0x41df300,0x41df300,0x41df300,0x41df300,0x41df300,0x41df300,0x41df300,0x41df300,0x41df300,0x41df300,0x41df300,0x41df300,299,1291,2099,3091,3899,4891,5699,6691,7683,8491,9483,10291,11283,12091,13083,13862,14652,15431,16405,17184,17941,17941,17941,17941,21,20,0,0,0,0,0,0,0x31b900,0x843f00,0xc4c380,0x1174980,0x157ce00,0x1aa5400,0x1ead880,0x23d5e80,0x28fe480,0x2d06900,0x322ef00,0x3637380,0x3b5f980,0x3f67e00,0x4490400,0x4885800,0x4c81f80,0x5077380,0x5593c80,0x5989080,0x5d6fd80,0x5d6fd80,0x5d6fd80,0x5d6fd80,0x5d6fd80,0x5d6fd80,0x5d6fd80,0x5d6fd80,0x5d6fd80,0x5d6fd80,0x5d6fd80,0x5d6fd80,300,1292,2100,3092,3900,4892,5700,6692,7684,8492,9484,10292,11284,12092,13084,13863,14653,15616,16406,17185,17942,17942,17942,17942,21,20,0,0,0,0,0,0,0x31c380,0x844980,0xc4ce00,0x1175400,0x157d880,0x1aa5e80,0x1eae300,0x23d6900,0x28fef00,0x2d07380,0x322f980,0x3637e00,0x3b60400,0x3f68880,0x4490e80,0x4886280,0x4c82a00,0x5197f80,0x5594700,0x5989b00,0x5d70800,0x5d70800,0x5d70800,0x5d70800,0x5d70800,0x5d70800,0x5d70800,0x5d70800,0x5d70800,0x5d70800,0x5d70800,0x5d70800,301,1293,2101,3093,3901,4893,5701,6693,7685,8493,9485,10293,11285,12093,13085,13864,14654,15617,16407,17186,17943,17943,17943,17943,21,20,0,0,0,0,0,0,0x31ce00,0x845400,0xc4d880,0x1175e80,0x157e300,0x1aa6900,0x1eaed80,0x23d7380,0x28ff980,0x2d07e00,0x3230400,0x3638880,0x3b60e80,0x3f69300,0x4491900,0x4886d00,0x4c83480,0x5198a00,0x5595180,0x598a580,0x5d71280,0x5d71280,0x5d71280,0x5d71280,0x5d71280,0x5d71280,0x5d71280,0x5d71280,0x5d71280,0x5d71280,0x5d71280,0x5d71280,302,1294,2102,3094,3902,4894,5702,6694,7686,8494,9486,10294,11286,12094,13086,18461,14655,840,16408,12872,12872,12872,12872,12872,20,19,0,0,0,0,0,0,0x31d880,0x845e80,0xc4e300,0x1176900,0x157ed80,0x1aa7380,0x1eaf800,0x23d7e00,0x2900400,0x2d08880,0x3230e80,0x3639300,0x3b61900,0x3f69d80,0x4492380,0x73b00,0x4c83f00,0x59d600,0x5595c00,0x432e880,0x432e880,0x432e880,0x432e880,0x432e880,0x432e880,0x432e880,0x432e880,0x432e880,0x432e880,0x432e880,0x432e880,0x432e880,303,1295,2103,3095,3903,4895,5703,6695,7687,8495,9487,10295,11287,12095,13087,13865,14656,15618,16409,17187,17944,17944,17944,17944,21,20,0,0,0,0,0,0,0x31e300,0x846900,0xc4ed80,0x1177380,0x157f800,0x1aa7e00,0x1eb0280,0x23d8880,0x2900e80,0x2d09300,0x3231900,0x3639d80,0x3b62380,0x3f6a800,0x4492e00,0x4887780,0x4c84980,0x5199480,0x5596680,0x598b000,0x5d71d00,0x5d71d00,0x5d71d00,0x5d71d00,0x5d71d00,0x5d71d00,0x5d71d00,0x5d71d00,0x5d71d00,0x5d71d00,0x5d71d00,0x5d71d00,304,1296,2104,3096,3904,4896,5888,6696,7688,8496,9488,10296,11288,12096,13088,13866,14657,15619,16410,17188,17945,17945,17945,17945,21,20,0,0,0,0,0,0,0x31ed80,0x847380,0xc4f800,0x1177e00,0x1580280,0x1aa8880,0x1fd0e80,0x23d9300,0x2901900,0x2d09d80,0x3232380,0x363a800,0x3b62e00,0x3f6b280,0x4493880,0x4888200,0x4c85400,0x5199f00,0x5597100,0x598ba80,0x5d72780,0x5d72780,0x5d72780,0x5d72780,0x5d72780,0x5d72780,0x5d72780,0x5d72780,0x5d72780,0x5d72780,0x5d72780,0x5d72780,305,1297,2105,3097,3905,4897,5889,6697,7689,8497,9489,10297,11289,12097,13089,13867,14658,15620,16411,17189,17946,17946,17946,17946,21,20,0,0,0,0,0,0,0x31f800,0x847e00,0xc50280,0x1178880,0x1580d00,0x1aa9300,0x1fd1900,0x23d9d80,0x2902380,0x2d0a800,0x3232e00,0x363b280,0x3b63880,0x3f6bd00,0x4494300,0x4888c80,0x4c85e80,0x519a980,0x5597b80,0x598c500,0x5d73200,0x5d73200,0x5d73200,0x5d73200,0x5d73200,0x5d73200,0x5d73200,0x5d73200,0x5d73200,0x5d73200,0x5d73200,0x5d73200,306,1298,2106,3098,3906,4898,5890,6698,7690,8498,9490,10298,11290,12098,13090,18462,14659,1096,16412,13128,13128,13128,13128,13128,20,19,0,0,0,0,0,0,0x320280,0x848880,0xc50d00,0x1179300,0x1581780,0x1aa9d80,0x1fd2380,0x23da800,0x2902e00,0x2d0b280,0x3233880,0x363bd00,0x3b64300,0x3f6c780,0x4494d80,0x74580,0x4c86900,0x6ecb80,0x5598600,0x447de00,0x447de00,0x447de00,0x447de00,0x447de00,0x447de00,0x447de00,0x447de00,0x447de00,0x447de00,0x447de00,0x447de00,0x447de00,307,1299,2107,3099,3907,4899,5891,6699,7691,8499,9491,10299,11291,12099,13091,13868,14660,15621,16413,17190,17947,17947,17947,17947,21,20,0,0,0,0,0,0,0x320d00,0x849300,0xc51780,0x1179d80,0x1582200,0x1aaa800,0x1fd2e00,0x23db280,0x2903880,0x2d0bd00,0x3234300,0x363c780,0x3b64d80,0x3f6d200,0x4495800,0x4889700,0x4c87380,0x519b400,0x5599080,0x598cf80,0x5d73c80,0x5d73c80,0x5d73c80,0x5d73c80,0x5d73c80,0x5d73c80,0x5d73c80,0x5d73c80,0x5d73c80,0x5d73c80,0x5d73c80,0x5d73c80,308,1300,2108,3100,3908,4900,5892,6700,7692,8500,9492,10300,11292,12100,13092,13869,14661,15622,16414,17191,17948,17948,17948,17948,21,20,0,0,0,0,0,0,0x321780,0x849d80,0xc52200,0x117a800,0x1582c80,0x1aab280,0x1fd3880,0x23dbd00,0x2904300,0x2d0c780,0x3234d80,0x363d200,0x3b65800,0x3f6dc80,0x4496280,0x488a180,0x4c87e00,0x519be80,0x5599b00,0x598da00,0x5d74700,0x5d74700,0x5d74700,0x5d74700,0x5d74700,0x5d74700,0x5d74700,0x5d74700,0x5d74700,0x5d74700,0x5d74700,0x5d74700,309,1301,2109,3101,3909,4901,5893,6701,7693,8501,9493,10301,11293,12101,13093,13870,14662,15623,16415,17192,17949,17949,17949,17949,21,20,0,0,0,0,0,0,0x322200,0x84a800,0xc52c80,0x117b280,0x1583700,0x1aabd00,0x1fd4300,0x23dc780,0x2904d80,0x2d0d200,0x3235800,0x363dc80,0x3b66280,0x3f6e700,0x4496d00,0x488ac00,0x4c88880,0x519c900,0x559a580,0x598e480,0x5d75180,0x5d75180,0x5d75180,0x5d75180,0x5d75180,0x5d75180,0x5d75180,0x5d75180,0x5d75180,0x5d75180,0x5d75180,0x5d75180,310,1302,2110,3102,3910,4902,5894,6702,7694,8502,9494,10302,11294,12102,13094,13871,14663,15624,16416,17193,17950,17950,17950,17950,21,20,0,0,0,0,0,0,0x322c80,0x84b280,0xc53700,0x117bd00,0x1584180,0x1aac780,0x1fd4d80,0x23dd200,0x2905800,0x2d0dc80,0x3236280,0x363e700,0x3b66d00,0x3f6f180,0x4497780,0x488b680,0x4c89300,0x519d380,0x559b000,0x598ef00,0x5d75c00,0x5d75c00,0x5d75c00,0x5d75c00,0x5d75c00,0x5d75c00,0x5d75c00,0x5d75c00,0x5d75c00,0x5d75c00,0x5d75c00,0x5d75c00,311,1303,2111,3103,3911,4903,5895,6703,7695,8503,9495,10303,11295,12103,13095,13872,14848,15625,16417,17194,17951,17951,17951,17951,21,20,0,0,0,0,0,0,0x323700,0x84bd00,0xc54180,0x117c780,0x1584c00,0x1aad200,0x1fd5800,0x23ddc80,0x2906280,0x2d0e700,0x3236d00,0x363f180,0x3b67780,0x3f6fc00,0x4498200,0x488c100,0x4da9f00,0x519de00,0x559ba80,0x598f980,0x5d76680,0x5d76680,0x5d76680,0x5d76680,0x5d76680,0x5d76680,0x5d76680,0x5d76680,0x5d76680,0x5d76680,0x5d76680,0x5d76680,312,1304,2112,3104,4096,4904,5896,6704,7696,8504,9496,10304,11296,12288,13096,13873,14849,15626,16418,17195,17952,17952,17952,17952,21,20,0,0,0,0,0,0,0x324180,0x84c780,0xc54c00,0x117d200,0x16a5800,0x1aadc80,0x1fd6280,0x23de700,0x2906d00,0x2d0f180,0x3237780,0x363fc00,0x3b68200,0x4090800,0x4498c80,0x488cb80,0x4daa980,0x519e880,0x559c500,0x5990400,0x5d77100,0x5d77100,0x5d77100,0x5d77100,0x5d77100,0x5d77100,0x5d77100,0x5d77100,0x5d77100,0x5d77100,0x5d77100,0x5d77100,313,1305,2113,3105,4097,4905,5897,6705,7697,8505,9497,10305,11297,12289,13097,13874,14850,15627,16419,17196,17953,17953,17953,17953,21,20,0,0,0,0,0,0,0x324c00,0x84d200,0xc55680,0x117dc80,0x16a6280,0x1aae700,0x1fd6d00,0x23df180,0x2907780,0x2d0fc00,0x3238200,0x3640680,0x3b68c80,0x4091280,0x4499700,0x488d600,0x4dab400,0x519f300,0x559cf80,0x5990e80,0x5d77b80,0x5d77b80,0x5d77b80,0x5d77b80,0x5d77b80,0x5d77b80,0x5d77b80,0x5d77b80,0x5d77b80,0x5d77b80,0x5d77b80,0x5d77b80,314,1306,2114,3106,4098,4906,5898,6706,7698,8506,9498,10306,11298,12290,13098,18463,14851,1352,16420,13384,13384,13384,13384,13384,20,19,0,0,0,0,0,0,0x325680,0x84dc80,0xc56100,0x117e700,0x16a6d00,0x1aaf180,0x1fd7780,0x23dfc00,0x2908200,0x2d10680,0x3238c80,0x3641100,0x3b69700,0x4091d00,0x449a180,0x75000,0x4dabe80,0x83c100,0x559da00,0x45cd380,0x45cd380,0x45cd380,0x45cd380,0x45cd380,0x45cd380,0x45cd380,0x45cd380,0x45cd380,0x45cd380,0x45cd380,0x45cd380,0x45cd380,315,1307,2115,3107,4099,4907,5899,6707,7699,8507,9499,10307,11299,12291,13099,13875,14852,15628,16421,17197,17954,17954,17954,17954,21,20,0,0,0,0,0,0,0x326100,0x84e700,0xc56b80,0x117f180,0x16a7780,0x1aafc00,0x1fd8200,0x23e0680,0x2908c80,0x2d11100,0x3239700,0x3641b80,0x3b6a180,0x4092780,0x449ac00,0x488e080,0x4dac900,0x519fd80,0x559e480,0x5991900,0x5d78600,0x5d78600,0x5d78600,0x5d78600,0x5d78600,0x5d78600,0x5d78600,0x5d78600,0x5d78600,0x5d78600,0x5d78600,0x5d78600,316,1308,2116,3108,4100,4908,5900,6708,7700,8508,9500,10308,11300,12292,13100,13876,14853,15629,16422,17198,17955,17955,17955,17955,21,20,0,0,0,0,0,0,0x326b80,0x84f180,0xc57600,0x117fc00,0x16a8200,0x1ab0680,0x1fd8c80,0x23e1100,0x2909700,0x2d11b80,0x323a180,0x3642600,0x3b6ac00,0x4093200,0x449b680,0x488eb00,0x4dad380,0x51a0800,0x559ef00,0x5992380,0x5d79080,0x5d79080,0x5d79080,0x5d79080,0x5d79080,0x5d79080,0x5d79080,0x5d79080,0x5d79080,0x5d79080,0x5d79080,0x5d79080,317,1309,2117,3109,4101,4909,5901,6709,7701,8509,9501,10309,11301,12293,13101,13877,14854,15630,16423,17199,17956,17956,17956,17956,21,20,0,0,0,0,0,0,0x327600,0x84fc00,0xc58080,0x1180680,0x16a8c80,0x1ab1100,0x1fd9700,0x23e1b80,0x290a180,0x2d12600,0x323ac00,0x3643080,0x3b6b680,0x4093c80,0x449c100,0x488f580,0x4dade00,0x51a1280,0x559f980,0x5992e00,0x5d79b00,0x5d79b00,0x5d79b00,0x5d79b00,0x5d79b00,0x5d79b00,0x5d79b00,0x5d79b00,0x5d79b00,0x5d79b00,0x5d79b00,0x5d79b00,318,1310,2118,3110,4102,4910,5902,6710,7702,8510,9502,10310,11302,12294,13102,13878,14855,15631,16424,17200,17957,17957,17957,17957,21,20,0,0,0,0,0,0,0x328080,0x850680,0xc58b00,0x1181100,0x16a9700,0x1ab1b80,0x1fda180,0x23e2600,0x290ac00,0x2d13080,0x323b680,0x3643b00,0x3b6c100,0x4094700,0x449cb80,0x4890000,0x4dae880,0x51a1d00,0x55a0400,0x5993880,0x5d7a580,0x5d7a580,0x5d7a580,0x5d7a580,0x5d7a580,0x5d7a580,0x5d7a580,0x5d7a580,0x5d7a580,0x5d7a580,0x5d7a580,0x5d7a580,319,1311,2119,3111,4103,4911,5903,6711,7703,8511,9503,10311,11303,12295,13103,13879,14856,15632,16425,17201,17958,17958,17958,17958,21,20,0,0,0,0,0,0,0x328b00,0x851100,0xc59580,0x1181b80,0x16aa180,0x1ab2600,0x1fdac00,0x23e3080,0x290b680,0x2d13b00,0x323c100,0x3644580,0x3b6cb80,0x4095180,0x449d600,0x4890a80,0x4daf300,0x51a2780,0x55a0e80,0x5994300,0x5d7b000,0x5d7b000,0x5d7b000,0x5d7b000,0x5d7b000,0x5d7b000,0x5d7b000,0x5d7b000,0x5d7b000,0x5d7b000,0x5d7b000,0x5d7b000,320,1312,2304,3112,4104,4912,5904,6712,7704,8512,9504,10496,11304,12296,13104,13880,14857,15633,16426,17202,17959,17959,17959,17959,21,20,0,0,0,0,0,0,0x329580,0x851b80,0xd7a180,0x1182600,0x16aac00,0x1ab3080,0x1fdb680,0x23e3b00,0x290c100,0x2d14580,0x323cb80,0x3765180,0x3b6d600,0x4095c00,0x449e080,0x4891500,0x4dafd80,0x51a3200,0x55a1900,0x5994d80,0x5d7ba80,0x5d7ba80,0x5d7ba80,0x5d7ba80,0x5d7ba80,0x5d7ba80,0x5d7ba80,0x5d7ba80,0x5d7ba80,0x5d7ba80,0x5d7ba80,0x5d7ba80,321,1313,2305,3113,4105,4913,5905,6713,7705,8513,9505,10497,11305,12297,13105,13881,14858,15634,16427,17203,17960,17960,17960,17960,21,20,0,0,0,0,0,0,0x32a000,0x852600,0xd7ac00,0x1183080,0x16ab680,0x1ab3b00,0x1fdc100,0x23e4580,0x290cb80,0x2d15000,0x323d600,0x3765c00,0x3b6e080,0x4096680,0x449eb00,0x4891f80,0x4db0800,0x51a3c80,0x55a2380,0x5995800,0x5d7c500,0x5d7c500,0x5d7c500,0x5d7c500,0x5d7c500,0x5d7c500,0x5d7c500,0x5d7c500,0x5d7c500,0x5d7c500,0x5d7c500,0x5d7c500,322,1314,2306,3114,4106,4914,5906,6714,7706,8514,9506,10498,11306,12298,13106,18464,14859,1608,16428,13640,13640,13640,13640,13640,20,19,0,0,0,0,0,0,0x32aa80,0x853080,0xd7b680,0x1183b00,0x16ac100,0x1ab4580,0x1fdcb80,0x23e5000,0x290d600,0x2d15a80,0x323e080,0x3766680,0x3b6eb00,0x4097100,0x449f580,0x75a80,0x4db1280,0x98b680,0x55a2e00,0x471c900,0x471c900,0x471c900,0x471c900,0x471c900,0x471c900,0x471c900,0x471c900,0x471c900,0x471c900,0x471c900,0x471c900,0x471c900,323,1315,2307,3115,4107,4915,5907,6715,7707,8515,9507,10499,11307,12299,13107,13882,14860,15635,16429,17204,17961,17961,17961,17961,21,20,0,0,0,0,0,0,0x32b500,0x853b00,0xd7c100,0x1184580,0x16acb80,0x1ab5000,0x1fdd600,0x23e5a80,0x290e080,0x2d16500,0x323eb00,0x3767100,0x3b6f580,0x4097b80,0x44a0000,0x4892a00,0x4db1d00,0x51a4700,0x55a3880,0x5996280,0x5d7cf80,0x5d7cf80,0x5d7cf80,0x5d7cf80,0x5d7cf80,0x5d7cf80,0x5d7cf80,0x5d7cf80,0x5d7cf80,0x5d7cf80,0x5d7cf80,0x5d7cf80,324,1316,2308,3116,4108,4916,5908,6716,7708,8516,9508,10500,11308,12300,13108,13883,14861,15636,16430,17205,17962,17962,17962,17962,21,20,0,0,0,0,0,0,0x32bf80,0x854580,0xd7cb80,0x1185000,0x16ad600,0x1ab5a80,0x1fde080,0x23e6500,0x290eb00,0x2d16f80,0x323f580,0x3767b80,0x3b70000,0x4098600,0x44a0a80,0x4893480,0x4db2780,0x51a5180,0x55a4300,0x5996d00,0x5d7da00,0x5d7da00,0x5d7da00,0x5d7da00,0x5d7da00,0x5d7da00,0x5d7da00,0x5d7da00,0x5d7da00,0x5d7da00,0x5d7da00,0x5d7da00,325,1317,2309,3117,4109,4917,5909,6717,7709,8517,9509,10501,11309,12301,13109,13884,14862,15637,16431,17206,17963,17963,17963,17963,21,20,0,0,0,0,0,0,0x32ca00,0x855000,0xd7d600,0x1185a80,0x16ae080,0x1ab6500,0x1fdeb00,0x23e6f80,0x290f580,0x2d17a00,0x3240000,0x3768600,0x3b70a80,0x4099080,0x44a1500,0x4893f00,0x4db3200,0x51a5c00,0x55a4d80,0x5997780,0x5d7e480,0x5d7e480,0x5d7e480,0x5d7e480,0x5d7e480,0x5d7e480,0x5d7e480,0x5d7e480,0x5d7e480,0x5d7e480,0x5d7e480,0x5d7e480,326,1318,2310,3118,4110,4918,5910,6718,7710,8518,9510,10502,11310,12302,13110,13885,14863,15638,16432,17207,17964,17964,17964,17964,21,20,0,0,0,0,0,0,0x32d480,0x855a80,0xd7e080,0x1186500,0x16aeb00,0x1ab6f80,0x1fdf580,0x23e7a00,0x2910000,0x2d18480,0x3240a80,0x3769080,0x3b71500,0x4099b00,0x44a1f80,0x4894980,0x4db3c80,0x51a6680,0x55a5800,0x5998200,0x5d7ef00,0x5d7ef00,0x5d7ef00,0x5d7ef00,0x5d7ef00,0x5d7ef00,0x5d7ef00,0x5d7ef00,0x5d7ef00,0x5d7ef00,0x5d7ef00,0x5d7ef00,327,1319,2311,3119,4111,4919,5911,6719,7711,8519,9511,10503,11311,12303,13111,13886,14864,15639,16433,17208,17965,17965,17965,17965,21,20,0,0,0,0,0,0,0x32df00,0x856500,0xd7eb00,0x1186f80,0x16af580,0x1ab7a00,0x1fe0000,0x23e8480,0x2910a80,0x2d18f00,0x3241500,0x3769b00,0x3b71f80,0x409a580,0x44a2a00,0x4895400,0x4db4700,0x51a7100,0x55a6280,0x5998c80,0x5d7f980,0x5d7f980,0x5d7f980,0x5d7f980,0x5d7f980,0x5d7f980,0x5d7f980,0x5d7f980,0x5d7f980,0x5d7f980,0x5d7f980,0x5d7f980,512,1320,2312,3120,4112,4920,5912,6720,7712,8704,9512,10504,11312,12304,13112,13887,14865,15640,16434,17209,17966,17966,17966,17966,21,20,0,0,0,0,0,0,0x44eb00,0x856f80,0xd7f580,0x1187a00,0x16b0000,0x1ab8480,0x1fe0a80,0x23e8f00,0x2911500,0x2e39b00,0x3241f80,0x376a580,0x3b72a00,0x409b000,0x44a3480,0x4895e80,0x4db5180,0x51a7b80,0x55a6d00,0x5999700,0x5d80400,0x5d80400,0x5d80400,0x5d80400,0x5d80400,0x5d80400,0x5d80400,0x5d80400,0x5d80400,0x5d80400,0x5d80400,0x5d80400,513,1321,2313,3121,4113,4921,5913,6721,7713,8705,9513,10505,11313,12305,13113,13888,14866,15641,16435,17210,17967,17967,17967,17967,21,20,0,0,0,0,0,0,0x44f580,0x857a00,0xd80000,0x1188480,0x16b0a80,0x1ab8f00,0x1fe1500,0x23e9980,0x2911f80,0x2e3a580,0x3242a00,0x376b000,0x3b73480,0x409ba80,0x44a3f00,0x4896900,0x4db5c00,0x51a8600,0x55a7780,0x599a180,0x5d80e80,0x5d80e80,0x5d80e80,0x5d80e80,0x5d80e80,0x5d80e80,0x5d80e80,0x5d80e80,0x5d80e80,0x5d80e80,0x5d80e80,0x5d80e80,514,1322,2314,3122,4114,4922,5914,6722,7714,8706,9514,10506,11314,12306,13114,18465,14867,1864,16436,13896,13896,13896,13896,13896,20,19,0,0,0,0,0,0,0x450000,0x858480,0xd80a80,0x1188f00,0x16b1500,0x1ab9980,0x1fe1f80,0x23ea400,0x2912a00,0x2e3b000,0x3243480,0x376ba80,0x3b73f00,0x409c500,0x44a4980,0x76500,0x4db6680,0xadac00,0x55a8200,0x486be80,0x486be80,0x486be80,0x486be80,0x486be80,0x486be80,0x486be80,0x486be80,0x486be80,0x486be80,0x486be80,0x486be80,0x486be80,515,1323,2315,3123,4115,4923,5915,6723,7715,8707,9515,10507,11315,12307,13115,13889,14868,15642,16437,17211,17968,17968,17968,17968,21,20,0,0,0,0,0,0,0x450a80,0x858f00,0xd81500,0x1189980,0x16b1f80,0x1aba400,0x1fe2a00,0x23eae80,0x2913480,0x2e3ba80,0x3243f00,0x376c500,0x3b74980,0x409cf80,0x44a5400,0x4897380,0x4db7100,0x51a9080,0x55a8c80,0x599ac00,0x5d81900,0x5d81900,0x5d81900,0x5d81900,0x5d81900,0x5d81900,0x5d81900,0x5d81900,0x5d81900,0x5d81900,0x5d81900,0x5d81900,516,1324,2316,3124,4116,4924,5916,6724,7716,8708,9516,10508,11316,12308,13116,13890,14869,15643,16438,17212,17969,17969,17969,17969,21,20,0,0,0,0,0,0,0x451500,0x859980,0xd81f80,0x118a400,0x16b2a00,0x1abae80,0x1fe3480,0x23eb900,0x2913f00,0x2e3c500,0x3244980,0x376cf80,0x3b75400,0x409da00,0x44a5e80,0x4897e00,0x4db7b80,0x51a9b00,0x55a9700,0x599b680,0x5d82380,0x5d82380,0x5d82380,0x5d82380,0x5d82380,0x5d82380,0x5d82380,0x5d82380,0x5d82380,0x5d82380,0x5d82380,0x5d82380,517,1325,2317,3125,4117,4925,5917,6725,7717,8709,9517,10509,11317,12309,13117,13891,14870,15644,16439,17213,17970,17970,17970,17970,21,20,0,0,0,0,0,0,0x451f80,0x85a400,0xd82a00,0x118ae80,0x16b3480,0x1abb900,0x1fe3f00,0x23ec380,0x2914980,0x2e3cf80,0x3245400,0x376da00,0x3b75e80,0x409e480,0x44a6900,0x4898880,0x4db8600,0x51aa580,0x55aa180,0x599c100,0x5d82e00,0x5d82e00,0x5d82e00,0x5d82e00,0x5d82e00,0x5d82e00,0x5d82e00,0x5d82e00,0x5d82e00,0x5d82e00,0x5d82e00,0x5d82e00,518,1326,2318,3126,4118,4926,5918,6726,7718,8710,9518,10510,11318,12310,13118,13892,14871,15645,16440,17214,17971,17971,17971,17971,21,20,0,0,0,0,0,0,0x452a00,0x85ae80,0xd83480,0x118b900,0x16b3f00,0x1abc380,0x1fe4980,0x23ece00,0x2915400,0x2e3da00,0x3245e80,0x376e480,0x3b76900,0x409ef00,0x44a7380,0x4899300,0x4db9080,0x51ab000,0x55aac00,0x599cb80,0x5d83880,0x5d83880,0x5d83880,0x5d83880,0x5d83880,0x5d83880,0x5d83880,0x5d83880,0x5d83880,0x5d83880,0x5d83880,0x5d83880,519,1327,2319,3127,4119,4927,5919,6727,7719,8711,9519,10511,11319,12311,13119,13893,14872,15646,16441,17215,17972,17972,17972,17972,21,20,0,0,0,0,0,0,0x453480,0x85b900,0xd83f00,0x118c380,0x16b4980,0x1abce00,0x1fe5400,0x23ed880,0x2915e80,0x2e3e480,0x3246900,0x376ef00,0x3b77380,0x409f980,0x44a7e00,0x4899d80,0x4db9b00,0x51aba80,0x55ab680,0x599d600,0x5d84300,0x5d84300,0x5d84300,0x5d84300,0x5d84300,0x5d84300,0x5d84300,0x5d84300,0x5d84300,0x5d84300,0x5d84300,0x5d84300,520,1328,2320,3128,4120,4928,5920,6912,7720,8712,9520,10512,11320,12312,13120,13894,14873,15647,16442,17216,17973,17973,17973,17973,21,20,0,0,0,0,0,0,0x453f00,0x85c380,0xd84980,0x118ce00,0x16b5400,0x1abd880,0x1fe5e80,0x250e480,0x2916900,0x2e3ef00,0x3247380,0x376f980,0x3b77e00,0x40a0400,0x44a8880,0x489a800,0x4dba580,0x51ac500,0x55ac100,0x599e080,0x5d84d80,0x5d84d80,0x5d84d80,0x5d84d80,0x5d84d80,0x5d84d80,0x5d84d80,0x5d84d80,0x5d84d80,0x5d84d80,0x5d84d80,0x5d84d80,521,1329,2321,3129,4121,4929,5921,6913,7721,8713,9521,10513,11321,12313,13121,13895,14874,15648,16443,17217,17974,17974,17974,17974,21,20,0,0,0,0,0,0,0x454980,0x85ce00,0xd85400,0x118d880,0x16b5e80,0x1abe300,0x1fe6900,0x250ef00,0x2917380,0x2e3f980,0x3247e00,0x3770400,0x3b78880,0x40a0e80,0x44a9300,0x489b280,0x4dbb000,0x51acf80,0x55acb80,0x599eb00,0x5d85800,0x5d85800,0x5d85800,0x5d85800,0x5d85800,0x5d85800,0x5d85800,0x5d85800,0x5d85800,0x5d85800,0x5d85800,0x5d85800,522,1330,2322,3130,4122,4930,5922,6914,7722,8714,9522,10514,11322,12314,13122,18466,14875,2120,16444,14152,14152,14152,14152,14152,20,19,0,0,0,0,0,0,0x455400,0x85d880,0xd85e80,0x118e300,0x16b6900,0x1abed80,0x1fe7380,0x250f980,0x2917e00,0x2e40400,0x3248880,0x3770e80,0x3b79300,0x40a1900,0x44a9d80,0x76f80,0x4dbba80,0xc2a180,0x55ad600,0x49bb400,0x49bb400,0x49bb400,0x49bb400,0x49bb400,0x49bb400,0x49bb400,0x49bb400,0x49bb400,0x49bb400,0x49bb400,0x49bb400,0x49bb400,523,1331,2323,3131,4123,4931,5923,6915,7723,8715,9523,10515,11323,12315,13123,14080,14876,15649,16445,17218,17975,17975,17975,17975,21,20,0,0,0,0,0,0,0x455e80,0x85e300,0xd86900,0x118ed80,0x16b7380,0x1abf800,0x1fe7e00,0x2510400,0x2918880,0x2e40e80,0x3249300,0x3771900,0x3b79d80,0x40a2380,0x44aa800,0x49bbe80,0x4dbc500,0x51ada00,0x55ae080,0x599f580,0x5d86280,0x5d86280,0x5d86280,0x5d86280,0x5d86280,0x5d86280,0x5d86280,0x5d86280,0x5d86280,0x5d86280,0x5d86280,0x5d86280,524,1332,2324,3132,4124,4932,5924,6916,7724,8716,9524,10516,11324,12316,13124,14081,14877,15650,16446,17219,17976,17976,17976,17976,21,20,0,0,0,0,0,0,0x456900,0x85ed80,0xd87380,0x118f800,0x16b7e00,0x1ac0280,0x1fe8880,0x2510e80,0x2919300,0x2e41900,0x3249d80,0x3772380,0x3b7a800,0x40a2e00,0x44ab280,0x49bc900,0x4dbcf80,0x51ae480,0x55aeb00,0x59a0000,0x5d86d00,0x5d86d00,0x5d86d00,0x5d86d00,0x5d86d00,0x5d86d00,0x5d86d00,0x5d86d00,0x5d86d00,0x5d86d00,0x5d86d00,0x5d86d00,525,1333,2325,3133,4125,4933,5925,6917,7725,8717,9525,10517,11325,12317,13125,14082,14878,15651,16447,17220,17977,17977,17977,17977,21,20,0,0,0,0,0,0,0x457380,0x85f800,0xd87e00,0x1190280,0x16b8880,0x1ac0d00,0x1fe9300,0x2511900,0x2919d80,0x2e42380,0x324a800,0x3772e00,0x3b7b280,0x40a3880,0x44abd00,0x49bd380,0x4dbda00,0x51aef00,0x55af580,0x59a0a80,0x5d87780,0x5d87780,0x5d87780,0x5d87780,0x5d87780,0x5d87780,0x5d87780,0x5d87780,0x5d87780,0x5d87780,0x5d87780,0x5d87780,526,1334,2326,3134,4126,4934,5926,6918,7726,8718,9526,10518,11326,12318,13126,14083,14879,15652,16448,17221,17978,17978,17978,17978,21,20,0,0,0,0,0,0,0x457e00,0x860280,0xd88880,0x1190d00,0x16b9300,0x1ac1780,0x1fe9d80,0x2512380,0x291a800,0x2e42e00,0x324b280,0x3773880,0x3b7bd00,0x40a4300,0x44ac780,0x49bde00,0x4dbe480,0x51af980,0x55b0000,0x59a1500,0x5d88200,0x5d88200,0x5d88200,0x5d88200,0x5d88200,0x5d88200,0x5d88200,0x5d88200,0x5d88200,0x5d88200,0x5d88200,0x5d88200,527,1335,2327,3135,4127,4935,5927,6919,7727,8719,9527,10519,11327,12319,13127,14084,14880,15653,16449,17222,17979,17979,17979,17979,21,20,0,0,0,0,0,0,0x458880,0x860d00,0xd89300,0x1191780,0x16b9d80,0x1ac2200,0x1fea800,0x2512e00,0x291b280,0x2e43880,0x324bd00,0x3774300,0x3b7c780,0x40a4d80,0x44ad200,0x49be880,0x4dbef00,0x51b0400,0x55b0a80,0x59a1f80,0x5d88c80,0x5d88c80,0x5d88c80,0x5d88c80,0x5d88c80,0x5d88c80,0x5d88c80,0x5d88c80,0x5d88c80,0x5d88c80,0x5d88c80,0x5d88c80,528,1336,2328,3136,4128,5120,5928,6920,7728,8720,9528,10520,11328,12320,13312,14085,14881,15654,16450,17223,17980,17980,17980,17980,21,20,0,0,0,0,0,0,0x459300,0x861780,0xd89d80,0x1192200,0x16ba800,0x1be2e00,0x1feb280,0x2513880,0x291bd00,0x2e44300,0x324c780,0x3774d80,0x3b7d200,0x40a5800,0x45cde00,0x49bf300,0x4dbf980,0x51b0e80,0x55b1500,0x59a2a00,0x5d89700,0x5d89700,0x5d89700,0x5d89700,0x5d89700,0x5d89700,0x5d89700,0x5d89700,0x5d89700,0x5d89700,0x5d89700,0x5d89700,529,1337,2329,3137,4129,5121,5929,6921,7729,8721,9529,10521,11329,12321,13313,14086,14882,15655,16451,17408,17981,17981,17981,17981,21,20,0,0,0,0,0,0,0x459d80,0x862200,0xd8a800,0x1192c80,0x16bb280,0x1be3880,0x1febd00,0x2514300,0x291c780,0x2e44d80,0x324d200,0x3775800,0x3b7dc80,0x40a6280,0x45ce880,0x49bfd80,0x4dc0400,0x51b1900,0x55b1f80,0x5ac3600,0x5d8a180,0x5d8a180,0x5d8a180,0x5d8a180,0x5d8a180,0x5d8a180,0x5d8a180,0x5d8a180,0x5d8a180,0x5d8a180,0x5d8a180,0x5d8a180,530,1338,2330,3138,4130,5122,5930,6922,7730,8722,9530,10522,11330,12322,13314,18467,14883,2376,16452,14408,14408,14408,14408,14408,20,19,0,0,0,0,0,0,0x45a800,0x862c80,0xd8b280,0x1193700,0x16bbd00,0x1be4300,0x1fec780,0x2514d80,0x291d200,0x2e45800,0x324dc80,0x3776280,0x3b7e700,0x40a6d00,0x45cf300,0x77a00,0x4dc0e80,0xd79700,0x55b2a00,0x4b0a980,0x4b0a980,0x4b0a980,0x4b0a980,0x4b0a980,0x4b0a980,0x4b0a980,0x4b0a980,0x4b0a980,0x4b0a980,0x4b0a980,0x4b0a980,0x4b0a980,531,1339,2331,3139,4131,5123,5931,6923,7731,8723,9531,10523,11331,12323,13315,14087,14884,15656,16453,17409,17982,17982,17982,17982,21,20,0,0,0,0,0,0,0x45b280,0x863700,0xd8bd00,0x1194180,0x16bc780,0x1be4d80,0x1fed200,0x2515800,0x291dc80,0x2e46280,0x324e700,0x3776d00,0x3b7f180,0x40a7780,0x45cfd80,0x49c0800,0x4dc1900,0x51b2380,0x55b3480,0x5ac4080,0x5d8ac00,0x5d8ac00,0x5d8ac00,0x5d8ac00,0x5d8ac00,0x5d8ac00,0x5d8ac00,0x5d8ac00,0x5d8ac00,0x5d8ac00,0x5d8ac00,0x5d8ac00,532,1340,2332,3140,4132,5124,5932,6924,7732,8724,9532,10524,11332,12324,13316,14088,14885,15657,16454,17410,17983,17983,17983,17983,21,20,0,0,0,0,0,0,0x45bd00,0x864180,0xd8c780,0x1194c00,0x16bd200,0x1be5800,0x1fedc80,0x2516280,0x291e700,0x2e46d00,0x324f180,0x3777780,0x3b7fc00,0x40a8200,0x45d0800,0x49c1280,0x4dc2380,0x51b2e00,0x55b3f00,0x5ac4b00,0x5d8b680,0x5d8b680,0x5d8b680,0x5d8b680,0x5d8b680,0x5d8b680,0x5d8b680,0x5d8b680,0x5d8b680,0x5d8b680,0x5d8b680,0x5d8b680,533,1341,2333,3141,4133,5125,5933,6925,7733,8725,9533,10525,11333,12325,13317,14089,14886,15658,16455,17411,17984,17984,17984,17984,21,20,0,0,0,0,0,0,0x45c780,0x864c00,0xd8d200,0x1195680,0x16bdc80,0x1be6280,0x1fee700,0x2516d00,0x291f180,0x2e47780,0x324fc00,0x3778200,0x3b80680,0x40a8c80,0x45d1280,0x49c1d00,0x4dc2e00,0x51b3880,0x55b4980,0x5ac5580,0x5d8c100,0x5d8c100,0x5d8c100,0x5d8c100,0x5d8c100,0x5d8c100,0x5d8c100,0x5d8c100,0x5d8c100,0x5d8c100,0x5d8c100,0x5d8c100,534,1342,2334,3142,4134,5126,5934,6926,7734,8726,9534,10526,11334,12326,13318,14090,14887,15659,16640,17412,17985,17985,17985,17985,21,20,0,0,0,0,0,0,0x45d200,0x865680,0xd8dc80,0x1196100,0x16be700,0x1be6d00,0x1fef180,0x2517780,0x291fc00,0x2e48200,0x3250680,0x3778c80,0x3b81100,0x40a9700,0x45d1d00,0x49c2780,0x4dc3880,0x51b4300,0x56d5580,0x5ac6000,0x5d8cb80,0x5d8cb80,0x5d8cb80,0x5d8cb80,0x5d8cb80,0x5d8cb80,0x5d8cb80,0x5d8cb80,0x5d8cb80,0x5d8cb80,0x5d8cb80,0x5d8cb80,535,1343,2335,3143,4135,5127,5935,6927,7735,8727,9535,10527,11335,12327,13319,14091,14888,15660,16641,17413,17986,17986,17986,17986,21,20,0,0,0,0,0,0,0x45dc80,0x866100,0xd8e700,0x1196b80,0x16bf180,0x1be7780,0x1fefc00,0x2518200,0x2920680,0x2e48c80,0x3251100,0x3779700,0x3b81b80,0x40aa180,0x45d2780,0x49c3200,0x4dc4300,0x51b4d80,0x56d6000,0x5ac6a80,0x5d8d600,0x5d8d600,0x5d8d600,0x5d8d600,0x5d8d600,0x5d8d600,0x5d8d600,0x5d8d600,0x5d8d600,0x5d8d600,0x5d8d600,0x5d8d600,536,1344,2336,3328,4136,5128,5936,6928,7736,8728,9536,10528,11520,12328,13320,14092,14889,15661,16642,17414,17987,17987,17987,17987,21,20,0,0,0,0,0,0,0x45e700,0x866b80,0xd8f180,0x12b7780,0x16bfc00,0x1be8200,0x1ff0680,0x2518c80,0x2921100,0x2e49700,0x3251b80,0x377a180,0x3ca2780,0x40aac00,0x45d3200,0x49c3c80,0x4dc4d80,0x51b5800,0x56d6a80,0x5ac7500,0x5d8e080,0x5d8e080,0x5d8e080,0x5d8e080,0x5d8e080,0x5d8e080,0x5d8e080,0x5d8e080,0x5d8e080,0x5d8e080,0x5d8e080,0x5d8e080,537,1345,2337,3329,4137,5129,5937,6929,7737,8729,9537,10529,11521,12329,13321,14093,14890,15662,16643,17415,17988,17988,17988,17988,21,20,0,0,0,0,0,0,0x45f180,0x867600,0xd8fc00,0x12b8200,0x16c0680,0x1be8c80,0x1ff1100,0x2519700,0x2921b80,0x2e4a180,0x3252600,0x377ac00,0x3ca3200,0x40ab680,0x45d3c80,0x49c4700,0x4dc5800,0x51b6280,0x56d7500,0x5ac7f80,0x5d8eb00,0x5d8eb00,0x5d8eb00,0x5d8eb00,0x5d8eb00,0x5d8eb00,0x5d8eb00,0x5d8eb00,0x5d8eb00,0x5d8eb00,0x5d8eb00,0x5d8eb00,538,1346,2338,3330,4138,5130,5938,6930,7738,8730,9538,10530,11522,12330,13322,18468,14891,2632,16644,14664,14664,14664,14664,14664,20,19,0,0,0,0,0,0,0x45fc00,0x868080,0xd90680,0x12b8c80,0x16c1100,0x1be9700,0x1ff1b80,0x251a180,0x2922600,0x2e4ac00,0x3253080,0x377b680,0x3ca3c80,0x40ac100,0x45d4700,0x78480,0x4dc6280,0xec8c80,0x56d7f80,0x4c59f00,0x4c59f00,0x4c59f00,0x4c59f00,0x4c59f00,0x4c59f00,0x4c59f00,0x4c59f00,0x4c59f00,0x4c59f00,0x4c59f00,0x4c59f00,0x4c59f00,539,1347,2339,3331,4139,5131,5939,6931,7739,8731,9539,10531,11523,12331,13323,14094,14892,15663,16645,17416,17989,17989,17989,17989,21,20,0,0,0,0,0,0,0x460680,0x868b00,0xd91100,0x12b9700,0x16c1b80,0x1bea180,0x1ff2600,0x251ac00,0x2923080,0x2e4b680,0x3253b00,0x377c100,0x3ca4700,0x40acb80,0x45d5180,0x49c5180,0x4dc6d00,0x51b6d00,0x56d8a00,0x5ac8a00,0x5d8f580,0x5d8f580,0x5d8f580,0x5d8f580,0x5d8f580,0x5d8f580,0x5d8f580,0x5d8f580,0x5d8f580,0x5d8f580,0x5d8f580,0x5d8f580,540,1348,2340,3332,4140,5132,5940,6932,7740,8732,9540,10532,11524,12332,13324,14095,14893,15664,16646,17417,17990,17990,17990,17990,21,20,0,0,0,0,0,0,0x461100,0x869580,0xd91b80,0x12ba180,0x16c2600,0x1beac00,0x1ff3080,0x251b680,0x2923b00,0x2e4c100,0x3254580,0x377cb80,0x3ca5180,0x40ad600,0x45d5c00,0x49c5c00,0x4dc7780,0x51b7780,0x56d9480,0x5ac9480,0x5d90000,0x5d90000,0x5d90000,0x5d90000,0x5d90000,0x5d90000,0x5d90000,0x5d90000,0x5d90000,0x5d90000,0x5d90000,0x5d90000,541,1349,2341,3333,4141,5133,5941,6933,7741,8733,9541,10533,11525,12333,13325,14096,14894,15665,16647,17418,17991,17991,17991,17991,21,20,0,0,0,0,0,0,0x461b80,0x86a000,0xd92600,0x12bac00,0x16c3080,0x1beb680,0x1ff3b00,0x251c100,0x2924580,0x2e4cb80,0x3255000,0x377d600,0x3ca5c00,0x40ae080,0x45d6680,0x49c6680,0x4dc8200,0x51b8200,0x56d9f00,0x5ac9f00,0x5d90a80,0x5d90a80,0x5d90a80,0x5d90a80,0x5d90a80,0x5d90a80,0x5d90a80,0x5d90a80,0x5d90a80,0x5d90a80,0x5d90a80,0x5d90a80,542,1350,2342,3334,4142,5134,5942,6934,7742,8734,9542,10534,11526,12334,13326,14097,14895,15666,16648,17419,18176,18176,18176,18176,21,20,0,0,0,0,0,0,0x462600,0x86aa80,0xd93080,0x12bb680,0x16c3b00,0x1bec100,0x1ff4580,0x251cb80,0x2925000,0x2e4d600,0x3255a80,0x377e080,0x3ca6680,0x40aeb00,0x45d7100,0x49c7100,0x4dc8c80,0x51b8c80,0x56da980,0x5aca980,0x5eb1680,0x5eb1680,0x5eb1680,0x5eb1680,0x5eb1680,0x5eb1680,0x5eb1680,0x5eb1680,0x5eb1680,0x5eb1680,0x5eb1680,0x5eb1680,543,1351,2343,3335,4143,5135,5943,6935,7743,8735,9543,10535,11527,12335,13327,14098,14896,15667,16649,17420,18177,18177,18177,18177,21,20,0,0,0,0,0,0,0x463080,0x86b500,0xd93b00,0x12bc100,0x16c4580,0x1becb80,0x1ff5000,0x251d600,0x2925a80,0x2e4e080,0x3256500,0x377eb00,0x3ca7100,0x40af580,0x45d7b80,0x49c7b80,0x4dc9700,0x51b9700,0x56db400,0x5acb400,0x5eb2100,0x5eb2100,0x5eb2100,0x5eb2100,0x5eb2100,0x5eb2100,0x5eb2100,0x5eb2100,0x5eb2100,0x5eb2100,0x5eb2100,0x5eb2100,544,1536,2344,3336,4144,5136,5944,6936,7744,8736,9728,10536,11528,12336,13328,14099,14897,15668,16650,17421,18178,18178,18178,18178,21,20,0,0,0,0,0,0,0x463b00,0x98c100,0xd94580,0x12bcb80,0x16c5000,0x1bed600,0x1ff5a80,0x251e080,0x2926500,0x2e4eb00,0x3377100,0x377f580,0x3ca7b80,0x40b0000,0x45d8600,0x49c8600,0x4dca180,0x51ba180,0x56dbe80,0x5acbe80,0x5eb2b80,0x5eb2b80,0x5eb2b80,0x5eb2b80,0x5eb2b80,0x5eb2b80,0x5eb2b80,0x5eb2b80,0x5eb2b80,0x5eb2b80,0x5eb2b80,0x5eb2b80,545,1537,2345,3337,4145,5137,5945,6937,7745,8737,9729,10537,11529,12337,13329,14100,14898,15669,16651,17422,18179,18179,18179,18179,21,20,0,0,0,0,0,0,0x464580,0x98cb80,0xd95000,0x12bd600,0x16c5a80,0x1bee080,0x1ff6500,0x251eb00,0x2926f80,0x2e4f580,0x3377b80,0x3780000,0x3ca8600,0x40b0a80,0x45d9080,0x49c9080,0x4dcac00,0x51bac00,0x56dc900,0x5acc900,0x5eb3600,0x5eb3600,0x5eb3600,0x5eb3600,0x5eb3600,0x5eb3600,0x5eb3600,0x5eb3600,0x5eb3600,0x5eb3600,0x5eb3600,0x5eb3600,546,1538,2346,3338,4146,5138,5946,6938,7746,8738,9730,10538,11530,12338,13330,18469,14899,2888,16652,14920,14920,14920,14920,14920,20,19,0,0,0,0,0,0,0x465000,0x98d600,0xd95a80,0x12be080,0x16c6500,0x1beeb00,0x1ff6f80,0x251f580,0x2927a00,0x2e50000,0x3378600,0x3780a80,0x3ca9080,0x40b1500,0x45d9b00,0x78f00,0x4dcb680,0x1018200,0x56dd380,0x4da9480,0x4da9480,0x4da9480,0x4da9480,0x4da9480,0x4da9480,0x4da9480,0x4da9480,0x4da9480,0x4da9480,0x4da9480,0x4da9480,0x4da9480,547,1539,2347,3339,4147,5139,5947,6939,7747,8739,9731,10539,11531,12339,13331,14101,14900,15670,16653,17423,18180,18180,18180,18180,21,20,0,0,0,0,0,0,0x465a80,0x98e080,0xd96500,0x12beb00,0x16c6f80,0x1bef580,0x1ff7a00,0x2520000,0x2928480,0x2e50a80,0x3379080,0x3781500,0x3ca9b00,0x40b1f80,0x45da580,0x49c9b00,0x4dcc100,0x51bb680,0x56dde00,0x5acd380,0x5eb4080,0x5eb4080,0x5eb4080,0x5eb4080,0x5eb4080,0x5eb4080,0x5eb4080,0x5eb4080,0x5eb4080,0x5eb4080,0x5eb4080,0x5eb4080,548,1540,2348,3340,4148,5140,5948,6940,7748,8740,9732,10540,11532,12340,13332,14102,14901,15671,16654,17424,18181,18181,18181,18181,21,20,0,0,0,0,0,0,0x466500,0x98eb00,0xd96f80,0x12bf580,0x16c7a00,0x1bf0000,0x1ff8480,0x2520a80,0x2928f00,0x2e51500,0x3379b00,0x3781f80,0x3caa580,0x40b2a00,0x45db000,0x49ca580,0x4dccb80,0x51bc100,0x56de880,0x5acde00,0x5eb4b00,0x5eb4b00,0x5eb4b00,0x5eb4b00,0x5eb4b00,0x5eb4b00,0x5eb4b00,0x5eb4b00,0x5eb4b00,0x5eb4b00,0x5eb4b00,0x5eb4b00,549,1541,2349,3341,4149,5141,5949,6941,7749,8741,9733,10541,11533,12341,13333,14103,14902,15672,16655,17425,18182,18182,18182,18182,21,20,0,0,0,0,0,0,0x466f80,0x98f580,0xd97a00,0x12c0000,0x16c8480,0x1bf0a80,0x1ff8f00,0x2521500,0x2929980,0x2e51f80,0x337a580,0x3782a00,0x3cab000,0x40b3480,0x45dba80,0x49cb000,0x4dcd600,0x51bcb80,0x56df300,0x5ace880,0x5eb5580,0x5eb5580,0x5eb5580,0x5eb5580,0x5eb5580,0x5eb5580,0x5eb5580,0x5eb5580,0x5eb5580,0x5eb5580,0x5eb5580,0x5eb5580,550,1542,2350,3342,4150,5142,5950,6942,7750,8742,9734,10542,11534,12342,13334,14104,14903,15673,16656,17426,18183,18183,18183,18183,21,20,0,0,0,0,0,0,0x467a00,0x990000,0xd98480,0x12c0a80,0x16c8f00,0x1bf1500,0x1ff9980,0x2521f80,0x292a400,0x2e52a00,0x337b000,0x3783480,0x3caba80,0x40b3f00,0x45dc500,0x49cba80,0x4dce080,0x51bd600,0x56dfd80,0x5acf300,0x5eb6000,0x5eb6000,0x5eb6000,0x5eb6000,0x5eb6000,0x5eb6000,0x5eb6000,0x5eb6000,0x5eb6000,0x5eb6000,0x5eb6000,0x5eb6000,551,1543,2351,3343,4151,5143,5951,6943,7751,8743,9735,10543,11535,12343,13335,14105,14904,15674,16657,17427,18184,18184,18184,18184,21,20,0,0,0,0,0,0,0x468480,0x990a80,0xd98f00,0x12c1500,0x16c9980,0x1bf1f80,0x1ffa400,0x2522a00,0x292ae80,0x2e53480,0x337ba80,0x3783f00,0x3cac500,0x40b4980,0x45dcf80,0x49cc500,0x4dceb00,0x51be080,0x56e0800,0x5acfd80,0x5eb6a80,0x5eb6a80,0x5eb6a80,0x5eb6a80,0x5eb6a80,0x5eb6a80,0x5eb6a80,0x5eb6a80,0x5eb6a80,0x5eb6a80,0x5eb6a80,0x5eb6a80,552,1544,2352,3344,4152,5144,5952,6944,7936,8744,9736,10544,11536,12344,13336,14106,14905,15675,16658,17428,18185,18185,18185,18185,21,20,0,0,0,0,0,0,0x468f00,0x991500,0xd99980,0x12c1f80,0x16ca400,0x1bf2a00,0x1ffae80,0x2523480,0x2a4ba80,0x2e53f00,0x337c500,0x3784980,0x3cacf80,0x40b5400,0x45dda00,0x49ccf80,0x4dcf580,0x51beb00,0x56e1280,0x5ad0800,0x5eb7500,0x5eb7500,0x5eb7500,0x5eb7500,0x5eb7500,0x5eb7500,0x5eb7500,0x5eb7500,0x5eb7500,0x5eb7500,0x5eb7500,0x5eb7500,553,1545,2353,3345,4153,5145,5953,6945,7937,8745,9737,10545,11537,12345,13337,14107,14906,15676,16659,17429,18186,18186,18186,18186,21,20,0,0,0,0,0,0,0x469980,0x991f80,0xd9a400,0x12c2a00,0x16cae80,0x1bf3480,0x1ffb900,0x2523f00,0x2a4c500,0x2e54980,0x337cf80,0x3785400,0x3cada00,0x40b5e80,0x45de480,0x49cda00,0x4dd0000,0x51bf580,0x56e1d00,0x5ad1280,0x5eb7f80,0x5eb7f80,0x5eb7f80,0x5eb7f80,0x5eb7f80,0x5eb7f80,0x5eb7f80,0x5eb7f80,0x5eb7f80,0x5eb7f80,0x5eb7f80,0x5eb7f80,554,1546,2354,3346,4154,5146,5954,6946,7938,8746,9738,10546,11538,12346,13338,18470,14907,3144,16660,15176,15176,15176,15176,15176,20,19,0,0,0,0,0,0,0x46a400,0x992a00,0xd9ae80,0x12c3480,0x16cb900,0x1bf3f00,0x1ffc380,0x2524980,0x2a4cf80,0x2e55400,0x337da00,0x3785e80,0x3cae480,0x40b6900,0x45def00,0x79980,0x4dd0a80,0x1167780,0x56e2780,0x4ef8a00,0x4ef8a00,0x4ef8a00,0x4ef8a00,0x4ef8a00,0x4ef8a00,0x4ef8a00,0x4ef8a00,0x4ef8a00,0x4ef8a00,0x4ef8a00,0x4ef8a00,0x4ef8a00,555,1547,2355,3347,4155,5147,5955,6947,7939,8747,9739,10547,11539,12347,13339,14108,14908,15677,16661,17430,18187,18187,18187,18187,21,20,0,0,0,0,0,0,0x46ae80,0x993480,0xd9b900,0x12c3f00,0x16cc380,0x1bf4980,0x1ffce00,0x2525400,0x2a4da00,0x2e55e80,0x337e480,0x3786900,0x3caef00,0x40b7380,0x45df980,0x49ce480,0x4dd1500,0x51c0000,0x56e3200,0x5ad1d00,0x5eb8a00,0x5eb8a00,0x5eb8a00,0x5eb8a00,0x5eb8a00,0x5eb8a00,0x5eb8a00,0x5eb8a00,0x5eb8a00,0x5eb8a00,0x5eb8a00,0x5eb8a00,556,1548,2356,3348,4156,5148,5956,6948,7940,8748,9740,10548,11540,12348,13340,14109,14909,15678,16662,17431,18188,18188,18188,18188,21,20,0,0,0,0,0,0,0x46b900,0x993f00,0xd9c380,0x12c4980,0x16cce00,0x1bf5400,0x1ffd880,0x2525e80,0x2a4e480,0x2e56900,0x337ef00,0x3787380,0x3caf980,0x40b7e00,0x45e0400,0x49cef00,0x4dd1f80,0x51c0a80,0x56e3c80,0x5ad2780,0x5eb9480,0x5eb9480,0x5eb9480,0x5eb9480,0x5eb9480,0x5eb9480,0x5eb9480,0x5eb9480,0x5eb9480,0x5eb9480,0x5eb9480,0x5eb9480,557,1549,2357,3349,4157,5149,5957,6949,7941,8749,9741,10549,11541,12349,13341,14110,14910,15679,16663,17432,18189,18189,18189,18189,21,20,0,0,0,0,0,0,0x46c380,0x994980,0xd9ce00,0x12c5400,0x16cd880,0x1bf5e80,0x1ffe300,0x2526900,0x2a4ef00,0x2e57380,0x337f980,0x3787e00,0x3cb0400,0x40b8880,0x45e0e80,0x49cf980,0x4dd2a00,0x51c1500,0x56e4700,0x5ad3200,0x5eb9f00,0x5eb9f00,0x5eb9f00,0x5eb9f00,0x5eb9f00,0x5eb9f00,0x5eb9f00,0x5eb9f00,0x5eb9f00,0x5eb9f00,0x5eb9f00,0x5eb9f00,558,1550,2358,3350,4158,5150,5958,6950,7942,8750,9742,10550,11542,12350,13342,14111,14911,15680,16664,17433,18190,18190,18190,18190,21,20,0,0,0,0,0,0,0x46ce00,0x995400,0xd9d880,0x12c5e80,0x16ce300,0x1bf6900,0x1ffed80,0x2527380,0x2a4f980,0x2e57e00,0x3380400,0x3788880,0x3cb0e80,0x40b9300,0x45e1900,0x49d0400,0x4dd3480,0x51c1f80,0x56e5180,0x5ad3c80,0x5eba980,0x5eba980,0x5eba980,0x5eba980,0x5eba980,0x5eba980,0x5eba980,0x5eba980,0x5eba980,0x5eba980,0x5eba980,0x5eba980,559,1551,2359,3351,4159,5151,5959,6951,7943,8751,9743,10551,11543,12351,13343,14112,14912,15681,16665,17434,18191,18191,18191,18191,21,20,0,0,0,0,0,0,0x46d880,0x995e80,0xd9e300,0x12c6900,0x16ced80,0x1bf7380,0x1fff800,0x2527e00,0x2a50400,0x2e58880,0x3380e80,0x3789300,0x3cb1900,0x40b9d80,0x45e2380,0x49d0e80,0x4dd3f00,0x51c2a00,0x56e5c00,0x5ad4700,0x5ebb400,0x5ebb400,0x5ebb400,0x5ebb400,0x5ebb400,0x5ebb400,0x5ebb400,0x5ebb400,0x5ebb400,0x5ebb400,0x5ebb400,0x5ebb400,560,1552,2360,3352,4160,5152,6144,6952,7944,8752,9744,10552,11544,12352,13344,14113,14913,15682,16666,17435,18192,18192,18192,18192,21,20,0,0,0,0,0,0,0x46e300,0x996900,0xd9ed80,0x12c7380,0x16cf800,0x1bf7e00,0x2120400,0x2528880,0x2a50e80,0x2e59300,0x3381900,0x3789d80,0x3cb2380,0x40ba800,0x45e2e00,0x49d1900,0x4dd4980,0x51c3480,0x56e6680,0x5ad5180,0x5ebbe80,0x5ebbe80,0x5ebbe80,0x5ebbe80,0x5ebbe80,0x5ebbe80,0x5ebbe80,0x5ebbe80,0x5ebbe80,0x5ebbe80,0x5ebbe80,0x5ebbe80,561,1553,2361,3353,4161,5153,6145,6953,7945,8753,9745,10553,11545,12353,13345,14114,14914,15683,16667,17436,18193,18193,18193,18193,21,20,0,0,0,0,0,0,0x46ed80,0x997380,0xd9f800,0x12c7e00,0x16d0280,0x1bf8880,0x2120e80,0x2529300,0x2a51900,0x2e59d80,0x3382380,0x378a800,0x3cb2e00,0x40bb280,0x45e3880,0x49d2380,0x4dd5400,0x51c3f00,0x56e7100,0x5ad5c00,0x5ebc900,0x5ebc900,0x5ebc900,0x5ebc900,0x5ebc900,0x5ebc900,0x5ebc900,0x5ebc900,0x5ebc900,0x5ebc900,0x5ebc900,0x5ebc900,562,1554,2362,3354,4162,5154,6146,6954,7946,8754,9746,10554,11546,12354,13346,18471,14915,3400,16668,15432,15432,15432,15432,15432,20,19,0,0,0,0,0,0,0x46f800,0x997e00,0xda0280,0x12c8880,0x16d0d00,0x1bf9300,0x2121900,0x2529d80,0x2a52380,0x2e5a800,0x3382e00,0x378b280,0x3cb3880,0x40bbd00,0x45e4300,0x7a400,0x4dd5e80,0x12b6d00,0x56e7b80,0x5047f80,0x5047f80,0x5047f80,0x5047f80,0x5047f80,0x5047f80,0x5047f80,0x5047f80,0x5047f80,0x5047f80,0x5047f80,0x5047f80,0x5047f80,563,1555,2363,3355,4163,5155,6147,6955,7947,8755,9747,10555,11547,12355,13347,14115,14916,15684,16669,17437,18194,18194,18194,18194,21,20,0,0,0,0,0,0,0x470280,0x998880,0xda0d00,0x12c9300,0x16d1780,0x1bf9d80,0x2122380,0x252a800,0x2a52e00,0x2e5b280,0x3383880,0x378bd00,0x3cb4300,0x40bc780,0x45e4d80,0x49d2e00,0x4dd6900,0x51c4980,0x56e8600,0x5ad6680,0x5ebd380,0x5ebd380,0x5ebd380,0x5ebd380,0x5ebd380,0x5ebd380,0x5ebd380,0x5ebd380,0x5ebd380,0x5ebd380,0x5ebd380,0x5ebd380,564,1556,2364,3356,4164,5156,6148,6956,7948,8756,9748,10556,11548,12356,13348,14116,14917,15685,16670,17438,18195,18195,18195,18195,21,20,0,0,0,0,0,0,0x470d00,0x999300,0xda1780,0x12c9d80,0x16d2200,0x1bfa800,0x2122e00,0x252b280,0x2a53880,0x2e5bd00,0x3384300,0x378c780,0x3cb4d80,0x40bd200,0x45e5800,0x49d3880,0x4dd7380,0x51c5400,0x56e9080,0x5ad7100,0x5ebde00,0x5ebde00,0x5ebde00,0x5ebde00,0x5ebde00,0x5ebde00,0x5ebde00,0x5ebde00,0x5ebde00,0x5ebde00,0x5ebde00,0x5ebde00,565,1557,2365,3357,4165,5157,6149,6957,7949,8757,9749,10557,11549,12357,13349,14117,14918,15686,16671,17439,18196,18196,18196,18196,21,20,0,0,0,0,0,0,0x471780,0x999d80,0xda2200,0x12ca800,0x16d2c80,0x1bfb280,0x2123880,0x252bd00,0x2a54300,0x2e5c780,0x3384d80,0x378d200,0x3cb5800,0x40bdc80,0x45e6280,0x49d4300,0x4dd7e00,0x51c5e80,0x56e9b00,0x5ad7b80,0x5ebe880,0x5ebe880,0x5ebe880,0x5ebe880,0x5ebe880,0x5ebe880,0x5ebe880,0x5ebe880,0x5ebe880,0x5ebe880,0x5ebe880,0x5ebe880,566,1558,2366,3358,4166,5158,6150,6958,7950,8758,9750,10558,11550,12358,13350,14118,14919,15687,16672,17440,18197,18197,18197,18197,21,20,0,0,0,0,0,0,0x472200,0x99a800,0xda2c80,0x12cb280,0x16d3700,0x1bfbd00,0x2124300,0x252c780,0x2a54d80,0x2e5d200,0x3385800,0x378dc80,0x3cb6280,0x40be700,0x45e6d00,0x49d4d80,0x4dd8880,0x51c6900,0x56ea580,0x5ad8600,0x5ebf300,0x5ebf300,0x5ebf300,0x5ebf300,0x5ebf300,0x5ebf300,0x5ebf300,0x5ebf300,0x5ebf300,0x5ebf300,0x5ebf300,0x5ebf300,567,1559,2367,3359,4167,5159,6151,6959,7951,8759,9751,10559,11551,12359,13351,14119,15104,15872,16673,17441,18198,18198,18198,18198,21,20,0,0,0,0,0,0,0x472c80,0x99b280,0xda3700,0x12cbd00,0x16d4180,0x1bfc780,0x2124d80,0x252d200,0x2a55800,0x2e5dc80,0x3386280,0x378e700,0x3cb6d00,0x40bf180,0x45e7780,0x49d5800,0x4ef9480,0x52e7500,0x56eb000,0x5ad9080,0x5ebfd80,0x5ebfd80,0x5ebfd80,0x5ebfd80,0x5ebfd80,0x5ebfd80,0x5ebfd80,0x5ebfd80,0x5ebfd80,0x5ebfd80,0x5ebfd80,0x5ebfd80,568,1560,2368,3360,4352,5160,6152,6960,7952,8760,9752,10560,11552,12544,13352,14120,15105,15873,16674,17442,18199,18199,18199,18199,21,20,0,0,0,0,0,0,0x473700,0x99bd00,0xda4180,0x12cc780,0x17f4d80,0x1bfd200,0x2125800,0x252dc80,0x2a56280,0x2e5e700,0x3386d00,0x378f180,0x3cb7780,0x41dfd80,0x45e8200,0x49d6280,0x4ef9f00,0x52e7f80,0x56eba80,0x5ad9b00,0x5ec0800,0x5ec0800,0x5ec0800,0x5ec0800,0x5ec0800,0x5ec0800,0x5ec0800,0x5ec0800,0x5ec0800,0x5ec0800,0x5ec0800,0x5ec0800,569,1561,2369,3361,4353,5161,6153,6961,7953,8761,9753,10561,11553,12545,13353,14121,15106,15874,16675,17443,18200,18200,18200,18200,21,20,0,0,0,0,0,0,0x474180,0x99c780,0xda4c00,0x12cd200,0x17f5800,0x1bfdc80,0x2126280,0x252e700,0x2a56d00,0x2e5f180,0x3387780,0x378fc00,0x3cb8200,0x41e0800,0x45e8c80,0x49d6d00,0x4efa980,0x52e8a00,0x56ec500,0x5ada580,0x5ec1280,0x5ec1280,0x5ec1280,0x5ec1280,0x5ec1280,0x5ec1280,0x5ec1280,0x5ec1280,0x5ec1280,0x5ec1280,0x5ec1280,0x5ec1280,570,1562,2370,3362,4354,5162,6154,6962,7954,8762,9754,10562,11554,12546,13354,18472,15107,3656,16676,15688,15688,15688,15688,15688,20,19,0,0,0,0,0,0,0x474c00,0x99d200,0xda5680,0x12cdc80,0x17f6280,0x1bfe700,0x2126d00,0x252f180,0x2a57780,0x2e5fc00,0x3388200,0x3790680,0x3cb8c80,0x41e1280,0x45e9700,0x7ae80,0x4efb400,0x1406280,0x56ecf80,0x5197500,0x5197500,0x5197500,0x5197500,0x5197500,0x5197500,0x5197500,0x5197500,0x5197500,0x5197500,0x5197500,0x5197500,0x5197500,571,1563,2371,3363,4355,5163,6155,6963,7955,8763,9755,10563,11555,12547,13355,14122,15108,15875,16677,17444,18201,18201,18201,18201,21,20,0,0,0,0,0,0,0x475680,0x99dc80,0xda6100,0x12ce700,0x17f6d00,0x1bff180,0x2127780,0x252fc00,0x2a58200,0x2e60680,0x3388c80,0x3791100,0x3cb9700,0x41e1d00,0x45ea180,0x49d7780,0x4efbe80,0x52e9480,0x56eda00,0x5adb000,0x5ec1d00,0x5ec1d00,0x5ec1d00,0x5ec1d00,0x5ec1d00,0x5ec1d00,0x5ec1d00,0x5ec1d00,0x5ec1d00,0x5ec1d00,0x5ec1d00,0x5ec1d00,572,1564,2372,3364,4356,5164,6156,6964,7956,8764,9756,10564,11556,12548,13356,14123,15109,15876,16678,17445,18202,18202,18202,18202,21,20,0,0,0,0,0,0,0x476100,0x99e700,0xda6b80,0x12cf180,0x17f7780,0x1bffc00,0x2128200,0x2530680,0x2a58c80,0x2e61100,0x3389700,0x3791b80,0x3cba180,0x41e2780,0x45eac00,0x49d8200,0x4efc900,0x52e9f00,0x56ee480,0x5adba80,0x5ec2780,0x5ec2780,0x5ec2780,0x5ec2780,0x5ec2780,0x5ec2780,0x5ec2780,0x5ec2780,0x5ec2780,0x5ec2780,0x5ec2780,0x5ec2780,573,1565,2373,3365,4357,5165,6157,6965,7957,8765,9757,10565,11557,12549,13357,14124,15110,15877,16679,17446,18203,18203,18203,18203,21,20,0,0,0,0,0,0,0x476b80,0x99f180,0xda7600,0x12cfc00,0x17f8200,0x1c00680,0x2128c80,0x2531100,0x2a59700,0x2e61b80,0x338a180,0x3792600,0x3cbac00,0x41e3200,0x45eb680,0x49d8c80,0x4efd380,0x52ea980,0x56eef00,0x5adc500,0x5ec3200,0x5ec3200,0x5ec3200,0x5ec3200,0x5ec3200,0x5ec3200,0x5ec3200,0x5ec3200,0x5ec3200,0x5ec3200,0x5ec3200,0x5ec3200,574,1566,2374,3366,4358,5166,6158,6966,7958,8766,9758,10566,11558,12550,13358,14125,15111,15878,16680,17447,18204,18204,18204,18204,21,20,0,0,0,0,0,0,0x477600,0x99fc00,0xda8080,0x12d0680,0x17f8c80,0x1c01100,0x2129700,0x2531b80,0x2a5a180,0x2e62600,0x338ac00,0x3793080,0x3cbb680,0x41e3c80,0x45ec100,0x49d9700,0x4efde00,0x52eb400,0x56ef980,0x5adcf80,0x5ec3c80,0x5ec3c80,0x5ec3c80,0x5ec3c80,0x5ec3c80,0x5ec3c80,0x5ec3c80,0x5ec3c80,0x5ec3c80,0x5ec3c80,0x5ec3c80,0x5ec3c80,575,1567,2375,3367,4359,5167,6159,6967,7959,8767,9759,10567,11559,12551,13359,14126,15112,15879,16681,17448,18205,18205,18205,18205,21,20,0,0,0,0,0,0,0x478080,0x9a0680,0xda8b00,0x12d1100,0x17f9700,0x1c01b80,0x212a180,0x2532600,0x2a5ac00,0x2e63080,0x338b680,0x3793b00,0x3cbc100,0x41e4700,0x45ecb80,0x49da180,0x4efe880,0x52ebe80,0x56f0400,0x5adda00,0x5ec4700,0x5ec4700,0x5ec4700,0x5ec4700,0x5ec4700,0x5ec4700,0x5ec4700,0x5ec4700,0x5ec4700,0x5ec4700,0x5ec4700,0x5ec4700,576,1568,2560,3368,4360,5168,6160,6968,7960,8768,9760,10752,11560,12552,13360,14127,15113,15880,16682,17449,18206,18206,18206,18206,21,20,0,0,0,0,0,0,0x478b00,0x9a1100,0xec9700,0x12d1b80,0x17fa180,0x1c02600,0x212ac00,0x2533080,0x2a5b680,0x2e63b00,0x338c100,0x38b4700,0x3cbcb80,0x41e5180,0x45ed600,0x49dac00,0x4eff300,0x52ec900,0x56f0e80,0x5ade480,0x5ec5180,0x5ec5180,0x5ec5180,0x5ec5180,0x5ec5180,0x5ec5180,0x5ec5180,0x5ec5180,0x5ec5180,0x5ec5180,0x5ec5180,0x5ec5180,577,1569,2561,3369,4361,5169,6161,6969,7961,8769,9761,10753,11561,12553,13361,14128,15114,15881,16683,17450,18207,18207,18207,18207,21,20,0,0,0,0,0,0,0x479580,0x9a1b80,0xeca180,0x12d2600,0x17fac00,0x1c03080,0x212b680,0x2533b00,0x2a5c100,0x2e64580,0x338cb80,0x38b5180,0x3cbd600,0x41e5c00,0x45ee080,0x49db680,0x4effd80,0x52ed380,0x56f1900,0x5adef00,0x5ec5c00,0x5ec5c00,0x5ec5c00,0x5ec5c00,0x5ec5c00,0x5ec5c00,0x5ec5c00,0x5ec5c00,0x5ec5c00,0x5ec5c00,0x5ec5c00,0x5ec5c00,578,1570,2562,3370,4362,5170,6162,6970,7962,8770,9762,10754,11562,12554,13362,18473,15115,3912,16684,15944,15944,15944,15944,15944,20,19,0,0,0,0,0,0,0x47a000,0x9a2600,0xecac00,0x12d3080,0x17fb680,0x1c03b00,0x212c100,0x2534580,0x2a5cb80,0x2e65000,0x338d600,0x38b5c00,0x3cbe080,0x41e6680,0x45eeb00,0x7b900,0x4f00800,0x1555800,0x56f2380,0x52e6a80,0x52e6a80,0x52e6a80,0x52e6a80,0x52e6a80,0x52e6a80,0x52e6a80,0x52e6a80,0x52e6a80,0x52e6a80,0x52e6a80,0x52e6a80,0x52e6a80,579,1571,2563,3371,4363,5171,6163,6971,7963,8771,9763,10755,11563,12555,13363,14129,15116,15882,16685,17451,18208,18208,18208,18208,21,20,0,0,0,0,0,0,0x47aa80,0x9a3080,0xecb680,0x12d3b00,0x17fc100,0x1c04580,0x212cb80,0x2535000,0x2a5d600,0x2e65a80,0x338e080,0x38b6680,0x3cbeb00,0x41e7100,0x45ef580,0x49dc100,0x4f01280,0x52ede00,0x56f2e00,0x5adf980,0x5ec6680,0x5ec6680,0x5ec6680,0x5ec6680,0x5ec6680,0x5ec6680,0x5ec6680,0x5ec6680,0x5ec6680,0x5ec6680,0x5ec6680,0x5ec6680,580,1572,2564,3372,4364,5172,6164,6972,7964,8772,9764,10756,11564,12556,13364,14130,15117,15883,16686,17452,18209,18209,18209,18209,21,20,0,0,0,0,0,0,0x47b500,0x9a3b00,0xecc100,0x12d4580,0x17fcb80,0x1c05000,0x212d600,0x2535a80,0x2a5e080,0x2e66500,0x338eb00,0x38b7100,0x3cbf580,0x41e7b80,0x45f0000,0x49dcb80,0x4f01d00,0x52ee880,0x56f3880,0x5ae0400,0x5ec7100,0x5ec7100,0x5ec7100,0x5ec7100,0x5ec7100,0x5ec7100,0x5ec7100,0x5ec7100,0x5ec7100,0x5ec7100,0x5ec7100,0x5ec7100,581,1573,2565,3373,4365,5173,6165,6973,7965,8773,9765,10757,11565,12557,13365,14131,15118,15884,16687,17453,18210,18210,18210,18210,21,20,0,0,0,0,0,0,0x47bf80,0x9a4580,0xeccb80,0x12d5000,0x17fd600,0x1c05a80,0x212e080,0x2536500,0x2a5eb00,0x2e66f80,0x338f580,0x38b7b80,0x3cc0000,0x41e8600,0x45f0a80,0x49dd600,0x4f02780,0x52ef300,0x56f4300,0x5ae0e80,0x5ec7b80,0x5ec7b80,0x5ec7b80,0x5ec7b80,0x5ec7b80,0x5ec7b80,0x5ec7b80,0x5ec7b80,0x5ec7b80,0x5ec7b80,0x5ec7b80,0x5ec7b80,582,1574,2566,3374,4366,5174,6166,6974,7966,8774,9766,10758,11566,12558,13366,14132,15119,15885,16688,17454,18211,18211,18211,18211,21,20,0,0,0,0,0,0,0x47ca00,0x9a5000,0xecd600,0x12d5a80,0x17fe080,0x1c06500,0x212eb00,0x2536f80,0x2a5f580,0x2e67a00,0x3390000,0x38b8600,0x3cc0a80,0x41e9080,0x45f1500,0x49de080,0x4f03200,0x52efd80,0x56f4d80,0x5ae1900,0x5ec8600,0x5ec8600,0x5ec8600,0x5ec8600,0x5ec8600,0x5ec8600,0x5ec8600,0x5ec8600,0x5ec8600,0x5ec8600,0x5ec8600,0x5ec8600,583,1575,2567,3375,4367,5175,6167,6975,7967,8775,9767,10759,11567,12559,13367,14133,15120,15886,16689,17455,18212,18212,18212,18212,21,20,0,0,0,0,0,0,0x47d480,0x9a5a80,0xece080,0x12d6500,0x17feb00,0x1c06f80,0x212f580,0x2537a00,0x2a60000,0x2e68480,0x3390a80,0x38b9080,0x3cc1500,0x41e9b00,0x45f1f80,0x49deb00,0x4f03c80,0x52f0800,0x56f5800,0x5ae2380,0x5ec9080,0x5ec9080,0x5ec9080,0x5ec9080,0x5ec9080,0x5ec9080,0x5ec9080,0x5ec9080,0x5ec9080,0x5ec9080,0x5ec9080,0x5ec9080,768,1576,2568,3376,4368,5176,6168,6976,7968,8960,9768,10760,11568,12560,13368,14134,15121,15887,16690,17456,18213,18213,18213,18213,21,20,0,0,0,0,0,0,0x59e080,0x9a6500,0xeceb00,0x12d6f80,0x17ff580,0x1c07a00,0x2130000,0x2538480,0x2a60a80,0x2f89080,0x3391500,0x38b9b00,0x3cc1f80,0x41ea580,0x45f2a00,0x49df580,0x4f04700,0x52f1280,0x56f6280,0x5ae2e00,0x5ec9b00,0x5ec9b00,0x5ec9b00,0x5ec9b00,0x5ec9b00,0x5ec9b00,0x5ec9b00,0x5ec9b00,0x5ec9b00,0x5ec9b00,0x5ec9b00,0x5ec9b00,769,1577,2569,3377,4369,5177,6169,6977,7969,8961,9769,10761,11569,12561,13369,14135,15122,15888,16691,17457,18214,18214,18214,18214,21,20,0,0,0,0,0,0,0x59eb00,0x9a6f80,0xecf580,0x12d7a00,0x1800000,0x1c08480,0x2130a80,0x2538f00,0x2a61500,0x2f89b00,0x3391f80,0x38ba580,0x3cc2a00,0x41eb000,0x45f3480,0x49e0000,0x4f05180,0x52f1d00,0x56f6d00,0x5ae3880,0x5eca580,0x5eca580,0x5eca580,0x5eca580,0x5eca580,0x5eca580,0x5eca580,0x5eca580,0x5eca580,0x5eca580,0x5eca580,0x5eca580,770,1578,2570,3378,4370,5178,6170,6978,7970,8962,9770,10762,11570,12562,13370,18474,15123,4168,16692,16200,16200,16200,16200,16200,20,19,0,0,0,0,0,0,0x59f580,0x9a7a00,0xed0000,0x12d8480,0x1800a80,0x1c08f00,0x2131500,0x2539980,0x2a61f80,0x2f8a580,0x3392a00,0x38bb000,0x3cc3480,0x41eba80,0x45f3f00,0x7c380,0x4f05c00,0x16a4d80,0x56f7780,0x5436000,0x5436000,0x5436000,0x5436000,0x5436000,0x5436000,0x5436000,0x5436000,0x5436000,0x5436000,0x5436000,0x5436000,0x5436000,771,1579,2571,3379,4371,5179,6171,6979,7971,8963,9771,10763,11571,12563,13371,14136,15124,15889,16693,17458,18215,18215,18215,18215,21,20,0,0,0,0,0,0,0x5a0000,0x9a8480,0xed0a80,0x12d8f00,0x1801500,0x1c09980,0x2131f80,0x253a400,0x2a62a00,0x2f8b000,0x3393480,0x38bba80,0x3cc3f00,0x41ec500,0x45f4980,0x49e0a80,0x4f06680,0x52f2780,0x56f8200,0x5ae4300,0x5ecb000,0x5ecb000,0x5ecb000,0x5ecb000,0x5ecb000,0x5ecb000,0x5ecb000,0x5ecb000,0x5ecb000,0x5ecb000,0x5ecb000,0x5ecb000,772,1580,2572,3380,4372,5180,6172,6980,7972,8964,9772,10764,11572,12564,13372,14137,15125,15890,16694,17459,18216,18216,18216,18216,21,20,0,0,0,0,0,0,0x5a0a80,0x9a8f00,0xed1500,0x12d9980,0x1801f80,0x1c0a400,0x2132a00,0x253ae80,0x2a63480,0x2f8ba80,0x3393f00,0x38bc500,0x3cc4980,0x41ecf80,0x45f5400,0x49e1500,0x4f07100,0x52f3200,0x56f8c80,0x5ae4d80,0x5ecba80,0x5ecba80,0x5ecba80,0x5ecba80,0x5ecba80,0x5ecba80,0x5ecba80,0x5ecba80,0x5ecba80,0x5ecba80,0x5ecba80,0x5ecba80,773,1581,2573,3381,4373,5181,6173,6981,7973,8965,9773,10765,11573,12565,13373,14138,15126,15891,16695,17460,18217,18217,18217,18217,21,20,0,0,0,0,0,0,0x5a1500,0x9a9980,0xed1f80,0x12da400,0x1802a00,0x1c0ae80,0x2133480,0x253b900,0x2a63f00,0x2f8c500,0x3394980,0x38bcf80,0x3cc5400,0x41eda00,0x45f5e80,0x49e1f80,0x4f07b80,0x52f3c80,0x56f9700,0x5ae5800,0x5ecc500,0x5ecc500,0x5ecc500,0x5ecc500,0x5ecc500,0x5ecc500,0x5ecc500,0x5ecc500,0x5ecc500,0x5ecc500,0x5ecc500,0x5ecc500,774,1582,2574,3382,4374,5182,6174,6982,7974,8966,9774,10766,11574,12566,13374,14139,15127,15892,16696,17461,18218,18218,18218,18218,21,20,0,0,0,0,0,0,0x5a1f80,0x9aa400,0xed2a00,0x12dae80,0x1803480,0x1c0b900,0x2133f00,0x253c380,0x2a64980,0x2f8cf80,0x3395400,0x38bda00,0x3cc5e80,0x41ee480,0x45f6900,0x49e2a00,0x4f08600,0x52f4700,0x56fa180,0x5ae6280,0x5eccf80,0x5eccf80,0x5eccf80,0x5eccf80,0x5eccf80,0x5eccf80,0x5eccf80,0x5eccf80,0x5eccf80,0x5eccf80,0x5eccf80,0x5eccf80,775,1583,2575,3383,4375,5183,6175,6983,7975,8967,9775,10767,11575,12567,13375,14140,15128,15893,16697,17462,18219,18219,18219,18219,21,20,0,0,0,0,0,0,0x5a2a00,0x9aae80,0xed3480,0x12db900,0x1803f00,0x1c0c380,0x2134980,0x253ce00,0x2a65400,0x2f8da00,0x3395e80,0x38be480,0x3cc6900,0x41eef00,0x45f7380,0x49e3480,0x4f09080,0x52f5180,0x56fac00,0x5ae6d00,0x5ecda00,0x5ecda00,0x5ecda00,0x5ecda00,0x5ecda00,0x5ecda00,0x5ecda00,0x5ecda00,0x5ecda00,0x5ecda00,0x5ecda00,0x5ecda00,776,1584,2576,3384,4376,5184,6176,7168,7976,8968,9776,10768,11576,12568,13376,14141,15129,15894,16698,17463,18220,18220,18220,18220,21,20,0,0,0,0,0,0,0x5a3480,0x9ab900,0xed3f00,0x12dc380,0x1804980,0x1c0ce00,0x2135400,0x265da00,0x2a65e80,0x2f8e480,0x3396900,0x38bef00,0x3cc7380,0x41ef980,0x45f7e00,0x49e3f00,0x4f09b00,0x52f5c00,0x56fb680,0x5ae7780,0x5ece480,0x5ece480,0x5ece480,0x5ece480,0x5ece480,0x5ece480,0x5ece480,0x5ece480,0x5ece480,0x5ece480,0x5ece480,0x5ece480,777,1585,2577,3385,4377,5185,6177,7169,7977,8969,9777,10769,11577,12569,13377,14142,15130,15895,16699,17464,18221,18221,18221,18221,21,20,0,0,0,0,0,0,0x5a3f00,0x9ac380,0xed4980,0x12dce00,0x1805400,0x1c0d880,0x2135e80,0x265e480,0x2a66900,0x2f8ef00,0x3397380,0x38bf980,0x3cc7e00,0x41f0400,0x45f8880,0x49e4980,0x4f0a580,0x52f6680,0x56fc100,0x5ae8200,0x5ecef00,0x5ecef00,0x5ecef00,0x5ecef00,0x5ecef00,0x5ecef00,0x5ecef00,0x5ecef00,0x5ecef00,0x5ecef00,0x5ecef00,0x5ecef00,778,1586,2578,3386,4378,5186,6178,7170,7978,8970,9778,10770,11578,12570,13378,18475,15131,4424,16700,16456,16456,16456,16456,16456,20,19,0,0,0,0,0,0,0x5a4980,0x9ace00,0xed5400,0x12dd880,0x1805e80,0x1c0e300,0x2136900,0x265ef00,0x2a67380,0x2f8f980,0x3397e00,0x38c0400,0x3cc8880,0x41f0e80,0x45f9300,0x7ce00,0x4f0b000,0x17f4300,0x56fcb80,0x5585580,0x5585580,0x5585580,0x5585580,0x5585580,0x5585580,0x5585580,0x5585580,0x5585580,0x5585580,0x5585580,0x5585580,0x5585580,779,1587,2579,3387,4379,5187,6179,7171,7979,8971,9779,10771,11579,12571,13379,14143,15132,15896,16701,17465,18222,18222,18222,18222,21,20,0,0,0,0,0,0,0x5a5400,0x9ad880,0xed5e80,0x12de300,0x1806900,0x1c0ed80,0x2137380,0x265f980,0x2a67e00,0x2f90400,0x3398880,0x38c0e80,0x3cc9300,0x41f1900,0x45f9d80,0x49e5400,0x4f0ba80,0x52f7100,0x56fd600,0x5ae8c80,0x5ecf980,0x5ecf980,0x5ecf980,0x5ecf980,0x5ecf980,0x5ecf980,0x5ecf980,0x5ecf980,0x5ecf980,0x5ecf980,0x5ecf980,0x5ecf980,780,1588,2580,3388,4380,5188,6180,7172,7980,8972,9780,10772,11580,12572,13380,14144,15133,15897,16702,17466,18223,18223,18223,18223,21,20,0,0,0,0,0,0,0x5a5e80,0x9ae300,0xed6900,0x12ded80,0x1807380,0x1c0f800,0x2137e00,0x2660400,0x2a68880,0x2f90e80,0x3399300,0x38c1900,0x3cc9d80,0x41f2380,0x45fa800,0x49e5e80,0x4f0c500,0x52f7b80,0x56fe080,0x5ae9700,0x5ed0400,0x5ed0400,0x5ed0400,0x5ed0400,0x5ed0400,0x5ed0400,0x5ed0400,0x5ed0400,0x5ed0400,0x5ed0400,0x5ed0400,0x5ed0400,781,1589,2581,3389,4381,5189,6181,7173,7981,8973,9781,10773,11581,12573,13381,14145,15134,15898,16703,17467,18224,18224,18224,18224,21,20,0,0,0,0,0,0,0x5a6900,0x9aed80,0xed7380,0x12df800,0x1807e00,0x1c10280,0x2138880,0x2660e80,0x2a69300,0x2f91900,0x3399d80,0x38c2380,0x3cca800,0x41f2e00,0x45fb280,0x49e6900,0x4f0cf80,0x52f8600,0x56feb00,0x5aea180,0x5ed0e80,0x5ed0e80,0x5ed0e80,0x5ed0e80,0x5ed0e80,0x5ed0e80,0x5ed0e80,0x5ed0e80,0x5ed0e80,0x5ed0e80,0x5ed0e80,0x5ed0e80,782,1590,2582,3390,4382,5190,6182,7174,7982,8974,9782,10774,11582,12574,13382,14146,15135,15899,16704,17468,18225,18225,18225,18225,21,20,0,0,0,0,0,0,0x5a7380,0x9af800,0xed7e00,0x12e0280,0x1808880,0x1c10d00,0x2139300,0x2661900,0x2a69d80,0x2f92380,0x339a800,0x38c2e00,0x3ccb280,0x41f3880,0x45fbd00,0x49e7380,0x4f0da00,0x52f9080,0x56ff580,0x5aeac00,0x5ed1900,0x5ed1900,0x5ed1900,0x5ed1900,0x5ed1900,0x5ed1900,0x5ed1900,0x5ed1900,0x5ed1900,0x5ed1900,0x5ed1900,0x5ed1900,783,1591,2583,3391,4383,5191,6183,7175,7983,8975,9783,10775,11583,12575,13383,14147,15136,15900,16705,17469,18226,18226,18226,18226,21,20,0,0,0,0,0,0,0x5a7e00,0x9b0280,0xed8880,0x12e0d00,0x1809300,0x1c11780,0x2139d80,0x2662380,0x2a6a800,0x2f92e00,0x339b280,0x38c3880,0x3ccbd00,0x41f4300,0x45fc780,0x49e7e00,0x4f0e480,0x52f9b00,0x5700000,0x5aeb680,0x5ed2380,0x5ed2380,0x5ed2380,0x5ed2380,0x5ed2380,0x5ed2380,0x5ed2380,0x5ed2380,0x5ed2380,0x5ed2380,0x5ed2380,0x5ed2380,784,1592,2584,3392,4384,5376,6184,7176,7984,8976,9784,10776,11584,12576,13568,14148,15137,15901,16706,17470,18227,18227,18227,18227,21,20,0,0,0,0,0,0,0x5a8880,0x9b0d00,0xed9300,0x12e1780,0x1809d80,0x1d32380,0x213a800,0x2662e00,0x2a6b280,0x2f93880,0x339bd00,0x38c4300,0x3ccc780,0x41f4d80,0x471d380,0x49e8880,0x4f0ef00,0x52fa580,0x5700a80,0x5aec100,0x5ed2e00,0x5ed2e00,0x5ed2e00,0x5ed2e00,0x5ed2e00,0x5ed2e00,0x5ed2e00,0x5ed2e00,0x5ed2e00,0x5ed2e00,0x5ed2e00,0x5ed2e00,785,1593,2585,3393,4385,5377,6185,7177,7985,8977,9785,10777,11585,12577,13569,14149,15138,15902,16707,17471,18228,18228,18228,18228,21,20,0,0,0,0,0,0,0x5a9300,0x9b1780,0xed9d80,0x12e2200,0x180a800,0x1d32e00,0x213b280,0x2663880,0x2a6bd00,0x2f94300,0x339c780,0x38c4d80,0x3ccd200,0x41f5800,0x471de00,0x49e9300,0x4f0f980,0x52fb000,0x5701500,0x5aecb80,0x5ed3880,0x5ed3880,0x5ed3880,0x5ed3880,0x5ed3880,0x5ed3880,0x5ed3880,0x5ed3880,0x5ed3880,0x5ed3880,0x5ed3880,0x5ed3880,786,1594,2586,3394,4386,5378,6186,7178,7986,8978,9786,10778,11586,12578,13570,18476,15139,4680,16708,16712,16712,16712,16712,16712,20,19,0,0,0,0,0,0,0x5a9d80,0x9b2200,0xeda800,0x12e2c80,0x180b280,0x1d33880,0x213bd00,0x2664300,0x2a6c780,0x2f94d80,0x339d200,0x38c5800,0x3ccdc80,0x41f6280,0x471e880,0x7d880,0x4f10400,0x1943880,0x5701f80,0x56d4b00,0x56d4b00,0x56d4b00,0x56d4b00,0x56d4b00,0x56d4b00,0x56d4b00,0x56d4b00,0x56d4b00,0x56d4b00,0x56d4b00,0x56d4b00,0x56d4b00,787,1595,2587,3395,4387,5379,6187,7179,7987,8979,9787,10779,11587,12579,13571,14150,15140,15903,16709,17472,18229,18229,18229,18229,21,20,0,0,0,0,0,0,0x5aa800,0x9b2c80,0xedb280,0x12e3700,0x180bd00,0x1d34300,0x213c780,0x2664d80,0x2a6d200,0x2f95800,0x339dc80,0x38c6280,0x3cce700,0x41f6d00,0x471f300,0x49e9d80,0x4f10e80,0x52fba80,0x5702a00,0x5aed600,0x5ed4300,0x5ed4300,0x5ed4300,0x5ed4300,0x5ed4300,0x5ed4300,0x5ed4300,0x5ed4300,0x5ed4300,0x5ed4300,0x5ed4300,0x5ed4300,788,1596,2588,3396,4388,5380,6188,7180,7988,8980,9788,10780,11588,12580,13572,14151,15141,15904,16710,17473,18230,18230,18230,18230,21,20,0,0,0,0,0,0,0x5ab280,0x9b3700,0xedbd00,0x12e4180,0x180c780,0x1d34d80,0x213d200,0x2665800,0x2a6dc80,0x2f96280,0x339e700,0x38c6d00,0x3ccf180,0x41f7780,0x471fd80,0x49ea800,0x4f11900,0x52fc500,0x5703480,0x5aee080,0x5ed4d80,0x5ed4d80,0x5ed4d80,0x5ed4d80,0x5ed4d80,0x5ed4d80,0x5ed4d80,0x5ed4d80,0x5ed4d80,0x5ed4d80,0x5ed4d80,0x5ed4d80,789,1597,2589,3397,4389,5381,6189,7181,7989,8981,9789,10781,11589,12581,13573,14336,15142,15905,16711,17474,18231,18231,18231,18231,21,20,0,0,0,0,0,0,0x5abd00,0x9b4180,0xedc780,0x12e4c00,0x180d200,0x1d35800,0x213dc80,0x2666280,0x2a6e700,0x2f96d00,0x339f180,0x38c7780,0x3ccfc00,0x41f8200,0x4720800,0x4b0b400,0x4f12380,0x52fcf80,0x5703f00,0x5aeeb00,0x5ed5800,0x5ed5800,0x5ed5800,0x5ed5800,0x5ed5800,0x5ed5800,0x5ed5800,0x5ed5800,0x5ed5800,0x5ed5800,0x5ed5800,0x5ed5800,790,1598,2590,3398,4390,5382,6190,7182,7990,8982,9790,10782,11590,12582,13574,14337,15143,15906,16896,17475,18232,18232,18232,18232,21,20,0,0,0,0,0,0,0x5ac780,0x9b4c00,0xedd200,0x12e5680,0x180dc80,0x1d36280,0x213e700,0x2666d00,0x2a6f180,0x2f97780,0x339fc00,0x38c8200,0x3cd0680,0x41f8c80,0x4721280,0x4b0be80,0x4f12e00,0x52fda00,0x5824b00,0x5aef580,0x5ed6280,0x5ed6280,0x5ed6280,0x5ed6280,0x5ed6280,0x5ed6280,0x5ed6280,0x5ed6280,0x5ed6280,0x5ed6280,0x5ed6280,0x5ed6280,791,1599,2591,3399,4391,5383,6191,7183,7991,8983,9791,10783,11591,12583,13575,14338,15144,15907,16897,17476,18233,18233,18233,18233,21,20,0,0,0,0,0,0,0x5ad200,0x9b5680,0xeddc80,0x12e6100,0x180e700,0x1d36d00,0x213f180,0x2667780,0x2a6fc00,0x2f98200,0x33a0680,0x38c8c80,0x3cd1100,0x41f9700,0x4721d00,0x4b0c900,0x4f13880,0x52fe480,0x5825580,0x5af0000,0x5ed6d00,0x5ed6d00,0x5ed6d00,0x5ed6d00,0x5ed6d00,0x5ed6d00,0x5ed6d00,0x5ed6d00,0x5ed6d00,0x5ed6d00,0x5ed6d00,0x5ed6d00,792,1600,2592,3584,4392,5384,6192,7184,7992,8984,9792,10784,11776,12584,13576,14339,15145,15908,16898,17477,18234,18234,18234,18234,21,20,0,0,0,0,0,0,0x5adc80,0x9b6100,0xede700,0x1406d00,0x180f180,0x1d37780,0x213fc00,0x2668200,0x2a70680,0x2f98c80,0x33a1100,0x38c9700,0x3df1d00,0x41fa180,0x4722780,0x4b0d380,0x4f14300,0x52fef00,0x5826000,0x5af0a80,0x5ed7780,0x5ed7780,0x5ed7780,0x5ed7780,0x5ed7780,0x5ed7780,0x5ed7780,0x5ed7780,0x5ed7780,0x5ed7780,0x5ed7780,0x5ed7780,793,1601,2593,3585,4393,5385,6193,7185,7993,8985,9793,10785,11777,12585,13577,14340,15146,15909,16899,17478,18235,18235,18235,18235,21,20,0,0,0,0,0,0,0x5ae700,0x9b6b80,0xedf180,0x1407780,0x180fc00,0x1d38200,0x2140680,0x2668c80,0x2a71100,0x2f99700,0x33a1b80,0x38ca180,0x3df2780,0x41fac00,0x4723200,0x4b0de00,0x4f14d80,0x52ff980,0x5826a80,0x5af1500,0x5ed8200,0x5ed8200,0x5ed8200,0x5ed8200,0x5ed8200,0x5ed8200,0x5ed8200,0x5ed8200,0x5ed8200,0x5ed8200,0x5ed8200,0x5ed8200,794,1602,2594,3586,4394,5386,6194,7186,7994,8986,9794,10786,11778,12586,13578,18477,15147,4936,16900,16968,16968,16968,16968,16968,20,19,0,0,0,0,0,0,0x5af180,0x9b7600,0xedfc00,0x1408200,0x1810680,0x1d38c80,0x2141100,0x2669700,0x2a71b80,0x2f9a180,0x33a2600,0x38cac00,0x3df3200,0x41fb680,0x4723c80,0x7e300,0x4f15800,0x1a92e00,0x5827500,0x5824080,0x5824080,0x5824080,0x5824080,0x5824080,0x5824080,0x5824080,0x5824080,0x5824080,0x5824080,0x5824080,0x5824080,0x5824080,795,1603,2595,3587,4395,5387,6195,7187,7995,8987,9795,10787,11779,12587,13579,14341,15148,15910,16901,17479,18236,18236,18236,18236,21,20,0,0,0,0,0,0,0x5afc00,0x9b8080,0xee0680,0x1408c80,0x1811100,0x1d39700,0x2141b80,0x266a180,0x2a72600,0x2f9ac00,0x33a3080,0x38cb680,0x3df3c80,0x41fc100,0x4724700,0x4b0e880,0x4f16280,0x5300400,0x5827f80,0x5af1f80,0x5ed8c80,0x5ed8c80,0x5ed8c80,0x5ed8c80,0x5ed8c80,0x5ed8c80,0x5ed8c80,0x5ed8c80,0x5ed8c80,0x5ed8c80,0x5ed8c80,0x5ed8c80,796,1604,2596,3588,4396,5388,6196,7188,7996,8988,9796,10788,11780,12588,13580,14342,15149,15911,16902,17664,18237,18237,18237,18237,21,20,0,0,0,0,0,0,0x5b0680,0x9b8b00,0xee1100,0x1409700,0x1811b80,0x1d3a180,0x2142600,0x266ac00,0x2a73080,0x2f9b680,0x33a3b00,0x38cc100,0x3df4700,0x41fcb80,0x4725180,0x4b0f300,0x4f16d00,0x5300e80,0x5828a00,0x5c12b80,0x5ed9700,0x5ed9700,0x5ed9700,0x5ed9700,0x5ed9700,0x5ed9700,0x5ed9700,0x5ed9700,0x5ed9700,0x5ed9700,0x5ed9700,0x5ed9700,797,1605,2597,3589,4397,5389,6197,7189,7997,8989,9797,10789,11781,12589,13581,14343,15150,15912,16903,17665,18238,18238,18238,18238,21,20,0,0,0,0,0,0,0x5b1100,0x9b9580,0xee1b80,0x140a180,0x1812600,0x1d3ac00,0x2143080,0x266b680,0x2a73b00,0x2f9c100,0x33a4580,0x38ccb80,0x3df5180,0x41fd600,0x4725c00,0x4b0fd80,0x4f17780,0x5301900,0x5829480,0x5c13600,0x5eda180,0x5eda180,0x5eda180,0x5eda180,0x5eda180,0x5eda180,0x5eda180,0x5eda180,0x5eda180,0x5eda180,0x5eda180,0x5eda180,798,1606,2598,3590,4398,5390,6198,7190,7998,8990,9798,10790,11782,12590,13582,14344,15151,15913,16904,17666,18239,18239,18239,18239,21,20,0,0,0,0,0,0,0x5b1b80,0x9ba000,0xee2600,0x140ac00,0x1813080,0x1d3b680,0x2143b00,0x266c100,0x2a74580,0x2f9cb80,0x33a5000,0x38cd600,0x3df5c00,0x41fe080,0x4726680,0x4b10800,0x4f18200,0x5302380,0x5829f00,0x5c14080,0x5edac00,0x5edac00,0x5edac00,0x5edac00,0x5edac00,0x5edac00,0x5edac00,0x5edac00,0x5edac00,0x5edac00,0x5edac00,0x5edac00,799,1607,2599,3591,4399,5391,6199,7191,7999,8991,9799,10791,11783,12591,13583,14345,15152,15914,16905,17667,18240,18240,18240,18240,21,20,0,0,0,0,0,0,0x5b2600,0x9baa80,0xee3080,0x140b680,0x1813b00,0x1d3c100,0x2144580,0x266cb80,0x2a75000,0x2f9d600,0x33a5a80,0x38ce080,0x3df6680,0x41feb00,0x4727100,0x4b11280,0x4f18c80,0x5302e00,0x582a980,0x5c14b00,0x5edb680,0x5edb680,0x5edb680,0x5edb680,0x5edb680,0x5edb680,0x5edb680,0x5edb680,0x5edb680,0x5edb680,0x5edb680,0x5edb680,800,1792,2600,3592,4400,5392,6200,7192,8000,8992,9984,10792,11784,12592,13584,14346,15153,15915,16906,17668,18241,18241,18241,18241,21,20,0,0,0,0,0,0,0x5b3080,0xadb680,0xee3b00,0x140c100,0x1814580,0x1d3cb80,0x2145000,0x266d600,0x2a75a80,0x2f9e080,0x34c6680,0x38ceb00,0x3df7100,0x41ff580,0x4727b80,0x4b11d00,0x4f19700,0x5303880,0x582b400,0x5c15580,0x5edc100,0x5edc100,0x5edc100,0x5edc100,0x5edc100,0x5edc100,0x5edc100,0x5edc100,0x5edc100,0x5edc100,0x5edc100,0x5edc100,801,1793,2601,3593,4401,5393,6201,7193,8001,8993,9985,10793,11785,12593,13585,14347,15154,15916,16907,17669,18242,18242,18242,18242,21,20,0,0,0,0,0,0,0x5b3b00,0xadc100,0xee4580,0x140cb80,0x1815000,0x1d3d600,0x2145a80,0x266e080,0x2a76500,0x2f9eb00,0x34c7100,0x38cf580,0x3df7b80,0x4200000,0x4728600,0x4b12780,0x4f1a180,0x5304300,0x582be80,0x5c16000,0x5edcb80,0x5edcb80,0x5edcb80,0x5edcb80,0x5edcb80,0x5edcb80,0x5edcb80,0x5edcb80,0x5edcb80,0x5edcb80,0x5edcb80,0x5edcb80,802,1794,2602,3594,4402,5394,6202,7194,8002,8994,9986,10794,11786,12594,13586,18478,15155,5192,16908,17224,17224,17224,17224,17224,20,19,0,0,0,0,0,0,0x5b4580,0xadcb80,0xee5000,0x140d600,0x1815a80,0x1d3e080,0x2146500,0x266eb00,0x2a76f80,0x2f9f580,0x34c7b80,0x38d0000,0x3df8600,0x4200a80,0x4729080,0x7ed80,0x4f1ac00,0x1be2380,0x582c900,0x5973600,0x5973600,0x5973600,0x5973600,0x5973600,0x5973600,0x5973600,0x5973600,0x5973600,0x5973600,0x5973600,0x5973600,0x5973600,803,1795,2603,3595,4403,5395,6203,7195,8003,8995,9987,10795,11787,12595,13587,14348,15156,15917,16909,17670,18243,18243,18243,18243,21,20,0,0,0,0,0,0,0x5b5000,0xadd600,0xee5a80,0x140e080,0x1816500,0x1d3eb00,0x2146f80,0x266f580,0x2a77a00,0x2fa0000,0x34c8600,0x38d0a80,0x3df9080,0x4201500,0x4729b00,0x4b13200,0x4f1b680,0x5304d80,0x582d380,0x5c16a80,0x5edd600,0x5edd600,0x5edd600,0x5edd600,0x5edd600,0x5edd600,0x5edd600,0x5edd600,0x5edd600,0x5edd600,0x5edd600,0x5edd600,804,1796,2604,3596,4404,5396,6204,7196,8004,8996,9988,10796,11788,12596,13588,14349,15157,15918,16910,17671,18244,18244,18244,18244,21,20,0,0,0,0,0,0,0x5b5a80,0xade080,0xee6500,0x140eb00,0x1816f80,0x1d3f580,0x2147a00,0x2670000,0x2a78480,0x2fa0a80,0x34c9080,0x38d1500,0x3df9b00,0x4201f80,0x472a580,0x4b13c80,0x4f1c100,0x5305800,0x582de00,0x5c17500,0x5ede080,0x5ede080,0x5ede080,0x5ede080,0x5ede080,0x5ede080,0x5ede080,0x5ede080,0x5ede080,0x5ede080,0x5ede080,0x5ede080,805,1797,2605,3597,4405,5397,6205,7197,8005,8997,9989,10797,11789,12597,13589,14350,15158,15919,16911,17672,18245,18245,18245,18245,21,20,0,0,0,0,0,0,0x5b6500,0xadeb00,0xee6f80,0x140f580,0x1817a00,0x1d40000,0x2148480,0x2670a80,0x2a78f00,0x2fa1500,0x34c9b00,0x38d1f80,0x3dfa580,0x4202a00,0x472b000,0x4b14700,0x4f1cb80,0x5306280,0x582e880,0x5c17f80,0x5edeb00,0x5edeb00,0x5edeb00,0x5edeb00,0x5edeb00,0x5edeb00,0x5edeb00,0x5edeb00,0x5edeb00,0x5edeb00,0x5edeb00,0x5edeb00,806,1798,2606,3598,4406,5398,6206,7198,8006,8998,9990,10798,11790,12598,13590,14351,15159,15920,16912,17673,18246,18246,18246,18246,21,20,0,0,0,0,0,0,0x5b6f80,0xadf580,0xee7a00,0x1410000,0x1818480,0x1d40a80,0x2148f00,0x2671500,0x2a79980,0x2fa1f80,0x34ca580,0x38d2a00,0x3dfb000,0x4203480,0x472ba80,0x4b15180,0x4f1d600,0x5306d00,0x582f300,0x5c18a00,0x5edf580,0x5edf580,0x5edf580,0x5edf580,0x5edf580,0x5edf580,0x5edf580,0x5edf580,0x5edf580,0x5edf580,0x5edf580,0x5edf580,807,1799,2607,3599,4407,5399,6207,7199,8007,8999,9991,10799,11791,12599,13591,14352,15160,15921,16913,17674,18247,18247,18247,18247,21,20,0,0,0,0,0,0,0x5b7a00,0xae0000,0xee8480,0x1410a80,0x1818f00,0x1d41500,0x2149980,0x2671f80,0x2a7a400,0x2fa2a00,0x34cb000,0x38d3480,0x3dfba80,0x4203f00,0x472c500,0x4b15c00,0x4f1e080,0x5307780,0x582fd80,0x5c19480,0x5ee0000,0x5ee0000,0x5ee0000,0x5ee0000,0x5ee0000,0x5ee0000,0x5ee0000,0x5ee0000,0x5ee0000,0x5ee0000,0x5ee0000,0x5ee0000};

__global__ __launch_bounds__(256, 1) void swin_main(const uint16_t* x, const uint16_t* mask, char* ws, uint16_t* out,
                                                    const uint16_t* wqkv, const uint16_t* wo, const uint16_t* ls,
                                                    const uint16_t* w1, const uint16_t* w2, const uint16_t* rc,
                                                    const int* ridx) {
  __shared__ __attribute__((aligned(16))) char smem[LDS_BYTES];
  const int wave = __builtin_amdgcn_readfirstlane(threadIdx.x >> 6);
  const unsigned lds0 = (unsigned)(size_t)smem;
  asm volatile(
      ".p2align 5\n"
      "s_load_dwordx8 s[16:23], %0, 0\n"
      "s_load_dwordx4 s[4:7], %0, 32\n"
      "s_load_dwordx8 s[80:87], %0, 48\n"
      "s_load_dwordx2 s[90:91], %0, 80\n"
      "s_mov_b32 s24, %1\n"
      "s_mov_b32 s25, %2\n"
      "s_mov_b64 s[60:61], %3\n"
      "s_getpc_b64 s[12:13]\n"
      "L_ipf0_%=:\n"
      "v_mbcnt_lo_u32_b32 v146, -1, 0\n"
      "v_mbcnt_hi_u32_b32 v146, -1, v146\n"
      "s_lshr_b32 s28, s25, 3\n"
      "s_lshl_b32 s29, s28, 2\n"
      "s_add_u32 s29, s29, s24\n"
      "s_mul_i32 s29, s29, 14\n"
      "v_min_u32_e64 v145, 13, v146\n"
      "v_add_lshl_u32 v145, v145, s29, 7\n"
      "v_min_u32 v145, 221056, v145\n"
      "v_lshlrev_b32_e64 v144, 4, v146\n"
      "s_lshl_b32 s29, s24, 6\n"
      "s_add_u32 s54, s60, s29\n"
      "s_addc_u32 s55, s61, 0\n"
      "s_and_b32 s29, s29, 64\n"
      "v_xor_b32_e64 v244, s29, v144\n"
      "s_mul_i32 s30, s24, 9216\n"
      "v_add_u32_e64 v244, s30, v244\n"
      "s_add_u32 s2, s30, 36864\n"
      "v_xor_b32 v147, 32, v244\n"
      "s_mov_b64 s[34:35], 0xffff\n"
      "v_cndmask_b32 v246, v147, v244, s[34:35]\n"
      "s_lshl_b64 s[34:35], s[34:35], 48\n"
      "v_cndmask_b32 v247, v244, v147, s[34:35]\n"
      "s_lshl_b64 s[34:35], -1, 32\n"
      "v_cndmask_b32 v245, v147, v244, s[34:35]\n"
      "s_waitcnt lgkmcnt(0)\n"
      "global_load_dword v31, v145, s[4:5]\n"
      "s_add_u32 m0, s2, 0\n"
      "s_nop 0\n"
      "global_load_lds_dwordx4 v244, s[4:5]\n"
      "global_load_lds_dwordx4 v244, s[4:5] offset:1024\n"
      "global_load_lds_dwordx4 v246, s[4:5] offset:2048\n"
      "global_load_lds_dwordx4 v147, s[4:5] offset:3072\n"
      "s_add_u32 s4, s4, 4096\n"
      "s_addc_u32 s5, s5, 0\n"
      "s_add_u32 m0, s2, 4096\n"
      "s_nop 0\n"
      "global_load_lds_dwordx4 v245, s[4:5]\n"
      "global_load_lds_dwordx4 v244, s[4:5] offset:1024\n"
      "global_load_lds_dwordx4 v247, s[4:5] offset:2048\n"
      "global_load_lds_dwordx4 v147, s[4:5] offset:3072\n"
      "s_add_u32 s4, s4, 4096\n"
      "s_addc_u32 s5, s5, 0\n"
      "s_add_u32 m0, s2, 8192\n"
      "s_nop 0\n"
      "global_load_lds_dwordx4 v147, s[4:5]\n"
      "s_add_u32 s4, s4, 28672\n"
      "s_addc_u32 s5, s5, 0\n"
      "s_load_dwordx16 s[36:51], s[82:83], 0\n"
      "s_load_dwordx2 s[8:9], s[80:81], 0\n"
      "s_load_dwordx16 s[64:79], s[54:55], 40960\n"
      "s_lshl_b32 s29, s24, 8\n"
      "v_lshl_add_u32 v201, v146, 2, s29\n"
      "v_min_u32 v201, 672, v201\n"
      "v_add_u32 v171, 149472, v201\n"
      "s_mul_i32 s29, s25, 4869\n"
      "s_add_u32 s34, s18, s29\n"
      "s_addc_u32 s35, s19, 0\n"
      "v_mov_b32 v168, s34\n"
      "v_mov_b32 v169, s35\n"
      "s_mul_i32 s29, s25, 19077\n"
      "s_add_u32 s34, s22, s29\n"
      "s_addc_u32 s35, s23, 0\n"
      "s_add_u32 s58, s16, s29\n"
      "s_addc_u32 s59, s17, 0\n"
      "s_add_u32 s29, s29, 116654080\n"
      "s_add_u32 s56, s20, s29\n"
      "s_addc_u32 s57, s21, 0\n"
      "v_writelane_b32 v168, s34, 1\n"
      "v_writelane_b32 v169, s35, 1\n"
      "v_writelane_b32 v168, s56, 2\n"
      "v_writelane_b32 v169, s57, 2\n"
      "v_writelane_b32 v168, s58, 3\n"
      "v_writelane_b32 v169, s59, 3\n"
      "v_writelane_b32 v168, s60, 4\n"
      "v_writelane_b32 v169, s61, 4\n"
      "v_writelane_b32 v168, s6, 5\n"
      "v_writelane_b32 v169, s7, 5\n"
      "v_writelane_b32 v168, s90, 6\n"
      "v_writelane_b32 v169, s91, 6\n"
      "v_writelane_b32 v168, s12, 7\n"
      "v_writelane_b32 v169, s13, 7\n"
      "s_add_u32 m0, s2, 36864\n"
      "s_nop 0\n"
      "global_load_lds_dwordx4 v244, s[4:5]\n"
      "global_load_lds_dwordx4 v244, s[4:5] offset:1024\n"
      "global_load_lds_dwordx4 v246, s[4:5] offset:2048\n"
      "global_load_lds_dwordx4 v147, s[4:5] offset:3072\n"
      "s_add_u32 s4, s4, 4096\n"
      "s_addc_u32 s5, s5, 0\n"
      "s_add_u32 m0, s2, 40960\n"
      "s_nop 0\n"
      "global_load_lds_dwordx4 v245, s[4:5]\n"
      "global_load_lds_dwordx4 v244, s[4:5] offset:1024\n"
      "global_load_lds_dwordx4 v247, s[4:5] offset:2048\n"
      "global_load_lds_dwordx4 v147, s[4:5] offset:3072\n"
      "s_add_u32 s4, s4, 4096\n"
      "s_addc_u32 s5, s5, 0\n"
      "s_add_u32 m0, s2, 45056\n"
      "s_nop 0\n"
      "global_load_lds_dwordx4 v147, s[4:5]\n"
      "s_add_u32 s4, s4, 32768\n"
      "s_addc_u32 s5, s5, 0\n"
      "global_load_dword v201, v201, s[86:87]\n"
      "global_load_ubyte v29, v[168:169], off\n"
      "v_add_u32 v203, 4096, v147\n"
      "v_add_u32 v168, s2, v144\n"
      "v_lshrrev_b32 v169, 2, v146\n"
      "global_load_dwordx4 a[72:75], v244, s[4:5] offset:-4096\n"
      "v_and_b32 v170, 3, v169\n"
      "v_lshrrev_b32 v202, 4, v146\n"
      "v_lshl_add_u32 v170, v202, 3, v170\n"
      "v_mul_u32_u24 v170, 1152, v170\n"
      "global_load_dwordx4 a[76:79], v244, s[4:5] offset:-3072\n"
      "v_and_b32 v202, 1, v146\n"
      "v_lshl_add_u32 v170, v202, 3, v170\n"
      "v_add_u32 v170, 36864, v170\n"
      "global_load_dwordx4 a[80:83], v246, s[4:5] offset:-2048\n"
      "v_and_b32 v169, 6, v169\n"
      "v_bfe_u32 v202, v146, 1, 1\n"
      "s_mul_i32 s29, s24, 6\n"
      "v_add_u32 v202, s29, v202\n"
      "global_load_dwordx4 a[84:87], v147, s[4:5] offset:-1024\n"
      "v_xor_b32_e64 v250, v202, v169\n"
      "v_lshl_add_u32 v250, v250, 4, v170\n"
      "v_add_u32 v202, 2, v202\n"
      "global_load_dwordx4 a[88:91], v245, s[4:5]\n"
      "v_xor_b32 v251, v202, v169\n"
      "v_lshl_add_u32 v251, v251, 4, v170\n"
      "v_add_u32 v202, 2, v202\n"
      "global_load_dwordx4 a[92:95], v244, s[4:5] offset:1024\n"
      "v_xor_b32_e64 v200, v202, v169\n"
      "v_lshl_add_u32 v200, v200, 4, v170\n"
      "s_movk_i32 s29, 18\n"
      "s_mov_b32 s30, 73600\n"
      "global_load_dwordx4 a[96:99], v247, s[4:5] offset:2048\n"
      "s_cmp_eq_u32 s24, 1\n"
      "s_cselect_b64 s[12:13], s[6:7], s[12:13]\n"
      "s_cmp_eq_u32 s24, 2\n"
      "global_load_dwordx4 a[100:103], v147, s[4:5] offset:3072\n"
      "s_cselect_b32 s29, 16, s29\n"
      "s_cselect_b32 s30, L_cend_%=-L_ipf0_%=-128, s30\n"
      "s_cmp_eq_u32 s24, 3\n"
      "s_cselect_b64 s[12:13], s[60:61], s[12:13]\n"
      "global_load_dwordx4 a[104:107], v203, s[4:5]\n"
      "s_cselect_b32 s29, 10, s29\n"
      "s_cselect_b32 s30, 40832, s30\n"
      "s_cmp_eq_u32 s24, 0\n"
      "s_cselect_b64 s[12:13], s[90:91], s[12:13]\n"
      "s_cselect_b32 s29, 3, s29\n"
      "s_cselect_b32 s30, 9476, s30\n"
      "s_mul_i32 s28, s28, s29\n"
      "s_sub_u32 s29, s29, 1\n"
      "v_add_u32 v244, 36864, v244\n"
      "v_add_u32 v147, 36864, v147\n"
      "v_add_u32 v246, 36864, v246\n"
      "v_add_u32 v245, 36864, v245\n"
      "v_add_u32 v247, 36864, v247\n"
      "v_add_u32 v203, 36864, v203\n"
      "global_load_dwordx4 a[108:111], v244, s[4:5] offset:-4096\n"
      "v_min_u32 v145, s29, v146\n"
      "v_add_lshl_u32 v145, v145, s28, 7\n"
      "v_min_u32 v145, s30, v145\n"
      "global_load_dword v30, v145, s[12:13]\n"
      "s_mul_i32 s28, s24, 10240\n"
      "s_add_u32 s28, s28, 4096\n"
      "v_add_u32 v169, s28, v144\n"
      "v_add_u32 v145, 8192, v169\n"
      "s_lshl_b32 s28, s25, 8\n"
      "global_load_dwordx4 a[112:115], v244, s[4:5] offset:-3072\n"
      "s_add_u32 s52, s60, s28\n"
      "s_addc_u32 s53, s61, 0\n"
      "s_lshl_b32 s28, s24, 5\n"
      "global_load_dwordx4 a[116:119], v246, s[4:5] offset:-2048\n"
      "v_lshrrev_b32 v202, 3, v146\n"
      "v_lshl_add_u32 v170, v202, 2, s28\n"
      "v_add_u32 v170, 41408, v170\n"
      "v_xor_b32 v202, v202, v146\n"
      "global_load_dwordx4 a[120:123], v147, s[4:5] offset:-1024\n"
      "v_and_b32_e64 v202, 7, v202\n"
      "s_waitcnt vmcnt(16)\n"
      "s_waitcnt lgkmcnt(0)\n"
      "s_barrier\n"
      "ds_read_b64_tr_b16 a[0:1], v250\n"
      "ds_read_b64_tr_b16 a[2:3], v250 offset:4608\n"
      "ds_read_b64_tr_b16 a[4:5], v251\n"
      "global_load_dwordx4 a[124:127], v245, s[4:5]\n"
      "ds_read_b64_tr_b16 a[6:7], v251 offset:4608\n"
      "ds_read_b64_tr_b16 a[8:9], v200\n"
      "ds_read_b64_tr_b16 a[10:11], v200 offset:4608\n"
      "ds_read_b64_tr_b16 a[12:13], v250 offset:384\n"
      "ds_read_b64_tr_b16 a[14:15], v250 offset:4992\n"
      "global_load_dwordx4 a[128:131], v244, s[4:5] offset:1024\n"
      "ds_read_b64_tr_b16 a[16:17], v251 offset:384\n"
      "ds_read_b64_tr_b16 a[18:19], v251 offset:4992\n"
      "ds_read_b64_tr_b16 a[20:21], v200 offset:384\n"
      "ds_read_b64_tr_b16 a[22:23], v200 offset:4992\n"
      "ds_read_b64_tr_b16 a[24:25], v250 offset:768\n"
      "global_load_dwordx4 a[132:135], v247, s[4:5] offset:2048\n"
      "ds_read_b64_tr_b16 a[26:27], v250 offset:5376\n"
      "ds_read_b64_tr_b16 a[28:29], v251 offset:768\n"
      "ds_read_b64_tr_b16 a[30:31], v251 offset:5376\n"
      "ds_read_b64_tr_b16 a[32:33], v200 offset:768\n"
      "ds_read_b64_tr_b16 a[34:35], v200 offset:5376\n"
      "ds_read_b64_tr_b16 a[36:37], v250 offset:36864\n"
      "global_load_dwordx4 a[136:139], v147, s[4:5] offset:3072\n"
      "ds_read_b64_tr_b16 a[38:39], v250 offset:41472\n"
      "ds_read_b64_tr_b16 a[40:41], v251 offset:36864\n"
      "ds_read_b64_tr_b16 a[42:43], v251 offset:41472\n"
      "ds_read_b64_tr_b16 a[44:45], v200 offset:36864\n"
      "ds_read_b64_tr_b16 a[46:47], v200 offset:41472\n"
      "global_load_dwordx4 a[140:143], v203, s[4:5]\n"
      "ds_read_b64_tr_b16 a[48:49], v250 offset:37248\n"
      "ds_read_b64_tr_b16 a[50:51], v250 offset:41856\n"
      "ds_read_b64_tr_b16 a[52:53], v251 offset:37248\n"
      "ds_read_b64_tr_b16 a[54:55], v251 offset:41856\n"
      "ds_read_b64_tr_b16 a[56:57], v200 offset:37248\n"
      "global_load_dword v170, v170, s[52:53]\n"
      "ds_read_b64_tr_b16 a[58:59], v200 offset:41856\n"
      "ds_read_b64_tr_b16 a[60:61], v250 offset:37632\n"
      "ds_read_b64_tr_b16 a[62:63], v250 offset:42240\n"
      "ds_read_b64_tr_b16 a[64:65], v251 offset:37632\n"
      "ds_read_b64_tr_b16 a[66:67], v251 offset:42240\n"
      "ds_read_b64_tr_b16 a[68:69], v200 offset:37632\n"
      "ds_read_b64_tr_b16 a[70:71], v200 offset:42240\n"
      "s_pack_ll_b32_b16 s28, s36, s44\n"
      "s_waitcnt vmcnt(21)\n"
      "v_dot2_f32_bf16 v248, s28, v201, 0\n"
      "s_pack_hh_b32_b16 s29, s36, s44\n"
      "v_dot2_f32_bf16 v146, s29, v201, 0\n"
      "s_pack_ll_b32_b16 s30, s37, s45\n"
      "v_dot2_f32_bf16 v249, s30, v201, 0\n"
      "v_add_u32 v244, 36864, v244\n"
      "v_add_u32 v147, 36864, v147\n"
      "v_add_u32 v246, 36864, v246\n"
      "v_add_u32 v245, 36864, v245\n"
      "v_add_u32 v247, 36864, v247\n"
      "v_add_u32 v203, 36864, v203\n"
      "global_load_dwordx4 a[144:147], v244, s[4:5] offset:-4096\n"
      "s_pack_hh_b32_b16 s31, s37, s45\n"
      "v_dot2_f32_bf16 v142, s31, v201, 0\n"
      "s_pack_ll_b32_b16 s28, s38, s46\n"
      "v_dot2_f32_bf16 v140, s28, v201, 0\n"
      "s_pack_hh_b32_b16 s29, s38, s46\n"
      "v_dot2_f32_bf16 v206, s29, v201, 0\n"
      "s_pack_ll_b32_b16 s30, s39, s47\n"
      "v_dot2_f32_bf16 v143, s30, v201, 0\n"
      "s_pack_hh_b32_b16 s31, s39, s47\n"
      "v_dot2_f32_bf16 v141, s31, v201, 0\n"
      "s_pack_ll_b32_b16 s28, s40, s48\n"
      "v_dot2_f32_bf16 v205, s28, v201, 0\n"
      "s_pack_hh_b32_b16 s29, s40, s48\n"
      "global_load_dwordx4 a[148:151], v244, s[4:5] offset:-3072\n"
      "v_dot2_f32_bf16 v213, s29, v201, 0\n"
      "s_pack_ll_b32_b16 s30, s41, s49\n"
      "v_dot2_f32_bf16 v207, s30, v201, 0\n"
      "s_pack_hh_b32_b16 s31, s41, s49\n"
      "v_dot2_f32_bf16 v212, s31, v201, 0\n"
      "global_load_dwordx4 a[152:155], v246, s[4:5] offset:-2048\n"
      "s_pack_ll_b32_b16 s28, s42, s50\n"
      "v_dot2_f32_bf16 v227, s28, v201, 0\n"
      "s_pack_hh_b32_b16 s29, s42, s50\n"
      "v_dot2_f32_bf16 v204, s29, v201, 0\n"
      "s_pack_ll_b32_b16 s30, s43, s51\n"
      "global_load_dwordx4 a[156:159], v147, s[4:5] offset:-1024\n"
      "v_dot2_f32_bf16 v225, s30, v201, 0\n"
      "s_pack_hh_b32_b16 s31, s43, s51\n"
      "v_dot2_f32_bf16 v215, s31, v201, 0\n"
      "s_load_dwordx16 s[36:51], s[84:85], 0\n"
      "v_max_f32 v248, 0, v248\n"
      "global_load_dwordx4 a[160:163], v245, s[4:5]\n"
      "v_max_f32 v146, 0, v146\n"
      "v_cvt_pk_bf16_f32 v248, v248, v146\n"
      "v_max_f32 v249, 0, v249\n"
      "v_max_f32_e64 v142, 0, v142\n"
      "v_cvt_pk_bf16_f32 v249, v249, v142\n"
      "global_load_dwordx4 a[164:167], v244, s[4:5] offset:1024\n"
      "v_max_f32 v140, 0, v140\n"
      "v_max_f32 v206, 0, v206\n"
      "v_cvt_pk_bf16_f32 v140, v140, v206\n"
      "v_max_f32 v143, 0, v143\n"
      "v_max_f32 v141, 0, v141\n"
      "v_cvt_pk_bf16_f32 v143, v143, v141\n"
      "global_load_dwordx4 a[168:171], v247, s[4:5] offset:2048\n"
      "v_max_f32 v205, 0, v205\n"
      "v_max_f32 v213, 0, v213\n"
      "v_cvt_pk_bf16_f32 v205, v205, v213\n"
      "v_max_f32 v207, 0, v207\n"
      "v_max_f32 v212, 0, v212\n"
      "global_load_dwordx4 a[172:175], v147, s[4:5] offset:3072\n"
      "v_cvt_pk_bf16_f32 v207, v207, v212\n"
      "v_max_f32 v227, 0, v227\n"
      "v_max_f32 v204, 0, v204\n"
      "v_cvt_pk_bf16_f32 v227, v227, v204\n"
      "v_max_f32 v225, 0, v225\n"
      "global_load_dwordx4 a[176:179], v203, s[4:5]\n"
      "v_max_f32_e64 v215, 0, v215\n"
      "v_cvt_pk_bf16_f32 v225, v225, v215\n"
      "s_waitcnt lgkmcnt(0)\n"
      "s_barrier\n"
      "s_pack_ll_b32_b16 s28, s36, s38\n"
      "v_dot2_f32_bf16 v212, s28, v248, 0\n"
      "s_pack_hh_b32_b16 s29, s36, s38\n"
      "v_dot2_f32_bf16 v213, s29, v248, 0\n"
      "s_pack_ll_b32_b16 s30, s37, s39\n"
      "v_dot2_f32_bf16 v214, s30, v248, 0\n"
      "v_add_u32 v244, 36864, v244\n"
      "v_add_u32 v147, 36864, v147\n"
      "v_add_u32 v246, 36864, v246\n"
      "v_add_u32 v245, 36864, v245\n"
      "v_add_u32 v247, 36864, v247\n"
      "v_add_u32 v203, 36864, v203\n"
      "global_load_dwordx4 a[180:183], v244, s[4:5] offset:-4096\n"
      "s_pack_hh_b32_b16 s31, s37, s39\n"
      "v_dot2_f32_bf16 v215, s31, v248, 0\n"
      "s_pack_ll_b32_b16 s28, s40, s42\n"
      "v_dot2_f32_bf16 v212, s28, v249, v212\n"
      "s_pack_hh_b32_b16 s29, s40, s42\n"
      "v_dot2_f32_bf16 v213, s29, v249, v213\n"
      "global_load_dwordx4 a[184:187], v244, s[4:5] offset:-3072\n"
      "s_pack_ll_b32_b16 s30, s41, s43\n"
      "v_dot2_f32_bf16 v214, s30, v249, v214\n"
      "s_pack_hh_b32_b16 s31, s41, s43\n"
      "global_load_dwordx4 a[188:191], v246, s[4:5] offset:-2048\n"
      "v_dot2_f32_bf16 v215, s31, v249, v215\n"
      "s_pack_ll_b32_b16 s28, s44, s46\n"
      "global_load_dwordx4 a[192:195], v147, s[4:5] offset:-1024\n"
      "v_dot2_f32_bf16 v212, s28, v140, v212\n"
      "s_pack_hh_b32_b16 s29, s44, s46\n"
      "v_dot2_f32_bf16 v213, s29, v140, v213\n"
      "global_load_dwordx4 a[196:199], v245, s[4:5]\n"
      "s_pack_ll_b32_b16 s30, s45, s47\n"
      "v_dot2_f32_bf16 v214, s30, v140, v214\n"
      "s_pack_hh_b32_b16 s31, s45, s47\n"
      "global_load_dwordx4 a[200:203], v244, s[4:5] offset:1024\n"
      "v_dot2_f32_bf16 v215, s31, v140, v215\n"
      "s_pack_ll_b32_b16 s28, s48, s50\n"
      "global_load_dwordx4 a[204:207], v247, s[4:5] offset:2048\n"
      "v_dot2_f32_bf16 v212, s28, v143, v212\n"
      "s_pack_hh_b32_b16 s29, s48, s50\n"
      "v_dot2_f32_bf16 v213, s29, v143, v213\n"
      "global_load_dwordx4 a[208:211], v147, s[4:5] offset:3072\n"
      "s_pack_ll_b32_b16 s30, s49, s51\n"
      "v_dot2_f32_bf16 v214, s30, v143, v214\n"
      "global_load_dwordx4 a[212:215], v203, s[4:5]\n"
      "s_pack_hh_b32_b16 s31, s49, s51\n"
      "v_dot2_f32_bf16 v215, s31, v143, v215\n"
      "s_load_dwordx16 s[36:51], s[84:85], 64\n"
      "s_lshl_b32 s28, s24, 4\n"
      "s_lshr_b64 s[8:9], s[8:9], s28\n"
      "s_lshl_b32 s8, s8, 16\n"
      "v_mov_b32_e64 v203, s8\n"
      "v_max_f32 v203, 0xc2c80000, v203\n"
      "v_min_f32 v203, 0x40930000, v203\n"
      "v_mul_f32 v203, 0x3fb8aa3b, v203\n"
      "v_exp_f32 v203, v203\n"
      "s_nop 0\n"
      "v_cvt_pk_bf16_f32 v203, v203, v203\n"
      "v_lshlrev_b32 v203, 16, v203\n"
      "s_waitcnt vmcnt(37)\n"
      "ds_write_b128 v168, a[72:75]\n"
      "s_waitcnt vmcnt(18)\n"
      "v_lshl_add_u32 v170, v202, 4, v170\n"
      "s_add_u32 m0, s76, 18432\n"
      "s_nop 0\n"
      "global_load_lds_dwordx4 v170, s[16:17]\n"
      "s_add_u32 m0, s76, 24448\n"
      "s_nop 0\n"
      "global_load_lds_dwordx4 v170, s[16:17] offset:128\n"
      "s_add_u32 m0, s76, 30464\n"
      "s_nop 0\n"
      "global_load_lds_dwordx4 v170, s[16:17] offset:256\n"
      "v_add_u32 v202, s76, v144\n"
      "s_add_u32 m0, s76, 124928\n"
      "s_nop 0\n"
      "global_load_lds_dwordx4 v202, s[90:91]\n"
      "v_add_u32 v202, 4096, v202\n"
      "s_add_u32 m0, s76, 129024\n"
      "s_nop 0\n"
      "global_load_lds_dwordx4 v202, s[90:91]\n"
      "v_add_u32 v202, 4096, v202\n"
      "v_min_u32 v202, 9600, v202\n"
      "s_add_u32 m0, s76, 133120\n"
      "s_nop 0\n"
      "global_load_lds_dwordx4 v202, s[90:91]\n"
      "ds_write_b128 v168, a[76:79] offset:1024\n"
      "ds_write_b128 v168, a[80:83] offset:2048\n"
      "ds_write_b128 v168, a[84:87] offset:3072\n"
      "ds_write_b128 v168, a[88:91] offset:4096\n"
      "ds_write_b128 v168, a[92:95] offset:5120\n"
      "ds_write_b128 v168, a[96:99] offset:6144\n"
      "ds_write_b128 v168, a[100:103] offset:7168\n"
      "ds_write_b128 v168, a[104:107] offset:8192\n"
      "ds_write_b128 v168, a[108:111] offset:36864\n"
      "ds_write_b128 v168, a[112:115] offset:37888\n"
      "ds_write_b128 v168, a[116:119] offset:38912\n"
      "ds_write_b128 v168, a[120:123] offset:39936\n"
      "ds_write_b128 v168, a[124:127] offset:40960\n"
      "ds_write_b128 v168, a[128:131] offset:41984\n"
      "global_load_dwordx4 v[244:247], v169, s[60:61] offset:3072\n"
      "ds_write_b128 v168, a[132:135] offset:43008\n"
      "ds_write_b128 v168, a[136:139] offset:44032\n"
      "global_load_dwordx4 v[114:117], v169, s[60:61] offset:-4096\n"
      "ds_write_b128 v168, a[140:143] offset:45056\n"
      "s_waitcnt lgkmcnt(0)\n"
      "s_barrier\n"
      "ds_read_b64_tr_b16 a[72:73], v250\n"
      "ds_read_b64_tr_b16 a[74:75], v250 offset:4608\n"
      "ds_read_b64_tr_b16 a[76:77], v251\n"
      "ds_read_b64_tr_b16 a[78:79], v251 offset:4608\n"
      "ds_read_b64_tr_b16 a[80:81], v200\n"
      "ds_read_b64_tr_b16 a[82:83], v200 offset:4608\n"
      "ds_read_b64_tr_b16 a[84:85], v250 offset:384\n"
      "ds_read_b64_tr_b16 a[86:87], v250 offset:4992\n"
      "ds_read_b64_tr_b16 a[88:89], v251 offset:384\n"
      "s_waitcnt vmcnt(1)\n"
      "v_add_u32 v201, 3072, v247\n"
      "global_load_dwordx4 a[216:219], v201, s[6:7] offset:-3072\n"
      "ds_read_b64_tr_b16 a[90:91], v251 offset:4992\n"
      "ds_read_b64_tr_b16 a[92:93], v200 offset:384\n"
      "ds_read_b64_tr_b16 a[94:95], v200 offset:4992\n"
      "ds_read_b64_tr_b16 a[96:97], v250 offset:768\n"
      "ds_read_b64_tr_b16 a[98:99], v250 offset:5376\n"
      "ds_read_b64_tr_b16 a[100:101], v251 offset:768\n"
      "ds_read_b64_tr_b16 a[102:103], v251 offset:5376\n"
      "ds_read_b64_tr_b16 a[104:105], v200 offset:768\n"
      "ds_read_b64_tr_b16 a[106:107], v200 offset:5376\n"
      "ds_read_b64_tr_b16 a[108:109], v250 offset:36864\n"
      "ds_read_b64_tr_b16 a[110:111], v250 offset:41472\n"
      "ds_read_b64_tr_b16 a[112:113], v251 offset:36864\n"
      "ds_read_b64_tr_b16 a[114:115], v251 offset:41472\n"
      "ds_read_b64_tr_b16 a[116:117], v200 offset:36864\n"
      "global_load_dwordx4 a[220:223], v201, s[6:7] offset:-2944\n"
      "ds_read_b64_tr_b16 a[118:119], v200 offset:41472\n"
      "ds_read_b64_tr_b16 a[120:121], v250 offset:37248\n"
      "ds_read_b64_tr_b16 a[122:123], v250 offset:41856\n"
      "ds_read_b64_tr_b16 a[124:125], v251 offset:37248\n"
      "ds_read_b64_tr_b16 a[126:127], v251 offset:41856\n"
      "ds_read_b64_tr_b16 a[128:129], v200 offset:37248\n"
      "ds_read_b64_tr_b16 a[130:131], v200 offset:41856\n"
      "ds_read_b64_tr_b16 a[132:133], v250 offset:37632\n"
      "ds_read_b64_tr_b16 a[134:135], v250 offset:42240\n"
      "ds_read_b64_tr_b16 a[136:137], v251 offset:37632\n"
      "ds_read_b64_tr_b16 a[138:139], v251 offset:42240\n"
      "ds_read_b64_tr_b16 a[140:141], v200 offset:37632\n"
      "global_load_dwordx4 a[224:227], v201, s[6:7] offset:-2816\n"
      "ds_read_b64_tr_b16 a[142:143], v200 offset:42240\n"
      "s_pack_ll_b32_b16 s28, s36, s38\n"
      "v_dot2_f32_bf16 v212, s28, v205, v212\n"
      "s_pack_hh_b32_b16 s29, s36, s38\n"
      "v_dot2_f32_bf16 v213, s29, v205, v213\n"
      "s_pack_ll_b32_b16 s30, s37, s39\n"
      "v_dot2_f32_bf16 v214, s30, v205, v214\n"
      "s_pack_hh_b32_b16 s31, s37, s39\n"
      "v_dot2_f32_bf16 v215, s31, v205, v215\n"
      "s_pack_ll_b32_b16 s28, s40, s42\n"
      "v_dot2_f32_bf16 v212, s28, v207, v212\n"
      "global_load_dwordx4 a[228:231], v201, s[6:7]\n"
      "s_pack_hh_b32_b16 s29, s40, s42\n"
      "v_dot2_f32_bf16 v213, s29, v207, v213\n"
      "s_pack_ll_b32_b16 s30, s41, s43\n"
      "v_dot2_f32_bf16 v214, s30, v207, v214\n"
      "s_pack_hh_b32_b16 s31, s41, s43\n"
      "v_dot2_f32_bf16 v215, s31, v207, v215\n"
      "s_pack_ll_b32_b16 s28, s44, s46\n"
      "v_dot2_f32_bf16 v212, s28, v227, v212\n"
      "s_pack_hh_b32_b16 s29, s44, s46\n"
      "v_dot2_f32_bf16 v213, s29, v227, v213\n"
      "s_pack_ll_b32_b16 s30, s45, s47\n"
      "v_dot2_f32_bf16 v214, s30, v227, v214\n"
      "global_load_dwordx4 a[232:235], v201, s[6:7] offset:128\n"
      "s_pack_hh_b32_b16 s31, s45, s47\n"
      "v_dot2_f32_bf16 v215, s31, v227, v215\n"
      "s_pack_ll_b32_b16 s28, s48, s50\n"
      "v_dot2_f32_bf16 v212, s28, v225, v212\n"
      "s_pack_hh_b32_b16 s29, s48, s50\n"
      "v_dot2_f32_bf16 v213, s29, v225, v213\n"
      "s_pack_ll_b32_b16 s30, s49, s51\n"
      "v_dot2_f32_bf16 v214, s30, v225, v214\n"
      "s_pack_hh_b32_b16 s31, s49, s51\n"
      "v_dot2_f32_bf16 v215, s31, v225, v215\n"
      "s_mov_b64 s[48:49], s[60:61]\n"
      "global_load_dwordx4 a[236:239], v201, s[6:7] offset:256\n"
      "s_load_dwordx8 s[36:43], s[60:61], 41216\n"
      "s_load_dwordx4 s[44:47], s[60:61], 41248\n"
      "s_load_dwordx16 s[80:95], s[52:53], 41280\n"
      "s_load_dwordx4 s[96:99], s[52:53], 41344\n"
      "s_load_dword s100, s[52:53], 41360\n"
      "s_load_dword s26, s[52:53], 41376\n"
      "s_load_dword s3, s[52:53], 41380\n"
      "v_cmp_nge_f32 vcc, 0x41f00000, v203\n"
      "v_mul_f32 v203, 0x3fb8aa3b, v203\n"
      "s_mov_b64 s[50:51], vcc\n"
      "v_cvt_pk_bf16_f32 v213, v212, v213\n"
      "v_lshlrev_b32 v212, 16, v213\n"
      "v_and_b32 v213, 0xffff0000, v213\n"
      "v_cvt_pk_bf16_f32 v215, v214, v215\n"
      "v_lshlrev_b32_e64 v214, 16, v215\n"
      "v_add_u32 v201, 9216, v247\n"
      "global_load_dwordx4 a[240:243], v201, s[6:7] offset:-3072\n"
      "v_and_b32 v215, 0xffff0000, v215\n"
      "v_mul_f32 v212, 0xbfb8aa3b, v212\n"
      "v_mul_f32 v213, 0xbfb8aa3b, v213\n"
      "v_mul_f32 v214, 0xbfb8aa3b, v214\n"
      "v_mul_f32 v215, 0xbfb8aa3b, v215\n"
      "v_exp_f32 v212, v212\n"
      "v_exp_f32 v213, v213\n"
      "v_exp_f32 v214, v214\n"
      "v_exp_f32 v215, v215\n"
      "v_add_f32 v212, 1.0, v212\n"
      "v_add_f32 v213, 1.0, v213\n"
      "v_add_f32 v214, 1.0, v214\n"
      "v_add_f32 v215, 1.0, v215\n"
      "v_rcp_f32 v212, v212\n"
      "global_load_dwordx4 a[244:247], v201, s[6:7] offset:-2944\n"
      "v_rcp_f32 v213, v213\n"
      "v_rcp_f32_e64 v214, v214\n"
      "v_rcp_f32_e64 v215, v215\n"
      "v_cvt_pk_bf16_f32 v213, v212, v213\n"
      "v_lshlrev_b32_e64 v212, 16, v213\n"
      "v_and_b32 v213, 0xffff0000, v213\n"
      "v_cvt_pk_bf16_f32 v215, v214, v215\n"
      "v_lshlrev_b32_e64 v214, 16, v215\n"
      "v_and_b32 v215, 0xffff0000, v215\n"
      "v_mul_f32 v212, 0x41b8aa3b, v212\n"
      "ds_write_b32 v171, v212\n"
      "v_mul_f32 v213, 0x41b8aa3b, v213\n"
      "global_load_dwordx4 a[248:251], v201, s[6:7] offset:-2816\n"
      "ds_write_b32 v171, v213 offset:768\n"
      "v_mul_f32 v214, 0x41b8aa3b, v214\n"
      "ds_write_b32 v171, v214 offset:1536\n"
      "v_mul_f32 v215, 0x41b8aa3b, v215\n"
      "ds_write_b32 v171, v215 offset:2304\n"
      "s_waitcnt lgkmcnt(0)\n"
      "s_barrier\n"
      "v_readfirstlane_b32 s36, v203\n"
      "L_sched_%=:\n"
      "s_mov_b32 s11, 100466688\n"
      "s_sub_u32 s16, s16, 99876864\n"
      "s_subb_u32 s17, s17, 0\n"
      "s_sub_u32 s22, s22, 99876864\n"
      "s_subb_u32 s23, s23, 0\n"
      "global_load_dwordx4 v[118:121], v169, s[48:49] offset:-3072\n"
      "s_add_u32 s12, s20, 16777216\n"
      "s_addc_u32 s13, s21, 0\n"
      "v_mov_b32 v36, 0\n"
      "v_mov_b32 v37, 0\n"
      "v_mov_b32 v42, 0\n"
      "v_mov_b32 v43, 0\n"
      "v_mov_b32 v48, 0\n"
      "v_mov_b32 v49, 0\n"
      "v_mov_b32 v53, 0\n"
      "global_load_dwordx4 v[122:125], v169, s[48:49] offset:-2048\n"
      "v_mov_b32 v57, 0\n"
      "v_mov_b32 v61, 0\n"
      "v_mov_b32 v65, 0\n"
      "ds_write_b128 v168, a[144:147]\n"
      "ds_write_b128 v168, a[148:151] offset:1024\n"
      "ds_write_b128 v168, a[152:155] offset:2048\n"
      "ds_write_b128 v168, a[156:159] offset:3072\n"
      "ds_write_b128 v168, a[160:163] offset:4096\n"
      "global_load_dwordx4 v[184:187], v145, s[48:49] offset:-3072\n"
      "ds_write_b128 v168, a[164:167] offset:5120\n"
      "ds_write_b128 v168, a[168:171] offset:6144\n"
      "ds_write_b128 v168, a[172:175] offset:7168\n"
      "ds_write_b128 v168, a[176:179] offset:8192\n"
      "ds_write_b128 v168, a[180:183] offset:36864\n"
      "ds_write_b128 v168, a[184:187] offset:37888\n"
      "ds_write_b128 v168, a[188:191] offset:38912\n"
      "ds_write_b128 v168, a[192:195] offset:39936\n"
      "ds_write_b128 v168, a[196:199] offset:40960\n"
      "global_load_dwordx4 v[212:215], v145, s[48:49] offset:-4096\n"
      "ds_write_b128 v168, a[200:203] offset:41984\n"
      "ds_write_b128 v168, a[204:207] offset:43008\n"
      "ds_write_b128 v168, a[208:211] offset:44032\n"
      "ds_write_b128 v168, a[212:215] offset:45056\n"
      "s_waitcnt lgkmcnt(0)\n"
      "s_barrier\n"
      "v_mov_b32 v189, 134528\n"
      "v_add_u32 v168, 3136, v244\n"
      "v_add_u32 v171, 6272, v244\n"
      "s_mov_b32 s60, 100270464\n"
      "ds_read_b64_tr_b16 a[144:145], v250\n"
      "ds_read_b64_tr_b16 a[146:147], v250 offset:4608\n"
      "s_mov_b32 s61, 196224\n"
      "ds_read_b64_tr_b16 a[148:149], v251\n"
      "s_lshr_b32 s8, s80, 8\n"
      "s_and_b32 s9, s80, 0xff\n"
      "ds_read_b64_tr_b16 a[150:151], v251 offset:4608\n"
      "s_mul_i32 s10, s8, 73\n"
      "ds_read_b64_tr_b16 a[152:153], v200\n"
      "s_add_u32 s10, s10, s9\n"
      "ds_read_b64_tr_b16 a[154:155], v200 offset:4608\n"
      "s_mul_i32 s28, s10, 4802\n"
      "ds_read_b64_tr_b16 a[156:157], v250 offset:384\n"
      "s_add_u32 s56, s18, s28\n"
      "s_addc_u32 s57, s19, 0\n"
      "ds_read_b64_tr_b16 a[158:159], v250 offset:4992\n"
      "s_and_b32 s56, s56, -16\n"
      "ds_read_b64_tr_b16 a[160:161], v251 offset:384\n"
      "s_sub_u32 s58, s56, 110592\n"
      "ds_read_b64_tr_b16 a[162:163], v251 offset:4992\n"
      "s_subb_u32 s59, s57, 0\n"
      "ds_read_b64_tr_b16 a[164:165], v200 offset:384\n"
      "s_sub_u32 s56, s56, 73728\n"
      "s_subb_u32 s57, s57, 0\n"
      "ds_read_b64_tr_b16 a[166:167], v200 offset:4992\n"
      "s_mul_i32 s28, s8, 1373568\n"
      "ds_read_b64_tr_b16 a[168:169], v250 offset:768\n"
      "v_add_u32 v170, 99876864, v170\n"
      "s_mul_i32 s29, s9, 2688\n"
      "ds_read_b64_tr_b16 a[170:171], v250 offset:5376\n"
      "s_add_u32 s28, s28, s29\n"
      "ds_read_b64_tr_b16 a[172:173], v251 offset:768\n"
      "s_waitcnt vmcnt(1)\n"
      "v_mov_b32 v66, v184\n"
      "s_cmp_eq_u32 s8, 72\n"
      "s_cselect_b32 s8, 0, 100270464\n"
      "ds_read_b64_tr_b16 a[174:175], v251 offset:5376\n"
      "v_mov_b32_e64 v156, v185\n"
      "s_cmp_eq_u32 s9, 72\n"
      "ds_read_b64_tr_b16 a[176:177], v200 offset:768\n"
      "v_mov_b32 v67, v184\n"
      "s_cselect_b32 s9, 0, 196224\n"
      "ds_read_b64_tr_b16 a[178:179], v200 offset:5376\n"
      "v_mov_b32 v157, v185\n"
      "s_add_u32 s29, s8, s9\n"
      "ds_read_b64_tr_b16 a[180:181], v250 offset:36864\n"
      "v_mov_b32 v68, v184\n"
      "s_add_u32 s28, s28, s29\n"
      "s_add_u32 s52, s16, s28\n"
      "ds_read_b64_tr_b16 a[182:183], v250 offset:41472\n"
      "v_mov_b32 v158, v185\n"
      "s_addc_u32 s53, s17, 0\n"
      "ds_read_b64_tr_b16 a[184:185], v251 offset:36864\n"
      "v_mov_b32 v69, v184\n"
      "s_cmp_lg_u32 s29, s11\n"
      "ds_read_b64_tr_b16 a[186:187], v251 offset:41472\n"
      "v_mov_b32_e64 v159, v185\n"
      "ds_read_b64_tr_b16 a[188:189], v200 offset:36864\n"
      "v_mov_b32_e64 v184, 0\n"
      "ds_read_b64_tr_b16 a[190:191], v200 offset:41472\n"
      "v_mov_b32_e64 v185, 0\n"
      "ds_read_b64_tr_b16 a[192:193], v250 offset:37248\n"
      "v_mov_b32_e64 v188, 0\n"
      "ds_read_b64_tr_b16 a[194:195], v250 offset:41856\n"
      "ds_read_b64_tr_b16 a[196:197], v251 offset:37248\n"
      "ds_read_b64_tr_b16 a[198:199], v251 offset:41856\n"
      "ds_read_b64_tr_b16 a[200:201], v200 offset:37248\n"
      "ds_read_b64_tr_b16 a[202:203], v200 offset:41856\n"
      "ds_read_b64_tr_b16 a[204:205], v250 offset:37632\n"
      "ds_read_b64_tr_b16 a[206:207], v250 offset:42240\n"
      "ds_read_b64_tr_b16 a[208:209], v251 offset:37632\n"
      "ds_read_b64_tr_b16 a[210:211], v251 offset:42240\n"
      "ds_read_b64_tr_b16 a[212:213], v200 offset:37632\n"
      "ds_read_b64_tr_b16 a[214:215], v200 offset:42240\n"
      "ds_read_b128 v[248:251], v114 offset:18432\n"
      "ds_read_b128 v[224:227], v115 offset:18432\n"
      "ds_read2_b32 v[78:79], v244 offset0:0 offset1:1\n"
      "ds_read2_b32 v[80:81], v244 offset0:2 offset1:3\n"
      "ds_read2_b32 v[82:83], v244 offset0:16 offset1:17\n"
      "ds_read2_b32 v[84:85], v244 offset0:18 offset1:19\n"
      "ds_read2_b32 v[86:87], v244 offset0:32 offset1:33\n"
      "ds_read2_b32 v[88:89], v244 offset0:34 offset1:35\n"
      "ds_read2_b32 v[90:91], v168 offset0:0 offset1:1\n"
      "ds_read2_b32 v[92:93], v168 offset0:2 offset1:3\n"
      "ds_read2_b32 v[94:95], v168 offset0:16 offset1:17\n"
      "ds_read2_b32 v[96:97], v168 offset0:18 offset1:19\n"
      "ds_read2_b32 v[98:99], v168 offset0:32 offset1:33\n"
      "ds_read2_b32 v[100:101], v168 offset0:34 offset1:35\n"
      "ds_read2_b32 v[102:103], v171 offset0:0 offset1:1\n"
      "ds_read2_b32 v[104:105], v171 offset0:2 offset1:3\n"
      "ds_read2_b32 v[106:107], v171 offset0:16 offset1:17\n"
      "ds_read2_b32 v[108:109], v171 offset0:18 offset1:19\n"
      "ds_read2_b32 v[110:111], v171 offset0:32 offset1:33\n"
      "ds_read2_b32 v[112:113], v171 offset0:34 offset1:35\n"
      "ds_read_b32 v70, v245 offset:192\n"
      "ds_read_b32 v72, v245 offset:3328\n"
      "ds_read_b32 v74, v245 offset:6464\n"
      "ds_read_b32 v71, v246\n"
      "ds_read_b32 v73, v246 offset:64\n"
      "ds_read_b32 v75, v246 offset:128\n"
      "ds_read_b32 v77, v189\n"
      "s_waitcnt vmcnt(0)\n"
      "ds_write_b32 v213, v170\n"
      "s_cbranch_scc1 L_edge_1_%=\n"
      "L_eb_2_%=:\n"
      "s_add_u32 m0, s76, 0\n"
      "s_nop 0\n"
      "global_load_lds_dwordx4 v116, s[52:53]\n"
      "s_waitcnt lgkmcnt(12)\n"
      "v_mfma_f32_16x16x32_bf16 v[192:195], a[0:3], v[248:251], 0\n"
      "s_add_u32 m0, s76, 6016\n"
      "v_mov_b32 v189, 0\n"
      "v_mfma_f32_16x16x32_bf16 v[192:195], a[36:39], v[224:227], v[192:195]\n"
      "global_load_lds_dwordx4 v116, s[52:53] offset:128\n"
      "v_mfma_f32_16x16x32_bf16 v[180:183], a[4:7], v[248:251], 0\n"
      "s_add_u32 m0, s76, 12032\n"
      "s_mul_i32 s2, s24, 768\n"
      "s_add_u32 s2, s2, 149472\n"
      "v_mfma_f32_16x16x32_bf16 v[180:183], a[40:43], v[224:227], v[180:183]\n"
      "global_load_lds_dwordx4 v116, s[52:53] offset:256\n"
      "v_mfma_f32_16x16x32_bf16 v[164:167], a[8:11], v[248:251], 0\n"
      "s_add_u32 m0, s77, 0\n"
      "v_lshl_add_u32 v78, v78, 2, s2\n"
      "v_lshl_add_u32 v79, v79, 2, s2\n"
      "v_mfma_f32_16x16x32_bf16 v[164:167], a[44:47], v[224:227], v[164:167]\n"
      "global_load_lds_dwordx4 v117, s[52:53]\n"
      "v_mfma_f32_16x16x32_bf16 v[232:235], a[12:15], v[248:251], 0\n"
      "ds_read_b32 v78, v78\n"
      "s_add_u32 m0, s78, 0\n"
      "v_mfma_f32_16x16x32_bf16 v[232:235], a[48:51], v[224:227], v[232:235]\n"
      "ds_read_b32 v79, v79\n"
      "v_lshl_add_u32 v80, v80, 2, s2\n"
      "v_lshl_add_u32 v81, v81, 2, s2\n"
      "v_lshl_add_u32 v82, v82, 2, s2\n"
      "v_lshl_add_u32 v83, v83, 2, s2\n"
      "ds_read_b32 v80, v80\n"
      "ds_read_b32 v81, v81\n"
      "ds_read_b32 v82, v82\n"
      "ds_read_b32 v83, v83\n"
      "s_mov_b32 exec_hi, 0\n"
      "global_load_lds_dwordx4 v118, s[52:53]\n"
      "s_mov_b32 exec_hi, -1\n"
      "v_mfma_f32_16x16x32_bf16 v[196:199], a[16:19], v[248:251], 0\n"
      "s_add_u32 m0, s76, 110592\n"
      "v_lshl_add_u32 v84, v84, 2, s2\n"
      "v_lshl_add_u32 v85, v85, 2, s2\n"
      "v_mfma_f32_16x16x32_bf16 v[196:199], a[52:55], v[224:227], v[196:199]\n"
      "global_load_lds_dwordx4 v119, s[56:57]\n"
      "v_mfma_f32_16x16x32_bf16 v[160:163], a[20:23], v[248:251], 0\n"
      "ds_read_b32 v84, v84\n"
      "s_add_u32 m0, s79, 0\n"
      "v_mfma_f32_16x16x32_bf16 v[160:163], a[56:59], v[224:227], v[160:163]\n"
      "ds_read_b32 v85, v85\n"
      "v_lshl_add_u32 v86, v86, 2, s2\n"
      "v_lshl_add_u32 v87, v87, 2, s2\n"
      "ds_read_b32 v86, v86\n"
      "ds_read_b128 v[152:155], v114 offset:24576\n"
      "ds_read_b128 v[236:239], v115 offset:24576\n"
      "ds_read_b32 v87, v87\n"
      "s_mov_b64 exec, 0xfff\n"
      "global_load_lds_dwordx4 v122, s[58:59]\n"
      "s_mov_b64 exec, -1\n"
      "v_mfma_f32_16x16x32_bf16 v[228:231], a[24:27], v[248:251], 0\n"
      "global_load_dwordx4 v[126:129], v169, s[48:49] offset:-1024\n"
      "v_mfma_f32_16x16x32_bf16 v[228:231], a[60:63], v[224:227], v[228:231]\n"
      "global_load_dwordx4 v[130:133], v169, s[48:49]\n"
      "v_mfma_f32_16x16x32_bf16 v[252:255], a[28:31], v[248:251], 0\n"
      "global_load_dwordx4 v[216:219], v169, s[48:49] offset:1024\n"
      "v_mfma_f32_16x16x32_bf16 v[252:255], a[64:67], v[224:227], v[252:255]\n"
      "ds_read_b128 v[168:171], v114 offset:30720\n"
      "v_mfma_f32_16x16x32_bf16 v[140:143], a[32:35], v[248:251], 0\n"
      "ds_read_b128 v[248:251], v115 offset:30720\n"
      "v_mfma_f32_16x16x32_bf16 v[140:143], a[68:71], v[224:227], v[140:143]\n"
      "v_lshl_add_u32 v88, v88, 2, s2\n"
      "v_lshl_add_u32 v89, v89, 2, s2\n"
      "v_lshl_add_u32 v90, v90, 2, s2\n"
      "v_lshl_add_u32 v91, v91, 2, s2\n"
      "v_lshl_add_u32 v92, v92, 2, s2\n"
      "v_lshl_add_u32 v93, v93, 2, s2\n"
      "s_waitcnt lgkmcnt(2)\n"
      "v_mfma_f32_16x16x32_bf16 v[192:195], a[72:75], v[152:155], v[192:195]\n"
      "ds_read_b32 v88, v88\n"
      "v_mfma_f32_16x16x32_bf16 v[192:195], a[108:111], v[236:239], v[192:195]\n"
      "ds_read_b32 v89, v89\n"
      "v_lshl_add_u32 v94, v94, 2, s2\n"
      "v_mfma_f32_16x16x32_bf16 v[180:183], a[76:79], v[152:155], v[180:183]\n"
      "global_load_dwordx4 a[252:255], v201, s[6:7]\n"
      "v_mfma_f32_16x16x32_bf16 v[180:183], a[112:115], v[236:239], v[180:183]\n"
      "ds_read_b32 v90, v90\n"
      "v_lshl_add_u32 v95, v95, 2, s2\n"
      "v_mfma_f32_16x16x32_bf16 v[164:167], a[80:83], v[152:155], v[164:167]\n"
      "ds_read_b32 v91, v91\n"
      "v_mfma_f32_16x16x32_bf16 v[164:167], a[116:119], v[236:239], v[164:167]\n"
      "ds_read_b32 v92, v92\n"
      "v_lshl_add_u32 v96, v96, 2, s2\n"
      "v_mfma_f32_16x16x32_bf16 v[232:235], a[84:87], v[152:155], v[232:235]\n"
      "ds_read_b32 v93, v93\n"
      "v_lshl_add_u32 v97, v97, 2, s2\n"
      "v_mfma_f32_16x16x32_bf16 v[232:235], a[120:123], v[236:239], v[232:235]\n"
      "global_load_dwordx4 v[0:3], v201, s[6:7] offset:128\n"
      "s_waitcnt lgkmcnt(6)\n"
      "v_mfma_f32_16x16x32_bf16 v[196:199], a[88:91], v[152:155], v[196:199]\n"
      "ds_read_b32 v94, v94\n"
      "v_mfma_f32_16x16x32_bf16 v[196:199], a[124:127], v[236:239], v[196:199]\n"
      "ds_read_b32 v95, v95\n"
      "v_mfma_f32_16x16x32_bf16 v[160:163], a[92:95], v[152:155], v[160:163]\n"
      "ds_read_b32 v96, v96\n"
      "v_lshl_add_u32 v98, v98, 2, s2\n"
      "v_mfma_f32_16x16x32_bf16 v[160:163], a[128:131], v[236:239], v[160:163]\n"
      "global_load_dwordx4 v[4:7], v201, s[6:7] offset:256\n"
      "v_mfma_f32_16x16x32_bf16 v[228:231], a[96:99], v[152:155], v[228:231]\n"
      "ds_read_b32 v97, v97\n"
      "v_mfma_f32_16x16x32_bf16 v[228:231], a[132:135], v[236:239], v[228:231]\n"
      "ds_read_b32 v98, v98\n"
      "v_lshl_add_u32 v99, v99, 2, s2\n"
      "v_mfma_f32_16x16x32_bf16 v[252:255], a[100:103], v[152:155], v[252:255]\n"
      "ds_read_b32 v99, v99\n"
      "v_mfma_f32_16x16x32_bf16 v[252:255], a[136:139], v[236:239], v[252:255]\n"
      "v_lshl_add_u32 v100, v100, 2, s2\n"
      "v_lshl_add_u32 v101, v101, 2, s2\n"
      "v_mfma_f32_16x16x32_bf16 v[140:143], a[104:107], v[152:155], v[140:143]\n"
      "ds_read_b32 v100, v100\n"
      "v_add_u32 v201, 15360, v247\n"
      "v_mfma_f32_16x16x32_bf16 v[140:143], a[140:143], v[236:239], v[140:143]\n"
      "ds_read_b32 v101, v101\n"
      "v_mfma_f32_16x16x32_bf16 v[192:195], a[144:147], v[168:171], v[192:195]\n"
      "global_load_dwordx4 v[8:11], v201, s[6:7] offset:-3072\n"
      "v_mfma_f32_16x16x32_bf16 v[192:195], a[180:183], v[248:251], v[192:195]\n"
      "v_lshl_add_u32 v102, v102, 2, s2\n"
      "v_lshl_add_u32 v103, v103, 2, s2\n"
      "v_mfma_f32_16x16x32_bf16 v[180:183], a[148:151], v[168:171], v[180:183]\n"
      "ds_read_b32 v102, v102\n"
      "v_mfma_f32_16x16x32_bf16 v[180:183], a[184:187], v[248:251], v[180:183]\n"
      "ds_read_b32 v103, v103\n"
      "v_mfma_f32_16x16x32_bf16 v[164:167], a[152:155], v[168:171], v[164:167]\n"
      "ds_read_b128 v[236:239], v114 offset:20480\n"
      "v_mfma_f32_16x16x32_bf16 v[164:167], a[188:191], v[248:251], v[164:167]\n"
      "v_lshl_add_u32 v104, v104, 2, s2\n"
      "v_lshl_add_u32 v105, v105, 2, s2\n"
      "v_mfma_f32_16x16x32_bf16 v[232:235], a[156:159], v[168:171], v[232:235]\n"
      "ds_read_b32 v104, v104\n"
      "v_lshl_add_u32 v106, v106, 2, s2\n"
      "v_mfma_f32_16x16x32_bf16 v[232:235], a[192:195], v[248:251], v[232:235]\n"
      "ds_read_b32 v105, v105\n"
      "v_mfma_f32_16x16x32_bf16 v[196:199], a[160:163], v[168:171], v[196:199]\n"
      "global_load_dwordx4 v[12:15], v201, s[6:7] offset:-2944\n"
      "v_mfma_f32_16x16x32_bf16 v[196:199], a[196:199], v[248:251], v[196:199]\n"
      "ds_read_b32 v106, v106\n"
      "v_lshl_add_u32 v107, v107, 2, s2\n"
      "v_mfma_f32_16x16x32_bf16 v[160:163], a[164:167], v[168:171], v[160:163]\n"
      "ds_read_b32 v107, v107\n"
      "v_mfma_f32_16x16x32_bf16 v[160:163], a[200:203], v[248:251], v[160:163]\n"
      "ds_read_b128 v[244:247], v115 offset:20480\n"
      "v_lshl_add_u32 v108, v108, 2, s2\n"
      "v_mfma_f32_16x16x32_bf16 v[228:231], a[168:171], v[168:171], v[228:231]\n"
      "ds_read_b32 v108, v108\n"
      "v_lshl_add_u32 v109, v109, 2, s2\n"
      "v_mfma_f32_16x16x32_bf16 v[228:231], a[204:207], v[248:251], v[228:231]\n"
      "ds_read_b32 v109, v109\n"
      "v_mfma_f32_16x16x32_bf16 v[252:255], a[172:175], v[168:171], v[252:255]\n"
      "global_load_dwordx4 v[16:19], v201, s[6:7] offset:-2816\n"
      "v_mfma_f32_16x16x32_bf16 v[252:255], a[208:211], v[248:251], v[252:255]\n"
      "v_lshl_add_u32 v110, v110, 2, s2\n"
      "v_lshl_add_u32 v111, v111, 2, s2\n"
      "v_mfma_f32_16x16x32_bf16 v[140:143], a[176:179], v[168:171], v[140:143]\n"
      "ds_read_b32 v110, v110\n"
      "v_mfma_f32_16x16x32_bf16 v[140:143], a[212:215], v[248:251], v[140:143]\n"
      "ds_read_b32 v111, v111\n"
      "v_lshl_add_u32 v112, v112, 2, s2\n"
      "v_lshl_add_u32 v113, v113, 2, s2\n"
      "v_fma_f32 v213, v192, v192, s38\n"
      "v_fma_f32 v190, v232, v232, s38\n"
      "v_lshl_add_u32 v70, v70, 2, s2\n"
      "v_lshl_add_u32 v71, v71, 2, s2\n"
      "v_lshl_add_u32 v72, v72, 2, s2\n"
      "s_waitcnt lgkmcnt(4)\n"
      "v_mfma_f32_16x16x32_bf16 v[152:155], a[0:3], v[236:239], 0\n"
      "ds_read_b32 v112, v112\n"
      "v_lshl_add_u32 v73, v73, 2, s2\n"
      "v_mfma_f32_16x16x32_bf16 v[152:155], a[36:39], v[244:247], v[152:155]\n"
      "global_load_dwordx4 v[20:23], v201, s[6:7]\n"
      "v_mfma_f32_16x16x32_bf16 v[248:251], a[4:7], v[236:239], 0\n"
      "ds_read_b32 v113, v113\n"
      "v_fmac_f32 v213, v193, v193\n"
      "v_mfma_f32_16x16x32_bf16 v[248:251], a[40:43], v[244:247], v[248:251]\n"
      "ds_read_b32 v70, v70\n"
      "v_fmac_f32 v190, v233, v233\n"
      "v_mfma_f32_16x16x32_bf16 v[172:175], a[8:11], v[236:239], 0\n"
      "ds_read_b32 v71, v71\n"
      "v_fmac_f32 v213, v194, v194\n"
      "v_mfma_f32_16x16x32_bf16 v[172:175], a[44:47], v[244:247], v[172:175]\n"
      "ds_read_b32 v72, v72\n"
      "v_fmac_f32_e64 v190, v234, v234\n"
      "v_mfma_f32_16x16x32_bf16 v[224:227], a[12:15], v[236:239], 0\n"
      "global_load_dwordx4 v[24:27], v201, s[6:7] offset:128\n"
      "v_mfma_f32_16x16x32_bf16 v[224:227], a[48:51], v[244:247], v[224:227]\n"
      "ds_read_b32 v73, v73\n"
      "v_fmac_f32_e64 v213, v195, v195\n"
      "v_mfma_f32_16x16x32_bf16 v[148:151], a[16:19], v[236:239], 0\n"
      "ds_read_b128 v[220:223], v114 offset:26624\n"
      "v_fmac_f32_e64 v190, v235, v235\n"
      "v_mfma_f32_16x16x32_bf16 v[148:151], a[52:55], v[244:247], v[148:151]\n"
      "ds_read_b128 v[168:171], v115 offset:26624\n"
      "v_fmac_f32 v213, v180, v180\n"
      "v_mfma_f32_16x16x32_bf16 v[240:243], a[20:23], v[236:239], 0\n"
      "v_lshl_add_u32 v74, v74, 2, s2\n"
      "v_fmac_f32 v190, v196, v196\n"
      "v_mfma_f32_16x16x32_bf16 v[240:243], a[56:59], v[244:247], v[240:243]\n"
      "ds_read_b32 v74, v74\n"
      "v_fmac_f32_e64 v213, v181, v181\n"
      "v_mfma_f32_16x16x32_bf16 v[204:207], a[24:27], v[236:239], 0\n"
      "v_lshl_add_u32 v75, v75, 2, s2\n"
      "v_fmac_f32 v190, v197, v197\n"
      "v_mfma_f32_16x16x32_bf16 v[204:207], a[60:63], v[244:247], v[204:207]\n"
      "ds_read_b32 v75, v75\n"
      "v_fmac_f32 v213, v182, v182\n"
      "v_mfma_f32_16x16x32_bf16 v[136:139], a[28:31], v[236:239], 0\n"
      "global_load_dwordx4 v[28:31], v201, s[6:7] offset:256\n"
      "v_mfma_f32_16x16x32_bf16 v[136:139], a[64:67], v[244:247], v[136:139]\n"
      "v_lshl_add_u32 v77, v77, 2, s2\n"
      "v_fmac_f32 v190, v198, v198\n"
      "v_fmac_f32 v213, v183, v183\n"
      "v_fmac_f32 v190, v199, v199\n"
      "v_fmac_f32 v213, v164, v164\n"
      "v_fmac_f32 v190, v160, v160\n"
      "v_fmac_f32 v213, v165, v165\n"
      "v_fmac_f32 v190, v161, v161\n"
      "v_fmac_f32 v213, v166, v166\n"
      "v_fmac_f32 v190, v162, v162\n"
      "v_fmac_f32 v213, v167, v167\n"
      "v_fmac_f32 v190, v163, v163\n"
      "s_waitcnt lgkmcnt(2)\n"
      "v_mfma_f32_16x16x32_bf16 v[152:155], a[72:75], v[220:223], v[152:155]\n"
      "ds_read_b32 v77, v77\n"
      "v_mfma_f32_16x16x32_bf16 v[152:155], a[108:111], v[168:171], v[152:155]\n"
      "v_permlane16_swap_b32 v213, v190\n"
      "v_mfma_f32_16x16x32_bf16 v[248:251], a[76:79], v[220:223], v[248:251]\n"
      "s_waitcnt lgkmcnt(2)\n"
      "v_add_f32_e64 v213, v213, v190\n"
      "v_mov_b32 v190, v213\n"
      "v_mfma_f32_16x16x32_bf16 v[248:251], a[112:115], v[168:171], v[248:251]\n"
      "s_mov_b32 exec_lo, 0xffff0000\n"
      "s_mov_b32 exec_hi, 0xffffffff\n"
      "v_mov_b32 v70, 0xf149f2ca\n"
      "v_mov_b32 v72, 0xf149f2ca\n"
      "v_mov_b32 v74, 0xf149f2ca\n"
      "s_mov_b64 exec, -1\n"
      "v_permlane32_swap_b32 v213, v190\n"
      "v_add_f32 v213, v213, v190\n"
      "v_rsq_f32 v213, v213\n"
      "s_nop 0\n"
      "v_mov_b32 v190, v213\n"
      "s_nop 1\n"
      "v_permlane16_swap_b32 v213, v190\n"
      "v_mul_f32 v232, v190, v232\n"
      "s_waitcnt lgkmcnt(0)\n"
      "v_mfma_f32_16x16x32_bf16 v[172:175], a[80:83], v[220:223], v[172:175]\n"
      "v_mfma_f32_16x16x32_bf16 v[172:175], a[116:119], v[168:171], v[172:175]\n"
      "s_mov_b32 exec_lo, 0x0\n"
      "s_mov_b32 exec_hi, 0xfffe0000\n"
      "v_mov_b32 v77, 0xf149f2ca\n"
      "s_mov_b64 exec, -1\n"
      "v_mfma_f32_16x16x32_bf16 v[144:147], a[32:35], v[236:239], 0\n"
      "s_mov_b32 s4, 0\n"
      "v_mul_f32 v233, v190, v233\n"
      "v_cvt_pk_bf16_f32 v232, v232, v233\n"
      "v_mfma_f32_16x16x32_bf16 v[144:147], a[68:71], v[244:247], v[144:147]\n"
      "ds_read_b128 v[236:239], v114 offset:32768\n"
      "v_mul_f32 v234, v190, v234\n"
      "v_mfma_f32_16x16x32_bf16 v[224:227], a[84:87], v[220:223], v[224:227]\n"
      "s_mov_b32 s5, 0\n"
      "v_mul_f32_e64 v235, v190, v235\n"
      "v_cvt_pk_bf16_f32 v233, v234, v235\n"
      "v_mfma_f32_16x16x32_bf16 v[224:227], a[120:123], v[168:171], v[224:227]\n"
      "v_mul_f32 v196, v190, v196\n"
      "v_mul_f32 v197, v190, v197\n"
      "v_mfma_f32_16x16x32_bf16 v[148:151], a[88:91], v[220:223], v[148:151]\n"
      "v_cvt_pk_bf16_f32 v234, v196, v197\n"
      "v_mul_f32 v198, v190, v198\n"
      "v_mfma_f32_16x16x32_bf16 v[148:151], a[124:127], v[168:171], v[148:151]\n"
      "v_mul_f32 v199, v190, v199\n"
      "v_cvt_pk_bf16_f32 v235, v198, v199\n"
      "v_mfma_f32_16x16x32_bf16 v[240:243], a[92:95], v[220:223], v[240:243]\n"
      "ds_read_b128 v[196:199], v115 offset:32768\n"
      "v_mul_f32 v160, v190, v160\n"
      "v_mfma_f32_16x16x32_bf16 v[240:243], a[128:131], v[168:171], v[240:243]\n"
      "v_mul_f32 v161, v190, v161\n"
      "v_cvt_pk_bf16_f32 v34, v160, v161\n"
      "v_mfma_f32_16x16x32_bf16 v[204:207], a[96:99], v[220:223], v[204:207]\n"
      "v_mul_f32 v162, v190, v162\n"
      "v_mul_f32 v163, v190, v163\n"
      "v_mfma_f32_16x16x32_bf16 v[204:207], a[132:135], v[168:171], v[204:207]\n"
      "v_cvt_pk_bf16_f32 v35, v162, v163\n"
      "v_mul_f32 v213, s36, v213\n"
      "v_mfma_f32_16x16x32_bf16 v[136:139], a[100:103], v[220:223], v[136:139]\n"
      "v_mul_f32 v192, v213, v192\n"
      "v_mul_f32_e64 v193, v213, v193\n"
      "v_mfma_f32_16x16x32_bf16 v[136:139], a[136:139], v[168:171], v[136:139]\n"
      "v_cvt_pk_bf16_f32 v192, v192, v193\n"
      "v_mul_f32 v194, v213, v194\n"
      "v_mfma_f32_16x16x32_bf16 v[144:147], a[104:107], v[220:223], v[144:147]\n"
      "v_mul_f32 v195, v213, v195\n"
      "v_cvt_pk_bf16_f32 v193, v194, v195\n"
      "v_mfma_f32_16x16x32_bf16 v[144:147], a[140:143], v[168:171], v[144:147]\n"
      "v_mul_f32 v180, v213, v180\n"
      "v_mul_f32 v181, v213, v181\n"
      "v_cvt_pk_bf16_f32 v194, v180, v181\n"
      "v_mul_f32 v182, v213, v182\n"
      "v_mul_f32 v183, v213, v183\n"
      "v_cvt_pk_bf16_f32 v195, v182, v183\n"
      "v_mul_f32 v164, v213, v164\n"
      "v_mul_f32 v165, v213, v165\n"
      "v_cvt_pk_bf16_f32 v32, v164, v165\n"
      "s_waitcnt lgkmcnt(0)\n"
      "v_mfma_f32_16x16x32_bf16 v[152:155], a[144:147], v[236:239], v[152:155]\n"
      "v_mul_f32 v166, v213, v166\n"
      "v_mul_f32_e64 v167, v213, v167\n"
      "v_mfma_f32_16x16x32_bf16 v[152:155], a[180:183], v[196:199], v[152:155]\n"
      "v_cvt_pk_bf16_f32 v33, v166, v167\n"
      "s_waitcnt vmcnt(9)\n"
      "v_mov_b32 v134, v216\n"
      "v_mfma_f32_16x16x32_bf16 v[248:251], a[148:151], v[236:239], v[248:251]\n"
      "v_mov_b32_e64 v135, v217\n"
      "v_add_u32 v217, 36864, v218\n"
      "v_mfma_f32_16x16x32_bf16 v[248:251], a[184:187], v[196:199], v[248:251]\n"
      "v_add_u32 v213, 36864, v219\n"
      "v_mov_b32_e64 v216, -1\n"
      "v_mfma_f32_16x16x32_bf16 v[172:175], a[152:155], v[236:239], v[172:175]\n"
      "v_mfma_f32_16x16x32_bf16 v[172:175], a[188:191], v[196:199], v[172:175]\n"
      "ds_write_b32 v129, v216\n"
      "v_mfma_f32_16x16x32_bf16 v[224:227], a[156:159], v[236:239], v[224:227]\n"
      "ds_read_b128 v[220:223], v119 offset:36864\n"
      "v_mfma_f32_16x16x32_bf16 v[224:227], a[192:195], v[196:199], v[224:227]\n"
      "ds_write_b32 v129, v216 offset:1024\n"
      "v_mfma_f32_16x16x32_bf16 v[148:151], a[160:163], v[236:239], v[148:151]\n"
      "ds_read_b128 v[160:163], v122\n"
      "v_mfma_f32_16x16x32_bf16 v[148:151], a[196:199], v[196:199], v[148:151]\n"
      "ds_write_b32 v129, v216 offset:3072\n"
      "v_mfma_f32_16x16x32_bf16 v[240:243], a[164:167], v[236:239], v[240:243]\n"
      "v_mfma_f32_16x16x32_bf16 v[240:243], a[200:203], v[196:199], v[240:243]\n"
      "v_mfma_f32_16x16x32_bf16 v[204:207], a[168:171], v[236:239], v[204:207]\n"
      "v_mfma_f32_16x16x32_bf16 v[204:207], a[204:207], v[196:199], v[204:207]\n"
      "v_mfma_f32_16x16x32_bf16 v[136:139], a[172:175], v[236:239], v[136:139]\n"
      "v_mfma_f32_16x16x32_bf16 v[136:139], a[208:211], v[196:199], v[136:139]\n"
      "s_waitcnt lgkmcnt(2)\n"
      "v_mfma_f32_16x16x32_bf16 v[144:147], a[176:179], v[236:239], v[144:147]\n"
      "v_or3_b32 v220, v220, v221, v222\n"
      "v_mfma_f32_16x16x32_bf16 v[144:147], a[212:215], v[196:199], v[144:147]\n"
      "s_waitcnt lgkmcnt(0)\n"
      "s_mov_b64 exec, 0xfff\n"
      "v_or3_b32 v220, v220, v160, v161\n"
      "v_or3_b32 v220, v220, v162, v163\n"
      "s_mov_b64 exec, -1\n"
      "v_bitop3_b32 v220, v220, v223, s40 bitop3:0xa8\n"
      "v_or_b32 v220, s4, v220\n"
      "ds_write_b32 v129, v220 offset:2048\n"
      "s_waitcnt lgkmcnt(0)\n"
      "s_barrier\n"
      "v_cvt_pk_bf16_f32 v228, v228, v229\n"
      "v_cvt_pk_bf16_f32 v229, v230, v231\n"
      "v_cvt_pk_bf16_f32 v252, v252, v253\n"
      "v_cvt_pk_bf16_f32 v253, v254, v255\n"
      "v_cvt_pk_bf16_f32 v140, v140, v141\n"
      "v_cvt_pk_bf16_f32 v141, v142, v143\n"
      "v_fma_f32 v254, v152, v152, s38\n"
      "v_fma_f32 v216, v224, v224, s38\n"
      "v_fmac_f32 v254, v153, v153\n"
      "v_fmac_f32 v216, v225, v225\n"
      "v_fmac_f32 v254, v154, v154\n"
      "v_fmac_f32 v216, v226, v226\n"
      "v_fmac_f32 v254, v155, v155\n"
      "v_fmac_f32 v216, v227, v227\n"
      "v_fmac_f32 v254, v248, v248\n"
      "v_fmac_f32 v216, v148, v148\n"
      "v_fmac_f32 v254, v249, v249\n"
      "v_fmac_f32 v216, v149, v149\n"
      "v_fmac_f32 v254, v250, v250\n"
      "v_fmac_f32 v216, v150, v150\n"
      "v_fmac_f32 v254, v251, v251\n"
      "v_fmac_f32 v216, v151, v151\n"
      "v_fmac_f32 v254, v172, v172\n"
      "v_fmac_f32 v216, v240, v240\n"
      "v_fmac_f32 v254, v173, v173\n"
      "v_fmac_f32 v216, v241, v241\n"
      "v_fmac_f32 v254, v174, v174\n"
      "v_fmac_f32 v216, v242, v242\n"
      "v_fmac_f32 v254, v175, v175\n"
      "v_fmac_f32 v216, v243, v243\n"
      "s_mov_b32 s27, 0\n"
      "s_nop 0\n"
      "v_permlane16_swap_b32 v254, v216\n"
      "v_add_f32 v254, v254, v216\n"
      "v_mov_b32 v216, v254\n"
      "s_add_u32 s28, s27, 1\n"
      "s_nop 0\n"
      "v_permlane32_swap_b32 v254, v216\n"
      "v_add_f32 v254, v254, v216\n"
      "v_rsq_f32 v254, v254\n"
      "s_nop 0\n"
      "v_mov_b32 v216, v254\n"
      "s_min_u32 m0, s28, s3\n"
      "s_nop 0\n"
      "v_permlane16_swap_b32_e64 v254, v216\n"
      "v_mul_f32_e64 v224, v216, v224\n"
      "v_mul_f32 v225, v216, v225\n"
      "v_cvt_pk_bf16_f32 v224, v224, v225\n"
      "s_movrels_b32 s9, s80\n"
      "ds_read_b128 v[160:163], v114\n"
      "ds_read_b128 v[236:239], v115\n"
      "ds_read_b128 v[168:171], v128 offset:2048\n"
      "v_mul_f32 v226, v216, v226\n"
      "v_mul_f32 v227, v216, v227\n"
      "v_cvt_pk_bf16_f32 v225, v226, v227\n"
      "v_mul_f32_e64 v148, v216, v148\n"
      "v_mul_f32 v149, v216, v149\n"
      "s_lshr_b32 s8, s9, 8\n"
      "ds_write_b128 v214, v[192:195]\n"
      "v_cvt_pk_bf16_f32 v226, v148, v149\n"
      "s_and_b32 s9, s9, 0xff\n"
      "ds_write_b128 v214, v[32:35] offset:16\n"
      "v_mul_f32 v150, v216, v150\n"
      "s_mul_i32 s10, s8, 73\n"
      "ds_write_b128 v214, v[232:235] offset:32\n"
      "v_mul_f32 v151, v216, v151\n"
      "s_add_u32 s10, s10, s9\n"
      "ds_write_b64 v215, v[228:229]\n"
      "s_waitcnt lgkmcnt(4)\n"
      "v_mfma_f32_16x16x32_bf16 v[192:195], a[0:3], v[160:163], 0\n"
      "ds_read_b128 v[220:223], v114 offset:6144\n"
      "s_mul_i32 s28, s10, 4802\n"
      "v_mfma_f32_16x16x32_bf16 v[192:195], a[36:39], v[236:239], v[192:195]\n"
      "ds_write_b64 v215, v[252:253] offset:32\n"
      "v_mfma_f32_16x16x32_bf16 v[228:231], a[4:7], v[160:163], 0\n"
      "ds_read_b128 v[196:199], v115 offset:6144\n"
      "v_cvt_pk_bf16_f32 v227, v150, v151\n"
      "v_mfma_f32_16x16x32_bf16 v[228:231], a[40:43], v[236:239], v[228:231]\n"
      "ds_write_b64 v215, v[140:141] offset:64\n"
      "v_mfma_f32_16x16x32_bf16 v[148:151], a[8:11], v[160:163], 0\n"
      "s_add_u32 s56, s18, s28\n"
      "v_mul_f32 v240, v216, v240\n"
      "v_mul_f32 v241, v216, v241\n"
      "v_mfma_f32_16x16x32_bf16 v[148:151], a[44:47], v[236:239], v[148:151]\n"
      "s_addc_u32 s57, s19, 0\n"
      "v_cvt_pk_bf16_f32 v40, v240, v241\n"
      "v_mul_f32 v242, v216, v242\n"
      "v_mfma_f32_16x16x32_bf16 v[180:183], a[12:15], v[160:163], 0\n"
      "s_and_b32 s56, s56, -16\n"
      "v_mul_f32_e64 v243, v216, v243\n"
      "v_cvt_pk_bf16_f32 v41, v242, v243\n"
      "v_mfma_f32_16x16x32_bf16 v[180:183], a[48:51], v[236:239], v[180:183]\n"
      "s_sub_u32 s58, s56, 110592\n"
      "v_mul_f32 v254, s36, v254\n"
      "v_mul_f32 v152, v254, v152\n"
      "v_mfma_f32_16x16x32_bf16 v[232:235], a[16:19], v[160:163], 0\n"
      "s_subb_u32 s59, s57, 0\n"
      "v_mul_f32 v153, v254, v153\n"
      "v_cvt_pk_bf16_f32 v152, v152, v153\n"
      "v_mfma_f32_16x16x32_bf16 v[232:235], a[52:55], v[236:239], v[232:235]\n"
      "s_sub_u32 s56, s56, 73728\n"
      "v_mul_f32 v154, v254, v154\n"
      "v_mul_f32_e64 v155, v254, v155\n"
      "v_mfma_f32_16x16x32_bf16 v[164:167], a[20:23], v[160:163], 0\n"
      "s_subb_u32 s57, s57, 0\n"
      "v_cvt_pk_bf16_f32 v153, v154, v155\n"
      "v_mul_f32 v248, v254, v248\n"
      "v_mfma_f32_16x16x32_bf16 v[164:167], a[56:59], v[236:239], v[164:167]\n"
      "s_waitcnt lgkmcnt(0)\n"
      "v_mul_f32_e64 v249, v254, v249\n"
      "v_cvt_pk_bf16_f32 v154, v248, v249\n"
      "v_mfma_f32_16x16x32_bf16 v[240:243], v[160:163], a[24:27], 0\n"
      "ds_read_b128 v[244:247], v114 offset:12288\n"
      "v_mul_f32_e64 v250, v254, v250\n"
      "v_mfma_f32_16x16x32_bf16 v[240:243], v[236:239], a[60:63], v[240:243]\n"
      "s_mul_i32 s28, s8, 1373568\n"
      "v_mul_f32_e64 v251, v254, v251\n"
      "v_cvt_pk_bf16_f32 v155, v250, v251\n"
      "v_mfma_f32_16x16x32_bf16 v[140:143], v[160:163], a[28:31], 0\n"
      "s_mul_i32 s29, s9, 2688\n"
      "v_mul_f32 v172, v254, v172\n"
      "v_mul_f32 v173, v254, v173\n"
      "v_mfma_f32_16x16x32_bf16 v[140:143], v[236:239], a[64:67], v[140:143]\n"
      "s_add_u32 s28, s28, s29\n"
      "v_cvt_pk_bf16_f32 v38, v172, v173\n"
      "v_mul_f32 v174, v254, v174\n"
      "v_mfma_f32_16x16x32_bf16 v[200:203], v[160:163], a[32:35], 0\n"
      "ds_read_b128 v[160:163], v115 offset:12288\n"
      "v_mul_f32_e64 v175, v254, v175\n"
      "v_mfma_f32_16x16x32_bf16 v[200:203], v[236:239], a[68:71], v[200:203]\n"
      "s_cmp_eq_u32 s8, 72\n"
      "v_cvt_pk_bf16_f32 v39, v174, v175\n"
      "v_cvt_pk_bf16_f32 v204, v204, v205\n"
      "v_mfma_f32_16x16x32_bf16 v[192:195], a[72:75], v[220:223], v[192:195]\n"
      "s_cselect_b32 s8, 0, 100270464\n"
      "v_cvt_pk_bf16_f32 v205, v206, v207\n"
      "v_cvt_pk_bf16_f32 v136, v136, v137\n"
      "v_mfma_f32_16x16x32_bf16 v[192:195], a[108:111], v[196:199], v[192:195]\n"
      "s_cmp_eq_u32 s9, 72\n"
      "v_cvt_pk_bf16_f32 v137, v138, v139\n"
      "v_cvt_pk_bf16_f32 v144, v144, v145\n"
      "v_mfma_f32_16x16x32_bf16 v[228:231], a[76:79], v[220:223], v[228:231]\n"
      "s_cselect_b32 s9, 0, 196224\n"
      "v_cvt_pk_bf16_f32 v145, v146, v147\n"
      "s_add_u32 s29, s8, s9\n"
      "v_mfma_f32_16x16x32_bf16 v[228:231], a[112:115], v[196:199], v[228:231]\n"
      "s_add_u32 s28, s28, s29\n"
      "s_add_u32 s52, s16, s28\n"
      "v_mfma_f32_16x16x32_bf16 v[148:151], a[80:83], v[220:223], v[148:151]\n"
      "s_addc_u32 s53, s17, 0\n"
      "s_cmp_lg_u32 s29, s11\n"
      "v_mfma_f32_16x16x32_bf16 v[148:151], a[116:119], v[196:199], v[148:151]\n"
      "s_cbranch_scc1 L_edge_6_%=\n"
      "L_eb_7_%=:\n"
      "v_mfma_f32_16x16x32_bf16 v[180:183], a[84:87], v[220:223], v[180:183]\n"
      "s_add_u32 m0, s76, 18432\n"
      "v_bitop3_b32 v168, v168, v169, s4 bitop3:0x7e\n"
      "v_bitop3_b32 v170, v170, v171, s4 bitop3:0x7e\n"
      "v_mfma_f32_16x16x32_bf16 v[180:183], a[120:123], v[196:199], v[180:183]\n"
      "global_load_lds_dwordx4 v116, s[52:53]\n"
      "s_waitcnt lgkmcnt(0)\n"
      "v_mfma_f32_16x16x32_bf16 v[232:235], a[88:91], v[220:223], v[232:235]\n"
      "v_or_b32_e64 v168, v168, v170\n"
      "v_cmp_ne_u32 vcc, 0, v168\n"
      "v_mfma_f32_16x16x32_bf16 v[232:235], a[124:127], v[196:199], v[232:235]\n"
      "s_mov_b32 exec_lo, 0x001f001f\n"
      "s_mov_b32 exec_hi, 0x001f001f\n"
      "ds_write_b128 v214, v[152:155] offset:18688\n"
      "ds_write_b128 v214, v[38:41] offset:18704\n"
      "ds_write_b128 v214, v[224:227] offset:18720\n"
      "ds_write_b64 v215, v[204:205] offset:18688\n"
      "ds_write_b64 v215, v[136:137] offset:18720\n"
      "ds_write_b64 v215, v[144:145] offset:18752\n"
      "s_mov_b64 exec, -1\n"
      "v_mfma_f32_16x16x32_bf16 v[144:147], v[156:159], a[216:219], 0\n"
      "s_mov_b64 s[48:49], vcc\n"
      "s_add_u32 m0, s76, 24448\n"
      "v_mfma_f32_16x16x32_bf16 v[164:167], a[92:95], v[220:223], v[164:167]\n"
      "global_load_lds_dwordx4 v116, s[52:53] offset:128\n"
      "v_mfma_f32_16x16x32_bf16 v[164:167], a[128:131], v[196:199], v[164:167]\n"
      "s_cbranch_vccnz L_flg_3_%=\n"
      "L_fb_4_%=:\n"
      "v_mfma_f32_16x16x32_bf16 v[144:147], v[156:159], a[220:223], v[144:147]\n"
      "s_add_u32 m0, s76, 30464\n"
      "v_mfma_f32_16x16x32_bf16 v[144:147], v[156:159], a[224:227], v[144:147]\n"
      "v_mfma_f32_16x16x32_bf16 v[240:243], v[220:223], a[96:99], v[240:243]\n"
      "v_mfma_f32_16x16x32_bf16 v[240:243], v[196:199], a[132:135], v[240:243]\n"
      "v_mfma_f32_16x16x32_bf16 v[140:143], v[220:223], a[100:103], v[140:143]\n"
      "v_mfma_f32_16x16x32_bf16 v[140:143], v[196:199], a[136:139], v[140:143]\n"
      "v_mfma_f32_16x16x32_bf16 v[200:203], v[220:223], a[104:107], v[200:203]\n"
      "v_mfma_f32_16x16x32_bf16 v[200:203], v[196:199], a[140:143], v[200:203]\n"
      "s_nop 1\n"
      "v_add_f32_dpp v144, v144, v144 quad_perm:[1,0,3,2] row_mask:0xf bank_mask:0xf\n"
      "v_mfma_f32_16x16x32_bf16 v[192:195], a[144:147], v[244:247], v[192:195]\n"
      "s_nop 0\n"
      "v_add_f32_dpp v144, v144, v144 quad_perm:[2,3,0,1] row_mask:0xf bank_mask:0xf\n"
      "v_mfma_f32_16x16x32_bf16 v[192:195], a[180:183], v[160:163], v[192:195]\n"
      "global_load_lds_dwordx4 v116, s[52:53] offset:256\n"
      "v_mfma_f32_16x16x32_bf16 v[228:231], a[148:151], v[244:247], v[228:231]\n"
      "s_add_u32 m0, s77, 18432\n"
      "v_add_f32_dpp v144, v144, v144 row_half_mirror row_mask:0xf bank_mask:0xf\n"
      "v_mul_f32 v147, 0xbbaaaaab, v144\n"
      "v_mfma_f32_16x16x32_bf16 v[228:231], a[184:187], v[160:163], v[228:231]\n"
      "ds_read_b128 v[224:227], v114 offset:2048\n"
      "v_mul_f32 v146, 0xbbaaaaab, v144\n"
      "v_mfma_f32_16x16x32_bf16 v[148:151], a[152:155], v[244:247], v[148:151]\n"
      "ds_read_b128 v[236:239], v115 offset:2048\n"
      "v_mul_f32 v145, 0xbbaaaaab, v144\n"
      "v_mfma_f32_16x16x32_bf16 v[148:151], a[188:191], v[160:163], v[148:151]\n"
      "v_mul_f32 v144, 0xbbaaaaab, v144\n"
      "v_mfma_f32_16x16x32_bf16 v[136:139], v[156:159], a[228:231], 0\n"
      "s_nop 0\n"
      "v_mfma_f32_16x16x32_bf16 v[172:175], v[186:189], a[216:219], v[144:147]\n"
      "v_mfma_f32_16x16x32_bf16 v[220:223], v[184:187], a[216:219], v[144:147]\n"
      "v_mfma_f32_16x16x32_bf16 v[168:171], v[186:189], a[220:223], v[144:147]\n"
      "v_mfma_f32_16x16x32_bf16 v[248:251], v[184:187], a[220:223], v[144:147]\n"
      "v_mfma_f32_16x16x32_bf16 v[196:199], v[186:189], a[224:227], v[144:147]\n"
      "v_mfma_f32_16x16x32_bf16 v[252:255], v[184:187], a[224:227], v[144:147]\n"
      "v_mfma_f32_16x16x32_bf16 v[136:139], v[156:159], a[232:235], v[136:139]\n"
      "s_nop 1\n"
      "v_cvt_pk_bf16_f32 v172, v172, v173\n"
      "v_mfma_f32_16x16x32_bf16 v[180:183], a[156:159], v[244:247], v[180:183]\n"
      "v_cvt_pk_bf16_f32 v173, v174, v175\n"
      "v_cvt_pk_bf16_f32 v174, v220, v221\n"
      "v_mfma_f32_16x16x32_bf16 v[180:183], a[192:195], v[160:163], v[180:183]\n"
      "v_cvt_pk_bf16_f32 v175, v222, v223\n"
      "v_cvt_pk_bf16_f32 v168, v168, v169\n"
      "v_mfma_f32_16x16x32_bf16 v[136:139], v[156:159], a[236:239], v[136:139]\n"
      "ds_write_b128 v212, v[172:175]\n"
      "v_mfma_f32_16x16x32_bf16 v[232:235], a[160:163], v[244:247], v[232:235]\n"
      "v_cvt_pk_bf16_f32 v169, v170, v171\n"
      "v_cvt_pk_bf16_f32 v170, v248, v249\n"
      "v_mfma_f32_16x16x32_bf16 v[232:235], a[196:199], v[160:163], v[232:235]\n"
      "s_waitcnt lgkmcnt(1)\n"
      "v_cvt_pk_bf16_f32 v171, v250, v251\n"
      "v_cvt_pk_bf16_f32 v196, v196, v197\n"
      "v_mfma_f32_16x16x32_bf16 v[164:167], a[164:167], v[244:247], v[164:167]\n"
      "v_cvt_pk_bf16_f32 v197, v198, v199\n"
      "v_cvt_pk_bf16_f32 v198, v252, v253\n"
      "v_mfma_f32_16x16x32_bf16 v[164:167], a[200:203], v[160:163], v[164:167]\n"
      "ds_write_b128 v212, v[168:171] offset:128\n"
      "v_mfma_f32_16x16x32_bf16 v[240:243], v[244:247], a[168:171], v[240:243]\n"
      "v_cvt_pk_bf16_f32 v199, v254, v255\n"
      "v_add_f32_dpp v136, v136, v136 quad_perm:[1,0,3,2] row_mask:0xf bank_mask:0xf\n"
      "v_mfma_f32_16x16x32_bf16 v[240:243], v[160:163], a[204:207], v[240:243]\n"
      "ds_write_b128 v212, v[196:199] offset:256\n"
      "v_mfma_f32_16x16x32_bf16 v[140:143], v[244:247], a[172:175], v[140:143]\n"
      "global_load_lds_dwordx4 v117, s[52:53]\n"
      "v_mfma_f32_16x16x32_bf16 v[140:143], v[160:163], a[208:211], v[140:143]\n"
      "s_add_u32 m0, s78, 18432\n"
      "v_add_f32_dpp v136, v136, v136 quad_perm:[2,3,0,1] row_mask:0xf bank_mask:0xf\n"
      "v_fma_f32 v216, v192, v192, s38\n"
      "v_mfma_f32_16x16x32_bf16 v[200:203], v[244:247], a[176:179], v[200:203]\n"
      "v_add_f32_dpp v136, v136, v136 row_half_mirror row_mask:0xf bank_mask:0xf\n"
      "v_mul_f32 v139, 0xbbaaaaab, v136\n"
      "v_mfma_f32_16x16x32_bf16 v[200:203], v[160:163], a[212:215], v[200:203]\n"
      "v_mul_f32 v138, 0xbbaaaaab, v136\n"
      "v_mul_f32 v137, 0xbbaaaaab, v136\n"
      "v_mfma_f32_16x16x32_bf16 v[172:175], a[0:3], v[224:227], 0\n"
      "v_mul_f32 v136, 0xbbaaaaab, v136\n"
      "v_fma_f32 v215, v180, v180, s38\n"
      "v_mfma_f32_16x16x32_bf16 v[172:175], a[36:39], v[236:239], v[172:175]\n"
      "v_fmac_f32 v216, v193, v193\n"
      "v_fmac_f32 v215, v181, v181\n"
      "v_mfma_f32_16x16x32_bf16 v[144:147], v[186:189], a[228:231], v[136:139]\n"
      "v_fmac_f32 v216, v194, v194\n"
      "v_fmac_f32 v215, v182, v182\n"
      "v_mfma_f32_16x16x32_bf16 v[196:199], v[184:187], a[228:231], v[136:139]\n"
      "v_fmac_f32 v216, v195, v195\n"
      "v_fmac_f32 v215, v183, v183\n"
      "v_mfma_f32_16x16x32_bf16 v[252:255], v[186:189], a[232:235], v[136:139]\n"
      "v_fmac_f32 v216, v228, v228\n"
      "v_fmac_f32 v215, v232, v232\n"
      "v_mfma_f32_16x16x32_bf16 v[160:163], v[184:187], a[232:235], v[136:139]\n"
      "v_cvt_pk_bf16_f32 v144, v144, v145\n"
      "v_cvt_pk_bf16_f32 v145, v146, v147\n"
      "v_mfma_f32_16x16x32_bf16 v[220:223], v[186:189], a[236:239], v[136:139]\n"
      "v_cvt_pk_bf16_f32 v146, v196, v197\n"
      "v_cvt_pk_bf16_f32 v147, v198, v199\n"
      "v_mfma_f32_16x16x32_bf16 v[152:155], v[184:187], a[236:239], v[136:139]\n"
      "ds_write_b128 v212, v[144:147] offset:3072\n"
      "v_mfma_f32_16x16x32_bf16 v[168:171], a[4:7], v[224:227], 0\n"
      "ds_read_b128 v[144:147], v114 offset:8192\n"
      "v_cvt_pk_bf16_f32 v252, v252, v253\n"
      "v_mfma_f32_16x16x32_bf16 v[168:171], a[40:43], v[236:239], v[168:171]\n"
      "ds_read_b128 v[196:199], v115 offset:8192\n"
      "v_cvt_pk_bf16_f32 v253, v254, v255\n"
      "v_mfma_f32_16x16x32_bf16 v[136:139], a[8:11], v[224:227], 0\n"
      "v_cvt_pk_bf16_f32 v254, v160, v161\n"
      "v_cvt_pk_bf16_f32 v255, v162, v163\n"
      "v_mfma_f32_16x16x32_bf16 v[136:139], a[44:47], v[236:239], v[136:139]\n"
      "ds_write_b128 v212, v[252:255] offset:3200\n"
      "v_mfma_f32_16x16x32_bf16 v[248:251], a[12:15], v[224:227], 0\n"
      "v_cvt_pk_bf16_f32 v220, v220, v221\n"
      "v_cvt_pk_bf16_f32 v221, v222, v223\n"
      "v_mfma_f32_16x16x32_bf16 v[248:251], a[48:51], v[236:239], v[248:251]\n"
      "v_cvt_pk_bf16_f32 v222, v152, v153\n"
      "v_cvt_pk_bf16_f32 v223, v154, v155\n"
      "v_mfma_f32_16x16x32_bf16 v[252:255], a[16:19], v[224:227], 0\n"
      "v_fmac_f32 v216, v229, v229\n"
      "v_fmac_f32 v215, v233, v233\n"
      "v_mfma_f32_16x16x32_bf16 v[252:255], a[52:55], v[236:239], v[252:255]\n"
      "ds_write_b128 v212, v[220:223] offset:3328\n"
      "v_mfma_f32_16x16x32_bf16 v[220:223], a[20:23], v[224:227], 0\n"
      "v_fmac_f32 v216, v230, v230\n"
      "v_fmac_f32 v215, v234, v234\n"
      "v_mfma_f32_16x16x32_bf16 v[220:223], a[56:59], v[236:239], v[220:223]\n"
      "s_waitcnt lgkmcnt(2)\n"
      "v_fmac_f32 v216, v231, v231\n"
      "v_fmac_f32 v215, v235, v235\n"
      "v_mfma_f32_16x16x32_bf16 v[152:155], v[224:227], a[24:27], 0\n"
      "v_fmac_f32 v216, v148, v148\n"
      "v_fmac_f32 v215, v164, v164\n"
      "v_mfma_f32_16x16x32_bf16 v[152:155], v[236:239], a[60:63], v[152:155]\n"
      "s_mov_b32 exec_hi, 0\n"
      "global_load_lds_dwordx4 v118, s[52:53]\n"
      "s_mov_b32 exec_hi, -1\n"
      "v_mfma_f32_16x16x32_bf16 v[244:247], v[224:227], a[28:31], 0\n"
      "s_add_u32 m0, s76, 115456\n"
      "v_fmac_f32 v216, v149, v149\n"
      "v_fmac_f32 v215, v165, v165\n"
      "v_mfma_f32_16x16x32_bf16 v[244:247], v[236:239], a[64:67], v[244:247]\n"
      "v_fmac_f32 v216, v150, v150\n"
      "v_fmac_f32 v215, v166, v166\n"
      "v_mfma_f32_16x16x32_bf16 v[172:175], a[72:75], v[144:147], v[172:175]\n"
      "v_fmac_f32 v216, v151, v151\n"
      "v_fmac_f32 v215, v167, v167\n"
      "v_mfma_f32_16x16x32_bf16 v[172:175], a[108:111], v[196:199], v[172:175]\n"
      "s_nop 0\n"
      "v_permlane16_swap_b32 v216, v215\n"
      "v_mfma_f32_16x16x32_bf16 v[168:171], a[76:79], v[144:147], v[168:171]\n"
      "v_add_f32 v216, v216, v215\n"
      "v_mov_b32 v215, v216\n"
      "v_mfma_f32_16x16x32_bf16 v[168:171], a[112:115], v[196:199], v[168:171]\n"
      "s_nop 0\n"
      "v_permlane32_swap_b32 v216, v215\n"
      "v_mfma_f32_16x16x32_bf16 v[136:139], a[80:83], v[144:147], v[136:139]\n"
      "v_add_f32 v216, v216, v215\n"
      "v_mfma_f32_16x16x32_bf16 v[136:139], a[116:119], v[196:199], v[136:139]\n"
      "v_rsq_f32 v216, v216\n"
      "v_mfma_f32_16x16x32_bf16 v[248:251], a[84:87], v[144:147], v[248:251]\n"
      "v_mov_b32 v215, v216\n"
      "v_mfma_f32_16x16x32_bf16 v[248:251], a[120:123], v[196:199], v[248:251]\n"
      "s_nop 0\n"
      "v_permlane16_swap_b32 v216, v215\n"
      "v_mfma_f32_16x16x32_bf16 v[252:255], a[88:91], v[144:147], v[252:255]\n"
      "v_mul_f32 v180, v215, v180\n"
      "v_mul_f32 v181, v215, v181\n"
      "v_mfma_f32_16x16x32_bf16 v[252:255], a[124:127], v[196:199], v[252:255]\n"
      "v_cvt_pk_bf16_f32 v180, v180, v181\n"
      "v_mul_f32 v182, v215, v182\n"
      "v_mfma_f32_16x16x32_bf16 v[220:223], a[92:95], v[144:147], v[220:223]\n"
      "v_mul_f32 v183, v215, v183\n"
      "v_cvt_pk_bf16_f32 v181, v182, v183\n"
      "v_mfma_f32_16x16x32_bf16 v[220:223], a[128:131], v[196:199], v[220:223]\n"
      "v_mul_f32 v232, v215, v232\n"
      "v_mul_f32 v233, v215, v233\n"
      "v_cvt_pk_bf16_f32 v182, v232, v233\n"
      "v_mfma_f32_16x16x32_bf16 v[160:163], v[224:227], a[32:35], 0\n"
      "ds_read_b128 v[224:227], v114 offset:14336\n"
      "v_mul_f32 v234, v215, v234\n"
      "v_mfma_f32_16x16x32_bf16 v[160:163], v[236:239], a[68:71], v[160:163]\n"
      "ds_read_b128 v[236:239], v115 offset:14336\n"
      "v_mul_f32 v235, v215, v235\n"
      "v_mfma_f32_16x16x32_bf16 v[152:155], v[144:147], a[96:99], v[152:155]\n"
      "v_cvt_pk_bf16_f32 v183, v234, v235\n"
      "v_mul_f32 v164, v215, v164\n"
      "v_mfma_f32_16x16x32_bf16 v[152:155], v[196:199], a[132:135], v[152:155]\n"
      "v_mul_f32 v165, v215, v165\n"
      "v_cvt_pk_bf16_f32 v34, v164, v165\n"
      "v_mfma_f32_16x16x32_bf16 v[232:235], v[156:159], a[240:243], 0\n"
      "global_load_lds_dwordx4 v119, s[56:57]\n"
      "v_mfma_f32_16x16x32_bf16 v[232:235], v[156:159], a[244:247], v[232:235]\n"
      "s_add_u32 m0, s79, 4864\n"
      "v_mul_f32 v166, v215, v166\n"
      "v_mul_f32 v167, v215, v167\n"
      "v_mfma_f32_16x16x32_bf16 v[232:235], v[156:159], a[248:251], v[232:235]\n"
      "v_cvt_pk_bf16_f32 v35, v166, v167\n"
      "v_mul_f32 v216, s36, v216\n"
      "v_mfma_f32_16x16x32_bf16 v[244:247], v[144:147], a[100:103], v[244:247]\n"
      "v_mul_f32 v192, v216, v192\n"
      "v_mul_f32_e64 v193, v216, v193\n"
      "v_mfma_f32_16x16x32_bf16 v[244:247], v[196:199], a[136:139], v[244:247]\n"
      "v_cvt_pk_bf16_f32 v192, v192, v193\n"
      "v_mul_f32 v194, v216, v194\n"
      "v_mfma_f32_16x16x32_bf16 v[160:163], v[144:147], a[104:107], v[160:163]\n"
      "v_mul_f32 v195, v216, v195\n"
      "v_cvt_pk_bf16_f32 v193, v194, v195\n"
      "v_mfma_f32_16x16x32_bf16 v[160:163], v[196:199], a[140:143], v[160:163]\n"
      "v_mul_f32 v228, v216, v228\n"
      "s_waitcnt lgkmcnt(0)\n"
      "v_mfma_f32_16x16x32_bf16 v[172:175], a[144:147], v[224:227], v[172:175]\n"
      "v_mul_f32_e64 v229, v216, v229\n"
      "v_cvt_pk_bf16_f32 v194, v228, v229\n"
      "v_mfma_f32_16x16x32_bf16 v[172:175], a[180:183], v[236:239], v[172:175]\n"
      "v_mul_f32 v230, v216, v230\n"
      "v_mul_f32 v231, v216, v231\n"
      "v_mfma_f32_16x16x32_bf16 v[168:171], a[148:151], v[224:227], v[168:171]\n"
      "v_cvt_pk_bf16_f32 v195, v230, v231\n"
      "v_mul_f32 v148, v216, v148\n"
      "v_mfma_f32_16x16x32_bf16 v[168:171], a[184:187], v[236:239], v[168:171]\n"
      "v_mul_f32 v149, v216, v149\n"
      "v_cvt_pk_bf16_f32 v32, v148, v149\n"
      "s_waitcnt vmcnt(14)\n"
      "v_mfma_f32_16x16x32_bf16 v[196:199], v[156:159], a[252:255], 0\n"
      "v_mul_f32 v150, v216, v150\n"
      "v_mul_f32 v151, v216, v151\n"
      "s_waitcnt vmcnt(13)\n"
      "v_mfma_f32_16x16x32_bf16 v[196:199], v[156:159], v[0:3], v[196:199]\n"
      "v_cvt_pk_bf16_f32 v33, v150, v151\n"
      "v_add_f32_dpp v232, v232, v232 quad_perm:[1,0,3,2] row_mask:0xf bank_mask:0xf\n"
      "v_mfma_f32_16x16x32_bf16 v[136:139], a[152:155], v[224:227], v[136:139]\n"
      "s_nop 0\n"
      "v_add_f32_dpp v232, v232, v232 quad_perm:[2,3,0,1] row_mask:0xf bank_mask:0xf\n"
      "v_mfma_f32_16x16x32_bf16 v[136:139], a[188:191], v[236:239], v[136:139]\n"
      "s_nop 0\n"
      "v_add_f32_dpp v232, v232, v232 row_half_mirror row_mask:0xf bank_mask:0xf\n"
      "v_mul_f32 v235, 0xbbaaaaab, v232\n"
      "s_waitcnt vmcnt(12)\n"
      "v_mfma_f32_16x16x32_bf16 v[196:199], v[156:159], v[4:7], v[196:199]\n"
      "v_mul_f32 v234, 0xbbaaaaab, v232\n"
      "v_mul_f32 v233, 0xbbaaaaab, v232\n"
      "v_mfma_f32_16x16x32_bf16 v[248:251], a[156:159], v[224:227], v[248:251]\n"
      "v_mul_f32 v232, 0xbbaaaaab, v232\n"
      "v_mfma_f32_16x16x32_bf16 v[248:251], a[192:195], v[236:239], v[248:251]\n"
      "s_nop 0\n"
      "v_mfma_f32_16x16x32_bf16 v[148:151], v[186:189], a[240:243], v[232:235]\n"
      "ds_read_b128 v[144:147], v114 offset:4096\n"
      "v_add_f32_dpp v196, v196, v196 quad_perm:[1,0,3,2] row_mask:0xf bank_mask:0xf\n"
      "v_mfma_f32_16x16x32_bf16 v[228:231], v[184:187], a[240:243], v[232:235]\n"
      "s_mov_b64 exec, 0xfff\n"
      "global_load_lds_dwordx4 v122, s[58:59]\n"
      "s_mov_b64 exec, -1\n"
      "v_mfma_f32_16x16x32_bf16 v[176:179], v[186:189], a[244:247], v[232:235]\n"
      "s_nop 0\n"
      "v_cvt_pk_bf16_f32 v148, v148, v149\n"
      "v_cvt_pk_bf16_f32 v149, v150, v151\n"
      "v_mfma_f32_16x16x32_bf16 v[252:255], a[160:163], v[224:227], v[252:255]\n"
      "v_cvt_pk_bf16_f32 v150, v228, v229\n"
      "v_cvt_pk_bf16_f32 v151, v230, v231\n"
      "v_mfma_f32_16x16x32_bf16 v[252:255], a[196:199], v[236:239], v[252:255]\n"
      "ds_write_b128 v212, v[148:151] offset:6144\n"
      "v_mfma_f32_16x16x32_bf16 v[148:151], v[184:187], a[244:247], v[232:235]\n"
      "ds_read_b128 v[228:231], v115 offset:4096\n"
      "v_cvt_pk_bf16_f32 v176, v176, v177\n"
      "v_mfma_f32_16x16x32_bf16 v[220:223], a[164:167], v[224:227], v[220:223]\n"
      "v_cvt_pk_bf16_f32 v177, v178, v179\n"
      "v_add_f32_dpp v196, v196, v196 quad_perm:[2,3,0,1] row_mask:0xf bank_mask:0xf\n"
      "v_mfma_f32_16x16x32_bf16 v[220:223], a[200:203], v[236:239], v[220:223]\n"
      "s_nop 0\n"
      "v_add_f32_dpp v196, v196, v196 row_half_mirror row_mask:0xf bank_mask:0xf\n"
      "v_cvt_pk_bf16_f32 v178, v148, v149\n"
      "v_mfma_f32_16x16x32_bf16 v[152:155], v[224:227], a[168:171], v[152:155]\n"
      "v_cvt_pk_bf16_f32 v179, v150, v151\n"
      "v_mul_f32 v199, 0xbbaaaaab, v196\n"
      "v_mfma_f32_16x16x32_bf16 v[152:155], v[236:239], a[204:207], v[152:155]\n"
      "ds_write_b128 v212, v[176:179] offset:6272\n"
      "v_mfma_f32_16x16x32_bf16 v[176:179], v[186:189], a[248:251], v[232:235]\n"
      "v_mul_f32 v198, 0xbbaaaaab, v196\n"
      "v_mul_f32 v197, 0xbbaaaaab, v196\n"
      "v_mfma_f32_16x16x32_bf16 v[148:151], v[184:187], a[248:251], v[232:235]\n"
      "v_mul_f32 v196, 0xbbaaaaab, v196\n"
      "v_cvt_pk_bf16_f32 v240, v240, v241\n"
      "v_mfma_f32_16x16x32_bf16 v[244:247], v[224:227], a[172:175], v[244:247]\n"
      "v_cvt_pk_bf16_f32 v241, v242, v243\n"
      "v_mfma_f32_16x16x32_bf16 v[244:247], v[236:239], a[208:211], v[244:247]\n"
      "v_cvt_pk_bf16_f32 v176, v176, v177\n"
      "v_cvt_pk_bf16_f32 v177, v178, v179\n"
      "s_waitcnt lgkmcnt(1)\n"
      "v_mfma_f32_16x16x32_bf16 v[164:167], v[186:189], a[252:255], v[196:199]\n"
      "v_cvt_pk_bf16_f32 v178, v148, v149\n"
      "v_cvt_pk_bf16_f32 v179, v150, v151\n"
      "v_mfma_f32_16x16x32_bf16 v[148:151], v[184:187], a[252:255], v[196:199]\n"
      "ds_write_b128 v212, v[176:179] offset:6400\n"
      "v_mfma_f32_16x16x32_bf16 v[176:179], v[186:189], v[0:3], v[196:199]\n"
      "v_cvt_pk_bf16_f32 v242, v152, v153\n"
      "v_cvt_pk_bf16_f32 v243, v154, v155\n"
      "v_mfma_f32_16x16x32_bf16 v[152:155], v[184:187], v[0:3], v[196:199]\n"
      "v_cvt_pk_bf16_f32 v164, v164, v165\n"
      "v_cvt_pk_bf16_f32 v165, v166, v167\n"
      "v_mfma_f32_16x16x32_bf16 v[160:163], v[224:227], a[176:179], v[160:163]\n"
      "v_cvt_pk_bf16_f32 v166, v148, v149\n"
      "v_cvt_pk_bf16_f32 v167, v150, v151\n"
      "v_mfma_f32_16x16x32_bf16 v[160:163], v[236:239], a[212:215], v[160:163]\n"
      "ds_write_b128 v212, v[164:167] offset:9216\n"
      "v_mfma_f32_16x16x32_bf16 v[236:239], v[186:189], v[4:7], v[196:199]\n"
      "v_cvt_pk_bf16_f32 v176, v176, v177\n"
      "v_cvt_pk_bf16_f32 v177, v178, v179\n"
      "v_mfma_f32_16x16x32_bf16 v[148:151], v[184:187], v[4:7], v[196:199]\n"
      "v_cvt_pk_bf16_f32 v178, v152, v153\n"
      "v_cvt_pk_bf16_f32 v179, v154, v155\n"
      "v_mfma_f32_16x16x32_bf16 v[224:227], a[0:3], v[144:147], 0\n"
      "ds_read_b128 v[152:155], v114 offset:10240\n"
      "v_cvt_pk_bf16_f32 v140, v140, v141\n"
      "v_mfma_f32_16x16x32_bf16 v[224:227], a[36:39], v[228:231], v[224:227]\n"
      "ds_write_b128 v212, v[176:179] offset:9344\n"
      "v_mfma_f32_16x16x32_bf16 v[196:199], a[4:7], v[144:147], 0\n"
      "ds_read_b128 v[164:167], v115 offset:10240\n"
      "v_cvt_pk_bf16_f32 v236, v236, v237\n"
      "v_mfma_f32_16x16x32_bf16 v[196:199], a[40:43], v[228:231], v[196:199]\n"
      "v_cvt_pk_bf16_f32 v237, v238, v239\n"
      "v_cvt_pk_bf16_f32 v238, v148, v149\n"
      "v_mfma_f32_16x16x32_bf16 v[232:235], a[8:11], v[144:147], 0\n"
      "v_cvt_pk_bf16_f32 v239, v150, v151\n"
      "v_cvt_pk_bf16_f32 v141, v142, v143\n"
      "v_mfma_f32_16x16x32_bf16 v[232:235], a[44:47], v[228:231], v[232:235]\n"
      "ds_write_b128 v212, v[236:239] offset:9472\n"
      "v_mfma_f32_16x16x32_bf16 v[176:179], a[12:15], v[144:147], 0\n"
      "v_cvt_pk_bf16_f32 v142, v244, v245\n"
      "v_cvt_pk_bf16_f32 v143, v246, v247\n"
      "v_mfma_f32_16x16x32_bf16 v[176:179], a[48:51], v[228:231], v[176:179]\n"
      "v_cvt_pk_bf16_f32 v200, v200, v201\n"
      "v_cvt_pk_bf16_f32 v201, v202, v203\n"
      "v_mfma_f32_16x16x32_bf16 v[244:247], a[16:19], v[144:147], 0\n"
      "v_cvt_pk_bf16_f32 v202, v160, v161\n"
      "v_cvt_pk_bf16_f32 v203, v162, v163\n"
      "v_mfma_f32_16x16x32_bf16 v[244:247], a[52:55], v[228:231], v[244:247]\n"
      "v_fma_f32 v215, v172, v172, s38\n"
      "v_fma_f32 v216, v248, v248, s38\n"
      "v_mfma_f32_16x16x32_bf16 v[236:239], a[20:23], v[144:147], 0\n"
      "v_fmac_f32 v215, v173, v173\n"
      "v_fmac_f32 v216, v249, v249\n"
      "v_mfma_f32_16x16x32_bf16 v[236:239], a[56:59], v[228:231], v[236:239]\n"
      "s_waitcnt lgkmcnt(1)\n"
      "v_fmac_f32 v215, v174, v174\n"
      "v_fmac_f32 v216, v250, v250\n"
      "v_mfma_f32_16x16x32_bf16 v[224:227], a[72:75], v[152:155], v[224:227]\n"
      "v_fmac_f32 v215, v175, v175\n"
      "v_fmac_f32 v216, v251, v251\n"
      "v_mfma_f32_16x16x32_bf16 v[224:227], a[108:111], v[164:167], v[224:227]\n"
      "v_fmac_f32 v215, v168, v168\n"
      "v_fmac_f32 v216, v252, v252\n"
      "v_fmac_f32 v215, v169, v169\n"
      "v_mfma_f32_16x16x32_bf16 v[196:199], a[76:79], v[152:155], v[196:199]\n"
      "v_fmac_f32 v216, v253, v253\n"
      "v_fmac_f32 v215, v170, v170\n"
      "v_mfma_f32_16x16x32_bf16 v[196:199], a[112:115], v[164:167], v[196:199]\n"
      "v_fmac_f32 v216, v254, v254\n"
      "v_fmac_f32 v215, v171, v171\n"
      "v_fmac_f32 v216, v255, v255\n"
      "v_fmac_f32 v215, v136, v136\n"
      "v_fmac_f32 v216, v220, v220\n"
      "v_mfma_f32_16x16x32_bf16 v[232:235], a[80:83], v[152:155], v[232:235]\n"
      "v_fmac_f32 v215, v137, v137\n"
      "v_fmac_f32 v216, v221, v221\n"
      "v_mfma_f32_16x16x32_bf16 v[232:235], a[116:119], v[164:167], v[232:235]\n"
      "v_fmac_f32 v215, v138, v138\n"
      "v_fmac_f32 v216, v222, v222\n"
      "v_fmac_f32 v215, v139, v139\n"
      "v_fmac_f32 v216, v223, v223\n"
      "s_nop 1\n"
      "v_permlane16_swap_b32 v215, v216\n"
      "v_mfma_f32_16x16x32_bf16 v[176:179], a[84:87], v[152:155], v[176:179]\n"
      "v_add_f32 v215, v215, v216\n"
      "v_mov_b32 v216, v215\n"
      "v_mfma_f32_16x16x32_bf16 v[176:179], a[120:123], v[164:167], v[176:179]\n"
      "s_nop 0\n"
      "v_permlane32_swap_b32 v215, v216\n"
      "v_mfma_f32_16x16x32_bf16 v[160:163], v[144:147], a[24:27], 0\n"
      "v_add_f32 v215, v215, v216\n"
      "v_mfma_f32_16x16x32_bf16 v[160:163], v[228:231], a[60:63], v[160:163]\n"
      "v_rsq_f32 v215, v215\n"
      "v_mfma_f32_16x16x32_bf16 v[244:247], a[88:91], v[152:155], v[244:247]\n"
      "v_mov_b32 v216, v215\n"
      "v_mfma_f32_16x16x32_bf16 v[244:247], a[124:127], v[164:167], v[244:247]\n"
      "s_nop 0\n"
      "v_permlane16_swap_b32 v215, v216\n"
      "v_mul_f32 v248, v216, v248\n"
      "v_mfma_f32_16x16x32_bf16 v[204:207], v[144:147], a[28:31], 0\n"
      "v_mul_f32 v249, v216, v249\n"
      "v_cvt_pk_bf16_f32 v248, v248, v249\n"
      "v_mfma_f32_16x16x32_bf16 v[204:207], v[228:231], a[64:67], v[204:207]\n"
      "v_mul_f32 v250, v216, v250\n"
      "v_mul_f32 v251, v216, v251\n"
      "v_mfma_f32_16x16x32_bf16 v[236:239], a[92:95], v[152:155], v[236:239]\n"
      "v_cvt_pk_bf16_f32 v249, v250, v251\n"
      "v_mul_f32 v252, v216, v252\n"
      "v_mfma_f32_16x16x32_bf16 v[236:239], a[128:131], v[164:167], v[236:239]\n"
      "v_mul_f32 v253, v216, v253\n"
      "v_cvt_pk_bf16_f32 v250, v252, v253\n"
      "v_mfma_f32_16x16x32_bf16 v[148:151], v[144:147], a[32:35], 0\n"
      "v_mul_f32 v254, v216, v254\n"
      "v_mul_f32 v255, v216, v255\n"
      "v_mfma_f32_16x16x32_bf16 v[148:151], v[228:231], a[68:71], v[148:151]\n"
      "v_cvt_pk_bf16_f32 v251, v254, v255\n"
      "v_mul_f32_e64 v220, v216, v220\n"
      "v_mfma_f32_16x16x32_bf16 v[160:163], v[152:155], a[96:99], v[160:163]\n"
      "v_mul_f32_e64 v221, v216, v221\n"
      "v_cvt_pk_bf16_f32 v40, v220, v221\n"
      "v_mfma_f32_16x16x32_bf16 v[160:163], v[164:167], a[132:135], v[160:163]\n"
      "ds_read_b128 v[228:231], v114 offset:16384\n"
      "v_mul_f32 v222, v216, v222\n"
      "v_mfma_f32_16x16x32_bf16 v[204:207], v[152:155], a[100:103], v[204:207]\n"
      "v_mul_f32 v223, v216, v223\n"
      "v_cvt_pk_bf16_f32 v41, v222, v223\n"
      "v_mfma_f32_16x16x32_bf16 v[204:207], v[164:167], a[136:139], v[204:207]\n"
      "v_mul_f32 v215, s36, v215\n"
      "v_mul_f32 v172, v215, v172\n"
      "s_waitcnt vmcnt(12)\n"
      "v_mfma_f32_16x16x32_bf16 v[220:223], v[156:159], v[8:11], 0\n"
      "v_mul_f32 v173, v215, v173\n"
      "v_cvt_pk_bf16_f32 v172, v172, v173\n"
      "s_waitcnt vmcnt(11)\n"
      "v_mfma_f32_16x16x32_bf16 v[220:223], v[156:159], v[12:15], v[220:223]\n"
      "v_mul_f32 v174, v215, v174\n"
      "v_mul_f32 v175, v215, v175\n"
      "s_waitcnt vmcnt(10)\n"
      "v_mfma_f32_16x16x32_bf16 v[220:223], v[156:159], v[16:19], v[220:223]\n"
      "v_cvt_pk_bf16_f32 v173, v174, v175\n"
      "v_mul_f32_e64 v168, v215, v168\n"
      "v_mfma_f32_16x16x32_bf16 v[148:151], v[152:155], a[104:107], v[148:151]\n"
      "ds_read_b128 v[152:155], v115 offset:16384\n"
      "v_mul_f32 v169, v215, v169\n"
      "v_mfma_f32_16x16x32_bf16 v[148:151], v[164:167], a[140:143], v[148:151]\n"
      "v_cvt_pk_bf16_f32 v174, v168, v169\n"
      "v_mul_f32 v170, v215, v170\n"
      "s_waitcnt vmcnt(9)\n"
      "v_mfma_f32_16x16x32_bf16 v[164:167], v[156:159], v[20:23], 0\n"
      "v_mul_f32_e64 v171, v215, v171\n"
      "v_cvt_pk_bf16_f32 v175, v170, v171\n"
      "s_waitcnt vmcnt(8)\n"
      "v_mfma_f32_16x16x32_bf16 v[164:167], v[156:159], v[24:27], v[164:167]\n"
      "v_mul_f32 v136, v215, v136\n"
      "v_mul_f32_e64 v137, v215, v137\n"
      "s_waitcnt vmcnt(7)\n"
      "v_mfma_f32_16x16x32_bf16 v[164:167], v[156:159], v[28:31], v[164:167]\n"
      "v_cvt_pk_bf16_f32 v38, v136, v137\n"
      "v_mul_f32_e64 v138, v215, v138\n"
      "v_mul_f32_e64 v139, v215, v139\n"
      "v_cvt_pk_bf16_f32 v39, v138, v139\n"
      "v_add_f32_dpp v220, v220, v220 quad_perm:[1,0,3,2] row_mask:0xf bank_mask:0xf\n"
      "s_nop 1\n"
      "v_add_f32_dpp v220, v220, v220 quad_perm:[2,3,0,1] row_mask:0xf bank_mask:0xf\n"
      "v_add_f32_dpp v164, v164, v164 quad_perm:[1,0,3,2] row_mask:0xf bank_mask:0xf\n"
      "s_nop 0\n"
      "v_add_f32_dpp v220, v220, v220 row_half_mirror row_mask:0xf bank_mask:0xf\n"
      "v_mul_f32 v223, 0xbbaaaaab, v220\n"
      "v_mul_f32 v222, 0xbbaaaaab, v220\n"
      "v_mul_f32 v221, 0xbbaaaaab, v220\n"
      "v_mul_f32 v220, 0xbbaaaaab, v220\n"
      "v_add_f32_dpp v164, v164, v164 quad_perm:[2,3,0,1] row_mask:0xf bank_mask:0xf\n"
      "s_nop 0\n"
      "v_mfma_f32_16x16x32_bf16 v[144:147], v[186:189], v[8:11], v[220:223]\n"
      "v_add_f32_dpp v164, v164, v164 row_half_mirror row_mask:0xf bank_mask:0xf\n"
      "v_mul_f32 v167, 0xbbaaaaab, v164\n"
      "s_waitcnt lgkmcnt(0)\n"
      "v_mfma_f32_16x16x32_bf16 v[136:139], v[184:187], v[8:11], v[220:223]\n"
      "v_mul_f32 v166, 0xbbaaaaab, v164\n"
      "v_mul_f32 v165, 0xbbaaaaab, v164\n"
      "v_mfma_f32_16x16x32_bf16 v[168:171], v[186:189], v[12:15], v[220:223]\n"
      "s_nop 0\n"
      "v_cvt_pk_bf16_f32 v144, v144, v145\n"
      "v_cvt_pk_bf16_f32 v145, v146, v147\n"
      "v_mfma_f32_16x16x32_bf16 v[224:227], a[144:147], v[228:231], v[224:227]\n"
      "s_nop 0\n"
      "v_cvt_pk_bf16_f32 v146, v136, v137\n"
      "v_cvt_pk_bf16_f32 v147, v138, v139\n"
      "v_mfma_f32_16x16x32_bf16 v[224:227], a[180:183], v[152:155], v[224:227]\n"
      "ds_write_b128 v212, v[144:147] offset:12288\n"
      "v_mfma_f32_16x16x32_bf16 v[136:139], v[184:187], v[12:15], v[220:223]\n"
      "v_cvt_pk_bf16_f32 v168, v168, v169\n"
      "v_cvt_pk_bf16_f32 v169, v170, v171\n"
      "v_mfma_f32_16x16x32_bf16 v[156:159], v[186:189], v[16:19], v[220:223]\n"
      "v_mul_f32 v164, 0xbbaaaaab, v164\n"
      "v_mfma_f32_16x16x32_bf16 v[196:199], a[148:151], v[228:231], v[196:199]\n"
      "v_mfma_f32_16x16x32_bf16 v[196:199], a[184:187], v[152:155], v[196:199]\n"
      "s_nop 1\n"
      "v_cvt_pk_bf16_f32 v170, v136, v137\n"
      "v_mfma_f32_16x16x32_bf16 v[232:235], a[152:155], v[228:231], v[232:235]\n"
      "v_cvt_pk_bf16_f32 v171, v138, v139\n"
      "v_cvt_pk_bf16_f32 v156, v156, v157\n"
      "v_mfma_f32_16x16x32_bf16 v[232:235], a[188:191], v[152:155], v[232:235]\n"
      "ds_write_b128 v212, v[168:171] offset:12416\n"
      "v_mfma_f32_16x16x32_bf16 v[136:139], v[184:187], v[16:19], v[220:223]\n"
      "v_cvt_pk_bf16_f32 v157, v158, v159\n"
      "v_mfma_f32_16x16x32_bf16 v[252:255], v[186:189], v[20:23], v[164:167]\n"
      "v_mfma_f32_16x16x32_bf16 v[168:171], v[184:187], v[20:23], v[164:167]\n"
      "v_mfma_f32_16x16x32_bf16 v[176:179], a[156:159], v[228:231], v[176:179]\n"
      "v_mfma_f32_16x16x32_bf16 v[176:179], a[192:195], v[152:155], v[176:179]\n"
      "v_mfma_f32_16x16x32_bf16 v[244:247], a[160:163], v[228:231], v[244:247]\n"
      "s_nop 1\n"
      "v_cvt_pk_bf16_f32 v158, v136, v137\n"
      "v_mfma_f32_16x16x32_bf16 v[244:247], a[196:199], v[152:155], v[244:247]\n"
      "v_cvt_pk_bf16_f32 v159, v138, v139\n"
      "v_cvt_pk_bf16_f32 v252, v252, v253\n"
      "v_mfma_f32_16x16x32_bf16 v[136:139], v[186:189], v[24:27], v[164:167]\n"
      "ds_write_b128 v212, v[156:159] offset:12544\n"
      "v_mfma_f32_16x16x32_bf16 v[220:223], v[184:187], v[24:27], v[164:167]\n"
      "ds_read_u16 v52, v134\n"
      "v_cvt_pk_bf16_f32 v253, v254, v255\n"
      "v_mfma_f32_16x16x32_bf16 v[236:239], a[164:167], v[228:231], v[236:239]\n"
      "ds_read_u16 v56, v134 offset:32\n"
      "v_cvt_pk_bf16_f32 v254, v168, v169\n"
      "v_mfma_f32_16x16x32_bf16 v[236:239], a[200:203], v[152:155], v[236:239]\n"
      "ds_read_b64 v[144:145], v132 offset:24\n"
      "v_cvt_pk_bf16_f32 v255, v170, v171\n"
      "v_mfma_f32_16x16x32_bf16 v[160:163], v[228:231], a[168:171], v[160:163]\n"
      "ds_read_b64 v[146:147], v133 offset:16\n"
      "v_cvt_pk_bf16_f32 v136, v136, v137\n"
      "v_mfma_f32_16x16x32_bf16 v[160:163], v[152:155], a[204:207], v[160:163]\n"
      "ds_write_b128 v212, v[252:255] offset:15360\n"
      "v_mfma_f32_16x16x32_bf16 v[168:171], v[186:189], v[28:31], v[164:167]\n"
      "ds_read_u16 v60, v134 offset:64\n"
      "v_cvt_pk_bf16_f32 v137, v138, v139\n"
      "v_mfma_f32_16x16x32_bf16 v[204:207], v[228:231], a[172:175], v[204:207]\n"
      "v_cvt_pk_bf16_f32 v138, v220, v221\n"
      "v_cvt_pk_bf16_f32 v139, v222, v223\n"
      "v_mfma_f32_16x16x32_bf16 v[204:207], v[152:155], a[208:211], v[204:207]\n"
      "ds_write_b128 v212, v[136:139] offset:15488\n"
      "v_mfma_f32_16x16x32_bf16 v[220:223], v[184:187], v[28:31], v[164:167]\n"
      "ds_read_b128 v[136:139], v132 offset:32\n"
      "v_cvt_pk_bf16_f32 v168, v168, v169\n"
      "v_mfma_f32_16x16x32_bf16 v[148:151], v[228:231], a[176:179], v[148:151]\n"
      "ds_read_b128 v[228:231], v133\n"
      "v_cvt_pk_bf16_f32 v169, v170, v171\n"
      "v_mfma_f32_16x16x32_bf16 v[148:151], v[152:155], a[212:215], v[148:151]\n"
      "s_waitcnt vmcnt(0)\n"
      "ds_read_b128 v[164:167], v119 offset:41728\n"
      "ds_read_b128 v[184:187], v122 offset:4864\n"
      "v_cvt_pk_bf16_f32 v170, v220, v221\n"
      "v_cvt_pk_bf16_f32 v171, v222, v223\n"
      "ds_write_b128 v212, v[168:171] offset:15616\n"
      "s_waitcnt lgkmcnt(1)\n"
      "v_or3_b32 v164, v164, v165, v166\n"
      "s_mov_b64 exec, 0xfff\n"
      "v_or3_b32 v164, v164, v184, v185\n"
      "v_or3_b32 v164, v164, v186, v187\n"
      "s_mov_b64 exec, -1\n"
      "v_bitop3_b32 v164, v164, v167, s40 bitop3:0xa8\n"
      "v_or_b32 v164, s4, v164\n"
      "ds_write_b32 v129, v164 offset:3072\n"
      "s_waitcnt lgkmcnt(0)\n"
      "s_barrier\n"
      "v_mfma_f32_16x16x32_bf16 v[184:187], v[180:183], v[192:195], v[78:81]\n"
      "ds_read_b64_tr_b16 a[216:217], v218\n"
      "v_fma_f32 v216, v224, v224, s38\n"
      "v_mfma_f32_16x16x32_bf16 v[184:187], v[34:37], v[32:35], v[184:187]\n"
      "ds_read_b64_tr_b16 a[220:221], v218 offset:8\n"
      "v_fma_f32 v212, v176, v176, s38\n"
      "v_mfma_f32_16x16x32_bf16 v[168:171], v[248:251], v[192:195], v[82:85]\n"
      "ds_read_b64_tr_b16 a[224:225], v219\n"
      "v_fmac_f32 v216, v225, v225\n"
      "v_mfma_f32_16x16x32_bf16 v[168:171], v[40:43], v[32:35], v[168:171]\n"
      "ds_read_b64_tr_b16 a[218:219], v218 offset:6144\n"
      "v_fmac_f32 v212, v177, v177\n"
      "v_mfma_f32_16x16x32_bf16 v[152:155], v[136:139], v[192:195], v[70:73]\n"
      "ds_read_b64_tr_b16 a[222:223], v218 offset:6152\n"
      "v_fmac_f32 v216, v226, v226\n"
      "v_mfma_f32_16x16x32_bf16 v[152:155], v[228:231], v[180:183], v[152:155]\n"
      "ds_read_b64_tr_b16 a[226:227], v219 offset:6144\n"
      "v_fmac_f32 v212, v178, v178\n"
      "v_fmac_f32 v216, v227, v227\n"
      "v_fmac_f32 v212, v179, v179\n"
      "v_fmac_f32 v216, v196, v196\n"
      "v_fmac_f32 v212, v244, v244\n"
      "v_fmac_f32 v216, v197, v197\n"
      "v_fmac_f32 v212, v245, v245\n"
      "v_fmac_f32 v216, v198, v198\n"
      "v_fmac_f32 v212, v246, v246\n"
      "v_fmac_f32 v216, v199, v199\n"
      "v_mfma_f32_16x16x32_bf16 v[152:155], v[144:147], v[32:35], v[152:155]\n"
      "ds_read_b64_tr_b16 a[228:229], v218 offset:18432\n"
      "v_fmac_f32 v212, v247, v247\n"
      "v_fmac_f32 v216, v232, v232\n"
      "v_fmac_f32 v212, v236, v236\n"
      "v_fmac_f32 v216, v233, v233\n"
      "v_fmac_f32 v212, v237, v237\n"
      "v_fmac_f32 v216, v234, v234\n"
      "v_fmac_f32 v212, v238, v238\n"
      "v_fmac_f32 v216, v235, v235\n"
      "v_fmac_f32 v212, v239, v239\n"
      "s_nop 1\n"
      "v_permlane16_swap_b32 v216, v212\n"
      "v_add_f32 v216, v216, v212\n"
      "v_mov_b32 v212, v216\n"
      "s_nop 1\n"
      "v_permlane32_swap_b32 v216, v212\n"
      "v_add_f32 v216, v216, v212\n"
      "v_rsq_f32 v216, v216\n"
      "s_nop 0\n"
      "v_mov_b32 v212, v216\n"
      "s_nop 1\n"
      "v_permlane16_swap_b32 v216, v212\n"
      "v_mul_f32 v176, v212, v176\n"
      "v_mul_f32 v177, v212, v177\n"
      "v_cvt_pk_bf16_f32 v176, v176, v177\n"
      "v_mul_f32 v178, v212, v178\n"
      "v_mul_f32 v179, v212, v179\n"
      "v_cvt_pk_bf16_f32 v177, v178, v179\n"
      "v_mul_f32 v244, v212, v244\n"
      "v_mul_f32 v245, v212, v245\n"
      "v_cvt_pk_bf16_f32 v178, v244, v245\n"
      "v_mul_f32 v246, v212, v246\n"
      "v_mul_f32 v247, v212, v247\n"
      "v_cvt_pk_bf16_f32 v179, v246, v247\n"
      "v_mul_f32 v236, v212, v236\n"
      "v_mul_f32 v237, v212, v237\n"
      "v_cvt_pk_bf16_f32 v46, v236, v237\n"
      "v_mul_f32 v238, v212, v238\n"
      "v_mul_f32 v239, v212, v239\n"
      "v_cvt_pk_bf16_f32 v47, v238, v239\n"
      "v_mul_f32 v216, s36, v216\n"
      "v_mul_f32 v224, v216, v224\n"
      "v_mfma_f32_16x16x32_bf16 v[156:159], v[176:179], v[192:195], v[86:89]\n"
      "ds_read_b64_tr_b16 a[232:233], v218 offset:18440\n"
      "v_mul_f32_e64 v225, v216, v225\n"
      "v_mfma_f32_16x16x32_bf16 v[156:159], v[46:49], v[32:35], v[156:159]\n"
      "ds_read_b64_tr_b16 a[236:237], v219 offset:18432\n"
      "ds_read_b64_tr_b16 a[230:231], v218 offset:24576\n"
      "ds_read_b64_tr_b16 a[234:235], v218 offset:24584\n"
      "ds_read_b64_tr_b16 a[238:239], v219 offset:24576\n"
      "ds_read_b64_tr_b16 a[240:241], v217\n"
      "ds_read_b64_tr_b16 a[244:245], v217 offset:8\n"
      "ds_read_b64_tr_b16 a[248:249], v213\n"
      "ds_read_b64_tr_b16 a[242:243], v217 offset:6144\n"
      "ds_read_b64_tr_b16 a[246:247], v217 offset:6152\n"
      "ds_read_b64_tr_b16 a[250:251], v213 offset:6144\n"
      "ds_read_b64_tr_b16 a[252:253], v217 offset:18432\n"
      "ds_read_b64_tr_b16 v[0:1], v217 offset:18440\n"
      "ds_read_b64_tr_b16 v[4:5], v213 offset:18432\n"
      "ds_read_b64_tr_b16 a[254:255], v217 offset:24576\n"
      "ds_read_b64_tr_b16 v[2:3], v217 offset:24584\n"
      "ds_read_b64_tr_b16 v[6:7], v213 offset:24576\n"
      "ds_read_b64_tr_b16 v[8:9], v218 offset:12288\n"
      "ds_read_b64_tr_b16 v[12:13], v218 offset:12296\n"
      "ds_read_b64_tr_b16 v[16:17], v219 offset:12288\n"
      "ds_read_b64_tr_b16 v[10:11], v218 offset:30720\n"
      "ds_read_b64_tr_b16 v[14:15], v218 offset:30728\n"
      "ds_read_b64_tr_b16 v[18:19], v219 offset:30720\n"
      "ds_read_b64_tr_b16 v[20:21], v217 offset:12288\n"
      "ds_read_b64_tr_b16 v[24:25], v217 offset:12296\n"
      "ds_read_b64_tr_b16 v[28:29], v213 offset:12288\n"
      "ds_read_b64_tr_b16 v[22:23], v217 offset:30720\n"
      "ds_read_b64_tr_b16 v[26:27], v217 offset:30728\n"
      "ds_read_b64_tr_b16 v[30:31], v213 offset:30720\n"
      "v_cvt_pk_bf16_f32 v224, v224, v225\n"
      "v_mul_f32 v226, v216, v226\n"
      "v_mul_f32 v227, v216, v227\n"
      "v_cvt_pk_bf16_f32 v225, v226, v227\n"
      "v_mul_f32 v196, v216, v196\n"
      "v_mul_f32 v197, v216, v197\n"
      "v_cvt_pk_bf16_f32 v226, v196, v197\n"
      "v_mul_f32 v198, v216, v198\n"
      "v_mul_f32 v199, v216, v199\n"
      "v_cvt_pk_bf16_f32 v227, v198, v199\n"
      "v_mul_f32 v232, v216, v232\n"
      "v_mul_f32 v233, v216, v233\n"
      "v_cvt_pk_bf16_f32 v44, v232, v233\n"
      "v_mul_f32 v234, v216, v234\n"
      "v_mul_f32 v235, v216, v235\n"
      "v_cvt_pk_bf16_f32 v45, v234, v235\n"
      "v_cvt_pk_bf16_f32 v50, v160, v161\n"
      "v_cvt_pk_bf16_f32 v51, v162, v163\n"
      "v_cvt_pk_bf16_f32 v54, v204, v205\n"
      "v_cvt_pk_bf16_f32 v55, v206, v207\n"
      "v_cvt_pk_bf16_f32 v58, v148, v149\n"
      "v_cvt_pk_bf16_f32 v59, v150, v151\n"
      "s_waitcnt lgkmcnt(0)\n"
      "s_barrier\n"
      "s_or_b64 s[34:35], s[48:49], s[50:51]\n"
      "s_cbranch_scc1 L_gen_8_%=\n"
      "L_fast_11_%=:\n"
      "v_mfma_f32_16x16x32_bf16 v[204:207], v[180:183], v[172:175], v[90:93]\n"
      "ds_read_b128 v[232:235], v128 offset:3072\n"
      "v_mfma_f32_16x16x32_bf16 v[204:207], v[34:37], v[38:41], v[204:207]\n"
      "v_exp_f32 v184, v184\n"
      "v_mfma_f32_16x16x32_bf16 v[244:247], v[248:251], v[172:175], v[94:97]\n"
      "v_exp_f32 v185, v185\n"
      "v_mfma_f32_16x16x32_bf16 v[244:247], v[40:43], v[38:41], v[244:247]\n"
      "v_exp_f32_e64 v186, v186\n"
      "v_mfma_f32_16x16x32_bf16 v[164:167], v[136:139], v[172:175], v[72:75]\n"
      "v_cvt_pk_bf16_f32 v184, v184, v185\n"
      "v_mfma_f32_16x16x32_bf16 v[164:167], v[228:231], v[248:251], v[164:167]\n"
      "v_exp_f32_e64 v187, v187\n"
      "v_mfma_f32_16x16x32_bf16 v[164:167], v[144:147], v[38:41], v[164:167]\n"
      "v_exp_f32_e64 v168, v168\n"
      "v_mfma_f32_16x16x32_bf16 v[148:151], v[176:179], v[172:175], v[98:101]\n"
      "v_cvt_pk_bf16_f32 v185, v186, v187\n"
      "v_mfma_f32_16x16x32_bf16 v[148:151], v[46:49], v[38:41], v[148:151]\n"
      "v_exp_f32 v169, v169\n"
      "v_mfma_f32_16x16x32_bf16 v[196:199], v[180:183], v[224:227], v[102:105]\n"
      "v_exp_f32 v170, v170\n"
      "v_mfma_f32_16x16x32_bf16 v[196:199], v[34:37], v[44:47], v[196:199]\n"
      "s_waitcnt lgkmcnt(0)\n"
      "v_cvt_pk_bf16_f32 v186, v168, v169\n"
      "v_bitop3_b32 v232, v232, v233, s4 bitop3:0x7e\n"
      "v_mfma_f32_16x16x32_bf16 v[236:239], v[248:251], v[224:227], v[106:109]\n"
      "v_exp_f32 v171, v171\n"
      "v_mfma_f32_16x16x32_bf16 v[236:239], v[40:43], v[44:47], v[236:239]\n"
      "v_exp_f32_e64 v156, v156\n"
      "v_mfma_f32_16x16x32_bf16 v[160:163], v[136:139], v[224:227], v[74:77]\n"
      "v_cvt_pk_bf16_f32 v187, v170, v171\n"
      "v_bitop3_b32 v234, v234, v235, s4 bitop3:0x7e\n"
      "v_mfma_f32_16x16x32_bf16 v[160:163], v[228:231], v[176:179], v[160:163]\n"
      "v_exp_f32 v157, v157\n"
      "v_mfma_f32_16x16x32_bf16 v[160:163], v[144:147], v[44:47], v[160:163]\n"
      "v_exp_f32 v158, v158\n"
      "v_mfma_f32_16x16x32_bf16 v[172:175], v[176:179], v[224:227], v[110:113]\n"
      "v_exp_f32 v159, v159\n"
      "v_mfma_f32_16x16x32_bf16 v[172:175], v[46:49], v[44:47], v[172:175]\n"
      "v_exp_f32 v152, v152\n"
      "v_mfma_f32_16x16x32_bf16 v[192:195], v[228:231], v[136:139], v[76:79]\n"
      "v_cvt_pk_bf16_f32 v62, v156, v157\n"
      "v_cvt_pk_bf16_f32 v63, v158, v159\n"
      "v_cvt_pk_bf16_f32 v64, v152, 0\n"
      "v_or_b32 v232, v232, v234\n"
      "v_cmp_ne_u32 vcc, 0, v232\n"
      "v_mfma_f32_16x16x32_bf16 v[224:227], v[66:69], v[184:187], 0\n"
      "s_mov_b64 s[48:49], vcc\n"
      "v_exp_f32_e64 v204, v204\n"
      "v_mfma_f32_16x16x32_bf16 v[224:227], v[66:69], v[62:65], v[224:227]\n"
      "s_cbranch_vccnz L_flg_12_%=\n"
      "L_fb_13_%=:\n"
      "v_mfma_f32_16x16x32_bf16 v[248:251], v[240:243], v[184:187], 0\n"
      "s_add_u32 s28, s27, 2\n"
      "v_exp_f32_e64 v205, v205\n"
      "v_mfma_f32_16x16x32_bf16 v[248:251], v[50:53], v[62:65], v[248:251]\n"
      "s_min_u32 m0, s28, s3\n"
      "v_exp_f32 v206, v206\n"
      "v_mfma_f32_16x16x32_bf16 v[176:179], v[140:143], v[184:187], 0\n"
      "s_movrels_b32 s9, s80\n"
      "v_cvt_pk_bf16_f32 v204, v204, v205\n"
      "v_cndmask_b32 v153, v153, v165, s[42:43]\n"
      "v_mfma_f32_16x16x32_bf16 v[176:179], v[54:57], v[62:65], v[176:179]\n"
      "s_lshr_b32 s8, s9, 8\n"
      "v_exp_f32 v207, v207\n"
      "v_mfma_f32_16x16x32_bf16 v[168:171], v[200:203], v[184:187], 0\n"
      "s_and_b32 s9, s9, 0xff\n"
      "v_exp_f32_e64 v244, v244\n"
      "v_mfma_f32_16x16x32_bf16 v[168:171], v[58:61], v[62:65], v[168:171]\n"
      "s_mul_i32 s10, s8, 73\n"
      "v_cvt_pk_bf16_f32 v205, v206, v207\n"
      "v_cndmask_b32 v153, v153, v161, s[44:45]\n"
      "v_mfma_f32_16x16x16_bf16 v[192:195], v[146:147], v[144:145], v[192:195]\n"
      "s_add_u32 s10, s10, s9\n"
      "v_exp_f32 v245, v245\n"
      "v_exp_f32_e64 v246, v246\n"
      "v_cvt_pk_bf16_f32 v206, v244, v245\n"
      "v_exp_f32 v247, v247\n"
      "v_exp_f32 v148, v148\n"
      "v_cvt_pk_bf16_f32 v207, v246, v247\n"
      "v_exp_f32 v149, v149\n"
      "v_exp_f32 v150, v150\n"
      "v_exp_f32 v151, v151\n"
      "v_exp_f32 v164, v164\n"
      "v_cvt_pk_bf16_f32 v62, v148, v149\n"
      "v_cvt_pk_bf16_f32 v63, v150, v151\n"
      "v_cvt_pk_bf16_f32 v64, v164, 0\n"
      "v_rcp_f32 v224, v224\n"
      "v_cndmask_b32 v153, v153, v193, s[46:47]\n"
      "v_mfma_f32_16x16x32_bf16 v[148:151], v[66:69], v[204:207], 0\n"
      "s_mul_i32 s28, s10, 4802\n"
      "v_mul_f32 v248, v224, v248\n"
      "v_mul_f32_e64 v249, v224, v249\n"
      "v_mfma_f32_16x16x32_bf16 v[148:151], v[66:69], v[62:65], v[148:151]\n"
      "ds_read_b128 v[164:167], v114 offset:18432\n"
      "v_cvt_pk_bf16_f32 v248, v248, v249\n"
      "v_mfma_f32_16x16x32_bf16 v[228:231], v[240:243], v[204:207], 0\n"
      "ds_read_b128 v[184:187], v115 offset:18432\n"
      "v_mul_f32 v250, v224, v250\n"
      "v_mfma_f32_16x16x32_bf16 v[228:231], v[50:53], v[62:65], v[228:231]\n"
      "s_add_u32 s56, s18, s28\n"
      "v_mul_f32 v251, v224, v251\n"
      "v_cvt_pk_bf16_f32 v249, v250, v251\n"
      "v_mfma_f32_16x16x32_bf16 v[192:195], v[140:143], v[204:207], 0\n"
      "s_addc_u32 s57, s19, 0\n"
      "v_mul_f32 v176, v224, v176\n"
      "v_mul_f32 v177, v224, v177\n"
      "v_mfma_f32_16x16x32_bf16 v[192:195], v[54:57], v[62:65], v[192:195]\n"
      "s_and_b32 s56, s56, -16\n"
      "v_cvt_pk_bf16_f32 v250, v176, v177\n"
      "v_mul_f32 v178, v224, v178\n"
      "v_mfma_f32_16x16x32_bf16 v[144:147], v[200:203], v[204:207], 0\n"
      "s_sub_u32 s58, s56, 110592\n"
      "v_mul_f32_e64 v179, v224, v179\n"
      "v_cvt_pk_bf16_f32 v251, v178, v179\n"
      "v_mfma_f32_16x16x32_bf16 v[144:147], v[58:61], v[62:65], v[144:147]\n"
      "ds_write_b128 v124, v[248:251]\n"
      "v_mul_f32 v168, v224, v168\n"
      "v_mul_f32 v169, v224, v169\n"
      "v_cvt_pk_bf16_f32 v168, v168, v169\n"
      "v_mul_f32 v170, v224, v170\n"
      "v_mul_f32 v171, v224, v171\n"
      "v_cvt_pk_bf16_f32 v169, v170, v171\n"
      "v_exp_f32 v153, v153\n"
      "v_exp_f32 v196, v196\n"
      "v_cvt_pk_bf16_f32 v153, v153, v153\n"
      "v_exp_f32 v197, v197\n"
      "v_exp_f32 v198, v198\n"
      "v_cvt_pk_bf16_f32 v196, v196, v197\n"
      "v_exp_f32 v199, v199\n"
      "v_exp_f32 v236, v236\n"
      "v_cvt_pk_bf16_f32 v197, v198, v199\n"
      "v_exp_f32 v237, v237\n"
      "v_exp_f32 v238, v238\n"
      "v_cvt_pk_bf16_f32 v198, v236, v237\n"
      "v_exp_f32 v239, v239\n"
      "v_exp_f32 v172, v172\n"
      "v_cvt_pk_bf16_f32 v199, v238, v239\n"
      "v_exp_f32 v173, v173\n"
      "v_exp_f32 v174, v174\n"
      "v_exp_f32 v175, v175\n"
      "v_exp_f32 v160, v160\n"
      "v_cvt_pk_bf16_f32 v62, v172, v173\n"
      "v_cvt_pk_bf16_f32 v63, v174, v175\n"
      "v_cvt_pk_bf16_f32 v64, v160, 0\n"
      "v_rcp_f32 v148, v148\n"
      "v_mfma_f32_16x16x32_bf16 v[136:139], v[66:69], v[196:199], 0\n"
      "s_subb_u32 s59, s57, 0\n"
      "v_mul_f32 v228, v148, v228\n"
      "v_mul_f32 v229, v148, v229\n"
      "v_mfma_f32_16x16x32_bf16 v[136:139], v[66:69], v[62:65], v[136:139]\n"
      "ds_write_b64 v125, v[168:169]\n"
      "v_mfma_f32_16x16x32_bf16 v[172:175], v[240:243], v[196:199], 0\n"
      "s_sub_u32 s56, s56, 73728\n"
      "v_cvt_pk_bf16_f32 v228, v228, v229\n"
      "v_mul_f32_e64 v230, v148, v230\n"
      "v_mfma_f32_16x16x32_bf16 v[172:175], v[50:53], v[62:65], v[172:175]\n"
      "ds_write_b16 v130, v153\n"
      "v_mfma_f32_16x16x32_bf16 v[156:159], v[140:143], v[196:199], 0\n"
      "ds_read_b128 v[236:239], v131\n"
      "v_mul_f32 v231, v148, v231\n"
      "v_mfma_f32_16x16x32_bf16 v[156:159], v[54:57], v[62:65], v[156:159]\n"
      "ds_read_b128 v[180:183], v131 offset:64\n"
      "s_waitcnt lgkmcnt(4)\n"
      "v_mfma_f32_16x16x32_bf16 v[168:171], v[200:203], v[196:199], 0\n"
      "s_subb_u32 s57, s57, 0\n"
      "v_cvt_pk_bf16_f32 v229, v230, v231\n"
      "v_mul_f32 v192, v148, v192\n"
      "v_mfma_f32_16x16x32_bf16 v[168:171], v[58:61], v[62:65], v[168:171]\n"
      "s_mul_i32 s28, s8, 1373568\n"
      "v_mul_f32_e64 v193, v148, v193\n"
      "v_cvt_pk_bf16_f32 v230, v192, v193\n"
      "v_mfma_f32_16x16x32_bf16 v[232:235], a[0:3], v[164:167], 0\n"
      "s_mul_i32 s29, s9, 2688\n"
      "v_mul_f32 v194, v148, v194\n"
      "v_mul_f32_e64 v195, v148, v195\n"
      "v_mfma_f32_16x16x32_bf16 v[232:235], a[36:39], v[184:187], v[232:235]\n"
      "s_add_u32 s28, s28, s29\n"
      "v_cvt_pk_bf16_f32 v231, v194, v195\n"
      "v_mul_f32 v144, v148, v144\n"
      "v_mfma_f32_16x16x32_bf16 v[192:195], a[4:7], v[164:167], 0\n"
      "s_cmp_eq_u32 s8, 72\n"
      "v_mul_f32 v145, v148, v145\n"
      "v_cvt_pk_bf16_f32 v144, v144, v145\n"
      "v_mfma_f32_16x16x32_bf16 v[192:195], a[40:43], v[184:187], v[192:195]\n"
      "ds_write_b128 v124, v[228:231] offset:256\n"
      "v_mul_f32 v146, v148, v146\n"
      "v_mul_f32 v147, v148, v147\n"
      "v_cvt_pk_bf16_f32 v145, v146, v147\n"
      "v_mov_b32 v146, 0\n"
      "s_cselect_b32 s8, 0, 100270464\n"
      "ds_write_b64 v125, v[144:145] offset:256\n"
      "s_waitcnt lgkmcnt(2)\n"
      "v_mfma_f32_16x16x32_bf16 v[196:199], v[236:239], v[66:69], 0\n"
      "s_cmp_eq_u32 s9, 72\n"
      "s_cselect_b32 s9, 0, 196224\n"
      "s_add_u32 s29, s8, s9\n"
      "v_mfma_f32_16x16x32_bf16 v[196:199], v[180:183], v[66:69], v[196:199]\n"
      "ds_write_b32 v129, v146\n"
      "v_mfma_f32_16x16x32_bf16 v[144:147], v[236:239], v[240:243], 0\n"
      "s_add_u32 s28, s28, s29\n"
      "s_add_u32 s52, s16, s28\n"
      "s_addc_u32 s53, s17, 0\n"
      "v_mfma_f32_16x16x32_bf16 v[144:147], v[180:183], v[50:53], v[144:147]\n"
      "s_cmp_lg_u32 s29, s11\n"
      "s_nop 0\n"
      "v_mov_b32_dpp v196, v196 quad_perm:[0,0,0,0] row_mask:0xf bank_mask:0xf\n"
      "v_mfma_f32_16x16x32_bf16 v[240:243], v[236:239], v[140:143], 0\n"
      "s_cbranch_scc1 L_edge_15_%=\n"
      "L_eb_16_%=:\n"
      "v_mfma_f32_16x16x32_bf16 v[240:243], v[180:183], v[54:57], v[240:243]\n"
      "v_rcp_f32 v196, v196\n"
      "v_mfma_f32_16x16x32_bf16 v[204:207], v[236:239], v[200:203], 0\n"
      "v_mfma_f32_16x16x32_bf16 v[204:207], v[180:183], v[58:61], v[204:207]\n"
      "s_nop 4\n"
      "v_cndmask_b32 v144, v144, v240, s[42:43]\n"
      "s_nop 1\n"
      "v_cndmask_b32 v144, v144, v204, s[44:45]\n"
      "v_mul_f32_e64 v144, v196, v144\n"
      "v_cvt_pk_bf16_f32 v144, v144, v144\n"
      "ds_write_b16 v135, v144\n"
      "L_join_10_%=:\n"
      "s_mov_b32 s27, 1\n"
      ".p2align 6\n"
      "L_loop_%=:\n"
      "v_mfma_f32_16x16x32_bf16 v[240:243], a[8:11], v[164:167], 0\n"
      "ds_read_b128 v[204:207], v114 offset:24576\n"
      "s_add_u32 m0, s76, 0\n"
      "v_mfma_f32_16x16x32_bf16 v[240:243], a[44:47], v[184:187], v[240:243]\n"
      "ds_read_b128 v[236:239], v115 offset:24576\n"
      "v_rcp_f32 v136, v136\n"
      "v_mfma_f32_16x16x32_bf16 v[228:231], a[12:15], v[164:167], 0\n"
      "v_mul_f32 v172, v136, v172\n"
      "v_mul_f32 v173, v136, v173\n"
      "v_mfma_f32_16x16x32_bf16 v[228:231], a[48:51], v[184:187], v[228:231]\n"
      "ds_read_b128 v[152:155], v114 offset:30720\n"
      "v_cvt_pk_bf16_f32 v172, v172, v173\n"
      "v_mfma_f32_16x16x32_bf16 v[144:147], a[16:19], v[164:167], 0\n"
      "v_mul_f32 v174, v136, v174\n"
      "v_mul_f32 v175, v136, v175\n"
      "v_mfma_f32_16x16x32_bf16 v[144:147], a[52:55], v[184:187], v[144:147]\n"
      "s_waitcnt lgkmcnt(3)\n"
      "s_barrier\n"
      "v_mfma_f32_16x16x32_bf16 v[188:191], a[20:23], v[164:167], 0\n"
      "ds_read_b128 v[224:227], v128\n"
      "v_cvt_pk_bf16_f32 v173, v174, v175\n"
      "v_mfma_f32_16x16x32_bf16 v[188:191], a[56:59], v[184:187], v[188:191]\n"
      "global_load_lds_dwordx4 v116, s[52:53]\n"
      "v_mfma_f32_16x16x32_bf16 v[160:163], v[164:167], a[24:27], 0\n"
      "s_add_u32 m0, s76, 6016\n"
      "v_mul_f32 v156, v136, v156\n"
      "v_mul_f32_e64 v157, v136, v157\n"
      "v_mfma_f32_16x16x32_bf16 v[160:163], v[184:187], a[60:63], v[160:163]\n"
      "s_waitcnt lgkmcnt(2)\n"
      "v_cvt_pk_bf16_f32 v174, v156, v157\n"
      "v_mul_f32 v158, v136, v158\n"
      "v_mfma_f32_16x16x32_bf16 v[220:223], v[164:167], a[28:31], 0\n"
      "v_mul_f32 v159, v136, v159\n"
      "v_cvt_pk_bf16_f32 v175, v158, v159\n"
      "v_mfma_f32_16x16x32_bf16 v[220:223], v[184:187], a[64:67], v[220:223]\n"
      "ds_write_b128 v124, v[172:175] offset:512\n"
      "v_mfma_f32_16x16x32_bf16 v[156:159], v[164:167], a[32:35], 0\n"
      "ds_read_b128 v[164:167], v115 offset:30720\n"
      "v_mul_f32 v168, v136, v168\n"
      "v_mfma_f32_16x16x32_bf16 v[156:159], v[184:187], a[68:71], v[156:159]\n"
      "v_mul_f32 v169, v136, v169\n"
      "v_cvt_pk_bf16_f32 v168, v168, v169\n"
      "v_mfma_f32_16x16x32_bf16 v[232:235], a[72:75], v[204:207], v[232:235]\n"
      "v_mul_f32 v170, v136, v170\n"
      "v_mul_f32_e64 v171, v136, v171\n"
      "v_mfma_f32_16x16x32_bf16 v[232:235], a[108:111], v[236:239], v[232:235]\n"
      "s_waitcnt lgkmcnt(2)\n"
      "v_cvt_pk_bf16_f32 v169, v170, v171\n"
      "v_add_f32 v224, |v224|, |v225|\n"
      "v_mfma_f32_16x16x32_bf16 v[192:195], a[76:79], v[204:207], v[192:195]\n"
      "v_add_f32 v226, |v226|, |v227|\n"
      "v_add_f32_e64 v224, v224, v226\n"
      "v_mfma_f32_16x16x32_bf16 v[192:195], a[112:115], v[236:239], v[192:195]\n"
      "ds_write_b64 v125, v[168:169] offset:512\n"
      "v_mfma_f32_16x16x32_bf16 v[240:243], a[80:83], v[204:207], v[240:243]\n"
      "global_load_lds_dwordx4 v116, s[52:53] offset:128\n"
      "v_mfma_f32_16x16x32_bf16 v[240:243], a[116:119], v[236:239], v[240:243]\n"
      "s_add_u32 m0, s76, 12032\n"
      "v_rsq_f32 v224, v224\n"
      "v_mfma_f32_16x16x32_bf16 v[228:231], a[84:87], v[204:207], v[228:231]\n"
      "v_mul_f32 v224, s41, v224\n"
      "v_mov_b32 v172, v224\n"
      "v_mfma_f32_16x16x32_bf16 v[228:231], a[120:123], v[236:239], v[228:231]\n"
      "s_waitcnt lgkmcnt(1)\n"
      "v_permlane32_swap_b32_e64 v224, v172\n"
      "v_mfma_f32_16x16x32_bf16 v[144:147], a[88:91], v[204:207], v[144:147]\n"
      "ds_read_b128 v[184:187], v114 offset:20480\n"
      "v_mov_b32 v173, v224\n"
      "v_mfma_f32_16x16x32_bf16 v[144:147], a[124:127], v[236:239], v[144:147]\n"
      "s_nop 0\n"
      "v_permlane16_swap_b32_e64 v224, v173\n"
      "v_mfma_f32_16x16x32_bf16 v[188:191], a[92:95], v[204:207], v[188:191]\n"
      "ds_read_b128 v[168:171], v115 offset:20480\n"
      "v_mul_f32 v212, v224, v212\n"
      "v_mfma_f32_16x16x32_bf16 v[188:191], a[128:131], v[236:239], v[188:191]\n"
      "v_mul_f32 v213, v224, v213\n"
      "v_cvt_pk_bf16_f32 v212, v212, v213\n"
      "v_mfma_f32_16x16x32_bf16 v[160:163], v[204:207], a[96:99], v[160:163]\n"
      "v_mul_f32 v214, v224, v214\n"
      "v_mul_f32 v215, v224, v215\n"
      "v_mfma_f32_16x16x32_bf16 v[160:163], v[236:239], a[132:135], v[160:163]\n"
      "global_load_lds_dwordx4 v116, s[52:53] offset:256\n"
      "v_mfma_f32_16x16x32_bf16 v[220:223], v[204:207], a[100:103], v[220:223]\n"
      "s_add_u32 m0, s77, 0\n"
      "v_cvt_pk_bf16_f32 v213, v214, v215\n"
      "v_mul_f32 v244, v224, v244\n"
      "v_mfma_f32_16x16x32_bf16 v[220:223], v[236:239], a[136:139], v[220:223]\n"
      "v_mul_f32 v245, v224, v245\n"
      "v_cvt_pk_bf16_f32 v214, v244, v245\n"
      "v_mfma_f32_16x16x32_bf16 v[156:159], v[204:207], a[104:107], v[156:159]\n"
      "v_mul_f32 v246, v224, v246\n"
      "v_mul_f32 v247, v224, v247\n"
      "v_mfma_f32_16x16x32_bf16 v[156:159], v[236:239], a[140:143], v[156:159]\n"
      "v_cvt_pk_bf16_f32 v215, v246, v247\n"
      "v_mul_f32 v180, v224, v180\n"
      "v_mfma_f32_16x16x32_bf16 v[232:235], a[144:147], v[152:155], v[232:235]\n"
      "v_mul_f32 v181, v224, v181\n"
      "v_cvt_pk_bf16_f32 v180, v180, v181\n"
      "v_mfma_f32_16x16x32_bf16 v[232:235], a[180:183], v[164:167], v[232:235]\n"
      "ds_write_b128 v126, v[212:215] offset:18432\n"
      "s_waitcnt lgkmcnt(1)\n"
      "v_mfma_f32_16x16x32_bf16 v[192:195], a[148:151], v[152:155], v[192:195]\n"
      "v_mul_f32 v182, v224, v182\n"
      "v_mul_f32 v183, v224, v183\n"
      "v_mfma_f32_16x16x32_bf16 v[192:195], a[184:187], v[164:167], v[192:195]\n"
      "global_load_lds_dwordx4 v117, s[52:53]\n"
      "v_mfma_f32_16x16x32_bf16 v[240:243], a[152:155], v[152:155], v[240:243]\n"
      "s_add_u32 m0, s78, 0\n"
      "v_cvt_pk_bf16_f32 v181, v182, v183\n"
      "v_mul_f32 v148, v173, v148\n"
      "v_mfma_f32_16x16x32_bf16 v[240:243], a[188:191], v[164:167], v[240:243]\n"
      "ds_write_b64 v127, v[180:181] offset:18432\n"
      "v_mfma_f32_16x16x32_bf16 v[228:231], a[156:159], v[152:155], v[228:231]\n"
      "v_mul_f32_e64 v149, v173, v149\n"
      "v_cvt_pk_bf16_f32 v148, v148, v149\n"
      "v_mfma_f32_16x16x32_bf16 v[228:231], a[192:195], v[164:167], v[228:231]\n"
      "ds_read_b128 v[212:215], v114 offset:26624\n"
      "v_mul_f32_e64 v150, v173, v150\n"
      "v_mfma_f32_16x16x32_bf16 v[144:147], a[160:163], v[152:155], v[144:147]\n"
      "ds_read_b128 v[180:183], v115 offset:26624\n"
      "v_mul_f32 v151, v173, v151\n"
      "v_mfma_f32_16x16x32_bf16 v[144:147], a[196:199], v[164:167], v[144:147]\n"
      "v_cvt_pk_bf16_f32 v149, v150, v151\n"
      "v_mul_f32 v248, v173, v248\n"
      "v_mfma_f32_16x16x32_bf16 v[188:191], a[164:167], v[152:155], v[188:191]\n"
      "v_mul_f32 v249, v173, v249\n"
      "v_cvt_pk_bf16_f32 v150, v248, v249\n"
      "v_mfma_f32_16x16x32_bf16 v[188:191], a[200:203], v[164:167], v[188:191]\n"
      "v_mul_f32 v250, v173, v250\n"
      "v_mul_f32 v251, v173, v251\n"
      "v_mfma_f32_16x16x32_bf16 v[160:163], v[152:155], a[168:171], v[160:163]\n"
      "v_cvt_pk_bf16_f32 v151, v250, v251\n"
      "v_mul_f32 v140, v173, v140\n"
      "v_mfma_f32_16x16x32_bf16 v[160:163], v[164:167], a[204:207], v[160:163]\n"
      "ds_write_b128 v126, v[148:151] offset:20480\n"
      "v_mfma_f32_16x16x32_bf16 v[220:223], v[152:155], a[172:175], v[220:223]\n"
      "v_mul_f32_e64 v141, v173, v141\n"
      "v_cvt_pk_bf16_f32 v140, v140, v141\n"
      "v_mfma_f32_16x16x32_bf16 v[220:223], v[164:167], a[208:211], v[220:223]\n"
      "s_mov_b32 exec_hi, 0\n"
      "global_load_lds_dwordx4 v118, s[52:53]\n"
      "s_mov_b32 exec_hi, -1\n"
      "v_mfma_f32_16x16x32_bf16 v[156:159], v[152:155], a[176:179], v[156:159]\n"
      "s_add_u32 m0, s76, 110592\n"
      "v_mul_f32 v142, v173, v142\n"
      "v_mul_f32_e64 v143, v173, v143\n"
      "v_mfma_f32_16x16x32_bf16 v[156:159], v[164:167], a[212:215], v[156:159]\n"
      "s_waitcnt lgkmcnt(1)\n"
      "v_cvt_pk_bf16_f32 v141, v142, v143\n"
      "v_mul_f32 v196, v172, v196\n"
      "v_mfma_f32_16x16x32_bf16 v[244:247], a[0:3], v[184:187], 0\n"
      "v_mul_f32 v197, v172, v197\n"
      "v_cvt_pk_bf16_f32 v196, v196, v197\n"
      "v_mfma_f32_16x16x32_bf16 v[244:247], a[36:39], v[168:171], v[244:247]\n"
      "ds_write_b64 v127, v[140:141] offset:20480\n"
      "v_mfma_f32_16x16x32_bf16 v[140:143], a[4:7], v[184:187], 0\n"
      "global_load_lds_dwordx4 v119, s[56:57]\n"
      "v_mfma_f32_16x16x32_bf16 v[140:143], a[40:43], v[168:171], v[140:143]\n"
      "s_add_u32 m0, s79, 0\n"
      "v_mul_f32 v198, v172, v198\n"
      "v_mul_f32_e64 v199, v172, v199\n"
      "v_mfma_f32_16x16x32_bf16 v[248:251], a[8:11], v[184:187], 0\n"
      "v_cvt_pk_bf16_f32 v197, v198, v199\n"
      "v_mul_f32 v176, v172, v176\n"
      "v_mfma_f32_16x16x32_bf16 v[248:251], a[44:47], v[168:171], v[248:251]\n"
      "v_mul_f32 v177, v172, v177\n"
      "v_cvt_pk_bf16_f32 v198, v176, v177\n"
      "v_mfma_f32_16x16x32_bf16 v[164:167], a[12:15], v[184:187], 0\n"
      "ds_read_b128 v[136:139], v114 offset:32768\n"
      "v_mul_f32 v178, v172, v178\n"
      "v_mfma_f32_16x16x32_bf16 v[164:167], a[48:51], v[168:171], v[164:167]\n"
      "v_mul_f32 v179, v172, v179\n"
      "v_cvt_pk_bf16_f32 v199, v178, v179\n"
      "v_mfma_f32_16x16x32_bf16 v[152:155], a[16:19], v[184:187], 0\n"
      "ds_read_b128 v[148:151], v115 offset:32768\n"
      "v_mul_f32_e64 v200, v172, v200\n"
      "v_mfma_f32_16x16x32_bf16 v[152:155], a[52:55], v[168:171], v[152:155]\n"
      "ds_write_b128 v126, v[196:199] offset:22528\n"
      "v_mfma_f32_16x16x32_bf16 v[176:179], a[20:23], v[184:187], 0\n"
      "v_mul_f32_e64 v201, v172, v201\n"
      "v_cvt_pk_bf16_f32 v200, v200, v201\n"
      "v_mfma_f32_16x16x32_bf16 v[176:179], a[56:59], v[168:171], v[176:179]\n"
      "s_mov_b64 exec, 0xfff\n"
      "global_load_lds_dwordx4 v122, s[58:59]\n"
      "s_mov_b64 exec, -1\n"
      "v_mfma_f32_16x16x32_bf16 v[236:239], v[184:187], a[24:27], 0\n"
      "s_sub_u32 s28, s27, 3\n"
      "v_mul_f32 v202, v172, v202\n"
      "v_mul_f32 v203, v172, v203\n"
      "v_mfma_f32_16x16x32_bf16 v[236:239], v[168:171], a[60:63], v[236:239]\n"
      "s_cselect_b64 s[54:55], s[12:13], s[22:23]\n"
      "v_cvt_pk_bf16_f32 v201, v202, v203\n"
      "v_fma_f32 v202, v232, v232, s38\n"
      "v_mfma_f32_16x16x32_bf16 v[172:175], v[184:187], a[28:31], 0\n"
      "s_max_i32 m0, s28, 0\n"
      "v_fma_f32 v203, v228, v228, s38\n"
      "v_fmac_f32_e64 v202, v233, v233\n"
      "v_mfma_f32_16x16x32_bf16 v[172:175], v[168:171], a[64:67], v[172:175]\n"
      "ds_write_b64 v127, v[200:201] offset:22528\n"
      "v_mfma_f32_16x16x32_bf16 v[196:199], v[184:187], a[32:35], 0\n"
      "s_waitcnt lgkmcnt(1)\n"
      "v_fmac_f32 v203, v229, v229\n"
      "v_fmac_f32 v202, v234, v234\n"
      "v_mfma_f32_16x16x32_bf16 v[196:199], v[168:171], a[68:71], v[196:199]\n"
      "s_movrels_b32 s9, s80\n"
      "v_fmac_f32 v203, v230, v230\n"
      "v_fmac_f32 v202, v235, v235\n"
      "v_mfma_f32_16x16x32_bf16 v[244:247], a[72:75], v[212:215], v[244:247]\n"
      "s_lshr_b32 s8, s9, 8\n"
      "v_fmac_f32 v203, v231, v231\n"
      "v_fmac_f32_e64 v202, v192, v192\n"
      "v_mfma_f32_16x16x32_bf16 v[244:247], a[108:111], v[180:183], v[244:247]\n"
      "s_and_b32 s9, s9, 0xff\n"
      "v_fmac_f32 v203, v144, v144\n"
      "v_fmac_f32 v202, v193, v193\n"
      "v_mfma_f32_16x16x32_bf16 v[140:143], a[76:79], v[212:215], v[140:143]\n"
      "s_mul_i32 s28, s8, 1373568\n"
      "v_fmac_f32 v203, v145, v145\n"
      "v_fmac_f32 v202, v194, v194\n"
      "v_mfma_f32_16x16x32_bf16 v[140:143], a[112:115], v[180:183], v[140:143]\n"
      "s_mul_i32 s29, s9, 2688\n"
      "v_fmac_f32 v203, v146, v146\n"
      "v_fmac_f32 v202, v195, v195\n"
      "v_mfma_f32_16x16x32_bf16 v[248:251], a[80:83], v[212:215], v[248:251]\n"
      "s_add_u32 s28, s28, s29\n"
      "v_fmac_f32 v203, v147, v147\n"
      "v_fmac_f32_e64 v202, v240, v240\n"
      "v_mfma_f32_16x16x32_bf16 v[248:251], a[116:119], v[180:183], v[248:251]\n"
      "s_cmp_eq_u32 s8, 72\n"
      "v_fmac_f32 v203, v188, v188\n"
      "v_fmac_f32 v202, v241, v241\n"
      "v_mfma_f32_16x16x32_bf16 v[164:167], a[84:87], v[212:215], v[164:167]\n"
      "s_cselect_b32 s8, 0, 100270464\n"
      "v_fmac_f32 v203, v189, v189\n"
      "v_fmac_f32 v202, v242, v242\n"
      "v_mfma_f32_16x16x32_bf16 v[164:167], a[120:123], v[180:183], v[164:167]\n"
      "ds_read_b128 v[184:187], v114 offset:22528\n"
      "v_fmac_f32 v203, v190, v190\n"
      "v_mfma_f32_16x16x32_bf16 v[152:155], a[88:91], v[212:215], v[152:155]\n"
      "ds_read_b128 v[168:171], v115 offset:22528\n"
      "v_fmac_f32 v202, v243, v243\n"
      "v_mfma_f32_16x16x32_bf16 v[152:155], a[124:127], v[180:183], v[152:155]\n"
      "s_cmp_eq_u32 s9, 72\n"
      "v_fmac_f32 v203, v191, v191\n"
      "v_mfma_f32_16x16x32_bf16 v[176:179], a[92:95], v[212:215], v[176:179]\n"
      "s_cselect_b32 s9, 0, 196224\n"
      "v_permlane16_swap_b32 v202, v203\n"
      "v_mfma_f32_16x16x32_bf16 v[176:179], a[128:131], v[180:183], v[176:179]\n"
      "s_add_u32 s29, s8, s9\n"
      "v_add_f32 v202, v202, v203\n"
      "v_mov_b32 v203, v202\n"
      "v_mfma_f32_16x16x32_bf16 v[236:239], v[212:215], a[96:99], v[236:239]\n"
      "s_add_u32 s28, s28, s29\n"
      "v_permlane32_swap_b32 v202, v203\n"
      "v_mfma_f32_16x16x32_bf16 v[236:239], v[180:183], a[132:135], v[236:239]\n"
      "s_add_u32 s54, s54, s28\n"
      "v_add_f32 v202, v202, v203\n"
      "v_rsq_f32 v202, v202\n"
      "v_mfma_f32_16x16x32_bf16 v[172:175], v[212:215], a[100:103], v[172:175]\n"
      "s_addc_u32 s55, s55, 0\n"
      "v_mov_b32 v203, v202\n"
      "s_cmp_lg_u32 s29, s11\n"
      "v_mfma_f32_16x16x32_bf16 v[172:175], v[180:183], a[136:139], v[172:175]\n"
      "s_cbranch_scc1 L_edge_17_%=\n"
      "L_eb_18_%=:\n"
      "v_mfma_f32_16x16x32_bf16 v[196:199], v[212:215], a[104:107], v[196:199]\n"
      "s_mov_b32 s5, s4\n"
      "v_permlane16_swap_b32 v202, v203\n"
      "v_mfma_f32_16x16x32_bf16 v[196:199], v[180:183], a[140:143], v[196:199]\n"
      "s_add_u32 s4, s27, 1\n"
      "v_mul_f32_e64 v228, v203, v228\n"
      "s_waitcnt lgkmcnt(0)\n"
      "v_mfma_f32_16x16x32_bf16 v[244:247], a[144:147], v[136:139], v[244:247]\n"
      "s_lshl_b32 s4, s4, 30\n"
      "v_mul_f32 v229, v203, v229\n"
      "v_cvt_pk_bf16_f32 v228, v228, v229\n"
      "v_mfma_f32_16x16x32_bf16 v[244:247], a[180:183], v[148:151], v[244:247]\n"
      "ds_read_b128 v[212:215], v119\n"
      "v_mul_f32 v230, v203, v230\n"
      "v_mfma_f32_16x16x32_bf16 v[140:143], a[148:151], v[136:139], v[140:143]\n"
      "s_and_b32 s4, s4, 0x80000000\n"
      "v_mul_f32 v231, v203, v231\n"
      "v_cvt_pk_bf16_f32 v229, v230, v231\n"
      "v_mfma_f32_16x16x32_bf16 v[140:143], a[184:187], v[148:151], v[140:143]\n"
      "v_mul_f32 v144, v203, v144\n"
      "v_mul_f32 v145, v203, v145\n"
      "v_mfma_f32_16x16x32_bf16 v[248:251], a[152:155], v[136:139], v[248:251]\n"
      "v_cvt_pk_bf16_f32 v230, v144, v145\n"
      "v_mul_f32 v146, v203, v146\n"
      "v_mfma_f32_16x16x32_bf16 v[248:251], a[188:191], v[148:151], v[248:251]\n"
      "v_mul_f32 v147, v203, v147\n"
      "v_cvt_pk_bf16_f32 v231, v146, v147\n"
      "v_mfma_f32_16x16x32_bf16 v[164:167], a[156:159], v[136:139], v[164:167]\n"
      "v_mul_f32 v188, v203, v188\n"
      "v_mul_f32 v189, v203, v189\n"
      "v_mfma_f32_16x16x32_bf16 v[164:167], a[192:195], v[148:151], v[164:167]\n"
      "v_cvt_pk_bf16_f32 v34, v188, v189\n"
      "v_mul_f32 v190, v203, v190\n"
      "v_mfma_f32_16x16x32_bf16 v[152:155], a[160:163], v[136:139], v[152:155]\n"
      "v_mul_f32 v191, v203, v191\n"
      "v_cvt_pk_bf16_f32 v35, v190, v191\n"
      "v_mfma_f32_16x16x32_bf16 v[152:155], a[196:199], v[148:151], v[152:155]\n"
      "ds_read_b128 v[180:183], v114 offset:28672\n"
      "v_mul_f32 v202, s36, v202\n"
      "v_mfma_f32_16x16x32_bf16 v[176:179], a[164:167], v[136:139], v[176:179]\n"
      "v_mul_f32 v232, v202, v232\n"
      "v_mul_f32_e64 v233, v202, v233\n"
      "v_mfma_f32_16x16x32_bf16 v[176:179], a[200:203], v[148:151], v[176:179]\n"
      "s_waitcnt lgkmcnt(1)\n"
      "v_cvt_pk_bf16_f32 v232, v232, v233\n"
      "v_mul_f32 v234, v202, v234\n"
      "v_mfma_f32_16x16x32_bf16 v[236:239], v[136:139], a[168:171], v[236:239]\n"
      "global_store_dwordx4 v116, v[212:215], s[54:55] sc1\n"
      "v_mfma_f32_16x16x32_bf16 v[236:239], v[148:151], a[204:207], v[236:239]\n"
      "ds_read_b128 v[252:255], v115 offset:28672\n"
      "v_mul_f32_e64 v235, v202, v235\n"
      "v_mfma_f32_16x16x32_bf16 v[172:175], v[136:139], a[172:175], v[172:175]\n"
      "v_cvt_pk_bf16_f32 v233, v234, v235\n"
      "v_mul_f32 v192, v202, v192\n"
      "v_mfma_f32_16x16x32_bf16 v[172:175], v[148:151], a[208:211], v[172:175]\n"
      "v_mul_f32 v193, v202, v193\n"
      "v_cvt_pk_bf16_f32 v234, v192, v193\n"
      "v_mfma_f32_16x16x32_bf16 v[196:199], v[136:139], a[176:179], v[196:199]\n"
      "v_mul_f32 v194, v202, v194\n"
      "v_mul_f32 v195, v202, v195\n"
      "v_mfma_f32_16x16x32_bf16 v[196:199], v[148:151], a[212:215], v[196:199]\n"
      "v_cvt_pk_bf16_f32 v235, v194, v195\n"
      "v_mul_f32 v240, v202, v240\n"
      "v_mfma_f32_16x16x32_bf16 v[212:215], a[0:3], v[184:187], 0\n"
      "v_mul_f32 v241, v202, v241\n"
      "v_cvt_pk_bf16_f32 v32, v240, v241\n"
      "v_mfma_f32_16x16x32_bf16 v[212:215], a[36:39], v[168:171], v[212:215]\n"
      "v_mul_f32 v242, v202, v242\n"
      "v_mul_f32_e64 v243, v202, v243\n"
      "v_mfma_f32_16x16x32_bf16 v[200:203], a[4:7], v[184:187], 0\n"
      "v_cvt_pk_bf16_f32 v33, v242, v243\n"
      "v_cvt_pk_bf16_f32 v160, v160, v161\n"
      "v_mfma_f32_16x16x32_bf16 v[200:203], a[40:43], v[168:171], v[200:203]\n"
      "v_cvt_pk_bf16_f32 v161, v162, v163\n"
      "v_cvt_pk_bf16_f32 v162, v236, v237\n"
      "v_mfma_f32_16x16x32_bf16 v[148:151], a[8:11], v[184:187], 0\n"
      "v_cvt_pk_bf16_f32 v163, v238, v239\n"
      "s_waitcnt lgkmcnt(0)\n"
      "v_mfma_f32_16x16x32_bf16 v[148:151], a[44:47], v[168:171], v[148:151]\n"
      "v_cvt_pk_bf16_f32 v220, v220, v221\n"
      "v_cvt_pk_bf16_f32 v221, v222, v223\n"
      "v_mfma_f32_16x16x32_bf16 v[136:139], a[12:15], v[184:187], 0\n"
      "ds_read_b128 v[192:195], v119 offset:6144\n"
      "v_cvt_pk_bf16_f32 v222, v172, v173\n"
      "v_mfma_f32_16x16x32_bf16 v[136:139], a[48:51], v[168:171], v[136:139]\n"
      "v_cvt_pk_bf16_f32 v223, v174, v175\n"
      "v_cvt_pk_bf16_f32 v156, v156, v157\n"
      "v_mfma_f32_16x16x32_bf16 v[172:175], a[16:19], v[184:187], 0\n"
      "v_cvt_pk_bf16_f32 v157, v158, v159\n"
      "v_cvt_pk_bf16_f32 v158, v196, v197\n"
      "v_mfma_f32_16x16x32_bf16 v[172:175], a[52:55], v[168:171], v[172:175]\n"
      "v_cvt_pk_bf16_f32 v159, v198, v199\n"
      "v_fma_f32 v196, v244, v244, s38\n"
      "v_mfma_f32_16x16x32_bf16 v[240:243], a[20:23], v[184:187], 0\n"
      "v_fma_f32 v199, v164, v164, s38\n"
      "v_fmac_f32 v196, v245, v245\n"
      "v_mfma_f32_16x16x32_bf16 v[240:243], a[56:59], v[168:171], v[240:243]\n"
      "v_fmac_f32 v199, v165, v165\n"
      "v_fmac_f32 v196, v246, v246\n"
      "v_mfma_f32_16x16x32_bf16 v[216:219], v[184:187], a[24:27], 0\n"
      "v_fmac_f32 v199, v166, v166\n"
      "v_fmac_f32 v196, v247, v247\n"
      "v_mfma_f32_16x16x32_bf16 v[216:219], v[168:171], a[60:63], v[216:219]\n"
      "v_fmac_f32 v199, v167, v167\n"
      "v_fmac_f32 v196, v140, v140\n"
      "v_mfma_f32_16x16x32_bf16 v[144:147], v[184:187], a[28:31], 0\n"
      "v_fmac_f32 v199, v152, v152\n"
      "v_fmac_f32 v196, v141, v141\n"
      "v_mfma_f32_16x16x32_bf16 v[144:147], v[168:171], a[64:67], v[144:147]\n"
      "v_fmac_f32 v199, v153, v153\n"
      "v_fmac_f32_e64 v196, v142, v142\n"
      "v_mfma_f32_16x16x32_bf16 v[224:227], v[184:187], a[32:35], 0\n"
      "ds_read_b128 v[184:187], v114 offset:34816\n"
      "v_fmac_f32 v199, v154, v154\n"
      "v_mfma_f32_16x16x32_bf16 v[224:227], v[168:171], a[68:71], v[224:227]\n"
      "ds_read_b128 v[168:171], v115 offset:34816\n"
      "v_fmac_f32 v196, v143, v143\n"
      "v_mfma_f32_16x16x32_bf16 v[212:215], a[72:75], v[180:183], v[212:215]\n"
      "s_waitcnt lgkmcnt(2)\n"
      "v_fmac_f32 v199, v155, v155\n"
      "v_fmac_f32_e64 v196, v248, v248\n"
      "v_mfma_f32_16x16x32_bf16 v[212:215], a[108:111], v[252:255], v[212:215]\n"
      "global_store_dwordx4 v116, v[192:195], s[54:55] offset:128 sc1\n"
      "v_mfma_f32_16x16x32_bf16 v[200:203], a[76:79], v[180:183], v[200:203]\n"
      "v_fmac_f32 v199, v176, v176\n"
      "v_fmac_f32 v196, v249, v249\n"
      "v_mfma_f32_16x16x32_bf16 v[200:203], a[112:115], v[252:255], v[200:203]\n"
      "v_fmac_f32 v199, v177, v177\n"
      "v_fmac_f32 v196, v250, v250\n"
      "v_mfma_f32_16x16x32_bf16 v[148:151], a[80:83], v[180:183], v[148:151]\n"
      "v_fmac_f32 v199, v178, v178\n"
      "v_fmac_f32 v196, v251, v251\n"
      "v_mfma_f32_16x16x32_bf16 v[148:151], a[116:119], v[252:255], v[148:151]\n"
      "v_fmac_f32 v199, v179, v179\n"
      "v_mfma_f32_16x16x32_bf16 v[136:139], a[84:87], v[180:183], v[136:139]\n"
      "s_nop 0\n"
      "v_permlane16_swap_b32 v196, v199\n"
      "v_mfma_f32_16x16x32_bf16 v[136:139], a[120:123], v[252:255], v[136:139]\n"
      "v_add_f32 v196, v196, v199\n"
      "v_mov_b32 v199, v196\n"
      "v_mfma_f32_16x16x32_bf16 v[172:175], a[88:91], v[180:183], v[172:175]\n"
      "s_nop 0\n"
      "v_permlane32_swap_b32 v196, v199\n"
      "v_mfma_f32_16x16x32_bf16 v[172:175], a[124:127], v[252:255], v[172:175]\n"
      "s_waitcnt lgkmcnt(0)\n"
      "v_add_f32 v196, v196, v199\n"
      "v_rsq_f32 v196, v196\n"
      "v_mfma_f32_16x16x32_bf16 v[240:243], a[92:95], v[180:183], v[240:243]\n"
      "v_mov_b32 v199, v196\n"
      "v_mfma_f32_16x16x32_bf16 v[240:243], a[128:131], v[252:255], v[240:243]\n"
      "s_nop 0\n"
      "v_permlane16_swap_b32 v196, v199\n"
      "v_mfma_f32_16x16x32_bf16 v[216:219], v[180:183], a[96:99], v[216:219]\n"
      "v_mul_f32 v164, v199, v164\n"
      "v_mul_f32_e64 v165, v199, v165\n"
      "v_mfma_f32_16x16x32_bf16 v[216:219], v[252:255], a[132:135], v[216:219]\n"
      "ds_read_b128 v[192:195], v132 offset:1200\n"
      "v_cvt_pk_bf16_f32 v164, v164, v165\n"
      "v_mfma_f32_16x16x32_bf16 v[144:147], v[180:183], a[100:103], v[144:147]\n"
      "ds_read_b64 v[188:189], v132 offset:1192\n"
      "v_mul_f32 v166, v199, v166\n"
      "v_mfma_f32_16x16x32_bf16 v[144:147], v[252:255], a[136:139], v[144:147]\n"
      "v_mul_f32 v167, v199, v167\n"
      "v_cvt_pk_bf16_f32 v165, v166, v167\n"
      "v_mfma_f32_16x16x32_bf16 v[224:227], v[180:183], a[104:107], v[224:227]\n"
      "ds_read_b128 v[180:183], v133 offset:1168\n"
      "v_mul_f32 v152, v199, v152\n"
      "v_mfma_f32_16x16x32_bf16 v[224:227], v[252:255], a[140:143], v[224:227]\n"
      "ds_read_b64 v[190:191], v133 offset:1184\n"
      "v_mul_f32 v153, v199, v153\n"
      "v_mfma_f32_16x16x32_bf16 v[212:215], a[144:147], v[184:187], v[212:215]\n"
      "ds_read_u16 v52, v134 offset:1168\n"
      "v_cvt_pk_bf16_f32 v166, v152, v153\n"
      "v_mfma_f32_16x16x32_bf16 v[212:215], a[180:183], v[168:171], v[212:215]\n"
      "ds_read_u16 v56, v134 offset:1200\n"
      "v_mul_f32 v154, v199, v154\n"
      "v_mfma_f32_16x16x32_bf16 v[200:203], a[148:151], v[184:187], v[200:203]\n"
      "ds_read_u16 v60, v134 offset:1232\n"
      "v_mul_f32 v155, v199, v155\n"
      "v_mfma_f32_16x16x32_bf16 v[200:203], a[184:187], v[168:171], v[200:203]\n"
      "ds_read_b128 v[236:239], v123\n"
      "v_cvt_pk_bf16_f32 v167, v154, v155\n"
      "v_mfma_f32_16x16x32_bf16 v[148:151], a[152:155], v[184:187], v[148:151]\n"
      "v_mul_f32 v176, v199, v176\n"
      "v_mul_f32 v177, v199, v177\n"
      "v_mfma_f32_16x16x32_bf16 v[148:151], a[188:191], v[168:171], v[148:151]\n"
      "v_cvt_pk_bf16_f32 v40, v176, v177\n"
      "v_mul_f32_e64 v178, v199, v178\n"
      "v_mfma_f32_16x16x32_bf16 v[136:139], a[156:159], v[184:187], v[136:139]\n"
      "ds_read_b128 v[252:255], v123 offset:3072\n"
      "v_mul_f32_e64 v179, v199, v179\n"
      "v_mfma_f32_16x16x32_bf16 v[136:139], a[192:195], v[168:171], v[136:139]\n"
      "v_cvt_pk_bf16_f32 v41, v178, v179\n"
      "v_mul_f32 v196, s36, v196\n"
      "v_mfma_f32_16x16x32_bf16 v[172:175], a[160:163], v[184:187], v[172:175]\n"
      "v_mul_f32 v244, v196, v244\n"
      "v_mul_f32_e64 v245, v196, v245\n"
      "v_mfma_f32_16x16x32_bf16 v[172:175], a[196:199], v[168:171], v[172:175]\n"
      "v_cvt_pk_bf16_f32 v244, v244, v245\n"
      "v_mul_f32 v246, v196, v246\n"
      "v_mfma_f32_16x16x32_bf16 v[240:243], a[164:167], v[184:187], v[240:243]\n"
      "v_mul_f32 v247, v196, v247\n"
      "v_cvt_pk_bf16_f32 v245, v246, v247\n"
      "v_mfma_f32_16x16x32_bf16 v[240:243], a[200:203], v[168:171], v[240:243]\n"
      "v_mul_f32 v140, v196, v140\n"
      "v_mul_f32 v141, v196, v141\n"
      "v_mfma_f32_16x16x32_bf16 v[216:219], v[184:187], a[168:171], v[216:219]\n"
      "v_cvt_pk_bf16_f32 v246, v140, v141\n"
      "v_mul_f32 v142, v196, v142\n"
      "v_mfma_f32_16x16x32_bf16 v[216:219], v[168:171], a[204:207], v[216:219]\n"
      "ds_read_b128 v[176:179], v123 offset:6144\n"
      "v_mul_f32 v143, v196, v143\n"
      "v_mfma_f32_16x16x32_bf16 v[144:147], v[184:187], a[172:175], v[144:147]\n"
      "v_cvt_pk_bf16_f32 v247, v142, v143\n"
      "v_mul_f32_e64 v248, v196, v248\n"
      "v_mfma_f32_16x16x32_bf16 v[144:147], v[168:171], a[208:211], v[144:147]\n"
      "v_mul_f32 v249, v196, v249\n"
      "s_waitcnt lgkmcnt(1)\n"
      "v_mfma_f32_16x16x32_bf16 v[224:227], v[184:187], a[176:179], v[224:227]\n"
      "ds_read_b128 v[184:187], v123 offset:9216\n"
      "v_cvt_pk_bf16_f32 v38, v248, v249\n"
      "v_mfma_f32_16x16x32_bf16 v[224:227], v[168:171], a[212:215], v[224:227]\n"
      "v_mul_f32 v250, v196, v250\n"
      "v_mul_f32 v251, v196, v251\n"
      "v_mfma_f32_16x16x32_bf16 v[140:143], a[216:219], v[236:239], 0\n"
      "v_cvt_pk_bf16_f32 v39, v250, v251\n"
      "v_fma_f32 v248, v212, v212, s38\n"
      "v_mfma_f32_16x16x32_bf16 v[140:143], a[228:231], v[252:255], v[140:143]\n"
      "v_fma_f32 v250, v136, v136, s38\n"
      "v_fmac_f32_e64 v248, v213, v213\n"
      "v_mfma_f32_16x16x32_bf16 v[196:199], a[220:223], v[236:239], 0\n"
      "v_fmac_f32 v250, v137, v137\n"
      "v_fmac_f32 v248, v214, v214\n"
      "v_mfma_f32_16x16x32_bf16 v[196:199], a[232:235], v[252:255], v[196:199]\n"
      "v_fmac_f32 v250, v138, v138\n"
      "v_fmac_f32 v248, v215, v215\n"
      "v_mfma_f32_16x16x32_bf16 v[168:171], a[224:227], v[236:239], 0\n"
      "v_fmac_f32 v250, v139, v139\n"
      "v_fmac_f32 v248, v200, v200\n"
      "v_mfma_f32_16x16x32_bf16 v[168:171], a[236:239], v[252:255], v[168:171]\n"
      "ds_read_b128 v[236:239], v123 offset:12288\n"
      "v_fmac_f32 v250, v172, v172\n"
      "v_fmac_f32 v248, v201, v201\n"
      "v_fmac_f32 v250, v173, v173\n"
      "v_fmac_f32 v248, v202, v202\n"
      "v_fmac_f32 v250, v174, v174\n"
      "v_fmac_f32 v248, v203, v203\n"
      "v_fmac_f32 v250, v175, v175\n"
      "v_fmac_f32 v248, v148, v148\n"
      "s_waitcnt lgkmcnt(1)\n"
      "v_mfma_f32_16x16x32_bf16 v[140:143], a[240:243], v[176:179], v[140:143]\n"
      "v_fmac_f32 v250, v240, v240\n"
      "v_fmac_f32_e64 v248, v149, v149\n"
      "v_mfma_f32_16x16x32_bf16 v[140:143], a[252:255], v[184:187], v[140:143]\n"
      "ds_read_b128 v[252:255], v123 offset:15360\n"
      "v_fmac_f32 v250, v241, v241\n"
      "v_mfma_f32_16x16x32_bf16 v[196:199], a[244:247], v[176:179], v[196:199]\n"
      "v_fmac_f32 v248, v150, v150\n"
      "v_fmac_f32_e64 v250, v242, v242\n"
      "v_mfma_f32_16x16x32_bf16 v[196:199], v[0:3], v[184:187], v[196:199]\n"
      "v_fmac_f32 v248, v151, v151\n"
      "v_fmac_f32 v250, v243, v243\n"
      "v_mfma_f32_16x16x32_bf16 v[168:171], a[248:251], v[176:179], v[168:171]\n"
      "s_nop 0\n"
      "v_permlane16_swap_b32 v248, v250\n"
      "v_mfma_f32_16x16x32_bf16 v[168:171], v[4:7], v[184:187], v[168:171]\n"
      "v_add_f32 v248, v248, v250\n"
      "v_mov_b32 v250, v248\n"
      "v_mfma_f32_16x16x32_bf16 v[176:179], v[228:231], v[232:235], v[78:81]\n"
      "s_nop 0\n"
      "v_permlane32_swap_b32 v248, v250\n"
      "v_mfma_f32_16x16x32_bf16 v[176:179], v[34:37], v[32:35], v[176:179]\n"
      "v_add_f32 v248, v248, v250\n"
      "v_rsq_f32 v248, v248\n"
      "v_add_u32 v132, 2336, v132\n"
      "v_mov_b32_e64 v250, v248\n"
      "v_add_u32 v133, 2336, v133\n"
      "v_add_u32 v134, 2336, v134\n"
      "v_permlane16_swap_b32 v248, v250\n"
      "v_mul_f32 v136, v250, v136\n"
      "v_mul_f32_e64 v137, v250, v137\n"
      "v_cvt_pk_bf16_f32 v136, v136, v137\n"
      "v_mul_f32 v138, v250, v138\n"
      "s_waitcnt lgkmcnt(0)\n"
      "v_mfma_f32_16x16x32_bf16 v[140:143], v[8:11], v[236:239], v[140:143]\n"
      "v_mul_f32_e64 v139, v250, v139\n"
      "v_cvt_pk_bf16_f32 v137, v138, v139\n"
      "v_mfma_f32_16x16x32_bf16 v[140:143], v[20:23], v[252:255], v[140:143]\n"
      "v_mul_f32 v172, v250, v172\n"
      "v_mul_f32 v173, v250, v173\n"
      "v_mfma_f32_16x16x32_bf16 v[196:199], v[12:15], v[236:239], v[196:199]\n"
      "v_cvt_pk_bf16_f32 v138, v172, v173\n"
      "v_mul_f32 v174, v250, v174\n"
      "v_mfma_f32_16x16x32_bf16 v[196:199], v[24:27], v[252:255], v[196:199]\n"
      "v_mul_f32 v175, v250, v175\n"
      "v_cvt_pk_bf16_f32 v139, v174, v175\n"
      "v_mfma_f32_16x16x32_bf16 v[168:171], v[16:19], v[236:239], v[168:171]\n"
      "v_mul_f32 v240, v250, v240\n"
      "v_mul_f32 v241, v250, v241\n"
      "v_mfma_f32_16x16x32_bf16 v[168:171], v[28:31], v[252:255], v[168:171]\n"
      "v_cvt_pk_bf16_f32 v46, v240, v241\n"
      "v_mul_f32 v242, v250, v242\n"
      "v_mfma_f32_16x16x32_bf16 v[184:187], v[164:167], v[232:235], v[82:85]\n"
      "v_mul_f32 v243, v250, v243\n"
      "v_cvt_pk_bf16_f32 v47, v242, v243\n"
      "v_mfma_f32_16x16x32_bf16 v[184:187], v[40:43], v[32:35], v[184:187]\n"
      "v_mul_f32 v248, s36, v248\n"
      "v_mul_f32 v212, v248, v212\n"
      "v_mfma_f32_16x16x32_bf16 v[172:175], v[192:195], v[232:235], v[70:73]\n"
      "v_mul_f32_e64 v213, v248, v213\n"
      "v_cvt_pk_bf16_f32 v212, v212, v213\n"
      "v_mfma_f32_16x16x32_bf16 v[172:175], v[180:183], v[228:231], v[172:175]\n"
      "s_waitcnt vmcnt(2)\n"
      "v_mul_f32 v214, v248, v214\n"
      "v_mul_f32_e64 v215, v248, v215\n"
      "v_mfma_f32_16x16x32_bf16 v[172:175], v[188:191], v[32:35], v[172:175]\n"
      "ds_read_b128 v[250:253], v119 offset:36864\n"
      "v_cvt_pk_bf16_f32 v213, v214, v215\n"
      "v_mfma_f32_16x16x32_bf16 v[236:239], v[136:139], v[232:235], v[86:89]\n"
      "ds_read_b128 v[232:235], v122\n"
      "v_mul_f32 v200, v248, v200\n"
      "v_mfma_f32_16x16x32_bf16 v[236:239], v[46:49], v[32:35], v[236:239]\n"
      "v_mul_f32 v201, v248, v201\n"
      "v_cvt_pk_bf16_f32 v214, v200, v201\n"
      "v_mul_f32 v202, v248, v202\n"
      "v_mul_f32 v203, v248, v203\n"
      "v_cvt_pk_bf16_f32 v215, v202, v203\n"
      "v_mul_f32 v148, v248, v148\n"
      "v_mul_f32 v149, v248, v149\n"
      "v_cvt_pk_bf16_f32 v44, v148, v149\n"
      "v_mul_f32 v150, v248, v150\n"
      "v_mul_f32 v151, v248, v151\n"
      "v_cvt_pk_bf16_f32 v45, v150, v151\n"
      "v_cvt_pk_bf16_f32 v50, v216, v217\n"
      "v_cvt_pk_bf16_f32 v51, v218, v219\n"
      "v_cvt_pk_bf16_f32 v54, v144, v145\n"
      "v_cvt_pk_bf16_f32 v55, v146, v147\n"
      "v_cvt_pk_bf16_f32 v58, v224, v225\n"
      "v_cvt_pk_bf16_f32 v59, v226, v227\n"
      "v_fma_f32 v248, v140, v140, s39\n"
      "v_fmac_f32 v248, v141, v141\n"
      "v_fmac_f32 v248, v142, v142\n"
      "v_fmac_f32 v248, v143, v143\n"
      "v_fmac_f32 v248, v196, v196\n"
      "v_fmac_f32 v248, v197, v197\n"
      "v_fmac_f32 v248, v198, v198\n"
      "v_fmac_f32 v248, v199, v199\n"
      "v_fmac_f32 v248, v168, v168\n"
      "v_fmac_f32 v248, v169, v169\n"
      "v_fmac_f32 v248, v170, v170\n"
      "v_fmac_f32 v248, v171, v171\n"
      "s_waitcnt lgkmcnt(0)\n"
      "v_or3_b32 v250, v250, v251, v252\n"
      "s_mov_b64 exec, 0xfff\n"
      "v_or3_b32 v250, v250, v232, v233\n"
      "v_or3_b32 v250, v250, v234, v235\n"
      "s_mov_b64 exec, -1\n"
      "v_bitop3_b32 v250, v250, v253, s40 bitop3:0xa8\n"
      "v_or_b32 v250, s4, v250\n"
      "ds_write_b32 v129, v250 offset:2048\n"
      "s_or_b64 s[34:35], s[48:49], s[50:51]\n"
      "s_cbranch_scc1 L_gen_19_%=\n"
      "L_fast_22_%=:\n"
      "v_mfma_f32_16x16x32_bf16 v[232:235], v[228:231], v[244:247], v[90:93]\n"
      "ds_read_b128 v[216:219], v123 offset:256\n"
      "v_mfma_f32_16x16x32_bf16 v[232:235], v[34:37], v[38:41], v[232:235]\n"
      "ds_read_b128 v[224:227], v123 offset:3328\n"
      "v_exp_f32_e64 v176, v176\n"
      "v_mfma_f32_16x16x32_bf16 v[250:253], v[164:167], v[244:247], v[94:97]\n"
      "ds_read_b128 v[200:203], v119 offset:12288\n"
      "v_mfma_f32_16x16x32_bf16 v[250:253], v[40:43], v[38:41], v[250:253]\n"
      "ds_read_b128 v[148:151], v128 offset:2048\n"
      "v_exp_f32_e64 v177, v177\n"
      "v_mfma_f32_16x16x32_bf16 v[240:243], v[192:195], v[244:247], v[72:75]\n"
      "v_exp_f32 v178, v178\n"
      "v_mfma_f32_16x16x32_bf16 v[240:243], v[180:183], v[164:167], v[240:243]\n"
      "v_cvt_pk_bf16_f32 v176, v176, v177\n"
      "v_exp_f32 v179, v179\n"
      "v_mfma_f32_16x16x32_bf16 v[240:243], v[188:191], v[38:41], v[240:243]\n"
      "v_exp_f32_e64 v184, v184\n"
      "v_mfma_f32_16x16x32_bf16 v[144:147], v[136:139], v[244:247], v[98:101]\n"
      "v_cvt_pk_bf16_f32 v177, v178, v179\n"
      "v_mfma_f32_16x16x32_bf16 v[144:147], v[46:49], v[38:41], v[144:147]\n"
      "v_exp_f32 v185, v185\n"
      "v_exp_f32 v186, v186\n"
      "v_cvt_pk_bf16_f32 v178, v184, v185\n"
      "v_exp_f32 v187, v187\n"
      "s_waitcnt lgkmcnt(1)\n"
      "v_mfma_f32_16x16x32_bf16 v[244:247], a[216:219], v[216:219], 0\n"
      "ds_read_b128 v[208:211], v123 offset:6400\n"
      "v_cvt_pk_bf16_f32 v179, v186, v187\n"
      "v_mfma_f32_16x16x32_bf16 v[244:247], a[228:231], v[224:227], v[244:247]\n"
      "ds_read_b128 v[184:187], v123 offset:9472\n"
      "v_exp_f32 v236, v236\n"
      "v_mfma_f32_16x16x32_bf16 v[152:155], a[220:223], v[216:219], 0\n"
      "v_exp_f32 v237, v237\n"
      "v_mfma_f32_16x16x32_bf16 v[152:155], a[232:235], v[224:227], v[152:155]\n"
      "global_store_dwordx4 v116, v[200:203], s[54:55] offset:256 sc1\n"
      "v_mfma_f32_16x16x32_bf16 v[204:207], a[224:227], v[216:219], 0\n"
      "s_waitcnt lgkmcnt(2)\n"
      "v_exp_f32 v238, v238\n"
      "v_mfma_f32_16x16x32_bf16 v[204:207], a[236:239], v[224:227], v[204:207]\n"
      "v_exp_f32 v239, v239\n"
      "v_exp_f32 v172, v172\n"
      "v_cvt_pk_bf16_f32 v62, v236, v237\n"
      "v_cvt_pk_bf16_f32 v63, v238, v239\n"
      "v_cvt_pk_bf16_f32 v64, v172, 0\n"
      "v_bitop3_b32 v148, v148, v149, s4 bitop3:0x7e\n"
      "v_bitop3_b32 v150, v150, v151, s4 bitop3:0x7e\n"
      "v_or_b32 v148, v148, v150\n"
      "v_cmp_ne_u32 vcc, 0, v148\n"
      "v_exp_f32 v232, v232\n"
      "v_exp_f32 v233, v233\n"
      "v_exp_f32 v234, v234\n"
      "v_cvt_pk_bf16_f32 v232, v232, v233\n"
      "v_exp_f32_e64 v235, v235\n"
      "s_waitcnt lgkmcnt(0)\n"
      "v_mfma_f32_16x16x32_bf16 v[244:247], a[240:243], v[208:211], v[244:247]\n"
      "ds_read_b128 v[236:239], v123 offset:12544\n"
      "v_cvt_pk_bf16_f32 v233, v234, v235\n"
      "v_mfma_f32_16x16x32_bf16 v[244:247], a[252:255], v[184:187], v[244:247]\n"
      "ds_read_b128 v[200:203], v123 offset:15616\n"
      "v_exp_f32 v250, v250\n"
      "v_mfma_f32_16x16x32_bf16 v[152:155], a[244:247], v[208:211], v[152:155]\n"
      "s_mov_b64 s[48:49], vcc\n"
      "v_exp_f32 v251, v251\n"
      "v_mfma_f32_16x16x32_bf16 v[152:155], v[0:3], v[184:187], v[152:155]\n"
      "v_exp_f32 v252, v252\n"
      "v_mfma_f32_16x16x32_bf16 v[204:207], a[248:251], v[208:211], v[204:207]\n"
      "ds_read_b128 v[208:211], v123 offset:512\n"
      "v_cvt_pk_bf16_f32 v234, v250, v251\n"
      "v_mfma_f32_16x16x32_bf16 v[204:207], v[4:7], v[184:187], v[204:207]\n"
      "s_cbranch_vccnz L_flg_23_%=\n"
      "L_fb_24_%=:\n"
      "v_mfma_f32_16x16x32_bf16 v[184:187], v[66:69], v[176:179], 0\n"
      "ds_read_b128 v[224:227], v123 offset:3584\n"
      "v_mfma_f32_16x16x32_bf16 v[184:187], v[66:69], v[62:65], v[184:187]\n"
      "v_exp_f32 v253, v253\n"
      "v_mfma_f32_16x16x32_bf16 v[148:151], v[160:163], v[176:179], 0\n"
      "v_exp_f32 v144, v144\n"
      "v_mfma_f32_16x16x32_bf16 v[148:151], v[50:53], v[62:65], v[148:151]\n"
      "v_cvt_pk_bf16_f32 v235, v252, v253\n"
      "v_exp_f32 v145, v145\n"
      "v_exp_f32 v146, v146\n"
      "s_waitcnt lgkmcnt(2)\n"
      "v_mfma_f32_16x16x32_bf16 v[244:247], v[8:11], v[236:239], v[244:247]\n"
      "v_exp_f32 v147, v147\n"
      "v_mfma_f32_16x16x32_bf16 v[244:247], v[20:23], v[200:203], v[244:247]\n"
      "v_exp_f32 v240, v240\n"
      "v_mfma_f32_16x16x32_bf16 v[152:155], v[12:15], v[236:239], v[152:155]\n"
      "v_rcp_f32 v184, v184\n"
      "v_mfma_f32_16x16x32_bf16 v[152:155], v[24:27], v[200:203], v[152:155]\n"
      "v_mul_f32 v148, v184, v148\n"
      "v_mul_f32_e64 v149, v184, v149\n"
      "v_mfma_f32_16x16x32_bf16 v[204:207], v[16:19], v[236:239], v[204:207]\n"
      "v_cvt_pk_bf16_f32 v148, v148, v149\n"
      "v_mul_f32 v150, v184, v150\n"
      "v_mfma_f32_16x16x32_bf16 v[204:207], v[28:31], v[200:203], v[204:207]\n"
      "v_mul_f32 v151, v184, v151\n"
      "v_cvt_pk_bf16_f32 v149, v150, v151\n"
      "v_mfma_f32_16x16x32_bf16 v[200:203], v[220:223], v[176:179], 0\n"
      "v_mfma_f32_16x16x32_bf16 v[200:203], v[54:57], v[62:65], v[200:203]\n"
      "v_cndmask_b32 v173, v173, v241, s[42:43]\n"
      "v_mfma_f32_16x16x32_bf16 v[250:253], v[156:159], v[176:179], 0\n"
      "ds_read_b128 v[236:239], v120\n"
      "v_mfma_f32_16x16x32_bf16 v[250:253], v[58:61], v[62:65], v[250:253]\n"
      "v_cvt_pk_bf16_f32 v62, v144, v145\n"
      "v_fma_f32 v249, v244, v244, s39\n"
      "v_mfma_f32_16x16x32_bf16 v[216:219], v[228:231], v[212:215], v[102:105]\n"
      "v_fmac_f32 v249, v245, v245\n"
      "v_fmac_f32 v249, v246, v246\n"
      "v_mfma_f32_16x16x32_bf16 v[216:219], v[34:37], v[44:47], v[216:219]\n"
      "v_fmac_f32 v249, v247, v247\n"
      "v_fmac_f32 v249, v152, v152\n"
      "v_mfma_f32_16x16x32_bf16 v[176:179], v[164:167], v[212:215], v[106:109]\n"
      "v_fmac_f32 v249, v153, v153\n"
      "v_fmac_f32 v249, v154, v154\n"
      "v_mfma_f32_16x16x32_bf16 v[176:179], v[40:43], v[44:47], v[176:179]\n"
      "v_fmac_f32 v249, v155, v155\n"
      "v_fmac_f32 v249, v204, v204\n"
      "v_mfma_f32_16x16x32_bf16 v[164:167], v[192:195], v[212:215], v[74:77]\n"
      "s_waitcnt lgkmcnt(1)\n"
      "v_fmac_f32 v249, v205, v205\n"
      "v_fmac_f32_e64 v249, v206, v206\n"
      "v_mfma_f32_16x16x32_bf16 v[164:167], v[180:183], v[136:139], v[164:167]\n"
      "v_fmac_f32_e64 v249, v207, v207\n"
      "v_cvt_pk_bf16_f32 v63, v146, v147\n"
      "v_mfma_f32_16x16x32_bf16 v[164:167], v[188:191], v[44:47], v[164:167]\n"
      "ds_read_b128 v[228:231], v123 offset:6656\n"
      "v_cvt_pk_bf16_f32 v64, v240, 0\n"
      "v_mul_f32 v200, v184, v200\n"
      "v_mul_f32 v201, v184, v201\n"
      "v_cvt_pk_bf16_f32 v150, v200, v201\n"
      "v_mfma_f32_16x16x32_bf16 v[144:147], v[136:139], v[212:215], v[110:113]\n"
      "ds_read_b128 v[136:139], v123 offset:9728\n"
      "v_mul_f32_e64 v202, v184, v202\n"
      "v_mfma_f32_16x16x32_bf16 v[144:147], v[46:49], v[44:47], v[144:147]\n"
      "ds_read_b128 v[212:215], v121\n"
      "v_mul_f32_e64 v203, v184, v203\n"
      "v_cvt_pk_bf16_f32 v151, v202, v203\n"
      "v_mul_f32_e64 v250, v184, v250\n"
      "v_mul_f32 v251, v184, v251\n"
      "v_cvt_pk_bf16_f32 v250, v250, v251\n"
      "v_mul_f32_e64 v252, v184, v252\n"
      "v_mul_f32_e64 v253, v184, v253\n"
      "v_cvt_pk_bf16_f32 v251, v252, v253\n"
      "v_mfma_f32_16x16x32_bf16 v[200:203], v[180:183], v[192:195], v[76:79]\n"
      "ds_write_b128 v124, v[148:151] offset:18432\n"
      "v_mfma_f32_16x16x32_bf16 v[180:183], v[66:69], v[232:235], 0\n"
      "s_waitcnt lgkmcnt(4)\n"
      "v_cndmask_b32 v173, v173, v165, s[44:45]\n"
      "v_mfma_f32_16x16x32_bf16 v[180:183], v[66:69], v[62:65], v[180:183]\n"
      "global_store_dwordx4 v117, v[236:239], s[54:55] sc1\n"
      "v_mfma_f32_16x16x16_bf16 v[200:203], v[190:191], v[188:189], v[200:203]\n"
      "ds_write_b64 v125, v[250:251] offset:18432\n"
      "v_mfma_f32_16x16x32_bf16 v[148:151], v[160:163], v[232:235], 0\n"
      "v_exp_f32 v216, v216\n"
      "v_mfma_f32_16x16x32_bf16 v[148:151], v[50:53], v[62:65], v[148:151]\n"
      "s_waitcnt lgkmcnt(2)\n"
      "v_exp_f32 v217, v217\n"
      "v_mfma_f32_16x16x32_bf16 v[184:187], v[220:223], v[232:235], 0\n"
      "s_nop 0\n"
      "v_cndmask_b32 v173, v173, v201, s[46:47]\n"
      "v_cvt_pk_bf16_f32 v216, v216, v217\n"
      "v_mfma_f32_16x16x32_bf16 v[184:187], v[54:57], v[62:65], v[184:187]\n"
      "v_exp_f32_e64 v173, v173\n"
      "v_mfma_f32_16x16x32_bf16 v[200:203], v[156:159], v[232:235], 0\n"
      "v_cvt_pk_bf16_f32 v173, v173, v173\n"
      "v_mfma_f32_16x16x32_bf16 v[200:203], v[58:61], v[62:65], v[200:203]\n"
      "ds_write_b16 v130, v173\n"
      "v_mfma_f32_16x16x32_bf16 v[192:195], a[216:219], v[208:211], 0\n"
      "ds_read_b128 v[232:235], v131\n"
      "v_mfma_f32_16x16x32_bf16 v[192:195], a[228:231], v[224:227], v[192:195]\n"
      "ds_read_b128 v[172:175], v131 offset:64\n"
      "v_exp_f32 v218, v218\n"
      "v_mfma_f32_16x16x32_bf16 v[250:253], a[220:223], v[208:211], 0\n"
      "v_exp_f32 v219, v219\n"
      "v_mfma_f32_16x16x32_bf16 v[250:253], a[232:235], v[224:227], v[250:253]\n"
      "s_mov_b32 exec_hi, 0\n"
      "global_store_dwordx4 v118, v[212:215], s[54:55] sc1\n"
      "s_mov_b32 exec_hi, -1\n"
      "v_mfma_f32_16x16x32_bf16 v[188:191], a[224:227], v[208:211], 0\n"
      "ds_read_b128 v[208:211], v123 offset:12800\n"
      "v_cvt_pk_bf16_f32 v217, v218, v219\n"
      "v_mfma_f32_16x16x32_bf16 v[188:191], a[236:239], v[224:227], v[188:191]\n"
      "ds_read_b128 v[224:227], v123 offset:15872\n"
      "v_exp_f32 v176, v176\n"
      "v_mfma_f32_16x16x32_bf16 v[192:195], a[240:243], v[228:231], v[192:195]\n"
      "ds_read_b128 v[212:215], v114\n"
      "s_add_u32 s28, s27, 2\n"
      "v_mfma_f32_16x16x32_bf16 v[192:195], a[252:255], v[136:139], v[192:195]\n"
      "s_min_u32 m0, s28, s3\n"
      "v_exp_f32 v177, v177\n"
      "v_mfma_f32_16x16x32_bf16 v[250:253], a[244:247], v[228:231], v[250:253]\n"
      "s_movrels_b32 s9, s80\n"
      "v_exp_f32 v178, v178\n"
      "v_mfma_f32_16x16x32_bf16 v[250:253], v[0:3], v[136:139], v[250:253]\n"
      "s_lshr_b32 s8, s9, 8\n"
      "v_cvt_pk_bf16_f32 v218, v176, v177\n"
      "v_exp_f32_e64 v179, v179\n"
      "s_waitcnt lgkmcnt(3)\n"
      "v_mfma_f32_16x16x32_bf16 v[188:191], a[248:251], v[228:231], v[188:191]\n"
      "ds_read_b128 v[228:231], v115\n"
      "v_cvt_pk_bf16_f32 v219, v178, v179\n"
      "v_mfma_f32_16x16x32_bf16 v[188:191], v[4:7], v[136:139], v[188:191]\n"
      "s_and_b32 s9, s9, 0xff\n"
      "v_exp_f32 v144, v144\n"
      "v_mfma_f32_16x16x32_bf16 v[176:179], v[232:235], v[66:69], 0\n"
      "s_mul_i32 s10, s8, 73\n"
      "v_exp_f32 v145, v145\n"
      "v_mfma_f32_16x16x32_bf16 v[176:179], v[172:175], v[66:69], v[176:179]\n"
      "s_add_u32 s10, s10, s9\n"
      "v_exp_f32_e64 v146, v146\n"
      "s_waitcnt lgkmcnt(2)\n"
      "v_mfma_f32_16x16x32_bf16 v[192:195], v[8:11], v[208:211], v[192:195]\n"
      "s_mul_i32 s28, s10, 4802\n"
      "v_exp_f32 v147, v147\n"
      "v_mfma_f32_16x16x32_bf16 v[192:195], v[20:23], v[224:227], v[192:195]\n"
      "s_add_u32 s56, s18, s28\n"
      "v_exp_f32_e64 v164, v164\n"
      "v_mfma_f32_16x16x32_bf16 v[250:253], v[12:15], v[208:211], v[250:253]\n"
      "s_addc_u32 s57, s19, 0\n"
      "v_cvt_pk_bf16_f32 v62, v144, v145\n"
      "v_cvt_pk_bf16_f32 v63, v146, v147\n"
      "v_mfma_f32_16x16x32_bf16 v[250:253], v[24:27], v[224:227], v[250:253]\n"
      "s_and_b32 s56, s56, -16\n"
      "v_cvt_pk_bf16_f32 v64, v164, 0\n"
      "v_fma_f32 v183, v192, v192, s39\n"
      "v_mfma_f32_16x16x32_bf16 v[188:191], v[16:19], v[208:211], v[188:191]\n"
      "s_sub_u32 s58, s56, 110592\n"
      "v_rcp_f32 v180, v180\n"
      "v_mfma_f32_16x16x32_bf16 v[188:191], v[28:31], v[224:227], v[188:191]\n"
      "s_waitcnt lgkmcnt(0)\n"
      "v_mul_f32 v148, v180, v148\n"
      "v_mul_f32 v149, v180, v149\n"
      "v_mfma_f32_16x16x32_bf16 v[136:139], v[66:69], v[216:219], 0\n"
      "s_subb_u32 s59, s57, 0\n"
      "v_cvt_pk_bf16_f32 v148, v148, v149\n"
      "v_mul_f32 v150, v180, v150\n"
      "v_mfma_f32_16x16x32_bf16 v[136:139], v[66:69], v[62:65], v[136:139]\n"
      "s_sub_u32 s56, s56, 73728\n"
      "v_mul_f32 v151, v180, v151\n"
      "v_cvt_pk_bf16_f32 v149, v150, v151\n"
      "v_mfma_f32_16x16x32_bf16 v[164:167], v[160:163], v[216:219], 0\n"
      "s_subb_u32 s57, s57, 0\n"
      "v_mul_f32 v184, v180, v184\n"
      "v_mul_f32 v185, v180, v185\n"
      "v_mfma_f32_16x16x32_bf16 v[164:167], v[50:53], v[62:65], v[164:167]\n"
      "s_mul_i32 s28, s8, 1373568\n"
      "v_cvt_pk_bf16_f32 v150, v184, v185\n"
      "v_mul_f32_e64 v186, v180, v186\n"
      "v_mfma_f32_16x16x32_bf16 v[208:211], v[220:223], v[216:219], 0\n"
      "s_mul_i32 s29, s9, 2688\n"
      "v_mul_f32_e64 v187, v180, v187\n"
      "v_cvt_pk_bf16_f32 v151, v186, v187\n"
      "v_mfma_f32_16x16x32_bf16 v[208:211], v[54:57], v[62:65], v[208:211]\n"
      "ds_write_b128 v124, v[148:151] offset:18688\n"
      "v_mfma_f32_16x16x32_bf16 v[148:151], v[156:159], v[216:219], 0\n"
      "s_add_u32 s28, s28, s29\n"
      "v_mul_f32 v200, v180, v200\n"
      "v_mul_f32 v201, v180, v201\n"
      "v_mfma_f32_16x16x32_bf16 v[148:151], v[58:61], v[62:65], v[148:151]\n"
      "s_cmp_eq_u32 s8, 72\n"
      "v_cvt_pk_bf16_f32 v200, v200, v201\n"
      "v_mul_f32 v202, v180, v202\n"
      "v_mfma_f32_16x16x32_bf16 v[184:187], v[232:235], v[160:163], 0\n"
      "s_cselect_b32 s8, 0, 100270464\n"
      "v_mul_f32_e64 v203, v180, v203\n"
      "v_cvt_pk_bf16_f32 v201, v202, v203\n"
      "v_mfma_f32_16x16x32_bf16 v[184:187], v[172:175], v[50:53], v[184:187]\n"
      "ds_write_b64 v125, v[200:201] offset:18688\n"
      "v_mfma_f32_16x16x32_bf16 v[200:203], v[232:235], v[220:223], 0\n"
      "s_cmp_eq_u32 s9, 72\n"
      "v_fmac_f32 v183, v193, v193\n"
      "v_fmac_f32 v183, v194, v194\n"
      "v_mfma_f32_16x16x32_bf16 v[200:203], v[172:175], v[54:57], v[200:203]\n"
      "s_cselect_b32 s9, 0, 196224\n"
      "v_fmac_f32 v183, v195, v195\n"
      "v_fmac_f32 v183, v250, v250\n"
      "v_mfma_f32_16x16x32_bf16 v[178:181], v[232:235], v[156:159], 0\n"
      "s_add_u32 s29, s8, s9\n"
      "v_fmac_f32 v183, v251, v251\n"
      "v_fmac_f32 v183, v252, v252\n"
      "v_mfma_f32_16x16x32_bf16 v[178:181], v[172:175], v[58:61], v[178:181]\n"
      "s_add_u32 s28, s28, s29\n"
      "v_fmac_f32 v183, v253, v253\n"
      "v_fmac_f32 v183, v188, v188\n"
      "v_mfma_f32_16x16x32_bf16 v[172:175], a[0:3], v[212:215], 0\n"
      "s_add_u32 s52, s16, s28\n"
      "v_fmac_f32 v183, v189, v189\n"
      "v_fmac_f32_e64 v183, v190, v190\n"
      "v_mfma_f32_16x16x32_bf16 v[172:175], a[36:39], v[228:231], v[172:175]\n"
      "s_addc_u32 s53, s17, 0\n"
      "v_fmac_f32 v183, v191, v191\n"
      "v_mov_b32 v181, v183\n"
      "v_mfma_f32_16x16x32_bf16 v[156:159], a[4:7], v[212:215], 0\n"
      "s_cmp_lg_u32 s29, s11\n"
      "v_permlane16_swap_b32 v248, v249\n"
      "v_mfma_f32_16x16x32_bf16 v[156:159], a[40:43], v[228:231], v[156:159]\n"
      "s_cbranch_scc1 L_edge_26_%=\n"
      "L_eb_27_%=:\n"
      "v_add_f32_e64 v248, v248, v249\n"
      "v_permlane16_swap_b32_e64 v183, v181\n"
      "v_add_f32_e64 v183, v183, v181\n"
      "v_mov_b32_dpp v176, v176 quad_perm:[0,0,0,0] row_mask:0xf bank_mask:0xf\n"
      "v_cndmask_b32 v184, v184, v200, s[42:43]\n"
      "v_permlane32_swap_b32_e64 v248, v183\n"
      "v_add_f32 v248, v248, v183\n"
      "ds_write_b32 v129, v248 offset:1024\n"
      "v_cndmask_b32 v184, v184, v178, s[44:45]\n"
      "v_rcp_f32_e64 v176, v176\n"
      "s_nop 0\n"
      "v_mul_f32 v184, v176, v184\n"
      "v_cvt_pk_bf16_f32 v184, v184, v184\n"
      "ds_write_b16 v135, v184 offset:16\n"
      "L_join_21_%=:\n"
      "s_cmp_ge_u32 s27, s3\n"
      "s_cbranch_scc1 L_tail1_%=\n"
      "s_add_u32 s27, s27, 1\n"
      "v_mfma_f32_16x16x32_bf16 v[180:183], a[8:11], v[212:215], 0\n"
      "ds_read_b128 v[200:203], v114 offset:6144\n"
      "s_add_u32 m0, s76, 18432\n"
      "v_mfma_f32_16x16x32_bf16 v[180:183], a[44:47], v[228:231], v[180:183]\n"
      "ds_read_b128 v[176:179], v115 offset:6144\n"
      "v_rcp_f32 v136, v136\n"
      "v_mfma_f32_16x16x32_bf16 v[184:187], a[12:15], v[212:215], 0\n"
      "v_mul_f32 v164, v136, v164\n"
      "v_mul_f32_e64 v165, v136, v165\n"
      "v_mfma_f32_16x16x32_bf16 v[184:187], a[48:51], v[228:231], v[184:187]\n"
      "ds_read_b128 v[236:239], v114 offset:12288\n"
      "v_cvt_pk_bf16_f32 v164, v164, v165\n"
      "v_mfma_f32_16x16x32_bf16 v[232:235], a[16:19], v[212:215], 0\n"
      "v_mul_f32 v166, v136, v166\n"
      "v_mul_f32 v167, v136, v167\n"
      "v_mfma_f32_16x16x32_bf16 v[232:235], a[52:55], v[228:231], v[232:235]\n"
      "s_waitcnt lgkmcnt(3)\n"
      "s_barrier\n"
      "v_mfma_f32_16x16x32_bf16 v[216:219], a[20:23], v[212:215], 0\n"
      "ds_read_b128 v[224:227], v128 offset:1024\n"
      "v_cvt_pk_bf16_f32 v165, v166, v167\n"
      "v_mfma_f32_16x16x32_bf16 v[216:219], a[56:59], v[228:231], v[216:219]\n"
      "global_load_lds_dwordx4 v116, s[52:53]\n"
      "v_mfma_f32_16x16x32_bf16 v[220:223], v[212:215], a[24:27], 0\n"
      "s_add_u32 m0, s76, 24448\n"
      "v_mul_f32 v208, v136, v208\n"
      "v_mul_f32 v209, v136, v209\n"
      "v_mfma_f32_16x16x32_bf16 v[220:223], v[228:231], a[60:63], v[220:223]\n"
      "s_waitcnt lgkmcnt(2)\n"
      "v_cvt_pk_bf16_f32 v166, v208, v209\n"
      "v_mul_f32 v210, v136, v210\n"
      "v_mfma_f32_16x16x32_bf16 v[160:163], v[212:215], a[28:31], 0\n"
      "v_mul_f32_e64 v211, v136, v211\n"
      "v_cvt_pk_bf16_f32 v167, v210, v211\n"
      "v_mfma_f32_16x16x32_bf16 v[160:163], v[228:231], a[64:67], v[160:163]\n"
      "ds_write_b128 v124, v[164:167] offset:18944\n"
      "v_mfma_f32_16x16x32_bf16 v[208:211], v[212:215], a[32:35], 0\n"
      "ds_read_b128 v[212:215], v115 offset:12288\n"
      "v_mul_f32 v148, v136, v148\n"
      "v_mfma_f32_16x16x32_bf16 v[208:211], v[228:231], a[68:71], v[208:211]\n"
      "v_mul_f32 v149, v136, v149\n"
      "v_cvt_pk_bf16_f32 v148, v148, v149\n"
      "v_mfma_f32_16x16x32_bf16 v[172:175], a[72:75], v[200:203], v[172:175]\n"
      "v_mul_f32 v150, v136, v150\n"
      "v_mul_f32_e64 v151, v136, v151\n"
      "v_mfma_f32_16x16x32_bf16 v[172:175], a[108:111], v[176:179], v[172:175]\n"
      "s_waitcnt lgkmcnt(2)\n"
      "v_cvt_pk_bf16_f32 v149, v150, v151\n"
      "v_add_f32 v224, |v224|, |v225|\n"
      "v_mfma_f32_16x16x32_bf16 v[156:159], a[76:79], v[200:203], v[156:159]\n"
      "v_add_f32 v226, |v226|, |v227|\n"
      "v_add_f32_e64 v224, v224, v226\n"
      "v_mfma_f32_16x16x32_bf16 v[156:159], a[112:115], v[176:179], v[156:159]\n"
      "ds_write_b64 v125, v[148:149] offset:18944\n"
      "v_mfma_f32_16x16x32_bf16 v[180:183], a[80:83], v[200:203], v[180:183]\n"
      "global_load_lds_dwordx4 v116, s[52:53] offset:128\n"
      "v_mfma_f32_16x16x32_bf16 v[180:183], a[116:119], v[176:179], v[180:183]\n"
      "s_add_u32 m0, s76, 30464\n"
      "v_rsq_f32_e64 v224, v224\n"
      "v_mfma_f32_16x16x32_bf16 v[184:187], a[84:87], v[200:203], v[184:187]\n"
      "v_mul_f32 v224, s41, v224\n"
      "v_mov_b32 v248, v224\n"
      "v_mfma_f32_16x16x32_bf16 v[184:187], a[120:123], v[176:179], v[184:187]\n"
      "s_waitcnt lgkmcnt(1)\n"
      "v_permlane32_swap_b32 v224, v248\n"
      "v_mfma_f32_16x16x32_bf16 v[232:235], a[88:91], v[200:203], v[232:235]\n"
      "ds_read_b128 v[164:167], v114 offset:2048\n"
      "v_mov_b32 v249, v224\n"
      "v_mfma_f32_16x16x32_bf16 v[232:235], a[124:127], v[176:179], v[232:235]\n"
      "s_nop 0\n"
      "v_permlane16_swap_b32 v224, v249\n"
      "v_mfma_f32_16x16x32_bf16 v[216:219], a[92:95], v[200:203], v[216:219]\n"
      "ds_read_b128 v[148:151], v115 offset:2048\n"
      "v_mul_f32 v140, v224, v140\n"
      "v_mfma_f32_16x16x32_bf16 v[216:219], a[128:131], v[176:179], v[216:219]\n"
      "v_mul_f32 v141, v224, v141\n"
      "v_cvt_pk_bf16_f32 v140, v140, v141\n"
      "v_mfma_f32_16x16x32_bf16 v[220:223], v[200:203], a[96:99], v[220:223]\n"
      "v_mul_f32 v142, v224, v142\n"
      "v_mul_f32_e64 v143, v224, v143\n"
      "v_mfma_f32_16x16x32_bf16 v[220:223], v[176:179], a[132:135], v[220:223]\n"
      "global_load_lds_dwordx4 v116, s[52:53] offset:256\n"
      "v_mfma_f32_16x16x32_bf16 v[160:163], v[200:203], a[100:103], v[160:163]\n"
      "s_add_u32 m0, s77, 18432\n"
      "v_cvt_pk_bf16_f32 v141, v142, v143\n"
      "v_mul_f32 v196, v224, v196\n"
      "v_mfma_f32_16x16x32_bf16 v[160:163], v[176:179], a[136:139], v[160:163]\n"
      "v_mul_f32 v197, v224, v197\n"
      "v_cvt_pk_bf16_f32 v142, v196, v197\n"
      "v_mfma_f32_16x16x32_bf16 v[208:211], v[200:203], a[104:107], v[208:211]\n"
      "v_mul_f32 v198, v224, v198\n"
      "v_mul_f32 v199, v224, v199\n"
      "v_mfma_f32_16x16x32_bf16 v[208:211], v[176:179], a[140:143], v[208:211]\n"
      "v_cvt_pk_bf16_f32 v143, v198, v199\n"
      "v_mul_f32 v168, v224, v168\n"
      "v_mfma_f32_16x16x32_bf16 v[172:175], a[144:147], v[236:239], v[172:175]\n"
      "v_mul_f32 v169, v224, v169\n"
      "v_cvt_pk_bf16_f32 v168, v168, v169\n"
      "v_mfma_f32_16x16x32_bf16 v[172:175], a[180:183], v[212:215], v[172:175]\n"
      "ds_write_b128 v126, v[140:143]\n"
      "s_waitcnt lgkmcnt(1)\n"
      "v_mfma_f32_16x16x32_bf16 v[156:159], a[148:151], v[236:239], v[156:159]\n"
      "v_mul_f32 v170, v224, v170\n"
      "v_mul_f32_e64 v171, v224, v171\n"
      "v_mfma_f32_16x16x32_bf16 v[156:159], a[184:187], v[212:215], v[156:159]\n"
      "global_load_lds_dwordx4 v117, s[52:53]\n"
      "v_mfma_f32_16x16x32_bf16 v[180:183], a[152:155], v[236:239], v[180:183]\n"
      "s_add_u32 m0, s78, 18432\n"
      "v_cvt_pk_bf16_f32 v169, v170, v171\n"
      "v_mul_f32_e64 v244, v249, v244\n"
      "v_mfma_f32_16x16x32_bf16 v[180:183], a[188:191], v[212:215], v[180:183]\n"
      "ds_write_b64 v127, v[168:169]\n"
      "v_mfma_f32_16x16x32_bf16 v[184:187], a[156:159], v[236:239], v[184:187]\n"
      "v_mul_f32_e64 v245, v249, v245\n"
      "v_cvt_pk_bf16_f32 v244, v244, v245\n"
      "v_mfma_f32_16x16x32_bf16 v[184:187], a[192:195], v[212:215], v[184:187]\n"
      "ds_read_b128 v[140:143], v114 offset:8192\n"
      "v_mul_f32 v246, v249, v246\n"
      "v_mfma_f32_16x16x32_bf16 v[232:235], a[160:163], v[236:239], v[232:235]\n"
      "ds_read_b128 v[196:199], v115 offset:8192\n"
      "v_mul_f32 v247, v249, v247\n"
      "v_mfma_f32_16x16x32_bf16 v[232:235], a[196:199], v[212:215], v[232:235]\n"
      "v_cvt_pk_bf16_f32 v245, v246, v247\n"
      "v_mul_f32 v152, v249, v152\n"
      "v_mfma_f32_16x16x32_bf16 v[216:219], a[164:167], v[236:239], v[216:219]\n"
      "v_mul_f32 v153, v249, v153\n"
      "v_cvt_pk_bf16_f32 v246, v152, v153\n"
      "v_mfma_f32_16x16x32_bf16 v[216:219], a[200:203], v[212:215], v[216:219]\n"
      "v_mul_f32 v154, v249, v154\n"
      "v_mul_f32 v155, v249, v155\n"
      "v_mfma_f32_16x16x32_bf16 v[220:223], v[236:239], a[168:171], v[220:223]\n"
      "v_cvt_pk_bf16_f32 v247, v154, v155\n"
      "v_mul_f32_e64 v204, v249, v204\n"
      "v_mfma_f32_16x16x32_bf16 v[220:223], v[212:215], a[204:207], v[220:223]\n"
      "ds_write_b128 v126, v[244:247] offset:2048\n"
      "v_mfma_f32_16x16x32_bf16 v[160:163], v[236:239], a[172:175], v[160:163]\n"
      "v_mul_f32 v205, v249, v205\n"
      "v_cvt_pk_bf16_f32 v204, v204, v205\n"
      "v_mfma_f32_16x16x32_bf16 v[160:163], v[212:215], a[208:211], v[160:163]\n"
      "s_mov_b32 exec_hi, 0\n"
      "global_load_lds_dwordx4 v118, s[52:53]\n"
      "s_mov_b32 exec_hi, -1\n"
      "v_mfma_f32_16x16x32_bf16 v[208:211], v[236:239], a[176:179], v[208:211]\n"
      "s_add_u32 m0, s76, 115456\n"
      "v_mul_f32 v206, v249, v206\n"
      "v_mul_f32 v207, v249, v207\n"
      "v_mfma_f32_16x16x32_bf16 v[208:211], v[212:215], a[212:215], v[208:211]\n"
      "s_waitcnt lgkmcnt(1)\n"
      "v_cvt_pk_bf16_f32 v205, v206, v207\n"
      "v_mul_f32_e64 v192, v248, v192\n"
      "v_mfma_f32_16x16x32_bf16 v[176:179], a[0:3], v[164:167], 0\n"
      "v_mul_f32_e64 v193, v248, v193\n"
      "v_cvt_pk_bf16_f32 v192, v192, v193\n"
      "v_mfma_f32_16x16x32_bf16 v[176:179], a[36:39], v[148:151], v[176:179]\n"
      "ds_write_b64 v127, v[204:205] offset:2048\n"
      "v_mfma_f32_16x16x32_bf16 v[212:215], a[4:7], v[164:167], 0\n"
      "global_load_lds_dwordx4 v119, s[56:57]\n"
      "v_mfma_f32_16x16x32_bf16 v[212:215], a[40:43], v[148:151], v[212:215]\n"
      "s_add_u32 m0, s79, 4864\n"
      "v_mul_f32 v194, v248, v194\n"
      "v_mul_f32 v195, v248, v195\n"
      "v_mfma_f32_16x16x32_bf16 v[244:247], a[8:11], v[164:167], 0\n"
      "v_cvt_pk_bf16_f32 v193, v194, v195\n"
      "v_mul_f32 v250, v248, v250\n"
      "v_mfma_f32_16x16x32_bf16 v[244:247], a[44:47], v[148:151], v[244:247]\n"
      "v_mul_f32 v251, v248, v251\n"
      "v_cvt_pk_bf16_f32 v194, v250, v251\n"
      "v_mfma_f32_16x16x32_bf16 v[168:171], a[12:15], v[164:167], 0\n"
      "ds_read_b128 v[136:139], v114 offset:14336\n"
      "v_mul_f32_e64 v252, v248, v252\n"
      "v_mfma_f32_16x16x32_bf16 v[168:171], a[48:51], v[148:151], v[168:171]\n"
      "v_mul_f32_e64 v253, v248, v253\n"
      "v_cvt_pk_bf16_f32 v195, v252, v253\n"
      "v_mfma_f32_16x16x32_bf16 v[200:203], a[16:19], v[164:167], 0\n"
      "ds_read_b128 v[250:253], v115 offset:14336\n"
      "v_mul_f32_e64 v188, v248, v188\n"
      "v_mfma_f32_16x16x32_bf16 v[200:203], a[52:55], v[148:151], v[200:203]\n"
      "ds_write_b128 v126, v[192:195] offset:4096\n"
      "v_mfma_f32_16x16x32_bf16 v[192:195], a[20:23], v[164:167], 0\n"
      "v_mul_f32_e64 v189, v248, v189\n"
      "v_cvt_pk_bf16_f32 v188, v188, v189\n"
      "v_mfma_f32_16x16x32_bf16 v[192:195], a[56:59], v[148:151], v[192:195]\n"
      "s_mov_b64 exec, 0xfff\n"
      "global_load_lds_dwordx4 v122, s[58:59]\n"
      "s_mov_b64 exec, -1\n"
      "v_mfma_f32_16x16x32_bf16 v[204:207], v[164:167], a[24:27], 0\n"
      "s_sub_u32 s28, s27, 3\n"
      "v_mul_f32 v190, v248, v190\n"
      "v_mul_f32 v191, v248, v191\n"
      "v_mfma_f32_16x16x32_bf16 v[204:207], v[148:151], a[60:63], v[204:207]\n"
      "s_cselect_b64 s[54:55], s[12:13], s[22:23]\n"
      "v_cvt_pk_bf16_f32 v189, v190, v191\n"
      "v_fma_f32 v249, v172, v172, s38\n"
      "v_mfma_f32_16x16x32_bf16 v[152:155], v[164:167], a[28:31], 0\n"
      "s_max_i32 m0, s28, 0\n"
      "v_fma_f32 v248, v184, v184, s38\n"
      "v_fmac_f32_e64 v249, v173, v173\n"
      "v_mfma_f32_16x16x32_bf16 v[152:155], v[148:151], a[64:67], v[152:155]\n"
      "ds_write_b64 v127, v[188:189] offset:4096\n"
      "v_mfma_f32_16x16x32_bf16 v[188:191], v[164:167], a[32:35], 0\n"
      "s_waitcnt lgkmcnt(1)\n"
      "v_fmac_f32 v248, v185, v185\n"
      "v_fmac_f32 v249, v174, v174\n"
      "v_mfma_f32_16x16x32_bf16 v[188:191], v[148:151], a[68:71], v[188:191]\n"
      "s_movrels_b32 s9, s80\n"
      "v_fmac_f32 v248, v186, v186\n"
      "v_fmac_f32 v249, v175, v175\n"
      "v_mfma_f32_16x16x32_bf16 v[176:179], a[72:75], v[140:143], v[176:179]\n"
      "s_lshr_b32 s8, s9, 8\n"
      "v_fmac_f32 v248, v187, v187\n"
      "v_fmac_f32_e64 v249, v156, v156\n"
      "v_mfma_f32_16x16x32_bf16 v[176:179], a[108:111], v[196:199], v[176:179]\n"
      "s_and_b32 s9, s9, 0xff\n"
      "v_fmac_f32 v248, v232, v232\n"
      "v_fmac_f32 v249, v157, v157\n"
      "v_mfma_f32_16x16x32_bf16 v[212:215], a[76:79], v[140:143], v[212:215]\n"
      "s_mul_i32 s28, s8, 1373568\n"
      "v_fmac_f32 v248, v233, v233\n"
      "v_fmac_f32 v249, v158, v158\n"
      "v_mfma_f32_16x16x32_bf16 v[212:215], a[112:115], v[196:199], v[212:215]\n"
      "s_mul_i32 s29, s9, 2688\n"
      "v_fmac_f32 v248, v234, v234\n"
      "v_fmac_f32 v249, v159, v159\n"
      "v_mfma_f32_16x16x32_bf16 v[244:247], a[80:83], v[140:143], v[244:247]\n"
      "s_add_u32 s28, s28, s29\n"
      "v_fmac_f32 v248, v235, v235\n"
      "v_fmac_f32_e64 v249, v180, v180\n"
      "v_mfma_f32_16x16x32_bf16 v[244:247], a[116:119], v[196:199], v[244:247]\n"
      "s_cmp_eq_u32 s8, 72\n"
      "v_fmac_f32 v248, v216, v216\n"
      "v_fmac_f32 v249, v181, v181\n"
      "v_mfma_f32_16x16x32_bf16 v[168:171], a[84:87], v[140:143], v[168:171]\n"
      "s_cselect_b32 s8, 0, 100270464\n"
      "v_fmac_f32 v248, v217, v217\n"
      "v_fmac_f32 v249, v182, v182\n"
      "v_mfma_f32_16x16x32_bf16 v[168:171], a[120:123], v[196:199], v[168:171]\n"
      "ds_read_b128 v[164:167], v114 offset:4096\n"
      "v_fmac_f32 v248, v218, v218\n"
      "v_mfma_f32_16x16x32_bf16 v[200:203], a[88:91], v[140:143], v[200:203]\n"
      "ds_read_b128 v[148:151], v115 offset:4096\n"
      "v_fmac_f32 v249, v183, v183\n"
      "v_mfma_f32_16x16x32_bf16 v[200:203], a[124:127], v[196:199], v[200:203]\n"
      "s_cmp_eq_u32 s9, 72\n"
      "v_fmac_f32 v248, v219, v219\n"
      "v_mfma_f32_16x16x32_bf16 v[192:195], a[92:95], v[140:143], v[192:195]\n"
      "s_cselect_b32 s9, 0, 196224\n"
      "v_permlane16_swap_b32 v249, v248\n"
      "v_mfma_f32_16x16x32_bf16 v[192:195], a[128:131], v[196:199], v[192:195]\n"
      "s_add_u32 s29, s8, s9\n"
      "v_add_f32 v249, v249, v248\n"
      "v_mov_b32 v248, v249\n"
      "v_mfma_f32_16x16x32_bf16 v[204:207], v[140:143], a[96:99], v[204:207]\n"
      "s_add_u32 s28, s28, s29\n"
      "v_permlane32_swap_b32 v249, v248\n"
      "v_mfma_f32_16x16x32_bf16 v[204:207], v[196:199], a[132:135], v[204:207]\n"
      "s_add_u32 s54, s54, s28\n"
      "v_add_f32 v249, v249, v248\n"
      "v_rsq_f32 v249, v249\n"
      "v_mfma_f32_16x16x32_bf16 v[152:155], v[140:143], a[100:103], v[152:155]\n"
      "s_addc_u32 s55, s55, 0\n"
      "v_mov_b32 v248, v249\n"
      "s_cmp_lg_u32 s29, s11\n"
      "v_mfma_f32_16x16x32_bf16 v[152:155], v[196:199], a[136:139], v[152:155]\n"
      "s_cbranch_scc1 L_edge_28_%=\n"
      "L_eb_29_%=:\n"
      "v_mfma_f32_16x16x32_bf16 v[188:191], v[140:143], a[104:107], v[188:191]\n"
      "s_mov_b32 s5, s4\n"
      "v_permlane16_swap_b32 v249, v248\n"
      "v_mfma_f32_16x16x32_bf16 v[188:191], v[196:199], a[140:143], v[188:191]\n"
      "s_add_u32 s4, s27, 1\n"
      "v_mul_f32_e64 v184, v248, v184\n"
      "s_waitcnt lgkmcnt(0)\n"
      "v_mfma_f32_16x16x32_bf16 v[176:179], a[144:147], v[136:139], v[176:179]\n"
      "s_lshl_b32 s4, s4, 30\n"
      "v_mul_f32 v185, v248, v185\n"
      "v_cvt_pk_bf16_f32 v184, v184, v185\n"
      "v_mfma_f32_16x16x32_bf16 v[176:179], a[180:183], v[250:253], v[176:179]\n"
      "ds_read_b128 v[140:143], v119 offset:18432\n"
      "v_mul_f32 v186, v248, v186\n"
      "v_mfma_f32_16x16x32_bf16 v[212:215], a[148:151], v[136:139], v[212:215]\n"
      "s_and_b32 s4, s4, 0x80000000\n"
      "v_mul_f32 v187, v248, v187\n"
      "v_cvt_pk_bf16_f32 v185, v186, v187\n"
      "v_mfma_f32_16x16x32_bf16 v[212:215], a[184:187], v[250:253], v[212:215]\n"
      "v_mul_f32 v232, v248, v232\n"
      "v_mul_f32 v233, v248, v233\n"
      "v_mfma_f32_16x16x32_bf16 v[244:247], a[152:155], v[136:139], v[244:247]\n"
      "v_cvt_pk_bf16_f32 v186, v232, v233\n"
      "v_mul_f32 v234, v248, v234\n"
      "v_mfma_f32_16x16x32_bf16 v[244:247], a[188:191], v[250:253], v[244:247]\n"
      "v_mul_f32 v235, v248, v235\n"
      "v_cvt_pk_bf16_f32 v187, v234, v235\n"
      "v_mfma_f32_16x16x32_bf16 v[168:171], a[156:159], v[136:139], v[168:171]\n"
      "v_mul_f32 v216, v248, v216\n"
      "v_mul_f32 v217, v248, v217\n"
      "v_mfma_f32_16x16x32_bf16 v[168:171], a[192:195], v[250:253], v[168:171]\n"
      "v_cvt_pk_bf16_f32 v34, v216, v217\n"
      "v_mul_f32 v218, v248, v218\n"
      "v_mfma_f32_16x16x32_bf16 v[200:203], a[160:163], v[136:139], v[200:203]\n"
      "v_mul_f32 v219, v248, v219\n"
      "v_cvt_pk_bf16_f32 v35, v218, v219\n"
      "v_mfma_f32_16x16x32_bf16 v[200:203], a[196:199], v[250:253], v[200:203]\n"
      "ds_read_b128 v[196:199], v114 offset:10240\n"
      "v_mul_f32 v249, s36, v249\n"
      "v_mfma_f32_16x16x32_bf16 v[192:195], a[164:167], v[136:139], v[192:195]\n"
      "v_mul_f32 v172, v249, v172\n"
      "v_mul_f32_e64 v173, v249, v173\n"
      "v_mfma_f32_16x16x32_bf16 v[192:195], a[200:203], v[250:253], v[192:195]\n"
      "s_waitcnt lgkmcnt(1)\n"
      "v_cvt_pk_bf16_f32 v172, v172, v173\n"
      "v_mul_f32 v174, v249, v174\n"
      "v_mfma_f32_16x16x32_bf16 v[204:207], v[136:139], a[168:171], v[204:207]\n"
      "global_store_dwordx4 v116, v[140:143], s[54:55] sc1\n"
      "v_mfma_f32_16x16x32_bf16 v[204:207], v[250:253], a[204:207], v[204:207]\n"
      "ds_read_b128 v[232:235], v115 offset:10240\n"
      "v_mul_f32_e64 v175, v249, v175\n"
      "v_mfma_f32_16x16x32_bf16 v[152:155], v[136:139], a[172:175], v[152:155]\n"
      "v_cvt_pk_bf16_f32 v173, v174, v175\n"
      "v_mul_f32 v156, v249, v156\n"
      "v_mfma_f32_16x16x32_bf16 v[152:155], v[250:253], a[208:211], v[152:155]\n"
      "v_mul_f32 v157, v249, v157\n"
      "v_cvt_pk_bf16_f32 v174, v156, v157\n"
      "v_mfma_f32_16x16x32_bf16 v[188:191], v[136:139], a[176:179], v[188:191]\n"
      "v_mul_f32 v158, v249, v158\n"
      "v_mul_f32 v159, v249, v159\n"
      "v_mfma_f32_16x16x32_bf16 v[188:191], v[250:253], a[212:215], v[188:191]\n"
      "v_cvt_pk_bf16_f32 v175, v158, v159\n"
      "v_mul_f32 v180, v249, v180\n"
      "v_mfma_f32_16x16x32_bf16 v[136:139], a[0:3], v[164:167], 0\n"
      "v_mul_f32 v181, v249, v181\n"
      "v_cvt_pk_bf16_f32 v32, v180, v181\n"
      "v_mfma_f32_16x16x32_bf16 v[136:139], a[36:39], v[148:151], v[136:139]\n"
      "v_mul_f32 v182, v249, v182\n"
      "v_mul_f32_e64 v183, v249, v183\n"
      "v_mfma_f32_16x16x32_bf16 v[248:251], a[4:7], v[164:167], 0\n"
      "v_cvt_pk_bf16_f32 v33, v182, v183\n"
      "v_cvt_pk_bf16_f32 v220, v220, v221\n"
      "v_mfma_f32_16x16x32_bf16 v[248:251], a[40:43], v[148:151], v[248:251]\n"
      "v_cvt_pk_bf16_f32 v221, v222, v223\n"
      "v_cvt_pk_bf16_f32 v222, v204, v205\n"
      "v_mfma_f32_16x16x32_bf16 v[252:255], a[8:11], v[164:167], 0\n"
      "v_cvt_pk_bf16_f32 v223, v206, v207\n"
      "s_waitcnt lgkmcnt(0)\n"
      "v_mfma_f32_16x16x32_bf16 v[252:255], a[44:47], v[148:151], v[252:255]\n"
      "v_cvt_pk_bf16_f32 v160, v160, v161\n"
      "v_cvt_pk_bf16_f32 v161, v162, v163\n"
      "v_mfma_f32_16x16x32_bf16 v[156:159], a[12:15], v[164:167], 0\n"
      "ds_read_b128 v[180:183], v119 offset:24576\n"
      "v_cvt_pk_bf16_f32 v162, v152, v153\n"
      "v_mfma_f32_16x16x32_bf16 v[156:159], a[48:51], v[148:151], v[156:159]\n"
      "v_cvt_pk_bf16_f32 v163, v154, v155\n"
      "v_cvt_pk_bf16_f32 v208, v208, v209\n"
      "v_mfma_f32_16x16x32_bf16 v[140:143], a[16:19], v[164:167], 0\n"
      "v_cvt_pk_bf16_f32 v209, v210, v211\n"
      "v_cvt_pk_bf16_f32 v210, v188, v189\n"
      "v_mfma_f32_16x16x32_bf16 v[140:143], a[52:55], v[148:151], v[140:143]\n"
      "v_cvt_pk_bf16_f32 v211, v190, v191\n"
      "v_fma_f32 v207, v176, v176, s38\n"
      "v_mfma_f32_16x16x32_bf16 v[188:191], a[20:23], v[164:167], 0\n"
      "v_fma_f32 v205, v168, v168, s38\n"
      "v_fmac_f32 v207, v177, v177\n"
      "v_mfma_f32_16x16x32_bf16 v[188:191], a[56:59], v[148:151], v[188:191]\n"
      "v_fmac_f32 v205, v169, v169\n"
      "v_fmac_f32 v207, v178, v178\n"
      "v_mfma_f32_16x16x32_bf16 v[152:155], v[164:167], a[24:27], 0\n"
      "v_fmac_f32 v205, v170, v170\n"
      "v_fmac_f32 v207, v179, v179\n"
      "v_mfma_f32_16x16x32_bf16 v[152:155], v[148:151], a[60:63], v[152:155]\n"
      "v_fmac_f32 v205, v171, v171\n"
      "v_fmac_f32 v207, v212, v212\n"
      "v_mfma_f32_16x16x32_bf16 v[216:219], v[164:167], a[28:31], 0\n"
      "v_fmac_f32 v205, v200, v200\n"
      "v_fmac_f32 v207, v213, v213\n"
      "v_mfma_f32_16x16x32_bf16 v[216:219], v[148:151], a[64:67], v[216:219]\n"
      "v_fmac_f32 v205, v201, v201\n"
      "v_fmac_f32_e64 v207, v214, v214\n"
      "v_mfma_f32_16x16x32_bf16 v[236:239], v[164:167], a[32:35], 0\n"
      "ds_read_b128 v[164:167], v114 offset:16384\n"
      "v_fmac_f32 v205, v202, v202\n"
      "v_mfma_f32_16x16x32_bf16 v[236:239], v[148:151], a[68:71], v[236:239]\n"
      "ds_read_b128 v[148:151], v115 offset:16384\n"
      "v_fmac_f32 v207, v215, v215\n"
      "v_mfma_f32_16x16x32_bf16 v[136:139], a[72:75], v[196:199], v[136:139]\n"
      "s_waitcnt lgkmcnt(2)\n"
      "v_fmac_f32 v205, v203, v203\n"
      "v_fmac_f32 v207, v244, v244\n"
      "v_mfma_f32_16x16x32_bf16 v[136:139], a[108:111], v[232:235], v[136:139]\n"
      "global_store_dwordx4 v116, v[180:183], s[54:55] offset:128 sc1\n"
      "v_mfma_f32_16x16x32_bf16 v[248:251], a[76:79], v[196:199], v[248:251]\n"
      "v_fmac_f32 v205, v192, v192\n"
      "v_fmac_f32 v207, v245, v245\n"
      "v_mfma_f32_16x16x32_bf16 v[248:251], a[112:115], v[232:235], v[248:251]\n"
      "v_fmac_f32 v205, v193, v193\n"
      "v_fmac_f32 v207, v246, v246\n"
      "v_mfma_f32_16x16x32_bf16 v[252:255], a[80:83], v[196:199], v[252:255]\n"
      "v_fmac_f32 v205, v194, v194\n"
      "v_fmac_f32 v207, v247, v247\n"
      "v_mfma_f32_16x16x32_bf16 v[252:255], a[116:119], v[232:235], v[252:255]\n"
      "v_fmac_f32 v205, v195, v195\n"
      "v_mfma_f32_16x16x32_bf16 v[156:159], a[84:87], v[196:199], v[156:159]\n"
      "s_nop 0\n"
      "v_permlane16_swap_b32 v207, v205\n"
      "v_mfma_f32_16x16x32_bf16 v[156:159], a[120:123], v[232:235], v[156:159]\n"
      "v_add_f32 v207, v207, v205\n"
      "v_mov_b32 v205, v207\n"
      "v_mfma_f32_16x16x32_bf16 v[140:143], a[88:91], v[196:199], v[140:143]\n"
      "s_nop 0\n"
      "v_permlane32_swap_b32 v207, v205\n"
      "v_mfma_f32_16x16x32_bf16 v[140:143], a[124:127], v[232:235], v[140:143]\n"
      "s_waitcnt lgkmcnt(0)\n"
      "v_add_f32 v207, v207, v205\n"
      "v_rsq_f32 v207, v207\n"
      "v_mfma_f32_16x16x32_bf16 v[188:191], a[92:95], v[196:199], v[188:191]\n"
      "v_mov_b32 v205, v207\n"
      "v_mfma_f32_16x16x32_bf16 v[188:191], a[128:131], v[232:235], v[188:191]\n"
      "s_nop 0\n"
      "v_permlane16_swap_b32 v207, v205\n"
      "v_mfma_f32_16x16x32_bf16 v[152:155], v[196:199], a[96:99], v[152:155]\n"
      "v_mul_f32 v168, v205, v168\n"
      "v_mul_f32 v169, v205, v169\n"
      "v_mfma_f32_16x16x32_bf16 v[152:155], v[232:235], a[132:135], v[152:155]\n"
      "ds_read_b128 v[144:147], v132 offset:32\n"
      "v_cvt_pk_bf16_f32 v168, v168, v169\n"
      "v_mfma_f32_16x16x32_bf16 v[216:219], v[196:199], a[100:103], v[216:219]\n"
      "v_mul_f32 v170, v205, v170\n"
      "v_mul_f32 v171, v205, v171\n"
      "v_mfma_f32_16x16x32_bf16 v[216:219], v[232:235], a[136:139], v[216:219]\n"
      "ds_read_b64 v[230:231], v132 offset:24\n"
      "v_cvt_pk_bf16_f32 v169, v170, v171\n"
      "v_mfma_f32_16x16x32_bf16 v[236:239], v[196:199], a[104:107], v[236:239]\n"
      "ds_read_b128 v[196:199], v133\n"
      "v_mul_f32 v200, v205, v200\n"
      "v_mfma_f32_16x16x32_bf16 v[236:239], v[232:235], a[140:143], v[236:239]\n"
      "ds_read_b64 v[232:233], v133 offset:16\n"
      "v_mul_f32 v201, v205, v201\n"
      "v_mfma_f32_16x16x32_bf16 v[136:139], a[144:147], v[164:167], v[136:139]\n"
      "ds_read_u16 v52, v134\n"
      "v_cvt_pk_bf16_f32 v170, v200, v201\n"
      "v_mfma_f32_16x16x32_bf16 v[136:139], a[180:183], v[148:151], v[136:139]\n"
      "ds_read_u16 v56, v134 offset:32\n"
      "v_mul_f32 v202, v205, v202\n"
      "v_mfma_f32_16x16x32_bf16 v[248:251], a[148:151], v[164:167], v[248:251]\n"
      "ds_read_u16 v60, v134 offset:64\n"
      "v_mul_f32 v203, v205, v203\n"
      "v_mfma_f32_16x16x32_bf16 v[248:251], a[184:187], v[148:151], v[248:251]\n"
      "ds_read_b128 v[240:243], v123 offset:18432\n"
      "v_cvt_pk_bf16_f32 v171, v202, v203\n"
      "v_mfma_f32_16x16x32_bf16 v[252:255], a[152:155], v[164:167], v[252:255]\n"
      "v_mul_f32 v192, v205, v192\n"
      "v_mul_f32 v193, v205, v193\n"
      "v_mfma_f32_16x16x32_bf16 v[252:255], a[188:191], v[148:151], v[252:255]\n"
      "v_cvt_pk_bf16_f32 v40, v192, v193\n"
      "v_mul_f32_e64 v194, v205, v194\n"
      "v_mfma_f32_16x16x32_bf16 v[156:159], a[156:159], v[164:167], v[156:159]\n"
      "ds_read_b128 v[200:203], v123 offset:21504\n"
      "v_mul_f32 v195, v205, v195\n"
      "v_mfma_f32_16x16x32_bf16 v[156:159], a[192:195], v[148:151], v[156:159]\n"
      "v_cvt_pk_bf16_f32 v41, v194, v195\n"
      "v_mul_f32 v207, s36, v207\n"
      "v_mfma_f32_16x16x32_bf16 v[140:143], a[160:163], v[164:167], v[140:143]\n"
      "v_mul_f32 v176, v207, v176\n"
      "v_mul_f32 v177, v207, v177\n"
      "v_mfma_f32_16x16x32_bf16 v[140:143], a[196:199], v[148:151], v[140:143]\n"
      "v_cvt_pk_bf16_f32 v176, v176, v177\n"
      "v_mul_f32 v178, v207, v178\n"
      "v_mfma_f32_16x16x32_bf16 v[188:191], a[164:167], v[164:167], v[188:191]\n"
      "v_mul_f32 v179, v207, v179\n"
      "v_cvt_pk_bf16_f32 v177, v178, v179\n"
      "v_mfma_f32_16x16x32_bf16 v[188:191], a[200:203], v[148:151], v[188:191]\n"
      "v_mul_f32 v212, v207, v212\n"
      "v_mul_f32 v213, v207, v213\n"
      "v_mfma_f32_16x16x32_bf16 v[152:155], v[164:167], a[168:171], v[152:155]\n"
      "v_cvt_pk_bf16_f32 v178, v212, v213\n"
      "v_mul_f32_e64 v214, v207, v214\n"
      "v_mfma_f32_16x16x32_bf16 v[152:155], v[148:151], a[204:207], v[152:155]\n"
      "ds_read_b128 v[192:195], v123 offset:24576\n"
      "v_mul_f32_e64 v215, v207, v215\n"
      "v_mfma_f32_16x16x32_bf16 v[216:219], v[164:167], a[172:175], v[216:219]\n"
      "v_cvt_pk_bf16_f32 v179, v214, v215\n"
      "v_mul_f32 v244, v207, v244\n"
      "v_mfma_f32_16x16x32_bf16 v[216:219], v[148:151], a[208:211], v[216:219]\n"
      "v_mul_f32_e64 v245, v207, v245\n"
      "s_waitcnt lgkmcnt(1)\n"
      "v_mfma_f32_16x16x32_bf16 v[236:239], v[164:167], a[176:179], v[236:239]\n"
      "ds_read_b128 v[164:167], v123 offset:27648\n"
      "v_cvt_pk_bf16_f32 v38, v244, v245\n"
      "v_mfma_f32_16x16x32_bf16 v[236:239], v[148:151], a[212:215], v[236:239]\n"
      "v_mul_f32 v246, v207, v246\n"
      "v_mul_f32 v247, v207, v247\n"
      "v_mfma_f32_16x16x32_bf16 v[212:215], a[216:219], v[240:243], 0\n"
      "v_cvt_pk_bf16_f32 v39, v246, v247\n"
      "v_add_u32_e64 v135, 32, v135\n"
      "v_mfma_f32_16x16x32_bf16 v[212:215], a[228:231], v[200:203], v[212:215]\n"
      "v_fma_f32 v234, v136, v136, s38\n"
      "v_fma_f32 v235, v156, v156, s38\n"
      "v_mfma_f32_16x16x32_bf16 v[244:247], a[220:223], v[240:243], 0\n"
      "v_fmac_f32 v234, v137, v137\n"
      "v_fmac_f32 v235, v157, v157\n"
      "v_mfma_f32_16x16x32_bf16 v[244:247], a[232:235], v[200:203], v[244:247]\n"
      "v_fmac_f32 v234, v138, v138\n"
      "v_fmac_f32 v235, v158, v158\n"
      "v_fmac_f32_e64 v234, v139, v139\n"
      "v_mfma_f32_16x16x32_bf16 v[180:183], a[224:227], v[240:243], 0\n"
      "v_fmac_f32 v235, v159, v159\n"
      "v_fmac_f32 v234, v248, v248\n"
      "v_mfma_f32_16x16x32_bf16 v[180:183], a[236:239], v[200:203], v[180:183]\n"
      "ds_read_b128 v[148:151], v123 offset:30720\n"
      "v_fmac_f32 v235, v140, v140\n"
      "v_fmac_f32 v234, v249, v249\n"
      "v_fmac_f32 v235, v141, v141\n"
      "v_fmac_f32 v234, v250, v250\n"
      "v_fmac_f32 v235, v142, v142\n"
      "v_fmac_f32_e64 v234, v251, v251\n"
      "s_waitcnt lgkmcnt(1)\n"
      "v_mfma_f32_16x16x32_bf16 v[212:215], a[240:243], v[192:195], v[212:215]\n"
      "v_fmac_f32 v235, v143, v143\n"
      "v_fmac_f32 v234, v252, v252\n"
      "v_mfma_f32_16x16x32_bf16 v[212:215], a[252:255], v[164:167], v[212:215]\n"
      "ds_read_b128 v[200:203], v123 offset:33792\n"
      "v_fmac_f32 v235, v188, v188\n"
      "v_mfma_f32_16x16x32_bf16 v[244:247], a[244:247], v[192:195], v[244:247]\n"
      "v_fmac_f32 v234, v253, v253\n"
      "v_fmac_f32 v235, v189, v189\n"
      "v_mfma_f32_16x16x32_bf16 v[244:247], v[0:3], v[164:167], v[244:247]\n"
      "v_fmac_f32 v234, v254, v254\n"
      "v_fmac_f32 v235, v190, v190\n"
      "v_mfma_f32_16x16x32_bf16 v[180:183], a[248:251], v[192:195], v[180:183]\n"
      "v_fmac_f32 v234, v255, v255\n"
      "v_fmac_f32 v235, v191, v191\n"
      "v_mfma_f32_16x16x32_bf16 v[180:183], v[4:7], v[164:167], v[180:183]\n"
      "s_nop 0\n"
      "v_permlane16_swap_b32 v234, v235\n"
      "v_mfma_f32_16x16x32_bf16 v[164:167], v[184:187], v[172:175], v[78:81]\n"
      "v_add_f32 v234, v234, v235\n"
      "v_mov_b32 v235, v234\n"
      "v_mfma_f32_16x16x32_bf16 v[164:167], v[34:37], v[32:35], v[164:167]\n"
      "s_nop 0\n"
      "v_permlane32_swap_b32 v234, v235\n"
      "v_add_f32 v234, v234, v235\n"
      "v_rsq_f32 v234, v234\n"
      "s_nop 0\n"
      "v_mov_b32 v235, v234\n"
      "s_nop 1\n"
      "v_permlane16_swap_b32 v234, v235\n"
      "v_mul_f32 v156, v235, v156\n"
      "v_mul_f32 v157, v235, v157\n"
      "s_waitcnt lgkmcnt(0)\n"
      "v_mfma_f32_16x16x32_bf16 v[212:215], v[8:11], v[148:151], v[212:215]\n"
      "v_cvt_pk_bf16_f32 v156, v156, v157\n"
      "v_mul_f32_e64 v158, v235, v158\n"
      "v_mfma_f32_16x16x32_bf16 v[212:215], v[20:23], v[200:203], v[212:215]\n"
      "v_mul_f32 v159, v235, v159\n"
      "v_cvt_pk_bf16_f32 v157, v158, v159\n"
      "v_mfma_f32_16x16x32_bf16 v[244:247], v[12:15], v[148:151], v[244:247]\n"
      "v_mul_f32 v140, v235, v140\n"
      "v_mul_f32 v141, v235, v141\n"
      "v_mfma_f32_16x16x32_bf16 v[244:247], v[24:27], v[200:203], v[244:247]\n"
      "v_cvt_pk_bf16_f32 v158, v140, v141\n"
      "v_mul_f32 v142, v235, v142\n"
      "v_mfma_f32_16x16x32_bf16 v[180:183], v[16:19], v[148:151], v[180:183]\n"
      "v_mul_f32 v143, v235, v143\n"
      "v_cvt_pk_bf16_f32 v159, v142, v143\n"
      "v_mfma_f32_16x16x32_bf16 v[180:183], v[28:31], v[200:203], v[180:183]\n"
      "v_mul_f32 v188, v235, v188\n"
      "v_mul_f32_e64 v189, v235, v189\n"
      "v_mfma_f32_16x16x32_bf16 v[140:143], v[168:171], v[172:175], v[82:85]\n"
      "v_cvt_pk_bf16_f32 v46, v188, v189\n"
      "v_mul_f32 v190, v235, v190\n"
      "v_mfma_f32_16x16x32_bf16 v[140:143], v[40:43], v[32:35], v[140:143]\n"
      "v_mul_f32 v191, v235, v191\n"
      "v_cvt_pk_bf16_f32 v47, v190, v191\n"
      "v_mfma_f32_16x16x32_bf16 v[200:203], v[144:147], v[172:175], v[70:73]\n"
      "v_mul_f32 v234, s36, v234\n"
      "v_mul_f32 v136, v234, v136\n"
      "v_mfma_f32_16x16x32_bf16 v[200:203], v[196:199], v[184:187], v[200:203]\n"
      "s_waitcnt vmcnt(2)\n"
      "v_mul_f32 v137, v234, v137\n"
      "v_cvt_pk_bf16_f32 v136, v136, v137\n"
      "v_mfma_f32_16x16x32_bf16 v[200:203], v[230:233], v[32:35], v[200:203]\n"
      "ds_read_b128 v[148:151], v119 offset:41728\n"
      "v_mul_f32 v138, v234, v138\n"
      "v_mfma_f32_16x16x32_bf16 v[192:195], v[156:159], v[172:175], v[86:89]\n"
      "ds_read_b128 v[172:175], v122 offset:4864\n"
      "v_mul_f32 v139, v234, v139\n"
      "v_mfma_f32_16x16x32_bf16 v[192:195], v[46:49], v[32:35], v[192:195]\n"
      "v_cvt_pk_bf16_f32 v137, v138, v139\n"
      "v_mul_f32 v248, v234, v248\n"
      "v_mul_f32 v249, v234, v249\n"
      "v_cvt_pk_bf16_f32 v138, v248, v249\n"
      "v_mul_f32 v250, v234, v250\n"
      "v_mul_f32 v251, v234, v251\n"
      "v_cvt_pk_bf16_f32 v139, v250, v251\n"
      "v_mul_f32 v252, v234, v252\n"
      "v_mul_f32 v253, v234, v253\n"
      "v_cvt_pk_bf16_f32 v44, v252, v253\n"
      "v_mul_f32 v254, v234, v254\n"
      "v_mul_f32 v255, v234, v255\n"
      "v_cvt_pk_bf16_f32 v45, v254, v255\n"
      "v_cvt_pk_bf16_f32 v50, v152, v153\n"
      "v_cvt_pk_bf16_f32 v51, v154, v155\n"
      "v_cvt_pk_bf16_f32 v54, v216, v217\n"
      "v_cvt_pk_bf16_f32 v55, v218, v219\n"
      "v_cvt_pk_bf16_f32 v58, v236, v237\n"
      "v_cvt_pk_bf16_f32 v59, v238, v239\n"
      "v_fma_f32 v253, v212, v212, s39\n"
      "v_fmac_f32 v253, v213, v213\n"
      "v_fmac_f32 v253, v214, v214\n"
      "v_fmac_f32 v253, v215, v215\n"
      "v_fmac_f32 v253, v244, v244\n"
      "v_fmac_f32 v253, v245, v245\n"
      "v_fmac_f32 v253, v246, v246\n"
      "v_fmac_f32 v253, v247, v247\n"
      "v_fmac_f32 v253, v180, v180\n"
      "v_fmac_f32 v253, v181, v181\n"
      "v_fmac_f32 v253, v182, v182\n"
      "v_fmac_f32 v253, v183, v183\n"
      "s_waitcnt lgkmcnt(0)\n"
      "v_or3_b32 v148, v148, v149, v150\n"
      "s_mov_b64 exec, 0xfff\n"
      "v_or3_b32 v148, v148, v172, v173\n"
      "v_or3_b32 v148, v148, v174, v175\n"
      "s_mov_b64 exec, -1\n"
      "v_bitop3_b32 v148, v148, v151, s40 bitop3:0xa8\n"
      "v_or_b32 v148, s4, v148\n"
      "ds_write_b32 v129, v148 offset:3072\n"
      "s_or_b64 s[34:35], s[48:49], s[50:51]\n"
      "s_cbranch_scc1 L_gen_30_%=\n"
      "L_fast_33_%=:\n"
      "v_mfma_f32_16x16x32_bf16 v[188:191], v[184:187], v[176:179], v[90:93]\n"
      "ds_read_b128 v[172:175], v123 offset:18688\n"
      "v_mfma_f32_16x16x32_bf16 v[188:191], v[34:37], v[38:41], v[188:191]\n"
      "ds_read_b128 v[240:243], v123 offset:21760\n"
      "v_exp_f32_e64 v164, v164\n"
      "v_mfma_f32_16x16x32_bf16 v[224:227], v[168:171], v[176:179], v[94:97]\n"
      "ds_read_b128 v[152:155], v119 offset:30720\n"
      "v_mfma_f32_16x16x32_bf16 v[224:227], v[40:43], v[38:41], v[224:227]\n"
      "ds_read_b128 v[202:205], v128 offset:3072\n"
      "v_exp_f32_e64 v165, v165\n"
      "v_mfma_f32_16x16x32_bf16 v[234:237], v[144:147], v[176:179], v[72:75]\n"
      "v_exp_f32 v166, v166\n"
      "v_mfma_f32_16x16x32_bf16 v[234:237], v[196:199], v[168:171], v[234:237]\n"
      "v_cvt_pk_bf16_f32 v164, v164, v165\n"
      "v_exp_f32 v167, v167\n"
      "v_mfma_f32_16x16x32_bf16 v[234:237], v[230:233], v[38:41], v[234:237]\n"
      "v_exp_f32_e64 v140, v140\n"
      "v_mfma_f32_16x16x32_bf16 v[216:219], v[156:159], v[176:179], v[98:101]\n"
      "v_cvt_pk_bf16_f32 v165, v166, v167\n"
      "v_mfma_f32_16x16x32_bf16 v[216:219], v[46:49], v[38:41], v[216:219]\n"
      "v_exp_f32 v141, v141\n"
      "v_exp_f32 v142, v142\n"
      "v_cvt_pk_bf16_f32 v166, v140, v141\n"
      "v_exp_f32 v143, v143\n"
      "s_waitcnt lgkmcnt(1)\n"
      "v_mfma_f32_16x16x32_bf16 v[148:151], a[216:219], v[172:175], 0\n"
      "ds_read_b128 v[176:179], v123 offset:24832\n"
      "v_cvt_pk_bf16_f32 v167, v142, v143\n"
      "v_mfma_f32_16x16x32_bf16 v[148:151], a[228:231], v[240:243], v[148:151]\n"
      "ds_read_b128 v[236:239], v123 offset:27904\n"
      "v_exp_f32 v192, v192\n"
      "v_mfma_f32_16x16x32_bf16 v[248:251], a[220:223], v[172:175], 0\n"
      "v_exp_f32 v193, v193\n"
      "v_mfma_f32_16x16x32_bf16 v[248:251], a[232:235], v[240:243], v[248:251]\n"
      "global_store_dwordx4 v116, v[152:155], s[54:55] offset:256 sc1\n"
      "v_mfma_f32_16x16x32_bf16 v[140:143], a[224:227], v[172:175], 0\n"
      "s_waitcnt lgkmcnt(2)\n"
      "v_exp_f32 v194, v194\n"
      "v_mfma_f32_16x16x32_bf16 v[140:143], a[236:239], v[240:243], v[140:143]\n"
      "v_exp_f32 v195, v195\n"
      "v_exp_f32 v200, v200\n"
      "v_cvt_pk_bf16_f32 v62, v192, v193\n"
      "v_cvt_pk_bf16_f32 v63, v194, v195\n"
      "v_cvt_pk_bf16_f32 v64, v200, 0\n"
      "v_bitop3_b32 v202, v202, v203, s4 bitop3:0x7e\n"
      "v_bitop3_b32 v204, v204, v205, s4 bitop3:0x7e\n"
      "v_or_b32 v202, v202, v204\n"
      "v_cmp_ne_u32 vcc, 0, v202\n"
      "v_exp_f32 v188, v188\n"
      "v_exp_f32 v189, v189\n"
      "v_exp_f32 v190, v190\n"
      "v_cvt_pk_bf16_f32 v188, v188, v189\n"
      "v_exp_f32_e64 v191, v191\n"
      "s_waitcnt lgkmcnt(0)\n"
      "v_mfma_f32_16x16x32_bf16 v[148:151], a[240:243], v[176:179], v[148:151]\n"
      "ds_read_b128 v[152:155], v123 offset:30976\n"
      "v_cvt_pk_bf16_f32 v189, v190, v191\n"
      "v_mfma_f32_16x16x32_bf16 v[148:151], a[252:255], v[236:239], v[148:151]\n"
      "ds_read_b128 v[172:175], v123 offset:34048\n"
      "v_exp_f32 v224, v224\n"
      "v_mfma_f32_16x16x32_bf16 v[248:251], a[244:247], v[176:179], v[248:251]\n"
      "s_mov_b64 s[48:49], vcc\n"
      "v_exp_f32 v225, v225\n"
      "v_mfma_f32_16x16x32_bf16 v[248:251], v[0:3], v[236:239], v[248:251]\n"
      "v_exp_f32 v226, v226\n"
      "v_mfma_f32_16x16x32_bf16 v[140:143], a[248:251], v[176:179], v[140:143]\n"
      "ds_read_b128 v[192:195], v123 offset:18944\n"
      "v_cvt_pk_bf16_f32 v190, v224, v225\n"
      "v_mfma_f32_16x16x32_bf16 v[140:143], v[4:7], v[236:239], v[140:143]\n"
      "s_cbranch_vccnz L_flg_34_%=\n"
      "L_fb_35_%=:\n"
      "v_mfma_f32_16x16x32_bf16 v[240:243], v[66:69], v[164:167], 0\n"
      "ds_read_b128 v[204:207], v123 offset:22016\n"
      "v_mfma_f32_16x16x32_bf16 v[240:243], v[66:69], v[62:65], v[240:243]\n"
      "v_exp_f32 v227, v227\n"
      "v_mfma_f32_16x16x32_bf16 v[176:179], v[220:223], v[164:167], 0\n"
      "v_exp_f32 v216, v216\n"
      "v_mfma_f32_16x16x32_bf16 v[176:179], v[50:53], v[62:65], v[176:179]\n"
      "v_cvt_pk_bf16_f32 v191, v226, v227\n"
      "v_exp_f32 v217, v217\n"
      "v_exp_f32 v218, v218\n"
      "s_waitcnt lgkmcnt(2)\n"
      "v_mfma_f32_16x16x32_bf16 v[148:151], v[8:11], v[152:155], v[148:151]\n"
      "v_exp_f32 v219, v219\n"
      "v_mfma_f32_16x16x32_bf16 v[148:151], v[20:23], v[172:175], v[148:151]\n"
      "v_exp_f32 v234, v234\n"
      "v_mfma_f32_16x16x32_bf16 v[248:251], v[12:15], v[152:155], v[248:251]\n"
      "v_rcp_f32 v240, v240\n"
      "v_mfma_f32_16x16x32_bf16 v[248:251], v[24:27], v[172:175], v[248:251]\n"
      "v_mul_f32 v176, v240, v176\n"
      "v_mul_f32_e64 v177, v240, v177\n"
      "v_mfma_f32_16x16x32_bf16 v[140:143], v[16:19], v[152:155], v[140:143]\n"
      "v_cvt_pk_bf16_f32 v176, v176, v177\n"
      "v_mul_f32 v178, v240, v178\n"
      "v_mfma_f32_16x16x32_bf16 v[140:143], v[28:31], v[172:175], v[140:143]\n"
      "v_mul_f32 v179, v240, v179\n"
      "v_cvt_pk_bf16_f32 v177, v178, v179\n"
      "v_mfma_f32_16x16x32_bf16 v[152:155], v[160:163], v[164:167], 0\n"
      "v_mfma_f32_16x16x32_bf16 v[152:155], v[54:57], v[62:65], v[152:155]\n"
      "v_cndmask_b32 v201, v201, v235, s[42:43]\n"
      "v_mfma_f32_16x16x32_bf16 v[172:175], v[208:211], v[164:167], 0\n"
      "ds_read_b128 v[164:167], v120 offset:18432\n"
      "v_mfma_f32_16x16x32_bf16 v[172:175], v[58:61], v[62:65], v[172:175]\n"
      "v_cvt_pk_bf16_f32 v62, v216, v217\n"
      "v_fma_f32 v252, v148, v148, s39\n"
      "v_mfma_f32_16x16x32_bf16 v[224:227], v[184:187], v[136:139], v[102:105]\n"
      "v_fmac_f32 v252, v149, v149\n"
      "v_fmac_f32 v252, v150, v150\n"
      "v_mfma_f32_16x16x32_bf16 v[224:227], v[34:37], v[44:47], v[224:227]\n"
      "v_fmac_f32 v252, v151, v151\n"
      "v_fmac_f32 v252, v248, v248\n"
      "v_mfma_f32_16x16x32_bf16 v[236:239], v[168:171], v[136:139], v[106:109]\n"
      "v_fmac_f32 v252, v249, v249\n"
      "v_fmac_f32 v252, v250, v250\n"
      "v_mfma_f32_16x16x32_bf16 v[236:239], v[40:43], v[44:47], v[236:239]\n"
      "v_fmac_f32 v252, v251, v251\n"
      "v_fmac_f32 v252, v140, v140\n"
      "v_mfma_f32_16x16x32_bf16 v[168:171], v[144:147], v[136:139], v[74:77]\n"
      "s_waitcnt lgkmcnt(1)\n"
      "v_fmac_f32 v252, v141, v141\n"
      "v_fmac_f32_e64 v252, v142, v142\n"
      "v_mfma_f32_16x16x32_bf16 v[168:171], v[196:199], v[156:159], v[168:171]\n"
      "v_fmac_f32_e64 v252, v143, v143\n"
      "v_cvt_pk_bf16_f32 v63, v218, v219\n"
      "v_mfma_f32_16x16x32_bf16 v[168:171], v[230:233], v[44:47], v[168:171]\n"
      "ds_read_b128 v[184:187], v123 offset:25088\n"
      "v_cvt_pk_bf16_f32 v64, v234, 0\n"
      "v_mul_f32 v152, v240, v152\n"
      "v_mul_f32 v153, v240, v153\n"
      "v_cvt_pk_bf16_f32 v178, v152, v153\n"
      "v_mfma_f32_16x16x32_bf16 v[216:219], v[156:159], v[136:139], v[110:113]\n"
      "ds_read_b128 v[136:139], v123 offset:28160\n"
      "v_mul_f32_e64 v154, v240, v154\n"
      "v_mfma_f32_16x16x32_bf16 v[216:219], v[46:49], v[44:47], v[216:219]\n"
      "ds_read_b128 v[156:159], v121 offset:18432\n"
      "v_mul_f32_e64 v155, v240, v155\n"
      "v_cvt_pk_bf16_f32 v179, v154, v155\n"
      "v_mul_f32_e64 v172, v240, v172\n"
      "v_mul_f32 v173, v240, v173\n"
      "v_cvt_pk_bf16_f32 v172, v172, v173\n"
      "v_mul_f32_e64 v174, v240, v174\n"
      "v_mul_f32_e64 v175, v240, v175\n"
      "v_cvt_pk_bf16_f32 v173, v174, v175\n"
      "v_mfma_f32_16x16x32_bf16 v[152:155], v[196:199], v[144:147], v[76:79]\n"
      "ds_write_b128 v124, v[176:179]\n"
      "v_mfma_f32_16x16x32_bf16 v[240:243], v[66:69], v[188:191], 0\n"
      "s_waitcnt lgkmcnt(4)\n"
      "v_cndmask_b32 v201, v201, v169, s[44:45]\n"
      "v_mfma_f32_16x16x32_bf16 v[240:243], v[66:69], v[62:65], v[240:243]\n"
      "global_store_dwordx4 v117, v[164:167], s[54:55] sc1\n"
      "v_mfma_f32_16x16x16_bf16 v[152:155], v[232:233], v[230:231], v[152:155]\n"
      "ds_write_b64 v125, v[172:173]\n"
      "v_mfma_f32_16x16x32_bf16 v[232:235], v[220:223], v[188:191], 0\n"
      "v_exp_f32 v224, v224\n"
      "v_mfma_f32_16x16x32_bf16 v[232:235], v[50:53], v[62:65], v[232:235]\n"
      "s_waitcnt lgkmcnt(2)\n"
      "v_exp_f32 v225, v225\n"
      "v_mfma_f32_16x16x32_bf16 v[228:231], v[160:163], v[188:191], 0\n"
      "s_nop 0\n"
      "v_cndmask_b32 v201, v201, v153, s[46:47]\n"
      "v_cvt_pk_bf16_f32 v224, v224, v225\n"
      "v_mfma_f32_16x16x32_bf16 v[228:231], v[54:57], v[62:65], v[228:231]\n"
      "v_exp_f32_e64 v201, v201\n"
      "v_mfma_f32_16x16x32_bf16 v[144:147], v[208:211], v[188:191], 0\n"
      "v_cvt_pk_bf16_f32 v201, v201, v201\n"
      "v_mfma_f32_16x16x32_bf16 v[144:147], v[58:61], v[62:65], v[144:147]\n"
      "ds_write_b16 v130, v201\n"
      "v_mfma_f32_16x16x32_bf16 v[196:199], a[216:219], v[192:195], 0\n"
      "ds_read_b128 v[188:191], v131\n"
      "v_mfma_f32_16x16x32_bf16 v[196:199], a[228:231], v[204:207], v[196:199]\n"
      "ds_read_b128 v[152:155], v131 offset:64\n"
      "v_exp_f32 v226, v226\n"
      "v_mfma_f32_16x16x32_bf16 v[176:179], a[220:223], v[192:195], 0\n"
      "v_exp_f32 v227, v227\n"
      "v_mfma_f32_16x16x32_bf16 v[176:179], a[232:235], v[204:207], v[176:179]\n"
      "s_mov_b32 exec_hi, 0\n"
      "global_store_dwordx4 v118, v[156:159], s[54:55] sc1\n"
      "s_mov_b32 exec_hi, -1\n"
      "v_mfma_f32_16x16x32_bf16 v[200:203], a[224:227], v[192:195], 0\n"
      "ds_read_b128 v[172:175], v123 offset:31232\n"
      "v_cvt_pk_bf16_f32 v225, v226, v227\n"
      "v_mfma_f32_16x16x32_bf16 v[200:203], a[236:239], v[204:207], v[200:203]\n"
      "ds_read_b128 v[156:159], v123 offset:34304\n"
      "v_exp_f32 v236, v236\n"
      "v_mfma_f32_16x16x32_bf16 v[196:199], a[240:243], v[184:187], v[196:199]\n"
      "ds_read_b128 v[164:167], v114 offset:18432\n"
      "s_add_u32 s28, s27, 2\n"
      "v_mfma_f32_16x16x32_bf16 v[196:199], a[252:255], v[136:139], v[196:199]\n"
      "s_min_u32 m0, s28, s3\n"
      "v_exp_f32 v237, v237\n"
      "v_mfma_f32_16x16x32_bf16 v[176:179], a[244:247], v[184:187], v[176:179]\n"
      "s_movrels_b32 s9, s80\n"
      "v_exp_f32 v238, v238\n"
      "v_mfma_f32_16x16x32_bf16 v[176:179], v[0:3], v[136:139], v[176:179]\n"
      "s_lshr_b32 s8, s9, 8\n"
      "v_cvt_pk_bf16_f32 v226, v236, v237\n"
      "v_exp_f32_e64 v239, v239\n"
      "s_waitcnt lgkmcnt(3)\n"
      "v_mfma_f32_16x16x32_bf16 v[200:203], a[248:251], v[184:187], v[200:203]\n"
      "ds_read_b128 v[184:187], v115 offset:18432\n"
      "v_cvt_pk_bf16_f32 v227, v238, v239\n"
      "v_mfma_f32_16x16x32_bf16 v[200:203], v[4:7], v[136:139], v[200:203]\n"
      "s_and_b32 s9, s9, 0xff\n"
      "v_exp_f32 v216, v216\n"
      "v_mfma_f32_16x16x32_bf16 v[204:207], v[188:191], v[66:69], 0\n"
      "s_mul_i32 s10, s8, 73\n"
      "v_exp_f32 v217, v217\n"
      "v_mfma_f32_16x16x32_bf16 v[204:207], v[152:155], v[66:69], v[204:207]\n"
      "s_add_u32 s10, s10, s9\n"
      "v_exp_f32_e64 v218, v218\n"
      "s_waitcnt lgkmcnt(2)\n"
      "v_mfma_f32_16x16x32_bf16 v[196:199], v[8:11], v[172:175], v[196:199]\n"
      "s_mul_i32 s28, s10, 4802\n"
      "v_exp_f32 v219, v219\n"
      "v_mfma_f32_16x16x32_bf16 v[196:199], v[20:23], v[156:159], v[196:199]\n"
      "s_add_u32 s56, s18, s28\n"
      "v_exp_f32_e64 v168, v168\n"
      "v_mfma_f32_16x16x32_bf16 v[176:179], v[12:15], v[172:175], v[176:179]\n"
      "s_addc_u32 s57, s19, 0\n"
      "v_cvt_pk_bf16_f32 v62, v216, v217\n"
      "v_cvt_pk_bf16_f32 v63, v218, v219\n"
      "v_mfma_f32_16x16x32_bf16 v[176:179], v[24:27], v[156:159], v[176:179]\n"
      "s_and_b32 s56, s56, -16\n"
      "v_cvt_pk_bf16_f32 v64, v168, 0\n"
      "v_fma_f32 v207, v196, v196, s39\n"
      "v_mfma_f32_16x16x32_bf16 v[200:203], v[16:19], v[172:175], v[200:203]\n"
      "s_sub_u32 s58, s56, 110592\n"
      "v_rcp_f32 v240, v240\n"
      "v_mfma_f32_16x16x32_bf16 v[200:203], v[28:31], v[156:159], v[200:203]\n"
      "s_waitcnt lgkmcnt(0)\n"
      "v_mul_f32 v232, v240, v232\n"
      "v_mul_f32 v233, v240, v233\n"
      "v_mfma_f32_16x16x32_bf16 v[136:139], v[66:69], v[224:227], 0\n"
      "s_subb_u32 s59, s57, 0\n"
      "v_cvt_pk_bf16_f32 v232, v232, v233\n"
      "v_mul_f32 v234, v240, v234\n"
      "v_mfma_f32_16x16x32_bf16 v[136:139], v[66:69], v[62:65], v[136:139]\n"
      "s_sub_u32 s56, s56, 73728\n"
      "v_mul_f32 v235, v240, v235\n"
      "v_cvt_pk_bf16_f32 v233, v234, v235\n"
      "v_mfma_f32_16x16x32_bf16 v[172:175], v[220:223], v[224:227], 0\n"
      "s_subb_u32 s57, s57, 0\n"
      "v_mul_f32 v228, v240, v228\n"
      "v_mul_f32 v229, v240, v229\n"
      "v_mfma_f32_16x16x32_bf16 v[172:175], v[50:53], v[62:65], v[172:175]\n"
      "s_mul_i32 s28, s8, 1373568\n"
      "v_cvt_pk_bf16_f32 v234, v228, v229\n"
      "v_mul_f32_e64 v230, v240, v230\n"
      "v_mfma_f32_16x16x32_bf16 v[156:159], v[160:163], v[224:227], 0\n"
      "s_mul_i32 s29, s9, 2688\n"
      "v_mul_f32_e64 v231, v240, v231\n"
      "v_cvt_pk_bf16_f32 v235, v230, v231\n"
      "v_mfma_f32_16x16x32_bf16 v[156:159], v[54:57], v[62:65], v[156:159]\n"
      "ds_write_b128 v124, v[232:235] offset:256\n"
      "v_mfma_f32_16x16x32_bf16 v[168:171], v[208:211], v[224:227], 0\n"
      "s_add_u32 s28, s28, s29\n"
      "v_mul_f32 v144, v240, v144\n"
      "v_mul_f32 v145, v240, v145\n"
      "v_mfma_f32_16x16x32_bf16 v[168:171], v[58:61], v[62:65], v[168:171]\n"
      "s_cmp_eq_u32 s8, 72\n"
      "v_cvt_pk_bf16_f32 v144, v144, v145\n"
      "v_mul_f32 v146, v240, v146\n"
      "v_mfma_f32_16x16x32_bf16 v[230:233], v[188:191], v[220:223], 0\n"
      "s_cselect_b32 s8, 0, 100270464\n"
      "v_mul_f32_e64 v147, v240, v147\n"
      "v_cvt_pk_bf16_f32 v145, v146, v147\n"
      "v_mfma_f32_16x16x32_bf16 v[230:233], v[152:155], v[50:53], v[230:233]\n"
      "ds_write_b64 v125, v[144:145] offset:256\n"
      "v_mfma_f32_16x16x32_bf16 v[220:223], v[188:191], v[160:163], 0\n"
      "s_cmp_eq_u32 s9, 72\n"
      "v_fmac_f32 v207, v197, v197\n"
      "v_fmac_f32 v207, v198, v198\n"
      "v_mfma_f32_16x16x32_bf16 v[220:223], v[152:155], v[54:57], v[220:223]\n"
      "s_cselect_b32 s9, 0, 196224\n"
      "v_fmac_f32 v207, v199, v199\n"
      "v_fmac_f32 v207, v176, v176\n"
      "v_mfma_f32_16x16x32_bf16 v[224:227], v[188:191], v[208:211], 0\n"
      "s_add_u32 s29, s8, s9\n"
      "v_fmac_f32 v207, v177, v177\n"
      "v_fmac_f32 v207, v178, v178\n"
      "v_mfma_f32_16x16x32_bf16 v[224:227], v[152:155], v[58:61], v[224:227]\n"
      "s_add_u32 s28, s28, s29\n"
      "v_fmac_f32 v207, v179, v179\n"
      "v_fmac_f32 v207, v200, v200\n"
      "v_mfma_f32_16x16x32_bf16 v[232:235], a[0:3], v[164:167], 0\n"
      "s_add_u32 s52, s16, s28\n"
      "v_fmac_f32 v207, v201, v201\n"
      "v_fmac_f32_e64 v207, v202, v202\n"
      "v_mfma_f32_16x16x32_bf16 v[232:235], a[36:39], v[184:187], v[232:235]\n"
      "s_addc_u32 s53, s17, 0\n"
      "v_fmac_f32 v207, v203, v203\n"
      "v_mov_b32 v205, v207\n"
      "v_mfma_f32_16x16x32_bf16 v[192:195], a[4:7], v[164:167], 0\n"
      "s_cmp_lg_u32 s29, s11\n"
      "v_permlane16_swap_b32 v253, v252\n"
      "v_mfma_f32_16x16x32_bf16 v[192:195], a[40:43], v[184:187], v[192:195]\n"
      "s_cbranch_scc1 L_edge_37_%=\n"
      "L_eb_38_%=:\n"
      "v_add_f32 v253, v253, v252\n"
      "v_permlane16_swap_b32 v207, v205\n"
      "v_add_f32_e64 v207, v207, v205\n"
      "v_mov_b32_dpp v204, v204 quad_perm:[0,0,0,0] row_mask:0xf bank_mask:0xf\n"
      "v_cndmask_b32 v230, v230, v220, s[42:43]\n"
      "v_permlane32_swap_b32 v253, v207\n"
      "v_add_f32 v253, v253, v207\n"
      "ds_write_b32 v129, v253\n"
      "v_cndmask_b32 v230, v230, v224, s[44:45]\n"
      "v_rcp_f32 v204, v204\n"
      "s_nop 0\n"
      "v_mul_f32_e64 v230, v204, v230\n"
      "v_cvt_pk_bf16_f32 v230, v230, v230\n"
      "ds_write_b16 v135, v230\n"
      "L_join_32_%=:\n"
      "s_cmp_ge_u32 s27, s3\n"
      "s_cbranch_scc1 L_tail0_%=\n"
      "s_add_u32 s27, s27, 1\n"
      "s_branch L_loop_%=\n"
      "L_tail0_%=:\n"
      "s_nop 7\n"
      "s_nop 7\n"
      "v_rcp_f32 v136, v136\n"
      "s_nop 0\n"
      "v_mul_f32 v172, v136, v172\n"
      "v_mul_f32 v173, v136, v173\n"
      "v_cvt_pk_bf16_f32 v172, v172, v173\n"
      "v_mul_f32 v174, v136, v174\n"
      "v_mul_f32 v175, v136, v175\n"
      "v_cvt_pk_bf16_f32 v173, v174, v175\n"
      "v_mul_f32 v156, v136, v156\n"
      "v_mul_f32 v157, v136, v157\n"
      "v_cvt_pk_bf16_f32 v174, v156, v157\n"
      "v_mul_f32 v158, v136, v158\n"
      "v_mul_f32 v159, v136, v159\n"
      "v_cvt_pk_bf16_f32 v175, v158, v159\n"
      "ds_write_b128 v124, v[172:175] offset:512\n"
      "v_mul_f32 v168, v136, v168\n"
      "v_mul_f32 v169, v136, v169\n"
      "v_cvt_pk_bf16_f32 v168, v168, v169\n"
      "v_mul_f32 v170, v136, v170\n"
      "v_mul_f32 v171, v136, v171\n"
      "v_cvt_pk_bf16_f32 v169, v170, v171\n"
      "ds_write_b64 v125, v[168:169] offset:512\n"
      "s_waitcnt lgkmcnt(0)\n"
      "s_barrier\n"
      "v_mbcnt_lo_u32_b32 v168, -1, 0\n"
      "v_mbcnt_hi_u32_b32 v168, -1, v168\n"
      "v_lshrrev_b32 v167, 4, v168\n"
      "v_mul_u32_u24 v167, 336, v167\n"
      "v_and_b32 v165, 15, v168\n"
      "v_lshl_add_u32 v167, v165, 4, v167\n"
      "v_add_u32 v167, 149472, v167\n"
      "s_add_u32 s27, s27, 1\n"
      "ds_read_b128 v[204:207], v128\n"
      "ds_read_b128 v[152:155], v123\n"
      "ds_read_b128 v[208:211], v123 offset:3072\n"
      "s_sub_u32 s28, s27, 3\n"
      "s_cselect_b64 s[54:55], s[12:13], s[22:23]\n"
      "s_max_i32 m0, s28, 0\n"
      "ds_read_b128 v[192:195], v119\n"
      "s_movrels_b32 s9, s80\n"
      "s_lshr_b32 s8, s9, 8\n"
      "s_and_b32 s9, s9, 0xff\n"
      "s_mul_i32 s28, s8, 1373568\n"
      "s_mul_i32 s29, s9, 2688\n"
      "s_add_u32 s28, s28, s29\n"
      "s_cmp_eq_u32 s8, 72\n"
      "s_cselect_b32 s8, 0, 100270464\n"
      "s_cmp_eq_u32 s9, 72\n"
      "s_cselect_b32 s9, 0, 196224\n"
      "s_add_u32 s29, s8, s9\n"
      "s_add_u32 s28, s28, s29\n"
      "s_add_u32 s54, s54, s28\n"
      "s_addc_u32 s55, s55, 0\n"
      "s_cmp_lg_u32 s29, s11\n"
      "ds_read_b128 v[188:191], v119 offset:6144\n"
      "ds_read_b128 v[162:165], v119 offset:12288\n"
      "ds_read_b128 v[170:173], v120\n"
      "ds_read_b128 v[184:187], v121\n"
      "ds_read_b128 v[252:255], v123 offset:6144\n"
      "ds_read_b128 v[136:139], v123 offset:9216\n"
      "s_waitcnt lgkmcnt(7)\n"
      "v_add_f32 v204, |v204|, |v205|\n"
      "v_add_f32 v206, |v206|, |v207|\n"
      "v_add_f32 v204, v204, v206\n"
      "v_mfma_f32_16x16x32_bf16 v[144:147], a[216:219], v[152:155], 0\n"
      "ds_read_b128 v[228:231], v123 offset:12288\n"
      "v_mfma_f32_16x16x32_bf16 v[144:147], a[228:231], v[208:211], v[144:147]\n"
      "ds_read_b128 v[220:223], v123 offset:15360\n"
      "v_rsq_f32 v204, v204\n"
      "v_mfma_f32_16x16x32_bf16 v[216:219], a[220:223], v[152:155], 0\n"
      "ds_read_b128 v[240:243], v123 offset:256\n"
      "v_mul_f32 v204, s41, v204\n"
      "v_mfma_f32_16x16x32_bf16 v[216:219], a[232:235], v[208:211], v[216:219]\n"
      "ds_read_b128 v[232:235], v123 offset:3328\n"
      "v_mov_b32 v169, v204\n"
      "v_mfma_f32_16x16x32_bf16 v[224:227], a[224:227], v[152:155], 0\n"
      "s_cbranch_scc1 L_edge_39_%=\n"
      "L_eb_40_%=:\n"
      "v_mfma_f32_16x16x32_bf16 v[224:227], a[236:239], v[208:211], v[224:227]\n"
      "ds_read_b128 v[152:155], v123 offset:6400\n"
      "v_permlane32_swap_b32 v204, v169\n"
      "v_mov_b32 v166, v204\n"
      "s_waitcnt lgkmcnt(5)\n"
      "s_nop 0\n"
      "v_permlane16_swap_b32_e64 v204, v166\n"
      "v_mfma_f32_16x16x32_bf16 v[144:147], a[240:243], v[252:255], v[144:147]\n"
      "global_store_dwordx4 v116, v[192:195], s[54:55] sc1\n"
      "v_mfma_f32_16x16x32_bf16 v[144:147], a[252:255], v[136:139], v[144:147]\n"
      "ds_read_b128 v[208:211], v123 offset:9472\n"
      "v_mul_f32 v212, v204, v212\n"
      "v_mfma_f32_16x16x32_bf16 v[216:219], a[244:247], v[252:255], v[216:219]\n"
      "v_mul_f32_e64 v213, v204, v213\n"
      "s_waitcnt lgkmcnt(4)\n"
      "v_mfma_f32_16x16x32_bf16 v[216:219], v[0:3], v[136:139], v[216:219]\n"
      "v_cvt_pk_bf16_f32 v212, v212, v213\n"
      "v_mul_f32 v214, v204, v214\n"
      "v_mfma_f32_16x16x32_bf16 v[224:227], a[248:251], v[252:255], v[224:227]\n"
      "ds_read_b128 v[192:195], v123 offset:12544\n"
      "v_mul_f32 v215, v204, v215\n"
      "v_mfma_f32_16x16x32_bf16 v[224:227], v[4:7], v[136:139], v[224:227]\n"
      "s_waitcnt lgkmcnt(3)\n"
      "v_cvt_pk_bf16_f32 v213, v214, v215\n"
      "v_mul_f32 v244, v204, v244\n"
      "v_mfma_f32_16x16x32_bf16 v[144:147], v[8:11], v[228:231], v[144:147]\n"
      "ds_read_b128 v[252:255], v123 offset:15616\n"
      "v_mul_f32 v245, v204, v245\n"
      "v_mfma_f32_16x16x32_bf16 v[144:147], v[20:23], v[220:223], v[144:147]\n"
      "v_cvt_pk_bf16_f32 v214, v244, v245\n"
      "v_mul_f32 v246, v204, v246\n"
      "v_mfma_f32_16x16x32_bf16 v[216:219], v[12:15], v[228:231], v[216:219]\n"
      "v_mul_f32_e64 v247, v204, v247\n"
      "v_cvt_pk_bf16_f32 v215, v246, v247\n"
      "v_mfma_f32_16x16x32_bf16 v[216:219], v[24:27], v[220:223], v[216:219]\n"
      "ds_write_b128 v126, v[212:215] offset:18432\n"
      "v_mfma_f32_16x16x32_bf16 v[224:227], v[16:19], v[228:231], v[224:227]\n"
      "ds_read_b128 v[136:139], v123 offset:512\n"
      "v_mul_f32_e64 v180, v204, v180\n"
      "v_mfma_f32_16x16x32_bf16 v[224:227], v[28:31], v[220:223], v[224:227]\n"
      "ds_read_b128 v[212:215], v123 offset:3584\n"
      "v_mul_f32 v181, v204, v181\n"
      "v_mfma_f32_16x16x32_bf16 v[244:247], a[216:219], v[240:243], 0\n"
      "s_waitcnt lgkmcnt(4)\n"
      "v_cvt_pk_bf16_f32 v180, v180, v181\n"
      "v_mul_f32 v182, v204, v182\n"
      "v_mfma_f32_16x16x32_bf16 v[244:247], a[228:231], v[232:235], v[244:247]\n"
      "v_mul_f32 v183, v204, v183\n"
      "v_cvt_pk_bf16_f32 v181, v182, v183\n"
      "v_mfma_f32_16x16x32_bf16 v[204:207], a[220:223], v[240:243], 0\n"
      "global_store_dwordx4 v116, v[188:191], s[54:55] offset:128 sc1\n"
      "v_mfma_f32_16x16x32_bf16 v[204:207], a[232:235], v[232:235], v[204:207]\n"
      "ds_write_b64 v127, v[180:181] offset:18432\n"
      "v_mfma_f32_16x16x32_bf16 v[228:231], a[224:227], v[240:243], 0\n"
      "ds_read_b128 v[188:191], v123 offset:6656\n"
      "v_mul_f32 v148, v166, v148\n"
      "v_mfma_f32_16x16x32_bf16 v[228:231], a[236:239], v[232:235], v[228:231]\n"
      "s_waitcnt lgkmcnt(4)\n"
      "v_mul_f32 v149, v166, v149\n"
      "v_cvt_pk_bf16_f32 v148, v148, v149\n"
      "v_mfma_f32_16x16x32_bf16 v[244:247], a[240:243], v[152:155], v[244:247]\n"
      "ds_read_b128 v[180:183], v123 offset:9728\n"
      "v_mul_f32 v150, v166, v150\n"
      "v_mfma_f32_16x16x32_bf16 v[244:247], a[252:255], v[208:211], v[244:247]\n"
      "s_lshl_b32 s29, s24, 8\n"
      "v_mul_f32 v151, v166, v151\n"
      "v_cvt_pk_bf16_f32 v149, v150, v151\n"
      "v_mfma_f32_16x16x32_bf16 v[204:207], a[244:247], v[152:155], v[204:207]\n"
      "s_waitcnt lgkmcnt(3)\n"
      "v_mul_f32 v248, v166, v248\n"
      "v_mfma_f32_16x16x32_bf16 v[204:207], v[0:3], v[208:211], v[204:207]\n"
      "s_add_u32 s29, s29, 157536\n"
      "v_mul_f32_e64 v249, v166, v249\n"
      "v_cvt_pk_bf16_f32 v150, v248, v249\n"
      "v_mfma_f32_16x16x32_bf16 v[228:231], a[248:251], v[152:155], v[228:231]\n"
      "v_mul_f32 v250, v166, v250\n"
      "v_mul_f32 v251, v166, v251\n"
      "v_mfma_f32_16x16x32_bf16 v[228:231], v[4:7], v[208:211], v[228:231]\n"
      "v_cvt_pk_bf16_f32 v151, v250, v251\n"
      "v_mul_f32 v140, v166, v140\n"
      "v_mfma_f32_16x16x32_bf16 v[244:247], v[8:11], v[192:195], v[244:247]\n"
      "ds_read_b128 v[208:211], v123 offset:12800\n"
      "v_mul_f32 v141, v166, v141\n"
      "v_mfma_f32_16x16x32_bf16 v[244:247], v[20:23], v[252:255], v[244:247]\n"
      "ds_write_b128 v126, v[148:151] offset:20480\n"
      "v_mfma_f32_16x16x32_bf16 v[204:207], v[12:15], v[192:195], v[204:207]\n"
      "ds_read_b128 v[148:151], v123 offset:15872\n"
      "v_cvt_pk_bf16_f32 v140, v140, v141\n"
      "v_mfma_f32_16x16x32_bf16 v[204:207], v[24:27], v[252:255], v[204:207]\n"
      "s_waitcnt lgkmcnt(3)\n"
      "v_mul_f32 v142, v166, v142\n"
      "v_mul_f32_e64 v143, v166, v143\n"
      "v_mfma_f32_16x16x32_bf16 v[228:231], v[16:19], v[192:195], v[228:231]\n"
      "v_cvt_pk_bf16_f32 v141, v142, v143\n"
      "v_fma_f32 v166, v144, v144, s39\n"
      "v_mfma_f32_16x16x32_bf16 v[228:231], v[28:31], v[252:255], v[228:231]\n"
      "ds_write_b64 v127, v[140:141] offset:20480\n"
      "v_mfma_f32_16x16x32_bf16 v[140:143], a[216:219], v[136:139], 0\n"
      "ds_read_b128 v[192:195], v167\n"
      "v_fmac_f32_e64 v166, v145, v145\n"
      "v_mfma_f32_16x16x32_bf16 v[140:143], a[228:231], v[212:215], v[140:143]\n"
      "ds_read_b128 v[152:155], v167 offset:1344\n"
      "v_fmac_f32 v166, v146, v146\n"
      "v_mfma_f32_16x16x32_bf16 v[250:253], a[220:223], v[136:139], 0\n"
      "v_fmac_f32 v166, v147, v147\n"
      "v_fmac_f32 v166, v216, v216\n"
      "v_mfma_f32_16x16x32_bf16 v[250:253], a[232:235], v[212:215], v[250:253]\n"
      "global_store_dwordx4 v116, v[162:165], s[54:55] offset:256 sc1\n"
      "v_mfma_f32_16x16x32_bf16 v[240:243], a[224:227], v[136:139], 0\n"
      "v_fmac_f32 v166, v217, v217\n"
      "v_fmac_f32 v166, v218, v218\n"
      "v_mfma_f32_16x16x32_bf16 v[240:243], a[236:239], v[212:215], v[240:243]\n"
      "s_waitcnt lgkmcnt(3)\n"
      "v_fmac_f32 v166, v219, v219\n"
      "v_fmac_f32 v166, v224, v224\n"
      "v_mfma_f32_16x16x32_bf16 v[140:143], a[240:243], v[188:191], v[140:143]\n"
      "v_fmac_f32 v166, v225, v225\n"
      "v_fmac_f32 v166, v226, v226\n"
      "v_mfma_f32_16x16x32_bf16 v[140:143], a[252:255], v[180:183], v[140:143]\n"
      "v_fmac_f32 v166, v227, v227\n"
      "v_mul_f32 v196, v169, v196\n"
      "v_mfma_f32_16x16x32_bf16 v[250:253], a[244:247], v[188:191], v[250:253]\n"
      "ds_read_b128 v[162:165], v167 offset:2688\n"
      "v_mul_f32_e64 v197, v169, v197\n"
      "v_mfma_f32_16x16x32_bf16 v[250:253], v[0:3], v[180:183], v[250:253]\n"
      "ds_read_b128 v[136:139], v167 offset:4032\n"
      "v_cvt_pk_bf16_f32 v196, v196, v197\n"
      "v_mfma_f32_16x16x32_bf16 v[240:243], a[248:251], v[188:191], v[240:243]\n"
      "v_mul_f32 v198, v169, v198\n"
      "s_waitcnt lgkmcnt(2)\n"
      "v_mfma_f32_16x16x32_bf16 v[240:243], v[4:7], v[180:183], v[240:243]\n"
      "v_mul_f32 v199, v169, v199\n"
      "v_cvt_pk_bf16_f32 v197, v198, v199\n"
      "v_mfma_f32_16x16x32_bf16 v[140:143], v[8:11], v[208:211], v[140:143]\n"
      "v_mul_f32 v176, v169, v176\n"
      "v_mul_f32_e64 v177, v169, v177\n"
      "v_mfma_f32_16x16x32_bf16 v[140:143], v[20:23], v[148:151], v[140:143]\n"
      "v_cvt_pk_bf16_f32 v198, v176, v177\n"
      "v_mul_f32_e64 v178, v169, v178\n"
      "v_mfma_f32_16x16x32_bf16 v[250:253], v[12:15], v[208:211], v[250:253]\n"
      "global_store_dwordx4 v117, v[170:173], s[54:55] sc1\n"
      "v_mfma_f32_16x16x32_bf16 v[250:253], v[24:27], v[148:151], v[250:253]\n"
      "ds_read_b128 v[188:191], v167 offset:5376\n"
      "v_mul_f32_e64 v179, v169, v179\n"
      "v_mfma_f32_16x16x32_bf16 v[240:243], v[16:19], v[208:211], v[240:243]\n"
      "ds_read_b128 v[208:211], v167 offset:6720\n"
      "v_cvt_pk_bf16_f32 v199, v178, v179\n"
      "v_mfma_f32_16x16x32_bf16 v[240:243], v[28:31], v[148:151], v[240:243]\n"
      "ds_write_b128 v126, v[196:199] offset:22528\n"
      "v_mfma_f32_16x16x32_bf16 v[196:199], a[216:219], v[192:195], 0\n"
      "s_waitcnt lgkmcnt(3)\n"
      "v_mul_f32 v200, v169, v200\n"
      "v_mul_f32_e64 v201, v169, v201\n"
      "v_mfma_f32_16x16x32_bf16 v[196:199], a[228:231], v[152:155], v[196:199]\n"
      "v_cvt_pk_bf16_f32 v200, v200, v201\n"
      "v_mul_f32_e64 v202, v169, v202\n"
      "v_mfma_f32_16x16x32_bf16 v[180:183], a[220:223], v[192:195], 0\n"
      "ds_read_b128 v[148:151], v167 offset:256\n"
      "v_mul_f32_e64 v203, v169, v203\n"
      "v_mfma_f32_16x16x32_bf16 v[180:183], a[232:235], v[152:155], v[180:183]\n"
      "v_cvt_pk_bf16_f32 v201, v202, v203\n"
      "v_fma_f32 v169, v244, v244, s39\n"
      "v_mfma_f32_16x16x32_bf16 v[170:173], a[224:227], v[192:195], 0\n"
      "ds_read_b128 v[192:195], v167 offset:1600\n"
      "v_fmac_f32_e64 v169, v245, v245\n"
      "v_mfma_f32_16x16x32_bf16 v[170:173], a[236:239], v[152:155], v[170:173]\n"
      "ds_write_b64 v127, v[200:201] offset:22528\n"
      "v_mfma_f32_16x16x32_bf16 v[196:199], a[240:243], v[162:165], v[196:199]\n"
      "v_fmac_f32 v169, v246, v246\n"
      "v_fmac_f32 v169, v247, v247\n"
      "v_mfma_f32_16x16x32_bf16 v[196:199], a[252:255], v[136:139], v[196:199]\n"
      "s_waitcnt lgkmcnt(3)\n"
      "v_fmac_f32 v169, v204, v204\n"
      "v_fmac_f32_e64 v169, v205, v205\n"
      "v_mfma_f32_16x16x32_bf16 v[180:183], a[244:247], v[162:165], v[180:183]\n"
      "ds_read_b128 v[152:155], v167 offset:2944\n"
      "v_fmac_f32 v169, v206, v206\n"
      "v_mfma_f32_16x16x32_bf16 v[180:183], v[0:3], v[136:139], v[180:183]\n"
      "ds_read_b128 v[200:203], v167 offset:4288\n"
      "v_fmac_f32 v169, v207, v207\n"
      "v_mfma_f32_16x16x32_bf16 v[170:173], a[248:251], v[162:165], v[170:173]\n"
      "v_fmac_f32 v169, v228, v228\n"
      "v_fmac_f32 v169, v229, v229\n"
      "v_mfma_f32_16x16x32_bf16 v[170:173], v[4:7], v[136:139], v[170:173]\n"
      "v_fmac_f32 v169, v230, v230\n"
      "v_fmac_f32 v169, v231, v231\n"
      "v_mfma_f32_16x16x32_bf16 v[196:199], v[8:11], v[188:191], v[196:199]\n"
      "ds_read_b128 v[136:139], v167 offset:5632\n"
      "v_lshl_add_u32 v164, v168, 2, s29\n"
      "v_mfma_f32_16x16x32_bf16 v[196:199], v[20:23], v[208:211], v[196:199]\n"
      "s_waitcnt lgkmcnt(3)\n"
      "v_lshrrev_b32 v165, 3, v168\n"
      "v_fma_f32 v168, v140, v140, s39\n"
      "v_mfma_f32_16x16x32_bf16 v[180:183], v[12:15], v[188:191], v[180:183]\n"
      "ds_read_b32 v164, v164\n"
      "v_fmac_f32 v168, v141, v141\n"
      "v_mfma_f32_16x16x32_bf16 v[180:183], v[24:27], v[208:211], v[180:183]\n"
      "ds_read_b128 v[232:235], v167 offset:6976\n"
      "v_fmac_f32 v168, v142, v142\n"
      "v_mfma_f32_16x16x32_bf16 v[170:173], v[16:19], v[188:191], v[170:173]\n"
      "s_lshl_b32 s29, s24, 3\n"
      "v_add_u32 v165, s29, v165\n"
      "v_cmp_gt_u32 s[48:49], s26, v165\n"
      "v_mfma_f32_16x16x32_bf16 v[170:173], v[28:31], v[208:211], v[170:173]\n"
      "v_fmac_f32 v168, v143, v143\n"
      "v_fmac_f32 v168, v250, v250\n"
      "v_mfma_f32_16x16x32_bf16 v[188:191], a[216:219], v[148:151], 0\n"
      "v_fmac_f32 v168, v251, v251\n"
      "v_fmac_f32 v168, v252, v252\n"
      "v_mfma_f32_16x16x32_bf16 v[188:191], a[228:231], v[192:195], v[188:191]\n"
      "s_waitcnt lgkmcnt(3)\n"
      "v_fmac_f32 v168, v253, v253\n"
      "v_fmac_f32 v168, v240, v240\n"
      "v_mfma_f32_16x16x32_bf16 v[208:211], a[220:223], v[148:151], 0\n"
      "v_fmac_f32 v168, v241, v241\n"
      "v_fmac_f32 v168, v242, v242\n"
      "v_mfma_f32_16x16x32_bf16 v[208:211], a[232:235], v[192:195], v[208:211]\n"
      "s_mov_b32 exec_hi, 0\n"
      "global_store_dwordx4 v118, v[184:187], s[54:55] sc1\n"
      "s_mov_b32 exec_hi, -1\n"
      "v_mfma_f32_16x16x32_bf16 v[176:179], a[224:227], v[148:151], 0\n"
      "v_fmac_f32 v168, v243, v243\n"
      "v_mov_b32_e64 v165, v168\n"
      "v_mfma_f32_16x16x32_bf16 v[176:179], a[236:239], v[192:195], v[176:179]\n"
      "v_permlane16_swap_b32 v166, v169\n"
      "v_mfma_f32_16x16x32_bf16 v[188:191], a[240:243], v[152:155], v[188:191]\n"
      "s_waitcnt lgkmcnt(0)\n"
      "v_add_f32_e64 v166, v166, v169\n"
      "v_fma_f32 v169, v196, v196, s39\n"
      "v_mfma_f32_16x16x32_bf16 v[188:191], a[252:255], v[200:203], v[188:191]\n"
      "v_permlane16_swap_b32 v168, v165\n"
      "v_mfma_f32_16x16x32_bf16 v[208:211], a[244:247], v[152:155], v[208:211]\n"
      "v_add_f32 v168, v168, v165\n"
      "v_fmac_f32 v169, v197, v197\n"
      "v_mfma_f32_16x16x32_bf16 v[208:211], v[0:3], v[200:203], v[208:211]\n"
      "v_permlane32_swap_b32 v166, v168\n"
      "v_mfma_f32_16x16x32_bf16 v[176:179], a[248:251], v[152:155], v[176:179]\n"
      "v_add_f32 v166, v166, v168\n"
      "v_fmac_f32 v169, v198, v198\n"
      "v_mfma_f32_16x16x32_bf16 v[176:179], v[4:7], v[200:203], v[176:179]\n"
      "ds_write_b32 v129, v166 offset:1024\n"
      "v_mfma_f32_16x16x32_bf16 v[188:191], v[8:11], v[136:139], v[188:191]\n"
      "v_fmac_f32 v169, v199, v199\n"
      "v_fmac_f32 v169, v180, v180\n"
      "v_mfma_f32_16x16x32_bf16 v[188:191], v[20:23], v[232:235], v[188:191]\n"
      "v_fmac_f32 v169, v181, v181\n"
      "v_fmac_f32 v169, v182, v182\n"
      "v_mfma_f32_16x16x32_bf16 v[208:211], v[12:15], v[136:139], v[208:211]\n"
      "v_fmac_f32 v169, v183, v183\n"
      "v_fmac_f32 v169, v170, v170\n"
      "v_mfma_f32_16x16x32_bf16 v[208:211], v[24:27], v[232:235], v[208:211]\n"
      "v_fmac_f32 v169, v171, v171\n"
      "v_fmac_f32 v169, v172, v172\n"
      "v_mfma_f32_16x16x32_bf16 v[176:179], v[16:19], v[136:139], v[176:179]\n"
      "v_fmac_f32_e64 v169, v173, v173\n"
      "v_fma_f32 v165, v188, v188, s39\n"
      "v_mfma_f32_16x16x32_bf16 v[176:179], v[28:31], v[232:235], v[176:179]\n"
      "v_fmac_f32 v165, v189, v189\n"
      "v_fmac_f32 v165, v190, v190\n"
      "v_fmac_f32 v165, v191, v191\n"
      "v_fmac_f32 v165, v208, v208\n"
      "v_fmac_f32 v165, v209, v209\n"
      "v_fmac_f32 v165, v210, v210\n"
      "v_fmac_f32 v165, v211, v211\n"
      "s_nop 0\n"
      "v_fmac_f32 v165, v176, v176\n"
      "v_fmac_f32 v165, v177, v177\n"
      "v_fmac_f32 v165, v178, v178\n"
      "v_fmac_f32 v165, v179, v179\n"
      "v_mov_b32 v166, v165\n"
      "v_mov_b32 v168, v166\n"
      "v_permlane16_swap_b32 v169, v165\n"
      "v_add_f32 v169, v169, v165\n"
      "v_permlane16_swap_b32 v166, v168\n"
      "v_add_f32 v166, v166, v168\n"
      "s_nop 1\n"
      "v_permlane32_swap_b32 v169, v166\n"
      "v_add_f32_e64 v169, v169, v166\n"
      "ds_write_b32 v129, v169 offset:2048\n"
      "s_waitcnt lgkmcnt(0)\n"
      "s_barrier\n"
      "s_add_u32 s27, s27, 1\n"
      "ds_read_b128 v[192:195], v128 offset:1024\n"
      "ds_read_b128 v[152:155], v128 offset:2048\n"
      "s_sub_u32 s28, s27, 3\n"
      "s_cselect_b64 s[54:55], s[12:13], s[22:23]\n"
      "s_max_i32 m0, s28, 0\n"
      "s_nop 0\n"
      "s_movrels_b32 s9, s80\n"
      "s_lshr_b32 s8, s9, 8\n"
      "s_and_b32 s9, s9, 0xff\n"
      "s_mul_i32 s28, s8, 1373568\n"
      "s_mul_i32 s29, s9, 2688\n"
      "s_add_u32 s28, s28, s29\n"
      "s_cmp_eq_u32 s8, 72\n"
      "s_cselect_b32 s8, 0, 100270464\n"
      "s_cmp_eq_u32 s9, 72\n"
      "s_cselect_b32 s9, 0, 196224\n"
      "s_add_u32 s29, s8, s9\n"
      "s_add_u32 s28, s28, s29\n"
      "s_add_u32 s54, s54, s28\n"
      "s_addc_u32 s55, s55, 0\n"
      "s_cmp_lg_u32 s29, s11\n"
      "s_cbranch_scc1 L_edge_41_%=\n"
      "L_eb_42_%=:\n"
      "ds_read_b128 v[136:139], v119 offset:18432\n"
      "ds_read_b128 v[148:151], v119 offset:24576\n"
      "ds_read_b128 v[232:235], v119 offset:30720\n"
      "ds_read_b128 v[166:169], v120 offset:18432\n"
      "ds_read_b128 v[200:203], v121 offset:18432\n"
      "v_subrev_u32 v165, 73728, v126\n"
      "s_waitcnt lgkmcnt(5)\n"
      "v_add_f32 v192, |v192|, |v193|\n"
      "v_add_f32 v194, |v194|, |v195|\n"
      "v_add_f32 v192, v192, v194\n"
      "v_rsq_f32 v192, v192\n"
      "s_nop 0\n"
      "v_mul_f32 v192, s41, v192\n"
      "v_mov_b32 v194, v192\n"
      "v_add_f32 v152, |v152|, |v153|\n"
      "v_add_f32 v154, |v154|, |v155|\n"
      "v_permlane32_swap_b32 v192, v194\n"
      "v_mov_b32 v153, v192\n"
      "v_add_f32 v152, v152, v154\n"
      "v_rsq_f32 v152, v152\n"
      "v_permlane16_swap_b32 v192, v153\n"
      "v_mul_f32 v152, s41, v152\n"
      "v_mov_b32_e64 v154, v152\n"
      "v_subrev_u32 v195, 73728, v127\n"
      "v_mul_f32 v144, v192, v144\n"
      "v_permlane32_swap_b32 v152, v154\n"
      "v_mov_b32 v193, v152\n"
      "v_mul_f32 v145, v192, v145\n"
      "v_cvt_pk_bf16_f32 v144, v144, v145\n"
      "v_permlane16_swap_b32 v152, v193\n"
      "v_mul_f32 v146, v192, v146\n"
      "v_mul_f32 v147, v192, v147\n"
      "v_cvt_pk_bf16_f32 v145, v146, v147\n"
      "v_mul_f32 v216, v192, v216\n"
      "v_mul_f32_e64 v217, v192, v217\n"
      "v_cvt_pk_bf16_f32 v146, v216, v217\n"
      "v_mul_f32 v218, v192, v218\n"
      "v_mul_f32 v219, v192, v219\n"
      "v_cvt_pk_bf16_f32 v147, v218, v219\n"
      "ds_write_b128 v126, v[144:147]\n"
      "v_mul_f32 v224, v192, v224\n"
      "v_mul_f32 v225, v192, v225\n"
      "v_cvt_pk_bf16_f32 v224, v224, v225\n"
      "v_mul_f32 v226, v192, v226\n"
      "v_mul_f32 v227, v192, v227\n"
      "v_cvt_pk_bf16_f32 v225, v226, v227\n"
      "ds_write_b64 v127, v[224:225]\n"
      "s_waitcnt lgkmcnt(2)\n"
      "global_store_dwordx4 v116, v[136:139], s[54:55] sc1\n"
      "v_mul_f32 v244, v153, v244\n"
      "v_mul_f32_e64 v245, v153, v245\n"
      "v_cvt_pk_bf16_f32 v244, v244, v245\n"
      "v_mul_f32 v246, v153, v246\n"
      "v_mul_f32 v247, v153, v247\n"
      "v_cvt_pk_bf16_f32 v245, v246, v247\n"
      "v_mul_f32 v204, v153, v204\n"
      "v_mul_f32 v205, v153, v205\n"
      "v_cvt_pk_bf16_f32 v246, v204, v205\n"
      "v_mul_f32 v206, v153, v206\n"
      "v_mul_f32 v207, v153, v207\n"
      "v_cvt_pk_bf16_f32 v247, v206, v207\n"
      "ds_write_b128 v126, v[244:247] offset:2048\n"
      "v_mul_f32 v228, v153, v228\n"
      "v_mul_f32 v229, v153, v229\n"
      "v_cvt_pk_bf16_f32 v228, v228, v229\n"
      "v_mul_f32 v230, v153, v230\n"
      "v_mul_f32 v231, v153, v231\n"
      "v_cvt_pk_bf16_f32 v229, v230, v231\n"
      "ds_write_b64 v127, v[228:229] offset:2048\n"
      "global_store_dwordx4 v116, v[148:151], s[54:55] offset:128 sc1\n"
      "v_mul_f32 v140, v194, v140\n"
      "v_mul_f32 v141, v194, v141\n"
      "v_cvt_pk_bf16_f32 v140, v140, v141\n"
      "v_mul_f32 v142, v194, v142\n"
      "v_mul_f32 v143, v194, v143\n"
      "v_cvt_pk_bf16_f32 v141, v142, v143\n"
      "v_mul_f32 v250, v194, v250\n"
      "v_mul_f32 v251, v194, v251\n"
      "v_cvt_pk_bf16_f32 v142, v250, v251\n"
      "v_mul_f32 v252, v194, v252\n"
      "v_mul_f32 v253, v194, v253\n"
      "v_cvt_pk_bf16_f32 v143, v252, v253\n"
      "ds_write_b128 v126, v[140:143] offset:4096\n"
      "v_mul_f32 v240, v194, v240\n"
      "v_mul_f32 v241, v194, v241\n"
      "v_cvt_pk_bf16_f32 v240, v240, v241\n"
      "v_mul_f32 v242, v194, v242\n"
      "v_mul_f32 v243, v194, v243\n"
      "v_cvt_pk_bf16_f32 v241, v242, v243\n"
      "ds_write_b64 v127, v[240:241] offset:4096\n"
      "global_store_dwordx4 v116, v[232:235], s[54:55] offset:256 sc1\n"
      "v_mul_f32 v196, v152, v196\n"
      "v_mul_f32 v197, v152, v197\n"
      "v_cvt_pk_bf16_f32 v196, v196, v197\n"
      "v_mul_f32 v198, v152, v198\n"
      "v_mul_f32 v199, v152, v199\n"
      "v_cvt_pk_bf16_f32 v197, v198, v199\n"
      "v_mul_f32 v180, v152, v180\n"
      "v_mul_f32 v181, v152, v181\n"
      "v_cvt_pk_bf16_f32 v198, v180, v181\n"
      "v_mul_f32 v182, v152, v182\n"
      "v_mul_f32 v183, v152, v183\n"
      "v_cvt_pk_bf16_f32 v199, v182, v183\n"
      "ds_write_b128 v165, v[196:199]\n"
      "v_mul_f32 v170, v152, v170\n"
      "v_mul_f32 v171, v152, v171\n"
      "v_cvt_pk_bf16_f32 v170, v170, v171\n"
      "v_mul_f32 v172, v152, v172\n"
      "v_mul_f32 v173, v152, v173\n"
      "v_cvt_pk_bf16_f32 v171, v172, v173\n"
      "ds_write_b64 v195, v[170:171]\n"
      "global_store_dwordx4 v117, v[166:169], s[54:55] sc1\n"
      "v_mul_f32 v188, v193, v188\n"
      "v_mul_f32 v189, v193, v189\n"
      "v_cvt_pk_bf16_f32 v188, v188, v189\n"
      "v_mul_f32 v190, v193, v190\n"
      "v_mul_f32 v191, v193, v191\n"
      "v_cvt_pk_bf16_f32 v189, v190, v191\n"
      "v_mul_f32 v208, v193, v208\n"
      "v_mul_f32 v209, v193, v209\n"
      "v_cvt_pk_bf16_f32 v190, v208, v209\n"
      "v_mul_f32 v210, v193, v210\n"
      "v_mul_f32 v211, v193, v211\n"
      "v_cvt_pk_bf16_f32 v191, v210, v211\n"
      "ds_write_b128 v165, v[188:191] offset:2048\n"
      "v_mul_f32 v176, v193, v176\n"
      "v_mul_f32 v177, v193, v177\n"
      "v_cvt_pk_bf16_f32 v176, v176, v177\n"
      "v_mul_f32_e64 v178, v193, v178\n"
      "v_mul_f32 v179, v193, v179\n"
      "v_cvt_pk_bf16_f32 v177, v178, v179\n"
      "ds_write_b64 v195, v[176:177] offset:2048\n"
      "s_mov_b32 exec_hi, 0\n"
      "global_store_dwordx4 v118, v[200:203], s[54:55] sc1\n"
      "s_mov_b32 exec_hi, -1\n"
      "s_waitcnt lgkmcnt(0)\n"
      "s_barrier\n"
      "s_add_u32 s27, s27, 1\n"
      "v_subrev_u32 v165, 73728, v119\n"
      "ds_read_b128 v[208:211], v119\n"
      "ds_read_b128 v[188:191], v119 offset:6144\n"
      "ds_read_b128 v[196:199], v119 offset:12288\n"
      "ds_read_b128 v[168:171], v120\n"
      "ds_read_b128 v[192:195], v121\n"
      "ds_read_b128 v[140:143], v165\n"
      "ds_read_b128 v[244:247], v165 offset:6144\n"
      "ds_read_b128 v[148:151], v165 offset:12288\n"
      "s_sub_u32 s28, s27, 3\n"
      "s_cselect_b64 s[54:55], s[12:13], s[22:23]\n"
      "s_max_i32 m0, s28, 0\n"
      "s_nop 0\n"
      "s_movrels_b32 s9, s80\n"
      "s_lshr_b32 s8, s9, 8\n"
      "s_and_b32 s9, s9, 0xff\n"
      "s_mul_i32 s28, s8, 1373568\n"
      "s_mul_i32 s29, s9, 2688\n"
      "s_add_u32 s28, s28, s29\n"
      "s_cmp_eq_u32 s8, 72\n"
      "s_cselect_b32 s8, 0, 100270464\n"
      "s_cmp_eq_u32 s9, 72\n"
      "s_cselect_b32 s9, 0, 196224\n"
      "s_add_u32 s29, s8, s9\n"
      "s_add_u32 s28, s28, s29\n"
      "s_add_u32 s54, s54, s28\n"
      "s_addc_u32 s55, s55, 0\n"
      "s_cmp_lg_u32 s29, s11\n"
      "s_cbranch_scc1 L_edge_43_%=\n"
      "L_eb_44_%=:\n"
      "s_waitcnt lgkmcnt(5)\n"
      "global_store_dwordx4 v116, v[208:211], s[54:55] sc1\n"
      "global_store_dwordx4 v116, v[188:191], s[54:55] offset:128 sc1\n"
      "global_store_dwordx4 v116, v[196:199], s[54:55] offset:256 sc1\n"
      "s_waitcnt lgkmcnt(0)\n"
      "global_store_dwordx4 v117, v[168:171], s[54:55] sc1\n"
      "s_mov_b32 exec_hi, 0\n"
      "global_store_dwordx4 v118, v[192:195], s[54:55] sc1\n"
      "s_mov_b32 exec_hi, -1\n"
      "s_nop 0\n"
      "s_mov_b64 exec, s[48:49]\n"
      "s_cbranch_execz L_np_45_%=\n"
      "global_store_dwordx4 v164, v[140:143], s[22:23] sc1\n"
      "global_store_dwordx4 v164, v[244:247], s[22:23] offset:128 sc1\n"
      "global_store_dwordx4 v164, v[148:151], s[22:23] offset:256 sc1\n"
      "L_np_45_%=:\n"
      "s_mov_b64 exec, -1\n"
      "s_endpgm\n"
      "L_tail1_%=:\n"
      "s_nop 7\n"
      "s_nop 7\n"
      "v_rcp_f32 v136, v136\n"
      "s_nop 0\n"
      "v_mul_f32 v164, v136, v164\n"
      "v_mul_f32 v165, v136, v165\n"
      "v_cvt_pk_bf16_f32 v164, v164, v165\n"
      "v_mul_f32 v166, v136, v166\n"
      "v_mul_f32 v167, v136, v167\n"
      "v_cvt_pk_bf16_f32 v165, v166, v167\n"
      "v_mul_f32 v208, v136, v208\n"
      "v_mul_f32 v209, v136, v209\n"
      "v_cvt_pk_bf16_f32 v166, v208, v209\n"
      "v_mul_f32 v210, v136, v210\n"
      "v_mul_f32 v211, v136, v211\n"
      "v_cvt_pk_bf16_f32 v167, v210, v211\n"
      "ds_write_b128 v124, v[164:167] offset:18944\n"
      "v_mul_f32 v148, v136, v148\n"
      "v_mul_f32 v149, v136, v149\n"
      "v_cvt_pk_bf16_f32 v148, v148, v149\n"
      "v_mul_f32 v150, v136, v150\n"
      "v_mul_f32 v151, v136, v151\n"
      "v_cvt_pk_bf16_f32 v149, v150, v151\n"
      "ds_write_b64 v125, v[148:149] offset:18944\n"
      "s_waitcnt lgkmcnt(0)\n"
      "s_barrier\n"
      "v_mbcnt_lo_u32_b32 v255, -1, 0\n"
      "v_mbcnt_hi_u32_b32 v255, -1, v255\n"
      "v_lshrrev_b32 v249, 4, v255\n"
      "v_mul_u32_u24 v249, 336, v249\n"
      "v_and_b32 v254, 15, v255\n"
      "v_lshl_add_u32 v249, v254, 4, v249\n"
      "v_add_u32 v249, 149472, v249\n"
      "s_add_u32 s27, s27, 1\n"
      "ds_read_b128 v[200:203], v128 offset:1024\n"
      "ds_read_b128 v[208:211], v123 offset:18432\n"
      "ds_read_b128 v[176:179], v123 offset:21504\n"
      "s_sub_u32 s28, s27, 3\n"
      "s_cselect_b64 s[54:55], s[12:13], s[22:23]\n"
      "s_max_i32 m0, s28, 0\n"
      "ds_read_b128 v[148:151], v119 offset:18432\n"
      "s_movrels_b32 s9, s80\n"
      "s_lshr_b32 s8, s9, 8\n"
      "s_and_b32 s9, s9, 0xff\n"
      "s_mul_i32 s28, s8, 1373568\n"
      "s_mul_i32 s29, s9, 2688\n"
      "s_add_u32 s28, s28, s29\n"
      "s_cmp_eq_u32 s8, 72\n"
      "s_cselect_b32 s8, 0, 100270464\n"
      "s_cmp_eq_u32 s9, 72\n"
      "s_cselect_b32 s9, 0, 196224\n"
      "s_add_u32 s29, s8, s9\n"
      "s_add_u32 s28, s28, s29\n"
      "s_add_u32 s54, s54, s28\n"
      "s_addc_u32 s55, s55, 0\n"
      "s_cmp_lg_u32 s29, s11\n"
      "ds_read_b128 v[180:183], v119 offset:24576\n"
      "ds_read_b128 v[232:235], v119 offset:30720\n"
      "ds_read_b128 v[164:167], v120 offset:18432\n"
      "ds_read_b128 v[216:219], v121 offset:18432\n"
      "ds_read_b128 v[228:231], v123 offset:24576\n"
      "ds_read_b128 v[240:243], v123 offset:27648\n"
      "s_waitcnt lgkmcnt(7)\n"
      "v_add_f32 v200, |v200|, |v201|\n"
      "v_add_f32 v202, |v202|, |v203|\n"
      "v_add_f32 v200, v200, v202\n"
      "v_mfma_f32_16x16x32_bf16 v[144:147], a[216:219], v[208:211], 0\n"
      "ds_read_b128 v[136:139], v123 offset:30720\n"
      "v_mfma_f32_16x16x32_bf16 v[144:147], a[228:231], v[176:179], v[144:147]\n"
      "ds_read_b128 v[172:175], v123 offset:33792\n"
      "v_rsq_f32_e64 v200, v200\n"
      "v_mfma_f32_16x16x32_bf16 v[220:223], a[220:223], v[208:211], 0\n"
      "ds_read_b128 v[184:187], v123 offset:18688\n"
      "v_mul_f32 v200, s41, v200\n"
      "v_mfma_f32_16x16x32_bf16 v[220:223], a[232:235], v[176:179], v[220:223]\n"
      "ds_read_b128 v[224:227], v123 offset:21760\n"
      "v_mov_b32 v248, v200\n"
      "v_mfma_f32_16x16x32_bf16 v[236:239], a[224:227], v[208:211], 0\n"
      "s_cbranch_scc1 L_edge_46_%=\n"
      "L_eb_47_%=:\n"
      "v_mfma_f32_16x16x32_bf16 v[236:239], a[236:239], v[176:179], v[236:239]\n"
      "ds_read_b128 v[212:215], v123 offset:24832\n"
      "v_permlane32_swap_b32 v200, v248\n"
      "v_mov_b32 v254, v200\n"
      "s_waitcnt lgkmcnt(5)\n"
      "s_nop 0\n"
      "v_permlane16_swap_b32 v200, v254\n"
      "v_mfma_f32_16x16x32_bf16 v[144:147], a[240:243], v[228:231], v[144:147]\n"
      "global_store_dwordx4 v116, v[148:151], s[54:55] sc1\n"
      "v_mfma_f32_16x16x32_bf16 v[144:147], a[252:255], v[240:243], v[144:147]\n"
      "ds_read_b128 v[176:179], v123 offset:27904\n"
      "v_mul_f32 v140, v200, v140\n"
      "v_mfma_f32_16x16x32_bf16 v[220:223], a[244:247], v[228:231], v[220:223]\n"
      "v_mul_f32 v141, v200, v141\n"
      "s_waitcnt lgkmcnt(4)\n"
      "v_mfma_f32_16x16x32_bf16 v[220:223], v[0:3], v[240:243], v[220:223]\n"
      "v_cvt_pk_bf16_f32 v140, v140, v141\n"
      "v_mul_f32 v142, v200, v142\n"
      "v_mfma_f32_16x16x32_bf16 v[236:239], a[248:251], v[228:231], v[236:239]\n"
      "ds_read_b128 v[228:231], v123 offset:30976\n"
      "v_mul_f32 v143, v200, v143\n"
      "v_mfma_f32_16x16x32_bf16 v[236:239], v[4:7], v[240:243], v[236:239]\n"
      "s_waitcnt lgkmcnt(3)\n"
      "v_cvt_pk_bf16_f32 v141, v142, v143\n"
      "v_mul_f32 v196, v200, v196\n"
      "v_mfma_f32_16x16x32_bf16 v[144:147], v[8:11], v[136:139], v[144:147]\n"
      "ds_read_b128 v[208:211], v123 offset:34048\n"
      "v_mul_f32 v197, v200, v197\n"
      "v_mfma_f32_16x16x32_bf16 v[144:147], v[20:23], v[172:175], v[144:147]\n"
      "v_cvt_pk_bf16_f32 v142, v196, v197\n"
      "v_mul_f32 v198, v200, v198\n"
      "v_mfma_f32_16x16x32_bf16 v[220:223], v[12:15], v[136:139], v[220:223]\n"
      "v_mul_f32 v199, v200, v199\n"
      "v_cvt_pk_bf16_f32 v143, v198, v199\n"
      "v_mfma_f32_16x16x32_bf16 v[220:223], v[24:27], v[172:175], v[220:223]\n"
      "ds_write_b128 v126, v[140:143]\n"
      "v_mfma_f32_16x16x32_bf16 v[236:239], v[16:19], v[136:139], v[236:239]\n"
      "ds_read_b128 v[196:199], v123 offset:18944\n"
      "v_mul_f32 v168, v200, v168\n"
      "v_mfma_f32_16x16x32_bf16 v[236:239], v[28:31], v[172:175], v[236:239]\n"
      "ds_read_b128 v[240:243], v123 offset:22016\n"
      "v_mul_f32 v169, v200, v169\n"
      "v_mfma_f32_16x16x32_bf16 v[140:143], a[216:219], v[184:187], 0\n"
      "s_waitcnt lgkmcnt(4)\n"
      "v_cvt_pk_bf16_f32 v168, v168, v169\n"
      "v_mul_f32 v170, v200, v170\n"
      "v_mfma_f32_16x16x32_bf16 v[140:143], a[228:231], v[224:227], v[140:143]\n"
      "v_mul_f32_e64 v171, v200, v171\n"
      "v_cvt_pk_bf16_f32 v169, v170, v171\n"
      "v_mfma_f32_16x16x32_bf16 v[172:175], a[220:223], v[184:187], 0\n"
      "global_store_dwordx4 v116, v[180:183], s[54:55] offset:128 sc1\n"
      "v_mfma_f32_16x16x32_bf16 v[172:175], a[232:235], v[224:227], v[172:175]\n"
      "ds_write_b64 v127, v[168:169]\n"
      "v_mfma_f32_16x16x32_bf16 v[168:171], a[224:227], v[184:187], 0\n"
      "ds_read_b128 v[184:187], v123 offset:25088\n"
      "v_mul_f32_e64 v244, v254, v244\n"
      "v_mfma_f32_16x16x32_bf16 v[168:171], a[236:239], v[224:227], v[168:171]\n"
      "s_waitcnt lgkmcnt(4)\n"
      "v_mul_f32 v245, v254, v245\n"
      "v_cvt_pk_bf16_f32 v244, v244, v245\n"
      "v_mfma_f32_16x16x32_bf16 v[140:143], a[240:243], v[212:215], v[140:143]\n"
      "ds_read_b128 v[200:203], v123 offset:28160\n"
      "v_mul_f32 v246, v254, v246\n"
      "v_mfma_f32_16x16x32_bf16 v[140:143], a[252:255], v[176:179], v[140:143]\n"
      "s_lshl_b32 s29, s24, 8\n"
      "v_mul_f32_e64 v247, v254, v247\n"
      "v_cvt_pk_bf16_f32 v245, v246, v247\n"
      "v_mfma_f32_16x16x32_bf16 v[172:175], a[244:247], v[212:215], v[172:175]\n"
      "s_waitcnt lgkmcnt(3)\n"
      "v_mul_f32 v152, v254, v152\n"
      "v_mfma_f32_16x16x32_bf16 v[172:175], v[0:3], v[176:179], v[172:175]\n"
      "s_add_u32 s29, s29, 157536\n"
      "v_mul_f32 v153, v254, v153\n"
      "v_cvt_pk_bf16_f32 v246, v152, v153\n"
      "v_mfma_f32_16x16x32_bf16 v[168:171], a[248:251], v[212:215], v[168:171]\n"
      "v_mul_f32 v154, v254, v154\n"
      "v_mul_f32 v155, v254, v155\n"
      "v_mfma_f32_16x16x32_bf16 v[168:171], v[4:7], v[176:179], v[168:171]\n"
      "v_cvt_pk_bf16_f32 v247, v154, v155\n"
      "v_mul_f32 v204, v254, v204\n"
      "v_mfma_f32_16x16x32_bf16 v[140:143], v[8:11], v[228:231], v[140:143]\n"
      "ds_read_b128 v[152:155], v123 offset:31232\n"
      "v_mul_f32_e64 v205, v254, v205\n"
      "v_mfma_f32_16x16x32_bf16 v[140:143], v[20:23], v[208:211], v[140:143]\n"
      "ds_write_b128 v126, v[244:247] offset:2048\n"
      "v_mfma_f32_16x16x32_bf16 v[172:175], v[12:15], v[228:231], v[172:175]\n"
      "ds_read_b128 v[212:215], v123 offset:34304\n"
      "v_cvt_pk_bf16_f32 v204, v204, v205\n"
      "v_mfma_f32_16x16x32_bf16 v[172:175], v[24:27], v[208:211], v[172:175]\n"
      "s_waitcnt lgkmcnt(3)\n"
      "v_mul_f32 v206, v254, v206\n"
      "v_mul_f32_e64 v207, v254, v207\n"
      "v_mfma_f32_16x16x32_bf16 v[168:171], v[16:19], v[228:231], v[168:171]\n"
      "v_cvt_pk_bf16_f32 v205, v206, v207\n"
      "v_fma_f32 v254, v144, v144, s39\n"
      "v_mfma_f32_16x16x32_bf16 v[168:171], v[28:31], v[208:211], v[168:171]\n"
      "ds_write_b64 v127, v[204:205] offset:2048\n"
      "v_mfma_f32_16x16x32_bf16 v[204:207], a[216:219], v[196:199], 0\n"
      "ds_read_b128 v[208:211], v249\n"
      "v_fmac_f32 v254, v145, v145\n"
      "v_mfma_f32_16x16x32_bf16 v[204:207], a[228:231], v[240:243], v[204:207]\n"
      "ds_read_b128 v[176:179], v249 offset:1344\n"
      "v_fmac_f32 v254, v146, v146\n"
      "v_mfma_f32_16x16x32_bf16 v[244:247], a[220:223], v[196:199], 0\n"
      "v_fmac_f32 v254, v147, v147\n"
      "v_fmac_f32 v254, v220, v220\n"
      "v_mfma_f32_16x16x32_bf16 v[244:247], a[232:235], v[240:243], v[244:247]\n"
      "global_store_dwordx4 v116, v[232:235], s[54:55] offset:256 sc1\n"
      "v_mfma_f32_16x16x32_bf16 v[228:231], a[224:227], v[196:199], 0\n"
      "v_fmac_f32 v254, v221, v221\n"
      "v_fmac_f32 v254, v222, v222\n"
      "v_mfma_f32_16x16x32_bf16 v[228:231], a[236:239], v[240:243], v[228:231]\n"
      "s_waitcnt lgkmcnt(3)\n"
      "v_fmac_f32 v254, v223, v223\n"
      "v_fmac_f32 v254, v236, v236\n"
      "v_mfma_f32_16x16x32_bf16 v[204:207], a[240:243], v[184:187], v[204:207]\n"
      "v_fmac_f32 v254, v237, v237\n"
      "v_fmac_f32 v254, v238, v238\n"
      "v_mfma_f32_16x16x32_bf16 v[204:207], a[252:255], v[200:203], v[204:207]\n"
      "v_fmac_f32 v254, v239, v239\n"
      "v_mul_f32 v192, v248, v192\n"
      "v_mfma_f32_16x16x32_bf16 v[244:247], a[244:247], v[184:187], v[244:247]\n"
      "ds_read_b128 v[196:199], v249 offset:2688\n"
      "v_mul_f32 v193, v248, v193\n"
      "v_mfma_f32_16x16x32_bf16 v[244:247], v[0:3], v[200:203], v[244:247]\n"
      "ds_read_b128 v[180:183], v249 offset:4032\n"
      "v_cvt_pk_bf16_f32 v192, v192, v193\n"
      "v_mfma_f32_16x16x32_bf16 v[228:231], a[248:251], v[184:187], v[228:231]\n"
      "v_mul_f32 v194, v248, v194\n"
      "s_waitcnt lgkmcnt(2)\n"
      "v_mfma_f32_16x16x32_bf16 v[228:231], v[4:7], v[200:203], v[228:231]\n"
      "v_mul_f32 v195, v248, v195\n"
      "v_cvt_pk_bf16_f32 v193, v194, v195\n"
      "v_mfma_f32_16x16x32_bf16 v[204:207], v[8:11], v[152:155], v[204:207]\n"
      "v_mul_f32 v250, v248, v250\n"
      "v_mul_f32 v251, v248, v251\n"
      "v_mfma_f32_16x16x32_bf16 v[204:207], v[20:23], v[212:215], v[204:207]\n"
      "v_cvt_pk_bf16_f32 v194, v250, v251\n"
      "v_mul_f32 v252, v248, v252\n"
      "v_mfma_f32_16x16x32_bf16 v[244:247], v[12:15], v[152:155], v[244:247]\n"
      "global_store_dwordx4 v117, v[164:167], s[54:55] sc1\n"
      "v_mfma_f32_16x16x32_bf16 v[244:247], v[24:27], v[212:215], v[244:247]\n"
      "ds_read_b128 v[232:235], v249 offset:5376\n"
      "v_mul_f32_e64 v253, v248, v253\n"
      "v_mfma_f32_16x16x32_bf16 v[228:231], v[16:19], v[152:155], v[228:231]\n"
      "ds_read_b128 v[200:203], v249 offset:6720\n"
      "v_cvt_pk_bf16_f32 v195, v252, v253\n"
      "v_mfma_f32_16x16x32_bf16 v[228:231], v[28:31], v[212:215], v[228:231]\n"
      "ds_write_b128 v126, v[192:195] offset:4096\n"
      "v_mfma_f32_16x16x32_bf16 v[212:215], a[216:219], v[208:211], 0\n"
      "s_waitcnt lgkmcnt(3)\n"
      "v_mul_f32 v188, v248, v188\n"
      "v_mul_f32_e64 v189, v248, v189\n"
      "v_mfma_f32_16x16x32_bf16 v[212:215], a[228:231], v[176:179], v[212:215]\n"
      "v_cvt_pk_bf16_f32 v188, v188, v189\n"
      "v_mul_f32_e64 v190, v248, v190\n"
      "v_mfma_f32_16x16x32_bf16 v[224:227], a[220:223], v[208:211], 0\n"
      "ds_read_b128 v[192:195], v249 offset:256\n"
      "v_mul_f32_e64 v191, v248, v191\n"
      "v_mfma_f32_16x16x32_bf16 v[224:227], a[232:235], v[176:179], v[224:227]\n"
      "v_cvt_pk_bf16_f32 v189, v190, v191\n"
      "v_fma_f32 v191, v140, v140, s39\n"
      "v_mfma_f32_16x16x32_bf16 v[152:155], a[224:227], v[208:211], 0\n"
      "ds_read_b128 v[184:187], v249 offset:1600\n"
      "v_fmac_f32_e64 v191, v141, v141\n"
      "v_mfma_f32_16x16x32_bf16 v[152:155], a[236:239], v[176:179], v[152:155]\n"
      "ds_write_b64 v127, v[188:189] offset:4096\n"
      "v_mfma_f32_16x16x32_bf16 v[212:215], a[240:243], v[196:199], v[212:215]\n"
      "v_fmac_f32 v191, v142, v142\n"
      "v_fmac_f32 v191, v143, v143\n"
      "v_mfma_f32_16x16x32_bf16 v[212:215], a[252:255], v[180:183], v[212:215]\n"
      "s_waitcnt lgkmcnt(3)\n"
      "v_fmac_f32 v191, v172, v172\n"
      "v_fmac_f32_e64 v191, v173, v173\n"
      "v_mfma_f32_16x16x32_bf16 v[224:227], a[244:247], v[196:199], v[224:227]\n"
      "ds_read_b128 v[208:211], v249 offset:2944\n"
      "v_fmac_f32 v191, v174, v174\n"
      "v_mfma_f32_16x16x32_bf16 v[224:227], v[0:3], v[180:183], v[224:227]\n"
      "ds_read_b128 v[136:139], v249 offset:4288\n"
      "v_fmac_f32 v191, v175, v175\n"
      "v_mfma_f32_16x16x32_bf16 v[152:155], a[248:251], v[196:199], v[152:155]\n"
      "v_fmac_f32 v191, v168, v168\n"
      "v_fmac_f32 v191, v169, v169\n"
      "v_mfma_f32_16x16x32_bf16 v[152:155], v[4:7], v[180:183], v[152:155]\n"
      "v_fmac_f32 v191, v170, v170\n"
      "v_fmac_f32 v191, v171, v171\n"
      "v_mfma_f32_16x16x32_bf16 v[212:215], v[8:11], v[232:235], v[212:215]\n"
      "ds_read_b128 v[176:179], v249 offset:5632\n"
      "v_lshl_add_u32 v252, v255, 2, s29\n"
      "v_mfma_f32_16x16x32_bf16 v[212:215], v[20:23], v[200:203], v[212:215]\n"
      "s_waitcnt lgkmcnt(3)\n"
      "v_lshrrev_b32 v253, 3, v255\n"
      "v_fma_f32 v255, v204, v204, s39\n"
      "v_mfma_f32_16x16x32_bf16 v[224:227], v[12:15], v[232:235], v[224:227]\n"
      "ds_read_b32 v252, v252\n"
      "v_fmac_f32 v255, v205, v205\n"
      "v_mfma_f32_16x16x32_bf16 v[224:227], v[24:27], v[200:203], v[224:227]\n"
      "ds_read_b128 v[180:183], v249 offset:6976\n"
      "v_fmac_f32 v255, v206, v206\n"
      "v_mfma_f32_16x16x32_bf16 v[152:155], v[16:19], v[232:235], v[152:155]\n"
      "s_lshl_b32 s29, s24, 3\n"
      "v_add_u32 v253, s29, v253\n"
      "v_cmp_gt_u32 s[48:49], s26, v253\n"
      "v_mfma_f32_16x16x32_bf16 v[152:155], v[28:31], v[200:203], v[152:155]\n"
      "v_fmac_f32 v255, v207, v207\n"
      "v_fmac_f32 v255, v244, v244\n"
      "v_mfma_f32_16x16x32_bf16 v[232:235], a[216:219], v[192:195], 0\n"
      "v_fmac_f32 v255, v245, v245\n"
      "v_fmac_f32 v255, v246, v246\n"
      "v_mfma_f32_16x16x32_bf16 v[232:235], a[228:231], v[184:187], v[232:235]\n"
      "s_waitcnt lgkmcnt(3)\n"
      "v_fmac_f32 v255, v247, v247\n"
      "v_fmac_f32 v255, v228, v228\n"
      "v_mfma_f32_16x16x32_bf16 v[196:199], a[220:223], v[192:195], 0\n"
      "v_fmac_f32 v255, v229, v229\n"
      "v_fmac_f32 v255, v230, v230\n"
      "v_mfma_f32_16x16x32_bf16 v[196:199], a[232:235], v[184:187], v[196:199]\n"
      "s_mov_b32 exec_hi, 0\n"
      "global_store_dwordx4 v118, v[216:219], s[54:55] sc1\n"
      "s_mov_b32 exec_hi, -1\n"
      "v_mfma_f32_16x16x32_bf16 v[240:243], a[224:227], v[192:195], 0\n"
      "v_fmac_f32 v255, v231, v231\n"
      "v_mov_b32_e64 v253, v255\n"
      "v_mfma_f32_16x16x32_bf16 v[240:243], a[236:239], v[184:187], v[240:243]\n"
      "v_permlane16_swap_b32 v254, v191\n"
      "v_mfma_f32_16x16x32_bf16 v[232:235], a[240:243], v[208:211], v[232:235]\n"
      "s_waitcnt lgkmcnt(0)\n"
      "v_add_f32_e64 v254, v254, v191\n"
      "v_fma_f32 v192, v212, v212, s39\n"
      "v_mfma_f32_16x16x32_bf16 v[232:235], a[252:255], v[136:139], v[232:235]\n"
      "v_permlane16_swap_b32 v255, v253\n"
      "v_mfma_f32_16x16x32_bf16 v[196:199], a[244:247], v[208:211], v[196:199]\n"
      "v_add_f32 v255, v255, v253\n"
      "v_fmac_f32 v192, v213, v213\n"
      "v_mfma_f32_16x16x32_bf16 v[196:199], v[0:3], v[136:139], v[196:199]\n"
      "v_permlane32_swap_b32 v254, v255\n"
      "v_mfma_f32_16x16x32_bf16 v[240:243], a[248:251], v[208:211], v[240:243]\n"
      "v_add_f32 v254, v254, v255\n"
      "v_fmac_f32 v192, v214, v214\n"
      "v_mfma_f32_16x16x32_bf16 v[240:243], v[4:7], v[136:139], v[240:243]\n"
      "ds_write_b32 v129, v254\n"
      "v_mfma_f32_16x16x32_bf16 v[232:235], v[8:11], v[176:179], v[232:235]\n"
      "v_fmac_f32 v192, v215, v215\n"
      "v_fmac_f32 v192, v224, v224\n"
      "v_mfma_f32_16x16x32_bf16 v[232:235], v[20:23], v[180:183], v[232:235]\n"
      "v_fmac_f32 v192, v225, v225\n"
      "v_fmac_f32 v192, v226, v226\n"
      "v_mfma_f32_16x16x32_bf16 v[196:199], v[12:15], v[176:179], v[196:199]\n"
      "v_fmac_f32 v192, v227, v227\n"
      "v_fmac_f32 v192, v152, v152\n"
      "v_mfma_f32_16x16x32_bf16 v[196:199], v[24:27], v[180:183], v[196:199]\n"
      "v_fmac_f32 v192, v153, v153\n"
      "v_fmac_f32 v192, v154, v154\n"
      "v_mfma_f32_16x16x32_bf16 v[240:243], v[16:19], v[176:179], v[240:243]\n"
      "v_fmac_f32_e64 v192, v155, v155\n"
      "v_fma_f32 v253, v232, v232, s39\n"
      "v_mfma_f32_16x16x32_bf16 v[240:243], v[28:31], v[180:183], v[240:243]\n"
      "v_fmac_f32 v253, v233, v233\n"
      "v_fmac_f32 v253, v234, v234\n"
      "v_fmac_f32 v253, v235, v235\n"
      "v_fmac_f32 v253, v196, v196\n"
      "v_fmac_f32 v253, v197, v197\n"
      "v_fmac_f32 v253, v198, v198\n"
      "v_fmac_f32 v253, v199, v199\n"
      "s_nop 0\n"
      "v_fmac_f32 v253, v240, v240\n"
      "v_fmac_f32 v253, v241, v241\n"
      "v_fmac_f32 v253, v242, v242\n"
      "v_fmac_f32 v253, v243, v243\n"
      "v_mov_b32 v195, v253\n"
      "v_mov_b32 v254, v195\n"
      "v_permlane16_swap_b32 v192, v253\n"
      "v_add_f32 v192, v192, v253\n"
      "v_permlane16_swap_b32 v195, v254\n"
      "v_add_f32 v195, v195, v254\n"
      "s_nop 1\n"
      "v_permlane32_swap_b32 v192, v195\n"
      "v_add_f32_e64 v192, v192, v195\n"
      "ds_write_b32 v129, v192 offset:2048\n"
      "s_waitcnt lgkmcnt(0)\n"
      "s_barrier\n"
      "s_add_u32 s27, s27, 1\n"
      "ds_read_b128 v[176:179], v128\n"
      "ds_read_b128 v[192:195], v128 offset:2048\n"
      "s_sub_u32 s28, s27, 3\n"
      "s_cselect_b64 s[54:55], s[12:13], s[22:23]\n"
      "s_max_i32 m0, s28, 0\n"
      "s_nop 0\n"
      "s_movrels_b32 s9, s80\n"
      "s_lshr_b32 s8, s9, 8\n"
      "s_and_b32 s9, s9, 0xff\n"
      "s_mul_i32 s28, s8, 1373568\n"
      "s_mul_i32 s29, s9, 2688\n"
      "s_add_u32 s28, s28, s29\n"
      "s_cmp_eq_u32 s8, 72\n"
      "s_cselect_b32 s8, 0, 100270464\n"
      "s_cmp_eq_u32 s9, 72\n"
      "s_cselect_b32 s9, 0, 196224\n"
      "s_add_u32 s29, s8, s9\n"
      "s_add_u32 s28, s28, s29\n"
      "s_add_u32 s54, s54, s28\n"
      "s_addc_u32 s55, s55, 0\n"
      "s_cmp_lg_u32 s29, s11\n"
      "s_cbranch_scc1 L_edge_48_%=\n"
      "L_eb_49_%=:\n"
      "ds_read_b128 v[180:183], v119\n"
      "ds_read_b128 v[248:251], v119 offset:6144\n"
      "ds_read_b128 v[208:211], v119 offset:12288\n"
      "ds_read_b128 v[216:219], v120\n"
      "ds_read_b128 v[188:191], v121\n"
      "v_subrev_u32 v254, 73728, v126\n"
      "s_waitcnt lgkmcnt(5)\n"
      "v_add_f32 v176, |v176|, |v177|\n"
      "v_add_f32 v178, |v178|, |v179|\n"
      "v_add_f32 v176, v176, v178\n"
      "v_rsq_f32 v176, v176\n"
      "s_nop 0\n"
      "v_mul_f32 v176, s41, v176\n"
      "v_mov_b32 v179, v176\n"
      "v_add_f32 v192, |v192|, |v193|\n"
      "v_add_f32 v194, |v194|, |v195|\n"
      "v_permlane32_swap_b32 v176, v179\n"
      "v_mov_b32 v193, v176\n"
      "v_add_f32 v192, v192, v194\n"
      "v_rsq_f32 v192, v192\n"
      "v_permlane16_swap_b32 v176, v193\n"
      "v_mul_f32 v192, s41, v192\n"
      "v_mov_b32_e64 v255, v192\n"
      "v_subrev_u32 v253, 73728, v127\n"
      "v_mul_f32 v144, v176, v144\n"
      "v_permlane32_swap_b32 v192, v255\n"
      "v_mov_b32 v255, v192\n"
      "v_mul_f32 v145, v176, v145\n"
      "v_cvt_pk_bf16_f32 v144, v144, v145\n"
      "v_permlane16_swap_b32 v192, v255\n"
      "v_mul_f32 v146, v176, v146\n"
      "v_mul_f32 v147, v176, v147\n"
      "v_cvt_pk_bf16_f32 v145, v146, v147\n"
      "v_mul_f32 v220, v176, v220\n"
      "v_mul_f32_e64 v221, v176, v221\n"
      "v_cvt_pk_bf16_f32 v146, v220, v221\n"
      "v_mul_f32 v222, v176, v222\n"
      "v_mul_f32 v223, v176, v223\n"
      "v_cvt_pk_bf16_f32 v147, v222, v223\n"
      "ds_write_b128 v126, v[144:147] offset:18432\n"
      "v_mul_f32 v236, v176, v236\n"
      "v_mul_f32 v237, v176, v237\n"
      "v_cvt_pk_bf16_f32 v236, v236, v237\n"
      "v_mul_f32 v238, v176, v238\n"
      "v_mul_f32 v239, v176, v239\n"
      "v_cvt_pk_bf16_f32 v237, v238, v239\n"
      "ds_write_b64 v127, v[236:237] offset:18432\n"
      "s_waitcnt lgkmcnt(2)\n"
      "global_store_dwordx4 v116, v[180:183], s[54:55] sc1\n"
      "v_mul_f32 v140, v193, v140\n"
      "v_mul_f32_e64 v141, v193, v141\n"
      "v_cvt_pk_bf16_f32 v140, v140, v141\n"
      "v_mul_f32 v142, v193, v142\n"
      "v_mul_f32 v143, v193, v143\n"
      "v_cvt_pk_bf16_f32 v141, v142, v143\n"
      "v_mul_f32 v172, v193, v172\n"
      "v_mul_f32 v173, v193, v173\n"
      "v_cvt_pk_bf16_f32 v142, v172, v173\n"
      "v_mul_f32 v174, v193, v174\n"
      "v_mul_f32 v175, v193, v175\n"
      "v_cvt_pk_bf16_f32 v143, v174, v175\n"
      "ds_write_b128 v126, v[140:143] offset:20480\n"
      "v_mul_f32 v168, v193, v168\n"
      "v_mul_f32 v169, v193, v169\n"
      "v_cvt_pk_bf16_f32 v168, v168, v169\n"
      "v_mul_f32 v170, v193, v170\n"
      "v_mul_f32 v171, v193, v171\n"
      "v_cvt_pk_bf16_f32 v169, v170, v171\n"
      "ds_write_b64 v127, v[168:169] offset:20480\n"
      "global_store_dwordx4 v116, v[248:251], s[54:55] offset:128 sc1\n"
      "v_mul_f32 v204, v179, v204\n"
      "v_mul_f32 v205, v179, v205\n"
      "v_cvt_pk_bf16_f32 v204, v204, v205\n"
      "v_mul_f32 v206, v179, v206\n"
      "v_mul_f32 v207, v179, v207\n"
      "v_cvt_pk_bf16_f32 v205, v206, v207\n"
      "v_mul_f32 v244, v179, v244\n"
      "v_mul_f32 v245, v179, v245\n"
      "v_cvt_pk_bf16_f32 v206, v244, v245\n"
      "v_mul_f32 v246, v179, v246\n"
      "v_mul_f32 v247, v179, v247\n"
      "v_cvt_pk_bf16_f32 v207, v246, v247\n"
      "ds_write_b128 v126, v[204:207] offset:22528\n"
      "v_mul_f32 v228, v179, v228\n"
      "v_mul_f32 v229, v179, v229\n"
      "v_cvt_pk_bf16_f32 v228, v228, v229\n"
      "v_mul_f32 v230, v179, v230\n"
      "v_mul_f32 v231, v179, v231\n"
      "v_cvt_pk_bf16_f32 v229, v230, v231\n"
      "ds_write_b64 v127, v[228:229] offset:22528\n"
      "global_store_dwordx4 v116, v[208:211], s[54:55] offset:256 sc1\n"
      "v_mul_f32 v212, v192, v212\n"
      "v_mul_f32 v213, v192, v213\n"
      "v_cvt_pk_bf16_f32 v212, v212, v213\n"
      "v_mul_f32 v214, v192, v214\n"
      "v_mul_f32 v215, v192, v215\n"
      "v_cvt_pk_bf16_f32 v213, v214, v215\n"
      "v_mul_f32 v224, v192, v224\n"
      "v_mul_f32 v225, v192, v225\n"
      "v_cvt_pk_bf16_f32 v214, v224, v225\n"
      "v_mul_f32 v226, v192, v226\n"
      "v_mul_f32 v227, v192, v227\n"
      "v_cvt_pk_bf16_f32 v215, v226, v227\n"
      "ds_write_b128 v254, v[212:215] offset:18432\n"
      "v_mul_f32 v152, v192, v152\n"
      "v_mul_f32 v153, v192, v153\n"
      "v_cvt_pk_bf16_f32 v152, v152, v153\n"
      "v_mul_f32 v154, v192, v154\n"
      "v_mul_f32 v155, v192, v155\n"
      "v_cvt_pk_bf16_f32 v153, v154, v155\n"
      "ds_write_b64 v253, v[152:153] offset:18432\n"
      "global_store_dwordx4 v117, v[216:219], s[54:55] sc1\n"
      "v_mul_f32 v232, v255, v232\n"
      "v_mul_f32 v233, v255, v233\n"
      "v_cvt_pk_bf16_f32 v232, v232, v233\n"
      "v_mul_f32 v234, v255, v234\n"
      "v_mul_f32 v235, v255, v235\n"
      "v_cvt_pk_bf16_f32 v233, v234, v235\n"
      "v_mul_f32 v196, v255, v196\n"
      "v_mul_f32 v197, v255, v197\n"
      "v_cvt_pk_bf16_f32 v234, v196, v197\n"
      "v_mul_f32 v198, v255, v198\n"
      "v_mul_f32 v199, v255, v199\n"
      "v_cvt_pk_bf16_f32 v235, v198, v199\n"
      "ds_write_b128 v254, v[232:235] offset:20480\n"
      "v_mul_f32 v240, v255, v240\n"
      "v_mul_f32 v241, v255, v241\n"
      "v_cvt_pk_bf16_f32 v240, v240, v241\n"
      "v_mul_f32_e64 v242, v255, v242\n"
      "v_mul_f32 v243, v255, v243\n"
      "v_cvt_pk_bf16_f32 v241, v242, v243\n"
      "ds_write_b64 v253, v[240:241] offset:20480\n"
      "s_mov_b32 exec_hi, 0\n"
      "global_store_dwordx4 v118, v[188:191], s[54:55] sc1\n"
      "s_mov_b32 exec_hi, -1\n"
      "s_waitcnt lgkmcnt(0)\n"
      "s_barrier\n"
      "s_add_u32 s27, s27, 1\n"
      "v_subrev_u32 v253, 73728, v119\n"
      "ds_read_b128 v[192:195], v119 offset:18432\n"
      "ds_read_b128 v[240:243], v119 offset:24576\n"
      "ds_read_b128 v[180:183], v119 offset:30720\n"
      "ds_read_b128 v[212:215], v120 offset:18432\n"
      "ds_read_b128 v[196:199], v121 offset:18432\n"
      "ds_read_b128 v[232:235], v253 offset:18432\n"
      "ds_read_b128 v[188:191], v253 offset:24576\n"
      "ds_read_b128 v[140:143], v253 offset:30720\n"
      "s_sub_u32 s28, s27, 3\n"
      "s_cselect_b64 s[54:55], s[12:13], s[22:23]\n"
      "s_max_i32 m0, s28, 0\n"
      "s_nop 0\n"
      "s_movrels_b32 s9, s80\n"
      "s_lshr_b32 s8, s9, 8\n"
      "s_and_b32 s9, s9, 0xff\n"
      "s_mul_i32 s28, s8, 1373568\n"
      "s_mul_i32 s29, s9, 2688\n"
      "s_add_u32 s28, s28, s29\n"
      "s_cmp_eq_u32 s8, 72\n"
      "s_cselect_b32 s8, 0, 100270464\n"
      "s_cmp_eq_u32 s9, 72\n"
      "s_cselect_b32 s9, 0, 196224\n"
      "s_add_u32 s29, s8, s9\n"
      "s_add_u32 s28, s28, s29\n"
      "s_add_u32 s54, s54, s28\n"
      "s_addc_u32 s55, s55, 0\n"
      "s_cmp_lg_u32 s29, s11\n"
      "s_cbranch_scc1 L_edge_50_%=\n"
      "L_eb_51_%=:\n"
      "s_waitcnt lgkmcnt(5)\n"
      "global_store_dwordx4 v116, v[192:195], s[54:55] sc1\n"
      "global_store_dwordx4 v116, v[240:243], s[54:55] offset:128 sc1\n"
      "global_store_dwordx4 v116, v[180:183], s[54:55] offset:256 sc1\n"
      "s_waitcnt lgkmcnt(0)\n"
      "global_store_dwordx4 v117, v[212:215], s[54:55] sc1\n"
      "s_mov_b32 exec_hi, 0\n"
      "global_store_dwordx4 v118, v[196:199], s[54:55] sc1\n"
      "s_mov_b32 exec_hi, -1\n"
      "s_nop 0\n"
      "s_mov_b64 exec, s[48:49]\n"
      "s_cbranch_execz L_np_52_%=\n"
      "global_store_dwordx4 v252, v[232:235], s[22:23] sc1\n"
      "global_store_dwordx4 v252, v[188:191], s[22:23] offset:128 sc1\n"
      "global_store_dwordx4 v252, v[140:143], s[22:23] offset:256 sc1\n"
      "L_np_52_%=:\n"
      "s_mov_b64 exec, -1\n"
      "s_endpgm\n"
      "L_gen_8_%=:\n"
      "s_waitcnt lgkmcnt(0)\n"
      "s_nop 7\n"
      "s_nop 7\n"
      "s_cmp_eq_u64 s[48:49], 0\n"
      "s_cbranch_scc1 L_go_53_%=\n"
      "s_mov_b32 m0, s27\n"
      "s_nop 0\n"
      "s_movrels_b32 s9, s80\n"
      "s_lshr_b32 s8, s9, 8\n"
      "s_and_b32 s9, s9, 0xff\n"
      "s_mul_i32 s10, s8, 73\n"
      "s_add_u32 s10, s10, s9\n"
      "s_mul_i32 s30, s10, 4802\n"
      "s_add_u32 s30, s30, s18\n"
      "s_and_b32 s29, s30, 15\n"
      "s_add_u32 s30, s29, 110592\n"
      "s_andn2_b64 s[34:35], s[48:49], 0x801\n"
      "s_cbranch_scc1 L_gen_9_%=\n"
      "v_mbcnt_lo_u32_b32 v164, -1, 0\n"
      "v_mbcnt_hi_u32_b32 v164, -1, v164\n"
      "v_lshlrev_b32 v165, 1, v164\n"
      "v_add_u32 v166, 4802, v165\n"
      "v_cmp_gt_u32 vcc, s29, v165\n"
      "s_nop 1\n"
      "v_cndmask_b32 v165, v166, v165, vcc\n"
      "v_add_u32 v165, 110592, v165\n"
      "v_mov_b32 v166, 0\n"
      "s_mov_b64 exec, 0x7f\n"
      "ds_write_b16 v165, v166\n"
      "s_mov_b64 exec, -1\n"
      "v_lshlrev_b32 v165, 4, v164\n"
      "v_add_u32 v165, 110592, v165\n"
      "v_min_u32 v164, 111296, v165\n"
      "ds_read_b128 v[232:235], v165\n"
      "ds_read_b128 v[192:195], v165 offset:1024\n"
      "ds_read_b128 v[148:151], v165 offset:2048\n"
      "ds_read_b128 v[160:163], v165 offset:3072\n"
      "ds_read_b128 v[188:191], v164 offset:4096\n"
      "s_waitcnt lgkmcnt(2)\n"
      "v_or3_b32 v232, v232, v233, v234\n"
      "v_or3_b32 v232, v232, v235, v192\n"
      "v_or3_b32 v232, v232, v193, v194\n"
      "v_or3_b32 v232, v232, v195, v148\n"
      "v_or3_b32 v232, v232, v149, v150\n"
      "s_waitcnt lgkmcnt(0)\n"
      "v_or3_b32 v232, v232, v151, v160\n"
      "v_or3_b32 v232, v232, v161, v162\n"
      "v_or3_b32 v232, v232, v163, v188\n"
      "v_or3_b32 v232, v232, v189, v190\n"
      "v_bitop3_b32 v232, v232, v191, s40 bitop3:0xa8\n"
      "v_cmp_ne_u32 s[48:49], 0, v232\n"
      "s_cmp_lg_u64 s[48:49], 0\n"
      "s_cbranch_scc1 L_gen_9_%=\n"
      "s_cmp_eq_u64 s[50:51], 0\n"
      "s_cbranch_scc1 L_fast_11_%=\n"
      "L_go_53_%=:\n"
      "v_mfma_f32_16x16x32_bf16 v[232:235], v[180:183], v[172:175], v[90:93]\n"
      "ds_read_b128 v[164:167], v128 offset:3072\n"
      "v_max3_f32 v195, v184, v185, v186\n"
      "v_mfma_f32_16x16x32_bf16 v[232:235], v[34:37], v[38:41], v[232:235]\n"
      "v_max3_f32 v193, v187, v168, v169\n"
      "v_max3_f32 v195, v195, v170, v171\n"
      "v_mfma_f32_16x16x32_bf16 v[188:191], v[248:251], v[172:175], v[94:97]\n"
      "v_max3_f32 v193, v193, v156, v157\n"
      "v_max3_f32 v195, v195, v158, v159\n"
      "v_mfma_f32_16x16x32_bf16 v[188:191], v[40:43], v[38:41], v[188:191]\n"
      "v_max3_f32 v195, v195, v193, v152\n"
      "v_mov_b32 v193, v195\n"
      "v_mfma_f32_16x16x32_bf16 v[160:163], v[136:139], v[172:175], v[72:75]\n"
      "s_nop 0\n"
      "v_permlane16_swap_b32 v195, v193\n"
      "v_mfma_f32_16x16x32_bf16 v[160:163], v[228:231], v[248:251], v[160:163]\n"
      "v_max_f32 v195, v195, v193\n"
      "v_mov_b32 v193, v195\n"
      "v_mfma_f32_16x16x32_bf16 v[160:163], v[144:147], v[38:41], v[160:163]\n"
      "s_nop 0\n"
      "v_permlane32_swap_b32 v195, v193\n"
      "v_mfma_f32_16x16x32_bf16 v[148:151], v[176:179], v[172:175], v[98:101]\n"
      "v_max_f32 v195, v195, v193\n"
      "v_sub_f32 v184, v184, v195\n"
      "v_mfma_f32_16x16x32_bf16 v[148:151], v[46:49], v[38:41], v[148:151]\n"
      "v_sub_f32 v185, v185, v195\n"
      "v_sub_f32 v186, v186, v195\n"
      "v_mfma_f32_16x16x32_bf16 v[196:199], v[180:183], v[224:227], v[102:105]\n"
      "v_sub_f32 v187, v187, v195\n"
      "v_sub_f32 v168, v168, v195\n"
      "v_mfma_f32_16x16x32_bf16 v[196:199], v[34:37], v[44:47], v[196:199]\n"
      "v_sub_f32 v169, v169, v195\n"
      "v_sub_f32 v170, v170, v195\n"
      "v_mfma_f32_16x16x32_bf16 v[172:175], v[248:251], v[224:227], v[106:109]\n"
      "s_waitcnt lgkmcnt(0)\n"
      "v_sub_f32 v171, v171, v195\n"
      "v_sub_f32 v156, v156, v195\n"
      "v_mfma_f32_16x16x32_bf16 v[172:175], v[40:43], v[44:47], v[172:175]\n"
      "v_sub_f32 v157, v157, v195\n"
      "v_sub_f32 v158, v158, v195\n"
      "v_mfma_f32_16x16x32_bf16 v[248:251], v[136:139], v[224:227], v[74:77]\n"
      "v_sub_f32 v159, v159, v195\n"
      "v_sub_f32 v152, v152, v195\n"
      "v_mfma_f32_16x16x32_bf16 v[248:251], v[228:231], v[176:179], v[248:251]\n"
      "v_exp_f32 v184, v184\n"
      "v_mfma_f32_16x16x32_bf16 v[248:251], v[144:147], v[44:47], v[248:251]\n"
      "v_exp_f32 v185, v185\n"
      "v_mfma_f32_16x16x32_bf16 v[192:195], v[176:179], v[224:227], v[110:113]\n"
      "v_exp_f32_e64 v186, v186\n"
      "v_mfma_f32_16x16x32_bf16 v[192:195], v[46:49], v[44:47], v[192:195]\n"
      "v_cvt_pk_bf16_f32 v184, v184, v185\n"
      "v_bitop3_b32 v164, v164, v165, s4 bitop3:0x7e\n"
      "v_mfma_f32_16x16x32_bf16 v[176:179], v[228:231], v[136:139], v[76:79]\n"
      "v_exp_f32 v187, v187\n"
      "v_exp_f32 v168, v168\n"
      "v_cvt_pk_bf16_f32 v185, v186, v187\n"
      "v_exp_f32 v169, v169\n"
      "v_exp_f32 v170, v170\n"
      "v_cvt_pk_bf16_f32 v186, v168, v169\n"
      "v_mfma_f32_16x16x16_bf16 v[176:179], v[146:147], v[144:145], v[176:179]\n"
      "v_exp_f32 v171, v171\n"
      "v_exp_f32 v156, v156\n"
      "v_cvt_pk_bf16_f32 v187, v170, v171\n"
      "v_exp_f32 v157, v157\n"
      "v_exp_f32 v158, v158\n"
      "v_exp_f32 v159, v159\n"
      "v_exp_f32 v152, v152\n"
      "v_cvt_pk_bf16_f32 v62, v156, v157\n"
      "v_cvt_pk_bf16_f32 v63, v158, v159\n"
      "v_cvt_pk_bf16_f32 v64, v152, 0\n"
      "v_bitop3_b32 v166, v166, v167, s4 bitop3:0x7e\n"
      "v_or_b32 v164, v164, v166\n"
      "v_mfma_f32_16x16x32_bf16 v[136:139], v[66:69], v[184:187], 0\n"
      "v_cmp_ne_u32 vcc, 0, v164\n"
      "v_max3_f32 v158, v232, v233, v234\n"
      "v_mfma_f32_16x16x32_bf16 v[136:139], v[66:69], v[62:65], v[136:139]\n"
      "s_mov_b64 s[48:49], vcc\n"
      "v_max3_f32 v159, v235, v188, v189\n"
      "v_max3_f32 v158, v158, v190, v191\n"
      "v_mfma_f32_16x16x32_bf16 v[168:171], v[240:243], v[184:187], 0\n"
      "s_cbranch_vccnz L_flg_54_%=\n"
      "L_fb_55_%=:\n"
      "v_mfma_f32_16x16x32_bf16 v[168:171], v[50:53], v[62:65], v[168:171]\n"
      "v_max3_f32 v159, v159, v148, v149\n"
      "v_max3_f32 v158, v158, v150, v151\n"
      "v_mfma_f32_16x16x32_bf16 v[164:167], v[140:143], v[184:187], 0\n"
      "v_max3_f32 v158, v158, v159, v160\n"
      "v_mov_b32 v137, v158\n"
      "v_mfma_f32_16x16x32_bf16 v[164:167], v[54:57], v[62:65], v[164:167]\n"
      "s_nop 0\n"
      "v_permlane16_swap_b32 v158, v137\n"
      "v_mfma_f32_16x16x32_bf16 v[154:157], v[200:203], v[184:187], 0\n"
      "v_max_f32 v158, v158, v137\n"
      "v_mov_b32 v159, v158\n"
      "v_mfma_f32_16x16x32_bf16 v[154:157], v[58:61], v[62:65], v[154:157]\n"
      "s_nop 0\n"
      "v_permlane32_swap_b32 v158, v159\n"
      "v_max_f32 v158, v158, v159\n"
      "v_sub_f32 v232, v232, v158\n"
      "v_sub_f32 v233, v233, v158\n"
      "v_sub_f32 v234, v234, v158\n"
      "v_sub_f32 v235, v235, v158\n"
      "v_sub_f32 v188, v188, v158\n"
      "v_sub_f32 v189, v189, v158\n"
      "v_sub_f32 v190, v190, v158\n"
      "v_sub_f32 v191, v191, v158\n"
      "v_sub_f32 v148, v148, v158\n"
      "v_sub_f32 v149, v149, v158\n"
      "v_sub_f32 v150, v150, v158\n"
      "v_sub_f32 v151, v151, v158\n"
      "v_sub_f32 v160, v160, v158\n"
      "v_exp_f32 v232, v232\n"
      "v_exp_f32 v233, v233\n"
      "v_exp_f32 v234, v234\n"
      "v_cvt_pk_bf16_f32 v232, v232, v233\n"
      "v_exp_f32 v235, v235\n"
      "v_exp_f32 v188, v188\n"
      "v_cvt_pk_bf16_f32 v233, v234, v235\n"
      "v_exp_f32 v189, v189\n"
      "v_exp_f32 v190, v190\n"
      "v_cvt_pk_bf16_f32 v234, v188, v189\n"
      "v_exp_f32 v191, v191\n"
      "v_exp_f32 v148, v148\n"
      "v_cvt_pk_bf16_f32 v235, v190, v191\n"
      "v_exp_f32 v149, v149\n"
      "v_exp_f32 v150, v150\n"
      "v_exp_f32 v151, v151\n"
      "v_exp_f32 v160, v160\n"
      "v_cvt_pk_bf16_f32 v62, v148, v149\n"
      "v_cvt_pk_bf16_f32 v63, v150, v151\n"
      "v_cvt_pk_bf16_f32 v64, v160, 0\n"
      "v_rcp_f32 v136, v136\n"
      "v_max3_f32 v158, v196, v197, v198\n"
      "v_mfma_f32_16x16x32_bf16 v[184:187], v[66:69], v[232:235], 0\n"
      "s_add_u32 s28, s27, 2\n"
      "v_mul_f32 v168, v136, v168\n"
      "v_mul_f32 v169, v136, v169\n"
      "v_mfma_f32_16x16x32_bf16 v[184:187], v[66:69], v[62:65], v[184:187]\n"
      "s_min_u32 m0, s28, s3\n"
      "v_cvt_pk_bf16_f32 v168, v168, v169\n"
      "v_mul_f32 v170, v136, v170\n"
      "v_mfma_f32_16x16x32_bf16 v[148:151], v[240:243], v[232:235], 0\n"
      "s_movrels_b32 s9, s80\n"
      "v_mul_f32 v171, v136, v171\n"
      "v_cvt_pk_bf16_f32 v169, v170, v171\n"
      "v_mfma_f32_16x16x32_bf16 v[148:151], v[50:53], v[62:65], v[148:151]\n"
      "s_lshr_b32 s8, s9, 8\n"
      "v_mul_f32 v164, v136, v164\n"
      "v_mul_f32_e64 v165, v136, v165\n"
      "v_mfma_f32_16x16x32_bf16 v[186:189], v[140:143], v[232:235], 0\n"
      "s_and_b32 s9, s9, 0xff\n"
      "v_cvt_pk_bf16_f32 v170, v164, v165\n"
      "v_mul_f32 v166, v136, v166\n"
      "v_mfma_f32_16x16x32_bf16 v[186:189], v[54:57], v[62:65], v[186:189]\n"
      "s_mul_i32 s10, s8, 73\n"
      "v_mul_f32 v167, v136, v167\n"
      "v_cvt_pk_bf16_f32 v171, v166, v167\n"
      "v_mfma_f32_16x16x32_bf16 v[164:167], v[200:203], v[232:235], 0\n"
      "s_add_u32 s10, s10, s9\n"
      "v_mul_f32 v154, v136, v154\n"
      "v_mul_f32_e64 v155, v136, v155\n"
      "v_mfma_f32_16x16x32_bf16 v[164:167], v[58:61], v[62:65], v[164:167]\n"
      "ds_write_b128 v124, v[168:171]\n"
      "v_cvt_pk_bf16_f32 v154, v154, v155\n"
      "v_mul_f32 v156, v136, v156\n"
      "v_mul_f32 v157, v136, v157\n"
      "v_cvt_pk_bf16_f32 v155, v156, v157\n"
      "v_max3_f32 v185, v199, v172, v173\n"
      "v_max3_f32 v158, v158, v174, v175\n"
      "v_max3_f32 v185, v185, v192, v193\n"
      "v_max3_f32 v158, v158, v194, v195\n"
      "v_max3_f32 v158, v158, v185, v248\n"
      "v_mov_b32 v185, v158\n"
      "s_mul_i32 s28, s10, 4802\n"
      "s_nop 0\n"
      "v_permlane16_swap_b32 v158, v185\n"
      "v_max_f32 v158, v158, v185\n"
      "v_mov_b32 v185, v158\n"
      "s_add_u32 s56, s18, s28\n"
      "s_nop 0\n"
      "v_permlane32_swap_b32 v158, v185\n"
      "v_max_f32 v158, v158, v185\n"
      "v_sub_f32 v196, v196, v158\n"
      "v_sub_f32 v197, v197, v158\n"
      "v_sub_f32 v198, v198, v158\n"
      "v_sub_f32 v199, v199, v158\n"
      "v_sub_f32 v172, v172, v158\n"
      "v_sub_f32 v173, v173, v158\n"
      "v_sub_f32 v174, v174, v158\n"
      "v_sub_f32 v175, v175, v158\n"
      "v_sub_f32 v192, v192, v158\n"
      "v_sub_f32 v193, v193, v158\n"
      "v_sub_f32 v194, v194, v158\n"
      "v_sub_f32 v195, v195, v158\n"
      "v_sub_f32 v248, v248, v158\n"
      "v_exp_f32 v196, v196\n"
      "v_exp_f32 v197, v197\n"
      "v_exp_f32 v198, v198\n"
      "v_cvt_pk_bf16_f32 v196, v196, v197\n"
      "v_exp_f32 v199, v199\n"
      "v_exp_f32 v172, v172\n"
      "v_cvt_pk_bf16_f32 v197, v198, v199\n"
      "s_addc_u32 s57, s19, 0\n"
      "ds_write_b64 v125, v[154:155]\n"
      "v_exp_f32 v173, v173\n"
      "v_exp_f32 v174, v174\n"
      "v_cvt_pk_bf16_f32 v198, v172, v173\n"
      "v_exp_f32 v175, v175\n"
      "v_exp_f32 v192, v192\n"
      "v_cvt_pk_bf16_f32 v199, v174, v175\n"
      "v_exp_f32 v193, v193\n"
      "v_exp_f32 v194, v194\n"
      "v_exp_f32 v195, v195\n"
      "v_exp_f32 v248, v248\n"
      "v_cvt_pk_bf16_f32 v62, v192, v193\n"
      "v_cvt_pk_bf16_f32 v63, v194, v195\n"
      "v_cvt_pk_bf16_f32 v64, v248, 0\n"
      "v_cndmask_b32 v153, v153, v161, s[42:43]\n"
      "v_cndmask_b32 v153, v153, v249, s[44:45]\n"
      "v_mfma_f32_16x16x32_bf16 v[136:139], v[66:69], v[196:199], 0\n"
      "s_and_b32 s56, s56, -16\n"
      "v_cndmask_b32 v153, v153, v177, s[46:47]\n"
      "s_sub_u32 s58, s56, 110592\n"
      "v_mfma_f32_16x16x32_bf16 v[136:139], v[66:69], v[62:65], v[136:139]\n"
      "s_subb_u32 s59, s57, 0\n"
      "v_max_f32_dpp v185, v153, v153 row_ror:8 row_mask:0xf bank_mask:0xf\n"
      "v_rcp_f32 v184, v184\n"
      "v_mfma_f32_16x16x32_bf16 v[172:175], v[240:243], v[196:199], 0\n"
      "s_sub_u32 s56, s56, 73728\n"
      "v_max_f32_dpp v185, v185, v185 row_ror:4 row_mask:0xf bank_mask:0xf\n"
      "v_mul_f32 v148, v184, v148\n"
      "v_mfma_f32_16x16x32_bf16 v[172:175], v[50:53], v[62:65], v[172:175]\n"
      "s_subb_u32 s57, s57, 0\n"
      "v_max_f32_dpp v185, v185, v185 row_ror:2 row_mask:0xf bank_mask:0xf\n"
      "v_mul_f32 v149, v184, v149\n"
      "v_mfma_f32_16x16x32_bf16 v[156:159], v[140:143], v[196:199], 0\n"
      "s_mul_i32 s28, s8, 1373568\n"
      "v_max_f32_dpp v185, v185, v185 row_ror:1 row_mask:0xf bank_mask:0xf\n"
      "v_cvt_pk_bf16_f32 v148, v148, v149\n"
      "v_mfma_f32_16x16x32_bf16 v[156:159], v[54:57], v[62:65], v[156:159]\n"
      "s_mul_i32 s29, s9, 2688\n"
      "v_max_f32_dpp v185, v185, v185 row_bcast:15 row_mask:0xa bank_mask:0xf\n"
      "v_mul_f32_e64 v150, v184, v150\n"
      "v_mfma_f32_16x16x32_bf16 v[168:171], v[200:203], v[196:199], 0\n"
      "s_add_u32 s28, s28, s29\n"
      "v_max_f32_dpp v185, v185, v185 row_bcast:31 row_mask:0xc bank_mask:0xf\n"
      "v_mul_f32 v151, v184, v151\n"
      "v_mfma_f32_16x16x32_bf16 v[168:171], v[58:61], v[62:65], v[168:171]\n"
      "s_cmp_eq_u32 s8, 72\n"
      "v_readlane_b32 s31, v185, 63\n"
      "v_cvt_pk_bf16_f32 v149, v150, v151\n"
      "v_mul_f32 v186, v184, v186\n"
      "v_subrev_f32 v153, s31, v153\n"
      "v_exp_f32 v153, v153\n"
      "v_mul_f32 v187, v184, v187\n"
      "v_cvt_pk_bf16_f32 v153, v153, v153\n"
      "v_cvt_pk_bf16_f32 v150, v186, v187\n"
      "v_mul_f32 v188, v184, v188\n"
      "v_mul_f32 v189, v184, v189\n"
      "v_cvt_pk_bf16_f32 v151, v188, v189\n"
      "v_mul_f32 v164, v184, v164\n"
      "v_mul_f32 v165, v184, v165\n"
      "v_cvt_pk_bf16_f32 v164, v164, v165\n"
      "v_mul_f32 v166, v184, v166\n"
      "v_mul_f32 v167, v184, v167\n"
      "v_cvt_pk_bf16_f32 v165, v166, v167\n"
      "s_cselect_b32 s8, 0, 100270464\n"
      "ds_write_b16 v130, v153\n"
      "v_mov_b32_e64 v166, 0\n"
      "s_cmp_eq_u32 s9, 72\n"
      "ds_read_b128 v[192:195], v131\n"
      "ds_read_b128 v[232:235], v131 offset:64\n"
      "ds_write_b128 v124, v[148:151] offset:256\n"
      "ds_write_b64 v125, v[164:165] offset:256\n"
      "ds_write_b32 v129, v166\n"
      "ds_read_b128 v[164:167], v114 offset:18432\n"
      "ds_read_b128 v[184:187], v115 offset:18432\n"
      "s_cselect_b32 s9, 0, 196224\n"
      "s_add_u32 s29, s8, s9\n"
      "s_add_u32 s28, s28, s29\n"
      "s_add_u32 s52, s16, s28\n"
      "s_addc_u32 s53, s17, 0\n"
      "s_cmp_lg_u32 s29, s11\n"
      "s_cbranch_scc1 L_edge_57_%=\n"
      "L_eb_58_%=:\n"
      "s_waitcnt lgkmcnt(4)\n"
      "v_mfma_f32_16x16x32_bf16 v[148:151], v[192:195], v[66:69], 0\n"
      "v_mfma_f32_16x16x32_bf16 v[148:151], v[232:235], v[66:69], v[148:151]\n"
      "v_mfma_f32_16x16x32_bf16 v[196:199], v[192:195], v[240:243], 0\n"
      "v_mfma_f32_16x16x32_bf16 v[196:199], v[232:235], v[50:53], v[196:199]\n"
      "v_mfma_f32_16x16x32_bf16 v[240:243], v[192:195], v[140:143], 0\n"
      "s_waitcnt lgkmcnt(0)\n"
      "v_mfma_f32_16x16x32_bf16 v[240:243], v[232:235], v[54:57], v[240:243]\n"
      "v_mfma_f32_16x16x32_bf16 v[140:143], v[192:195], v[200:203], 0\n"
      "s_nop 1\n"
      "v_mov_b32_dpp v148, v148 quad_perm:[0,0,0,0] row_mask:0xf bank_mask:0xf\n"
      "v_mfma_f32_16x16x32_bf16 v[140:143], v[232:235], v[58:61], v[140:143]\n"
      "v_rcp_f32 v148, v148\n"
      "v_mfma_f32_16x16x32_bf16 v[232:235], a[0:3], v[164:167], 0\n"
      "s_nop 0\n"
      "v_cndmask_b32 v196, v196, v240, s[42:43]\n"
      "v_mfma_f32_16x16x32_bf16 v[232:235], a[36:39], v[184:187], v[232:235]\n"
      "v_mfma_f32_16x16x32_bf16 v[192:195], a[4:7], v[164:167], 0\n"
      "s_nop 1\n"
      "v_cndmask_b32 v196, v196, v140, s[44:45]\n"
      "v_mfma_f32_16x16x32_bf16 v[192:195], a[40:43], v[184:187], v[192:195]\n"
      "v_mul_f32_e64 v196, v148, v196\n"
      "v_cvt_pk_bf16_f32 v196, v196, v196\n"
      "ds_write_b16 v135, v196\n"
      "s_branch L_join_10_%=\n"
      "L_flg_54_%=:\n"
      "v_cmp_gt_i32 vcc, 0, v164\n"
      "s_cbranch_vccz L_fb_55_%=\n"
      "s_sleep 1\n"
      "ds_read_b128 v[164:167], v128 offset:3072\n"
      "s_waitcnt lgkmcnt(0)\n"
      "v_bitop3_b32 v164, v164, v165, s4 bitop3:0x7e\n"
      "v_bitop3_b32 v166, v166, v167, s4 bitop3:0x7e\n"
      "v_or_b32 v164, v164, v166\n"
      "v_cmp_ne_u32 s[48:49], 0, v164\n"
      "s_branch L_flg_54_%=\n"
      "L_edge_57_%=:\n"
      "s_mov_b32 s11, s29\n"
      "s_sub_u32 s28, s60, s8\n"
      "s_sub_u32 s29, s61, s9\n"
      "s_mov_b32 s60, s8\n"
      "s_mov_b32 s61, s9\n"
      "s_mov_b64 exec, s[64:65]\n"
      "v_add_u32 v116, s28, v116\n"
      "s_mov_b64 exec, s[70:71]\n"
      "v_add_u32 v116, s29, v116\n"
      "s_mov_b64 exec, s[66:67]\n"
      "v_add_u32 v117, s28, v117\n"
      "s_mov_b64 exec, s[72:73]\n"
      "v_add_u32 v117, s29, v117\n"
      "s_mov_b64 exec, s[68:69]\n"
      "v_add_u32 v118, s28, v118\n"
      "s_mov_b64 exec, s[74:75]\n"
      "v_add_u32_e64 v118, s29, v118\n"
      "s_mov_b64 exec, -1\n"
      "s_nop 4\n"
      "s_branch L_eb_58_%=\n"
      "L_gen_9_%=:\n"
      "s_waitcnt lgkmcnt(0)\n"
      "s_nop 7\n"
      "s_nop 7\n"
      "v_mfma_f32_16x16x32_bf16 v[164:167], v[180:183], v[172:175], v[90:93]\n"
      "s_movk_i32 s31, 0x3fb9\n"
      "v_mbcnt_lo_u32_b32 v232, -1, 0\n"
      "v_mbcnt_hi_u32_b32 v232, -1, v232\n"
      "v_mfma_f32_16x16x32_bf16 v[164:167], v[34:37], v[38:41], v[164:167]\n"
      "v_and_b32 v233, 15, v232\n"
      "v_lshrrev_b32 v234, 4, v232\n"
      "v_mfma_f32_16x16x32_bf16 v[192:195], v[248:251], v[172:175], v[94:97]\n"
      "v_mul_u32_u24 v235, 0x62, v233\n"
      "v_lshl_add_u32 v235, v234, 3, v235\n"
      "v_mfma_f32_16x16x32_bf16 v[192:195], v[40:43], v[38:41], v[192:195]\n"
      "v_add_u32 v235, s30, v235\n"
      "v_and_b32 v232, -4, v235\n"
      "v_and_b32 v235, 2, v235\n"
      "v_lshlrev_b32 v234, 2, v234\n"
      "v_sub_u32_e64 v233, v233, v234\n"
      "v_and_b32_e64 v234, 1, v233\n"
      "v_lshlrev_b32_e64 v234, 4, v234\n"
      "v_lshlrev_b32 v234, v234, s31\n"
      "v_lshrrev_b32_e64 v233, 1, v233\n"
      "v_cmp_eq_u32 vcc, 0, v233\n"
      "s_nop 1\n"
      "v_cndmask_b32 v154, 0, v234, vcc\n"
      "v_cmp_eq_u32 vcc, 1, v233\n"
      "s_nop 1\n"
      "v_cndmask_b32 v155, 0, v234, vcc\n"
      "v_mfma_f32_16x16x32_bf16 v[148:151], v[136:139], v[172:175], v[72:75]\n"
      "ds_read2_b32 v[160:161], v232 offset0:0 offset1:1\n"
      "v_mfma_f32_16x16x32_bf16 v[148:151], v[228:231], v[248:251], v[148:151]\n"
      "ds_read2_b32 v[162:163], v232 offset0:8 offset1:9\n"
      "v_mfma_f32_16x16x32_bf16 v[148:151], v[144:147], v[38:41], v[148:151]\n"
      "ds_read2_b32 v[188:189], v232 offset0:16 offset1:17\n"
      "ds_read2_b32 v[190:191], v232 offset0:2 offset1:10\n"
      "ds_read2_b32 v[196:197], v232 offset0:18 offset1:24\n"
      "v_add_u32 v232, 1568, v232\n"
      "ds_read2_b32 v[198:199], v232 offset0:0 offset1:1\n"
      "ds_read2_b32 v[204:205], v232 offset0:8 offset1:9\n"
      "ds_read2_b32 v[206:207], v232 offset0:16 offset1:17\n"
      "ds_read2_b32 v[208:209], v232 offset0:2 offset1:10\n"
      "ds_read2_b32 v[210:211], v232 offset0:18 offset1:24\n"
      "v_add_u32 v232, 1568, v232\n"
      "ds_read2_b32 v[150:151], v232 offset0:0 offset1:1\n"
      "ds_read2_b32 v[212:213], v232 offset0:8 offset1:9\n"
      "ds_read2_b32 v[214:215], v232 offset0:16 offset1:17\n"
      "ds_read2_b32 v[216:217], v232 offset0:2 offset1:10\n"
      "ds_read2_b32 v[218:219], v232 offset0:18 offset1:24\n"
      "s_waitcnt lgkmcnt(13)\n"
      "v_alignbyte_b32 v160, v161, v160, v235\n"
      "v_alignbyte_b32 v162, v163, v162, v235\n"
      "s_waitcnt lgkmcnt(10)\n"
      "v_alignbyte_b32 v188, v189, v188, v235\n"
      "v_alignbyte_b32 v161, v190, v161, v235\n"
      "v_alignbyte_b32 v163, v191, v163, v235\n"
      "v_mfma_f32_16x16x32_bf16 v[220:223], v[176:179], v[172:175], v[98:101]\n"
      "v_alignbyte_b32 v189, v196, v189, v235\n"
      "v_alignbyte_b32 v197, v197, v197, v235\n"
      "v_mfma_f32_16x16x32_bf16 v[220:223], v[46:49], v[38:41], v[220:223]\n"
      "s_waitcnt lgkmcnt(6)\n"
      "v_lshlrev_b32 v197, 16, v197\n"
      "v_fmac_f32_e64 v152, s37, v197\n"
      "v_mfma_f32_16x16x16_bf16 v[184:187], v[154:155], v[160:161], v[184:187]\n"
      "ds_read_b128 v[172:175], v128 offset:3072\n"
      "v_alignbyte_b32 v198, v199, v198, v235\n"
      "v_mfma_f32_16x16x16_bf16 v[168:171], v[154:155], v[162:163], v[168:171]\n"
      "v_alignbyte_b32 v199, v208, v199, v235\n"
      "v_mfma_f32_16x16x16_bf16 v[156:159], v[154:155], v[188:189], v[156:159]\n"
      "v_alignbyte_b32 v204, v205, v204, v235\n"
      "v_alignbyte_b32 v205, v209, v205, v235\n"
      "v_mfma_f32_16x16x32_bf16 v[160:163], v[180:183], v[224:227], v[102:105]\n"
      "v_alignbyte_b32 v206, v207, v206, v235\n"
      "v_max3_f32 v234, v184, v185, v186\n"
      "v_mfma_f32_16x16x32_bf16 v[160:163], v[34:37], v[44:47], v[160:163]\n"
      "s_waitcnt lgkmcnt(1)\n"
      "v_alignbyte_b32 v207, v210, v207, v235\n"
      "v_alignbyte_b32 v211, v211, v211, v235\n"
      "v_mfma_f32_16x16x32_bf16 v[180:183], v[248:251], v[224:227], v[106:109]\n"
      "v_lshlrev_b32 v211, 16, v211\n"
      "v_alignbyte_b32 v150, v151, v150, v235\n"
      "v_mfma_f32_16x16x32_bf16 v[180:183], v[40:43], v[44:47], v[180:183]\n"
      "v_alignbyte_b32 v151, v216, v151, v235\n"
      "v_max3_f32 v233, v187, v168, v169\n"
      "v_mfma_f32_16x16x32_bf16 v[248:251], v[136:139], v[224:227], v[74:77]\n"
      "v_alignbyte_b32 v212, v213, v212, v235\n"
      "v_alignbyte_b32 v213, v217, v213, v235\n"
      "v_mfma_f32_16x16x32_bf16 v[248:251], v[228:231], v[176:179], v[248:251]\n"
      "v_alignbyte_b32 v214, v215, v214, v235\n"
      "v_max3_f32 v234, v234, v170, v171\n"
      "v_mfma_f32_16x16x32_bf16 v[248:251], v[144:147], v[44:47], v[248:251]\n"
      "v_alignbyte_b32 v215, v218, v215, v235\n"
      "v_alignbyte_b32 v219, v219, v219, v235\n"
      "v_mfma_f32_16x16x32_bf16 v[188:191], v[176:179], v[224:227], v[110:113]\n"
      "v_lshlrev_b32_e64 v219, 16, v219\n"
      "v_max3_f32 v233, v233, v156, v157\n"
      "v_mfma_f32_16x16x32_bf16 v[188:191], v[46:49], v[44:47], v[188:191]\n"
      "v_max3_f32 v234, v234, v158, v159\n"
      "v_max3_f32 v234, v234, v233, v152\n"
      "v_mfma_f32_16x16x32_bf16 v[176:179], v[228:231], v[136:139], v[76:79]\n"
      "v_mov_b32 v233, v234\n"
      "s_nop 1\n"
      "v_permlane16_swap_b32 v234, v233\n"
      "v_max_f32 v234, v234, v233\n"
      "v_mfma_f32_16x16x16_bf16 v[164:167], v[154:155], v[198:199], v[164:167]\n"
      "v_mov_b32_e64 v233, v234\n"
      "v_mfma_f32_16x16x16_bf16 v[176:179], v[146:147], v[144:145], v[176:179]\n"
      "s_nop 0\n"
      "v_permlane32_swap_b32 v234, v233\n"
      "v_mfma_f32_16x16x16_bf16 v[192:195], v[154:155], v[204:205], v[192:195]\n"
      "v_max_f32 v234, v234, v233\n"
      "v_sub_f32 v184, v184, v234\n"
      "v_mfma_f32_16x16x16_bf16 v[220:223], v[154:155], v[206:207], v[220:223]\n"
      "v_sub_f32 v185, v185, v234\n"
      "v_sub_f32 v186, v186, v234\n"
      "v_sub_f32 v187, v187, v234\n"
      "v_sub_f32 v168, v168, v234\n"
      "v_sub_f32 v169, v169, v234\n"
      "v_sub_f32 v170, v170, v234\n"
      "v_sub_f32 v171, v171, v234\n"
      "v_sub_f32 v156, v156, v234\n"
      "v_sub_f32 v157, v157, v234\n"
      "v_sub_f32 v158, v158, v234\n"
      "v_sub_f32 v159, v159, v234\n"
      "v_sub_f32 v152, v152, v234\n"
      "v_exp_f32 v184, v184\n"
      "v_exp_f32 v185, v185\n"
      "v_exp_f32 v186, v186\n"
      "v_cvt_pk_bf16_f32 v184, v184, v185\n"
      "v_exp_f32 v187, v187\n"
      "v_exp_f32 v168, v168\n"
      "v_cvt_pk_bf16_f32 v185, v186, v187\n"
      "v_exp_f32 v169, v169\n"
      "v_exp_f32 v170, v170\n"
      "v_cvt_pk_bf16_f32 v186, v168, v169\n"
      "v_exp_f32 v171, v171\n"
      "v_exp_f32_e64 v156, v156\n"
      "v_cvt_pk_bf16_f32 v187, v170, v171\n"
      "v_mfma_f32_16x16x16_bf16 v[160:163], v[154:155], v[150:151], v[160:163]\n"
      "s_waitcnt lgkmcnt(0)\n"
      "v_exp_f32 v157, v157\n"
      "v_mfma_f32_16x16x16_bf16 v[180:183], v[154:155], v[212:213], v[180:183]\n"
      "v_exp_f32_e64 v158, v158\n"
      "v_mfma_f32_16x16x16_bf16 v[188:191], v[154:155], v[214:215], v[188:191]\n"
      "v_exp_f32_e64 v159, v159\n"
      "v_exp_f32_e64 v152, v152\n"
      "v_fmac_f32_e64 v148, s37, v211\n"
      "v_cvt_pk_bf16_f32 v62, v156, v157\n"
      "v_cvt_pk_bf16_f32 v63, v158, v159\n"
      "v_cvt_pk_bf16_f32 v64, v152, 0\n"
      "v_bitop3_b32 v172, v172, v173, s4 bitop3:0x7e\n"
      "v_bitop3_b32 v174, v174, v175, s4 bitop3:0x7e\n"
      "v_mfma_f32_16x16x32_bf16 v[136:139], v[66:69], v[184:187], 0\n"
      "v_or_b32 v172, v172, v174\n"
      "v_cmp_ne_u32 vcc, 0, v172\n"
      "v_mfma_f32_16x16x32_bf16 v[136:139], v[66:69], v[62:65], v[136:139]\n"
      "s_mov_b64 s[48:49], vcc\n"
      "v_max3_f32 v234, v164, v165, v166\n"
      "v_max3_f32 v233, v167, v192, v193\n"
      "v_mfma_f32_16x16x32_bf16 v[156:159], v[240:243], v[184:187], 0\n"
      "s_cbranch_vccnz L_flg_65_%=\n"
      "L_fb_66_%=:\n"
      "v_mfma_f32_16x16x32_bf16 v[156:159], v[50:53], v[62:65], v[156:159]\n"
      "v_max3_f32 v234, v234, v194, v195\n"
      "v_max3_f32 v233, v233, v220, v221\n"
      "v_mfma_f32_16x16x32_bf16 v[172:175], v[140:143], v[184:187], 0\n"
      "v_max3_f32 v234, v234, v222, v223\n"
      "v_max3_f32 v234, v234, v233, v148\n"
      "v_mfma_f32_16x16x32_bf16 v[172:175], v[54:57], v[62:65], v[172:175]\n"
      "v_mov_b32 v233, v234\n"
      "v_fmac_f32 v248, s37, v219\n"
      "v_mfma_f32_16x16x32_bf16 v[168:171], v[200:203], v[184:187], 0\n"
      "v_permlane16_swap_b32 v234, v233\n"
      "v_mfma_f32_16x16x32_bf16 v[168:171], v[58:61], v[62:65], v[168:171]\n"
      "v_max_f32 v234, v234, v233\n"
      "v_mov_b32 v233, v234\n"
      "s_nop 1\n"
      "v_permlane32_swap_b32 v234, v233\n"
      "v_max_f32 v234, v234, v233\n"
      "v_sub_f32 v164, v164, v234\n"
      "v_sub_f32 v165, v165, v234\n"
      "v_sub_f32 v166, v166, v234\n"
      "v_sub_f32 v167, v167, v234\n"
      "v_sub_f32 v192, v192, v234\n"
      "v_sub_f32 v193, v193, v234\n"
      "v_sub_f32 v194, v194, v234\n"
      "v_sub_f32 v195, v195, v234\n"
      "v_sub_f32 v220, v220, v234\n"
      "v_sub_f32 v221, v221, v234\n"
      "v_sub_f32 v222, v222, v234\n"
      "v_sub_f32 v223, v223, v234\n"
      "v_sub_f32 v148, v148, v234\n"
      "v_exp_f32 v164, v164\n"
      "v_exp_f32 v165, v165\n"
      "v_exp_f32 v166, v166\n"
      "v_cvt_pk_bf16_f32 v164, v164, v165\n"
      "v_exp_f32 v167, v167\n"
      "v_exp_f32 v192, v192\n"
      "v_cvt_pk_bf16_f32 v165, v166, v167\n"
      "v_exp_f32 v193, v193\n"
      "v_exp_f32 v194, v194\n"
      "v_cvt_pk_bf16_f32 v166, v192, v193\n"
      "v_exp_f32 v195, v195\n"
      "v_exp_f32 v220, v220\n"
      "v_cvt_pk_bf16_f32 v167, v194, v195\n"
      "v_exp_f32 v221, v221\n"
      "v_exp_f32 v222, v222\n"
      "v_exp_f32 v223, v223\n"
      "v_exp_f32 v148, v148\n"
      "v_cvt_pk_bf16_f32 v62, v220, v221\n"
      "v_cvt_pk_bf16_f32 v63, v222, v223\n"
      "v_cvt_pk_bf16_f32 v64, v148, 0\n"
      "v_rcp_f32 v136, v136\n"
      "v_max3_f32 v137, v160, v161, v162\n"
      "v_mfma_f32_16x16x32_bf16 v[184:187], v[66:69], v[164:167], 0\n"
      "v_mul_f32 v156, v136, v156\n"
      "v_mul_f32 v157, v136, v157\n"
      "v_mfma_f32_16x16x32_bf16 v[184:187], v[66:69], v[62:65], v[184:187]\n"
      "s_add_u32 s28, s27, 2\n"
      "v_cvt_pk_bf16_f32 v156, v156, v157\n"
      "v_mul_f32_e64 v158, v136, v158\n"
      "v_mfma_f32_16x16x32_bf16 v[192:195], v[240:243], v[164:167], 0\n"
      "s_min_u32 m0, s28, s3\n"
      "v_mul_f32 v159, v136, v159\n"
      "v_cvt_pk_bf16_f32 v157, v158, v159\n"
      "v_mfma_f32_16x16x32_bf16 v[192:195], v[50:53], v[62:65], v[192:195]\n"
      "s_movrels_b32 s9, s80\n"
      "v_mul_f32 v172, v136, v172\n"
      "v_mul_f32 v173, v136, v173\n"
      "v_mfma_f32_16x16x32_bf16 v[234:237], v[140:143], v[164:167], 0\n"
      "s_lshr_b32 s8, s9, 8\n"
      "v_cvt_pk_bf16_f32 v158, v172, v173\n"
      "v_mul_f32 v174, v136, v174\n"
      "v_mfma_f32_16x16x32_bf16 v[234:237], v[54:57], v[62:65], v[234:237]\n"
      "s_and_b32 s9, s9, 0xff\n"
      "v_mul_f32 v175, v136, v175\n"
      "v_cvt_pk_bf16_f32 v159, v174, v175\n"
      "v_mfma_f32_16x16x32_bf16 v[220:223], v[200:203], v[164:167], 0\n"
      "s_mul_i32 s10, s8, 73\n"
      "v_mul_f32 v168, v136, v168\n"
      "v_mul_f32 v169, v136, v169\n"
      "v_mfma_f32_16x16x32_bf16 v[220:223], v[58:61], v[62:65], v[220:223]\n"
      "ds_write_b128 v124, v[156:159]\n"
      "v_cvt_pk_bf16_f32 v168, v168, v169\n"
      "v_mul_f32 v170, v136, v170\n"
      "v_mul_f32 v171, v136, v171\n"
      "v_cvt_pk_bf16_f32 v169, v170, v171\n"
      "v_max3_f32 v233, v163, v180, v181\n"
      "v_max3_f32 v137, v137, v182, v183\n"
      "v_max3_f32 v233, v233, v188, v189\n"
      "v_max3_f32 v137, v137, v190, v191\n"
      "v_max3_f32 v137, v137, v233, v248\n"
      "v_mov_b32 v233, v137\n"
      "s_add_u32 s10, s10, s9\n"
      "s_nop 0\n"
      "v_permlane16_swap_b32 v137, v233\n"
      "v_max_f32 v137, v137, v233\n"
      "v_mov_b32 v233, v137\n"
      "s_mul_i32 s28, s10, 4802\n"
      "s_nop 0\n"
      "v_permlane32_swap_b32 v137, v233\n"
      "v_max_f32 v137, v137, v233\n"
      "v_sub_f32 v160, v160, v137\n"
      "v_sub_f32 v161, v161, v137\n"
      "v_sub_f32 v162, v162, v137\n"
      "v_sub_f32 v163, v163, v137\n"
      "v_sub_f32 v180, v180, v137\n"
      "v_sub_f32 v181, v181, v137\n"
      "v_sub_f32 v182, v182, v137\n"
      "v_sub_f32 v183, v183, v137\n"
      "v_sub_f32 v188, v188, v137\n"
      "v_sub_f32 v189, v189, v137\n"
      "v_sub_f32 v190, v190, v137\n"
      "v_sub_f32 v191, v191, v137\n"
      "v_sub_f32 v248, v248, v137\n"
      "v_exp_f32 v160, v160\n"
      "v_exp_f32 v161, v161\n"
      "v_exp_f32 v162, v162\n"
      "v_cvt_pk_bf16_f32 v160, v160, v161\n"
      "v_exp_f32 v163, v163\n"
      "v_exp_f32 v180, v180\n"
      "v_cvt_pk_bf16_f32 v161, v162, v163\n"
      "s_add_u32 s56, s18, s28\n"
      "ds_write_b64 v125, v[168:169]\n"
      "v_exp_f32 v181, v181\n"
      "v_exp_f32 v182, v182\n"
      "v_cvt_pk_bf16_f32 v162, v180, v181\n"
      "v_exp_f32 v183, v183\n"
      "v_exp_f32 v188, v188\n"
      "v_cvt_pk_bf16_f32 v163, v182, v183\n"
      "v_exp_f32 v189, v189\n"
      "v_exp_f32 v190, v190\n"
      "v_exp_f32_e64 v191, v191\n"
      "v_exp_f32_e64 v248, v248\n"
      "v_cvt_pk_bf16_f32 v62, v188, v189\n"
      "v_cvt_pk_bf16_f32 v63, v190, v191\n"
      "v_cvt_pk_bf16_f32 v64, v248, 0\n"
      "v_cndmask_b32 v153, v153, v149, s[42:43]\n"
      "v_cndmask_b32 v153, v153, v249, s[44:45]\n"
      "v_mfma_f32_16x16x32_bf16 v[136:139], v[66:69], v[160:163], 0\n"
      "s_addc_u32 s57, s19, 0\n"
      "v_cndmask_b32 v153, v153, v177, s[46:47]\n"
      "v_mbcnt_lo_u32_b32 v232, -1, 0\n"
      "v_mfma_f32_16x16x32_bf16 v[136:139], v[66:69], v[62:65], v[136:139]\n"
      "s_and_b32 s56, s56, -16\n"
      "v_mbcnt_hi_u32_b32 v232, -1, v232\n"
      "v_min_u32 v232, 48, v232\n"
      "v_mfma_f32_16x16x32_bf16 v[172:175], v[240:243], v[160:163], 0\n"
      "s_sub_u32 s58, s56, 110592\n"
      "v_lshlrev_b32 v232, 1, v232\n"
      "v_add_u32 v232, s30, v232\n"
      "v_mfma_f32_16x16x32_bf16 v[172:175], v[50:53], v[62:65], v[172:175]\n"
      "ds_read_u16 v233, v232 offset:4704\n"
      "v_rcp_f32 v184, v184\n"
      "v_mfma_f32_16x16x32_bf16 v[156:159], v[140:143], v[160:163], 0\n"
      "s_subb_u32 s59, s57, 0\n"
      "v_mul_f32 v192, v184, v192\n"
      "v_mul_f32_e64 v193, v184, v193\n"
      "v_mfma_f32_16x16x32_bf16 v[156:159], v[54:57], v[62:65], v[156:159]\n"
      "s_sub_u32 s56, s56, 73728\n"
      "v_cvt_pk_bf16_f32 v192, v192, v193\n"
      "v_mul_f32 v194, v184, v194\n"
      "v_mfma_f32_16x16x32_bf16 v[168:171], v[200:203], v[160:163], 0\n"
      "s_subb_u32 s57, s57, 0\n"
      "v_mul_f32_e64 v195, v184, v195\n"
      "v_cvt_pk_bf16_f32 v193, v194, v195\n"
      "v_mfma_f32_16x16x32_bf16 v[168:171], v[58:61], v[62:65], v[168:171]\n"
      "s_mul_i32 s28, s8, 1373568\n"
      "v_mul_f32 v234, v184, v234\n"
      "v_mul_f32 v235, v184, v235\n"
      "v_cvt_pk_bf16_f32 v194, v234, v235\n"
      "v_mul_f32 v236, v184, v236\n"
      "v_mul_f32 v237, v184, v237\n"
      "v_cvt_pk_bf16_f32 v195, v236, v237\n"
      "v_mul_f32 v220, v184, v220\n"
      "v_mul_f32 v221, v184, v221\n"
      "v_cvt_pk_bf16_f32 v220, v220, v221\n"
      "v_mul_f32 v222, v184, v222\n"
      "s_mul_i32 s29, s9, 2688\n"
      "ds_write_b128 v124, v[192:195] offset:256\n"
      "s_add_u32 s28, s28, s29\n"
      "s_cmp_eq_u32 s8, 72\n"
      "s_cselect_b32 s8, 0, 100270464\n"
      "s_waitcnt lgkmcnt(1)\n"
      "v_lshlrev_b32 v233, 16, v233\n"
      "v_fmac_f32_e64 v153, s37, v233\n"
      "v_mul_f32 v223, v184, v223\n"
      "v_cvt_pk_bf16_f32 v221, v222, v223\n"
      "v_max_f32_dpp v184, v153, v153 row_ror:8 row_mask:0xf bank_mask:0xf\n"
      "s_cmp_eq_u32 s9, 72\n"
      "s_nop 0\n"
      "v_max_f32_dpp v184, v184, v184 row_ror:4 row_mask:0xf bank_mask:0xf\n"
      "s_cselect_b32 s9, 0, 196224\n"
      "s_nop 0\n"
      "v_max_f32_dpp v184, v184, v184 row_ror:2 row_mask:0xf bank_mask:0xf\n"
      "s_add_u32 s29, s8, s9\n"
      "s_nop 0\n"
      "v_max_f32_dpp v184, v184, v184 row_ror:1 row_mask:0xf bank_mask:0xf\n"
      "s_add_u32 s28, s28, s29\n"
      "s_nop 0\n"
      "v_max_f32_dpp v184, v184, v184 row_bcast:15 row_mask:0xa bank_mask:0xf\n"
      "s_add_u32 s52, s16, s28\n"
      "s_nop 0\n"
      "v_max_f32_dpp v184, v184, v184 row_bcast:31 row_mask:0xc bank_mask:0xf\n"
      "s_nop 0\n"
      "v_readlane_b32 s31, v184, 63\n"
      "s_addc_u32 s53, s17, 0\n"
      "s_nop 0\n"
      "v_subrev_f32 v153, s31, v153\n"
      "v_exp_f32 v153, v153\n"
      "s_nop 0\n"
      "v_cvt_pk_bf16_f32 v153, v153, v153\n"
      "s_cmp_lg_u32 s29, s11\n"
      "ds_write_b16 v130, v153\n"
      "v_mov_b32_e64 v184, 0\n"
      "ds_read_b128 v[232:235], v131\n"
      "ds_read_b128 v[192:195], v131 offset:64\n"
      "ds_write_b64 v125, v[220:221] offset:256\n"
      "ds_write_b32 v129, v184\n"
      "ds_read_b128 v[164:167], v114 offset:18432\n"
      "ds_read_b128 v[184:187], v115 offset:18432\n"
      "s_cbranch_scc1 L_edge_68_%=\n"
      "L_eb_69_%=:\n"
      "s_waitcnt lgkmcnt(3)\n"
      "v_mfma_f32_16x16x32_bf16 v[220:223], v[232:235], v[66:69], 0\n"
      "v_mfma_f32_16x16x32_bf16 v[220:223], v[192:195], v[66:69], v[220:223]\n"
      "v_mfma_f32_16x16x32_bf16 v[160:163], v[232:235], v[240:243], 0\n"
      "s_waitcnt lgkmcnt(0)\n"
      "v_mfma_f32_16x16x32_bf16 v[160:163], v[192:195], v[50:53], v[160:163]\n"
      "v_mfma_f32_16x16x32_bf16 v[240:243], v[232:235], v[140:143], 0\n"
      "v_mfma_f32_16x16x32_bf16 v[240:243], v[192:195], v[54:57], v[240:243]\n"
      "v_mfma_f32_16x16x32_bf16 v[140:143], v[232:235], v[200:203], 0\n"
      "s_nop 1\n"
      "v_mov_b32_dpp v220, v220 quad_perm:[0,0,0,0] row_mask:0xf bank_mask:0xf\n"
      "v_mfma_f32_16x16x32_bf16 v[140:143], v[192:195], v[58:61], v[140:143]\n"
      "v_rcp_f32_e64 v220, v220\n"
      "v_mfma_f32_16x16x32_bf16 v[232:235], a[0:3], v[164:167], 0\n"
      "s_nop 0\n"
      "v_cndmask_b32 v160, v160, v240, s[42:43]\n"
      "v_mfma_f32_16x16x32_bf16 v[232:235], a[36:39], v[184:187], v[232:235]\n"
      "v_mfma_f32_16x16x32_bf16 v[192:195], a[4:7], v[164:167], 0\n"
      "s_nop 1\n"
      "v_cndmask_b32 v160, v160, v140, s[44:45]\n"
      "v_mfma_f32_16x16x32_bf16 v[192:195], a[40:43], v[184:187], v[192:195]\n"
      "v_mul_f32_e64 v160, v220, v160\n"
      "v_cvt_pk_bf16_f32 v160, v160, v160\n"
      "ds_write_b16 v135, v160\n"
      "s_branch L_join_10_%=\n"
      "L_flg_65_%=:\n"
      "v_cmp_gt_i32 vcc, 0, v172\n"
      "s_cbranch_vccz L_fb_66_%=\n"
      "s_sleep 1\n"
      "ds_read_b128 v[172:175], v128 offset:3072\n"
      "s_waitcnt lgkmcnt(0)\n"
      "v_bitop3_b32 v172, v172, v173, s4 bitop3:0x7e\n"
      "v_bitop3_b32 v174, v174, v175, s4 bitop3:0x7e\n"
      "v_or_b32 v172, v172, v174\n"
      "v_cmp_ne_u32 s[48:49], 0, v172\n"
      "s_branch L_flg_65_%=\n"
      "L_edge_68_%=:\n"
      "s_mov_b32 s11, s29\n"
      "s_sub_u32 s28, s60, s8\n"
      "s_sub_u32 s29, s61, s9\n"
      "s_mov_b32 s60, s8\n"
      "s_mov_b32 s61, s9\n"
      "s_mov_b64 exec, s[64:65]\n"
      "v_add_u32 v116, s28, v116\n"
      "s_mov_b64 exec, s[70:71]\n"
      "v_add_u32 v116, s29, v116\n"
      "s_mov_b64 exec, s[66:67]\n"
      "v_add_u32 v117, s28, v117\n"
      "s_mov_b64 exec, s[72:73]\n"
      "v_add_u32_e64 v117, s29, v117\n"
      "s_mov_b64 exec, s[68:69]\n"
      "v_add_u32 v118, s28, v118\n"
      "s_mov_b64 exec, s[74:75]\n"
      "v_add_u32_e64 v118, s29, v118\n"
      "s_mov_b64 exec, -1\n"
      "s_nop 4\n"
      "s_branch L_eb_69_%=\n"
      "L_gen_19_%=:\n"
      "s_waitcnt lgkmcnt(0)\n"
      "s_nop 7\n"
      "s_nop 7\n"
      "s_cmp_eq_u64 s[48:49], 0\n"
      "s_cbranch_scc1 L_go_81_%=\n"
      "s_mov_b32 m0, s27\n"
      "s_nop 0\n"
      "s_movrels_b32 s9, s80\n"
      "s_lshr_b32 s8, s9, 8\n"
      "s_and_b32 s9, s9, 0xff\n"
      "s_mul_i32 s10, s8, 73\n"
      "s_add_u32 s10, s10, s9\n"
      "s_mul_i32 s30, s10, 4802\n"
      "s_add_u32 s30, s30, s18\n"
      "s_and_b32 s29, s30, 15\n"
      "s_add_u32 s30, s29, 115456\n"
      "s_andn2_b64 s[34:35], s[48:49], 0x801\n"
      "s_cbranch_scc1 L_gen_20_%=\n"
      "v_mbcnt_lo_u32_b32 v174, -1, 0\n"
      "v_mbcnt_hi_u32_b32 v174, -1, v174\n"
      "v_lshlrev_b32 v175, 1, v174\n"
      "v_add_u32 v250, 4802, v175\n"
      "v_cmp_gt_u32 vcc, s29, v175\n"
      "s_nop 1\n"
      "v_cndmask_b32 v175, v250, v175, vcc\n"
      "v_add_u32 v175, 115456, v175\n"
      "v_mov_b32 v250, 0\n"
      "s_mov_b64 exec, 0x7f\n"
      "ds_write_b16 v175, v250\n"
      "s_mov_b64 exec, -1\n"
      "v_lshlrev_b32_e64 v175, 4, v174\n"
      "v_add_u32 v175, 115456, v175\n"
      "v_min_u32 v174, 116160, v175\n"
      "ds_read_b128 v[208:211], v175\n"
      "ds_read_b128 v[148:151], v175 offset:1024\n"
      "ds_read_b128 v[152:155], v175 offset:2048\n"
      "ds_read_b128 v[204:207], v175 offset:3072\n"
      "ds_read_b128 v[250:253], v174 offset:4096\n"
      "s_waitcnt lgkmcnt(2)\n"
      "v_or3_b32 v208, v208, v209, v210\n"
      "v_or3_b32 v208, v208, v211, v148\n"
      "v_or3_b32 v208, v208, v149, v150\n"
      "v_or3_b32 v208, v208, v151, v152\n"
      "v_or3_b32 v208, v208, v153, v154\n"
      "s_waitcnt lgkmcnt(0)\n"
      "v_or3_b32 v208, v208, v155, v204\n"
      "v_or3_b32 v208, v208, v205, v206\n"
      "v_or3_b32 v208, v208, v207, v250\n"
      "v_or3_b32 v208, v208, v251, v252\n"
      "v_bitop3_b32 v208, v208, v253, s40 bitop3:0xa8\n"
      "v_cmp_ne_u32 s[48:49], 0, v208\n"
      "s_cmp_lg_u64 s[48:49], 0\n"
      "s_cbranch_scc1 L_gen_20_%=\n"
      "s_cmp_eq_u64 s[50:51], 0\n"
      "s_cbranch_scc1 L_fast_22_%=\n"
      "L_go_81_%=:\n"
      "v_mfma_f32_16x16x32_bf16 v[208:211], v[228:231], v[244:247], v[90:93]\n"
      "ds_read_b128 v[148:151], v128 offset:2048\n"
      "v_max3_f32 v174, v176, v177, v178\n"
      "v_mfma_f32_16x16x32_bf16 v[208:211], v[34:37], v[38:41], v[208:211]\n"
      "v_max3_f32 v175, v179, v184, v185\n"
      "v_max3_f32 v174, v174, v186, v187\n"
      "v_mfma_f32_16x16x32_bf16 v[152:155], v[164:167], v[244:247], v[94:97]\n"
      "v_max3_f32 v175, v175, v236, v237\n"
      "v_max3_f32 v174, v174, v238, v239\n"
      "v_mfma_f32_16x16x32_bf16 v[152:155], v[40:43], v[38:41], v[152:155]\n"
      "v_max3_f32 v174, v174, v175, v172\n"
      "v_mov_b32 v175, v174\n"
      "v_mfma_f32_16x16x32_bf16 v[204:207], v[192:195], v[244:247], v[72:75]\n"
      "s_nop 0\n"
      "v_permlane16_swap_b32_e64 v174, v175\n"
      "v_mfma_f32_16x16x32_bf16 v[204:207], v[180:183], v[164:167], v[204:207]\n"
      "v_max_f32 v174, v174, v175\n"
      "v_mov_b32 v175, v174\n"
      "v_mfma_f32_16x16x32_bf16 v[204:207], v[188:191], v[38:41], v[204:207]\n"
      "s_nop 0\n"
      "v_permlane32_swap_b32 v174, v175\n"
      "v_mfma_f32_16x16x32_bf16 v[250:253], v[136:139], v[244:247], v[98:101]\n"
      "v_max_f32 v174, v174, v175\n"
      "v_sub_f32 v176, v176, v174\n"
      "v_mfma_f32_16x16x32_bf16 v[250:253], v[46:49], v[38:41], v[250:253]\n"
      "v_sub_f32 v177, v177, v174\n"
      "v_sub_f32 v178, v178, v174\n"
      "v_sub_f32 v179, v179, v174\n"
      "v_sub_f32 v184, v184, v174\n"
      "v_sub_f32 v185, v185, v174\n"
      "v_sub_f32 v186, v186, v174\n"
      "v_sub_f32 v187, v187, v174\n"
      "v_sub_f32 v236, v236, v174\n"
      "v_sub_f32 v237, v237, v174\n"
      "v_sub_f32 v238, v238, v174\n"
      "v_sub_f32 v239, v239, v174\n"
      "v_sub_f32 v172, v172, v174\n"
      "v_exp_f32 v176, v176\n"
      "v_exp_f32 v177, v177\n"
      "v_exp_f32 v178, v178\n"
      "v_cvt_pk_bf16_f32 v176, v176, v177\n"
      "v_exp_f32 v179, v179\n"
      "v_exp_f32 v184, v184\n"
      "v_cvt_pk_bf16_f32 v177, v178, v179\n"
      "v_exp_f32 v185, v185\n"
      "v_exp_f32 v186, v186\n"
      "v_cvt_pk_bf16_f32 v178, v184, v185\n"
      "v_exp_f32 v187, v187\n"
      "v_exp_f32 v236, v236\n"
      "v_cvt_pk_bf16_f32 v179, v186, v187\n"
      "v_exp_f32 v237, v237\n"
      "v_exp_f32 v238, v238\n"
      "v_exp_f32 v239, v239\n"
      "v_mfma_f32_16x16x32_bf16 v[244:247], v[228:231], v[212:215], v[102:105]\n"
      "s_waitcnt lgkmcnt(0)\n"
      "v_exp_f32 v172, v172\n"
      "v_mfma_f32_16x16x32_bf16 v[244:247], v[34:37], v[44:47], v[244:247]\n"
      "v_cvt_pk_bf16_f32 v62, v236, v237\n"
      "v_cvt_pk_bf16_f32 v63, v238, v239\n"
      "v_cvt_pk_bf16_f32 v64, v172, 0\n"
      "v_bitop3_b32 v148, v148, v149, s4 bitop3:0x7e\n"
      "v_bitop3_b32 v150, v150, v151, s4 bitop3:0x7e\n"
      "v_mfma_f32_16x16x32_bf16 v[228:231], v[66:69], v[176:179], 0\n"
      "ds_read_b128 v[236:239], v119 offset:12288\n"
      "v_or_b32 v148, v148, v150\n"
      "v_mfma_f32_16x16x32_bf16 v[228:231], v[66:69], v[62:65], v[228:231]\n"
      "v_cmp_ne_u32 vcc, 0, v148\n"
      "v_max3_f32 v206, v208, v209, v210\n"
      "v_mfma_f32_16x16x32_bf16 v[184:187], v[160:163], v[176:179], 0\n"
      "s_mov_b64 s[48:49], vcc\n"
      "v_max3_f32 v207, v211, v152, v153\n"
      "v_max3_f32 v206, v206, v154, v155\n"
      "v_mfma_f32_16x16x32_bf16 v[184:187], v[50:53], v[62:65], v[184:187]\n"
      "s_cbranch_vccnz L_flg_82_%=\n"
      "L_fb_83_%=:\n"
      "v_mfma_f32_16x16x32_bf16 v[148:151], v[220:223], v[176:179], 0\n"
      "v_max3_f32 v207, v207, v250, v251\n"
      "v_max3_f32 v206, v206, v252, v253\n"
      "v_mfma_f32_16x16x32_bf16 v[148:151], v[54:57], v[62:65], v[148:151]\n"
      "v_max3_f32 v206, v206, v207, v204\n"
      "v_mov_b32 v207, v206\n"
      "v_mfma_f32_16x16x32_bf16 v[230:233], v[156:159], v[176:179], 0\n"
      "s_nop 0\n"
      "v_permlane16_swap_b32 v206, v207\n"
      "v_mfma_f32_16x16x32_bf16 v[230:233], v[58:61], v[62:65], v[230:233]\n"
      "v_max_f32 v206, v206, v207\n"
      "v_mov_b32 v207, v206\n"
      "v_mfma_f32_16x16x32_bf16 v[174:177], v[164:167], v[212:215], v[106:109]\n"
      "s_nop 0\n"
      "v_permlane32_swap_b32 v206, v207\n"
      "v_mfma_f32_16x16x32_bf16 v[174:177], v[40:43], v[44:47], v[174:177]\n"
      "v_max_f32 v206, v206, v207\n"
      "v_sub_f32 v208, v208, v206\n"
      "v_mfma_f32_16x16x32_bf16 v[144:147], v[192:195], v[212:215], v[74:77]\n"
      "s_waitcnt lgkmcnt(0)\n"
      "v_sub_f32 v209, v209, v206\n"
      "v_sub_f32 v210, v210, v206\n"
      "v_mfma_f32_16x16x32_bf16 v[144:147], v[180:183], v[136:139], v[144:147]\n"
      "global_store_dwordx4 v116, v[236:239], s[54:55] offset:256 sc1\n"
      "v_mfma_f32_16x16x32_bf16 v[144:147], v[188:191], v[44:47], v[144:147]\n"
      "v_sub_f32 v211, v211, v206\n"
      "v_sub_f32 v152, v152, v206\n"
      "v_mfma_f32_16x16x32_bf16 v[164:167], v[136:139], v[212:215], v[110:113]\n"
      "v_sub_f32 v153, v153, v206\n"
      "v_sub_f32 v154, v154, v206\n"
      "v_mfma_f32_16x16x32_bf16 v[164:167], v[46:49], v[44:47], v[164:167]\n"
      "v_sub_f32 v155, v155, v206\n"
      "v_sub_f32 v250, v250, v206\n"
      "v_sub_f32 v251, v251, v206\n"
      "v_sub_f32 v252, v252, v206\n"
      "v_sub_f32 v253, v253, v206\n"
      "v_sub_f32 v204, v204, v206\n"
      "v_mfma_f32_16x16x32_bf16 v[212:215], v[180:183], v[192:195], v[76:79]\n"
      "v_exp_f32 v208, v208\n"
      "v_exp_f32 v209, v209\n"
      "v_exp_f32 v210, v210\n"
      "v_cvt_pk_bf16_f32 v208, v208, v209\n"
      "v_exp_f32 v211, v211\n"
      "v_exp_f32_e64 v152, v152\n"
      "v_mfma_f32_16x16x16_bf16 v[212:215], v[190:191], v[188:189], v[212:215]\n"
      "v_cvt_pk_bf16_f32 v209, v210, v211\n"
      "v_exp_f32 v153, v153\n"
      "v_exp_f32 v154, v154\n"
      "v_cvt_pk_bf16_f32 v210, v152, v153\n"
      "v_exp_f32 v155, v155\n"
      "v_exp_f32 v250, v250\n"
      "v_cvt_pk_bf16_f32 v211, v154, v155\n"
      "v_exp_f32 v251, v251\n"
      "v_exp_f32 v252, v252\n"
      "v_exp_f32 v253, v253\n"
      "v_exp_f32 v204, v204\n"
      "v_cvt_pk_bf16_f32 v62, v250, v251\n"
      "v_cvt_pk_bf16_f32 v63, v252, v253\n"
      "v_cvt_pk_bf16_f32 v64, v204, 0\n"
      "v_rcp_f32_e64 v228, v228\n"
      "v_max3_f32 v229, v244, v245, v246\n"
      "v_mfma_f32_16x16x32_bf16 v[152:155], v[66:69], v[208:211], 0\n"
      "ds_read_b128 v[192:195], v120\n"
      "v_mul_f32_e64 v184, v228, v184\n"
      "v_mfma_f32_16x16x32_bf16 v[152:155], v[66:69], v[62:65], v[152:155]\n"
      "v_mul_f32 v185, v228, v185\n"
      "v_cvt_pk_bf16_f32 v184, v184, v185\n"
      "v_mfma_f32_16x16x32_bf16 v[188:191], v[160:163], v[208:211], 0\n"
      "v_mul_f32 v186, v228, v186\n"
      "v_mul_f32 v187, v228, v187\n"
      "v_mfma_f32_16x16x32_bf16 v[188:191], v[50:53], v[62:65], v[188:191]\n"
      "v_cvt_pk_bf16_f32 v185, v186, v187\n"
      "v_mul_f32 v148, v228, v148\n"
      "v_mfma_f32_16x16x32_bf16 v[250:253], v[220:223], v[208:211], 0\n"
      "v_mul_f32 v149, v228, v149\n"
      "v_cvt_pk_bf16_f32 v186, v148, v149\n"
      "v_mfma_f32_16x16x32_bf16 v[250:253], v[54:57], v[62:65], v[250:253]\n"
      "v_mul_f32 v150, v228, v150\n"
      "v_mul_f32_e64 v151, v228, v151\n"
      "v_mfma_f32_16x16x32_bf16 v[214:217], v[156:159], v[208:211], 0\n"
      "v_cvt_pk_bf16_f32 v187, v150, v151\n"
      "v_mul_f32_e64 v230, v228, v230\n"
      "v_mfma_f32_16x16x32_bf16 v[214:217], v[58:61], v[62:65], v[214:217]\n"
      "ds_write_b128 v124, v[184:187] offset:18432\n"
      "v_mul_f32 v231, v228, v231\n"
      "v_cvt_pk_bf16_f32 v230, v230, v231\n"
      "v_mul_f32 v232, v228, v232\n"
      "v_mul_f32_e64 v233, v228, v233\n"
      "v_cvt_pk_bf16_f32 v231, v232, v233\n"
      "v_max3_f32 v228, v247, v174, v175\n"
      "v_max3_f32 v229, v229, v176, v177\n"
      "v_max3_f32 v228, v228, v164, v165\n"
      "v_max3_f32 v229, v229, v166, v167\n"
      "v_max3_f32 v229, v229, v228, v144\n"
      "v_mov_b32 v228, v229\n"
      "s_waitcnt lgkmcnt(1)\n"
      "s_nop 0\n"
      "v_permlane16_swap_b32 v229, v228\n"
      "v_max_f32 v229, v229, v228\n"
      "v_mov_b32 v228, v229\n"
      "s_nop 1\n"
      "v_permlane32_swap_b32 v229, v228\n"
      "v_max_f32 v229, v229, v228\n"
      "v_sub_f32 v244, v244, v229\n"
      "v_sub_f32 v245, v245, v229\n"
      "v_sub_f32 v246, v246, v229\n"
      "v_sub_f32 v247, v247, v229\n"
      "v_sub_f32 v174, v174, v229\n"
      "v_sub_f32 v175, v175, v229\n"
      "v_sub_f32 v176, v176, v229\n"
      "v_sub_f32 v177, v177, v229\n"
      "v_sub_f32 v164, v164, v229\n"
      "v_sub_f32 v165, v165, v229\n"
      "v_sub_f32 v166, v166, v229\n"
      "v_sub_f32 v167, v167, v229\n"
      "v_sub_f32 v144, v144, v229\n"
      "v_exp_f32 v244, v244\n"
      "v_exp_f32 v245, v245\n"
      "v_exp_f32 v246, v246\n"
      "v_cvt_pk_bf16_f32 v244, v244, v245\n"
      "v_exp_f32 v247, v247\n"
      "v_exp_f32 v174, v174\n"
      "v_cvt_pk_bf16_f32 v245, v246, v247\n"
      "v_exp_f32 v175, v175\n"
      "v_exp_f32 v176, v176\n"
      "v_cvt_pk_bf16_f32 v246, v174, v175\n"
      "v_exp_f32 v177, v177\n"
      "v_exp_f32 v164, v164\n"
      "ds_write_b64 v125, v[230:231] offset:18432\n"
      "v_cvt_pk_bf16_f32 v247, v176, v177\n"
      "v_exp_f32 v165, v165\n"
      "v_exp_f32 v166, v166\n"
      "v_exp_f32 v167, v167\n"
      "v_exp_f32 v144, v144\n"
      "v_cvt_pk_bf16_f32 v62, v164, v165\n"
      "v_cvt_pk_bf16_f32 v63, v166, v167\n"
      "v_cvt_pk_bf16_f32 v64, v144, 0\n"
      "v_rcp_f32 v152, v152\n"
      "v_mfma_f32_16x16x32_bf16 v[136:139], v[66:69], v[244:247], 0\n"
      "global_store_dwordx4 v117, v[192:195], s[54:55] sc1\n"
      "v_mfma_f32_16x16x32_bf16 v[136:139], v[66:69], v[62:65], v[136:139]\n"
      "ds_read_b128 v[228:231], v123 offset:256\n"
      "v_mul_f32 v188, v152, v188\n"
      "v_mfma_f32_16x16x32_bf16 v[164:167], v[160:163], v[244:247], 0\n"
      "ds_read_b128 v[192:195], v123 offset:3328\n"
      "v_mul_f32 v189, v152, v189\n"
      "v_mfma_f32_16x16x32_bf16 v[164:167], v[50:53], v[62:65], v[164:167]\n"
      "ds_read_b128 v[174:177], v123 offset:6400\n"
      "v_cvt_pk_bf16_f32 v188, v188, v189\n"
      "v_mfma_f32_16x16x32_bf16 v[208:211], v[220:223], v[244:247], 0\n"
      "ds_read_b128 v[184:187], v121\n"
      "v_mul_f32 v190, v152, v190\n"
      "v_mfma_f32_16x16x32_bf16 v[208:211], v[54:57], v[62:65], v[208:211]\n"
      "ds_read_b128 v[180:183], v123 offset:9472\n"
      "v_mul_f32 v191, v152, v191\n"
      "v_mfma_f32_16x16x32_bf16 v[148:151], v[156:159], v[244:247], 0\n"
      "ds_read_b128 v[236:239], v123 offset:12544\n"
      "v_cvt_pk_bf16_f32 v189, v190, v191\n"
      "v_mfma_f32_16x16x32_bf16 v[148:151], v[58:61], v[62:65], v[148:151]\n"
      "v_mul_f32 v250, v152, v250\n"
      "v_mul_f32 v251, v152, v251\n"
      "v_cvt_pk_bf16_f32 v190, v250, v251\n"
      "v_mul_f32 v252, v152, v252\n"
      "v_mul_f32 v253, v152, v253\n"
      "v_cvt_pk_bf16_f32 v191, v252, v253\n"
      "v_mul_f32 v214, v152, v214\n"
      "v_mul_f32 v215, v152, v215\n"
      "v_cvt_pk_bf16_f32 v214, v214, v215\n"
      "v_mul_f32 v216, v152, v216\n"
      "v_mul_f32_e64 v217, v152, v217\n"
      "v_cvt_pk_bf16_f32 v215, v216, v217\n"
      "v_cndmask_b32 v173, v173, v205, s[42:43]\n"
      "v_cndmask_b32 v173, v173, v145, s[44:45]\n"
      "v_cndmask_b32 v173, v173, v213, s[46:47]\n"
      "s_nop 1\n"
      "v_max_f32_dpp v172, v173, v173 row_ror:8 row_mask:0xf bank_mask:0xf\n"
      "s_waitcnt lgkmcnt(3)\n"
      "v_mfma_f32_16x16x32_bf16 v[244:247], a[216:219], v[228:231], 0\n"
      "ds_read_b128 v[250:253], v123 offset:15616\n"
      "v_max_f32_dpp v172, v172, v172 row_ror:4 row_mask:0xf bank_mask:0xf\n"
      "v_mfma_f32_16x16x32_bf16 v[244:247], a[228:231], v[192:195], v[244:247]\n"
      "ds_write_b128 v124, v[188:191] offset:18688\n"
      "s_waitcnt lgkmcnt(2)\n"
      "v_mfma_f32_16x16x32_bf16 v[152:155], a[220:223], v[228:231], 0\n"
      "ds_read_b128 v[144:147], v123 offset:512\n"
      "v_max_f32_dpp v172, v172, v172 row_ror:2 row_mask:0xf bank_mask:0xf\n"
      "v_mfma_f32_16x16x32_bf16 v[152:155], a[232:235], v[192:195], v[152:155]\n"
      "ds_write_b64 v125, v[214:215] offset:18688\n"
      "v_mfma_f32_16x16x32_bf16 v[204:207], a[224:227], v[228:231], 0\n"
      "ds_read_b128 v[228:231], v123 offset:3584\n"
      "v_max_f32_dpp v172, v172, v172 row_ror:1 row_mask:0xf bank_mask:0xf\n"
      "v_mfma_f32_16x16x32_bf16 v[204:207], a[236:239], v[192:195], v[204:207]\n"
      "ds_read_b128 v[212:215], v123 offset:6656\n"
      "v_max_f32_dpp v172, v172, v172 row_bcast:15 row_mask:0xa bank_mask:0xf\n"
      "v_mfma_f32_16x16x32_bf16 v[244:247], a[240:243], v[174:177], v[244:247]\n"
      "s_nop 0\n"
      "v_max_f32_dpp v172, v172, v172 row_bcast:31 row_mask:0xc bank_mask:0xf\n"
      "v_mfma_f32_16x16x32_bf16 v[244:247], a[252:255], v[180:183], v[244:247]\n"
      "s_mov_b32 exec_hi, 0\n"
      "global_store_dwordx4 v118, v[184:187], s[54:55] sc1\n"
      "s_mov_b32 exec_hi, -1\n"
      "s_waitcnt lgkmcnt(4)\n"
      "v_mfma_f32_16x16x32_bf16 v[152:155], a[244:247], v[174:177], v[152:155]\n"
      "ds_read_b128 v[184:187], v123 offset:9728\n"
      "v_readlane_b32 s31, v172, 63\n"
      "v_mfma_f32_16x16x32_bf16 v[152:155], v[0:3], v[180:183], v[152:155]\n"
      "s_add_u32 s28, s27, 2\n"
      "v_subrev_f32 v173, s31, v173\n"
      "v_exp_f32_e64 v173, v173\n"
      "v_mfma_f32_16x16x32_bf16 v[204:207], a[248:251], v[174:177], v[204:207]\n"
      "ds_read_b128 v[174:177], v123 offset:12800\n"
      "v_cvt_pk_bf16_f32 v173, v173, v173\n"
      "v_mfma_f32_16x16x32_bf16 v[204:207], v[4:7], v[180:183], v[204:207]\n"
      "ds_write_b16 v130, v173\n"
      "v_mfma_f32_16x16x32_bf16 v[244:247], v[8:11], v[236:239], v[244:247]\n"
      "ds_read_b128 v[180:183], v131\n"
      "s_waitcnt lgkmcnt(4)\n"
      "v_mfma_f32_16x16x32_bf16 v[244:247], v[20:23], v[250:253], v[244:247]\n"
      "s_min_u32 m0, s28, s3\n"
      "v_mfma_f32_16x16x32_bf16 v[152:155], v[12:15], v[236:239], v[152:155]\n"
      "s_movrels_b32 s9, s80\n"
      "s_lshr_b32 s8, s9, 8\n"
      "s_and_b32 s9, s9, 0xff\n"
      "v_mfma_f32_16x16x32_bf16 v[152:155], v[24:27], v[250:253], v[152:155]\n"
      "s_mul_i32 s10, s8, 73\n"
      "s_nop 0\n"
      "v_fma_f32 v173, v244, v244, s39\n"
      "v_mfma_f32_16x16x32_bf16 v[204:207], v[16:19], v[236:239], v[204:207]\n"
      "ds_read_b128 v[236:239], v131 offset:64\n"
      "v_fmac_f32 v173, v245, v245\n"
      "v_mfma_f32_16x16x32_bf16 v[204:207], v[28:31], v[250:253], v[204:207]\n"
      "s_waitcnt lgkmcnt(4)\n"
      "v_fmac_f32 v173, v246, v246\n"
      "v_fmac_f32 v173, v247, v247\n"
      "v_mfma_f32_16x16x32_bf16 v[192:195], a[216:219], v[144:147], 0\n"
      "s_add_u32 s10, s10, s9\n"
      "v_fmac_f32 v173, v152, v152\n"
      "v_fmac_f32 v173, v153, v153\n"
      "v_mfma_f32_16x16x32_bf16 v[192:195], a[228:231], v[228:231], v[192:195]\n"
      "s_mul_i32 s28, s10, 4802\n"
      "v_fmac_f32 v173, v154, v154\n"
      "v_fmac_f32 v173, v155, v155\n"
      "v_mfma_f32_16x16x32_bf16 v[250:253], a[220:223], v[144:147], 0\n"
      "s_add_u32 s56, s18, s28\n"
      "v_fmac_f32 v173, v204, v204\n"
      "v_fmac_f32 v173, v205, v205\n"
      "v_mfma_f32_16x16x32_bf16 v[250:253], a[232:235], v[228:231], v[250:253]\n"
      "s_addc_u32 s57, s19, 0\n"
      "v_fmac_f32_e64 v173, v206, v206\n"
      "v_fmac_f32 v173, v207, v207\n"
      "v_mfma_f32_16x16x32_bf16 v[188:191], a[224:227], v[144:147], 0\n"
      "ds_read_b128 v[144:147], v123 offset:15872\n"
      "s_and_b32 s56, s56, -16\n"
      "v_mfma_f32_16x16x32_bf16 v[188:191], a[236:239], v[228:231], v[188:191]\n"
      "s_sub_u32 s58, s56, 110592\n"
      "s_subb_u32 s59, s57, 0\n"
      "v_mfma_f32_16x16x32_bf16 v[192:195], a[240:243], v[212:215], v[192:195]\n"
      "s_sub_u32 s56, s56, 73728\n"
      "s_subb_u32 s57, s57, 0\n"
      "s_mul_i32 s28, s8, 1373568\n"
      "v_mfma_f32_16x16x32_bf16 v[192:195], a[252:255], v[184:187], v[192:195]\n"
      "s_mul_i32 s29, s9, 2688\n"
      "s_add_u32 s28, s28, s29\n"
      "s_waitcnt lgkmcnt(1)\n"
      "v_mfma_f32_16x16x32_bf16 v[250:253], a[244:247], v[212:215], v[250:253]\n"
      "s_cmp_eq_u32 s8, 72\n"
      "v_permlane16_swap_b32_e64 v248, v173\n"
      "v_mfma_f32_16x16x32_bf16 v[250:253], v[0:3], v[184:187], v[250:253]\n"
      "s_cselect_b32 s8, 0, 100270464\n"
      "v_add_f32 v248, v248, v173\n"
      "v_mfma_f32_16x16x32_bf16 v[188:191], a[248:251], v[212:215], v[188:191]\n"
      "s_cmp_eq_u32 s9, 72\n"
      "s_cselect_b32 s9, 0, 196224\n"
      "s_add_u32 s29, s8, s9\n"
      "v_mfma_f32_16x16x32_bf16 v[188:191], v[4:7], v[184:187], v[188:191]\n"
      "s_add_u32 s28, s28, s29\n"
      "s_add_u32 s52, s16, s28\n"
      "v_mfma_f32_16x16x32_bf16 v[184:187], v[180:183], v[66:69], 0\n"
      "s_addc_u32 s53, s17, 0\n"
      "s_cmp_lg_u32 s29, s11\n"
      "v_mfma_f32_16x16x32_bf16 v[184:187], v[236:239], v[66:69], v[184:187]\n"
      "s_cbranch_scc1 L_edge_85_%=\n"
      "L_eb_86_%=:\n"
      "s_waitcnt lgkmcnt(0)\n"
      "v_mfma_f32_16x16x32_bf16 v[192:195], v[8:11], v[174:177], v[192:195]\n"
      "v_mfma_f32_16x16x32_bf16 v[192:195], v[20:23], v[144:147], v[192:195]\n"
      "v_mfma_f32_16x16x32_bf16 v[250:253], v[12:15], v[174:177], v[250:253]\n"
      "v_mfma_f32_16x16x32_bf16 v[250:253], v[24:27], v[144:147], v[250:253]\n"
      "ds_read_b128 v[212:215], v114\n"
      "v_mfma_f32_16x16x32_bf16 v[188:191], v[16:19], v[174:177], v[188:191]\n"
      "ds_read_b128 v[228:231], v115\n"
      "v_mov_b32_dpp v184, v184 quad_perm:[0,0,0,0] row_mask:0xf bank_mask:0xf\n"
      "v_mfma_f32_16x16x32_bf16 v[188:191], v[28:31], v[144:147], v[188:191]\n"
      "s_nop 0\n"
      "v_fma_f32 v249, v192, v192, s39\n"
      "v_fmac_f32 v249, v193, v193\n"
      "v_mfma_f32_16x16x32_bf16 v[144:147], v[180:183], v[160:163], 0\n"
      "v_fmac_f32 v249, v194, v194\n"
      "v_fmac_f32 v249, v195, v195\n"
      "v_mfma_f32_16x16x32_bf16 v[144:147], v[236:239], v[50:53], v[144:147]\n"
      "v_fmac_f32 v249, v250, v250\n"
      "v_fmac_f32 v249, v251, v251\n"
      "v_mfma_f32_16x16x32_bf16 v[172:175], v[180:183], v[220:223], 0\n"
      "v_fmac_f32 v249, v252, v252\n"
      "v_fmac_f32 v249, v253, v253\n"
      "v_mfma_f32_16x16x32_bf16 v[172:175], v[236:239], v[54:57], v[172:175]\n"
      "v_fmac_f32 v249, v188, v188\n"
      "v_fmac_f32 v249, v189, v189\n"
      "v_mfma_f32_16x16x32_bf16 v[220:223], v[180:183], v[156:159], 0\n"
      "v_fmac_f32 v249, v190, v190\n"
      "v_fmac_f32 v249, v191, v191\n"
      "v_mfma_f32_16x16x32_bf16 v[220:223], v[236:239], v[58:61], v[220:223]\n"
      "v_mov_b32 v156, v249\n"
      "v_rcp_f32 v184, v184\n"
      "v_cndmask_b32 v144, v144, v172, s[42:43]\n"
      "v_permlane16_swap_b32 v249, v156\n"
      "v_add_f32 v249, v249, v156\n"
      "s_nop 1\n"
      "v_permlane32_swap_b32 v248, v249\n"
      "s_waitcnt lgkmcnt(0)\n"
      "v_mfma_f32_16x16x32_bf16 v[172:175], a[0:3], v[212:215], 0\n"
      "v_add_f32 v248, v248, v249\n"
      "v_cndmask_b32 v144, v144, v220, s[44:45]\n"
      "v_mfma_f32_16x16x32_bf16 v[172:175], a[36:39], v[228:231], v[172:175]\n"
      "ds_write_b32 v129, v248 offset:1024\n"
      "v_mfma_f32_16x16x32_bf16 v[156:159], a[4:7], v[212:215], 0\n"
      "v_mul_f32 v144, v184, v144\n"
      "v_cvt_pk_bf16_f32 v144, v144, v144\n"
      "v_mfma_f32_16x16x32_bf16 v[156:159], a[40:43], v[228:231], v[156:159]\n"
      "ds_write_b16 v135, v144 offset:16\n"
      "s_branch L_join_21_%=\n"
      "L_flg_82_%=:\n"
      "v_cmp_gt_i32 vcc, 0, v148\n"
      "s_cbranch_vccz L_fb_83_%=\n"
      "s_sleep 1\n"
      "ds_read_b128 v[148:151], v128 offset:2048\n"
      "s_waitcnt lgkmcnt(0)\n"
      "v_bitop3_b32 v148, v148, v149, s4 bitop3:0x7e\n"
      "v_bitop3_b32 v150, v150, v151, s4 bitop3:0x7e\n"
      "v_or_b32 v148, v148, v150\n"
      "v_cmp_ne_u32 s[48:49], 0, v148\n"
      "s_branch L_flg_82_%=\n"
      "L_edge_85_%=:\n"
      "s_mov_b32 s11, s29\n"
      "s_sub_u32 s28, s60, s8\n"
      "s_sub_u32 s29, s61, s9\n"
      "s_mov_b32 s60, s8\n"
      "s_mov_b32 s61, s9\n"
      "s_mov_b64 exec, s[64:65]\n"
      "v_add_u32 v116, s28, v116\n"
      "s_mov_b64 exec, s[70:71]\n"
      "v_add_u32 v116, s29, v116\n"
      "s_mov_b64 exec, s[66:67]\n"
      "v_add_u32 v117, s28, v117\n"
      "s_mov_b64 exec, s[72:73]\n"
      "v_add_u32 v117, s29, v117\n"
      "s_mov_b64 exec, s[68:69]\n"
      "v_add_u32 v118, s28, v118\n"
      "s_mov_b64 exec, s[74:75]\n"
      "v_add_u32 v118, s29, v118\n"
      "s_mov_b64 exec, -1\n"
      "s_nop 4\n"
      "s_branch L_eb_86_%=\n"
      "L_gen_20_%=:\n"
      "s_waitcnt lgkmcnt(0)\n"
      "s_nop 7\n"
      "s_nop 7\n"
      "v_mbcnt_lo_u32_b32 v174, -1, 0\n"
      "v_mbcnt_hi_u32_b32 v174, -1, v174\n"
      "v_and_b32 v175, 15, v174\n"
      "v_lshrrev_b32 v250, 4, v174\n"
      "v_mul_u32_u24 v251, 0x62, v175\n"
      "v_lshl_add_u32 v251, v250, 3, v251\n"
      "v_add_u32 v251, s30, v251\n"
      "v_and_b32 v174, -4, v251\n"
      "v_and_b32 v251, 2, v251\n"
      "v_lshlrev_b32 v250, 2, v250\n"
      "v_sub_u32 v175, v175, v250\n"
      "v_and_b32 v250, 1, v175\n"
      "v_lshlrev_b32 v250, 4, v250\n"
      "v_lshrrev_b32_e64 v175, 1, v175\n"
      "v_cmp_eq_u32 vcc, 0, v175\n"
      "s_movk_i32 s31, 0x3fb9\n"
      "v_lshlrev_b32 v250, v250, s31\n"
      "ds_read2_b32 v[208:209], v174 offset0:0 offset1:1\n"
      "ds_read2_b32 v[210:211], v174 offset0:8 offset1:9\n"
      "ds_read2_b32 v[148:149], v174 offset0:16 offset1:17\n"
      "ds_read2_b32 v[150:151], v174 offset0:2 offset1:10\n"
      "ds_read2_b32 v[152:153], v174 offset0:18 offset1:24\n"
      "v_add_u32 v174, 1568, v174\n"
      "ds_read2_b32 v[154:155], v174 offset0:0 offset1:1\n"
      "ds_read2_b32 v[204:205], v174 offset0:8 offset1:9\n"
      "ds_read2_b32 v[206:207], v174 offset0:16 offset1:17\n"
      "ds_read2_b32 v[252:253], v174 offset0:2 offset1:10\n"
      "ds_read2_b32 v[254:255], v174 offset0:18 offset1:24\n"
      "v_add_u32 v174, 1568, v174\n"
      "ds_read2_b32 v[144:145], v174 offset0:0 offset1:1\n"
      "ds_read2_b32 v[146:147], v174 offset0:8 offset1:9\n"
      "ds_read2_b32 v[200:201], v174 offset0:16 offset1:17\n"
      "ds_read2_b32 v[202:203], v174 offset0:2 offset1:10\n"
      "ds_read2_b32 v[216:217], v174 offset0:18 offset1:24\n"
      "v_cndmask_b32 v218, 0, v250, vcc\n"
      "v_cmp_eq_u32 vcc, 1, v175\n"
      "s_nop 1\n"
      "v_cndmask_b32 v219, 0, v250, vcc\n"
      "v_mfma_f32_16x16x32_bf16 v[224:227], v[228:231], v[244:247], v[90:93]\n"
      "s_waitcnt lgkmcnt(11)\n"
      "v_alignbyte_b32 v208, v209, v208, v251\n"
      "v_mfma_f32_16x16x32_bf16 v[224:227], v[34:37], v[38:41], v[224:227]\n"
      "v_alignbyte_b32 v209, v150, v209, v251\n"
      "v_alignbyte_b32 v210, v211, v210, v251\n"
      "v_mfma_f32_16x16x32_bf16 v[232:235], v[164:167], v[244:247], v[94:97]\n"
      "v_alignbyte_b32 v211, v151, v211, v251\n"
      "v_alignbyte_b32 v148, v149, v148, v251\n"
      "v_mfma_f32_16x16x32_bf16 v[232:235], v[40:43], v[38:41], v[232:235]\n"
      "s_waitcnt lgkmcnt(6)\n"
      "v_alignbyte_b32 v149, v152, v149, v251\n"
      "v_alignbyte_b32 v153, v153, v153, v251\n"
      "v_mfma_f32_16x16x32_bf16 v[240:243], v[192:195], v[244:247], v[72:75]\n"
      "v_lshlrev_b32 v153, 16, v153\n"
      "v_fmac_f32 v172, s37, v153\n"
      "v_mfma_f32_16x16x32_bf16 v[240:243], v[180:183], v[164:167], v[240:243]\n"
      "v_alignbyte_b32 v154, v155, v154, v251\n"
      "v_alignbyte_b32 v155, v252, v155, v251\n"
      "v_mfma_f32_16x16x32_bf16 v[240:243], v[188:191], v[38:41], v[240:243]\n"
      "v_alignbyte_b32 v204, v205, v204, v251\n"
      "v_alignbyte_b32 v205, v253, v205, v251\n"
      "v_mfma_f32_16x16x32_bf16 v[150:153], v[136:139], v[244:247], v[98:101]\n"
      "v_alignbyte_b32 v206, v207, v206, v251\n"
      "s_waitcnt lgkmcnt(0)\n"
      "v_mfma_f32_16x16x32_bf16 v[150:153], v[46:49], v[38:41], v[150:153]\n"
      "v_alignbyte_b32 v207, v254, v207, v251\n"
      "v_alignbyte_b32 v255, v255, v255, v251\n"
      "v_mfma_f32_16x16x16_bf16 v[176:179], v[218:219], v[208:209], v[176:179]\n"
      "v_lshlrev_b32 v255, 16, v255\n"
      "v_mfma_f32_16x16x16_bf16 v[184:187], v[218:219], v[210:211], v[184:187]\n"
      "v_mfma_f32_16x16x16_bf16 v[236:239], v[218:219], v[148:149], v[236:239]\n"
      "ds_read_b128 v[208:211], v128 offset:2048\n"
      "s_nop 3\n"
      "v_max3_f32 v148, v176, v177, v178\n"
      "s_nop 0\n"
      "v_max3_f32 v149, v179, v184, v185\n"
      "v_mfma_f32_16x16x16_bf16 v[224:227], v[218:219], v[154:155], v[224:227]\n"
      "v_max3_f32 v148, v148, v186, v187\n"
      "v_max3_f32 v149, v149, v236, v237\n"
      "v_mfma_f32_16x16x16_bf16 v[232:235], v[218:219], v[204:205], v[232:235]\n"
      "v_max3_f32 v148, v148, v238, v239\n"
      "v_max3_f32 v148, v148, v149, v172\n"
      "v_mfma_f32_16x16x16_bf16 v[150:153], v[218:219], v[206:207], v[150:153]\n"
      "v_mov_b32 v149, v148\n"
      "v_fmac_f32 v240, s37, v255\n"
      "s_nop 0\n"
      "v_permlane16_swap_b32 v148, v149\n"
      "v_max_f32 v148, v148, v149\n"
      "v_mov_b32 v149, v148\n"
      "v_alignbyte_b32 v144, v145, v144, v251\n"
      "s_nop 0\n"
      "v_permlane32_swap_b32 v148, v149\n"
      "v_max_f32 v148, v148, v149\n"
      "v_sub_f32 v176, v176, v148\n"
      "v_sub_f32 v177, v177, v148\n"
      "v_sub_f32 v178, v178, v148\n"
      "v_sub_f32 v179, v179, v148\n"
      "v_sub_f32 v184, v184, v148\n"
      "v_sub_f32 v185, v185, v148\n"
      "v_sub_f32 v186, v186, v148\n"
      "v_sub_f32 v187, v187, v148\n"
      "v_sub_f32 v236, v236, v148\n"
      "v_sub_f32 v237, v237, v148\n"
      "v_sub_f32 v238, v238, v148\n"
      "v_sub_f32 v239, v239, v148\n"
      "v_sub_f32 v172, v172, v148\n"
      "v_exp_f32 v176, v176\n"
      "v_exp_f32 v177, v177\n"
      "v_exp_f32 v178, v178\n"
      "v_cvt_pk_bf16_f32 v176, v176, v177\n"
      "v_exp_f32 v179, v179\n"
      "v_exp_f32_e64 v184, v184\n"
      "v_cvt_pk_bf16_f32 v177, v178, v179\n"
      "v_exp_f32 v185, v185\n"
      "v_exp_f32 v186, v186\n"
      "v_cvt_pk_bf16_f32 v178, v184, v185\n"
      "v_exp_f32 v187, v187\n"
      "v_exp_f32 v236, v236\n"
      "v_cvt_pk_bf16_f32 v179, v186, v187\n"
      "v_exp_f32 v237, v237\n"
      "v_exp_f32 v238, v238\n"
      "v_exp_f32 v239, v239\n"
      "v_exp_f32 v172, v172\n"
      "v_cvt_pk_bf16_f32 v62, v236, v237\n"
      "v_cvt_pk_bf16_f32 v63, v238, v239\n"
      "v_cvt_pk_bf16_f32 v64, v172, 0\n"
      "v_max3_f32 v148, v224, v225, v226\n"
      "v_max3_f32 v149, v227, v232, v233\n"
      "v_mfma_f32_16x16x32_bf16 v[244:247], v[66:69], v[176:179], 0\n"
      "v_alignbyte_b32 v145, v202, v145, v251\n"
      "v_max3_f32 v148, v148, v234, v235\n"
      "v_mfma_f32_16x16x32_bf16 v[244:247], v[66:69], v[62:65], v[244:247]\n"
      "ds_read_b128 v[204:207], v119 offset:12288\n"
      "v_max3_f32 v149, v149, v150, v151\n"
      "v_max3_f32 v148, v148, v152, v153\n"
      "v_max3_f32 v148, v148, v149, v240\n"
      "v_mfma_f32_16x16x32_bf16 v[252:255], v[160:163], v[176:179], 0\n"
      "v_alignbyte_b32 v146, v147, v146, v251\n"
      "v_alignbyte_b32 v147, v203, v147, v251\n"
      "v_mfma_f32_16x16x32_bf16 v[252:255], v[50:53], v[62:65], v[252:255]\n"
      "v_alignbyte_b32 v200, v201, v200, v251\n"
      "v_mov_b32 v149, v148\n"
      "v_alignbyte_b32 v201, v216, v201, v251\n"
      "v_alignbyte_b32 v217, v217, v217, v251\n"
      "v_lshlrev_b32 v217, 16, v217\n"
      "v_permlane16_swap_b32 v148, v149\n"
      "v_max_f32 v148, v148, v149\n"
      "v_mov_b32 v149, v148\n"
      "s_waitcnt lgkmcnt(1)\n"
      "v_bitop3_b32 v208, v208, v209, s4 bitop3:0x7e\n"
      "v_bitop3_b32 v210, v210, v211, s4 bitop3:0x7e\n"
      "v_or_b32 v208, v208, v210\n"
      "v_cmp_ne_u32 vcc, 0, v208\n"
      "v_permlane32_swap_b32 v148, v149\n"
      "v_max_f32 v148, v148, v149\n"
      "v_sub_f32 v224, v224, v148\n"
      "v_sub_f32 v225, v225, v148\n"
      "v_sub_f32 v226, v226, v148\n"
      "v_sub_f32 v227, v227, v148\n"
      "v_sub_f32 v232, v232, v148\n"
      "v_sub_f32 v233, v233, v148\n"
      "v_sub_f32 v234, v234, v148\n"
      "v_sub_f32 v235, v235, v148\n"
      "v_sub_f32 v150, v150, v148\n"
      "v_sub_f32 v151, v151, v148\n"
      "v_sub_f32 v152, v152, v148\n"
      "v_sub_f32 v153, v153, v148\n"
      "v_sub_f32 v240, v240, v148\n"
      "v_mfma_f32_16x16x32_bf16 v[236:239], v[220:223], v[176:179], 0\n"
      "s_mov_b64 s[48:49], vcc\n"
      "v_exp_f32 v224, v224\n"
      "v_mfma_f32_16x16x32_bf16 v[236:239], v[54:57], v[62:65], v[236:239]\n"
      "s_cbranch_vccnz L_flg_92_%=\n"
      "L_fb_93_%=:\n"
      "v_mfma_f32_16x16x32_bf16 v[208:211], v[156:159], v[176:179], 0\n"
      "s_waitcnt lgkmcnt(0)\n"
      "v_exp_f32 v225, v225\n"
      "v_mfma_f32_16x16x32_bf16 v[208:211], v[58:61], v[62:65], v[208:211]\n"
      "global_store_dwordx4 v116, v[204:207], s[54:55] offset:256 sc1\n"
      "v_mfma_f32_16x16x32_bf16 v[176:179], v[228:231], v[212:215], v[102:105]\n"
      "v_exp_f32 v226, v226\n"
      "v_mfma_f32_16x16x32_bf16 v[176:179], v[34:37], v[44:47], v[176:179]\n"
      "v_cvt_pk_bf16_f32 v224, v224, v225\n"
      "v_exp_f32 v227, v227\n"
      "v_mfma_f32_16x16x32_bf16 v[204:207], v[164:167], v[212:215], v[106:109]\n"
      "v_exp_f32 v232, v232\n"
      "v_mfma_f32_16x16x32_bf16 v[204:207], v[40:43], v[44:47], v[204:207]\n"
      "v_cvt_pk_bf16_f32 v225, v226, v227\n"
      "v_exp_f32 v233, v233\n"
      "v_mfma_f32_16x16x32_bf16 v[228:231], v[192:195], v[212:215], v[74:77]\n"
      "v_exp_f32 v234, v234\n"
      "v_mfma_f32_16x16x32_bf16 v[228:231], v[180:183], v[136:139], v[228:231]\n"
      "v_cvt_pk_bf16_f32 v226, v232, v233\n"
      "v_exp_f32 v235, v235\n"
      "v_mfma_f32_16x16x32_bf16 v[228:231], v[188:191], v[44:47], v[228:231]\n"
      "v_exp_f32_e64 v150, v150\n"
      "v_mfma_f32_16x16x32_bf16 v[164:167], v[136:139], v[212:215], v[110:113]\n"
      "v_cvt_pk_bf16_f32 v227, v234, v235\n"
      "v_mfma_f32_16x16x32_bf16 v[164:167], v[46:49], v[44:47], v[164:167]\n"
      "v_exp_f32 v151, v151\n"
      "v_mfma_f32_16x16x32_bf16 v[212:215], v[180:183], v[192:195], v[76:79]\n"
      "v_exp_f32 v152, v152\n"
      "v_mfma_f32_16x16x16_bf16 v[176:179], v[218:219], v[144:145], v[176:179]\n"
      "v_exp_f32 v153, v153\n"
      "v_mfma_f32_16x16x16_bf16 v[204:207], v[218:219], v[146:147], v[204:207]\n"
      "v_exp_f32 v240, v240\n"
      "v_mfma_f32_16x16x16_bf16 v[164:167], v[218:219], v[200:201], v[164:167]\n"
      "ds_read_b128 v[192:195], v120\n"
      "v_fmac_f32_e64 v228, s37, v217\n"
      "v_mfma_f32_16x16x16_bf16 v[212:215], v[190:191], v[188:189], v[212:215]\n"
      "v_cvt_pk_bf16_f32 v62, v150, v151\n"
      "v_cvt_pk_bf16_f32 v63, v152, v153\n"
      "v_cvt_pk_bf16_f32 v64, v240, 0\n"
      "v_rcp_f32 v244, v244\n"
      "v_max3_f32 v230, v176, v177, v178\n"
      "v_mfma_f32_16x16x32_bf16 v[152:155], v[66:69], v[224:227], 0\n"
      "v_mul_f32 v252, v244, v252\n"
      "v_mul_f32 v253, v244, v253\n"
      "v_mfma_f32_16x16x32_bf16 v[152:155], v[66:69], v[62:65], v[152:155]\n"
      "v_cvt_pk_bf16_f32 v252, v252, v253\n"
      "v_mul_f32 v254, v244, v254\n"
      "v_mfma_f32_16x16x32_bf16 v[188:191], v[160:163], v[224:227], 0\n"
      "v_mul_f32 v255, v244, v255\n"
      "v_cvt_pk_bf16_f32 v253, v254, v255\n"
      "v_mfma_f32_16x16x32_bf16 v[188:191], v[50:53], v[62:65], v[188:191]\n"
      "v_mul_f32 v236, v244, v236\n"
      "v_mul_f32_e64 v237, v244, v237\n"
      "v_mfma_f32_16x16x32_bf16 v[214:217], v[220:223], v[224:227], 0\n"
      "v_cvt_pk_bf16_f32 v254, v236, v237\n"
      "v_mul_f32 v238, v244, v238\n"
      "v_mfma_f32_16x16x32_bf16 v[214:217], v[54:57], v[62:65], v[214:217]\n"
      "v_mul_f32 v239, v244, v239\n"
      "v_cvt_pk_bf16_f32 v255, v238, v239\n"
      "v_mfma_f32_16x16x32_bf16 v[236:239], v[156:159], v[224:227], 0\n"
      "v_mul_f32 v208, v244, v208\n"
      "v_mul_f32 v209, v244, v209\n"
      "v_mfma_f32_16x16x32_bf16 v[236:239], v[58:61], v[62:65], v[236:239]\n"
      "ds_write_b128 v124, v[252:255] offset:18432\n"
      "v_cvt_pk_bf16_f32 v208, v208, v209\n"
      "v_mul_f32 v210, v244, v210\n"
      "v_mul_f32 v211, v244, v211\n"
      "v_cvt_pk_bf16_f32 v209, v210, v211\n"
      "v_max3_f32 v212, v179, v204, v205\n"
      "v_max3_f32 v230, v230, v206, v207\n"
      "v_max3_f32 v212, v212, v164, v165\n"
      "v_max3_f32 v230, v230, v166, v167\n"
      "v_max3_f32 v230, v230, v212, v228\n"
      "v_mov_b32 v212, v230\n"
      "s_waitcnt lgkmcnt(1)\n"
      "s_nop 0\n"
      "v_permlane16_swap_b32 v230, v212\n"
      "v_max_f32 v230, v230, v212\n"
      "v_mov_b32 v212, v230\n"
      "s_nop 1\n"
      "v_permlane32_swap_b32 v230, v212\n"
      "v_max_f32 v230, v230, v212\n"
      "v_sub_f32 v176, v176, v230\n"
      "v_sub_f32 v177, v177, v230\n"
      "v_sub_f32 v178, v178, v230\n"
      "v_sub_f32 v179, v179, v230\n"
      "v_sub_f32 v204, v204, v230\n"
      "v_sub_f32 v205, v205, v230\n"
      "v_sub_f32 v206, v206, v230\n"
      "v_sub_f32 v207, v207, v230\n"
      "v_sub_f32 v164, v164, v230\n"
      "v_sub_f32 v165, v165, v230\n"
      "v_sub_f32 v166, v166, v230\n"
      "v_sub_f32 v167, v167, v230\n"
      "v_sub_f32 v228, v228, v230\n"
      "v_exp_f32 v176, v176\n"
      "v_exp_f32 v177, v177\n"
      "v_exp_f32 v178, v178\n"
      "v_cvt_pk_bf16_f32 v176, v176, v177\n"
      "v_exp_f32 v179, v179\n"
      "v_exp_f32_e64 v204, v204\n"
      "v_cvt_pk_bf16_f32 v177, v178, v179\n"
      "v_exp_f32 v205, v205\n"
      "v_exp_f32 v206, v206\n"
      "v_cvt_pk_bf16_f32 v178, v204, v205\n"
      "v_exp_f32 v207, v207\n"
      "v_exp_f32 v164, v164\n"
      "v_cvt_pk_bf16_f32 v179, v206, v207\n"
      "ds_write_b64 v125, v[208:209] offset:18432\n"
      "v_exp_f32 v165, v165\n"
      "v_exp_f32 v166, v166\n"
      "v_exp_f32 v167, v167\n"
      "v_exp_f32 v228, v228\n"
      "v_cvt_pk_bf16_f32 v62, v164, v165\n"
      "v_cvt_pk_bf16_f32 v63, v166, v167\n"
      "v_cvt_pk_bf16_f32 v64, v228, 0\n"
      "v_rcp_f32_e64 v152, v152\n"
      "v_mfma_f32_16x16x32_bf16 v[136:139], v[66:69], v[176:179], 0\n"
      "global_store_dwordx4 v117, v[192:195], s[54:55] sc1\n"
      "v_mfma_f32_16x16x32_bf16 v[136:139], v[66:69], v[62:65], v[136:139]\n"
      "ds_read_b128 v[250:253], v123 offset:256\n"
      "v_mul_f32 v188, v152, v188\n"
      "v_mfma_f32_16x16x32_bf16 v[164:167], v[160:163], v[176:179], 0\n"
      "ds_read_b128 v[192:195], v123 offset:3328\n"
      "v_mul_f32 v189, v152, v189\n"
      "v_mfma_f32_16x16x32_bf16 v[164:167], v[50:53], v[62:65], v[164:167]\n"
      "ds_read_b128 v[230:233], v121\n"
      "v_cvt_pk_bf16_f32 v188, v188, v189\n"
      "v_mfma_f32_16x16x32_bf16 v[208:211], v[220:223], v[176:179], 0\n"
      "ds_read_b128 v[224:227], v123 offset:6400\n"
      "v_mul_f32 v190, v152, v190\n"
      "v_mfma_f32_16x16x32_bf16 v[208:211], v[54:57], v[62:65], v[208:211]\n"
      "ds_read_b128 v[144:147], v123 offset:9472\n"
      "v_mul_f32 v191, v152, v191\n"
      "v_mfma_f32_16x16x32_bf16 v[148:151], v[156:159], v[176:179], 0\n"
      "v_cvt_pk_bf16_f32 v189, v190, v191\n"
      "v_mul_f32 v214, v152, v214\n"
      "v_mfma_f32_16x16x32_bf16 v[148:151], v[58:61], v[62:65], v[148:151]\n"
      "ds_read_b128 v[176:179], v123 offset:12544\n"
      "v_mul_f32 v215, v152, v215\n"
      "v_cvt_pk_bf16_f32 v190, v214, v215\n"
      "v_mul_f32 v216, v152, v216\n"
      "v_mul_f32 v217, v152, v217\n"
      "v_cvt_pk_bf16_f32 v191, v216, v217\n"
      "v_mul_f32 v236, v152, v236\n"
      "v_mul_f32 v237, v152, v237\n"
      "v_cvt_pk_bf16_f32 v236, v236, v237\n"
      "v_mul_f32 v238, v152, v238\n"
      "v_mul_f32 v239, v152, v239\n"
      "v_cvt_pk_bf16_f32 v237, v238, v239\n"
      "v_cndmask_b32 v173, v173, v241, s[42:43]\n"
      "v_cndmask_b32 v173, v173, v229, s[44:45]\n"
      "v_cndmask_b32 v173, v173, v213, s[46:47]\n"
      "v_mbcnt_lo_u32_b32 v174, -1, 0\n"
      "v_mbcnt_hi_u32_b32 v174, -1, v174\n"
      "s_waitcnt lgkmcnt(3)\n"
      "v_mfma_f32_16x16x32_bf16 v[244:247], a[216:219], v[250:253], 0\n"
      "ds_read_b128 v[212:215], v123 offset:15616\n"
      "v_min_u32 v174, 48, v174\n"
      "v_mfma_f32_16x16x32_bf16 v[244:247], a[228:231], v[192:195], v[244:247]\n"
      "ds_write_b128 v124, v[188:191] offset:18688\n"
      "s_waitcnt lgkmcnt(3)\n"
      "v_mfma_f32_16x16x32_bf16 v[152:155], a[220:223], v[250:253], 0\n"
      "v_lshlrev_b32 v174, 1, v174\n"
      "v_add_u32_e64 v174, s30, v174\n"
      "v_mfma_f32_16x16x32_bf16 v[152:155], a[232:235], v[192:195], v[152:155]\n"
      "ds_write_b64 v125, v[236:237] offset:18688\n"
      "v_mfma_f32_16x16x32_bf16 v[204:207], a[224:227], v[250:253], 0\n"
      "ds_read_u16 v229, v174 offset:4704\n"
      "v_mfma_f32_16x16x32_bf16 v[204:207], a[236:239], v[192:195], v[204:207]\n"
      "ds_read_b128 v[236:239], v123 offset:512\n"
      "v_mfma_f32_16x16x32_bf16 v[244:247], a[240:243], v[224:227], v[244:247]\n"
      "ds_read_b128 v[216:219], v123 offset:3584\n"
      "v_mfma_f32_16x16x32_bf16 v[244:247], a[252:255], v[144:147], v[244:247]\n"
      "ds_read_b128 v[180:183], v123 offset:6656\n"
      "v_mfma_f32_16x16x32_bf16 v[152:155], a[244:247], v[224:227], v[152:155]\n"
      "s_waitcnt lgkmcnt(5)\n"
      "v_mfma_f32_16x16x32_bf16 v[152:155], v[0:3], v[144:147], v[152:155]\n"
      "s_mov_b32 exec_hi, 0\n"
      "global_store_dwordx4 v118, v[230:233], s[54:55] sc1\n"
      "s_mov_b32 exec_hi, -1\n"
      "v_mfma_f32_16x16x32_bf16 v[204:207], a[248:251], v[224:227], v[204:207]\n"
      "ds_read_b128 v[224:227], v123 offset:9728\n"
      "s_add_u32 s28, s27, 2\n"
      "v_mfma_f32_16x16x32_bf16 v[204:207], v[4:7], v[144:147], v[204:207]\n"
      "s_min_u32 m0, s28, s3\n"
      "v_mfma_f32_16x16x32_bf16 v[244:247], v[8:11], v[176:179], v[244:247]\n"
      "s_movrels_b32 s9, s80\n"
      "s_lshr_b32 s8, s9, 8\n"
      "s_and_b32 s9, s9, 0xff\n"
      "v_mfma_f32_16x16x32_bf16 v[244:247], v[20:23], v[212:215], v[244:247]\n"
      "s_waitcnt lgkmcnt(3)\n"
      "v_lshlrev_b32 v229, 16, v229\n"
      "v_fmac_f32 v173, s37, v229\n"
      "v_mfma_f32_16x16x32_bf16 v[152:155], v[12:15], v[176:179], v[152:155]\n"
      "ds_read_b128 v[228:231], v123 offset:12800\n"
      "v_max_f32_dpp v174, v173, v173 row_ror:8 row_mask:0xf bank_mask:0xf\n"
      "v_mfma_f32_16x16x32_bf16 v[152:155], v[24:27], v[212:215], v[152:155]\n"
      "s_waitcnt lgkmcnt(2)\n"
      "v_max_f32_dpp v174, v174, v174 row_ror:4 row_mask:0xf bank_mask:0xf\n"
      "v_fma_f32 v172, v244, v244, s39\n"
      "v_mfma_f32_16x16x32_bf16 v[204:207], v[16:19], v[176:179], v[204:207]\n"
      "s_mul_i32 s10, s8, 73\n"
      "v_max_f32_dpp v174, v174, v174 row_ror:2 row_mask:0xf bank_mask:0xf\n"
      "v_fmac_f32 v172, v245, v245\n"
      "v_mfma_f32_16x16x32_bf16 v[204:207], v[28:31], v[212:215], v[204:207]\n"
      "s_add_u32 s10, s10, s9\n"
      "v_max_f32_dpp v174, v174, v174 row_ror:1 row_mask:0xf bank_mask:0xf\n"
      "v_fmac_f32 v172, v246, v246\n"
      "v_mfma_f32_16x16x32_bf16 v[192:195], a[216:219], v[236:239], 0\n"
      "s_mul_i32 s28, s10, 4802\n"
      "v_max_f32_dpp v174, v174, v174 row_bcast:15 row_mask:0xa bank_mask:0xf\n"
      "v_fmac_f32_e64 v172, v247, v247\n"
      "v_mfma_f32_16x16x32_bf16 v[192:195], a[228:231], v[216:219], v[192:195]\n"
      "s_add_u32 s56, s18, s28\n"
      "v_max_f32_dpp v174, v174, v174 row_bcast:31 row_mask:0xc bank_mask:0xf\n"
      "s_waitcnt lgkmcnt(1)\n"
      "v_mfma_f32_16x16x32_bf16 v[250:253], a[220:223], v[236:239], 0\n"
      "s_addc_u32 s57, s19, 0\n"
      "v_readlane_b32 s31, v174, 63\n"
      "v_fmac_f32 v172, v152, v152\n"
      "v_mfma_f32_16x16x32_bf16 v[250:253], a[232:235], v[216:219], v[250:253]\n"
      "s_and_b32 s56, s56, -16\n"
      "v_subrev_f32 v173, s31, v173\n"
      "v_fmac_f32 v172, v153, v153\n"
      "v_mfma_f32_16x16x32_bf16 v[188:191], a[224:227], v[236:239], 0\n"
      "ds_read_b128 v[174:177], v123 offset:15872\n"
      "v_fmac_f32 v172, v154, v154\n"
      "v_mfma_f32_16x16x32_bf16 v[188:191], a[236:239], v[216:219], v[188:191]\n"
      "s_sub_u32 s58, s56, 110592\n"
      "v_exp_f32 v173, v173\n"
      "v_mfma_f32_16x16x32_bf16 v[192:195], a[240:243], v[180:183], v[192:195]\n"
      "s_subb_u32 s59, s57, 0\n"
      "v_cvt_pk_bf16_f32 v173, v173, v173\n"
      "v_fmac_f32_e64 v172, v155, v155\n"
      "v_mfma_f32_16x16x32_bf16 v[192:195], a[252:255], v[224:227], v[192:195]\n"
      "ds_write_b16 v130, v173\n"
      "v_mfma_f32_16x16x32_bf16 v[250:253], a[244:247], v[180:183], v[250:253]\n"
      "ds_read_b128 v[216:219], v131\n"
      "v_fmac_f32_e64 v172, v204, v204\n"
      "v_mfma_f32_16x16x32_bf16 v[250:253], v[0:3], v[224:227], v[250:253]\n"
      "ds_read_b128 v[236:239], v131 offset:64\n"
      "v_fmac_f32 v172, v205, v205\n"
      "v_mfma_f32_16x16x32_bf16 v[188:191], a[248:251], v[180:183], v[188:191]\n"
      "s_sub_u32 s56, s56, 73728\n"
      "v_fmac_f32 v172, v206, v206\n"
      "v_fmac_f32 v172, v207, v207\n"
      "v_mfma_f32_16x16x32_bf16 v[188:191], v[4:7], v[224:227], v[188:191]\n"
      "s_subb_u32 s57, s57, 0\n"
      "v_permlane16_swap_b32 v248, v172\n"
      "v_add_f32_e64 v248, v248, v172\n"
      "s_mul_i32 s28, s8, 1373568\n"
      "s_mul_i32 s29, s9, 2688\n"
      "s_add_u32 s28, s28, s29\n"
      "s_cmp_eq_u32 s8, 72\n"
      "s_cselect_b32 s8, 0, 100270464\n"
      "s_cmp_eq_u32 s9, 72\n"
      "s_waitcnt lgkmcnt(3)\n"
      "v_mfma_f32_16x16x32_bf16 v[192:195], v[8:11], v[228:231], v[192:195]\n"
      "s_cselect_b32 s9, 0, 196224\n"
      "s_add_u32 s29, s8, s9\n"
      "s_add_u32 s28, s28, s29\n"
      "v_mfma_f32_16x16x32_bf16 v[192:195], v[20:23], v[174:177], v[192:195]\n"
      "s_add_u32 s52, s16, s28\n"
      "s_addc_u32 s53, s17, 0\n"
      "v_mfma_f32_16x16x32_bf16 v[250:253], v[12:15], v[228:231], v[250:253]\n"
      "s_cmp_lg_u32 s29, s11\n"
      "v_mfma_f32_16x16x32_bf16 v[250:253], v[24:27], v[174:177], v[250:253]\n"
      "ds_read_b128 v[212:215], v114\n"
      "v_mfma_f32_16x16x32_bf16 v[188:191], v[16:19], v[228:231], v[188:191]\n"
      "ds_read_b128 v[228:231], v115\n"
      "s_waitcnt lgkmcnt(2)\n"
      "v_mfma_f32_16x16x32_bf16 v[188:191], v[28:31], v[174:177], v[188:191]\n"
      "s_cbranch_scc1 L_edge_95_%=\n"
      "L_eb_96_%=:\n"
      "v_mfma_f32_16x16x32_bf16 v[224:227], v[216:219], v[66:69], 0\n"
      "v_fma_f32 v249, v192, v192, s39\n"
      "v_fmac_f32 v249, v193, v193\n"
      "v_mfma_f32_16x16x32_bf16 v[224:227], v[236:239], v[66:69], v[224:227]\n"
      "v_fmac_f32 v249, v194, v194\n"
      "v_fmac_f32 v249, v195, v195\n"
      "v_mfma_f32_16x16x32_bf16 v[180:183], v[216:219], v[160:163], 0\n"
      "v_fmac_f32 v249, v250, v250\n"
      "v_fmac_f32 v249, v251, v251\n"
      "v_mfma_f32_16x16x32_bf16 v[180:183], v[236:239], v[50:53], v[180:183]\n"
      "v_fmac_f32 v249, v252, v252\n"
      "v_fmac_f32 v249, v253, v253\n"
      "v_mfma_f32_16x16x32_bf16 v[160:163], v[216:219], v[220:223], 0\n"
      "v_fmac_f32 v249, v188, v188\n"
      "v_fmac_f32 v249, v189, v189\n"
      "v_mfma_f32_16x16x32_bf16 v[160:163], v[236:239], v[54:57], v[160:163]\n"
      "v_fmac_f32 v249, v190, v190\n"
      "v_fmac_f32 v249, v191, v191\n"
      "v_mfma_f32_16x16x32_bf16 v[220:223], v[216:219], v[156:159], 0\n"
      "v_mov_b32 v156, v249\n"
      "v_mov_b32_dpp v224, v224 quad_perm:[0,0,0,0] row_mask:0xf bank_mask:0xf\n"
      "v_mfma_f32_16x16x32_bf16 v[220:223], v[236:239], v[58:61], v[220:223]\n"
      "v_permlane16_swap_b32 v249, v156\n"
      "s_waitcnt lgkmcnt(0)\n"
      "v_mfma_f32_16x16x32_bf16 v[172:175], a[0:3], v[212:215], 0\n"
      "v_add_f32 v249, v249, v156\n"
      "v_cndmask_b32 v180, v180, v160, s[42:43]\n"
      "v_mfma_f32_16x16x32_bf16 v[172:175], a[36:39], v[228:231], v[172:175]\n"
      "v_permlane32_swap_b32 v248, v249\n"
      "v_mfma_f32_16x16x32_bf16 v[156:159], a[4:7], v[212:215], 0\n"
      "v_add_f32 v248, v248, v249\n"
      "v_cndmask_b32 v180, v180, v220, s[44:45]\n"
      "v_mfma_f32_16x16x32_bf16 v[156:159], a[40:43], v[228:231], v[156:159]\n"
      "ds_write_b32 v129, v248 offset:1024\n"
      "v_rcp_f32 v224, v224\n"
      "s_nop 0\n"
      "v_mul_f32_e64 v180, v224, v180\n"
      "v_cvt_pk_bf16_f32 v180, v180, v180\n"
      "ds_write_b16 v135, v180 offset:16\n"
      "s_branch L_join_21_%=\n"
      "L_flg_92_%=:\n"
      "v_cmp_gt_i32 vcc, 0, v208\n"
      "s_cbranch_vccz L_fb_93_%=\n"
      "s_sleep 1\n"
      "ds_read_b128 v[208:211], v128 offset:2048\n"
      "s_waitcnt lgkmcnt(0)\n"
      "v_bitop3_b32 v208, v208, v209, s4 bitop3:0x7e\n"
      "v_bitop3_b32 v210, v210, v211, s4 bitop3:0x7e\n"
      "v_or_b32 v208, v208, v210\n"
      "v_cmp_ne_u32 s[48:49], 0, v208\n"
      "s_branch L_flg_92_%=\n"
      "L_edge_95_%=:\n"
      "s_mov_b32 s11, s29\n"
      "s_sub_u32 s28, s60, s8\n"
      "s_sub_u32 s29, s61, s9\n"
      "s_mov_b32 s60, s8\n"
      "s_mov_b32 s61, s9\n"
      "s_mov_b64 exec, s[64:65]\n"
      "v_add_u32 v116, s28, v116\n"
      "s_mov_b64 exec, s[70:71]\n"
      "v_add_u32 v116, s29, v116\n"
      "s_mov_b64 exec, s[66:67]\n"
      "v_add_u32 v117, s28, v117\n"
      "s_mov_b64 exec, s[72:73]\n"
      "v_add_u32 v117, s29, v117\n"
      "s_mov_b64 exec, s[68:69]\n"
      "v_add_u32 v118, s28, v118\n"
      "s_mov_b64 exec, s[74:75]\n"
      "v_add_u32 v118, s29, v118\n"
      "s_mov_b64 exec, -1\n"
      "s_nop 4\n"
      "s_branch L_eb_96_%=\n"
      "L_gen_30_%=:\n"
      "s_waitcnt lgkmcnt(0)\n"
      "s_nop 7\n"
      "s_nop 7\n"
      "s_cmp_eq_u64 s[48:49], 0\n"
      "s_cbranch_scc1 L_go_103_%=\n"
      "s_mov_b32 m0, s27\n"
      "s_nop 0\n"
      "s_movrels_b32 s9, s80\n"
      "s_lshr_b32 s8, s9, 8\n"
      "s_and_b32 s9, s9, 0xff\n"
      "s_mul_i32 s10, s8, 73\n"
      "s_add_u32 s10, s10, s9\n"
      "s_mul_i32 s30, s10, 4802\n"
      "s_add_u32 s30, s30, s18\n"
      "s_and_b32 s29, s30, 15\n"
      "s_add_u32 s30, s29, 110592\n"
      "s_andn2_b64 s[34:35], s[48:49], 0x801\n"
      "s_cbranch_scc1 L_gen_31_%=\n"
      "v_mbcnt_lo_u32_b32 v202, -1, 0\n"
      "v_mbcnt_hi_u32_b32 v202, -1, v202\n"
      "v_lshlrev_b32 v203, 1, v202\n"
      "v_add_u32 v234, 4802, v203\n"
      "v_cmp_gt_u32 vcc, s29, v203\n"
      "s_nop 1\n"
      "v_cndmask_b32 v203, v234, v203, vcc\n"
      "v_add_u32 v203, 110592, v203\n"
      "v_mov_b32 v234, 0\n"
      "s_mov_b64 exec, 0x7f\n"
      "ds_write_b16 v203, v234\n"
      "s_mov_b64 exec, -1\n"
      "v_lshlrev_b32 v203, 4, v202\n"
      "v_add_u32 v203, 110592, v203\n"
      "v_min_u32 v202, 111296, v203\n"
      "ds_read_b128 v[172:175], v203\n"
      "ds_read_b128 v[148:151], v203 offset:1024\n"
      "ds_read_b128 v[248:251], v203 offset:2048\n"
      "ds_read_b128 v[234:237], v203 offset:3072\n"
      "ds_read_b128 v[152:155], v202 offset:4096\n"
      "s_waitcnt lgkmcnt(2)\n"
      "v_or3_b32 v172, v172, v173, v174\n"
      "v_or3_b32 v172, v172, v175, v148\n"
      "v_or3_b32 v172, v172, v149, v150\n"
      "v_or3_b32 v172, v172, v151, v248\n"
      "v_or3_b32 v172, v172, v249, v250\n"
      "s_waitcnt lgkmcnt(0)\n"
      "v_or3_b32 v172, v172, v251, v234\n"
      "v_or3_b32 v172, v172, v235, v236\n"
      "v_or3_b32 v172, v172, v237, v152\n"
      "v_or3_b32 v172, v172, v153, v154\n"
      "v_bitop3_b32 v172, v172, v155, s40 bitop3:0xa8\n"
      "v_cmp_ne_u32 s[48:49], 0, v172\n"
      "s_cmp_lg_u64 s[48:49], 0\n"
      "s_cbranch_scc1 L_gen_31_%=\n"
      "s_cmp_eq_u64 s[50:51], 0\n"
      "s_cbranch_scc1 L_fast_33_%=\n"
      "L_go_103_%=:\n"
      "v_mfma_f32_16x16x32_bf16 v[172:175], v[184:187], v[176:179], v[90:93]\n"
      "ds_read_b128 v[148:151], v128 offset:3072\n"
      "v_max3_f32 v202, v164, v165, v166\n"
      "v_mfma_f32_16x16x32_bf16 v[172:175], v[34:37], v[38:41], v[172:175]\n"
      "v_max3_f32 v203, v167, v140, v141\n"
      "v_max3_f32 v202, v202, v142, v143\n"
      "v_mfma_f32_16x16x32_bf16 v[248:251], v[168:171], v[176:179], v[94:97]\n"
      "v_max3_f32 v203, v203, v192, v193\n"
      "v_max3_f32 v202, v202, v194, v195\n"
      "v_mfma_f32_16x16x32_bf16 v[248:251], v[40:43], v[38:41], v[248:251]\n"
      "v_max3_f32 v202, v202, v203, v200\n"
      "v_mov_b32 v203, v202\n"
      "v_mfma_f32_16x16x32_bf16 v[234:237], v[144:147], v[176:179], v[72:75]\n"
      "s_nop 0\n"
      "v_permlane16_swap_b32 v202, v203\n"
      "v_mfma_f32_16x16x32_bf16 v[234:237], v[196:199], v[168:171], v[234:237]\n"
      "v_max_f32 v202, v202, v203\n"
      "v_mov_b32 v203, v202\n"
      "v_mfma_f32_16x16x32_bf16 v[234:237], v[230:233], v[38:41], v[234:237]\n"
      "s_nop 0\n"
      "v_permlane32_swap_b32 v202, v203\n"
      "v_mfma_f32_16x16x32_bf16 v[152:155], v[156:159], v[176:179], v[98:101]\n"
      "v_max_f32 v202, v202, v203\n"
      "v_sub_f32 v164, v164, v202\n"
      "v_mfma_f32_16x16x32_bf16 v[152:155], v[46:49], v[38:41], v[152:155]\n"
      "v_sub_f32 v165, v165, v202\n"
      "v_sub_f32 v166, v166, v202\n"
      "v_sub_f32 v167, v167, v202\n"
      "v_sub_f32 v140, v140, v202\n"
      "v_sub_f32 v141, v141, v202\n"
      "v_sub_f32 v142, v142, v202\n"
      "v_sub_f32 v143, v143, v202\n"
      "v_sub_f32 v192, v192, v202\n"
      "v_sub_f32 v193, v193, v202\n"
      "v_sub_f32 v194, v194, v202\n"
      "v_sub_f32 v195, v195, v202\n"
      "v_sub_f32 v200, v200, v202\n"
      "v_exp_f32 v164, v164\n"
      "v_exp_f32 v165, v165\n"
      "v_exp_f32 v166, v166\n"
      "v_cvt_pk_bf16_f32 v164, v164, v165\n"
      "v_exp_f32 v167, v167\n"
      "v_exp_f32 v140, v140\n"
      "v_cvt_pk_bf16_f32 v165, v166, v167\n"
      "v_exp_f32 v141, v141\n"
      "v_exp_f32 v142, v142\n"
      "v_cvt_pk_bf16_f32 v166, v140, v141\n"
      "v_exp_f32 v143, v143\n"
      "v_exp_f32 v192, v192\n"
      "v_cvt_pk_bf16_f32 v167, v142, v143\n"
      "v_exp_f32_e64 v193, v193\n"
      "v_exp_f32_e64 v194, v194\n"
      "v_exp_f32 v195, v195\n"
      "v_mfma_f32_16x16x32_bf16 v[140:143], v[184:187], v[136:139], v[102:105]\n"
      "s_waitcnt lgkmcnt(0)\n"
      "v_exp_f32_e64 v200, v200\n"
      "v_mfma_f32_16x16x32_bf16 v[140:143], v[34:37], v[44:47], v[140:143]\n"
      "v_cvt_pk_bf16_f32 v62, v192, v193\n"
      "v_cvt_pk_bf16_f32 v63, v194, v195\n"
      "v_cvt_pk_bf16_f32 v64, v200, 0\n"
      "v_bitop3_b32 v148, v148, v149, s4 bitop3:0x7e\n"
      "v_bitop3_b32 v150, v150, v151, s4 bitop3:0x7e\n"
      "v_mfma_f32_16x16x32_bf16 v[176:179], v[66:69], v[164:167], 0\n"
      "v_or_b32 v148, v148, v150\n"
      "v_cmp_ne_u32 vcc, 0, v148\n"
      "v_mfma_f32_16x16x32_bf16 v[176:179], v[66:69], v[62:65], v[176:179]\n"
      "s_mov_b64 s[48:49], vcc\n"
      "v_max3_f32 v200, v172, v173, v174\n"
      "v_max3_f32 v202, v175, v248, v249\n"
      "v_mfma_f32_16x16x32_bf16 v[184:187], v[220:223], v[164:167], 0\n"
      "s_cbranch_vccnz L_flg_104_%=\n"
      "L_fb_105_%=:\n"
      "v_mfma_f32_16x16x32_bf16 v[184:187], v[50:53], v[62:65], v[184:187]\n"
      "v_max3_f32 v200, v200, v250, v251\n"
      "v_max3_f32 v202, v202, v152, v153\n"
      "v_mfma_f32_16x16x32_bf16 v[148:151], v[160:163], v[164:167], 0\n"
      "v_max3_f32 v200, v200, v154, v155\n"
      "v_max3_f32 v200, v200, v202, v234\n"
      "v_mfma_f32_16x16x32_bf16 v[148:151], v[54:57], v[62:65], v[148:151]\n"
      "v_mov_b32_e64 v202, v200\n"
      "v_mfma_f32_16x16x32_bf16 v[192:195], v[208:211], v[164:167], 0\n"
      "s_nop 0\n"
      "v_permlane16_swap_b32 v200, v202\n"
      "v_mfma_f32_16x16x32_bf16 v[192:195], v[58:61], v[62:65], v[192:195]\n"
      "v_max_f32 v200, v200, v202\n"
      "v_mov_b32 v202, v200\n"
      "s_nop 1\n"
      "v_permlane32_swap_b32 v200, v202\n"
      "v_max_f32 v200, v200, v202\n"
      "v_sub_f32 v172, v172, v200\n"
      "v_sub_f32 v173, v173, v200\n"
      "v_sub_f32 v174, v174, v200\n"
      "v_sub_f32 v175, v175, v200\n"
      "v_sub_f32 v248, v248, v200\n"
      "v_sub_f32 v249, v249, v200\n"
      "v_sub_f32 v250, v250, v200\n"
      "v_sub_f32 v251, v251, v200\n"
      "v_sub_f32 v152, v152, v200\n"
      "v_sub_f32 v153, v153, v200\n"
      "v_sub_f32 v154, v154, v200\n"
      "v_sub_f32 v155, v155, v200\n"
      "v_sub_f32 v234, v234, v200\n"
      "v_exp_f32 v172, v172\n"
      "v_exp_f32 v173, v173\n"
      "v_exp_f32 v174, v174\n"
      "v_cvt_pk_bf16_f32 v172, v172, v173\n"
      "v_exp_f32 v175, v175\n"
      "v_exp_f32_e64 v248, v248\n"
      "v_cvt_pk_bf16_f32 v173, v174, v175\n"
      "v_exp_f32 v249, v249\n"
      "v_exp_f32 v250, v250\n"
      "v_cvt_pk_bf16_f32 v174, v248, v249\n"
      "v_exp_f32 v251, v251\n"
      "v_exp_f32 v152, v152\n"
      "v_cvt_pk_bf16_f32 v175, v250, v251\n"
      "v_exp_f32 v153, v153\n"
      "v_exp_f32 v154, v154\n"
      "v_exp_f32 v155, v155\n"
      "v_exp_f32 v234, v234\n"
      "v_cvt_pk_bf16_f32 v62, v152, v153\n"
      "v_mfma_f32_16x16x32_bf16 v[248:251], v[168:171], v[136:139], v[106:109]\n"
      "ds_read_b128 v[168:171], v119 offset:30720\n"
      "v_cvt_pk_bf16_f32 v63, v154, v155\n"
      "v_mfma_f32_16x16x32_bf16 v[248:251], v[40:43], v[44:47], v[248:251]\n"
      "v_cvt_pk_bf16_f32 v64, v234, 0\n"
      "v_rcp_f32 v176, v176\n"
      "v_mfma_f32_16x16x32_bf16 v[164:167], v[144:147], v[136:139], v[74:77]\n"
      "v_mul_f32 v184, v176, v184\n"
      "v_mul_f32_e64 v185, v176, v185\n"
      "v_mfma_f32_16x16x32_bf16 v[164:167], v[196:199], v[156:159], v[164:167]\n"
      "v_cvt_pk_bf16_f32 v184, v184, v185\n"
      "v_mul_f32 v186, v176, v186\n"
      "v_mfma_f32_16x16x32_bf16 v[164:167], v[230:233], v[44:47], v[164:167]\n"
      "v_mul_f32 v187, v176, v187\n"
      "v_cvt_pk_bf16_f32 v185, v186, v187\n"
      "v_mfma_f32_16x16x32_bf16 v[202:205], v[156:159], v[136:139], v[110:113]\n"
      "v_mul_f32 v148, v176, v148\n"
      "v_mul_f32 v149, v176, v149\n"
      "v_mfma_f32_16x16x32_bf16 v[202:205], v[46:49], v[44:47], v[202:205]\n"
      "v_cvt_pk_bf16_f32 v186, v148, v149\n"
      "v_mul_f32 v150, v176, v150\n"
      "v_mfma_f32_16x16x32_bf16 v[154:157], v[196:199], v[144:147], v[76:79]\n"
      "v_mul_f32 v151, v176, v151\n"
      "v_cvt_pk_bf16_f32 v187, v150, v151\n"
      "v_mul_f32 v192, v176, v192\n"
      "v_mul_f32 v193, v176, v193\n"
      "v_cvt_pk_bf16_f32 v192, v192, v193\n"
      "v_mul_f32_e64 v194, v176, v194\n"
      "v_mfma_f32_16x16x16_bf16 v[154:157], v[232:233], v[230:231], v[154:157]\n"
      "ds_write_b128 v124, v[184:187]\n"
      "v_mfma_f32_16x16x32_bf16 v[148:151], v[66:69], v[172:175], 0\n"
      "s_waitcnt lgkmcnt(1)\n"
      "v_mul_f32 v195, v176, v195\n"
      "v_cvt_pk_bf16_f32 v193, v194, v195\n"
      "v_mfma_f32_16x16x32_bf16 v[148:151], v[66:69], v[62:65], v[148:151]\n"
      "global_store_dwordx4 v116, v[168:171], s[54:55] offset:256 sc1\n"
      "v_mfma_f32_16x16x32_bf16 v[196:199], v[220:223], v[172:175], 0\n"
      "ds_read_b128 v[176:179], v120 offset:18432\n"
      "v_mfma_f32_16x16x32_bf16 v[196:199], v[50:53], v[62:65], v[196:199]\n"
      "ds_write_b64 v125, v[192:193]\n"
      "v_mfma_f32_16x16x32_bf16 v[184:187], v[160:163], v[172:175], 0\n"
      "v_max3_f32 v200, v140, v141, v142\n"
      "v_max3_f32 v166, v143, v248, v249\n"
      "v_mfma_f32_16x16x32_bf16 v[184:187], v[54:57], v[62:65], v[184:187]\n"
      "v_max3_f32 v200, v200, v250, v251\n"
      "v_max3_f32 v166, v166, v202, v203\n"
      "v_mfma_f32_16x16x32_bf16 v[192:195], v[208:211], v[172:175], 0\n"
      "v_max3_f32 v200, v200, v204, v205\n"
      "v_max3_f32 v200, v200, v166, v164\n"
      "v_mfma_f32_16x16x32_bf16 v[192:195], v[58:61], v[62:65], v[192:195]\n"
      "v_mov_b32 v166, v200\n"
      "s_nop 1\n"
      "v_permlane16_swap_b32 v200, v166\n"
      "v_max_f32 v200, v200, v166\n"
      "v_mov_b32 v166, v200\n"
      "s_nop 1\n"
      "v_permlane32_swap_b32 v200, v166\n"
      "v_max_f32 v200, v200, v166\n"
      "v_sub_f32 v140, v140, v200\n"
      "v_sub_f32 v141, v141, v200\n"
      "v_sub_f32 v142, v142, v200\n"
      "v_sub_f32 v143, v143, v200\n"
      "v_sub_f32 v248, v248, v200\n"
      "v_sub_f32 v249, v249, v200\n"
      "v_sub_f32 v250, v250, v200\n"
      "v_sub_f32 v251, v251, v200\n"
      "v_sub_f32 v202, v202, v200\n"
      "v_sub_f32 v203, v203, v200\n"
      "v_sub_f32 v204, v204, v200\n"
      "v_sub_f32 v205, v205, v200\n"
      "v_sub_f32 v164, v164, v200\n"
      "v_exp_f32 v140, v140\n"
      "v_exp_f32 v141, v141\n"
      "v_exp_f32 v142, v142\n"
      "v_cvt_pk_bf16_f32 v140, v140, v141\n"
      "v_exp_f32 v143, v143\n"
      "v_exp_f32 v248, v248\n"
      "v_cvt_pk_bf16_f32 v141, v142, v143\n"
      "v_exp_f32 v249, v249\n"
      "v_exp_f32 v250, v250\n"
      "v_cvt_pk_bf16_f32 v142, v248, v249\n"
      "v_exp_f32 v251, v251\n"
      "v_exp_f32 v202, v202\n"
      "v_cvt_pk_bf16_f32 v143, v250, v251\n"
      "v_exp_f32 v203, v203\n"
      "v_exp_f32 v204, v204\n"
      "v_exp_f32 v205, v205\n"
      "v_exp_f32 v164, v164\n"
      "v_cvt_pk_bf16_f32 v62, v202, v203\n"
      "v_cvt_pk_bf16_f32 v63, v204, v205\n"
      "v_cvt_pk_bf16_f32 v64, v164, 0\n"
      "v_rcp_f32 v148, v148\n"
      "v_mfma_f32_16x16x32_bf16 v[136:139], v[66:69], v[140:143], 0\n"
      "ds_read_b128 v[202:205], v123 offset:18688\n"
      "v_mul_f32 v196, v148, v196\n"
      "v_mfma_f32_16x16x32_bf16 v[136:139], v[66:69], v[62:65], v[136:139]\n"
      "ds_read_b128 v[230:233], v123 offset:21760\n"
      "v_mul_f32 v197, v148, v197\n"
      "v_mfma_f32_16x16x32_bf16 v[172:175], v[220:223], v[140:143], 0\n"
      "s_waitcnt lgkmcnt(2)\n"
      "v_cvt_pk_bf16_f32 v196, v196, v197\n"
      "v_mul_f32_e64 v198, v148, v198\n"
      "v_mfma_f32_16x16x32_bf16 v[172:175], v[50:53], v[62:65], v[172:175]\n"
      "global_store_dwordx4 v117, v[176:179], s[54:55] sc1\n"
      "v_mfma_f32_16x16x32_bf16 v[156:159], v[160:163], v[140:143], 0\n"
      "ds_read_b128 v[144:147], v123 offset:24832\n"
      "v_mul_f32 v199, v148, v199\n"
      "v_mfma_f32_16x16x32_bf16 v[156:159], v[54:57], v[62:65], v[156:159]\n"
      "v_cvt_pk_bf16_f32 v197, v198, v199\n"
      "v_mul_f32 v184, v148, v184\n"
      "v_mfma_f32_16x16x32_bf16 v[168:171], v[208:211], v[140:143], 0\n"
      "ds_read_b128 v[176:179], v121 offset:18432\n"
      "v_mul_f32_e64 v185, v148, v185\n"
      "v_mfma_f32_16x16x32_bf16 v[168:171], v[58:61], v[62:65], v[168:171]\n"
      "v_cvt_pk_bf16_f32 v198, v184, v185\n"
      "v_mul_f32 v186, v148, v186\n"
      "v_mul_f32 v187, v148, v187\n"
      "v_cvt_pk_bf16_f32 v199, v186, v187\n"
      "v_mul_f32_e64 v192, v148, v192\n"
      "v_mul_f32 v193, v148, v193\n"
      "v_cvt_pk_bf16_f32 v192, v192, v193\n"
      "v_mul_f32_e64 v194, v148, v194\n"
      "v_mul_f32_e64 v195, v148, v195\n"
      "v_cvt_pk_bf16_f32 v193, v194, v195\n"
      "v_cndmask_b32 v201, v201, v235, s[42:43]\n"
      "v_cndmask_b32 v201, v201, v165, s[44:45]\n"
      "v_cndmask_b32 v201, v201, v155, s[46:47]\n"
      "s_waitcnt lgkmcnt(2)\n"
      "v_mfma_f32_16x16x32_bf16 v[148:151], a[216:219], v[202:205], 0\n"
      "ds_read_b128 v[184:187], v123 offset:27904\n"
      "v_max_f32_dpp v200, v201, v201 row_ror:8 row_mask:0xf bank_mask:0xf\n"
      "v_mfma_f32_16x16x32_bf16 v[148:151], a[228:231], v[230:233], v[148:151]\n"
      "ds_write_b128 v124, v[196:199] offset:256\n"
      "v_mfma_f32_16x16x32_bf16 v[248:251], a[220:223], v[202:205], 0\n"
      "ds_read_b128 v[196:199], v123 offset:30976\n"
      "v_max_f32_dpp v200, v200, v200 row_ror:4 row_mask:0xf bank_mask:0xf\n"
      "v_mfma_f32_16x16x32_bf16 v[248:251], a[232:235], v[230:233], v[248:251]\n"
      "ds_write_b64 v125, v[192:193] offset:256\n"
      "v_mfma_f32_16x16x32_bf16 v[140:143], a[224:227], v[202:205], 0\n"
      "ds_read_b128 v[164:167], v123 offset:34048\n"
      "v_max_f32_dpp v200, v200, v200 row_ror:2 row_mask:0xf bank_mask:0xf\n"
      "v_mfma_f32_16x16x32_bf16 v[140:143], a[236:239], v[230:233], v[140:143]\n"
      "ds_read_b128 v[192:195], v123 offset:18944\n"
      "v_max_f32_dpp v200, v200, v200 row_ror:1 row_mask:0xf bank_mask:0xf\n"
      "s_waitcnt lgkmcnt(6)\n"
      "s_nop 0\n"
      "v_max_f32_dpp v200, v200, v200 row_bcast:15 row_mask:0xa bank_mask:0xf\n"
      "s_nop 1\n"
      "v_max_f32_dpp v200, v200, v200 row_bcast:31 row_mask:0xc bank_mask:0xf\n"
      "s_nop 0\n"
      "v_readlane_b32 s31, v200, 63\n"
      "s_nop 1\n"
      "v_subrev_f32 v201, s31, v201\n"
      "s_waitcnt lgkmcnt(4)\n"
      "v_mfma_f32_16x16x32_bf16 v[148:151], a[240:243], v[144:147], v[148:151]\n"
      "v_exp_f32 v201, v201\n"
      "v_mfma_f32_16x16x32_bf16 v[148:151], a[252:255], v[184:187], v[148:151]\n"
      "s_mov_b32 exec_hi, 0\n"
      "global_store_dwordx4 v118, v[176:179], s[54:55] sc1\n"
      "s_mov_b32 exec_hi, -1\n"
      "v_mfma_f32_16x16x32_bf16 v[248:251], a[244:247], v[144:147], v[248:251]\n"
      "ds_read_b128 v[232:235], v123 offset:22016\n"
      "v_cvt_pk_bf16_f32 v201, v201, v201\n"
      "v_mfma_f32_16x16x32_bf16 v[248:251], v[0:3], v[184:187], v[248:251]\n"
      "ds_write_b16 v130, v201\n"
      "v_mfma_f32_16x16x32_bf16 v[140:143], a[248:251], v[144:147], v[140:143]\n"
      "s_waitcnt lgkmcnt(2)\n"
      "ds_read_b128 v[144:147], v131\n"
      "v_mfma_f32_16x16x32_bf16 v[140:143], v[4:7], v[184:187], v[140:143]\n"
      "ds_read_b128 v[152:155], v131 offset:64\n"
      "v_mfma_f32_16x16x32_bf16 v[148:151], v[8:11], v[196:199], v[148:151]\n"
      "s_add_u32 s28, s27, 2\n"
      "s_min_u32 m0, s28, s3\n"
      "v_mfma_f32_16x16x32_bf16 v[148:151], v[20:23], v[164:167], v[148:151]\n"
      "s_movrels_b32 s9, s80\n"
      "s_lshr_b32 s8, s9, 8\n"
      "v_mfma_f32_16x16x32_bf16 v[248:251], v[12:15], v[196:199], v[248:251]\n"
      "s_and_b32 s9, s9, 0xff\n"
      "s_mul_i32 s10, s8, 73\n"
      "s_add_u32 s10, s10, s9\n"
      "v_mfma_f32_16x16x32_bf16 v[248:251], v[24:27], v[164:167], v[248:251]\n"
      "s_mul_i32 s28, s10, 4802\n"
      "v_fma_f32 v184, v148, v148, s39\n"
      "v_fmac_f32_e64 v184, v149, v149\n"
      "v_mfma_f32_16x16x32_bf16 v[140:143], v[16:19], v[196:199], v[140:143]\n"
      "ds_read_b128 v[186:189], v123 offset:25088\n"
      "v_fmac_f32_e64 v184, v150, v150\n"
      "v_mfma_f32_16x16x32_bf16 v[140:143], v[28:31], v[164:167], v[140:143]\n"
      "ds_read_b128 v[164:167], v123 offset:28160\n"
      "s_waitcnt lgkmcnt(4)\n"
      "v_mfma_f32_16x16x32_bf16 v[196:199], a[216:219], v[192:195], 0\n"
      "s_add_u32 s56, s18, s28\n"
      "v_fmac_f32 v184, v151, v151\n"
      "v_fmac_f32 v184, v248, v248\n"
      "v_mfma_f32_16x16x32_bf16 v[196:199], a[228:231], v[232:235], v[196:199]\n"
      "s_addc_u32 s57, s19, 0\n"
      "v_fmac_f32 v184, v249, v249\n"
      "v_fmac_f32 v184, v250, v250\n"
      "v_mfma_f32_16x16x32_bf16 v[176:179], a[220:223], v[192:195], 0\n"
      "s_and_b32 s56, s56, -16\n"
      "v_fmac_f32 v184, v251, v251\n"
      "s_waitcnt lgkmcnt(2)\n"
      "v_mfma_f32_16x16x32_bf16 v[176:179], a[232:235], v[232:235], v[176:179]\n"
      "s_sub_u32 s58, s56, 110592\n"
      "v_fmac_f32_e64 v184, v140, v140\n"
      "v_fmac_f32_e64 v184, v141, v141\n"
      "v_mfma_f32_16x16x32_bf16 v[200:203], a[224:227], v[192:195], 0\n"
      "ds_read_b128 v[192:195], v123 offset:31232\n"
      "v_fmac_f32_e64 v184, v142, v142\n"
      "v_mfma_f32_16x16x32_bf16 v[200:203], a[236:239], v[232:235], v[200:203]\n"
      "ds_read_b128 v[232:235], v123 offset:34304\n"
      "v_fmac_f32 v184, v143, v143\n"
      "s_subb_u32 s59, s57, 0\n"
      "s_sub_u32 s56, s56, 73728\n"
      "s_subb_u32 s57, s57, 0\n"
      "s_mul_i32 s28, s8, 1373568\n"
      "s_mul_i32 s29, s9, 2688\n"
      "s_add_u32 s28, s28, s29\n"
      "s_cmp_eq_u32 s8, 72\n"
      "s_cselect_b32 s8, 0, 100270464\n"
      "s_cmp_eq_u32 s9, 72\n"
      "s_cselect_b32 s9, 0, 196224\n"
      "s_add_u32 s29, s8, s9\n"
      "s_waitcnt lgkmcnt(2)\n"
      "v_mfma_f32_16x16x32_bf16 v[196:199], a[240:243], v[186:189], v[196:199]\n"
      "s_add_u32 s28, s28, s29\n"
      "s_add_u32 s52, s16, s28\n"
      "s_addc_u32 s53, s17, 0\n"
      "v_mfma_f32_16x16x32_bf16 v[196:199], a[252:255], v[164:167], v[196:199]\n"
      "s_cmp_lg_u32 s29, s11\n"
      "v_mfma_f32_16x16x32_bf16 v[176:179], a[244:247], v[186:189], v[176:179]\n"
      "s_cbranch_scc1 L_edge_107_%=\n"
      "L_eb_108_%=:\n"
      "v_mfma_f32_16x16x32_bf16 v[176:179], v[0:3], v[164:167], v[176:179]\n"
      "v_permlane16_swap_b32 v253, v184\n"
      "v_mfma_f32_16x16x32_bf16 v[200:203], a[248:251], v[186:189], v[200:203]\n"
      "v_add_f32 v253, v253, v184\n"
      "v_mfma_f32_16x16x32_bf16 v[200:203], v[4:7], v[164:167], v[200:203]\n"
      "s_waitcnt lgkmcnt(0)\n"
      "v_mfma_f32_16x16x32_bf16 v[196:199], v[8:11], v[192:195], v[196:199]\n"
      "v_mfma_f32_16x16x32_bf16 v[196:199], v[20:23], v[232:235], v[196:199]\n"
      "v_mfma_f32_16x16x32_bf16 v[176:179], v[12:15], v[192:195], v[176:179]\n"
      "v_mfma_f32_16x16x32_bf16 v[176:179], v[24:27], v[232:235], v[176:179]\n"
      "ds_read_b128 v[164:167], v114 offset:18432\n"
      "v_mfma_f32_16x16x32_bf16 v[200:203], v[16:19], v[192:195], v[200:203]\n"
      "ds_read_b128 v[184:187], v115 offset:18432\n"
      "v_mfma_f32_16x16x32_bf16 v[200:203], v[28:31], v[232:235], v[200:203]\n"
      "s_nop 1\n"
      "v_fma_f32 v192, v196, v196, s39\n"
      "v_mfma_f32_16x16x32_bf16 v[230:233], v[144:147], v[66:69], 0\n"
      "v_fmac_f32 v192, v197, v197\n"
      "v_fmac_f32 v192, v198, v198\n"
      "v_mfma_f32_16x16x32_bf16 v[230:233], v[152:155], v[66:69], v[230:233]\n"
      "v_fmac_f32 v192, v199, v199\n"
      "v_fmac_f32_e64 v192, v176, v176\n"
      "v_mfma_f32_16x16x32_bf16 v[188:191], v[144:147], v[220:223], 0\n"
      "v_fmac_f32 v192, v177, v177\n"
      "v_fmac_f32 v192, v178, v178\n"
      "v_mfma_f32_16x16x32_bf16 v[188:191], v[152:155], v[50:53], v[188:191]\n"
      "v_fmac_f32 v192, v179, v179\n"
      "v_fmac_f32 v192, v200, v200\n"
      "v_mfma_f32_16x16x32_bf16 v[220:223], v[144:147], v[160:163], 0\n"
      "v_fmac_f32 v192, v201, v201\n"
      "v_fmac_f32 v192, v202, v202\n"
      "v_mfma_f32_16x16x32_bf16 v[220:223], v[152:155], v[54:57], v[220:223]\n"
      "v_fmac_f32 v192, v203, v203\n"
      "v_mov_b32 v193, v192\n"
      "v_mfma_f32_16x16x32_bf16 v[160:163], v[144:147], v[208:211], 0\n"
      "s_nop 0\n"
      "v_permlane16_swap_b32 v192, v193\n"
      "v_mfma_f32_16x16x32_bf16 v[160:163], v[152:155], v[58:61], v[160:163]\n"
      "v_add_f32 v192, v192, v193\n"
      "s_waitcnt lgkmcnt(0)\n"
      "v_mfma_f32_16x16x32_bf16 v[232:235], a[0:3], v[164:167], 0\n"
      "v_permlane32_swap_b32 v253, v192\n"
      "v_mfma_f32_16x16x32_bf16 v[232:235], a[36:39], v[184:187], v[232:235]\n"
      "v_add_f32 v253, v253, v192\n"
      "v_mov_b32_dpp v230, v230 quad_perm:[0,0,0,0] row_mask:0xf bank_mask:0xf\n"
      "v_mfma_f32_16x16x32_bf16 v[192:195], a[4:7], v[164:167], 0\n"
      "v_cndmask_b32 v188, v188, v220, s[42:43]\n"
      "v_cndmask_b32 v188, v188, v160, s[44:45]\n"
      "v_mfma_f32_16x16x32_bf16 v[192:195], a[40:43], v[184:187], v[192:195]\n"
      "ds_write_b32 v129, v253\n"
      "v_rcp_f32 v230, v230\n"
      "s_nop 0\n"
      "v_mul_f32_e64 v188, v230, v188\n"
      "v_cvt_pk_bf16_f32 v188, v188, v188\n"
      "ds_write_b16 v135, v188\n"
      "s_branch L_join_32_%=\n"
      "L_flg_104_%=:\n"
      "v_cmp_gt_i32 vcc, 0, v148\n"
      "s_cbranch_vccz L_fb_105_%=\n"
      "s_sleep 1\n"
      "ds_read_b128 v[148:151], v128 offset:3072\n"
      "s_waitcnt lgkmcnt(0)\n"
      "v_bitop3_b32 v148, v148, v149, s4 bitop3:0x7e\n"
      "v_bitop3_b32 v150, v150, v151, s4 bitop3:0x7e\n"
      "v_or_b32 v148, v148, v150\n"
      "v_cmp_ne_u32 s[48:49], 0, v148\n"
      "s_branch L_flg_104_%=\n"
      "L_edge_107_%=:\n"
      "s_mov_b32 s11, s29\n"
      "s_sub_u32 s28, s60, s8\n"
      "s_sub_u32 s29, s61, s9\n"
      "s_mov_b32 s60, s8\n"
      "s_mov_b32 s61, s9\n"
      "s_mov_b64 exec, s[64:65]\n"
      "v_add_u32 v116, s28, v116\n"
      "s_mov_b64 exec, s[70:71]\n"
      "v_add_u32 v116, s29, v116\n"
      "s_mov_b64 exec, s[66:67]\n"
      "v_add_u32 v117, s28, v117\n"
      "s_mov_b64 exec, s[72:73]\n"
      "v_add_u32 v117, s29, v117\n"
      "s_mov_b64 exec, s[68:69]\n"
      "v_add_u32 v118, s28, v118\n"
      "s_mov_b64 exec, s[74:75]\n"
      "v_add_u32 v118, s29, v118\n"
      "s_mov_b64 exec, -1\n"
      "s_nop 4\n"
      "s_branch L_eb_108_%=\n"
      "L_gen_31_%=:\n"
      "s_waitcnt lgkmcnt(0)\n"
      "s_nop 7\n"
      "s_nop 7\n"
      "v_mbcnt_lo_u32_b32 v202, -1, 0\n"
      "v_mbcnt_hi_u32_b32 v202, -1, v202\n"
      "v_and_b32 v203, 15, v202\n"
      "v_lshrrev_b32 v234, 4, v202\n"
      "v_mul_u32_u24 v235, 0x62, v203\n"
      "v_lshl_add_u32 v235, v234, 3, v235\n"
      "v_add_u32 v235, s30, v235\n"
      "v_and_b32 v202, -4, v235\n"
      "v_and_b32 v235, 2, v235\n"
      "v_lshlrev_b32 v234, 2, v234\n"
      "v_sub_u32 v203, v203, v234\n"
      "v_and_b32 v234, 1, v203\n"
      "v_lshlrev_b32 v234, 4, v234\n"
      "v_lshrrev_b32 v203, 1, v203\n"
      "v_cmp_eq_u32 vcc, 0, v203\n"
      "s_movk_i32 s31, 0x3fb9\n"
      "v_lshlrev_b32 v234, v234, s31\n"
      "ds_read2_b32 v[172:173], v202 offset0:0 offset1:1\n"
      "ds_read2_b32 v[174:175], v202 offset0:8 offset1:9\n"
      "ds_read2_b32 v[148:149], v202 offset0:16 offset1:17\n"
      "ds_read2_b32 v[150:151], v202 offset0:2 offset1:10\n"
      "ds_read2_b32 v[248:249], v202 offset0:18 offset1:24\n"
      "v_add_u32 v202, 1568, v202\n"
      "ds_read2_b32 v[250:251], v202 offset0:0 offset1:1\n"
      "ds_read2_b32 v[228:229], v202 offset0:8 offset1:9\n"
      "ds_read2_b32 v[254:255], v202 offset0:16 offset1:17\n"
      "ds_read2_b32 v[152:153], v202 offset0:2 offset1:10\n"
      "ds_read2_b32 v[154:155], v202 offset0:18 offset1:24\n"
      "v_add_u32 v202, 1568, v202\n"
      "ds_read2_b32 v[188:189], v202 offset0:0 offset1:1\n"
      "ds_read2_b32 v[190:191], v202 offset0:8 offset1:9\n"
      "ds_read2_b32 v[204:205], v202 offset0:16 offset1:17\n"
      "ds_read2_b32 v[206:207], v202 offset0:2 offset1:10\n"
      "ds_read2_b32 v[216:217], v202 offset0:18 offset1:24\n"
      "v_cndmask_b32 v218, 0, v234, vcc\n"
      "v_cmp_eq_u32 vcc, 1, v203\n"
      "s_nop 1\n"
      "v_cndmask_b32 v219, 0, v234, vcc\n"
      "v_mfma_f32_16x16x32_bf16 v[224:227], v[184:187], v[176:179], v[90:93]\n"
      "s_waitcnt lgkmcnt(11)\n"
      "v_alignbyte_b32 v172, v173, v172, v235\n"
      "v_mfma_f32_16x16x32_bf16 v[224:227], v[34:37], v[38:41], v[224:227]\n"
      "v_alignbyte_b32 v173, v150, v173, v235\n"
      "v_alignbyte_b32 v174, v175, v174, v235\n"
      "v_mfma_f32_16x16x32_bf16 v[236:239], v[168:171], v[176:179], v[94:97]\n"
      "v_alignbyte_b32 v175, v151, v175, v235\n"
      "v_alignbyte_b32 v148, v149, v148, v235\n"
      "v_mfma_f32_16x16x32_bf16 v[236:239], v[40:43], v[38:41], v[236:239]\n"
      "s_waitcnt lgkmcnt(6)\n"
      "v_alignbyte_b32 v149, v248, v149, v235\n"
      "v_alignbyte_b32 v249, v249, v249, v235\n"
      "v_mfma_f32_16x16x32_bf16 v[240:243], v[144:147], v[176:179], v[72:75]\n"
      "v_lshlrev_b32 v249, 16, v249\n"
      "v_fmac_f32 v200, s37, v249\n"
      "v_mfma_f32_16x16x32_bf16 v[240:243], v[196:199], v[168:171], v[240:243]\n"
      "v_alignbyte_b32 v250, v251, v250, v235\n"
      "v_alignbyte_b32 v251, v152, v251, v235\n"
      "v_mfma_f32_16x16x32_bf16 v[240:243], v[230:233], v[38:41], v[240:243]\n"
      "v_alignbyte_b32 v228, v229, v228, v235\n"
      "v_alignbyte_b32 v229, v153, v229, v235\n"
      "v_mfma_f32_16x16x32_bf16 v[150:153], v[156:159], v[176:179], v[98:101]\n"
      "v_alignbyte_b32 v254, v255, v254, v235\n"
      "s_waitcnt lgkmcnt(0)\n"
      "v_mfma_f32_16x16x32_bf16 v[150:153], v[46:49], v[38:41], v[150:153]\n"
      "v_alignbyte_b32 v255, v154, v255, v235\n"
      "v_alignbyte_b32 v155, v155, v155, v235\n"
      "v_mfma_f32_16x16x16_bf16 v[164:167], v[218:219], v[172:173], v[164:167]\n"
      "v_lshlrev_b32 v155, 16, v155\n"
      "v_mfma_f32_16x16x16_bf16 v[140:143], v[218:219], v[174:175], v[140:143]\n"
      "v_mfma_f32_16x16x16_bf16 v[192:195], v[218:219], v[148:149], v[192:195]\n"
      "ds_read_b128 v[172:175], v128 offset:3072\n"
      "s_nop 3\n"
      "v_max3_f32 v203, v164, v165, v166\n"
      "s_nop 0\n"
      "v_max3_f32 v148, v167, v140, v141\n"
      "v_mfma_f32_16x16x16_bf16 v[224:227], v[218:219], v[250:251], v[224:227]\n"
      "v_max3_f32 v203, v203, v142, v143\n"
      "v_max3_f32 v148, v148, v192, v193\n"
      "v_mfma_f32_16x16x16_bf16 v[236:239], v[218:219], v[228:229], v[236:239]\n"
      "v_max3_f32 v203, v203, v194, v195\n"
      "v_max3_f32 v203, v203, v148, v200\n"
      "v_mfma_f32_16x16x16_bf16 v[150:153], v[218:219], v[254:255], v[150:153]\n"
      "v_mov_b32 v148, v203\n"
      "v_fmac_f32 v240, s37, v155\n"
      "s_nop 0\n"
      "v_permlane16_swap_b32 v203, v148\n"
      "v_max_f32 v203, v203, v148\n"
      "v_mov_b32 v148, v203\n"
      "v_alignbyte_b32 v188, v189, v188, v235\n"
      "s_nop 0\n"
      "v_permlane32_swap_b32 v203, v148\n"
      "v_max_f32 v203, v203, v148\n"
      "v_sub_f32 v164, v164, v203\n"
      "v_sub_f32 v165, v165, v203\n"
      "v_sub_f32 v166, v166, v203\n"
      "v_sub_f32 v167, v167, v203\n"
      "v_sub_f32 v140, v140, v203\n"
      "v_sub_f32 v141, v141, v203\n"
      "v_sub_f32 v142, v142, v203\n"
      "v_sub_f32 v143, v143, v203\n"
      "v_sub_f32 v192, v192, v203\n"
      "v_sub_f32 v193, v193, v203\n"
      "v_sub_f32 v194, v194, v203\n"
      "v_sub_f32 v195, v195, v203\n"
      "v_sub_f32 v200, v200, v203\n"
      "v_exp_f32 v164, v164\n"
      "v_exp_f32 v165, v165\n"
      "v_exp_f32_e64 v166, v166\n"
      "v_cvt_pk_bf16_f32 v164, v164, v165\n"
      "v_exp_f32 v167, v167\n"
      "v_exp_f32 v140, v140\n"
      "v_cvt_pk_bf16_f32 v165, v166, v167\n"
      "v_exp_f32 v141, v141\n"
      "v_exp_f32 v142, v142\n"
      "v_cvt_pk_bf16_f32 v166, v140, v141\n"
      "v_exp_f32 v143, v143\n"
      "v_exp_f32 v192, v192\n"
      "v_cvt_pk_bf16_f32 v167, v142, v143\n"
      "v_exp_f32 v193, v193\n"
      "v_exp_f32 v194, v194\n"
      "v_exp_f32 v195, v195\n"
      "v_exp_f32 v200, v200\n"
      "v_cvt_pk_bf16_f32 v62, v192, v193\n"
      "v_cvt_pk_bf16_f32 v63, v194, v195\n"
      "v_cvt_pk_bf16_f32 v64, v200, 0\n"
      "v_max3_f32 v148, v224, v225, v226\n"
      "v_max3_f32 v148, v148, v238, v239\n"
      "v_max3_f32 v148, v148, v152, v153\n"
      "v_alignbyte_b32 v189, v206, v189, v235\n"
      "v_max3_f32 v149, v227, v236, v237\n"
      "v_max3_f32 v149, v149, v150, v151\n"
      "v_max3_f32 v148, v148, v149, v240\n"
      "v_mov_b32 v149, v148\n"
      "v_alignbyte_b32 v190, v191, v190, v235\n"
      "v_alignbyte_b32 v191, v207, v191, v235\n"
      "v_permlane16_swap_b32 v148, v149\n"
      "v_max_f32 v148, v148, v149\n"
      "v_mov_b32 v149, v148\n"
      "v_alignbyte_b32 v204, v205, v204, v235\n"
      "s_nop 0\n"
      "v_permlane32_swap_b32 v148, v149\n"
      "v_max_f32 v148, v148, v149\n"
      "v_sub_f32 v224, v224, v148\n"
      "v_sub_f32 v225, v225, v148\n"
      "v_sub_f32 v226, v226, v148\n"
      "v_sub_f32 v227, v227, v148\n"
      "v_sub_f32 v236, v236, v148\n"
      "v_sub_f32 v237, v237, v148\n"
      "v_sub_f32 v238, v238, v148\n"
      "v_sub_f32 v239, v239, v148\n"
      "v_sub_f32 v150, v150, v148\n"
      "v_sub_f32_e64 v151, v151, v148\n"
      "v_alignbyte_b32 v205, v216, v205, v235\n"
      "v_alignbyte_b32 v217, v217, v217, v235\n"
      "v_mfma_f32_16x16x32_bf16 v[140:143], v[66:69], v[164:167], 0\n"
      "s_waitcnt lgkmcnt(0)\n"
      "v_lshlrev_b32 v217, 16, v217\n"
      "v_bitop3_b32 v172, v172, v173, s4 bitop3:0x7e\n"
      "v_mfma_f32_16x16x32_bf16 v[140:143], v[66:69], v[62:65], v[140:143]\n"
      "v_bitop3_b32 v174, v174, v175, s4 bitop3:0x7e\n"
      "v_or_b32 v172, v172, v174\n"
      "v_cmp_ne_u32 vcc, 0, v172\n"
      "v_sub_f32 v152, v152, v148\n"
      "v_sub_f32 v153, v153, v148\n"
      "v_sub_f32 v240, v240, v148\n"
      "v_mfma_f32_16x16x32_bf16 v[248:251], v[220:223], v[164:167], 0\n"
      "s_mov_b64 s[48:49], vcc\n"
      "v_exp_f32 v224, v224\n"
      "v_mfma_f32_16x16x32_bf16 v[248:251], v[50:53], v[62:65], v[248:251]\n"
      "s_cbranch_vccnz L_flg_114_%=\n"
      "L_fb_115_%=:\n"
      "v_mfma_f32_16x16x32_bf16 v[172:175], v[160:163], v[164:167], 0\n"
      "v_exp_f32_e64 v225, v225\n"
      "v_mfma_f32_16x16x32_bf16 v[172:175], v[54:57], v[62:65], v[172:175]\n"
      "ds_read_b128 v[176:179], v119 offset:30720\n"
      "v_cvt_pk_bf16_f32 v224, v224, v225\n"
      "v_mfma_f32_16x16x32_bf16 v[192:195], v[208:211], v[164:167], 0\n"
      "v_exp_f32 v226, v226\n"
      "v_mfma_f32_16x16x32_bf16 v[192:195], v[58:61], v[62:65], v[192:195]\n"
      "v_exp_f32 v227, v227\n"
      "v_mfma_f32_16x16x32_bf16 v[164:167], v[184:187], v[136:139], v[102:105]\n"
      "v_exp_f32 v236, v236\n"
      "v_mfma_f32_16x16x32_bf16 v[164:167], v[34:37], v[44:47], v[164:167]\n"
      "v_cvt_pk_bf16_f32 v225, v226, v227\n"
      "v_exp_f32 v237, v237\n"
      "v_mfma_f32_16x16x32_bf16 v[184:187], v[168:171], v[136:139], v[106:109]\n"
      "v_exp_f32 v238, v238\n"
      "v_mfma_f32_16x16x32_bf16 v[184:187], v[40:43], v[44:47], v[184:187]\n"
      "v_cvt_pk_bf16_f32 v226, v236, v237\n"
      "v_exp_f32 v239, v239\n"
      "v_mfma_f32_16x16x16_bf16 v[164:167], v[218:219], v[188:189], v[164:167]\n"
      "v_exp_f32 v150, v150\n"
      "v_cvt_pk_bf16_f32 v227, v238, v239\n"
      "v_exp_f32 v151, v151\n"
      "v_mfma_f32_16x16x16_bf16 v[184:187], v[218:219], v[190:191], v[184:187]\n"
      "s_waitcnt lgkmcnt(0)\n"
      "v_exp_f32 v152, v152\n"
      "v_exp_f32 v153, v153\n"
      "v_exp_f32 v240, v240\n"
      "v_cvt_pk_bf16_f32 v62, v150, v151\n"
      "v_cvt_pk_bf16_f32 v63, v152, v153\n"
      "v_cvt_pk_bf16_f32 v64, v240, 0\n"
      "v_rcp_f32 v140, v140\n"
      "s_nop 0\n"
      "v_mul_f32 v248, v140, v248\n"
      "v_mul_f32 v249, v140, v249\n"
      "v_cvt_pk_bf16_f32 v248, v248, v249\n"
      "v_mul_f32 v250, v140, v250\n"
      "v_mul_f32 v251, v140, v251\n"
      "v_cvt_pk_bf16_f32 v249, v250, v251\n"
      "v_mul_f32 v172, v140, v172\n"
      "v_mul_f32 v173, v140, v173\n"
      "v_cvt_pk_bf16_f32 v250, v172, v173\n"
      "global_store_dwordx4 v116, v[176:179], s[54:55] offset:256 sc1\n"
      "v_mfma_f32_16x16x32_bf16 v[148:151], v[144:147], v[136:139], v[74:77]\n"
      "v_mul_f32 v174, v140, v174\n"
      "v_mul_f32 v175, v140, v175\n"
      "v_mfma_f32_16x16x32_bf16 v[148:151], v[196:199], v[156:159], v[148:151]\n"
      "v_cvt_pk_bf16_f32 v251, v174, v175\n"
      "v_mul_f32_e64 v192, v140, v192\n"
      "v_mfma_f32_16x16x32_bf16 v[148:151], v[230:233], v[44:47], v[148:151]\n"
      "ds_write_b128 v124, v[248:251]\n"
      "v_mfma_f32_16x16x32_bf16 v[172:175], v[156:159], v[136:139], v[110:113]\n"
      "v_mul_f32 v193, v140, v193\n"
      "v_cvt_pk_bf16_f32 v192, v192, v193\n"
      "v_mfma_f32_16x16x32_bf16 v[172:175], v[46:49], v[44:47], v[172:175]\n"
      "v_mul_f32 v194, v140, v194\n"
      "v_mul_f32 v195, v140, v195\n"
      "v_mfma_f32_16x16x32_bf16 v[248:251], v[196:199], v[144:147], v[76:79]\n"
      "v_fmac_f32 v148, s37, v217\n"
      "v_cvt_pk_bf16_f32 v193, v194, v195\n"
      "v_mfma_f32_16x16x32_bf16 v[140:143], v[66:69], v[224:227], 0\n"
      "v_max3_f32 v150, v164, v165, v166\n"
      "v_max3_f32 v151, v167, v184, v185\n"
      "v_mfma_f32_16x16x32_bf16 v[140:143], v[66:69], v[62:65], v[140:143]\n"
      "ds_write_b64 v125, v[192:193]\n"
      "v_mfma_f32_16x16x16_bf16 v[248:251], v[232:233], v[230:231], v[248:251]\n"
      "v_max3_f32 v150, v150, v186, v187\n"
      "v_mfma_f32_16x16x16_bf16 v[172:175], v[218:219], v[204:205], v[172:175]\n"
      "v_mfma_f32_16x16x32_bf16 v[196:199], v[220:223], v[224:227], 0\n"
      "v_mfma_f32_16x16x32_bf16 v[196:199], v[50:53], v[62:65], v[196:199]\n"
      "v_mfma_f32_16x16x32_bf16 v[176:179], v[160:163], v[224:227], 0\n"
      "v_mfma_f32_16x16x32_bf16 v[176:179], v[54:57], v[62:65], v[176:179]\n"
      "v_mfma_f32_16x16x32_bf16 v[192:195], v[208:211], v[224:227], 0\n"
      "ds_read_b128 v[168:171], v120 offset:18432\n"
      "v_mfma_f32_16x16x32_bf16 v[192:195], v[58:61], v[62:65], v[192:195]\n"
      "s_nop 0\n"
      "v_max3_f32 v151, v151, v172, v173\n"
      "v_max3_f32 v150, v150, v174, v175\n"
      "v_max3_f32 v150, v150, v151, v148\n"
      "v_mov_b32 v151, v150\n"
      "s_nop 1\n"
      "v_permlane16_swap_b32 v150, v151\n"
      "v_max_f32 v150, v150, v151\n"
      "v_mov_b32 v151, v150\n"
      "v_rcp_f32 v140, v140\n"
      "s_nop 0\n"
      "v_permlane32_swap_b32 v150, v151\n"
      "v_max_f32 v150, v150, v151\n"
      "v_sub_f32 v164, v164, v150\n"
      "v_sub_f32 v165, v165, v150\n"
      "v_sub_f32 v166, v166, v150\n"
      "v_sub_f32 v167, v167, v150\n"
      "v_sub_f32 v184, v184, v150\n"
      "v_sub_f32 v185, v185, v150\n"
      "v_sub_f32 v186, v186, v150\n"
      "v_sub_f32 v187, v187, v150\n"
      "v_sub_f32 v172, v172, v150\n"
      "v_sub_f32 v173, v173, v150\n"
      "v_sub_f32 v174, v174, v150\n"
      "v_sub_f32 v175, v175, v150\n"
      "v_sub_f32 v148, v148, v150\n"
      "v_exp_f32 v164, v164\n"
      "v_exp_f32 v165, v165\n"
      "v_exp_f32 v166, v166\n"
      "v_cvt_pk_bf16_f32 v164, v164, v165\n"
      "v_exp_f32 v167, v167\n"
      "v_exp_f32 v184, v184\n"
      "v_cvt_pk_bf16_f32 v165, v166, v167\n"
      "v_exp_f32 v185, v185\n"
      "v_exp_f32 v186, v186\n"
      "v_cvt_pk_bf16_f32 v166, v184, v185\n"
      "v_exp_f32 v187, v187\n"
      "v_exp_f32 v172, v172\n"
      "v_cvt_pk_bf16_f32 v167, v186, v187\n"
      "v_exp_f32 v173, v173\n"
      "v_exp_f32 v174, v174\n"
      "v_exp_f32 v175, v175\n"
      "v_exp_f32 v148, v148\n"
      "v_cvt_pk_bf16_f32 v62, v172, v173\n"
      "v_cvt_pk_bf16_f32 v63, v174, v175\n"
      "v_cvt_pk_bf16_f32 v64, v148, 0\n"
      "v_mul_f32 v196, v140, v196\n"
      "v_mul_f32 v197, v140, v197\n"
      "v_mfma_f32_16x16x32_bf16 v[136:139], v[66:69], v[164:167], 0\n"
      "ds_read_b128 v[184:187], v123 offset:18688\n"
      "v_cvt_pk_bf16_f32 v196, v196, v197\n"
      "v_mfma_f32_16x16x32_bf16 v[136:139], v[66:69], v[62:65], v[136:139]\n"
      "ds_read_b128 v[232:235], v123 offset:21760\n"
      "v_mul_f32 v198, v140, v198\n"
      "v_mfma_f32_16x16x32_bf16 v[172:175], v[220:223], v[164:167], 0\n"
      "s_waitcnt lgkmcnt(2)\n"
      "v_mul_f32_e64 v199, v140, v199\n"
      "v_cvt_pk_bf16_f32 v197, v198, v199\n"
      "v_mfma_f32_16x16x32_bf16 v[172:175], v[50:53], v[62:65], v[172:175]\n"
      "global_store_dwordx4 v117, v[168:171], s[54:55] sc1\n"
      "v_mfma_f32_16x16x32_bf16 v[156:159], v[160:163], v[164:167], 0\n"
      "ds_read_b128 v[224:227], v121 offset:18432\n"
      "v_mul_f32 v176, v140, v176\n"
      "v_mfma_f32_16x16x32_bf16 v[156:159], v[54:57], v[62:65], v[156:159]\n"
      "v_mul_f32 v177, v140, v177\n"
      "v_cvt_pk_bf16_f32 v198, v176, v177\n"
      "v_mfma_f32_16x16x32_bf16 v[168:171], v[208:211], v[164:167], 0\n"
      "ds_read_b128 v[164:167], v123 offset:24832\n"
      "v_mul_f32 v178, v140, v178\n"
      "v_mfma_f32_16x16x32_bf16 v[168:171], v[58:61], v[62:65], v[168:171]\n"
      "ds_read_b128 v[144:147], v123 offset:27904\n"
      "v_mul_f32 v179, v140, v179\n"
      "v_cvt_pk_bf16_f32 v199, v178, v179\n"
      "v_mul_f32 v192, v140, v192\n"
      "v_mul_f32 v193, v140, v193\n"
      "v_cvt_pk_bf16_f32 v192, v192, v193\n"
      "v_mul_f32_e64 v194, v140, v194\n"
      "v_mul_f32 v195, v140, v195\n"
      "v_cvt_pk_bf16_f32 v193, v194, v195\n"
      "v_cndmask_b32 v201, v201, v241, s[42:43]\n"
      "v_cndmask_b32 v201, v201, v149, s[44:45]\n"
      "v_cndmask_b32 v201, v201, v249, s[46:47]\n"
      "v_mbcnt_lo_u32_b32 v202, -1, 0\n"
      "s_waitcnt lgkmcnt(3)\n"
      "v_mfma_f32_16x16x32_bf16 v[148:151], a[216:219], v[184:187], 0\n"
      "ds_read_b128 v[176:179], v123 offset:30976\n"
      "v_mbcnt_hi_u32_b32 v202, -1, v202\n"
      "v_mfma_f32_16x16x32_bf16 v[148:151], a[228:231], v[232:235], v[148:151]\n"
      "ds_write_b128 v124, v[196:199] offset:256\n"
      "v_mfma_f32_16x16x32_bf16 v[248:251], a[220:223], v[184:187], 0\n"
      "s_waitcnt lgkmcnt(4)\n"
      "v_min_u32_e64 v202, 48, v202\n"
      "v_lshlrev_b32_e64 v202, 1, v202\n"
      "v_mfma_f32_16x16x32_bf16 v[248:251], a[232:235], v[232:235], v[248:251]\n"
      "ds_write_b64 v125, v[192:193] offset:256\n"
      "v_mfma_f32_16x16x32_bf16 v[140:143], a[224:227], v[184:187], 0\n"
      "ds_read_b128 v[196:199], v123 offset:34048\n"
      "s_waitcnt lgkmcnt(4)\n"
      "v_mfma_f32_16x16x32_bf16 v[140:143], a[236:239], v[232:235], v[140:143]\n"
      "ds_read_b128 v[184:187], v123 offset:18944\n"
      "v_add_u32_e64 v202, s30, v202\n"
      "v_mfma_f32_16x16x32_bf16 v[148:151], a[240:243], v[164:167], v[148:151]\n"
      "ds_read_u16 v200, v202 offset:4704\n"
      "v_mfma_f32_16x16x32_bf16 v[148:151], a[252:255], v[144:147], v[148:151]\n"
      "s_mov_b32 exec_hi, 0\n"
      "global_store_dwordx4 v118, v[224:227], s[54:55] sc1\n"
      "s_mov_b32 exec_hi, -1\n"
      "v_mfma_f32_16x16x32_bf16 v[248:251], a[244:247], v[164:167], v[248:251]\n"
      "ds_read_b128 v[232:235], v123 offset:22016\n"
      "s_add_u32 s28, s27, 2\n"
      "v_mfma_f32_16x16x32_bf16 v[248:251], v[0:3], v[144:147], v[248:251]\n"
      "s_min_u32 m0, s28, s3\n"
      "v_mfma_f32_16x16x32_bf16 v[140:143], a[248:251], v[164:167], v[140:143]\n"
      "ds_read_b128 v[164:167], v123 offset:25088\n"
      "s_movrels_b32 s9, s80\n"
      "v_mfma_f32_16x16x32_bf16 v[140:143], v[4:7], v[144:147], v[140:143]\n"
      "ds_read_b128 v[192:195], v123 offset:28160\n"
      "s_lshr_b32 s8, s9, 8\n"
      "s_and_b32 s9, s9, 0xff\n"
      "s_mul_i32 s10, s8, 73\n"
      "s_add_u32 s10, s10, s9\n"
      "s_mul_i32 s28, s10, 4802\n"
      "s_add_u32 s56, s18, s28\n"
      "s_waitcnt lgkmcnt(4)\n"
      "v_mfma_f32_16x16x32_bf16 v[148:151], v[8:11], v[176:179], v[148:151]\n"
      "s_addc_u32 s57, s19, 0\n"
      "s_and_b32 s56, s56, -16\n"
      "s_sub_u32 s58, s56, 110592\n"
      "v_mfma_f32_16x16x32_bf16 v[148:151], v[20:23], v[196:199], v[148:151]\n"
      "s_subb_u32 s59, s57, 0\n"
      "s_sub_u32 s56, s56, 73728\n"
      "v_mfma_f32_16x16x32_bf16 v[248:251], v[12:15], v[176:179], v[248:251]\n"
      "s_waitcnt lgkmcnt(3)\n"
      "v_lshlrev_b32 v200, 16, v200\n"
      "v_fmac_f32 v201, s37, v200\n"
      "v_mfma_f32_16x16x32_bf16 v[248:251], v[24:27], v[196:199], v[248:251]\n"
      "s_subb_u32 s57, s57, 0\n"
      "v_max_f32_dpp v200, v201, v201 row_ror:8 row_mask:0xf bank_mask:0xf\n"
      "v_fma_f32 v252, v148, v148, s39\n"
      "v_mfma_f32_16x16x32_bf16 v[140:143], v[16:19], v[176:179], v[140:143]\n"
      "s_waitcnt lgkmcnt(2)\n"
      "v_max_f32_dpp v200, v200, v200 row_ror:4 row_mask:0xf bank_mask:0xf\n"
      "v_fmac_f32 v252, v149, v149\n"
      "v_mfma_f32_16x16x32_bf16 v[140:143], v[28:31], v[196:199], v[140:143]\n"
      "s_mul_i32 s28, s8, 1373568\n"
      "v_max_f32_dpp v200, v200, v200 row_ror:2 row_mask:0xf bank_mask:0xf\n"
      "v_fmac_f32_e64 v252, v150, v150\n"
      "v_mfma_f32_16x16x32_bf16 v[196:199], a[216:219], v[184:187], 0\n"
      "s_mul_i32 s29, s9, 2688\n"
      "v_max_f32_dpp v200, v200, v200 row_ror:1 row_mask:0xf bank_mask:0xf\n"
      "v_fmac_f32 v252, v151, v151\n"
      "v_mfma_f32_16x16x32_bf16 v[196:199], a[228:231], v[232:235], v[196:199]\n"
      "s_waitcnt lgkmcnt(0)\n"
      "v_max_f32_dpp v200, v200, v200 row_bcast:15 row_mask:0xa bank_mask:0xf\n"
      "v_fmac_f32 v252, v248, v248\n"
      "v_mfma_f32_16x16x32_bf16 v[176:179], a[220:223], v[184:187], 0\n"
      "s_add_u32 s28, s28, s29\n"
      "v_max_f32_dpp v200, v200, v200 row_bcast:31 row_mask:0xc bank_mask:0xf\n"
      "v_fmac_f32 v252, v249, v249\n"
      "v_mfma_f32_16x16x32_bf16 v[176:179], a[232:235], v[232:235], v[176:179]\n"
      "s_cmp_eq_u32 s8, 72\n"
      "v_readlane_b32 s31, v200, 63\n"
      "v_fmac_f32 v252, v250, v250\n"
      "v_mfma_f32_16x16x32_bf16 v[196:199], a[240:243], v[164:167], v[196:199]\n"
      "s_cselect_b32 s8, 0, 100270464\n"
      "v_subrev_f32 v201, s31, v201\n"
      "v_fmac_f32 v252, v251, v251\n"
      "v_mfma_f32_16x16x32_bf16 v[196:199], a[252:255], v[192:195], v[196:199]\n"
      "s_cmp_eq_u32 s9, 72\n"
      "v_exp_f32 v201, v201\n"
      "v_mfma_f32_16x16x32_bf16 v[176:179], a[244:247], v[164:167], v[176:179]\n"
      "s_cselect_b32 s9, 0, 196224\n"
      "v_cvt_pk_bf16_f32 v201, v201, v201\n"
      "v_fmac_f32 v252, v140, v140\n"
      "v_mfma_f32_16x16x32_bf16 v[176:179], v[0:3], v[192:195], v[176:179]\n"
      "ds_write_b16 v130, v201\n"
      "v_mfma_f32_16x16x32_bf16 v[200:203], a[224:227], v[184:187], 0\n"
      "ds_read_b128 v[144:147], v131\n"
      "v_fmac_f32 v252, v141, v141\n"
      "v_mfma_f32_16x16x32_bf16 v[200:203], a[236:239], v[232:235], v[200:203]\n"
      "ds_read_b128 v[232:235], v131 offset:64\n"
      "v_fmac_f32 v252, v142, v142\n"
      "v_mfma_f32_16x16x32_bf16 v[200:203], a[248:251], v[164:167], v[200:203]\n"
      "ds_read_b128 v[184:187], v123 offset:31232\n"
      "v_fmac_f32_e64 v252, v143, v143\n"
      "v_mfma_f32_16x16x32_bf16 v[200:203], v[4:7], v[192:195], v[200:203]\n"
      "ds_read_b128 v[192:195], v123 offset:34304\n"
      "v_permlane16_swap_b32 v253, v252\n"
      "v_add_f32 v253, v253, v252\n"
      "s_add_u32 s29, s8, s9\n"
      "s_add_u32 s28, s28, s29\n"
      "s_add_u32 s52, s16, s28\n"
      "s_addc_u32 s53, s17, 0\n"
      "s_cmp_lg_u32 s29, s11\n"
      "s_cbranch_scc1 L_edge_117_%=\n"
      "L_eb_118_%=:\n"
      "s_waitcnt lgkmcnt(1)\n"
      "v_mfma_f32_16x16x32_bf16 v[224:227], v[144:147], v[66:69], 0\n"
      "v_mfma_f32_16x16x32_bf16 v[224:227], v[232:235], v[66:69], v[224:227]\n"
      "s_waitcnt lgkmcnt(0)\n"
      "v_mfma_f32_16x16x32_bf16 v[196:199], v[8:11], v[184:187], v[196:199]\n"
      "v_mfma_f32_16x16x32_bf16 v[196:199], v[20:23], v[192:195], v[196:199]\n"
      "v_mfma_f32_16x16x32_bf16 v[176:179], v[12:15], v[184:187], v[176:179]\n"
      "v_mfma_f32_16x16x32_bf16 v[176:179], v[24:27], v[192:195], v[176:179]\n"
      "ds_read_b128 v[164:167], v114 offset:18432\n"
      "v_mfma_f32_16x16x32_bf16 v[200:203], v[16:19], v[184:187], v[200:203]\n"
      "ds_read_b128 v[184:187], v115 offset:18432\n"
      "v_mov_b32_dpp v224, v224 quad_perm:[0,0,0,0] row_mask:0xf bank_mask:0xf\n"
      "v_mfma_f32_16x16x32_bf16 v[200:203], v[28:31], v[192:195], v[200:203]\n"
      "s_nop 0\n"
      "v_fma_f32 v192, v196, v196, s39\n"
      "v_fmac_f32 v192, v197, v197\n"
      "v_mfma_f32_16x16x32_bf16 v[216:219], v[144:147], v[220:223], 0\n"
      "v_fmac_f32 v192, v198, v198\n"
      "v_fmac_f32 v192, v199, v199\n"
      "v_mfma_f32_16x16x32_bf16 v[216:219], v[232:235], v[50:53], v[216:219]\n"
      "v_fmac_f32 v192, v176, v176\n"
      "v_fmac_f32 v192, v177, v177\n"
      "v_mfma_f32_16x16x32_bf16 v[220:223], v[144:147], v[160:163], 0\n"
      "v_fmac_f32 v192, v178, v178\n"
      "v_fmac_f32 v192, v179, v179\n"
      "v_mfma_f32_16x16x32_bf16 v[220:223], v[232:235], v[54:57], v[220:223]\n"
      "v_fmac_f32 v192, v200, v200\n"
      "v_fmac_f32 v192, v201, v201\n"
      "v_mfma_f32_16x16x32_bf16 v[160:163], v[144:147], v[208:211], 0\n"
      "v_fmac_f32 v192, v202, v202\n"
      "v_fmac_f32 v192, v203, v203\n"
      "v_mfma_f32_16x16x32_bf16 v[160:163], v[232:235], v[58:61], v[160:163]\n"
      "v_mov_b32 v193, v192\n"
      "v_rcp_f32 v224, v224\n"
      "v_cndmask_b32 v216, v216, v220, s[42:43]\n"
      "v_permlane16_swap_b32 v192, v193\n"
      "v_add_f32 v192, v192, v193\n"
      "s_nop 1\n"
      "v_permlane32_swap_b32 v253, v192\n"
      "s_waitcnt lgkmcnt(0)\n"
      "v_mfma_f32_16x16x32_bf16 v[232:235], a[0:3], v[164:167], 0\n"
      "v_add_f32 v253, v253, v192\n"
      "v_cndmask_b32 v216, v216, v160, s[44:45]\n"
      "v_mfma_f32_16x16x32_bf16 v[232:235], a[36:39], v[184:187], v[232:235]\n"
      "ds_write_b32 v129, v253\n"
      "v_mfma_f32_16x16x32_bf16 v[192:195], a[4:7], v[164:167], 0\n"
      "v_mul_f32 v216, v224, v216\n"
      "v_cvt_pk_bf16_f32 v216, v216, v216\n"
      "v_mfma_f32_16x16x32_bf16 v[192:195], a[40:43], v[184:187], v[192:195]\n"
      "ds_write_b16 v135, v216\n"
      "s_branch L_join_32_%=\n"
      "L_flg_114_%=:\n"
      "v_cmp_gt_i32 vcc, 0, v172\n"
      "s_cbranch_vccz L_fb_115_%=\n"
      "s_sleep 1\n"
      "ds_read_b128 v[172:175], v128 offset:3072\n"
      "s_waitcnt lgkmcnt(0)\n"
      "v_bitop3_b32 v172, v172, v173, s4 bitop3:0x7e\n"
      "v_bitop3_b32 v174, v174, v175, s4 bitop3:0x7e\n"
      "v_or_b32 v172, v172, v174\n"
      "v_cmp_ne_u32 s[48:49], 0, v172\n"
      "s_branch L_flg_114_%=\n"
      "L_edge_117_%=:\n"
      "s_mov_b32 s11, s29\n"
      "s_sub_u32 s28, s60, s8\n"
      "s_sub_u32 s29, s61, s9\n"
      "s_mov_b32 s60, s8\n"
      "s_mov_b32 s61, s9\n"
      "s_mov_b64 exec, s[64:65]\n"
      "v_add_u32 v116, s28, v116\n"
      "s_mov_b64 exec, s[70:71]\n"
      "v_add_u32 v116, s29, v116\n"
      "s_mov_b64 exec, s[66:67]\n"
      "v_add_u32 v117, s28, v117\n"
      "s_mov_b64 exec, s[72:73]\n"
      "v_add_u32 v117, s29, v117\n"
      "s_mov_b64 exec, s[68:69]\n"
      "v_add_u32 v118, s28, v118\n"
      "s_mov_b64 exec, s[74:75]\n"
      "v_add_u32 v118, s29, v118\n"
      "s_mov_b64 exec, -1\n"
      "s_nop 4\n"
      "s_branch L_eb_118_%=\n"
      "L_edge_1_%=:\n"
      "s_mov_b32 s11, s29\n"
      "s_sub_u32 s28, s60, s8\n"
      "s_sub_u32 s29, s61, s9\n"
      "s_mov_b32 s60, s8\n"
      "s_mov_b32 s61, s9\n"
      "s_mov_b64 exec, s[64:65]\n"
      "v_add_u32 v116, s28, v116\n"
      "s_mov_b64 exec, s[70:71]\n"
      "v_add_u32 v116, s29, v116\n"
      "s_mov_b64 exec, s[66:67]\n"
      "v_add_u32 v117, s28, v117\n"
      "s_mov_b64 exec, s[72:73]\n"
      "v_add_u32 v117, s29, v117\n"
      "s_mov_b64 exec, s[68:69]\n"
      "v_add_u32 v118, s28, v118\n"
      "s_mov_b64 exec, s[74:75]\n"
      "v_add_u32 v118, s29, v118\n"
      "s_mov_b64 exec, -1\n"
      "s_nop 4\n"
      "s_branch L_eb_2_%=\n"
      "L_flg_3_%=:\n"
      "v_cmp_gt_i32 vcc, 0, v168\n"
      "s_cbranch_vccz L_fb_4_%=\n"
      "s_sleep 1\n"
      "ds_read_b128 v[168:171], v128 offset:2048\n"
      "s_waitcnt lgkmcnt(0)\n"
      "v_bitop3_b32 v168, v168, v169, s4 bitop3:0x7e\n"
      "v_bitop3_b32 v170, v170, v171, s4 bitop3:0x7e\n"
      "v_or_b32 v168, v168, v170\n"
      "v_cmp_ne_u32 s[48:49], 0, v168\n"
      "s_branch L_flg_3_%=\n"
      "L_edge_6_%=:\n"
      "s_mov_b32 s11, s29\n"
      "s_sub_u32 s28, s60, s8\n"
      "s_sub_u32 s29, s61, s9\n"
      "s_mov_b32 s60, s8\n"
      "s_mov_b32 s61, s9\n"
      "s_mov_b64 exec, s[64:65]\n"
      "v_add_u32 v116, s28, v116\n"
      "s_mov_b64 exec, s[70:71]\n"
      "v_add_u32 v116, s29, v116\n"
      "s_mov_b64 exec, s[66:67]\n"
      "v_add_u32 v117, s28, v117\n"
      "s_mov_b64 exec, s[72:73]\n"
      "v_add_u32 v117, s29, v117\n"
      "s_mov_b64 exec, s[68:69]\n"
      "v_add_u32 v118, s28, v118\n"
      "s_mov_b64 exec, s[74:75]\n"
      "v_add_u32 v118, s29, v118\n"
      "s_mov_b64 exec, -1\n"
      "s_nop 4\n"
      "s_branch L_eb_7_%=\n"
      "L_flg_12_%=:\n"
      "v_cmp_gt_i32 vcc, 0, v232\n"
      "s_cbranch_vccz L_fb_13_%=\n"
      "s_sleep 1\n"
      "ds_read_b128 v[232:235], v128 offset:3072\n"
      "s_waitcnt lgkmcnt(0)\n"
      "v_bitop3_b32 v232, v232, v233, s4 bitop3:0x7e\n"
      "v_bitop3_b32 v234, v234, v235, s4 bitop3:0x7e\n"
      "v_or_b32 v232, v232, v234\n"
      "v_cmp_ne_u32 s[48:49], 0, v232\n"
      "s_branch L_flg_12_%=\n"
      "L_edge_15_%=:\n"
      "s_mov_b32 s11, s29\n"
      "s_sub_u32 s28, s60, s8\n"
      "s_sub_u32 s29, s61, s9\n"
      "s_mov_b32 s60, s8\n"
      "s_mov_b32 s61, s9\n"
      "s_mov_b64 exec, s[64:65]\n"
      "v_add_u32 v116, s28, v116\n"
      "s_mov_b64 exec, s[70:71]\n"
      "v_add_u32 v116, s29, v116\n"
      "s_mov_b64 exec, s[66:67]\n"
      "v_add_u32 v117, s28, v117\n"
      "s_mov_b64 exec, s[72:73]\n"
      "v_add_u32 v117, s29, v117\n"
      "s_mov_b64 exec, s[68:69]\n"
      "v_add_u32 v118, s28, v118\n"
      "s_mov_b64 exec, s[74:75]\n"
      "v_add_u32 v118, s29, v118\n"
      "s_mov_b64 exec, -1\n"
      "s_nop 4\n"
      "s_branch L_eb_16_%=\n"
      "L_edge_17_%=:\n"
      "s_mov_b32 s11, s29\n"
      "s_sub_u32 s28, s60, s8\n"
      "s_sub_u32 s29, s61, s9\n"
      "s_mov_b32 s60, s8\n"
      "s_mov_b32 s61, s9\n"
      "s_mov_b64 exec, s[64:65]\n"
      "v_add_u32 v116, s28, v116\n"
      "s_mov_b64 exec, s[70:71]\n"
      "v_add_u32 v116, s29, v116\n"
      "s_mov_b64 exec, s[66:67]\n"
      "v_add_u32 v117, s28, v117\n"
      "s_mov_b64 exec, s[72:73]\n"
      "v_add_u32 v117, s29, v117\n"
      "s_mov_b64 exec, s[68:69]\n"
      "v_add_u32 v118, s28, v118\n"
      "s_mov_b64 exec, s[74:75]\n"
      "v_add_u32 v118, s29, v118\n"
      "s_mov_b64 exec, -1\n"
      "s_nop 4\n"
      "s_branch L_eb_18_%=\n"
      "L_flg_23_%=:\n"
      "v_cmp_gt_i32 vcc, 0, v148\n"
      "s_cbranch_vccz L_fb_24_%=\n"
      "s_sleep 1\n"
      "ds_read_b128 v[148:151], v128 offset:2048\n"
      "s_waitcnt lgkmcnt(0)\n"
      "v_bitop3_b32 v148, v148, v149, s4 bitop3:0x7e\n"
      "v_bitop3_b32 v150, v150, v151, s4 bitop3:0x7e\n"
      "v_or_b32 v148, v148, v150\n"
      "v_cmp_ne_u32 s[48:49], 0, v148\n"
      "s_branch L_flg_23_%=\n"
      "L_edge_26_%=:\n"
      "s_mov_b32 s11, s29\n"
      "s_sub_u32 s28, s60, s8\n"
      "s_sub_u32 s29, s61, s9\n"
      "s_mov_b32 s60, s8\n"
      "s_mov_b32 s61, s9\n"
      "s_mov_b64 exec, s[64:65]\n"
      "v_add_u32 v116, s28, v116\n"
      "s_mov_b64 exec, s[70:71]\n"
      "v_add_u32 v116, s29, v116\n"
      "s_mov_b64 exec, s[66:67]\n"
      "v_add_u32 v117, s28, v117\n"
      "s_mov_b64 exec, s[72:73]\n"
      "v_add_u32 v117, s29, v117\n"
      "s_mov_b64 exec, s[68:69]\n"
      "v_add_u32 v118, s28, v118\n"
      "s_mov_b64 exec, s[74:75]\n"
      "v_add_u32 v118, s29, v118\n"
      "s_mov_b64 exec, -1\n"
      "s_nop 4\n"
      "s_branch L_eb_27_%=\n"
      "L_edge_28_%=:\n"
      "s_mov_b32 s11, s29\n"
      "s_sub_u32 s28, s60, s8\n"
      "s_sub_u32 s29, s61, s9\n"
      "s_mov_b32 s60, s8\n"
      "s_mov_b32 s61, s9\n"
      "s_mov_b64 exec, s[64:65]\n"
      "v_add_u32 v116, s28, v116\n"
      "s_mov_b64 exec, s[70:71]\n"
      "v_add_u32 v116, s29, v116\n"
      "s_mov_b64 exec, s[66:67]\n"
      "v_add_u32 v117, s28, v117\n"
      "s_mov_b64 exec, s[72:73]\n"
      "v_add_u32 v117, s29, v117\n"
      "s_mov_b64 exec, s[68:69]\n"
      "v_add_u32 v118, s28, v118\n"
      "s_mov_b64 exec, s[74:75]\n"
      "v_add_u32_e64 v118, s29, v118\n"
      "s_mov_b64 exec, -1\n"
      "s_nop 4\n"
      "s_branch L_eb_29_%=\n"
      "L_flg_34_%=:\n"
      "v_cmp_gt_i32 vcc, 0, v202\n"
      "s_cbranch_vccz L_fb_35_%=\n"
      "s_sleep 1\n"
      "ds_read_b128 v[202:205], v128 offset:3072\n"
      "s_waitcnt lgkmcnt(0)\n"
      "v_bitop3_b32 v202, v202, v203, s4 bitop3:0x7e\n"
      "v_bitop3_b32 v204, v204, v205, s4 bitop3:0x7e\n"
      "v_or_b32 v202, v202, v204\n"
      "v_cmp_ne_u32 s[48:49], 0, v202\n"
      "s_branch L_flg_34_%=\n"
      "L_edge_37_%=:\n"
      "s_mov_b32 s11, s29\n"
      "s_sub_u32 s28, s60, s8\n"
      "s_sub_u32 s29, s61, s9\n"
      "s_mov_b32 s60, s8\n"
      "s_mov_b32 s61, s9\n"
      "s_mov_b64 exec, s[64:65]\n"
      "v_add_u32 v116, s28, v116\n"
      "s_mov_b64 exec, s[70:71]\n"
      "v_add_u32 v116, s29, v116\n"
      "s_mov_b64 exec, s[66:67]\n"
      "v_add_u32 v117, s28, v117\n"
      "s_mov_b64 exec, s[72:73]\n"
      "v_add_u32 v117, s29, v117\n"
      "s_mov_b64 exec, s[68:69]\n"
      "v_add_u32 v118, s28, v118\n"
      "s_mov_b64 exec, s[74:75]\n"
      "v_add_u32 v118, s29, v118\n"
      "s_mov_b64 exec, -1\n"
      "s_nop 4\n"
      "s_branch L_eb_38_%=\n"
      "L_edge_39_%=:\n"
      "s_mov_b32 s11, s29\n"
      "s_sub_u32 s28, s60, s8\n"
      "s_sub_u32 s29, s61, s9\n"
      "s_mov_b32 s60, s8\n"
      "s_mov_b32 s61, s9\n"
      "s_mov_b64 exec, s[64:65]\n"
      "v_add_u32 v116, s28, v116\n"
      "s_mov_b64 exec, s[70:71]\n"
      "v_add_u32 v116, s29, v116\n"
      "s_mov_b64 exec, s[66:67]\n"
      "v_add_u32 v117, s28, v117\n"
      "s_mov_b64 exec, s[72:73]\n"
      "v_add_u32 v117, s29, v117\n"
      "s_mov_b64 exec, s[68:69]\n"
      "v_add_u32 v118, s28, v118\n"
      "s_mov_b64 exec, s[74:75]\n"
      "v_add_u32 v118, s29, v118\n"
      "s_mov_b64 exec, -1\n"
      "s_nop 4\n"
      "s_branch L_eb_40_%=\n"
      "L_edge_41_%=:\n"
      "s_mov_b32 s11, s29\n"
      "s_sub_u32 s28, s60, s8\n"
      "s_sub_u32 s29, s61, s9\n"
      "s_mov_b32 s60, s8\n"
      "s_mov_b32 s61, s9\n"
      "s_mov_b64 exec, s[64:65]\n"
      "v_add_u32 v116, s28, v116\n"
      "s_mov_b64 exec, s[70:71]\n"
      "v_add_u32 v116, s29, v116\n"
      "s_mov_b64 exec, s[66:67]\n"
      "v_add_u32 v117, s28, v117\n"
      "s_mov_b64 exec, s[72:73]\n"
      "v_add_u32 v117, s29, v117\n"
      "s_mov_b64 exec, s[68:69]\n"
      "v_add_u32 v118, s28, v118\n"
      "s_mov_b64 exec, s[74:75]\n"
      "v_add_u32 v118, s29, v118\n"
      "s_mov_b64 exec, -1\n"
      "s_nop 4\n"
      "s_branch L_eb_42_%=\n"
      "L_edge_43_%=:\n"
      "s_mov_b32 s11, s29\n"
      "s_sub_u32 s28, s60, s8\n"
      "s_sub_u32 s29, s61, s9\n"
      "s_mov_b32 s60, s8\n"
      "s_mov_b32 s61, s9\n"
      "s_mov_b64 exec, s[64:65]\n"
      "v_add_u32 v116, s28, v116\n"
      "s_mov_b64 exec, s[70:71]\n"
      "v_add_u32 v116, s29, v116\n"
      "s_mov_b64 exec, s[66:67]\n"
      "v_add_u32 v117, s28, v117\n"
      "s_mov_b64 exec, s[72:73]\n"
      "v_add_u32 v117, s29, v117\n"
      "s_mov_b64 exec, s[68:69]\n"
      "v_add_u32 v118, s28, v118\n"
      "s_mov_b64 exec, s[74:75]\n"
      "v_add_u32 v118, s29, v118\n"
      "s_mov_b64 exec, -1\n"
      "s_nop 4\n"
      "s_branch L_eb_44_%=\n"
      "L_edge_46_%=:\n"
      "s_mov_b32 s11, s29\n"
      "s_sub_u32 s28, s60, s8\n"
      "s_sub_u32 s29, s61, s9\n"
      "s_mov_b32 s60, s8\n"
      "s_mov_b32 s61, s9\n"
      "s_mov_b64 exec, s[64:65]\n"
      "v_add_u32 v116, s28, v116\n"
      "s_mov_b64 exec, s[70:71]\n"
      "v_add_u32 v116, s29, v116\n"
      "s_mov_b64 exec, s[66:67]\n"
      "v_add_u32 v117, s28, v117\n"
      "s_mov_b64 exec, s[72:73]\n"
      "v_add_u32 v117, s29, v117\n"
      "s_mov_b64 exec, s[68:69]\n"
      "v_add_u32 v118, s28, v118\n"
      "s_mov_b64 exec, s[74:75]\n"
      "v_add_u32 v118, s29, v118\n"
      "s_mov_b64 exec, -1\n"
      "s_nop 4\n"
      "s_branch L_eb_47_%=\n"
      "L_edge_48_%=:\n"
      "s_mov_b32 s11, s29\n"
      "s_sub_u32 s28, s60, s8\n"
      "s_sub_u32 s29, s61, s9\n"
      "s_mov_b32 s60, s8\n"
      "s_mov_b32 s61, s9\n"
      "s_mov_b64 exec, s[64:65]\n"
      "v_add_u32 v116, s28, v116\n"
      "s_mov_b64 exec, s[70:71]\n"
      "v_add_u32 v116, s29, v116\n"
      "s_mov_b64 exec, s[66:67]\n"
      "v_add_u32 v117, s28, v117\n"
      "s_mov_b64 exec, s[72:73]\n"
      "v_add_u32 v117, s29, v117\n"
      "s_mov_b64 exec, s[68:69]\n"
      "v_add_u32 v118, s28, v118\n"
      "s_mov_b64 exec, s[74:75]\n"
      "v_add_u32 v118, s29, v118\n"
      "s_mov_b64 exec, -1\n"
      "s_nop 4\n"
      "s_branch L_eb_49_%=\n"
      "L_edge_50_%=:\n"
      "s_mov_b32 s11, s29\n"
      "s_sub_u32 s28, s60, s8\n"
      "s_sub_u32 s29, s61, s9\n"
      "s_mov_b32 s60, s8\n"
      "s_mov_b32 s61, s9\n"
      "s_mov_b64 exec, s[64:65]\n"
      "v_add_u32 v116, s28, v116\n"
      "s_mov_b64 exec, s[70:71]\n"
      "v_add_u32 v116, s29, v116\n"
      "s_mov_b64 exec, s[66:67]\n"
      "v_add_u32 v117, s28, v117\n"
      "s_mov_b64 exec, s[72:73]\n"
      "v_add_u32 v117, s29, v117\n"
      "s_mov_b64 exec, s[68:69]\n"
      "v_add_u32 v118, s28, v118\n"
      "s_mov_b64 exec, s[74:75]\n"
      "v_add_u32 v118, s29, v118\n"
      "s_mov_b64 exec, -1\n"
      "s_nop 4\n"
      "s_branch L_eb_51_%=\n"
      "L_cend_%=:\n"
      :
      : "s"(__builtin_amdgcn_kernarg_segment_ptr()), "s"(wave), "s"(blockIdx.x), "s"(CT_TAB), "s"(lds0)
      : "v0","v1","v2","v3","v4","v5","v6","v7","v8","v9","v10","v11","v12","v13","v14","v15","v16","v17","v18","v19","v20","v21","v22","v23","v24","v25","v26","v27","v28","v29","v30","v31","v32","v33","v34","v35","v36","v37","v38","v39","v40","v41","v42","v43","v44","v45","v46","v47","v48","v49","v50","v51","v52","v53","v54","v55","v56","v57","v58","v59","v60","v61","v62","v63","v64","v65","v66","v67","v68","v69","v70","v71","v72","v73","v74","v75","v76","v77","v78","v79","v80","v81","v82","v83","v84","v85","v86","v87","v88","v89","v90","v91","v92","v93","v94","v95","v96","v97","v98","v99","v100","v101","v102","v103","v104","v105","v106","v107","v108","v109","v110","v111","v112","v113","v114","v115","v116","v117","v118","v119","v120","v121","v122","v123","v124","v125","v126","v127","v128","v129","v130","v131","v132","v133","v134","v135","v136","v137","v138","v139","v140","v141","v142","v143","v144","v145","v146","v147","v148","v149","v150","v151","v152","v153","v154","v155","v156","v157","v158","v159","v160","v161","v162","v163","v164","v165","v166","v167","v168","v169","v170","v171","v172","v173","v174","v175","v176","v177","v178","v179","v180","v181","v182","v183","v184","v185","v186","v187","v188","v189","v190","v191","v192","v193","v194","v195","v196","v197","v198","v199","v200","v201","v202","v203","v204","v205","v206","v207","v208","v209","v210","v211","v212","v213","v214","v215","v216","v217","v218","v219","v220","v221","v222","v223","v224","v225","v226","v227","v228","v229","v230","v231","v232","v233","v234","v235","v236","v237","v238","v239","v240","v241","v242","v243","v244","v245","v246","v247","v248","v249","v250","v251","v252","v253","v254","v255","a0","a1","a2","a3","a4","a5","a6","a7","a8","a9","a10","a11","a12","a13","a14","a15","a16","a17","a18","a19","a20","a21","a22","a23","a24","a25","a26","a27","a28","a29","a30","a31","a32","a33","a34","a35","a36","a37","a38","a39","a40","a41","a42","a43","a44","a45","a46","a47","a48","a49","a50","a51","a52","a53","a54","a55","a56","a57","a58","a59","a60","a61","a62","a63","a64","a65","a66","a67","a68","a69","a70","a71","a72","a73","a74","a75","a76","a77","a78","a79","a80","a81","a82","a83","a84","a85","a86","a87","a88","a89","a90","a91","a92","a93","a94","a95","a96","a97","a98","a99","a100","a101","a102","a103","a104","a105","a106","a107","a108","a109","a110","a111","a112","a113","a114","a115","a116","a117","a118","a119","a120","a121","a122","a123","a124","a125","a126","a127","a128","a129","a130","a131","a132","a133","a134","a135","a136","a137","a138","a139","a140","a141","a142","a143","a144","a145","a146","a147","a148","a149","a150","a151","a152","a153","a154","a155","a156","a157","a158","a159","a160","a161","a162","a163","a164","a165","a166","a167","a168","a169","a170","a171","a172","a173","a174","a175","a176","a177","a178","a179","a180","a181","a182","a183","a184","a185","a186","a187","a188","a189","a190","a191","a192","a193","a194","a195","a196","a197","a198","a199","a200","a201","a202","a203","a204","a205","a206","a207","a208","a209","a210","a211","a212","a213","a214","a215","a216","a217","a218","a219","a220","a221","a222","a223","a224","a225","a226","a227","a228","a229","a230","a231","a232","a233","a234","a235","a236","a237","a238","a239","a240","a241","a242","a243","a244","a245","a246","a247","a248","a249","a250","a251","a252","a253","a254","a255","s2","s3","s4","s5","s6","s7","s8","s9","s10","s11","s12","s13","s16","s17","s18","s19","s20","s21","s22","s23","s24","s25","s26","s27","s28","s29","s30","s31","s34","s35","s36","s37","s38","s39","s40","s41","s42","s43","s44","s45","s46","s47","s48","s49","s50","s51","s52","s53","s54","s55","s60","s61","s62","s63","s64","s65","s66","s67","s68","s69","s70","s71","s72","s73","s74","s75","s76","s77","s78","s79","s80","s81","s82","s83","s84","s85","s86","s87","s88","s89","s90","s91","s92","s93","s94","s95","s96","s97","s98","s99","s100", "vcc", "scc", "memory");
}

}

extern "C" void kernel_launch(void* const* d_in, const int* in_sizes, int n_in, void* d_out, int out_size, void* d_ws,
                              size_t ws_size, hipStream_t stream) {
  const uint16_t* x = (const uint16_t*)d_in[0];
  const uint16_t* wqkv = (const uint16_t*)d_in[1];
  const uint16_t* wo = (const uint16_t*)d_in[2];
  const uint16_t* ls = (const uint16_t*)d_in[3];
  const uint16_t* w1 = (const uint16_t*)d_in[4];
  const uint16_t* w2 = (const uint16_t*)d_in[5];
  const uint16_t* rc = (const uint16_t*)d_in[6];
  const int* ridx = (const int*)d_in[7];
  const uint16_t* mask = (const uint16_t*)d_in[8];
  swin::swin_main<<<256, 256, 0, stream>>>(x, mask, (char*)d_ws, (uint16_t*)d_out, wqkv, wo, ls, w1, w2, rc, ridx);
}
```

```cpp
#include <hip/hip_runtime.h>
#include <hip/hip_bf16.h>
#include <stdint.h>

namespace swin {

constexpr unsigned LDS_BYTES = 163840u;

__device__ __attribute__((aligned(256))) const unsigned CT_TAB[26704] = {0,64,0,0xc0000,144,208,16,0xc0010,288,352,32,0xc0020,432,496,48,0xc0030,576,512,64,0xc0040,720,656,80,0xc0050,864,800,96,0xc0060,1008,944,112,0xc0070,1024,1088,400,0xc0190,1168,1232,384,0xc0180,1312,1376,432,0xc01b0,1456,1520,416,0xc01a0,1600,1536,464,0xc01d0,1744,1680,448,0xc01c0,1888,1824,496,0xc01f0,2032,1968,480,0xc01e0,16,80,800,0xc0320,128,192,816,0xc0330,304,368,768,0xc0300,416,480,784,0xc0310,592,528,864,0xc0360,704,640,880,0xc0370,880,816,832,0xc0340,992,928,848,0xc0350,1040,1104,1200,0xef8b0,1152,1216,1184,0xef8a0,1328,1392,1168,0xef890,1440,1504,1152,0xef880,1616,1552,1264,0xef8f0,1728,1664,1248,0xef8e0,1904,1840,1232,0xef8d0,2016,1952,1216,0xef8c0,32,96,1600,0xefa40,176,240,1616,0xefa50,256,320,1632,0xefa60,400,464,1648,0xefa70,608,544,1536,0xefa00,752,688,1552,0xefa10,832,768,1568,0xefa20,976,912,1584,0xefa30,1056,1120,2000,0xefbd0,1200,1264,1984,0xefbc0,1280,1344,2032,0xefbf0,1424,1488,2016,0xefbe0,1632,1568,1936,0xefb90,1776,1712,1920,0xefb80,1856,1792,1968,0xefbb0,2000,1936,1952,0xefba0,48,112,2400,0xefd60,160,224,2416,0xefd70,272,336,2368,0xefd40,384,448,2384,0xefd50,624,560,2336,0xefd20,736,672,2352,0xefd30,848,784,2304,0xefd00,960,896,2320,0xefd10,1072,1136,0x2fef0,0xefef0,1184,1248,0x2fee0,0xefee0,1296,1360,0x2fed0,0xefed0,1408,1472,0x2fec0,0xefec0,1648,1584,0x2feb0,0xefeb0,1760,1696,0x2fea0,0xefea0,1872,1808,0x2fe90,0xefe90,1984,1920,0x2fe80,0xefe80,0xc0100,73728,77824,90112,0xc0110,73744,77840,90128,0xc0120,73760,77856,90144,0xc0130,73776,77872,90160,0xc0140,73792,77888,90176,0xc0150,73808,77904,90192,0xc0160,73824,77920,90208,0xc0170,73840,77936,90224,0xc0290,73856,77952,90240,0xc0280,73872,77968,90256,0xc02b0,73888,77984,90272,0xc02a0,73904,78000,90288,0xc02d0,73920,78016,90304,0xc02c0,73936,78032,90320,0xc02f0,73952,78048,90336,0xc02e0,73968,78064,90352,0xc0420,73984,78080,90368,0xc0430,74000,78096,90384,0xc0400,74016,78112,90400,0xc0410,74032,78128,90416,0xc0460,74048,78144,90432,0xc0470,74064,78160,90448,0xc0440,74080,78176,90464,0xc0450,74096,78192,90480,0xef9b0,74112,78208,90496,0xef9a0,74128,78224,90512,0xef990,74144,78240,90528,0xef980,74160,78256,90544,0xef9f0,74176,78272,90560,0xef9e0,74192,78288,90576,0xef9d0,74208,78304,90592,0xef9c0,74224,78320,90608,0xc0100,74240,78336,90624,0xc0110,74256,78352,90640,0xc0120,74272,78368,90656,0xc0130,74288,78384,90672,0xc0140,74304,78400,90688,0xc0150,74320,78416,90704,0xc0160,74336,78432,90720,0xc0170,74352,78448,90736,0xc0290,74368,78464,90752,0xc0280,74384,78480,90768,0xc02b0,74400,78496,90784,0xc02a0,74416,78512,90800,0xc02d0,74432,78528,90816,0xc02c0,74448,78544,90832,0xc02f0,74464,78560,90848,0xc02e0,74480,78576,90864,0xc0420,74496,78592,90880,0xc0430,74512,78608,90896,0xc0400,74528,78624,90912,0xc0410,74544,78640,90928,0xc0460,74560,78656,90944,0xc0470,74576,78672,90960,0xc0440,74592,78688,90976,0xc0450,74608,78704,90992,0xef9b0,74624,78720,91008,0xef9a0,74640,78736,91024,0xef990,74656,78752,91040,0xef980,74672,78768,91056,0xef9f0,74688,78784,91072,0xef9e0,74704,78800,91088,0xef9d0,74720,78816,91104,0xef9c0,74736,78832,91120,0x1c000,36864,36864,49152,0x1c004,36880,36880,49168,0x1c008,36896,36896,49184,0x1c00c,36912,36912,49200,0x1c010,36928,36928,49216,0x1c014,36944,36944,49232,0x1c018,36960,36960,49248,0x1c01c,36976,36976,49264,0x1c020,36992,36992,49280,0x1c024,37008,37008,49296,0x1c028,37024,37024,49312,0x1c02c,37040,37040,49328,0x1c030,37056,37056,49344,0x1c034,37072,37072,49360,0x1c038,37088,37088,49376,0x1c03c,37104,37104,49392,0x1c040,37632,37632,49920,0x1c044,37648,37648,49936,0x1c048,37664,37664,49952,0x1c04c,37680,37680,49968,0x1c050,37696,37696,49984,0x1c054,37712,37712,50000,0x1c058,37728,37728,50016,0x1c05c,37744,37744,50032,0x1c060,37760,37760,50048,0x1c064,37776,37776,50064,0x1c068,37792,37792,50080,0x1c06c,37808,37808,50096,0x1c070,37824,37824,50112,0x1c074,37840,37840,50128,0x1c078,37856,37856,50144,0x1c07c,37872,37872,50160,0x1c080,38400,38400,50688,0x1c084,38416,38416,50704,0x1c088,38432,38432,50720,0x1c08c,38448,38448,50736,0x1c090,38464,38464,50752,0x1c094,38480,38480,50768,0x1c098,38496,38496,50784,0x1c09c,38512,38512,50800,0x1c0a0,38528,38528,50816,0x1c0a4,38544,38544,50832,0x1c0a8,38560,38560,50848,0x1c0ac,38576,38576,50864,0x1c0b0,38592,38592,50880,0x1c0b4,38608,38608,50896,0x1c0b8,38624,38624,50912,0x1c0bc,38640,38640,50928,0x1c0c0,39168,39168,51456,0x1c0c4,39184,39184,51472,0x1c0c8,39200,39200,51488,0x1c0cc,39216,39216,51504,0x1c0d0,39232,39232,51520,0x1c0d4,39248,39248,51536,0x1c0d8,39264,39264,51552,0x1c0dc,39280,39280,51568,0x1c0e0,39296,39296,51584,0x1c0e4,39312,39312,51600,0x1c0e8,39328,39328,51616,0x1c0ec,39344,39344,51632,0x1c0f0,39360,39360,51648,0x1c0f4,39376,39376,51664,0x1c0f8,39392,39392,51680,0x1c0fc,39408,39408,51696,73728,73792,0x1d600,0x1d600,73872,73936,0x1d610,0x1d610,74016,74080,0x1d620,0x1d620,74160,74224,0x1d630,0x1d630,74304,74240,0x1d640,0x1d640,74448,74384,0x1d650,0x1d650,74592,74528,0x1d660,0x1d660,74736,74672,0x1d670,0x1d670,74752,74816,0x1d680,0x1d680,74896,74960,0x1d690,0x1d690,75040,75104,0x1d6a0,0x1d6a0,75184,75248,0x1d6b0,0x1d6b0,75328,75264,0x1d6c0,0x1d6c0,75472,75408,0x1d6d0,0x1d6d0,75616,75552,0x1d6e0,0x1d6e0,75760,75696,0x1d6f0,0x1d6f0,73744,73800,0x1d700,0x1d700,73856,73944,0x1d710,0x1d710,74032,74088,0x1d720,0x1d720,74144,74232,0x1d730,0x1d730,74320,74248,0x1d740,0x1d740,74432,74392,0x1d750,0x1d750,74608,74536,0x1d760,0x1d760,74720,74680,0x1d770,0x1d770,74768,74824,0x1d780,0x1d780,74880,74968,0x1d790,0x1d790,75056,75112,0x1d7a0,0x1d7a0,75168,75256,0x1d7b0,0x1d7b0,75344,75272,0x1d7c0,0x1d7c0,75456,75416,0x1d7d0,0x1d7d0,75632,75560,0x1d7e0,0x1d7e0,75744,75704,0x1d7f0,0x1d7f0,73760,73808,0x1d800,0x1d800,73904,73920,0x1d810,0x1d810,73984,74096,0x1d820,0x1d820,74128,74208,0x1d830,0x1d830,74336,74256,0x1d840,0x1d840,74480,74368,0x1d850,0x1d850,74560,74544,0x1d860,0x1d860,74704,74656,0x1d870,0x1d870,74784,74832,0x1d880,0x1d880,74928,74944,0x1d890,0x1d890,75008,75120,0x1d8a0,0x1d8a0,75152,75232,0x1d8b0,0x1d8b0,75360,75280,0x1d8c0,0x1d8c0,75504,75392,0x1d8d0,0x1d8d0,75584,75568,0x1d8e0,0x1d8e0,75728,75680,0x1d8f0,0x1d8f0,73776,73816,0x1d900,0x1d900,73888,73928,0x1d910,0x1d910,74000,74104,0x1d920,0x1d920,74112,74216,0x1d930,0x1d930,74352,74264,0x1d940,0x1d940,74464,74376,0x1d950,0x1d950,74576,74552,0x1d960,0x1d960,74688,74664,0x1d970,0x1d970,74800,74840,0x1d980,0x1d980,74912,74952,0x1d990,0x1d990,75024,75128,0x1d9a0,0x1d9a0,75136,75240,0x1d9b0,0x1d9b0,75376,75288,0x1d9c0,0x1d9c0,75488,75400,0x1d9d0,0x1d9d0,75600,75576,0x1d9e0,0x1d9e0,75712,75688,0x1d9f0,0x1d9f0,0x1e600,0x1e600,0x1e800,0x100000,0x1e602,0x100000,0x100000,0x1e800,0x1e604,0x100000,0x100000,0x100000,0x1e606,0x100000,0x100000,0x100000,0x1e610,0x1e600,0x100000,0x100000,0x1e612,0x100000,0x100000,0x1e800,0x1e614,0x100000,0x100000,0x100000,0x1e616,0x100000,0x100000,0x100000,0x1e620,0x1e600,0x100000,0x100000,0x1e622,0x100000,0x100000,0x1e800,0x1e624,0x100000,0x100000,0x100000,0x1e626,0x100000,0x100000,0x100000,0x1e630,0x1e600,0x100000,0x100000,0x1e632,0x100000,0x100000,0x1e800,0x1e634,0x100000,0x100000,0x100000,0x1e636,0x100000,0x100000,0x100000,0x1e608,0x1e610,0x1e830,0x100000,0x1e60a,0x100000,0x100000,0x1e830,0x1e60c,0x100000,0x100000,0x100000,0x1e60e,0x100000,0x100000,0x100000,0x1e618,0x1e610,0x100000,0x100000,0x1e61a,0x100000,0x100000,0x1e830,0x1e61c,0x100000,0x100000,0x100000,0x1e61e,0x100000,0x100000,0x100000,0x1e628,0x1e610,0x100000,0x100000,0x1e62a,0x100000,0x100000,0x1e830,0x1e62c,0x100000,0x100000,0x100000,0x1e62e,0x100000,0x100000,0x100000,0x1e638,0x1e610,0x100000,0x100000,0x1e63a,0x100000,0x100000,0x1e830,0x1e63c,0x100000,0x100000,0x100000,0x1e63e,0x100000,0x100000,0x100000,0x1e640,0x1e620,0x1e860,0x100000,0x1e642,0x100000,0x100000,0x1e860,0x1e644,0x100000,0x100000,0x100000,0x1e646,0x100000,0x100000,0x100000,0x1e650,0x1e620,0x100000,0x100000,0x1e652,0x100000,0x100000,0x1e860,0x1e654,0x100000,0x100000,0x100000,0x1e656,0x100000,0x100000,0x100000,0x1e660,0x1e620,0x100000,0x100000,0x1e662,0x100000,0x100000,0x1e860,0x1e664,0x100000,0x100000,0x100000,0x1e666,0x100000,0x100000,0x100000,0x1e670,0x1e620,0x100000,0x100000,0x1e672,0x100000,0x100000,0x1e860,0x1e674,0x100000,0x100000,0x100000,0x1e676,0x100000,0x100000,0x100000,0x1e648,0x1e630,0x1e890,0x100000,0x1e64a,0x100000,0x100000,0x1e890,0x1e64c,0x100000,0x100000,0x100000,0x1e64e,0x100000,0x100000,0x100000,0x1e658,0x1e630,0x100000,0x100000,0x1e65a,0x100000,0x100000,0x1e890,0x1e65c,0x100000,0x100000,0x100000,0x1e65e,0x100000,0x100000,0x100000,0x1e668,0x1e630,0x100000,0x100000,0x1e66a,0x100000,0x100000,0x1e890,0x1e66c,0x100000,0x100000,0x100000,0x1e66e,0x100000,0x100000,0x100000,0x1e678,0x1e630,0x100000,0x100000,0x1e67a,0x100000,0x100000,0x1e890,0x1e67c,0x100000,0x100000,0x100000,0x1e67e,0x100000,0x100000,0x100000,0x1eb00,0x24c70,36864,36928,0x1eb02,0x24c72,36880,36936,0x1eb04,0x24c74,36896,36944,0x1eb06,0x24c76,36912,36952,0x1eb08,0x24dd0,37248,37312,0x1eb0a,0x24dd2,37264,37320,0x1eb0c,0x24dd4,37280,37328,0x1eb0e,0x24dd6,37296,37336,0x1eb10,0x24f30,37664,37728,0x1eb12,0x24f32,37680,37736,0x1eb14,0x24f34,37632,37744,0x1eb16,0x24f36,37648,37752,0x1eb18,0x25090,38048,38112,0x1eb1a,0x25092,38064,38120,0x1eb1c,0x25094,38016,38128,0x1eb1e,0x25096,38032,38136,0x1eb00,0x24c78,38464,38400,0x1eb02,0x24c7a,38480,38408,0x1eb04,0x24c7c,38496,38416,0x1eb06,0x24c7e,38512,38424,0x1eb08,0x24dd8,38848,38784,0x1eb0a,0x24dda,38864,38792,0x1eb0c,0x24ddc,38880,38800,0x1eb0e,0x24dde,38896,38808,0x1eb10,0x24f38,39264,39200,0x1eb12,0x24f3a,39280,39208,0x1eb14,0x24f3c,39232,39216,0x1eb16,0x24f3e,39248,39224,0x1eb18,0x25098,39648,39584,0x1eb1a,0x2509a,39664,39592,0x1eb1c,0x2509c,39616,39600,0x1eb1e,0x2509e,39632,39608,0x1eb00,0x26270,39936,40000,0x1eb02,0x26272,39952,40008,0x1eb04,0x26274,39968,40016,0x1eb06,0x26276,39984,40024,0x1eb08,0x263d0,40320,40384,0x1eb0a,0x263d2,40336,40392,0x1eb0c,0x263d4,40352,40400,0x1eb0e,0x263d6,40368,40408,0x1eb10,0x26530,40736,40800,0x1eb12,0x26532,40752,40808,0x1eb14,0x26534,40704,40816,0x1eb16,0x26536,40720,40824,0x1eb18,0x26690,41120,41184,0x1eb1a,0x26692,41136,41192,0x1eb1c,0x26694,41088,41200,0x1eb1e,0x26696,41104,41208,0x1eb00,0x100000,41536,41472,0x1eb02,0x100000,41552,41480,0x1eb04,0x100000,41568,41488,0x1eb06,0x100000,41584,41496,0x1eb08,0x100000,41920,41856,0x1eb0a,0x100000,41936,41864,0x1eb0c,0x100000,41952,41872,0x1eb0e,0x100000,41968,41880,0x1eb10,0x100000,42336,42272,0x1eb12,0x100000,42352,42280,0x1eb14,0x100000,42304,42288,0x1eb16,0x100000,42320,42296,0x1eb18,0x100000,42720,42656,0x1eb1a,0x100000,42736,42664,0x1eb1c,0x100000,42688,42672,0x1eb1e,0x100000,42704,42680,36864,36896,36928,36864,36872,36904,36936,36880,36880,36912,36944,36896,36888,36920,36952,36912,38016,38048,38080,36928,38024,38056,38088,36944,38032,38064,38096,36960,38040,38072,38104,36976,39200,39168,39264,36992,39208,39176,39272,37008,39216,39184,39280,37024,39224,39192,39288,37040,40352,40320,40416,37056,40360,40328,40424,37072,40368,40336,40432,37088,40376,40344,40440,37104,46144,46176,46080,37120,46152,46184,46088,37136,46160,46192,46096,37152,46168,46200,46104,37168,47296,47328,47232,37184,47304,47336,47240,37200,47312,47344,47248,37216,47320,47352,47256,37232,48480,48448,48416,37248,48488,48456,48424,37264,48496,48464,48432,37280,48504,48472,48440,37296,49632,49600,49568,37312,49640,49608,49576,37328,49648,49616,49584,37344,49656,49624,49592,37360,55296,55328,55360,37376,55304,55336,55368,37392,55312,55344,55376,37408,55320,55352,55384,37424,56448,56480,56512,37440,56456,56488,56520,37456,56464,56496,56528,37472,56472,56504,56536,37488,57632,57600,57696,37504,57640,57608,57704,37520,57648,57616,57712,37536,57656,57624,57720,37552,58784,58752,58848,37568,58792,58760,58856,37584,58800,58768,58864,37600,58808,58776,58872,37616,64576,64608,64512,37632,64584,64616,64520,37648,64592,64624,64528,37664,64600,64632,64536,37680,65728,65760,65664,37696,65736,65768,65672,37712,65744,65776,65680,37728,65752,65784,65688,37744,66912,66880,66848,37760,66920,66888,66856,37776,66928,66896,66864,37792,66936,66904,66872,37808,68064,68032,68000,37824,68072,68040,68008,37840,68080,68048,68016,37856,68088,68056,68024,37872,0x1e800,0x1e800,0x20cc0,0,0x1e8c4,0x1e8c4,0x20cc4,16,0x1e988,0x1e988,0x20cc8,32,0x1ea4c,0x1ea4c,0x20ccc,48,0x1eb10,0x1eb10,0x20cd0,64,0x1ebd4,0x1ebd4,0x20cd4,80,0x1ec98,0x1ec98,0x20cd8,96,0x1ed5c,0x1ed5c,0x20cdc,112,0x1ee20,0x1ee20,0x20ce0,384,0x1eee4,0x1eee4,0x20ce4,400,0x1efa8,0x1efa8,0x20ce8,416,0x1f06c,0x1f06c,0x20cec,432,0x1f130,0x1f130,0x20cf0,448,0x1f1f4,0x1f1f4,0x20cf4,464,0x1f2b8,0x1f2b8,0x20cf8,480,0x1f37c,0x1f37c,0x20cfc,496,0x1e810,0x1e800,0x20cc0,768,0x1e8d4,0x1e8c4,0x20cc4,784,0x1e998,0x1e988,0x20cc8,800,0x1ea5c,0x1ea4c,0x20ccc,816,0x1eb20,0x1eb10,0x20cd0,832,0x1ebe4,0x1ebd4,0x20cd4,848,0x1eca8,0x1ec98,0x20cd8,864,0x1ed6c,0x1ed5c,0x20cdc,880,0x1ee30,0x1ee20,0x20ce0,1152,0x1eef4,0x1eee4,0x20ce4,1168,0x1efb8,0x1efa8,0x20ce8,1184,0x1f07c,0x1f06c,0x20cec,1200,0x1f140,0x1f130,0x20cf0,1216,0x1f204,0x1f1f4,0x20cf4,1232,0x1f2c8,0x1f2b8,0x20cf8,1248,0x1f38c,0x1f37c,0x20cfc,1264,0x1e820,0x1e800,0x20cc0,1536,0x1e8e4,0x1e8c4,0x20cc4,1552,0x1e9a8,0x1e988,0x20cc8,1568,0x1ea6c,0x1ea4c,0x20ccc,1584,0x1eb30,0x1eb10,0x20cd0,1600,0x1ebf4,0x1ebd4,0x20cd4,1616,0x1ecb8,0x1ec98,0x20cd8,1632,0x1ed7c,0x1ed5c,0x20cdc,1648,0x1ee40,0x1ee20,0x20ce0,1920,0x1ef04,0x1eee4,0x20ce4,1936,0x1efc8,0x1efa8,0x20ce8,1952,0x1f08c,0x1f06c,0x20cec,1968,0x1f150,0x1f130,0x20cf0,1984,0x1f214,0x1f1f4,0x20cf4,2000,0x1f2d8,0x1f2b8,0x20cf8,2016,0x1f39c,0x1f37c,0x20cfc,2032,0x1e830,0x1e800,0x20cc0,2304,0x1e8f4,0x1e8c4,0x20cc4,2320,0x1e9b8,0x1e988,0x20cc8,2336,0x1ea7c,0x1ea4c,0x20ccc,2352,0x1eb40,0x1eb10,0x20cd0,2368,0x1ec04,0x1ebd4,0x20cd4,2384,0x1ecc8,0x1ec98,0x20cd8,2400,0x1ed8c,0x1ed5c,0x20cdc,2416,0x1ee50,0x1ee20,0x20ce0,2688,0x1ef14,0x1eee4,0x20ce4,2704,0x1efd8,0x1efa8,0x20ce8,2720,0x1f09c,0x1f06c,0x20cec,2736,0x1f160,0x1f130,0x20cf0,2752,0x1f224,0x1f1f4,0x20cf4,2768,0x1f2e8,0x1f2b8,0x20cf8,2784,0x1f3ac,0x1f37c,0x20cfc,2800,36864,0x26d70,0x1e800,0x1eb00,36880,0x26d74,0x1ec90,0x1ef90,36896,0x26d78,0x1f120,0x1f420,36912,0x26d7c,0x1f5b0,0x1f8b0,36928,0x26d80,0x1fa40,0x1fd40,36944,0x26d84,0x1fed0,0x201d0,36960,0x26d88,0x20360,0x20660,36976,0x26d8c,0x207f0,0x20af0,37248,0x26d90,0x20c80,0x20f80,37264,0x26d94,0x21110,0x21410,37280,0x26d98,0x215a0,0x218a0,37296,0x26d9c,0x21a30,0x21d30,37312,0x26da0,0x21ec0,0x221c0,37328,0x26da4,0x22350,0x22650,37344,0x26da8,0x227e0,0x22ae0,37360,0x26dac,0x22c70,0x22f70,37664,0x26db0,0x1e830,0x1eb08,37680,0x26db4,0x1ecc0,0x1ef98,37632,0x26db8,0x1f150,0x1f428,37648,0x26dbc,0x1f5e0,0x1f8b8,37728,0x26dc0,0x1fa70,0x1fd48,37744,0x26dc4,0x1ff00,0x201d8,37696,0x26dc8,0x20390,0x20668,37712,0x26dcc,0x20820,0x20af8,38048,0x26dd0,0x20cb0,0x20f88,38064,0x26dd4,0x21140,0x21418,38016,0x26dd8,0x215d0,0x218a8,38032,0x26ddc,0x21a60,0x21d38,38112,0x26de0,0x21ef0,0x221c8,38128,0x26de4,0x22380,0x22658,38080,0x26de8,0x22810,0x22ae8,38096,0x26dec,0x22ca0,0x22f78,38464,0x26df0,0x1e860,0x1eb10,38480,0x26df4,0x1ecf0,0x1efa0,38496,0x26df8,0x1f180,0x1f430,38512,0x26dfc,0x1f610,0x1f8c0,38400,0x26e00,0x1faa0,0x1fd50,38416,0x26e04,0x1ff30,0x201e0,38432,0x26e08,0x203c0,0x20670,38448,0x26e0c,0x20850,0x20b00,38848,0x26e10,0x20ce0,0x20f90,38864,0x26e14,0x21170,0x21420,38880,0x26e18,0x21600,0x218b0,38896,0x26e1c,0x21a90,0x21d40,38784,0x26e20,0x21f20,0x221d0,38800,0x26e24,0x223b0,0x22660,38816,0x26e28,0x22840,0x22af0,38832,0x26e2c,0x22cd0,0x22f80,39264,0x26e30,0x1e890,0x1eb18,39280,0x26e34,0x1ed20,0x1efa8,39232,0x26e38,0x1f1b0,0x1f438,39248,0x26e3c,0x1f640,0x1f8c8,39200,0x26e40,0x1fad0,0x1fd58,39216,0x26e44,0x1ff60,0x201e8,39168,0x26e48,0x203f0,0x20678,39184,0x26e4c,0x20880,0x20b08,39648,0x26e50,0x20d10,0x20f98,39664,0x26e54,0x211a0,0x21428,39616,0x26e58,0x21630,0x218b8,39632,0x26e5c,0x21ac0,0x21d48,39584,0x26e60,0x21f50,0x221d8,39600,0x26e64,0x223e0,0x22668,39552,0x26e68,0x22870,0x22af8,39568,0x26e6c,0x22d00,0x22f88,0x3f803f80,0x3f803f80,16256,0,0,0x3f803f80,0x3f800000,0,0,0x3f803f80,0,16256,0,0x3f803f80,0,0x3f800000,0x3f803f80,0,0,0,0,0,0,0,0,0,0,0,0,0,0,0,0x3f803f80,0,0,0,0,0,0,0,0,0,0,0,0,0,0,0,0x3f803f80,0,0,0,0,0,0,0,0,0,0,0,0,0,0,0,0x3f803f80,0,0,0,0,0,0,0,0,0,0,0,0,0,0,0,0x3f803f80,0x3f803f80,16256,0,0,0x3f803f80,0x3f800000,0,0,0x3f803f80,0,16256,0,0x3f803f80,0,0x3f800000,0x3f803f80,0,0,0,0,0,0,0,0,0,0,0,0,0,0,0,0x3f803f80,0,0,0,0,0,0,0,0,0,0,0,0,0,0,0,0x3f803f80,0,0,0,0,0,0,0,0,0,0,0,0,0,0,0,0x3f803f80,0,0,0,0,0,0,0,0,0,0,0,0,0,0,0,0x3f803f80,0x3f803f80,16256,0,0,0x3f803f80,0x3f800000,0,0,0x3f803f80,0,16256,0,0x3f803f80,0,0x3f800000,0x3f803f80,0,0,0,0,0,0,0,0,0,0,0,0,0,0,0,0x3f803f80,0,0,0,0,0,0,0,0,0,0,0,0,0,0,0,0x3f803f80,0,0,0,0,0,0,0,0,0,0,0,0,0,0,0,0x3f803f80,0,0,0,0,0,0,0,0,0,0,0,0,0,0,0,0x3f803f80,0x3f803f80,16256,0,0,0x3f803f80,0x3f800000,0,0,0x3f803f80,0,16256,0,0x3f803f80,0,0x3f800000,0,64,0x30000,0xf0000,144,208,0x30010,0xf0010,288,352,0x30020,0xf0020,432,496,0x30030,0xf0030,576,512,0x30040,0xf0040,720,656,0x30050,0xf0050,864,800,0x30060,0xf0060,1008,944,0x30070,0xf0070,1024,1088,0x30190,0xf0190,1168,1232,0x30180,0xf0180,1312,1376,0x301b0,0xf01b0,1456,1520,0x301a0,0xf01a0,1600,1536,0x301d0,0xf01d0,1744,1680,0x301c0,0xf01c0,1888,1824,0x301f0,0xf01f0,2032,1968,0x301e0,0xf01e0,16,80,0x30320,0x11f720,128,192,0x30330,0x11f730,304,368,0x30300,0x11f700,416,480,0x30310,0x11f710,592,528,0x30360,0x11f760,704,640,0x30370,0x11f770,880,816,0x30340,0x11f740,992,928,0x30350,0x11f750,1040,1104,0x304b0,0x11f8b0,1152,1216,0x304a0,0x11f8a0,1328,1392,0x30490,0x11f890,1440,1504,0x30480,0x11f880,1616,1552,0x304f0,0x11f8f0,1728,1664,0x304e0,0x11f8e0,1904,1840,0x304d0,0x11f8d0,2016,1952,0x304c0,0x11f8c0,32,96,0x30640,0x11fa40,176,240,0x30650,0x11fa50,256,320,0x30660,0x11fa60,400,464,0x30670,0x11fa70,608,544,0x30600,0x11fa00,752,688,0x30610,0x11fa10,832,768,0x30620,0x11fa20,976,912,0x30630,0x11fa30,1056,1120,0x307d0,0x11fbd0,1200,1264,0x307c0,0x11fbc0,1280,1344,0x307f0,0x11fbf0,1424,1488,0x307e0,0x11fbe0,1632,1568,0x30790,0x11fb90,1776,1712,0x30780,0x11fb80,1856,1792,0x307b0,0x11fbb0,2000,1936,0x307a0,0x11fba0,48,112,0x5fd60,0x11fd60,160,224,0x5fd70,0x11fd70,272,336,0x5fd40,0x11fd40,384,448,0x5fd50,0x11fd50,624,560,0x5fd20,0x11fd20,736,672,0x5fd30,0x11fd30,848,784,0x5fd00,0x11fd00,960,896,0x5fd10,0x11fd10,1072,1136,0x5fef0,0x11fef0,1184,1248,0x5fee0,0x11fee0,1296,1360,0x5fed0,0x11fed0,1408,1472,0x5fec0,0x11fec0,1648,1584,0x5feb0,0x11feb0,1760,1696,0x5fea0,0x11fea0,1872,1808,0x5fe90,0x11fe90,1984,1920,0x5fe80,0x11fe80,0xefb40,74752,78848,90624,0xefb50,74768,78864,90640,0xefb60,74784,78880,90656,0xefb70,74800,78896,90672,0xefb00,74816,78912,90688,0xefb10,74832,78928,90704,0xefb20,74848,78944,90720,0xefb30,74864,78960,90736,0xefcd0,74880,78976,90752,0xefcc0,74896,78992,90768,0xefcf0,74912,79008,90784,0xefce0,74928,79024,90800,0xefc90,74944,79040,90816,0xefc80,74960,79056,90832,0xefcb0,74976,79072,90848,0xefca0,74992,79088,90864,0xefe60,75008,79104,90880,0xefe70,75024,79120,90896,0xefe40,75040,79136,90912,0xefe50,75056,79152,90928,0xefe20,75072,79168,90944,0xefe30,75088,79184,90960,0xefe00,75104,79200,90976,0xefe10,75120,79216,90992,0xefff0,75136,79232,91008,0xeffe0,75152,79248,91024,0xeffd0,75168,79264,91040,0xeffc0,75184,79280,91056,0xeffb0,75200,79296,91072,0xeffa0,75216,79312,91088,0xeff90,75232,79328,91104,0xeff80,75248,79344,91120,0xefb40,75264,79360,91136,0xefb50,75280,79376,91152,0xefb60,75296,79392,91168,0xefb70,75312,79408,91184,0xefb00,75328,79424,91200,0xefb10,75344,79440,91216,0xefb20,75360,79456,91232,0xefb30,75376,79472,91248,0xefcd0,75392,79488,91264,0xefcc0,75408,79504,91280,0xefcf0,75424,79520,91296,0xefce0,75440,79536,91312,0xefc90,75456,79552,91328,0xefc80,75472,79568,91344,0xefcb0,75488,79584,91360,0xefca0,75504,79600,91376,0xefe60,75520,79616,91392,0xefe70,75536,79632,91408,0xefe40,75552,79648,91424,0xefe50,75568,79664,91440,0xefe20,75584,79680,91456,0xefe30,75600,79696,91472,0xefe00,75616,79712,91488,0xefe10,75632,79728,91504,0xefff0,75648,79744,91520,0xeffe0,75664,79760,91536,0xeffd0,75680,79776,91552,0xeffc0,75696,79792,91568,0xeffb0,75712,79808,91584,0xeffa0,75728,79824,91600,0xeff90,75744,79840,91616,0xeff80,75760,79856,91632,0x1c0a0,36864,39936,49160,0x1c0a4,36880,39952,49176,0x1c0a8,36896,39968,49192,0x1c0ac,36912,39984,49208,0x1c0b0,36928,40000,49224,0x1c0b4,36944,40016,49240,0x1c0b8,36960,40032,49256,0x1c0bc,36976,40048,49272,0x1c0c0,36992,40064,49288,0x1c0c4,37008,40080,49304,0x1c0c8,37024,40096,49320,0x1c0cc,37040,40112,49336,0x1c0d0,37056,40128,49352,0x1c0d4,37072,40144,49368,0x1c0d8,37088,40160,49384,0x1c0dc,37104,40176,49400,0x1c0e0,37632,40704,49928,0x1c0e4,37648,40720,49944,0x1c0e8,37664,40736,49960,0x1c0ec,37680,40752,49976,0x1c0f0,37696,40768,49992,0x1c0f4,37712,40784,50008,0x1c0f8,37728,40800,50024,0x1c0fc,37744,40816,50040,0x1c100,37760,40832,50056,0x1c104,37776,40848,50072,0x1c108,37792,40864,50088,0x1c10c,37808,40880,50104,0x1c110,37824,40896,50120,0x1c114,37840,40912,50136,0x1c118,37856,40928,50152,0x1c11c,37872,40944,50168,0x1c120,38400,41472,50696,0x1c124,38416,41488,50712,0x1c128,38432,41504,50728,0x1c12c,38448,41520,50744,0x1c130,38464,41536,50760,0x1c134,38480,41552,50776,0x1c138,38496,41568,50792,0x1c13c,38512,41584,50808,0x1c140,38528,41600,50824,0x1c144,38544,41616,50840,0x1c148,38560,41632,50856,0x1c14c,38576,41648,50872,0x1c150,38592,41664,50888,0x1c154,38608,41680,50904,0x1c158,38624,41696,50920,0x1c15c,38640,41712,50936,0x1c160,39168,42240,51464,0x1c164,39184,42256,51480,0x1c168,39200,42272,51496,0x1c16c,39216,42288,51512,0x1c170,39232,42304,51528,0x1c174,39248,42320,51544,0x1c178,39264,42336,51560,0x1c17c,39280,42352,51576,0x1c180,39296,42368,51592,0x1c184,39312,42384,51608,0x1c188,39328,42400,51624,0x1c18c,39344,42416,51640,0x1c190,39360,42432,51656,0x1c194,39376,42448,51672,0x1c198,39392,42464,51688,0x1c19c,39408,42480,51704,73824,79904,0x1d600,0x1d604,73968,80048,0x1d610,0x1d614,74048,80128,0x1d620,0x1d624,74192,80272,0x1d630,0x1d634,74272,80480,0x1d640,0x1d644,74416,80624,0x1d650,0x1d654,74496,80704,0x1d660,0x1d664,74640,80848,0x1d670,0x1d674,74848,80928,0x1d680,0x1d684,74992,81072,0x1d690,0x1d694,75072,81152,0x1d6a0,0x1d6a4,75216,81296,0x1d6b0,0x1d6b4,75296,81504,0x1d6c0,0x1d6c4,75440,81648,0x1d6d0,0x1d6d4,75520,81728,0x1d6e0,0x1d6e4,75664,81872,0x1d6f0,0x1d6f4,73840,79912,0x1d700,0x1d704,73952,80056,0x1d710,0x1d714,74064,80136,0x1d720,0x1d724,74176,80280,0x1d730,0x1d734,74288,80488,0x1d740,0x1d744,74400,80632,0x1d750,0x1d754,74512,80712,0x1d760,0x1d764,74624,80856,0x1d770,0x1d774,74864,80936,0x1d780,0x1d784,74976,81080,0x1d790,0x1d794,75088,81160,0x1d7a0,0x1d7a4,75200,81304,0x1d7b0,0x1d7b4,75312,81512,0x1d7c0,0x1d7c4,75424,81656,0x1d7d0,0x1d7d4,75536,81736,0x1d7e0,0x1d7e4,75648,81880,0x1d7f0,0x1d7f4,79872,79920,0x1d800,0x1d804,80016,80032,0x1d810,0x1d814,80160,80144,0x1d820,0x1d824,80304,80256,0x1d830,0x1d834,80448,80496,0x1d840,0x1d844,80592,80608,0x1d850,0x1d854,80736,80720,0x1d860,0x1d864,80880,80832,0x1d870,0x1d874,80896,80944,0x1d880,0x1d884,81040,81056,0x1d890,0x1d894,81184,81168,0x1d8a0,0x1d8a4,81328,81280,0x1d8b0,0x1d8b4,81472,81520,0x1d8c0,0x1d8c4,81616,81632,0x1d8d0,0x1d8d4,81760,81744,0x1d8e0,0x1d8e4,81904,81856,0x1d8f0,0x1d8f4,79888,79928,0x1d900,0x1d904,80000,80040,0x1d910,0x1d914,80176,80152,0x1d920,0x1d924,80288,80264,0x1d930,0x1d934,80464,80504,0x1d940,0x1d944,80576,80616,0x1d950,0x1d954,80752,80728,0x1d960,0x1d964,80864,80840,0x1d970,0x1d974,80912,80952,0x1d980,0x1d984,81024,81064,0x1d990,0x1d994,81200,81176,0x1d9a0,0x1d9a4,81312,81288,0x1d9b0,0x1d9b4,81488,81528,0x1d9c0,0x1d9c4,81600,81640,0x1d9d0,0x1d9d4,81776,81752,0x1d9e0,0x1d9e4,81888,81864,0x1d9f0,0x1d9f4,0x1e680,0x1e680,0x1e8c0,0x100000,0x1e682,0x100000,0x100000,0x1e8c0,0x1e684,0x100000,0x100000,0x100000,0x1e686,0x100000,0x100000,0x100000,0x1e690,0x1e680,0x100000,0x100000,0x1e692,0x100000,0x100000,0x1e8c0,0x1e694,0x100000,0x100000,0x100000,0x1e696,0x100000,0x100000,0x100000,0x1e6a0,0x1e680,0x100000,0x100000,0x1e6a2,0x100000,0x100000,0x1e8c0,0x1e6a4,0x100000,0x100000,0x100000,0x1e6a6,0x100000,0x100000,0x100000,0x1e6b0,0x1e680,0x100000,0x100000,0x1e6b2,0x100000,0x100000,0x1e8c0,0x1e6b4,0x100000,0x100000,0x100000,0x1e6b6,0x100000,0x100000,0x100000,0x1e688,0x1e690,0x1e8f0,0x100000,0x1e68a,0x100000,0x100000,0x1e8f0,0x1e68c,0x100000,0x100000,0x100000,0x1e68e,0x100000,0x100000,0x100000,0x1e698,0x1e690,0x100000,0x100000,0x1e69a,0x100000,0x100000,0x1e8f0,0x1e69c,0x100000,0x100000,0x100000,0x1e69e,0x100000,0x100000,0x100000,0x1e6a8,0x1e690,0x100000,0x100000,0x1e6aa,0x100000,0x100000,0x1e8f0,0x1e6ac,0x100000,0x100000,0x100000,0x1e6ae,0x100000,0x100000,0x100000,0x1e6b8,0x1e690,0x100000,0x100000,0x1e6ba,0x100000,0x100000,0x1e8f0,0x1e6bc,0x100000,0x100000,0x100000,0x1e6be,0x100000,0x100000,0x100000,0x1e6c0,0x1e6a0,0x1e920,0x100000,0x1e6c2,0x100000,0x100000,0x1e920,0x1e6c4,0x100000,0x100000,0x100000,0x1e6c6,0x100000,0x100000,0x100000,0x1e6d0,0x1e6a0,0x100000,0x100000,0x1e6d2,0x100000,0x100000,0x1e920,0x1e6d4,0x100000,0x100000,0x100000,0x1e6d6,0x100000,0x100000,0x100000,0x1e6e0,0x1e6a0,0x100000,0x100000,0x1e6e2,0x100000,0x100000,0x1e920,0x1e6e4,0x100000,0x100000,0x100000,0x1e6e6,0x100000,0x100000,0x100000,0x1e6f0,0x1e6a0,0x100000,0x100000,0x1e6f2,0x100000,0x100000,0x1e920,0x1e6f4,0x100000,0x100000,0x100000,0x1e6f6,0x100000,0x100000,0x100000,0x1e6c8,0x1e6b0,0x1e950,0x100000,0x1e6ca,0x100000,0x100000,0x1e950,0x1e6cc,0x100000,0x100000,0x100000,0x1e6ce,0x100000,0x100000,0x100000,0x1e6d8,0x1e6b0,0x100000,0x100000,0x1e6da,0x100000,0x100000,0x1e950,0x1e6dc,0x100000,0x100000,0x100000,0x1e6de,0x100000,0x100000,0x100000,0x1e6e8,0x1e6b0,0x100000,0x100000,0x1e6ea,0x100000,0x100000,0x1e950,0x1e6ec,0x100000,0x100000,0x100000,0x1e6ee,0x100000,0x100000,0x100000,0x1e6f8,0x1e6b0,0x100000,0x100000,0x1e6fa,0x100000,0x100000,0x1e950,0x1e6fc,0x100000,0x100000,0x100000,0x1e6fe,0x100000,0x100000,0x100000,0x1eb60,0x251f0,36960,37024,0x1eb62,0x251f2,36976,37032,0x1eb64,0x251f4,36992,37040,0x1eb66,0x251f6,37008,37048,0x1eb68,0x25350,37344,37408,0x1eb6a,0x25352,37360,37416,0x1eb6c,0x25354,37376,37424,0x1eb6e,0x25356,37392,37432,0x1eb70,0x254b0,37696,37760,0x1eb72,0x254b2,37712,37768,0x1eb74,0x254b4,37792,37776,0x1eb76,0x254b6,37808,37784,0x1eb78,0x25610,38080,38144,0x1eb7a,0x25612,38096,38152,0x1eb7c,0x25614,38176,38160,0x1eb7e,0x25616,38192,38168,0x1eb60,0x251f8,38432,38624,0x1eb62,0x251fa,38448,38632,0x1eb64,0x251fc,38592,38640,0x1eb66,0x251fe,38608,38648,0x1eb68,0x25358,38816,39008,0x1eb6a,0x2535a,38832,39016,0x1eb6c,0x2535c,38976,39024,0x1eb6e,0x2535e,38992,39032,0x1eb70,0x254b8,39168,39360,0x1eb72,0x254ba,39184,39368,0x1eb74,0x254bc,39392,39376,0x1eb76,0x254be,39408,39384,0x1eb78,0x25618,39552,39744,0x1eb7a,0x2561a,39568,39752,0x1eb7c,0x2561c,39776,39760,0x1eb7e,0x2561e,39792,39768,0x1eb60,0x26278,40032,40096,0x1eb62,0x2627a,40048,40104,0x1eb64,0x2627c,40064,40112,0x1eb66,0x2627e,40080,40120,0x1eb68,0x263d8,40416,40480,0x1eb6a,0x263da,40432,40488,0x1eb6c,0x263dc,40448,40496,0x1eb6e,0x263de,40464,40504,0x1eb70,0x26538,40768,40832,0x1eb72,0x2653a,40784,40840,0x1eb74,0x2653c,40864,40848,0x1eb76,0x2653e,40880,40856,0x1eb78,0x26698,41152,41216,0x1eb7a,0x2669a,41168,41224,0x1eb7c,0x2669c,41248,41232,0x1eb7e,0x2669e,41264,41240,0x1eb60,0x100000,41504,41696,0x1eb62,0x100000,41520,41704,0x1eb64,0x100000,41664,41712,0x1eb66,0x100000,41680,41720,0x1eb68,0x100000,41888,42080,0x1eb6a,0x100000,41904,42088,0x1eb6c,0x100000,42048,42096,0x1eb6e,0x100000,42064,42104,0x1eb70,0x100000,42240,42432,0x1eb72,0x100000,42256,42440,0x1eb74,0x100000,42464,42448,0x1eb76,0x100000,42480,42456,0x1eb78,0x100000,42624,42816,0x1eb7a,0x100000,42640,42824,0x1eb7c,0x100000,42848,42832,0x1eb7e,0x100000,42864,42840,36960,36992,37024,46080,36968,37000,37032,46096,36976,37008,37040,46112,36984,37016,37048,46128,38112,38144,38176,46144,38120,38152,38184,46160,38128,38160,38192,46176,38136,38168,38200,46192,39232,39328,39296,46208,39240,39336,39304,46224,39248,39344,39312,46240,39256,39352,39320,46256,40384,40480,40448,46272,40392,40488,40456,46288,40400,40496,40464,46304,40408,40504,40472,46320,46112,46272,46304,46336,46120,46280,46312,46352,46128,46288,46320,46368,46136,46296,46328,46384,47264,47424,47456,46400,47272,47432,47464,46416,47280,47440,47472,46432,47288,47448,47480,46448,48384,48608,48576,46464,48392,48616,48584,46480,48400,48624,48592,46496,48408,48632,48600,46512,49536,49760,49728,46528,49544,49768,49736,46544,49552,49776,49744,46560,49560,49784,49752,46576,55392,55424,55456,46592,55400,55432,55464,46608,55408,55440,55472,46624,55416,55448,55480,46640,56544,56576,56608,46656,56552,56584,56616,46672,56560,56592,56624,46688,56568,56600,56632,46704,57664,57760,57728,46720,57672,57768,57736,46736,57680,57776,57744,46752,57688,57784,57752,46768,58816,58912,58880,46784,58824,58920,58888,46800,58832,58928,58896,46816,58840,58936,58904,46832,64544,64704,64736,46848,64552,64712,64744,46864,64560,64720,64752,46880,64568,64728,64760,46896,65696,65856,65888,46912,65704,65864,65896,46928,65712,65872,65904,46944,65720,65880,65912,46960,66816,67040,67008,46976,66824,67048,67016,46992,66832,67056,67024,47008,66840,67064,67032,47024,67968,68192,68160,47040,67976,68200,68168,47056,67984,68208,68176,47072,67992,68216,68184,47088,0x1e800,0x1e800,0x20cc0,18432,0x1e8c4,0x1e8c4,0x20cc4,18448,0x1e988,0x1e988,0x20cc8,18464,0x1ea4c,0x1ea4c,0x20ccc,18480,0x1eb10,0x1eb10,0x20cd0,18496,0x1ebd4,0x1ebd4,0x20cd4,18512,0x1ec98,0x1ec98,0x20cd8,18528,0x1ed5c,0x1ed5c,0x20cdc,18544,0x1ee20,0x1ee20,0x20ce0,18816,0x1eee4,0x1eee4,0x20ce4,18832,0x1efa8,0x1efa8,0x20ce8,18848,0x1f06c,0x1f06c,0x20cec,18864,0x1f130,0x1f130,0x20cf0,18880,0x1f1f4,0x1f1f4,0x20cf4,18896,0x1f2b8,0x1f2b8,0x20cf8,18912,0x1f37c,0x1f37c,0x20cfc,18928,0x1e810,0x1e800,0x20cc0,19200,0x1e8d4,0x1e8c4,0x20cc4,19216,0x1e998,0x1e988,0x20cc8,19232,0x1ea5c,0x1ea4c,0x20ccc,19248,0x1eb20,0x1eb10,0x20cd0,19264,0x1ebe4,0x1ebd4,0x20cd4,19280,0x1eca8,0x1ec98,0x20cd8,19296,0x1ed6c,0x1ed5c,0x20cdc,19312,0x1ee30,0x1ee20,0x20ce0,19584,0x1eef4,0x1eee4,0x20ce4,19600,0x1efb8,0x1efa8,0x20ce8,19616,0x1f07c,0x1f06c,0x20cec,19632,0x1f140,0x1f130,0x20cf0,19648,0x1f204,0x1f1f4,0x20cf4,19664,0x1f2c8,0x1f2b8,0x20cf8,19680,0x1f38c,0x1f37c,0x20cfc,19696,0x1e820,0x1e800,0x20cc0,19968,0x1e8e4,0x1e8c4,0x20cc4,19984,0x1e9a8,0x1e988,0x20cc8,20000,0x1ea6c,0x1ea4c,0x20ccc,20016,0x1eb30,0x1eb10,0x20cd0,20032,0x1ebf4,0x1ebd4,0x20cd4,20048,0x1ecb8,0x1ec98,0x20cd8,20064,0x1ed7c,0x1ed5c,0x20cdc,20080,0x1ee40,0x1ee20,0x20ce0,20352,0x1ef04,0x1eee4,0x20ce4,20368,0x1efc8,0x1efa8,0x20ce8,20384,0x1f08c,0x1f06c,0x20cec,20400,0x1f150,0x1f130,0x20cf0,20416,0x1f214,0x1f1f4,0x20cf4,20432,0x1f2d8,0x1f2b8,0x20cf8,20448,0x1f39c,0x1f37c,0x20cfc,20464,0x1e830,0x1e800,0x20cc0,20736,0x1e8f4,0x1e8c4,0x20cc4,20752,0x1e9b8,0x1e988,0x20cc8,20768,0x1ea7c,0x1ea4c,0x20ccc,20784,0x1eb40,0x1eb10,0x20cd0,20800,0x1ec04,0x1ebd4,0x20cd4,20816,0x1ecc8,0x1ec98,0x20cd8,20832,0x1ed8c,0x1ed5c,0x20cdc,20848,0x1ee50,0x1ee20,0x20ce0,21120,0x1ef14,0x1eee4,0x20ce4,21136,0x1efd8,0x1efa8,0x20ce8,21152,0x1f09c,0x1f06c,0x20cec,21168,0x1f160,0x1f130,0x20cf0,21184,0x1f224,0x1f1f4,0x20cf4,21200,0x1f2e8,0x1f2b8,0x20cf8,21216,0x1f3ac,0x1f37c,0x20cfc,21232,55296,0x26e70,0x1e8c0,0x1eb60,55312,0x26e74,0x1ed50,0x1eff0,55328,0x26e78,0x1f1e0,0x1f480,55344,0x26e7c,0x1f670,0x1f910,55360,0x26e80,0x1fb00,0x1fda0,55376,0x26e84,0x1ff90,0x20230,55392,0x26e88,0x20420,0x206c0,55408,0x26e8c,0x208b0,0x20b50,55680,0x26e90,0x20d40,0x20fe0,55696,0x26e94,0x211d0,0x21470,55712,0x26e98,0x21660,0x21900,55728,0x26e9c,0x21af0,0x21d90,55744,0x26ea0,0x21f80,0x22220,55760,0x26ea4,0x22410,0x226b0,55776,0x26ea8,0x228a0,0x22b40,55792,0x26eac,0x22d30,0x22fd0,56096,0x26eb0,0x1e8f0,0x1eb68,56112,0x26eb4,0x1ed80,0x1eff8,56064,0x26eb8,0x1f210,0x1f488,56080,0x26ebc,0x1f6a0,0x1f918,56160,0x26ec0,0x1fb30,0x1fda8,56176,0x26ec4,0x1ffc0,0x20238,56128,0x26ec8,0x20450,0x206c8,56144,0x26ecc,0x208e0,0x20b58,56480,0x26ed0,0x20d70,0x20fe8,56496,0x26ed4,0x21200,0x21478,56448,0x26ed8,0x21690,0x21908,56464,0x26edc,0x21b20,0x21d98,56544,0x26ee0,0x21fb0,0x22228,56560,0x26ee4,0x22440,0x226b8,56512,0x26ee8,0x228d0,0x22b48,56528,0x26eec,0x22d60,0x22fd8,56896,0x26ef0,0x1e920,0x1eb70,56912,0x26ef4,0x1edb0,0x1f000,56928,0x26ef8,0x1f240,0x1f490,56944,0x26efc,0x1f6d0,0x1f920,56832,0x26f00,0x1fb60,0x1fdb0,56848,0x26f04,0x1fff0,0x20240,56864,0x26f08,0x20480,0x206d0,56880,0x26f0c,0x20910,0x20b60,57280,0x26f10,0x20da0,0x20ff0,57296,0x26f14,0x21230,0x21480,57312,0x26f18,0x216c0,0x21910,57328,0x26f1c,0x21b50,0x21da0,57216,0x26f20,0x21fe0,0x22230,57232,0x26f24,0x22470,0x226c0,57248,0x26f28,0x22900,0x22b50,57264,0x26f2c,0x22d90,0x22fe0,57696,0x26f30,0x1e950,0x1eb78,57712,0x26f34,0x1ede0,0x1f008,57664,0x26f38,0x1f270,0x1f498,57680,0x26f3c,0x1f700,0x1f928,57632,0x26f40,0x1fb90,0x1fdb8,57648,0x26f44,0x20020,0x20248,57600,0x26f48,0x204b0,0x206d8,57616,0x26f4c,0x20940,0x20b68,58080,0x26f50,0x20dd0,0x20ff8,58096,0x26f54,0x21260,0x21488,58048,0x26f58,0x216f0,0x21918,58064,0x26f5c,0x21b80,0x21da8,58016,0x26f60,0x22010,0x22238,58032,0x26f64,0x224a0,0x226c8,57984,0x26f68,0x22930,0x22b58,58000,0x26f6c,0x22dc0,0x22fe8,0x3f803f80,0x3f803f80,16256,0,0,0x3f803f80,0x3f800000,0,0,0x3f803f80,0,16256,0,0x3f803f80,0,0x3f800000,0x3f803f80,0,0,0,0,0,0,0,0,0,0,0,0,0,0,0,0x3f803f80,0,0,0,0,0,0,0,0,0,0,0,0,0,0,0,0x3f803f80,0,0,0,0,0,0,0,0,0,0,0,0,0,0,0,0x3f803f80,0,0,0,0,0,0,0,0,0,0,0,0,0,0,0,0x3f803f80,0x3f803f80,16256,0,0,0x3f803f80,0x3f800000,0,0,0x3f803f80,0,16256,0,0x3f803f80,0,0x3f800000,0x3f803f80,0,0,0,0,0,0,0,0,0,0,0,0,0,0,0,0x3f803f80,0,0,0,0,0,0,0,0,0,0,0,0,0,0,0,0x3f803f80,0,0,0,0,0,0,0,0,0,0,0,0,0,0,0,0x3f803f80,0,0,0,0,0,0,0,0,0,0,0,0,0,0,0,0x3f803f80,0x3f803f80,16256,0,0,0x3f803f80,0x3f800000,0,0,0x3f803f80,0,16256,0,0x3f803f80,0,0x3f800000,0x3f803f80,0,0,0,0,0,0,0,0,0,0,0,0,0,0,0,0x3f803f80,0,0,0,0,0,0,0,0,0,0,0,0,0,0,0,0x3f803f80,0,0,0,0,0,0,0,0,0,0,0,0,0,0,0,0x3f803f80,0,0,0,0,0,0,0,0,0,0,0,0,0,0,0,0x3f803f80,0x3f803f80,16256,0,0,0x3f803f80,0x3f800000,0,0,0x3f803f80,0,16256,0,0x3f803f80,0,0x3f800000,0,64,0x60000,0xc0080,144,208,0x60010,0xc0090,288,352,0x60020,0xc00a0,432,496,0x60030,0xc00b0,576,512,0x60040,0xc00c0,720,656,0x60050,0xc00d0,864,800,0x60060,0xc00e0,1008,944,0x60070,0xc00f0,1024,1088,0x60190,0xc0210,1168,1232,0x60180,0xc0200,1312,1376,0x601b0,0xc0230,1456,1520,0x601a0,0xc0220,1600,1536,0x601d0,0xc0250,1744,1680,0x601c0,0xc0240,1888,1824,0x601f0,0xc0270,2032,1968,0x601e0,0xc0260,16,80,0x60320,0xc03a0,128,192,0x60330,0xc03b0,304,368,0x60300,0xc0380,416,480,0x60310,0xc0390,592,528,0x60360,0xc03e0,704,640,0x60370,0xc03f0,880,816,0x60340,0xc03c0,992,928,0x60350,0xc03d0,1040,1104,0x604b0,0xef930,1152,1216,0x604a0,0xef920,1328,1392,0x60490,0xef910,1440,1504,0x60480,0xef900,1616,1552,0x604f0,0xef970,1728,1664,0x604e0,0xef960,1904,1840,0x604d0,0xef950,2016,1952,0x604c0,0xef940,32,96,0x60640,0xefac0,176,240,0x60650,0xefad0,256,320,0x60660,0xefae0,400,464,0x60670,0xefaf0,608,544,0x60600,0xefa80,752,688,0x60610,0xefa90,832,768,0x60620,0xefaa0,976,912,0x60630,0xefab0,1056,1120,0x8fbd0,0xefc50,1200,1264,0x8fbc0,0xefc40,1280,1344,0x8fbf0,0xefc70,1424,1488,0x8fbe0,0xefc60,1632,1568,0x8fb90,0xefc10,1776,1712,0x8fb80,0xefc00,1856,1792,0x8fbb0,0xefc30,2000,1936,0x8fba0,0xefc20,48,112,0x8fd60,0xefde0,160,224,0x8fd70,0xefdf0,272,336,0x8fd40,0xefdc0,384,448,0x8fd50,0xefdd0,624,560,0x8fd20,0xefda0,736,672,0x8fd30,0xefdb0,848,784,0x8fd00,0xefd80,960,896,0x8fd10,0xefd90,1072,1136,0x8fef0,0xeff70,1184,1248,0x8fee0,0xeff60,1296,1360,0x8fed0,0xeff50,1408,1472,0x8fec0,0xeff40,1648,1584,0x8feb0,0xeff30,1760,1696,0x8fea0,0xeff20,1872,1808,0x8fe90,0xeff10,1984,1920,0x8fe80,0xeff00,0xf0100,75776,83968,91136,0xf0110,75792,83984,91152,0xf0120,75808,84000,91168,0xf0130,75824,84016,91184,0xf0140,75840,84032,91200,0xf0150,75856,84048,91216,0xf0160,75872,84064,91232,0xf0170,75888,84080,91248,0xf0290,75904,84096,91264,0xf0280,75920,84112,91280,0xf02b0,75936,84128,91296,0xf02a0,75952,84144,91312,0xf02d0,75968,84160,91328,0xf02c0,75984,84176,91344,0xf02f0,76000,84192,91360,0xf02e0,76016,84208,91376,0x11f820,76032,84224,91392,0x11f830,76048,84240,91408,0x11f800,76064,84256,91424,0x11f810,76080,84272,91440,0x11f860,76096,84288,91456,0x11f870,76112,84304,91472,0x11f840,76128,84320,91488,0x11f850,76144,84336,91504,0x11f9b0,76160,84352,91520,0x11f9a0,76176,84368,91536,0x11f990,76192,84384,91552,0x11f980,76208,84400,91568,0x11f9f0,76224,84416,91584,0x11f9e0,76240,84432,91600,0x11f9d0,76256,84448,91616,0x11f9c0,76272,84464,91632,0xf0100,76288,84480,91648,0xf0110,76304,84496,91664,0xf0120,76320,84512,91680,0xf0130,76336,84528,91696,0xf0140,76352,84544,91712,0xf0150,76368,84560,91728,0xf0160,76384,84576,91744,0xf0170,76400,84592,91760,0xf0290,76416,84608,91776,0xf0280,76432,84624,91792,0xf02b0,76448,84640,91808,0xf02a0,76464,84656,91824,0xf02d0,76480,84672,91840,0xf02c0,76496,84688,91856,0xf02f0,76512,84704,91872,0xf02e0,76528,84720,91888,0x11f820,76544,84736,91904,0x11f830,76560,84752,91920,0x11f800,76576,84768,91936,0x11f810,76592,84784,91952,0x11f860,76608,84800,91968,0x11f870,76624,84816,91984,0x11f840,76640,84832,92000,0x11f850,76656,84848,92016,0x11f9b0,76672,84864,92032,0x11f9a0,76688,84880,92048,0x11f990,76704,84896,92064,0x11f980,76720,84912,92080,0x11f9f0,76736,84928,92096,0x11f9e0,76752,84944,92112,0x11f9d0,76768,84960,92128,0x11f9c0,76784,84976,92144,0x1c140,36864,43008,52224,0x1c144,36880,43024,52240,0x1c148,36896,43040,52256,0x1c14c,36912,43056,52272,0x1c150,36928,43072,52288,0x1c154,36944,43088,52304,0x1c158,36960,43104,52320,0x1c15c,36976,43120,52336,0x1c160,36992,43136,52352,0x1c164,37008,43152,52368,0x1c168,37024,43168,52384,0x1c16c,37040,43184,52400,0x1c170,37056,43200,52416,0x1c174,37072,43216,52432,0x1c178,37088,43232,52448,0x1c17c,37104,43248,52464,0x1c180,37632,43776,52992,0x1c184,37648,43792,53008,0x1c188,37664,43808,53024,0x1c18c,37680,43824,53040,0x1c190,37696,43840,53056,0x1c194,37712,43856,53072,0x1c198,37728,43872,53088,0x1c19c,37744,43888,53104,0x1c1a0,37760,43904,53120,0x1c1a4,37776,43920,53136,0x1c1a8,37792,43936,53152,0x1c1ac,37808,43952,53168,0x1c1b0,37824,43968,53184,0x1c1b4,37840,43984,53200,0x1c1b8,37856,44000,53216,0x1c1bc,37872,44016,53232,0x1c1c0,38400,44544,53760,0x1c1c4,38416,44560,53776,0x1c1c8,38432,44576,53792,0x1c1cc,38448,44592,53808,0x1c1d0,38464,44608,53824,0x1c1d4,38480,44624,53840,0x1c1d8,38496,44640,53856,0x1c1dc,38512,44656,53872,0x1c1e0,38528,44672,53888,0x1c1e4,38544,44688,53904,0x1c1e8,38560,44704,53920,0x1c1ec,38576,44720,53936,0x1c1f0,38592,44736,53952,0x1c1f4,38608,44752,53968,0x1c1f8,38624,44768,53984,0x1c1fc,38640,44784,54000,0x1c200,39168,45312,54528,0x1c204,39184,45328,54544,0x1c208,39200,45344,54560,0x1c20c,39216,45360,54576,0x1c210,39232,45376,54592,0x1c214,39248,45392,54608,0x1c218,39264,45408,54624,0x1c21c,39280,45424,54640,0x1c220,39296,45440,54656,0x1c224,39312,45456,54672,0x1c228,39328,45472,54688,0x1c22c,39344,45488,54704,0x1c230,39360,45504,54720,0x1c234,39376,45520,54736,0x1c238,39392,45536,54752,0x1c23c,39408,45552,54768,79936,86016,0x1d600,0x1d608,80080,86160,0x1d610,0x1d618,80224,86304,0x1d620,0x1d628,80368,86448,0x1d630,0x1d638,80384,86592,0x1d640,0x1d648,80528,86736,0x1d650,0x1d658,80672,86880,0x1d660,0x1d668,80816,87024,0x1d670,0x1d678,80960,87040,0x1d680,0x1d688,81104,87184,0x1d690,0x1d698,81248,87328,0x1d6a0,0x1d6a8,81392,87472,0x1d6b0,0x1d6b8,81408,87616,0x1d6c0,0x1d6c8,81552,87760,0x1d6d0,0x1d6d8,81696,87904,0x1d6e0,0x1d6e8,81840,88048,0x1d6f0,0x1d6f8,79952,86024,0x1d700,0x1d708,80064,86168,0x1d710,0x1d718,80240,86312,0x1d720,0x1d728,80352,86456,0x1d730,0x1d738,80400,86600,0x1d740,0x1d748,80512,86744,0x1d750,0x1d758,80688,86888,0x1d760,0x1d768,80800,87032,0x1d770,0x1d778,80976,87048,0x1d780,0x1d788,81088,87192,0x1d790,0x1d798,81264,87336,0x1d7a0,0x1d7a8,81376,87480,0x1d7b0,0x1d7b8,81424,87624,0x1d7c0,0x1d7c8,81536,87768,0x1d7d0,0x1d7d8,81712,87912,0x1d7e0,0x1d7e8,81824,88056,0x1d7f0,0x1d7f8,79968,86032,0x1d800,0x1d808,80112,86144,0x1d810,0x1d818,80192,86320,0x1d820,0x1d828,80336,86432,0x1d830,0x1d838,80416,86608,0x1d840,0x1d848,80560,86720,0x1d850,0x1d858,80640,86896,0x1d860,0x1d868,80784,87008,0x1d870,0x1d878,80992,87056,0x1d880,0x1d888,81136,87168,0x1d890,0x1d898,81216,87344,0x1d8a0,0x1d8a8,81360,87456,0x1d8b0,0x1d8b8,81440,87632,0x1d8c0,0x1d8c8,81584,87744,0x1d8d0,0x1d8d8,81664,87920,0x1d8e0,0x1d8e8,81808,88032,0x1d8f0,0x1d8f8,79984,86040,0x1d900,0x1d908,80096,86152,0x1d910,0x1d918,80208,86328,0x1d920,0x1d928,80320,86440,0x1d930,0x1d938,80432,86616,0x1d940,0x1d948,80544,86728,0x1d950,0x1d958,80656,86904,0x1d960,0x1d968,80768,87016,0x1d970,0x1d978,81008,87064,0x1d980,0x1d988,81120,87176,0x1d990,0x1d998,81232,87352,0x1d9a0,0x1d9a8,81344,87464,0x1d9b0,0x1d9b8,81456,87640,0x1d9c0,0x1d9c8,81568,87752,0x1d9d0,0x1d9d8,81680,87928,0x1d9e0,0x1d9e8,81792,88040,0x1d9f0,0x1d9f8,0x1e700,0x1e700,0x1e980,0x100000,0x1e702,0x100000,0x100000,0x1e980,0x1e704,0x100000,0x100000,0x100000,0x1e706,0x100000,0x100000,0x100000,0x1e710,0x1e700,0x100000,0x100000,0x1e712,0x100000,0x100000,0x1e980,0x1e714,0x100000,0x100000,0x100000,0x1e716,0x100000,0x100000,0x100000,0x1e720,0x1e700,0x100000,0x100000,0x1e722,0x100000,0x100000,0x1e980,0x1e724,0x100000,0x100000,0x100000,0x1e726,0x100000,0x100000,0x100000,0x1e730,0x1e700,0x100000,0x100000,0x1e732,0x100000,0x100000,0x1e980,0x1e734,0x100000,0x100000,0x100000,0x1e736,0x100000,0x100000,0x100000,0x1e708,0x1e710,0x1e9b0,0x100000,0x1e70a,0x100000,0x100000,0x1e9b0,0x1e70c,0x100000,0x100000,0x100000,0x1e70e,0x100000,0x100000,0x100000,0x1e718,0x1e710,0x100000,0x100000,0x1e71a,0x100000,0x100000,0x1e9b0,0x1e71c,0x100000,0x100000,0x100000,0x1e71e,0x100000,0x100000,0x100000,0x1e728,0x1e710,0x100000,0x100000,0x1e72a,0x100000,0x100000,0x1e9b0,0x1e72c,0x100000,0x100000,0x100000,0x1e72e,0x100000,0x100000,0x100000,0x1e738,0x1e710,0x100000,0x100000,0x1e73a,0x100000,0x100000,0x1e9b0,0x1e73c,0x100000,0x100000,0x100000,0x1e73e,0x100000,0x100000,0x100000,0x1e740,0x1e720,0x1e9e0,0x100000,0x1e742,0x100000,0x100000,0x1e9e0,0x1e744,0x100000,0x100000,0x100000,0x1e746,0x100000,0x100000,0x100000,0x1e750,0x1e720,0x100000,0x100000,0x1e752,0x100000,0x100000,0x1e9e0,0x1e754,0x100000,0x100000,0x100000,0x1e756,0x100000,0x100000,0x100000,0x1e760,0x1e720,0x100000,0x100000,0x1e762,0x100000,0x100000,0x1e9e0,0x1e764,0x100000,0x100000,0x100000,0x1e766,0x100000,0x100000,0x100000,0x1e770,0x1e720,0x100000,0x100000,0x1e772,0x100000,0x100000,0x1e9e0,0x1e774,0x100000,0x100000,0x100000,0x1e776,0x100000,0x100000,0x100000,0x1e748,0x1e730,0x1ea10,0x100000,0x1e74a,0x100000,0x100000,0x1ea10,0x1e74c,0x100000,0x100000,0x100000,0x1e74e,0x100000,0x100000,0x100000,0x1e758,0x1e730,0x100000,0x100000,0x1e75a,0x100000,0x100000,0x1ea10,0x1e75c,0x100000,0x100000,0x100000,0x1e75e,0x100000,0x100000,0x100000,0x1e768,0x1e730,0x100000,0x100000,0x1e76a,0x100000,0x100000,0x1ea10,0x1e76c,0x100000,0x100000,0x100000,0x1e76e,0x100000,0x100000,0x100000,0x1e778,0x1e730,0x100000,0x100000,0x1e77a,0x100000,0x100000,0x1ea10,0x1e77c,0x100000,0x100000,0x100000,0x1e77e,0x100000,0x100000,0x100000,0x1ebc0,0x25770,37056,37120,0x1ebc2,0x25772,37072,37128,0x1ebc4,0x25774,37088,37136,0x1ebc6,0x25776,37104,37144,0x1ebc8,0x258d0,37440,37504,0x1ebca,0x258d2,37456,37512,0x1ebcc,0x258d4,37472,37520,0x1ebce,0x258d6,37488,37528,0x1ebd0,0x25a30,37856,37920,0x1ebd2,0x25a32,37872,37928,0x1ebd4,0x25a34,37824,37936,0x1ebd6,0x25a36,37840,37944,0x1ebd8,0x25b90,38240,38304,0x1ebda,0x25b92,38256,38312,0x1ebdc,0x25b94,38208,38320,0x1ebde,0x25b96,38224,38328,0x1ebc0,0x25778,38528,38720,0x1ebc2,0x2577a,38544,38728,0x1ebc4,0x2577c,38560,38736,0x1ebc6,0x2577e,38576,38744,0x1ebc8,0x258d8,38912,39104,0x1ebca,0x258da,38928,39112,0x1ebcc,0x258dc,38944,39120,0x1ebce,0x258de,38960,39128,0x1ebd0,0x25a38,39328,39520,0x1ebd2,0x25a3a,39344,39528,0x1ebd4,0x25a3c,39296,39536,0x1ebd6,0x25a3e,39312,39544,0x1ebd8,0x25b98,39712,39904,0x1ebda,0x25b9a,39728,39912,0x1ebdc,0x25b9c,39680,39920,0x1ebde,0x25b9e,39696,39928,0x1ebc0,0x267f0,40128,40192,0x1ebc2,0x267f2,40144,40200,0x1ebc4,0x267f4,40160,40208,0x1ebc6,0x267f6,40176,40216,0x1ebc8,0x26950,40512,40576,0x1ebca,0x26952,40528,40584,0x1ebcc,0x26954,40544,40592,0x1ebce,0x26956,40560,40600,0x1ebd0,0x26ab0,40928,40992,0x1ebd2,0x26ab2,40944,41000,0x1ebd4,0x26ab4,40896,41008,0x1ebd6,0x26ab6,40912,41016,0x1ebd8,0x26c10,41312,41376,0x1ebda,0x26c12,41328,41384,0x1ebdc,0x26c14,41280,41392,0x1ebde,0x26c16,41296,41400,0x1ebc0,0x100000,41600,41792,0x1ebc2,0x100000,41616,41800,0x1ebc4,0x100000,41632,41808,0x1ebc6,0x100000,41648,41816,0x1ebc8,0x100000,41984,42176,0x1ebca,0x100000,42000,42184,0x1ebcc,0x100000,42016,42192,0x1ebce,0x100000,42032,42200,0x1ebd0,0x100000,42400,42592,0x1ebd2,0x100000,42416,42600,0x1ebd4,0x100000,42368,42608,0x1ebd6,0x100000,42384,42616,0x1ebd8,0x100000,42784,42976,0x1ebda,0x100000,42800,42984,0x1ebdc,0x100000,42752,42992,0x1ebde,0x100000,42768,43000,37056,37088,37120,55296,37064,37096,37128,55312,37072,37104,37136,55328,37080,37112,37144,55344,38208,38240,38272,55360,38216,38248,38280,55376,38224,38256,38288,55392,38232,38264,38296,55408,39392,39360,39456,55424,39400,39368,39464,55440,39408,39376,39472,55456,39416,39384,39480,55472,40544,40512,40608,55488,40552,40520,40616,55504,40560,40528,40624,55520,40568,40536,40632,55536,46208,46240,46400,55552,46216,46248,46408,55568,46224,46256,46416,55584,46232,46264,46424,55600,47360,47392,47552,55616,47368,47400,47560,55632,47376,47408,47568,55648,47384,47416,47576,55664,48544,48512,48736,55680,48552,48520,48744,55696,48560,48528,48752,55712,48568,48536,48760,55728,49696,49664,49888,55744,49704,49672,49896,55760,49712,49680,49904,55776,49720,49688,49912,55792,55488,55520,55552,55808,55496,55528,55560,55824,55504,55536,55568,55840,55512,55544,55576,55856,56640,56672,56704,55872,56648,56680,56712,55888,56656,56688,56720,55904,56664,56696,56728,55920,57824,57792,57888,55936,57832,57800,57896,55952,57840,57808,57904,55968,57848,57816,57912,55984,58976,58944,59040,56000,58984,58952,59048,56016,58992,58960,59056,56032,59000,58968,59064,56048,64640,64672,64832,56064,64648,64680,64840,56080,64656,64688,64848,56096,64664,64696,64856,56112,65792,65824,65984,56128,65800,65832,65992,56144,65808,65840,66000,56160,65816,65848,66008,56176,66976,66944,67168,56192,66984,66952,67176,56208,66992,66960,67184,56224,67000,66968,67192,56240,68128,68096,68320,56256,68136,68104,68328,56272,68144,68112,68336,56288,68152,68120,68344,56304,0x1e800,0x1e800,0x20cc0,36864,0x1e8c4,0x1e8c4,0x20cc4,36880,0x1e988,0x1e988,0x20cc8,36896,0x1ea4c,0x1ea4c,0x20ccc,36912,0x1eb10,0x1eb10,0x20cd0,36928,0x1ebd4,0x1ebd4,0x20cd4,36944,0x1ec98,0x1ec98,0x20cd8,36960,0x1ed5c,0x1ed5c,0x20cdc,36976,0x1ee20,0x1ee20,0x20ce0,37248,0x1eee4,0x1eee4,0x20ce4,37264,0x1efa8,0x1efa8,0x20ce8,37280,0x1f06c,0x1f06c,0x20cec,37296,0x1f130,0x1f130,0x20cf0,37312,0x1f1f4,0x1f1f4,0x20cf4,37328,0x1f2b8,0x1f2b8,0x20cf8,37344,0x1f37c,0x1f37c,0x20cfc,37360,0x1e810,0x1e800,0x20cc0,37632,0x1e8d4,0x1e8c4,0x20cc4,37648,0x1e998,0x1e988,0x20cc8,37664,0x1ea5c,0x1ea4c,0x20ccc,37680,0x1eb20,0x1eb10,0x20cd0,37696,0x1ebe4,0x1ebd4,0x20cd4,37712,0x1eca8,0x1ec98,0x20cd8,37728,0x1ed6c,0x1ed5c,0x20cdc,37744,0x1ee30,0x1ee20,0x20ce0,38016,0x1eef4,0x1eee4,0x20ce4,38032,0x1efb8,0x1efa8,0x20ce8,38048,0x1f07c,0x1f06c,0x20cec,38064,0x1f140,0x1f130,0x20cf0,38080,0x1f204,0x1f1f4,0x20cf4,38096,0x1f2c8,0x1f2b8,0x20cf8,38112,0x1f38c,0x1f37c,0x20cfc,38128,0x1e820,0x1e800,0x20cc0,38400,0x1e8e4,0x1e8c4,0x20cc4,38416,0x1e9a8,0x1e988,0x20cc8,38432,0x1ea6c,0x1ea4c,0x20ccc,38448,0x1eb30,0x1eb10,0x20cd0,38464,0x1ebf4,0x1ebd4,0x20cd4,38480,0x1ecb8,0x1ec98,0x20cd8,38496,0x1ed7c,0x1ed5c,0x20cdc,38512,0x1ee40,0x1ee20,0x20ce0,38784,0x1ef04,0x1eee4,0x20ce4,38800,0x1efc8,0x1efa8,0x20ce8,38816,0x1f08c,0x1f06c,0x20cec,38832,0x1f150,0x1f130,0x20cf0,38848,0x1f214,0x1f1f4,0x20cf4,38864,0x1f2d8,0x1f2b8,0x20cf8,38880,0x1f39c,0x1f37c,0x20cfc,38896,0x1e830,0x1e800,0x20cc0,39168,0x1e8f4,0x1e8c4,0x20cc4,39184,0x1e9b8,0x1e988,0x20cc8,39200,0x1ea7c,0x1ea4c,0x20ccc,39216,0x1eb40,0x1eb10,0x20cd0,39232,0x1ec04,0x1ebd4,0x20cd4,39248,0x1ecc8,0x1ec98,0x20cd8,39264,0x1ed8c,0x1ed5c,0x20cdc,39280,0x1ee50,0x1ee20,0x20ce0,39552,0x1ef14,0x1eee4,0x20ce4,39568,0x1efd8,0x1efa8,0x20ce8,39584,0x1f09c,0x1f06c,0x20cec,39600,0x1f160,0x1f130,0x20cf0,39616,0x1f224,0x1f1f4,0x20cf4,39632,0x1f2e8,0x1f2b8,0x20cf8,39648,0x1f3ac,0x1f37c,0x20cfc,39664,73728,0x26f70,0x1e980,0x1ebc0,73744,0x26f74,0x1ee10,0x1f050,73760,0x26f78,0x1f2a0,0x1f4e0,73776,0x26f7c,0x1f730,0x1f970,73792,0x26f80,0x1fbc0,0x1fe00,73808,0x26f84,0x20050,0x20290,73824,0x26f88,0x204e0,0x20720,73840,0x26f8c,0x20970,0x20bb0,74112,0x26f90,0x20e00,0x21040,74128,0x26f94,0x21290,0x214d0,74144,0x26f98,0x21720,0x21960,74160,0x26f9c,0x21bb0,0x21df0,74176,0x26fa0,0x22040,0x22280,74192,0x26fa4,0x224d0,0x22710,74208,0x26fa8,0x22960,0x22ba0,74224,0x26fac,0x22df0,0x23030,74528,0x26fb0,0x1e9b0,0x1ebc8,74544,0x26fb4,0x1ee40,0x1f058,74496,0x26fb8,0x1f2d0,0x1f4e8,74512,0x26fbc,0x1f760,0x1f978,74592,0x26fc0,0x1fbf0,0x1fe08,74608,0x26fc4,0x20080,0x20298,74560,0x26fc8,0x20510,0x20728,74576,0x26fcc,0x209a0,0x20bb8,74912,0x26fd0,0x20e30,0x21048,74928,0x26fd4,0x212c0,0x214d8,74880,0x26fd8,0x21750,0x21968,74896,0x26fdc,0x21be0,0x21df8,74976,0x26fe0,0x22070,0x22288,74992,0x26fe4,0x22500,0x22718,74944,0x26fe8,0x22990,0x22ba8,74960,0x26fec,0x22e20,0x23038,75328,0x26ff0,0x1e9e0,0x1ebd0,75344,0x26ff4,0x1ee70,0x1f060,75360,0x26ff8,0x1f300,0x1f4f0,75376,0x26ffc,0x1f790,0x1f980,75264,0x27000,0x1fc20,0x1fe10,75280,0x27004,0x200b0,0x202a0,75296,0x27008,0x20540,0x20730,75312,0x2700c,0x209d0,0x20bc0,75712,0x27010,0x20e60,0x21050,75728,0x27014,0x212f0,0x214e0,75744,0x27018,0x21780,0x21970,75760,0x2701c,0x21c10,0x21e00,75648,0x27020,0x220a0,0x22290,75664,0x27024,0x22530,0x22720,75680,0x27028,0x229c0,0x22bb0,75696,0x2702c,0x22e50,0x23040,76128,0x27030,0x1ea10,0x1ebd8,76144,0x27034,0x1eea0,0x1f068,76096,0x27038,0x1f330,0x1f4f8,76112,0x2703c,0x1f7c0,0x1f988,76064,0x27040,0x1fc50,0x1fe18,76080,0x27044,0x200e0,0x202a8,76032,0x27048,0x20570,0x20738,76048,0x2704c,0x20a00,0x20bc8,76512,0x27050,0x20e90,0x21058,76528,0x27054,0x21320,0x214e8,76480,0x27058,0x217b0,0x21978,76496,0x2705c,0x21c40,0x21e08,76448,0x27060,0x220d0,0x22298,76464,0x27064,0x22560,0x22728,76416,0x27068,0x229f0,0x22bb8,76432,0x2706c,0x22e80,0x23048,0x3f803f80,0x3f803f80,16256,0,0,0x3f803f80,0x3f800000,0,0,0x3f803f80,0,16256,0,0x3f803f80,0,0x3f800000,0x3f803f80,0,0,0,0,0,0,0,0,0,0,0,0,0,0,0,0x3f803f80,0,0,0,0,0,0,0,0,0,0,0,0,0,0,0,0x3f803f80,0,0,0,0,0,0,0,0,0,0,0,0,0,0,0,0x3f803f80,0,0,0,0,0,0,0,0,0,0,0,0,0,0,0,0x3f803f80,0x3f803f80,16256,0,0,0x3f803f80,0x3f800000,0,0,0x3f803f80,0,16256,0,0x3f803f80,0,0x3f800000,0x3f803f80,0,0,0,0,0,0,0,0,0,0,0,0,0,0,0,0x3f803f80,0,0,0,0,0,0,0,0,0,0,0,0,0,0,0,0x3f803f80,0,0,0,0,0,0,0,0,0,0,0,0,0,0,0,0x3f803f80,0,0,0,0,0,0,0,0,0,0,0,0,0,0,0,0x3f803f80,0x3f803f80,16256,0,0,0x3f803f80,0x3f800000,0,0,0x3f803f80,0,16256,0,0x3f803f80,0,0x3f800000,0x3f803f80,0,0,0,0,0,0,0,0,0,0,0,0,0,0,0,0x3f803f80,0,0,0,0,0,0,0,0,0,0,0,0,0,0,0,0x3f803f80,0,0,0,0,0,0,0,0,0,0,0,0,0,0,0,0x3f803f80,0,0,0,0,0,0,0,0,0,0,0,0,0,0,0,0x3f803f80,0x3f803f80,16256,0,0,0x3f803f80,0x3f800000,0,0,0x3f803f80,0,16256,0,0x3f803f80,0,0x3f800000,0,64,0x90000,0xf0080,144,208,0x90010,0xf0090,288,352,0x90020,0xf00a0,432,496,0x90030,0xf00b0,576,512,0x90040,0xf00c0,720,656,0x90050,0xf00d0,864,800,0x90060,0xf00e0,1008,944,0x90070,0xf00f0,1024,1088,0x90190,0xf0210,1168,1232,0x90180,0xf0200,1312,1376,0x901b0,0xf0230,1456,1520,0x901a0,0xf0220,1600,1536,0x901d0,0xf0250,1744,1680,0x901c0,0xf0240,1888,1824,0x901f0,0xf0270,2032,1968,0x901e0,0xf0260,16,80,0x90320,0x11f7a0,128,192,0x90330,0x11f7b0,304,368,0x90300,0x11f780,416,480,0x90310,0x11f790,592,528,0x90360,0x11f7e0,704,640,0x90370,0x11f7f0,880,816,0x90340,0x11f7c0,992,928,0x90350,0x11f7d0,1040,1104,0x904b0,0x11f930,1152,1216,0x904a0,0x11f920,1328,1392,0x90490,0x11f910,1440,1504,0x90480,0x11f900,1616,1552,0x904f0,0x11f970,1728,1664,0x904e0,0x11f960,1904,1840,0x904d0,0x11f950,2016,1952,0x904c0,0x11f940,32,96,0xbfa40,0x11fac0,176,240,0xbfa50,0x11fad0,256,320,0xbfa60,0x11fae0,400,464,0xbfa70,0x11faf0,608,544,0xbfa00,0x11fa80,752,688,0xbfa10,0x11fa90,832,768,0xbfa20,0x11faa0,976,912,0xbfa30,0x11fab0,1056,1120,0xbfbd0,0x11fc50,1200,1264,0xbfbc0,0x11fc40,1280,1344,0xbfbf0,0x11fc70,1424,1488,0xbfbe0,0x11fc60,1632,1568,0xbfb90,0x11fc10,1776,1712,0xbfb80,0x11fc00,1856,1792,0xbfbb0,0x11fc30,2000,1936,0xbfba0,0x11fc20,48,112,0xbfd60,0x11fde0,160,224,0xbfd70,0x11fdf0,272,336,0xbfd40,0x11fdc0,384,448,0xbfd50,0x11fdd0,624,560,0xbfd20,0x11fda0,736,672,0xbfd30,0x11fdb0,848,784,0xbfd00,0x11fd80,960,896,0xbfd10,0x11fd90,1072,1136,0xbfef0,0x11ff70,1184,1248,0xbfee0,0x11ff60,1296,1360,0xbfed0,0x11ff50,1408,1472,0xbfec0,0x11ff40,1648,1584,0xbfeb0,0x11ff30,1760,1696,0xbfea0,0x11ff20,1872,1808,0xbfe90,0x11ff10,1984,1920,0xbfe80,0x11ff00,0x11fb40,76800,84992,91648,0x11fb50,76816,85008,91664,0x11fb60,76832,85024,91680,0x11fb70,76848,85040,91696,0x11fb00,76864,85056,91712,0x11fb10,76880,85072,91728,0x11fb20,76896,85088,91744,0x11fb30,76912,85104,91760,0x11fcd0,76928,85120,91776,0x11fcc0,76944,85136,91792,0x11fcf0,76960,85152,91808,0x11fce0,76976,85168,91824,0x11fc90,76992,85184,91840,0x11fc80,77008,85200,91856,0x11fcb0,77024,85216,91872,0x11fca0,77040,85232,91888,0x11fe60,77056,85248,91904,0x11fe70,77072,85264,91920,0x11fe40,77088,85280,91936,0x11fe50,77104,85296,91952,0x11fe20,77120,85312,91968,0x11fe30,77136,85328,91984,0x11fe00,77152,85344,92000,0x11fe10,77168,85360,92016,0x11fff0,77184,85376,92032,0x11ffe0,77200,85392,92048,0x11ffd0,77216,85408,92064,0x11ffc0,77232,85424,92080,0x11ffb0,77248,85440,92096,0x11ffa0,77264,85456,92112,0x11ff90,77280,85472,92128,0x11ff80,77296,85488,92144,0x11fb40,77312,85504,92160,0x11fb50,77328,85520,92176,0x11fb60,77344,85536,92192,0x11fb70,77360,85552,92208,0x11fb00,77376,85568,92224,0x11fb10,77392,85584,92240,0x11fb20,77408,85600,92256,0x11fb30,77424,85616,92272,0x11fcd0,77440,85632,92288,0x11fcc0,77456,85648,92304,0x11fcf0,77472,85664,92320,0x11fce0,77488,85680,92336,0x11fc90,77504,85696,92352,0x11fc80,77520,85712,92368,0x11fcb0,77536,85728,92384,0x11fca0,77552,85744,92400,0x11fe60,77568,85760,92416,0x11fe70,77584,85776,92432,0x11fe40,77600,85792,92448,0x11fe50,77616,85808,92464,0x11fe20,77632,85824,92480,0x11fe30,77648,85840,92496,0x11fe00,77664,85856,92512,0x11fe10,77680,85872,92528,0x11fff0,77696,85888,92544,0x11ffe0,77712,85904,92560,0x11ffd0,77728,85920,92576,0x11ffc0,77744,85936,92592,0x11ffb0,77760,85952,92608,0x11ffa0,77776,85968,92624,0x11ff90,77792,85984,92640,0x11ff80,77808,86000,92656,0x1c1d0,36864,46080,52232,0x1c1d4,36880,46096,52248,0x1c1d8,36896,46112,52264,0x1c1dc,36912,46128,52280,0x1c1e0,36928,46144,52296,0x1c1e4,36944,46160,52312,0x1c1e8,36960,46176,52328,0x1c1ec,36976,46192,52344,0x1c1f0,36992,46208,52360,0x1c1f4,37008,46224,52376,0x1c1f8,37024,46240,52392,0x1c1fc,37040,46256,52408,0x1c200,37056,46272,52424,0x1c204,37072,46288,52440,0x1c208,37088,46304,52456,0x1c20c,37104,46320,52472,0x1c210,37632,46848,53000,0x1c214,37648,46864,53016,0x1c218,37664,46880,53032,0x1c21c,37680,46896,53048,0x1c220,37696,46912,53064,0x1c224,37712,46928,53080,0x1c228,37728,46944,53096,0x1c22c,37744,46960,53112,0x1c230,37760,46976,53128,0x1c234,37776,46992,53144,0x1c238,37792,47008,53160,0x1c23c,37808,47024,53176,0x1c240,37824,47040,53192,0x1c244,37840,47056,53208,0x1c248,37856,47072,53224,0x1c24c,37872,47088,53240,0x1c250,38400,47616,53768,0x1c254,38416,47632,53784,0x1c258,38432,47648,53800,0x1c25c,38448,47664,53816,0x1c260,38464,47680,53832,0x1c264,38480,47696,53848,0x1c268,38496,47712,53864,0x1c26c,38512,47728,53880,0x1c270,38528,47744,53896,0x1c274,38544,47760,53912,0x1c278,38560,47776,53928,0x1c27c,38576,47792,53944,0x1c280,38592,47808,53960,0x1c284,38608,47824,53976,0x1c288,38624,47840,53992,0x1c28c,38640,47856,54008,0x1c290,39168,48384,54536,0x1c294,39184,48400,54552,0x1c298,39200,48416,54568,0x1c29c,39216,48432,54584,0x1c2a0,39232,48448,54600,0x1c2a4,39248,48464,54616,0x1c2a8,39264,48480,54632,0x1c2ac,39280,48496,54648,0x1c2b0,39296,48512,54664,0x1c2b4,39312,48528,54680,0x1c2b8,39328,48544,54696,0x1c2bc,39344,48560,54712,0x1c2c0,39360,48576,54728,0x1c2c4,39376,48592,54744,0x1c2c8,39392,48608,54760,0x1c2cc,39408,48624,54776,86048,86112,0x1d600,0x1d60c,86192,86256,0x1d610,0x1d61c,86272,86336,0x1d620,0x1d62c,86416,86480,0x1d630,0x1d63c,86624,86560,0x1d640,0x1d64c,86768,86704,0x1d650,0x1d65c,86848,86784,0x1d660,0x1d66c,86992,86928,0x1d670,0x1d67c,87072,87136,0x1d680,0x1d68c,87216,87280,0x1d690,0x1d69c,87296,87360,0x1d6a0,0x1d6ac,87440,87504,0x1d6b0,0x1d6bc,87648,87584,0x1d6c0,0x1d6cc,87792,87728,0x1d6d0,0x1d6dc,87872,87808,0x1d6e0,0x1d6ec,88016,87952,0x1d6f0,0x1d6fc,86064,86120,0x1d700,0x1d70c,86176,86264,0x1d710,0x1d71c,86288,86344,0x1d720,0x1d72c,86400,86488,0x1d730,0x1d73c,86640,86568,0x1d740,0x1d74c,86752,86712,0x1d750,0x1d75c,86864,86792,0x1d760,0x1d76c,86976,86936,0x1d770,0x1d77c,87088,87144,0x1d780,0x1d78c,87200,87288,0x1d790,0x1d79c,87312,87368,0x1d7a0,0x1d7ac,87424,87512,0x1d7b0,0x1d7bc,87664,87592,0x1d7c0,0x1d7cc,87776,87736,0x1d7d0,0x1d7dc,87888,87816,0x1d7e0,0x1d7ec,88000,87960,0x1d7f0,0x1d7fc,86080,86128,0x1d800,0x1d80c,86224,86240,0x1d810,0x1d81c,86368,86352,0x1d820,0x1d82c,86512,86464,0x1d830,0x1d83c,86528,86576,0x1d840,0x1d84c,86672,86688,0x1d850,0x1d85c,86816,86800,0x1d860,0x1d86c,86960,86912,0x1d870,0x1d87c,87104,87152,0x1d880,0x1d88c,87248,87264,0x1d890,0x1d89c,87392,87376,0x1d8a0,0x1d8ac,87536,87488,0x1d8b0,0x1d8bc,87552,87600,0x1d8c0,0x1d8cc,87696,87712,0x1d8d0,0x1d8dc,87840,87824,0x1d8e0,0x1d8ec,87984,87936,0x1d8f0,0x1d8fc,86096,86136,0x1d900,0x1d90c,86208,86248,0x1d910,0x1d91c,86384,86360,0x1d920,0x1d92c,86496,86472,0x1d930,0x1d93c,86544,86584,0x1d940,0x1d94c,86656,86696,0x1d950,0x1d95c,86832,86808,0x1d960,0x1d96c,86944,86920,0x1d970,0x1d97c,87120,87160,0x1d980,0x1d98c,87232,87272,0x1d990,0x1d99c,87408,87384,0x1d9a0,0x1d9ac,87520,87496,0x1d9b0,0x1d9bc,87568,87608,0x1d9c0,0x1d9cc,87680,87720,0x1d9d0,0x1d9dc,87856,87832,0x1d9e0,0x1d9ec,87968,87944,0x1d9f0,0x1d9fc,0x1e780,0x1e780,0x1ea40,0x100000,0x1e782,0x100000,0x100000,0x1ea40,0x1e784,0x100000,0x100000,0x100000,0x1e786,0x100000,0x100000,0x100000,0x1e790,0x1e780,0x100000,0x100000,0x1e792,0x100000,0x100000,0x1ea40,0x1e794,0x100000,0x100000,0x100000,0x1e796,0x100000,0x100000,0x100000,0x1e7a0,0x1e780,0x100000,0x100000,0x1e7a2,0x100000,0x100000,0x1ea40,0x1e7a4,0x100000,0x100000,0x100000,0x1e7a6,0x100000,0x100000,0x100000,0x1e7b0,0x1e780,0x100000,0x100000,0x1e7b2,0x100000,0x100000,0x1ea40,0x1e7b4,0x100000,0x100000,0x100000,0x1e7b6,0x100000,0x100000,0x100000,0x1e788,0x1e790,0x1ea70,0x100000,0x1e78a,0x100000,0x100000,0x1ea70,0x1e78c,0x100000,0x100000,0x100000,0x1e78e,0x100000,0x100000,0x100000,0x1e798,0x1e790,0x100000,0x100000,0x1e79a,0x100000,0x100000,0x1ea70,0x1e79c,0x100000,0x100000,0x100000,0x1e79e,0x100000,0x100000,0x100000,0x1e7a8,0x1e790,0x100000,0x100000,0x1e7aa,0x100000,0x100000,0x1ea70,0x1e7ac,0x100000,0x100000,0x100000,0x1e7ae,0x100000,0x100000,0x100000,0x1e7b8,0x1e790,0x100000,0x100000,0x1e7ba,0x100000,0x100000,0x1ea70,0x1e7bc,0x100000,0x100000,0x100000,0x1e7be,0x100000,0x100000,0x100000,0x1e7c0,0x1e7a0,0x1eaa0,0x100000,0x1e7c2,0x100000,0x100000,0x1eaa0,0x1e7c4,0x100000,0x100000,0x100000,0x1e7c6,0x100000,0x100000,0x100000,0x1e7d0,0x1e7a0,0x100000,0x100000,0x1e7d2,0x100000,0x100000,0x1eaa0,0x1e7d4,0x100000,0x100000,0x100000,0x1e7d6,0x100000,0x100000,0x100000,0x1e7e0,0x1e7a0,0x100000,0x100000,0x1e7e2,0x100000,0x100000,0x1eaa0,0x1e7e4,0x100000,0x100000,0x100000,0x1e7e6,0x100000,0x100000,0x100000,0x1e7f0,0x1e7a0,0x100000,0x100000,0x1e7f2,0x100000,0x100000,0x1eaa0,0x1e7f4,0x100000,0x100000,0x100000,0x1e7f6,0x100000,0x100000,0x100000,0x1e7c8,0x1e7b0,0x1ead0,0x100000,0x1e7ca,0x100000,0x100000,0x1ead0,0x1e7cc,0x100000,0x100000,0x100000,0x1e7ce,0x100000,0x100000,0x100000,0x1e7d8,0x1e7b0,0x100000,0x100000,0x1e7da,0x100000,0x100000,0x1ead0,0x1e7dc,0x100000,0x100000,0x100000,0x1e7de,0x100000,0x100000,0x100000,0x1e7e8,0x1e7b0,0x100000,0x100000,0x1e7ea,0x100000,0x100000,0x1ead0,0x1e7ec,0x100000,0x100000,0x100000,0x1e7ee,0x100000,0x100000,0x100000,0x1e7f8,0x1e7b0,0x100000,0x100000,0x1e7fa,0x100000,0x100000,0x1ead0,0x1e7fc,0x100000,0x100000,0x100000,0x1e7fe,0x100000,0x100000,0x100000,0x1ec20,0x25cf0,37152,37216,0x1ec22,0x25cf2,37168,37224,0x1ec24,0x25cf4,37184,37232,0x1ec26,0x25cf6,37200,37240,0x1ec28,0x25e50,37536,37600,0x1ec2a,0x25e52,37552,37608,0x1ec2c,0x25e54,37568,37616,0x1ec2e,0x25e56,37584,37624,0x1ec30,0x25fb0,37888,37952,0x1ec32,0x25fb2,37904,37960,0x1ec34,0x25fb4,37984,37968,0x1ec36,0x25fb6,38000,37976,0x1ec38,0x26110,38272,38336,0x1ec3a,0x26112,38288,38344,0x1ec3c,0x26114,38368,38352,0x1ec3e,0x26116,38384,38360,0x1ec20,0x25cf8,38752,38688,0x1ec22,0x25cfa,38768,38696,0x1ec24,0x25cfc,38656,38704,0x1ec26,0x25cfe,38672,38712,0x1ec28,0x25e58,39136,39072,0x1ec2a,0x25e5a,39152,39080,0x1ec2c,0x25e5c,39040,39088,0x1ec2e,0x25e5e,39056,39096,0x1ec30,0x25fb8,39488,39424,0x1ec32,0x25fba,39504,39432,0x1ec34,0x25fbc,39456,39440,0x1ec36,0x25fbe,39472,39448,0x1ec38,0x26118,39872,39808,0x1ec3a,0x2611a,39888,39816,0x1ec3c,0x2611c,39840,39824,0x1ec3e,0x2611e,39856,39832,0x1ec20,0x267f8,40224,40288,0x1ec22,0x267fa,40240,40296,0x1ec24,0x267fc,40256,40304,0x1ec26,0x267fe,40272,40312,0x1ec28,0x26958,40608,40672,0x1ec2a,0x2695a,40624,40680,0x1ec2c,0x2695c,40640,40688,0x1ec2e,0x2695e,40656,40696,0x1ec30,0x26ab8,40960,41024,0x1ec32,0x26aba,40976,41032,0x1ec34,0x26abc,41056,41040,0x1ec36,0x26abe,41072,41048,0x1ec38,0x26c18,41344,41408,0x1ec3a,0x26c1a,41360,41416,0x1ec3c,0x26c1c,41440,41424,0x1ec3e,0x26c1e,41456,41432,0x1ec20,0x100000,41824,41760,0x1ec22,0x100000,41840,41768,0x1ec24,0x100000,41728,41776,0x1ec26,0x100000,41744,41784,0x1ec28,0x100000,42208,42144,0x1ec2a,0x100000,42224,42152,0x1ec2c,0x100000,42112,42160,0x1ec2e,0x100000,42128,42168,0x1ec30,0x100000,42560,42496,0x1ec32,0x100000,42576,42504,0x1ec34,0x100000,42528,42512,0x1ec36,0x100000,42544,42520,0x1ec38,0x100000,42944,42880,0x1ec3a,0x100000,42960,42888,0x1ec3c,0x100000,42912,42896,0x1ec3e,0x100000,42928,42904,37152,37184,37216,64512,37160,37192,37224,64528,37168,37200,37232,64544,37176,37208,37240,64560,38304,38336,38368,64576,38312,38344,38376,64592,38320,38352,38384,64608,38328,38360,38392,64624,39424,39520,39488,64640,39432,39528,39496,64656,39440,39536,39504,64672,39448,39544,39512,64688,40576,40672,40640,64704,40584,40680,40648,64720,40592,40688,40656,64736,40600,40696,40664,64752,46432,46336,46368,64768,46440,46344,46376,64784,46448,46352,46384,64800,46456,46360,46392,64816,47584,47488,47520,64832,47592,47496,47528,64848,47600,47504,47536,64864,47608,47512,47544,64880,48704,48672,48640,64896,48712,48680,48648,64912,48720,48688,48656,64928,48728,48696,48664,64944,49856,49824,49792,64960,49864,49832,49800,64976,49872,49840,49808,64992,49880,49848,49816,65008,55584,55616,55648,65024,55592,55624,55656,65040,55600,55632,55664,65056,55608,55640,55672,65072,56736,56768,56800,65088,56744,56776,56808,65104,56752,56784,56816,65120,56760,56792,56824,65136,57856,57952,57920,65152,57864,57960,57928,65168,57872,57968,57936,65184,57880,57976,57944,65200,59008,59104,59072,65216,59016,59112,59080,65232,59024,59120,59088,65248,59032,59128,59096,65264,64864,64768,64800,65280,64872,64776,64808,65296,64880,64784,64816,65312,64888,64792,64824,65328,66016,65920,65952,65344,66024,65928,65960,65360,66032,65936,65968,65376,66040,65944,65976,65392,67136,67104,67072,65408,67144,67112,67080,65424,67152,67120,67088,65440,67160,67128,67096,65456,68288,68256,68224,65472,68296,68264,68232,65488,68304,68272,68240,65504,68312,68280,68248,65520,0x1e800,0x1e800,0x20cc0,55296,0x1e8c4,0x1e8c4,0x20cc4,55312,0x1e988,0x1e988,0x20cc8,55328,0x1ea4c,0x1ea4c,0x20ccc,55344,0x1eb10,0x1eb10,0x20cd0,55360,0x1ebd4,0x1ebd4,0x20cd4,55376,0x1ec98,0x1ec98,0x20cd8,55392,0x1ed5c,0x1ed5c,0x20cdc,55408,0x1ee20,0x1ee20,0x20ce0,55680,0x1eee4,0x1eee4,0x20ce4,55696,0x1efa8,0x1efa8,0x20ce8,55712,0x1f06c,0x1f06c,0x20cec,55728,0x1f130,0x1f130,0x20cf0,55744,0x1f1f4,0x1f1f4,0x20cf4,55760,0x1f2b8,0x1f2b8,0x20cf8,55776,0x1f37c,0x1f37c,0x20cfc,55792,0x1e810,0x1e800,0x20cc0,56064,0x1e8d4,0x1e8c4,0x20cc4,56080,0x1e998,0x1e988,0x20cc8,56096,0x1ea5c,0x1ea4c,0x20ccc,56112,0x1eb20,0x1eb10,0x20cd0,56128,0x1ebe4,0x1ebd4,0x20cd4,56144,0x1eca8,0x1ec98,0x20cd8,56160,0x1ed6c,0x1ed5c,0x20cdc,56176,0x1ee30,0x1ee20,0x20ce0,56448,0x1eef4,0x1eee4,0x20ce4,56464,0x1efb8,0x1efa8,0x20ce8,56480,0x1f07c,0x1f06c,0x20cec,56496,0x1f140,0x1f130,0x20cf0,56512,0x1f204,0x1f1f4,0x20cf4,56528,0x1f2c8,0x1f2b8,0x20cf8,56544,0x1f38c,0x1f37c,0x20cfc,56560,0x1e820,0x1e800,0x20cc0,56832,0x1e8e4,0x1e8c4,0x20cc4,56848,0x1e9a8,0x1e988,0x20cc8,56864,0x1ea6c,0x1ea4c,0x20ccc,56880,0x1eb30,0x1eb10,0x20cd0,56896,0x1ebf4,0x1ebd4,0x20cd4,56912,0x1ecb8,0x1ec98,0x20cd8,56928,0x1ed7c,0x1ed5c,0x20cdc,56944,0x1ee40,0x1ee20,0x20ce0,57216,0x1ef04,0x1eee4,0x20ce4,57232,0x1efc8,0x1efa8,0x20ce8,57248,0x1f08c,0x1f06c,0x20cec,57264,0x1f150,0x1f130,0x20cf0,57280,0x1f214,0x1f1f4,0x20cf4,57296,0x1f2d8,0x1f2b8,0x20cf8,57312,0x1f39c,0x1f37c,0x20cfc,57328,0x1e830,0x1e800,0x20cc0,57600,0x1e8f4,0x1e8c4,0x20cc4,57616,0x1e9b8,0x1e988,0x20cc8,57632,0x1ea7c,0x1ea4c,0x20ccc,57648,0x1eb40,0x1eb10,0x20cd0,57664,0x1ec04,0x1ebd4,0x20cd4,57680,0x1ecc8,0x1ec98,0x20cd8,57696,0x1ed8c,0x1ed5c,0x20cdc,57712,0x1ee50,0x1ee20,0x20ce0,57984,0x1ef14,0x1eee4,0x20ce4,58000,0x1efd8,0x1efa8,0x20ce8,58016,0x1f09c,0x1f06c,0x20cec,58032,0x1f160,0x1f130,0x20cf0,58048,0x1f224,0x1f1f4,0x20cf4,58064,0x1f2e8,0x1f2b8,0x20cf8,58080,0x1f3ac,0x1f37c,0x20cfc,58096,92160,0x27070,0x1ea40,0x1ec20,92176,0x27074,0x1eed0,0x1f0b0,92192,0x27078,0x1f360,0x1f540,92208,0x2707c,0x1f7f0,0x1f9d0,92224,0x27080,0x1fc80,0x1fe60,92240,0x27084,0x20110,0x202f0,92256,0x27088,0x205a0,0x20780,92272,0x2708c,0x20a30,0x20c10,92544,0x27090,0x20ec0,0x210a0,92560,0x27094,0x21350,0x21530,92576,0x27098,0x217e0,0x219c0,92592,0x2709c,0x21c70,0x21e50,92608,0x270a0,0x22100,0x222e0,92624,0x270a4,0x22590,0x22770,92640,0x270a8,0x22a20,0x22c00,92656,0x270ac,0x22eb0,0x23090,92960,0x270b0,0x1ea70,0x1ec28,92976,0x270b4,0x1ef00,0x1f0b8,92928,0x270b8,0x1f390,0x1f548,92944,0x270bc,0x1f820,0x1f9d8,93024,0x270c0,0x1fcb0,0x1fe68,93040,0x270c4,0x20140,0x202f8,92992,0x270c8,0x205d0,0x20788,93008,0x270cc,0x20a60,0x20c18,93344,0x270d0,0x20ef0,0x210a8,93360,0x270d4,0x21380,0x21538,93312,0x270d8,0x21810,0x219c8,93328,0x270dc,0x21ca0,0x21e58,93408,0x270e0,0x22130,0x222e8,93424,0x270e4,0x225c0,0x22778,93376,0x270e8,0x22a50,0x22c08,93392,0x270ec,0x22ee0,0x23098,93760,0x270f0,0x1eaa0,0x1ec30,93776,0x270f4,0x1ef30,0x1f0c0,93792,0x270f8,0x1f3c0,0x1f550,93808,0x270fc,0x1f850,0x1f9e0,93696,0x27100,0x1fce0,0x1fe70,93712,0x27104,0x20170,0x20300,93728,0x27108,0x20600,0x20790,93744,0x2710c,0x20a90,0x20c20,94144,0x27110,0x20f20,0x210b0,94160,0x27114,0x213b0,0x21540,94176,0x27118,0x21840,0x219d0,94192,0x2711c,0x21cd0,0x21e60,94080,0x27120,0x22160,0x222f0,94096,0x27124,0x225f0,0x22780,94112,0x27128,0x22a80,0x22c10,94128,0x2712c,0x22f10,0x230a0,94560,0x27130,0x1ead0,0x1ec38,94576,0x27134,0x1ef60,0x1f0c8,94528,0x27138,0x1f3f0,0x1f558,94544,0x2713c,0x1f880,0x1f9e8,94496,0x27140,0x1fd10,0x1fe78,94512,0x27144,0x201a0,0x20308,94464,0x27148,0x20630,0x20798,94480,0x2714c,0x20ac0,0x20c28,94944,0x27150,0x20f50,0x210b8,94960,0x27154,0x213e0,0x21548,94912,0x27158,0x21870,0x219d8,94928,0x2715c,0x21d00,0x21e68,94880,0x27160,0x22190,0x222f8,94896,0x27164,0x22620,0x22788,94848,0x27168,0x22ab0,0x22c18,94864,0x2716c,0x22f40,0x230a8,0x3f803f80,0x3f803f80,16256,0,0,0x3f803f80,0x3f800000,0,0,0x3f803f80,0,16256,0,0x3f803f80,0,0x3f800000,0x3f803f80,0,0,0,0,0,0,0,0,0,0,0,0,0,0,0,0x3f803f80,0,0,0,0,0,0,0,0,0,0,0,0,0,0,0,0x3f803f80,0,0,0,0,0,0,0,0,0,0,0,0,0,0,0,0x3f803f80,0,0,0,0,0,0,0,0,0,0,0,0,0,0,0,0x3f803f80,0x3f803f80,16256,0,0,0x3f803f80,0x3f800000,0,0,0x3f803f80,0,16256,0,0x3f803f80,0,0x3f800000,0x3f803f80,0,0,0,0,0,0,0,0,0,0,0,0,0,0,0,0x3f803f80,0,0,0,0,0,0,0,0,0,0,0,0,0,0,0,0x3f803f80,0,0,0,0,0,0,0,0,0,0,0,0,0,0,0,0x3f803f80,0,0,0,0,0,0,0,0,0,0,0,0,0,0,0,0x3f803f80,0x3f803f80,16256,0,0,0x3f803f80,0x3f800000,0,0,0x3f803f80,0,16256,0,0x3f803f80,0,0x3f800000,0x3f803f80,0,0,0,0,0,0,0,0,0,0,0,0,0,0,0,0x3f803f80,0,0,0,0,0,0,0,0,0,0,0,0,0,0,0,0x3f803f80,0,0,0,0,0,0,0,0,0,0,0,0,0,0,0,0x3f803f80,0,0,0,0,0,0,0,0,0,0,0,0,0,0,0,0x3f803f80,0x3f803f80,16256,0,0,0x3f803f80,0x3f800000,0,0,0x3f803f80,0,16256,0,0x3f803f80,0,0x3f800000,0xffffffff,0xffffffff,0,0,0,0,0xffffffff,0xff000000,0xff000000,0xffffff,0xff000000,0xff000000,0,4096,16384,0x1c000,0xffffffff,0xffffffff,0,0,0,0,0xffffff,0xffff0000,0xffff0000,65535,0xffffff,0xffffff,1024,5120,16896,0x1c0a0,0xffffffff,0xffffffff,0,0,0,0,65535,0xffffff00,0xff000000,0xffffff,0xffff0000,0xffff0000,2048,10240,17408,0x1c140,0xffffffff,0,0,0,0,0,255,0xffffffff,0xffff0000,65535,65535,65535,3072,11264,17920,0x1c1d0,0,0x3fb8aa3b,0x2a8cbccc,0x38fba882,0x7fff7fff,0x415db3d7,0xffff0000,0,0,65535,0,0xffff0000,0,0,0,0,0,808,1800,2608,3600,4408,5400,6208,7200,8192,9000,9992,10800,11792,12600,18432,14385,18447,16170,18462,18462,18462,18462,18462,20,19,0,0,0,0,0,0,0x1b0000,0x5b8480,0xae0a80,0xee8f00,0x1411500,0x1819980,0x1d41f80,0x214a400,0x2672a00,0x2b9b000,0x2fa3480,0x34cba80,0x38d3f00,0x3dfc500,0x4204980,0x60a80,0x4b2b680,0x6a800,0x5452380,0x74580,0x74580,0x74580,0x74580,0x74580,0x74580,0x74580,0x74580,0x74580,0x74580,0x74580,0x74580,0x74580,1,809,1801,2609,3601,4409,5401,6209,7201,8193,9001,9993,10801,11793,12601,13592,14386,15377,16171,18463,18463,18463,18463,18463,20,19,0,0,0,0,0,0,0x1b0a80,0x5b8f00,0xae1500,0xee9980,0x1411f80,0x181a400,0x1d42a00,0x214ae80,0x2673480,0x2b9ba80,0x2fa3f00,0x34cc500,0x38d4980,0x3dfcf80,0x4205400,0x472cf80,0x4b2c100,0x5053c80,0x5452e00,0x75000,0x75000,0x75000,0x75000,0x75000,0x75000,0x75000,0x75000,0x75000,0x75000,0x75000,0x75000,0x75000,2,810,1802,2610,3602,4410,5402,6210,7202,8194,9002,9994,10802,11794,12602,13593,14387,15378,16172,17162,17671,18216,18216,18216,22,21,0,0,0,0,0,0,0x1b1500,0x5b9980,0xae1f80,0xeea400,0x1412a00,0x181ae80,0x1d43480,0x214b900,0x2673f00,0x2b9c500,0x2fa4980,0x34ccf80,0x38d5400,0x3dfda00,0x4205e80,0x472da00,0x4b2cb80,0x5054700,0x5453880,0x597a980,0x5c17500,0x5ecba80,0x5ecba80,0x5ecba80,0x5ecba80,0x5ecba80,0x5ecba80,0x5ecba80,0x5ecba80,0x5ecba80,0x5ecba80,0x5ecba80,3,811,1803,2611,3603,4411,5403,6211,7203,8195,9003,9995,10803,11795,12603,13594,14388,15379,16173,17163,17672,17672,17672,17672,21,20,0,0,0,0,0,0,0x1b1f80,0x5ba400,0xae2a00,0xeeae80,0x1413480,0x181b900,0x1d43f00,0x214c380,0x2674980,0x2b9cf80,0x2fa5400,0x34cda00,0x38d5e80,0x3dfe480,0x4206900,0x472e480,0x4b2d600,0x5055180,0x5454300,0x597b400,0x5c17f80,0x5c17f80,0x5c17f80,0x5c17f80,0x5c17f80,0x5c17f80,0x5c17f80,0x5c17f80,0x5c17f80,0x5c17f80,0x5c17f80,0x5c17f80,4,812,1804,2612,3604,4412,5404,6212,7204,8196,9004,9996,10804,11796,12604,13595,14389,15380,16174,18464,17673,17673,17673,17673,21,20,0,0,0,0,0,0,0x1b2a00,0x5bae80,0xae3480,0xeeb900,0x1413f00,0x181c380,0x1d44980,0x214ce00,0x2675400,0x2b9da00,0x2fa5e80,0x34ce480,0x38d6900,0x3dfef00,0x4207380,0x472ef00,0x4b2e080,0x5055c00,0x5454d80,0x75a80,0x5c18a00,0x5c18a00,0x5c18a00,0x5c18a00,0x5c18a00,0x5c18a00,0x5c18a00,0x5c18a00,0x5c18a00,0x5c18a00,0x5c18a00,0x5c18a00,5,813,1805,2613,3605,4413,5405,6213,7205,8197,9005,9997,10805,11797,12605,13596,14390,15381,16175,18465,18465,18465,18465,18465,20,19,0,0,0,0,0,0,0x1b3480,0x5bb900,0xae3f00,0xeec380,0x1414980,0x181ce00,0x1d45400,0x214d880,0x2675e80,0x2b9e480,0x2fa6900,0x34cef00,0x38d7380,0x3dff980,0x4207e00,0x472f980,0x4b2eb00,0x5056680,0x5455800,0x76500,0x76500,0x76500,0x76500,0x76500,0x76500,0x76500,0x76500,0x76500,0x76500,0x76500,0x76500,0x76500,6,814,1806,2614,3606,4414,5406,6214,7206,8198,9006,9998,10806,11798,12606,13597,14391,15382,16176,18466,17674,17674,17674,17674,21,20,0,0,0,0,0,0,0x1b3f00,0x5bc380,0xae4980,0xeece00,0x1415400,0x181d880,0x1d45e80,0x214e300,0x2676900,0x2b9ef00,0x2fa7380,0x34cf980,0x38d7e00,0x3e00400,0x4208880,0x4730400,0x4b2f580,0x5057100,0x5456280,0x76f80,0x5c19480,0x5c19480,0x5c19480,0x5c19480,0x5c19480,0x5c19480,0x5c19480,0x5c19480,0x5c19480,0x5c19480,0x5c19480,0x5c19480,7,815,1807,2615,3607,4415,5407,6215,7207,8199,9007,9999,10807,11799,12607,13598,14392,15383,16177,17164,17675,17675,17675,17675,21,20,0,0,0,0,0,0,0x1b4980,0x5bce00,0xae5400,0xeed880,0x1415e80,0x181e300,0x1d46900,0x214ed80,0x2677380,0x2b9f980,0x2fa7e00,0x34d0400,0x38d8880,0x3e00e80,0x4209300,0x4730e80,0x4b30000,0x5057b80,0x5456d00,0x597be80,0x5c19f00,0x5c19f00,0x5c19f00,0x5c19f00,0x5c19f00,0x5c19f00,0x5c19f00,0x5c19f00,0x5c19f00,0x5c19f00,0x5c19f00,0x5c19f00,8,816,1808,2616,3608,4416,5408,6400,7208,8200,9008,10000,10808,11800,12608,18433,14393,18448,16178,18467,18467,18467,18467,18467,20,19,0,0,0,0,0,0,0x1b5400,0x5bd880,0xae5e80,0xeee300,0x1416900,0x181ed80,0x1d47380,0x226f980,0x2677e00,0x2ba0400,0x2fa8880,0x34d0e80,0x38d9300,0x3e01900,0x4209d80,0x61500,0x4b30a80,0x6b280,0x5457780,0x77a00,0x77a00,0x77a00,0x77a00,0x77a00,0x77a00,0x77a00,0x77a00,0x77a00,0x77a00,0x77a00,0x77a00,0x77a00,9,817,1809,2617,3609,4417,5409,6401,7209,8201,9009,10001,10809,11801,12609,13599,14394,15384,16179,18468,18468,18468,18468,18468,20,19,0,0,0,0,0,0,0x1b5e80,0x5be300,0xae6900,0xeeed80,0x1417380,0x181f800,0x1d47e00,0x2270400,0x2678880,0x2ba0e80,0x2fa9300,0x34d1900,0x38d9d80,0x3e02380,0x420a800,0x4731900,0x4b31500,0x5058600,0x5458200,0x78480,0x78480,0x78480,0x78480,0x78480,0x78480,0x78480,0x78480,0x78480,0x78480,0x78480,0x78480,0x78480,10,818,1810,2618,3610,4418,5410,6402,7210,8202,9010,10002,10810,11802,12610,13600,14395,15385,16180,17165,17676,18217,18217,18217,22,21,0,0,0,0,0,0,0x1b6900,0x5bed80,0xae7380,0xeef800,0x1417e00,0x1820280,0x1d48880,0x2270e80,0x2679300,0x2ba1900,0x2fa9d80,0x34d2380,0x38da800,0x3e02e00,0x420b280,0x4732380,0x4b31f80,0x5059080,0x5458c80,0x597c900,0x5c1a980,0x5ecc500,0x5ecc500,0x5ecc500,0x5ecc500,0x5ecc500,0x5ecc500,0x5ecc500,0x5ecc500,0x5ecc500,0x5ecc500,0x5ecc500,11,819,1811,2619,3611,4419,5411,6403,7211,8203,9011,10003,10811,11803,12611,13601,14396,15386,16181,17166,17677,17677,17677,17677,21,20,0,0,0,0,0,0,0x1b7380,0x5bf800,0xae7e00,0xef0280,0x1418880,0x1820d00,0x1d49300,0x2271900,0x2679d80,0x2ba2380,0x2faa800,0x34d2e00,0x38db280,0x3e03880,0x420bd00,0x4732e00,0x4b32a00,0x5059b00,0x5459700,0x597d380,0x5c1b400,0x5c1b400,0x5c1b400,0x5c1b400,0x5c1b400,0x5c1b400,0x5c1b400,0x5c1b400,0x5c1b400,0x5c1b400,0x5c1b400,0x5c1b400,12,820,1812,2620,3612,4420,5412,6404,7212,8204,9012,10004,10812,11804,12612,13602,14397,15387,16182,18469,17678,17678,17678,17678,21,20,0,0,0,0,0,0,0x1b7e00,0x5c0280,0xae8880,0xef0d00,0x1419300,0x1821780,0x1d49d80,0x2272380,0x267a800,0x2ba2e00,0x2fab280,0x34d3880,0x38dbd00,0x3e04300,0x420c780,0x4733880,0x4b33480,0x505a580,0x545a180,0x78f00,0x5c1be80,0x5c1be80,0x5c1be80,0x5c1be80,0x5c1be80,0x5c1be80,0x5c1be80,0x5c1be80,0x5c1be80,0x5c1be80,0x5c1be80,0x5c1be80,13,821,1813,2621,3613,4421,5413,6405,7213,8205,9013,10005,10813,11805,12613,13603,14398,15388,16183,18470,18470,18470,18470,18470,20,19,0,0,0,0,0,0,0x1b8880,0x5c0d00,0xae9300,0xef1780,0x1419d80,0x1822200,0x1d4a800,0x2272e00,0x267b280,0x2ba3880,0x2fabd00,0x34d4300,0x38dc780,0x3e04d80,0x420d200,0x4734300,0x4b33f00,0x505b000,0x545ac00,0x79980,0x79980,0x79980,0x79980,0x79980,0x79980,0x79980,0x79980,0x79980,0x79980,0x79980,0x79980,0x79980,14,822,1814,2622,3614,4422,5414,6406,7214,8206,9014,10006,10814,11806,12614,13604,14399,15389,16184,18471,17679,17679,17679,17679,21,20,0,0,0,0,0,0,0x1b9300,0x5c1780,0xae9d80,0xef2200,0x141a800,0x1822c80,0x1d4b280,0x2273880,0x267bd00,0x2ba4300,0x2fac780,0x34d4d80,0x38dd200,0x3e05800,0x420dc80,0x4734d80,0x4b34980,0x505ba80,0x545b680,0x7a400,0x5c1c900,0x5c1c900,0x5c1c900,0x5c1c900,0x5c1c900,0x5c1c900,0x5c1c900,0x5c1c900,0x5c1c900,0x5c1c900,0x5c1c900,0x5c1c900,15,823,1815,2623,3615,4423,5415,6407,7215,8207,9015,10007,10815,11807,12615,13605,14400,15390,16185,17167,17680,17680,17680,17680,21,20,0,0,0,0,0,0,0x1b9d80,0x5c2200,0xaea800,0xef2c80,0x141b280,0x1823700,0x1d4bd00,0x2274300,0x267c780,0x2ba4d80,0x2fad200,0x34d5800,0x38ddc80,0x3e06280,0x420e700,0x4735800,0x4b35400,0x505c500,0x545c100,0x597de00,0x5c1d380,0x5c1d380,0x5c1d380,0x5c1d380,0x5c1d380,0x5c1d380,0x5c1d380,0x5c1d380,0x5c1d380,0x5c1d380,0x5c1d380,0x5c1d380,16,824,1816,2624,3616,4608,5416,6408,7216,8208,9016,10008,10816,11808,12800,18434,14401,18449,16186,18472,18472,18472,18472,18472,20,19,0,0,0,0,0,0,0x1ba800,0x5c2c80,0xaeb280,0xef3700,0x141bd00,0x1944300,0x1d4c780,0x2274d80,0x267d200,0x2ba5800,0x2fadc80,0x34d6280,0x38de700,0x3e06d00,0x432f300,0x61f80,0x4b35e80,0x6bd00,0x545cb80,0x7ae80,0x7ae80,0x7ae80,0x7ae80,0x7ae80,0x7ae80,0x7ae80,0x7ae80,0x7ae80,0x7ae80,0x7ae80,0x7ae80,0x7ae80,17,825,1817,2625,3617,4609,5417,6409,7217,8209,9017,10009,10817,11809,12801,13606,14402,15391,16187,18473,18473,18473,18473,18473,20,19,0,0,0,0,0,0,0x1bb280,0x5c3700,0xaebd00,0xef4180,0x141c780,0x1944d80,0x1d4d200,0x2275800,0x267dc80,0x2ba6280,0x2fae700,0x34d6d00,0x38df180,0x3e07780,0x432fd80,0x4736280,0x4b36900,0x505cf80,0x545d600,0x7b900,0x7b900,0x7b900,0x7b900,0x7b900,0x7b900,0x7b900,0x7b900,0x7b900,0x7b900,0x7b900,0x7b900,0x7b900,18,826,1818,2626,3618,4610,5418,6410,7218,8210,9018,10010,10818,11810,12802,13607,14403,15392,16188,17168,17681,18218,18218,18218,22,21,0,0,0,0,0,0,0x1bbd00,0x5c4180,0xaec780,0xef4c00,0x141d200,0x1945800,0x1d4dc80,0x2276280,0x267e700,0x2ba6d00,0x2faf180,0x34d7780,0x38dfc00,0x3e08200,0x4330800,0x4736d00,0x4b37380,0x505da00,0x545e080,0x597e880,0x5c1de00,0x5eccf80,0x5eccf80,0x5eccf80,0x5eccf80,0x5eccf80,0x5eccf80,0x5eccf80,0x5eccf80,0x5eccf80,0x5eccf80,0x5eccf80,19,827,1819,2627,3619,4611,5419,6411,7219,8211,9019,10011,10819,11811,12803,13608,14404,15393,16189,17169,17682,17682,17682,17682,21,20,0,0,0,0,0,0,0x1bc780,0x5c4c00,0xaed200,0xef5680,0x141dc80,0x1946280,0x1d4e700,0x2276d00,0x267f180,0x2ba7780,0x2fafc00,0x34d8200,0x38e0680,0x3e08c80,0x4331280,0x4737780,0x4b37e00,0x505e480,0x545eb00,0x597f300,0x5c1e880,0x5c1e880,0x5c1e880,0x5c1e880,0x5c1e880,0x5c1e880,0x5c1e880,0x5c1e880,0x5c1e880,0x5c1e880,0x5c1e880,0x5c1e880,20,828,1820,2628,3620,4612,5420,6412,7220,8212,9020,10012,10820,11812,12804,13609,14405,15394,16190,18474,17683,17683,17683,17683,21,20,0,0,0,0,0,0,0x1bd200,0x5c5680,0xaedc80,0xef6100,0x141e700,0x1946d00,0x1d4f180,0x2277780,0x267fc00,0x2ba8200,0x2fb0680,0x34d8c80,0x38e1100,0x3e09700,0x4331d00,0x4738200,0x4b38880,0x505ef00,0x545f580,0x7c380,0x5c1f300,0x5c1f300,0x5c1f300,0x5c1f300,0x5c1f300,0x5c1f300,0x5c1f300,0x5c1f300,0x5c1f300,0x5c1f300,0x5c1f300,0x5c1f300,21,829,1821,2629,3621,4613,5421,6413,7221,8213,9021,10013,10821,11813,12805,13610,14406,15395,16191,18475,18475,18475,18475,18475,20,19,0,0,0,0,0,0,0x1bdc80,0x5c6100,0xaee700,0xef6b80,0x141f180,0x1947780,0x1d4fc00,0x2278200,0x2680680,0x2ba8c80,0x2fb1100,0x34d9700,0x38e1b80,0x3e0a180,0x4332780,0x4738c80,0x4b39300,0x505f980,0x5460000,0x7ce00,0x7ce00,0x7ce00,0x7ce00,0x7ce00,0x7ce00,0x7ce00,0x7ce00,0x7ce00,0x7ce00,0x7ce00,0x7ce00,0x7ce00,22,830,1822,2630,3622,4614,5422,6414,7222,8214,9022,10014,10822,11814,12806,13611,14407,15396,16192,18476,17684,17684,17684,17684,21,20,0,0,0,0,0,0,0x1be700,0x5c6b80,0xaef180,0xef7600,0x141fc00,0x1948200,0x1d50680,0x2278c80,0x2681100,0x2ba9700,0x2fb1b80,0x34da180,0x38e2600,0x3e0ac00,0x4333200,0x4739700,0x4b39d80,0x5060400,0x5460a80,0x7d880,0x5c1fd80,0x5c1fd80,0x5c1fd80,0x5c1fd80,0x5c1fd80,0x5c1fd80,0x5c1fd80,0x5c1fd80,0x5c1fd80,0x5c1fd80,0x5c1fd80,0x5c1fd80,23,831,1823,2631,3623,4615,5423,6415,7223,8215,9023,10015,10823,11815,12807,13612,14592,15397,16193,17170,17685,17685,17685,17685,21,20,0,0,0,0,0,0,0x1bf180,0x5c7600,0xaefc00,0xef8080,0x1420680,0x1948c80,0x1d51100,0x2279700,0x2681b80,0x2baa180,0x2fb2600,0x34dac00,0x38e3080,0x3e0b680,0x4333c80,0x473a180,0x4c5a980,0x5060e80,0x5461500,0x597fd80,0x5c20800,0x5c20800,0x5c20800,0x5c20800,0x5c20800,0x5c20800,0x5c20800,0x5c20800,0x5c20800,0x5c20800,0x5c20800,0x5c20800,24,832,1824,2816,3624,4616,5424,6416,7224,8216,9024,10016,11008,11816,12808,18435,14593,18450,16194,18477,18477,18477,18477,18477,20,19,0,0,0,0,0,0,0x1bfc00,0x5c8080,0xaf0680,0x1018c80,0x1421100,0x1949700,0x1d51b80,0x227a180,0x2682600,0x2baac00,0x2fb3080,0x34db680,0x3a03c80,0x3e0c100,0x4334700,0x62a00,0x4c5b400,0x6c780,0x5461f80,0x7e300,0x7e300,0x7e300,0x7e300,0x7e300,0x7e300,0x7e300,0x7e300,0x7e300,0x7e300,0x7e300,0x7e300,0x7e300,25,833,1825,2817,3625,4617,5425,6417,7225,8217,9025,10017,11009,11817,12809,13613,14594,15398,16195,18478,18478,18478,18478,18478,20,19,0,0,0,0,0,0,0x1c0680,0x5c8b00,0xaf1100,0x1019700,0x1421b80,0x194a180,0x1d52600,0x227ac00,0x2683080,0x2bab680,0x2fb3b00,0x34dc100,0x3a04700,0x3e0cb80,0x4335180,0x473ac00,0x4c5be80,0x5061900,0x5462a00,0x7ed80,0x7ed80,0x7ed80,0x7ed80,0x7ed80,0x7ed80,0x7ed80,0x7ed80,0x7ed80,0x7ed80,0x7ed80,0x7ed80,0x7ed80,26,834,1826,2818,3626,4618,5426,6418,7226,8218,9026,10018,11010,11818,12810,13614,14595,15399,16196,17171,17686,18219,18219,18219,22,21,0,0,0,0,0,0,0x1c1100,0x5c9580,0xaf1b80,0x101a180,0x1422600,0x194ac00,0x1d53080,0x227b680,0x2683b00,0x2bac100,0x2fb4580,0x34dcb80,0x3a05180,0x3e0d600,0x4335c00,0x473b680,0x4c5c900,0x5062380,0x5463480,0x5980800,0x5c21280,0x5ecda00,0x5ecda00,0x5ecda00,0x5ecda00,0x5ecda00,0x5ecda00,0x5ecda00,0x5ecda00,0x5ecda00,0x5ecda00,0x5ecda00,27,835,1827,2819,3627,4619,5427,6419,7227,8219,9027,10019,11011,11819,12811,13615,14596,15400,16197,17172,17687,17687,17687,17687,21,20,0,0,0,0,0,0,0x1c1b80,0x5ca000,0xaf2600,0x101ac00,0x1423080,0x194b680,0x1d53b00,0x227c100,0x2684580,0x2bacb80,0x2fb5000,0x34dd600,0x3a05c00,0x3e0e080,0x4336680,0x473c100,0x4c5d380,0x5062e00,0x5463f00,0x5981280,0x5c21d00,0x5c21d00,0x5c21d00,0x5c21d00,0x5c21d00,0x5c21d00,0x5c21d00,0x5c21d00,0x5c21d00,0x5c21d00,0x5c21d00,0x5c21d00,28,836,1828,2820,3628,4620,5428,6420,7228,8220,9028,10020,11012,11820,12812,13616,14597,15401,16198,18479,17688,17688,17688,17688,21,20,0,0,0,0,0,0,0x1c2600,0x5caa80,0xaf3080,0x101b680,0x1423b00,0x194c100,0x1d54580,0x227cb80,0x2685000,0x2bad600,0x2fb5a80,0x34de080,0x3a06680,0x3e0eb00,0x4337100,0x473cb80,0x4c5de00,0x5063880,0x5464980,0x7f800,0x5c22780,0x5c22780,0x5c22780,0x5c22780,0x5c22780,0x5c22780,0x5c22780,0x5c22780,0x5c22780,0x5c22780,0x5c22780,0x5c22780,29,837,1829,2821,3629,4621,5429,6421,7229,8221,9029,10021,11013,11821,12813,13617,14598,15402,16199,18480,18480,18480,18480,18480,20,19,0,0,0,0,0,0,0x1c3080,0x5cb500,0xaf3b00,0x101c100,0x1424580,0x194cb80,0x1d55000,0x227d600,0x2685a80,0x2bae080,0x2fb6500,0x34deb00,0x3a07100,0x3e0f580,0x4337b80,0x473d600,0x4c5e880,0x5064300,0x5465400,0x80280,0x80280,0x80280,0x80280,0x80280,0x80280,0x80280,0x80280,0x80280,0x80280,0x80280,0x80280,0x80280,30,838,1830,2822,3630,4622,5430,6422,7230,8222,9030,10022,11014,11822,12814,13618,14599,15403,16384,18481,17689,17689,17689,17689,21,20,0,0,0,0,0,0,0x1c3b00,0x5cbf80,0xaf4580,0x101cb80,0x1425000,0x194d600,0x1d55a80,0x227e080,0x2686500,0x2baeb00,0x2fb6f80,0x34df580,0x3a07b80,0x3e10000,0x4338600,0x473e080,0x4c5f300,0x5064d80,0x5586000,0x80d00,0x5c23200,0x5c23200,0x5c23200,0x5c23200,0x5c23200,0x5c23200,0x5c23200,0x5c23200,0x5c23200,0x5c23200,0x5c23200,0x5c23200,31,839,1831,2823,3631,4623,5431,6423,7231,8223,9031,10023,11015,11823,12815,13619,14600,15404,16385,17173,17690,17690,17690,17690,21,20,0,0,0,0,0,0,0x1c4580,0x5cca00,0xaf5000,0x101d600,0x1425a80,0x194e080,0x1d56500,0x227eb00,0x2686f80,0x2baf580,0x2fb7a00,0x34e0000,0x3a08600,0x3e10a80,0x4339080,0x473eb00,0x4c5fd80,0x5065800,0x5586a80,0x5981d00,0x5c23c80,0x5c23c80,0x5c23c80,0x5c23c80,0x5c23c80,0x5c23c80,0x5c23c80,0x5c23c80,0x5c23c80,0x5c23c80,0x5c23c80,0x5c23c80,32,1024,1832,2824,3632,4624,5432,6424,7232,8224,9216,10024,11016,11824,12816,18436,14601,18451,16386,18482,18482,18482,18482,18482,20,19,0,0,0,0,0,0,0x1c5000,0x6ed600,0xaf5a80,0x101e080,0x1426500,0x194eb00,0x1d56f80,0x227f580,0x2687a00,0x2bb0000,0x30d8600,0x34e0a80,0x3a09080,0x3e11500,0x4339b00,0x63480,0x4c60800,0x6d200,0x5587500,0x81780,0x81780,0x81780,0x81780,0x81780,0x81780,0x81780,0x81780,0x81780,0x81780,0x81780,0x81780,0x81780,33,1025,1833,2825,3633,4625,5433,6425,7233,8225,9217,10025,11017,11825,12817,13620,14602,15405,16387,18483,18483,18483,18483,18483,20,19,0,0,0,0,0,0,0x1c5a80,0x6ee080,0xaf6500,0x101eb00,0x1426f80,0x194f580,0x1d57a00,0x2280000,0x2688480,0x2bb0a80,0x30d9080,0x34e1500,0x3a09b00,0x3e11f80,0x433a580,0x473f580,0x4c61280,0x5066280,0x5587f80,0x82200,0x82200,0x82200,0x82200,0x82200,0x82200,0x82200,0x82200,0x82200,0x82200,0x82200,0x82200,0x82200,34,1026,1834,2826,3634,4626,5434,6426,7234,8226,9218,10026,11018,11826,12818,13621,14603,15406,16388,17174,17691,18220,18220,18220,22,21,0,0,0,0,0,0,0x1c6500,0x6eeb00,0xaf6f80,0x101f580,0x1427a00,0x1950000,0x1d58480,0x2280a80,0x2688f00,0x2bb1500,0x30d9b00,0x34e1f80,0x3a0a580,0x3e12a00,0x433b000,0x4740000,0x4c61d00,0x5066d00,0x5588a00,0x5982780,0x5c24700,0x5ece480,0x5ece480,0x5ece480,0x5ece480,0x5ece480,0x5ece480,0x5ece480,0x5ece480,0x5ece480,0x5ece480,0x5ece480,35,1027,1835,2827,3635,4627,5435,6427,7235,8227,9219,10027,11019,11827,12819,13622,14604,15407,16389,17175,17692,17692,17692,17692,21,20,0,0,0,0,0,0,0x1c6f80,0x6ef580,0xaf7a00,0x1020000,0x1428480,0x1950a80,0x1d58f00,0x2281500,0x2689980,0x2bb1f80,0x30da580,0x34e2a00,0x3a0b000,0x3e13480,0x433ba80,0x4740a80,0x4c62780,0x5067780,0x5589480,0x5983200,0x5c25180,0x5c25180,0x5c25180,0x5c25180,0x5c25180,0x5c25180,0x5c25180,0x5c25180,0x5c25180,0x5c25180,0x5c25180,0x5c25180,36,1028,1836,2828,3636,4628,5436,6428,7236,8228,9220,10028,11020,11828,12820,13623,14605,15408,16390,17176,17693,17693,17693,17693,21,20,0,0,0,0,0,0,0x1c7a00,0x6f0000,0xaf8480,0x1020a80,0x1428f00,0x1951500,0x1d59980,0x2281f80,0x268a400,0x2bb2a00,0x30db000,0x34e3480,0x3a0ba80,0x3e13f00,0x433c500,0x4741500,0x4c63200,0x5068200,0x5589f00,0x5983c80,0x5c25c00,0x5c25c00,0x5c25c00,0x5c25c00,0x5c25c00,0x5c25c00,0x5c25c00,0x5c25c00,0x5c25c00,0x5c25c00,0x5c25c00,0x5c25c00,37,1029,1837,2829,3637,4629,5437,6429,7237,8229,9221,10029,11021,11829,12821,13624,14606,15409,16391,18484,18484,18484,18484,18484,20,19,0,0,0,0,0,0,0x1c8480,0x6f0a80,0xaf8f00,0x1021500,0x1429980,0x1951f80,0x1d5a400,0x2282a00,0x268ae80,0x2bb3480,0x30dba80,0x34e3f00,0x3a0c500,0x3e14980,0x433cf80,0x4741f80,0x4c63c80,0x5068c80,0x558a980,0x82c80,0x82c80,0x82c80,0x82c80,0x82c80,0x82c80,0x82c80,0x82c80,0x82c80,0x82c80,0x82c80,0x82c80,0x82c80,38,1030,1838,2830,3638,4630,5438,6430,7238,8230,9222,10030,11022,11830,12822,13625,14607,15410,16392,18485,17694,17694,17694,17694,21,20,0,0,0,0,0,0,0x1c8f00,0x6f1500,0xaf9980,0x1021f80,0x142a400,0x1952a00,0x1d5ae80,0x2283480,0x268b900,0x2bb3f00,0x30dc500,0x34e4980,0x3a0cf80,0x3e15400,0x433da00,0x4742a00,0x4c64700,0x5069700,0x558b400,0x83700,0x5c26680,0x5c26680,0x5c26680,0x5c26680,0x5c26680,0x5c26680,0x5c26680,0x5c26680,0x5c26680,0x5c26680,0x5c26680,0x5c26680,39,1031,1839,2831,3639,4631,5439,6431,7239,8231,9223,10031,11023,11831,12823,13626,14608,15411,16393,17177,17695,17695,17695,17695,21,20,0,0,0,0,0,0,0x1c9980,0x6f1f80,0xafa400,0x1022a00,0x142ae80,0x1953480,0x1d5b900,0x2283f00,0x268c380,0x2bb4980,0x30dcf80,0x34e5400,0x3a0da00,0x3e15e80,0x433e480,0x4743480,0x4c65180,0x506a180,0x558be80,0x5984700,0x5c27100,0x5c27100,0x5c27100,0x5c27100,0x5c27100,0x5c27100,0x5c27100,0x5c27100,0x5c27100,0x5c27100,0x5c27100,0x5c27100,40,1032,1840,2832,3640,4632,5440,6432,7424,8232,9224,10032,11024,11832,12824,18437,14609,18452,16394,18486,18486,18486,18486,18486,20,19,0,0,0,0,0,0,0x1ca400,0x6f2a00,0xafae80,0x1023480,0x142b900,0x1953f00,0x1d5c380,0x2284980,0x27acf80,0x2bb5400,0x30dda00,0x34e5e80,0x3a0e480,0x3e16900,0x433ef00,0x63f00,0x4c65c00,0x6dc80,0x558c900,0x84180,0x84180,0x84180,0x84180,0x84180,0x84180,0x84180,0x84180,0x84180,0x84180,0x84180,0x84180,0x84180,41,1033,1841,2833,3641,4633,5441,6433,7425,8233,9225,10033,11025,11833,12825,13627,14610,15412,16395,18487,18487,18487,18487,18487,20,19,0,0,0,0,0,0,0x1cae80,0x6f3480,0xafb900,0x1023f00,0x142c380,0x1954980,0x1d5ce00,0x2285400,0x27ada00,0x2bb5e80,0x30de480,0x34e6900,0x3a0ef00,0x3e17380,0x433f980,0x4743f00,0x4c66680,0x506ac00,0x558d380,0x84c00,0x84c00,0x84c00,0x84c00,0x84c00,0x84c00,0x84c00,0x84c00,0x84c00,0x84c00,0x84c00,0x84c00,0x84c00,42,1034,1842,2834,3642,4634,5442,6434,7426,8234,9226,10034,11026,11834,12826,13628,14611,15413,16396,17178,17696,18221,18221,18221,22,21,0,0,0,0,0,0,0x1cb900,0x6f3f00,0xafc380,0x1024980,0x142ce00,0x1955400,0x1d5d880,0x2285e80,0x27ae480,0x2bb6900,0x30def00,0x34e7380,0x3a0f980,0x3e17e00,0x4340400,0x4744980,0x4c67100,0x506b680,0x558de00,0x5985180,0x5c27b80,0x5ecef00,0x5ecef00,0x5ecef00,0x5ecef00,0x5ecef00,0x5ecef00,0x5ecef00,0x5ecef00,0x5ecef00,0x5ecef00,0x5ecef00,43,1035,1843,2835,3643,4635,5443,6435,7427,8235,9227,10035,11027,11835,12827,13629,14612,15414,16397,17179,17697,17697,17697,17697,21,20,0,0,0,0,0,0,0x1cc380,0x6f4980,0xafce00,0x1025400,0x142d880,0x1955e80,0x1d5e300,0x2286900,0x27aef00,0x2bb7380,0x30df980,0x34e7e00,0x3a10400,0x3e18880,0x4340e80,0x4745400,0x4c67b80,0x506c100,0x558e880,0x5985c00,0x5c28600,0x5c28600,0x5c28600,0x5c28600,0x5c28600,0x5c28600,0x5c28600,0x5c28600,0x5c28600,0x5c28600,0x5c28600,0x5c28600,44,1036,1844,2836,3644,4636,5444,6436,7428,8236,9228,10036,11028,11836,12828,13630,14613,15415,16398,17180,17698,17698,17698,17698,21,20,0,0,0,0,0,0,0x1cce00,0x6f5400,0xafd880,0x1025e80,0x142e300,0x1956900,0x1d5ed80,0x2287380,0x27af980,0x2bb7e00,0x30e0400,0x34e8880,0x3a10e80,0x3e19300,0x4341900,0x4745e80,0x4c68600,0x506cb80,0x558f300,0x5986680,0x5c29080,0x5c29080,0x5c29080,0x5c29080,0x5c29080,0x5c29080,0x5c29080,0x5c29080,0x5c29080,0x5c29080,0x5c29080,0x5c29080,45,1037,1845,2837,3645,4637,5445,6437,7429,8237,9229,10037,11029,11837,12829,13631,14614,15416,16399,18488,18488,18488,18488,18488,20,19,0,0,0,0,0,0,0x1cd880,0x6f5e80,0xafe300,0x1026900,0x142ed80,0x1957380,0x1d5f800,0x2287e00,0x27b0400,0x2bb8880,0x30e0e80,0x34e9300,0x3a11900,0x3e19d80,0x4342380,0x4746900,0x4c69080,0x506d600,0x558fd80,0x85680,0x85680,0x85680,0x85680,0x85680,0x85680,0x85680,0x85680,0x85680,0x85680,0x85680,0x85680,0x85680,46,1038,1846,2838,3646,4638,5446,6438,7430,8238,9230,10038,11030,11838,12830,13632,14615,15417,16400,18489,17699,17699,17699,17699,21,20,0,0,0,0,0,0,0x1ce300,0x6f6900,0xafed80,0x1027380,0x142f800,0x1957e00,0x1d60280,0x2288880,0x27b0e80,0x2bb9300,0x30e1900,0x34e9d80,0x3a12380,0x3e1a800,0x4342e00,0x4747380,0x4c69b00,0x506e080,0x5590800,0x86100,0x5c29b00,0x5c29b00,0x5c29b00,0x5c29b00,0x5c29b00,0x5c29b00,0x5c29b00,0x5c29b00,0x5c29b00,0x5c29b00,0x5c29b00,0x5c29b00,47,1039,1847,2839,3647,4639,5447,6439,7431,8239,9231,10039,11031,11839,12831,13633,14616,15418,16401,17181,17700,17700,17700,17700,21,20,0,0,0,0,0,0,0x1ced80,0x6f7380,0xaff800,0x1027e00,0x1430280,0x1958880,0x1d60d00,0x2289300,0x27b1900,0x2bb9d80,0x30e2380,0x34ea800,0x3a12e00,0x3e1b280,0x4343880,0x4747e00,0x4c6a580,0x506eb00,0x5591280,0x5987100,0x5c2a580,0x5c2a580,0x5c2a580,0x5c2a580,0x5c2a580,0x5c2a580,0x5c2a580,0x5c2a580,0x5c2a580,0x5c2a580,0x5c2a580,0x5c2a580,48,1040,1848,2840,3648,4640,5632,6440,7432,8240,9232,10040,11032,11840,12832,18438,14617,18453,16402,18490,18490,18490,18490,18490,20,19,0,0,0,0,0,0,0x1cf800,0x6f7e00,0xb00280,0x1028880,0x1430d00,0x1959300,0x1e81900,0x2289d80,0x27b2380,0x2bba800,0x30e2e00,0x34eb280,0x3a13880,0x3e1bd00,0x4344300,0x64980,0x4c6b000,0x6e700,0x5591d00,0x86b80,0x86b80,0x86b80,0x86b80,0x86b80,0x86b80,0x86b80,0x86b80,0x86b80,0x86b80,0x86b80,0x86b80,0x86b80,49,1041,1849,2841,3649,4641,5633,6441,7433,8241,9233,10041,11033,11841,12833,13634,14618,15419,16403,18491,18491,18491,18491,18491,20,19,0,0,0,0,0,0,0x1d0280,0x6f8880,0xb00d00,0x1029300,0x1431780,0x1959d80,0x1e82380,0x228a800,0x27b2e00,0x2bbb280,0x30e3880,0x34ebd00,0x3a14300,0x3e1c780,0x4344d80,0x4748880,0x4c6ba80,0x506f580,0x5592780,0x87600,0x87600,0x87600,0x87600,0x87600,0x87600,0x87600,0x87600,0x87600,0x87600,0x87600,0x87600,0x87600,50,1042,1850,2842,3650,4642,5634,6442,7434,8242,9234,10042,11034,11842,12834,13635,14619,15420,16404,17182,17701,18222,18222,18222,22,21,0,0,0,0,0,0,0x1d0d00,0x6f9300,0xb01780,0x1029d80,0x1432200,0x195a800,0x1e82e00,0x228b280,0x27b3880,0x2bbbd00,0x30e4300,0x34ec780,0x3a14d80,0x3e1d200,0x4345800,0x4749300,0x4c6c500,0x5070000,0x5593200,0x5987b80,0x5c2b000,0x5ecf980,0x5ecf980,0x5ecf980,0x5ecf980,0x5ecf980,0x5ecf980,0x5ecf980,0x5ecf980,0x5ecf980,0x5ecf980,0x5ecf980,51,1043,1851,2843,3651,4643,5635,6443,7435,8243,9235,10043,11035,11843,12835,13636,14620,15421,16405,17183,17702,17702,17702,17702,21,20,0,0,0,0,0,0,0x1d1780,0x6f9d80,0xb02200,0x102a800,0x1432c80,0x195b280,0x1e83880,0x228bd00,0x27b4300,0x2bbc780,0x30e4d80,0x34ed200,0x3a15800,0x3e1dc80,0x4346280,0x4749d80,0x4c6cf80,0x5070a80,0x5593c80,0x5988600,0x5c2ba80,0x5c2ba80,0x5c2ba80,0x5c2ba80,0x5c2ba80,0x5c2ba80,0x5c2ba80,0x5c2ba80,0x5c2ba80,0x5c2ba80,0x5c2ba80,0x5c2ba80,52,1044,1852,2844,3652,4644,5636,6444,7436,8244,9236,10044,11036,11844,12836,13637,14621,15422,16406,17184,17703,17703,17703,17703,21,20,0,0,0,0,0,0,0x1d2200,0x6fa800,0xb02c80,0x102b280,0x1433700,0x195bd00,0x1e84300,0x228c780,0x27b4d80,0x2bbd200,0x30e5800,0x34edc80,0x3a16280,0x3e1e700,0x4346d00,0x474a800,0x4c6da00,0x5071500,0x5594700,0x5989080,0x5c2c500,0x5c2c500,0x5c2c500,0x5c2c500,0x5c2c500,0x5c2c500,0x5c2c500,0x5c2c500,0x5c2c500,0x5c2c500,0x5c2c500,0x5c2c500,53,1045,1853,2845,3653,4645,5637,6445,7437,8245,9237,10045,11037,11845,12837,13638,14622,15423,16407,18492,18492,18492,18492,18492,20,19,0,0,0,0,0,0,0x1d2c80,0x6fb280,0xb03700,0x102bd00,0x1434180,0x195c780,0x1e84d80,0x228d200,0x27b5800,0x2bbdc80,0x30e6280,0x34ee700,0x3a16d00,0x3e1f180,0x4347780,0x474b280,0x4c6e480,0x5071f80,0x5595180,0x88080,0x88080,0x88080,0x88080,0x88080,0x88080,0x88080,0x88080,0x88080,0x88080,0x88080,0x88080,0x88080,54,1046,1854,2846,3654,4646,5638,6446,7438,8246,9238,10046,11038,11846,12838,13639,14623,15424,16408,18493,17704,17704,17704,17704,21,20,0,0,0,0,0,0,0x1d3700,0x6fbd00,0xb04180,0x102c780,0x1434c00,0x195d200,0x1e85800,0x228dc80,0x27b6280,0x2bbe700,0x30e6d00,0x34ef180,0x3a17780,0x3e1fc00,0x4348200,0x474bd00,0x4c6ef00,0x5072a00,0x5595c00,0x88b00,0x5c2cf80,0x5c2cf80,0x5c2cf80,0x5c2cf80,0x5c2cf80,0x5c2cf80,0x5c2cf80,0x5c2cf80,0x5c2cf80,0x5c2cf80,0x5c2cf80,0x5c2cf80,55,1047,1855,2847,3655,4647,5639,6447,7439,8247,9239,10047,11039,11847,12839,13824,14624,15425,16409,17185,17705,17705,17705,17705,21,20,0,0,0,0,0,0,0x1d4180,0x6fc780,0xb04c00,0x102d200,0x1435680,0x195dc80,0x1e86280,0x228e700,0x27b6d00,0x2bbf180,0x30e7780,0x34efc00,0x3a18200,0x3e20680,0x4348c80,0x486c900,0x4c6f980,0x5073480,0x5596680,0x5989b00,0x5c2da00,0x5c2da00,0x5c2da00,0x5c2da00,0x5c2da00,0x5c2da00,0x5c2da00,0x5c2da00,0x5c2da00,0x5c2da00,0x5c2da00,0x5c2da00,56,1048,1856,2848,3840,4648,5640,6448,7440,8248,9240,10048,11040,12032,12840,18439,14625,18454,16410,18494,18494,18494,18494,18494,20,19,0,0,0,0,0,0,0x1d4c00,0x6fd200,0xb05680,0x102dc80,0x1556280,0x195e700,0x1e86d00,0x228f180,0x27b7780,0x2bbfc00,0x30e8200,0x34f0680,0x3a18c80,0x3f41280,0x4349700,0x65400,0x4c70400,0x6f180,0x5597100,0x89580,0x89580,0x89580,0x89580,0x89580,0x89580,0x89580,0x89580,0x89580,0x89580,0x89580,0x89580,0x89580,57,1049,1857,2849,3841,4649,5641,6449,7441,8249,9241,10049,11041,12033,12841,13825,14626,15426,16411,18495,18495,18495,18495,18495,20,19,0,0,0,0,0,0,0x1d5680,0x6fdc80,0xb06100,0x102e700,0x1556d00,0x195f180,0x1e87780,0x228fc00,0x27b8200,0x2bc0680,0x30e8c80,0x34f1100,0x3a19700,0x3f41d00,0x434a180,0x486d380,0x4c70e80,0x5073f00,0x5597b80,0x8a000,0x8a000,0x8a000,0x8a000,0x8a000,0x8a000,0x8a000,0x8a000,0x8a000,0x8a000,0x8a000,0x8a000,0x8a000,58,1050,1858,2850,3842,4650,5642,6450,7442,8250,9242,10050,11042,12034,12842,13826,14627,15427,16412,17186,17706,18223,18223,18223,22,21,0,0,0,0,0,0,0x1d6100,0x6fe700,0xb06b80,0x102f180,0x1557780,0x195fc00,0x1e88200,0x2290680,0x27b8c80,0x2bc1100,0x30e9700,0x34f1b80,0x3a1a180,0x3f42780,0x434ac00,0x486de00,0x4c71900,0x5074980,0x5598600,0x598a580,0x5c2e480,0x5ed0400,0x5ed0400,0x5ed0400,0x5ed0400,0x5ed0400,0x5ed0400,0x5ed0400,0x5ed0400,0x5ed0400,0x5ed0400,0x5ed0400,59,1051,1859,2851,3843,4651,5643,6451,7443,8251,9243,10051,11043,12035,12843,13827,14628,15428,16413,17187,17707,17707,17707,17707,21,20,0,0,0,0,0,0,0x1d6b80,0x6ff180,0xb07600,0x102fc00,0x1558200,0x1960680,0x1e88c80,0x2291100,0x27b9700,0x2bc1b80,0x30ea180,0x34f2600,0x3a1ac00,0x3f43200,0x434b680,0x486e880,0x4c72380,0x5075400,0x5599080,0x598b000,0x5c2ef00,0x5c2ef00,0x5c2ef00,0x5c2ef00,0x5c2ef00,0x5c2ef00,0x5c2ef00,0x5c2ef00,0x5c2ef00,0x5c2ef00,0x5c2ef00,0x5c2ef00,60,1052,1860,2852,3844,4652,5644,6452,7444,8252,9244,10052,11044,12036,12844,13828,14629,15429,16414,17188,17708,17708,17708,17708,21,20,0,0,0,0,0,0,0x1d7600,0x6ffc00,0xb08080,0x1030680,0x1558c80,0x1961100,0x1e89700,0x2291b80,0x27ba180,0x2bc2600,0x30eac00,0x34f3080,0x3a1b680,0x3f43c80,0x434c100,0x486f300,0x4c72e00,0x5075e80,0x5599b00,0x598ba80,0x5c2f980,0x5c2f980,0x5c2f980,0x5c2f980,0x5c2f980,0x5c2f980,0x5c2f980,0x5c2f980,0x5c2f980,0x5c2f980,0x5c2f980,0x5c2f980,61,1053,1861,2853,3845,4653,5645,6453,7445,8253,9245,10053,11045,12037,12845,13829,14630,15430,16415,18496,18496,18496,18496,18496,20,19,0,0,0,0,0,0,0x1d8080,0x700680,0xb08b00,0x1031100,0x1559700,0x1961b80,0x1e8a180,0x2292600,0x27bac00,0x2bc3080,0x30eb680,0x34f3b00,0x3a1c100,0x3f44700,0x434cb80,0x486fd80,0x4c73880,0x5076900,0x559a580,0x8aa80,0x8aa80,0x8aa80,0x8aa80,0x8aa80,0x8aa80,0x8aa80,0x8aa80,0x8aa80,0x8aa80,0x8aa80,0x8aa80,0x8aa80,62,1054,1862,2854,3846,4654,5646,6454,7446,8254,9246,10054,11046,12038,12846,13830,14631,15431,16416,18497,17709,17709,17709,17709,21,20,0,0,0,0,0,0,0x1d8b00,0x701100,0xb09580,0x1031b80,0x155a180,0x1962600,0x1e8ac00,0x2293080,0x27bb680,0x2bc3b00,0x30ec100,0x34f4580,0x3a1cb80,0x3f45180,0x434d600,0x4870800,0x4c74300,0x5077380,0x559b000,0x8b500,0x5c30400,0x5c30400,0x5c30400,0x5c30400,0x5c30400,0x5c30400,0x5c30400,0x5c30400,0x5c30400,0x5c30400,0x5c30400,0x5c30400,63,1055,1863,2855,3847,4655,5647,6455,7447,8255,9247,10055,11047,12039,12847,13831,14632,15616,16417,17189,17710,17710,17710,17710,21,20,0,0,0,0,0,0,0x1d9580,0x701b80,0xb0a000,0x1032600,0x155ac00,0x1963080,0x1e8b680,0x2293b00,0x27bc100,0x2bc4580,0x30ecb80,0x34f5000,0x3a1d600,0x3f45c00,0x434e080,0x4871280,0x4c74d80,0x5197f80,0x559ba80,0x598c500,0x5c30e80,0x5c30e80,0x5c30e80,0x5c30e80,0x5c30e80,0x5c30e80,0x5c30e80,0x5c30e80,0x5c30e80,0x5c30e80,0x5c30e80,0x5c30e80,64,1056,2048,2856,3848,4656,5648,6456,7448,8256,9248,10240,11048,12040,12848,18440,14633,18455,16418,18498,18498,18498,18498,18498,20,19,0,0,0,0,0,0,0x1da000,0x702600,0xc2ac00,0x1033080,0x155b680,0x1963b00,0x1e8c100,0x2294580,0x27bcb80,0x2bc5000,0x30ed600,0x3615c00,0x3a1e080,0x3f46680,0x434eb00,0x65e80,0x4c75800,0x6fc00,0x559c500,0x8bf80,0x8bf80,0x8bf80,0x8bf80,0x8bf80,0x8bf80,0x8bf80,0x8bf80,0x8bf80,0x8bf80,0x8bf80,0x8bf80,0x8bf80,65,1057,2049,2857,3849,4657,5649,6457,7449,8257,9249,10241,11049,12041,12849,13832,14634,15617,16419,18499,18499,18499,18499,18499,20,19,0,0,0,0,0,0,0x1daa80,0x703080,0xc2b680,0x1033b00,0x155c100,0x1964580,0x1e8cb80,0x2295000,0x27bd600,0x2bc5a80,0x30ee080,0x3616680,0x3a1eb00,0x3f47100,0x434f580,0x4871d00,0x4c76280,0x5198a00,0x559cf80,0x8ca00,0x8ca00,0x8ca00,0x8ca00,0x8ca00,0x8ca00,0x8ca00,0x8ca00,0x8ca00,0x8ca00,0x8ca00,0x8ca00,0x8ca00,66,1058,2050,2858,3850,4658,5650,6458,7450,8258,9250,10242,11050,12042,12850,13833,14635,15618,16420,17190,17711,18224,18224,18224,22,21,0,0,0,0,0,0,0x1db500,0x703b00,0xc2c100,0x1034580,0x155cb80,0x1965000,0x1e8d600,0x2295a80,0x27be080,0x2bc6500,0x30eeb00,0x3617100,0x3a1f580,0x3f47b80,0x4350000,0x4872780,0x4c76d00,0x5199480,0x559da00,0x598cf80,0x5c31900,0x5ed0e80,0x5ed0e80,0x5ed0e80,0x5ed0e80,0x5ed0e80,0x5ed0e80,0x5ed0e80,0x5ed0e80,0x5ed0e80,0x5ed0e80,0x5ed0e80,67,1059,2051,2859,3851,4659,5651,6459,7451,8259,9251,10243,11051,12043,12851,13834,14636,15619,16421,17191,17712,17712,17712,17712,21,20,0,0,0,0,0,0,0x1dbf80,0x704580,0xc2cb80,0x1035000,0x155d600,0x1965a80,0x1e8e080,0x2296500,0x27beb00,0x2bc6f80,0x30ef580,0x3617b80,0x3a20000,0x3f48600,0x4350a80,0x4873200,0x4c77780,0x5199f00,0x559e480,0x598da00,0x5c32380,0x5c32380,0x5c32380,0x5c32380,0x5c32380,0x5c32380,0x5c32380,0x5c32380,0x5c32380,0x5c32380,0x5c32380,0x5c32380,68,1060,2052,2860,3852,4660,5652,6460,7452,8260,9252,10244,11052,12044,12852,13835,14637,15620,16422,17192,17713,17713,17713,17713,21,20,0,0,0,0,0,0,0x1dca00,0x705000,0xc2d600,0x1035a80,0x155e080,0x1966500,0x1e8eb00,0x2296f80,0x27bf580,0x2bc7a00,0x30f0000,0x3618600,0x3a20a80,0x3f49080,0x4351500,0x4873c80,0x4c78200,0x519a980,0x559ef00,0x598e480,0x5c32e00,0x5c32e00,0x5c32e00,0x5c32e00,0x5c32e00,0x5c32e00,0x5c32e00,0x5c32e00,0x5c32e00,0x5c32e00,0x5c32e00,0x5c32e00,69,1061,2053,2861,3853,4661,5653,6461,7453,8261,9253,10245,11053,12045,12853,13836,14638,15621,16423,18500,18500,18500,18500,18500,20,19,0,0,0,0,0,0,0x1dd480,0x705a80,0xc2e080,0x1036500,0x155eb00,0x1966f80,0x1e8f580,0x2297a00,0x27c0000,0x2bc8480,0x30f0a80,0x3619080,0x3a21500,0x3f49b00,0x4351f80,0x4874700,0x4c78c80,0x519b400,0x559f980,0x8d480,0x8d480,0x8d480,0x8d480,0x8d480,0x8d480,0x8d480,0x8d480,0x8d480,0x8d480,0x8d480,0x8d480,0x8d480,70,1062,2054,2862,3854,4662,5654,6462,7454,8262,9254,10246,11054,12046,12854,13837,14639,15622,16424,18501,17714,17714,17714,17714,21,20,0,0,0,0,0,0,0x1ddf00,0x706500,0xc2eb00,0x1036f80,0x155f580,0x1967a00,0x1e90000,0x2298480,0x27c0a80,0x2bc8f00,0x30f1500,0x3619b00,0x3a21f80,0x3f4a580,0x4352a00,0x4875180,0x4c79700,0x519be80,0x55a0400,0x8df00,0x5c33880,0x5c33880,0x5c33880,0x5c33880,0x5c33880,0x5c33880,0x5c33880,0x5c33880,0x5c33880,0x5c33880,0x5c33880,0x5c33880,71,1063,2055,2863,3855,4663,5655,6463,7455,8263,9255,10247,11055,12047,12855,13838,14640,15623,16425,17193,17715,17715,17715,17715,21,20,0,0,0,0,0,0,0x1de980,0x706f80,0xc2f580,0x1037a00,0x1560000,0x1968480,0x1e90a80,0x2298f00,0x27c1500,0x2bc9980,0x30f1f80,0x361a580,0x3a22a00,0x3f4b000,0x4353480,0x4875c00,0x4c7a180,0x519c900,0x55a0e80,0x598ef00,0x5c34300,0x5c34300,0x5c34300,0x5c34300,0x5c34300,0x5c34300,0x5c34300,0x5c34300,0x5c34300,0x5c34300,0x5c34300,0x5c34300,256,1064,2056,2864,3856,4664,5656,6464,7456,8448,9256,10248,11056,12048,12856,18441,14641,18456,16426,18502,18502,18502,18502,18502,20,19,0,0,0,0,0,0,0x2ff580,0x707a00,0xc30000,0x1038480,0x1560a80,0x1968f00,0x1e91500,0x2299980,0x27c1f80,0x2cea580,0x30f2a00,0x361b000,0x3a23480,0x3f4ba80,0x4353f00,0x66900,0x4c7ac00,0x70680,0x55a1900,0x8e980,0x8e980,0x8e980,0x8e980,0x8e980,0x8e980,0x8e980,0x8e980,0x8e980,0x8e980,0x8e980,0x8e980,0x8e980,257,1065,2057,2865,3857,4665,5657,6465,7457,8449,9257,10249,11057,12049,12857,13839,14642,15624,16427,18503,18503,18503,18503,18503,20,19,0,0,0,0,0,0,0x300000,0x708480,0xc30a80,0x1038f00,0x1561500,0x1969980,0x1e91f80,0x229a400,0x27c2a00,0x2ceb000,0x30f3480,0x361ba80,0x3a23f00,0x3f4c500,0x4354980,0x4876680,0x4c7b680,0x519d380,0x55a2380,0x8f400,0x8f400,0x8f400,0x8f400,0x8f400,0x8f400,0x8f400,0x8f400,0x8f400,0x8f400,0x8f400,0x8f400,0x8f400,258,1066,2058,2866,3858,4666,5658,6466,7458,8450,9258,10250,11058,12050,12858,13840,14643,15625,16428,17194,17716,18225,18225,18225,22,21,0,0,0,0,0,0,0x300a80,0x708f00,0xc31500,0x1039980,0x1561f80,0x196a400,0x1e92a00,0x229ae80,0x27c3480,0x2ceba80,0x30f3f00,0x361c500,0x3a24980,0x3f4cf80,0x4355400,0x4877100,0x4c7c100,0x519de00,0x55a2e00,0x598f980,0x5c34d80,0x5ed1900,0x5ed1900,0x5ed1900,0x5ed1900,0x5ed1900,0x5ed1900,0x5ed1900,0x5ed1900,0x5ed1900,0x5ed1900,0x5ed1900,259,1067,2059,2867,3859,4667,5659,6467,7459,8451,9259,10251,11059,12051,12859,13841,14644,15626,16429,17195,17717,17717,17717,17717,21,20,0,0,0,0,0,0,0x301500,0x709980,0xc31f80,0x103a400,0x1562a00,0x196ae80,0x1e93480,0x229b900,0x27c3f00,0x2cec500,0x30f4980,0x361cf80,0x3a25400,0x3f4da00,0x4355e80,0x4877b80,0x4c7cb80,0x519e880,0x55a3880,0x5990400,0x5c35800,0x5c35800,0x5c35800,0x5c35800,0x5c35800,0x5c35800,0x5c35800,0x5c35800,0x5c35800,0x5c35800,0x5c35800,0x5c35800,260,1068,2060,2868,3860,4668,5660,6468,7460,8452,9260,10252,11060,12052,12860,13842,14645,15627,16430,17196,17718,17718,17718,17718,21,20,0,0,0,0,0,0,0x301f80,0x70a400,0xc32a00,0x103ae80,0x1563480,0x196b900,0x1e93f00,0x229c380,0x27c4980,0x2cecf80,0x30f5400,0x361da00,0x3a25e80,0x3f4e480,0x4356900,0x4878600,0x4c7d600,0x519f300,0x55a4300,0x5990e80,0x5c36280,0x5c36280,0x5c36280,0x5c36280,0x5c36280,0x5c36280,0x5c36280,0x5c36280,0x5c36280,0x5c36280,0x5c36280,0x5c36280,261,1069,2061,2869,3861,4669,5661,6469,7461,8453,9261,10253,11061,12053,12861,13843,14646,15628,16431,18504,18504,18504,18504,18504,20,19,0,0,0,0,0,0,0x302a00,0x70ae80,0xc33480,0x103b900,0x1563f00,0x196c380,0x1e94980,0x229ce00,0x27c5400,0x2ceda00,0x30f5e80,0x361e480,0x3a26900,0x3f4ef00,0x4357380,0x4879080,0x4c7e080,0x519fd80,0x55a4d80,0x60000,0x60000,0x60000,0x60000,0x60000,0x60000,0x60000,0x60000,0x60000,0x60000,0x60000,0x60000,0x60000,262,1070,2062,2870,3862,4670,5662,6470,7462,8454,9262,10254,11062,12054,12862,13844,14647,15629,16432,72,17719,17719,17719,17719,21,20,0,0,0,0,0,0,0x303480,0x70b900,0xc33f00,0x103c380,0x1564980,0x196ce00,0x1e95400,0x229d880,0x27c5e80,0x2cee480,0x30f6900,0x361ef00,0x3a27380,0x3f4f980,0x4357e00,0x4879b00,0x4c7eb00,0x51a0800,0x55a5800,0x1af580,0x5c36d00,0x5c36d00,0x5c36d00,0x5c36d00,0x5c36d00,0x5c36d00,0x5c36d00,0x5c36d00,0x5c36d00,0x5c36d00,0x5c36d00,0x5c36d00,263,1071,2063,2871,3863,4671,5663,6471,7463,8455,9263,10255,11063,12055,12863,13845,14648,15630,16433,17197,17720,17720,17720,17720,21,20,0,0,0,0,0,0,0x303f00,0x70c380,0xc34980,0x103ce00,0x1565400,0x196d880,0x1e95e80,0x229e300,0x27c6900,0x2ceef00,0x30f7380,0x361f980,0x3a27e00,0x3f50400,0x4358880,0x487a580,0x4c7f580,0x51a1280,0x55a6280,0x5991900,0x5c37780,0x5c37780,0x5c37780,0x5c37780,0x5c37780,0x5c37780,0x5c37780,0x5c37780,0x5c37780,0x5c37780,0x5c37780,0x5c37780,264,1072,2064,2872,3864,4672,5664,6656,7464,8456,9264,10256,11064,12056,12864,18442,14649,18457,16434,328,328,328,328,328,20,19,0,0,0,0,0,0,0x304980,0x70ce00,0xc35400,0x103d880,0x1565e80,0x196e300,0x1e96900,0x23bef00,0x27c7380,0x2cef980,0x30f7e00,0x3620400,0x3a28880,0x3f50e80,0x4359300,0x67380,0x4c80000,0x71100,0x55a6d00,0x2feb00,0x2feb00,0x2feb00,0x2feb00,0x2feb00,0x2feb00,0x2feb00,0x2feb00,0x2feb00,0x2feb00,0x2feb00,0x2feb00,0x2feb00,265,1073,2065,2873,3865,4673,5665,6657,7465,8457,9265,10257,11065,12057,12865,13846,14650,15631,16435,584,584,584,584,584,20,19,0,0,0,0,0,0,0x305400,0x70d880,0xc35e80,0x103e300,0x1566900,0x196ed80,0x1e97380,0x23bf980,0x27c7e00,0x2cf0400,0x30f8880,0x3620e80,0x3a29300,0x3f51900,0x4359d80,0x487b000,0x4c80a80,0x51a1d00,0x55a7780,0x44e080,0x44e080,0x44e080,0x44e080,0x44e080,0x44e080,0x44e080,0x44e080,0x44e080,0x44e080,0x44e080,0x44e080,0x44e080,266,1074,2066,2874,3866,4674,5666,6658,7466,8458,9266,10258,11066,12058,12866,13847,14651,15632,16436,17198,17721,18226,18226,18226,22,21,0,0,0,0,0,0,0x305e80,0x70e300,0xc36900,0x103ed80,0x1567380,0x196f800,0x1e97e00,0x23c0400,0x27c8880,0x2cf0e80,0x30f9300,0x3621900,0x3a29d80,0x3f52380,0x435a800,0x487ba80,0x4c81500,0x51a2780,0x55a8200,0x5992380,0x5c38200,0x5ed2380,0x5ed2380,0x5ed2380,0x5ed2380,0x5ed2380,0x5ed2380,0x5ed2380,0x5ed2380,0x5ed2380,0x5ed2380,0x5ed2380,267,1075,2067,2875,3867,4675,5667,6659,7467,8459,9267,10259,11067,12059,12867,13848,14652,15633,16437,17199,17722,17722,17722,17722,21,20,0,0,0,0,0,0,0x306900,0x70ed80,0xc37380,0x103f800,0x1567e00,0x1970280,0x1e98880,0x23c0e80,0x27c9300,0x2cf1900,0x30f9d80,0x3622380,0x3a2a800,0x3f52e00,0x435b280,0x487c500,0x4c81f80,0x51a3200,0x55a8c80,0x5992e00,0x5c38c80,0x5c38c80,0x5c38c80,0x5c38c80,0x5c38c80,0x5c38c80,0x5c38c80,0x5c38c80,0x5c38c80,0x5c38c80,0x5c38c80,0x5c38c80,268,1076,2068,2876,3868,4676,5668,6660,7468,8460,9268,10260,11068,12060,12868,13849,14653,15634,16438,17200,17723,17723,17723,17723,21,20,0,0,0,0,0,0,0x307380,0x70f800,0xc37e00,0x1040280,0x1568880,0x1970d00,0x1e99300,0x23c1900,0x27c9d80,0x2cf2380,0x30fa800,0x3622e00,0x3a2b280,0x3f53880,0x435bd00,0x487cf80,0x4c82a00,0x51a3c80,0x55a9700,0x5993880,0x5c39700,0x5c39700,0x5c39700,0x5c39700,0x5c39700,0x5c39700,0x5c39700,0x5c39700,0x5c39700,0x5c39700,0x5c39700,0x5c39700,269,1077,2069,2877,3869,4677,5669,6661,7469,8461,9269,10261,11069,12061,12869,13850,14654,15635,16439,840,840,840,840,840,20,19,0,0,0,0,0,0,0x307e00,0x710280,0xc38880,0x1040d00,0x1569300,0x1971780,0x1e99d80,0x23c2380,0x27ca800,0x2cf2e00,0x30fb280,0x3623880,0x3a2bd00,0x3f54300,0x435c780,0x487da00,0x4c83480,0x51a4700,0x55aa180,0x59d600,0x59d600,0x59d600,0x59d600,0x59d600,0x59d600,0x59d600,0x59d600,0x59d600,0x59d600,0x59d600,0x59d600,0x59d600,270,1078,2070,2878,3870,4678,5670,6662,7470,8462,9270,10262,11070,12062,12870,13851,14655,15636,16440,1096,17724,17724,17724,17724,21,20,0,0,0,0,0,0,0x308880,0x710d00,0xc39300,0x1041780,0x1569d80,0x1972200,0x1e9a800,0x23c2e00,0x27cb280,0x2cf3880,0x30fbd00,0x3624300,0x3a2c780,0x3f54d80,0x435d200,0x487e480,0x4c83f00,0x51a5180,0x55aac00,0x6ecb80,0x5c3a180,0x5c3a180,0x5c3a180,0x5c3a180,0x5c3a180,0x5c3a180,0x5c3a180,0x5c3a180,0x5c3a180,0x5c3a180,0x5c3a180,0x5c3a180,271,1079,2071,2879,3871,4679,5671,6663,7471,8463,9271,10263,11071,12063,12871,13852,14656,15637,16441,17201,17725,17725,17725,17725,21,20,0,0,0,0,0,0,0x309300,0x711780,0xc39d80,0x1042200,0x156a800,0x1972c80,0x1e9b280,0x23c3880,0x27cbd00,0x2cf4300,0x30fc780,0x3624d80,0x3a2d200,0x3f55800,0x435dc80,0x487ef00,0x4c84980,0x51a5c00,0x55ab680,0x5994300,0x5c3ac00,0x5c3ac00,0x5c3ac00,0x5c3ac00,0x5c3ac00,0x5c3ac00,0x5c3ac00,0x5c3ac00,0x5c3ac00,0x5c3ac00,0x5c3ac00,0x5c3ac00,272,1080,2072,2880,3872,4864,5672,6664,7472,8464,9272,10264,11072,12064,13056,18443,14657,18458,16442,1352,1352,1352,1352,1352,20,19,0,0,0,0,0,0,0x309d80,0x712200,0xc3a800,0x1042c80,0x156b280,0x1a93880,0x1e9bd00,0x23c4300,0x27cc780,0x2cf4d80,0x30fd200,0x3625800,0x3a2dc80,0x3f56280,0x447e880,0x67e00,0x4c85400,0x71b80,0x55ac100,0x83c100,0x83c100,0x83c100,0x83c100,0x83c100,0x83c100,0x83c100,0x83c100,0x83c100,0x83c100,0x83c100,0x83c100,0x83c100,273,1081,2073,2881,3873,4865,5673,6665,7473,8465,9273,10265,11073,12065,13057,13853,14658,15638,16443,1608,1608,1608,1608,1608,20,19,0,0,0,0,0,0,0x30a800,0x712c80,0xc3b280,0x1043700,0x156bd00,0x1a94300,0x1e9c780,0x23c4d80,0x27cd200,0x2cf5800,0x30fdc80,0x3626280,0x3a2e700,0x3f56d00,0x447f300,0x487f980,0x4c85e80,0x51a6680,0x55acb80,0x98b680,0x98b680,0x98b680,0x98b680,0x98b680,0x98b680,0x98b680,0x98b680,0x98b680,0x98b680,0x98b680,0x98b680,0x98b680,274,1082,2074,2882,3874,4866,5674,6666,7474,8466,9274,10266,11074,12066,13058,13854,14659,15639,16444,17202,17726,18227,18227,18227,22,21,0,0,0,0,0,0,0x30b280,0x713700,0xc3bd00,0x1044180,0x156c780,0x1a94d80,0x1e9d200,0x23c5800,0x27cdc80,0x2cf6280,0x30fe700,0x3626d00,0x3a2f180,0x3f57780,0x447fd80,0x4880400,0x4c86900,0x51a7100,0x55ad600,0x5994d80,0x5c3b680,0x5ed2e00,0x5ed2e00,0x5ed2e00,0x5ed2e00,0x5ed2e00,0x5ed2e00,0x5ed2e00,0x5ed2e00,0x5ed2e00,0x5ed2e00,0x5ed2e00,275,1083,2075,2883,3875,4867,5675,6667,7475,8467,9275,10267,11075,12067,13059,13855,14660,15640,16445,17203,17727,17727,17727,17727,21,20,0,0,0,0,0,0,0x30bd00,0x714180,0xc3c780,0x1044c00,0x156d200,0x1a95800,0x1e9dc80,0x23c6280,0x27ce700,0x2cf6d00,0x30ff180,0x3627780,0x3a2fc00,0x3f58200,0x4480800,0x4880e80,0x4c87380,0x51a7b80,0x55ae080,0x5995800,0x5c3c100,0x5c3c100,0x5c3c100,0x5c3c100,0x5c3c100,0x5c3c100,0x5c3c100,0x5c3c100,0x5c3c100,0x5c3c100,0x5c3c100,0x5c3c100,276,1084,2076,2884,3876,4868,5676,6668,7476,8468,9276,10268,11076,12068,13060,13856,14661,15641,16446,17204,17728,17728,17728,17728,21,20,0,0,0,0,0,0,0x30c780,0x714c00,0xc3d200,0x1045680,0x156dc80,0x1a96280,0x1e9e700,0x23c6d00,0x27cf180,0x2cf7780,0x30ffc00,0x3628200,0x3a30680,0x3f58c80,0x4481280,0x4881900,0x4c87e00,0x51a8600,0x55aeb00,0x5996280,0x5c3cb80,0x5c3cb80,0x5c3cb80,0x5c3cb80,0x5c3cb80,0x5c3cb80,0x5c3cb80,0x5c3cb80,0x5c3cb80,0x5c3cb80,0x5c3cb80,0x5c3cb80,277,1085,2077,2885,3877,4869,5677,6669,7477,8469,9277,10269,11077,12069,13061,13857,14662,15642,16447,1864,1864,1864,1864,1864,20,19,0,0,0,0,0,0,0x30d200,0x715680,0xc3dc80,0x1046100,0x156e700,0x1a96d00,0x1e9f180,0x23c7780,0x27cfc00,0x2cf8200,0x3100680,0x3628c80,0x3a31100,0x3f59700,0x4481d00,0x4882380,0x4c88880,0x51a9080,0x55af580,0xadac00,0xadac00,0xadac00,0xadac00,0xadac00,0xadac00,0xadac00,0xadac00,0xadac00,0xadac00,0xadac00,0xadac00,0xadac00,278,1086,2078,2886,3878,4870,5678,6670,7478,8470,9278,10270,11078,12070,13062,13858,14663,15643,16448,2120,17729,17729,17729,17729,21,20,0,0,0,0,0,0,0x30dc80,0x716100,0xc3e700,0x1046b80,0x156f180,0x1a97780,0x1e9fc00,0x23c8200,0x27d0680,0x2cf8c80,0x3101100,0x3629700,0x3a31b80,0x3f5a180,0x4482780,0x4882e00,0x4c89300,0x51a9b00,0x55b0000,0xc2a180,0x5c3d600,0x5c3d600,0x5c3d600,0x5c3d600,0x5c3d600,0x5c3d600,0x5c3d600,0x5c3d600,0x5c3d600,0x5c3d600,0x5c3d600,0x5c3d600,279,1087,2079,2887,3879,4871,5679,6671,7479,8471,9279,10271,11079,12071,13063,13859,14848,15644,16449,17205,17730,17730,17730,17730,21,20,0,0,0,0,0,0,0x30e700,0x716b80,0xc3f180,0x1047600,0x156fc00,0x1a98200,0x1ea0680,0x23c8c80,0x27d1100,0x2cf9700,0x3101b80,0x362a180,0x3a32600,0x3f5ac00,0x4483200,0x4883880,0x4da9f00,0x51aa580,0x55b0a80,0x5996d00,0x5c3e080,0x5c3e080,0x5c3e080,0x5c3e080,0x5c3e080,0x5c3e080,0x5c3e080,0x5c3e080,0x5c3e080,0x5c3e080,0x5c3e080,0x5c3e080,280,1088,2080,3072,3880,4872,5680,6672,7480,8472,9280,10272,11264,12072,13064,18444,14849,18459,16450,2376,2376,2376,2376,2376,20,19,0,0,0,0,0,0,0x30f180,0x717600,0xc3fc00,0x1168200,0x1570680,0x1a98c80,0x1ea1100,0x23c9700,0x27d1b80,0x2cfa180,0x3102600,0x362ac00,0x3b53200,0x3f5b680,0x4483c80,0x68880,0x4daa980,0x72600,0x55b1500,0xd79700,0xd79700,0xd79700,0xd79700,0xd79700,0xd79700,0xd79700,0xd79700,0xd79700,0xd79700,0xd79700,0xd79700,0xd79700,281,1089,2081,3073,3881,4873,5681,6673,7481,8473,9281,10273,11265,12073,13065,13860,14850,15645,16451,2632,2632,2632,2632,2632,20,19,0,0,0,0,0,0,0x30fc00,0x718080,0xc40680,0x1168c80,0x1571100,0x1a99700,0x1ea1b80,0x23ca180,0x27d2600,0x2cfac00,0x3103080,0x362b680,0x3b53c80,0x3f5c100,0x4484700,0x4884300,0x4dab400,0x51ab000,0x55b1f80,0xec8c80,0xec8c80,0xec8c80,0xec8c80,0xec8c80,0xec8c80,0xec8c80,0xec8c80,0xec8c80,0xec8c80,0xec8c80,0xec8c80,0xec8c80,282,1090,2082,3074,3882,4874,5682,6674,7482,8474,9282,10274,11266,12074,13066,13861,14851,15646,16452,17206,17731,18228,18228,18228,22,21,0,0,0,0,0,0,0x310680,0x718b00,0xc41100,0x1169700,0x1571b80,0x1a9a180,0x1ea2600,0x23cac00,0x27d3080,0x2cfb680,0x3103b00,0x362c100,0x3b54700,0x3f5cb80,0x4485180,0x4884d80,0x4dabe80,0x51aba80,0x55b2a00,0x5997780,0x5c3eb00,0x5ed3880,0x5ed3880,0x5ed3880,0x5ed3880,0x5ed3880,0x5ed3880,0x5ed3880,0x5ed3880,0x5ed3880,0x5ed3880,0x5ed3880,283,1091,2083,3075,3883,4875,5683,6675,7483,8475,9283,10275,11267,12075,13067,13862,14852,15647,16453,17207,17732,17732,17732,17732,21,20,0,0,0,0,0,0,0x311100,0x719580,0xc41b80,0x116a180,0x1572600,0x1a9ac00,0x1ea3080,0x23cb680,0x27d3b00,0x2cfc100,0x3104580,0x362cb80,0x3b55180,0x3f5d600,0x4485c00,0x4885800,0x4dac900,0x51ac500,0x55b3480,0x5998200,0x5c3f580,0x5c3f580,0x5c3f580,0x5c3f580,0x5c3f580,0x5c3f580,0x5c3f580,0x5c3f580,0x5c3f580,0x5c3f580,0x5c3f580,0x5c3f580,284,1092,2084,3076,3884,4876,5684,6676,7484,8476,9284,10276,11268,12076,13068,13863,14853,15648,16454,17208,17733,17733,17733,17733,21,20,0,0,0,0,0,0,0x311b80,0x71a000,0xc42600,0x116ac00,0x1573080,0x1a9b680,0x1ea3b00,0x23cc100,0x27d4580,0x2cfcb80,0x3105000,0x362d600,0x3b55c00,0x3f5e080,0x4486680,0x4886280,0x4dad380,0x51acf80,0x55b3f00,0x5998c80,0x5c40000,0x5c40000,0x5c40000,0x5c40000,0x5c40000,0x5c40000,0x5c40000,0x5c40000,0x5c40000,0x5c40000,0x5c40000,0x5c40000,285,1093,2085,3077,3885,4877,5685,6677,7485,8477,9285,10277,11269,12077,13069,13864,14854,15649,16455,2888,2888,2888,2888,2888,20,19,0,0,0,0,0,0,0x312600,0x71aa80,0xc43080,0x116b680,0x1573b00,0x1a9c100,0x1ea4580,0x23ccb80,0x27d5000,0x2cfd600,0x3105a80,0x362e080,0x3b56680,0x3f5eb00,0x4487100,0x4886d00,0x4dade00,0x51ada00,0x55b4980,0x1018200,0x1018200,0x1018200,0x1018200,0x1018200,0x1018200,0x1018200,0x1018200,0x1018200,0x1018200,0x1018200,0x1018200,0x1018200,286,1094,2086,3078,3886,4878,5686,6678,7486,8478,9286,10278,11270,12078,13070,13865,14855,15650,16640,3144,17734,17734,17734,17734,21,20,0,0,0,0,0,0,0x313080,0x71b500,0xc43b00,0x116c100,0x1574580,0x1a9cb80,0x1ea5000,0x23cd600,0x27d5a80,0x2cfe080,0x3106500,0x362eb00,0x3b57100,0x3f5f580,0x4487b80,0x4887780,0x4dae880,0x51ae480,0x56d5580,0x1167780,0x5c40a80,0x5c40a80,0x5c40a80,0x5c40a80,0x5c40a80,0x5c40a80,0x5c40a80,0x5c40a80,0x5c40a80,0x5c40a80,0x5c40a80,0x5c40a80,287,1095,2087,3079,3887,4879,5687,6679,7487,8479,9287,10279,11271,12079,13071,13866,14856,15651,16641,17209,17735,17735,17735,17735,21,20,0,0,0,0,0,0,0x313b00,0x71bf80,0xc44580,0x116cb80,0x1575000,0x1a9d600,0x1ea5a80,0x23ce080,0x27d6500,0x2cfeb00,0x3106f80,0x362f580,0x3b57b80,0x3f60000,0x4488600,0x4888200,0x4daf300,0x51aef00,0x56d6000,0x5999700,0x5c41500,0x5c41500,0x5c41500,0x5c41500,0x5c41500,0x5c41500,0x5c41500,0x5c41500,0x5c41500,0x5c41500,0x5c41500,0x5c41500,288,1280,2088,3080,3888,4880,5688,6680,7488,8480,9472,10280,11272,12080,13072,18445,14857,18460,16642,3400,3400,3400,3400,3400,20,19,0,0,0,0,0,0,0x314580,0x83cb80,0xc45000,0x116d600,0x1575a80,0x1a9e080,0x1ea6500,0x23ceb00,0x27d6f80,0x2cff580,0x3227b80,0x3630000,0x3b58600,0x3f60a80,0x4489080,0x69300,0x4dafd80,0x73080,0x56d6a80,0x12b6d00,0x12b6d00,0x12b6d00,0x12b6d00,0x12b6d00,0x12b6d00,0x12b6d00,0x12b6d00,0x12b6d00,0x12b6d00,0x12b6d00,0x12b6d00,0x12b6d00,289,1281,2089,3081,3889,4881,5689,6681,7489,8481,9473,10281,11273,12081,13073,13867,14858,15652,16643,3656,3656,3656,3656,3656,20,19,0,0,0,0,0,0,0x315000,0x83d600,0xc45a80,0x116e080,0x1576500,0x1a9eb00,0x1ea6f80,0x23cf580,0x27d7a00,0x2d00000,0x3228600,0x3630a80,0x3b59080,0x3f61500,0x4489b00,0x4888c80,0x4db0800,0x51af980,0x56d7500,0x1406280,0x1406280,0x1406280,0x1406280,0x1406280,0x1406280,0x1406280,0x1406280,0x1406280,0x1406280,0x1406280,0x1406280,0x1406280,290,1282,2090,3082,3890,4882,5690,6682,7490,8482,9474,10282,11274,12082,13074,13868,14859,15653,16644,17210,17920,18229,18229,18229,22,21,0,0,0,0,0,0,0x315a80,0x83e080,0xc46500,0x116eb00,0x1576f80,0x1a9f580,0x1ea7a00,0x23d0000,0x27d8480,0x2d00a80,0x3229080,0x3631500,0x3b59b00,0x3f61f80,0x448a580,0x4889700,0x4db1280,0x51b0400,0x56d7f80,0x599a180,0x5d62100,0x5ed4300,0x5ed4300,0x5ed4300,0x5ed4300,0x5ed4300,0x5ed4300,0x5ed4300,0x5ed4300,0x5ed4300,0x5ed4300,0x5ed4300,291,1283,2091,3083,3891,4883,5691,6683,7491,8483,9475,10283,11275,12083,13075,13869,14860,15654,16645,17211,17921,17921,17921,17921,21,20,0,0,0,0,0,0,0x316500,0x83eb00,0xc46f80,0x116f580,0x1577a00,0x1aa0000,0x1ea8480,0x23d0a80,0x27d8f00,0x2d01500,0x3229b00,0x3631f80,0x3b5a580,0x3f62a00,0x448b000,0x488a180,0x4db1d00,0x51b0e80,0x56d8a00,0x599ac00,0x5d62b80,0x5d62b80,0x5d62b80,0x5d62b80,0x5d62b80,0x5d62b80,0x5d62b80,0x5d62b80,0x5d62b80,0x5d62b80,0x5d62b80,0x5d62b80,292,1284,2092,3084,3892,4884,5692,6684,7492,8484,9476,10284,11276,12084,13076,13870,14861,15655,16646,17212,17922,17922,17922,17922,21,20,0,0,0,0,0,0,0x316f80,0x83f580,0xc47a00,0x1170000,0x1578480,0x1aa0a80,0x1ea8f00,0x23d1500,0x27d9980,0x2d01f80,0x322a580,0x3632a00,0x3b5b000,0x3f63480,0x448ba80,0x488ac00,0x4db2780,0x51b1900,0x56d9480,0x599b680,0x5d63600,0x5d63600,0x5d63600,0x5d63600,0x5d63600,0x5d63600,0x5d63600,0x5d63600,0x5d63600,0x5d63600,0x5d63600,0x5d63600,293,1285,2093,3085,3893,4885,5693,6685,7493,8485,9477,10285,11277,12085,13077,13871,14862,15656,16647,3912,3912,3912,3912,3912,20,19,0,0,0,0,0,0,0x317a00,0x840000,0xc48480,0x1170a80,0x1578f00,0x1aa1500,0x1ea9980,0x23d1f80,0x27da400,0x2d02a00,0x322b000,0x3633480,0x3b5ba80,0x3f63f00,0x448c500,0x488b680,0x4db3200,0x51b2380,0x56d9f00,0x1555800,0x1555800,0x1555800,0x1555800,0x1555800,0x1555800,0x1555800,0x1555800,0x1555800,0x1555800,0x1555800,0x1555800,0x1555800,294,1286,2094,3086,3894,4886,5694,6686,7494,8486,9478,10286,11278,12086,13078,13872,14863,15657,16648,4168,17923,17923,17923,17923,21,20,0,0,0,0,0,0,0x318480,0x840a80,0xc48f00,0x1171500,0x1579980,0x1aa1f80,0x1eaa400,0x23d2a00,0x27dae80,0x2d03480,0x322ba80,0x3633f00,0x3b5c500,0x3f64980,0x448cf80,0x488c100,0x4db3c80,0x51b2e00,0x56da980,0x16a4d80,0x5d64080,0x5d64080,0x5d64080,0x5d64080,0x5d64080,0x5d64080,0x5d64080,0x5d64080,0x5d64080,0x5d64080,0x5d64080,0x5d64080,295,1287,2095,3087,3895,4887,5695,6687,7495,8487,9479,10287,11279,12087,13079,13873,14864,15658,16649,17213,17924,17924,17924,17924,21,20,0,0,0,0,0,0,0x318f00,0x841500,0xc49980,0x1171f80,0x157a400,0x1aa2a00,0x1eaae80,0x23d3480,0x27db900,0x2d03f00,0x322c500,0x3634980,0x3b5cf80,0x3f65400,0x448da00,0x488cb80,0x4db4700,0x51b3880,0x56db400,0x599c100,0x5d64b00,0x5d64b00,0x5d64b00,0x5d64b00,0x5d64b00,0x5d64b00,0x5d64b00,0x5d64b00,0x5d64b00,0x5d64b00,0x5d64b00,0x5d64b00,296,1288,2096,3088,3896,4888,5696,6688,7680,8488,9480,10288,11280,12088,13080,18446,14865,18461,16650,4424,4424,4424,4424,4424,20,19,0,0,0,0,0,0,0x319980,0x841f80,0xc4a400,0x1172a00,0x157ae80,0x1aa3480,0x1eab900,0x23d3f00,0x28fc500,0x2d04980,0x322cf80,0x3635400,0x3b5da00,0x3f65e80,0x448e480,0x69d80,0x4db5180,0x73b00,0x56dbe80,0x17f4300,0x17f4300,0x17f4300,0x17f4300,0x17f4300,0x17f4300,0x17f4300,0x17f4300,0x17f4300,0x17f4300,0x17f4300,0x17f4300,0x17f4300,297,1289,2097,3089,3897,4889,5697,6689,7681,8489,9481,10289,11281,12089,13081,13874,14866,15659,16651,4680,4680,4680,4680,4680,20,19,0,0,0,0,0,0,0x31a400,0x842a00,0xc4ae80,0x1173480,0x157b900,0x1aa3f00,0x1eac380,0x23d4980,0x28fcf80,0x2d05400,0x322da00,0x3635e80,0x3b5e480,0x3f66900,0x448ef00,0x488d600,0x4db5c00,0x51b4300,0x56dc900,0x1943880,0x1943880,0x1943880,0x1943880,0x1943880,0x1943880,0x1943880,0x1943880,0x1943880,0x1943880,0x1943880,0x1943880,0x1943880,298,1290,2098,3090,3898,4890,5698,6690,7682,8490,9482,10290,11282,12090,13082,13875,14867,15660,16652,17214,17925,18230,18230,18230,22,21,0,0,0,0,0,0,0x31ae80,0x843480,0xc4b900,0x1173f00,0x157c380,0x1aa4980,0x1eace00,0x23d5400,0x28fda00,0x2d05e80,0x322e480,0x3636900,0x3b5ef00,0x3f67380,0x448f980,0x488e080,0x4db6680,0x51b4d80,0x56dd380,0x599cb80,0x5d65580,0x5ed4d80,0x5ed4d80,0x5ed4d80,0x5ed4d80,0x5ed4d80,0x5ed4d80,0x5ed4d80,0x5ed4d80,0x5ed4d80,0x5ed4d80,0x5ed4d80,299,1291,2099,3091,3899,4891,5699,6691,7683,8491,9483,10291,11283,12091,13083,13876,14868,15661,16653,17215,17926,17926,17926,17926,21,20,0,0,0,0,0,0,0x31b900,0x843f00,0xc4c380,0x1174980,0x157ce00,0x1aa5400,0x1ead880,0x23d5e80,0x28fe480,0x2d06900,0x322ef00,0x3637380,0x3b5f980,0x3f67e00,0x4490400,0x488eb00,0x4db7100,0x51b5800,0x56dde00,0x599d600,0x5d66000,0x5d66000,0x5d66000,0x5d66000,0x5d66000,0x5d66000,0x5d66000,0x5d66000,0x5d66000,0x5d66000,0x5d66000,0x5d66000,300,1292,2100,3092,3900,4892,5700,6692,7684,8492,9484,10292,11284,12092,13084,13877,14869,15662,16654,17216,17927,17927,17927,17927,21,20,0,0,0,0,0,0,0x31c380,0x844980,0xc4ce00,0x1175400,0x157d880,0x1aa5e80,0x1eae300,0x23d6900,0x28fef00,0x2d07380,0x322f980,0x3637e00,0x3b60400,0x3f68880,0x4490e80,0x488f580,0x4db7b80,0x51b6280,0x56de880,0x599e080,0x5d66a80,0x5d66a80,0x5d66a80,0x5d66a80,0x5d66a80,0x5d66a80,0x5d66a80,0x5d66a80,0x5d66a80,0x5d66a80,0x5d66a80,0x5d66a80,301,1293,2101,3093,3901,4893,5701,6693,7685,8493,9485,10293,11285,12093,13085,13878,14870,15663,16655,4936,4936,4936,4936,4936,20,19,0,0,0,0,0,0,0x31ce00,0x845400,0xc4d880,0x1175e80,0x157e300,0x1aa6900,0x1eaed80,0x23d7380,0x28ff980,0x2d07e00,0x3230400,0x3638880,0x3b60e80,0x3f69300,0x4491900,0x4890000,0x4db8600,0x51b6d00,0x56df300,0x1a92e00,0x1a92e00,0x1a92e00,0x1a92e00,0x1a92e00,0x1a92e00,0x1a92e00,0x1a92e00,0x1a92e00,0x1a92e00,0x1a92e00,0x1a92e00,0x1a92e00,302,1294,2102,3094,3902,4894,5702,6694,7686,8494,9486,10294,11286,12094,13086,13879,14871,15664,16656,5192,17928,17928,17928,17928,21,20,0,0,0,0,0,0,0x31d880,0x845e80,0xc4e300,0x1176900,0x157ed80,0x1aa7380,0x1eaf800,0x23d7e00,0x2900400,0x2d08880,0x3230e80,0x3639300,0x3b61900,0x3f69d80,0x4492380,0x4890a80,0x4db9080,0x51b7780,0x56dfd80,0x1be2380,0x5d67500,0x5d67500,0x5d67500,0x5d67500,0x5d67500,0x5d67500,0x5d67500,0x5d67500,0x5d67500,0x5d67500,0x5d67500,0x5d67500,303,1295,2103,3095,3903,4895,5703,6695,7687,8495,9487,10295,11287,12095,13087,13880,14872,15665,16657,17217,17929,17929,17929,17929,21,20,0,0,0,0,0,0,0x31e300,0x846900,0xc4ed80,0x1177380,0x157f800,0x1aa7e00,0x1eb0280,0x23d8880,0x2900e80,0x2d09300,0x3231900,0x3639d80,0x3b62380,0x3f6a800,0x4492e00,0x4891500,0x4db9b00,0x51b8200,0x56e0800,0x599eb00,0x5d67f80,0x5d67f80,0x5d67f80,0x5d67f80,0x5d67f80,0x5d67f80,0x5d67f80,0x5d67f80,0x5d67f80,0x5d67f80,0x5d67f80,0x5d67f80,304,1296,2104,3096,3904,4896,5888,6696,7688,8496,9488,10296,11288,12096,13088,13881,14873,15666,16658,17218,17930,17930,17930,17930,21,20,0,0,0,0,0,0,0x31ed80,0x847380,0xc4f800,0x1177e00,0x1580280,0x1aa8880,0x1fd0e80,0x23d9300,0x2901900,0x2d09d80,0x3232380,0x363a800,0x3b62e00,0x3f6b280,0x4493880,0x4891f80,0x4dba580,0x51b8c80,0x56e1280,0x599f580,0x5d68a00,0x5d68a00,0x5d68a00,0x5d68a00,0x5d68a00,0x5d68a00,0x5d68a00,0x5d68a00,0x5d68a00,0x5d68a00,0x5d68a00,0x5d68a00,305,1297,2105,3097,3905,4897,5889,6697,7689,8497,9489,10297,11289,12097,13089,13882,14874,15667,16659,5448,5448,5448,5448,5448,20,19,0,0,0,0,0,0,0x31f800,0x847e00,0xc50280,0x1178880,0x1580d00,0x1aa9300,0x1fd1900,0x23d9d80,0x2902380,0x2d0a800,0x3232e00,0x363b280,0x3b63880,0x3f6bd00,0x4494300,0x4892a00,0x4dbb000,0x51b9700,0x56e1d00,0x1d31900,0x1d31900,0x1d31900,0x1d31900,0x1d31900,0x1d31900,0x1d31900,0x1d31900,0x1d31900,0x1d31900,0x1d31900,0x1d31900,0x1d31900,306,1298,2106,3098,3906,4898,5890,6698,7690,8498,9490,10298,11290,12098,13090,13883,14875,15668,16660,17219,17931,18231,18231,18231,22,21,0,0,0,0,0,0,0x320280,0x848880,0xc50d00,0x1179300,0x1581780,0x1aa9d80,0x1fd2380,0x23da800,0x2902e00,0x2d0b280,0x3233880,0x363bd00,0x3b64300,0x3f6c780,0x4494d80,0x4893480,0x4dbba80,0x51ba180,0x56e2780,0x59a0000,0x5d69480,0x5ed5800,0x5ed5800,0x5ed5800,0x5ed5800,0x5ed5800,0x5ed5800,0x5ed5800,0x5ed5800,0x5ed5800,0x5ed5800,0x5ed5800,307,1299,2107,3099,3907,4899,5891,6699,7691,8499,9491,10299,11291,12099,13091,13884,14876,15669,16661,17220,17932,17932,17932,17932,21,20,0,0,0,0,0,0,0x320d00,0x849300,0xc51780,0x1179d80,0x1582200,0x1aaa800,0x1fd2e00,0x23db280,0x2903880,0x2d0bd00,0x3234300,0x363c780,0x3b64d80,0x3f6d200,0x4495800,0x4893f00,0x4dbc500,0x51bac00,0x56e3200,0x59a0a80,0x5d69f00,0x5d69f00,0x5d69f00,0x5d69f00,0x5d69f00,0x5d69f00,0x5d69f00,0x5d69f00,0x5d69f00,0x5d69f00,0x5d69f00,0x5d69f00,308,1300,2108,3100,3908,4900,5892,6700,7692,8500,9492,10300,11292,12100,13092,13885,14877,15670,16662,17221,17933,17933,17933,17933,21,20,0,0,0,0,0,0,0x321780,0x849d80,0xc52200,0x117a800,0x1582c80,0x1aab280,0x1fd3880,0x23dbd00,0x2904300,0x2d0c780,0x3234d80,0x363d200,0x3b65800,0x3f6dc80,0x4496280,0x4894980,0x4dbcf80,0x51bb680,0x56e3c80,0x59a1500,0x5d6a980,0x5d6a980,0x5d6a980,0x5d6a980,0x5d6a980,0x5d6a980,0x5d6a980,0x5d6a980,0x5d6a980,0x5d6a980,0x5d6a980,0x5d6a980,309,1301,2109,3101,3909,4901,5893,6701,7693,8501,9493,10301,11293,12101,13093,13886,14878,15671,16663,5704,5704,5704,5704,5704,20,19,0,0,0,0,0,0,0x322200,0x84a800,0xc52c80,0x117b280,0x1583700,0x1aabd00,0x1fd4300,0x23dc780,0x2904d80,0x2d0d200,0x3235800,0x363dc80,0x3b66280,0x3f6e700,0x4496d00,0x4895400,0x4dbda00,0x51bc100,0x56e4700,0x1e80e80,0x1e80e80,0x1e80e80,0x1e80e80,0x1e80e80,0x1e80e80,0x1e80e80,0x1e80e80,0x1e80e80,0x1e80e80,0x1e80e80,0x1e80e80,0x1e80e80,310,1302,2110,3102,3910,4902,5894,6702,7694,8502,9494,10302,11294,12102,13094,13887,14879,15672,16664,5960,17934,17934,17934,17934,21,20,0,0,0,0,0,0,0x322c80,0x84b280,0xc53700,0x117bd00,0x1584180,0x1aac780,0x1fd4d80,0x23dd200,0x2905800,0x2d0dc80,0x3236280,0x363e700,0x3b66d00,0x3f6f180,0x4497780,0x4895e80,0x4dbe480,0x51bcb80,0x56e5180,0x1fd0400,0x5d6b400,0x5d6b400,0x5d6b400,0x5d6b400,0x5d6b400,0x5d6b400,0x5d6b400,0x5d6b400,0x5d6b400,0x5d6b400,0x5d6b400,0x5d6b400,311,1303,2111,3103,3911,4903,5895,6703,7695,8503,9495,10303,11295,12103,13095,13888,14880,15673,16665,17222,17935,17935,17935,17935,21,20,0,0,0,0,0,0,0x323700,0x84bd00,0xc54180,0x117c780,0x1584c00,0x1aad200,0x1fd5800,0x23ddc80,0x2906280,0x2d0e700,0x3236d00,0x363f180,0x3b67780,0x3f6fc00,0x4498200,0x4896900,0x4dbef00,0x51bd600,0x56e5c00,0x59a1f80,0x5d6be80,0x5d6be80,0x5d6be80,0x5d6be80,0x5d6be80,0x5d6be80,0x5d6be80,0x5d6be80,0x5d6be80,0x5d6be80,0x5d6be80,0x5d6be80,312,1304,2112,3104,4096,4904,5896,6704,7696,8504,9496,10304,11296,12288,13096,13889,14881,15674,16666,17223,17936,17936,17936,17936,21,20,0,0,0,0,0,0,0x324180,0x84c780,0xc54c00,0x117d200,0x16a5800,0x1aadc80,0x1fd6280,0x23de700,0x2906d00,0x2d0f180,0x3237780,0x363fc00,0x3b68200,0x4090800,0x4498c80,0x4897380,0x4dbf980,0x51be080,0x56e6680,0x59a2a00,0x5d6c900,0x5d6c900,0x5d6c900,0x5d6c900,0x5d6c900,0x5d6c900,0x5d6c900,0x5d6c900,0x5d6c900,0x5d6c900,0x5d6c900,0x5d6c900,313,1305,2113,3105,4097,4905,5897,6705,7697,8505,9497,10305,11297,12289,13097,13890,14882,15675,16667,6216,6216,6216,6216,6216,20,19,0,0,0,0,0,0,0x324c00,0x84d200,0xc55680,0x117dc80,0x16a6280,0x1aae700,0x1fd6d00,0x23df180,0x2907780,0x2d0fc00,0x3238200,0x3640680,0x3b68c80,0x4091280,0x4499700,0x4897e00,0x4dc0400,0x51beb00,0x56e7100,0x211f980,0x211f980,0x211f980,0x211f980,0x211f980,0x211f980,0x211f980,0x211f980,0x211f980,0x211f980,0x211f980,0x211f980,0x211f980,314,1306,2114,3106,4098,4906,5898,6706,7698,8506,9498,10306,11298,12290,13098,13891,14883,15676,16668,17408,17937,18232,18232,18232,22,21,0,0,0,0,0,0,0x325680,0x84dc80,0xc56100,0x117e700,0x16a6d00,0x1aaf180,0x1fd7780,0x23dfc00,0x2908200,0x2d10680,0x3238c80,0x3641100,0x3b69700,0x4091d00,0x449a180,0x4898880,0x4dc0e80,0x51bf580,0x56e7b80,0x5ac3600,0x5d6d380,0x5ed6280,0x5ed6280,0x5ed6280,0x5ed6280,0x5ed6280,0x5ed6280,0x5ed6280,0x5ed6280,0x5ed6280,0x5ed6280,0x5ed6280,315,1307,2115,3107,4099,4907,5899,6707,7699,8507,9499,10307,11299,12291,13099,13892,14884,15677,16669,17409,17938,17938,17938,17938,21,20,0,0,0,0,0,0,0x326100,0x84e700,0xc56b80,0x117f180,0x16a7780,0x1aafc00,0x1fd8200,0x23e0680,0x2908c80,0x2d11100,0x3239700,0x3641b80,0x3b6a180,0x4092780,0x449ac00,0x4899300,0x4dc1900,0x51c0000,0x56e8600,0x5ac4080,0x5d6de00,0x5d6de00,0x5d6de00,0x5d6de00,0x5d6de00,0x5d6de00,0x5d6de00,0x5d6de00,0x5d6de00,0x5d6de00,0x5d6de00,0x5d6de00,316,1308,2116,3108,4100,4908,5900,6708,7700,8508,9500,10308,11300,12292,13100,13893,14885,15678,16670,17410,17939,17939,17939,17939,21,20,0,0,0,0,0,0,0x326b80,0x84f180,0xc57600,0x117fc00,0x16a8200,0x1ab0680,0x1fd8c80,0x23e1100,0x2909700,0x2d11b80,0x323a180,0x3642600,0x3b6ac00,0x4093200,0x449b680,0x4899d80,0x4dc2380,0x51c0a80,0x56e9080,0x5ac4b00,0x5d6e880,0x5d6e880,0x5d6e880,0x5d6e880,0x5d6e880,0x5d6e880,0x5d6e880,0x5d6e880,0x5d6e880,0x5d6e880,0x5d6e880,0x5d6e880,317,1309,2117,3109,4101,4909,5901,6709,7701,8509,9501,10309,11301,12293,13101,13894,14886,15679,16671,6472,6472,6472,6472,6472,20,19,0,0,0,0,0,0,0x327600,0x84fc00,0xc58080,0x1180680,0x16a8c80,0x1ab1100,0x1fd9700,0x23e1b80,0x290a180,0x2d12600,0x323ac00,0x3643080,0x3b6b680,0x4093c80,0x449c100,0x489a800,0x4dc2e00,0x51c1500,0x56e9b00,0x226ef00,0x226ef00,0x226ef00,0x226ef00,0x226ef00,0x226ef00,0x226ef00,0x226ef00,0x226ef00,0x226ef00,0x226ef00,0x226ef00,0x226ef00,318,1310,2118,3110,4102,4910,5902,6710,7702,8510,9502,10310,11302,12294,13102,13895,14887,15680,16672,6728,17940,17940,17940,17940,21,20,0,0,0,0,0,0,0x328080,0x850680,0xc58b00,0x1181100,0x16a9700,0x1ab1b80,0x1fda180,0x23e2600,0x290ac00,0x2d13080,0x323b680,0x3643b00,0x3b6c100,0x4094700,0x449cb80,0x489b280,0x4dc3880,0x51c1f80,0x56ea580,0x23be480,0x5d6f300,0x5d6f300,0x5d6f300,0x5d6f300,0x5d6f300,0x5d6f300,0x5d6f300,0x5d6f300,0x5d6f300,0x5d6f300,0x5d6f300,0x5d6f300,319,1311,2119,3111,4103,4911,5903,6711,7703,8511,9503,10311,11303,12295,13103,14080,14888,15681,16673,17411,17941,17941,17941,17941,21,20,0,0,0,0,0,0,0x328b00,0x851100,0xc59580,0x1181b80,0x16aa180,0x1ab2600,0x1fdac00,0x23e3080,0x290b680,0x2d13b00,0x323c100,0x3644580,0x3b6cb80,0x4095180,0x449d600,0x49bbe80,0x4dc4300,0x51c2a00,0x56eb000,0x5ac5580,0x5d6fd80,0x5d6fd80,0x5d6fd80,0x5d6fd80,0x5d6fd80,0x5d6fd80,0x5d6fd80,0x5d6fd80,0x5d6fd80,0x5d6fd80,0x5d6fd80,0x5d6fd80,320,1312,2304,3112,4104,4912,5904,6712,7704,8512,9504,10496,11304,12296,13104,14081,14889,15682,16674,17412,17942,17942,17942,17942,21,20,0,0,0,0,0,0,0x329580,0x851b80,0xd7a180,0x1182600,0x16aac00,0x1ab3080,0x1fdb680,0x23e3b00,0x290c100,0x2d14580,0x323cb80,0x3765180,0x3b6d600,0x4095c00,0x449e080,0x49bc900,0x4dc4d80,0x51c3480,0x56eba80,0x5ac6000,0x5d70800,0x5d70800,0x5d70800,0x5d70800,0x5d70800,0x5d70800,0x5d70800,0x5d70800,0x5d70800,0x5d70800,0x5d70800,0x5d70800,321,1313,2305,3113,4105,4913,5905,6713,7705,8513,9505,10497,11305,12297,13105,14082,14890,15683,16675,6984,6984,6984,6984,6984,20,19,0,0,0,0,0,0,0x32a000,0x852600,0xd7ac00,0x1183080,0x16ab680,0x1ab3b00,0x1fdc100,0x23e4580,0x290cb80,0x2d15000,0x323d600,0x3765c00,0x3b6e080,0x4096680,0x449eb00,0x49bd380,0x4dc5800,0x51c3f00,0x56ec500,0x250da00,0x250da00,0x250da00,0x250da00,0x250da00,0x250da00,0x250da00,0x250da00,0x250da00,0x250da00,0x250da00,0x250da00,0x250da00,322,1314,2306,3114,4106,4914,5906,6714,7706,8514,9506,10498,11306,12298,13106,14083,14891,15684,16676,17413,17943,18233,18233,18233,22,21,0,0,0,0,0,0,0x32aa80,0x853080,0xd7b680,0x1183b00,0x16ac100,0x1ab4580,0x1fdcb80,0x23e5000,0x290d600,0x2d15a80,0x323e080,0x3766680,0x3b6eb00,0x4097100,0x449f580,0x49bde00,0x4dc6280,0x51c4980,0x56ecf80,0x5ac6a80,0x5d71280,0x5ed6d00,0x5ed6d00,0x5ed6d00,0x5ed6d00,0x5ed6d00,0x5ed6d00,0x5ed6d00,0x5ed6d00,0x5ed6d00,0x5ed6d00,0x5ed6d00,323,1315,2307,3115,4107,4915,5907,6715,7707,8515,9507,10499,11307,12299,13107,14084,14892,15685,16677,17414,17944,17944,17944,17944,21,20,0,0,0,0,0,0,0x32b500,0x853b00,0xd7c100,0x1184580,0x16acb80,0x1ab5000,0x1fdd600,0x23e5a80,0x290e080,0x2d16500,0x323eb00,0x3767100,0x3b6f580,0x4097b80,0x44a0000,0x49be880,0x4dc6d00,0x51c5400,0x56eda00,0x5ac7500,0x5d71d00,0x5d71d00,0x5d71d00,0x5d71d00,0x5d71d00,0x5d71d00,0x5d71d00,0x5d71d00,0x5d71d00,0x5d71d00,0x5d71d00,0x5d71d00,324,1316,2308,3116,4108,4916,5908,6716,7708,8516,9508,10500,11308,12300,13108,14085,14893,15686,16678,17415,17945,17945,17945,17945,21,20,0,0,0,0,0,0,0x32bf80,0x854580,0xd7cb80,0x1185000,0x16ad600,0x1ab5a80,0x1fde080,0x23e6500,0x290eb00,0x2d16f80,0x323f580,0x3767b80,0x3b70000,0x4098600,0x44a0a80,0x49bf300,0x4dc7780,0x51c5e80,0x56ee480,0x5ac7f80,0x5d72780,0x5d72780,0x5d72780,0x5d72780,0x5d72780,0x5d72780,0x5d72780,0x5d72780,0x5d72780,0x5d72780,0x5d72780,0x5d72780,325,1317,2309,3117,4109,4917,5909,6717,7709,8517,9509,10501,11309,12301,13109,14086,14894,15687,16679,7240,7240,7240,7240,7240,20,19,0,0,0,0,0,0,0x32ca00,0x855000,0xd7d600,0x1185a80,0x16ae080,0x1ab6500,0x1fdeb00,0x23e6f80,0x290f580,0x2d17a00,0x3240000,0x3768600,0x3b70a80,0x4099080,0x44a1500,0x49bfd80,0x4dc8200,0x51c6900,0x56eef00,0x265cf80,0x265cf80,0x265cf80,0x265cf80,0x265cf80,0x265cf80,0x265cf80,0x265cf80,0x265cf80,0x265cf80,0x265cf80,0x265cf80,0x265cf80,326,1318,2310,3118,4110,4918,5910,6718,7710,8518,9510,10502,11310,12302,13110,14087,14895,15872,16680,7496,17946,17946,17946,17946,21,20,0,0,0,0,0,0,0x32d480,0x855a80,0xd7e080,0x1186500,0x16aeb00,0x1ab6f80,0x1fdf580,0x23e7a00,0x2910000,0x2d18480,0x3240a80,0x3769080,0x3b71500,0x4099b00,0x44a1f80,0x49c0800,0x4dc8c80,0x52e7500,0x56ef980,0x27ac500,0x5d73200,0x5d73200,0x5d73200,0x5d73200,0x5d73200,0x5d73200,0x5d73200,0x5d73200,0x5d73200,0x5d73200,0x5d73200,0x5d73200,327,1319,2311,3119,4111,4919,5911,6719,7711,8519,9511,10503,11311,12303,13111,14088,14896,15873,16681,17416,17947,17947,17947,17947,21,20,0,0,0,0,0,0,0x32df00,0x856500,0xd7eb00,0x1186f80,0x16af580,0x1ab7a00,0x1fe0000,0x23e8480,0x2910a80,0x2d18f00,0x3241500,0x3769b00,0x3b71f80,0x409a580,0x44a2a00,0x49c1280,0x4dc9700,0x52e7f80,0x56f0400,0x5ac8a00,0x5d73c80,0x5d73c80,0x5d73c80,0x5d73c80,0x5d73c80,0x5d73c80,0x5d73c80,0x5d73c80,0x5d73c80,0x5d73c80,0x5d73c80,0x5d73c80,512,1320,2312,3120,4112,4920,5912,6720,7712,8704,9512,10504,11312,12304,13112,14089,14897,15874,16682,17417,17948,17948,17948,17948,21,20,0,0,0,0,0,0,0x44eb00,0x856f80,0xd7f580,0x1187a00,0x16b0000,0x1ab8480,0x1fe0a80,0x23e8f00,0x2911500,0x2e39b00,0x3241f80,0x376a580,0x3b72a00,0x409b000,0x44a3480,0x49c1d00,0x4dca180,0x52e8a00,0x56f0e80,0x5ac9480,0x5d74700,0x5d74700,0x5d74700,0x5d74700,0x5d74700,0x5d74700,0x5d74700,0x5d74700,0x5d74700,0x5d74700,0x5d74700,0x5d74700,513,1321,2313,3121,4113,4921,5913,6721,7713,8705,9513,10505,11313,12305,13113,14090,14898,15875,16683,7752,7752,7752,7752,7752,20,19,0,0,0,0,0,0,0x44f580,0x857a00,0xd80000,0x1188480,0x16b0a80,0x1ab8f00,0x1fe1500,0x23e9980,0x2911f80,0x2e3a580,0x3242a00,0x376b000,0x3b73480,0x409ba80,0x44a3f00,0x49c2780,0x4dcac00,0x52e9480,0x56f1900,0x28fba80,0x28fba80,0x28fba80,0x28fba80,0x28fba80,0x28fba80,0x28fba80,0x28fba80,0x28fba80,0x28fba80,0x28fba80,0x28fba80,0x28fba80,514,1322,2314,3122,4114,4922,5914,6722,7714,8706,9514,10506,11314,12306,13114,14091,14899,15876,16684,17418,17949,18234,18234,18234,22,21,0,0,0,0,0,0,0x450000,0x858480,0xd80a80,0x1188f00,0x16b1500,0x1ab9980,0x1fe1f80,0x23ea400,0x2912a00,0x2e3b000,0x3243480,0x376ba80,0x3b73f00,0x409c500,0x44a4980,0x49c3200,0x4dcb680,0x52e9f00,0x56f2380,0x5ac9f00,0x5d75180,0x5ed7780,0x5ed7780,0x5ed7780,0x5ed7780,0x5ed7780,0x5ed7780,0x5ed7780,0x5ed7780,0x5ed7780,0x5ed7780,0x5ed7780,515,1323,2315,3123,4115,4923,5915,6723,7715,8707,9515,10507,11315,12307,13115,14092,14900,15877,16685,17419,17950,17950,17950,17950,21,20,0,0,0,0,0,0,0x450a80,0x858f00,0xd81500,0x1189980,0x16b1f80,0x1aba400,0x1fe2a00,0x23eae80,0x2913480,0x2e3ba80,0x3243f00,0x376c500,0x3b74980,0x409cf80,0x44a5400,0x49c3c80,0x4dcc100,0x52ea980,0x56f2e00,0x5aca980,0x5d75c00,0x5d75c00,0x5d75c00,0x5d75c00,0x5d75c00,0x5d75c00,0x5d75c00,0x5d75c00,0x5d75c00,0x5d75c00,0x5d75c00,0x5d75c00,516,1324,2316,3124,4116,4924,5916,6724,7716,8708,9516,10508,11316,12308,13116,14093,14901,15878,16686,17420,17951,17951,17951,17951,21,20,0,0,0,0,0,0,0x451500,0x859980,0xd81f80,0x118a400,0x16b2a00,0x1abae80,0x1fe3480,0x23eb900,0x2913f00,0x2e3c500,0x3244980,0x376cf80,0x3b75400,0x409da00,0x44a5e80,0x49c4700,0x4dccb80,0x52eb400,0x56f3880,0x5acb400,0x5d76680,0x5d76680,0x5d76680,0x5d76680,0x5d76680,0x5d76680,0x5d76680,0x5d76680,0x5d76680,0x5d76680,0x5d76680,0x5d76680,517,1325,2317,3125,4117,4925,5917,6725,7717,8709,9517,10509,11317,12309,13117,14094,14902,15879,16687,8008,8008,8008,8008,8008,20,19,0,0,0,0,0,0,0x451f80,0x85a400,0xd82a00,0x118ae80,0x16b3480,0x1abb900,0x1fe3f00,0x23ec380,0x2914980,0x2e3cf80,0x3245400,0x376da00,0x3b75e80,0x409e480,0x44a6900,0x49c5180,0x4dcd600,0x52ebe80,0x56f4300,0x2a4b000,0x2a4b000,0x2a4b000,0x2a4b000,0x2a4b000,0x2a4b000,0x2a4b000,0x2a4b000,0x2a4b000,0x2a4b000,0x2a4b000,0x2a4b000,0x2a4b000,518,1326,2318,3126,4118,4926,5918,6726,7718,8710,9518,10510,11318,12310,13118,14095,14903,15880,16688,8264,17952,17952,17952,17952,21,20,0,0,0,0,0,0,0x452a00,0x85ae80,0xd83480,0x118b900,0x16b3f00,0x1abc380,0x1fe4980,0x23ece00,0x2915400,0x2e3da00,0x3245e80,0x376e480,0x3b76900,0x409ef00,0x44a7380,0x49c5c00,0x4dce080,0x52ec900,0x56f4d80,0x2b9a580,0x5d77100,0x5d77100,0x5d77100,0x5d77100,0x5d77100,0x5d77100,0x5d77100,0x5d77100,0x5d77100,0x5d77100,0x5d77100,0x5d77100,519,1327,2319,3127,4119,4927,5919,6727,7719,8711,9519,10511,11319,12311,13119,14096,14904,15881,16689,17421,17953,17953,17953,17953,21,20,0,0,0,0,0,0,0x453480,0x85b900,0xd83f00,0x118c380,0x16b4980,0x1abce00,0x1fe5400,0x23ed880,0x2915e80,0x2e3e480,0x3246900,0x376ef00,0x3b77380,0x409f980,0x44a7e00,0x49c6680,0x4dceb00,0x52ed380,0x56f5800,0x5acbe80,0x5d77b80,0x5d77b80,0x5d77b80,0x5d77b80,0x5d77b80,0x5d77b80,0x5d77b80,0x5d77b80,0x5d77b80,0x5d77b80,0x5d77b80,0x5d77b80,520,1328,2320,3128,4120,4928,5920,6912,7720,8712,9520,10512,11320,12312,13120,14097,14905,15882,16690,17422,17954,17954,17954,17954,21,20,0,0,0,0,0,0,0x453f00,0x85c380,0xd84980,0x118ce00,0x16b5400,0x1abd880,0x1fe5e80,0x250e480,0x2916900,0x2e3ef00,0x3247380,0x376f980,0x3b77e00,0x40a0400,0x44a8880,0x49c7100,0x4dcf580,0x52ede00,0x56f6280,0x5acc900,0x5d78600,0x5d78600,0x5d78600,0x5d78600,0x5d78600,0x5d78600,0x5d78600,0x5d78600,0x5d78600,0x5d78600,0x5d78600,0x5d78600,521,1329,2321,3129,4121,4929,5921,6913,7721,8713,9521,10513,11321,12313,13121,14098,14906,15883,16691,8520,8520,8520,8520,8520,20,19,0,0,0,0,0,0,0x454980,0x85ce00,0xd85400,0x118d880,0x16b5e80,0x1abe300,0x1fe6900,0x250ef00,0x2917380,0x2e3f980,0x3247e00,0x3770400,0x3b78880,0x40a0e80,0x44a9300,0x49c7b80,0x4dd0000,0x52ee880,0x56f6d00,0x2ce9b00,0x2ce9b00,0x2ce9b00,0x2ce9b00,0x2ce9b00,0x2ce9b00,0x2ce9b00,0x2ce9b00,0x2ce9b00,0x2ce9b00,0x2ce9b00,0x2ce9b00,0x2ce9b00,522,1330,2322,3130,4122,4930,5922,6914,7722,8714,9522,10514,11322,12314,13122,14099,14907,15884,16692,17423,17955,18235,18235,18235,22,21,0,0,0,0,0,0,0x455400,0x85d880,0xd85e80,0x118e300,0x16b6900,0x1abed80,0x1fe7380,0x250f980,0x2917e00,0x2e40400,0x3248880,0x3770e80,0x3b79300,0x40a1900,0x44a9d80,0x49c8600,0x4dd0a80,0x52ef300,0x56f7780,0x5acd380,0x5d79080,0x5ed8200,0x5ed8200,0x5ed8200,0x5ed8200,0x5ed8200,0x5ed8200,0x5ed8200,0x5ed8200,0x5ed8200,0x5ed8200,0x5ed8200,523,1331,2323,3131,4123,4931,5923,6915,7723,8715,9523,10515,11323,12315,13123,14100,14908,15885,16693,17424,17956,17956,17956,17956,21,20,0,0,0,0,0,0,0x455e80,0x85e300,0xd86900,0x118ed80,0x16b7380,0x1abf800,0x1fe7e00,0x2510400,0x2918880,0x2e40e80,0x3249300,0x3771900,0x3b79d80,0x40a2380,0x44aa800,0x49c9080,0x4dd1500,0x52efd80,0x56f8200,0x5acde00,0x5d79b00,0x5d79b00,0x5d79b00,0x5d79b00,0x5d79b00,0x5d79b00,0x5d79b00,0x5d79b00,0x5d79b00,0x5d79b00,0x5d79b00,0x5d79b00,524,1332,2324,3132,4124,4932,5924,6916,7724,8716,9524,10516,11324,12316,13124,14101,14909,15886,16694,17425,17957,17957,17957,17957,21,20,0,0,0,0,0,0,0x456900,0x85ed80,0xd87380,0x118f800,0x16b7e00,0x1ac0280,0x1fe8880,0x2510e80,0x2919300,0x2e41900,0x3249d80,0x3772380,0x3b7a800,0x40a2e00,0x44ab280,0x49c9b00,0x4dd1f80,0x52f0800,0x56f8c80,0x5ace880,0x5d7a580,0x5d7a580,0x5d7a580,0x5d7a580,0x5d7a580,0x5d7a580,0x5d7a580,0x5d7a580,0x5d7a580,0x5d7a580,0x5d7a580,0x5d7a580,525,1333,2325,3133,4125,4933,5925,6917,7725,8717,9525,10517,11325,12317,13125,14102,14910,15887,16695,8776,8776,8776,8776,8776,20,19,0,0,0,0,0,0,0x457380,0x85f800,0xd87e00,0x1190280,0x16b8880,0x1ac0d00,0x1fe9300,0x2511900,0x2919d80,0x2e42380,0x324a800,0x3772e00,0x3b7b280,0x40a3880,0x44abd00,0x49ca580,0x4dd2a00,0x52f1280,0x56f9700,0x2e39080,0x2e39080,0x2e39080,0x2e39080,0x2e39080,0x2e39080,0x2e39080,0x2e39080,0x2e39080,0x2e39080,0x2e39080,0x2e39080,0x2e39080,526,1334,2326,3134,4126,4934,5926,6918,7726,8718,9526,10518,11326,12318,13126,14103,14911,15888,16696,9032,17958,17958,17958,17958,21,20,0,0,0,0,0,0,0x457e00,0x860280,0xd88880,0x1190d00,0x16b9300,0x1ac1780,0x1fe9d80,0x2512380,0x291a800,0x2e42e00,0x324b280,0x3773880,0x3b7bd00,0x40a4300,0x44ac780,0x49cb000,0x4dd3480,0x52f1d00,0x56fa180,0x2f88600,0x5d7b000,0x5d7b000,0x5d7b000,0x5d7b000,0x5d7b000,0x5d7b000,0x5d7b000,0x5d7b000,0x5d7b000,0x5d7b000,0x5d7b000,0x5d7b000,527,1335,2327,3135,4127,4935,5927,6919,7727,8719,9527,10519,11327,12319,13127,14104,14912,15889,16697,17426,17959,17959,17959,17959,21,20,0,0,0,0,0,0,0x458880,0x860d00,0xd89300,0x1191780,0x16b9d80,0x1ac2200,0x1fea800,0x2512e00,0x291b280,0x2e43880,0x324bd00,0x3774300,0x3b7c780,0x40a4d80,0x44ad200,0x49cba80,0x4dd3f00,0x52f2780,0x56fac00,0x5acf300,0x5d7ba80,0x5d7ba80,0x5d7ba80,0x5d7ba80,0x5d7ba80,0x5d7ba80,0x5d7ba80,0x5d7ba80,0x5d7ba80,0x5d7ba80,0x5d7ba80,0x5d7ba80,528,1336,2328,3136,4128,5120,5928,6920,7728,8720,9528,10520,11328,12320,13312,14105,14913,15890,16698,17427,17960,17960,17960,17960,21,20,0,0,0,0,0,0,0x459300,0x861780,0xd89d80,0x1192200,0x16ba800,0x1be2e00,0x1feb280,0x2513880,0x291bd00,0x2e44300,0x324c780,0x3774d80,0x3b7d200,0x40a5800,0x45cde00,0x49cc500,0x4dd4980,0x52f3200,0x56fb680,0x5acfd80,0x5d7c500,0x5d7c500,0x5d7c500,0x5d7c500,0x5d7c500,0x5d7c500,0x5d7c500,0x5d7c500,0x5d7c500,0x5d7c500,0x5d7c500,0x5d7c500,529,1337,2329,3137,4129,5121,5929,6921,7729,8721,9529,10521,11329,12321,13313,14106,14914,15891,16699,9288,9288,9288,9288,9288,20,19,0,0,0,0,0,0,0x459d80,0x862200,0xd8a800,0x1192c80,0x16bb280,0x1be3880,0x1febd00,0x2514300,0x291c780,0x2e44d80,0x324d200,0x3775800,0x3b7dc80,0x40a6280,0x45ce880,0x49ccf80,0x4dd5400,0x52f3c80,0x56fc100,0x30d7b80,0x30d7b80,0x30d7b80,0x30d7b80,0x30d7b80,0x30d7b80,0x30d7b80,0x30d7b80,0x30d7b80,0x30d7b80,0x30d7b80,0x30d7b80,0x30d7b80,530,1338,2330,3138,4130,5122,5930,6922,7730,8722,9530,10522,11330,12322,13314,14107,14915,15892,16700,17428,17961,18236,18236,18236,22,21,0,0,0,0,0,0,0x45a800,0x862c80,0xd8b280,0x1193700,0x16bbd00,0x1be4300,0x1fec780,0x2514d80,0x291d200,0x2e45800,0x324dc80,0x3776280,0x3b7e700,0x40a6d00,0x45cf300,0x49cda00,0x4dd5e80,0x52f4700,0x56fcb80,0x5ad0800,0x5d7cf80,0x5ed8c80,0x5ed8c80,0x5ed8c80,0x5ed8c80,0x5ed8c80,0x5ed8c80,0x5ed8c80,0x5ed8c80,0x5ed8c80,0x5ed8c80,0x5ed8c80,531,1339,2331,3139,4131,5123,5931,6923,7731,8723,9531,10523,11331,12323,13315,14108,14916,15893,16701,17429,17962,17962,17962,17962,21,20,0,0,0,0,0,0,0x45b280,0x863700,0xd8bd00,0x1194180,0x16bc780,0x1be4d80,0x1fed200,0x2515800,0x291dc80,0x2e46280,0x324e700,0x3776d00,0x3b7f180,0x40a7780,0x45cfd80,0x49ce480,0x4dd6900,0x52f5180,0x56fd600,0x5ad1280,0x5d7da00,0x5d7da00,0x5d7da00,0x5d7da00,0x5d7da00,0x5d7da00,0x5d7da00,0x5d7da00,0x5d7da00,0x5d7da00,0x5d7da00,0x5d7da00,532,1340,2332,3140,4132,5124,5932,6924,7732,8724,9532,10524,11332,12324,13316,14109,14917,15894,16702,17430,17963,17963,17963,17963,21,20,0,0,0,0,0,0,0x45bd00,0x864180,0xd8c780,0x1194c00,0x16bd200,0x1be5800,0x1fedc80,0x2516280,0x291e700,0x2e46d00,0x324f180,0x3777780,0x3b7fc00,0x40a8200,0x45d0800,0x49cef00,0x4dd7380,0x52f5c00,0x56fe080,0x5ad1d00,0x5d7e480,0x5d7e480,0x5d7e480,0x5d7e480,0x5d7e480,0x5d7e480,0x5d7e480,0x5d7e480,0x5d7e480,0x5d7e480,0x5d7e480,0x5d7e480,533,1341,2333,3141,4133,5125,5933,6925,7733,8725,9533,10525,11333,12325,13317,14110,14918,15895,16703,9544,9544,9544,9544,9544,20,19,0,0,0,0,0,0,0x45c780,0x864c00,0xd8d200,0x1195680,0x16bdc80,0x1be6280,0x1fee700,0x2516d00,0x291f180,0x2e47780,0x324fc00,0x3778200,0x3b80680,0x40a8c80,0x45d1280,0x49cf980,0x4dd7e00,0x52f6680,0x56feb00,0x3227100,0x3227100,0x3227100,0x3227100,0x3227100,0x3227100,0x3227100,0x3227100,0x3227100,0x3227100,0x3227100,0x3227100,0x3227100,534,1342,2334,3142,4134,5126,5934,6926,7734,8726,9534,10526,11334,12326,13318,14111,14919,15896,16704,9800,17964,17964,17964,17964,21,20,0,0,0,0,0,0,0x45d200,0x865680,0xd8dc80,0x1196100,0x16be700,0x1be6d00,0x1fef180,0x2517780,0x291fc00,0x2e48200,0x3250680,0x3778c80,0x3b81100,0x40a9700,0x45d1d00,0x49d0400,0x4dd8880,0x52f7100,0x56ff580,0x3376680,0x5d7ef00,0x5d7ef00,0x5d7ef00,0x5d7ef00,0x5d7ef00,0x5d7ef00,0x5d7ef00,0x5d7ef00,0x5d7ef00,0x5d7ef00,0x5d7ef00,0x5d7ef00,535,1343,2335,3143,4135,5127,5935,6927,7735,8727,9535,10527,11335,12327,13319,14112,15104,15897,16705,17431,17965,17965,17965,17965,21,20,0,0,0,0,0,0,0x45dc80,0x866100,0xd8e700,0x1196b80,0x16bf180,0x1be7780,0x1fefc00,0x2518200,0x2920680,0x2e48c80,0x3251100,0x3779700,0x3b81b80,0x40aa180,0x45d2780,0x49d0e80,0x4ef9480,0x52f7b80,0x5700000,0x5ad2780,0x5d7f980,0x5d7f980,0x5d7f980,0x5d7f980,0x5d7f980,0x5d7f980,0x5d7f980,0x5d7f980,0x5d7f980,0x5d7f980,0x5d7f980,0x5d7f980,536,1344,2336,3328,4136,5128,5936,6928,7736,8728,9536,10528,11520,12328,13320,14113,15105,15898,16706,17432,17966,17966,17966,17966,21,20,0,0,0,0,0,0,0x45e700,0x866b80,0xd8f180,0x12b7780,0x16bfc00,0x1be8200,0x1ff0680,0x2518c80,0x2921100,0x2e49700,0x3251b80,0x377a180,0x3ca2780,0x40aac00,0x45d3200,0x49d1900,0x4ef9f00,0x52f8600,0x5700a80,0x5ad3200,0x5d80400,0x5d80400,0x5d80400,0x5d80400,0x5d80400,0x5d80400,0x5d80400,0x5d80400,0x5d80400,0x5d80400,0x5d80400,0x5d80400,537,1345,2337,3329,4137,5129,5937,6929,7737,8729,9537,10529,11521,12329,13321,14114,15106,15899,16707,10056,10056,10056,10056,10056,20,19,0,0,0,0,0,0,0x45f180,0x867600,0xd8fc00,0x12b8200,0x16c0680,0x1be8c80,0x1ff1100,0x2519700,0x2921b80,0x2e4a180,0x3252600,0x377ac00,0x3ca3200,0x40ab680,0x45d3c80,0x49d2380,0x4efa980,0x52f9080,0x5701500,0x34c5c00,0x34c5c00,0x34c5c00,0x34c5c00,0x34c5c00,0x34c5c00,0x34c5c00,0x34c5c00,0x34c5c00,0x34c5c00,0x34c5c00,0x34c5c00,0x34c5c00,538,1346,2338,3330,4138,5130,5938,6930,7738,8730,9538,10530,11522,12330,13322,14115,15107,15900,16708,17433,17967,18237,18237,18237,22,21,0,0,0,0,0,0,0x45fc00,0x868080,0xd90680,0x12b8c80,0x16c1100,0x1be9700,0x1ff1b80,0x251a180,0x2922600,0x2e4ac00,0x3253080,0x377b680,0x3ca3c80,0x40ac100,0x45d4700,0x49d2e00,0x4efb400,0x52f9b00,0x5701f80,0x5ad3c80,0x5d80e80,0x5ed9700,0x5ed9700,0x5ed9700,0x5ed9700,0x5ed9700,0x5ed9700,0x5ed9700,0x5ed9700,0x5ed9700,0x5ed9700,0x5ed9700,539,1347,2339,3331,4139,5131,5939,6931,7739,8731,9539,10531,11523,12331,13323,14116,15108,15901,16709,17434,17968,17968,17968,17968,21,20,0,0,0,0,0,0,0x460680,0x868b00,0xd91100,0x12b9700,0x16c1b80,0x1bea180,0x1ff2600,0x251ac00,0x2923080,0x2e4b680,0x3253b00,0x377c100,0x3ca4700,0x40acb80,0x45d5180,0x49d3880,0x4efbe80,0x52fa580,0x5702a00,0x5ad4700,0x5d81900,0x5d81900,0x5d81900,0x5d81900,0x5d81900,0x5d81900,0x5d81900,0x5d81900,0x5d81900,0x5d81900,0x5d81900,0x5d81900,540,1348,2340,3332,4140,5132,5940,6932,7740,8732,9540,10532,11524,12332,13324,14117,15109,15902,16710,17435,17969,17969,17969,17969,21,20,0,0,0,0,0,0,0x461100,0x869580,0xd91b80,0x12ba180,0x16c2600,0x1beac00,0x1ff3080,0x251b680,0x2923b00,0x2e4c100,0x3254580,0x377cb80,0x3ca5180,0x40ad600,0x45d5c00,0x49d4300,0x4efc900,0x52fb000,0x5703480,0x5ad5180,0x5d82380,0x5d82380,0x5d82380,0x5d82380,0x5d82380,0x5d82380,0x5d82380,0x5d82380,0x5d82380,0x5d82380,0x5d82380,0x5d82380,541,1349,2341,3333,4141,5133,5941,6933,7741,8733,9541,10533,11525,12333,13325,14118,15110,15903,16711,10312,10312,10312,10312,10312,20,19,0,0,0,0,0,0,0x461b80,0x86a000,0xd92600,0x12bac00,0x16c3080,0x1beb680,0x1ff3b00,0x251c100,0x2924580,0x2e4cb80,0x3255000,0x377d600,0x3ca5c00,0x40ae080,0x45d6680,0x49d4d80,0x4efd380,0x52fba80,0x5703f00,0x3615180,0x3615180,0x3615180,0x3615180,0x3615180,0x3615180,0x3615180,0x3615180,0x3615180,0x3615180,0x3615180,0x3615180,0x3615180,542,1350,2342,3334,4142,5134,5942,6934,7742,8734,9542,10534,11526,12334,13326,14119,15111,15904,16896,10568,17970,17970,17970,17970,21,20,0,0,0,0,0,0,0x462600,0x86aa80,0xd93080,0x12bb680,0x16c3b00,0x1bec100,0x1ff4580,0x251cb80,0x2925000,0x2e4d600,0x3255a80,0x377e080,0x3ca6680,0x40aeb00,0x45d7100,0x49d5800,0x4efde00,0x52fc500,0x5824b00,0x3764700,0x5d82e00,0x5d82e00,0x5d82e00,0x5d82e00,0x5d82e00,0x5d82e00,0x5d82e00,0x5d82e00,0x5d82e00,0x5d82e00,0x5d82e00,0x5d82e00,543,1351,2343,3335,4143,5135,5943,6935,7743,8735,9543,10535,11527,12335,13327,14120,15112,15905,16897,17436,17971,17971,17971,17971,21,20,0,0,0,0,0,0,0x463080,0x86b500,0xd93b00,0x12bc100,0x16c4580,0x1becb80,0x1ff5000,0x251d600,0x2925a80,0x2e4e080,0x3256500,0x377eb00,0x3ca7100,0x40af580,0x45d7b80,0x49d6280,0x4efe880,0x52fcf80,0x5825580,0x5ad5c00,0x5d83880,0x5d83880,0x5d83880,0x5d83880,0x5d83880,0x5d83880,0x5d83880,0x5d83880,0x5d83880,0x5d83880,0x5d83880,0x5d83880,544,1536,2344,3336,4144,5136,5944,6936,7744,8736,9728,10536,11528,12336,13328,14121,15113,15906,16898,17437,17972,17972,17972,17972,21,20,0,0,0,0,0,0,0x463b00,0x98c100,0xd94580,0x12bcb80,0x16c5000,0x1bed600,0x1ff5a80,0x251e080,0x2926500,0x2e4eb00,0x3377100,0x377f580,0x3ca7b80,0x40b0000,0x45d8600,0x49d6d00,0x4eff300,0x52fda00,0x5826000,0x5ad6680,0x5d84300,0x5d84300,0x5d84300,0x5d84300,0x5d84300,0x5d84300,0x5d84300,0x5d84300,0x5d84300,0x5d84300,0x5d84300,0x5d84300,545,1537,2345,3337,4145,5137,5945,6937,7745,8737,9729,10537,11529,12337,13329,14122,15114,15907,16899,10824,10824,10824,10824,10824,20,19,0,0,0,0,0,0,0x464580,0x98cb80,0xd95000,0x12bd600,0x16c5a80,0x1bee080,0x1ff6500,0x251eb00,0x2926f80,0x2e4f580,0x3377b80,0x3780000,0x3ca8600,0x40b0a80,0x45d9080,0x49d7780,0x4effd80,0x52fe480,0x5826a80,0x38b3c80,0x38b3c80,0x38b3c80,0x38b3c80,0x38b3c80,0x38b3c80,0x38b3c80,0x38b3c80,0x38b3c80,0x38b3c80,0x38b3c80,0x38b3c80,0x38b3c80,546,1538,2346,3338,4146,5138,5946,6938,7746,8738,9730,10538,11530,12338,13330,14123,15115,15908,16900,17438,17973,18238,18238,18238,22,21,0,0,0,0,0,0,0x465000,0x98d600,0xd95a80,0x12be080,0x16c6500,0x1beeb00,0x1ff6f80,0x251f580,0x2927a00,0x2e50000,0x3378600,0x3780a80,0x3ca9080,0x40b1500,0x45d9b00,0x49d8200,0x4f00800,0x52fef00,0x5827500,0x5ad7100,0x5d84d80,0x5eda180,0x5eda180,0x5eda180,0x5eda180,0x5eda180,0x5eda180,0x5eda180,0x5eda180,0x5eda180,0x5eda180,0x5eda180,547,1539,2347,3339,4147,5139,5947,6939,7747,8739,9731,10539,11531,12339,13331,14124,15116,15909,16901,17439,17974,17974,17974,17974,21,20,0,0,0,0,0,0,0x465a80,0x98e080,0xd96500,0x12beb00,0x16c6f80,0x1bef580,0x1ff7a00,0x2520000,0x2928480,0x2e50a80,0x3379080,0x3781500,0x3ca9b00,0x40b1f80,0x45da580,0x49d8c80,0x4f01280,0x52ff980,0x5827f80,0x5ad7b80,0x5d85800,0x5d85800,0x5d85800,0x5d85800,0x5d85800,0x5d85800,0x5d85800,0x5d85800,0x5d85800,0x5d85800,0x5d85800,0x5d85800,548,1540,2348,3340,4148,5140,5948,6940,7748,8740,9732,10540,11532,12340,13332,14125,15117,15910,16902,17440,17975,17975,17975,17975,21,20,0,0,0,0,0,0,0x466500,0x98eb00,0xd96f80,0x12bf580,0x16c7a00,0x1bf0000,0x1ff8480,0x2520a80,0x2928f00,0x2e51500,0x3379b00,0x3781f80,0x3caa580,0x40b2a00,0x45db000,0x49d9700,0x4f01d00,0x5300400,0x5828a00,0x5ad8600,0x5d86280,0x5d86280,0x5d86280,0x5d86280,0x5d86280,0x5d86280,0x5d86280,0x5d86280,0x5d86280,0x5d86280,0x5d86280,0x5d86280,549,1541,2349,3341,4149,5141,5949,6941,7749,8741,9733,10541,11533,12341,13333,14126,15118,15911,16903,11080,11080,11080,11080,11080,20,19,0,0,0,0,0,0,0x466f80,0x98f580,0xd97a00,0x12c0000,0x16c8480,0x1bf0a80,0x1ff8f00,0x2521500,0x2929980,0x2e51f80,0x337a580,0x3782a00,0x3cab000,0x40b3480,0x45dba80,0x49da180,0x4f02780,0x5300e80,0x5829480,0x3a03200,0x3a03200,0x3a03200,0x3a03200,0x3a03200,0x3a03200,0x3a03200,0x3a03200,0x3a03200,0x3a03200,0x3a03200,0x3a03200,0x3a03200,550,1542,2350,3342,4150,5142,5950,6942,7750,8742,9734,10542,11534,12342,13334,14127,15119,15912,16904,11336,17976,17976,17976,17976,21,20,0,0,0,0,0,0,0x467a00,0x990000,0xd98480,0x12c0a80,0x16c8f00,0x1bf1500,0x1ff9980,0x2521f80,0x292a400,0x2e52a00,0x337b000,0x3783480,0x3caba80,0x40b3f00,0x45dc500,0x49dac00,0x4f03200,0x5301900,0x5829f00,0x3b52780,0x5d86d00,0x5d86d00,0x5d86d00,0x5d86d00,0x5d86d00,0x5d86d00,0x5d86d00,0x5d86d00,0x5d86d00,0x5d86d00,0x5d86d00,0x5d86d00,551,1543,2351,3343,4151,5143,5951,6943,7751,8743,9735,10543,11535,12343,13335,14128,15120,15913,16905,17441,17977,17977,17977,17977,21,20,0,0,0,0,0,0,0x468480,0x990a80,0xd98f00,0x12c1500,0x16c9980,0x1bf1f80,0x1ffa400,0x2522a00,0x292ae80,0x2e53480,0x337ba80,0x3783f00,0x3cac500,0x40b4980,0x45dcf80,0x49db680,0x4f03c80,0x5302380,0x582a980,0x5ad9080,0x5d87780,0x5d87780,0x5d87780,0x5d87780,0x5d87780,0x5d87780,0x5d87780,0x5d87780,0x5d87780,0x5d87780,0x5d87780,0x5d87780,552,1544,2352,3344,4152,5144,5952,6944,7936,8744,9736,10544,11536,12344,13336,14129,15121,15914,16906,17442,17978,17978,17978,17978,21,20,0,0,0,0,0,0,0x468f00,0x991500,0xd99980,0x12c1f80,0x16ca400,0x1bf2a00,0x1ffae80,0x2523480,0x2a4ba80,0x2e53f00,0x337c500,0x3784980,0x3cacf80,0x40b5400,0x45dda00,0x49dc100,0x4f04700,0x5302e00,0x582b400,0x5ad9b00,0x5d88200,0x5d88200,0x5d88200,0x5d88200,0x5d88200,0x5d88200,0x5d88200,0x5d88200,0x5d88200,0x5d88200,0x5d88200,0x5d88200,553,1545,2353,3345,4153,5145,5953,6945,7937,8745,9737,10545,11537,12345,13337,14130,15122,15915,16907,11592,11592,11592,11592,11592,20,19,0,0,0,0,0,0,0x469980,0x991f80,0xd9a400,0x12c2a00,0x16cae80,0x1bf3480,0x1ffb900,0x2523f00,0x2a4c500,0x2e54980,0x337cf80,0x3785400,0x3cada00,0x40b5e80,0x45de480,0x49dcb80,0x4f05180,0x5303880,0x582be80,0x3ca1d00,0x3ca1d00,0x3ca1d00,0x3ca1d00,0x3ca1d00,0x3ca1d00,0x3ca1d00,0x3ca1d00,0x3ca1d00,0x3ca1d00,0x3ca1d00,0x3ca1d00,0x3ca1d00,554,1546,2354,3346,4154,5146,5954,6946,7938,8746,9738,10546,11538,12346,13338,14131,15123,15916,16908,17443,17979,18239,18239,18239,22,21,0,0,0,0,0,0,0x46a400,0x992a00,0xd9ae80,0x12c3480,0x16cb900,0x1bf3f00,0x1ffc380,0x2524980,0x2a4cf80,0x2e55400,0x337da00,0x3785e80,0x3cae480,0x40b6900,0x45def00,0x49dd600,0x4f05c00,0x5304300,0x582c900,0x5ada580,0x5d88c80,0x5edac00,0x5edac00,0x5edac00,0x5edac00,0x5edac00,0x5edac00,0x5edac00,0x5edac00,0x5edac00,0x5edac00,0x5edac00,555,1547,2355,3347,4155,5147,5955,6947,7939,8747,9739,10547,11539,12347,13339,14132,15124,15917,16909,17444,17980,17980,17980,17980,21,20,0,0,0,0,0,0,0x46ae80,0x993480,0xd9b900,0x12c3f00,0x16cc380,0x1bf4980,0x1ffce00,0x2525400,0x2a4da00,0x2e55e80,0x337e480,0x3786900,0x3caef00,0x40b7380,0x45df980,0x49de080,0x4f06680,0x5304d80,0x582d380,0x5adb000,0x5d89700,0x5d89700,0x5d89700,0x5d89700,0x5d89700,0x5d89700,0x5d89700,0x5d89700,0x5d89700,0x5d89700,0x5d89700,0x5d89700,556,1548,2356,3348,4156,5148,5956,6948,7940,8748,9740,10548,11540,12348,13340,14133,15125,15918,16910,17445,17981,17981,17981,17981,21,20,0,0,0,0,0,0,0x46b900,0x993f00,0xd9c380,0x12c4980,0x16cce00,0x1bf5400,0x1ffd880,0x2525e80,0x2a4e480,0x2e56900,0x337ef00,0x3787380,0x3caf980,0x40b7e00,0x45e0400,0x49deb00,0x4f07100,0x5305800,0x582de00,0x5adba80,0x5d8a180,0x5d8a180,0x5d8a180,0x5d8a180,0x5d8a180,0x5d8a180,0x5d8a180,0x5d8a180,0x5d8a180,0x5d8a180,0x5d8a180,0x5d8a180,557,1549,2357,3349,4157,5149,5957,6949,7941,8749,9741,10549,11541,12349,13341,14134,15126,15919,16911,11848,11848,11848,11848,11848,20,19,0,0,0,0,0,0,0x46c380,0x994980,0xd9ce00,0x12c5400,0x16cd880,0x1bf5e80,0x1ffe300,0x2526900,0x2a4ef00,0x2e57380,0x337f980,0x3787e00,0x3cb0400,0x40b8880,0x45e0e80,0x49df580,0x4f07b80,0x5306280,0x582e880,0x3df1280,0x3df1280,0x3df1280,0x3df1280,0x3df1280,0x3df1280,0x3df1280,0x3df1280,0x3df1280,0x3df1280,0x3df1280,0x3df1280,0x3df1280,558,1550,2358,3350,4158,5150,5958,6950,7942,8750,9742,10550,11542,12350,13342,14135,15127,15920,16912,12104,17982,17982,17982,17982,21,20,0,0,0,0,0,0,0x46ce00,0x995400,0xd9d880,0x12c5e80,0x16ce300,0x1bf6900,0x1ffed80,0x2527380,0x2a4f980,0x2e57e00,0x3380400,0x3788880,0x3cb0e80,0x40b9300,0x45e1900,0x49e0000,0x4f08600,0x5306d00,0x582f300,0x3f40800,0x5d8ac00,0x5d8ac00,0x5d8ac00,0x5d8ac00,0x5d8ac00,0x5d8ac00,0x5d8ac00,0x5d8ac00,0x5d8ac00,0x5d8ac00,0x5d8ac00,0x5d8ac00,559,1551,2359,3351,4159,5151,5959,6951,7943,8751,9743,10551,11543,12351,13343,14136,15128,15921,16913,17446,17983,17983,17983,17983,21,20,0,0,0,0,0,0,0x46d880,0x995e80,0xd9e300,0x12c6900,0x16ced80,0x1bf7380,0x1fff800,0x2527e00,0x2a50400,0x2e58880,0x3380e80,0x3789300,0x3cb1900,0x40b9d80,0x45e2380,0x49e0a80,0x4f09080,0x5307780,0x582fd80,0x5adc500,0x5d8b680,0x5d8b680,0x5d8b680,0x5d8b680,0x5d8b680,0x5d8b680,0x5d8b680,0x5d8b680,0x5d8b680,0x5d8b680,0x5d8b680,0x5d8b680,560,1552,2360,3352,4160,5152,6144,6952,7944,8752,9744,10552,11544,12352,13344,14137,15129,15922,16914,17447,17984,17984,17984,17984,21,20,0,0,0,0,0,0,0x46e300,0x996900,0xd9ed80,0x12c7380,0x16cf800,0x1bf7e00,0x2120400,0x2528880,0x2a50e80,0x2e59300,0x3381900,0x3789d80,0x3cb2380,0x40ba800,0x45e2e00,0x49e1500,0x4f09b00,0x5308200,0x5830800,0x5adcf80,0x5d8c100,0x5d8c100,0x5d8c100,0x5d8c100,0x5d8c100,0x5d8c100,0x5d8c100,0x5d8c100,0x5d8c100,0x5d8c100,0x5d8c100,0x5d8c100,561,1553,2361,3353,4161,5153,6145,6953,7945,8753,9745,10553,11545,12353,13345,14138,15130,15923,16915,12360,12360,12360,12360,12360,20,19,0,0,0,0,0,0,0x46ed80,0x997380,0xd9f800,0x12c7e00,0x16d0280,0x1bf8880,0x2120e80,0x2529300,0x2a51900,0x2e59d80,0x3382380,0x378a800,0x3cb2e00,0x40bb280,0x45e3880,0x49e1f80,0x4f0a580,0x5308c80,0x5831280,0x408fd80,0x408fd80,0x408fd80,0x408fd80,0x408fd80,0x408fd80,0x408fd80,0x408fd80,0x408fd80,0x408fd80,0x408fd80,0x408fd80,0x408fd80,562,1554,2362,3354,4162,5154,6146,6954,7946,8754,9746,10554,11546,12354,13346,14139,15131,15924,16916,17448,17985,18240,18240,18240,22,21,0,0,0,0,0,0,0x46f800,0x997e00,0xda0280,0x12c8880,0x16d0d00,0x1bf9300,0x2121900,0x2529d80,0x2a52380,0x2e5a800,0x3382e00,0x378b280,0x3cb3880,0x40bbd00,0x45e4300,0x49e2a00,0x4f0b000,0x5309700,0x5831d00,0x5adda00,0x5d8cb80,0x5edb680,0x5edb680,0x5edb680,0x5edb680,0x5edb680,0x5edb680,0x5edb680,0x5edb680,0x5edb680,0x5edb680,0x5edb680,563,1555,2363,3355,4163,5155,6147,6955,7947,8755,9747,10555,11547,12355,13347,14140,15132,15925,16917,17449,17986,17986,17986,17986,21,20,0,0,0,0,0,0,0x470280,0x998880,0xda0d00,0x12c9300,0x16d1780,0x1bf9d80,0x2122380,0x252a800,0x2a52e00,0x2e5b280,0x3383880,0x378bd00,0x3cb4300,0x40bc780,0x45e4d80,0x49e3480,0x4f0ba80,0x530a180,0x5832780,0x5ade480,0x5d8d600,0x5d8d600,0x5d8d600,0x5d8d600,0x5d8d600,0x5d8d600,0x5d8d600,0x5d8d600,0x5d8d600,0x5d8d600,0x5d8d600,0x5d8d600,564,1556,2364,3356,4164,5156,6148,6956,7948,8756,9748,10556,11548,12356,13348,14141,15133,15926,16918,17450,17987,17987,17987,17987,21,20,0,0,0,0,0,0,0x470d00,0x999300,0xda1780,0x12c9d80,0x16d2200,0x1bfa800,0x2122e00,0x252b280,0x2a53880,0x2e5bd00,0x3384300,0x378c780,0x3cb4d80,0x40bd200,0x45e5800,0x49e3f00,0x4f0c500,0x530ac00,0x5833200,0x5adef00,0x5d8e080,0x5d8e080,0x5d8e080,0x5d8e080,0x5d8e080,0x5d8e080,0x5d8e080,0x5d8e080,0x5d8e080,0x5d8e080,0x5d8e080,0x5d8e080,565,1557,2365,3357,4165,5157,6149,6957,7949,8757,9749,10557,11549,12357,13349,14142,15134,15927,16919,12616,12616,12616,12616,12616,20,19,0,0,0,0,0,0,0x471780,0x999d80,0xda2200,0x12ca800,0x16d2c80,0x1bfb280,0x2123880,0x252bd00,0x2a54300,0x2e5c780,0x3384d80,0x378d200,0x3cb5800,0x40bdc80,0x45e6280,0x49e4980,0x4f0cf80,0x530b680,0x5833c80,0x41df300,0x41df300,0x41df300,0x41df300,0x41df300,0x41df300,0x41df300,0x41df300,0x41df300,0x41df300,0x41df300,0x41df300,0x41df300,566,1558,2366,3358,4166,5158,6150,6958,7950,8758,9750,10558,11550,12358,13350,14143,15135,15928,16920,12872,17988,17988,17988,17988,21,20,0,0,0,0,0,0,0x472200,0x99a800,0xda2c80,0x12cb280,0x16d3700,0x1bfbd00,0x2124300,0x252c780,0x2a54d80,0x2e5d200,0x3385800,0x378dc80,0x3cb6280,0x40be700,0x45e6d00,0x49e5400,0x4f0da00,0x530c100,0x5834700,0x432e880,0x5d8eb00,0x5d8eb00,0x5d8eb00,0x5d8eb00,0x5d8eb00,0x5d8eb00,0x5d8eb00,0x5d8eb00,0x5d8eb00,0x5d8eb00,0x5d8eb00,0x5d8eb00,567,1559,2367,3359,4167,5159,6151,6959,7951,8759,9751,10559,11551,12359,13351,14144,15136,15929,16921,17451,17989,17989,17989,17989,21,20,0,0,0,0,0,0,0x472c80,0x99b280,0xda3700,0x12cbd00,0x16d4180,0x1bfc780,0x2124d80,0x252d200,0x2a55800,0x2e5dc80,0x3386280,0x378e700,0x3cb6d00,0x40bf180,0x45e7780,0x49e5e80,0x4f0e480,0x530cb80,0x5835180,0x5adf980,0x5d8f580,0x5d8f580,0x5d8f580,0x5d8f580,0x5d8f580,0x5d8f580,0x5d8f580,0x5d8f580,0x5d8f580,0x5d8f580,0x5d8f580,0x5d8f580,568,1560,2368,3360,4352,5160,6152,6960,7952,8760,9752,10560,11552,12544,13352,14145,15137,15930,16922,17452,17990,17990,17990,17990,21,20,0,0,0,0,0,0,0x473700,0x99bd00,0xda4180,0x12cc780,0x17f4d80,0x1bfd200,0x2125800,0x252dc80,0x2a56280,0x2e5e700,0x3386d00,0x378f180,0x3cb7780,0x41dfd80,0x45e8200,0x49e6900,0x4f0ef00,0x530d600,0x5835c00,0x5ae0400,0x5d90000,0x5d90000,0x5d90000,0x5d90000,0x5d90000,0x5d90000,0x5d90000,0x5d90000,0x5d90000,0x5d90000,0x5d90000,0x5d90000,569,1561,2369,3361,4353,5161,6153,6961,7953,8761,9753,10561,11553,12545,13353,14146,15138,15931,16923,13128,13128,13128,13128,13128,20,19,0,0,0,0,0,0,0x474180,0x99c780,0xda4c00,0x12cd200,0x17f5800,0x1bfdc80,0x2126280,0x252e700,0x2a56d00,0x2e5f180,0x3387780,0x378fc00,0x3cb8200,0x41e0800,0x45e8c80,0x49e7380,0x4f0f980,0x530e080,0x5836680,0x447de00,0x447de00,0x447de00,0x447de00,0x447de00,0x447de00,0x447de00,0x447de00,0x447de00,0x447de00,0x447de00,0x447de00,0x447de00,570,1562,2370,3362,4354,5162,6154,6962,7954,8762,9754,10562,11554,12546,13354,14147,15139,15932,16924,17453,17991,18241,18241,18241,22,21,0,0,0,0,0,0,0x474c00,0x99d200,0xda5680,0x12cdc80,0x17f6280,0x1bfe700,0x2126d00,0x252f180,0x2a57780,0x2e5fc00,0x3388200,0x3790680,0x3cb8c80,0x41e1280,0x45e9700,0x49e7e00,0x4f10400,0x530eb00,0x5837100,0x5ae0e80,0x5d90a80,0x5edc100,0x5edc100,0x5edc100,0x5edc100,0x5edc100,0x5edc100,0x5edc100,0x5edc100,0x5edc100,0x5edc100,0x5edc100,571,1563,2371,3363,4355,5163,6155,6963,7955,8763,9755,10563,11555,12547,13355,14148,15140,15933,16925,17454,18176,18176,18176,18176,21,20,0,0,0,0,0,0,0x475680,0x99dc80,0xda6100,0x12ce700,0x17f6d00,0x1bff180,0x2127780,0x252fc00,0x2a58200,0x2e60680,0x3388c80,0x3791100,0x3cb9700,0x41e1d00,0x45ea180,0x49e8880,0x4f10e80,0x530f580,0x5837b80,0x5ae1900,0x5eb1680,0x5eb1680,0x5eb1680,0x5eb1680,0x5eb1680,0x5eb1680,0x5eb1680,0x5eb1680,0x5eb1680,0x5eb1680,0x5eb1680,0x5eb1680,572,1564,2372,3364,4356,5164,6156,6964,7956,8764,9756,10564,11556,12548,13356,14149,15141,15934,16926,17455,18177,18177,18177,18177,21,20,0,0,0,0,0,0,0x476100,0x99e700,0xda6b80,0x12cf180,0x17f7780,0x1bffc00,0x2128200,0x2530680,0x2a58c80,0x2e61100,0x3389700,0x3791b80,0x3cba180,0x41e2780,0x45eac00,0x49e9300,0x4f11900,0x5310000,0x5838600,0x5ae2380,0x5eb2100,0x5eb2100,0x5eb2100,0x5eb2100,0x5eb2100,0x5eb2100,0x5eb2100,0x5eb2100,0x5eb2100,0x5eb2100,0x5eb2100,0x5eb2100,573,1565,2373,3365,4357,5165,6157,6965,7957,8765,9757,10565,11557,12549,13357,14150,15142,15935,16927,13384,13384,13384,13384,13384,20,19,0,0,0,0,0,0,0x476b80,0x99f180,0xda7600,0x12cfc00,0x17f8200,0x1c00680,0x2128c80,0x2531100,0x2a59700,0x2e61b80,0x338a180,0x3792600,0x3cbac00,0x41e3200,0x45eb680,0x49e9d80,0x4f12380,0x5310a80,0x5839080,0x45cd380,0x45cd380,0x45cd380,0x45cd380,0x45cd380,0x45cd380,0x45cd380,0x45cd380,0x45cd380,0x45cd380,0x45cd380,0x45cd380,0x45cd380,574,1566,2374,3366,4358,5166,6158,6966,7958,8766,9758,10566,11558,12550,13358,14151,15143,15936,16928,13640,18178,18178,18178,18178,21,20,0,0,0,0,0,0,0x477600,0x99fc00,0xda8080,0x12d0680,0x17f8c80,0x1c01100,0x2129700,0x2531b80,0x2a5a180,0x2e62600,0x338ac00,0x3793080,0x3cbb680,0x41e3c80,0x45ec100,0x49ea800,0x4f12e00,0x5311500,0x5839b00,0x471c900,0x5eb2b80,0x5eb2b80,0x5eb2b80,0x5eb2b80,0x5eb2b80,0x5eb2b80,0x5eb2b80,0x5eb2b80,0x5eb2b80,0x5eb2b80,0x5eb2b80,0x5eb2b80,575,1567,2375,3367,4359,5167,6159,6967,7959,8767,9759,10567,11559,12551,13359,14336,15144,15937,16929,17456,18179,18179,18179,18179,21,20,0,0,0,0,0,0,0x478080,0x9a0680,0xda8b00,0x12d1100,0x17f9700,0x1c01b80,0x212a180,0x2532600,0x2a5ac00,0x2e63080,0x338b680,0x3793b00,0x3cbc100,0x41e4700,0x45ecb80,0x4b0b400,0x4f13880,0x5311f80,0x583a580,0x5ae2e00,0x5eb3600,0x5eb3600,0x5eb3600,0x5eb3600,0x5eb3600,0x5eb3600,0x5eb3600,0x5eb3600,0x5eb3600,0x5eb3600,0x5eb3600,0x5eb3600,576,1568,2560,3368,4360,5168,6160,6968,7960,8768,9760,10752,11560,12552,13360,14337,15145,15938,16930,17457,18180,18180,18180,18180,21,20,0,0,0,0,0,0,0x478b00,0x9a1100,0xec9700,0x12d1b80,0x17fa180,0x1c02600,0x212ac00,0x2533080,0x2a5b680,0x2e63b00,0x338c100,0x38b4700,0x3cbcb80,0x41e5180,0x45ed600,0x4b0be80,0x4f14300,0x5312a00,0x583b000,0x5ae3880,0x5eb4080,0x5eb4080,0x5eb4080,0x5eb4080,0x5eb4080,0x5eb4080,0x5eb4080,0x5eb4080,0x5eb4080,0x5eb4080,0x5eb4080,0x5eb4080,577,1569,2561,3369,4361,5169,6161,6969,7961,8769,9761,10753,11561,12553,13361,14338,15146,15939,16931,13896,13896,13896,13896,13896,20,19,0,0,0,0,0,0,0x479580,0x9a1b80,0xeca180,0x12d2600,0x17fac00,0x1c03080,0x212b680,0x2533b00,0x2a5c100,0x2e64580,0x338cb80,0x38b5180,0x3cbd600,0x41e5c00,0x45ee080,0x4b0c900,0x4f14d80,0x5313480,0x583ba80,0x486be80,0x486be80,0x486be80,0x486be80,0x486be80,0x486be80,0x486be80,0x486be80,0x486be80,0x486be80,0x486be80,0x486be80,0x486be80,578,1570,2562,3370,4362,5170,6162,6970,7962,8770,9762,10754,11562,12554,13362,14339,15147,15940,16932,17458,18181,18242,18242,18242,22,21,0,0,0,0,0,0,0x47a000,0x9a2600,0xecac00,0x12d3080,0x17fb680,0x1c03b00,0x212c100,0x2534580,0x2a5cb80,0x2e65000,0x338d600,0x38b5c00,0x3cbe080,0x41e6680,0x45eeb00,0x4b0d380,0x4f15800,0x5313f00,0x583c500,0x5ae4300,0x5eb4b00,0x5edcb80,0x5edcb80,0x5edcb80,0x5edcb80,0x5edcb80,0x5edcb80,0x5edcb80,0x5edcb80,0x5edcb80,0x5edcb80,0x5edcb80,579,1571,2563,3371,4363,5171,6163,6971,7963,8771,9763,10755,11563,12555,13363,14340,15148,15941,16933,17459,18182,18182,18182,18182,21,20,0,0,0,0,0,0,0x47aa80,0x9a3080,0xecb680,0x12d3b00,0x17fc100,0x1c04580,0x212cb80,0x2535000,0x2a5d600,0x2e65a80,0x338e080,0x38b6680,0x3cbeb00,0x41e7100,0x45ef580,0x4b0de00,0x4f16280,0x5314980,0x583cf80,0x5ae4d80,0x5eb5580,0x5eb5580,0x5eb5580,0x5eb5580,0x5eb5580,0x5eb5580,0x5eb5580,0x5eb5580,0x5eb5580,0x5eb5580,0x5eb5580,0x5eb5580,580,1572,2564,3372,4364,5172,6164,6972,7964,8772,9764,10756,11564,12556,13364,14341,15149,15942,16934,17460,18183,18183,18183,18183,21,20,0,0,0,0,0,0,0x47b500,0x9a3b00,0xecc100,0x12d4580,0x17fcb80,0x1c05000,0x212d600,0x2535a80,0x2a5e080,0x2e66500,0x338eb00,0x38b7100,0x3cbf580,0x41e7b80,0x45f0000,0x4b0e880,0x4f16d00,0x5315400,0x583da00,0x5ae5800,0x5eb6000,0x5eb6000,0x5eb6000,0x5eb6000,0x5eb6000,0x5eb6000,0x5eb6000,0x5eb6000,0x5eb6000,0x5eb6000,0x5eb6000,0x5eb6000,581,1573,2565,3373,4365,5173,6165,6973,7965,8773,9765,10757,11565,12557,13365,14342,15150,15943,16935,14152,14152,14152,14152,14152,20,19,0,0,0,0,0,0,0x47bf80,0x9a4580,0xeccb80,0x12d5000,0x17fd600,0x1c05a80,0x212e080,0x2536500,0x2a5eb00,0x2e66f80,0x338f580,0x38b7b80,0x3cc0000,0x41e8600,0x45f0a80,0x4b0f300,0x4f17780,0x5315e80,0x583e480,0x49bb400,0x49bb400,0x49bb400,0x49bb400,0x49bb400,0x49bb400,0x49bb400,0x49bb400,0x49bb400,0x49bb400,0x49bb400,0x49bb400,0x49bb400,582,1574,2566,3374,4366,5174,6166,6974,7966,8774,9766,10758,11566,12558,13366,14343,15151,16128,16936,14408,18184,18184,18184,18184,21,20,0,0,0,0,0,0,0x47ca00,0x9a5000,0xecd600,0x12d5a80,0x17fe080,0x1c06500,0x212eb00,0x2536f80,0x2a5f580,0x2e67a00,0x3390000,0x38b8600,0x3cc0a80,0x41e9080,0x45f1500,0x4b0fd80,0x4f18200,0x5436a80,0x583ef00,0x4b0a980,0x5eb6a80,0x5eb6a80,0x5eb6a80,0x5eb6a80,0x5eb6a80,0x5eb6a80,0x5eb6a80,0x5eb6a80,0x5eb6a80,0x5eb6a80,0x5eb6a80,0x5eb6a80,583,1575,2567,3375,4367,5175,6167,6975,7967,8775,9767,10759,11567,12559,13367,14344,15152,16129,16937,17461,18185,18185,18185,18185,21,20,0,0,0,0,0,0,0x47d480,0x9a5a80,0xece080,0x12d6500,0x17feb00,0x1c06f80,0x212f580,0x2537a00,0x2a60000,0x2e68480,0x3390a80,0x38b9080,0x3cc1500,0x41e9b00,0x45f1f80,0x4b10800,0x4f18c80,0x5437500,0x583f980,0x5ae6280,0x5eb7500,0x5eb7500,0x5eb7500,0x5eb7500,0x5eb7500,0x5eb7500,0x5eb7500,0x5eb7500,0x5eb7500,0x5eb7500,0x5eb7500,0x5eb7500,768,1576,2568,3376,4368,5176,6168,6976,7968,8960,9768,10760,11568,12560,13368,14345,15153,16130,16938,17462,18186,18186,18186,18186,21,20,0,0,0,0,0,0,0x59e080,0x9a6500,0xeceb00,0x12d6f80,0x17ff580,0x1c07a00,0x2130000,0x2538480,0x2a60a80,0x2f89080,0x3391500,0x38b9b00,0x3cc1f80,0x41ea580,0x45f2a00,0x4b11280,0x4f19700,0x5437f80,0x5840400,0x5ae6d00,0x5eb7f80,0x5eb7f80,0x5eb7f80,0x5eb7f80,0x5eb7f80,0x5eb7f80,0x5eb7f80,0x5eb7f80,0x5eb7f80,0x5eb7f80,0x5eb7f80,0x5eb7f80,769,1577,2569,3377,4369,5177,6169,6977,7969,8961,9769,10761,11569,12561,13369,14346,15154,16131,16939,14664,14664,14664,14664,14664,20,19,0,0,0,0,0,0,0x59eb00,0x9a6f80,0xecf580,0x12d7a00,0x1800000,0x1c08480,0x2130a80,0x2538f00,0x2a61500,0x2f89b00,0x3391f80,0x38ba580,0x3cc2a00,0x41eb000,0x45f3480,0x4b11d00,0x4f1a180,0x5438a00,0x5840e80,0x4c59f00,0x4c59f00,0x4c59f00,0x4c59f00,0x4c59f00,0x4c59f00,0x4c59f00,0x4c59f00,0x4c59f00,0x4c59f00,0x4c59f00,0x4c59f00,0x4c59f00,770,1578,2570,3378,4370,5178,6170,6978,7970,8962,9770,10762,11570,12562,13370,14347,15155,16132,16940,17463,18187,18243,18243,18243,22,21,0,0,0,0,0,0,0x59f580,0x9a7a00,0xed0000,0x12d8480,0x1800a80,0x1c08f00,0x2131500,0x2539980,0x2a61f80,0x2f8a580,0x3392a00,0x38bb000,0x3cc3480,0x41eba80,0x45f3f00,0x4b12780,0x4f1ac00,0x5439480,0x5841900,0x5ae7780,0x5eb8a00,0x5edd600,0x5edd600,0x5edd600,0x5edd600,0x5edd600,0x5edd600,0x5edd600,0x5edd600,0x5edd600,0x5edd600,0x5edd600,771,1579,2571,3379,4371,5179,6171,6979,7971,8963,9771,10763,11571,12563,13371,14348,15156,16133,16941,17464,18188,18188,18188,18188,21,20,0,0,0,0,0,0,0x5a0000,0x9a8480,0xed0a80,0x12d8f00,0x1801500,0x1c09980,0x2131f80,0x253a400,0x2a62a00,0x2f8b000,0x3393480,0x38bba80,0x3cc3f00,0x41ec500,0x45f4980,0x4b13200,0x4f1b680,0x5439f00,0x5842380,0x5ae8200,0x5eb9480,0x5eb9480,0x5eb9480,0x5eb9480,0x5eb9480,0x5eb9480,0x5eb9480,0x5eb9480,0x5eb9480,0x5eb9480,0x5eb9480,0x5eb9480,772,1580,2572,3380,4372,5180,6172,6980,7972,8964,9772,10764,11572,12564,13372,14349,15157,16134,16942,17465,18189,18189,18189,18189,21,20,0,0,0,0,0,0,0x5a0a80,0x9a8f00,0xed1500,0x12d9980,0x1801f80,0x1c0a400,0x2132a00,0x253ae80,0x2a63480,0x2f8ba80,0x3393f00,0x38bc500,0x3cc4980,0x41ecf80,0x45f5400,0x4b13c80,0x4f1c100,0x543a980,0x5842e00,0x5ae8c80,0x5eb9f00,0x5eb9f00,0x5eb9f00,0x5eb9f00,0x5eb9f00,0x5eb9f00,0x5eb9f00,0x5eb9f00,0x5eb9f00,0x5eb9f00,0x5eb9f00,0x5eb9f00,773,1581,2573,3381,4373,5181,6173,6981,7973,8965,9773,10765,11573,12565,13373,14350,15158,16135,16943,14920,14920,14920,14920,14920,20,19,0,0,0,0,0,0,0x5a1500,0x9a9980,0xed1f80,0x12da400,0x1802a00,0x1c0ae80,0x2133480,0x253b900,0x2a63f00,0x2f8c500,0x3394980,0x38bcf80,0x3cc5400,0x41eda00,0x45f5e80,0x4b14700,0x4f1cb80,0x543b400,0x5843880,0x4da9480,0x4da9480,0x4da9480,0x4da9480,0x4da9480,0x4da9480,0x4da9480,0x4da9480,0x4da9480,0x4da9480,0x4da9480,0x4da9480,0x4da9480,774,1582,2574,3382,4374,5182,6174,6982,7974,8966,9774,10766,11574,12566,13374,14351,15159,16136,16944,15176,18190,18190,18190,18190,21,20,0,0,0,0,0,0,0x5a1f80,0x9aa400,0xed2a00,0x12dae80,0x1803480,0x1c0b900,0x2133f00,0x253c380,0x2a64980,0x2f8cf80,0x3395400,0x38bda00,0x3cc5e80,0x41ee480,0x45f6900,0x4b15180,0x4f1d600,0x543be80,0x5844300,0x4ef8a00,0x5eba980,0x5eba980,0x5eba980,0x5eba980,0x5eba980,0x5eba980,0x5eba980,0x5eba980,0x5eba980,0x5eba980,0x5eba980,0x5eba980,775,1583,2575,3383,4375,5183,6175,6983,7975,8967,9775,10767,11575,12567,13375,14352,15160,16137,16945,17466,18191,18191,18191,18191,21,20,0,0,0,0,0,0,0x5a2a00,0x9aae80,0xed3480,0x12db900,0x1803f00,0x1c0c380,0x2134980,0x253ce00,0x2a65400,0x2f8da00,0x3395e80,0x38be480,0x3cc6900,0x41eef00,0x45f7380,0x4b15c00,0x4f1e080,0x543c900,0x5844d80,0x5ae9700,0x5ebb400,0x5ebb400,0x5ebb400,0x5ebb400,0x5ebb400,0x5ebb400,0x5ebb400,0x5ebb400,0x5ebb400,0x5ebb400,0x5ebb400,0x5ebb400,776,1584,2576,3384,4376,5184,6176,7168,7976,8968,9776,10768,11576,12568,13376,14353,15161,16138,16946,17467,18192,18192,18192,18192,21,20,0,0,0,0,0,0,0x5a3480,0x9ab900,0xed3f00,0x12dc380,0x1804980,0x1c0ce00,0x2135400,0x265da00,0x2a65e80,0x2f8e480,0x3396900,0x38bef00,0x3cc7380,0x41ef980,0x45f7e00,0x4b16680,0x4f1eb00,0x543d380,0x5845800,0x5aea180,0x5ebbe80,0x5ebbe80,0x5ebbe80,0x5ebbe80,0x5ebbe80,0x5ebbe80,0x5ebbe80,0x5ebbe80,0x5ebbe80,0x5ebbe80,0x5ebbe80,0x5ebbe80,777,1585,2577,3385,4377,5185,6177,7169,7977,8969,9777,10769,11577,12569,13377,14354,15162,16139,16947,15432,15432,15432,15432,15432,20,19,0,0,0,0,0,0,0x5a3f00,0x9ac380,0xed4980,0x12dce00,0x1805400,0x1c0d880,0x2135e80,0x265e480,0x2a66900,0x2f8ef00,0x3397380,0x38bf980,0x3cc7e00,0x41f0400,0x45f8880,0x4b17100,0x4f1f580,0x543de00,0x5846280,0x5047f80,0x5047f80,0x5047f80,0x5047f80,0x5047f80,0x5047f80,0x5047f80,0x5047f80,0x5047f80,0x5047f80,0x5047f80,0x5047f80,0x5047f80,778,1586,2578,3386,4378,5186,6178,7170,7978,8970,9778,10770,11578,12570,13378,14355,15163,16140,16948,17468,18193,18244,18244,18244,22,21,0,0,0,0,0,0,0x5a4980,0x9ace00,0xed5400,0x12dd880,0x1805e80,0x1c0e300,0x2136900,0x265ef00,0x2a67380,0x2f8f980,0x3397e00,0x38c0400,0x3cc8880,0x41f0e80,0x45f9300,0x4b17b80,0x4f20000,0x543e880,0x5846d00,0x5aeac00,0x5ebc900,0x5ede080,0x5ede080,0x5ede080,0x5ede080,0x5ede080,0x5ede080,0x5ede080,0x5ede080,0x5ede080,0x5ede080,0x5ede080,779,1587,2579,3387,4379,5187,6179,7171,7979,8971,9779,10771,11579,12571,13379,14356,15164,16141,16949,17469,18194,18194,18194,18194,21,20,0,0,0,0,0,0,0x5a5400,0x9ad880,0xed5e80,0x12de300,0x1806900,0x1c0ed80,0x2137380,0x265f980,0x2a67e00,0x2f90400,0x3398880,0x38c0e80,0x3cc9300,0x41f1900,0x45f9d80,0x4b18600,0x4f20a80,0x543f300,0x5847780,0x5aeb680,0x5ebd380,0x5ebd380,0x5ebd380,0x5ebd380,0x5ebd380,0x5ebd380,0x5ebd380,0x5ebd380,0x5ebd380,0x5ebd380,0x5ebd380,0x5ebd380,780,1588,2580,3388,4380,5188,6180,7172,7980,8972,9780,10772,11580,12572,13380,14357,15165,16142,16950,17470,18195,18195,18195,18195,21,20,0,0,0,0,0,0,0x5a5e80,0x9ae300,0xed6900,0x12ded80,0x1807380,0x1c0f800,0x2137e00,0x2660400,0x2a68880,0x2f90e80,0x3399300,0x38c1900,0x3cc9d80,0x41f2380,0x45fa800,0x4b19080,0x4f21500,0x543fd80,0x5848200,0x5aec100,0x5ebde00,0x5ebde00,0x5ebde00,0x5ebde00,0x5ebde00,0x5ebde00,0x5ebde00,0x5ebde00,0x5ebde00,0x5ebde00,0x5ebde00,0x5ebde00,781,1589,2581,3389,4381,5189,6181,7173,7981,8973,9781,10773,11581,12573,13381,14358,15166,16143,16951,15688,15688,15688,15688,15688,20,19,0,0,0,0,0,0,0x5a6900,0x9aed80,0xed7380,0x12df800,0x1807e00,0x1c10280,0x2138880,0x2660e80,0x2a69300,0x2f91900,0x3399d80,0x38c2380,0x3cca800,0x41f2e00,0x45fb280,0x4b19b00,0x4f21f80,0x5440800,0x5848c80,0x5197500,0x5197500,0x5197500,0x5197500,0x5197500,0x5197500,0x5197500,0x5197500,0x5197500,0x5197500,0x5197500,0x5197500,0x5197500,782,1590,2582,3390,4382,5190,6182,7174,7982,8974,9782,10774,11582,12574,13382,14359,15167,16144,16952,15944,18196,18196,18196,18196,21,20,0,0,0,0,0,0,0x5a7380,0x9af800,0xed7e00,0x12e0280,0x1808880,0x1c10d00,0x2139300,0x2661900,0x2a69d80,0x2f92380,0x339a800,0x38c2e00,0x3ccb280,0x41f3880,0x45fbd00,0x4b1a580,0x4f22a00,0x5441280,0x5849700,0x52e6a80,0x5ebe880,0x5ebe880,0x5ebe880,0x5ebe880,0x5ebe880,0x5ebe880,0x5ebe880,0x5ebe880,0x5ebe880,0x5ebe880,0x5ebe880,0x5ebe880,783,1591,2583,3391,4383,5191,6183,7175,7983,8975,9783,10775,11583,12575,13383,14360,15168,16145,16953,17471,18197,18197,18197,18197,21,20,0,0,0,0,0,0,0x5a7e00,0x9b0280,0xed8880,0x12e0d00,0x1809300,0x1c11780,0x2139d80,0x2662380,0x2a6a800,0x2f92e00,0x339b280,0x38c3880,0x3ccbd00,0x41f4300,0x45fc780,0x4b1b000,0x4f23480,0x5441d00,0x584a180,0x5aecb80,0x5ebf300,0x5ebf300,0x5ebf300,0x5ebf300,0x5ebf300,0x5ebf300,0x5ebf300,0x5ebf300,0x5ebf300,0x5ebf300,0x5ebf300,0x5ebf300,784,1592,2584,3392,4384,5376,6184,7176,7984,8976,9784,10776,11584,12576,13568,14361,15169,16146,16954,17472,18198,18198,18198,18198,21,20,0,0,0,0,0,0,0x5a8880,0x9b0d00,0xed9300,0x12e1780,0x1809d80,0x1d32380,0x213a800,0x2662e00,0x2a6b280,0x2f93880,0x339bd00,0x38c4300,0x3ccc780,0x41f4d80,0x471d380,0x4b1ba80,0x4f23f00,0x5442780,0x584ac00,0x5aed600,0x5ebfd80,0x5ebfd80,0x5ebfd80,0x5ebfd80,0x5ebfd80,0x5ebfd80,0x5ebfd80,0x5ebfd80,0x5ebfd80,0x5ebfd80,0x5ebfd80,0x5ebfd80,785,1593,2585,3393,4385,5377,6185,7177,7985,8977,9785,10777,11585,12577,13569,14362,15170,16147,16955,16200,16200,16200,16200,16200,20,19,0,0,0,0,0,0,0x5a9300,0x9b1780,0xed9d80,0x12e2200,0x180a800,0x1d32e00,0x213b280,0x2663880,0x2a6bd00,0x2f94300,0x339c780,0x38c4d80,0x3ccd200,0x41f5800,0x471de00,0x4b1c500,0x4f24980,0x5443200,0x584b680,0x5436000,0x5436000,0x5436000,0x5436000,0x5436000,0x5436000,0x5436000,0x5436000,0x5436000,0x5436000,0x5436000,0x5436000,0x5436000,786,1594,2586,3394,4386,5378,6186,7178,7986,8978,9786,10778,11586,12578,13570,14363,15171,16148,16956,17473,18199,18245,18245,18245,22,21,0,0,0,0,0,0,0x5a9d80,0x9b2200,0xeda800,0x12e2c80,0x180b280,0x1d33880,0x213bd00,0x2664300,0x2a6c780,0x2f94d80,0x339d200,0x38c5800,0x3ccdc80,0x41f6280,0x471e880,0x4b1cf80,0x4f25400,0x5443c80,0x584c100,0x5aee080,0x5ec0800,0x5edeb00,0x5edeb00,0x5edeb00,0x5edeb00,0x5edeb00,0x5edeb00,0x5edeb00,0x5edeb00,0x5edeb00,0x5edeb00,0x5edeb00,787,1595,2587,3395,4387,5379,6187,7179,7987,8979,9787,10779,11587,12579,13571,14364,15172,16149,16957,17474,18200,18200,18200,18200,21,20,0,0,0,0,0,0,0x5aa800,0x9b2c80,0xedb280,0x12e3700,0x180bd00,0x1d34300,0x213c780,0x2664d80,0x2a6d200,0x2f95800,0x339dc80,0x38c6280,0x3cce700,0x41f6d00,0x471f300,0x4b1da00,0x4f25e80,0x5444700,0x584cb80,0x5aeeb00,0x5ec1280,0x5ec1280,0x5ec1280,0x5ec1280,0x5ec1280,0x5ec1280,0x5ec1280,0x5ec1280,0x5ec1280,0x5ec1280,0x5ec1280,0x5ec1280,788,1596,2588,3396,4388,5380,6188,7180,7988,8980,9788,10780,11588,12580,13572,14365,15173,16150,16958,17475,18201,18201,18201,18201,21,20,0,0,0,0,0,0,0x5ab280,0x9b3700,0xedbd00,0x12e4180,0x180c780,0x1d34d80,0x213d200,0x2665800,0x2a6dc80,0x2f96280,0x339e700,0x38c6d00,0x3ccf180,0x41f7780,0x471fd80,0x4b1e480,0x4f26900,0x5445180,0x584d600,0x5aef580,0x5ec1d00,0x5ec1d00,0x5ec1d00,0x5ec1d00,0x5ec1d00,0x5ec1d00,0x5ec1d00,0x5ec1d00,0x5ec1d00,0x5ec1d00,0x5ec1d00,0x5ec1d00,789,1597,2589,3397,4389,5381,6189,7181,7989,8981,9789,10781,11589,12581,13573,14366,15174,16151,16959,16456,16456,16456,16456,16456,20,19,0,0,0,0,0,0,0x5abd00,0x9b4180,0xedc780,0x12e4c00,0x180d200,0x1d35800,0x213dc80,0x2666280,0x2a6e700,0x2f96d00,0x339f180,0x38c7780,0x3ccfc00,0x41f8200,0x4720800,0x4b1ef00,0x4f27380,0x5445c00,0x584e080,0x5585580,0x5585580,0x5585580,0x5585580,0x5585580,0x5585580,0x5585580,0x5585580,0x5585580,0x5585580,0x5585580,0x5585580,0x5585580,790,1598,2590,3398,4390,5382,6190,7182,7990,8982,9790,10782,11590,12582,13574,14367,15175,16152,16960,16712,18202,18202,18202,18202,21,20,0,0,0,0,0,0,0x5ac780,0x9b4c00,0xedd200,0x12e5680,0x180dc80,0x1d36280,0x213e700,0x2666d00,0x2a6f180,0x2f97780,0x339fc00,0x38c8200,0x3cd0680,0x41f8c80,0x4721280,0x4b1f980,0x4f27e00,0x5446680,0x584eb00,0x56d4b00,0x5ec2780,0x5ec2780,0x5ec2780,0x5ec2780,0x5ec2780,0x5ec2780,0x5ec2780,0x5ec2780,0x5ec2780,0x5ec2780,0x5ec2780,0x5ec2780,791,1599,2591,3399,4391,5383,6191,7183,7991,8983,9791,10783,11591,12583,13575,14368,15360,16153,16961,17476,18203,18203,18203,18203,21,20,0,0,0,0,0,0,0x5ad200,0x9b5680,0xeddc80,0x12e6100,0x180e700,0x1d36d00,0x213f180,0x2667780,0x2a6fc00,0x2f98200,0x33a0680,0x38c8c80,0x3cd1100,0x41f9700,0x4721d00,0x4b20400,0x5048a00,0x5447100,0x584f580,0x5af0000,0x5ec3200,0x5ec3200,0x5ec3200,0x5ec3200,0x5ec3200,0x5ec3200,0x5ec3200,0x5ec3200,0x5ec3200,0x5ec3200,0x5ec3200,0x5ec3200,792,1600,2592,3584,4392,5384,6192,7184,7992,8984,9792,10784,11776,12584,13576,14369,15361,16154,16962,17477,18204,18204,18204,18204,21,20,0,0,0,0,0,0,0x5adc80,0x9b6100,0xede700,0x1406d00,0x180f180,0x1d37780,0x213fc00,0x2668200,0x2a70680,0x2f98c80,0x33a1100,0x38c9700,0x3df1d00,0x41fa180,0x4722780,0x4b20e80,0x5049480,0x5447b80,0x5850000,0x5af0a80,0x5ec3c80,0x5ec3c80,0x5ec3c80,0x5ec3c80,0x5ec3c80,0x5ec3c80,0x5ec3c80,0x5ec3c80,0x5ec3c80,0x5ec3c80,0x5ec3c80,0x5ec3c80,793,1601,2593,3585,4393,5385,6193,7185,7993,8985,9793,10785,11777,12585,13577,14370,15362,16155,16963,16968,16968,16968,16968,16968,20,19,0,0,0,0,0,0,0x5ae700,0x9b6b80,0xedf180,0x1407780,0x180fc00,0x1d38200,0x2140680,0x2668c80,0x2a71100,0x2f99700,0x33a1b80,0x38ca180,0x3df2780,0x41fac00,0x4723200,0x4b21900,0x5049f00,0x5448600,0x5850a80,0x5824080,0x5824080,0x5824080,0x5824080,0x5824080,0x5824080,0x5824080,0x5824080,0x5824080,0x5824080,0x5824080,0x5824080,0x5824080,794,1602,2594,3586,4394,5386,6194,7186,7994,8986,9794,10786,11778,12586,13578,14371,15363,16156,16964,17478,18205,18246,18246,18246,22,21,0,0,0,0,0,0,0x5af180,0x9b7600,0xedfc00,0x1408200,0x1810680,0x1d38c80,0x2141100,0x2669700,0x2a71b80,0x2f9a180,0x33a2600,0x38cac00,0x3df3200,0x41fb680,0x4723c80,0x4b22380,0x504a980,0x5449080,0x5851500,0x5af1500,0x5ec4700,0x5edf580,0x5edf580,0x5edf580,0x5edf580,0x5edf580,0x5edf580,0x5edf580,0x5edf580,0x5edf580,0x5edf580,0x5edf580,795,1603,2595,3587,4395,5387,6195,7187,7995,8987,9795,10787,11779,12587,13579,14372,15364,16157,16965,17479,18206,18206,18206,18206,21,20,0,0,0,0,0,0,0x5afc00,0x9b8080,0xee0680,0x1408c80,0x1811100,0x1d39700,0x2141b80,0x266a180,0x2a72600,0x2f9ac00,0x33a3080,0x38cb680,0x3df3c80,0x41fc100,0x4724700,0x4b22e00,0x504b400,0x5449b00,0x5851f80,0x5af1f80,0x5ec5180,0x5ec5180,0x5ec5180,0x5ec5180,0x5ec5180,0x5ec5180,0x5ec5180,0x5ec5180,0x5ec5180,0x5ec5180,0x5ec5180,0x5ec5180,796,1604,2596,3588,4396,5388,6196,7188,7996,8988,9796,10788,11780,12588,13580,14373,15365,16158,16966,17664,18207,18207,18207,18207,21,20,0,0,0,0,0,0,0x5b0680,0x9b8b00,0xee1100,0x1409700,0x1811b80,0x1d3a180,0x2142600,0x266ac00,0x2a73080,0x2f9b680,0x33a3b00,0x38cc100,0x3df4700,0x41fcb80,0x4725180,0x4b23880,0x504be80,0x544a580,0x5852a00,0x5c12b80,0x5ec5c00,0x5ec5c00,0x5ec5c00,0x5ec5c00,0x5ec5c00,0x5ec5c00,0x5ec5c00,0x5ec5c00,0x5ec5c00,0x5ec5c00,0x5ec5c00,0x5ec5c00,797,1605,2597,3589,4397,5389,6197,7189,7997,8989,9797,10789,11781,12589,13581,14374,15366,16159,16967,17224,17224,17224,17224,17224,20,19,0,0,0,0,0,0,0x5b1100,0x9b9580,0xee1b80,0x140a180,0x1812600,0x1d3ac00,0x2143080,0x266b680,0x2a73b00,0x2f9c100,0x33a4580,0x38ccb80,0x3df5180,0x41fd600,0x4725c00,0x4b24300,0x504c900,0x544b000,0x5853480,0x5973600,0x5973600,0x5973600,0x5973600,0x5973600,0x5973600,0x5973600,0x5973600,0x5973600,0x5973600,0x5973600,0x5973600,0x5973600,798,1606,2598,3590,4398,5390,6198,7190,7998,8990,9798,10790,11782,12590,13582,14375,15367,16160,17152,17480,18208,18208,18208,18208,21,20,0,0,0,0,0,0,0x5b1b80,0x9ba000,0xee2600,0x140ac00,0x1813080,0x1d3b680,0x2143b00,0x266c100,0x2a74580,0x2f9cb80,0x33a5000,0x38cd600,0x3df5c00,0x41fe080,0x4726680,0x4b24d80,0x504d380,0x544ba80,0x5974080,0x5ac2b80,0x5ec6680,0x5ec6680,0x5ec6680,0x5ec6680,0x5ec6680,0x5ec6680,0x5ec6680,0x5ec6680,0x5ec6680,0x5ec6680,0x5ec6680,0x5ec6680,799,1607,2599,3591,4399,5391,6199,7191,7999,8991,9799,10791,11783,12591,13583,14376,15368,16161,17153,17665,18209,18209,18209,18209,21,20,0,0,0,0,0,0,0x5b2600,0x9baa80,0xee3080,0x140b680,0x1813b00,0x1d3c100,0x2144580,0x266cb80,0x2a75000,0x2f9d600,0x33a5a80,0x38ce080,0x3df6680,0x41feb00,0x4727100,0x4b25800,0x504de00,0x544c500,0x5974b00,0x5c13600,0x5ec7100,0x5ec7100,0x5ec7100,0x5ec7100,0x5ec7100,0x5ec7100,0x5ec7100,0x5ec7100,0x5ec7100,0x5ec7100,0x5ec7100,0x5ec7100,800,1792,2600,3592,4400,5392,6200,7192,8000,8992,9984,10792,11784,12592,13584,14377,15369,16162,17154,17666,18210,18210,18210,18210,21,20,0,0,0,0,0,0,0x5b3080,0xadb680,0xee3b00,0x140c100,0x1814580,0x1d3cb80,0x2145000,0x266d600,0x2a75a80,0x2f9e080,0x34c6680,0x38ceb00,0x3df7100,0x41ff580,0x4727b80,0x4b26280,0x504e880,0x544cf80,0x5975580,0x5c14080,0x5ec7b80,0x5ec7b80,0x5ec7b80,0x5ec7b80,0x5ec7b80,0x5ec7b80,0x5ec7b80,0x5ec7b80,0x5ec7b80,0x5ec7b80,0x5ec7b80,0x5ec7b80,801,1793,2601,3593,4401,5393,6201,7193,8001,8993,9985,10793,11785,12593,13585,14378,15370,16163,17155,17736,17736,17736,17736,17736,20,19,0,0,0,0,0,0,0x5b3b00,0xadc100,0xee4580,0x140cb80,0x1815000,0x1d3d600,0x2145a80,0x266e080,0x2a76500,0x2f9eb00,0x34c7100,0x38cf580,0x3df7b80,0x4200000,0x4728600,0x4b26d00,0x504f300,0x544da00,0x5976000,0x5c12100,0x5c12100,0x5c12100,0x5c12100,0x5c12100,0x5c12100,0x5c12100,0x5c12100,0x5c12100,0x5c12100,0x5c12100,0x5c12100,0x5c12100,802,1794,2602,3594,4402,5394,6202,7194,8002,8994,9986,10794,11786,12594,13586,14379,15371,16164,17156,17667,18211,18247,18247,18247,22,21,0,0,0,0,0,0,0x5b4580,0xadcb80,0xee5000,0x140d600,0x1815a80,0x1d3e080,0x2146500,0x266eb00,0x2a76f80,0x2f9f580,0x34c7b80,0x38d0000,0x3df8600,0x4200a80,0x4729080,0x4b27780,0x504fd80,0x544e480,0x5976a80,0x5c14b00,0x5ec8600,0x5ee0000,0x5ee0000,0x5ee0000,0x5ee0000,0x5ee0000,0x5ee0000,0x5ee0000,0x5ee0000,0x5ee0000,0x5ee0000,0x5ee0000,803,1795,2603,3595,4403,5395,6203,7195,8003,8995,9987,10795,11787,12595,13587,14380,15372,16165,17157,17668,18212,18212,18212,18212,21,20,0,0,0,0,0,0,0x5b5000,0xadd600,0xee5a80,0x140e080,0x1816500,0x1d3eb00,0x2146f80,0x266f580,0x2a77a00,0x2fa0000,0x34c8600,0x38d0a80,0x3df9080,0x4201500,0x4729b00,0x4b28200,0x5050800,0x544ef00,0x5977500,0x5c15580,0x5ec9080,0x5ec9080,0x5ec9080,0x5ec9080,0x5ec9080,0x5ec9080,0x5ec9080,0x5ec9080,0x5ec9080,0x5ec9080,0x5ec9080,0x5ec9080,804,1796,2604,3596,4404,5396,6204,7196,8004,8996,9988,10796,11788,12596,13588,14381,15373,16166,17158,17669,18213,18213,18213,18213,21,20,0,0,0,0,0,0,0x5b5a80,0xade080,0xee6500,0x140eb00,0x1816f80,0x1d3f580,0x2147a00,0x2670000,0x2a78480,0x2fa0a80,0x34c9080,0x38d1500,0x3df9b00,0x4201f80,0x472a580,0x4b28c80,0x5051280,0x544f980,0x5977f80,0x5c16000,0x5ec9b00,0x5ec9b00,0x5ec9b00,0x5ec9b00,0x5ec9b00,0x5ec9b00,0x5ec9b00,0x5ec9b00,0x5ec9b00,0x5ec9b00,0x5ec9b00,0x5ec9b00,805,1797,2605,3597,4405,5397,6205,7197,8005,8997,9989,10797,11789,12597,13589,14382,15374,16167,17159,17992,17992,17992,17992,17992,20,19,0,0,0,0,0,0,0x5b6500,0xadeb00,0xee6f80,0x140f580,0x1817a00,0x1d40000,0x2148480,0x2670a80,0x2a78f00,0x2fa1500,0x34c9b00,0x38d1f80,0x3dfa580,0x4202a00,0x472b000,0x4b29700,0x5051d00,0x5450400,0x5978a00,0x5d61680,0x5d61680,0x5d61680,0x5d61680,0x5d61680,0x5d61680,0x5d61680,0x5d61680,0x5d61680,0x5d61680,0x5d61680,0x5d61680,0x5d61680,806,1798,2606,3598,4406,5398,6206,7198,8006,8998,9990,10798,11790,12598,13590,14383,15375,16168,17160,18248,18214,18214,18214,18214,21,20,0,0,0,0,0,0,0x5b6f80,0xadf580,0xee7a00,0x1410000,0x1818480,0x1d40a80,0x2148f00,0x2671500,0x2a79980,0x2fa1f80,0x34ca580,0x38d2a00,0x3dfb000,0x4203480,0x472ba80,0x4b2a180,0x5052780,0x5450e80,0x5979480,0x5eb0c00,0x5eca580,0x5eca580,0x5eca580,0x5eca580,0x5eca580,0x5eca580,0x5eca580,0x5eca580,0x5eca580,0x5eca580,0x5eca580,0x5eca580,807,1799,2607,3599,4407,5399,6207,7199,8007,8999,9991,10799,11791,12599,13591,14384,15376,16169,17161,17670,18215,18215,18215,18215,21,20,0,0,0,0,0,0,0x5b7a00,0xae0000,0xee8480,0x1410a80,0x1818f00,0x1d41500,0x2149980,0x2671f80,0x2a7a400,0x2fa2a00,0x34cb000,0x38d3480,0x3dfba80,0x4203f00,0x472c500,0x4b2ac00,0x5053200,0x5451900,0x5979f00,0x5c16a80,0x5ecb000,0x5ecb000,0x5ecb000,0x5ecb000,0x5ecb000,0x5ecb000,0x5ecb000,0x5ecb000,0x5ecb000,0x5ecb000,0x5ecb000,0x5ecb000};

__global__ __launch_bounds__(256, 1) void swin_main(const uint16_t* x, const uint16_t* mask, char* ws, uint16_t* out,
                                                    const uint16_t* wqkv, const uint16_t* wo, const uint16_t* ls,
                                                    const uint16_t* w1, const uint16_t* w2, const uint16_t* rc,
                                                    const int* ridx) {
  __shared__ __attribute__((aligned(16))) char smem[LDS_BYTES];
  const int wave = __builtin_amdgcn_readfirstlane(threadIdx.x >> 6);
  const unsigned lds0 = (unsigned)(size_t)smem;
  asm volatile(
      ".p2align 5\n"
      "s_load_dwordx8 s[16:23], %0, 0\n"
      "s_load_dwordx4 s[4:7], %0, 32\n"
      "s_load_dwordx8 s[80:87], %0, 48\n"
      "s_load_dwordx2 s[90:91], %0, 80\n"
      "s_mov_b32 s24, %1\n"
      "s_mov_b32 s25, %2\n"
      "s_mov_b64 s[60:61], %3\n"
      "s_getpc_b64 s[12:13]\n"
      "L_ipf0_%=:\n"
      "v_mbcnt_lo_u32_b32 v136, -1, 0\n"
      "v_mbcnt_hi_u32_b32 v136, -1, v136\n"
      "s_lshr_b32 s28, s25, 3\n"
      "s_lshl_b32 s29, s28, 2\n"
      "s_add_u32 s29, s29, s24\n"
      "s_mul_i32 s29, s29, 14\n"
      "v_min_u32_e64 v137, 13, v136\n"
      "v_add_lshl_u32 v137, v137, s29, 7\n"
      "v_min_u32 v137, 221056, v137\n"
      "v_lshlrev_b32_e64 v138, 4, v136\n"
      "s_lshl_b32 s29, s24, 6\n"
      "s_add_u32 s54, s60, s29\n"
      "s_addc_u32 s55, s61, 0\n"
      "s_and_b32 s29, s29, 64\n"
      "v_xor_b32_e64 v139, s29, v138\n"
      "s_mul_i32 s30, s24, 9216\n"
      "v_add_u32_e64 v139, s30, v139\n"
      "s_add_u32 s2, s30, 36864\n"
      "v_xor_b32 v140, 32, v139\n"
      "s_mov_b64 s[34:35], 0xffff\n"
      "v_cndmask_b32 v141, v140, v139, s[34:35]\n"
      "s_lshl_b64 s[34:35], s[34:35], 48\n"
      "v_cndmask_b32 v142, v139, v140, s[34:35]\n"
      "s_lshl_b64 s[34:35], -1, 32\n"
      "v_cndmask_b32 v143, v140, v139, s[34:35]\n"
      "s_lshl_b32 s29, s25, 8\n"
      "s_lshl_b32 s30, s24, 5\n"
      "s_add_u32 s29, s29, s30\n"
      "s_add_u32 s34, s60, s29\n"
      "s_addc_u32 s35, s61, 0\n"
      "s_waitcnt lgkmcnt(0)\n"
      "s_load_dwordx8 s[92:99], s[34:35], 41408\n"
      "global_load_dword v31, v137, s[4:5]\n"
      "s_add_u32 m0, s2, 0\n"
      "s_nop 0\n"
      "global_load_lds_dwordx4 v139, s[4:5]\n"
      "global_load_lds_dwordx4 v139, s[4:5] offset:1024\n"
      "global_load_lds_dwordx4 v141, s[4:5] offset:2048\n"
      "global_load_lds_dwordx4 v140, s[4:5] offset:3072\n"
      "s_add_u32 s4, s4, 4096\n"
      "s_addc_u32 s5, s5, 0\n"
      "s_add_u32 m0, s2, 4096\n"
      "s_nop 0\n"
      "global_load_lds_dwordx4 v143, s[4:5]\n"
      "global_load_lds_dwordx4 v139, s[4:5] offset:1024\n"
      "global_load_lds_dwordx4 v142, s[4:5] offset:2048\n"
      "global_load_lds_dwordx4 v140, s[4:5] offset:3072\n"
      "s_add_u32 s4, s4, 4096\n"
      "s_addc_u32 s5, s5, 0\n"
      "s_add_u32 m0, s2, 8192\n"
      "s_nop 0\n"
      "global_load_lds_dwordx4 v140, s[4:5]\n"
      "s_add_u32 s4, s4, 28672\n"
      "s_addc_u32 s5, s5, 0\n"
      "s_load_dwordx16 s[36:51], s[82:83], 0\n"
      "s_load_dwordx2 s[8:9], s[80:81], 0\n"
      "s_load_dwordx16 s[64:79], s[54:55], 40960\n"
      "s_lshl_b32 s29, s24, 8\n"
      "v_lshl_add_u32 v144, v136, 2, s29\n"
      "v_min_u32 v144, 672, v144\n"
      "v_add_u32 v145, 150640, v144\n"
      "s_add_u32 m0, s2, 36864\n"
      "s_nop 0\n"
      "global_load_lds_dwordx4 v139, s[4:5]\n"
      "global_load_lds_dwordx4 v139, s[4:5] offset:1024\n"
      "global_load_lds_dwordx4 v141, s[4:5] offset:2048\n"
      "global_load_lds_dwordx4 v140, s[4:5] offset:3072\n"
      "s_add_u32 s4, s4, 4096\n"
      "s_addc_u32 s5, s5, 0\n"
      "s_add_u32 m0, s2, 40960\n"
      "s_nop 0\n"
      "global_load_lds_dwordx4 v143, s[4:5]\n"
      "global_load_lds_dwordx4 v139, s[4:5] offset:1024\n"
      "global_load_lds_dwordx4 v142, s[4:5] offset:2048\n"
      "global_load_lds_dwordx4 v140, s[4:5] offset:3072\n"
      "s_add_u32 s4, s4, 4096\n"
      "s_addc_u32 s5, s5, 0\n"
      "s_add_u32 m0, s2, 45056\n"
      "s_nop 0\n"
      "global_load_lds_dwordx4 v140, s[4:5]\n"
      "s_add_u32 s4, s4, 32768\n"
      "s_addc_u32 s5, s5, 0\n"
      "s_waitcnt lgkmcnt(0)\n"
      "s_load_dword s100, s[16:17], s92\n"
      "s_load_dword s100, s[16:17], s93\n"
      "s_load_dword s100, s[16:17], s94\n"
      "s_load_dword s100, s[16:17], s95\n"
      "s_load_dword s100, s[16:17], s96\n"
      "s_load_dword s100, s[16:17], s97\n"
      "s_load_dword s100, s[16:17], s98\n"
      "s_load_dword s100, s[16:17], s99\n"
      "s_mul_i32 s29, s25, 4868\n"
      "s_load_dword s100, s[18:19], s29\n"
      "s_load_dword s100, s[22:23], s92\n"
      "s_add_u32 s30, s92, 116654080\n"
      "s_load_dword s100, s[20:21], s30\n"
      "s_load_dword s100, s[6:7], 0\n"
      "s_load_dword s100, s[90:91], 0\n"
      "s_load_dword s100, s[86:87], 0\n"
      "global_load_dword v144, v144, s[86:87]\n"
      "v_add_u32 v146, 4096, v140\n"
      "v_add_u32 v147, s2, v138\n"
      "v_lshrrev_b32 v148, 2, v136\n"
      "global_load_dwordx4 a[72:75], v139, s[4:5] offset:-4096\n"
      "v_and_b32 v149, 3, v148\n"
      "v_lshrrev_b32 v150, 4, v136\n"
      "v_lshl_add_u32 v149, v150, 3, v149\n"
      "v_mul_u32_u24 v149, 1152, v149\n"
      "global_load_dwordx4 a[76:79], v139, s[4:5] offset:-3072\n"
      "v_and_b32_e64 v150, 1, v136\n"
      "v_lshl_add_u32 v149, v150, 3, v149\n"
      "v_add_u32 v149, 36864, v149\n"
      "global_load_dwordx4 a[80:83], v141, s[4:5] offset:-2048\n"
      "v_and_b32 v148, 6, v148\n"
      "v_bfe_u32 v150, v136, 1, 1\n"
      "s_mul_i32 s29, s24, 6\n"
      "v_add_u32_e64 v150, s29, v150\n"
      "global_load_dwordx4 a[84:87], v140, s[4:5] offset:-1024\n"
      "v_xor_b32 v151, v150, v148\n"
      "v_lshl_add_u32 v151, v151, 4, v149\n"
      "v_add_u32 v150, 2, v150\n"
      "global_load_dwordx4 a[88:91], v143, s[4:5]\n"
      "v_xor_b32 v152, v150, v148\n"
      "v_lshl_add_u32 v152, v152, 4, v149\n"
      "v_add_u32 v150, 2, v150\n"
      "global_load_dwordx4 a[92:95], v139, s[4:5] offset:1024\n"
      "v_xor_b32 v153, v150, v148\n"
      "v_lshl_add_u32 v153, v153, 4, v149\n"
      "s_movk_i32 s29, 18\n"
      "s_mov_b32 s30, 73600\n"
      "global_load_dwordx4 a[96:99], v142, s[4:5] offset:2048\n"
      "s_cmp_eq_u32 s24, 1\n"
      "s_cselect_b64 s[12:13], s[6:7], s[12:13]\n"
      "s_cmp_eq_u32 s24, 2\n"
      "global_load_dwordx4 a[100:103], v140, s[4:5] offset:3072\n"
      "s_cselect_b32 s29, 16, s29\n"
      "s_cselect_b32 s30, L_cend_%=-L_ipf0_%=-128, s30\n"
      "s_cmp_eq_u32 s24, 3\n"
      "s_cselect_b64 s[12:13], s[60:61], s[12:13]\n"
      "global_load_dwordx4 a[104:107], v146, s[4:5]\n"
      "s_cselect_b32 s29, 10, s29\n"
      "s_cselect_b32 s30, 40832, s30\n"
      "s_cmp_eq_u32 s24, 0\n"
      "s_cselect_b64 s[12:13], s[90:91], s[12:13]\n"
      "s_cselect_b32 s29, 3, s29\n"
      "s_cselect_b32 s30, 9476, s30\n"
      "s_mul_i32 s28, s28, s29\n"
      "s_sub_u32 s29, s29, 1\n"
      "v_add_u32 v139, 36864, v139\n"
      "v_add_u32 v140, 36864, v140\n"
      "v_add_u32 v141, 36864, v141\n"
      "v_add_u32 v143, 36864, v143\n"
      "v_add_u32 v142, 36864, v142\n"
      "v_add_u32 v146, 36864, v146\n"
      "global_load_dwordx4 a[108:111], v139, s[4:5] offset:-4096\n"
      "v_min_u32 v137, s29, v136\n"
      "v_add_lshl_u32 v137, v137, s28, 7\n"
      "v_min_u32 v137, s30, v137\n"
      "global_load_dword v30, v137, s[12:13]\n"
      "s_mul_i32 s28, s24, 10240\n"
      "s_add_u32 s28, s28, 4096\n"
      "v_add_u32 v137, s28, v138\n"
      "v_add_u32 v154, 8192, v137\n"
      "s_lshl_b32 s28, s25, 8\n"
      "global_load_dwordx4 a[112:115], v139, s[4:5] offset:-3072\n"
      "s_add_u32 s52, s60, s28\n"
      "s_addc_u32 s53, s61, 0\n"
      "s_lshl_b32 s28, s24, 5\n"
      "global_load_dwordx4 a[116:119], v141, s[4:5] offset:-2048\n"
      "v_lshrrev_b32 v155, 3, v136\n"
      "v_lshl_add_u32 v149, v155, 2, s28\n"
      "v_add_u32 v149, 41408, v149\n"
      "v_xor_b32_e64 v155, v155, v136\n"
      "global_load_dwordx4 a[120:123], v140, s[4:5] offset:-1024\n"
      "v_and_b32 v155, 7, v155\n"
      "s_waitcnt vmcnt(15)\n"
      "s_waitcnt lgkmcnt(0)\n"
      "s_barrier\n"
      "ds_read_b64_tr_b16 a[0:1], v151\n"
      "ds_read_b64_tr_b16 a[2:3], v151 offset:4608\n"
      "ds_read_b64_tr_b16 a[4:5], v152\n"
      "global_load_dwordx4 a[124:127], v143, s[4:5]\n"
      "ds_read_b64_tr_b16 a[6:7], v152 offset:4608\n"
      "ds_read_b64_tr_b16 a[8:9], v153\n"
      "ds_read_b64_tr_b16 a[10:11], v153 offset:4608\n"
      "ds_read_b64_tr_b16 a[12:13], v151 offset:384\n"
      "ds_read_b64_tr_b16 a[14:15], v151 offset:4992\n"
      "global_load_dwordx4 a[128:131], v139, s[4:5] offset:1024\n"
      "ds_read_b64_tr_b16 a[16:17], v152 offset:384\n"
      "ds_read_b64_tr_b16 a[18:19], v152 offset:4992\n"
      "ds_read_b64_tr_b16 a[20:21], v153 offset:384\n"
      "ds_read_b64_tr_b16 a[22:23], v153 offset:4992\n"
      "ds_read_b64_tr_b16 a[24:25], v151 offset:768\n"
      "global_load_dwordx4 a[132:135], v142, s[4:5] offset:2048\n"
      "ds_read_b64_tr_b16 a[26:27], v151 offset:5376\n"
      "ds_read_b64_tr_b16 a[28:29], v152 offset:768\n"
      "ds_read_b64_tr_b16 a[30:31], v152 offset:5376\n"
      "ds_read_b64_tr_b16 a[32:33], v153 offset:768\n"
      "ds_read_b64_tr_b16 a[34:35], v153 offset:5376\n"
      "ds_read_b64_tr_b16 a[36:37], v151 offset:36864\n"
      "global_load_dwordx4 a[136:139], v140, s[4:5] offset:3072\n"
      "ds_read_b64_tr_b16 a[38:39], v151 offset:41472\n"
      "ds_read_b64_tr_b16 a[40:41], v152 offset:36864\n"
      "ds_read_b64_tr_b16 a[42:43], v152 offset:41472\n"
      "ds_read_b64_tr_b16 a[44:45], v153 offset:36864\n"
      "ds_read_b64_tr_b16 a[46:47], v153 offset:41472\n"
      "global_load_dwordx4 a[140:143], v146, s[4:5]\n"
      "ds_read_b64_tr_b16 a[48:49], v151 offset:37248\n"
      "ds_read_b64_tr_b16 a[50:51], v151 offset:41856\n"
      "ds_read_b64_tr_b16 a[52:53], v152 offset:37248\n"
      "ds_read_b64_tr_b16 a[54:55], v152 offset:41856\n"
      "ds_read_b64_tr_b16 a[56:57], v153 offset:37248\n"
      "global_load_dword v149, v149, s[52:53]\n"
      "ds_read_b64_tr_b16 a[58:59], v153 offset:41856\n"
      "ds_read_b64_tr_b16 a[60:61], v151 offset:37632\n"
      "ds_read_b64_tr_b16 a[62:63], v151 offset:42240\n"
      "ds_read_b64_tr_b16 a[64:65], v152 offset:37632\n"
      "ds_read_b64_tr_b16 a[66:67], v152 offset:42240\n"
      "ds_read_b64_tr_b16 a[68:69], v153 offset:37632\n"
      "ds_read_b64_tr_b16 a[70:71], v153 offset:42240\n"
      "s_pack_ll_b32_b16 s28, s36, s44\n"
      "s_waitcnt vmcnt(20)\n"
      "v_dot2_f32_bf16 v136, s28, v144, 0\n"
      "s_pack_hh_b32_b16 s29, s36, s44\n"
      "v_dot2_f32_bf16 v148, s29, v144, 0\n"
      "s_pack_ll_b32_b16 s30, s37, s45\n"
      "v_dot2_f32_bf16 v150, s30, v144, 0\n"
      "v_add_u32 v139, 36864, v139\n"
      "v_add_u32 v140, 36864, v140\n"
      "v_add_u32 v141, 36864, v141\n"
      "v_add_u32 v143, 36864, v143\n"
      "v_add_u32 v142, 36864, v142\n"
      "v_add_u32 v146, 36864, v146\n"
      "global_load_dwordx4 a[144:147], v139, s[4:5] offset:-4096\n"
      "s_pack_hh_b32_b16 s31, s37, s45\n"
      "v_dot2_f32_bf16 v156, s31, v144, 0\n"
      "s_pack_ll_b32_b16 s28, s38, s46\n"
      "v_dot2_f32_bf16 v157, s28, v144, 0\n"
      "s_pack_hh_b32_b16 s29, s38, s46\n"
      "v_dot2_f32_bf16 v158, s29, v144, 0\n"
      "s_pack_ll_b32_b16 s30, s39, s47\n"
      "v_dot2_f32_bf16 v159, s30, v144, 0\n"
      "s_pack_hh_b32_b16 s31, s39, s47\n"
      "v_dot2_f32_bf16 v160, s31, v144, 0\n"
      "s_pack_ll_b32_b16 s28, s40, s48\n"
      "v_dot2_f32_bf16 v161, s28, v144, 0\n"
      "s_pack_hh_b32_b16 s29, s40, s48\n"
      "global_load_dwordx4 a[148:151], v139, s[4:5] offset:-3072\n"
      "v_dot2_f32_bf16 v162, s29, v144, 0\n"
      "s_pack_ll_b32_b16 s30, s41, s49\n"
      "v_dot2_f32_bf16 v163, s30, v144, 0\n"
      "s_pack_hh_b32_b16 s31, s41, s49\n"
      "v_dot2_f32_bf16 v164, s31, v144, 0\n"
      "global_load_dwordx4 a[152:155], v141, s[4:5] offset:-2048\n"
      "s_pack_ll_b32_b16 s28, s42, s50\n"
      "v_dot2_f32_bf16 v165, s28, v144, 0\n"
      "s_pack_hh_b32_b16 s29, s42, s50\n"
      "v_dot2_f32_bf16 v168, s29, v144, 0\n"
      "s_pack_ll_b32_b16 s30, s43, s51\n"
      "global_load_dwordx4 a[156:159], v140, s[4:5] offset:-1024\n"
      "v_dot2_f32_bf16 v166, s30, v144, 0\n"
      "s_pack_hh_b32_b16 s31, s43, s51\n"
      "v_dot2_f32_bf16 v169, s31, v144, 0\n"
      "s_load_dwordx16 s[36:51], s[84:85], 0\n"
      "v_max_f32 v136, 0, v136\n"
      "global_load_dwordx4 a[160:163], v143, s[4:5]\n"
      "v_max_f32 v148, 0, v148\n"
      "v_cvt_pk_bf16_f32 v136, v136, v148\n"
      "v_max_f32 v150, 0, v150\n"
      "v_max_f32_e64 v156, 0, v156\n"
      "v_cvt_pk_bf16_f32 v150, v150, v156\n"
      "global_load_dwordx4 a[164:167], v139, s[4:5] offset:1024\n"
      "v_max_f32 v157, 0, v157\n"
      "v_max_f32 v158, 0, v158\n"
      "v_cvt_pk_bf16_f32 v157, v157, v158\n"
      "v_max_f32 v159, 0, v159\n"
      "v_max_f32 v160, 0, v160\n"
      "v_cvt_pk_bf16_f32 v159, v159, v160\n"
      "global_load_dwordx4 a[168:171], v142, s[4:5] offset:2048\n"
      "v_max_f32 v161, 0, v161\n"
      "v_max_f32 v162, 0, v162\n"
      "v_cvt_pk_bf16_f32 v161, v161, v162\n"
      "v_max_f32 v163, 0, v163\n"
      "v_max_f32 v164, 0, v164\n"
      "global_load_dwordx4 a[172:175], v140, s[4:5] offset:3072\n"
      "v_cvt_pk_bf16_f32 v163, v163, v164\n"
      "v_max_f32 v165, 0, v165\n"
      "v_max_f32 v168, 0, v168\n"
      "v_cvt_pk_bf16_f32 v165, v165, v168\n"
      "v_max_f32_e64 v166, 0, v166\n"
      "global_load_dwordx4 a[176:179], v146, s[4:5]\n"
      "v_max_f32 v169, 0, v169\n"
      "v_cvt_pk_bf16_f32 v166, v166, v169\n"
      "s_waitcnt lgkmcnt(0)\n"
      "s_barrier\n"
      "s_pack_ll_b32_b16 s28, s36, s38\n"
      "v_dot2_f32_bf16 v168, s28, v136, 0\n"
      "s_pack_hh_b32_b16 s29, s36, s38\n"
      "v_dot2_f32_bf16 v169, s29, v136, 0\n"
      "s_pack_ll_b32_b16 s30, s37, s39\n"
      "v_dot2_f32_bf16 v170, s30, v136, 0\n"
      "v_add_u32 v139, 36864, v139\n"
      "v_add_u32 v140, 36864, v140\n"
      "v_add_u32 v141, 36864, v141\n"
      "v_add_u32 v143, 36864, v143\n"
      "v_add_u32 v142, 36864, v142\n"
      "v_add_u32 v146, 36864, v146\n"
      "global_load_dwordx4 a[180:183], v139, s[4:5] offset:-4096\n"
      "s_pack_hh_b32_b16 s31, s37, s39\n"
      "v_dot2_f32_bf16 v171, s31, v136, 0\n"
      "s_pack_ll_b32_b16 s28, s40, s42\n"
      "v_dot2_f32_bf16 v168, s28, v150, v168\n"
      "s_pack_hh_b32_b16 s29, s40, s42\n"
      "v_dot2_f32_bf16 v169, s29, v150, v169\n"
      "global_load_dwordx4 a[184:187], v139, s[4:5] offset:-3072\n"
      "s_pack_ll_b32_b16 s30, s41, s43\n"
      "v_dot2_f32_bf16 v170, s30, v150, v170\n"
      "s_pack_hh_b32_b16 s31, s41, s43\n"
      "global_load_dwordx4 a[188:191], v141, s[4:5] offset:-2048\n"
      "v_dot2_f32_bf16 v171, s31, v150, v171\n"
      "s_pack_ll_b32_b16 s28, s44, s46\n"
      "global_load_dwordx4 a[192:195], v140, s[4:5] offset:-1024\n"
      "v_dot2_f32_bf16 v168, s28, v157, v168\n"
      "s_pack_hh_b32_b16 s29, s44, s46\n"
      "v_dot2_f32_bf16 v169, s29, v157, v169\n"
      "global_load_dwordx4 a[196:199], v143, s[4:5]\n"
      "s_pack_ll_b32_b16 s30, s45, s47\n"
      "v_dot2_f32_bf16 v170, s30, v157, v170\n"
      "s_pack_hh_b32_b16 s31, s45, s47\n"
      "global_load_dwordx4 a[200:203], v139, s[4:5] offset:1024\n"
      "v_dot2_f32_bf16 v171, s31, v157, v171\n"
      "s_pack_ll_b32_b16 s28, s48, s50\n"
      "global_load_dwordx4 a[204:207], v142, s[4:5] offset:2048\n"
      "v_dot2_f32_bf16 v168, s28, v159, v168\n"
      "s_pack_hh_b32_b16 s29, s48, s50\n"
      "v_dot2_f32_bf16 v169, s29, v159, v169\n"
      "global_load_dwordx4 a[208:211], v140, s[4:5] offset:3072\n"
      "s_pack_ll_b32_b16 s30, s49, s51\n"
      "v_dot2_f32_bf16 v170, s30, v159, v170\n"
      "global_load_dwordx4 a[212:215], v146, s[4:5]\n"
      "s_pack_hh_b32_b16 s31, s49, s51\n"
      "v_dot2_f32_bf16 v171, s31, v159, v171\n"
      "s_load_dwordx16 s[36:51], s[84:85], 64\n"
      "s_lshl_b32 s28, s24, 4\n"
      "s_lshr_b64 s[8:9], s[8:9], s28\n"
      "s_lshl_b32 s8, s8, 16\n"
      "v_mov_b32 v139, s8\n"
      "v_max_f32 v139, 0xc2c80000, v139\n"
      "v_min_f32 v139, 0x40930000, v139\n"
      "v_mul_f32 v139, 0x3fb8aa3b, v139\n"
      "v_exp_f32 v139, v139\n"
      "s_nop 0\n"
      "v_cvt_pk_bf16_f32 v139, v139, v139\n"
      "v_lshlrev_b32_e64 v139, 16, v139\n"
      "s_waitcnt vmcnt(37)\n"
      "ds_write_b128 v147, a[72:75]\n"
      "s_waitcnt vmcnt(18)\n"
      "v_lshl_add_u32 v149, v155, 4, v149\n"
      "s_add_u32 m0, s76, 18432\n"
      "s_nop 0\n"
      "global_load_lds_dwordx4 v149, s[16:17]\n"
      "s_add_u32 m0, s76, 24448\n"
      "s_nop 0\n"
      "global_load_lds_dwordx4 v149, s[16:17] offset:128\n"
      "s_add_u32 m0, s76, 30464\n"
      "s_nop 0\n"
      "global_load_lds_dwordx4 v149, s[16:17] offset:256\n"
      "v_add_u32 v140, s76, v138\n"
      "s_add_u32 m0, s76, 124928\n"
      "s_nop 0\n"
      "global_load_lds_dwordx4 v140, s[90:91]\n"
      "v_add_u32 v140, 4096, v140\n"
      "s_add_u32 m0, s76, 129024\n"
      "s_nop 0\n"
      "global_load_lds_dwordx4 v140, s[90:91]\n"
      "v_add_u32 v140, 4096, v140\n"
      "v_min_u32 v140, 9600, v140\n"
      "s_add_u32 m0, s76, 133120\n"
      "s_nop 0\n"
      "global_load_lds_dwordx4 v140, s[90:91]\n"
      "ds_write_b128 v147, a[76:79] offset:1024\n"
      "ds_write_b128 v147, a[80:83] offset:2048\n"
      "ds_write_b128 v147, a[84:87] offset:3072\n"
      "ds_write_b128 v147, a[88:91] offset:4096\n"
      "ds_write_b128 v147, a[92:95] offset:5120\n"
      "ds_write_b128 v147, a[96:99] offset:6144\n"
      "ds_write_b128 v147, a[100:103] offset:7168\n"
      "ds_write_b128 v147, a[104:107] offset:8192\n"
      "ds_write_b128 v147, a[108:111] offset:36864\n"
      "ds_write_b128 v147, a[112:115] offset:37888\n"
      "ds_write_b128 v147, a[116:119] offset:38912\n"
      "ds_write_b128 v147, a[120:123] offset:39936\n"
      "ds_write_b128 v147, a[124:127] offset:40960\n"
      "ds_write_b128 v147, a[128:131] offset:41984\n"
      "global_load_dwordx4 v[140:143], v137, s[60:61] offset:3072\n"
      "ds_write_b128 v147, a[132:135] offset:43008\n"
      "ds_write_b128 v147, a[136:139] offset:44032\n"
      "global_load_dwordx4 v[114:117], v137, s[60:61] offset:-4096\n"
      "ds_write_b128 v147, a[140:143] offset:45056\n"
      "s_waitcnt lgkmcnt(0)\n"
      "s_barrier\n"
      "ds_read_b64_tr_b16 a[72:73], v151\n"
      "ds_read_b64_tr_b16 a[74:75], v151 offset:4608\n"
      "ds_read_b64_tr_b16 a[76:77], v152\n"
      "ds_read_b64_tr_b16 a[78:79], v152 offset:4608\n"
      "ds_read_b64_tr_b16 a[80:81], v153\n"
      "ds_read_b64_tr_b16 a[82:83], v153 offset:4608\n"
      "ds_read_b64_tr_b16 a[84:85], v151 offset:384\n"
      "ds_read_b64_tr_b16 a[86:87], v151 offset:4992\n"
      "ds_read_b64_tr_b16 a[88:89], v152 offset:384\n"
      "s_waitcnt vmcnt(1)\n"
      "v_add_u32 v155, 3072, v143\n"
      "global_load_dwordx4 a[216:219], v155, s[6:7] offset:-3072\n"
      "ds_read_b64_tr_b16 a[90:91], v152 offset:4992\n"
      "ds_read_b64_tr_b16 a[92:93], v153 offset:384\n"
      "ds_read_b64_tr_b16 a[94:95], v153 offset:4992\n"
      "ds_read_b64_tr_b16 a[96:97], v151 offset:768\n"
      "ds_read_b64_tr_b16 a[98:99], v151 offset:5376\n"
      "ds_read_b64_tr_b16 a[100:101], v152 offset:768\n"
      "ds_read_b64_tr_b16 a[102:103], v152 offset:5376\n"
      "ds_read_b64_tr_b16 a[104:105], v153 offset:768\n"
      "ds_read_b64_tr_b16 a[106:107], v153 offset:5376\n"
      "ds_read_b64_tr_b16 a[108:109], v151 offset:36864\n"
      "ds_read_b64_tr_b16 a[110:111], v151 offset:41472\n"
      "ds_read_b64_tr_b16 a[112:113], v152 offset:36864\n"
      "ds_read_b64_tr_b16 a[114:115], v152 offset:41472\n"
      "ds_read_b64_tr_b16 a[116:117], v153 offset:36864\n"
      "ds_read_b64_tr_b16 a[118:119], v153 offset:41472\n"
      "global_load_dwordx4 a[220:223], v155, s[6:7] offset:-2944\n"
      "ds_read_b64_tr_b16 a[120:121], v151 offset:37248\n"
      "ds_read_b64_tr_b16 a[122:123], v151 offset:41856\n"
      "ds_read_b64_tr_b16 a[124:125], v152 offset:37248\n"
      "ds_read_b64_tr_b16 a[126:127], v152 offset:41856\n"
      "ds_read_b64_tr_b16 a[128:129], v153 offset:37248\n"
      "ds_read_b64_tr_b16 a[130:131], v153 offset:41856\n"
      "ds_read_b64_tr_b16 a[132:133], v151 offset:37632\n"
      "ds_read_b64_tr_b16 a[134:135], v151 offset:42240\n"
      "ds_read_b64_tr_b16 a[136:137], v152 offset:37632\n"
      "ds_read_b64_tr_b16 a[138:139], v152 offset:42240\n"
      "ds_read_b64_tr_b16 a[140:141], v153 offset:37632\n"
      "ds_read_b64_tr_b16 a[142:143], v153 offset:42240\n"
      "global_load_dwordx4 a[224:227], v155, s[6:7] offset:-2816\n"
      "s_pack_ll_b32_b16 s28, s36, s38\n"
      "v_dot2_f32_bf16 v168, s28, v161, v168\n"
      "s_pack_hh_b32_b16 s29, s36, s38\n"
      "v_dot2_f32_bf16 v169, s29, v161, v169\n"
      "s_pack_ll_b32_b16 s30, s37, s39\n"
      "v_dot2_f32_bf16 v170, s30, v161, v170\n"
      "s_pack_hh_b32_b16 s31, s37, s39\n"
      "v_dot2_f32_bf16 v171, s31, v161, v171\n"
      "s_pack_ll_b32_b16 s28, s40, s42\n"
      "v_dot2_f32_bf16 v168, s28, v163, v168\n"
      "s_pack_hh_b32_b16 s29, s40, s42\n"
      "v_dot2_f32_bf16 v169, s29, v163, v169\n"
      "global_load_dwordx4 a[228:231], v155, s[6:7]\n"
      "s_pack_ll_b32_b16 s30, s41, s43\n"
      "v_dot2_f32_bf16 v170, s30, v163, v170\n"
      "s_pack_hh_b32_b16 s31, s41, s43\n"
      "v_dot2_f32_bf16 v171, s31, v163, v171\n"
      "s_pack_ll_b32_b16 s28, s44, s46\n"
      "v_dot2_f32_bf16 v168, s28, v165, v168\n"
      "s_pack_hh_b32_b16 s29, s44, s46\n"
      "v_dot2_f32_bf16 v169, s29, v165, v169\n"
      "s_pack_ll_b32_b16 s30, s45, s47\n"
      "v_dot2_f32_bf16 v170, s30, v165, v170\n"
      "s_pack_hh_b32_b16 s31, s45, s47\n"
      "v_dot2_f32_bf16 v171, s31, v165, v171\n"
      "global_load_dwordx4 a[232:235], v155, s[6:7] offset:128\n"
      "s_pack_ll_b32_b16 s28, s48, s50\n"
      "v_dot2_f32_bf16 v168, s28, v166, v168\n"
      "s_pack_hh_b32_b16 s29, s48, s50\n"
      "v_dot2_f32_bf16 v169, s29, v166, v169\n"
      "s_pack_ll_b32_b16 s30, s49, s51\n"
      "v_dot2_f32_bf16 v170, s30, v166, v170\n"
      "s_pack_hh_b32_b16 s31, s49, s51\n"
      "v_dot2_f32_bf16 v171, s31, v166, v171\n"
      "s_mov_b64 s[48:49], s[60:61]\n"
      "s_load_dwordx8 s[36:43], s[60:61], 41216\n"
      "s_load_dwordx4 s[44:47], s[60:61], 41248\n"
      "s_load_dwordx16 s[80:95], s[52:53], 41280\n"
      "global_load_dwordx4 a[236:239], v155, s[6:7] offset:256\n"
      "s_load_dwordx4 s[96:99], s[52:53], 41344\n"
      "s_load_dwordx2 s[100:101], s[52:53], 41360\n"
      "s_load_dword s26, s[52:53], 41376\n"
      "s_load_dword s3, s[52:53], 41380\n"
      "v_cmp_nge_f32 vcc, 0x42480000, v139\n"
      "v_mul_f32 v139, 0x3fb8aa3b, v139\n"
      "s_mov_b64 s[50:51], vcc\n"
      "v_cvt_pk_bf16_f32 v169, v168, v169\n"
      "v_lshlrev_b32 v168, 16, v169\n"
      "v_and_b32 v169, 0xffff0000, v169\n"
      "v_cvt_pk_bf16_f32 v171, v170, v171\n"
      "v_lshlrev_b32_e64 v170, 16, v171\n"
      "v_and_b32 v171, 0xffff0000, v171\n"
      "v_mul_f32 v168, 0xbfb8aa3b, v168\n"
      "v_mul_f32 v169, 0xbfb8aa3b, v169\n"
      "v_add_u32 v155, 9216, v143\n"
      "global_load_dwordx4 a[240:243], v155, s[6:7] offset:-3072\n"
      "v_mul_f32 v170, 0xbfb8aa3b, v170\n"
      "v_mul_f32 v171, 0xbfb8aa3b, v171\n"
      "v_exp_f32 v168, v168\n"
      "v_exp_f32 v169, v169\n"
      "v_exp_f32 v170, v170\n"
      "v_exp_f32 v171, v171\n"
      "v_add_f32 v168, 1.0, v168\n"
      "v_add_f32 v169, 1.0, v169\n"
      "v_add_f32 v170, 1.0, v170\n"
      "v_add_f32 v171, 1.0, v171\n"
      "v_rcp_f32 v168, v168\n"
      "v_rcp_f32 v169, v169\n"
      "v_rcp_f32 v170, v170\n"
      "v_rcp_f32 v171, v171\n"
      "v_cvt_pk_bf16_f32 v169, v168, v169\n"
      "global_load_dwordx4 a[244:247], v155, s[6:7] offset:-2944\n"
      "v_lshlrev_b32 v168, 16, v169\n"
      "v_and_b32 v169, 0xffff0000, v169\n"
      "v_cvt_pk_bf16_f32 v171, v170, v171\n"
      "v_lshlrev_b32 v170, 16, v171\n"
      "v_and_b32 v171, 0xffff0000, v171\n"
      "v_add_f32 v168, -0.5, v168\n"
      "v_mul_f32 v168, 0x41b8aa3b, v168\n"
      "ds_write_b32 v145, v168\n"
      "v_add_f32 v169, -0.5, v169\n"
      "v_mul_f32 v169, 0x41b8aa3b, v169\n"
      "ds_write_b32 v145, v169 offset:768\n"
      "v_add_f32_e64 v170, -0.5, v170\n"
      "global_load_dwordx4 a[248:251], v155, s[6:7] offset:-2816\n"
      "v_mul_f32 v170, 0x41b8aa3b, v170\n"
      "ds_write_b32 v145, v170 offset:1536\n"
      "v_add_f32_e64 v171, -0.5, v171\n"
      "v_mul_f32 v171, 0x41b8aa3b, v171\n"
      "ds_write_b32 v145, v171 offset:2304\n"
      "s_waitcnt lgkmcnt(0)\n"
      "s_barrier\n"
      "v_readfirstlane_b32 s36, v139\n"
      "L_sched_%=:\n"
      "v_mov_b32 v36, 0\n"
      "s_mov_b32 s11, 100466688\n"
      "global_load_dwordx4 v[118:121], v137, s[48:49] offset:-3072\n"
      "ds_write_b128 v147, a[144:147]\n"
      "ds_write_b128 v147, a[212:215] offset:45056\n"
      "ds_write_b128 v147, a[160:163] offset:4096\n"
      "v_mov_b32 v37, 0\n"
      "v_mov_b32 v57, 0\n"
      "v_mov_b32 v53, 0\n"
      "v_mov_b32 v48, 0\n"
      "v_mov_b32_e64 v65, 0\n"
      "v_mov_b32_e64 v42, 0\n"
      "v_mov_b32 v43, 0\n"
      "s_sub_u32 s16, s16, 99876864\n"
      "ds_write_b128 v147, a[164:167] offset:5120\n"
      "global_load_dwordx4 v[122:125], v137, s[48:49] offset:-2048\n"
      "ds_write_b128 v147, a[200:203] offset:41984\n"
      "ds_write_b128 v147, a[168:171] offset:6144\n"
      "ds_write_b128 v147, a[188:191] offset:38912\n"
      "ds_write_b128 v147, a[184:187] offset:37888\n"
      "s_subb_u32 s17, s17, 0\n"
      "ds_write_b128 v147, a[148:151] offset:1024\n"
      "ds_write_b128 v147, a[172:175] offset:7168\n"
      "ds_write_b128 v147, a[152:155] offset:2048\n"
      "s_sub_u32 s22, s22, 99876864\n"
      "ds_write_b128 v147, a[208:211] offset:44032\n"
      "global_load_dwordx4 v[160:163], v154, s[48:49] offset:-3072\n"
      "v_mov_b32 v61, 0\n"
      "s_subb_u32 s23, s23, 0\n"
      "ds_write_b128 v147, a[192:195] offset:39936\n"
      "ds_write_b128 v147, a[156:159] offset:3072\n"
      "ds_write_b128 v147, a[176:179] offset:8192\n"
      "ds_write_b128 v147, a[180:183] offset:36864\n"
      "s_add_u32 s12, s20, 16777216\n"
      "global_load_dwordx4 v[168:171], v154, s[48:49] offset:-4096\n"
      "ds_write_b128 v147, a[196:199] offset:40960\n"
      "ds_write_b128 v147, a[204:207] offset:43008\n"
      "v_mov_b32_e64 v49, 0\n"
      "s_addc_u32 s13, s21, 0\n"
      "s_waitcnt lgkmcnt(0)\n"
      "s_barrier\n"
      "s_waitcnt vmcnt(1)\n"
      "v_mov_b32_e64 v146, v161\n"
      "v_mov_b32_e64 v164, 0\n"
      "s_mov_b32 s60, 100270464\n"
      "ds_read_b64_tr_b16 a[144:145], v151\n"
      "ds_read_b64_tr_b16 a[196:197], v152 offset:37248\n"
      "ds_read_b64_tr_b16 a[158:159], v151 offset:4992\n"
      "v_mov_b32 v145, v161\n"
      "s_mov_b32 s61, 196224\n"
      "v_mov_b32 v154, 134528\n"
      "s_lshr_b32 s8, s80, 8\n"
      "v_mov_b32_e64 v147, v161\n"
      "s_and_b32 s9, s80, 0xff\n"
      "v_mov_b32_e64 v165, 0\n"
      "v_mov_b32_e64 v67, v160\n"
      "s_mul_i32 s10, s8, 73\n"
      "v_add_u32 v150, 3136, v140\n"
      "v_add_u32 v148, 6272, v140\n"
      "v_mov_b32 v144, v161\n"
      "v_add_u32 v149, 99876864, v149\n"
      "s_add_u32 s10, s10, s9\n"
      "ds_read_b64_tr_b16 a[208:209], v152 offset:37632\n"
      "v_mov_b32_e64 v69, v160\n"
      "s_mul_i32 s28, s10, 4802\n"
      "ds_read_b64_tr_b16 a[178:179], v153 offset:5376\n"
      "ds_read_b64_tr_b16 a[156:157], v151 offset:384\n"
      "ds_read_b64_tr_b16 a[168:169], v151 offset:768\n"
      "v_mov_b32 v68, v160\n"
      "s_add_u32 s56, s18, s28\n"
      "ds_read_b64_tr_b16 a[172:173], v152 offset:768\n"
      "s_addc_u32 s57, s19, 0\n"
      "ds_read2_b32 v[100:101], v150 offset0:34 offset1:35\n"
      "s_and_b32 s56, s56, -16\n"
      "ds_read_b32 v71, v142\n"
      "v_mov_b32_e64 v66, v160\n"
      "s_sub_u32 s58, s56, 110592\n"
      "ds_read2_b32 v[88:89], v140 offset0:34 offset1:35\n"
      "s_subb_u32 s59, s57, 0\n"
      "s_sub_u32 s56, s56, 73728\n"
      "v_mov_b32 v160, 0\n"
      "s_subb_u32 s57, s57, 0\n"
      "ds_read_b64_tr_b16 a[194:195], v151 offset:41856\n"
      "ds_read_b64_tr_b16 a[198:199], v152 offset:41856\n"
      "ds_read_b64_tr_b16 a[148:149], v152\n"
      "ds_read_b64_tr_b16 a[152:153], v153\n"
      "ds_read_b128 v[156:159], v114 offset:18432\n"
      "s_mul_i32 s28, s8, 1373568\n"
      "ds_read_b64_tr_b16 a[176:177], v153 offset:768\n"
      "s_mul_i32 s29, s9, 2688\n"
      "ds_read_b64_tr_b16 a[180:181], v151 offset:36864\n"
      "ds_read_b64_tr_b16 a[182:183], v151 offset:41472\n"
      "ds_read_b64_tr_b16 a[164:165], v153 offset:384\n"
      "s_add_u32 s28, s28, s29\n"
      "ds_read_b128 v[172:175], v115 offset:18432\n"
      "ds_read_b64_tr_b16 a[206:207], v151 offset:42240\n"
      "ds_read_b64_tr_b16 a[162:163], v152 offset:4992\n"
      "ds_read2_b32 v[94:95], v150 offset0:16 offset1:17\n"
      "s_cmp_eq_u32 s8, 72\n"
      "s_cselect_b32 s8, 0, 100270464\n"
      "ds_read_b64_tr_b16 a[150:151], v152 offset:4608\n"
      "ds_read_b64_tr_b16 a[154:155], v153 offset:4608\n"
      "ds_read2_b32 v[106:107], v148 offset0:16 offset1:17\n"
      "s_cmp_eq_u32 s9, 72\n"
      "ds_read_b64_tr_b16 a[192:193], v151 offset:37248\n"
      "s_cselect_b32 s9, 0, 196224\n"
      "ds_read2_b32 v[80:81], v140 offset0:2 offset1:3\n"
      "ds_read2_b32 v[96:97], v150 offset0:18 offset1:19\n"
      "s_add_u32 s10, s8, s9\n"
      "v_mov_b32_e64 v161, 0\n"
      "s_add_u32 s28, s28, s10\n"
      "s_add_u32 s52, s16, s28\n"
      "ds_read_b64_tr_b16 a[146:147], v151 offset:4608\n"
      "s_addc_u32 s53, s17, 0\n"
      "ds_read_b64_tr_b16 a[214:215], v153 offset:42240\n"
      "ds_read_b64_tr_b16 a[166:167], v153 offset:4992\n"
      "s_waitcnt lgkmcnt(12)\n"
      "ds_read2_b32 v[82:83], v140 offset0:16 offset1:17\n"
      "ds_read2_b32 v[92:93], v150 offset0:2 offset1:3\n"
      "ds_read2_b32 v[110:111], v148 offset0:32 offset1:33\n"
      "ds_read2_b32 v[84:85], v140 offset0:18 offset1:19\n"
      "ds_read2_b32 v[98:99], v150 offset0:32 offset1:33\n"
      "v_mfma_f32_16x16x32_bf16 v[180:183], a[4:7], v[156:159], 0\n"
      "ds_read_b64_tr_b16 a[170:171], v151 offset:5376\n"
      "ds_read_b32 v73, v142 offset:64\n"
      "ds_read_b64_tr_b16 a[184:185], v152 offset:36864\n"
      "s_cmp_lg_u32 s10, s11\n"
      "s_cbranch_scc1 L_edge_1_%=\n"
      "L_eb_2_%=:\n"
      "s_add_u32 m0, s76, 0\n"
      "v_mfma_f32_16x16x32_bf16 v[176:179], a[0:3], v[156:159], 0\n"
      "ds_read_b64_tr_b16 a[204:205], v151 offset:37632\n"
      "ds_read2_b32 v[90:91], v150 offset0:0 offset1:1\n"
      "ds_read2_b32 v[86:87], v140 offset0:32 offset1:33\n"
      "ds_read2_b32 v[78:79], v140 offset0:0 offset1:1\n"
      "global_load_lds_dwordx4 v116, s[52:53]\n"
      "ds_read2_b32 v[104:105], v148 offset0:2 offset1:3\n"
      "ds_read_b64_tr_b16 a[212:213], v153 offset:37632\n"
      "ds_read_b64_tr_b16 a[200:201], v153 offset:37248\n"
      "ds_read2_b32 v[108:109], v148 offset0:18 offset1:19\n"
      "ds_read_b32 v70, v141 offset:192\n"
      "ds_read_b64_tr_b16 a[174:175], v152 offset:5376\n"
      "ds_read2_b32 v[102:103], v148 offset0:0 offset1:1\n"
      "v_mfma_f32_16x16x32_bf16 v[176:179], a[36:39], v[172:175], v[176:179]\n"
      "ds_read_b64_tr_b16 a[186:187], v152 offset:41472\n"
      "s_waitcnt vmcnt(1)\n"
      "ds_write_b32 v169, v149\n"
      "v_mfma_f32_16x16x32_bf16 v[180:183], a[40:43], v[172:175], v[180:183]\n"
      "ds_read_b128 v[204:207], v115 offset:24576\n"
      "ds_read_b64_tr_b16 a[202:203], v153 offset:41856\n"
      "v_mfma_f32_16x16x32_bf16 v[184:187], a[8:11], v[156:159], 0\n"
      "ds_read_b32 v74, v141 offset:6464\n"
      "s_waitcnt lgkmcnt(12)\n"
      "v_mfma_f32_16x16x32_bf16 v[184:187], a[44:47], v[172:175], v[184:187]\n"
      "ds_read_b32 v75, v142 offset:128\n"
      "ds_read_b32 v77, v154\n"
      "ds_read_b64_tr_b16 a[160:161], v152 offset:384\n"
      "ds_read2_b32 v[112:113], v148 offset0:34 offset1:35\n"
      "v_mfma_f32_16x16x32_bf16 v[148:151], a[12:15], v[156:159], 0\n"
      "ds_read_b64_tr_b16 a[210:211], v152 offset:42240\n"
      "s_add_u32 m0, s76, 6016\n"
      "v_mfma_f32_16x16x32_bf16 v[148:151], a[48:51], v[172:175], v[148:151]\n"
      "global_load_lds_dwordx4 v116, s[52:53] offset:128\n"
      "v_mfma_f32_16x16x32_bf16 v[188:191], a[16:19], v[156:159], 0\n"
      "s_add_u32 m0, s76, 12032\n"
      "s_mul_i32 s2, s24, 768\n"
      "s_add_u32 s2, s2, 150640\n"
      "v_mfma_f32_16x16x32_bf16 v[188:191], a[52:55], v[172:175], v[188:191]\n"
      "ds_read_b64_tr_b16 a[190:191], v153 offset:41472\n"
      "global_load_lds_dwordx4 v116, s[52:53] offset:256\n"
      "ds_read_b64_tr_b16 a[188:189], v153 offset:36864\n"
      "ds_read_b32 v72, v141 offset:3328\n"
      "v_lshl_add_u32 v81, v81, 2, s2\n"
      "v_mfma_f32_16x16x32_bf16 v[192:195], a[20:23], v[156:159], 0\n"
      "s_add_u32 m0, s77, 0\n"
      "v_lshl_add_u32 v78, v78, 2, s2\n"
      "v_lshl_add_u32 v79, v79, 2, s2\n"
      "v_mfma_f32_16x16x32_bf16 v[192:195], a[56:59], v[172:175], v[192:195]\n"
      "global_load_lds_dwordx4 v117, s[52:53]\n"
      "v_lshl_add_u32 v83, v83, 2, s2\n"
      "v_lshl_add_u32 v84, v84, 2, s2\n"
      "v_lshl_add_u32 v87, v87, 2, s2\n"
      "v_lshl_add_u32 v82, v82, 2, s2\n"
      "v_lshl_add_u32 v86, v86, 2, s2\n"
      "v_mfma_f32_16x16x32_bf16 v[196:199], a[24:27], v[156:159], 0\n"
      "ds_read_b32 v78, v78\n"
      "v_lshl_add_u32 v80, v80, 2, s2\n"
      "v_mfma_f32_16x16x32_bf16 v[196:199], a[60:63], v[172:175], v[196:199]\n"
      "ds_read_b32 v80, v80\n"
      "s_add_u32 m0, s78, 0\n"
      "ds_read_b32 v83, v83\n"
      "ds_read_b32 v81, v81\n"
      "ds_read_b32 v79, v79\n"
      "ds_read_b32 v82, v82\n"
      "s_mov_b32 exec_hi, 0\n"
      "global_load_lds_dwordx4 v118, s[52:53]\n"
      "s_mov_b32 exec_hi, -1\n"
      "s_add_u32 m0, s76, 110592\n"
      "v_mfma_f32_16x16x32_bf16 v[200:203], a[28:31], v[156:159], 0\n"
      "ds_read_b128 v[138:141], v114 offset:24576\n"
      "v_lshl_add_u32 v89, v89, 2, s2\n"
      "v_mfma_f32_16x16x32_bf16 v[200:203], a[64:67], v[172:175], v[200:203]\n"
      "global_load_lds_dwordx4 v119, s[56:57]\n"
      "v_lshl_add_u32 v85, v85, 2, s2\n"
      "v_mfma_f32_16x16x32_bf16 v[208:211], a[32:35], v[156:159], 0\n"
      "ds_read_b32 v85, v85\n"
      "s_add_u32 m0, s79, 0\n"
      "v_mfma_f32_16x16x32_bf16 v[208:211], a[68:71], v[172:175], v[208:211]\n"
      "global_load_lds_dword v122, s[58:59]\n"
      "global_load_dwordx4 v[126:129], v137, s[48:49] offset:-1024\n"
      "s_waitcnt lgkmcnt(1)\n"
      "v_mfma_f32_16x16x32_bf16 v[176:179], a[72:75], v[138:141], v[176:179]\n"
      "global_load_dwordx4 v[130:133], v137, s[48:49]\n"
      "global_load_dwordx4 v[156:159], v137, s[48:49] offset:1024\n"
      "v_lshl_add_u32 v90, v90, 2, s2\n"
      "v_mfma_f32_16x16x32_bf16 v[176:179], a[108:111], v[204:207], v[176:179]\n"
      "ds_read_b128 v[172:175], v114 offset:30720\n"
      "ds_read_b32 v84, v84\n"
      "v_lshl_add_u32 v88, v88, 2, s2\n"
      "v_mfma_f32_16x16x32_bf16 v[180:183], a[76:79], v[138:141], v[180:183]\n"
      "ds_read_b32 v86, v86\n"
      "v_lshl_add_u32 v91, v91, 2, s2\n"
      "v_mfma_f32_16x16x32_bf16 v[180:183], a[112:115], v[204:207], v[180:183]\n"
      "ds_read_b32 v90, v90\n"
      "v_lshl_add_u32 v92, v92, 2, s2\n"
      "v_mfma_f32_16x16x32_bf16 v[184:187], a[80:83], v[138:141], v[184:187]\n"
      "ds_read_b32 v92, v92\n"
      "v_lshl_add_u32 v93, v93, 2, s2\n"
      "v_mfma_f32_16x16x32_bf16 v[184:187], a[116:119], v[204:207], v[184:187]\n"
      "global_load_dwordx4 a[252:255], v155, s[6:7]\n"
      "v_mfma_f32_16x16x32_bf16 v[148:151], a[84:87], v[138:141], v[148:151]\n"
      "global_load_dwordx4 v[0:3], v155, s[6:7] offset:128\n"
      "ds_read_b128 v[212:215], v115 offset:30720\n"
      "v_mfma_f32_16x16x32_bf16 v[148:151], a[120:123], v[204:207], v[148:151]\n"
      "ds_read_b32 v89, v89\n"
      "v_lshl_add_u32 v94, v94, 2, s2\n"
      "v_mfma_f32_16x16x32_bf16 v[188:191], a[88:91], v[138:141], v[188:191]\n"
      "ds_read_b32 v91, v91\n"
      "v_lshl_add_u32 v95, v95, 2, s2\n"
      "v_mfma_f32_16x16x32_bf16 v[188:191], a[124:127], v[204:207], v[188:191]\n"
      "ds_read_b32 v94, v94\n"
      "v_lshl_add_u32 v99, v99, 2, s2\n"
      "v_mfma_f32_16x16x32_bf16 v[192:195], a[92:95], v[138:141], v[192:195]\n"
      "ds_read_b32 v87, v87\n"
      "ds_read_b32 v95, v95\n"
      "v_mfma_f32_16x16x32_bf16 v[192:195], a[128:131], v[204:207], v[192:195]\n"
      "ds_read_b32 v88, v88\n"
      "v_lshl_add_u32 v96, v96, 2, s2\n"
      "v_mfma_f32_16x16x32_bf16 v[196:199], a[96:99], v[138:141], v[196:199]\n"
      "ds_read_b32 v93, v93\n"
      "v_lshl_add_u32 v101, v101, 2, s2\n"
      "v_mfma_f32_16x16x32_bf16 v[196:199], a[132:135], v[204:207], v[196:199]\n"
      "global_load_dwordx4 v[4:7], v155, s[6:7] offset:256\n"
      "v_mfma_f32_16x16x32_bf16 v[200:203], a[100:103], v[138:141], v[200:203]\n"
      "ds_read_b32 v96, v96\n"
      "v_mfma_f32_16x16x32_bf16 v[200:203], a[136:139], v[204:207], v[200:203]\n"
      "s_nop 0\n"
      "v_lshl_add_u32 v97, v97, 2, s2\n"
      "v_lshl_add_u32 v98, v98, 2, s2\n"
      "v_mfma_f32_16x16x32_bf16 v[208:211], a[104:107], v[138:141], v[208:211]\n"
      "ds_read_b32 v97, v97\n"
      "v_add_u32 v155, 15360, v143\n"
      "v_mfma_f32_16x16x32_bf16 v[208:211], a[140:143], v[204:207], v[208:211]\n"
      "ds_read_b32 v99, v99\n"
      "s_waitcnt lgkmcnt(7)\n"
      "v_mfma_f32_16x16x32_bf16 v[176:179], a[144:147], v[172:175], v[176:179]\n"
      "ds_read_b32 v98, v98\n"
      "v_lshl_add_u32 v103, v103, 2, s2\n"
      "v_mfma_f32_16x16x32_bf16 v[176:179], a[180:183], v[212:215], v[176:179]\n"
      "ds_read_b128 v[140:143], v114 offset:20480\n"
      "v_lshl_add_u32 v100, v100, 2, s2\n"
      "v_mfma_f32_16x16x32_bf16 v[180:183], a[148:151], v[172:175], v[180:183]\n"
      "ds_read_b32 v103, v103\n"
      "v_mfma_f32_16x16x32_bf16 v[180:183], a[184:187], v[212:215], v[180:183]\n"
      "global_load_dwordx4 v[8:11], v155, s[6:7] offset:-3072\n"
      "v_mfma_f32_16x16x32_bf16 v[184:187], a[152:155], v[172:175], v[184:187]\n"
      "ds_read_b32 v101, v101\n"
      "v_lshl_add_u32 v102, v102, 2, s2\n"
      "v_mfma_f32_16x16x32_bf16 v[184:187], a[188:191], v[212:215], v[184:187]\n"
      "ds_read_b32 v102, v102\n"
      "v_lshl_add_u32 v104, v104, 2, s2\n"
      "v_mfma_f32_16x16x32_bf16 v[148:151], a[156:159], v[172:175], v[148:151]\n"
      "ds_read_b128 v[136:139], v115 offset:20480\n"
      "v_mfma_f32_16x16x32_bf16 v[148:151], a[192:195], v[212:215], v[148:151]\n"
      "ds_read_b32 v104, v104\n"
      "v_lshl_add_u32 v105, v105, 2, s2\n"
      "v_mfma_f32_16x16x32_bf16 v[188:191], a[160:163], v[172:175], v[188:191]\n"
      "v_lshl_add_u32 v106, v106, 2, s2\n"
      "v_lshl_add_u32 v109, v109, 2, s2\n"
      "v_mfma_f32_16x16x32_bf16 v[188:191], a[196:199], v[212:215], v[188:191]\n"
      "ds_read_b32 v105, v105\n"
      "v_lshl_add_u32 v107, v107, 2, s2\n"
      "v_mfma_f32_16x16x32_bf16 v[192:195], a[164:167], v[172:175], v[192:195]\n"
      "ds_read_b32 v100, v100\n"
      "v_mfma_f32_16x16x32_bf16 v[192:195], a[200:203], v[212:215], v[192:195]\n"
      "global_load_dwordx4 v[12:15], v155, s[6:7] offset:-2944\n"
      "v_mfma_f32_16x16x32_bf16 v[196:199], a[168:171], v[172:175], v[196:199]\n"
      "v_fma_f32 v166, v148, v148, s38\n"
      "v_lshl_add_u32 v108, v108, 2, s2\n"
      "v_mfma_f32_16x16x32_bf16 v[196:199], a[204:207], v[212:215], v[196:199]\n"
      "v_fma_f32 v169, v176, v176, s38\n"
      "v_mfma_f32_16x16x32_bf16 v[200:203], a[172:175], v[172:175], v[200:203]\n"
      "ds_read_b32 v107, v107\n"
      "v_fmac_f32_e64 v169, v177, v177\n"
      "v_mfma_f32_16x16x32_bf16 v[200:203], a[208:211], v[212:215], v[200:203]\n"
      "ds_read_b32 v106, v106\n"
      "v_lshl_add_u32 v111, v111, 2, s2\n"
      "v_mfma_f32_16x16x32_bf16 v[208:211], a[176:179], v[172:175], v[208:211]\n"
      "s_waitcnt lgkmcnt(4)\n"
      "v_mfma_f32_16x16x32_bf16 v[208:211], a[212:215], v[212:215], v[208:211]\n"
      "ds_read_b32 v109, v109\n"
      "v_fmac_f32 v169, v178, v178\n"
      "v_mfma_f32_16x16x32_bf16 v[172:175], a[0:3], v[140:143], 0\n"
      "global_load_dwordx4 v[16:19], v155, s[6:7] offset:-2816\n"
      "v_mfma_f32_16x16x32_bf16 v[172:175], a[36:39], v[136:139], v[172:175]\n"
      "ds_read_b32 v108, v108\n"
      "v_mfma_f32_16x16x32_bf16 v[220:223], a[16:19], v[140:143], 0\n"
      "ds_read_b32 v111, v111\n"
      "v_lshl_add_u32 v112, v112, 2, s2\n"
      "v_mfma_f32_16x16x32_bf16 v[212:215], a[4:7], v[140:143], 0\n"
      "v_lshl_add_u32 v110, v110, 2, s2\n"
      "v_fmac_f32 v166, v149, v149\n"
      "v_mfma_f32_16x16x32_bf16 v[212:215], a[40:43], v[136:139], v[212:215]\n"
      "ds_read_b32 v110, v110\n"
      "v_lshl_add_u32 v113, v113, 2, s2\n"
      "v_mfma_f32_16x16x32_bf16 v[204:207], a[8:11], v[140:143], 0\n"
      "v_fmac_f32 v166, v150, v150\n"
      "v_fmac_f32 v166, v151, v151\n"
      "v_mfma_f32_16x16x32_bf16 v[204:207], a[44:47], v[136:139], v[204:207]\n"
      "ds_read_b32 v112, v112\n"
      "v_fmac_f32 v169, v179, v179\n"
      "v_mfma_f32_16x16x32_bf16 v[216:219], a[12:15], v[140:143], 0\n"
      "global_load_dwordx4 v[20:23], v155, s[6:7]\n"
      "v_mfma_f32_16x16x32_bf16 v[216:219], a[48:51], v[136:139], v[216:219]\n"
      "ds_read_b32 v113, v113\n"
      "v_lshl_add_u32 v70, v70, 2, s2\n"
      "v_mfma_f32_16x16x32_bf16 v[220:223], a[52:55], v[136:139], v[220:223]\n"
      "ds_read_b128 v[236:239], v115 offset:26624\n"
      "ds_read_b32 v70, v70\n"
      "v_mfma_f32_16x16x32_bf16 v[224:227], a[20:23], v[140:143], 0\n"
      "v_fmac_f32_e64 v169, v180, v180\n"
      "v_lshl_add_u32 v71, v71, 2, s2\n"
      "v_mfma_f32_16x16x32_bf16 v[224:227], a[56:59], v[136:139], v[224:227]\n"
      "ds_read_b32 v71, v71\n"
      "v_fmac_f32 v166, v188, v188\n"
      "v_mfma_f32_16x16x32_bf16 v[228:231], a[24:27], v[140:143], 0\n"
      "ds_read_b128 v[232:235], v114 offset:26624\n"
      "v_fmac_f32 v169, v181, v181\n"
      "v_mfma_f32_16x16x32_bf16 v[228:231], a[60:63], v[136:139], v[228:231]\n"
      "v_fmac_f32 v169, v182, v182\n"
      "v_lshl_add_u32 v72, v72, 2, s2\n"
      "v_fmac_f32 v166, v189, v189\n"
      "v_mfma_f32_16x16x32_bf16 v[240:243], a[28:31], v[140:143], 0\n"
      "ds_read_b32 v72, v72\n"
      "v_fmac_f32 v166, v190, v190\n"
      "v_mfma_f32_16x16x32_bf16 v[240:243], a[64:67], v[136:139], v[240:243]\n"
      "global_load_dwordx4 v[24:27], v155, s[6:7] offset:128\n"
      "v_fmac_f32 v166, v191, v191\n"
      "v_fmac_f32 v169, v183, v183\n"
      "v_fmac_f32 v166, v192, v192\n"
      "v_fmac_f32 v169, v184, v184\n"
      "v_lshl_add_u32 v74, v74, 2, s2\n"
      "v_fmac_f32 v169, v185, v185\n"
      "v_fmac_f32 v169, v186, v186\n"
      "v_fmac_f32 v166, v193, v193\n"
      "v_fmac_f32 v169, v187, v187\n"
      "v_fmac_f32 v166, v194, v194\n"
      "v_fmac_f32 v166, v195, v195\n"
      "v_lshl_add_u32 v75, v75, 2, s2\n"
      "v_lshl_add_u32 v73, v73, 2, s2\n"
      "v_permlane16_swap_b32 v169, v166\n"
      "v_add_f32 v169, v169, v166\n"
      "s_waitcnt lgkmcnt(1)\n"
      "v_mfma_f32_16x16x32_bf16 v[172:175], a[72:75], v[232:235], v[172:175]\n"
      "ds_read_b32 v73, v73\n"
      "v_mov_b32_e64 v166, v169\n"
      "v_mfma_f32_16x16x32_bf16 v[172:175], a[108:111], v[236:239], v[172:175]\n"
      "ds_read_b32 v74, v74\n"
      "v_permlane32_swap_b32_e64 v169, v166\n"
      "v_mfma_f32_16x16x32_bf16 v[212:215], a[76:79], v[232:235], v[212:215]\n"
      "v_lshl_add_u32 v77, v77, 2, s2\n"
      "v_add_f32 v169, v169, v166\n"
      "v_mfma_f32_16x16x32_bf16 v[212:215], a[112:115], v[236:239], v[212:215]\n"
      "ds_read_b32 v75, v75\n"
      "v_rsq_f32 v169, v169\n"
      "v_mfma_f32_16x16x32_bf16 v[204:207], a[80:83], v[232:235], v[204:207]\n"
      "global_load_dwordx4 v[28:31], v155, s[6:7] offset:256\n"
      "v_mfma_f32_16x16x32_bf16 v[204:207], a[116:119], v[236:239], v[204:207]\n"
      "ds_read_b32 v77, v77\n"
      "v_mov_b32 v166, v169\n"
      "v_mfma_f32_16x16x32_bf16 v[216:219], a[84:87], v[232:235], v[216:219]\n"
      "s_nop 0\n"
      "v_permlane16_swap_b32 v169, v166\n"
      "v_mfma_f32_16x16x32_bf16 v[216:219], a[120:123], v[236:239], v[216:219]\n"
      "v_mul_f32 v188, v166, v188\n"
      "v_mul_f32 v149, v166, v149\n"
      "v_mul_f32 v191, v166, v191\n"
      "v_mul_f32 v148, v166, v148\n"
      "v_cvt_pk_bf16_f32 v148, v148, v149\n"
      "v_mul_f32 v151, v166, v151\n"
      "v_mul_f32_e64 v150, v166, v150\n"
      "v_cvt_pk_bf16_f32 v149, v150, v151\n"
      "v_mul_f32 v190, v166, v190\n"
      "v_mfma_f32_16x16x32_bf16 v[220:223], a[88:91], v[232:235], v[220:223]\n"
      "s_waitcnt lgkmcnt(2)\n"
      "v_mul_f32 v192, v166, v192\n"
      "v_mul_f32 v189, v166, v189\n"
      "v_mfma_f32_16x16x32_bf16 v[220:223], a[124:127], v[236:239], v[220:223]\n"
      "s_mov_b32 exec_lo, 0xffff0000\n"
      "s_mov_b32 exec_hi, 0xffffffff\n"
      "v_mov_b32 v70, 0xf149f2ca\n"
      "v_mov_b32 v72, 0xf149f2ca\n"
      "v_mov_b32 v74, 0xf149f2ca\n"
      "s_mov_b64 exec, -1\n"
      "v_cvt_pk_bf16_f32 v151, v190, v191\n"
      "v_cvt_pk_bf16_f32 v150, v188, v189\n"
      "v_mfma_f32_16x16x32_bf16 v[224:227], a[92:95], v[232:235], v[224:227]\n"
      "s_waitcnt lgkmcnt(0)\n"
      "s_mov_b32 exec_lo, 0x0\n"
      "s_mov_b32 exec_hi, 0xfffe0000\n"
      "v_mov_b32 v77, 0xf149f2ca\n"
      "s_mov_b64 exec, -1\n"
      "v_mfma_f32_16x16x32_bf16 v[188:191], a[32:35], v[140:143], 0\n"
      "ds_read_b128 v[140:143], v114 offset:32768\n"
      "v_mul_f32 v193, v166, v193\n"
      "v_mfma_f32_16x16x32_bf16 v[188:191], a[68:71], v[136:139], v[188:191]\n"
      "ds_read_b128 v[136:139], v115 offset:32768\n"
      "v_mul_f32 v195, v166, v195\n"
      "v_mfma_f32_16x16x32_bf16 v[224:227], a[128:131], v[236:239], v[224:227]\n"
      "v_mul_f32 v194, v166, v194\n"
      "v_mul_f32_e64 v169, s36, v169\n"
      "v_mfma_f32_16x16x32_bf16 v[228:231], a[96:99], v[232:235], v[228:231]\n"
      "s_mov_b32 s4, 0\n"
      "v_mul_f32 v177, v169, v177\n"
      "v_cvt_pk_bf16_f32 v34, v192, v193\n"
      "v_mfma_f32_16x16x32_bf16 v[228:231], a[132:135], v[236:239], v[228:231]\n"
      "v_mul_f32 v181, v169, v181\n"
      "v_mul_f32 v182, v169, v182\n"
      "v_mfma_f32_16x16x32_bf16 v[240:243], a[100:103], v[232:235], v[240:243]\n"
      "v_mul_f32 v176, v169, v176\n"
      "v_mul_f32 v179, v169, v179\n"
      "v_mfma_f32_16x16x32_bf16 v[240:243], a[136:139], v[236:239], v[240:243]\n"
      "v_cvt_pk_bf16_f32 v176, v176, v177\n"
      "v_mul_f32 v185, v169, v185\n"
      "v_mfma_f32_16x16x32_bf16 v[188:191], a[104:107], v[232:235], v[188:191]\n"
      "v_mul_f32_e64 v178, v169, v178\n"
      "s_mov_b32 s5, 0\n"
      "v_mfma_f32_16x16x32_bf16 v[188:191], a[140:143], v[236:239], v[188:191]\n"
      "v_cvt_pk_bf16_f32 v35, v194, v195\n"
      "v_cvt_pk_bf16_f32 v177, v178, v179\n"
      "v_mul_f32 v184, v169, v184\n"
      "s_waitcnt lgkmcnt(0)\n"
      "v_mfma_f32_16x16x32_bf16 v[172:175], a[144:147], v[140:143], v[172:175]\n"
      "v_mul_f32 v183, v169, v183\n"
      "v_mul_f32 v180, v169, v180\n"
      "v_mfma_f32_16x16x32_bf16 v[172:175], a[180:183], v[136:139], v[172:175]\n"
      "v_mul_f32 v186, v169, v186\n"
      "v_mul_f32 v187, v169, v187\n"
      "v_mfma_f32_16x16x32_bf16 v[212:215], a[148:151], v[140:143], v[212:215]\n"
      "s_waitcnt vmcnt(9)\n"
      "v_mov_b32 v134, v156\n"
      "v_cvt_pk_bf16_f32 v33, v186, v187\n"
      "v_mfma_f32_16x16x32_bf16 v[212:215], a[184:187], v[136:139], v[212:215]\n"
      "v_cvt_pk_bf16_f32 v32, v184, v185\n"
      "v_add_u32 v169, 36864, v158\n"
      "v_mfma_f32_16x16x32_bf16 v[204:207], a[152:155], v[140:143], v[204:207]\n"
      "v_mov_b32_e64 v156, -1\n"
      "v_mfma_f32_16x16x32_bf16 v[204:207], a[188:191], v[136:139], v[204:207]\n"
      "ds_write_b32 v129, v156\n"
      "v_mfma_f32_16x16x32_bf16 v[216:219], a[156:159], v[140:143], v[216:219]\n"
      "ds_write_b32 v129, v156 offset:1024\n"
      "v_mfma_f32_16x16x32_bf16 v[220:223], a[160:163], v[140:143], v[220:223]\n"
      "ds_read_b128 v[184:187], v119 offset:36864\n"
      "v_mfma_f32_16x16x32_bf16 v[220:223], a[196:199], v[136:139], v[220:223]\n"
      "ds_write_b32 v129, v156 offset:3072\n"
      "v_mfma_f32_16x16x32_bf16 v[224:227], a[164:167], v[140:143], v[224:227]\n"
      "ds_read_b32 v156, v122\n"
      "v_mfma_f32_16x16x32_bf16 v[224:227], a[200:203], v[136:139], v[224:227]\n"
      "v_cvt_pk_bf16_f32 v178, v180, v181\n"
      "v_mfma_f32_16x16x32_bf16 v[228:231], a[168:171], v[140:143], v[228:231]\n"
      "v_mfma_f32_16x16x32_bf16 v[228:231], a[204:207], v[136:139], v[228:231]\n"
      "v_mfma_f32_16x16x32_bf16 v[240:243], a[172:175], v[140:143], v[240:243]\n"
      "v_mov_b32_e64 v135, v157\n"
      "v_cvt_pk_bf16_f32 v179, v182, v183\n"
      "v_mfma_f32_16x16x32_bf16 v[240:243], a[208:211], v[136:139], v[240:243]\n"
      "v_mfma_f32_16x16x32_bf16 v[188:191], a[176:179], v[140:143], v[188:191]\n"
      "v_add_u32 v157, 36864, v159\n"
      "s_waitcnt lgkmcnt(2)\n"
      "v_mfma_f32_16x16x32_bf16 v[188:191], a[212:215], v[136:139], v[188:191]\n"
      "v_or3_b32 v184, v184, v185, v186\n"
      "v_and_b32_e64 v184, s40, v184\n"
      "s_waitcnt lgkmcnt(0)\n"
      "v_bitop3_b32 v156, v156, v187, s40 bitop3:0xa8\n"
      "v_or3_b32 v184, v184, v156, s4\n"
      "v_mfma_f32_16x16x32_bf16 v[216:219], a[192:195], v[136:139], v[216:219]\n"
      "ds_write_b32 v129, v184 offset:2048\n"
      "s_waitcnt lgkmcnt(0)\n"
      "s_barrier\n"
      "s_nop 4\n"
      "v_fma_f32 v166, v216, v216, s38\n"
      "v_fma_f32 v156, v172, v172, s38\n"
      "v_fmac_f32 v156, v173, v173\n"
      "v_fmac_f32 v156, v174, v174\n"
      "v_fmac_f32 v156, v175, v175\n"
      "v_fmac_f32 v166, v217, v217\n"
      "v_fmac_f32 v166, v218, v218\n"
      "v_fmac_f32 v166, v219, v219\n"
      "v_fmac_f32 v166, v220, v220\n"
      "v_fmac_f32 v166, v221, v221\n"
      "v_fmac_f32 v156, v212, v212\n"
      "v_fmac_f32 v166, v222, v222\n"
      "v_fmac_f32 v156, v213, v213\n"
      "v_fmac_f32 v156, v214, v214\n"
      "v_fmac_f32 v166, v223, v223\n"
      "v_fmac_f32 v166, v224, v224\n"
      "v_fmac_f32 v156, v215, v215\n"
      "v_fmac_f32 v156, v204, v204\n"
      "v_fmac_f32 v156, v205, v205\n"
      "v_fmac_f32 v166, v225, v225\n"
      "v_fmac_f32 v156, v206, v206\n"
      "v_fmac_f32 v166, v226, v226\n"
      "v_fmac_f32 v156, v207, v207\n"
      "v_fmac_f32_e64 v166, v227, v227\n"
      "v_cvt_pk_bf16_f32 v200, v200, v201\n"
      "v_cvt_pk_bf16_f32 v201, v202, v203\n"
      "v_permlane16_swap_b32 v156, v166\n"
      "v_cvt_pk_bf16_f32 v196, v196, v197\n"
      "v_add_f32 v156, v156, v166\n"
      "v_mov_b32 v166, v156\n"
      "s_mov_b32 s27, 0\n"
      "ds_read_b128 v[140:143], v115\n"
      "v_cvt_pk_bf16_f32 v208, v208, v209\n"
      "v_permlane32_swap_b32 v156, v166\n"
      "s_add_u32 s28, s27, 1\n"
      "v_add_f32 v156, v156, v166\n"
      "v_rsq_f32 v156, v156\n"
      "v_cvt_pk_bf16_f32 v209, v210, v211\n"
      "v_mov_b32 v166, v156\n"
      "s_min_u32 m0, s28, s3\n"
      "ds_read_b128 v[136:139], v114\n"
      "ds_write_b128 v170, v[176:179]\n"
      "v_permlane16_swap_b32 v156, v166\n"
      "v_cvt_pk_bf16_f32 v197, v198, v199\n"
      "v_mul_f32 v156, s36, v156\n"
      "v_mul_f32 v172, v156, v172\n"
      "v_mul_f32 v217, v166, v217\n"
      "v_mul_f32 v223, v166, v223\n"
      "v_mul_f32 v173, v156, v173\n"
      "v_mul_f32 v174, v156, v174\n"
      "v_mul_f32 v226, v166, v226\n"
      "v_mul_f32 v224, v166, v224\n"
      "v_mul_f32 v218, v166, v218\n"
      "s_movrels_b32 s9, s80\n"
      "s_lshr_b32 s8, s9, 8\n"
      "ds_write_b128 v170, v[32:35] offset:16\n"
      "v_mul_f32_e64 v220, v166, v220\n"
      "v_mul_f32_e64 v219, v166, v219\n"
      "v_mul_f32_e64 v227, v166, v227\n"
      "s_and_b32 s9, s9, 0xff\n"
      "v_mul_f32_e64 v225, v166, v225\n"
      "v_mul_f32_e64 v222, v166, v222\n"
      "s_mul_i32 s10, s8, 73\n"
      "v_mul_f32_e64 v221, v166, v221\n"
      "v_mul_f32 v216, v166, v216\n"
      "v_cvt_pk_bf16_f32 v216, v216, v217\n"
      "v_cvt_pk_bf16_f32 v217, v218, v219\n"
      "s_waitcnt lgkmcnt(2)\n"
      "ds_write_b128 v170, v[148:151] offset:32\n"
      "v_cvt_pk_bf16_f32 v218, v220, v221\n"
      "s_add_u32 s10, s10, s9\n"
      "v_mul_f32 v215, v156, v215\n"
      "v_cvt_pk_bf16_f32 v219, v222, v223\n"
      "v_cvt_pk_bf16_f32 v40, v224, v225\n"
      "s_mul_i32 s28, s10, 4802\n"
      "v_mul_f32_e64 v175, v156, v175\n"
      "v_cvt_pk_bf16_f32 v172, v172, v173\n"
      "v_cvt_pk_bf16_f32 v173, v174, v175\n"
      "v_mul_f32_e64 v212, v156, v212\n"
      "v_cvt_pk_bf16_f32 v41, v226, v227\n"
      "v_mfma_f32_16x16x32_bf16 v[148:151], a[0:3], v[136:139], 0\n"
      "ds_read_b128 v[176:179], v114 offset:6144\n"
      "v_mul_f32 v213, v156, v213\n"
      "v_mfma_f32_16x16x32_bf16 v[148:151], a[36:39], v[140:143], v[148:151]\n"
      "ds_write_b64 v171, v[196:197]\n"
      "v_mfma_f32_16x16x32_bf16 v[196:199], a[4:7], v[136:139], 0\n"
      "s_add_u32 s56, s18, s28\n"
      "ds_read_b128 v[184:187], v115 offset:6144\n"
      "v_mfma_f32_16x16x32_bf16 v[196:199], a[40:43], v[140:143], v[196:199]\n"
      "ds_write_b64 v171, v[200:201] offset:32\n"
      "v_mfma_f32_16x16x32_bf16 v[200:203], a[8:11], v[136:139], 0\n"
      "s_addc_u32 s57, s19, 0\n"
      "s_and_b32 s56, s56, -16\n"
      "s_sub_u32 s58, s56, 110592\n"
      "v_mfma_f32_16x16x32_bf16 v[200:203], a[44:47], v[140:143], v[200:203]\n"
      "ds_write_b64 v171, v[208:209] offset:64\n"
      "v_mfma_f32_16x16x32_bf16 v[180:183], a[12:15], v[136:139], 0\n"
      "s_subb_u32 s59, s57, 0\n"
      "v_cvt_pk_bf16_f32 v174, v212, v213\n"
      "v_mul_f32 v214, v156, v214\n"
      "v_mfma_f32_16x16x32_bf16 v[180:183], a[48:51], v[140:143], v[180:183]\n"
      "ds_read_b128 v[152:155], v128 offset:2048\n"
      "v_cvt_pk_bf16_f32 v175, v214, v215\n"
      "s_sub_u32 s56, s56, 73728\n"
      "v_mfma_f32_16x16x32_bf16 v[192:195], a[16:19], v[136:139], 0\n"
      "s_subb_u32 s57, s57, 0\n"
      "v_mul_f32 v205, v156, v205\n"
      "v_mul_f32 v204, v156, v204\n"
      "v_mfma_f32_16x16x32_bf16 v[192:195], a[52:55], v[140:143], v[192:195]\n"
      "ds_read_b128 v[212:215], v114 offset:12288\n"
      "v_mul_f32 v206, v156, v206\n"
      "v_mfma_f32_16x16x32_bf16 v[224:227], a[20:23], v[136:139], 0\n"
      "v_cvt_pk_bf16_f32 v38, v204, v205\n"
      "s_mul_i32 s28, s8, 1373568\n"
      "v_mfma_f32_16x16x32_bf16 v[224:227], a[56:59], v[140:143], v[224:227]\n"
      "ds_read_b128 v[220:223], v115 offset:12288\n"
      "v_mul_f32_e64 v207, v156, v207\n"
      "v_mfma_f32_16x16x32_bf16 v[208:211], v[136:139], a[24:27], 0\n"
      "s_mul_i32 s29, s9, 2688\n"
      "v_cvt_pk_bf16_f32 v39, v206, v207\n"
      "v_cvt_pk_bf16_f32 v228, v228, v229\n"
      "v_mfma_f32_16x16x32_bf16 v[208:211], v[140:143], a[60:63], v[208:211]\n"
      "s_waitcnt lgkmcnt(4)\n"
      "v_cvt_pk_bf16_f32 v240, v240, v241\n"
      "v_cvt_pk_bf16_f32 v229, v230, v231\n"
      "v_mfma_f32_16x16x32_bf16 v[204:207], v[136:139], a[28:31], 0\n"
      "s_add_u32 s28, s28, s29\n"
      "v_cvt_pk_bf16_f32 v241, v242, v243\n"
      "v_cvt_pk_bf16_f32 v188, v188, v189\n"
      "v_mfma_f32_16x16x32_bf16 v[204:207], v[140:143], a[64:67], v[204:207]\n"
      "s_cmp_eq_u32 s8, 72\n"
      "s_cselect_b32 s8, 0, 100270464\n"
      "v_cvt_pk_bf16_f32 v189, v190, v191\n"
      "v_mfma_f32_16x16x32_bf16 v[236:239], v[136:139], a[32:35], 0\n"
      "s_cmp_eq_u32 s9, 72\n"
      "s_cselect_b32 s9, 0, 196224\n"
      "v_mfma_f32_16x16x32_bf16 v[236:239], v[140:143], a[68:71], v[236:239]\n"
      "s_add_u32 s10, s8, s9\n"
      "s_add_u32 s28, s28, s10\n"
      "s_add_u32 s52, s16, s28\n"
      "v_mfma_f32_16x16x32_bf16 v[148:151], a[72:75], v[176:179], v[148:151]\n"
      "s_addc_u32 s53, s17, 0\n"
      "s_waitcnt lgkmcnt(1)\n"
      "v_mfma_f32_16x16x32_bf16 v[148:151], a[108:111], v[184:187], v[148:151]\n"
      "v_bitop3_b32 v152, v152, v153, s4 bitop3:0x7e\n"
      "s_cmp_lg_u32 s10, s11\n"
      "v_mfma_f32_16x16x32_bf16 v[192:195], a[88:91], v[176:179], v[192:195]\n"
      "s_cbranch_scc1 L_edge_6_%=\n"
      "L_eb_7_%=:\n"
      "v_mfma_f32_16x16x32_bf16 v[196:199], a[76:79], v[176:179], v[196:199]\n"
      "s_waitcnt lgkmcnt(0)\n"
      "v_bitop3_b32 v154, v154, v155, s4 bitop3:0x7e\n"
      "v_mfma_f32_16x16x32_bf16 v[196:199], a[112:115], v[184:187], v[196:199]\n"
      "s_add_u32 m0, s76, 18432\n"
      "v_or_b32 v152, v152, v154\n"
      "v_cmp_ne_u32 vcc, 0, v152\n"
      "v_mfma_f32_16x16x32_bf16 v[200:203], a[80:83], v[176:179], v[200:203]\n"
      "global_load_lds_dwordx4 v116, s[52:53]\n"
      "v_mfma_f32_16x16x32_bf16 v[200:203], a[116:119], v[184:187], v[200:203]\n"
      "s_mov_b32 exec_lo, 0x003f003f\n"
      "s_mov_b32 exec_hi, 0x003f003f\n"
      "ds_write_b128 v170, v[172:175] offset:18688\n"
      "ds_write_b128 v170, v[38:41] offset:18704\n"
      "ds_write_b128 v170, v[216:219] offset:18720\n"
      "ds_write_b64 v171, v[228:229] offset:18688\n"
      "ds_write_b64 v171, v[240:241] offset:18720\n"
      "ds_write_b64 v171, v[188:189] offset:18752\n"
      "s_mov_b64 exec, -1\n"
      "v_mfma_f32_16x16x32_bf16 v[180:183], a[84:87], v[176:179], v[180:183]\n"
      "s_mov_b64 s[48:49], vcc\n"
      "s_add_u32 m0, s76, 24448\n"
      "v_mfma_f32_16x16x32_bf16 v[180:183], a[120:123], v[184:187], v[180:183]\n"
      "global_load_lds_dwordx4 v116, s[52:53] offset:128\n"
      "v_mfma_f32_16x16x32_bf16 v[224:227], a[92:95], v[176:179], v[224:227]\n"
      "v_mfma_f32_16x16x32_bf16 v[224:227], a[128:131], v[184:187], v[224:227]\n"
      "s_cbranch_vccnz L_flg_3_%=\n"
      "L_fb_4_%=:\n"
      "v_mfma_f32_16x16x32_bf16 v[140:143], v[144:147], a[216:219], 0\n"
      "v_mfma_f32_16x16x32_bf16 v[140:143], v[144:147], a[220:223], v[140:143]\n"
      "s_nop 0\n"
      "v_mfma_f32_16x16x32_bf16 v[140:143], v[144:147], a[224:227], v[140:143]\n"
      "s_add_u32 m0, s76, 30464\n"
      "ds_read_b128 v[152:155], v115 offset:2048\n"
      "v_mfma_f32_16x16x32_bf16 v[208:211], v[176:179], a[96:99], v[208:211]\n"
      "v_mfma_f32_16x16x32_bf16 v[208:211], v[184:187], a[132:135], v[208:211]\n"
      "global_load_lds_dwordx4 v116, s[52:53] offset:256\n"
      "v_mfma_f32_16x16x32_bf16 v[204:207], v[176:179], a[100:103], v[204:207]\n"
      "s_add_u32 m0, s77, 18432\n"
      "v_mfma_f32_16x16x32_bf16 v[204:207], v[184:187], a[136:139], v[204:207]\n"
      "s_nop 0\n"
      "v_add_f32_dpp v140, v140, v140 quad_perm:[1,0,3,2] row_mask:0xf bank_mask:0xf\n"
      "v_mfma_f32_16x16x32_bf16 v[236:239], v[176:179], a[104:107], v[236:239]\n"
      "s_nop 0\n"
      "v_add_f32_dpp v140, v140, v140 quad_perm:[2,3,0,1] row_mask:0xf bank_mask:0xf\n"
      "v_mfma_f32_16x16x32_bf16 v[236:239], v[184:187], a[140:143], v[236:239]\n"
      "ds_read_b128 v[136:139], v114 offset:2048\n"
      "v_mfma_f32_16x16x32_bf16 v[192:195], a[124:127], v[184:187], v[192:195]\n"
      "v_add_f32_dpp v140, v140, v140 row_half_mirror row_mask:0xf bank_mask:0xf\n"
      "v_mul_f32 v141, 0xbbaaaaab, v140\n"
      "v_mfma_f32_16x16x32_bf16 v[148:151], a[144:147], v[212:215], v[148:151]\n"
      "v_mul_f32 v142, 0xbbaaaaab, v140\n"
      "v_mul_f32 v143, 0xbbaaaaab, v140\n"
      "v_mfma_f32_16x16x32_bf16 v[148:151], a[180:183], v[220:223], v[148:151]\n"
      "v_mfma_f32_16x16x32_bf16 v[196:199], a[148:151], v[212:215], v[196:199]\n"
      "v_mul_f32 v140, 0xbbaaaaab, v140\n"
      "v_mfma_f32_16x16x32_bf16 v[196:199], a[184:187], v[220:223], v[196:199]\n"
      "s_nop 0\n"
      "v_mfma_f32_16x16x32_bf16 v[184:187], v[162:165], a[216:219], v[140:143]\n"
      "v_mfma_f32_16x16x32_bf16 v[176:179], v[160:163], a[216:219], v[140:143]\n"
      "s_nop 1\n"
      "v_fma_f32 v156, v148, v148, s38\n"
      "v_mfma_f32_16x16x32_bf16 v[188:191], v[160:163], a[220:223], v[140:143]\n"
      "v_mfma_f32_16x16x32_bf16 v[172:175], v[162:165], a[220:223], v[140:143]\n"
      "s_nop 1\n"
      "v_cvt_pk_bf16_f32 v184, v184, v185\n"
      "v_mfma_f32_16x16x32_bf16 v[216:219], v[162:165], a[224:227], v[140:143]\n"
      "v_cvt_pk_bf16_f32 v185, v186, v187\n"
      "v_cvt_pk_bf16_f32 v186, v176, v177\n"
      "v_mfma_f32_16x16x32_bf16 v[240:243], v[160:163], a[224:227], v[140:143]\n"
      "s_waitcnt lgkmcnt(0)\n"
      "v_cvt_pk_bf16_f32 v187, v178, v179\n"
      "v_cvt_pk_bf16_f32 v172, v172, v173\n"
      "v_mfma_f32_16x16x32_bf16 v[200:203], a[152:155], v[212:215], v[200:203]\n"
      "v_cvt_pk_bf16_f32 v173, v174, v175\n"
      "v_cvt_pk_bf16_f32 v174, v188, v189\n"
      "v_mfma_f32_16x16x32_bf16 v[200:203], a[188:191], v[220:223], v[200:203]\n"
      "ds_write_b128 v168, v[184:187]\n"
      "v_mfma_f32_16x16x32_bf16 v[140:143], v[144:147], a[228:231], 0\n"
      "global_load_lds_dwordx4 v117, s[52:53]\n"
      "v_mfma_f32_16x16x32_bf16 v[140:143], v[144:147], a[232:235], v[140:143]\n"
      "s_add_u32 m0, s78, 18432\n"
      "v_cvt_pk_bf16_f32 v175, v190, v191\n"
      "v_cvt_pk_bf16_f32 v216, v216, v217\n"
      "v_mfma_f32_16x16x32_bf16 v[180:183], a[156:159], v[212:215], v[180:183]\n"
      "v_cvt_pk_bf16_f32 v217, v218, v219\n"
      "v_cvt_pk_bf16_f32 v218, v240, v241\n"
      "v_mfma_f32_16x16x32_bf16 v[180:183], a[192:195], v[220:223], v[180:183]\n"
      "ds_write_b128 v168, v[172:175] offset:128\n"
      "v_mfma_f32_16x16x32_bf16 v[140:143], v[144:147], a[236:239], v[140:143]\n"
      "v_fmac_f32_e64 v156, v149, v149\n"
      "v_cvt_pk_bf16_f32 v219, v242, v243\n"
      "v_mfma_f32_16x16x32_bf16 v[192:195], a[160:163], v[212:215], v[192:195]\n"
      "v_fmac_f32 v156, v150, v150\n"
      "v_fmac_f32 v156, v151, v151\n"
      "v_mfma_f32_16x16x32_bf16 v[192:195], a[196:199], v[220:223], v[192:195]\n"
      "ds_write_b128 v168, v[216:219] offset:256\n"
      "v_mfma_f32_16x16x32_bf16 v[224:227], a[164:167], v[212:215], v[224:227]\n"
      "v_add_f32_dpp v140, v140, v140 quad_perm:[1,0,3,2] row_mask:0xf bank_mask:0xf\n"
      "v_fma_f32 v166, v180, v180, s38\n"
      "v_mfma_f32_16x16x32_bf16 v[224:227], a[200:203], v[220:223], v[224:227]\n"
      "v_fmac_f32 v166, v181, v181\n"
      "v_add_f32_dpp v140, v140, v140 quad_perm:[2,3,0,1] row_mask:0xf bank_mask:0xf\n"
      "v_mfma_f32_16x16x32_bf16 v[208:211], v[212:215], a[168:171], v[208:211]\n"
      "s_nop 0\n"
      "v_add_f32_dpp v140, v140, v140 row_half_mirror row_mask:0xf bank_mask:0xf\n"
      "v_mul_f32 v143, 0xbbaaaaab, v140\n"
      "v_mfma_f32_16x16x32_bf16 v[208:211], v[220:223], a[204:207], v[208:211]\n"
      "v_mul_f32 v141, 0xbbaaaaab, v140\n"
      "v_mul_f32 v142, 0xbbaaaaab, v140\n"
      "v_mfma_f32_16x16x32_bf16 v[204:207], v[212:215], a[172:175], v[204:207]\n"
      "v_mul_f32 v140, 0xbbaaaaab, v140\n"
      "v_fmac_f32_e64 v166, v182, v182\n"
      "v_mfma_f32_16x16x32_bf16 v[204:207], v[220:223], a[208:211], v[204:207]\n"
      "v_fmac_f32 v166, v183, v183\n"
      "v_fmac_f32 v156, v196, v196\n"
      "v_mfma_f32_16x16x32_bf16 v[172:175], v[162:165], a[228:231], v[140:143]\n"
      "v_fmac_f32 v166, v192, v192\n"
      "v_fmac_f32 v156, v197, v197\n"
      "v_mfma_f32_16x16x32_bf16 v[188:191], v[160:163], a[228:231], v[140:143]\n"
      "ds_read_b128 v[184:187], v114 offset:8192\n"
      "v_mfma_f32_16x16x32_bf16 v[176:179], v[162:165], a[232:235], v[140:143]\n"
      "v_fmac_f32_e64 v156, v198, v198\n"
      "v_fmac_f32_e64 v166, v193, v193\n"
      "v_mfma_f32_16x16x32_bf16 v[216:219], v[160:163], a[232:235], v[140:143]\n"
      "v_cvt_pk_bf16_f32 v172, v172, v173\n"
      "v_cvt_pk_bf16_f32 v173, v174, v175\n"
      "v_mfma_f32_16x16x32_bf16 v[240:243], v[162:165], a[236:239], v[140:143]\n"
      "v_cvt_pk_bf16_f32 v175, v190, v191\n"
      "v_cvt_pk_bf16_f32 v174, v188, v189\n"
      "v_mfma_f32_16x16x32_bf16 v[188:191], v[160:163], a[236:239], v[140:143]\n"
      "ds_write_b128 v168, v[172:175] offset:3072\n"
      "v_mfma_f32_16x16x32_bf16 v[236:239], v[212:215], a[176:179], v[236:239]\n"
      "ds_read_b128 v[172:175], v115 offset:8192\n"
      "v_cvt_pk_bf16_f32 v176, v176, v177\n"
      "v_mfma_f32_16x16x32_bf16 v[236:239], v[220:223], a[212:215], v[236:239]\n"
      "v_cvt_pk_bf16_f32 v177, v178, v179\n"
      "v_cvt_pk_bf16_f32 v178, v216, v217\n"
      "v_mfma_f32_16x16x32_bf16 v[140:143], a[0:3], v[136:139], 0\n"
      "v_cvt_pk_bf16_f32 v240, v240, v241\n"
      "v_cvt_pk_bf16_f32 v179, v218, v219\n"
      "v_mfma_f32_16x16x32_bf16 v[140:143], a[36:39], v[152:155], v[140:143]\n"
      "ds_write_b128 v168, v[176:179] offset:3200\n"
      "v_mfma_f32_16x16x32_bf16 v[176:179], a[4:7], v[136:139], 0\n"
      "v_fmac_f32_e64 v166, v194, v194\n"
      "v_cvt_pk_bf16_f32 v241, v242, v243\n"
      "v_mfma_f32_16x16x32_bf16 v[176:179], a[40:43], v[152:155], v[176:179]\n"
      "s_mov_b32 exec_hi, 0\n"
      "global_load_lds_dwordx4 v118, s[52:53]\n"
      "s_mov_b32 exec_hi, -1\n"
      "v_mfma_f32_16x16x32_bf16 v[216:219], a[8:11], v[136:139], 0\n"
      "v_cvt_pk_bf16_f32 v243, v190, v191\n"
      "v_cvt_pk_bf16_f32 v242, v188, v189\n"
      "v_mfma_f32_16x16x32_bf16 v[216:219], a[44:47], v[152:155], v[216:219]\n"
      "s_waitcnt lgkmcnt(1)\n"
      "v_fmac_f32 v156, v199, v199\n"
      "v_fmac_f32_e64 v156, v200, v200\n"
      "v_mfma_f32_16x16x32_bf16 v[188:191], a[12:15], v[136:139], 0\n"
      "v_fmac_f32 v166, v195, v195\n"
      "v_fmac_f32 v166, v224, v224\n"
      "v_mfma_f32_16x16x32_bf16 v[188:191], a[48:51], v[152:155], v[188:191]\n"
      "ds_write_b128 v168, v[240:243] offset:3328\n"
      "v_mfma_f32_16x16x32_bf16 v[240:243], a[16:19], v[136:139], 0\n"
      "v_fmac_f32 v166, v225, v225\n"
      "v_fmac_f32 v166, v226, v226\n"
      "v_mfma_f32_16x16x32_bf16 v[240:243], a[52:55], v[152:155], v[240:243]\n"
      "v_fmac_f32 v166, v227, v227\n"
      "v_fmac_f32 v156, v201, v201\n"
      "v_mfma_f32_16x16x32_bf16 v[220:223], a[20:23], v[136:139], 0\n"
      "v_fmac_f32 v156, v202, v202\n"
      "v_fmac_f32 v156, v203, v203\n"
      "v_mfma_f32_16x16x32_bf16 v[220:223], a[56:59], v[152:155], v[220:223]\n"
      "s_nop 0\n"
      "v_permlane16_swap_b32 v156, v166\n"
      "v_mfma_f32_16x16x32_bf16 v[212:215], v[136:139], a[24:27], 0\n"
      "s_add_u32 m0, s76, 115456\n"
      "v_add_f32 v156, v156, v166\n"
      "v_mov_b32 v166, v156\n"
      "v_mfma_f32_16x16x32_bf16 v[212:215], v[152:155], a[60:63], v[212:215]\n"
      "s_nop 0\n"
      "v_permlane32_swap_b32 v156, v166\n"
      "v_mfma_f32_16x16x32_bf16 v[228:231], v[136:139], a[28:31], 0\n"
      "v_add_f32 v156, v156, v166\n"
      "v_mfma_f32_16x16x32_bf16 v[228:231], v[152:155], a[64:67], v[228:231]\n"
      "v_rsq_f32 v156, v156\n"
      "v_mfma_f32_16x16x32_bf16 v[140:143], a[72:75], v[184:187], v[140:143]\n"
      "v_mov_b32 v166, v156\n"
      "v_mfma_f32_16x16x32_bf16 v[140:143], a[108:111], v[172:175], v[140:143]\n"
      "s_nop 0\n"
      "v_permlane16_swap_b32_e64 v156, v166\n"
      "v_mfma_f32_16x16x32_bf16 v[176:179], a[76:79], v[184:187], v[176:179]\n"
      "global_load_lds_dwordx4 v119, s[56:57]\n"
      "v_mfma_f32_16x16x32_bf16 v[176:179], a[112:115], v[172:175], v[176:179]\n"
      "v_mul_f32 v181, v166, v181\n"
      "v_mul_f32 v180, v166, v180\n"
      "v_mfma_f32_16x16x32_bf16 v[216:219], a[80:83], v[184:187], v[216:219]\n"
      "v_cvt_pk_bf16_f32 v180, v180, v181\n"
      "v_mul_f32_e64 v182, v166, v182\n"
      "v_mfma_f32_16x16x32_bf16 v[216:219], a[116:119], v[172:175], v[216:219]\n"
      "s_add_u32 m0, s79, 4864\n"
      "v_mul_f32 v183, v166, v183\n"
      "v_cvt_pk_bf16_f32 v181, v182, v183\n"
      "v_mfma_f32_16x16x32_bf16 v[188:191], a[84:87], v[184:187], v[188:191]\n"
      "v_mul_f32 v193, v166, v193\n"
      "v_mul_f32 v192, v166, v192\n"
      "v_mfma_f32_16x16x32_bf16 v[188:191], a[120:123], v[172:175], v[188:191]\n"
      "v_mul_f32 v194, v166, v194\n"
      "v_cvt_pk_bf16_f32 v182, v192, v193\n"
      "v_mfma_f32_16x16x32_bf16 v[232:235], v[136:139], a[32:35], 0\n"
      "ds_read_b128 v[136:139], v114 offset:14336\n"
      "v_mul_f32_e64 v195, v166, v195\n"
      "v_mfma_f32_16x16x32_bf16 v[232:235], v[152:155], a[68:71], v[232:235]\n"
      "ds_read_b128 v[152:155], v115 offset:14336\n"
      "v_cvt_pk_bf16_f32 v183, v194, v195\n"
      "v_mfma_f32_16x16x32_bf16 v[240:243], a[88:91], v[184:187], v[240:243]\n"
      "v_mul_f32 v225, v166, v225\n"
      "v_mul_f32 v224, v166, v224\n"
      "v_mfma_f32_16x16x32_bf16 v[240:243], a[124:127], v[172:175], v[240:243]\n"
      "v_mul_f32 v226, v166, v226\n"
      "v_cvt_pk_bf16_f32 v34, v224, v225\n"
      "v_mfma_f32_16x16x32_bf16 v[192:195], v[144:147], a[240:243], 0\n"
      "v_mul_f32 v227, v166, v227\n"
      "v_cvt_pk_bf16_f32 v35, v226, v227\n"
      "v_mfma_f32_16x16x32_bf16 v[192:195], v[144:147], a[244:247], v[192:195]\n"
      "v_mul_f32 v156, s36, v156\n"
      "v_mul_f32 v148, v156, v148\n"
      "v_mfma_f32_16x16x32_bf16 v[192:195], v[144:147], a[248:251], v[192:195]\n"
      "v_mul_f32_e64 v149, v156, v149\n"
      "v_cvt_pk_bf16_f32 v148, v148, v149\n"
      "v_mfma_f32_16x16x32_bf16 v[220:223], a[92:95], v[184:187], v[220:223]\n"
      "v_mul_f32 v150, v156, v150\n"
      "v_mul_f32 v151, v156, v151\n"
      "v_mfma_f32_16x16x32_bf16 v[220:223], a[128:131], v[172:175], v[220:223]\n"
      "v_mul_f32 v196, v156, v196\n"
      "v_cvt_pk_bf16_f32 v149, v150, v151\n"
      "v_mfma_f32_16x16x32_bf16 v[212:215], v[184:187], a[96:99], v[212:215]\n"
      "v_mul_f32 v197, v156, v197\n"
      "v_mul_f32_e64 v198, v156, v198\n"
      "v_mfma_f32_16x16x32_bf16 v[212:215], v[172:175], a[132:135], v[212:215]\n"
      "v_mul_f32 v201, v156, v201\n"
      "v_cvt_pk_bf16_f32 v150, v196, v197\n"
      "v_mfma_f32_16x16x32_bf16 v[228:231], v[184:187], a[100:103], v[228:231]\n"
      "v_mul_f32 v199, v156, v199\n"
      "v_cvt_pk_bf16_f32 v151, v198, v199\n"
      "v_mfma_f32_16x16x32_bf16 v[228:231], v[172:175], a[136:139], v[228:231]\n"
      "v_mul_f32_e64 v200, v156, v200\n"
      "v_add_f32_dpp v192, v192, v192 quad_perm:[1,0,3,2] row_mask:0xf bank_mask:0xf\n"
      "v_mfma_f32_16x16x32_bf16 v[232:235], v[184:187], a[104:107], v[232:235]\n"
      "s_waitcnt lgkmcnt(0)\n"
      "v_mul_f32 v202, v156, v202\n"
      "v_cvt_pk_bf16_f32 v32, v200, v201\n"
      "v_mfma_f32_16x16x32_bf16 v[232:235], v[172:175], a[140:143], v[232:235]\n"
      "global_load_lds_dword v122, s[58:59]\n"
      "v_mfma_f32_16x16x32_bf16 v[140:143], a[144:147], v[136:139], v[140:143]\n"
      "v_mul_f32_e64 v203, v156, v203\n"
      "v_add_f32_dpp v192, v192, v192 quad_perm:[2,3,0,1] row_mask:0xf bank_mask:0xf\n"
      "v_mfma_f32_16x16x32_bf16 v[140:143], a[180:183], v[152:155], v[140:143]\n"
      "v_cvt_pk_bf16_f32 v33, v202, v203\n"
      "v_cvt_pk_bf16_f32 v208, v208, v209\n"
      "v_mfma_f32_16x16x32_bf16 v[176:179], a[148:151], v[136:139], v[176:179]\n"
      "ds_read_b128 v[184:187], v115 offset:4096\n"
      "v_mfma_f32_16x16x32_bf16 v[176:179], a[184:187], v[152:155], v[176:179]\n"
      "v_add_f32_dpp v192, v192, v192 row_half_mirror row_mask:0xf bank_mask:0xf\n"
      "v_mul_f32 v195, 0xbbaaaaab, v192\n"
      "s_waitcnt vmcnt(15)\n"
      "v_mfma_f32_16x16x32_bf16 v[200:203], v[144:147], a[252:255], 0\n"
      "v_cvt_pk_bf16_f32 v209, v210, v211\n"
      "v_mul_f32 v193, 0xbbaaaaab, v192\n"
      "s_waitcnt vmcnt(14)\n"
      "v_mfma_f32_16x16x32_bf16 v[200:203], v[144:147], v[0:3], v[200:203]\n"
      "s_nop 0\n"
      "v_mul_f32 v194, 0xbbaaaaab, v192\n"
      "v_mul_f32 v192, 0xbbaaaaab, v192\n"
      "s_waitcnt vmcnt(13)\n"
      "v_mfma_f32_16x16x32_bf16 v[200:203], v[144:147], v[4:7], v[200:203]\n"
      "v_mfma_f32_16x16x32_bf16 v[196:199], v[162:165], a[240:243], v[192:195]\n"
      "v_mfma_f32_16x16x32_bf16 v[224:227], v[160:163], a[240:243], v[192:195]\n"
      "ds_read_b128 v[172:175], v114 offset:4096\n"
      "v_mfma_f32_16x16x32_bf16 v[216:219], a[152:155], v[136:139], v[216:219]\n"
      "v_mfma_f32_16x16x32_bf16 v[216:219], a[188:191], v[152:155], v[216:219]\n"
      "v_mfma_f32_16x16x32_bf16 v[188:191], a[156:159], v[136:139], v[188:191]\n"
      "v_mfma_f32_16x16x32_bf16 v[188:191], a[192:195], v[152:155], v[188:191]\n"
      "s_nop 0\n"
      "v_mfma_f32_16x16x32_bf16 v[240:243], a[160:163], v[136:139], v[240:243]\n"
      "s_nop 0\n"
      "v_cvt_pk_bf16_f32 v196, v196, v197\n"
      "v_cvt_pk_bf16_f32 v197, v198, v199\n"
      "v_mfma_f32_16x16x32_bf16 v[240:243], a[196:199], v[152:155], v[240:243]\n"
      "v_mfma_f32_16x16x32_bf16 v[220:223], a[164:167], v[136:139], v[220:223]\n"
      "v_add_f32_dpp v200, v200, v200 quad_perm:[1,0,3,2] row_mask:0xf bank_mask:0xf\n"
      "v_cvt_pk_bf16_f32 v198, v224, v225\n"
      "v_mfma_f32_16x16x32_bf16 v[220:223], a[200:203], v[152:155], v[220:223]\n"
      "v_cvt_pk_bf16_f32 v199, v226, v227\n"
      "v_mfma_f32_16x16x32_bf16 v[224:227], v[162:165], a[244:247], v[192:195]\n"
      "ds_write_b128 v168, v[196:199] offset:6144\n"
      "v_mfma_f32_16x16x32_bf16 v[196:199], v[160:163], a[244:247], v[192:195]\n"
      "s_nop 0\n"
      "v_add_f32_dpp v200, v200, v200 quad_perm:[2,3,0,1] row_mask:0xf bank_mask:0xf\n"
      "v_mfma_f32_16x16x32_bf16 v[212:215], v[136:139], a[168:171], v[212:215]\n"
      "s_nop 0\n"
      "v_add_f32_dpp v200, v200, v200 row_half_mirror row_mask:0xf bank_mask:0xf\n"
      "v_mul_f32 v203, 0xbbaaaaab, v200\n"
      "v_mfma_f32_16x16x32_bf16 v[212:215], v[152:155], a[204:207], v[212:215]\n"
      "v_cvt_pk_bf16_f32 v224, v224, v225\n"
      "v_cvt_pk_bf16_f32 v225, v226, v227\n"
      "v_mfma_f32_16x16x32_bf16 v[228:231], v[136:139], a[172:175], v[228:231]\n"
      "v_cvt_pk_bf16_f32 v226, v196, v197\n"
      "v_cvt_pk_bf16_f32 v227, v198, v199\n"
      "v_mfma_f32_16x16x32_bf16 v[228:231], v[152:155], a[208:211], v[228:231]\n"
      "ds_write_b128 v168, v[224:227] offset:6272\n"
      "v_mfma_f32_16x16x32_bf16 v[196:199], v[162:165], a[248:251], v[192:195]\n"
      "v_cvt_pk_bf16_f32 v210, v212, v213\n"
      "v_mul_f32 v202, 0xbbaaaaab, v200\n"
      "v_mfma_f32_16x16x32_bf16 v[224:227], v[160:163], a[248:251], v[192:195]\n"
      "v_mul_f32 v201, 0xbbaaaaab, v200\n"
      "v_mul_f32 v200, 0xbbaaaaab, v200\n"
      "v_mfma_f32_16x16x32_bf16 v[232:235], v[136:139], a[176:179], v[232:235]\n"
      "s_waitcnt lgkmcnt(1)\n"
      "v_cvt_pk_bf16_f32 v211, v214, v215\n"
      "v_mfma_f32_16x16x32_bf16 v[232:235], v[152:155], a[212:215], v[232:235]\n"
      "s_nop 0\n"
      "v_cvt_pk_bf16_f32 v196, v196, v197\n"
      "v_cvt_pk_bf16_f32 v197, v198, v199\n"
      "v_mfma_f32_16x16x32_bf16 v[136:139], v[162:165], a[252:255], v[200:203]\n"
      "v_cvt_pk_bf16_f32 v199, v226, v227\n"
      "v_cvt_pk_bf16_f32 v198, v224, v225\n"
      "v_mfma_f32_16x16x32_bf16 v[152:155], v[160:163], a[252:255], v[200:203]\n"
      "ds_write_b128 v168, v[196:199] offset:6400\n"
      "v_mfma_f32_16x16x32_bf16 v[196:199], v[162:165], v[0:3], v[200:203]\n"
      "v_cvt_pk_bf16_f32 v204, v204, v205\n"
      "v_cvt_pk_bf16_f32 v205, v206, v207\n"
      "v_mfma_f32_16x16x32_bf16 v[192:195], v[160:163], v[0:3], v[200:203]\n"
      "v_cvt_pk_bf16_f32 v136, v136, v137\n"
      "v_cvt_pk_bf16_f32 v137, v138, v139\n"
      "v_mfma_f32_16x16x32_bf16 v[224:227], v[162:165], v[4:7], v[200:203]\n"
      "v_cvt_pk_bf16_f32 v138, v152, v153\n"
      "v_cvt_pk_bf16_f32 v139, v154, v155\n"
      "v_mfma_f32_16x16x32_bf16 v[152:155], v[160:163], v[4:7], v[200:203]\n"
      "ds_write_b128 v168, v[136:139] offset:9216\n"
      "v_mfma_f32_16x16x32_bf16 v[136:139], a[0:3], v[172:175], 0\n"
      "ds_read_b128 v[212:215], v114 offset:10240\n"
      "v_cvt_pk_bf16_f32 v196, v196, v197\n"
      "v_mfma_f32_16x16x32_bf16 v[136:139], a[36:39], v[184:187], v[136:139]\n"
      "ds_read_b128 v[200:203], v115 offset:10240\n"
      "v_cvt_pk_bf16_f32 v197, v198, v199\n"
      "v_cvt_pk_bf16_f32 v199, v194, v195\n"
      "v_cvt_pk_bf16_f32 v198, v192, v193\n"
      "v_mfma_f32_16x16x32_bf16 v[192:195], a[4:7], v[172:175], 0\n"
      "v_cvt_pk_bf16_f32 v224, v224, v225\n"
      "v_cvt_pk_bf16_f32 v225, v226, v227\n"
      "v_mfma_f32_16x16x32_bf16 v[192:195], a[40:43], v[184:187], v[192:195]\n"
      "ds_write_b128 v168, v[196:199] offset:9344\n"
      "v_mfma_f32_16x16x32_bf16 v[196:199], a[8:11], v[172:175], 0\n"
      "v_cvt_pk_bf16_f32 v226, v152, v153\n"
      "v_cvt_pk_bf16_f32 v227, v154, v155\n"
      "v_mfma_f32_16x16x32_bf16 v[196:199], a[44:47], v[184:187], v[196:199]\n"
      "ds_write_b128 v168, v[224:227] offset:9472\n"
      "v_mfma_f32_16x16x32_bf16 v[152:155], a[12:15], v[172:175], 0\n"
      "v_cvt_pk_bf16_f32 v206, v228, v229\n"
      "v_cvt_pk_bf16_f32 v207, v230, v231\n"
      "v_mfma_f32_16x16x32_bf16 v[152:155], a[48:51], v[184:187], v[152:155]\n"
      "s_nop 0\n"
      "v_cvt_pk_bf16_f32 v236, v236, v237\n"
      "v_cvt_pk_bf16_f32 v237, v238, v239\n"
      "v_mfma_f32_16x16x32_bf16 v[228:231], a[16:19], v[172:175], 0\n"
      "s_waitcnt lgkmcnt(2)\n"
      "v_cvt_pk_bf16_f32 v238, v232, v233\n"
      "v_cvt_pk_bf16_f32 v239, v234, v235\n"
      "v_mfma_f32_16x16x32_bf16 v[228:231], a[52:55], v[184:187], v[228:231]\n"
      "v_fma_f32 v156, v140, v140, s38\n"
      "v_fma_f32 v166, v188, v188, s38\n"
      "v_mfma_f32_16x16x32_bf16 v[232:235], a[20:23], v[172:175], 0\n"
      "v_fmac_f32 v156, v141, v141\n"
      "v_fmac_f32 v166, v189, v189\n"
      "v_mfma_f32_16x16x32_bf16 v[232:235], a[56:59], v[184:187], v[232:235]\n"
      "v_fmac_f32 v156, v142, v142\n"
      "v_fmac_f32 v166, v190, v190\n"
      "v_mfma_f32_16x16x32_bf16 v[136:139], a[72:75], v[212:215], v[136:139]\n"
      "v_fmac_f32 v156, v143, v143\n"
      "v_fmac_f32 v166, v191, v191\n"
      "v_mfma_f32_16x16x32_bf16 v[136:139], a[108:111], v[200:203], v[136:139]\n"
      "v_fmac_f32 v166, v240, v240\n"
      "v_fmac_f32 v156, v176, v176\n"
      "v_mfma_f32_16x16x32_bf16 v[192:195], a[76:79], v[212:215], v[192:195]\n"
      "v_fmac_f32 v166, v241, v241\n"
      "v_fmac_f32 v156, v177, v177\n"
      "v_mfma_f32_16x16x32_bf16 v[192:195], a[112:115], v[200:203], v[192:195]\n"
      "v_fmac_f32 v166, v242, v242\n"
      "v_fmac_f32 v156, v178, v178\n"
      "v_mfma_f32_16x16x32_bf16 v[196:199], a[80:83], v[212:215], v[196:199]\n"
      "v_fmac_f32 v166, v243, v243\n"
      "v_fmac_f32 v156, v179, v179\n"
      "v_mfma_f32_16x16x32_bf16 v[196:199], a[116:119], v[200:203], v[196:199]\n"
      "v_fmac_f32 v166, v220, v220\n"
      "v_fmac_f32 v156, v216, v216\n"
      "v_mfma_f32_16x16x32_bf16 v[152:155], a[84:87], v[212:215], v[152:155]\n"
      "v_fmac_f32 v166, v221, v221\n"
      "v_fmac_f32 v156, v217, v217\n"
      "v_mfma_f32_16x16x32_bf16 v[152:155], a[120:123], v[200:203], v[152:155]\n"
      "v_fmac_f32 v156, v218, v218\n"
      "v_fmac_f32 v166, v222, v222\n"
      "v_mfma_f32_16x16x32_bf16 v[228:231], a[88:91], v[212:215], v[228:231]\n"
      "v_fmac_f32 v166, v223, v223\n"
      "v_fmac_f32 v156, v219, v219\n"
      "v_mfma_f32_16x16x32_bf16 v[228:231], a[124:127], v[200:203], v[228:231]\n"
      "s_nop 0\n"
      "v_permlane16_swap_b32 v156, v166\n"
      "v_mfma_f32_16x16x32_bf16 v[232:235], a[92:95], v[212:215], v[232:235]\n"
      "v_add_f32 v156, v156, v166\n"
      "v_mov_b32 v166, v156\n"
      "v_mfma_f32_16x16x32_bf16 v[232:235], a[128:131], v[200:203], v[232:235]\n"
      "s_nop 0\n"
      "v_permlane32_swap_b32 v156, v166\n"
      "v_mfma_f32_16x16x32_bf16 v[224:227], v[172:175], a[24:27], 0\n"
      "v_add_f32 v156, v156, v166\n"
      "v_mfma_f32_16x16x32_bf16 v[224:227], v[184:187], a[60:63], v[224:227]\n"
      "v_rsq_f32 v156, v156\n"
      "v_mfma_f32_16x16x32_bf16 v[224:227], v[212:215], a[96:99], v[224:227]\n"
      "v_mov_b32 v166, v156\n"
      "v_mfma_f32_16x16x32_bf16 v[224:227], v[200:203], a[132:135], v[224:227]\n"
      "s_nop 0\n"
      "v_permlane16_swap_b32 v156, v166\n"
      "v_mfma_f32_16x16x32_bf16 v[244:247], v[172:175], a[28:31], 0\n"
      "v_mul_f32 v189, v166, v189\n"
      "v_mul_f32 v188, v166, v188\n"
      "v_mfma_f32_16x16x32_bf16 v[244:247], v[184:187], a[64:67], v[244:247]\n"
      "v_mul_f32 v190, v166, v190\n"
      "v_cvt_pk_bf16_f32 v188, v188, v189\n"
      "v_mfma_f32_16x16x32_bf16 v[244:247], v[212:215], a[100:103], v[244:247]\n"
      "v_mul_f32 v191, v166, v191\n"
      "v_cvt_pk_bf16_f32 v189, v190, v191\n"
      "v_mfma_f32_16x16x32_bf16 v[244:247], v[200:203], a[136:139], v[244:247]\n"
      "v_mul_f32 v241, v166, v241\n"
      "v_mul_f32 v240, v166, v240\n"
      "v_mfma_f32_16x16x32_bf16 v[248:251], v[172:175], a[32:35], 0\n"
      "v_cvt_pk_bf16_f32 v190, v240, v241\n"
      "v_mul_f32 v242, v166, v242\n"
      "v_mfma_f32_16x16x32_bf16 v[248:251], v[184:187], a[68:71], v[248:251]\n"
      "v_mul_f32_e64 v243, v166, v243\n"
      "v_mfma_f32_16x16x32_bf16 v[248:251], v[212:215], a[104:107], v[248:251]\n"
      "ds_read_b128 v[184:187], v114 offset:16384\n"
      "v_cvt_pk_bf16_f32 v191, v242, v243\n"
      "v_mfma_f32_16x16x32_bf16 v[248:251], v[200:203], a[140:143], v[248:251]\n"
      "ds_read_b128 v[200:203], v115 offset:16384\n"
      "v_mul_f32 v222, v166, v222\n"
      "v_mul_f32 v221, v166, v221\n"
      "v_mul_f32 v220, v166, v220\n"
      "s_waitcnt vmcnt(12)\n"
      "v_mfma_f32_16x16x32_bf16 v[172:175], v[144:147], v[8:11], 0\n"
      "v_cvt_pk_bf16_f32 v40, v220, v221\n"
      "v_mul_f32 v223, v166, v223\n"
      "s_waitcnt vmcnt(11)\n"
      "v_mfma_f32_16x16x32_bf16 v[172:175], v[144:147], v[12:15], v[172:175]\n"
      "v_mul_f32 v156, s36, v156\n"
      "v_cvt_pk_bf16_f32 v41, v222, v223\n"
      "s_waitcnt vmcnt(10)\n"
      "v_mfma_f32_16x16x32_bf16 v[172:175], v[144:147], v[16:19], v[172:175]\n"
      "v_mul_f32 v217, v156, v217\n"
      "v_mul_f32 v140, v156, v140\n"
      "v_mul_f32 v142, v156, v142\n"
      "v_mul_f32 v141, v156, v141\n"
      "v_mul_f32 v143, v156, v143\n"
      "v_mul_f32 v177, v156, v177\n"
      "s_waitcnt vmcnt(9)\n"
      "v_mfma_f32_16x16x32_bf16 v[220:223], v[144:147], v[20:23], 0\n"
      "v_mul_f32 v178, v156, v178\n"
      "v_mul_f32 v219, v156, v219\n"
      "s_waitcnt vmcnt(8)\n"
      "v_mfma_f32_16x16x32_bf16 v[220:223], v[144:147], v[24:27], v[220:223]\n"
      "v_cvt_pk_bf16_f32 v140, v140, v141\n"
      "v_cvt_pk_bf16_f32 v141, v142, v143\n"
      "s_waitcnt vmcnt(7)\n"
      "v_mfma_f32_16x16x32_bf16 v[220:223], v[144:147], v[28:31], v[220:223]\n"
      "v_mul_f32 v179, v156, v179\n"
      "s_waitcnt lgkmcnt(0)\n"
      "v_mfma_f32_16x16x32_bf16 v[136:139], a[144:147], v[184:187], v[136:139]\n"
      "v_mul_f32 v216, v156, v216\n"
      "v_mul_f32_e64 v218, v156, v218\n"
      "v_mfma_f32_16x16x32_bf16 v[136:139], a[180:183], v[200:203], v[136:139]\n"
      "v_mul_f32_e64 v176, v156, v176\n"
      "v_add_f32_dpp v172, v172, v172 quad_perm:[1,0,3,2] row_mask:0xf bank_mask:0xf\n"
      "v_mfma_f32_16x16x32_bf16 v[192:195], a[148:151], v[184:187], v[192:195]\n"
      "v_cvt_pk_bf16_f32 v143, v178, v179\n"
      "v_add_f32_dpp v220, v220, v220 quad_perm:[1,0,3,2] row_mask:0xf bank_mask:0xf\n"
      "v_mfma_f32_16x16x32_bf16 v[192:195], a[184:187], v[200:203], v[192:195]\n"
      "s_nop 0\n"
      "v_cvt_pk_bf16_f32 v39, v218, v219\n"
      "v_add_f32_dpp v172, v172, v172 quad_perm:[2,3,0,1] row_mask:0xf bank_mask:0xf\n"
      "v_mfma_f32_16x16x32_bf16 v[196:199], a[152:155], v[184:187], v[196:199]\n"
      "s_nop 0\n"
      "v_add_f32_dpp v172, v172, v172 row_half_mirror row_mask:0xf bank_mask:0xf\n"
      "v_mul_f32 v175, 0xbbaaaaab, v172\n"
      "v_mfma_f32_16x16x32_bf16 v[196:199], a[188:191], v[200:203], v[196:199]\n"
      "v_mul_f32 v173, 0xbbaaaaab, v172\n"
      "v_mul_f32 v174, 0xbbaaaaab, v172\n"
      "v_mfma_f32_16x16x32_bf16 v[152:155], a[156:159], v[184:187], v[152:155]\n"
      "v_cvt_pk_bf16_f32 v38, v216, v217\n"
      "v_mul_f32 v172, 0xbbaaaaab, v172\n"
      "v_mfma_f32_16x16x32_bf16 v[152:155], a[192:195], v[200:203], v[152:155]\n"
      "v_add_f32_dpp v220, v220, v220 quad_perm:[2,3,0,1] row_mask:0xf bank_mask:0xf\n"
      "v_mfma_f32_16x16x32_bf16 v[144:147], v[162:165], v[8:11], v[172:175]\n"
      "v_cvt_pk_bf16_f32 v142, v176, v177\n"
      "v_add_f32_dpp v220, v220, v220 row_half_mirror row_mask:0xf bank_mask:0xf\n"
      "v_mfma_f32_16x16x32_bf16 v[176:179], v[160:163], v[8:11], v[172:175]\n"
      "v_mul_f32 v221, 0xbbaaaaab, v220\n"
      "v_mul_f32 v222, 0xbbaaaaab, v220\n"
      "v_mfma_f32_16x16x32_bf16 v[216:219], v[162:165], v[12:15], v[172:175]\n"
      "v_mul_f32 v223, 0xbbaaaaab, v220\n"
      "v_mfma_f32_16x16x32_bf16 v[228:231], a[160:163], v[184:187], v[228:231]\n"
      "v_cvt_pk_bf16_f32 v144, v144, v145\n"
      "v_cvt_pk_bf16_f32 v145, v146, v147\n"
      "v_mfma_f32_16x16x32_bf16 v[228:231], a[196:199], v[200:203], v[228:231]\n"
      "v_mfma_f32_16x16x32_bf16 v[232:235], a[164:167], v[184:187], v[232:235]\n"
      "ds_read_u16 v52, v134\n"
      "v_cvt_pk_bf16_f32 v147, v178, v179\n"
      "v_mfma_f32_16x16x32_bf16 v[232:235], a[200:203], v[200:203], v[232:235]\n"
      "v_mul_f32 v220, 0xbbaaaaab, v220\n"
      "v_cvt_pk_bf16_f32 v146, v176, v177\n"
      "v_mfma_f32_16x16x32_bf16 v[176:179], v[162:165], v[16:19], v[172:175]\n"
      "ds_write_b128 v168, v[144:147] offset:12288\n"
      "v_mfma_f32_16x16x32_bf16 v[224:227], v[184:187], a[168:171], v[224:227]\n"
      "v_mfma_f32_16x16x32_bf16 v[224:227], v[200:203], a[204:207], v[224:227]\n"
      "v_mfma_f32_16x16x32_bf16 v[144:147], v[160:163], v[12:15], v[172:175]\n"
      "v_cvt_pk_bf16_f32 v216, v216, v217\n"
      "v_cvt_pk_bf16_f32 v217, v218, v219\n"
      "v_mfma_f32_16x16x32_bf16 v[244:247], v[184:187], a[172:175], v[244:247]\n"
      "ds_read_u16 v56, v134 offset:32\n"
      "v_mfma_f32_16x16x32_bf16 v[244:247], v[200:203], a[208:211], v[244:247]\n"
      "s_nop 0\n"
      "v_mfma_f32_16x16x32_bf16 v[248:251], v[184:187], a[176:179], v[248:251]\n"
      "s_nop 1\n"
      "v_cvt_pk_bf16_f32 v218, v144, v145\n"
      "v_mfma_f32_16x16x32_bf16 v[248:251], v[200:203], a[212:215], v[248:251]\n"
      "ds_read_u16 v60, v134 offset:64\n"
      "v_cvt_pk_bf16_f32 v219, v146, v147\n"
      "v_mfma_f32_16x16x32_bf16 v[144:147], v[160:163], v[16:19], v[172:175]\n"
      "ds_write_b128 v168, v[216:219] offset:12416\n"
      "v_mfma_f32_16x16x32_bf16 v[184:187], v[160:163], v[20:23], v[220:223]\n"
      "v_cvt_pk_bf16_f32 v176, v176, v177\n"
      "v_mfma_f32_16x16x32_bf16 v[200:203], v[162:165], v[20:23], v[220:223]\n"
      "v_cvt_pk_bf16_f32 v177, v178, v179\n"
      "v_mfma_f32_16x16x32_bf16 v[216:219], v[162:165], v[24:27], v[220:223]\n"
      "v_mfma_f32_16x16x32_bf16 v[212:215], v[162:165], v[28:31], v[220:223]\n"
      "s_nop 0\n"
      "v_cvt_pk_bf16_f32 v178, v144, v145\n"
      "v_cvt_pk_bf16_f32 v179, v146, v147\n"
      "v_mfma_f32_16x16x32_bf16 v[172:175], v[160:163], v[24:27], v[220:223]\n"
      "s_nop 0\n"
      "v_cvt_pk_bf16_f32 v200, v200, v201\n"
      "v_cvt_pk_bf16_f32 v201, v202, v203\n"
      "v_mfma_f32_16x16x32_bf16 v[164:167], v[160:163], v[28:31], v[220:223]\n"
      "v_cvt_pk_bf16_f32 v216, v216, v217\n"
      "v_cvt_pk_bf16_f32 v203, v186, v187\n"
      "v_cvt_pk_bf16_f32 v212, v212, v213\n"
      "v_cvt_pk_bf16_f32 v217, v218, v219\n"
      "v_cvt_pk_bf16_f32 v218, v172, v173\n"
      "v_cvt_pk_bf16_f32 v202, v184, v185\n"
      "v_cvt_pk_bf16_f32 v213, v214, v215\n"
      "v_cvt_pk_bf16_f32 v219, v174, v175\n"
      "v_cvt_pk_bf16_f32 v214, v164, v165\n"
      "v_cvt_pk_bf16_f32 v215, v166, v167\n"
      "s_waitcnt vmcnt(0)\n"
      "ds_read_b32 v156, v122 offset:4864\n"
      "ds_read_b128 v[164:167], v119 offset:41728\n"
      "ds_write_b128 v168, v[200:203] offset:15360\n"
      "ds_write_b128 v168, v[212:215] offset:15616\n"
      "ds_write_b128 v168, v[176:179] offset:12544\n"
      "ds_read_b128 v[144:147], v132 offset:32\n"
      "ds_read_b128 v[160:163], v133\n"
      "ds_read_b64 v[176:177], v132 offset:24\n"
      "ds_read_b64 v[178:179], v133 offset:16\n"
      "ds_write_b128 v168, v[216:219] offset:15488\n"
      "s_waitcnt lgkmcnt(8)\n"
      "v_bitop3_b32 v156, v156, v167, s40 bitop3:0xa8\n"
      "v_or3_b32 v164, v164, v165, v166\n"
      "v_and_b32_e64 v164, s40, v164\n"
      "v_or3_b32 v164, v164, v156, s4\n"
      "ds_write_b32 v129, v164 offset:3072\n"
      "s_waitcnt lgkmcnt(0)\n"
      "s_barrier\n"
      "v_mfma_f32_16x16x32_bf16 v[164:167], v[180:183], v[148:151], v[78:81]\n"
      "ds_read_b64_tr_b16 a[216:217], v158\n"
      "v_fma_f32 v156, v136, v136, s38\n"
      "v_mfma_f32_16x16x32_bf16 v[164:167], v[34:37], v[32:35], v[164:167]\n"
      "ds_read_b64_tr_b16 a[220:221], v158 offset:8\n"
      "v_fma_f32 v168, v152, v152, s38\n"
      "v_mfma_f32_16x16x32_bf16 v[172:175], v[188:191], v[148:151], v[82:85]\n"
      "ds_read_b64_tr_b16 a[224:225], v159\n"
      "v_fmac_f32_e64 v156, v137, v137\n"
      "v_mfma_f32_16x16x32_bf16 v[172:175], v[40:43], v[32:35], v[172:175]\n"
      "ds_read_b64_tr_b16 a[218:219], v158 offset:6144\n"
      "v_fmac_f32 v168, v153, v153\n"
      "v_mfma_f32_16x16x32_bf16 v[200:203], v[144:147], v[148:151], v[70:73]\n"
      "ds_read_b64_tr_b16 a[222:223], v158 offset:6152\n"
      "v_fmac_f32 v156, v138, v138\n"
      "v_mfma_f32_16x16x32_bf16 v[200:203], v[160:163], v[180:183], v[200:203]\n"
      "ds_read_b64_tr_b16 a[226:227], v159 offset:6144\n"
      "v_fmac_f32_e64 v168, v154, v154\n"
      "v_mfma_f32_16x16x32_bf16 v[200:203], v[176:179], v[32:35], v[200:203]\n"
      "ds_read_b64_tr_b16 a[228:229], v158 offset:18432\n"
      "v_fmac_f32 v156, v139, v139\n"
      "v_fmac_f32 v168, v155, v155\n"
      "v_fmac_f32 v156, v192, v192\n"
      "v_fmac_f32 v168, v228, v228\n"
      "v_fmac_f32 v156, v193, v193\n"
      "v_fmac_f32 v168, v229, v229\n"
      "v_fmac_f32 v156, v194, v194\n"
      "v_fmac_f32 v168, v230, v230\n"
      "v_fmac_f32 v156, v195, v195\n"
      "v_fmac_f32 v168, v231, v231\n"
      "v_fmac_f32 v156, v196, v196\n"
      "v_fmac_f32 v168, v232, v232\n"
      "v_fmac_f32 v156, v197, v197\n"
      "v_fmac_f32 v168, v233, v233\n"
      "v_fmac_f32 v156, v198, v198\n"
      "v_fmac_f32 v168, v234, v234\n"
      "v_fmac_f32 v156, v199, v199\n"
      "v_fmac_f32 v168, v235, v235\n"
      "s_nop 1\n"
      "v_permlane16_swap_b32 v156, v168\n"
      "v_add_f32 v156, v156, v168\n"
      "v_mov_b32 v168, v156\n"
      "s_nop 1\n"
      "v_permlane32_swap_b32 v156, v168\n"
      "v_add_f32 v156, v156, v168\n"
      "v_rsq_f32 v156, v156\n"
      "v_cvt_pk_bf16_f32 v50, v224, v225\n"
      "v_mov_b32_e64 v168, v156\n"
      "v_cvt_pk_bf16_f32 v51, v226, v227\n"
      "s_nop 0\n"
      "v_permlane16_swap_b32 v156, v168\n"
      "v_mul_f32 v152, v168, v152\n"
      "v_mul_f32 v153, v168, v153\n"
      "v_cvt_pk_bf16_f32 v152, v152, v153\n"
      "v_mul_f32 v154, v168, v154\n"
      "v_mul_f32 v155, v168, v155\n"
      "v_cvt_pk_bf16_f32 v153, v154, v155\n"
      "v_mul_f32 v228, v168, v228\n"
      "v_mul_f32 v229, v168, v229\n"
      "v_cvt_pk_bf16_f32 v154, v228, v229\n"
      "v_mul_f32 v230, v168, v230\n"
      "v_mul_f32 v231, v168, v231\n"
      "v_cvt_pk_bf16_f32 v155, v230, v231\n"
      "v_mul_f32 v232, v168, v232\n"
      "v_mul_f32 v233, v168, v233\n"
      "v_cvt_pk_bf16_f32 v46, v232, v233\n"
      "v_mul_f32 v234, v168, v234\n"
      "v_mul_f32 v235, v168, v235\n"
      "v_cvt_pk_bf16_f32 v47, v234, v235\n"
      "v_mul_f32 v156, s36, v156\n"
      "v_mul_f32 v136, v156, v136\n"
      "v_mfma_f32_16x16x32_bf16 v[184:187], v[152:155], v[148:151], v[86:89]\n"
      "ds_read_b64_tr_b16 a[232:233], v158 offset:18440\n"
      "v_mul_f32_e64 v137, v156, v137\n"
      "v_mfma_f32_16x16x32_bf16 v[184:187], v[46:49], v[32:35], v[184:187]\n"
      "ds_read_b64_tr_b16 a[236:237], v159 offset:18432\n"
      "ds_read_b64_tr_b16 a[230:231], v158 offset:24576\n"
      "ds_read_b64_tr_b16 a[234:235], v158 offset:24584\n"
      "ds_read_b64_tr_b16 a[238:239], v159 offset:24576\n"
      "ds_read_b64_tr_b16 a[240:241], v169\n"
      "ds_read_b64_tr_b16 a[244:245], v169 offset:8\n"
      "ds_read_b64_tr_b16 a[248:249], v157\n"
      "ds_read_b64_tr_b16 a[242:243], v169 offset:6144\n"
      "ds_read_b64_tr_b16 a[246:247], v169 offset:6152\n"
      "ds_read_b64_tr_b16 a[250:251], v157 offset:6144\n"
      "ds_read_b64_tr_b16 a[252:253], v169 offset:18432\n"
      "ds_read_b64_tr_b16 v[0:1], v169 offset:18440\n"
      "ds_read_b64_tr_b16 v[4:5], v157 offset:18432\n"
      "ds_read_b64_tr_b16 a[254:255], v169 offset:24576\n"
      "ds_read_b64_tr_b16 v[2:3], v169 offset:24584\n"
      "ds_read_b64_tr_b16 v[6:7], v157 offset:24576\n"
      "ds_read_b64_tr_b16 v[8:9], v158 offset:12288\n"
      "ds_read_b64_tr_b16 v[12:13], v158 offset:12296\n"
      "ds_read_b64_tr_b16 v[16:17], v159 offset:12288\n"
      "ds_read_b64_tr_b16 v[10:11], v158 offset:30720\n"
      "ds_read_b64_tr_b16 v[14:15], v158 offset:30728\n"
      "ds_read_b64_tr_b16 v[18:19], v159 offset:30720\n"
      "ds_read_b64_tr_b16 v[20:21], v169 offset:12288\n"
      "ds_read_b64_tr_b16 v[24:25], v169 offset:12296\n"
      "ds_read_b64_tr_b16 v[28:29], v157 offset:12288\n"
      "ds_read_b64_tr_b16 v[22:23], v169 offset:30720\n"
      "ds_read_b64_tr_b16 v[26:27], v169 offset:30728\n"
      "ds_read_b64_tr_b16 v[30:31], v157 offset:30720\n"
      "v_cvt_pk_bf16_f32 v136, v136, v137\n"
      "v_mul_f32 v138, v156, v138\n"
      "v_mul_f32 v139, v156, v139\n"
      "v_cvt_pk_bf16_f32 v137, v138, v139\n"
      "v_mul_f32 v192, v156, v192\n"
      "v_mul_f32 v193, v156, v193\n"
      "v_cvt_pk_bf16_f32 v138, v192, v193\n"
      "v_mul_f32 v194, v156, v194\n"
      "v_mul_f32 v195, v156, v195\n"
      "v_cvt_pk_bf16_f32 v139, v194, v195\n"
      "v_mul_f32 v196, v156, v196\n"
      "v_mul_f32 v197, v156, v197\n"
      "v_cvt_pk_bf16_f32 v44, v196, v197\n"
      "v_mul_f32 v198, v156, v198\n"
      "v_mul_f32 v199, v156, v199\n"
      "v_cvt_pk_bf16_f32 v45, v198, v199\n"
      "v_cvt_pk_bf16_f32 v54, v244, v245\n"
      "v_cvt_pk_bf16_f32 v55, v246, v247\n"
      "v_cvt_pk_bf16_f32 v58, v248, v249\n"
      "v_cvt_pk_bf16_f32 v59, v250, v251\n"
      "s_waitcnt lgkmcnt(0)\n"
      "s_barrier\n"
      "s_or_b64 s[34:35], s[48:49], s[50:51]\n"
      "s_cbranch_scc1 L_gen_8_%=\n"
      "L_fast_11_%=:\n"
      "v_mfma_f32_16x16x32_bf16 v[148:151], v[180:183], v[140:143], v[90:93]\n"
      "v_exp_f32 v173, v173\n"
      "v_mfma_f32_16x16x32_bf16 v[148:151], v[34:37], v[38:41], v[148:151]\n"
      "v_exp_f32 v164, v164\n"
      "v_exp_f32 v200, v200\n"
      "v_exp_f32 v172, v172\n"
      "v_mfma_f32_16x16x32_bf16 v[196:199], v[188:191], v[140:143], v[94:97]\n"
      "v_exp_f32 v165, v165\n"
      "v_mfma_f32_16x16x32_bf16 v[196:199], v[40:43], v[38:41], v[196:199]\n"
      "ds_read_b128 v[156:159], v128 offset:3072\n"
      "v_exp_f32 v166, v166\n"
      "v_mfma_f32_16x16x32_bf16 v[192:195], v[144:147], v[140:143], v[72:75]\n"
      "v_exp_f32 v185, v185\n"
      "v_mfma_f32_16x16x32_bf16 v[192:195], v[160:163], v[188:191], v[192:195]\n"
      "v_exp_f32 v167, v167\n"
      "v_mfma_f32_16x16x32_bf16 v[192:195], v[176:179], v[38:41], v[192:195]\n"
      "v_exp_f32_e64 v175, v175\n"
      "v_mfma_f32_16x16x32_bf16 v[168:171], v[152:155], v[140:143], v[98:101]\n"
      "v_cvt_pk_bf16_f32 v164, v164, v165\n"
      "v_cvt_pk_bf16_f32 v165, v166, v167\n"
      "v_mfma_f32_16x16x32_bf16 v[168:171], v[46:49], v[38:41], v[168:171]\n"
      "v_exp_f32 v198, v198\n"
      "v_mfma_f32_16x16x32_bf16 v[248:251], v[180:183], v[136:139], v[102:105]\n"
      "v_exp_f32 v174, v174\n"
      "v_mfma_f32_16x16x32_bf16 v[248:251], v[34:37], v[44:47], v[248:251]\n"
      "v_exp_f32 v197, v197\n"
      "v_mfma_f32_16x16x32_bf16 v[180:183], v[188:191], v[136:139], v[106:109]\n"
      "v_exp_f32 v184, v184\n"
      "v_mfma_f32_16x16x32_bf16 v[180:183], v[40:43], v[44:47], v[180:183]\n"
      "v_cvt_pk_bf16_f32 v167, v174, v175\n"
      "v_cvt_pk_bf16_f32 v166, v172, v173\n"
      "v_mfma_f32_16x16x32_bf16 v[172:175], v[144:147], v[136:139], v[74:77]\n"
      "v_exp_f32 v186, v186\n"
      "v_mfma_f32_16x16x32_bf16 v[172:175], v[160:163], v[152:155], v[172:175]\n"
      "v_exp_f32 v187, v187\n"
      "v_mfma_f32_16x16x32_bf16 v[172:175], v[176:179], v[44:47], v[172:175]\n"
      "s_waitcnt lgkmcnt(0)\n"
      "v_exp_f32 v149, v149\n"
      "v_mfma_f32_16x16x32_bf16 v[188:191], v[152:155], v[136:139], v[110:113]\n"
      "v_cvt_pk_bf16_f32 v63, v186, v187\n"
      "v_cvt_pk_bf16_f32 v62, v184, v185\n"
      "v_mfma_f32_16x16x32_bf16 v[188:191], v[46:49], v[44:47], v[188:191]\n"
      "v_cvt_pk_bf16_f32 v64, v200, 0\n"
      "v_bitop3_b32 v156, v156, v157, s4 bitop3:0x7e\n"
      "v_exp_f32 v170, v170\n"
      "v_bitop3_b32 v158, v158, v159, s4 bitop3:0x7e\n"
      "v_mfma_f32_16x16x32_bf16 v[152:155], v[160:163], v[144:147], v[76:79]\n"
      "v_exp_f32 v151, v151\n"
      "v_or_b32 v156, v156, v158\n"
      "v_exp_f32 v199, v199\n"
      "v_mfma_f32_16x16x32_bf16 v[144:147], v[66:69], v[164:167], 0\n"
      "v_cndmask_b32 v201, v201, v193, s[42:43]\n"
      "v_cmp_ne_u32 vcc, 0, v156\n"
      "v_mfma_f32_16x16x32_bf16 v[144:147], v[66:69], v[62:65], v[144:147]\n"
      "s_mov_b64 s[48:49], vcc\n"
      "v_exp_f32 v171, v171\n"
      "v_exp_f32 v192, v192\n"
      "v_mfma_f32_16x16x32_bf16 v[160:163], v[208:211], v[164:167], 0\n"
      "s_cbranch_vccnz L_flg_12_%=\n"
      "L_fb_13_%=:\n"
      "v_mfma_f32_16x16x32_bf16 v[160:163], v[50:53], v[62:65], v[160:163]\n"
      "s_add_u32 s28, s27, 2\n"
      "v_exp_f32 v169, v169\n"
      "v_exp_f32 v168, v168\n"
      "v_exp_f32 v148, v148\n"
      "v_mfma_f32_16x16x32_bf16 v[156:159], v[204:207], v[164:167], 0\n"
      "s_min_u32 m0, s28, s3\n"
      "v_exp_f32_e64 v150, v150\n"
      "v_mfma_f32_16x16x32_bf16 v[156:159], v[54:57], v[62:65], v[156:159]\n"
      "s_movrels_b32 s9, s80\n"
      "v_rcp_f32 v144, v144\n"
      "v_mfma_f32_16x16x32_bf16 v[184:187], v[236:239], v[164:167], 0\n"
      "s_lshr_b32 s8, s9, 8\n"
      "v_exp_f32 v196, v196\n"
      "v_mfma_f32_16x16x32_bf16 v[184:187], v[58:61], v[62:65], v[184:187]\n"
      "s_and_b32 s9, s9, 0xff\n"
      "v_cvt_pk_bf16_f32 v64, v192, 0\n"
      "v_cvt_pk_bf16_f32 v148, v148, v149\n"
      "v_cvt_pk_bf16_f32 v149, v150, v151\n"
      "v_cvt_pk_bf16_f32 v62, v168, v169\n"
      "v_cvt_pk_bf16_f32 v150, v196, v197\n"
      "v_cvt_pk_bf16_f32 v151, v198, v199\n"
      "v_cndmask_b32 v201, v201, v173, s[44:45]\n"
      "v_mfma_f32_16x16x16_bf16 v[152:155], v[178:179], v[176:177], v[152:155]\n"
      "s_mul_i32 s10, s8, 73\n"
      "v_cvt_pk_bf16_f32 v63, v170, v171\n"
      "v_exp_f32 v189, v189\n"
      "v_mfma_f32_16x16x32_bf16 v[168:171], v[66:69], v[148:151], 0\n"
      "s_add_u32 s10, s10, s9\n"
      "v_mul_f32 v161, v144, v161\n"
      "v_mul_f32 v160, v144, v160\n"
      "v_mfma_f32_16x16x32_bf16 v[168:171], v[66:69], v[62:65], v[168:171]\n"
      "ds_read_b128 v[136:139], v114 offset:18432\n"
      "v_cvt_pk_bf16_f32 v160, v160, v161\n"
      "v_mul_f32 v186, v144, v186\n"
      "v_mul_f32 v163, v144, v163\n"
      "v_mfma_f32_16x16x32_bf16 v[196:199], v[208:211], v[148:151], 0\n"
      "ds_read_b128 v[140:143], v115 offset:18432\n"
      "v_mul_f32_e64 v162, v144, v162\n"
      "v_mfma_f32_16x16x32_bf16 v[196:199], v[50:53], v[62:65], v[196:199]\n"
      "s_mul_i32 s28, s10, 4802\n"
      "v_cndmask_b32 v201, v201, v153, s[46:47]\n"
      "v_exp_f32 v172, v172\n"
      "v_mfma_f32_16x16x32_bf16 v[176:179], v[204:207], v[148:151], 0\n"
      "s_add_u32 s56, s18, s28\n"
      "v_mul_f32 v156, v144, v156\n"
      "v_mul_f32 v157, v144, v157\n"
      "v_mfma_f32_16x16x32_bf16 v[176:179], v[54:57], v[62:65], v[176:179]\n"
      "s_addc_u32 s57, s19, 0\n"
      "v_mul_f32 v158, v144, v158\n"
      "v_exp_f32 v190, v190\n"
      "v_exp_f32 v251, v251\n"
      "v_cvt_pk_bf16_f32 v161, v162, v163\n"
      "v_exp_f32 v181, v181\n"
      "v_mul_f32 v184, v144, v184\n"
      "v_cvt_pk_bf16_f32 v162, v156, v157\n"
      "v_mul_f32 v185, v144, v185\n"
      "v_mfma_f32_16x16x32_bf16 v[192:195], v[236:239], v[148:151], 0\n"
      "s_and_b32 s56, s56, -16\n"
      "v_mul_f32 v159, v144, v159\n"
      "v_cvt_pk_bf16_f32 v163, v158, v159\n"
      "v_mfma_f32_16x16x32_bf16 v[192:195], v[58:61], v[62:65], v[192:195]\n"
      "ds_write_b128 v124, v[160:163]\n"
      "v_exp_f32 v183, v183\n"
      "v_exp_f32 v188, v188\n"
      "v_mul_f32 v187, v144, v187\n"
      "s_waitcnt lgkmcnt(1)\n"
      "v_cvt_pk_bf16_f32 v184, v184, v185\n"
      "v_exp_f32 v191, v191\n"
      "v_exp_f32 v249, v249\n"
      "v_cvt_pk_bf16_f32 v185, v186, v187\n"
      "v_exp_f32 v180, v180\n"
      "v_exp_f32 v248, v248\n"
      "v_cvt_pk_bf16_f32 v62, v188, v189\n"
      "v_exp_f32 v250, v250\n"
      "v_mfma_f32_16x16x32_bf16 v[144:147], a[0:3], v[136:139], 0\n"
      "s_sub_u32 s58, s56, 110592\n"
      "v_exp_f32 v201, v201\n"
      "v_mfma_f32_16x16x32_bf16 v[144:147], a[36:39], v[140:143], v[144:147]\n"
      "ds_write_b64 v125, v[184:185]\n"
      "v_exp_f32 v182, v182\n"
      "v_mfma_f32_16x16x32_bf16 v[148:151], a[4:7], v[136:139], 0\n"
      "s_subb_u32 s59, s57, 0\n"
      "v_cvt_pk_bf16_f32 v201, v201, v201\n"
      "v_mov_b32 v169, 0\n"
      "v_mfma_f32_16x16x32_bf16 v[148:151], a[40:43], v[140:143], v[148:151]\n"
      "ds_write_b16 v130, v201\n"
      "v_cvt_pk_bf16_f32 v248, v248, v249\n"
      "v_cvt_pk_bf16_f32 v249, v250, v251\n"
      "v_cvt_pk_bf16_f32 v64, v172, 0\n"
      "v_cvt_pk_bf16_f32 v251, v182, v183\n"
      "v_cvt_pk_bf16_f32 v250, v180, v181\n"
      "v_rcp_f32_e64 v168, v168\n"
      "v_cvt_pk_bf16_f32 v63, v190, v191\n"
      "v_mfma_f32_16x16x32_bf16 v[152:155], v[66:69], v[248:251], 0\n"
      "ds_read_b128 v[188:191], v131\n"
      "v_mul_f32_e64 v196, v168, v196\n"
      "v_mfma_f32_16x16x32_bf16 v[152:155], v[66:69], v[62:65], v[152:155]\n"
      "ds_read_b128 v[180:183], v131 offset:64\n"
      "v_mul_f32 v178, v168, v178\n"
      "v_mul_f32 v199, v168, v199\n"
      "v_mul_f32_e64 v197, v168, v197\n"
      "v_mfma_f32_16x16x32_bf16 v[156:159], v[208:211], v[248:251], 0\n"
      "s_sub_u32 s56, s56, 73728\n"
      "v_mul_f32 v198, v168, v198\n"
      "v_cvt_pk_bf16_f32 v196, v196, v197\n"
      "v_mfma_f32_16x16x32_bf16 v[156:159], v[50:53], v[62:65], v[156:159]\n"
      "s_subb_u32 s57, s57, 0\n"
      "v_mul_f32 v193, v168, v193\n"
      "v_cvt_pk_bf16_f32 v197, v198, v199\n"
      "v_mfma_f32_16x16x32_bf16 v[160:163], v[204:207], v[248:251], 0\n"
      "s_mul_i32 s28, s8, 1373568\n"
      "v_mul_f32 v176, v168, v176\n"
      "v_mul_f32 v177, v168, v177\n"
      "v_mfma_f32_16x16x32_bf16 v[160:163], v[54:57], v[62:65], v[160:163]\n"
      "s_mul_i32 s29, s9, 2688\n"
      "v_mul_f32 v192, v168, v192\n"
      "v_cvt_pk_bf16_f32 v198, v176, v177\n"
      "v_mfma_f32_16x16x32_bf16 v[164:167], v[236:239], v[248:251], 0\n"
      "s_add_u32 s28, s28, s29\n"
      "v_mul_f32_e64 v179, v168, v179\n"
      "v_cvt_pk_bf16_f32 v199, v178, v179\n"
      "v_mfma_f32_16x16x32_bf16 v[164:167], v[58:61], v[62:65], v[164:167]\n"
      "ds_write_b128 v124, v[196:199] offset:256\n"
      "s_waitcnt lgkmcnt(1)\n"
      "v_mfma_f32_16x16x32_bf16 v[196:199], v[188:191], v[66:69], 0\n"
      "s_cmp_eq_u32 s8, 72\n"
      "v_cvt_pk_bf16_f32 v192, v192, v193\n"
      "v_mul_f32_e64 v195, v168, v195\n"
      "v_mfma_f32_16x16x32_bf16 v[196:199], v[180:183], v[66:69], v[196:199]\n"
      "s_cselect_b32 s8, 0, 100270464\n"
      "v_mul_f32 v194, v168, v194\n"
      "s_cmp_eq_u32 s9, 72\n"
      "v_mfma_f32_16x16x32_bf16 v[176:179], v[188:191], v[208:211], 0\n"
      "s_cselect_b32 s9, 0, 196224\n"
      "s_add_u32 s10, s8, s9\n"
      "v_cvt_pk_bf16_f32 v193, v194, v195\n"
      "v_mfma_f32_16x16x32_bf16 v[176:179], v[180:183], v[50:53], v[176:179]\n"
      "ds_write_b64 v125, v[192:193] offset:256\n"
      "v_mfma_f32_16x16x32_bf16 v[192:195], v[188:191], v[204:207], 0\n"
      "s_add_u32 s28, s28, s10\n"
      "s_add_u32 s52, s16, s28\n"
      "v_mov_b32_dpp v196, v196 quad_perm:[0,0,0,0] row_mask:0xf bank_mask:0xf\n"
      "v_mfma_f32_16x16x32_bf16 v[192:195], v[180:183], v[54:57], v[192:195]\n"
      "ds_write_b32 v129, v169\n"
      "v_mfma_f32_16x16x32_bf16 v[204:207], v[188:191], v[236:239], 0\n"
      "s_addc_u32 s53, s17, 0\n"
      "v_rcp_f32 v196, v196\n"
      "v_mfma_f32_16x16x32_bf16 v[204:207], v[180:183], v[58:61], v[204:207]\n"
      "s_cmp_lg_u32 s10, s11\n"
      "s_nop 1\n"
      "v_cndmask_b32 v176, v176, v192, s[42:43]\n"
      "s_nop 3\n"
      "v_cndmask_b32 v176, v176, v204, s[44:45]\n"
      "v_mul_f32 v176, v196, v176\n"
      "v_cvt_pk_bf16_f32 v176, v176, v176\n"
      "s_cbranch_scc1 L_edge_15_%=\n"
      "L_eb_16_%=:\n"
      "ds_write_b16 v135, v176\n"
      "L_join_10_%=:\n"
      "s_mov_b32 s27, 1\n"
      ".p2align 6\n"
      "L_loop_%=:\n"
      "v_mfma_f32_16x16x32_bf16 v[204:207], a[8:11], v[136:139], 0\n"
      "s_add_u32 m0, s76, 0\n"
      "ds_read_b128 v[236:239], v114 offset:24576\n"
      "v_mfma_f32_16x16x32_bf16 v[204:207], a[44:47], v[140:143], v[204:207]\n"
      "ds_read_b128 v[208:211], v115 offset:24576\n"
      "v_rcp_f32 v152, v152\n"
      "v_mfma_f32_16x16x32_bf16 v[248:251], a[12:15], v[136:139], 0\n"
      "v_mul_f32 v157, v152, v157\n"
      "v_mul_f32 v156, v152, v156\n"
      "v_mfma_f32_16x16x32_bf16 v[248:251], a[48:51], v[140:143], v[248:251]\n"
      "ds_read_b128 v[244:247], v114 offset:30720\n"
      "v_cvt_pk_bf16_f32 v156, v156, v157\n"
      "v_mul_f32 v162, v152, v162\n"
      "v_mfma_f32_16x16x32_bf16 v[232:235], a[16:19], v[136:139], 0\n"
      "v_mul_f32 v159, v152, v159\n"
      "v_mul_f32_e64 v158, v152, v158\n"
      "v_mfma_f32_16x16x32_bf16 v[232:235], a[52:55], v[140:143], v[232:235]\n"
      "s_waitcnt lgkmcnt(3)\n"
      "s_barrier\n"
      "v_mfma_f32_16x16x32_bf16 v[212:215], a[20:23], v[136:139], 0\n"
      "ds_read_b128 v[216:219], v128\n"
      "v_cvt_pk_bf16_f32 v157, v158, v159\n"
      "v_mfma_f32_16x16x32_bf16 v[212:215], a[56:59], v[140:143], v[212:215]\n"
      "global_load_lds_dwordx4 v116, s[52:53]\n"
      "v_mfma_f32_16x16x32_bf16 v[220:223], v[136:139], a[24:27], 0\n"
      "s_waitcnt lgkmcnt(1)\n"
      "v_mul_f32 v160, v152, v160\n"
      "v_mul_f32 v161, v152, v161\n"
      "v_mfma_f32_16x16x32_bf16 v[220:223], v[140:143], a[60:63], v[220:223]\n"
      "v_mul_f32 v163, v152, v163\n"
      "v_cvt_pk_bf16_f32 v158, v160, v161\n"
      "v_mfma_f32_16x16x32_bf16 v[224:227], v[136:139], a[28:31], 0\n"
      "v_cvt_pk_bf16_f32 v159, v162, v163\n"
      "v_mul_f32_e64 v164, v152, v164\n"
      "v_mfma_f32_16x16x32_bf16 v[224:227], v[140:143], a[64:67], v[224:227]\n"
      "ds_write_b128 v124, v[156:159] offset:512\n"
      "v_mfma_f32_16x16x32_bf16 v[160:163], v[136:139], a[32:35], 0\n"
      "ds_read_b128 v[136:139], v115 offset:30720\n"
      "v_mul_f32 v165, v152, v165\n"
      "v_mfma_f32_16x16x32_bf16 v[160:163], v[140:143], a[68:71], v[160:163]\n"
      "s_waitcnt lgkmcnt(2)\n"
      "v_cvt_pk_bf16_f32 v164, v164, v165\n"
      "v_mfma_f32_16x16x32_bf16 v[144:147], a[72:75], v[236:239], v[144:147]\n"
      "s_add_u32 m0, s76, 6016\n"
      "v_mul_f32 v166, v152, v166\n"
      "v_mul_f32 v167, v152, v167\n"
      "v_mfma_f32_16x16x32_bf16 v[144:147], a[108:111], v[208:211], v[144:147]\n"
      "global_load_lds_dwordx4 v116, s[52:53] offset:128\n"
      "v_mfma_f32_16x16x32_bf16 v[148:151], a[76:79], v[236:239], v[148:151]\n"
      "v_cvt_pk_bf16_f32 v165, v166, v167\n"
      "v_add_f32 v216, |v216|, |v217|\n"
      "v_mfma_f32_16x16x32_bf16 v[148:151], a[112:115], v[208:211], v[148:151]\n"
      "ds_write_b64 v125, v[164:165] offset:512\n"
      "v_mfma_f32_16x16x32_bf16 v[204:207], a[80:83], v[236:239], v[204:207]\n"
      "v_add_f32 v218, |v218|, |v219|\n"
      "v_add_f32_e64 v216, v216, v218\n"
      "v_mfma_f32_16x16x32_bf16 v[204:207], a[116:119], v[208:211], v[204:207]\n"
      "s_add_u32 m0, s76, 12032\n"
      "v_rsq_f32 v216, v216\n"
      "v_mfma_f32_16x16x32_bf16 v[248:251], a[84:87], v[236:239], v[248:251]\n"
      "v_mul_f32 v216, s41, v216\n"
      "v_mov_b32 v152, v216\n"
      "v_mfma_f32_16x16x32_bf16 v[248:251], a[120:123], v[208:211], v[248:251]\n"
      "s_nop 0\n"
      "v_permlane32_swap_b32_e64 v216, v152\n"
      "v_mfma_f32_16x16x32_bf16 v[232:235], a[88:91], v[236:239], v[232:235]\n"
      "ds_read_b128 v[140:143], v114 offset:20480\n"
      "v_mov_b32_e64 v153, v216\n"
      "v_mfma_f32_16x16x32_bf16 v[232:235], a[124:127], v[208:211], v[232:235]\n"
      "global_load_lds_dwordx4 v116, s[52:53] offset:256\n"
      "v_mfma_f32_16x16x32_bf16 v[212:215], a[92:95], v[236:239], v[212:215]\n"
      "s_waitcnt lgkmcnt(2)\n"
      "v_permlane16_swap_b32 v216, v153\n"
      "v_mfma_f32_16x16x32_bf16 v[212:215], a[128:131], v[208:211], v[212:215]\n"
      "ds_read_b128 v[156:159], v115 offset:20480\n"
      "v_mul_f32 v168, v216, v168\n"
      "v_mfma_f32_16x16x32_bf16 v[220:223], v[236:239], a[96:99], v[220:223]\n"
      "v_mul_f32 v169, v216, v169\n"
      "v_cvt_pk_bf16_f32 v168, v168, v169\n"
      "v_mfma_f32_16x16x32_bf16 v[220:223], v[208:211], a[132:135], v[220:223]\n"
      "v_mul_f32 v171, v216, v171\n"
      "v_mul_f32 v170, v216, v170\n"
      "v_mfma_f32_16x16x32_bf16 v[224:227], v[236:239], a[100:103], v[224:227]\n"
      "v_mul_f32_e64 v172, v216, v172\n"
      "v_cvt_pk_bf16_f32 v169, v170, v171\n"
      "v_mfma_f32_16x16x32_bf16 v[224:227], v[208:211], a[136:139], v[224:227]\n"
      "s_add_u32 m0, s77, 0\n"
      "v_mul_f32 v173, v216, v173\n"
      "v_cvt_pk_bf16_f32 v170, v172, v173\n"
      "v_mfma_f32_16x16x32_bf16 v[160:163], v[236:239], a[104:107], v[160:163]\n"
      "v_mul_f32 v174, v216, v174\n"
      "v_mul_f32 v175, v216, v175\n"
      "v_mfma_f32_16x16x32_bf16 v[160:163], v[208:211], a[140:143], v[160:163]\n"
      "v_cvt_pk_bf16_f32 v171, v174, v175\n"
      "v_mul_f32_e64 v176, v216, v176\n"
      "v_mfma_f32_16x16x32_bf16 v[144:147], a[144:147], v[244:247], v[144:147]\n"
      "global_load_lds_dwordx4 v117, s[52:53]\n"
      "v_mfma_f32_16x16x32_bf16 v[144:147], a[180:183], v[136:139], v[144:147]\n"
      "v_mul_f32_e64 v177, v216, v177\n"
      "v_cvt_pk_bf16_f32 v176, v176, v177\n"
      "v_mfma_f32_16x16x32_bf16 v[148:151], a[148:151], v[244:247], v[148:151]\n"
      "v_mul_f32 v178, v216, v178\n"
      "v_mul_f32 v179, v216, v179\n"
      "v_mfma_f32_16x16x32_bf16 v[148:151], a[184:187], v[136:139], v[148:151]\n"
      "ds_write_b128 v126, v[168:171] offset:18432\n"
      "v_mfma_f32_16x16x32_bf16 v[204:207], a[152:155], v[244:247], v[204:207]\n"
      "s_waitcnt lgkmcnt(1)\n"
      "v_cvt_pk_bf16_f32 v177, v178, v179\n"
      "v_mul_f32 v180, v153, v180\n"
      "v_mfma_f32_16x16x32_bf16 v[204:207], a[188:191], v[136:139], v[204:207]\n"
      "v_mul_f32 v181, v153, v181\n"
      "v_cvt_pk_bf16_f32 v180, v180, v181\n"
      "v_mfma_f32_16x16x32_bf16 v[248:251], a[156:159], v[244:247], v[248:251]\n"
      "v_mul_f32 v183, v153, v183\n"
      "v_mul_f32 v182, v153, v182\n"
      "v_mfma_f32_16x16x32_bf16 v[248:251], a[192:195], v[136:139], v[248:251]\n"
      "s_add_u32 m0, s78, 0\n"
      "v_cvt_pk_bf16_f32 v181, v182, v183\n"
      "v_mul_f32_e64 v184, v153, v184\n"
      "v_mfma_f32_16x16x32_bf16 v[232:235], a[160:163], v[244:247], v[232:235]\n"
      "ds_read_b128 v[168:171], v114 offset:26624\n"
      "v_mul_f32_e64 v185, v153, v185\n"
      "v_mfma_f32_16x16x32_bf16 v[232:235], a[196:199], v[136:139], v[232:235]\n"
      "ds_read_b128 v[172:175], v115 offset:26624\n"
      "v_cvt_pk_bf16_f32 v182, v184, v185\n"
      "v_mfma_f32_16x16x32_bf16 v[212:215], a[164:167], v[244:247], v[212:215]\n"
      "v_mul_f32 v186, v153, v186\n"
      "v_mul_f32 v187, v153, v187\n"
      "v_mfma_f32_16x16x32_bf16 v[212:215], a[200:203], v[136:139], v[212:215]\n"
      "s_mov_b32 exec_hi, 0\n"
      "global_load_lds_dwordx4 v118, s[52:53]\n"
      "s_mov_b32 exec_hi, -1\n"
      "v_mfma_f32_16x16x32_bf16 v[220:223], v[244:247], a[168:171], v[220:223]\n"
      "v_cvt_pk_bf16_f32 v183, v186, v187\n"
      "v_mul_f32_e64 v188, v153, v188\n"
      "v_mfma_f32_16x16x32_bf16 v[220:223], v[136:139], a[204:207], v[220:223]\n"
      "ds_write_b128 v126, v[180:183] offset:20480\n"
      "v_mfma_f32_16x16x32_bf16 v[224:227], v[244:247], a[172:175], v[224:227]\n"
      "v_mul_f32_e64 v189, v153, v189\n"
      "v_cvt_pk_bf16_f32 v188, v188, v189\n"
      "v_mfma_f32_16x16x32_bf16 v[224:227], v[136:139], a[208:211], v[224:227]\n"
      "s_add_u32 m0, s76, 110592\n"
      "v_mul_f32 v191, v153, v191\n"
      "v_mul_f32 v190, v153, v190\n"
      "v_mfma_f32_16x16x32_bf16 v[160:163], v[244:247], a[176:179], v[160:163]\n"
      "s_waitcnt lgkmcnt(1)\n"
      "v_mul_f32 v192, v152, v192\n"
      "v_cvt_pk_bf16_f32 v189, v190, v191\n"
      "v_mfma_f32_16x16x32_bf16 v[160:163], v[136:139], a[212:215], v[160:163]\n"
      "ds_write2st64_b64 v127, v[176:177], v[188:189] offset0:36 offset1:40\n"
      "v_mfma_f32_16x16x32_bf16 v[180:183], a[0:3], v[140:143], 0\n"
      "global_load_lds_dwordx4 v119, s[56:57]\n"
      "v_mfma_f32_16x16x32_bf16 v[180:183], a[36:39], v[156:159], v[180:183]\n"
      "s_add_u32 m0, s79, 0\n"
      "v_mul_f32 v193, v152, v193\n"
      "v_cvt_pk_bf16_f32 v192, v192, v193\n"
      "v_mfma_f32_16x16x32_bf16 v[176:179], a[4:7], v[140:143], 0\n"
      "s_sub_u32 s28, s27, 3\n"
      "v_mul_f32 v195, v152, v195\n"
      "v_mul_f32 v194, v152, v194\n"
      "v_mfma_f32_16x16x32_bf16 v[176:179], a[40:43], v[156:159], v[176:179]\n"
      "v_mul_f32 v196, v152, v196\n"
      "v_cvt_pk_bf16_f32 v193, v194, v195\n"
      "v_mfma_f32_16x16x32_bf16 v[184:187], a[8:11], v[140:143], 0\n"
      "v_mul_f32_e64 v197, v152, v197\n"
      "v_cvt_pk_bf16_f32 v194, v196, v197\n"
      "v_mfma_f32_16x16x32_bf16 v[184:187], a[44:47], v[156:159], v[184:187]\n"
      "s_cselect_b64 s[54:55], s[12:13], s[22:23]\n"
      "v_mul_f32 v198, v152, v198\n"
      "v_mul_f32_e64 v199, v152, v199\n"
      "v_mfma_f32_16x16x32_bf16 v[136:139], a[12:15], v[140:143], 0\n"
      "ds_read_b128 v[188:191], v114 offset:32768\n"
      "v_cvt_pk_bf16_f32 v195, v198, v199\n"
      "v_mfma_f32_16x16x32_bf16 v[136:139], a[48:51], v[156:159], v[136:139]\n"
      "ds_write_b128 v126, v[192:195] offset:22528\n"
      "v_mfma_f32_16x16x32_bf16 v[192:195], a[16:19], v[140:143], 0\n"
      "global_load_lds_dword v122, s[58:59]\n"
      "v_mfma_f32_16x16x32_bf16 v[192:195], a[52:55], v[156:159], v[192:195]\n"
      "ds_read_b128 v[196:199], v115 offset:32768\n"
      "v_mul_f32 v200, v152, v200\n"
      "v_mfma_f32_16x16x32_bf16 v[164:167], a[20:23], v[140:143], 0\n"
      "s_max_i32 m0, s28, 0\n"
      "v_mul_f32_e64 v201, v152, v201\n"
      "v_cvt_pk_bf16_f32 v200, v200, v201\n"
      "v_mfma_f32_16x16x32_bf16 v[164:167], a[56:59], v[156:159], v[164:167]\n"
      "s_movrels_b32 s9, s80\n"
      "v_mul_f32 v202, v152, v202\n"
      "v_mul_f32 v203, v152, v203\n"
      "v_mfma_f32_16x16x32_bf16 v[152:155], v[140:143], a[24:27], 0\n"
      "s_lshr_b32 s8, s9, 8\n"
      "v_fma_f32 v216, v144, v144, s38\n"
      "v_cvt_pk_bf16_f32 v201, v202, v203\n"
      "v_mfma_f32_16x16x32_bf16 v[152:155], v[156:159], a[60:63], v[152:155]\n"
      "ds_write_b64 v127, v[200:201] offset:22528\n"
      "v_mfma_f32_16x16x32_bf16 v[200:203], v[140:143], a[28:31], 0\n"
      "s_and_b32 s9, s9, 0xff\n"
      "v_fmac_f32_e64 v216, v145, v145\n"
      "v_fma_f32 v218, v248, v248, s38\n"
      "v_mfma_f32_16x16x32_bf16 v[200:203], v[156:159], a[64:67], v[200:203]\n"
      "s_mul_i32 s28, s8, 1373568\n"
      "v_fmac_f32 v216, v146, v146\n"
      "v_fmac_f32 v218, v249, v249\n"
      "v_mfma_f32_16x16x32_bf16 v[244:247], v[140:143], a[32:35], 0\n"
      "s_mul_i32 s29, s9, 2688\n"
      "v_fmac_f32 v218, v250, v250\n"
      "v_fmac_f32 v216, v147, v147\n"
      "v_mfma_f32_16x16x32_bf16 v[244:247], v[156:159], a[68:71], v[244:247]\n"
      "s_waitcnt lgkmcnt(1)\n"
      "v_fmac_f32 v216, v148, v148\n"
      "v_fmac_f32 v218, v251, v251\n"
      "v_mfma_f32_16x16x32_bf16 v[180:183], a[72:75], v[168:171], v[180:183]\n"
      "s_add_u32 s28, s28, s29\n"
      "v_fmac_f32 v218, v232, v232\n"
      "v_fmac_f32 v216, v149, v149\n"
      "v_mfma_f32_16x16x32_bf16 v[180:183], a[108:111], v[172:175], v[180:183]\n"
      "s_cmp_eq_u32 s8, 72\n"
      "v_fmac_f32 v216, v150, v150\n"
      "v_fmac_f32 v218, v233, v233\n"
      "v_mfma_f32_16x16x32_bf16 v[176:179], a[76:79], v[168:171], v[176:179]\n"
      "s_cselect_b32 s8, 0, 100270464\n"
      "v_fmac_f32 v216, v151, v151\n"
      "v_fmac_f32 v218, v234, v234\n"
      "v_mfma_f32_16x16x32_bf16 v[176:179], a[112:115], v[172:175], v[176:179]\n"
      "s_cmp_eq_u32 s9, 72\n"
      "v_fmac_f32 v216, v204, v204\n"
      "v_fmac_f32 v218, v235, v235\n"
      "v_mfma_f32_16x16x32_bf16 v[184:187], a[80:83], v[168:171], v[184:187]\n"
      "ds_read_b128 v[140:143], v114 offset:22528\n"
      "v_fmac_f32_e64 v218, v212, v212\n"
      "v_mfma_f32_16x16x32_bf16 v[184:187], a[116:119], v[172:175], v[184:187]\n"
      "s_cselect_b32 s9, 0, 196224\n"
      "v_fmac_f32 v216, v205, v205\n"
      "v_fmac_f32 v218, v213, v213\n"
      "v_mfma_f32_16x16x32_bf16 v[136:139], a[84:87], v[168:171], v[136:139]\n"
      "ds_read_b128 v[156:159], v115 offset:22528\n"
      "v_fmac_f32 v216, v206, v206\n"
      "v_mfma_f32_16x16x32_bf16 v[136:139], a[120:123], v[172:175], v[136:139]\n"
      "s_add_u32 s10, s8, s9\n"
      "v_fmac_f32 v218, v214, v214\n"
      "v_fmac_f32 v216, v207, v207\n"
      "v_mfma_f32_16x16x32_bf16 v[192:195], a[88:91], v[168:171], v[192:195]\n"
      "s_add_u32 s28, s28, s10\n"
      "v_cvt_pk_bf16_f32 v220, v220, v221\n"
      "v_fmac_f32 v218, v215, v215\n"
      "v_mfma_f32_16x16x32_bf16 v[192:195], a[124:127], v[172:175], v[192:195]\n"
      "s_add_u32 s54, s54, s28\n"
      "v_permlane16_swap_b32 v216, v218\n"
      "v_mfma_f32_16x16x32_bf16 v[164:167], a[92:95], v[168:171], v[164:167]\n"
      "s_addc_u32 s55, s55, 0\n"
      "v_add_f32 v216, v216, v218\n"
      "v_mov_b32 v218, v216\n"
      "v_mfma_f32_16x16x32_bf16 v[164:167], a[128:131], v[172:175], v[164:167]\n"
      "s_cmp_lg_u32 s10, s11\n"
      "v_permlane32_swap_b32 v216, v218\n"
      "v_add_f32 v216, v216, v218\n"
      "v_mfma_f32_16x16x32_bf16 v[152:155], v[168:171], a[96:99], v[152:155]\n"
      "s_cbranch_scc1 L_edge_17_%=\n"
      "L_eb_18_%=:\n"
      "v_mfma_f32_16x16x32_bf16 v[152:155], v[172:175], a[132:135], v[152:155]\n"
      "s_mov_b32 s5, s4\n"
      "v_rsq_f32 v216, v216\n"
      "v_mfma_f32_16x16x32_bf16 v[200:203], v[168:171], a[100:103], v[200:203]\n"
      "s_waitcnt lgkmcnt(0)\n"
      "v_cvt_pk_bf16_f32 v221, v222, v223\n"
      "v_mov_b32_e64 v218, v216\n"
      "v_mfma_f32_16x16x32_bf16 v[200:203], v[172:175], a[136:139], v[200:203]\n"
      "s_add_u32 s4, s27, 1\n"
      "v_permlane16_swap_b32 v216, v218\n"
      "v_mfma_f32_16x16x32_bf16 v[244:247], v[168:171], a[104:107], v[244:247]\n"
      "v_mul_f32 v248, v218, v248\n"
      "v_mul_f32 v249, v218, v249\n"
      "v_mfma_f32_16x16x32_bf16 v[244:247], v[172:175], a[140:143], v[244:247]\n"
      "v_cvt_pk_bf16_f32 v248, v248, v249\n"
      "v_mul_f32 v250, v218, v250\n"
      "v_mfma_f32_16x16x32_bf16 v[180:183], a[144:147], v[188:191], v[180:183]\n"
      "v_mul_f32 v251, v218, v251\n"
      "v_cvt_pk_bf16_f32 v249, v250, v251\n"
      "v_mfma_f32_16x16x32_bf16 v[180:183], a[180:183], v[196:199], v[180:183]\n"
      "ds_read_b128 v[168:171], v119\n"
      "v_mul_f32_e64 v232, v218, v232\n"
      "v_mfma_f32_16x16x32_bf16 v[176:179], a[148:151], v[188:191], v[176:179]\n"
      "s_lshl_b32 s4, s4, 30\n"
      "v_mul_f32 v233, v218, v233\n"
      "v_cvt_pk_bf16_f32 v250, v232, v233\n"
      "v_mfma_f32_16x16x32_bf16 v[176:179], a[184:187], v[196:199], v[176:179]\n"
      "v_mul_f32 v234, v218, v234\n"
      "v_mul_f32 v235, v218, v235\n"
      "v_mfma_f32_16x16x32_bf16 v[184:187], a[152:155], v[188:191], v[184:187]\n"
      "v_cvt_pk_bf16_f32 v251, v234, v235\n"
      "v_mul_f32_e64 v212, v218, v212\n"
      "v_mfma_f32_16x16x32_bf16 v[184:187], a[188:191], v[196:199], v[184:187]\n"
      "s_and_b32 s4, s4, 0x80000000\n"
      "v_mul_f32_e64 v213, v218, v213\n"
      "v_cvt_pk_bf16_f32 v34, v212, v213\n"
      "v_mfma_f32_16x16x32_bf16 v[136:139], a[156:159], v[188:191], v[136:139]\n"
      "ds_read_b128 v[172:175], v114 offset:28672\n"
      "v_mul_f32 v214, v218, v214\n"
      "v_mfma_f32_16x16x32_bf16 v[136:139], a[192:195], v[196:199], v[136:139]\n"
      "v_mul_f32 v215, v218, v215\n"
      "v_cvt_pk_bf16_f32 v35, v214, v215\n"
      "v_mfma_f32_16x16x32_bf16 v[192:195], a[160:163], v[188:191], v[192:195]\n"
      "ds_read_b128 v[212:215], v115 offset:28672\n"
      "v_mul_f32_e64 v216, s36, v216\n"
      "v_mfma_f32_16x16x32_bf16 v[192:195], a[196:199], v[196:199], v[192:195]\n"
      "s_waitcnt lgkmcnt(2)\n"
      "v_mul_f32 v145, v216, v145\n"
      "v_mul_f32_e64 v144, v216, v144\n"
      "v_mfma_f32_16x16x32_bf16 v[164:167], a[164:167], v[188:191], v[164:167]\n"
      "global_store_dwordx4 v116, v[168:171], s[54:55] sc1\n"
      "v_mfma_f32_16x16x32_bf16 v[164:167], a[200:203], v[196:199], v[164:167]\n"
      "v_mul_f32_e64 v146, v216, v146\n"
      "v_cvt_pk_bf16_f32 v144, v144, v145\n"
      "v_mfma_f32_16x16x32_bf16 v[152:155], v[188:191], a[168:171], v[152:155]\n"
      "v_mul_f32 v147, v216, v147\n"
      "v_cvt_pk_bf16_f32 v145, v146, v147\n"
      "v_mfma_f32_16x16x32_bf16 v[152:155], v[196:199], a[204:207], v[152:155]\n"
      "v_mul_f32 v148, v216, v148\n"
      "v_mul_f32 v149, v216, v149\n"
      "v_mfma_f32_16x16x32_bf16 v[200:203], v[188:191], a[172:175], v[200:203]\n"
      "v_mul_f32 v150, v216, v150\n"
      "v_cvt_pk_bf16_f32 v146, v148, v149\n"
      "v_mfma_f32_16x16x32_bf16 v[200:203], v[196:199], a[208:211], v[200:203]\n"
      "v_mul_f32 v151, v216, v151\n"
      "v_cvt_pk_bf16_f32 v147, v150, v151\n"
      "v_mfma_f32_16x16x32_bf16 v[244:247], v[188:191], a[176:179], v[244:247]\n"
      "v_mul_f32 v205, v216, v205\n"
      "v_mul_f32_e64 v204, v216, v204\n"
      "v_mfma_f32_16x16x32_bf16 v[244:247], v[196:199], a[212:215], v[244:247]\n"
      "v_mul_f32 v206, v216, v206\n"
      "v_cvt_pk_bf16_f32 v32, v204, v205\n"
      "v_mfma_f32_16x16x32_bf16 v[196:199], a[0:3], v[140:143], 0\n"
      "v_mul_f32 v207, v216, v207\n"
      "v_cvt_pk_bf16_f32 v33, v206, v207\n"
      "v_mfma_f32_16x16x32_bf16 v[196:199], a[36:39], v[156:159], v[196:199]\n"
      "v_cvt_pk_bf16_f32 v222, v152, v153\n"
      "v_cvt_pk_bf16_f32 v223, v154, v155\n"
      "v_mfma_f32_16x16x32_bf16 v[188:191], a[4:7], v[140:143], 0\n"
      "v_cvt_pk_bf16_f32 v224, v224, v225\n"
      "v_cvt_pk_bf16_f32 v225, v226, v227\n"
      "v_mfma_f32_16x16x32_bf16 v[188:191], a[40:43], v[156:159], v[188:191]\n"
      "ds_read_b128 v[168:171], v119 offset:6144\n"
      "v_cvt_pk_bf16_f32 v226, v200, v201\n"
      "v_mfma_f32_16x16x32_bf16 v[152:155], a[8:11], v[140:143], 0\n"
      "v_cvt_pk_bf16_f32 v227, v202, v203\n"
      "v_cvt_pk_bf16_f32 v160, v160, v161\n"
      "v_mfma_f32_16x16x32_bf16 v[152:155], a[44:47], v[156:159], v[152:155]\n"
      "v_cvt_pk_bf16_f32 v161, v162, v163\n"
      "v_cvt_pk_bf16_f32 v162, v244, v245\n"
      "v_mfma_f32_16x16x32_bf16 v[200:203], a[12:15], v[140:143], 0\n"
      "v_fma_f32 v236, v180, v180, s38\n"
      "v_cvt_pk_bf16_f32 v163, v246, v247\n"
      "v_mfma_f32_16x16x32_bf16 v[200:203], a[48:51], v[156:159], v[200:203]\n"
      "v_fmac_f32 v236, v181, v181\n"
      "v_fma_f32 v237, v136, v136, s38\n"
      "v_mfma_f32_16x16x32_bf16 v[148:151], a[16:19], v[140:143], 0\n"
      "v_fmac_f32 v236, v182, v182\n"
      "v_fmac_f32_e64 v237, v137, v137\n"
      "v_mfma_f32_16x16x32_bf16 v[148:151], a[52:55], v[156:159], v[148:151]\n"
      "v_fmac_f32 v237, v138, v138\n"
      "v_fmac_f32 v236, v183, v183\n"
      "v_mfma_f32_16x16x32_bf16 v[244:247], a[20:23], v[140:143], 0\n"
      "v_fmac_f32 v236, v176, v176\n"
      "v_fmac_f32 v237, v139, v139\n"
      "v_mfma_f32_16x16x32_bf16 v[244:247], a[56:59], v[156:159], v[244:247]\n"
      "s_waitcnt lgkmcnt(0)\n"
      "v_fmac_f32 v237, v192, v192\n"
      "v_fmac_f32_e64 v236, v177, v177\n"
      "v_mfma_f32_16x16x32_bf16 v[204:207], v[140:143], a[24:27], 0\n"
      "global_store_dwordx4 v116, v[168:171], s[54:55] offset:128 sc1\n"
      "v_mfma_f32_16x16x32_bf16 v[204:207], v[156:159], a[60:63], v[204:207]\n"
      "ds_read_b128 v[232:235], v114 offset:34816\n"
      "v_fmac_f32_e64 v237, v193, v193\n"
      "v_mfma_f32_16x16x32_bf16 v[216:219], v[140:143], a[28:31], 0\n"
      "v_fmac_f32 v236, v178, v178\n"
      "v_fmac_f32 v237, v194, v194\n"
      "v_mfma_f32_16x16x32_bf16 v[216:219], v[156:159], a[64:67], v[216:219]\n"
      "v_fmac_f32 v236, v179, v179\n"
      "v_fmac_f32 v237, v195, v195\n"
      "v_mfma_f32_16x16x32_bf16 v[208:211], v[140:143], a[32:35], 0\n"
      "ds_read_b128 v[168:171], v115 offset:34816\n"
      "v_fmac_f32 v236, v184, v184\n"
      "v_mfma_f32_16x16x32_bf16 v[208:211], v[156:159], a[68:71], v[208:211]\n"
      "v_fmac_f32 v237, v164, v164\n"
      "v_fmac_f32 v236, v185, v185\n"
      "v_mfma_f32_16x16x32_bf16 v[196:199], a[72:75], v[172:175], v[196:199]\n"
      "v_fmac_f32 v237, v165, v165\n"
      "v_fmac_f32_e64 v237, v166, v166\n"
      "v_mfma_f32_16x16x32_bf16 v[196:199], a[108:111], v[212:215], v[196:199]\n"
      "ds_read_b128 v[140:143], v132 offset:1200\n"
      "v_fmac_f32 v236, v186, v186\n"
      "v_mfma_f32_16x16x32_bf16 v[188:191], a[76:79], v[172:175], v[188:191]\n"
      "v_fmac_f32 v237, v167, v167\n"
      "v_fmac_f32_e64 v236, v187, v187\n"
      "v_mfma_f32_16x16x32_bf16 v[188:191], a[112:115], v[212:215], v[188:191]\n"
      "s_nop 0\n"
      "v_permlane16_swap_b32 v236, v237\n"
      "v_mfma_f32_16x16x32_bf16 v[152:155], a[80:83], v[172:175], v[152:155]\n"
      "v_add_f32 v236, v236, v237\n"
      "v_mov_b32 v237, v236\n"
      "v_mfma_f32_16x16x32_bf16 v[152:155], a[116:119], v[212:215], v[152:155]\n"
      "s_nop 0\n"
      "v_permlane32_swap_b32 v236, v237\n"
      "v_mfma_f32_16x16x32_bf16 v[200:203], a[84:87], v[172:175], v[200:203]\n"
      "v_add_f32_e64 v236, v236, v237\n"
      "v_mfma_f32_16x16x32_bf16 v[200:203], a[120:123], v[212:215], v[200:203]\n"
      "v_rsq_f32 v236, v236\n"
      "v_mfma_f32_16x16x32_bf16 v[148:151], a[88:91], v[172:175], v[148:151]\n"
      "v_mov_b32 v237, v236\n"
      "v_mfma_f32_16x16x32_bf16 v[148:151], a[124:127], v[212:215], v[148:151]\n"
      "s_nop 0\n"
      "v_permlane16_swap_b32 v236, v237\n"
      "v_mfma_f32_16x16x32_bf16 v[244:247], a[92:95], v[172:175], v[244:247]\n"
      "s_waitcnt lgkmcnt(0)\n"
      "v_mul_f32 v192, v237, v192\n"
      "v_mul_f32_e64 v136, v237, v136\n"
      "v_mfma_f32_16x16x32_bf16 v[244:247], a[128:131], v[212:215], v[244:247]\n"
      "ds_read_b64 v[156:157], v132 offset:1192\n"
      "v_mul_f32 v137, v237, v137\n"
      "v_mfma_f32_16x16x32_bf16 v[204:207], v[172:175], a[96:99], v[204:207]\n"
      "v_mul_f32 v138, v237, v138\n"
      "v_cvt_pk_bf16_f32 v136, v136, v137\n"
      "v_mfma_f32_16x16x32_bf16 v[204:207], v[212:215], a[132:135], v[204:207]\n"
      "v_mul_f32 v194, v237, v194\n"
      "v_mul_f32 v139, v237, v139\n"
      "v_mfma_f32_16x16x32_bf16 v[216:219], v[172:175], a[100:103], v[216:219]\n"
      "ds_read_u16 v52, v134 offset:1168\n"
      "v_cvt_pk_bf16_f32 v137, v138, v139\n"
      "v_mfma_f32_16x16x32_bf16 v[216:219], v[212:215], a[136:139], v[216:219]\n"
      "ds_read_u16 v56, v134 offset:1200\n"
      "v_mul_f32_e64 v193, v237, v193\n"
      "v_mfma_f32_16x16x32_bf16 v[208:211], v[172:175], a[104:107], v[208:211]\n"
      "ds_read_b128 v[228:231], v133 offset:1168\n"
      "v_cvt_pk_bf16_f32 v138, v192, v193\n"
      "v_mfma_f32_16x16x32_bf16 v[208:211], v[212:215], a[140:143], v[208:211]\n"
      "v_mul_f32 v164, v237, v164\n"
      "v_mul_f32 v195, v237, v195\n"
      "v_mfma_f32_16x16x32_bf16 v[196:199], a[144:147], v[232:235], v[196:199]\n"
      "ds_read_u16 v60, v134 offset:1232\n"
      "v_mfma_f32_16x16x32_bf16 v[196:199], a[180:183], v[168:171], v[196:199]\n"
      "ds_read_b128 v[212:215], v123\n"
      "v_cvt_pk_bf16_f32 v139, v194, v195\n"
      "v_mfma_f32_16x16x32_bf16 v[188:191], a[148:151], v[232:235], v[188:191]\n"
      "ds_read_b64 v[158:159], v133 offset:1184\n"
      "v_mul_f32 v165, v237, v165\n"
      "v_mfma_f32_16x16x32_bf16 v[188:191], a[184:187], v[168:171], v[188:191]\n"
      "v_mul_f32 v166, v237, v166\n"
      "v_cvt_pk_bf16_f32 v40, v164, v165\n"
      "v_mfma_f32_16x16x32_bf16 v[152:155], a[152:155], v[232:235], v[152:155]\n"
      "ds_read_b128 v[192:195], v123 offset:3072\n"
      "v_mul_f32 v167, v237, v167\n"
      "v_mfma_f32_16x16x32_bf16 v[152:155], a[188:191], v[168:171], v[152:155]\n"
      "v_mul_f32 v236, s36, v236\n"
      "v_cvt_pk_bf16_f32 v41, v166, v167\n"
      "v_mfma_f32_16x16x32_bf16 v[200:203], a[156:159], v[232:235], v[200:203]\n"
      "v_mul_f32 v180, v236, v180\n"
      "v_mul_f32 v181, v236, v181\n"
      "v_mfma_f32_16x16x32_bf16 v[200:203], a[192:195], v[168:171], v[200:203]\n"
      "v_mul_f32 v182, v236, v182\n"
      "v_cvt_pk_bf16_f32 v180, v180, v181\n"
      "v_mfma_f32_16x16x32_bf16 v[148:151], a[160:163], v[232:235], v[148:151]\n"
      "v_mul_f32 v183, v236, v183\n"
      "v_cvt_pk_bf16_f32 v181, v182, v183\n"
      "v_mfma_f32_16x16x32_bf16 v[148:151], a[196:199], v[168:171], v[148:151]\n"
      "v_mul_f32 v179, v236, v179\n"
      "v_mul_f32 v176, v236, v176\n"
      "v_mfma_f32_16x16x32_bf16 v[244:247], a[164:167], v[232:235], v[244:247]\n"
      "v_mul_f32_e64 v177, v236, v177\n"
      "v_cvt_pk_bf16_f32 v182, v176, v177\n"
      "v_mfma_f32_16x16x32_bf16 v[244:247], a[200:203], v[168:171], v[244:247]\n"
      "ds_read_b128 v[164:167], v123 offset:6144\n"
      "v_mfma_f32_16x16x32_bf16 v[204:207], v[232:235], a[168:171], v[204:207]\n"
      "v_mul_f32 v178, v236, v178\n"
      "v_mfma_f32_16x16x32_bf16 v[204:207], v[168:171], a[204:207], v[204:207]\n"
      "ds_read_b128 v[240:243], v123 offset:9216\n"
      "v_mul_f32 v186, v236, v186\n"
      "v_mfma_f32_16x16x32_bf16 v[216:219], v[232:235], a[172:175], v[216:219]\n"
      "v_mul_f32 v187, v236, v187\n"
      "v_mul_f32 v184, v236, v184\n"
      "v_mfma_f32_16x16x32_bf16 v[216:219], v[168:171], a[208:211], v[216:219]\n"
      "s_waitcnt lgkmcnt(2)\n"
      "v_mul_f32 v185, v236, v185\n"
      "v_cvt_pk_bf16_f32 v39, v186, v187\n"
      "v_mfma_f32_16x16x32_bf16 v[208:211], v[232:235], a[176:179], v[208:211]\n"
      "v_fma_f32 v232, v196, v196, s38\n"
      "v_cvt_pk_bf16_f32 v183, v178, v179\n"
      "v_mfma_f32_16x16x32_bf16 v[208:211], v[168:171], a[212:215], v[208:211]\n"
      "v_fma_f32 v233, v200, v200, s38\n"
      "v_fmac_f32 v232, v197, v197\n"
      "v_mfma_f32_16x16x32_bf16 v[168:171], a[216:219], v[212:215], 0\n"
      "v_fmac_f32 v232, v198, v198\n"
      "v_fmac_f32_e64 v233, v201, v201\n"
      "v_mfma_f32_16x16x32_bf16 v[168:171], a[228:231], v[192:195], v[168:171]\n"
      "v_cvt_pk_bf16_f32 v38, v184, v185\n"
      "v_fmac_f32 v233, v202, v202\n"
      "v_mfma_f32_16x16x32_bf16 v[172:175], a[220:223], v[212:215], 0\n"
      "v_fmac_f32 v232, v199, v199\n"
      "v_fmac_f32 v233, v203, v203\n"
      "v_mfma_f32_16x16x32_bf16 v[172:175], a[232:235], v[192:195], v[172:175]\n"
      "v_fmac_f32 v232, v188, v188\n"
      "v_fmac_f32 v233, v148, v148\n"
      "v_mfma_f32_16x16x32_bf16 v[176:179], a[224:227], v[212:215], 0\n"
      "s_or_b64 s[34:35], s[48:49], s[50:51]\n"
      "v_fmac_f32 v232, v189, v189\n"
      "v_fmac_f32 v233, v149, v149\n"
      "v_mfma_f32_16x16x32_bf16 v[176:179], a[236:239], v[192:195], v[176:179]\n"
      "s_waitcnt lgkmcnt(0)\n"
      "v_fmac_f32 v233, v150, v150\n"
      "v_fmac_f32_e64 v232, v190, v190\n"
      "v_mfma_f32_16x16x32_bf16 v[168:171], a[240:243], v[164:167], v[168:171]\n"
      "ds_read_b128 v[184:187], v123 offset:12288\n"
      "v_fmac_f32_e64 v232, v191, v191\n"
      "v_mfma_f32_16x16x32_bf16 v[168:171], a[252:255], v[240:243], v[168:171]\n"
      "ds_read_b128 v[192:195], v123 offset:15360\n"
      "v_fmac_f32 v233, v151, v151\n"
      "v_mfma_f32_16x16x32_bf16 v[172:175], a[244:247], v[164:167], v[172:175]\n"
      "v_fmac_f32 v233, v244, v244\n"
      "v_fmac_f32_e64 v232, v152, v152\n"
      "v_mfma_f32_16x16x32_bf16 v[172:175], v[0:3], v[240:243], v[172:175]\n"
      "v_fmac_f32 v233, v245, v245\n"
      "v_fmac_f32 v232, v153, v153\n"
      "v_mfma_f32_16x16x32_bf16 v[176:179], a[248:251], v[164:167], v[176:179]\n"
      "v_fmac_f32 v233, v246, v246\n"
      "v_fmac_f32 v232, v154, v154\n"
      "v_mfma_f32_16x16x32_bf16 v[176:179], v[4:7], v[240:243], v[176:179]\n"
      "v_fmac_f32 v233, v247, v247\n"
      "v_fmac_f32 v232, v155, v155\n"
      "v_mfma_f32_16x16x32_bf16 v[164:167], v[248:251], v[144:147], v[78:81]\n"
      "s_nop 0\n"
      "v_permlane16_swap_b32 v232, v233\n"
      "v_mfma_f32_16x16x32_bf16 v[164:167], v[34:37], v[32:35], v[164:167]\n"
      "v_add_f32 v232, v232, v233\n"
      "v_mov_b32 v233, v232\n"
      "v_add_u32 v132, 2336, v132\n"
      "v_add_u32 v133, 2336, v133\n"
      "v_permlane32_swap_b32 v232, v233\n"
      "v_add_f32 v232, v232, v233\n"
      "v_rsq_f32 v232, v232\n"
      "s_waitcnt lgkmcnt(0)\n"
      "v_mfma_f32_16x16x32_bf16 v[168:171], v[8:11], v[184:187], v[168:171]\n"
      "v_add_u32 v134, 2336, v134\n"
      "v_mov_b32 v233, v232\n"
      "v_mfma_f32_16x16x32_bf16 v[168:171], v[20:23], v[192:195], v[168:171]\n"
      "s_nop 0\n"
      "v_permlane16_swap_b32 v232, v233\n"
      "v_mul_f32 v202, v233, v202\n"
      "v_mfma_f32_16x16x32_bf16 v[172:175], v[12:15], v[184:187], v[172:175]\n"
      "v_mul_f32 v200, v233, v200\n"
      "v_mul_f32 v201, v233, v201\n"
      "v_mfma_f32_16x16x32_bf16 v[172:175], v[24:27], v[192:195], v[172:175]\n"
      "v_mul_f32_e64 v148, v233, v148\n"
      "v_cvt_pk_bf16_f32 v200, v200, v201\n"
      "v_mfma_f32_16x16x32_bf16 v[176:179], v[16:19], v[184:187], v[176:179]\n"
      "v_mul_f32 v203, v233, v203\n"
      "v_cvt_pk_bf16_f32 v201, v202, v203\n"
      "v_mfma_f32_16x16x32_bf16 v[176:179], v[28:31], v[192:195], v[176:179]\n"
      "v_mul_f32 v149, v233, v149\n"
      "v_mul_f32 v151, v233, v151\n"
      "v_mul_f32 v247, v233, v247\n"
      "v_mfma_f32_16x16x32_bf16 v[184:187], v[136:139], v[144:147], v[82:85]\n"
      "v_mul_f32_e64 v150, v233, v150\n"
      "v_cvt_pk_bf16_f32 v202, v148, v149\n"
      "v_mfma_f32_16x16x32_bf16 v[184:187], v[40:43], v[32:35], v[184:187]\n"
      "v_mul_f32 v232, s36, v232\n"
      "v_cvt_pk_bf16_f32 v203, v150, v151\n"
      "v_mfma_f32_16x16x32_bf16 v[148:151], v[140:143], v[144:147], v[70:73]\n"
      "v_mul_f32 v244, v233, v244\n"
      "v_mul_f32 v245, v233, v245\n"
      "v_mfma_f32_16x16x32_bf16 v[148:151], v[228:231], v[248:251], v[148:151]\n"
      "v_mul_f32 v246, v233, v246\n"
      "v_cvt_pk_bf16_f32 v46, v244, v245\n"
      "v_mfma_f32_16x16x32_bf16 v[148:151], v[156:159], v[32:35], v[148:151]\n"
      "v_mul_f32 v154, v232, v154\n"
      "v_mul_f32 v152, v232, v152\n"
      "v_mul_f32 v196, v232, v196\n"
      "v_cvt_pk_bf16_f32 v47, v246, v247\n"
      "v_mfma_f32_16x16x32_bf16 v[192:195], v[200:203], v[144:147], v[86:89]\n"
      "s_waitcnt vmcnt(2)\n"
      "v_mul_f32_e64 v197, v232, v197\n"
      "v_cvt_pk_bf16_f32 v196, v196, v197\n"
      "v_mfma_f32_16x16x32_bf16 v[192:195], v[46:49], v[32:35], v[192:195]\n"
      "ds_read_b32 v150, v122\n"
      "ds_read_b128 v[144:147], v119 offset:36864\n"
      "v_mul_f32 v190, v232, v190\n"
      "v_fma_f32 v151, v168, v168, s39\n"
      "v_cvt_pk_bf16_f32 v58, v208, v209\n"
      "v_mul_f32 v199, v232, v199\n"
      "v_cvt_pk_bf16_f32 v54, v216, v217\n"
      "v_mul_f32 v155, v232, v155\n"
      "v_cvt_pk_bf16_f32 v51, v206, v207\n"
      "v_cvt_pk_bf16_f32 v55, v218, v219\n"
      "v_mul_f32 v153, v232, v153\n"
      "v_mul_f32 v198, v232, v198\n"
      "v_fmac_f32 v151, v169, v169\n"
      "v_mul_f32 v188, v232, v188\n"
      "v_mul_f32 v189, v232, v189\n"
      "v_cvt_pk_bf16_f32 v197, v198, v199\n"
      "v_fmac_f32 v151, v170, v170\n"
      "v_fmac_f32 v151, v171, v171\n"
      "v_cvt_pk_bf16_f32 v59, v210, v211\n"
      "v_cvt_pk_bf16_f32 v50, v204, v205\n"
      "v_fmac_f32 v151, v172, v172\n"
      "v_cvt_pk_bf16_f32 v44, v152, v153\n"
      "v_fmac_f32 v151, v173, v173\n"
      "v_mul_f32 v191, v232, v191\n"
      "v_fmac_f32 v151, v174, v174\n"
      "v_fmac_f32_e64 v151, v175, v175\n"
      "v_cvt_pk_bf16_f32 v45, v154, v155\n"
      "v_cvt_pk_bf16_f32 v198, v188, v189\n"
      "v_cvt_pk_bf16_f32 v199, v190, v191\n"
      "v_fmac_f32 v151, v176, v176\n"
      "v_fmac_f32 v151, v177, v177\n"
      "v_fmac_f32 v151, v178, v178\n"
      "v_fmac_f32_e64 v151, v179, v179\n"
      "s_waitcnt lgkmcnt(0)\n"
      "v_or3_b32 v144, v144, v145, v146\n"
      "v_and_b32_e64 v144, s40, v144\n"
      "v_bitop3_b32 v150, v150, v147, s40 bitop3:0xa8\n"
      "v_or3_b32 v144, v144, v150, s4\n"
      "ds_write_b32 v129, v144 offset:2048\n"
      "s_cbranch_scc1 L_gen_19_%=\n"
      "L_fast_22_%=:\n"
      "v_mfma_f32_16x16x32_bf16 v[152:155], v[248:251], v[180:183], v[90:93]\n"
      "ds_read_b128 v[188:191], v119 offset:12288\n"
      "v_exp_f32 v184, v184\n"
      "v_exp_f32 v165, v165\n"
      "v_exp_f32 v167, v167\n"
      "v_mfma_f32_16x16x32_bf16 v[152:155], v[34:37], v[38:41], v[152:155]\n"
      "ds_read_b128 v[144:147], v123 offset:256\n"
      "v_exp_f32 v164, v164\n"
      "v_mfma_f32_16x16x32_bf16 v[208:211], v[136:139], v[180:183], v[94:97]\n"
      "ds_read_b128 v[216:219], v123 offset:3328\n"
      "v_exp_f32 v187, v187\n"
      "v_mfma_f32_16x16x32_bf16 v[244:247], v[140:143], v[180:183], v[72:75]\n"
      "ds_read_b128 v[232:235], v123 offset:6400\n"
      "v_cvt_pk_bf16_f32 v164, v164, v165\n"
      "v_mfma_f32_16x16x32_bf16 v[244:247], v[228:231], v[136:139], v[244:247]\n"
      "v_exp_f32_e64 v166, v166\n"
      "v_mfma_f32_16x16x32_bf16 v[244:247], v[156:159], v[38:41], v[244:247]\n"
      "ds_read_b128 v[204:207], v128 offset:2048\n"
      "v_exp_f32 v186, v186\n"
      "v_mfma_f32_16x16x32_bf16 v[208:211], v[40:43], v[38:41], v[208:211]\n"
      "ds_read_b128 v[212:215], v123 offset:9472\n"
      "v_exp_f32 v193, v193\n"
      "v_exp_f32 v195, v195\n"
      "v_exp_f32 v185, v185\n"
      "v_exp_f32 v194, v194\n"
      "v_cvt_pk_bf16_f32 v165, v166, v167\n"
      "v_mfma_f32_16x16x32_bf16 v[240:243], v[200:203], v[180:183], v[98:101]\n"
      "s_waitcnt lgkmcnt(4)\n"
      "v_exp_f32 v192, v192\n"
      "v_mfma_f32_16x16x32_bf16 v[240:243], v[46:49], v[38:41], v[240:243]\n"
      "s_waitcnt lgkmcnt(3)\n"
      "v_cvt_pk_bf16_f32 v166, v184, v185\n"
      "v_cvt_pk_bf16_f32 v167, v186, v187\n"
      "v_mfma_f32_16x16x32_bf16 v[180:183], a[216:219], v[144:147], 0\n"
      "global_store_dwordx4 v116, v[188:191], s[54:55] offset:256 sc1\n"
      "v_cvt_pk_bf16_f32 v62, v192, v193\n"
      "v_cvt_pk_bf16_f32 v63, v194, v195\n"
      "v_mfma_f32_16x16x32_bf16 v[180:183], a[228:231], v[216:219], v[180:183]\n"
      "v_exp_f32 v148, v148\n"
      "v_mfma_f32_16x16x32_bf16 v[184:187], a[220:223], v[144:147], 0\n"
      "s_waitcnt lgkmcnt(1)\n"
      "v_bitop3_b32 v204, v204, v205, s4 bitop3:0x7e\n"
      "v_bitop3_b32 v206, v206, v207, s4 bitop3:0x7e\n"
      "v_mfma_f32_16x16x32_bf16 v[184:187], a[232:235], v[216:219], v[184:187]\n"
      "s_waitcnt lgkmcnt(0)\n"
      "v_or_b32 v204, v204, v206\n"
      "v_exp_f32_e64 v152, v152\n"
      "v_cmp_ne_u32 vcc, 0, v204\n"
      "v_mfma_f32_16x16x32_bf16 v[188:191], a[224:227], v[144:147], 0\n"
      "s_mov_b64 s[48:49], vcc\n"
      "ds_read_b128 v[192:195], v123 offset:12544\n"
      "v_mfma_f32_16x16x32_bf16 v[188:191], a[236:239], v[216:219], v[188:191]\n"
      "ds_read_b128 v[144:147], v123 offset:15616\n"
      "v_exp_f32 v208, v208\n"
      "v_mfma_f32_16x16x32_bf16 v[180:183], a[240:243], v[232:235], v[180:183]\n"
      "s_cbranch_vccnz L_flg_23_%=\n"
      "L_fb_24_%=:\n"
      "v_mfma_f32_16x16x32_bf16 v[180:183], a[252:255], v[212:215], v[180:183]\n"
      "ds_read_b128 v[204:207], v120\n"
      "v_exp_f32 v209, v209\n"
      "v_mfma_f32_16x16x32_bf16 v[184:187], a[244:247], v[232:235], v[184:187]\n"
      "v_exp_f32 v154, v154\n"
      "v_mfma_f32_16x16x32_bf16 v[184:187], v[0:3], v[212:215], v[184:187]\n"
      "v_exp_f32 v153, v153\n"
      "v_exp_f32 v155, v155\n"
      "v_cvt_pk_bf16_f32 v64, v148, 0\n"
      "v_cvt_pk_bf16_f32 v152, v152, v153\n"
      "v_mfma_f32_16x16x32_bf16 v[188:191], a[248:251], v[232:235], v[188:191]\n"
      "ds_read_b128 v[232:235], v123 offset:512\n"
      "v_cvt_pk_bf16_f32 v153, v154, v155\n"
      "v_mfma_f32_16x16x32_bf16 v[188:191], v[4:7], v[212:215], v[188:191]\n"
      "ds_read_b128 v[212:215], v123 offset:3584\n"
      "s_waitcnt lgkmcnt(3)\n"
      "v_cvt_pk_bf16_f32 v154, v208, v209\n"
      "v_mfma_f32_16x16x32_bf16 v[180:183], v[8:11], v[192:195], v[180:183]\n"
      "v_exp_f32 v210, v210\n"
      "v_mfma_f32_16x16x32_bf16 v[180:183], v[20:23], v[144:147], v[180:183]\n"
      "v_exp_f32 v211, v211\n"
      "v_mfma_f32_16x16x32_bf16 v[184:187], v[12:15], v[192:195], v[184:187]\n"
      "s_waitcnt lgkmcnt(2)\n"
      "v_exp_f32 v240, v240\n"
      "v_mfma_f32_16x16x32_bf16 v[184:187], v[24:27], v[144:147], v[184:187]\n"
      "global_store_dwordx4 v117, v[204:207], s[54:55] sc1\n"
      "v_mfma_f32_16x16x32_bf16 v[188:191], v[16:19], v[192:195], v[188:191]\n"
      "s_nop 0\n"
      "v_fma_f32 v148, v180, v180, s39\n"
      "v_cvt_pk_bf16_f32 v155, v210, v211\n"
      "v_mfma_f32_16x16x32_bf16 v[188:191], v[28:31], v[144:147], v[188:191]\n"
      "v_exp_f32 v241, v241\n"
      "v_mfma_f32_16x16x32_bf16 v[192:195], v[66:69], v[164:167], 0\n"
      "v_exp_f32 v242, v242\n"
      "v_mfma_f32_16x16x32_bf16 v[192:195], v[66:69], v[62:65], v[192:195]\n"
      "v_exp_f32 v243, v243\n"
      "v_mfma_f32_16x16x32_bf16 v[144:147], v[220:223], v[164:167], 0\n"
      "s_waitcnt lgkmcnt(0)\n"
      "v_exp_f32 v244, v244\n"
      "v_mfma_f32_16x16x32_bf16 v[144:147], v[50:53], v[62:65], v[144:147]\n"
      "v_fmac_f32 v148, v181, v181\n"
      "v_fmac_f32 v148, v182, v182\n"
      "v_mfma_f32_16x16x32_bf16 v[204:207], v[224:227], v[164:167], 0\n"
      "v_fmac_f32 v148, v183, v183\n"
      "v_fmac_f32 v148, v184, v184\n"
      "v_mfma_f32_16x16x32_bf16 v[204:207], v[54:57], v[62:65], v[204:207]\n"
      "v_fmac_f32 v148, v185, v185\n"
      "v_fmac_f32 v148, v186, v186\n"
      "v_mfma_f32_16x16x32_bf16 v[208:211], v[160:163], v[164:167], 0\n"
      "v_fmac_f32 v148, v187, v187\n"
      "v_fmac_f32 v148, v188, v188\n"
      "v_mfma_f32_16x16x32_bf16 v[208:211], v[58:61], v[62:65], v[208:211]\n"
      "v_fmac_f32 v148, v189, v189\n"
      "v_fmac_f32 v148, v190, v190\n"
      "v_mfma_f32_16x16x32_bf16 v[164:167], v[248:251], v[196:199], v[102:105]\n"
      "v_fmac_f32 v148, v191, v191\n"
      "v_cvt_pk_bf16_f32 v62, v240, v241\n"
      "v_mfma_f32_16x16x32_bf16 v[164:167], v[34:37], v[44:47], v[164:167]\n"
      "ds_read_b128 v[248:251], v121\n"
      "v_cvt_pk_bf16_f32 v63, v242, v243\n"
      "v_mfma_f32_16x16x32_bf16 v[240:243], v[136:139], v[196:199], v[106:109]\n"
      "v_cndmask_b32 v149, v149, v245, s[42:43]\n"
      "v_cvt_pk_bf16_f32 v64, v244, 0\n"
      "v_mfma_f32_16x16x32_bf16 v[240:243], v[40:43], v[44:47], v[240:243]\n"
      "v_rcp_f32 v192, v192\n"
      "v_mfma_f32_16x16x32_bf16 v[136:139], v[140:143], v[196:199], v[74:77]\n"
      "v_mul_f32 v145, v192, v145\n"
      "v_mul_f32 v144, v192, v144\n"
      "v_mfma_f32_16x16x32_bf16 v[136:139], v[228:231], v[200:203], v[136:139]\n"
      "v_mul_f32 v146, v192, v146\n"
      "v_cvt_pk_bf16_f32 v144, v144, v145\n"
      "v_mfma_f32_16x16x32_bf16 v[136:139], v[156:159], v[44:47], v[136:139]\n"
      "v_mul_f32 v147, v192, v147\n"
      "v_cvt_pk_bf16_f32 v145, v146, v147\n"
      "v_mfma_f32_16x16x32_bf16 v[244:247], v[200:203], v[196:199], v[110:113]\n"
      "ds_read_b128 v[216:219], v123 offset:6656\n"
      "v_mul_f32 v204, v192, v204\n"
      "v_mfma_f32_16x16x32_bf16 v[244:247], v[46:49], v[44:47], v[244:247]\n"
      "v_mul_f32_e64 v205, v192, v205\n"
      "v_cvt_pk_bf16_f32 v146, v204, v205\n"
      "v_mfma_f32_16x16x32_bf16 v[196:199], v[228:231], v[140:143], v[76:79]\n"
      "ds_read_b128 v[140:143], v123 offset:9728\n"
      "v_mul_f32 v206, v192, v206\n"
      "v_mfma_f32_16x16x32_bf16 v[228:231], v[66:69], v[152:155], 0\n"
      "ds_read_b128 v[236:239], v123 offset:12800\n"
      "v_mul_f32 v207, v192, v207\n"
      "v_mfma_f32_16x16x32_bf16 v[228:231], v[66:69], v[62:65], v[228:231]\n"
      "s_waitcnt lgkmcnt(3)\n"
      "v_cvt_pk_bf16_f32 v147, v206, v207\n"
      "v_mul_f32 v208, v192, v208\n"
      "v_mfma_f32_16x16x16_bf16 v[196:199], v[158:159], v[156:157], v[196:199]\n"
      "ds_write_b128 v124, v[144:147] offset:18432\n"
      "v_mfma_f32_16x16x32_bf16 v[144:147], v[220:223], v[152:155], 0\n"
      "v_mul_f32_e64 v209, v192, v209\n"
      "v_cvt_pk_bf16_f32 v208, v208, v209\n"
      "v_mfma_f32_16x16x32_bf16 v[144:147], v[50:53], v[62:65], v[144:147]\n"
      "s_mov_b32 exec_hi, 0\n"
      "global_store_dwordx4 v118, v[248:251], s[54:55] sc1\n"
      "s_mov_b32 exec_hi, -1\n"
      "v_mfma_f32_16x16x32_bf16 v[156:159], v[224:227], v[152:155], 0\n"
      "s_add_u32 s28, s27, 2\n"
      "v_mul_f32 v210, v192, v210\n"
      "v_mul_f32 v211, v192, v211\n"
      "v_mfma_f32_16x16x32_bf16 v[156:159], v[54:57], v[62:65], v[156:159]\n"
      "s_waitcnt lgkmcnt(2)\n"
      "v_cndmask_b32 v149, v149, v137, s[44:45]\n"
      "v_cvt_pk_bf16_f32 v209, v210, v211\n"
      "v_mfma_f32_16x16x32_bf16 v[248:251], v[160:163], v[152:155], 0\n"
      "s_min_u32 m0, s28, s3\n"
      "v_cndmask_b32 v149, v149, v197, s[46:47]\n"
      "s_movrels_b32 s9, s80\n"
      "v_mfma_f32_16x16x32_bf16 v[248:251], v[58:61], v[62:65], v[248:251]\n"
      "ds_write_b64 v125, v[208:209] offset:18432\n"
      "v_mfma_f32_16x16x32_bf16 v[192:195], a[216:219], v[232:235], 0\n"
      "s_lshr_b32 s8, s9, 8\n"
      "v_exp_f32 v149, v149\n"
      "v_mfma_f32_16x16x32_bf16 v[192:195], a[228:231], v[212:215], v[192:195]\n"
      "s_and_b32 s9, s9, 0xff\n"
      "v_cvt_pk_bf16_f32 v149, v149, v149\n"
      "v_exp_f32 v164, v164\n"
      "v_mfma_f32_16x16x32_bf16 v[196:199], a[220:223], v[232:235], 0\n"
      "s_mul_i32 s10, s8, 73\n"
      "v_exp_f32 v165, v165\n"
      "v_mfma_f32_16x16x32_bf16 v[196:199], a[232:235], v[212:215], v[196:199]\n"
      "ds_write_b16 v130, v149\n"
      "v_mfma_f32_16x16x32_bf16 v[200:203], a[224:227], v[232:235], 0\n"
      "ds_read_b128 v[232:235], v131\n"
      "v_cvt_pk_bf16_f32 v164, v164, v165\n"
      "v_mfma_f32_16x16x32_bf16 v[200:203], a[236:239], v[212:215], v[200:203]\n"
      "ds_read_b128 v[212:215], v131 offset:64\n"
      "v_exp_f32 v166, v166\n"
      "v_mfma_f32_16x16x32_bf16 v[192:195], a[240:243], v[216:219], v[192:195]\n"
      "ds_read_b128 v[152:155], v123 offset:15872\n"
      "s_add_u32 s10, s10, s9\n"
      "v_mfma_f32_16x16x32_bf16 v[192:195], a[252:255], v[140:143], v[192:195]\n"
      "ds_read_b128 v[208:211], v114\n"
      "v_exp_f32_e64 v167, v167\n"
      "v_mfma_f32_16x16x32_bf16 v[196:199], a[244:247], v[216:219], v[196:199]\n"
      "s_mul_i32 s28, s10, 4802\n"
      "v_exp_f32 v240, v240\n"
      "v_mfma_f32_16x16x32_bf16 v[196:199], v[0:3], v[140:143], v[196:199]\n"
      "s_add_u32 s56, s18, s28\n"
      "v_cvt_pk_bf16_f32 v165, v166, v167\n"
      "v_exp_f32_e64 v241, v241\n"
      "v_mfma_f32_16x16x32_bf16 v[200:203], a[248:251], v[216:219], v[200:203]\n"
      "ds_read_b128 v[216:219], v115\n"
      "v_cvt_pk_bf16_f32 v166, v240, v241\n"
      "v_mfma_f32_16x16x32_bf16 v[200:203], v[4:7], v[140:143], v[200:203]\n"
      "s_addc_u32 s57, s19, 0\n"
      "v_exp_f32 v244, v244\n"
      "v_exp_f32 v243, v243\n"
      "v_exp_f32_e64 v242, v242\n"
      "s_waitcnt lgkmcnt(3)\n"
      "v_mfma_f32_16x16x32_bf16 v[204:207], v[232:235], v[66:69], 0\n"
      "s_and_b32 s56, s56, -16\n"
      "v_cvt_pk_bf16_f32 v167, v242, v243\n"
      "s_waitcnt lgkmcnt(2)\n"
      "v_mfma_f32_16x16x32_bf16 v[204:207], v[212:215], v[66:69], v[204:207]\n"
      "s_sub_u32 s58, s56, 110592\n"
      "v_exp_f32 v245, v245\n"
      "v_mfma_f32_16x16x32_bf16 v[192:195], v[8:11], v[236:239], v[192:195]\n"
      "s_subb_u32 s59, s57, 0\n"
      "v_exp_f32_e64 v246, v246\n"
      "v_mfma_f32_16x16x32_bf16 v[192:195], v[20:23], v[152:155], v[192:195]\n"
      "s_sub_u32 s56, s56, 73728\n"
      "v_exp_f32 v247, v247\n"
      "v_mfma_f32_16x16x32_bf16 v[196:199], v[12:15], v[236:239], v[196:199]\n"
      "s_subb_u32 s57, s57, 0\n"
      "v_exp_f32 v136, v136\n"
      "v_mfma_f32_16x16x32_bf16 v[196:199], v[24:27], v[152:155], v[196:199]\n"
      "s_waitcnt lgkmcnt(0)\n"
      "v_cvt_pk_bf16_f32 v63, v246, v247\n"
      "v_cvt_pk_bf16_f32 v62, v244, v245\n"
      "v_mfma_f32_16x16x32_bf16 v[200:203], v[16:19], v[236:239], v[200:203]\n"
      "s_mul_i32 s28, s8, 1373568\n"
      "v_cvt_pk_bf16_f32 v64, v136, 0\n"
      "v_fma_f32 v149, v192, v192, s39\n"
      "v_mfma_f32_16x16x32_bf16 v[200:203], v[28:31], v[152:155], v[200:203]\n"
      "s_mul_i32 s29, s9, 2688\n"
      "v_rcp_f32 v228, v228\n"
      "v_mfma_f32_16x16x32_bf16 v[136:139], v[66:69], v[164:167], 0\n"
      "s_add_u32 s28, s28, s29\n"
      "v_mul_f32 v144, v228, v144\n"
      "v_mul_f32 v145, v228, v145\n"
      "v_mfma_f32_16x16x32_bf16 v[136:139], v[66:69], v[62:65], v[136:139]\n"
      "s_cmp_eq_u32 s8, 72\n"
      "v_mul_f32_e64 v146, v228, v146\n"
      "v_cvt_pk_bf16_f32 v144, v144, v145\n"
      "v_mfma_f32_16x16x32_bf16 v[140:143], v[220:223], v[164:167], 0\n"
      "s_cselect_b32 s8, 0, 100270464\n"
      "v_mul_f32_e64 v147, v228, v147\n"
      "v_cvt_pk_bf16_f32 v145, v146, v147\n"
      "v_mfma_f32_16x16x32_bf16 v[140:143], v[50:53], v[62:65], v[140:143]\n"
      "s_cmp_eq_u32 s9, 72\n"
      "v_mul_f32 v157, v228, v157\n"
      "v_mul_f32 v156, v228, v156\n"
      "v_mfma_f32_16x16x32_bf16 v[152:155], v[224:227], v[164:167], 0\n"
      "s_cselect_b32 s9, 0, 196224\n"
      "v_mul_f32_e64 v158, v228, v158\n"
      "v_cvt_pk_bf16_f32 v146, v156, v157\n"
      "v_mfma_f32_16x16x32_bf16 v[152:155], v[54:57], v[62:65], v[152:155]\n"
      "s_add_u32 s10, s8, s9\n"
      "v_mul_f32 v159, v228, v159\n"
      "v_cvt_pk_bf16_f32 v147, v158, v159\n"
      "v_mfma_f32_16x16x32_bf16 v[156:159], v[160:163], v[164:167], 0\n"
      "s_add_u32 s28, s28, s10\n"
      "v_mul_f32 v249, v228, v249\n"
      "v_mul_f32_e64 v248, v228, v248\n"
      "v_mfma_f32_16x16x32_bf16 v[156:159], v[58:61], v[62:65], v[156:159]\n"
      "ds_write_b128 v124, v[144:147] offset:18688\n"
      "v_mfma_f32_16x16x32_bf16 v[164:167], v[232:235], v[220:223], 0\n"
      "s_add_u32 s52, s16, s28\n"
      "v_cvt_pk_bf16_f32 v248, v248, v249\n"
      "v_mul_f32 v250, v228, v250\n"
      "v_mfma_f32_16x16x32_bf16 v[164:167], v[212:215], v[50:53], v[164:167]\n"
      "s_addc_u32 s53, s17, 0\n"
      "v_mul_f32 v251, v228, v251\n"
      "v_cvt_pk_bf16_f32 v249, v250, v251\n"
      "v_mfma_f32_16x16x32_bf16 v[144:147], v[232:235], v[224:227], 0\n"
      "s_add_u32 s27, s27, 1\n"
      "v_fmac_f32 v149, v193, v193\n"
      "v_fmac_f32 v149, v194, v194\n"
      "v_mfma_f32_16x16x32_bf16 v[144:147], v[212:215], v[54:57], v[144:147]\n"
      "ds_write_b64 v125, v[248:249] offset:18688\n"
      "v_mfma_f32_16x16x32_bf16 v[248:251], v[232:235], v[160:163], 0\n"
      "s_cmp_ge_u32 s27, s26\n"
      "v_fmac_f32 v149, v195, v195\n"
      "v_fmac_f32 v149, v196, v196\n"
      "v_mfma_f32_16x16x32_bf16 v[248:251], v[212:215], v[58:61], v[248:251]\n"
      "v_fmac_f32 v149, v197, v197\n"
      "v_fmac_f32 v149, v198, v198\n"
      "v_fmac_f32 v149, v199, v199\n"
      "v_fmac_f32 v149, v200, v200\n"
      "v_fmac_f32 v149, v201, v201\n"
      "v_permlane16_swap_b32 v151, v148\n"
      "v_mov_b32_dpp v204, v204 quad_perm:[0,0,0,0] row_mask:0xf bank_mask:0xf\n"
      "v_add_f32 v151, v151, v148\n"
      "v_rcp_f32 v204, v204\n"
      "v_fmac_f32_e64 v149, v202, v202\n"
      "v_mfma_f32_16x16x32_bf16 v[212:215], a[4:7], v[208:211], 0\n"
      "s_cselect_b32 s28, 1, 0\n"
      "v_fmac_f32 v149, v203, v203\n"
      "v_mov_b32 v150, v149\n"
      "v_mfma_f32_16x16x32_bf16 v[212:215], a[40:43], v[216:219], v[212:215]\n"
      "s_xor_b32 s29, s10, s11\n"
      "v_permlane16_swap_b32 v149, v150\n"
      "v_cndmask_b32 v164, v164, v144, s[42:43]\n"
      "v_mfma_f32_16x16x32_bf16 v[160:163], a[0:3], v[208:211], 0\n"
      "s_or_b32 s28, s28, s29\n"
      "v_add_f32_e64 v149, v149, v150\n"
      "v_cndmask_b32 v164, v164, v248, s[44:45]\n"
      "v_mul_f32_e64 v164, v204, v164\n"
      "v_permlane32_swap_b32_e64 v151, v149\n"
      "v_cvt_pk_bf16_f32 v164, v164, v164\n"
      "v_add_f32 v151, v151, v149\n"
      "v_mfma_f32_16x16x32_bf16 v[160:163], a[36:39], v[216:219], v[160:163]\n"
      "ds_write_b32 v129, v151 offset:1024\n"
      "ds_write_b16 v135, v164 offset:16\n"
      "L_join_21_%=:\n"
      "s_cbranch_scc1 L_tail1_%=\n"
      "L_mid_%=:\n"
      "v_mfma_f32_16x16x32_bf16 v[164:167], a[8:11], v[208:211], 0\n"
      "ds_read_b128 v[144:147], v114 offset:6144\n"
      "s_add_u32 m0, s76, 18432\n"
      "v_mfma_f32_16x16x32_bf16 v[164:167], a[44:47], v[216:219], v[164:167]\n"
      "ds_read_b128 v[148:151], v115 offset:6144\n"
      "v_rcp_f32 v136, v136\n"
      "v_mfma_f32_16x16x32_bf16 v[248:251], a[12:15], v[208:211], 0\n"
      "v_mul_f32 v140, v136, v140\n"
      "v_mul_f32_e64 v141, v136, v141\n"
      "v_mfma_f32_16x16x32_bf16 v[248:251], a[48:51], v[216:219], v[248:251]\n"
      "ds_read_b128 v[232:235], v114 offset:12288\n"
      "v_cvt_pk_bf16_f32 v140, v140, v141\n"
      "v_mfma_f32_16x16x32_bf16 v[224:227], a[16:19], v[208:211], 0\n"
      "v_mul_f32 v143, v136, v143\n"
      "v_mul_f32 v142, v136, v142\n"
      "v_mfma_f32_16x16x32_bf16 v[224:227], a[52:55], v[216:219], v[224:227]\n"
      "s_waitcnt lgkmcnt(3)\n"
      "s_barrier\n"
      "v_mfma_f32_16x16x32_bf16 v[220:223], a[20:23], v[208:211], 0\n"
      "ds_read_b128 v[204:207], v128 offset:1024\n"
      "v_cvt_pk_bf16_f32 v141, v142, v143\n"
      "v_mfma_f32_16x16x32_bf16 v[220:223], a[56:59], v[216:219], v[220:223]\n"
      "global_load_lds_dwordx4 v116, s[52:53]\n"
      "v_mfma_f32_16x16x32_bf16 v[236:239], v[208:211], a[24:27], 0\n"
      "v_mul_f32 v153, v136, v153\n"
      "v_mul_f32 v152, v136, v152\n"
      "v_mfma_f32_16x16x32_bf16 v[236:239], v[216:219], a[60:63], v[236:239]\n"
      "v_mul_f32_e64 v154, v136, v154\n"
      "v_cvt_pk_bf16_f32 v142, v152, v153\n"
      "v_mfma_f32_16x16x32_bf16 v[244:247], v[208:211], a[28:31], 0\n"
      "v_mul_f32_e64 v155, v136, v155\n"
      "v_cvt_pk_bf16_f32 v143, v154, v155\n"
      "v_mfma_f32_16x16x32_bf16 v[244:247], v[216:219], a[64:67], v[244:247]\n"
      "ds_write_b128 v124, v[140:143] offset:18944\n"
      "v_mfma_f32_16x16x32_bf16 v[240:243], v[208:211], a[32:35], 0\n"
      "ds_read_b128 v[140:143], v115 offset:12288\n"
      "v_mul_f32 v156, v136, v156\n"
      "v_mfma_f32_16x16x32_bf16 v[240:243], v[216:219], a[68:71], v[240:243]\n"
      "s_waitcnt lgkmcnt(3)\n"
      "v_mul_f32_e64 v157, v136, v157\n"
      "v_cvt_pk_bf16_f32 v156, v156, v157\n"
      "v_mfma_f32_16x16x32_bf16 v[160:163], a[72:75], v[144:147], v[160:163]\n"
      "s_add_u32 m0, s76, 24448\n"
      "v_mul_f32 v158, v136, v158\n"
      "v_mul_f32_e64 v159, v136, v159\n"
      "v_mfma_f32_16x16x32_bf16 v[160:163], a[108:111], v[148:151], v[160:163]\n"
      "global_load_lds_dwordx4 v116, s[52:53] offset:128\n"
      "v_mfma_f32_16x16x32_bf16 v[212:215], a[76:79], v[144:147], v[212:215]\n"
      "s_waitcnt lgkmcnt(2)\n"
      "v_add_f32 v204, |v204|, |v205|\n"
      "v_cvt_pk_bf16_f32 v157, v158, v159\n"
      "v_mfma_f32_16x16x32_bf16 v[212:215], a[112:115], v[148:151], v[212:215]\n"
      "ds_write_b64 v125, v[156:157] offset:18944\n"
      "v_mfma_f32_16x16x32_bf16 v[164:167], a[80:83], v[144:147], v[164:167]\n"
      "s_add_u32 m0, s76, 30464\n"
      "v_add_f32 v206, |v206|, |v207|\n"
      "v_add_f32 v204, v204, v206\n"
      "v_mfma_f32_16x16x32_bf16 v[164:167], a[116:119], v[148:151], v[164:167]\n"
      "v_rsq_f32 v204, v204\n"
      "v_mfma_f32_16x16x32_bf16 v[248:251], a[84:87], v[144:147], v[248:251]\n"
      "v_mul_f32 v204, s41, v204\n"
      "v_mov_b32 v156, v204\n"
      "v_mfma_f32_16x16x32_bf16 v[248:251], a[120:123], v[148:151], v[248:251]\n"
      "s_nop 0\n"
      "v_permlane32_swap_b32 v204, v156\n"
      "v_mfma_f32_16x16x32_bf16 v[224:227], a[88:91], v[144:147], v[224:227]\n"
      "ds_read_b128 v[136:139], v114 offset:2048\n"
      "v_mov_b32_e64 v157, v204\n"
      "v_mfma_f32_16x16x32_bf16 v[224:227], a[124:127], v[148:151], v[224:227]\n"
      "global_load_lds_dwordx4 v116, s[52:53] offset:256\n"
      "v_mfma_f32_16x16x32_bf16 v[220:223], a[92:95], v[144:147], v[220:223]\n"
      "s_add_u32 m0, s77, 18432\n"
      "v_permlane16_swap_b32_e64 v204, v157\n"
      "v_mfma_f32_16x16x32_bf16 v[220:223], a[128:131], v[148:151], v[220:223]\n"
      "ds_read_b128 v[152:155], v115 offset:2048\n"
      "v_mul_f32 v168, v204, v168\n"
      "v_mfma_f32_16x16x32_bf16 v[236:239], v[144:147], a[96:99], v[236:239]\n"
      "s_waitcnt lgkmcnt(2)\n"
      "v_mul_f32 v169, v204, v169\n"
      "v_cvt_pk_bf16_f32 v168, v168, v169\n"
      "v_mfma_f32_16x16x32_bf16 v[236:239], v[148:151], a[132:135], v[236:239]\n"
      "v_mul_f32 v170, v204, v170\n"
      "v_mul_f32_e64 v171, v204, v171\n"
      "v_mfma_f32_16x16x32_bf16 v[244:247], v[144:147], a[100:103], v[244:247]\n"
      "v_cvt_pk_bf16_f32 v169, v170, v171\n"
      "v_mul_f32 v172, v204, v172\n"
      "v_mfma_f32_16x16x32_bf16 v[244:247], v[148:151], a[136:139], v[244:247]\n"
      "v_mul_f32 v173, v204, v173\n"
      "v_cvt_pk_bf16_f32 v170, v172, v173\n"
      "v_mfma_f32_16x16x32_bf16 v[240:243], v[144:147], a[104:107], v[240:243]\n"
      "v_mul_f32 v175, v204, v175\n"
      "v_mul_f32 v174, v204, v174\n"
      "v_mfma_f32_16x16x32_bf16 v[240:243], v[148:151], a[140:143], v[240:243]\n"
      "v_cvt_pk_bf16_f32 v171, v174, v175\n"
      "v_mul_f32_e64 v176, v204, v176\n"
      "v_mfma_f32_16x16x32_bf16 v[160:163], a[144:147], v[232:235], v[160:163]\n"
      "global_load_lds_dwordx4 v117, s[52:53]\n"
      "v_mfma_f32_16x16x32_bf16 v[160:163], a[180:183], v[140:143], v[160:163]\n"
      "v_mul_f32 v177, v204, v177\n"
      "v_cvt_pk_bf16_f32 v176, v176, v177\n"
      "v_mfma_f32_16x16x32_bf16 v[212:215], a[148:151], v[232:235], v[212:215]\n"
      "s_waitcnt lgkmcnt(0)\n"
      "v_mul_f32 v179, v204, v179\n"
      "v_mul_f32 v178, v204, v178\n"
      "v_mfma_f32_16x16x32_bf16 v[212:215], a[184:187], v[140:143], v[212:215]\n"
      "ds_write_b128 v126, v[168:171]\n"
      "v_mfma_f32_16x16x32_bf16 v[164:167], a[152:155], v[232:235], v[164:167]\n"
      "v_cvt_pk_bf16_f32 v177, v178, v179\n"
      "v_mul_f32 v180, v157, v180\n"
      "v_mfma_f32_16x16x32_bf16 v[164:167], a[188:191], v[140:143], v[164:167]\n"
      "v_mul_f32 v181, v157, v181\n"
      "v_cvt_pk_bf16_f32 v180, v180, v181\n"
      "v_mfma_f32_16x16x32_bf16 v[248:251], a[156:159], v[232:235], v[248:251]\n"
      "s_add_u32 m0, s78, 18432\n"
      "v_mul_f32 v183, v157, v183\n"
      "v_mul_f32 v182, v157, v182\n"
      "v_mfma_f32_16x16x32_bf16 v[248:251], a[192:195], v[140:143], v[248:251]\n"
      "v_mul_f32 v184, v157, v184\n"
      "v_cvt_pk_bf16_f32 v181, v182, v183\n"
      "v_mfma_f32_16x16x32_bf16 v[224:227], a[160:163], v[232:235], v[224:227]\n"
      "ds_read_b128 v[168:171], v114 offset:8192\n"
      "v_mul_f32 v185, v157, v185\n"
      "v_mfma_f32_16x16x32_bf16 v[224:227], a[196:199], v[140:143], v[224:227]\n"
      "ds_read_b128 v[172:175], v115 offset:8192\n"
      "v_cvt_pk_bf16_f32 v182, v184, v185\n"
      "v_mfma_f32_16x16x32_bf16 v[220:223], a[164:167], v[232:235], v[220:223]\n"
      "v_mul_f32 v187, v157, v187\n"
      "v_mul_f32 v186, v157, v186\n"
      "v_mfma_f32_16x16x32_bf16 v[220:223], a[200:203], v[140:143], v[220:223]\n"
      "s_mov_b32 exec_hi, 0\n"
      "global_load_lds_dwordx4 v118, s[52:53]\n"
      "s_mov_b32 exec_hi, -1\n"
      "v_mfma_f32_16x16x32_bf16 v[236:239], v[232:235], a[168:171], v[236:239]\n"
      "v_cvt_pk_bf16_f32 v183, v186, v187\n"
      "v_mul_f32_e64 v188, v157, v188\n"
      "v_mfma_f32_16x16x32_bf16 v[236:239], v[140:143], a[204:207], v[236:239]\n"
      "ds_write_b128 v126, v[180:183] offset:2048\n"
      "v_mfma_f32_16x16x32_bf16 v[244:247], v[232:235], a[172:175], v[244:247]\n"
      "v_mul_f32_e64 v189, v157, v189\n"
      "v_cvt_pk_bf16_f32 v188, v188, v189\n"
      "v_mfma_f32_16x16x32_bf16 v[244:247], v[140:143], a[208:211], v[244:247]\n"
      "s_add_u32 m0, s76, 115456\n"
      "v_mul_f32 v190, v157, v190\n"
      "v_mul_f32 v191, v157, v191\n"
      "v_mfma_f32_16x16x32_bf16 v[240:243], v[232:235], a[176:179], v[240:243]\n"
      "s_waitcnt lgkmcnt(1)\n"
      "v_mul_f32 v192, v156, v192\n"
      "v_cvt_pk_bf16_f32 v189, v190, v191\n"
      "v_mfma_f32_16x16x32_bf16 v[240:243], v[140:143], a[212:215], v[240:243]\n"
      "ds_write2st64_b64 v127, v[176:177], v[188:189] offset0:0 offset1:4\n"
      "v_mfma_f32_16x16x32_bf16 v[180:183], a[0:3], v[136:139], 0\n"
      "global_load_lds_dwordx4 v119, s[56:57]\n"
      "v_mfma_f32_16x16x32_bf16 v[180:183], a[36:39], v[152:155], v[180:183]\n"
      "s_add_u32 m0, s79, 4864\n"
      "v_mul_f32 v193, v156, v193\n"
      "v_cvt_pk_bf16_f32 v192, v192, v193\n"
      "v_mfma_f32_16x16x32_bf16 v[176:179], a[4:7], v[136:139], 0\n"
      "s_sub_u32 s28, s27, 3\n"
      "v_mul_f32 v195, v156, v195\n"
      "v_mul_f32 v194, v156, v194\n"
      "v_mfma_f32_16x16x32_bf16 v[176:179], a[40:43], v[152:155], v[176:179]\n"
      "v_cvt_pk_bf16_f32 v193, v194, v195\n"
      "v_mul_f32 v196, v156, v196\n"
      "v_mfma_f32_16x16x32_bf16 v[184:187], a[8:11], v[136:139], 0\n"
      "s_cselect_b64 s[54:55], s[12:13], s[22:23]\n"
      "v_mul_f32_e64 v197, v156, v197\n"
      "v_cvt_pk_bf16_f32 v194, v196, v197\n"
      "v_mfma_f32_16x16x32_bf16 v[184:187], a[44:47], v[152:155], v[184:187]\n"
      "v_mul_f32_e64 v198, v156, v198\n"
      "v_mul_f32_e64 v199, v156, v199\n"
      "v_mfma_f32_16x16x32_bf16 v[140:143], a[12:15], v[136:139], 0\n"
      "ds_read_b128 v[188:191], v114 offset:14336\n"
      "v_cvt_pk_bf16_f32 v195, v198, v199\n"
      "v_mfma_f32_16x16x32_bf16 v[140:143], a[48:51], v[152:155], v[140:143]\n"
      "ds_write_b128 v126, v[192:195] offset:4096\n"
      "v_mfma_f32_16x16x32_bf16 v[192:195], a[16:19], v[136:139], 0\n"
      "global_load_lds_dword v122, s[58:59]\n"
      "v_mfma_f32_16x16x32_bf16 v[192:195], a[52:55], v[152:155], v[192:195]\n"
      "ds_read_b128 v[196:199], v115 offset:14336\n"
      "v_mul_f32 v200, v156, v200\n"
      "v_mfma_f32_16x16x32_bf16 v[144:147], a[20:23], v[136:139], 0\n"
      "s_max_i32 m0, s28, 0\n"
      "v_mul_f32 v201, v156, v201\n"
      "v_cvt_pk_bf16_f32 v200, v200, v201\n"
      "v_mfma_f32_16x16x32_bf16 v[144:147], a[56:59], v[152:155], v[144:147]\n"
      "s_movrels_b32 s9, s80\n"
      "v_mul_f32_e64 v202, v156, v202\n"
      "v_mul_f32 v203, v156, v203\n"
      "v_mfma_f32_16x16x32_bf16 v[156:159], v[136:139], a[24:27], 0\n"
      "s_lshr_b32 s8, s9, 8\n"
      "v_cvt_pk_bf16_f32 v201, v202, v203\n"
      "v_fma_f32 v232, v160, v160, s38\n"
      "v_mfma_f32_16x16x32_bf16 v[156:159], v[152:155], a[60:63], v[156:159]\n"
      "ds_write_b64 v127, v[200:201] offset:4096\n"
      "v_mfma_f32_16x16x32_bf16 v[200:203], v[136:139], a[28:31], 0\n"
      "s_and_b32 s9, s9, 0xff\n"
      "v_fma_f32 v233, v248, v248, s38\n"
      "v_fmac_f32 v232, v161, v161\n"
      "v_mfma_f32_16x16x32_bf16 v[200:203], v[152:155], a[64:67], v[200:203]\n"
      "s_mul_i32 s28, s8, 1373568\n"
      "v_fmac_f32 v233, v249, v249\n"
      "v_fmac_f32 v232, v162, v162\n"
      "v_mfma_f32_16x16x32_bf16 v[148:151], v[136:139], a[32:35], 0\n"
      "s_mul_i32 s29, s9, 2688\n"
      "v_fmac_f32 v232, v163, v163\n"
      "v_fmac_f32_e64 v233, v250, v250\n"
      "v_mfma_f32_16x16x32_bf16 v[148:151], v[152:155], a[68:71], v[148:151]\n"
      "s_waitcnt lgkmcnt(1)\n"
      "v_fmac_f32 v232, v212, v212\n"
      "v_fmac_f32 v233, v251, v251\n"
      "v_mfma_f32_16x16x32_bf16 v[180:183], a[72:75], v[168:171], v[180:183]\n"
      "s_add_u32 s28, s28, s29\n"
      "v_fmac_f32 v232, v213, v213\n"
      "v_fmac_f32 v233, v224, v224\n"
      "v_mfma_f32_16x16x32_bf16 v[180:183], a[108:111], v[172:175], v[180:183]\n"
      "s_cmp_eq_u32 s8, 72\n"
      "v_fmac_f32 v232, v214, v214\n"
      "v_fmac_f32 v233, v225, v225\n"
      "v_mfma_f32_16x16x32_bf16 v[176:179], a[76:79], v[168:171], v[176:179]\n"
      "s_cselect_b32 s8, 0, 100270464\n"
      "v_fmac_f32 v232, v215, v215\n"
      "v_fmac_f32 v233, v226, v226\n"
      "v_mfma_f32_16x16x32_bf16 v[176:179], a[112:115], v[172:175], v[176:179]\n"
      "s_cmp_eq_u32 s9, 72\n"
      "v_fmac_f32 v232, v164, v164\n"
      "v_fmac_f32 v233, v227, v227\n"
      "v_mfma_f32_16x16x32_bf16 v[184:187], a[80:83], v[168:171], v[184:187]\n"
      "ds_read_b128 v[136:139], v114 offset:4096\n"
      "v_fmac_f32 v233, v220, v220\n"
      "v_mfma_f32_16x16x32_bf16 v[184:187], a[116:119], v[172:175], v[184:187]\n"
      "s_cselect_b32 s9, 0, 196224\n"
      "v_fmac_f32 v232, v165, v165\n"
      "v_fmac_f32_e64 v233, v221, v221\n"
      "v_mfma_f32_16x16x32_bf16 v[140:143], a[84:87], v[168:171], v[140:143]\n"
      "ds_read_b128 v[152:155], v115 offset:4096\n"
      "v_fmac_f32 v232, v166, v166\n"
      "v_mfma_f32_16x16x32_bf16 v[140:143], a[120:123], v[172:175], v[140:143]\n"
      "s_add_u32 s10, s8, s9\n"
      "v_fmac_f32 v233, v222, v222\n"
      "v_fmac_f32 v232, v167, v167\n"
      "v_mfma_f32_16x16x32_bf16 v[192:195], a[88:91], v[168:171], v[192:195]\n"
      "s_add_u32 s28, s28, s10\n"
      "v_fmac_f32 v233, v223, v223\n"
      "v_cvt_pk_bf16_f32 v236, v236, v237\n"
      "v_mfma_f32_16x16x32_bf16 v[192:195], a[124:127], v[172:175], v[192:195]\n"
      "s_add_u32 s54, s54, s28\n"
      "v_permlane16_swap_b32 v232, v233\n"
      "v_mfma_f32_16x16x32_bf16 v[144:147], a[92:95], v[168:171], v[144:147]\n"
      "s_addc_u32 s55, s55, 0\n"
      "v_add_f32 v232, v232, v233\n"
      "v_mov_b32 v233, v232\n"
      "v_mfma_f32_16x16x32_bf16 v[144:147], a[128:131], v[172:175], v[144:147]\n"
      "s_cmp_lg_u32 s10, s11\n"
      "v_permlane32_swap_b32 v232, v233\n"
      "v_mfma_f32_16x16x32_bf16 v[156:159], v[168:171], a[96:99], v[156:159]\n"
      "s_cbranch_scc1 L_edge_26_%=\n"
      "L_eb_27_%=:\n"
      "v_add_f32 v232, v232, v233\n"
      "v_mfma_f32_16x16x32_bf16 v[156:159], v[172:175], a[132:135], v[156:159]\n"
      "v_rsq_f32_e64 v232, v232\n"
      "v_mfma_f32_16x16x32_bf16 v[200:203], v[168:171], a[100:103], v[200:203]\n"
      "s_mov_b32 s5, s4\n"
      "v_cvt_pk_bf16_f32 v237, v238, v239\n"
      "v_mov_b32 v233, v232\n"
      "v_mfma_f32_16x16x32_bf16 v[200:203], v[172:175], a[136:139], v[200:203]\n"
      "s_add_u32 s4, s27, 1\n"
      "v_permlane16_swap_b32_e64 v232, v233\n"
      "v_mfma_f32_16x16x32_bf16 v[148:151], v[168:171], a[104:107], v[148:151]\n"
      "s_lshl_b32 s4, s4, 30\n"
      "v_mul_f32 v249, v233, v249\n"
      "v_mul_f32 v248, v233, v248\n"
      "v_mfma_f32_16x16x32_bf16 v[148:151], v[172:175], a[140:143], v[148:151]\n"
      "v_mul_f32 v250, v233, v250\n"
      "v_cvt_pk_bf16_f32 v248, v248, v249\n"
      "v_mfma_f32_16x16x32_bf16 v[180:183], a[144:147], v[188:191], v[180:183]\n"
      "v_mul_f32_e64 v251, v233, v251\n"
      "v_cvt_pk_bf16_f32 v249, v250, v251\n"
      "v_mfma_f32_16x16x32_bf16 v[180:183], a[180:183], v[196:199], v[180:183]\n"
      "ds_read_b128 v[168:171], v119 offset:18432\n"
      "v_mul_f32 v224, v233, v224\n"
      "v_mfma_f32_16x16x32_bf16 v[176:179], a[148:151], v[188:191], v[176:179]\n"
      "v_mul_f32 v225, v233, v225\n"
      "v_cvt_pk_bf16_f32 v250, v224, v225\n"
      "v_mfma_f32_16x16x32_bf16 v[176:179], a[184:187], v[196:199], v[176:179]\n"
      "s_and_b32 s4, s4, 0x80000000\n"
      "v_mul_f32 v226, v233, v226\n"
      "v_mul_f32 v227, v233, v227\n"
      "v_mfma_f32_16x16x32_bf16 v[184:187], a[152:155], v[188:191], v[184:187]\n"
      "v_mul_f32 v220, v233, v220\n"
      "v_cvt_pk_bf16_f32 v251, v226, v227\n"
      "v_mfma_f32_16x16x32_bf16 v[184:187], a[188:191], v[196:199], v[184:187]\n"
      "v_mul_f32 v221, v233, v221\n"
      "v_cvt_pk_bf16_f32 v34, v220, v221\n"
      "v_mfma_f32_16x16x32_bf16 v[140:143], a[156:159], v[188:191], v[140:143]\n"
      "v_mul_f32 v223, v233, v223\n"
      "v_mul_f32 v222, v233, v222\n"
      "v_mfma_f32_16x16x32_bf16 v[140:143], a[192:195], v[196:199], v[140:143]\n"
      "ds_read_b128 v[172:175], v114 offset:10240\n"
      "v_cvt_pk_bf16_f32 v35, v222, v223\n"
      "v_mfma_f32_16x16x32_bf16 v[192:195], a[160:163], v[188:191], v[192:195]\n"
      "ds_read_b128 v[220:223], v115 offset:10240\n"
      "v_mul_f32 v232, s36, v232\n"
      "v_mfma_f32_16x16x32_bf16 v[192:195], a[196:199], v[196:199], v[192:195]\n"
      "s_waitcnt lgkmcnt(2)\n"
      "v_mul_f32 v161, v232, v161\n"
      "v_mul_f32 v160, v232, v160\n"
      "v_mfma_f32_16x16x32_bf16 v[144:147], a[164:167], v[188:191], v[144:147]\n"
      "global_store_dwordx4 v116, v[168:171], s[54:55] sc1\n"
      "v_mfma_f32_16x16x32_bf16 v[144:147], a[200:203], v[196:199], v[144:147]\n"
      "v_mul_f32_e64 v162, v232, v162\n"
      "v_cvt_pk_bf16_f32 v160, v160, v161\n"
      "v_mfma_f32_16x16x32_bf16 v[156:159], v[188:191], a[168:171], v[156:159]\n"
      "v_mul_f32 v163, v232, v163\n"
      "v_cvt_pk_bf16_f32 v161, v162, v163\n"
      "v_mfma_f32_16x16x32_bf16 v[156:159], v[196:199], a[204:207], v[156:159]\n"
      "v_mul_f32 v212, v232, v212\n"
      "v_mul_f32 v213, v232, v213\n"
      "v_mfma_f32_16x16x32_bf16 v[200:203], v[188:191], a[172:175], v[200:203]\n"
      "v_mul_f32 v214, v232, v214\n"
      "v_cvt_pk_bf16_f32 v162, v212, v213\n"
      "v_mfma_f32_16x16x32_bf16 v[200:203], v[196:199], a[208:211], v[200:203]\n"
      "v_mul_f32 v215, v232, v215\n"
      "v_cvt_pk_bf16_f32 v163, v214, v215\n"
      "v_mfma_f32_16x16x32_bf16 v[148:151], v[188:191], a[176:179], v[148:151]\n"
      "s_waitcnt lgkmcnt(0)\n"
      "v_mul_f32 v164, v232, v164\n"
      "v_mul_f32 v165, v232, v165\n"
      "v_mfma_f32_16x16x32_bf16 v[148:151], v[196:199], a[212:215], v[148:151]\n"
      "v_mul_f32 v166, v232, v166\n"
      "v_cvt_pk_bf16_f32 v32, v164, v165\n"
      "v_mfma_f32_16x16x32_bf16 v[196:199], a[0:3], v[136:139], 0\n"
      "v_mul_f32 v167, v232, v167\n"
      "v_cvt_pk_bf16_f32 v33, v166, v167\n"
      "v_mfma_f32_16x16x32_bf16 v[196:199], a[36:39], v[152:155], v[196:199]\n"
      "v_cvt_pk_bf16_f32 v238, v156, v157\n"
      "v_cvt_pk_bf16_f32 v239, v158, v159\n"
      "v_mfma_f32_16x16x32_bf16 v[188:191], a[4:7], v[136:139], 0\n"
      "v_cvt_pk_bf16_f32 v244, v244, v245\n"
      "v_cvt_pk_bf16_f32 v245, v246, v247\n"
      "v_mfma_f32_16x16x32_bf16 v[188:191], a[40:43], v[152:155], v[188:191]\n"
      "v_cvt_pk_bf16_f32 v247, v202, v203\n"
      "v_cvt_pk_bf16_f32 v246, v200, v201\n"
      "v_mfma_f32_16x16x32_bf16 v[200:203], a[8:11], v[136:139], 0\n"
      "ds_read_b128 v[168:171], v119 offset:24576\n"
      "v_cvt_pk_bf16_f32 v240, v240, v241\n"
      "v_mfma_f32_16x16x32_bf16 v[200:203], a[44:47], v[152:155], v[200:203]\n"
      "v_cvt_pk_bf16_f32 v241, v242, v243\n"
      "v_cvt_pk_bf16_f32 v242, v148, v149\n"
      "v_mfma_f32_16x16x32_bf16 v[156:159], a[12:15], v[136:139], 0\n"
      "v_cvt_pk_bf16_f32 v243, v150, v151\n"
      "v_fma_f32 v208, v180, v180, s38\n"
      "v_mfma_f32_16x16x32_bf16 v[156:159], a[48:51], v[152:155], v[156:159]\n"
      "v_fmac_f32 v208, v181, v181\n"
      "v_fma_f32 v209, v140, v140, s38\n"
      "v_mfma_f32_16x16x32_bf16 v[164:167], a[16:19], v[136:139], 0\n"
      "v_fmac_f32 v209, v141, v141\n"
      "v_fmac_f32_e64 v208, v182, v182\n"
      "v_mfma_f32_16x16x32_bf16 v[164:167], a[52:55], v[152:155], v[164:167]\n"
      "v_fmac_f32 v209, v142, v142\n"
      "v_fmac_f32 v208, v183, v183\n"
      "v_mfma_f32_16x16x32_bf16 v[148:151], a[20:23], v[136:139], 0\n"
      "v_fmac_f32 v208, v176, v176\n"
      "v_fmac_f32 v209, v143, v143\n"
      "v_mfma_f32_16x16x32_bf16 v[148:151], a[56:59], v[152:155], v[148:151]\n"
      "v_fmac_f32 v208, v177, v177\n"
      "v_fmac_f32 v209, v192, v192\n"
      "v_mfma_f32_16x16x32_bf16 v[212:215], v[136:139], a[24:27], 0\n"
      "s_waitcnt lgkmcnt(0)\n"
      "v_fmac_f32 v209, v193, v193\n"
      "v_mfma_f32_16x16x32_bf16 v[212:215], v[152:155], a[60:63], v[212:215]\n"
      "global_store_dwordx4 v116, v[168:171], s[54:55] offset:128 sc1\n"
      "v_mfma_f32_16x16x32_bf16 v[224:227], v[136:139], a[28:31], 0\n"
      "ds_read_b128 v[232:235], v114 offset:16384\n"
      "v_fmac_f32 v208, v178, v178\n"
      "v_mfma_f32_16x16x32_bf16 v[224:227], v[152:155], a[64:67], v[224:227]\n"
      "v_fmac_f32 v208, v179, v179\n"
      "v_fmac_f32 v209, v194, v194\n"
      "v_mfma_f32_16x16x32_bf16 v[204:207], v[136:139], a[32:35], 0\n"
      "ds_read_b128 v[168:171], v115 offset:16384\n"
      "v_fmac_f32 v209, v195, v195\n"
      "v_mfma_f32_16x16x32_bf16 v[204:207], v[152:155], a[68:71], v[204:207]\n"
      "v_fmac_f32 v208, v184, v184\n"
      "v_fmac_f32 v209, v144, v144\n"
      "v_mfma_f32_16x16x32_bf16 v[196:199], a[72:75], v[172:175], v[196:199]\n"
      "v_fmac_f32 v208, v185, v185\n"
      "v_fmac_f32 v209, v145, v145\n"
      "v_mfma_f32_16x16x32_bf16 v[196:199], a[108:111], v[220:223], v[196:199]\n"
      "v_fmac_f32 v208, v186, v186\n"
      "v_fmac_f32 v209, v146, v146\n"
      "v_mfma_f32_16x16x32_bf16 v[188:191], a[76:79], v[172:175], v[188:191]\n"
      "v_fmac_f32 v208, v187, v187\n"
      "v_fmac_f32 v209, v147, v147\n"
      "v_mfma_f32_16x16x32_bf16 v[188:191], a[112:115], v[220:223], v[188:191]\n"
      "s_nop 0\n"
      "v_permlane16_swap_b32 v208, v209\n"
      "v_mfma_f32_16x16x32_bf16 v[200:203], a[80:83], v[172:175], v[200:203]\n"
      "v_add_f32 v208, v208, v209\n"
      "v_mov_b32 v209, v208\n"
      "v_mfma_f32_16x16x32_bf16 v[200:203], a[116:119], v[220:223], v[200:203]\n"
      "s_nop 0\n"
      "v_permlane32_swap_b32 v208, v209\n"
      "v_mfma_f32_16x16x32_bf16 v[156:159], a[84:87], v[172:175], v[156:159]\n"
      "v_add_f32 v208, v208, v209\n"
      "v_mfma_f32_16x16x32_bf16 v[156:159], a[120:123], v[220:223], v[156:159]\n"
      "s_waitcnt lgkmcnt(0)\n"
      "v_rsq_f32 v208, v208\n"
      "v_mfma_f32_16x16x32_bf16 v[164:167], a[88:91], v[172:175], v[164:167]\n"
      "v_mov_b32 v209, v208\n"
      "v_add_u32 v135, 32, v135\n"
      "v_mfma_f32_16x16x32_bf16 v[164:167], a[124:127], v[220:223], v[164:167]\n"
      "v_permlane16_swap_b32 v208, v209\n"
      "v_mfma_f32_16x16x32_bf16 v[148:151], a[92:95], v[172:175], v[148:151]\n"
      "v_mul_f32 v140, v209, v140\n"
      "v_mul_f32 v141, v209, v141\n"
      "v_mfma_f32_16x16x32_bf16 v[148:151], a[128:131], v[220:223], v[148:151]\n"
      "ds_read_b128 v[136:139], v132 offset:32\n"
      "v_cvt_pk_bf16_f32 v140, v140, v141\n"
      "v_mfma_f32_16x16x32_bf16 v[212:215], v[172:175], a[96:99], v[212:215]\n"
      "v_mul_f32 v142, v209, v142\n"
      "v_mul_f32 v143, v209, v143\n"
      "v_mfma_f32_16x16x32_bf16 v[212:215], v[220:223], a[132:135], v[212:215]\n"
      "ds_read_b64 v[152:153], v132 offset:24\n"
      "v_cvt_pk_bf16_f32 v141, v142, v143\n"
      "v_mfma_f32_16x16x32_bf16 v[224:227], v[172:175], a[100:103], v[224:227]\n"
      "ds_read_u16 v52, v134\n"
      "v_mul_f32 v192, v209, v192\n"
      "v_mfma_f32_16x16x32_bf16 v[224:227], v[220:223], a[136:139], v[224:227]\n"
      "v_mul_f32 v194, v209, v194\n"
      "v_mul_f32_e64 v193, v209, v193\n"
      "v_mfma_f32_16x16x32_bf16 v[204:207], v[172:175], a[104:107], v[204:207]\n"
      "ds_read_b128 v[216:219], v133\n"
      "v_cvt_pk_bf16_f32 v142, v192, v193\n"
      "v_mfma_f32_16x16x32_bf16 v[204:207], v[220:223], a[140:143], v[204:207]\n"
      "ds_read_b64 v[154:155], v133 offset:16\n"
      "v_mfma_f32_16x16x32_bf16 v[196:199], a[144:147], v[232:235], v[196:199]\n"
      "ds_read_u16 v60, v134 offset:64\n"
      "v_mul_f32_e64 v195, v209, v195\n"
      "v_mfma_f32_16x16x32_bf16 v[196:199], a[180:183], v[168:171], v[196:199]\n"
      "ds_read_b128 v[220:223], v123 offset:18432\n"
      "v_cvt_pk_bf16_f32 v143, v194, v195\n"
      "v_mfma_f32_16x16x32_bf16 v[188:191], a[148:151], v[232:235], v[188:191]\n"
      "v_mul_f32 v144, v209, v144\n"
      "v_mul_f32 v145, v209, v145\n"
      "v_mfma_f32_16x16x32_bf16 v[188:191], a[184:187], v[168:171], v[188:191]\n"
      "ds_read_b128 v[192:195], v123 offset:21504\n"
      "v_mul_f32_e64 v146, v209, v146\n"
      "v_mfma_f32_16x16x32_bf16 v[200:203], a[152:155], v[232:235], v[200:203]\n"
      "ds_read_u16 v56, v134 offset:32\n"
      "v_cvt_pk_bf16_f32 v40, v144, v145\n"
      "v_mfma_f32_16x16x32_bf16 v[200:203], a[188:191], v[168:171], v[200:203]\n"
      "v_mul_f32 v208, s36, v208\n"
      "v_mul_f32 v181, v208, v181\n"
      "v_mfma_f32_16x16x32_bf16 v[156:159], a[156:159], v[232:235], v[156:159]\n"
      "v_mul_f32 v147, v209, v147\n"
      "v_mul_f32 v182, v208, v182\n"
      "v_mfma_f32_16x16x32_bf16 v[156:159], a[192:195], v[168:171], v[156:159]\n"
      "v_mul_f32 v180, v208, v180\n"
      "v_cvt_pk_bf16_f32 v180, v180, v181\n"
      "v_mfma_f32_16x16x32_bf16 v[164:167], a[160:163], v[232:235], v[164:167]\n"
      "v_mul_f32 v178, v208, v178\n"
      "v_mul_f32 v183, v208, v183\n"
      "v_mfma_f32_16x16x32_bf16 v[164:167], a[196:199], v[168:171], v[164:167]\n"
      "v_cvt_pk_bf16_f32 v41, v146, v147\n"
      "v_mul_f32 v176, v208, v176\n"
      "v_mfma_f32_16x16x32_bf16 v[148:151], a[164:167], v[232:235], v[148:151]\n"
      "ds_read_b128 v[144:147], v123 offset:24576\n"
      "v_cvt_pk_bf16_f32 v181, v182, v183\n"
      "v_mfma_f32_16x16x32_bf16 v[148:151], a[200:203], v[168:171], v[148:151]\n"
      "v_mul_f32 v177, v208, v177\n"
      "v_mul_f32 v179, v208, v179\n"
      "v_mfma_f32_16x16x32_bf16 v[212:215], v[232:235], a[168:171], v[212:215]\n"
      "s_waitcnt lgkmcnt(2)\n"
      "v_cvt_pk_bf16_f32 v182, v176, v177\n"
      "v_mul_f32 v184, v208, v184\n"
      "v_mfma_f32_16x16x32_bf16 v[212:215], v[168:171], a[204:207], v[212:215]\n"
      "ds_read_b128 v[228:231], v123 offset:27648\n"
      "v_mul_f32 v187, v208, v187\n"
      "v_mfma_f32_16x16x32_bf16 v[224:227], v[232:235], a[172:175], v[224:227]\n"
      "v_mul_f32 v185, v208, v185\n"
      "v_cvt_pk_bf16_f32 v38, v184, v185\n"
      "v_mfma_f32_16x16x32_bf16 v[224:227], v[168:171], a[208:211], v[224:227]\n"
      "s_or_b64 s[34:35], s[48:49], s[50:51]\n"
      "v_mul_f32 v186, v208, v186\n"
      "v_mfma_f32_16x16x32_bf16 v[204:207], v[232:235], a[176:179], v[204:207]\n"
      "v_cvt_pk_bf16_f32 v183, v178, v179\n"
      "v_cvt_pk_bf16_f32 v39, v186, v187\n"
      "v_mfma_f32_16x16x32_bf16 v[204:207], v[168:171], a[212:215], v[204:207]\n"
      "v_fma_f32 v233, v156, v156, s38\n"
      "v_fma_f32 v232, v196, v196, s38\n"
      "v_mfma_f32_16x16x32_bf16 v[168:171], a[216:219], v[220:223], 0\n"
      "v_fmac_f32 v232, v197, v197\n"
      "v_fmac_f32 v233, v157, v157\n"
      "v_mfma_f32_16x16x32_bf16 v[168:171], a[228:231], v[192:195], v[168:171]\n"
      "v_fmac_f32 v232, v198, v198\n"
      "v_fmac_f32 v233, v158, v158\n"
      "v_mfma_f32_16x16x32_bf16 v[172:175], a[220:223], v[220:223], 0\n"
      "v_fmac_f32 v233, v159, v159\n"
      "v_fmac_f32 v232, v199, v199\n"
      "v_mfma_f32_16x16x32_bf16 v[172:175], a[232:235], v[192:195], v[172:175]\n"
      "v_fmac_f32 v233, v164, v164\n"
      "v_fmac_f32 v232, v188, v188\n"
      "v_mfma_f32_16x16x32_bf16 v[176:179], a[224:227], v[220:223], 0\n"
      "v_fmac_f32 v233, v165, v165\n"
      "v_fmac_f32 v232, v189, v189\n"
      "v_mfma_f32_16x16x32_bf16 v[176:179], a[236:239], v[192:195], v[176:179]\n"
      "s_waitcnt lgkmcnt(0)\n"
      "v_fmac_f32 v232, v190, v190\n"
      "v_fmac_f32 v233, v166, v166\n"
      "v_mfma_f32_16x16x32_bf16 v[168:171], a[240:243], v[144:147], v[168:171]\n"
      "ds_read_b128 v[184:187], v123 offset:30720\n"
      "v_fmac_f32 v232, v191, v191\n"
      "v_mfma_f32_16x16x32_bf16 v[168:171], a[252:255], v[228:231], v[168:171]\n"
      "ds_read_b128 v[192:195], v123 offset:33792\n"
      "v_fmac_f32 v233, v167, v167\n"
      "v_mfma_f32_16x16x32_bf16 v[172:175], a[244:247], v[144:147], v[172:175]\n"
      "v_fmac_f32 v233, v148, v148\n"
      "v_fmac_f32_e64 v232, v200, v200\n"
      "v_mfma_f32_16x16x32_bf16 v[172:175], v[0:3], v[228:231], v[172:175]\n"
      "v_fmac_f32 v233, v149, v149\n"
      "v_fmac_f32 v232, v201, v201\n"
      "v_mfma_f32_16x16x32_bf16 v[176:179], a[248:251], v[144:147], v[176:179]\n"
      "v_fmac_f32 v233, v150, v150\n"
      "v_fmac_f32 v232, v202, v202\n"
      "v_mfma_f32_16x16x32_bf16 v[176:179], v[4:7], v[228:231], v[176:179]\n"
      "v_cvt_pk_bf16_f32 v50, v212, v213\n"
      "v_fmac_f32 v233, v151, v151\n"
      "v_fmac_f32 v232, v203, v203\n"
      "v_mfma_f32_16x16x32_bf16 v[144:147], v[248:251], v[160:163], v[78:81]\n"
      "s_nop 0\n"
      "v_permlane16_swap_b32 v232, v233\n"
      "v_mfma_f32_16x16x32_bf16 v[144:147], v[34:37], v[32:35], v[144:147]\n"
      "v_add_f32 v232, v232, v233\n"
      "v_mov_b32 v233, v232\n"
      "v_mfma_f32_16x16x32_bf16 v[228:231], v[140:143], v[160:163], v[82:85]\n"
      "s_nop 0\n"
      "v_permlane32_swap_b32 v232, v233\n"
      "v_mfma_f32_16x16x32_bf16 v[228:231], v[40:43], v[32:35], v[228:231]\n"
      "s_waitcnt lgkmcnt(0)\n"
      "v_add_f32 v232, v232, v233\n"
      "v_rsq_f32_e64 v232, v232\n"
      "v_mfma_f32_16x16x32_bf16 v[168:171], v[8:11], v[184:187], v[168:171]\n"
      "v_cvt_pk_bf16_f32 v51, v214, v215\n"
      "v_mov_b32 v233, v232\n"
      "v_mfma_f32_16x16x32_bf16 v[168:171], v[20:23], v[192:195], v[168:171]\n"
      "s_nop 0\n"
      "v_permlane16_swap_b32 v232, v233\n"
      "v_mfma_f32_16x16x32_bf16 v[172:175], v[12:15], v[184:187], v[172:175]\n"
      "v_mul_f32 v157, v233, v157\n"
      "v_mul_f32 v156, v233, v156\n"
      "v_mfma_f32_16x16x32_bf16 v[172:175], v[24:27], v[192:195], v[172:175]\n"
      "v_mul_f32 v148, v233, v148\n"
      "v_cvt_pk_bf16_f32 v156, v156, v157\n"
      "v_mul_f32 v158, v233, v158\n"
      "v_mul_f32 v151, v233, v151\n"
      "v_mul_f32 v165, v233, v165\n"
      "v_mul_f32 v232, s36, v232\n"
      "v_mul_f32 v164, v233, v164\n"
      "v_mul_f32 v199, v232, v199\n"
      "v_mfma_f32_16x16x32_bf16 v[176:179], v[16:19], v[184:187], v[176:179]\n"
      "v_mul_f32 v159, v233, v159\n"
      "v_cvt_pk_bf16_f32 v157, v158, v159\n"
      "v_mfma_f32_16x16x32_bf16 v[176:179], v[28:31], v[192:195], v[176:179]\n"
      "v_mul_f32 v149, v233, v149\n"
      "v_mul_f32 v167, v233, v167\n"
      "v_mul_f32 v150, v233, v150\n"
      "v_mul_f32_e64 v166, v233, v166\n"
      "v_cvt_pk_bf16_f32 v159, v166, v167\n"
      "v_cvt_pk_bf16_f32 v47, v150, v151\n"
      "v_cvt_pk_bf16_f32 v158, v164, v165\n"
      "v_cvt_pk_bf16_f32 v46, v148, v149\n"
      "v_mul_f32 v196, v232, v196\n"
      "v_mfma_f32_16x16x32_bf16 v[184:187], v[156:159], v[160:163], v[86:89]\n"
      "s_waitcnt vmcnt(2)\n"
      "v_mul_f32 v197, v232, v197\n"
      "v_cvt_pk_bf16_f32 v196, v196, v197\n"
      "v_mfma_f32_16x16x32_bf16 v[184:187], v[46:49], v[32:35], v[184:187]\n"
      "ds_read_b128 v[192:195], v119 offset:41728\n"
      "v_mul_f32 v202, v232, v202\n"
      "v_mfma_f32_16x16x32_bf16 v[212:215], v[136:139], v[160:163], v[70:73]\n"
      "ds_read_b32 v160, v122 offset:4864\n"
      "v_cvt_pk_bf16_f32 v54, v224, v225\n"
      "v_mfma_f32_16x16x32_bf16 v[212:215], v[216:219], v[248:251], v[212:215]\n"
      "v_mul_f32 v189, v232, v189\n"
      "v_mul_f32 v188, v232, v188\n"
      "v_mfma_f32_16x16x32_bf16 v[212:215], v[152:155], v[32:35], v[212:215]\n"
      "v_cvt_pk_bf16_f32 v59, v206, v207\n"
      "v_mul_f32 v198, v232, v198\n"
      "v_mul_f32 v190, v232, v190\n"
      "v_cvt_pk_bf16_f32 v55, v226, v227\n"
      "v_cvt_pk_bf16_f32 v197, v198, v199\n"
      "v_mul_f32 v203, v232, v203\n"
      "v_mul_f32 v191, v232, v191\n"
      "v_cvt_pk_bf16_f32 v199, v190, v191\n"
      "v_fma_f32 v214, v168, v168, s39\n"
      "v_fmac_f32 v214, v169, v169\n"
      "v_cvt_pk_bf16_f32 v198, v188, v189\n"
      "v_fmac_f32 v214, v170, v170\n"
      "v_fmac_f32_e64 v214, v171, v171\n"
      "v_cvt_pk_bf16_f32 v45, v202, v203\n"
      "v_mul_f32 v201, v232, v201\n"
      "v_fmac_f32 v214, v172, v172\n"
      "v_fmac_f32 v214, v173, v173\n"
      "v_fmac_f32 v214, v174, v174\n"
      "v_fmac_f32 v214, v175, v175\n"
      "v_fmac_f32 v214, v176, v176\n"
      "v_mul_f32_e64 v200, v232, v200\n"
      "v_fmac_f32_e64 v214, v177, v177\n"
      "v_fmac_f32_e64 v214, v178, v178\n"
      "v_cvt_pk_bf16_f32 v58, v204, v205\n"
      "v_fmac_f32 v214, v179, v179\n"
      "s_waitcnt lgkmcnt(0)\n"
      "v_cvt_pk_bf16_f32 v44, v200, v201\n"
      "v_bitop3_b32 v160, v160, v195, s40 bitop3:0xa8\n"
      "v_or3_b32 v192, v192, v193, v194\n"
      "v_and_b32 v192, s40, v192\n"
      "v_or3_b32 v192, v192, v160, s4\n"
      "ds_write_b32 v129, v192 offset:3072\n"
      "s_cbranch_scc1 L_gen_28_%=\n"
      "L_fast_31_%=:\n"
      "v_mfma_f32_16x16x32_bf16 v[192:195], v[248:251], v[180:183], v[90:93]\n"
      "ds_read_b128 v[188:191], v119 offset:30720\n"
      "v_exp_f32 v145, v145\n"
      "v_exp_f32 v187, v187\n"
      "v_exp_f32_e64 v144, v144\n"
      "v_mfma_f32_16x16x32_bf16 v[192:195], v[34:37], v[38:41], v[192:195]\n"
      "ds_read_b128 v[200:203], v123 offset:18688\n"
      "v_exp_f32 v185, v185\n"
      "v_mfma_f32_16x16x32_bf16 v[160:163], v[140:143], v[180:183], v[94:97]\n"
      "ds_read_b128 v[164:167], v123 offset:21760\n"
      "v_exp_f32 v229, v229\n"
      "v_mfma_f32_16x16x32_bf16 v[204:207], v[136:139], v[180:183], v[72:75]\n"
      "ds_read_b128 v[224:227], v123 offset:24832\n"
      "v_cvt_pk_bf16_f32 v144, v144, v145\n"
      "v_mfma_f32_16x16x32_bf16 v[204:207], v[216:219], v[140:143], v[204:207]\n"
      "v_exp_f32 v146, v146\n"
      "v_mfma_f32_16x16x32_bf16 v[204:207], v[152:155], v[38:41], v[204:207]\n"
      "ds_read_b128 v[148:151], v128 offset:3072\n"
      "v_exp_f32 v184, v184\n"
      "v_exp_f32_e64 v147, v147\n"
      "v_mfma_f32_16x16x32_bf16 v[160:163], v[40:43], v[38:41], v[160:163]\n"
      "ds_read_b128 v[220:223], v123 offset:27904\n"
      "v_exp_f32 v228, v228\n"
      "v_exp_f32 v212, v212\n"
      "v_cvt_pk_bf16_f32 v145, v146, v147\n"
      "v_cvt_pk_bf16_f32 v146, v228, v229\n"
      "v_mfma_f32_16x16x32_bf16 v[232:235], v[156:159], v[180:183], v[98:101]\n"
      "v_exp_f32 v186, v186\n"
      "v_mfma_f32_16x16x32_bf16 v[232:235], v[46:49], v[38:41], v[232:235]\n"
      "s_waitcnt lgkmcnt(3)\n"
      "v_exp_f32 v231, v231\n"
      "v_cvt_pk_bf16_f32 v63, v186, v187\n"
      "v_cvt_pk_bf16_f32 v62, v184, v185\n"
      "v_mfma_f32_16x16x32_bf16 v[180:183], a[216:219], v[200:203], 0\n"
      "v_exp_f32 v230, v230\n"
      "v_mfma_f32_16x16x32_bf16 v[180:183], a[228:231], v[164:167], v[180:183]\n"
      "global_store_dwordx4 v116, v[188:191], s[54:55] offset:256 sc1\n"
      "v_cvt_pk_bf16_f32 v64, v212, 0\n"
      "v_mfma_f32_16x16x32_bf16 v[184:187], a[220:223], v[200:203], 0\n"
      "s_waitcnt lgkmcnt(1)\n"
      "v_bitop3_b32 v148, v148, v149, s4 bitop3:0x7e\n"
      "v_bitop3_b32 v150, v150, v151, s4 bitop3:0x7e\n"
      "v_mfma_f32_16x16x32_bf16 v[184:187], a[232:235], v[164:167], v[184:187]\n"
      "s_waitcnt lgkmcnt(0)\n"
      "v_or_b32_e64 v148, v148, v150\n"
      "v_cmp_ne_u32 vcc, 0, v148\n"
      "v_mfma_f32_16x16x32_bf16 v[188:191], a[224:227], v[200:203], 0\n"
      "ds_read_b128 v[200:203], v123 offset:30976\n"
      "s_mov_b64 s[48:49], vcc\n"
      "v_mfma_f32_16x16x32_bf16 v[188:191], a[236:239], v[164:167], v[188:191]\n"
      "ds_read_b128 v[164:167], v123 offset:34048\n"
      "v_exp_f32 v192, v192\n"
      "v_mfma_f32_16x16x32_bf16 v[180:183], a[240:243], v[224:227], v[180:183]\n"
      "s_cbranch_vccnz L_flg_32_%=\n"
      "L_fb_33_%=:\n"
      "v_mfma_f32_16x16x32_bf16 v[180:183], a[252:255], v[220:223], v[180:183]\n"
      "ds_read_b128 v[148:151], v120 offset:18432\n"
      "v_exp_f32 v195, v195\n"
      "v_exp_f32 v193, v193\n"
      "v_mfma_f32_16x16x32_bf16 v[184:187], a[244:247], v[224:227], v[184:187]\n"
      "v_exp_f32 v194, v194\n"
      "v_mfma_f32_16x16x32_bf16 v[184:187], v[0:3], v[220:223], v[184:187]\n"
      "v_cvt_pk_bf16_f32 v192, v192, v193\n"
      "v_exp_f32 v161, v161\n"
      "v_exp_f32_e64 v160, v160\n"
      "v_mfma_f32_16x16x32_bf16 v[188:191], a[248:251], v[224:227], v[188:191]\n"
      "ds_read_b128 v[224:227], v123 offset:18944\n"
      "v_cvt_pk_bf16_f32 v193, v194, v195\n"
      "v_mfma_f32_16x16x32_bf16 v[188:191], v[4:7], v[220:223], v[188:191]\n"
      "ds_read_b128 v[220:223], v123 offset:22016\n"
      "s_waitcnt lgkmcnt(3)\n"
      "v_mfma_f32_16x16x32_bf16 v[180:183], v[8:11], v[200:203], v[180:183]\n"
      "v_exp_f32 v162, v162\n"
      "v_mfma_f32_16x16x32_bf16 v[180:183], v[20:23], v[164:167], v[180:183]\n"
      "v_cvt_pk_bf16_f32 v194, v160, v161\n"
      "v_exp_f32 v163, v163\n"
      "v_mfma_f32_16x16x32_bf16 v[184:187], v[12:15], v[200:203], v[184:187]\n"
      "s_waitcnt lgkmcnt(2)\n"
      "v_exp_f32_e64 v232, v232\n"
      "v_mfma_f32_16x16x32_bf16 v[184:187], v[24:27], v[164:167], v[184:187]\n"
      "global_store_dwordx4 v117, v[148:151], s[54:55] sc1\n"
      "v_cvt_pk_bf16_f32 v147, v230, v231\n"
      "v_mfma_f32_16x16x32_bf16 v[188:191], v[16:19], v[200:203], v[188:191]\n"
      "v_fma_f32 v212, v180, v180, s39\n"
      "v_cvt_pk_bf16_f32 v195, v162, v163\n"
      "v_mfma_f32_16x16x32_bf16 v[188:191], v[28:31], v[164:167], v[188:191]\n"
      "v_exp_f32_e64 v233, v233\n"
      "v_mfma_f32_16x16x32_bf16 v[200:203], v[66:69], v[144:147], 0\n"
      "v_exp_f32 v234, v234\n"
      "v_mfma_f32_16x16x32_bf16 v[200:203], v[66:69], v[62:65], v[200:203]\n"
      "v_exp_f32 v235, v235\n"
      "v_mfma_f32_16x16x32_bf16 v[160:163], v[236:239], v[144:147], 0\n"
      "v_exp_f32 v204, v204\n"
      "v_mfma_f32_16x16x32_bf16 v[160:163], v[50:53], v[62:65], v[160:163]\n"
      "s_waitcnt lgkmcnt(0)\n"
      "v_fmac_f32 v212, v181, v181\n"
      "v_fmac_f32 v212, v182, v182\n"
      "v_mfma_f32_16x16x32_bf16 v[164:167], v[244:247], v[144:147], 0\n"
      "v_fmac_f32 v212, v183, v183\n"
      "v_fmac_f32 v212, v184, v184\n"
      "v_mfma_f32_16x16x32_bf16 v[164:167], v[54:57], v[62:65], v[164:167]\n"
      "v_fmac_f32 v212, v185, v185\n"
      "v_fmac_f32 v212, v186, v186\n"
      "v_mfma_f32_16x16x32_bf16 v[148:151], v[240:243], v[144:147], 0\n"
      "v_fmac_f32 v212, v187, v187\n"
      "v_fmac_f32 v212, v188, v188\n"
      "v_mfma_f32_16x16x32_bf16 v[148:151], v[58:61], v[62:65], v[148:151]\n"
      "v_fmac_f32 v212, v189, v189\n"
      "v_fmac_f32 v212, v190, v190\n"
      "v_mfma_f32_16x16x32_bf16 v[144:147], v[248:251], v[196:199], v[102:105]\n"
      "v_cvt_pk_bf16_f32 v62, v232, v233\n"
      "v_fmac_f32_e64 v212, v191, v191\n"
      "v_mfma_f32_16x16x32_bf16 v[144:147], v[34:37], v[44:47], v[144:147]\n"
      "ds_read_b128 v[248:251], v121 offset:18432\n"
      "v_cvt_pk_bf16_f32 v63, v234, v235\n"
      "v_mfma_f32_16x16x32_bf16 v[232:235], v[140:143], v[196:199], v[106:109]\n"
      "v_cndmask_b32 v213, v213, v205, s[42:43]\n"
      "v_cvt_pk_bf16_f32 v64, v204, 0\n"
      "v_mfma_f32_16x16x32_bf16 v[232:235], v[40:43], v[44:47], v[232:235]\n"
      "v_rcp_f32 v200, v200\n"
      "v_mfma_f32_16x16x32_bf16 v[204:207], v[136:139], v[196:199], v[74:77]\n"
      "v_mul_f32 v161, v200, v161\n"
      "v_mul_f32_e64 v160, v200, v160\n"
      "v_mfma_f32_16x16x32_bf16 v[204:207], v[216:219], v[156:159], v[204:207]\n"
      "v_cvt_pk_bf16_f32 v160, v160, v161\n"
      "v_mul_f32 v162, v200, v162\n"
      "v_mfma_f32_16x16x32_bf16 v[204:207], v[152:155], v[44:47], v[204:207]\n"
      "v_mul_f32 v163, v200, v163\n"
      "v_cvt_pk_bf16_f32 v161, v162, v163\n"
      "v_mfma_f32_16x16x32_bf16 v[228:231], v[156:159], v[196:199], v[110:113]\n"
      "ds_read_b128 v[140:143], v123 offset:25088\n"
      "v_mul_f32 v164, v200, v164\n"
      "v_mfma_f32_16x16x32_bf16 v[228:231], v[46:49], v[44:47], v[228:231]\n"
      "v_mul_f32 v165, v200, v165\n"
      "v_cvt_pk_bf16_f32 v162, v164, v165\n"
      "v_mfma_f32_16x16x32_bf16 v[196:199], v[216:219], v[136:139], v[76:79]\n"
      "ds_read_b128 v[156:159], v123 offset:28160\n"
      "v_mul_f32 v166, v200, v166\n"
      "v_mfma_f32_16x16x32_bf16 v[216:219], v[66:69], v[192:195], 0\n"
      "ds_read_b128 v[208:211], v123 offset:31232\n"
      "v_mul_f32 v167, v200, v167\n"
      "v_mfma_f32_16x16x32_bf16 v[216:219], v[66:69], v[62:65], v[216:219]\n"
      "v_mul_f32_e64 v148, v200, v148\n"
      "v_cvt_pk_bf16_f32 v163, v166, v167\n"
      "v_mfma_f32_16x16x16_bf16 v[196:199], v[154:155], v[152:153], v[196:199]\n"
      "ds_write_b128 v124, v[160:163]\n"
      "v_mfma_f32_16x16x32_bf16 v[252:255], v[236:239], v[192:195], 0\n"
      "s_waitcnt lgkmcnt(4)\n"
      "v_mul_f32 v149, v200, v149\n"
      "v_cvt_pk_bf16_f32 v148, v148, v149\n"
      "v_mfma_f32_16x16x32_bf16 v[252:255], v[50:53], v[62:65], v[252:255]\n"
      "s_mov_b32 exec_hi, 0\n"
      "global_store_dwordx4 v118, v[248:251], s[54:55] sc1\n"
      "s_mov_b32 exec_hi, -1\n"
      "v_mfma_f32_16x16x32_bf16 v[164:167], v[244:247], v[192:195], 0\n"
      "s_add_u32 s28, s27, 2\n"
      "v_mul_f32 v151, v200, v151\n"
      "v_mul_f32 v150, v200, v150\n"
      "v_mfma_f32_16x16x32_bf16 v[164:167], v[54:57], v[62:65], v[164:167]\n"
      "s_waitcnt lgkmcnt(2)\n"
      "v_cvt_pk_bf16_f32 v149, v150, v151\n"
      "v_cndmask_b32 v213, v213, v205, s[44:45]\n"
      "v_mfma_f32_16x16x32_bf16 v[248:251], v[240:243], v[192:195], 0\n"
      "s_min_u32 m0, s28, s3\n"
      "v_cndmask_b32 v213, v213, v197, s[46:47]\n"
      "s_movrels_b32 s9, s80\n"
      "v_mfma_f32_16x16x32_bf16 v[248:251], v[58:61], v[62:65], v[248:251]\n"
      "ds_write_b64 v125, v[148:149]\n"
      "v_mfma_f32_16x16x32_bf16 v[192:195], a[216:219], v[224:227], 0\n"
      "s_lshr_b32 s8, s9, 8\n"
      "v_exp_f32 v213, v213\n"
      "v_mfma_f32_16x16x32_bf16 v[192:195], a[228:231], v[220:223], v[192:195]\n"
      "s_and_b32 s9, s9, 0xff\n"
      "v_exp_f32_e64 v144, v144\n"
      "v_cvt_pk_bf16_f32 v213, v213, v213\n"
      "v_mfma_f32_16x16x32_bf16 v[196:199], a[220:223], v[224:227], 0\n"
      "s_mul_i32 s10, s8, 73\n"
      "v_exp_f32_e64 v145, v145\n"
      "v_mfma_f32_16x16x32_bf16 v[196:199], a[232:235], v[220:223], v[196:199]\n"
      "ds_write_b16 v130, v213\n"
      "v_mfma_f32_16x16x32_bf16 v[200:203], a[224:227], v[224:227], 0\n"
      "ds_read_b128 v[148:151], v131\n"
      "v_cvt_pk_bf16_f32 v144, v144, v145\n"
      "v_mfma_f32_16x16x32_bf16 v[200:203], a[236:239], v[220:223], v[200:203]\n"
      "ds_read_b128 v[220:223], v131 offset:64\n"
      "v_exp_f32 v146, v146\n"
      "v_mfma_f32_16x16x32_bf16 v[192:195], a[240:243], v[140:143], v[192:195]\n"
      "ds_read_b128 v[152:155], v123 offset:34304\n"
      "s_add_u32 s10, s10, s9\n"
      "v_mfma_f32_16x16x32_bf16 v[192:195], a[252:255], v[156:159], v[192:195]\n"
      "ds_read_b128 v[136:139], v114 offset:18432\n"
      "v_exp_f32_e64 v147, v147\n"
      "v_mfma_f32_16x16x32_bf16 v[196:199], a[244:247], v[140:143], v[196:199]\n"
      "s_mul_i32 s28, s10, 4802\n"
      "v_exp_f32 v232, v232\n"
      "v_mfma_f32_16x16x32_bf16 v[196:199], v[0:3], v[156:159], v[196:199]\n"
      "s_add_u32 s56, s18, s28\n"
      "v_cvt_pk_bf16_f32 v145, v146, v147\n"
      "v_exp_f32_e64 v233, v233\n"
      "v_mfma_f32_16x16x32_bf16 v[200:203], a[248:251], v[140:143], v[200:203]\n"
      "ds_read_b128 v[140:143], v115 offset:18432\n"
      "v_cvt_pk_bf16_f32 v146, v232, v233\n"
      "v_mfma_f32_16x16x32_bf16 v[200:203], v[4:7], v[156:159], v[200:203]\n"
      "s_addc_u32 s57, s19, 0\n"
      "v_exp_f32 v234, v234\n"
      "v_exp_f32 v235, v235\n"
      "v_exp_f32_e64 v228, v228\n"
      "s_waitcnt lgkmcnt(3)\n"
      "v_mfma_f32_16x16x32_bf16 v[224:227], v[148:151], v[66:69], 0\n"
      "s_and_b32 s56, s56, -16\n"
      "v_cvt_pk_bf16_f32 v147, v234, v235\n"
      "s_waitcnt lgkmcnt(2)\n"
      "v_mfma_f32_16x16x32_bf16 v[224:227], v[220:223], v[66:69], v[224:227]\n"
      "s_sub_u32 s58, s56, 110592\n"
      "v_exp_f32 v229, v229\n"
      "v_mfma_f32_16x16x32_bf16 v[192:195], v[8:11], v[208:211], v[192:195]\n"
      "s_subb_u32 s59, s57, 0\n"
      "v_exp_f32_e64 v230, v230\n"
      "v_mfma_f32_16x16x32_bf16 v[192:195], v[20:23], v[152:155], v[192:195]\n"
      "s_sub_u32 s56, s56, 73728\n"
      "v_exp_f32 v231, v231\n"
      "v_mfma_f32_16x16x32_bf16 v[196:199], v[12:15], v[208:211], v[196:199]\n"
      "s_subb_u32 s57, s57, 0\n"
      "v_exp_f32 v204, v204\n"
      "v_mfma_f32_16x16x32_bf16 v[196:199], v[24:27], v[152:155], v[196:199]\n"
      "s_waitcnt lgkmcnt(0)\n"
      "v_cvt_pk_bf16_f32 v62, v228, v229\n"
      "v_cvt_pk_bf16_f32 v63, v230, v231\n"
      "v_mfma_f32_16x16x32_bf16 v[200:203], v[16:19], v[208:211], v[200:203]\n"
      "s_mul_i32 s28, s8, 1373568\n"
      "v_fma_f32 v213, v192, v192, s39\n"
      "v_cvt_pk_bf16_f32 v64, v204, 0\n"
      "v_mfma_f32_16x16x32_bf16 v[200:203], v[28:31], v[152:155], v[200:203]\n"
      "s_mul_i32 s29, s9, 2688\n"
      "v_rcp_f32 v216, v216\n"
      "v_mfma_f32_16x16x32_bf16 v[152:155], v[66:69], v[144:147], 0\n"
      "s_add_u32 s28, s28, s29\n"
      "v_mul_f32 v252, v216, v252\n"
      "v_mul_f32 v253, v216, v253\n"
      "v_mfma_f32_16x16x32_bf16 v[152:155], v[66:69], v[62:65], v[152:155]\n"
      "s_cmp_eq_u32 s8, 72\n"
      "v_mul_f32 v254, v216, v254\n"
      "v_mul_f32 v165, v216, v165\n"
      "v_cvt_pk_bf16_f32 v252, v252, v253\n"
      "v_mfma_f32_16x16x32_bf16 v[156:159], v[236:239], v[144:147], 0\n"
      "s_cselect_b32 s8, 0, 100270464\n"
      "v_mul_f32_e64 v255, v216, v255\n"
      "v_cvt_pk_bf16_f32 v253, v254, v255\n"
      "v_mfma_f32_16x16x32_bf16 v[156:159], v[50:53], v[62:65], v[156:159]\n"
      "s_cmp_eq_u32 s9, 72\n"
      "v_mul_f32 v251, v216, v251\n"
      "v_mul_f32 v164, v216, v164\n"
      "v_mfma_f32_16x16x32_bf16 v[160:163], v[244:247], v[144:147], 0\n"
      "s_cselect_b32 s9, 0, 196224\n"
      "v_cvt_pk_bf16_f32 v254, v164, v165\n"
      "v_mul_f32_e64 v166, v216, v166\n"
      "v_mfma_f32_16x16x32_bf16 v[160:163], v[54:57], v[62:65], v[160:163]\n"
      "s_add_u32 s10, s8, s9\n"
      "v_mul_f32 v167, v216, v167\n"
      "v_cvt_pk_bf16_f32 v255, v166, v167\n"
      "v_mfma_f32_16x16x32_bf16 v[164:167], v[240:243], v[144:147], 0\n"
      "s_add_u32 s28, s28, s10\n"
      "v_mul_f32 v249, v216, v249\n"
      "v_mul_f32_e64 v248, v216, v248\n"
      "v_mfma_f32_16x16x32_bf16 v[164:167], v[58:61], v[62:65], v[164:167]\n"
      "ds_write_b128 v124, v[252:255] offset:256\n"
      "v_mfma_f32_16x16x32_bf16 v[252:255], v[148:151], v[236:239], 0\n"
      "s_add_u32 s52, s16, s28\n"
      "v_cvt_pk_bf16_f32 v248, v248, v249\n"
      "v_mul_f32 v250, v216, v250\n"
      "v_mfma_f32_16x16x32_bf16 v[252:255], v[220:223], v[50:53], v[252:255]\n"
      "s_addc_u32 s53, s17, 0\n"
      "v_mov_b32_dpp v224, v224 quad_perm:[0,0,0,0] row_mask:0xf bank_mask:0xf\n"
      "v_cvt_pk_bf16_f32 v249, v250, v251\n"
      "v_mfma_f32_16x16x32_bf16 v[236:239], v[148:151], v[244:247], 0\n"
      "s_add_u32 s27, s27, 1\n"
      "v_fmac_f32 v213, v193, v193\n"
      "v_fmac_f32 v213, v194, v194\n"
      "v_mfma_f32_16x16x32_bf16 v[236:239], v[220:223], v[54:57], v[236:239]\n"
      "ds_write_b64 v125, v[248:249] offset:256\n"
      "v_mfma_f32_16x16x32_bf16 v[248:251], v[148:151], v[240:243], 0\n"
      "s_cmp_ge_u32 s27, s26\n"
      "v_fmac_f32 v213, v195, v195\n"
      "v_fmac_f32 v213, v196, v196\n"
      "v_mfma_f32_16x16x32_bf16 v[248:251], v[220:223], v[58:61], v[248:251]\n"
      "s_cselect_b32 s28, 1, 0\n"
      "v_fmac_f32 v213, v197, v197\n"
      "v_fmac_f32 v213, v198, v198\n"
      "v_fmac_f32 v213, v199, v199\n"
      "v_permlane16_swap_b32 v214, v212\n"
      "v_fmac_f32 v213, v200, v200\n"
      "v_add_f32 v214, v214, v212\n"
      "v_fmac_f32 v213, v201, v201\n"
      "v_fmac_f32 v213, v202, v202\n"
      "v_cndmask_b32 v252, v252, v236, s[42:43]\n"
      "v_rcp_f32 v224, v224\n"
      "v_mfma_f32_16x16x32_bf16 v[148:151], a[4:7], v[136:139], 0\n"
      "s_xor_b32 s29, s10, s11\n"
      "v_fmac_f32 v213, v203, v203\n"
      "v_mov_b32 v215, v213\n"
      "v_mfma_f32_16x16x32_bf16 v[148:151], a[40:43], v[140:143], v[148:151]\n"
      "s_or_b32 s28, s28, s29\n"
      "v_permlane16_swap_b32 v213, v215\n"
      "v_cndmask_b32 v252, v252, v248, s[44:45]\n"
      "v_mul_f32 v252, v224, v252\n"
      "v_add_f32 v213, v213, v215\n"
      "v_mfma_f32_16x16x32_bf16 v[144:147], a[0:3], v[136:139], 0\n"
      "s_nop 0\n"
      "v_permlane32_swap_b32 v214, v213\n"
      "v_cvt_pk_bf16_f32 v252, v252, v252\n"
      "v_add_f32_e64 v214, v214, v213\n"
      "v_mfma_f32_16x16x32_bf16 v[144:147], a[36:39], v[140:143], v[144:147]\n"
      "ds_write_b16 v135, v252\n"
      "ds_write_b32 v129, v214\n"
      "L_join_30_%=:\n"
      "s_cbranch_scc0 L_loop_%=\n"
      "L_tail0_%=:\n"
      "s_mov_b32 s11, s10\n"
      "s_sub_u32 s28, s60, s8\n"
      "s_sub_u32 s29, s61, s9\n"
      "s_mov_b32 s60, s8\n"
      "s_mov_b32 s61, s9\n"
      "s_mov_b64 exec, s[64:65]\n"
      "v_add_u32 v116, s28, v116\n"
      "s_mov_b64 exec, s[70:71]\n"
      "v_add_u32 v116, s29, v116\n"
      "s_mov_b64 exec, s[66:67]\n"
      "v_add_u32 v117, s28, v117\n"
      "s_mov_b64 exec, s[72:73]\n"
      "v_add_u32 v117, s29, v117\n"
      "s_mov_b64 exec, s[68:69]\n"
      "v_add_u32_e64 v118, s28, v118\n"
      "s_mov_b64 exec, s[74:75]\n"
      "v_add_u32_e64 v118, s29, v118\n"
      "s_mov_b64 exec, -1\n"
      "s_nop 4\n"
      "s_cmp_lt_u32 s27, s26\n"
      "s_cbranch_scc1 L_loop_%=\n"
      "s_nop 7\n"
      "s_nop 7\n"
      "v_rcp_f32_e64 v152, v152\n"
      "v_mbcnt_lo_u32_b32 v144, -1, 0\n"
      "v_pk_mul_f32 v[162:163], v[162:163], v[152:153] op_sel_hi:[1,0]\n"
      "v_pk_mul_f32 v[156:157], v[156:157], v[152:153] op_sel_hi:[1,0]\n"
      "v_pk_mul_f32 v[166:167], v[166:167], v[152:153] op_sel_hi:[1,0]\n"
      "v_mbcnt_hi_u32_b32 v144, -1, v144\n"
      "v_pk_mul_f32 v[158:159], v[158:159], v[152:153] op_sel_hi:[1,0]\n"
      "v_pk_mul_f32 v[160:161], v[160:161], v[152:153] op_sel_hi:[1,0]\n"
      "v_cvt_pk_bf16_f32 v156, v156, v157\n"
      "v_lshrrev_b32_e64 v145, 4, v144\n"
      "v_and_b32_e64 v153, 15, v144\n"
      "v_pk_mul_f32 v[164:165], v[164:165], v[152:153] op_sel_hi:[1,0]\n"
      "v_mul_u32_u24 v145, 352, v145\n"
      "v_cvt_pk_bf16_f32 v157, v158, v159\n"
      "v_cvt_pk_bf16_f32 v159, v162, v163\n"
      "v_lshl_add_u32 v145, v153, 4, v145\n"
      "v_cvt_pk_bf16_f32 v158, v160, v161\n"
      "v_add_u32 v145, 150640, v145\n"
      "v_cvt_pk_bf16_f32 v164, v164, v165\n"
      "v_cvt_pk_bf16_f32 v165, v166, v167\n"
      "ds_write_b64 v125, v[164:165] offset:512\n"
      "ds_write_b128 v124, v[156:159] offset:512\n"
      "s_waitcnt lgkmcnt(0)\n"
      "s_barrier\n"
      "ds_read_b128 v[140:143], v123 offset:3072\n"
      "ds_read_b128 v[152:155], v123\n"
      "ds_read_b128 v[156:159], v128\n"
      "s_sub_u32 s28, s27, 3\n"
      "s_cselect_b64 s[54:55], s[12:13], s[22:23]\n"
      "s_max_i32 m0, s28, 0\n"
      "ds_read_b128 v[136:139], v119\n"
      "s_movrels_b32 s9, s80\n"
      "s_lshr_b32 s8, s9, 8\n"
      "s_and_b32 s9, s9, 0xff\n"
      "s_mul_i32 s28, s8, 1373568\n"
      "s_mul_i32 s29, s9, 2688\n"
      "ds_read_b128 v[248:251], v123 offset:6144\n"
      "ds_read_b128 v[164:167], v119 offset:6144\n"
      "s_add_u32 s28, s28, s29\n"
      "ds_read_b128 v[160:163], v119 offset:12288\n"
      "ds_read_b128 v[148:151], v120\n"
      "s_cmp_eq_u32 s8, 72\n"
      "s_cselect_b32 s8, 0, 100270464\n"
      "s_cmp_eq_u32 s9, 72\n"
      "s_cselect_b32 s9, 0, 196224\n"
      "ds_read_b128 v[212:215], v121\n"
      "s_add_u32 s10, s8, s9\n"
      "s_add_u32 s28, s28, s10\n"
      "s_add_u32 s54, s54, s28\n"
      "s_waitcnt lgkmcnt(7)\n"
      "v_mfma_f32_16x16x32_bf16 v[236:239], a[216:219], v[152:155], 0\n"
      "ds_read_b128 v[252:255], v123 offset:9216\n"
      "s_waitcnt lgkmcnt(7)\n"
      "v_mfma_f32_16x16x32_bf16 v[236:239], a[228:231], v[140:143], v[236:239]\n"
      "ds_read_b128 v[220:223], v123 offset:12288\n"
      "v_add_f32 v158, |v158|, |v159|\n"
      "v_add_f32 v156, |v156|, |v157|\n"
      "v_mfma_f32_16x16x32_bf16 v[240:243], a[220:223], v[152:155], 0\n"
      "ds_read_b128 v[244:247], v123 offset:15360\n"
      "v_add_f32 v156, v156, v158\n"
      "v_mfma_f32_16x16x32_bf16 v[240:243], a[232:235], v[140:143], v[240:243]\n"
      "s_addc_u32 s55, s55, 0\n"
      "v_rsq_f32 v156, v156\n"
      "v_mfma_f32_16x16x32_bf16 v[208:211], a[224:227], v[152:155], 0\n"
      "ds_read_b128 v[152:155], v123 offset:3328\n"
      "v_mul_f32 v156, s41, v156\n"
      "v_mov_b32 v158, v156\n"
      "v_mfma_f32_16x16x32_bf16 v[208:211], a[236:239], v[140:143], v[208:211]\n"
      "ds_read_b128 v[140:143], v123 offset:6400\n"
      "v_permlane32_swap_b32_e64 v156, v158\n"
      "v_mov_b32_e64 v159, v156\n"
      "s_cmp_lg_u32 s10, s11\n"
      "s_waitcnt lgkmcnt(5)\n"
      "ds_read_b128 v[224:227], v123 offset:256\n"
      "v_permlane16_swap_b32_e64 v156, v159\n"
      "s_waitcnt lgkmcnt(5)\n"
      "v_mfma_f32_16x16x32_bf16 v[236:239], a[240:243], v[248:251], v[236:239]\n"
      "s_cbranch_scc1 L_edge_35_%=\n"
      "L_eb_36_%=:\n"
      "v_mfma_f32_16x16x32_bf16 v[236:239], a[252:255], v[252:255], v[236:239]\n"
      "global_store_dwordx4 v116, v[136:139], s[54:55] sc1\n"
      "v_mfma_f32_16x16x32_bf16 v[240:243], a[244:247], v[248:251], v[240:243]\n"
      "ds_read_b128 v[228:231], v123 offset:9472\n"
      "v_mul_f32 v168, v156, v168\n"
      "v_mfma_f32_16x16x32_bf16 v[240:243], v[0:3], v[252:255], v[240:243]\n"
      "v_mul_f32 v169, v156, v169\n"
      "v_mul_f32_e64 v170, v156, v170\n"
      "v_mfma_f32_16x16x32_bf16 v[208:211], a[248:251], v[248:251], v[208:211]\n"
      "ds_read_b128 v[136:139], v123 offset:12544\n"
      "v_cvt_pk_bf16_f32 v168, v168, v169\n"
      "v_mfma_f32_16x16x32_bf16 v[208:211], v[4:7], v[252:255], v[208:211]\n"
      "s_waitcnt lgkmcnt(3)\n"
      "ds_read_b128 v[252:255], v123 offset:15616\n"
      "v_mfma_f32_16x16x32_bf16 v[236:239], v[8:11], v[220:223], v[236:239]\n"
      "v_mul_f32 v171, v156, v171\n"
      "v_cvt_pk_bf16_f32 v169, v170, v171\n"
      "v_mfma_f32_16x16x32_bf16 v[236:239], v[20:23], v[244:247], v[236:239]\n"
      "s_waitcnt lgkmcnt(3)\n"
      "v_mul_f32 v173, v156, v173\n"
      "v_mul_f32_e64 v172, v156, v172\n"
      "v_mfma_f32_16x16x32_bf16 v[240:243], v[12:15], v[220:223], v[240:243]\n"
      "v_mul_f32 v174, v156, v174\n"
      "v_mul_f32 v175, v156, v175\n"
      "v_mfma_f32_16x16x32_bf16 v[240:243], v[24:27], v[244:247], v[240:243]\n"
      "global_store_dwordx4 v116, v[164:167], s[54:55] offset:128 sc1\n"
      "v_mfma_f32_16x16x32_bf16 v[208:211], v[16:19], v[220:223], v[208:211]\n"
      "v_mul_f32 v177, v156, v177\n"
      "v_cvt_pk_bf16_f32 v170, v172, v173\n"
      "v_mfma_f32_16x16x32_bf16 v[208:211], v[28:31], v[244:247], v[208:211]\n"
      "v_mul_f32 v176, v156, v176\n"
      "v_cvt_pk_bf16_f32 v171, v174, v175\n"
      "v_mfma_f32_16x16x32_bf16 v[172:175], a[216:219], v[224:227], 0\n"
      "ds_read_b128 v[244:247], v123 offset:512\n"
      "v_mul_f32_e64 v178, v156, v178\n"
      "v_mfma_f32_16x16x32_bf16 v[172:175], a[228:231], v[152:155], v[172:175]\n"
      "ds_write_b128 v126, v[168:171] offset:18432\n"
      "v_mfma_f32_16x16x32_bf16 v[168:171], a[220:223], v[224:227], 0\n"
      "ds_read_b128 v[164:167], v123 offset:3584\n"
      "v_cvt_pk_bf16_f32 v176, v176, v177\n"
      "v_mfma_f32_16x16x32_bf16 v[168:171], a[232:235], v[152:155], v[168:171]\n"
      "s_waitcnt lgkmcnt(3)\n"
      "v_mul_f32 v179, v156, v179\n"
      "v_mfma_f32_16x16x32_bf16 v[220:223], a[224:227], v[224:227], 0\n"
      "s_lshl_b32 s29, s24, 8\n"
      "v_cvt_pk_bf16_f32 v177, v178, v179\n"
      "v_mul_f32 v180, v159, v180\n"
      "v_mfma_f32_16x16x32_bf16 v[220:223], a[236:239], v[152:155], v[220:223]\n"
      "ds_write_b64 v127, v[176:177] offset:18432\n"
      "v_mfma_f32_16x16x32_bf16 v[172:175], a[240:243], v[140:143], v[172:175]\n"
      "ds_read_b128 v[152:155], v123 offset:6656\n"
      "v_mul_f32_e64 v181, v159, v181\n"
      "v_mfma_f32_16x16x32_bf16 v[172:175], a[252:255], v[228:231], v[172:175]\n"
      "ds_read_b128 v[176:179], v123 offset:9728\n"
      "v_cvt_pk_bf16_f32 v180, v180, v181\n"
      "v_mfma_f32_16x16x32_bf16 v[168:171], a[244:247], v[140:143], v[168:171]\n"
      "v_mul_f32 v183, v159, v183\n"
      "v_mul_f32 v182, v159, v182\n"
      "v_mfma_f32_16x16x32_bf16 v[168:171], v[0:3], v[228:231], v[168:171]\n"
      "s_add_u32 s29, s29, 159088\n"
      "v_cvt_pk_bf16_f32 v181, v182, v183\n"
      "v_mul_f32_e64 v184, v159, v184\n"
      "v_mfma_f32_16x16x32_bf16 v[220:223], a[248:251], v[140:143], v[220:223]\n"
      "v_mul_f32 v185, v159, v185\n"
      "v_cvt_pk_bf16_f32 v182, v184, v185\n"
      "v_mfma_f32_16x16x32_bf16 v[220:223], v[4:7], v[228:231], v[220:223]\n"
      "s_waitcnt lgkmcnt(3)\n"
      "v_mul_f32 v187, v159, v187\n"
      "v_mul_f32 v186, v159, v186\n"
      "v_mfma_f32_16x16x32_bf16 v[172:175], v[8:11], v[136:139], v[172:175]\n"
      "v_mul_f32_e64 v188, v159, v188\n"
      "v_cvt_pk_bf16_f32 v183, v186, v187\n"
      "v_mfma_f32_16x16x32_bf16 v[172:175], v[20:23], v[252:255], v[172:175]\n"
      "ds_write_b128 v126, v[180:183] offset:20480\n"
      "v_mfma_f32_16x16x32_bf16 v[168:171], v[12:15], v[136:139], v[168:171]\n"
      "ds_read_b128 v[180:183], v123 offset:12800\n"
      "v_mul_f32_e64 v189, v159, v189\n"
      "v_mfma_f32_16x16x32_bf16 v[168:171], v[24:27], v[252:255], v[168:171]\n"
      "ds_read_b128 v[184:187], v123 offset:15872\n"
      "v_cvt_pk_bf16_f32 v188, v188, v189\n"
      "v_mfma_f32_16x16x32_bf16 v[220:223], v[16:19], v[136:139], v[220:223]\n"
      "v_mul_f32 v191, v159, v191\n"
      "v_mul_f32 v190, v159, v190\n"
      "v_mfma_f32_16x16x32_bf16 v[220:223], v[28:31], v[252:255], v[220:223]\n"
      "v_fma_f32 v159, v236, v236, s39\n"
      "v_cvt_pk_bf16_f32 v189, v190, v191\n"
      "v_mfma_f32_16x16x32_bf16 v[140:143], a[216:219], v[244:247], 0\n"
      "global_store_dwordx4 v116, v[160:163], s[54:55] offset:256 sc1\n"
      "v_mfma_f32_16x16x32_bf16 v[140:143], a[228:231], v[164:167], v[140:143]\n"
      "ds_write_b64 v127, v[188:189] offset:20480\n"
      "v_mfma_f32_16x16x32_bf16 v[188:191], a[220:223], v[244:247], 0\n"
      "ds_read_b128 v[136:139], v145\n"
      "v_fmac_f32_e64 v159, v237, v237\n"
      "v_mfma_f32_16x16x32_bf16 v[188:191], a[232:235], v[164:167], v[188:191]\n"
      "ds_read_b128 v[160:163], v145 offset:1408\n"
      "v_fmac_f32 v159, v238, v238\n"
      "v_mfma_f32_16x16x32_bf16 v[252:255], a[224:227], v[244:247], 0\n"
      "v_fmac_f32 v159, v239, v239\n"
      "v_fmac_f32 v159, v240, v240\n"
      "v_mfma_f32_16x16x32_bf16 v[252:255], a[236:239], v[164:167], v[252:255]\n"
      "s_waitcnt lgkmcnt(3)\n"
      "v_fmac_f32 v159, v241, v241\n"
      "v_fmac_f32 v159, v242, v242\n"
      "v_mfma_f32_16x16x32_bf16 v[140:143], a[240:243], v[152:155], v[140:143]\n"
      "v_fmac_f32 v159, v243, v243\n"
      "v_fmac_f32 v159, v208, v208\n"
      "v_mfma_f32_16x16x32_bf16 v[140:143], a[252:255], v[176:179], v[140:143]\n"
      "v_fmac_f32 v159, v209, v209\n"
      "v_fmac_f32 v159, v210, v210\n"
      "v_mfma_f32_16x16x32_bf16 v[188:191], a[244:247], v[152:155], v[188:191]\n"
      "global_store_dwordx4 v117, v[148:151], s[54:55] sc1\n"
      "v_mfma_f32_16x16x32_bf16 v[188:191], v[0:3], v[176:179], v[188:191]\n"
      "v_mul_f32 v192, v158, v192\n"
      "v_fmac_f32 v159, v211, v211\n"
      "v_mfma_f32_16x16x32_bf16 v[252:255], a[248:251], v[152:155], v[252:255]\n"
      "ds_read_b128 v[152:155], v145 offset:2816\n"
      "v_mul_f32 v193, v158, v193\n"
      "v_mfma_f32_16x16x32_bf16 v[252:255], v[4:7], v[176:179], v[252:255]\n"
      "v_cvt_pk_bf16_f32 v192, v192, v193\n"
      "v_mul_f32 v194, v158, v194\n"
      "v_mfma_f32_16x16x32_bf16 v[140:143], v[8:11], v[180:183], v[140:143]\n"
      "ds_read_b128 v[176:179], v145 offset:4224\n"
      "v_mul_f32 v195, v158, v195\n"
      "v_mfma_f32_16x16x32_bf16 v[140:143], v[20:23], v[184:187], v[140:143]\n"
      "v_mul_f32 v196, v158, v196\n"
      "v_cvt_pk_bf16_f32 v193, v194, v195\n"
      "v_mfma_f32_16x16x32_bf16 v[188:191], v[12:15], v[180:183], v[188:191]\n"
      "v_mul_f32_e64 v197, v158, v197\n"
      "v_cvt_pk_bf16_f32 v194, v196, v197\n"
      "v_mfma_f32_16x16x32_bf16 v[188:191], v[24:27], v[184:187], v[188:191]\n"
      "ds_read_b128 v[148:151], v145 offset:5632\n"
      "v_mul_f32 v198, v158, v198\n"
      "v_mfma_f32_16x16x32_bf16 v[252:255], v[16:19], v[180:183], v[252:255]\n"
      "s_waitcnt lgkmcnt(3)\n"
      "v_mul_f32_e64 v199, v158, v199\n"
      "v_cvt_pk_bf16_f32 v195, v198, v199\n"
      "v_mfma_f32_16x16x32_bf16 v[252:255], v[28:31], v[184:187], v[252:255]\n"
      "ds_write_b128 v126, v[192:195] offset:22528\n"
      "v_mfma_f32_16x16x32_bf16 v[192:195], a[216:219], v[136:139], 0\n"
      "ds_read_b128 v[184:187], v145 offset:7040\n"
      "v_mul_f32 v201, v158, v201\n"
      "v_mfma_f32_16x16x32_bf16 v[192:195], a[228:231], v[160:163], v[192:195]\n"
      "v_mul_f32 v200, v158, v200\n"
      "v_mul_f32 v202, v158, v202\n"
      "v_mfma_f32_16x16x32_bf16 v[180:183], a[220:223], v[136:139], 0\n"
      "v_cvt_pk_bf16_f32 v200, v200, v201\n"
      "s_waitcnt lgkmcnt(3)\n"
      "v_mfma_f32_16x16x32_bf16 v[180:183], a[232:235], v[160:163], v[180:183]\n"
      "ds_read_b128 v[196:199], v145 offset:256\n"
      "v_mul_f32_e64 v203, v158, v203\n"
      "v_mfma_f32_16x16x32_bf16 v[164:167], a[224:227], v[136:139], 0\n"
      "v_fma_f32 v158, v172, v172, s39\n"
      "v_cvt_pk_bf16_f32 v201, v202, v203\n"
      "v_mfma_f32_16x16x32_bf16 v[164:167], a[236:239], v[160:163], v[164:167]\n"
      "ds_write_b64 v127, v[200:201] offset:22528\n"
      "v_mfma_f32_16x16x32_bf16 v[192:195], a[240:243], v[152:155], v[192:195]\n"
      "ds_read_b128 v[200:203], v145 offset:1664\n"
      "v_fmac_f32 v158, v173, v173\n"
      "v_mfma_f32_16x16x32_bf16 v[192:195], a[252:255], v[176:179], v[192:195]\n"
      "v_fmac_f32 v158, v174, v174\n"
      "v_fmac_f32_e64 v158, v175, v175\n"
      "v_mfma_f32_16x16x32_bf16 v[180:183], a[244:247], v[152:155], v[180:183]\n"
      "s_waitcnt lgkmcnt(3)\n"
      "v_fmac_f32 v158, v168, v168\n"
      "v_fmac_f32_e64 v158, v169, v169\n"
      "v_mfma_f32_16x16x32_bf16 v[180:183], v[0:3], v[176:179], v[180:183]\n"
      "ds_read_b128 v[136:139], v145 offset:3072\n"
      "v_fmac_f32 v158, v170, v170\n"
      "v_mfma_f32_16x16x32_bf16 v[164:167], a[248:251], v[152:155], v[164:167]\n"
      "ds_read_b128 v[152:155], v145 offset:4480\n"
      "v_fmac_f32 v158, v171, v171\n"
      "v_mfma_f32_16x16x32_bf16 v[164:167], v[4:7], v[176:179], v[164:167]\n"
      "v_fmac_f32 v158, v220, v220\n"
      "v_fmac_f32 v158, v221, v221\n"
      "v_mfma_f32_16x16x32_bf16 v[192:195], v[8:11], v[148:151], v[192:195]\n"
      "v_fmac_f32 v158, v222, v222\n"
      "v_fmac_f32 v158, v223, v223\n"
      "v_mfma_f32_16x16x32_bf16 v[192:195], v[20:23], v[184:187], v[192:195]\n"
      "ds_read_b128 v[176:179], v145 offset:5888\n"
      "v_lshl_add_u32 v156, v144, 2, s29\n"
      "v_mfma_f32_16x16x32_bf16 v[180:183], v[12:15], v[148:151], v[180:183]\n"
      "s_waitcnt lgkmcnt(3)\n"
      "v_lshrrev_b32 v157, 3, v144\n"
      "v_mfma_f32_16x16x32_bf16 v[180:183], v[24:27], v[184:187], v[180:183]\n"
      "ds_read_b32 v156, v156\n"
      "v_fma_f32 v144, v140, v140, s39\n"
      "v_mfma_f32_16x16x32_bf16 v[164:167], v[16:19], v[148:151], v[164:167]\n"
      "ds_read_b128 v[148:151], v145 offset:7296\n"
      "v_mfma_f32_16x16x32_bf16 v[164:167], v[28:31], v[184:187], v[164:167]\n"
      "s_lshl_b32 s29, s24, 3\n"
      "v_fmac_f32 v144, v141, v141\n"
      "v_add_u32 v157, s29, v157\n"
      "v_mfma_f32_16x16x32_bf16 v[184:187], a[216:219], v[196:199], 0\n"
      "v_cmp_gt_u32 s[48:49], s26, v157\n"
      "v_fmac_f32 v144, v142, v142\n"
      "v_mfma_f32_16x16x32_bf16 v[184:187], a[228:231], v[200:203], v[184:187]\n"
      "s_mov_b32 exec_hi, 0\n"
      "global_store_dwordx4 v118, v[212:215], s[54:55] sc1\n"
      "s_mov_b32 exec_hi, -1\n"
      "v_mfma_f32_16x16x32_bf16 v[160:163], a[220:223], v[196:199], 0\n"
      "s_waitcnt lgkmcnt(3)\n"
      "v_fmac_f32 v144, v143, v143\n"
      "v_fmac_f32 v144, v188, v188\n"
      "v_mfma_f32_16x16x32_bf16 v[160:163], a[232:235], v[200:203], v[160:163]\n"
      "v_fmac_f32 v144, v189, v189\n"
      "v_fmac_f32 v144, v190, v190\n"
      "v_mfma_f32_16x16x32_bf16 v[212:215], a[224:227], v[196:199], 0\n"
      "v_fmac_f32 v144, v191, v191\n"
      "v_fmac_f32 v144, v252, v252\n"
      "v_mfma_f32_16x16x32_bf16 v[212:215], a[236:239], v[200:203], v[212:215]\n"
      "v_fmac_f32 v144, v253, v253\n"
      "v_fmac_f32 v144, v254, v254\n"
      "v_mfma_f32_16x16x32_bf16 v[184:187], a[240:243], v[136:139], v[184:187]\n"
      "v_fmac_f32 v144, v255, v255\n"
      "v_mov_b32 v157, v144\n"
      "v_mfma_f32_16x16x32_bf16 v[184:187], a[252:255], v[152:155], v[184:187]\n"
      "v_permlane16_swap_b32 v159, v158\n"
      "v_mfma_f32_16x16x32_bf16 v[160:163], a[244:247], v[136:139], v[160:163]\n"
      "v_fma_f32 v200, v192, v192, s39\n"
      "v_add_f32 v159, v159, v158\n"
      "v_mfma_f32_16x16x32_bf16 v[160:163], v[0:3], v[152:155], v[160:163]\n"
      "s_waitcnt lgkmcnt(0)\n"
      "v_permlane16_swap_b32 v144, v157\n"
      "v_mfma_f32_16x16x32_bf16 v[212:215], a[248:251], v[136:139], v[212:215]\n"
      "v_add_f32 v144, v144, v157\n"
      "v_mfma_f32_16x16x32_bf16 v[212:215], v[4:7], v[152:155], v[212:215]\n"
      "s_nop 0\n"
      "v_permlane32_swap_b32 v159, v144\n"
      "v_mfma_f32_16x16x32_bf16 v[184:187], v[8:11], v[176:179], v[184:187]\n"
      "v_add_f32 v159, v159, v144\n"
      "v_fmac_f32 v200, v193, v193\n"
      "v_mfma_f32_16x16x32_bf16 v[184:187], v[20:23], v[148:151], v[184:187]\n"
      "ds_write_b32 v129, v159 offset:1024\n"
      "v_mfma_f32_16x16x32_bf16 v[160:163], v[12:15], v[176:179], v[160:163]\n"
      "v_fmac_f32 v200, v194, v194\n"
      "v_fmac_f32 v200, v195, v195\n"
      "v_mfma_f32_16x16x32_bf16 v[160:163], v[24:27], v[148:151], v[160:163]\n"
      "v_fmac_f32 v200, v180, v180\n"
      "v_mfma_f32_16x16x32_bf16 v[212:215], v[16:19], v[176:179], v[212:215]\n"
      "v_fmac_f32 v200, v181, v181\n"
      "v_fmac_f32 v200, v182, v182\n"
      "v_mfma_f32_16x16x32_bf16 v[212:215], v[28:31], v[148:151], v[212:215]\n"
      "v_fmac_f32 v200, v183, v183\n"
      "v_fmac_f32 v200, v164, v164\n"
      "v_fmac_f32 v200, v165, v165\n"
      "v_fmac_f32 v200, v166, v166\n"
      "v_fmac_f32 v200, v167, v167\n"
      "v_pk_mul_f32 v[158:159], v[184:185], v[184:185]\n"
      "v_pk_fma_f32 v[158:159], v[186:187], v[186:187], v[158:159]\n"
      "v_pk_fma_f32 v[158:159], v[160:161], v[160:161], v[158:159]\n"
      "v_pk_fma_f32 v[158:159], v[162:163], v[162:163], v[158:159]\n"
      "v_pk_fma_f32 v[158:159], v[212:213], v[212:213], v[158:159]\n"
      "v_pk_fma_f32 v[158:159], v[214:215], v[214:215], v[158:159]\n"
      "v_add_f32 v158, s39, v158\n"
      "v_add_f32 v157, v158, v159\n"
      "v_mov_b32 v201, v157\n"
      "v_mov_b32 v202, v201\n"
      "v_permlane16_swap_b32 v200, v157\n"
      "v_add_f32 v200, v200, v157\n"
      "v_permlane16_swap_b32 v201, v202\n"
      "v_add_f32 v201, v201, v202\n"
      "s_nop 1\n"
      "v_permlane32_swap_b32 v200, v201\n"
      "v_add_f32_e64 v200, v200, v201\n"
      "ds_write_b32 v129, v200 offset:2048\n"
      "s_waitcnt lgkmcnt(0)\n"
      "s_barrier\n"
      "s_add_u32 s27, s27, 1\n"
      "ds_read_b128 v[176:179], v128 offset:1024\n"
      "ds_read_b128 v[152:155], v128 offset:2048\n"
      "s_sub_u32 s28, s27, 3\n"
      "ds_read_b128 v[200:203], v119 offset:18432\n"
      "s_cselect_b64 s[54:55], s[12:13], s[22:23]\n"
      "ds_read_b128 v[144:147], v121 offset:18432\n"
      "s_max_i32 m0, s28, 0\n"
      "ds_read_b128 v[148:151], v120 offset:18432\n"
      "s_movrels_b32 s9, s80\n"
      "s_lshr_b32 s8, s9, 8\n"
      "s_and_b32 s9, s9, 0xff\n"
      "s_mul_i32 s28, s8, 1373568\n"
      "ds_read_b128 v[196:199], v119 offset:24576\n"
      "s_mul_i32 s29, s9, 2688\n"
      "s_add_u32 s28, s28, s29\n"
      "s_cmp_eq_u32 s8, 72\n"
      "s_cselect_b32 s8, 0, 100270464\n"
      "s_cmp_eq_u32 s9, 72\n"
      "s_cselect_b32 s9, 0, 196224\n"
      "s_add_u32 s10, s8, s9\n"
      "s_add_u32 s28, s28, s10\n"
      "s_add_u32 s54, s54, s28\n"
      "s_addc_u32 s55, s55, 0\n"
      "s_cmp_lg_u32 s10, s11\n"
      "ds_read_b128 v[136:139], v119 offset:30720\n"
      "s_cbranch_scc1 L_edge_37_%=\n"
      "L_eb_38_%=:\n"
      "s_waitcnt lgkmcnt(5)\n"
      "v_add_f32 v178, |v178|, |v179|\n"
      "v_add_f32 v176, |v176|, |v177|\n"
      "v_add_f32 v176, v176, v178\n"
      "v_rsq_f32 v176, v176\n"
      "v_subrev_u32 v179, 73728, v127\n"
      "v_mul_f32 v176, s41, v176\n"
      "v_mov_b32 v178, v176\n"
      "v_add_f32 v154, |v154|, |v155|\n"
      "v_add_f32 v152, |v152|, |v153|\n"
      "v_permlane32_swap_b32 v176, v178\n"
      "v_pk_mul_f32 v[190:191], v[190:191], v[178:179] op_sel_hi:[1,0]\n"
      "v_mov_b32 v155, v176\n"
      "v_add_f32 v152, v152, v154\n"
      "v_rsq_f32 v152, v152\n"
      "v_permlane16_swap_b32 v176, v155\n"
      "v_mul_f32 v152, s41, v152\n"
      "v_mov_b32_e64 v154, v152\n"
      "v_pk_mul_f32 v[238:239], v[238:239], v[176:177] op_sel_hi:[1,0]\n"
      "v_pk_mul_f32 v[236:237], v[236:237], v[176:177] op_sel_hi:[1,0]\n"
      "v_pk_mul_f32 v[242:243], v[242:243], v[176:177] op_sel_hi:[1,0]\n"
      "v_pk_mul_f32 v[188:189], v[188:189], v[178:179] op_sel_hi:[1,0]\n"
      "v_pk_mul_f32 v[140:141], v[140:141], v[178:179] op_sel_hi:[1,0]\n"
      "v_permlane32_swap_b32_e64 v152, v154\n"
      "v_cvt_pk_bf16_f32 v236, v236, v237\n"
      "v_pk_mul_f32 v[208:209], v[208:209], v[176:177] op_sel_hi:[1,0]\n"
      "v_pk_mul_f32 v[210:211], v[210:211], v[176:177] op_sel_hi:[1,0]\n"
      "v_pk_mul_f32 v[168:169], v[168:169], v[154:155] op_sel:[0,1]\n"
      "v_pk_mul_f32 v[222:223], v[222:223], v[154:155] op_sel:[0,1]\n"
      "v_cvt_pk_bf16_f32 v208, v208, v209\n"
      "v_cvt_pk_bf16_f32 v209, v210, v211\n"
      "v_mov_b32 v154, v152\n"
      "v_cvt_pk_bf16_f32 v237, v238, v239\n"
      "v_pk_mul_f32 v[240:241], v[240:241], v[176:177] op_sel_hi:[1,0]\n"
      "v_cvt_pk_bf16_f32 v239, v242, v243\n"
      "v_cvt_pk_bf16_f32 v238, v240, v241\n"
      "s_waitcnt lgkmcnt(0)\n"
      "global_store_dwordx4 v116, v[200:203], s[54:55] sc1\n"
      "ds_write_b64 v127, v[208:209]\n"
      "ds_write_b128 v126, v[236:239]\n"
      "v_pk_mul_f32 v[172:173], v[172:173], v[154:155] op_sel:[0,1]\n"
      "v_pk_mul_f32 v[174:175], v[174:175], v[154:155] op_sel:[0,1]\n"
      "v_cvt_pk_bf16_f32 v140, v140, v141\n"
      "v_cvt_pk_bf16_f32 v172, v172, v173\n"
      "v_permlane16_swap_b32_e64 v152, v154\n"
      "v_pk_mul_f32 v[142:143], v[142:143], v[178:179] op_sel_hi:[1,0]\n"
      "v_pk_mul_f32 v[170:171], v[170:171], v[154:155] op_sel:[0,1]\n"
      "v_cvt_pk_bf16_f32 v141, v142, v143\n"
      "v_pk_mul_f32 v[220:221], v[220:221], v[154:155] op_sel:[0,1]\n"
      "v_cvt_pk_bf16_f32 v143, v190, v191\n"
      "v_cvt_pk_bf16_f32 v173, v174, v175\n"
      "v_cvt_pk_bf16_f32 v175, v170, v171\n"
      "global_store_dwordx4 v116, v[196:199], s[54:55] offset:128 sc1\n"
      "v_pk_mul_f32 v[164:165], v[164:165], v[152:153] op_sel_hi:[1,0]\n"
      "v_pk_mul_f32 v[192:193], v[192:193], v[152:153] op_sel_hi:[1,0]\n"
      "v_cvt_pk_bf16_f32 v174, v168, v169\n"
      "v_pk_mul_f32 v[254:255], v[254:255], v[178:179] op_sel_hi:[1,0]\n"
      "v_pk_mul_f32 v[182:183], v[182:183], v[152:153] op_sel_hi:[1,0]\n"
      "v_cvt_pk_bf16_f32 v220, v220, v221\n"
      "v_pk_mul_f32 v[252:253], v[252:253], v[178:179] op_sel_hi:[1,0]\n"
      "v_pk_mul_f32 v[166:167], v[166:167], v[152:153] op_sel_hi:[1,0]\n"
      "v_cvt_pk_bf16_f32 v142, v188, v189\n"
      "v_cvt_pk_bf16_f32 v252, v252, v253\n"
      "v_cvt_pk_bf16_f32 v253, v254, v255\n"
      "v_pk_mul_f32 v[180:181], v[180:181], v[152:153] op_sel_hi:[1,0]\n"
      "ds_write_b128 v126, v[172:175] offset:2048\n"
      "ds_write_b64 v127, v[252:253] offset:4096\n"
      "ds_write_b128 v126, v[140:143] offset:4096\n"
      "global_store_dwordx4 v116, v[136:139], s[54:55] offset:256 sc1\n"
      "v_cvt_pk_bf16_f32 v164, v164, v165\n"
      "v_cvt_pk_bf16_f32 v165, v166, v167\n"
      "v_cvt_pk_bf16_f32 v192, v192, v193\n"
      "v_pk_mul_f32 v[194:195], v[194:195], v[152:153] op_sel_hi:[1,0]\n"
      "v_subrev_u32 v153, 73728, v126\n"
      "v_cvt_pk_bf16_f32 v193, v194, v195\n"
      "v_cvt_pk_bf16_f32 v195, v182, v183\n"
      "v_cvt_pk_bf16_f32 v221, v222, v223\n"
      "v_cvt_pk_bf16_f32 v194, v180, v181\n"
      "ds_write_b64 v179, v[164:165]\n"
      "ds_write_b64 v127, v[220:221] offset:2048\n"
      "ds_write_b128 v153, v[192:195]\n"
      "global_store_dwordx4 v117, v[148:151], s[54:55] sc1\n"
      "v_pk_mul_f32 v[162:163], v[162:163], v[154:155] op_sel_hi:[1,0]\n"
      "v_pk_mul_f32 v[212:213], v[212:213], v[154:155] op_sel_hi:[1,0]\n"
      "v_cvt_pk_bf16_f32 v212, v212, v213\n"
      "v_pk_mul_f32 v[214:215], v[214:215], v[154:155] op_sel_hi:[1,0]\n"
      "v_pk_mul_f32 v[160:161], v[160:161], v[154:155] op_sel_hi:[1,0]\n"
      "v_pk_mul_f32 v[184:185], v[184:185], v[154:155] op_sel_hi:[1,0]\n"
      "v_pk_mul_f32 v[186:187], v[186:187], v[154:155] op_sel_hi:[1,0]\n"
      "v_cvt_pk_bf16_f32 v213, v214, v215\n"
      "v_cvt_pk_bf16_f32 v184, v184, v185\n"
      "v_cvt_pk_bf16_f32 v185, v186, v187\n"
      "v_cvt_pk_bf16_f32 v187, v162, v163\n"
      "v_cvt_pk_bf16_f32 v186, v160, v161\n"
      "ds_write_b128 v153, v[184:187] offset:2048\n"
      "ds_write_b64 v179, v[212:213] offset:2048\n"
      "s_mov_b32 exec_hi, 0\n"
      "global_store_dwordx4 v118, v[144:147], s[54:55] sc1\n"
      "s_mov_b32 exec_hi, -1\n"
      "s_waitcnt lgkmcnt(0)\n"
      "s_barrier\n"
      "ds_read_b128 v[152:155], v119 offset:6144\n"
      "ds_read_b128 v[184:187], v119\n"
      "s_add_u32 s27, s27, 1\n"
      "v_subrev_u32 v157, 73728, v119\n"
      "s_sub_u32 s28, s27, 3\n"
      "ds_read_b128 v[188:191], v157\n"
      "ds_read_b128 v[140:143], v121\n"
      "ds_read_b128 v[196:199], v157 offset:6144\n"
      "s_cselect_b64 s[54:55], s[12:13], s[22:23]\n"
      "ds_read_b128 v[176:179], v157 offset:12288\n"
      "ds_read_b128 v[180:183], v120\n"
      "s_max_i32 m0, s28, 0\n"
      "ds_read_b128 v[192:195], v119 offset:12288\n"
      "s_movrels_b32 s9, s80\n"
      "s_lshr_b32 s8, s9, 8\n"
      "s_and_b32 s9, s9, 0xff\n"
      "s_mul_i32 s28, s8, 1373568\n"
      "s_mul_i32 s29, s9, 2688\n"
      "s_add_u32 s28, s28, s29\n"
      "s_cmp_eq_u32 s8, 72\n"
      "s_cselect_b32 s8, 0, 100270464\n"
      "s_cmp_eq_u32 s9, 72\n"
      "s_cselect_b32 s9, 0, 196224\n"
      "s_add_u32 s10, s8, s9\n"
      "s_add_u32 s28, s28, s10\n"
      "s_add_u32 s54, s54, s28\n"
      "s_addc_u32 s55, s55, 0\n"
      "s_cmp_lg_u32 s10, s11\n"
      "s_waitcnt lgkmcnt(6)\n"
      "s_cbranch_scc1 L_edge_39_%=\n"
      "L_eb_40_%=:\n"
      "global_store_dwordx4 v116, v[184:187], s[54:55] sc1\n"
      "global_store_dwordx4 v116, v[152:155], s[54:55] offset:128 sc1\n"
      "s_waitcnt lgkmcnt(2)\n"
      "s_waitcnt lgkmcnt(1)\n"
      "s_waitcnt lgkmcnt(0)\n"
      "global_store_dwordx4 v116, v[192:195], s[54:55] offset:256 sc1\n"
      "global_store_dwordx4 v117, v[180:183], s[54:55] sc1\n"
      "s_mov_b32 exec_hi, 0\n"
      "global_store_dwordx4 v118, v[140:143], s[54:55] sc1\n"
      "s_mov_b32 exec_hi, -1\n"
      "s_mov_b64 exec, s[48:49]\n"
      "s_cbranch_execz L_np_41_%=\n"
      "global_store_dwordx4 v156, v[188:191], s[22:23] sc1\n"
      "global_store_dwordx4 v156, v[196:199], s[22:23] offset:128 sc1\n"
      "global_store_dwordx4 v156, v[176:179], s[22:23] offset:256 sc1\n"
      "L_np_41_%=:\n"
      "s_mov_b64 exec, -1\n"
      "s_nop 0\n"
      "s_endpgm\n"
      "L_tail1_%=:\n"
      "s_mov_b32 s11, s10\n"
      "s_sub_u32 s28, s60, s8\n"
      "s_sub_u32 s29, s61, s9\n"
      "s_mov_b32 s60, s8\n"
      "s_mov_b32 s61, s9\n"
      "s_mov_b64 exec, s[64:65]\n"
      "v_add_u32 v116, s28, v116\n"
      "s_mov_b64 exec, s[70:71]\n"
      "v_add_u32 v116, s29, v116\n"
      "s_mov_b64 exec, s[66:67]\n"
      "v_add_u32 v117, s28, v117\n"
      "s_mov_b64 exec, s[72:73]\n"
      "v_add_u32 v117, s29, v117\n"
      "s_mov_b64 exec, s[68:69]\n"
      "v_add_u32 v118, s28, v118\n"
      "s_mov_b64 exec, s[74:75]\n"
      "v_add_u32 v118, s29, v118\n"
      "s_mov_b64 exec, -1\n"
      "s_nop 4\n"
      "s_cmp_lt_u32 s27, s26\n"
      "s_cbranch_scc1 L_mid_%=\n"
      "s_nop 7\n"
      "s_nop 7\n"
      "v_rcp_f32 v136, v136\n"
      "v_mbcnt_lo_u32_b32 v137, -1, 0\n"
      "v_pk_mul_f32 v[154:155], v[154:155], v[136:137] op_sel_hi:[1,0]\n"
      "v_pk_mul_f32 v[140:141], v[140:141], v[136:137] op_sel_hi:[1,0]\n"
      "v_pk_mul_f32 v[158:159], v[158:159], v[136:137] op_sel_hi:[1,0]\n"
      "v_mbcnt_hi_u32_b32 v137, -1, v137\n"
      "v_pk_mul_f32 v[142:143], v[142:143], v[136:137] op_sel_hi:[1,0]\n"
      "v_pk_mul_f32 v[152:153], v[152:153], v[136:137] op_sel_hi:[1,0]\n"
      "v_cvt_pk_bf16_f32 v140, v140, v141\n"
      "v_lshrrev_b32_e64 v138, 4, v137\n"
      "v_and_b32_e64 v144, 15, v137\n"
      "v_pk_mul_f32 v[156:157], v[156:157], v[136:137] op_sel_hi:[1,0]\n"
      "v_mul_u32_u24 v138, 352, v138\n"
      "v_cvt_pk_bf16_f32 v141, v142, v143\n"
      "v_cvt_pk_bf16_f32 v143, v154, v155\n"
      "v_lshl_add_u32 v138, v144, 4, v138\n"
      "v_cvt_pk_bf16_f32 v142, v152, v153\n"
      "v_add_u32 v138, 150640, v138\n"
      "v_cvt_pk_bf16_f32 v156, v156, v157\n"
      "v_cvt_pk_bf16_f32 v157, v158, v159\n"
      "ds_write_b64 v125, v[156:157] offset:18944\n"
      "ds_write_b128 v124, v[140:143] offset:18944\n"
      "s_waitcnt lgkmcnt(0)\n"
      "s_barrier\n"
      "s_sub_u32 s28, s27, 3\n"
      "ds_read_b128 v[140:143], v123 offset:18432\n"
      "ds_read_b128 v[152:155], v123 offset:21504\n"
      "ds_read_b128 v[156:159], v128 offset:1024\n"
      "ds_read_b128 v[212:215], v119 offset:24576\n"
      "ds_read_b128 v[216:219], v123 offset:18688\n"
      "s_cselect_b64 s[54:55], s[12:13], s[22:23]\n"
      "s_max_i32 m0, s28, 0\n"
      "ds_read_b128 v[144:147], v119 offset:18432\n"
      "ds_read_b128 v[208:211], v123 offset:27648\n"
      "s_movrels_b32 s9, s80\n"
      "ds_read_b128 v[252:255], v123 offset:24576\n"
      "s_lshr_b32 s8, s9, 8\n"
      "s_and_b32 s9, s9, 0xff\n"
      "s_mul_i32 s28, s8, 1373568\n"
      "ds_read_b128 v[148:151], v120 offset:18432\n"
      "s_mul_i32 s29, s9, 2688\n"
      "s_add_u32 s28, s28, s29\n"
      "s_cmp_eq_u32 s8, 72\n"
      "ds_read_b128 v[160:163], v119 offset:30720\n"
      "s_cselect_b32 s8, 0, 100270464\n"
      "ds_read_b128 v[164:167], v121 offset:18432\n"
      "s_cmp_eq_u32 s9, 72\n"
      "s_cselect_b32 s9, 0, 196224\n"
      "s_add_u32 s10, s8, s9\n"
      "s_add_u32 s28, s28, s10\n"
      "s_waitcnt lgkmcnt(9)\n"
      "v_mfma_f32_16x16x32_bf16 v[220:223], a[216:219], v[140:143], 0\n"
      "s_add_u32 s54, s54, s28\n"
      "s_waitcnt lgkmcnt(8)\n"
      "v_add_f32 v158, |v158|, |v159|\n"
      "v_mfma_f32_16x16x32_bf16 v[220:223], a[228:231], v[152:155], v[220:223]\n"
      "ds_read_b128 v[236:239], v123 offset:30720\n"
      "v_add_f32 v156, |v156|, |v157|\n"
      "v_mfma_f32_16x16x32_bf16 v[240:243], a[220:223], v[140:143], 0\n"
      "ds_read_b128 v[204:207], v123 offset:33792\n"
      "v_add_f32 v156, v156, v158\n"
      "v_mfma_f32_16x16x32_bf16 v[240:243], a[232:235], v[152:155], v[240:243]\n"
      "s_addc_u32 s55, s55, 0\n"
      "v_rsq_f32 v156, v156\n"
      "v_mfma_f32_16x16x32_bf16 v[224:227], a[224:227], v[140:143], 0\n"
      "s_waitcnt lgkmcnt(5)\n"
      "v_mul_f32 v156, s41, v156\n"
      "v_mov_b32_e64 v136, v156\n"
      "v_mfma_f32_16x16x32_bf16 v[224:227], a[236:239], v[152:155], v[224:227]\n"
      "ds_read_b128 v[140:143], v123 offset:21760\n"
      "v_permlane32_swap_b32 v156, v136\n"
      "v_mov_b32 v139, v156\n"
      "v_mfma_f32_16x16x32_bf16 v[240:243], a[244:247], v[252:255], v[240:243]\n"
      "s_cmp_lg_u32 s10, s11\n"
      "v_permlane16_swap_b32 v156, v139\n"
      "v_mfma_f32_16x16x32_bf16 v[220:223], a[240:243], v[252:255], v[220:223]\n"
      "s_cbranch_scc1 L_edge_42_%=\n"
      "L_eb_43_%=:\n"
      "v_mul_f32 v171, v156, v171\n"
      "v_mfma_f32_16x16x32_bf16 v[220:223], a[252:255], v[208:211], v[220:223]\n"
      "global_store_dwordx4 v116, v[144:147], s[54:55] sc1\n"
      "v_mfma_f32_16x16x32_bf16 v[240:243], v[0:3], v[208:211], v[240:243]\n"
      "v_mul_f32 v169, v156, v169\n"
      "v_mul_f32 v168, v156, v168\n"
      "v_mfma_f32_16x16x32_bf16 v[224:227], a[248:251], v[252:255], v[224:227]\n"
      "ds_read_b128 v[252:255], v123 offset:30976\n"
      "v_mul_f32 v175, v156, v175\n"
      "v_mfma_f32_16x16x32_bf16 v[224:227], v[4:7], v[208:211], v[224:227]\n"
      "ds_read_b128 v[208:211], v123 offset:34048\n"
      "s_waitcnt lgkmcnt(3)\n"
      "v_cvt_pk_bf16_f32 v168, v168, v169\n"
      "v_mfma_f32_16x16x32_bf16 v[220:223], v[8:11], v[236:239], v[220:223]\n"
      "ds_read_b128 v[152:155], v123 offset:24832\n"
      "v_mul_f32_e64 v170, v156, v170\n"
      "v_mfma_f32_16x16x32_bf16 v[220:223], v[20:23], v[204:207], v[220:223]\n"
      "ds_read_b128 v[228:231], v123 offset:27904\n"
      "v_cvt_pk_bf16_f32 v169, v170, v171\n"
      "v_mfma_f32_16x16x32_bf16 v[240:243], v[12:15], v[236:239], v[240:243]\n"
      "v_mul_f32 v174, v156, v174\n"
      "v_mul_f32 v173, v156, v173\n"
      "v_mfma_f32_16x16x32_bf16 v[240:243], v[24:27], v[204:207], v[240:243]\n"
      "global_store_dwordx4 v116, v[212:215], s[54:55] offset:128 sc1\n"
      "v_mfma_f32_16x16x32_bf16 v[224:227], v[16:19], v[236:239], v[224:227]\n"
      "s_waitcnt lgkmcnt(4)\n"
      "v_mul_f32 v172, v156, v172\n"
      "v_mul_f32_e64 v176, v156, v176\n"
      "v_mfma_f32_16x16x32_bf16 v[224:227], v[28:31], v[204:207], v[224:227]\n"
      "v_cvt_pk_bf16_f32 v171, v174, v175\n"
      "v_cvt_pk_bf16_f32 v170, v172, v173\n"
      "v_mfma_f32_16x16x32_bf16 v[204:207], a[216:219], v[216:219], 0\n"
      "ds_read_b128 v[212:215], v123 offset:18944\n"
      "v_mul_f32_e64 v177, v156, v177\n"
      "v_mfma_f32_16x16x32_bf16 v[204:207], a[228:231], v[140:143], v[204:207]\n"
      "ds_write_b128 v126, v[168:171]\n"
      "v_mfma_f32_16x16x32_bf16 v[168:171], a[220:223], v[216:219], 0\n"
      "ds_read_b128 v[172:175], v123 offset:22016\n"
      "v_cvt_pk_bf16_f32 v176, v176, v177\n"
      "v_mfma_f32_16x16x32_bf16 v[168:171], a[232:235], v[140:143], v[168:171]\n"
      "s_waitcnt lgkmcnt(3)\n"
      "v_mul_f32 v178, v156, v178\n"
      "v_mul_f32_e64 v179, v156, v179\n"
      "v_mfma_f32_16x16x32_bf16 v[236:239], a[224:227], v[216:219], 0\n"
      "v_cvt_pk_bf16_f32 v177, v178, v179\n"
      "v_mul_f32_e64 v180, v139, v180\n"
      "v_mfma_f32_16x16x32_bf16 v[236:239], a[236:239], v[140:143], v[236:239]\n"
      "ds_write_b64 v127, v[176:177]\n"
      "v_mfma_f32_16x16x32_bf16 v[204:207], a[240:243], v[152:155], v[204:207]\n"
      "ds_read_b128 v[140:143], v123 offset:25088\n"
      "v_mul_f32_e64 v181, v139, v181\n"
      "v_mfma_f32_16x16x32_bf16 v[204:207], a[252:255], v[228:231], v[204:207]\n"
      "ds_read_b128 v[176:179], v123 offset:28160\n"
      "v_cvt_pk_bf16_f32 v180, v180, v181\n"
      "v_mfma_f32_16x16x32_bf16 v[168:171], a[244:247], v[152:155], v[168:171]\n"
      "s_lshl_b32 s29, s24, 8\n"
      "v_mul_f32 v182, v139, v182\n"
      "v_mul_f32 v183, v139, v183\n"
      "v_mfma_f32_16x16x32_bf16 v[168:171], v[0:3], v[228:231], v[168:171]\n"
      "v_cvt_pk_bf16_f32 v181, v182, v183\n"
      "v_mul_f32 v184, v139, v184\n"
      "v_mfma_f32_16x16x32_bf16 v[236:239], a[248:251], v[152:155], v[236:239]\n"
      "v_mul_f32 v185, v139, v185\n"
      "v_cvt_pk_bf16_f32 v182, v184, v185\n"
      "v_mfma_f32_16x16x32_bf16 v[236:239], v[4:7], v[228:231], v[236:239]\n"
      "v_mul_f32 v187, v139, v187\n"
      "v_mul_f32 v186, v139, v186\n"
      "v_mfma_f32_16x16x32_bf16 v[204:207], v[8:11], v[252:255], v[204:207]\n"
      "v_mul_f32 v188, v139, v188\n"
      "v_cvt_pk_bf16_f32 v183, v186, v187\n"
      "v_mfma_f32_16x16x32_bf16 v[204:207], v[20:23], v[208:211], v[204:207]\n"
      "ds_write_b128 v126, v[180:183] offset:2048\n"
      "v_mfma_f32_16x16x32_bf16 v[168:171], v[12:15], v[252:255], v[168:171]\n"
      "ds_read_b128 v[180:183], v123 offset:31232\n"
      "v_mul_f32_e64 v189, v139, v189\n"
      "v_mfma_f32_16x16x32_bf16 v[168:171], v[24:27], v[208:211], v[168:171]\n"
      "ds_read_b128 v[184:187], v123 offset:34304\n"
      "v_cvt_pk_bf16_f32 v188, v188, v189\n"
      "v_mfma_f32_16x16x32_bf16 v[236:239], v[16:19], v[252:255], v[236:239]\n"
      "s_waitcnt lgkmcnt(3)\n"
      "v_mul_f32 v190, v139, v190\n"
      "v_mul_f32_e64 v191, v139, v191\n"
      "v_mfma_f32_16x16x32_bf16 v[236:239], v[28:31], v[208:211], v[236:239]\n"
      "s_add_u32 s29, s29, 159088\n"
      "v_cvt_pk_bf16_f32 v189, v190, v191\n"
      "v_fma_f32 v139, v220, v220, s39\n"
      "v_mfma_f32_16x16x32_bf16 v[208:211], a[216:219], v[212:215], 0\n"
      "global_store_dwordx4 v116, v[160:163], s[54:55] offset:256 sc1\n"
      "v_mfma_f32_16x16x32_bf16 v[208:211], a[228:231], v[172:175], v[208:211]\n"
      "ds_write_b64 v127, v[188:189] offset:2048\n"
      "v_mfma_f32_16x16x32_bf16 v[188:191], a[220:223], v[212:215], 0\n"
      "ds_read_b128 v[160:163], v138\n"
      "v_fmac_f32 v139, v221, v221\n"
      "v_mfma_f32_16x16x32_bf16 v[188:191], a[232:235], v[172:175], v[188:191]\n"
      "ds_read_b128 v[252:255], v138 offset:1408\n"
      "v_fmac_f32 v139, v222, v222\n"
      "v_mfma_f32_16x16x32_bf16 v[228:231], a[224:227], v[212:215], 0\n"
      "v_fmac_f32 v139, v223, v223\n"
      "v_fmac_f32 v139, v240, v240\n"
      "v_mfma_f32_16x16x32_bf16 v[228:231], a[236:239], v[172:175], v[228:231]\n"
      "s_waitcnt lgkmcnt(3)\n"
      "v_fmac_f32 v139, v241, v241\n"
      "v_fmac_f32_e64 v139, v242, v242\n"
      "v_mfma_f32_16x16x32_bf16 v[208:211], a[240:243], v[140:143], v[208:211]\n"
      "v_fmac_f32 v139, v243, v243\n"
      "v_fmac_f32 v139, v224, v224\n"
      "v_mfma_f32_16x16x32_bf16 v[208:211], a[252:255], v[176:179], v[208:211]\n"
      "v_fmac_f32 v139, v225, v225\n"
      "v_fmac_f32 v139, v226, v226\n"
      "v_mfma_f32_16x16x32_bf16 v[188:191], a[244:247], v[140:143], v[188:191]\n"
      "global_store_dwordx4 v117, v[148:151], s[54:55] sc1\n"
      "v_mfma_f32_16x16x32_bf16 v[188:191], v[0:3], v[176:179], v[188:191]\n"
      "v_fmac_f32 v139, v227, v227\n"
      "v_mul_f32 v192, v136, v192\n"
      "v_mfma_f32_16x16x32_bf16 v[228:231], a[248:251], v[140:143], v[228:231]\n"
      "ds_read_b128 v[140:143], v138 offset:2816\n"
      "v_mul_f32 v193, v136, v193\n"
      "v_mfma_f32_16x16x32_bf16 v[228:231], v[4:7], v[176:179], v[228:231]\n"
      "v_mul_f32 v194, v136, v194\n"
      "v_cvt_pk_bf16_f32 v192, v192, v193\n"
      "v_mfma_f32_16x16x32_bf16 v[208:211], v[8:11], v[180:183], v[208:211]\n"
      "ds_read_b128 v[176:179], v138 offset:4224\n"
      "v_mul_f32_e64 v195, v136, v195\n"
      "v_mfma_f32_16x16x32_bf16 v[208:211], v[20:23], v[184:187], v[208:211]\n"
      "v_cvt_pk_bf16_f32 v193, v194, v195\n"
      "v_mul_f32 v196, v136, v196\n"
      "v_mfma_f32_16x16x32_bf16 v[188:191], v[12:15], v[180:183], v[188:191]\n"
      "v_mul_f32 v197, v136, v197\n"
      "v_cvt_pk_bf16_f32 v194, v196, v197\n"
      "v_mfma_f32_16x16x32_bf16 v[188:191], v[24:27], v[184:187], v[188:191]\n"
      "ds_read_b128 v[148:151], v138 offset:5632\n"
      "v_mul_f32_e64 v198, v136, v198\n"
      "v_mfma_f32_16x16x32_bf16 v[228:231], v[16:19], v[180:183], v[228:231]\n"
      "s_waitcnt lgkmcnt(3)\n"
      "v_mul_f32 v199, v136, v199\n"
      "v_cvt_pk_bf16_f32 v195, v198, v199\n"
      "v_mfma_f32_16x16x32_bf16 v[228:231], v[28:31], v[184:187], v[228:231]\n"
      "ds_write_b128 v126, v[192:195] offset:4096\n"
      "v_mfma_f32_16x16x32_bf16 v[192:195], a[216:219], v[160:163], 0\n"
      "ds_read_b128 v[184:187], v138 offset:7040\n"
      "v_mul_f32_e64 v202, v136, v202\n"
      "v_mfma_f32_16x16x32_bf16 v[192:195], a[228:231], v[252:255], v[192:195]\n"
      "v_mul_f32 v200, v136, v200\n"
      "v_mul_f32 v201, v136, v201\n"
      "v_mfma_f32_16x16x32_bf16 v[180:183], a[220:223], v[160:163], 0\n"
      "v_cvt_pk_bf16_f32 v200, v200, v201\n"
      "v_mul_f32 v203, v136, v203\n"
      "v_mfma_f32_16x16x32_bf16 v[180:183], a[232:235], v[252:255], v[180:183]\n"
      "ds_read_b128 v[196:199], v138 offset:256\n"
      "v_fma_f32 v136, v204, v204, s39\n"
      "v_mfma_f32_16x16x32_bf16 v[172:175], a[224:227], v[160:163], 0\n"
      "s_waitcnt lgkmcnt(4)\n"
      "v_cvt_pk_bf16_f32 v201, v202, v203\n"
      "v_mfma_f32_16x16x32_bf16 v[172:175], a[236:239], v[252:255], v[172:175]\n"
      "ds_write_b64 v127, v[200:201] offset:4096\n"
      "v_mfma_f32_16x16x32_bf16 v[192:195], a[240:243], v[140:143], v[192:195]\n"
      "ds_read_b128 v[252:255], v138 offset:1664\n"
      "v_fmac_f32 v136, v205, v205\n"
      "v_mfma_f32_16x16x32_bf16 v[192:195], a[252:255], v[176:179], v[192:195]\n"
      "v_fmac_f32 v136, v206, v206\n"
      "v_fmac_f32 v136, v207, v207\n"
      "v_mfma_f32_16x16x32_bf16 v[180:183], a[244:247], v[140:143], v[180:183]\n"
      "s_waitcnt lgkmcnt(3)\n"
      "v_fmac_f32 v136, v168, v168\n"
      "v_fmac_f32 v136, v169, v169\n"
      "v_mfma_f32_16x16x32_bf16 v[180:183], v[0:3], v[176:179], v[180:183]\n"
      "ds_read_b128 v[200:203], v138 offset:3072\n"
      "v_fmac_f32 v136, v170, v170\n"
      "v_mfma_f32_16x16x32_bf16 v[172:175], a[248:251], v[140:143], v[172:175]\n"
      "ds_read_b128 v[140:143], v138 offset:4480\n"
      "v_fmac_f32 v136, v171, v171\n"
      "v_mfma_f32_16x16x32_bf16 v[172:175], v[4:7], v[176:179], v[172:175]\n"
      "v_fmac_f32 v136, v236, v236\n"
      "v_fmac_f32 v136, v237, v237\n"
      "v_mfma_f32_16x16x32_bf16 v[192:195], v[8:11], v[148:151], v[192:195]\n"
      "v_fmac_f32 v136, v238, v238\n"
      "v_fmac_f32 v136, v239, v239\n"
      "v_mfma_f32_16x16x32_bf16 v[192:195], v[20:23], v[184:187], v[192:195]\n"
      "ds_read_b128 v[176:179], v138 offset:5888\n"
      "v_lshl_add_u32 v212, v137, 2, s29\n"
      "v_mfma_f32_16x16x32_bf16 v[180:183], v[12:15], v[148:151], v[180:183]\n"
      "v_lshrrev_b32 v160, 3, v137\n"
      "s_waitcnt lgkmcnt(3)\n"
      "v_mfma_f32_16x16x32_bf16 v[180:183], v[24:27], v[184:187], v[180:183]\n"
      "ds_read_b32 v212, v212\n"
      "v_fma_f32 v137, v208, v208, s39\n"
      "v_mfma_f32_16x16x32_bf16 v[172:175], v[16:19], v[148:151], v[172:175]\n"
      "ds_read_b128 v[148:151], v138 offset:7296\n"
      "v_fmac_f32 v137, v209, v209\n"
      "v_mfma_f32_16x16x32_bf16 v[172:175], v[28:31], v[184:187], v[172:175]\n"
      "s_lshl_b32 s29, s24, 3\n"
      "v_add_u32 v160, s29, v160\n"
      "v_fmac_f32 v137, v210, v210\n"
      "v_mfma_f32_16x16x32_bf16 v[184:187], a[216:219], v[196:199], 0\n"
      "v_fmac_f32_e64 v137, v211, v211\n"
      "v_cmp_gt_u32 s[48:49], s26, v160\n"
      "v_mfma_f32_16x16x32_bf16 v[184:187], a[228:231], v[252:255], v[184:187]\n"
      "s_mov_b32 exec_hi, 0\n"
      "global_store_dwordx4 v118, v[164:167], s[54:55] sc1\n"
      "s_mov_b32 exec_hi, -1\n"
      "v_mfma_f32_16x16x32_bf16 v[160:163], a[220:223], v[196:199], 0\n"
      "v_fmac_f32 v137, v188, v188\n"
      "s_waitcnt lgkmcnt(3)\n"
      "v_mfma_f32_16x16x32_bf16 v[160:163], a[232:235], v[252:255], v[160:163]\n"
      "v_fmac_f32 v137, v189, v189\n"
      "v_fmac_f32 v137, v190, v190\n"
      "v_mfma_f32_16x16x32_bf16 v[164:167], a[224:227], v[196:199], 0\n"
      "v_fmac_f32 v137, v191, v191\n"
      "v_fmac_f32 v137, v228, v228\n"
      "v_mfma_f32_16x16x32_bf16 v[164:167], a[236:239], v[252:255], v[164:167]\n"
      "v_fmac_f32 v137, v229, v229\n"
      "v_fmac_f32 v137, v230, v230\n"
      "v_mfma_f32_16x16x32_bf16 v[184:187], a[240:243], v[200:203], v[184:187]\n"
      "s_waitcnt lgkmcnt(0)\n"
      "v_fmac_f32 v137, v231, v231\n"
      "v_mfma_f32_16x16x32_bf16 v[184:187], a[252:255], v[140:143], v[184:187]\n"
      "v_permlane16_swap_b32 v139, v136\n"
      "v_mfma_f32_16x16x32_bf16 v[160:163], a[244:247], v[200:203], v[160:163]\n"
      "v_mov_b32 v214, v137\n"
      "v_add_f32 v139, v139, v136\n"
      "v_mfma_f32_16x16x32_bf16 v[160:163], v[0:3], v[140:143], v[160:163]\n"
      "v_permlane16_swap_b32 v137, v214\n"
      "v_mfma_f32_16x16x32_bf16 v[164:167], a[248:251], v[200:203], v[164:167]\n"
      "v_add_f32_e64 v137, v137, v214\n"
      "v_fma_f32 v136, v192, v192, s39\n"
      "v_mfma_f32_16x16x32_bf16 v[164:167], v[4:7], v[140:143], v[164:167]\n"
      "v_permlane32_swap_b32 v139, v137\n"
      "v_mfma_f32_16x16x32_bf16 v[184:187], v[8:11], v[176:179], v[184:187]\n"
      "v_fmac_f32 v136, v193, v193\n"
      "v_add_f32 v139, v139, v137\n"
      "v_mfma_f32_16x16x32_bf16 v[184:187], v[20:23], v[148:151], v[184:187]\n"
      "ds_write_b32 v129, v139\n"
      "v_mfma_f32_16x16x32_bf16 v[160:163], v[12:15], v[176:179], v[160:163]\n"
      "v_fmac_f32 v136, v194, v194\n"
      "v_fmac_f32 v136, v195, v195\n"
      "v_mfma_f32_16x16x32_bf16 v[160:163], v[24:27], v[148:151], v[160:163]\n"
      "v_fmac_f32 v136, v180, v180\n"
      "v_fmac_f32 v136, v181, v181\n"
      "v_mfma_f32_16x16x32_bf16 v[164:167], v[16:19], v[176:179], v[164:167]\n"
      "v_fmac_f32 v136, v182, v182\n"
      "v_fmac_f32 v136, v183, v183\n"
      "v_mfma_f32_16x16x32_bf16 v[164:167], v[28:31], v[148:151], v[164:167]\n"
      "v_fmac_f32 v136, v172, v172\n"
      "v_fmac_f32 v136, v173, v173\n"
      "v_fmac_f32 v136, v174, v174\n"
      "v_fmac_f32_e64 v136, v175, v175\n"
      "v_pk_mul_f32 v[214:215], v[184:185], v[184:185]\n"
      "v_pk_fma_f32 v[214:215], v[186:187], v[186:187], v[214:215]\n"
      "v_pk_fma_f32 v[214:215], v[160:161], v[160:161], v[214:215]\n"
      "v_pk_fma_f32 v[214:215], v[162:163], v[162:163], v[214:215]\n"
      "v_pk_fma_f32 v[214:215], v[164:165], v[164:165], v[214:215]\n"
      "v_pk_fma_f32 v[214:215], v[166:167], v[166:167], v[214:215]\n"
      "v_add_f32 v214, s39, v214\n"
      "v_add_f32 v139, v214, v215\n"
      "v_mov_b32 v137, v139\n"
      "v_mov_b32 v138, v137\n"
      "v_permlane16_swap_b32 v136, v139\n"
      "v_add_f32_e64 v136, v136, v139\n"
      "v_permlane16_swap_b32_e64 v137, v138\n"
      "v_add_f32_e64 v137, v137, v138\n"
      "s_nop 1\n"
      "v_permlane32_swap_b32 v136, v137\n"
      "v_add_f32_e64 v136, v136, v137\n"
      "ds_write_b32 v129, v136 offset:2048\n"
      "s_waitcnt lgkmcnt(0)\n"
      "s_barrier\n"
      "s_add_u32 s27, s27, 1\n"
      "ds_read_b128 v[148:151], v128\n"
      "ds_read_b128 v[176:179], v128 offset:2048\n"
      "s_sub_u32 s28, s27, 3\n"
      "ds_read_b128 v[136:139], v119\n"
      "s_cselect_b64 s[54:55], s[12:13], s[22:23]\n"
      "ds_read_b128 v[196:199], v121\n"
      "s_max_i32 m0, s28, 0\n"
      "ds_read_b128 v[252:255], v120\n"
      "s_movrels_b32 s9, s80\n"
      "s_lshr_b32 s8, s9, 8\n"
      "s_and_b32 s9, s9, 0xff\n"
      "s_mul_i32 s28, s8, 1373568\n"
      "ds_read_b128 v[140:143], v119 offset:6144\n"
      "s_mul_i32 s29, s9, 2688\n"
      "s_add_u32 s28, s28, s29\n"
      "s_cmp_eq_u32 s8, 72\n"
      "s_cselect_b32 s8, 0, 100270464\n"
      "s_cmp_eq_u32 s9, 72\n"
      "s_cselect_b32 s9, 0, 196224\n"
      "s_add_u32 s10, s8, s9\n"
      "s_add_u32 s28, s28, s10\n"
      "s_add_u32 s54, s54, s28\n"
      "s_addc_u32 s55, s55, 0\n"
      "s_cmp_lg_u32 s10, s11\n"
      "ds_read_b128 v[200:203], v119 offset:12288\n"
      "s_cbranch_scc1 L_edge_44_%=\n"
      "L_eb_45_%=:\n"
      "s_waitcnt lgkmcnt(5)\n"
      "v_add_f32 v150, |v150|, |v151|\n"
      "v_add_f32 v148, |v148|, |v149|\n"
      "v_add_f32 v148, v148, v150\n"
      "v_rsq_f32_e64 v148, v148\n"
      "v_subrev_u32 v151, 73728, v127\n"
      "v_mul_f32 v148, s41, v148\n"
      "v_mov_b32 v150, v148\n"
      "v_add_f32 v178, |v178|, |v179|\n"
      "v_add_f32 v176, |v176|, |v177|\n"
      "v_permlane32_swap_b32 v148, v150\n"
      "v_pk_mul_f32 v[190:191], v[190:191], v[150:151] op_sel_hi:[1,0]\n"
      "v_mov_b32 v179, v148\n"
      "v_add_f32 v176, v176, v178\n"
      "v_rsq_f32 v176, v176\n"
      "v_permlane16_swap_b32 v148, v179\n"
      "v_mul_f32 v176, s41, v176\n"
      "v_mov_b32_e64 v178, v176\n"
      "v_pk_mul_f32 v[222:223], v[222:223], v[148:149] op_sel_hi:[1,0]\n"
      "v_pk_mul_f32 v[220:221], v[220:221], v[148:149] op_sel_hi:[1,0]\n"
      "v_pk_mul_f32 v[242:243], v[242:243], v[148:149] op_sel_hi:[1,0]\n"
      "v_pk_mul_f32 v[188:189], v[188:189], v[150:151] op_sel_hi:[1,0]\n"
      "v_pk_mul_f32 v[208:209], v[208:209], v[150:151] op_sel_hi:[1,0]\n"
      "v_permlane32_swap_b32_e64 v176, v178\n"
      "v_cvt_pk_bf16_f32 v220, v220, v221\n"
      "v_pk_mul_f32 v[224:225], v[224:225], v[148:149] op_sel_hi:[1,0]\n"
      "v_pk_mul_f32 v[226:227], v[226:227], v[148:149] op_sel_hi:[1,0]\n"
      "v_pk_mul_f32 v[168:169], v[168:169], v[178:179] op_sel:[0,1]\n"
      "v_pk_mul_f32 v[238:239], v[238:239], v[178:179] op_sel:[0,1]\n"
      "v_cvt_pk_bf16_f32 v224, v224, v225\n"
      "v_cvt_pk_bf16_f32 v225, v226, v227\n"
      "v_mov_b32 v178, v176\n"
      "v_cvt_pk_bf16_f32 v221, v222, v223\n"
      "v_pk_mul_f32 v[240:241], v[240:241], v[148:149] op_sel_hi:[1,0]\n"
      "v_cvt_pk_bf16_f32 v223, v242, v243\n"
      "v_cvt_pk_bf16_f32 v222, v240, v241\n"
      "s_waitcnt lgkmcnt(0)\n"
      "global_store_dwordx4 v116, v[136:139], s[54:55] sc1\n"
      "ds_write_b64 v127, v[224:225] offset:18432\n"
      "ds_write_b128 v126, v[220:223] offset:18432\n"
      "v_pk_mul_f32 v[204:205], v[204:205], v[178:179] op_sel:[0,1]\n"
      "v_pk_mul_f32 v[206:207], v[206:207], v[178:179] op_sel:[0,1]\n"
      "v_cvt_pk_bf16_f32 v208, v208, v209\n"
      "v_cvt_pk_bf16_f32 v204, v204, v205\n"
      "v_permlane16_swap_b32_e64 v176, v178\n"
      "v_pk_mul_f32 v[210:211], v[210:211], v[150:151] op_sel_hi:[1,0]\n"
      "v_pk_mul_f32 v[170:171], v[170:171], v[178:179] op_sel:[0,1]\n"
      "v_cvt_pk_bf16_f32 v209, v210, v211\n"
      "v_pk_mul_f32 v[236:237], v[236:237], v[178:179] op_sel:[0,1]\n"
      "v_cvt_pk_bf16_f32 v211, v190, v191\n"
      "v_cvt_pk_bf16_f32 v205, v206, v207\n"
      "v_cvt_pk_bf16_f32 v207, v170, v171\n"
      "global_store_dwordx4 v116, v[140:143], s[54:55] offset:128 sc1\n"
      "v_pk_mul_f32 v[172:173], v[172:173], v[176:177] op_sel_hi:[1,0]\n"
      "v_pk_mul_f32 v[192:193], v[192:193], v[176:177] op_sel_hi:[1,0]\n"
      "v_cvt_pk_bf16_f32 v206, v168, v169\n"
      "v_pk_mul_f32 v[230:231], v[230:231], v[150:151] op_sel_hi:[1,0]\n"
      "v_pk_mul_f32 v[182:183], v[182:183], v[176:177] op_sel_hi:[1,0]\n"
      "v_cvt_pk_bf16_f32 v236, v236, v237\n"
      "v_pk_mul_f32 v[228:229], v[228:229], v[150:151] op_sel_hi:[1,0]\n"
      "v_pk_mul_f32 v[174:175], v[174:175], v[176:177] op_sel_hi:[1,0]\n"
      "v_cvt_pk_bf16_f32 v210, v188, v189\n"
      "v_cvt_pk_bf16_f32 v228, v228, v229\n"
      "v_cvt_pk_bf16_f32 v229, v230, v231\n"
      "v_pk_mul_f32 v[180:181], v[180:181], v[176:177] op_sel_hi:[1,0]\n"
      "ds_write_b128 v126, v[204:207] offset:20480\n"
      "ds_write_b64 v127, v[228:229] offset:22528\n"
      "ds_write_b128 v126, v[208:211] offset:22528\n"
      "global_store_dwordx4 v116, v[200:203], s[54:55] offset:256 sc1\n"
      "v_cvt_pk_bf16_f32 v172, v172, v173\n"
      "v_cvt_pk_bf16_f32 v173, v174, v175\n"
      "v_cvt_pk_bf16_f32 v192, v192, v193\n"
      "v_pk_mul_f32 v[194:195], v[194:195], v[176:177] op_sel_hi:[1,0]\n"
      "v_subrev_u32 v177, 73728, v126\n"
      "v_cvt_pk_bf16_f32 v193, v194, v195\n"
      "v_cvt_pk_bf16_f32 v195, v182, v183\n"
      "v_cvt_pk_bf16_f32 v237, v238, v239\n"
      "v_cvt_pk_bf16_f32 v194, v180, v181\n"
      "ds_write_b64 v151, v[172:173] offset:18432\n"
      "ds_write_b64 v127, v[236:237] offset:20480\n"
      "ds_write_b128 v177, v[192:195] offset:18432\n"
      "global_store_dwordx4 v117, v[252:255], s[54:55] sc1\n"
      "v_pk_mul_f32 v[162:163], v[162:163], v[178:179] op_sel_hi:[1,0]\n"
      "v_pk_mul_f32 v[164:165], v[164:165], v[178:179] op_sel_hi:[1,0]\n"
      "v_cvt_pk_bf16_f32 v164, v164, v165\n"
      "v_pk_mul_f32 v[166:167], v[166:167], v[178:179] op_sel_hi:[1,0]\n"
      "v_pk_mul_f32 v[160:161], v[160:161], v[178:179] op_sel_hi:[1,0]\n"
      "v_pk_mul_f32 v[184:185], v[184:185], v[178:179] op_sel_hi:[1,0]\n"
      "v_pk_mul_f32 v[186:187], v[186:187], v[178:179] op_sel_hi:[1,0]\n"
      "v_cvt_pk_bf16_f32 v165, v166, v167\n"
      "v_cvt_pk_bf16_f32 v184, v184, v185\n"
      "v_cvt_pk_bf16_f32 v185, v186, v187\n"
      "v_cvt_pk_bf16_f32 v187, v162, v163\n"
      "v_cvt_pk_bf16_f32 v186, v160, v161\n"
      "ds_write_b128 v177, v[184:187] offset:20480\n"
      "ds_write_b64 v151, v[164:165] offset:20480\n"
      "s_mov_b32 exec_hi, 0\n"
      "global_store_dwordx4 v118, v[196:199], s[54:55] sc1\n"
      "s_mov_b32 exec_hi, -1\n"
      "s_waitcnt lgkmcnt(0)\n"
      "s_barrier\n"
      "ds_read_b128 v[164:167], v119 offset:24576\n"
      "ds_read_b128 v[196:199], v119 offset:18432\n"
      "s_add_u32 s27, s27, 1\n"
      "v_subrev_u32 v214, 73728, v119\n"
      "s_sub_u32 s28, s27, 3\n"
      "ds_read_b128 v[172:175], v214 offset:18432\n"
      "ds_read_b128 v[252:255], v121 offset:18432\n"
      "ds_read_b128 v[176:179], v214 offset:24576\n"
      "s_cselect_b64 s[54:55], s[12:13], s[22:23]\n"
      "ds_read_b128 v[192:195], v214 offset:30720\n"
      "ds_read_b128 v[160:163], v120 offset:18432\n"
      "s_max_i32 m0, s28, 0\n"
      "ds_read_b128 v[184:187], v119 offset:30720\n"
      "s_movrels_b32 s9, s80\n"
      "s_lshr_b32 s8, s9, 8\n"
      "s_and_b32 s9, s9, 0xff\n"
      "s_mul_i32 s28, s8, 1373568\n"
      "s_mul_i32 s29, s9, 2688\n"
      "s_add_u32 s28, s28, s29\n"
      "s_cmp_eq_u32 s8, 72\n"
      "s_cselect_b32 s8, 0, 100270464\n"
      "s_cmp_eq_u32 s9, 72\n"
      "s_cselect_b32 s9, 0, 196224\n"
      "s_add_u32 s10, s8, s9\n"
      "s_add_u32 s28, s28, s10\n"
      "s_add_u32 s54, s54, s28\n"
      "s_addc_u32 s55, s55, 0\n"
      "s_cmp_lg_u32 s10, s11\n"
      "s_waitcnt lgkmcnt(6)\n"
      "s_cbranch_scc1 L_edge_46_%=\n"
      "L_eb_47_%=:\n"
      "global_store_dwordx4 v116, v[196:199], s[54:55] sc1\n"
      "global_store_dwordx4 v116, v[164:167], s[54:55] offset:128 sc1\n"
      "s_waitcnt lgkmcnt(2)\n"
      "s_waitcnt lgkmcnt(1)\n"
      "s_waitcnt lgkmcnt(0)\n"
      "global_store_dwordx4 v116, v[184:187], s[54:55] offset:256 sc1\n"
      "global_store_dwordx4 v117, v[160:163], s[54:55] sc1\n"
      "s_mov_b32 exec_hi, 0\n"
      "global_store_dwordx4 v118, v[252:255], s[54:55] sc1\n"
      "s_mov_b32 exec_hi, -1\n"
      "s_mov_b64 exec, s[48:49]\n"
      "s_cbranch_execz L_np_48_%=\n"
      "global_store_dwordx4 v212, v[172:175], s[22:23] sc1\n"
      "global_store_dwordx4 v212, v[176:179], s[22:23] offset:128 sc1\n"
      "global_store_dwordx4 v212, v[192:195], s[22:23] offset:256 sc1\n"
      "L_np_48_%=:\n"
      "s_mov_b64 exec, -1\n"
      "s_nop 0\n"
      "s_endpgm\n"
      "L_gen_8_%=:\n"
      "s_waitcnt lgkmcnt(0)\n"
      "s_nop 7\n"
      "s_nop 7\n"
      "s_cmp_eq_u64 s[48:49], 0\n"
      "s_cbranch_scc1 L_go_49_%=\n"
      "s_mov_b32 m0, s27\n"
      "s_nop 0\n"
      "s_movrels_b32 s9, s80\n"
      "s_lshr_b32 s8, s9, 8\n"
      "s_and_b32 s9, s9, 0xff\n"
      "s_mul_i32 s10, s8, 73\n"
      "s_add_u32 s10, s10, s9\n"
      "s_mul_i32 s30, s10, 4802\n"
      "s_add_u32 s30, s30, s18\n"
      "s_and_b32 s29, s30, 15\n"
      "s_add_u32 s30, s29, 110592\n"
      "s_lshl_b64 s[34:35], s[48:49], 4\n"
      "s_lshr_b64 s[34:35], s[34:35], 5\n"
      "s_cbranch_scc1 L_gen_9_%=\n"
      "v_mbcnt_lo_u32_b32 v156, -1, 0\n"
      "v_mbcnt_hi_u32_b32 v156, -1, v156\n"
      "v_lshlrev_b32 v157, 1, v156\n"
      "v_add_u32 v158, 4802, v157\n"
      "v_cmp_gt_u32 vcc, s29, v157\n"
      "s_nop 1\n"
      "v_cndmask_b32 v157, v158, v157, vcc\n"
      "v_add_u32 v157, 110592, v157\n"
      "v_mov_b32 v158, 0\n"
      "s_mov_b64 exec, 0x7f\n"
      "ds_write_b16 v157, v158\n"
      "s_mov_b64 exec, -1\n"
      "v_lshlrev_b32 v157, 4, v156\n"
      "v_add_u32 v157, 110592, v157\n"
      "v_min_u32 v156, 111296, v157\n"
      "ds_read_b128 v[148:151], v157\n"
      "ds_read_b128 v[168:171], v157 offset:1024\n"
      "ds_read_b128 v[192:195], v157 offset:2048\n"
      "ds_read_b128 v[196:199], v157 offset:3072\n"
      "ds_read_b128 v[212:215], v156 offset:4096\n"
      "s_waitcnt lgkmcnt(4)\n"
      "v_or3_b32 v148, v148, v149, v150\n"
      "s_waitcnt lgkmcnt(3)\n"
      "v_or3_b32 v148, v148, v151, v168\n"
      "v_or3_b32 v148, v148, v169, v170\n"
      "s_waitcnt lgkmcnt(2)\n"
      "v_or3_b32 v148, v148, v171, v192\n"
      "v_or3_b32 v148, v148, v193, v194\n"
      "s_waitcnt lgkmcnt(1)\n"
      "v_or3_b32 v148, v148, v195, v196\n"
      "v_or3_b32 v148, v148, v197, v198\n"
      "s_waitcnt lgkmcnt(0)\n"
      "v_or3_b32 v148, v148, v199, v212\n"
      "v_or3_b32 v148, v148, v213, v214\n"
      "v_bitop3_b32 v148, v148, v215, s40 bitop3:0xa8\n"
      "v_cmp_ne_u32 s[48:49], 0, v148\n"
      "s_cmp_lg_u64 s[48:49], 0\n"
      "s_cbranch_scc1 L_gen_9_%=\n"
      "s_cmp_eq_u64 s[50:51], 0\n"
      "s_cbranch_scc1 L_fast_11_%=\n"
      "L_go_49_%=:\n"
      "v_mfma_f32_16x16x32_bf16 v[156:159], v[180:183], v[140:143], v[90:93]\n"
      "ds_read_b128 v[148:151], v128 offset:3072\n"
      "v_max3_f32 v202, v164, v165, v166\n"
      "v_mfma_f32_16x16x32_bf16 v[156:159], v[34:37], v[38:41], v[156:159]\n"
      "v_max3_f32 v203, v167, v172, v173\n"
      "v_max3_f32 v202, v202, v174, v175\n"
      "v_mfma_f32_16x16x32_bf16 v[212:215], v[188:191], v[140:143], v[94:97]\n"
      "s_nop 0\n"
      "v_max3_f32 v203, v203, v184, v185\n"
      "v_max3_f32 v202, v202, v186, v187\n"
      "v_mfma_f32_16x16x32_bf16 v[212:215], v[40:43], v[38:41], v[212:215]\n"
      "v_max3_f32 v202, v202, v203, v200\n"
      "v_mov_b32 v203, v202\n"
      "v_mfma_f32_16x16x32_bf16 v[196:199], v[144:147], v[140:143], v[72:75]\n"
      "s_nop 0\n"
      "v_permlane16_swap_b32 v202, v203\n"
      "v_mfma_f32_16x16x32_bf16 v[196:199], v[160:163], v[188:191], v[196:199]\n"
      "v_max_f32 v202, v202, v203\n"
      "v_mov_b32 v203, v202\n"
      "v_mfma_f32_16x16x32_bf16 v[196:199], v[176:179], v[38:41], v[196:199]\n"
      "s_nop 0\n"
      "v_permlane32_swap_b32 v202, v203\n"
      "v_mfma_f32_16x16x32_bf16 v[192:195], v[152:155], v[140:143], v[98:101]\n"
      "v_max_f32 v202, v202, v203\n"
      "v_sub_f32 v164, v164, v202\n"
      "v_mfma_f32_16x16x32_bf16 v[192:195], v[46:49], v[38:41], v[192:195]\n"
      "v_sub_f32 v165, v165, v202\n"
      "v_sub_f32 v166, v166, v202\n"
      "v_mfma_f32_16x16x32_bf16 v[140:143], v[180:183], v[136:139], v[102:105]\n"
      "v_sub_f32 v167, v167, v202\n"
      "v_sub_f32 v172, v172, v202\n"
      "v_mfma_f32_16x16x32_bf16 v[140:143], v[34:37], v[44:47], v[140:143]\n"
      "v_sub_f32 v173, v173, v202\n"
      "v_sub_f32 v174, v174, v202\n"
      "v_mfma_f32_16x16x32_bf16 v[180:183], v[188:191], v[136:139], v[106:109]\n"
      "s_waitcnt lgkmcnt(0)\n"
      "v_sub_f32 v175, v175, v202\n"
      "v_sub_f32 v184, v184, v202\n"
      "v_mfma_f32_16x16x32_bf16 v[180:183], v[40:43], v[44:47], v[180:183]\n"
      "v_sub_f32 v185, v185, v202\n"
      "v_sub_f32 v186, v186, v202\n"
      "v_mfma_f32_16x16x32_bf16 v[188:191], v[144:147], v[136:139], v[74:77]\n"
      "v_sub_f32 v187, v187, v202\n"
      "v_sub_f32 v200, v200, v202\n"
      "v_mfma_f32_16x16x32_bf16 v[188:191], v[160:163], v[152:155], v[188:191]\n"
      "v_exp_f32 v164, v164\n"
      "v_mfma_f32_16x16x32_bf16 v[188:191], v[176:179], v[44:47], v[188:191]\n"
      "v_exp_f32 v165, v165\n"
      "v_mfma_f32_16x16x32_bf16 v[168:171], v[152:155], v[136:139], v[110:113]\n"
      "v_exp_f32 v166, v166\n"
      "v_mfma_f32_16x16x32_bf16 v[168:171], v[46:49], v[44:47], v[168:171]\n"
      "v_cvt_pk_bf16_f32 v164, v164, v165\n"
      "v_exp_f32 v167, v167\n"
      "v_mfma_f32_16x16x32_bf16 v[136:139], v[160:163], v[144:147], v[76:79]\n"
      "v_exp_f32 v172, v172\n"
      "v_cvt_pk_bf16_f32 v165, v166, v167\n"
      "v_exp_f32 v173, v173\n"
      "v_exp_f32_e64 v174, v174\n"
      "v_cvt_pk_bf16_f32 v166, v172, v173\n"
      "v_exp_f32 v175, v175\n"
      "v_mfma_f32_16x16x16_bf16 v[136:139], v[178:179], v[176:177], v[136:139]\n"
      "v_exp_f32 v184, v184\n"
      "v_cvt_pk_bf16_f32 v167, v174, v175\n"
      "v_exp_f32 v185, v185\n"
      "v_exp_f32 v186, v186\n"
      "v_exp_f32 v187, v187\n"
      "v_exp_f32 v200, v200\n"
      "v_cvt_pk_bf16_f32 v62, v184, v185\n"
      "v_cvt_pk_bf16_f32 v63, v186, v187\n"
      "v_cvt_pk_bf16_f32 v64, v200, 0\n"
      "v_bitop3_b32 v148, v148, v149, s4 bitop3:0x7e\n"
      "v_bitop3_b32 v150, v150, v151, s4 bitop3:0x7e\n"
      "v_mfma_f32_16x16x32_bf16 v[152:155], v[66:69], v[164:167], 0\n"
      "v_or_b32 v148, v148, v150\n"
      "v_cmp_ne_u32 vcc, 0, v148\n"
      "v_mfma_f32_16x16x32_bf16 v[152:155], v[66:69], v[62:65], v[152:155]\n"
      "s_mov_b64 s[48:49], vcc\n"
      "v_max3_f32 v136, v156, v157, v158\n"
      "v_max3_f32 v138, v159, v212, v213\n"
      "v_mfma_f32_16x16x32_bf16 v[160:163], v[208:211], v[164:167], 0\n"
      "s_cbranch_vccnz L_flg_50_%=\n"
      "L_fb_51_%=:\n"
      "v_mfma_f32_16x16x32_bf16 v[160:163], v[50:53], v[62:65], v[160:163]\n"
      "v_max3_f32 v136, v136, v214, v215\n"
      "v_max3_f32 v138, v138, v192, v193\n"
      "v_mfma_f32_16x16x32_bf16 v[148:151], v[204:207], v[164:167], 0\n"
      "v_max3_f32 v136, v136, v194, v195\n"
      "v_max3_f32 v136, v136, v138, v196\n"
      "v_mfma_f32_16x16x32_bf16 v[148:151], v[54:57], v[62:65], v[148:151]\n"
      "v_mov_b32 v138, v136\n"
      "v_mfma_f32_16x16x32_bf16 v[144:147], v[236:239], v[164:167], 0\n"
      "s_nop 0\n"
      "v_permlane16_swap_b32_e64 v136, v138\n"
      "v_mfma_f32_16x16x32_bf16 v[144:147], v[58:61], v[62:65], v[144:147]\n"
      "v_max_f32 v136, v136, v138\n"
      "v_mov_b32 v138, v136\n"
      "v_rcp_f32 v152, v152\n"
      "s_nop 0\n"
      "v_permlane32_swap_b32 v136, v138\n"
      "v_max_f32 v136, v136, v138\n"
      "v_sub_f32 v156, v156, v136\n"
      "v_sub_f32 v157, v157, v136\n"
      "v_sub_f32 v158, v158, v136\n"
      "v_sub_f32 v159, v159, v136\n"
      "v_sub_f32 v212, v212, v136\n"
      "v_sub_f32 v213, v213, v136\n"
      "v_sub_f32 v214, v214, v136\n"
      "v_sub_f32 v215, v215, v136\n"
      "v_sub_f32 v192, v192, v136\n"
      "v_sub_f32 v193, v193, v136\n"
      "v_sub_f32 v194, v194, v136\n"
      "v_sub_f32 v195, v195, v136\n"
      "v_sub_f32 v196, v196, v136\n"
      "v_exp_f32 v156, v156\n"
      "v_exp_f32 v157, v157\n"
      "v_exp_f32 v158, v158\n"
      "v_cvt_pk_bf16_f32 v156, v156, v157\n"
      "v_exp_f32 v159, v159\n"
      "v_exp_f32 v212, v212\n"
      "v_cvt_pk_bf16_f32 v157, v158, v159\n"
      "v_exp_f32 v213, v213\n"
      "v_exp_f32 v214, v214\n"
      "v_cvt_pk_bf16_f32 v158, v212, v213\n"
      "v_exp_f32 v215, v215\n"
      "v_exp_f32 v192, v192\n"
      "v_cvt_pk_bf16_f32 v159, v214, v215\n"
      "v_exp_f32 v193, v193\n"
      "v_exp_f32 v194, v194\n"
      "v_exp_f32 v195, v195\n"
      "v_exp_f32 v196, v196\n"
      "v_cvt_pk_bf16_f32 v62, v192, v193\n"
      "v_cvt_pk_bf16_f32 v63, v194, v195\n"
      "v_cvt_pk_bf16_f32 v64, v196, 0\n"
      "v_mul_f32 v160, v152, v160\n"
      "v_mul_f32 v161, v152, v161\n"
      "v_mfma_f32_16x16x32_bf16 v[192:195], v[66:69], v[156:159], 0\n"
      "s_add_u32 s28, s27, 2\n"
      "v_cvt_pk_bf16_f32 v160, v160, v161\n"
      "v_mul_f32 v162, v152, v162\n"
      "v_mfma_f32_16x16x32_bf16 v[192:195], v[66:69], v[62:65], v[192:195]\n"
      "s_min_u32 m0, s28, s3\n"
      "v_mul_f32 v163, v152, v163\n"
      "v_cvt_pk_bf16_f32 v161, v162, v163\n"
      "v_mfma_f32_16x16x32_bf16 v[212:215], v[208:211], v[156:159], 0\n"
      "s_movrels_b32 s9, s80\n"
      "v_mul_f32 v148, v152, v148\n"
      "v_mul_f32 v149, v152, v149\n"
      "v_mfma_f32_16x16x32_bf16 v[212:215], v[50:53], v[62:65], v[212:215]\n"
      "s_lshr_b32 s8, s9, 8\n"
      "v_cvt_pk_bf16_f32 v162, v148, v149\n"
      "v_mul_f32 v150, v152, v150\n"
      "v_mfma_f32_16x16x32_bf16 v[184:187], v[204:207], v[156:159], 0\n"
      "s_and_b32 s9, s9, 0xff\n"
      "v_mul_f32 v151, v152, v151\n"
      "v_cvt_pk_bf16_f32 v163, v150, v151\n"
      "v_mfma_f32_16x16x32_bf16 v[184:187], v[54:57], v[62:65], v[184:187]\n"
      "ds_write_b128 v124, v[160:163]\n"
      "v_mfma_f32_16x16x32_bf16 v[148:151], v[236:239], v[156:159], 0\n"
      "s_mul_i32 s10, s8, 73\n"
      "v_mul_f32 v144, v152, v144\n"
      "v_mul_f32 v145, v152, v145\n"
      "v_mfma_f32_16x16x32_bf16 v[148:151], v[58:61], v[62:65], v[148:151]\n"
      "s_add_u32 s10, s10, s9\n"
      "v_cvt_pk_bf16_f32 v144, v144, v145\n"
      "v_mul_f32 v146, v152, v146\n"
      "v_mul_f32_e64 v147, v152, v147\n"
      "v_cvt_pk_bf16_f32 v145, v146, v147\n"
      "v_max3_f32 v146, v140, v141, v142\n"
      "v_max3_f32 v147, v143, v180, v181\n"
      "v_max3_f32 v146, v146, v182, v183\n"
      "v_max3_f32 v147, v147, v168, v169\n"
      "v_max3_f32 v146, v146, v170, v171\n"
      "v_max3_f32 v146, v146, v147, v188\n"
      "v_mov_b32 v147, v146\n"
      "s_mul_i32 s28, s10, 4802\n"
      "s_nop 0\n"
      "v_permlane16_swap_b32 v146, v147\n"
      "v_max_f32 v146, v146, v147\n"
      "v_mov_b32 v147, v146\n"
      "s_add_u32 s56, s18, s28\n"
      "s_nop 0\n"
      "v_permlane32_swap_b32 v146, v147\n"
      "v_max_f32 v146, v146, v147\n"
      "v_sub_f32 v140, v140, v146\n"
      "v_sub_f32 v141, v141, v146\n"
      "v_sub_f32 v142, v142, v146\n"
      "v_sub_f32 v143, v143, v146\n"
      "v_sub_f32 v180, v180, v146\n"
      "v_sub_f32 v181, v181, v146\n"
      "v_sub_f32 v182, v182, v146\n"
      "v_sub_f32 v183, v183, v146\n"
      "v_sub_f32 v168, v168, v146\n"
      "v_sub_f32 v169, v169, v146\n"
      "v_sub_f32 v170, v170, v146\n"
      "v_sub_f32 v171, v171, v146\n"
      "v_sub_f32 v188, v188, v146\n"
      "v_exp_f32 v140, v140\n"
      "v_exp_f32 v141, v141\n"
      "v_exp_f32 v142, v142\n"
      "v_cvt_pk_bf16_f32 v140, v140, v141\n"
      "v_exp_f32 v143, v143\n"
      "v_exp_f32_e64 v180, v180\n"
      "v_cvt_pk_bf16_f32 v141, v142, v143\n"
      "s_addc_u32 s57, s19, 0\n"
      "ds_write_b64 v125, v[144:145]\n"
      "v_exp_f32 v181, v181\n"
      "v_exp_f32_e64 v182, v182\n"
      "v_cvt_pk_bf16_f32 v142, v180, v181\n"
      "v_exp_f32 v183, v183\n"
      "v_exp_f32 v168, v168\n"
      "v_cvt_pk_bf16_f32 v143, v182, v183\n"
      "v_exp_f32 v169, v169\n"
      "v_exp_f32 v170, v170\n"
      "v_exp_f32_e64 v171, v171\n"
      "v_exp_f32_e64 v188, v188\n"
      "v_cvt_pk_bf16_f32 v62, v168, v169\n"
      "v_cvt_pk_bf16_f32 v63, v170, v171\n"
      "v_cvt_pk_bf16_f32 v64, v188, 0\n"
      "v_cndmask_b32 v201, v201, v197, s[42:43]\n"
      "v_cndmask_b32 v201, v201, v189, s[44:45]\n"
      "v_mfma_f32_16x16x32_bf16 v[152:155], v[66:69], v[140:143], 0\n"
      "s_and_b32 s56, s56, -16\n"
      "v_cndmask_b32 v201, v201, v137, s[46:47]\n"
      "s_sub_u32 s58, s56, 110592\n"
      "v_mfma_f32_16x16x32_bf16 v[152:155], v[66:69], v[62:65], v[152:155]\n"
      "s_subb_u32 s59, s57, 0\n"
      "v_max_f32_dpp v137, v201, v201 row_ror:8 row_mask:0xf bank_mask:0xf\n"
      "v_rcp_f32_e64 v192, v192\n"
      "v_mfma_f32_16x16x32_bf16 v[156:159], v[208:211], v[140:143], 0\n"
      "s_sub_u32 s56, s56, 73728\n"
      "v_max_f32_dpp v137, v137, v137 row_ror:4 row_mask:0xf bank_mask:0xf\n"
      "v_mul_f32 v212, v192, v212\n"
      "v_mfma_f32_16x16x32_bf16 v[156:159], v[50:53], v[62:65], v[156:159]\n"
      "s_subb_u32 s57, s57, 0\n"
      "v_max_f32_dpp v137, v137, v137 row_ror:2 row_mask:0xf bank_mask:0xf\n"
      "v_mul_f32_e64 v213, v192, v213\n"
      "v_mfma_f32_16x16x32_bf16 v[160:163], v[204:207], v[140:143], 0\n"
      "s_mul_i32 s28, s8, 1373568\n"
      "v_max_f32_dpp v137, v137, v137 row_ror:1 row_mask:0xf bank_mask:0xf\n"
      "v_cvt_pk_bf16_f32 v212, v212, v213\n"
      "v_mfma_f32_16x16x32_bf16 v[160:163], v[54:57], v[62:65], v[160:163]\n"
      "s_mul_i32 s29, s9, 2688\n"
      "v_max_f32_dpp v137, v137, v137 row_bcast:15 row_mask:0xa bank_mask:0xf\n"
      "v_mul_f32 v214, v192, v214\n"
      "v_mfma_f32_16x16x32_bf16 v[164:167], v[236:239], v[140:143], 0\n"
      "s_add_u32 s28, s28, s29\n"
      "v_max_f32_dpp v137, v137, v137 row_bcast:31 row_mask:0xc bank_mask:0xf\n"
      "v_mul_f32_e64 v215, v192, v215\n"
      "v_mfma_f32_16x16x32_bf16 v[164:167], v[58:61], v[62:65], v[164:167]\n"
      "s_cmp_eq_u32 s8, 72\n"
      "v_readlane_b32 s31, v137, 63\n"
      "v_cvt_pk_bf16_f32 v213, v214, v215\n"
      "v_mul_f32 v184, v192, v184\n"
      "v_subrev_f32 v201, s31, v201\n"
      "v_exp_f32 v201, v201\n"
      "v_mul_f32 v185, v192, v185\n"
      "v_cvt_pk_bf16_f32 v201, v201, v201\n"
      "v_cvt_pk_bf16_f32 v214, v184, v185\n"
      "v_mul_f32 v186, v192, v186\n"
      "v_mul_f32 v187, v192, v187\n"
      "v_cvt_pk_bf16_f32 v215, v186, v187\n"
      "v_mul_f32 v148, v192, v148\n"
      "v_mul_f32 v149, v192, v149\n"
      "v_cvt_pk_bf16_f32 v148, v148, v149\n"
      "v_mul_f32 v150, v192, v150\n"
      "v_mul_f32 v151, v192, v151\n"
      "v_cvt_pk_bf16_f32 v149, v150, v151\n"
      "s_cselect_b32 s8, 0, 100270464\n"
      "ds_write_b16 v130, v201\n"
      "v_mov_b32_e64 v150, 0\n"
      "s_cmp_eq_u32 s9, 72\n"
      "ds_read_b128 v[144:147], v131\n"
      "ds_read_b128 v[184:187], v131 offset:64\n"
      "ds_write_b128 v124, v[212:215] offset:256\n"
      "ds_write_b64 v125, v[148:149] offset:256\n"
      "ds_write_b32 v129, v150\n"
      "ds_read_b128 v[136:139], v114 offset:18432\n"
      "ds_read_b128 v[140:143], v115 offset:18432\n"
      "s_cselect_b32 s9, 0, 196224\n"
      "s_add_u32 s10, s8, s9\n"
      "s_add_u32 s28, s28, s10\n"
      "s_add_u32 s52, s16, s28\n"
      "s_addc_u32 s53, s17, 0\n"
      "s_cmp_lg_u32 s10, s11\n"
      "s_cbranch_scc1 L_edge_53_%=\n"
      "L_eb_54_%=:\n"
      "s_waitcnt lgkmcnt(5)\n"
      "v_mfma_f32_16x16x32_bf16 v[212:215], v[144:147], v[66:69], 0\n"
      "v_mfma_f32_16x16x32_bf16 v[212:215], v[184:187], v[66:69], v[212:215]\n"
      "v_mfma_f32_16x16x32_bf16 v[168:171], v[144:147], v[208:211], 0\n"
      "v_mfma_f32_16x16x32_bf16 v[168:171], v[184:187], v[50:53], v[168:171]\n"
      "v_mfma_f32_16x16x32_bf16 v[208:211], v[144:147], v[204:207], 0\n"
      "s_waitcnt lgkmcnt(0)\n"
      "v_mfma_f32_16x16x32_bf16 v[208:211], v[184:187], v[54:57], v[208:211]\n"
      "v_mfma_f32_16x16x32_bf16 v[204:207], v[144:147], v[236:239], 0\n"
      "s_nop 1\n"
      "v_mov_b32_dpp v212, v212 quad_perm:[0,0,0,0] row_mask:0xf bank_mask:0xf\n"
      "v_mfma_f32_16x16x32_bf16 v[204:207], v[184:187], v[58:61], v[204:207]\n"
      "v_rcp_f32 v212, v212\n"
      "v_mfma_f32_16x16x32_bf16 v[144:147], a[0:3], v[136:139], 0\n"
      "s_nop 0\n"
      "v_cndmask_b32 v168, v168, v208, s[42:43]\n"
      "v_mfma_f32_16x16x32_bf16 v[144:147], a[36:39], v[140:143], v[144:147]\n"
      "s_nop 0\n"
      "v_mfma_f32_16x16x32_bf16 v[148:151], a[4:7], v[136:139], 0\n"
      "s_nop 1\n"
      "v_cndmask_b32 v168, v168, v204, s[44:45]\n"
      "v_mfma_f32_16x16x32_bf16 v[148:151], a[40:43], v[140:143], v[148:151]\n"
      "v_mul_f32 v168, v212, v168\n"
      "v_cvt_pk_bf16_f32 v168, v168, v168\n"
      "ds_write_b16 v135, v168\n"
      "s_branch L_join_10_%=\n"
      "L_flg_50_%=:\n"
      "v_cmp_gt_i32 vcc, 0, v148\n"
      "s_cbranch_vccz L_fb_51_%=\n"
      "s_sleep 1\n"
      "ds_read_b128 v[148:151], v128 offset:3072\n"
      "s_waitcnt lgkmcnt(0)\n"
      "v_bitop3_b32 v148, v148, v149, s4 bitop3:0x7e\n"
      "v_bitop3_b32 v150, v150, v151, s4 bitop3:0x7e\n"
      "v_or_b32 v148, v148, v150\n"
      "v_cmp_ne_u32 s[48:49], 0, v148\n"
      "s_branch L_flg_50_%=\n"
      "L_edge_53_%=:\n"
      "s_mov_b32 s11, s10\n"
      "s_sub_u32 s28, s60, s8\n"
      "s_sub_u32 s29, s61, s9\n"
      "s_mov_b32 s60, s8\n"
      "s_mov_b32 s61, s9\n"
      "s_mov_b64 exec, s[64:65]\n"
      "v_add_u32 v116, s28, v116\n"
      "s_mov_b64 exec, s[70:71]\n"
      "v_add_u32 v116, s29, v116\n"
      "s_mov_b64 exec, s[66:67]\n"
      "v_add_u32 v117, s28, v117\n"
      "s_mov_b64 exec, s[72:73]\n"
      "v_add_u32 v117, s29, v117\n"
      "s_mov_b64 exec, s[68:69]\n"
      "v_add_u32 v118, s28, v118\n"
      "s_mov_b64 exec, s[74:75]\n"
      "v_add_u32_e64 v118, s29, v118\n"
      "s_mov_b64 exec, -1\n"
      "s_nop 4\n"
      "s_branch L_eb_54_%=\n"
      "L_gen_9_%=:\n"
      "s_waitcnt lgkmcnt(0)\n"
      "s_nop 7\n"
      "s_nop 7\n"
      "v_mfma_f32_16x16x32_bf16 v[156:159], v[180:183], v[140:143], v[90:93]\n"
      "s_movk_i32 s31, 0x3fb9\n"
      "v_mbcnt_lo_u32_b32 v148, -1, 0\n"
      "v_mbcnt_hi_u32_b32 v148, -1, v148\n"
      "v_mfma_f32_16x16x32_bf16 v[156:159], v[34:37], v[38:41], v[156:159]\n"
      "v_and_b32 v149, 15, v148\n"
      "v_lshrrev_b32 v150, 4, v148\n"
      "v_mfma_f32_16x16x32_bf16 v[168:171], v[188:191], v[140:143], v[94:97]\n"
      "v_mul_u32_u24 v151, 0x62, v149\n"
      "v_lshl_add_u32 v151, v150, 3, v151\n"
      "v_mfma_f32_16x16x32_bf16 v[168:171], v[40:43], v[38:41], v[168:171]\n"
      "v_add_u32 v151, s30, v151\n"
      "v_and_b32 v148, -4, v151\n"
      "v_and_b32 v151, 2, v151\n"
      "v_lshlrev_b32 v150, 2, v150\n"
      "v_sub_u32 v149, v149, v150\n"
      "v_and_b32 v150, 1, v149\n"
      "v_lshlrev_b32_e64 v150, 4, v150\n"
      "v_lshlrev_b32 v150, v150, s31\n"
      "v_lshrrev_b32_e64 v149, 1, v149\n"
      "v_cmp_eq_u32 vcc, 0, v149\n"
      "s_nop 1\n"
      "v_cndmask_b32 v202, 0, v150, vcc\n"
      "v_cmp_eq_u32 vcc, 1, v149\n"
      "s_nop 1\n"
      "v_cndmask_b32 v203, 0, v150, vcc\n"
      "v_mfma_f32_16x16x32_bf16 v[192:195], v[144:147], v[140:143], v[72:75]\n"
      "ds_read2_b32 v[196:197], v148 offset0:0 offset1:1\n"
      "v_mfma_f32_16x16x32_bf16 v[192:195], v[160:163], v[188:191], v[192:195]\n"
      "ds_read2_b32 v[198:199], v148 offset0:8 offset1:9\n"
      "v_mfma_f32_16x16x32_bf16 v[192:195], v[176:179], v[38:41], v[192:195]\n"
      "ds_read2_b32 v[212:213], v148 offset0:16 offset1:17\n"
      "ds_read2_b32 v[214:215], v148 offset0:2 offset1:10\n"
      "ds_read2_b32 v[216:217], v148 offset0:18 offset1:24\n"
      "v_add_u32 v148, 1568, v148\n"
      "ds_read2_b32 v[218:219], v148 offset0:0 offset1:1\n"
      "ds_read2_b32 v[220:221], v148 offset0:8 offset1:9\n"
      "ds_read2_b32 v[222:223], v148 offset0:16 offset1:17\n"
      "ds_read2_b32 v[224:225], v148 offset0:2 offset1:10\n"
      "ds_read2_b32 v[226:227], v148 offset0:18 offset1:24\n"
      "v_add_u32 v148, 1568, v148\n"
      "ds_read2_b32 v[194:195], v148 offset0:0 offset1:1\n"
      "ds_read2_b32 v[228:229], v148 offset0:8 offset1:9\n"
      "ds_read2_b32 v[230:231], v148 offset0:16 offset1:17\n"
      "ds_read2_b32 v[232:233], v148 offset0:2 offset1:10\n"
      "ds_read2_b32 v[234:235], v148 offset0:18 offset1:24\n"
      "s_waitcnt lgkmcnt(13)\n"
      "v_alignbyte_b32 v196, v197, v196, v151\n"
      "v_alignbyte_b32 v198, v199, v198, v151\n"
      "s_waitcnt lgkmcnt(12)\n"
      "v_alignbyte_b32 v212, v213, v212, v151\n"
      "s_waitcnt lgkmcnt(11)\n"
      "v_alignbyte_b32 v197, v214, v197, v151\n"
      "v_alignbyte_b32 v199, v215, v199, v151\n"
      "v_mfma_f32_16x16x32_bf16 v[240:243], v[152:155], v[140:143], v[98:101]\n"
      "s_waitcnt lgkmcnt(10)\n"
      "v_alignbyte_b32 v213, v216, v213, v151\n"
      "v_alignbyte_b32 v217, v217, v217, v151\n"
      "v_mfma_f32_16x16x32_bf16 v[240:243], v[46:49], v[38:41], v[240:243]\n"
      "s_waitcnt lgkmcnt(9)\n"
      "v_lshlrev_b32 v217, 16, v217\n"
      "v_fmac_f32_e64 v200, s37, v217\n"
      "v_mfma_f32_16x16x16_bf16 v[164:167], v[202:203], v[196:197], v[164:167]\n"
      "ds_read_b128 v[140:143], v128 offset:3072\n"
      "v_alignbyte_b32 v218, v219, v218, v151\n"
      "v_mfma_f32_16x16x16_bf16 v[172:175], v[202:203], v[198:199], v[172:175]\n"
      "s_waitcnt lgkmcnt(6)\n"
      "v_alignbyte_b32 v219, v224, v219, v151\n"
      "v_mfma_f32_16x16x16_bf16 v[184:187], v[202:203], v[212:213], v[184:187]\n"
      "s_nop 0\n"
      "v_alignbyte_b32 v220, v221, v220, v151\n"
      "v_alignbyte_b32 v221, v225, v221, v151\n"
      "v_mfma_f32_16x16x32_bf16 v[196:199], v[180:183], v[136:139], v[102:105]\n"
      "v_alignbyte_b32 v222, v223, v222, v151\n"
      "v_max3_f32 v150, v164, v165, v166\n"
      "v_mfma_f32_16x16x32_bf16 v[196:199], v[34:37], v[44:47], v[196:199]\n"
      "v_alignbyte_b32 v223, v226, v223, v151\n"
      "v_alignbyte_b32 v227, v227, v227, v151\n"
      "v_mfma_f32_16x16x32_bf16 v[180:183], v[188:191], v[136:139], v[106:109]\n"
      "s_waitcnt lgkmcnt(1)\n"
      "v_lshlrev_b32 v227, 16, v227\n"
      "v_alignbyte_b32 v194, v195, v194, v151\n"
      "v_mfma_f32_16x16x32_bf16 v[180:183], v[40:43], v[44:47], v[180:183]\n"
      "v_alignbyte_b32 v195, v232, v195, v151\n"
      "v_max3_f32 v149, v167, v172, v173\n"
      "v_mfma_f32_16x16x32_bf16 v[188:191], v[144:147], v[136:139], v[74:77]\n"
      "v_alignbyte_b32 v228, v229, v228, v151\n"
      "v_alignbyte_b32 v229, v233, v229, v151\n"
      "v_mfma_f32_16x16x32_bf16 v[188:191], v[160:163], v[152:155], v[188:191]\n"
      "v_alignbyte_b32 v230, v231, v230, v151\n"
      "v_max3_f32 v150, v150, v174, v175\n"
      "v_mfma_f32_16x16x32_bf16 v[188:191], v[176:179], v[44:47], v[188:191]\n"
      "v_alignbyte_b32 v231, v234, v231, v151\n"
      "v_alignbyte_b32 v235, v235, v235, v151\n"
      "v_mfma_f32_16x16x32_bf16 v[212:215], v[152:155], v[136:139], v[110:113]\n"
      "v_lshlrev_b32_e64 v235, 16, v235\n"
      "v_max3_f32 v149, v149, v184, v185\n"
      "v_mfma_f32_16x16x32_bf16 v[212:215], v[46:49], v[44:47], v[212:215]\n"
      "v_max3_f32 v150, v150, v186, v187\n"
      "v_max3_f32 v150, v150, v149, v200\n"
      "v_mov_b32 v149, v150\n"
      "s_nop 1\n"
      "v_permlane16_swap_b32 v150, v149\n"
      "v_max_f32 v150, v150, v149\n"
      "v_mfma_f32_16x16x16_bf16 v[156:159], v[202:203], v[218:219], v[156:159]\n"
      "v_mov_b32 v149, v150\n"
      "v_mfma_f32_16x16x32_bf16 v[136:139], v[160:163], v[144:147], v[76:79]\n"
      "s_nop 0\n"
      "v_permlane32_swap_b32 v150, v149\n"
      "v_mfma_f32_16x16x16_bf16 v[168:171], v[202:203], v[220:221], v[168:171]\n"
      "v_max_f32 v150, v150, v149\n"
      "v_sub_f32 v164, v164, v150\n"
      "v_mfma_f32_16x16x16_bf16 v[240:243], v[202:203], v[222:223], v[240:243]\n"
      "v_sub_f32 v165, v165, v150\n"
      "v_sub_f32 v166, v166, v150\n"
      "v_mfma_f32_16x16x16_bf16 v[136:139], v[178:179], v[176:177], v[136:139]\n"
      "s_waitcnt lgkmcnt(0)\n"
      "v_sub_f32 v167, v167, v150\n"
      "v_sub_f32 v172, v172, v150\n"
      "v_sub_f32 v173, v173, v150\n"
      "v_sub_f32 v174, v174, v150\n"
      "v_sub_f32 v175, v175, v150\n"
      "v_sub_f32 v184, v184, v150\n"
      "v_sub_f32 v185, v185, v150\n"
      "v_sub_f32 v186, v186, v150\n"
      "v_sub_f32 v187, v187, v150\n"
      "v_sub_f32 v200, v200, v150\n"
      "v_exp_f32 v164, v164\n"
      "v_exp_f32 v165, v165\n"
      "v_exp_f32 v166, v166\n"
      "v_cvt_pk_bf16_f32 v164, v164, v165\n"
      "v_exp_f32 v167, v167\n"
      "v_exp_f32 v172, v172\n"
      "v_cvt_pk_bf16_f32 v165, v166, v167\n"
      "v_exp_f32 v173, v173\n"
      "v_exp_f32 v174, v174\n"
      "v_cvt_pk_bf16_f32 v166, v172, v173\n"
      "v_exp_f32 v175, v175\n"
      "v_exp_f32 v184, v184\n"
      "v_cvt_pk_bf16_f32 v167, v174, v175\n"
      "v_mfma_f32_16x16x16_bf16 v[196:199], v[202:203], v[194:195], v[196:199]\n"
      "v_exp_f32 v185, v185\n"
      "v_mfma_f32_16x16x16_bf16 v[180:183], v[202:203], v[228:229], v[180:183]\n"
      "v_exp_f32 v186, v186\n"
      "v_mfma_f32_16x16x16_bf16 v[212:215], v[202:203], v[230:231], v[212:215]\n"
      "v_exp_f32 v187, v187\n"
      "v_exp_f32_e64 v200, v200\n"
      "v_fmac_f32_e64 v192, s37, v227\n"
      "v_cvt_pk_bf16_f32 v62, v184, v185\n"
      "v_cvt_pk_bf16_f32 v63, v186, v187\n"
      "v_cvt_pk_bf16_f32 v64, v200, 0\n"
      "v_bitop3_b32 v140, v140, v141, s4 bitop3:0x7e\n"
      "v_bitop3_b32 v142, v142, v143, s4 bitop3:0x7e\n"
      "v_mfma_f32_16x16x32_bf16 v[152:155], v[66:69], v[164:167], 0\n"
      "v_or_b32 v140, v140, v142\n"
      "v_cmp_ne_u32 vcc, 0, v140\n"
      "v_mfma_f32_16x16x32_bf16 v[152:155], v[66:69], v[62:65], v[152:155]\n"
      "s_mov_b64 s[48:49], vcc\n"
      "v_max3_f32 v136, v156, v157, v158\n"
      "v_max3_f32 v138, v159, v168, v169\n"
      "v_mfma_f32_16x16x32_bf16 v[160:163], v[208:211], v[164:167], 0\n"
      "s_cbranch_vccnz L_flg_61_%=\n"
      "L_fb_62_%=:\n"
      "v_mfma_f32_16x16x32_bf16 v[160:163], v[50:53], v[62:65], v[160:163]\n"
      "v_max3_f32 v136, v136, v170, v171\n"
      "v_max3_f32 v138, v138, v240, v241\n"
      "v_mfma_f32_16x16x32_bf16 v[140:143], v[204:207], v[164:167], 0\n"
      "v_max3_f32 v136, v136, v242, v243\n"
      "v_max3_f32 v136, v136, v138, v192\n"
      "v_mfma_f32_16x16x32_bf16 v[140:143], v[54:57], v[62:65], v[140:143]\n"
      "v_mov_b32 v138, v136\n"
      "v_fmac_f32 v188, s37, v235\n"
      "v_mfma_f32_16x16x32_bf16 v[144:147], v[236:239], v[164:167], 0\n"
      "v_permlane16_swap_b32 v136, v138\n"
      "v_mfma_f32_16x16x32_bf16 v[144:147], v[58:61], v[62:65], v[144:147]\n"
      "v_max_f32 v136, v136, v138\n"
      "v_mov_b32 v138, v136\n"
      "s_nop 1\n"
      "v_permlane32_swap_b32 v136, v138\n"
      "v_max_f32 v136, v136, v138\n"
      "v_sub_f32 v156, v156, v136\n"
      "v_sub_f32 v157, v157, v136\n"
      "v_sub_f32 v158, v158, v136\n"
      "v_sub_f32 v159, v159, v136\n"
      "v_sub_f32 v168, v168, v136\n"
      "v_sub_f32 v169, v169, v136\n"
      "v_sub_f32 v170, v170, v136\n"
      "v_sub_f32 v171, v171, v136\n"
      "v_sub_f32 v240, v240, v136\n"
      "v_sub_f32 v241, v241, v136\n"
      "v_sub_f32 v242, v242, v136\n"
      "v_sub_f32 v243, v243, v136\n"
      "v_sub_f32 v192, v192, v136\n"
      "v_exp_f32 v156, v156\n"
      "v_exp_f32 v157, v157\n"
      "v_exp_f32 v158, v158\n"
      "v_cvt_pk_bf16_f32 v156, v156, v157\n"
      "v_exp_f32 v159, v159\n"
      "v_exp_f32 v168, v168\n"
      "v_cvt_pk_bf16_f32 v157, v158, v159\n"
      "v_exp_f32 v169, v169\n"
      "v_exp_f32 v170, v170\n"
      "v_cvt_pk_bf16_f32 v158, v168, v169\n"
      "v_exp_f32 v171, v171\n"
      "v_exp_f32 v240, v240\n"
      "v_cvt_pk_bf16_f32 v159, v170, v171\n"
      "v_exp_f32 v241, v241\n"
      "v_exp_f32 v242, v242\n"
      "v_exp_f32 v243, v243\n"
      "v_exp_f32 v192, v192\n"
      "v_cvt_pk_bf16_f32 v62, v240, v241\n"
      "v_cvt_pk_bf16_f32 v63, v242, v243\n"
      "v_cvt_pk_bf16_f32 v64, v192, 0\n"
      "v_rcp_f32 v152, v152\n"
      "v_max3_f32 v153, v196, v197, v198\n"
      "v_mfma_f32_16x16x32_bf16 v[240:243], v[66:69], v[156:159], 0\n"
      "v_mul_f32 v160, v152, v160\n"
      "v_mul_f32 v161, v152, v161\n"
      "v_mfma_f32_16x16x32_bf16 v[240:243], v[66:69], v[62:65], v[240:243]\n"
      "s_add_u32 s28, s27, 2\n"
      "v_cvt_pk_bf16_f32 v160, v160, v161\n"
      "v_mul_f32_e64 v162, v152, v162\n"
      "v_mfma_f32_16x16x32_bf16 v[168:171], v[208:211], v[156:159], 0\n"
      "s_min_u32 m0, s28, s3\n"
      "v_mul_f32 v163, v152, v163\n"
      "v_cvt_pk_bf16_f32 v161, v162, v163\n"
      "v_mfma_f32_16x16x32_bf16 v[168:171], v[50:53], v[62:65], v[168:171]\n"
      "s_movrels_b32 s9, s80\n"
      "v_mul_f32 v140, v152, v140\n"
      "v_mul_f32 v141, v152, v141\n"
      "v_mfma_f32_16x16x32_bf16 v[184:187], v[204:207], v[156:159], 0\n"
      "s_lshr_b32 s8, s9, 8\n"
      "v_cvt_pk_bf16_f32 v162, v140, v141\n"
      "v_mul_f32 v142, v152, v142\n"
      "v_mfma_f32_16x16x32_bf16 v[184:187], v[54:57], v[62:65], v[184:187]\n"
      "s_and_b32 s9, s9, 0xff\n"
      "v_mul_f32 v143, v152, v143\n"
      "v_cvt_pk_bf16_f32 v163, v142, v143\n"
      "v_mfma_f32_16x16x32_bf16 v[140:143], v[236:239], v[156:159], 0\n"
      "s_mul_i32 s10, s8, 73\n"
      "v_mul_f32 v144, v152, v144\n"
      "v_mul_f32 v145, v152, v145\n"
      "v_mfma_f32_16x16x32_bf16 v[140:143], v[58:61], v[62:65], v[140:143]\n"
      "ds_write_b128 v124, v[160:163]\n"
      "v_cvt_pk_bf16_f32 v144, v144, v145\n"
      "v_mul_f32 v146, v152, v146\n"
      "v_mul_f32 v147, v152, v147\n"
      "v_cvt_pk_bf16_f32 v145, v146, v147\n"
      "v_max3_f32 v146, v199, v180, v181\n"
      "v_max3_f32 v153, v153, v182, v183\n"
      "v_max3_f32 v146, v146, v212, v213\n"
      "v_max3_f32 v153, v153, v214, v215\n"
      "v_max3_f32 v153, v153, v146, v188\n"
      "v_mov_b32 v146, v153\n"
      "s_add_u32 s10, s10, s9\n"
      "s_nop 0\n"
      "v_permlane16_swap_b32 v153, v146\n"
      "v_max_f32 v153, v153, v146\n"
      "v_mov_b32 v146, v153\n"
      "s_mul_i32 s28, s10, 4802\n"
      "s_nop 0\n"
      "v_permlane32_swap_b32 v153, v146\n"
      "v_max_f32 v153, v153, v146\n"
      "v_sub_f32 v196, v196, v153\n"
      "v_sub_f32 v197, v197, v153\n"
      "v_sub_f32 v198, v198, v153\n"
      "v_sub_f32 v199, v199, v153\n"
      "v_sub_f32 v180, v180, v153\n"
      "v_sub_f32 v181, v181, v153\n"
      "v_sub_f32 v182, v182, v153\n"
      "v_sub_f32 v183, v183, v153\n"
      "v_sub_f32 v212, v212, v153\n"
      "v_sub_f32 v213, v213, v153\n"
      "v_sub_f32 v214, v214, v153\n"
      "v_sub_f32 v215, v215, v153\n"
      "v_sub_f32 v188, v188, v153\n"
      "v_exp_f32 v196, v196\n"
      "v_exp_f32 v197, v197\n"
      "v_exp_f32 v198, v198\n"
      "v_cvt_pk_bf16_f32 v196, v196, v197\n"
      "v_exp_f32 v199, v199\n"
      "v_exp_f32 v180, v180\n"
      "v_cvt_pk_bf16_f32 v197, v198, v199\n"
      "s_add_u32 s56, s18, s28\n"
      "ds_write_b64 v125, v[144:145]\n"
      "v_exp_f32 v181, v181\n"
      "v_exp_f32 v182, v182\n"
      "v_cvt_pk_bf16_f32 v198, v180, v181\n"
      "v_exp_f32 v183, v183\n"
      "v_exp_f32 v212, v212\n"
      "v_cvt_pk_bf16_f32 v199, v182, v183\n"
      "v_exp_f32 v213, v213\n"
      "v_exp_f32 v214, v214\n"
      "v_exp_f32_e64 v215, v215\n"
      "v_exp_f32_e64 v188, v188\n"
      "v_cvt_pk_bf16_f32 v62, v212, v213\n"
      "v_cvt_pk_bf16_f32 v63, v214, v215\n"
      "v_cvt_pk_bf16_f32 v64, v188, 0\n"
      "v_cndmask_b32 v201, v201, v193, s[42:43]\n"
      "v_cndmask_b32 v201, v201, v189, s[44:45]\n"
      "v_mfma_f32_16x16x32_bf16 v[152:155], v[66:69], v[196:199], 0\n"
      "s_addc_u32 s57, s19, 0\n"
      "v_cndmask_b32 v201, v201, v137, s[46:47]\n"
      "v_mbcnt_lo_u32_b32 v148, -1, 0\n"
      "v_mfma_f32_16x16x32_bf16 v[152:155], v[66:69], v[62:65], v[152:155]\n"
      "s_and_b32 s56, s56, -16\n"
      "v_mbcnt_hi_u32_b32 v148, -1, v148\n"
      "v_min_u32 v148, 48, v148\n"
      "v_mfma_f32_16x16x32_bf16 v[156:159], v[208:211], v[196:199], 0\n"
      "s_sub_u32 s58, s56, 110592\n"
      "v_lshlrev_b32 v148, 1, v148\n"
      "v_add_u32 v148, s30, v148\n"
      "v_mfma_f32_16x16x32_bf16 v[156:159], v[50:53], v[62:65], v[156:159]\n"
      "ds_read_u16 v150, v148 offset:4704\n"
      "v_rcp_f32 v240, v240\n"
      "v_mfma_f32_16x16x32_bf16 v[160:163], v[204:207], v[196:199], 0\n"
      "s_subb_u32 s59, s57, 0\n"
      "v_mul_f32 v168, v240, v168\n"
      "v_mul_f32_e64 v169, v240, v169\n"
      "v_mfma_f32_16x16x32_bf16 v[160:163], v[54:57], v[62:65], v[160:163]\n"
      "s_sub_u32 s56, s56, 73728\n"
      "v_cvt_pk_bf16_f32 v168, v168, v169\n"
      "v_mul_f32 v170, v240, v170\n"
      "v_mfma_f32_16x16x32_bf16 v[164:167], v[236:239], v[196:199], 0\n"
      "s_subb_u32 s57, s57, 0\n"
      "v_mul_f32_e64 v171, v240, v171\n"
      "v_cvt_pk_bf16_f32 v169, v170, v171\n"
      "v_mfma_f32_16x16x32_bf16 v[164:167], v[58:61], v[62:65], v[164:167]\n"
      "s_mul_i32 s28, s8, 1373568\n"
      "v_mul_f32 v184, v240, v184\n"
      "v_mul_f32 v185, v240, v185\n"
      "v_cvt_pk_bf16_f32 v170, v184, v185\n"
      "v_mul_f32 v186, v240, v186\n"
      "v_mul_f32 v187, v240, v187\n"
      "v_cvt_pk_bf16_f32 v171, v186, v187\n"
      "v_mul_f32 v140, v240, v140\n"
      "v_mul_f32 v141, v240, v141\n"
      "v_cvt_pk_bf16_f32 v140, v140, v141\n"
      "v_mul_f32 v142, v240, v142\n"
      "s_mul_i32 s29, s9, 2688\n"
      "ds_write_b128 v124, v[168:171] offset:256\n"
      "s_waitcnt lgkmcnt(1)\n"
      "v_lshlrev_b32 v150, 16, v150\n"
      "v_fmac_f32 v201, s37, v150\n"
      "v_mul_f32 v143, v240, v143\n"
      "v_cvt_pk_bf16_f32 v141, v142, v143\n"
      "v_max_f32_dpp v142, v201, v201 row_ror:8 row_mask:0xf bank_mask:0xf\n"
      "s_add_u32 s28, s28, s29\n"
      "s_nop 0\n"
      "v_max_f32_dpp v142, v142, v142 row_ror:4 row_mask:0xf bank_mask:0xf\n"
      "s_cmp_eq_u32 s8, 72\n"
      "s_nop 0\n"
      "v_max_f32_dpp v142, v142, v142 row_ror:2 row_mask:0xf bank_mask:0xf\n"
      "s_cselect_b32 s8, 0, 100270464\n"
      "s_nop 0\n"
      "v_max_f32_dpp v142, v142, v142 row_ror:1 row_mask:0xf bank_mask:0xf\n"
      "s_cmp_eq_u32 s9, 72\n"
      "s_nop 0\n"
      "v_max_f32_dpp v142, v142, v142 row_bcast:15 row_mask:0xa bank_mask:0xf\n"
      "s_cselect_b32 s9, 0, 196224\n"
      "s_nop 0\n"
      "v_max_f32_dpp v142, v142, v142 row_bcast:31 row_mask:0xc bank_mask:0xf\n"
      "s_nop 0\n"
      "v_readlane_b32 s31, v142, 63\n"
      "s_add_u32 s10, s8, s9\n"
      "s_nop 0\n"
      "v_subrev_f32 v201, s31, v201\n"
      "v_exp_f32_e64 v201, v201\n"
      "s_nop 0\n"
      "v_cvt_pk_bf16_f32 v201, v201, v201\n"
      "s_add_u32 s28, s28, s10\n"
      "ds_write_b16 v130, v201\n"
      "v_mov_b32_e64 v142, 0\n"
      "s_add_u32 s52, s16, s28\n"
      "ds_read_b128 v[144:147], v131\n"
      "ds_read_b128 v[148:151], v131 offset:64\n"
      "ds_write_b64 v125, v[140:141] offset:256\n"
      "ds_write_b32 v129, v142\n"
      "ds_read_b128 v[136:139], v114 offset:18432\n"
      "ds_read_b128 v[140:143], v115 offset:18432\n"
      "s_addc_u32 s53, s17, 0\n"
      "s_cmp_lg_u32 s10, s11\n"
      "s_cbranch_scc1 L_edge_64_%=\n"
      "L_eb_65_%=:\n"
      "s_waitcnt lgkmcnt(4)\n"
      "v_mfma_f32_16x16x32_bf16 v[168:171], v[144:147], v[66:69], 0\n"
      "v_mfma_f32_16x16x32_bf16 v[168:171], v[148:151], v[66:69], v[168:171]\n"
      "s_nop 0\n"
      "v_mfma_f32_16x16x32_bf16 v[184:187], v[144:147], v[208:211], 0\n"
      "s_waitcnt lgkmcnt(0)\n"
      "v_mfma_f32_16x16x32_bf16 v[184:187], v[148:151], v[50:53], v[184:187]\n"
      "v_mfma_f32_16x16x32_bf16 v[208:211], v[144:147], v[204:207], 0\n"
      "v_mfma_f32_16x16x32_bf16 v[208:211], v[148:151], v[54:57], v[208:211]\n"
      "v_mfma_f32_16x16x32_bf16 v[204:207], v[144:147], v[236:239], 0\n"
      "s_nop 1\n"
      "v_mov_b32_dpp v168, v168 quad_perm:[0,0,0,0] row_mask:0xf bank_mask:0xf\n"
      "v_mfma_f32_16x16x32_bf16 v[204:207], v[148:151], v[58:61], v[204:207]\n"
      "v_rcp_f32 v168, v168\n"
      "v_mfma_f32_16x16x32_bf16 v[144:147], a[0:3], v[136:139], 0\n"
      "s_nop 0\n"
      "v_cndmask_b32 v184, v184, v208, s[42:43]\n"
      "v_mfma_f32_16x16x32_bf16 v[144:147], a[36:39], v[140:143], v[144:147]\n"
      "v_mfma_f32_16x16x32_bf16 v[148:151], a[4:7], v[136:139], 0\n"
      "s_nop 1\n"
      "v_cndmask_b32 v184, v184, v204, s[44:45]\n"
      "v_mfma_f32_16x16x32_bf16 v[148:151], a[40:43], v[140:143], v[148:151]\n"
      "v_mul_f32_e64 v184, v168, v184\n"
      "v_cvt_pk_bf16_f32 v184, v184, v184\n"
      "ds_write_b16 v135, v184\n"
      "s_branch L_join_10_%=\n"
      "L_flg_61_%=:\n"
      "v_cmp_gt_i32 vcc, 0, v140\n"
      "s_cbranch_vccz L_fb_62_%=\n"
      "s_sleep 1\n"
      "ds_read_b128 v[140:143], v128 offset:3072\n"
      "s_waitcnt lgkmcnt(0)\n"
      "v_bitop3_b32 v140, v140, v141, s4 bitop3:0x7e\n"
      "v_bitop3_b32 v142, v142, v143, s4 bitop3:0x7e\n"
      "v_or_b32 v140, v140, v142\n"
      "v_cmp_ne_u32 s[48:49], 0, v140\n"
      "s_branch L_flg_61_%=\n"
      "L_edge_64_%=:\n"
      "s_mov_b32 s11, s10\n"
      "s_sub_u32 s28, s60, s8\n"
      "s_sub_u32 s29, s61, s9\n"
      "s_mov_b32 s60, s8\n"
      "s_mov_b32 s61, s9\n"
      "s_mov_b64 exec, s[64:65]\n"
      "v_add_u32 v116, s28, v116\n"
      "s_mov_b64 exec, s[70:71]\n"
      "v_add_u32 v116, s29, v116\n"
      "s_mov_b64 exec, s[66:67]\n"
      "v_add_u32 v117, s28, v117\n"
      "s_mov_b64 exec, s[72:73]\n"
      "v_add_u32_e64 v117, s29, v117\n"
      "s_mov_b64 exec, s[68:69]\n"
      "v_add_u32 v118, s28, v118\n"
      "s_mov_b64 exec, s[74:75]\n"
      "v_add_u32_e64 v118, s29, v118\n"
      "s_mov_b64 exec, -1\n"
      "s_nop 4\n"
      "s_branch L_eb_65_%=\n"
      "L_gen_19_%=:\n"
      "s_waitcnt lgkmcnt(0)\n"
      "s_nop 7\n"
      "s_nop 7\n"
      "s_cmp_eq_u64 s[48:49], 0\n"
      "s_cbranch_scc1 L_go_75_%=\n"
      "s_mov_b32 m0, s27\n"
      "s_nop 0\n"
      "s_movrels_b32 s9, s80\n"
      "s_lshr_b32 s8, s9, 8\n"
      "s_and_b32 s9, s9, 0xff\n"
      "s_mul_i32 s10, s8, 73\n"
      "s_add_u32 s10, s10, s9\n"
      "s_mul_i32 s30, s10, 4802\n"
      "s_add_u32 s30, s30, s18\n"
      "s_and_b32 s29, s30, 15\n"
      "s_add_u32 s30, s29, 115456\n"
      "s_lshl_b64 s[34:35], s[48:49], 4\n"
      "s_lshr_b64 s[34:35], s[34:35], 5\n"
      "s_cbranch_scc1 L_gen_20_%=\n"
      "v_mbcnt_lo_u32_b32 v152, -1, 0\n"
      "v_mbcnt_hi_u32_b32 v152, -1, v152\n"
      "v_lshlrev_b32 v153, 1, v152\n"
      "v_add_u32 v154, 4802, v153\n"
      "v_cmp_gt_u32 vcc, s29, v153\n"
      "s_nop 1\n"
      "v_cndmask_b32 v153, v154, v153, vcc\n"
      "v_add_u32 v153, 115456, v153\n"
      "v_mov_b32 v154, 0\n"
      "s_mov_b64 exec, 0x7f\n"
      "ds_write_b16 v153, v154\n"
      "s_mov_b64 exec, -1\n"
      "v_lshlrev_b32 v153, 4, v152\n"
      "v_add_u32 v153, 115456, v153\n"
      "v_min_u32 v152, 116160, v153\n"
      "ds_read_b128 v[188:191], v153\n"
      "ds_read_b128 v[208:211], v153 offset:1024\n"
      "ds_read_b128 v[216:219], v153 offset:2048\n"
      "ds_read_b128 v[212:215], v153 offset:3072\n"
      "ds_read_b128 v[144:147], v152 offset:4096\n"
      "s_waitcnt lgkmcnt(4)\n"
      "v_or3_b32 v188, v188, v189, v190\n"
      "s_waitcnt lgkmcnt(3)\n"
      "v_or3_b32 v188, v188, v191, v208\n"
      "v_or3_b32 v188, v188, v209, v210\n"
      "s_waitcnt lgkmcnt(2)\n"
      "v_or3_b32 v188, v188, v211, v216\n"
      "v_or3_b32 v188, v188, v217, v218\n"
      "s_waitcnt lgkmcnt(1)\n"
      "v_or3_b32 v188, v188, v219, v212\n"
      "v_or3_b32 v188, v188, v213, v214\n"
      "s_waitcnt lgkmcnt(0)\n"
      "v_or3_b32 v188, v188, v215, v144\n"
      "v_or3_b32 v188, v188, v145, v146\n"
      "v_bitop3_b32 v188, v188, v147, s40 bitop3:0xa8\n"
      "v_cmp_ne_u32 s[48:49], 0, v188\n"
      "s_cmp_lg_u64 s[48:49], 0\n"
      "s_cbranch_scc1 L_gen_20_%=\n"
      "s_cmp_eq_u64 s[50:51], 0\n"
      "s_cbranch_scc1 L_fast_22_%=\n"
      "L_go_75_%=:\n"
      "v_mfma_f32_16x16x32_bf16 v[152:155], v[248:251], v[180:183], v[90:93]\n"
      "ds_read_b128 v[188:191], v128 offset:2048\n"
      "v_max3_f32 v211, v164, v165, v166\n"
      "v_mfma_f32_16x16x32_bf16 v[152:155], v[34:37], v[38:41], v[152:155]\n"
      "v_max3_f32 v209, v167, v184, v185\n"
      "v_max3_f32 v211, v211, v186, v187\n"
      "v_mfma_f32_16x16x32_bf16 v[216:219], v[136:139], v[180:183], v[94:97]\n"
      "v_max3_f32 v209, v209, v192, v193\n"
      "v_max3_f32 v211, v211, v194, v195\n"
      "v_mfma_f32_16x16x32_bf16 v[216:219], v[40:43], v[38:41], v[216:219]\n"
      "v_max3_f32 v211, v211, v209, v148\n"
      "v_mov_b32 v209, v211\n"
      "v_mfma_f32_16x16x32_bf16 v[212:215], v[140:143], v[180:183], v[72:75]\n"
      "s_nop 0\n"
      "v_permlane16_swap_b32 v211, v209\n"
      "v_mfma_f32_16x16x32_bf16 v[212:215], v[228:231], v[136:139], v[212:215]\n"
      "v_max_f32 v211, v211, v209\n"
      "v_mov_b32 v209, v211\n"
      "v_mfma_f32_16x16x32_bf16 v[212:215], v[156:159], v[38:41], v[212:215]\n"
      "s_nop 0\n"
      "v_permlane32_swap_b32 v211, v209\n"
      "v_mfma_f32_16x16x32_bf16 v[144:147], v[200:203], v[180:183], v[98:101]\n"
      "v_max_f32 v211, v211, v209\n"
      "v_sub_f32 v164, v164, v211\n"
      "v_mfma_f32_16x16x32_bf16 v[144:147], v[46:49], v[38:41], v[144:147]\n"
      "s_waitcnt lgkmcnt(0)\n"
      "v_sub_f32 v165, v165, v211\n"
      "v_sub_f32 v166, v166, v211\n"
      "v_sub_f32 v167, v167, v211\n"
      "v_sub_f32 v184, v184, v211\n"
      "v_sub_f32 v185, v185, v211\n"
      "v_sub_f32 v186, v186, v211\n"
      "v_sub_f32 v187, v187, v211\n"
      "v_sub_f32 v192, v192, v211\n"
      "v_sub_f32 v193, v193, v211\n"
      "v_sub_f32 v194, v194, v211\n"
      "v_sub_f32 v195, v195, v211\n"
      "v_sub_f32 v148, v148, v211\n"
      "v_exp_f32 v164, v164\n"
      "v_exp_f32 v165, v165\n"
      "v_exp_f32_e64 v166, v166\n"
      "v_cvt_pk_bf16_f32 v164, v164, v165\n"
      "v_exp_f32 v167, v167\n"
      "v_exp_f32 v184, v184\n"
      "v_cvt_pk_bf16_f32 v165, v166, v167\n"
      "v_exp_f32 v185, v185\n"
      "v_exp_f32 v186, v186\n"
      "v_cvt_pk_bf16_f32 v166, v184, v185\n"
      "v_exp_f32 v187, v187\n"
      "v_exp_f32 v192, v192\n"
      "v_cvt_pk_bf16_f32 v167, v186, v187\n"
      "v_exp_f32 v193, v193\n"
      "v_exp_f32 v194, v194\n"
      "v_exp_f32 v195, v195\n"
      "v_mfma_f32_16x16x32_bf16 v[184:187], v[248:251], v[196:199], v[102:105]\n"
      "v_exp_f32 v148, v148\n"
      "v_mfma_f32_16x16x32_bf16 v[184:187], v[34:37], v[44:47], v[184:187]\n"
      "v_cvt_pk_bf16_f32 v62, v192, v193\n"
      "v_cvt_pk_bf16_f32 v63, v194, v195\n"
      "v_cvt_pk_bf16_f32 v64, v148, 0\n"
      "v_bitop3_b32 v188, v188, v189, s4 bitop3:0x7e\n"
      "v_bitop3_b32 v190, v190, v191, s4 bitop3:0x7e\n"
      "v_mfma_f32_16x16x32_bf16 v[180:183], v[66:69], v[164:167], 0\n"
      "ds_read_b128 v[192:195], v119 offset:12288\n"
      "v_or_b32 v188, v188, v190\n"
      "v_mfma_f32_16x16x32_bf16 v[180:183], v[66:69], v[62:65], v[180:183]\n"
      "v_cmp_ne_u32 vcc, 0, v188\n"
      "v_max3_f32 v214, v152, v153, v154\n"
      "v_mfma_f32_16x16x32_bf16 v[208:211], v[220:223], v[164:167], 0\n"
      "s_mov_b64 s[48:49], vcc\n"
      "v_max3_f32 v215, v155, v216, v217\n"
      "v_max3_f32 v214, v214, v218, v219\n"
      "v_mfma_f32_16x16x32_bf16 v[208:211], v[50:53], v[62:65], v[208:211]\n"
      "s_cbranch_vccnz L_flg_76_%=\n"
      "L_fb_77_%=:\n"
      "v_max3_f32 v215, v215, v144, v145\n"
      "v_mfma_f32_16x16x32_bf16 v[188:191], v[224:227], v[164:167], 0\n"
      "v_max3_f32 v214, v214, v146, v147\n"
      "v_max3_f32 v214, v214, v215, v212\n"
      "v_mfma_f32_16x16x32_bf16 v[188:191], v[54:57], v[62:65], v[188:191]\n"
      "v_mov_b32_e64 v215, v214\n"
      "v_mfma_f32_16x16x32_bf16 v[248:251], v[160:163], v[164:167], 0\n"
      "s_nop 0\n"
      "v_permlane16_swap_b32 v214, v215\n"
      "v_mfma_f32_16x16x32_bf16 v[248:251], v[58:61], v[62:65], v[248:251]\n"
      "v_max_f32 v214, v214, v215\n"
      "v_mov_b32 v215, v214\n"
      "v_mfma_f32_16x16x32_bf16 v[164:167], v[136:139], v[196:199], v[106:109]\n"
      "s_nop 0\n"
      "v_permlane32_swap_b32 v214, v215\n"
      "v_mfma_f32_16x16x32_bf16 v[164:167], v[40:43], v[44:47], v[164:167]\n"
      "v_max_f32 v214, v214, v215\n"
      "v_sub_f32 v152, v152, v214\n"
      "v_sub_f32 v153, v153, v214\n"
      "v_sub_f32 v154, v154, v214\n"
      "v_sub_f32 v155, v155, v214\n"
      "v_sub_f32 v216, v216, v214\n"
      "v_sub_f32 v217, v217, v214\n"
      "v_sub_f32 v218, v218, v214\n"
      "v_sub_f32 v219, v219, v214\n"
      "v_sub_f32 v144, v144, v214\n"
      "v_sub_f32 v145, v145, v214\n"
      "v_sub_f32 v146, v146, v214\n"
      "v_sub_f32 v147, v147, v214\n"
      "v_sub_f32 v212, v212, v214\n"
      "v_exp_f32 v152, v152\n"
      "v_exp_f32 v153, v153\n"
      "v_exp_f32_e64 v154, v154\n"
      "v_cvt_pk_bf16_f32 v152, v152, v153\n"
      "v_exp_f32 v155, v155\n"
      "v_mfma_f32_16x16x32_bf16 v[204:207], v[140:143], v[196:199], v[74:77]\n"
      "s_waitcnt lgkmcnt(0)\n"
      "v_exp_f32_e64 v216, v216\n"
      "v_mfma_f32_16x16x32_bf16 v[204:207], v[228:231], v[200:203], v[204:207]\n"
      "global_store_dwordx4 v116, v[192:195], s[54:55] offset:256 sc1\n"
      "v_mfma_f32_16x16x32_bf16 v[204:207], v[156:159], v[44:47], v[204:207]\n"
      "v_cvt_pk_bf16_f32 v153, v154, v155\n"
      "v_exp_f32 v217, v217\n"
      "v_mfma_f32_16x16x32_bf16 v[136:139], v[200:203], v[196:199], v[110:113]\n"
      "v_exp_f32 v218, v218\n"
      "v_mfma_f32_16x16x32_bf16 v[136:139], v[46:49], v[44:47], v[136:139]\n"
      "v_cvt_pk_bf16_f32 v154, v216, v217\n"
      "v_exp_f32 v219, v219\n"
      "v_exp_f32 v144, v144\n"
      "v_cvt_pk_bf16_f32 v155, v218, v219\n"
      "v_mfma_f32_16x16x32_bf16 v[192:195], v[228:231], v[140:143], v[76:79]\n"
      "v_exp_f32 v145, v145\n"
      "v_exp_f32 v146, v146\n"
      "v_exp_f32 v147, v147\n"
      "v_exp_f32 v212, v212\n"
      "v_cvt_pk_bf16_f32 v62, v144, v145\n"
      "v_cvt_pk_bf16_f32 v63, v146, v147\n"
      "v_mfma_f32_16x16x16_bf16 v[192:195], v[158:159], v[156:157], v[192:195]\n"
      "v_cvt_pk_bf16_f32 v64, v212, 0\n"
      "v_rcp_f32_e64 v180, v180\n"
      "v_max3_f32 v181, v184, v185, v186\n"
      "v_mfma_f32_16x16x32_bf16 v[196:199], v[66:69], v[152:155], 0\n"
      "ds_read_b128 v[140:143], v120\n"
      "v_mul_f32 v208, v180, v208\n"
      "v_mfma_f32_16x16x32_bf16 v[196:199], v[66:69], v[62:65], v[196:199]\n"
      "v_mul_f32 v209, v180, v209\n"
      "v_cvt_pk_bf16_f32 v208, v208, v209\n"
      "v_mfma_f32_16x16x32_bf16 v[200:203], v[220:223], v[152:155], 0\n"
      "v_mul_f32 v210, v180, v210\n"
      "v_mul_f32 v211, v180, v211\n"
      "v_mfma_f32_16x16x32_bf16 v[200:203], v[50:53], v[62:65], v[200:203]\n"
      "v_cvt_pk_bf16_f32 v209, v210, v211\n"
      "v_mul_f32 v188, v180, v188\n"
      "v_mul_f32 v189, v180, v189\n"
      "v_cvt_pk_bf16_f32 v210, v188, v189\n"
      "v_mfma_f32_16x16x32_bf16 v[216:219], v[224:227], v[152:155], 0\n"
      "v_mul_f32 v190, v180, v190\n"
      "v_mul_f32 v191, v180, v191\n"
      "v_mfma_f32_16x16x32_bf16 v[216:219], v[54:57], v[62:65], v[216:219]\n"
      "v_cvt_pk_bf16_f32 v211, v190, v191\n"
      "v_mul_f32 v248, v180, v248\n"
      "v_mul_f32 v249, v180, v249\n"
      "v_cvt_pk_bf16_f32 v248, v248, v249\n"
      "v_mul_f32 v250, v180, v250\n"
      "v_mul_f32 v251, v180, v251\n"
      "v_mfma_f32_16x16x32_bf16 v[188:191], v[160:163], v[152:155], 0\n"
      "v_cvt_pk_bf16_f32 v249, v250, v251\n"
      "v_max3_f32 v180, v187, v164, v165\n"
      "v_mfma_f32_16x16x32_bf16 v[188:191], v[58:61], v[62:65], v[188:191]\n"
      "ds_write_b128 v124, v[208:211] offset:18432\n"
      "v_max3_f32 v181, v181, v166, v167\n"
      "v_max3_f32 v180, v180, v136, v137\n"
      "v_max3_f32 v181, v181, v138, v139\n"
      "v_max3_f32 v181, v181, v180, v204\n"
      "v_mov_b32 v180, v181\n"
      "s_waitcnt lgkmcnt(1)\n"
      "s_nop 0\n"
      "v_permlane16_swap_b32 v181, v180\n"
      "v_max_f32 v181, v181, v180\n"
      "v_mov_b32 v180, v181\n"
      "s_nop 1\n"
      "v_permlane32_swap_b32 v181, v180\n"
      "v_max_f32 v181, v181, v180\n"
      "v_sub_f32 v184, v184, v181\n"
      "v_sub_f32 v185, v185, v181\n"
      "v_sub_f32 v186, v186, v181\n"
      "v_sub_f32 v187, v187, v181\n"
      "v_sub_f32 v164, v164, v181\n"
      "v_sub_f32 v165, v165, v181\n"
      "v_sub_f32 v166, v166, v181\n"
      "v_sub_f32 v167, v167, v181\n"
      "v_sub_f32 v136, v136, v181\n"
      "v_sub_f32 v137, v137, v181\n"
      "v_sub_f32 v138, v138, v181\n"
      "v_sub_f32 v139, v139, v181\n"
      "v_sub_f32 v204, v204, v181\n"
      "v_exp_f32 v184, v184\n"
      "v_exp_f32 v185, v185\n"
      "v_exp_f32 v186, v186\n"
      "v_cvt_pk_bf16_f32 v184, v184, v185\n"
      "v_exp_f32 v187, v187\n"
      "v_exp_f32 v164, v164\n"
      "v_cvt_pk_bf16_f32 v185, v186, v187\n"
      "v_exp_f32 v165, v165\n"
      "v_exp_f32 v166, v166\n"
      "v_cvt_pk_bf16_f32 v186, v164, v165\n"
      "v_exp_f32 v167, v167\n"
      "v_exp_f32 v136, v136\n"
      "ds_write_b64 v125, v[248:249] offset:18432\n"
      "v_cvt_pk_bf16_f32 v187, v166, v167\n"
      "v_exp_f32 v137, v137\n"
      "v_exp_f32 v138, v138\n"
      "v_exp_f32 v139, v139\n"
      "v_exp_f32 v204, v204\n"
      "v_cvt_pk_bf16_f32 v62, v136, v137\n"
      "v_cvt_pk_bf16_f32 v63, v138, v139\n"
      "v_cvt_pk_bf16_f32 v64, v204, 0\n"
      "v_rcp_f32 v196, v196\n"
      "v_mfma_f32_16x16x32_bf16 v[136:139], v[66:69], v[184:187], 0\n"
      "global_store_dwordx4 v117, v[140:143], s[54:55] sc1\n"
      "v_mfma_f32_16x16x32_bf16 v[136:139], v[66:69], v[62:65], v[136:139]\n"
      "ds_read_b128 v[208:211], v123 offset:256\n"
      "v_mul_f32 v200, v196, v200\n"
      "v_mfma_f32_16x16x32_bf16 v[140:143], v[220:223], v[184:187], 0\n"
      "ds_read_b128 v[164:167], v123 offset:3328\n"
      "v_mul_f32 v201, v196, v201\n"
      "v_mfma_f32_16x16x32_bf16 v[140:143], v[50:53], v[62:65], v[140:143]\n"
      "ds_read_b128 v[248:251], v123 offset:6400\n"
      "v_cvt_pk_bf16_f32 v200, v200, v201\n"
      "v_mfma_f32_16x16x32_bf16 v[152:155], v[224:227], v[184:187], 0\n"
      "ds_read_b128 v[144:147], v121\n"
      "v_mul_f32 v202, v196, v202\n"
      "v_mfma_f32_16x16x32_bf16 v[152:155], v[54:57], v[62:65], v[152:155]\n"
      "ds_read_b128 v[228:231], v123 offset:9472\n"
      "v_mul_f32 v203, v196, v203\n"
      "v_mfma_f32_16x16x32_bf16 v[156:159], v[160:163], v[184:187], 0\n"
      "ds_read_b128 v[232:235], v123 offset:12544\n"
      "v_cvt_pk_bf16_f32 v201, v202, v203\n"
      "v_mfma_f32_16x16x32_bf16 v[156:159], v[58:61], v[62:65], v[156:159]\n"
      "v_mul_f32 v216, v196, v216\n"
      "v_mul_f32 v217, v196, v217\n"
      "v_cvt_pk_bf16_f32 v202, v216, v217\n"
      "v_mul_f32 v218, v196, v218\n"
      "v_mul_f32 v219, v196, v219\n"
      "v_cvt_pk_bf16_f32 v203, v218, v219\n"
      "v_mul_f32 v188, v196, v188\n"
      "v_mul_f32 v189, v196, v189\n"
      "v_cvt_pk_bf16_f32 v188, v188, v189\n"
      "v_mul_f32 v190, v196, v190\n"
      "v_mul_f32 v191, v196, v191\n"
      "v_cvt_pk_bf16_f32 v189, v190, v191\n"
      "s_waitcnt lgkmcnt(4)\n"
      "v_mfma_f32_16x16x32_bf16 v[180:183], a[216:219], v[208:211], 0\n"
      "ds_read_b128 v[196:199], v123 offset:15616\n"
      "v_cndmask_b32 v149, v149, v213, s[42:43]\n"
      "v_mfma_f32_16x16x32_bf16 v[180:183], a[228:231], v[164:167], v[180:183]\n"
      "ds_write_b128 v124, v[200:203] offset:18688\n"
      "s_waitcnt lgkmcnt(3)\n"
      "v_mfma_f32_16x16x32_bf16 v[184:187], a[220:223], v[208:211], 0\n"
      "ds_read_b128 v[216:219], v123 offset:512\n"
      "v_cndmask_b32 v149, v149, v205, s[44:45]\n"
      "v_mfma_f32_16x16x32_bf16 v[184:187], a[232:235], v[164:167], v[184:187]\n"
      "ds_write_b64 v125, v[188:189] offset:18688\n"
      "v_mfma_f32_16x16x32_bf16 v[188:191], a[224:227], v[208:211], 0\n"
      "ds_read_b128 v[208:211], v123 offset:3584\n"
      "v_cndmask_b32 v149, v149, v193, s[46:47]\n"
      "v_mfma_f32_16x16x32_bf16 v[188:191], a[236:239], v[164:167], v[188:191]\n"
      "ds_read_b128 v[212:215], v123 offset:6656\n"
      "v_max_f32_dpp v193, v149, v149 row_ror:8 row_mask:0xf bank_mask:0xf\n"
      "v_mfma_f32_16x16x32_bf16 v[180:183], a[240:243], v[248:251], v[180:183]\n"
      "ds_read_b128 v[164:167], v123 offset:9728\n"
      "v_max_f32_dpp v193, v193, v193 row_ror:4 row_mask:0xf bank_mask:0xf\n"
      "v_mfma_f32_16x16x32_bf16 v[180:183], a[252:255], v[228:231], v[180:183]\n"
      "s_waitcnt lgkmcnt(6)\n"
      "v_max_f32_dpp v193, v193, v193 row_ror:2 row_mask:0xf bank_mask:0xf\n"
      "v_mfma_f32_16x16x32_bf16 v[184:187], a[244:247], v[248:251], v[184:187]\n"
      "s_nop 0\n"
      "v_max_f32_dpp v193, v193, v193 row_ror:1 row_mask:0xf bank_mask:0xf\n"
      "v_mfma_f32_16x16x32_bf16 v[184:187], v[0:3], v[228:231], v[184:187]\n"
      "s_mov_b32 exec_hi, 0\n"
      "global_store_dwordx4 v118, v[144:147], s[54:55] sc1\n"
      "s_mov_b32 exec_hi, -1\n"
      "v_mfma_f32_16x16x32_bf16 v[188:191], a[248:251], v[248:251], v[188:191]\n"
      "ds_read_b128 v[248:251], v123 offset:12800\n"
      "v_max_f32_dpp v193, v193, v193 row_bcast:15 row_mask:0xa bank_mask:0xf\n"
      "v_mfma_f32_16x16x32_bf16 v[188:191], v[4:7], v[228:231], v[188:191]\n"
      "ds_read_b128 v[228:231], v123 offset:15872\n"
      "v_max_f32_dpp v193, v193, v193 row_bcast:31 row_mask:0xc bank_mask:0xf\n"
      "s_waitcnt lgkmcnt(4)\n"
      "v_mfma_f32_16x16x32_bf16 v[180:183], v[8:11], v[232:235], v[180:183]\n"
      "s_add_u32 s28, s27, 2\n"
      "v_readlane_b32 s31, v193, 63\n"
      "s_min_u32 m0, s28, s3\n"
      "v_mfma_f32_16x16x32_bf16 v[180:183], v[20:23], v[196:199], v[180:183]\n"
      "s_movrels_b32 s9, s80\n"
      "v_subrev_f32 v149, s31, v149\n"
      "v_exp_f32 v149, v149\n"
      "v_mfma_f32_16x16x32_bf16 v[184:187], v[12:15], v[232:235], v[184:187]\n"
      "s_waitcnt lgkmcnt(2)\n"
      "v_cvt_pk_bf16_f32 v149, v149, v149\n"
      "s_lshr_b32 s8, s9, 8\n"
      "v_mfma_f32_16x16x32_bf16 v[184:187], v[24:27], v[196:199], v[184:187]\n"
      "ds_write_b16 v130, v149\n"
      "v_mfma_f32_16x16x32_bf16 v[188:191], v[16:19], v[232:235], v[188:191]\n"
      "ds_read_b128 v[232:235], v131\n"
      "v_fma_f32 v149, v180, v180, s39\n"
      "v_mfma_f32_16x16x32_bf16 v[188:191], v[28:31], v[196:199], v[188:191]\n"
      "ds_read_b128 v[144:147], v131 offset:64\n"
      "v_fmac_f32 v149, v181, v181\n"
      "v_mfma_f32_16x16x32_bf16 v[192:195], a[216:219], v[216:219], 0\n"
      "s_and_b32 s9, s9, 0xff\n"
      "v_fmac_f32 v149, v182, v182\n"
      "v_fmac_f32 v149, v183, v183\n"
      "v_mfma_f32_16x16x32_bf16 v[192:195], a[228:231], v[208:211], v[192:195]\n"
      "s_waitcnt lgkmcnt(3)\n"
      "v_fmac_f32 v149, v184, v184\n"
      "v_fmac_f32 v149, v185, v185\n"
      "v_mfma_f32_16x16x32_bf16 v[196:199], a[220:223], v[216:219], 0\n"
      "s_mul_i32 s10, s8, 73\n"
      "v_fmac_f32 v149, v186, v186\n"
      "v_fmac_f32 v149, v187, v187\n"
      "v_mfma_f32_16x16x32_bf16 v[196:199], a[232:235], v[208:211], v[196:199]\n"
      "s_add_u32 s10, s10, s9\n"
      "v_fmac_f32 v149, v188, v188\n"
      "v_fmac_f32_e64 v149, v189, v189\n"
      "v_mfma_f32_16x16x32_bf16 v[200:203], a[224:227], v[216:219], 0\n"
      "s_mul_i32 s28, s10, 4802\n"
      "v_fmac_f32_e64 v149, v190, v190\n"
      "v_fmac_f32 v149, v191, v191\n"
      "v_mfma_f32_16x16x32_bf16 v[200:203], a[236:239], v[208:211], v[200:203]\n"
      "s_add_u32 s56, s18, s28\n"
      "s_addc_u32 s57, s19, 0\n"
      "v_mfma_f32_16x16x32_bf16 v[192:195], a[240:243], v[212:215], v[192:195]\n"
      "s_and_b32 s56, s56, -16\n"
      "s_sub_u32 s58, s56, 110592\n"
      "s_subb_u32 s59, s57, 0\n"
      "v_mfma_f32_16x16x32_bf16 v[192:195], a[252:255], v[164:167], v[192:195]\n"
      "s_waitcnt lgkmcnt(0)\n"
      "s_sub_u32 s56, s56, 73728\n"
      "v_mfma_f32_16x16x32_bf16 v[196:199], a[244:247], v[212:215], v[196:199]\n"
      "s_subb_u32 s57, s57, 0\n"
      "v_permlane16_swap_b32 v151, v149\n"
      "v_mfma_f32_16x16x32_bf16 v[196:199], v[0:3], v[164:167], v[196:199]\n"
      "s_mul_i32 s28, s8, 1373568\n"
      "v_add_f32 v151, v151, v149\n"
      "v_mfma_f32_16x16x32_bf16 v[200:203], a[248:251], v[212:215], v[200:203]\n"
      "s_mul_i32 s29, s9, 2688\n"
      "s_add_u32 s28, s28, s29\n"
      "s_cmp_eq_u32 s8, 72\n"
      "v_mfma_f32_16x16x32_bf16 v[200:203], v[4:7], v[164:167], v[200:203]\n"
      "s_cselect_b32 s8, 0, 100270464\n"
      "s_cmp_eq_u32 s9, 72\n"
      "v_mfma_f32_16x16x32_bf16 v[192:195], v[8:11], v[248:251], v[192:195]\n"
      "s_cselect_b32 s9, 0, 196224\n"
      "s_add_u32 s10, s8, s9\n"
      "s_add_u32 s28, s28, s10\n"
      "v_mfma_f32_16x16x32_bf16 v[192:195], v[20:23], v[228:231], v[192:195]\n"
      "s_add_u32 s52, s16, s28\n"
      "s_addc_u32 s53, s17, 0\n"
      "v_mfma_f32_16x16x32_bf16 v[196:199], v[12:15], v[248:251], v[196:199]\n"
      "s_add_u32 s27, s27, 1\n"
      "s_cmp_ge_u32 s27, s26\n"
      "s_cselect_b32 s28, 1, 0\n"
      "v_mfma_f32_16x16x32_bf16 v[196:199], v[24:27], v[228:231], v[196:199]\n"
      "ds_read_b128 v[208:211], v114\n"
      "v_fma_f32 v212, v192, v192, s39\n"
      "v_mfma_f32_16x16x32_bf16 v[200:203], v[16:19], v[248:251], v[200:203]\n"
      "ds_read_b128 v[216:219], v115\n"
      "v_fmac_f32 v212, v193, v193\n"
      "v_mfma_f32_16x16x32_bf16 v[200:203], v[28:31], v[228:231], v[200:203]\n"
      "s_xor_b32 s29, s10, s11\n"
      "v_fmac_f32 v212, v194, v194\n"
      "v_fmac_f32 v212, v195, v195\n"
      "v_mfma_f32_16x16x32_bf16 v[228:231], v[232:235], v[66:69], 0\n"
      "s_or_b32 s28, s28, s29\n"
      "v_fmac_f32 v212, v196, v196\n"
      "v_fmac_f32 v212, v197, v197\n"
      "v_mfma_f32_16x16x32_bf16 v[228:231], v[144:147], v[66:69], v[228:231]\n"
      "v_fmac_f32 v212, v198, v198\n"
      "v_fmac_f32 v212, v199, v199\n"
      "v_mfma_f32_16x16x32_bf16 v[248:251], v[232:235], v[220:223], 0\n"
      "v_fmac_f32 v212, v200, v200\n"
      "v_fmac_f32 v212, v201, v201\n"
      "v_mfma_f32_16x16x32_bf16 v[248:251], v[144:147], v[50:53], v[248:251]\n"
      "v_fmac_f32 v212, v202, v202\n"
      "v_fmac_f32 v212, v203, v203\n"
      "v_mfma_f32_16x16x32_bf16 v[220:223], v[232:235], v[224:227], 0\n"
      "v_mov_b32 v213, v212\n"
      "v_mov_b32_dpp v228, v228 quad_perm:[0,0,0,0] row_mask:0xf bank_mask:0xf\n"
      "v_mfma_f32_16x16x32_bf16 v[220:223], v[144:147], v[54:57], v[220:223]\n"
      "v_permlane16_swap_b32 v212, v213\n"
      "v_mfma_f32_16x16x32_bf16 v[224:227], v[232:235], v[160:163], 0\n"
      "s_waitcnt lgkmcnt(0)\n"
      "v_add_f32 v212, v212, v213\n"
      "v_mfma_f32_16x16x32_bf16 v[224:227], v[144:147], v[58:61], v[224:227]\n"
      "s_nop 0\n"
      "v_permlane32_swap_b32 v151, v212\n"
      "v_mfma_f32_16x16x32_bf16 v[160:163], a[0:3], v[208:211], 0\n"
      "v_add_f32_e64 v151, v151, v212\n"
      "v_cndmask_b32 v248, v248, v220, s[42:43]\n"
      "v_mfma_f32_16x16x32_bf16 v[160:163], a[36:39], v[216:219], v[160:163]\n"
      "ds_write_b32 v129, v151 offset:1024\n"
      "v_mfma_f32_16x16x32_bf16 v[212:215], a[4:7], v[208:211], 0\n"
      "v_cndmask_b32 v248, v248, v224, s[44:45]\n"
      "v_mfma_f32_16x16x32_bf16 v[212:215], a[40:43], v[216:219], v[212:215]\n"
      "v_rcp_f32 v228, v228\n"
      "s_nop 0\n"
      "v_mul_f32 v248, v228, v248\n"
      "v_cvt_pk_bf16_f32 v248, v248, v248\n"
      "ds_write_b16 v135, v248 offset:16\n"
      "s_branch L_join_21_%=\n"
      "L_flg_76_%=:\n"
      "v_cmp_gt_i32 vcc, 0, v188\n"
      "s_cbranch_vccz L_fb_77_%=\n"
      "s_sleep 1\n"
      "ds_read_b128 v[188:191], v128 offset:2048\n"
      "s_waitcnt lgkmcnt(0)\n"
      "v_bitop3_b32 v188, v188, v189, s4 bitop3:0x7e\n"
      "v_bitop3_b32 v190, v190, v191, s4 bitop3:0x7e\n"
      "v_or_b32_e64 v188, v188, v190\n"
      "v_cmp_ne_u32 s[48:49], 0, v188\n"
      "s_branch L_flg_76_%=\n"
      "L_gen_20_%=:\n"
      "s_waitcnt lgkmcnt(0)\n"
      "s_nop 7\n"
      "s_nop 7\n"
      "v_mbcnt_lo_u32_b32 v208, -1, 0\n"
      "v_mbcnt_hi_u32_b32 v208, -1, v208\n"
      "v_and_b32 v153, 15, v208\n"
      "v_lshrrev_b32 v209, 4, v208\n"
      "v_mul_u32_u24 v154, 0x62, v153\n"
      "v_lshl_add_u32 v154, v209, 3, v154\n"
      "v_add_u32 v154, s30, v154\n"
      "v_and_b32 v208, -4, v154\n"
      "v_and_b32 v154, 2, v154\n"
      "v_lshlrev_b32 v209, 2, v209\n"
      "v_sub_u32 v153, v153, v209\n"
      "v_and_b32 v209, 1, v153\n"
      "v_lshlrev_b32_e64 v209, 4, v209\n"
      "v_lshrrev_b32_e64 v153, 1, v153\n"
      "v_cmp_eq_u32 vcc, 0, v153\n"
      "s_movk_i32 s31, 0x3fb9\n"
      "v_lshlrev_b32 v209, v209, s31\n"
      "ds_read2_b32 v[212:213], v208 offset0:0 offset1:1\n"
      "ds_read2_b32 v[210:211], v208 offset0:8 offset1:9\n"
      "ds_read2_b32 v[240:241], v208 offset0:16 offset1:17\n"
      "ds_read2_b32 v[216:217], v208 offset0:2 offset1:10\n"
      "ds_read2_b32 v[214:215], v208 offset0:18 offset1:24\n"
      "v_add_u32 v208, 1568, v208\n"
      "ds_read2_b32 v[190:191], v208 offset0:0 offset1:1\n"
      "ds_read2_b32 v[218:219], v208 offset0:8 offset1:9\n"
      "ds_read2_b32 v[188:189], v208 offset0:16 offset1:17\n"
      "ds_read2_b32 v[242:243], v208 offset0:2 offset1:10\n"
      "ds_read2_b32 v[252:253], v208 offset0:18 offset1:24\n"
      "v_add_u32 v208, 1568, v208\n"
      "ds_read2_b32 v[206:207], v208 offset0:0 offset1:1\n"
      "ds_read2_b32 v[204:205], v208 offset0:8 offset1:9\n"
      "ds_read2_b32 v[234:235], v208 offset0:16 offset1:17\n"
      "ds_read2_b32 v[232:233], v208 offset0:2 offset1:10\n"
      "ds_read2_b32 v[254:255], v208 offset0:18 offset1:24\n"
      "v_cndmask_b32 v152, 0, v209, vcc\n"
      "v_cmp_eq_u32 vcc, 1, v153\n"
      "s_waitcnt lgkmcnt(13)\n"
      "v_alignbyte_b32 v212, v213, v212, v154\n"
      "v_cndmask_b32 v153, 0, v209, vcc\n"
      "v_mfma_f32_16x16x32_bf16 v[236:239], v[248:251], v[180:183], v[90:93]\n"
      "s_waitcnt lgkmcnt(11)\n"
      "v_alignbyte_b32 v213, v216, v213, v154\n"
      "v_mfma_f32_16x16x32_bf16 v[236:239], v[34:37], v[38:41], v[236:239]\n"
      "v_alignbyte_b32 v210, v211, v210, v154\n"
      "v_alignbyte_b32 v211, v217, v211, v154\n"
      "v_mfma_f32_16x16x32_bf16 v[144:147], v[136:139], v[180:183], v[94:97]\n"
      "s_waitcnt lgkmcnt(8)\n"
      "v_alignbyte_b32 v240, v241, v240, v154\n"
      "v_alignbyte_b32 v241, v214, v241, v154\n"
      "v_mfma_f32_16x16x32_bf16 v[144:147], v[40:43], v[38:41], v[144:147]\n"
      "v_alignbyte_b32 v215, v215, v215, v154\n"
      "v_lshlrev_b32_e64 v215, 16, v215\n"
      "v_mfma_f32_16x16x32_bf16 v[244:247], v[140:143], v[180:183], v[72:75]\n"
      "s_waitcnt lgkmcnt(5)\n"
      "v_fmac_f32 v148, s37, v215\n"
      "v_alignbyte_b32 v190, v191, v190, v154\n"
      "v_mfma_f32_16x16x32_bf16 v[244:247], v[228:231], v[136:139], v[244:247]\n"
      "v_alignbyte_b32 v191, v242, v191, v154\n"
      "v_alignbyte_b32 v218, v219, v218, v154\n"
      "v_mfma_f32_16x16x32_bf16 v[244:247], v[156:159], v[38:41], v[244:247]\n"
      "v_alignbyte_b32 v219, v243, v219, v154\n"
      "v_alignbyte_b32 v188, v189, v188, v154\n"
      "v_mfma_f32_16x16x32_bf16 v[214:217], v[200:203], v[180:183], v[98:101]\n"
      "v_alignbyte_b32 v189, v252, v189, v154\n"
      "v_alignbyte_b32 v253, v253, v253, v154\n"
      "v_mfma_f32_16x16x32_bf16 v[214:217], v[46:49], v[38:41], v[214:217]\n"
      "v_lshlrev_b32_e64 v253, 16, v253\n"
      "v_mfma_f32_16x16x16_bf16 v[164:167], v[152:153], v[212:213], v[164:167]\n"
      "v_mfma_f32_16x16x16_bf16 v[184:187], v[152:153], v[210:211], v[184:187]\n"
      "s_nop 0\n"
      "v_mfma_f32_16x16x16_bf16 v[192:195], v[152:153], v[240:241], v[192:195]\n"
      "ds_read_b128 v[180:183], v128 offset:2048\n"
      "s_nop 4\n"
      "v_max3_f32 v155, v164, v165, v166\n"
      "v_max3_f32 v213, v167, v184, v185\n"
      "v_mfma_f32_16x16x16_bf16 v[236:239], v[152:153], v[190:191], v[236:239]\n"
      "v_max3_f32 v155, v155, v186, v187\n"
      "v_max3_f32 v213, v213, v192, v193\n"
      "v_mfma_f32_16x16x16_bf16 v[144:147], v[152:153], v[218:219], v[144:147]\n"
      "v_max3_f32 v155, v155, v194, v195\n"
      "v_max3_f32 v155, v155, v213, v148\n"
      "v_mfma_f32_16x16x16_bf16 v[214:217], v[152:153], v[188:189], v[214:217]\n"
      "v_mov_b32 v218, v155\n"
      "v_fmac_f32 v244, s37, v253\n"
      "s_nop 0\n"
      "v_permlane16_swap_b32 v155, v218\n"
      "v_max_f32 v155, v155, v218\n"
      "v_mov_b32_e64 v212, v155\n"
      "s_waitcnt lgkmcnt(1)\n"
      "v_alignbyte_b32 v206, v207, v206, v154\n"
      "v_permlane32_swap_b32 v155, v212\n"
      "v_max_f32 v155, v155, v212\n"
      "v_sub_f32 v164, v164, v155\n"
      "v_sub_f32 v165, v165, v155\n"
      "v_sub_f32 v166, v166, v155\n"
      "v_sub_f32 v167, v167, v155\n"
      "v_sub_f32 v184, v184, v155\n"
      "v_sub_f32 v185, v185, v155\n"
      "v_sub_f32 v186, v186, v155\n"
      "v_sub_f32 v187, v187, v155\n"
      "v_sub_f32 v192, v192, v155\n"
      "v_sub_f32 v193, v193, v155\n"
      "v_sub_f32 v194, v194, v155\n"
      "v_sub_f32 v195, v195, v155\n"
      "v_sub_f32 v148, v148, v155\n"
      "v_exp_f32 v164, v164\n"
      "v_exp_f32 v165, v165\n"
      "v_exp_f32 v166, v166\n"
      "v_cvt_pk_bf16_f32 v164, v164, v165\n"
      "v_exp_f32 v167, v167\n"
      "v_exp_f32 v184, v184\n"
      "v_cvt_pk_bf16_f32 v165, v166, v167\n"
      "v_exp_f32 v185, v185\n"
      "v_exp_f32 v186, v186\n"
      "v_cvt_pk_bf16_f32 v166, v184, v185\n"
      "v_exp_f32 v187, v187\n"
      "v_exp_f32 v192, v192\n"
      "v_cvt_pk_bf16_f32 v167, v186, v187\n"
      "v_exp_f32 v193, v193\n"
      "v_exp_f32 v194, v194\n"
      "v_exp_f32 v195, v195\n"
      "v_exp_f32 v148, v148\n"
      "v_cvt_pk_bf16_f32 v62, v192, v193\n"
      "v_cvt_pk_bf16_f32 v63, v194, v195\n"
      "v_cvt_pk_bf16_f32 v64, v148, 0\n"
      "v_max3_f32 v155, v236, v237, v238\n"
      "v_max3_f32 v218, v239, v144, v145\n"
      "v_max3_f32 v155, v155, v146, v147\n"
      "v_max3_f32 v218, v218, v214, v215\n"
      "v_max3_f32 v155, v155, v216, v217\n"
      "v_max3_f32 v155, v155, v218, v244\n"
      "v_mov_b32 v218, v155\n"
      "v_alignbyte_b32 v207, v232, v207, v154\n"
      "s_nop 0\n"
      "v_permlane16_swap_b32 v155, v218\n"
      "v_max_f32 v155, v155, v218\n"
      "v_mov_b32_e64 v218, v155\n"
      "v_alignbyte_b32 v204, v205, v204, v154\n"
      "v_alignbyte_b32 v205, v233, v205, v154\n"
      "v_permlane32_swap_b32 v155, v218\n"
      "v_max_f32 v155, v155, v218\n"
      "v_sub_f32 v236, v236, v155\n"
      "v_sub_f32 v237, v237, v155\n"
      "v_sub_f32 v238, v238, v155\n"
      "v_sub_f32 v239, v239, v155\n"
      "v_sub_f32 v144, v144, v155\n"
      "v_sub_f32 v145, v145, v155\n"
      "v_sub_f32 v146, v146, v155\n"
      "v_sub_f32 v147, v147, v155\n"
      "v_alignbyte_b32 v234, v235, v234, v154\n"
      "v_sub_f32 v214, v214, v155\n"
      "v_sub_f32_e64 v215, v215, v155\n"
      "v_alignbyte_b32 v235, v254, v235, v154\n"
      "v_mfma_f32_16x16x32_bf16 v[184:187], v[66:69], v[164:167], 0\n"
      "s_waitcnt lgkmcnt(0)\n"
      "v_alignbyte_b32 v255, v255, v255, v154\n"
      "v_lshlrev_b32_e64 v255, 16, v255\n"
      "v_mfma_f32_16x16x32_bf16 v[184:187], v[66:69], v[62:65], v[184:187]\n"
      "v_bitop3_b32 v180, v180, v181, s4 bitop3:0x7e\n"
      "v_bitop3_b32 v182, v182, v183, s4 bitop3:0x7e\n"
      "v_mfma_f32_16x16x32_bf16 v[188:191], v[220:223], v[164:167], 0\n"
      "v_or_b32 v180, v180, v182\n"
      "v_cmp_ne_u32 vcc, 0, v180\n"
      "v_mfma_f32_16x16x32_bf16 v[188:191], v[50:53], v[62:65], v[188:191]\n"
      "s_mov_b64 s[48:49], vcc\n"
      "v_sub_f32 v216, v216, v155\n"
      "v_sub_f32 v217, v217, v155\n"
      "v_sub_f32 v244, v244, v155\n"
      "v_exp_f32 v236, v236\n"
      "v_exp_f32 v237, v237\n"
      "v_exp_f32 v238, v238\n"
      "s_cbranch_vccnz L_flg_82_%=\n"
      "L_fb_83_%=:\n"
      "v_mfma_f32_16x16x32_bf16 v[180:183], v[224:227], v[164:167], 0\n"
      "v_cvt_pk_bf16_f32 v236, v236, v237\n"
      "v_mfma_f32_16x16x32_bf16 v[180:183], v[54:57], v[62:65], v[180:183]\n"
      "ds_read_b128 v[210:213], v119 offset:12288\n"
      "v_exp_f32 v239, v239\n"
      "v_mfma_f32_16x16x32_bf16 v[240:243], v[160:163], v[164:167], 0\n"
      "v_exp_f32 v144, v144\n"
      "v_mfma_f32_16x16x32_bf16 v[240:243], v[58:61], v[62:65], v[240:243]\n"
      "v_cvt_pk_bf16_f32 v237, v238, v239\n"
      "v_exp_f32 v145, v145\n"
      "v_mfma_f32_16x16x32_bf16 v[192:195], v[248:251], v[196:199], v[102:105]\n"
      "v_exp_f32 v146, v146\n"
      "v_mfma_f32_16x16x32_bf16 v[192:195], v[34:37], v[44:47], v[192:195]\n"
      "v_cvt_pk_bf16_f32 v238, v144, v145\n"
      "v_exp_f32 v147, v147\n"
      "v_mfma_f32_16x16x32_bf16 v[164:167], v[136:139], v[196:199], v[106:109]\n"
      "v_exp_f32 v214, v214\n"
      "v_mfma_f32_16x16x32_bf16 v[164:167], v[40:43], v[44:47], v[164:167]\n"
      "v_cvt_pk_bf16_f32 v239, v146, v147\n"
      "v_exp_f32 v215, v215\n"
      "v_mfma_f32_16x16x16_bf16 v[192:195], v[152:153], v[206:207], v[192:195]\n"
      "s_waitcnt lgkmcnt(0)\n"
      "v_exp_f32 v216, v216\n"
      "v_exp_f32 v217, v217\n"
      "v_mfma_f32_16x16x16_bf16 v[164:167], v[152:153], v[204:205], v[164:167]\n"
      "global_store_dwordx4 v116, v[210:213], s[54:55] offset:256 sc1\n"
      "v_mfma_f32_16x16x32_bf16 v[204:207], v[140:143], v[196:199], v[74:77]\n"
      "v_exp_f32_e64 v244, v244\n"
      "v_mfma_f32_16x16x32_bf16 v[204:207], v[228:231], v[200:203], v[204:207]\n"
      "v_cvt_pk_bf16_f32 v62, v214, v215\n"
      "v_cvt_pk_bf16_f32 v63, v216, v217\n"
      "v_mfma_f32_16x16x32_bf16 v[204:207], v[156:159], v[44:47], v[204:207]\n"
      "v_cvt_pk_bf16_f32 v64, v244, 0\n"
      "v_rcp_f32 v184, v184\n"
      "v_mfma_f32_16x16x32_bf16 v[136:139], v[200:203], v[196:199], v[110:113]\n"
      "v_mul_f32 v188, v184, v188\n"
      "v_mul_f32_e64 v189, v184, v189\n"
      "v_mfma_f32_16x16x32_bf16 v[136:139], v[46:49], v[44:47], v[136:139]\n"
      "v_cvt_pk_bf16_f32 v188, v188, v189\n"
      "v_mul_f32 v190, v184, v190\n"
      "v_mfma_f32_16x16x32_bf16 v[196:199], v[228:231], v[140:143], v[76:79]\n"
      "ds_read_b128 v[140:143], v120\n"
      "v_fmac_f32 v204, s37, v255\n"
      "v_mul_f32 v191, v184, v191\n"
      "v_mfma_f32_16x16x16_bf16 v[136:139], v[152:153], v[234:235], v[136:139]\n"
      "v_cvt_pk_bf16_f32 v189, v190, v191\n"
      "v_mul_f32 v180, v184, v180\n"
      "v_mfma_f32_16x16x16_bf16 v[196:199], v[158:159], v[156:157], v[196:199]\n"
      "v_mul_f32 v181, v184, v181\n"
      "v_cvt_pk_bf16_f32 v190, v180, v181\n"
      "v_mfma_f32_16x16x32_bf16 v[200:203], v[66:69], v[236:239], 0\n"
      "v_mul_f32 v182, v184, v182\n"
      "v_mul_f32_e64 v183, v184, v183\n"
      "v_mfma_f32_16x16x32_bf16 v[200:203], v[66:69], v[62:65], v[200:203]\n"
      "v_cvt_pk_bf16_f32 v191, v182, v183\n"
      "v_mul_f32 v240, v184, v240\n"
      "v_mfma_f32_16x16x32_bf16 v[216:219], v[220:223], v[236:239], 0\n"
      "v_mul_f32 v241, v184, v241\n"
      "v_cvt_pk_bf16_f32 v240, v240, v241\n"
      "v_mfma_f32_16x16x32_bf16 v[216:219], v[50:53], v[62:65], v[216:219]\n"
      "ds_write_b128 v124, v[188:191] offset:18432\n"
      "v_mfma_f32_16x16x32_bf16 v[180:183], v[224:227], v[236:239], 0\n"
      "s_waitcnt lgkmcnt(1)\n"
      "v_mul_f32 v242, v184, v242\n"
      "v_mfma_f32_16x16x32_bf16 v[180:183], v[54:57], v[62:65], v[180:183]\n"
      "global_store_dwordx4 v117, v[140:143], s[54:55] sc1\n"
      "v_mfma_f32_16x16x32_bf16 v[188:191], v[160:163], v[236:239], 0\n"
      "v_mul_f32_e64 v243, v184, v243\n"
      "v_cvt_pk_bf16_f32 v241, v242, v243\n"
      "v_mfma_f32_16x16x32_bf16 v[188:191], v[58:61], v[62:65], v[188:191]\n"
      "ds_write_b64 v125, v[240:241] offset:18432\n"
      "v_max3_f32 v203, v192, v193, v194\n"
      "v_max3_f32 v201, v195, v164, v165\n"
      "v_max3_f32 v203, v203, v166, v167\n"
      "v_max3_f32 v201, v201, v136, v137\n"
      "v_max3_f32 v203, v203, v138, v139\n"
      "v_max3_f32 v203, v203, v201, v204\n"
      "v_mov_b32 v201, v203\n"
      "s_nop 1\n"
      "v_permlane16_swap_b32 v203, v201\n"
      "v_max_f32 v203, v203, v201\n"
      "v_mov_b32 v201, v203\n"
      "v_rcp_f32 v200, v200\n"
      "s_nop 0\n"
      "v_permlane32_swap_b32 v203, v201\n"
      "v_max_f32 v203, v203, v201\n"
      "v_sub_f32 v192, v192, v203\n"
      "v_sub_f32 v193, v193, v203\n"
      "v_sub_f32 v194, v194, v203\n"
      "v_sub_f32 v195, v195, v203\n"
      "v_sub_f32 v164, v164, v203\n"
      "v_sub_f32 v165, v165, v203\n"
      "v_sub_f32 v166, v166, v203\n"
      "v_sub_f32 v167, v167, v203\n"
      "v_sub_f32 v136, v136, v203\n"
      "v_sub_f32 v137, v137, v203\n"
      "v_sub_f32 v138, v138, v203\n"
      "v_sub_f32 v139, v139, v203\n"
      "v_sub_f32 v204, v204, v203\n"
      "v_exp_f32 v192, v192\n"
      "v_exp_f32 v193, v193\n"
      "v_exp_f32 v194, v194\n"
      "v_cvt_pk_bf16_f32 v192, v192, v193\n"
      "v_exp_f32 v195, v195\n"
      "v_exp_f32 v164, v164\n"
      "v_cvt_pk_bf16_f32 v193, v194, v195\n"
      "v_exp_f32 v165, v165\n"
      "v_exp_f32 v166, v166\n"
      "v_cvt_pk_bf16_f32 v194, v164, v165\n"
      "v_exp_f32 v167, v167\n"
      "v_exp_f32 v136, v136\n"
      "v_cvt_pk_bf16_f32 v195, v166, v167\n"
      "v_exp_f32 v137, v137\n"
      "v_exp_f32 v138, v138\n"
      "v_exp_f32 v139, v139\n"
      "v_exp_f32_e64 v204, v204\n"
      "v_cvt_pk_bf16_f32 v62, v136, v137\n"
      "v_cvt_pk_bf16_f32 v63, v138, v139\n"
      "v_cvt_pk_bf16_f32 v64, v204, 0\n"
      "v_mul_f32 v216, v200, v216\n"
      "v_mul_f32 v217, v200, v217\n"
      "v_mfma_f32_16x16x32_bf16 v[136:139], v[66:69], v[192:195], 0\n"
      "ds_read_b128 v[212:215], v123 offset:256\n"
      "v_cvt_pk_bf16_f32 v216, v216, v217\n"
      "v_mfma_f32_16x16x32_bf16 v[136:139], v[66:69], v[62:65], v[136:139]\n"
      "ds_read_b128 v[248:251], v123 offset:3328\n"
      "v_mul_f32 v218, v200, v218\n"
      "v_mfma_f32_16x16x32_bf16 v[140:143], v[220:223], v[192:195], 0\n"
      "ds_read_b128 v[240:243], v121\n"
      "v_mul_f32 v219, v200, v219\n"
      "v_mfma_f32_16x16x32_bf16 v[140:143], v[50:53], v[62:65], v[140:143]\n"
      "ds_read_b128 v[228:231], v123 offset:6400\n"
      "v_cvt_pk_bf16_f32 v217, v218, v219\n"
      "v_mfma_f32_16x16x32_bf16 v[152:155], v[224:227], v[192:195], 0\n"
      "ds_read_b128 v[232:235], v123 offset:9472\n"
      "v_mul_f32 v180, v200, v180\n"
      "v_mfma_f32_16x16x32_bf16 v[152:155], v[54:57], v[62:65], v[152:155]\n"
      "v_mul_f32 v181, v200, v181\n"
      "v_cvt_pk_bf16_f32 v218, v180, v181\n"
      "v_mfma_f32_16x16x32_bf16 v[156:159], v[160:163], v[192:195], 0\n"
      "ds_read_b128 v[192:195], v123 offset:12544\n"
      "v_mul_f32 v182, v200, v182\n"
      "v_mfma_f32_16x16x32_bf16 v[156:159], v[58:61], v[62:65], v[156:159]\n"
      "v_mul_f32 v183, v200, v183\n"
      "v_cvt_pk_bf16_f32 v219, v182, v183\n"
      "v_mul_f32 v188, v200, v188\n"
      "v_mul_f32 v189, v200, v189\n"
      "v_cvt_pk_bf16_f32 v188, v188, v189\n"
      "v_mul_f32 v190, v200, v190\n"
      "v_mul_f32 v191, v200, v191\n"
      "v_cvt_pk_bf16_f32 v189, v190, v191\n"
      "s_waitcnt lgkmcnt(4)\n"
      "v_mfma_f32_16x16x32_bf16 v[180:183], a[216:219], v[212:215], 0\n"
      "ds_read_b128 v[200:203], v123 offset:15616\n"
      "v_cndmask_b32 v149, v149, v245, s[42:43]\n"
      "v_mfma_f32_16x16x32_bf16 v[180:183], a[228:231], v[248:251], v[180:183]\n"
      "ds_write_b128 v124, v[216:219] offset:18688\n"
      "s_waitcnt lgkmcnt(3)\n"
      "v_mfma_f32_16x16x32_bf16 v[184:187], a[220:223], v[212:215], 0\n"
      "v_cndmask_b32 v149, v149, v205, s[44:45]\n"
      "v_cndmask_b32 v149, v149, v197, s[46:47]\n"
      "v_mfma_f32_16x16x32_bf16 v[184:187], a[232:235], v[248:251], v[184:187]\n"
      "ds_write_b64 v125, v[188:189] offset:18688\n"
      "v_mfma_f32_16x16x32_bf16 v[188:191], a[224:227], v[212:215], 0\n"
      "ds_read_b128 v[216:219], v123 offset:512\n"
      "v_mbcnt_lo_u32_b32 v208, -1, 0\n"
      "v_mfma_f32_16x16x32_bf16 v[188:191], a[236:239], v[248:251], v[188:191]\n"
      "ds_read_b128 v[212:215], v123 offset:3584\n"
      "v_mbcnt_hi_u32_b32 v208, -1, v208\n"
      "v_mfma_f32_16x16x32_bf16 v[180:183], a[240:243], v[228:231], v[180:183]\n"
      "v_min_u32 v208, 48, v208\n"
      "v_lshlrev_b32 v208, 1, v208\n"
      "v_mfma_f32_16x16x32_bf16 v[180:183], a[252:255], v[232:235], v[180:183]\n"
      "s_waitcnt lgkmcnt(4)\n"
      "v_add_u32 v208, s30, v208\n"
      "v_mfma_f32_16x16x32_bf16 v[184:187], a[244:247], v[228:231], v[184:187]\n"
      "ds_read_u16 v211, v208 offset:4704\n"
      "v_mfma_f32_16x16x32_bf16 v[184:187], v[0:3], v[232:235], v[184:187]\n"
      "ds_read_b128 v[206:209], v123 offset:6656\n"
      "v_mfma_f32_16x16x32_bf16 v[188:191], a[248:251], v[228:231], v[188:191]\n"
      "ds_read_b128 v[144:147], v123 offset:9728\n"
      "v_mfma_f32_16x16x32_bf16 v[188:191], v[4:7], v[232:235], v[188:191]\n"
      "s_mov_b32 exec_hi, 0\n"
      "global_store_dwordx4 v118, v[240:243], s[54:55] sc1\n"
      "s_mov_b32 exec_hi, -1\n"
      "v_mfma_f32_16x16x32_bf16 v[180:183], v[8:11], v[192:195], v[180:183]\n"
      "s_add_u32 s28, s27, 2\n"
      "s_min_u32 m0, s28, s3\n"
      "v_mfma_f32_16x16x32_bf16 v[180:183], v[20:23], v[200:203], v[180:183]\n"
      "s_waitcnt lgkmcnt(3)\n"
      "s_movrels_b32 s9, s80\n"
      "v_mfma_f32_16x16x32_bf16 v[184:187], v[12:15], v[192:195], v[184:187]\n"
      "s_lshr_b32 s8, s9, 8\n"
      "s_and_b32 s9, s9, 0xff\n"
      "s_mul_i32 s10, s8, 73\n"
      "v_mfma_f32_16x16x32_bf16 v[184:187], v[24:27], v[200:203], v[184:187]\n"
      "s_add_u32 s10, s10, s9\n"
      "s_mul_i32 s28, s10, 4802\n"
      "v_mfma_f32_16x16x32_bf16 v[188:191], v[16:19], v[192:195], v[188:191]\n"
      "s_waitcnt lgkmcnt(2)\n"
      "v_lshlrev_b32 v211, 16, v211\n"
      "v_fmac_f32_e64 v149, s37, v211\n"
      "v_mfma_f32_16x16x32_bf16 v[188:191], v[28:31], v[200:203], v[188:191]\n"
      "ds_read_b128 v[240:243], v123 offset:12800\n"
      "v_max_f32_dpp v211, v149, v149 row_ror:8 row_mask:0xf bank_mask:0xf\n"
      "v_mfma_f32_16x16x32_bf16 v[192:195], a[216:219], v[216:219], 0\n"
      "ds_read_b128 v[236:239], v123 offset:15872\n"
      "v_max_f32_dpp v211, v211, v211 row_ror:4 row_mask:0xf bank_mask:0xf\n"
      "v_mfma_f32_16x16x32_bf16 v[192:195], a[228:231], v[212:215], v[192:195]\n"
      "s_waitcnt lgkmcnt(2)\n"
      "v_max_f32_dpp v211, v211, v211 row_ror:2 row_mask:0xf bank_mask:0xf\n"
      "v_fma_f32 v150, v180, v180, s39\n"
      "v_mfma_f32_16x16x32_bf16 v[196:199], a[220:223], v[216:219], 0\n"
      "s_add_u32 s56, s18, s28\n"
      "v_max_f32_dpp v211, v211, v211 row_ror:1 row_mask:0xf bank_mask:0xf\n"
      "v_fmac_f32 v150, v181, v181\n"
      "v_mfma_f32_16x16x32_bf16 v[196:199], a[232:235], v[212:215], v[196:199]\n"
      "s_addc_u32 s57, s19, 0\n"
      "v_max_f32_dpp v211, v211, v211 row_bcast:15 row_mask:0xa bank_mask:0xf\n"
      "v_fmac_f32_e64 v150, v182, v182\n"
      "v_mfma_f32_16x16x32_bf16 v[200:203], a[224:227], v[216:219], 0\n"
      "s_and_b32 s56, s56, -16\n"
      "v_max_f32_dpp v211, v211, v211 row_bcast:31 row_mask:0xc bank_mask:0xf\n"
      "v_fmac_f32 v150, v183, v183\n"
      "v_mfma_f32_16x16x32_bf16 v[200:203], a[236:239], v[212:215], v[200:203]\n"
      "s_sub_u32 s58, s56, 110592\n"
      "v_readlane_b32 s31, v211, 63\n"
      "v_fmac_f32_e64 v150, v184, v184\n"
      "v_mfma_f32_16x16x32_bf16 v[192:195], a[240:243], v[206:209], v[192:195]\n"
      "s_subb_u32 s59, s57, 0\n"
      "v_subrev_f32 v149, s31, v149\n"
      "v_fmac_f32_e64 v150, v185, v185\n"
      "v_mfma_f32_16x16x32_bf16 v[192:195], a[252:255], v[144:147], v[192:195]\n"
      "s_sub_u32 s56, s56, 73728\n"
      "v_exp_f32 v149, v149\n"
      "v_mfma_f32_16x16x32_bf16 v[196:199], a[244:247], v[206:209], v[196:199]\n"
      "s_waitcnt lgkmcnt(0)\n"
      "v_cvt_pk_bf16_f32 v149, v149, v149\n"
      "v_mfma_f32_16x16x32_bf16 v[196:199], v[0:3], v[144:147], v[196:199]\n"
      "ds_write_b16 v130, v149\n"
      "v_mfma_f32_16x16x32_bf16 v[200:203], a[248:251], v[206:209], v[200:203]\n"
      "ds_read_b128 v[212:215], v131\n"
      "v_fmac_f32_e64 v150, v186, v186\n"
      "v_mfma_f32_16x16x32_bf16 v[200:203], v[4:7], v[144:147], v[200:203]\n"
      "ds_read_b128 v[244:247], v131 offset:64\n"
      "v_fmac_f32 v150, v187, v187\n"
      "v_mfma_f32_16x16x32_bf16 v[192:195], v[8:11], v[240:243], v[192:195]\n"
      "s_subb_u32 s57, s57, 0\n"
      "v_fmac_f32 v150, v188, v188\n"
      "v_fmac_f32 v150, v189, v189\n"
      "v_mfma_f32_16x16x32_bf16 v[192:195], v[20:23], v[236:239], v[192:195]\n"
      "s_mul_i32 s28, s8, 1373568\n"
      "v_fmac_f32 v150, v190, v190\n"
      "v_fmac_f32 v150, v191, v191\n"
      "v_mfma_f32_16x16x32_bf16 v[196:199], v[12:15], v[240:243], v[196:199]\n"
      "s_mul_i32 s29, s9, 2688\n"
      "v_permlane16_swap_b32_e64 v151, v150\n"
      "v_mfma_f32_16x16x32_bf16 v[196:199], v[24:27], v[236:239], v[196:199]\n"
      "ds_read_b128 v[208:211], v114\n"
      "v_fma_f32 v233, v192, v192, s39\n"
      "v_mfma_f32_16x16x32_bf16 v[200:203], v[16:19], v[240:243], v[200:203]\n"
      "ds_read_b128 v[216:219], v115\n"
      "v_fmac_f32 v233, v193, v193\n"
      "v_mfma_f32_16x16x32_bf16 v[200:203], v[28:31], v[236:239], v[200:203]\n"
      "s_add_u32 s28, s28, s29\n"
      "v_fmac_f32 v233, v194, v194\n"
      "v_fmac_f32 v233, v195, v195\n"
      "v_fmac_f32 v233, v196, v196\n"
      "v_fmac_f32 v233, v197, v197\n"
      "v_fmac_f32 v233, v198, v198\n"
      "v_fmac_f32 v233, v199, v199\n"
      "v_add_f32 v151, v151, v150\n"
      "v_fmac_f32_e64 v233, v200, v200\n"
      "s_waitcnt lgkmcnt(2)\n"
      "v_mfma_f32_16x16x32_bf16 v[204:207], v[212:215], v[66:69], 0\n"
      "s_cmp_eq_u32 s8, 72\n"
      "v_fmac_f32 v233, v201, v201\n"
      "v_fmac_f32 v233, v202, v202\n"
      "v_mfma_f32_16x16x32_bf16 v[204:207], v[244:247], v[66:69], v[204:207]\n"
      "s_cselect_b32 s8, 0, 100270464\n"
      "v_fmac_f32 v233, v203, v203\n"
      "v_mov_b32 v241, v233\n"
      "v_mfma_f32_16x16x32_bf16 v[236:239], v[212:215], v[220:223], 0\n"
      "s_cmp_eq_u32 s9, 72\n"
      "v_permlane16_swap_b32_e64 v233, v241\n"
      "v_mfma_f32_16x16x32_bf16 v[236:239], v[244:247], v[50:53], v[236:239]\n"
      "s_cselect_b32 s9, 0, 196224\n"
      "v_add_f32_e64 v233, v233, v241\n"
      "v_mov_b32_dpp v204, v204 quad_perm:[0,0,0,0] row_mask:0xf bank_mask:0xf\n"
      "v_mfma_f32_16x16x32_bf16 v[240:243], v[212:215], v[224:227], 0\n"
      "s_add_u32 s10, s8, s9\n"
      "v_permlane32_swap_b32 v151, v233\n"
      "v_mfma_f32_16x16x32_bf16 v[240:243], v[244:247], v[54:57], v[240:243]\n"
      "s_waitcnt lgkmcnt(0)\n"
      "v_add_f32 v151, v151, v233\n"
      "v_rcp_f32 v204, v204\n"
      "v_mfma_f32_16x16x32_bf16 v[144:147], v[212:215], v[160:163], 0\n"
      "s_add_u32 s28, s28, s10\n"
      "s_add_u32 s52, s16, s28\n"
      "s_addc_u32 s53, s17, 0\n"
      "v_mfma_f32_16x16x32_bf16 v[144:147], v[244:247], v[58:61], v[144:147]\n"
      "ds_write_b32 v129, v151 offset:1024\n"
      "v_mfma_f32_16x16x32_bf16 v[160:163], a[0:3], v[208:211], 0\n"
      "s_add_u32 s27, s27, 1\n"
      "v_cndmask_b32 v236, v236, v240, s[42:43]\n"
      "s_cmp_ge_u32 s27, s26\n"
      "v_mfma_f32_16x16x32_bf16 v[160:163], a[36:39], v[216:219], v[160:163]\n"
      "s_cselect_b32 s28, 1, 0\n"
      "s_nop 0\n"
      "v_cndmask_b32 v236, v236, v144, s[44:45]\n"
      "v_mfma_f32_16x16x32_bf16 v[212:215], a[4:7], v[208:211], 0\n"
      "s_xor_b32 s29, s10, s11\n"
      "v_mul_f32 v236, v204, v236\n"
      "v_cvt_pk_bf16_f32 v236, v236, v236\n"
      "v_mfma_f32_16x16x32_bf16 v[212:215], a[40:43], v[216:219], v[212:215]\n"
      "ds_write_b16 v135, v236 offset:16\n"
      "s_or_b32 s28, s28, s29\n"
      "s_branch L_join_21_%=\n"
      "L_flg_82_%=:\n"
      "v_cmp_gt_i32 vcc, 0, v180\n"
      "s_cbranch_vccz L_fb_83_%=\n"
      "s_sleep 1\n"
      "ds_read_b128 v[180:183], v128 offset:2048\n"
      "s_waitcnt lgkmcnt(0)\n"
      "v_bitop3_b32 v180, v180, v181, s4 bitop3:0x7e\n"
      "v_bitop3_b32 v182, v182, v183, s4 bitop3:0x7e\n"
      "v_or_b32 v180, v180, v182\n"
      "v_cmp_ne_u32 s[48:49], 0, v180\n"
      "s_branch L_flg_82_%=\n"
      "L_gen_28_%=:\n"
      "s_waitcnt lgkmcnt(0)\n"
      "s_nop 7\n"
      "s_nop 7\n"
      "s_cmp_eq_u64 s[48:49], 0\n"
      "s_cbranch_scc1 L_go_89_%=\n"
      "s_mov_b32 m0, s27\n"
      "s_nop 0\n"
      "s_movrels_b32 s9, s80\n"
      "s_lshr_b32 s8, s9, 8\n"
      "s_and_b32 s9, s9, 0xff\n"
      "s_mul_i32 s10, s8, 73\n"
      "s_add_u32 s10, s10, s9\n"
      "s_mul_i32 s30, s10, 4802\n"
      "s_add_u32 s30, s30, s18\n"
      "s_and_b32 s29, s30, 15\n"
      "s_add_u32 s30, s29, 110592\n"
      "s_lshl_b64 s[34:35], s[48:49], 4\n"
      "s_lshr_b64 s[34:35], s[34:35], 5\n"
      "s_cbranch_scc1 L_gen_29_%=\n"
      "v_mbcnt_lo_u32_b32 v160, -1, 0\n"
      "v_mbcnt_hi_u32_b32 v160, -1, v160\n"
      "v_lshlrev_b32 v161, 1, v160\n"
      "v_add_u32 v162, 4802, v161\n"
      "v_cmp_gt_u32 vcc, s29, v161\n"
      "s_nop 1\n"
      "v_cndmask_b32 v161, v162, v161, vcc\n"
      "v_add_u32 v161, 110592, v161\n"
      "v_mov_b32 v162, 0\n"
      "s_mov_b64 exec, 0x7f\n"
      "ds_write_b16 v161, v162\n"
      "s_mov_b64 exec, -1\n"
      "v_lshlrev_b32 v161, 4, v160\n"
      "v_add_u32 v161, 110592, v161\n"
      "v_min_u32 v160, 111296, v161\n"
      "ds_read_b128 v[164:167], v161\n"
      "ds_read_b128 v[188:191], v161 offset:1024\n"
      "ds_read_b128 v[192:195], v161 offset:2048\n"
      "ds_read_b128 v[200:203], v161 offset:3072\n"
      "ds_read_b128 v[148:151], v160 offset:4096\n"
      "s_waitcnt lgkmcnt(4)\n"
      "v_or3_b32 v164, v164, v165, v166\n"
      "s_waitcnt lgkmcnt(3)\n"
      "v_or3_b32 v164, v164, v167, v188\n"
      "v_or3_b32 v164, v164, v189, v190\n"
      "s_waitcnt lgkmcnt(2)\n"
      "v_or3_b32 v164, v164, v191, v192\n"
      "v_or3_b32 v164, v164, v193, v194\n"
      "s_waitcnt lgkmcnt(1)\n"
      "v_or3_b32 v164, v164, v195, v200\n"
      "v_or3_b32 v164, v164, v201, v202\n"
      "s_waitcnt lgkmcnt(0)\n"
      "v_or3_b32 v164, v164, v203, v148\n"
      "v_or3_b32 v164, v164, v149, v150\n"
      "v_bitop3_b32 v164, v164, v151, s40 bitop3:0xa8\n"
      "v_cmp_ne_u32 s[48:49], 0, v164\n"
      "s_cmp_lg_u64 s[48:49], 0\n"
      "s_cbranch_scc1 L_gen_29_%=\n"
      "s_cmp_eq_u64 s[50:51], 0\n"
      "s_cbranch_scc1 L_fast_31_%=\n"
      "L_go_89_%=:\n"
      "v_mfma_f32_16x16x32_bf16 v[164:167], v[248:251], v[180:183], v[90:93]\n"
      "ds_read_b128 v[160:163], v128 offset:3072\n"
      "v_max3_f32 v191, v144, v145, v146\n"
      "v_mfma_f32_16x16x32_bf16 v[164:167], v[34:37], v[38:41], v[164:167]\n"
      "s_nop 0\n"
      "v_max3_f32 v189, v147, v228, v229\n"
      "v_max3_f32 v191, v191, v230, v231\n"
      "v_mfma_f32_16x16x32_bf16 v[192:195], v[140:143], v[180:183], v[94:97]\n"
      "v_max3_f32 v189, v189, v184, v185\n"
      "v_max3_f32 v191, v191, v186, v187\n"
      "v_mfma_f32_16x16x32_bf16 v[192:195], v[40:43], v[38:41], v[192:195]\n"
      "v_max3_f32 v191, v191, v189, v212\n"
      "v_mov_b32_e64 v189, v191\n"
      "v_mfma_f32_16x16x32_bf16 v[200:203], v[136:139], v[180:183], v[72:75]\n"
      "s_nop 0\n"
      "v_permlane16_swap_b32 v191, v189\n"
      "v_mfma_f32_16x16x32_bf16 v[200:203], v[216:219], v[140:143], v[200:203]\n"
      "v_max_f32 v191, v191, v189\n"
      "v_mov_b32 v189, v191\n"
      "v_mfma_f32_16x16x32_bf16 v[200:203], v[152:155], v[38:41], v[200:203]\n"
      "s_nop 0\n"
      "v_permlane32_swap_b32 v191, v189\n"
      "v_mfma_f32_16x16x32_bf16 v[148:151], v[156:159], v[180:183], v[98:101]\n"
      "v_max_f32 v191, v191, v189\n"
      "v_sub_f32 v144, v144, v191\n"
      "v_mfma_f32_16x16x32_bf16 v[148:151], v[46:49], v[38:41], v[148:151]\n"
      "s_waitcnt lgkmcnt(0)\n"
      "v_sub_f32 v145, v145, v191\n"
      "v_sub_f32 v146, v146, v191\n"
      "v_sub_f32 v147, v147, v191\n"
      "v_sub_f32 v228, v228, v191\n"
      "v_sub_f32 v229, v229, v191\n"
      "v_sub_f32 v230, v230, v191\n"
      "v_sub_f32 v231, v231, v191\n"
      "v_sub_f32 v184, v184, v191\n"
      "v_sub_f32 v185, v185, v191\n"
      "v_sub_f32 v186, v186, v191\n"
      "v_sub_f32 v187, v187, v191\n"
      "v_sub_f32 v212, v212, v191\n"
      "v_exp_f32 v144, v144\n"
      "v_exp_f32 v145, v145\n"
      "v_exp_f32 v146, v146\n"
      "v_cvt_pk_bf16_f32 v144, v144, v145\n"
      "v_exp_f32 v147, v147\n"
      "v_exp_f32 v228, v228\n"
      "v_cvt_pk_bf16_f32 v145, v146, v147\n"
      "v_exp_f32 v229, v229\n"
      "v_exp_f32 v230, v230\n"
      "v_cvt_pk_bf16_f32 v146, v228, v229\n"
      "v_exp_f32 v231, v231\n"
      "v_exp_f32 v184, v184\n"
      "v_cvt_pk_bf16_f32 v147, v230, v231\n"
      "v_exp_f32 v185, v185\n"
      "v_exp_f32 v186, v186\n"
      "v_exp_f32_e64 v187, v187\n"
      "v_mfma_f32_16x16x32_bf16 v[180:183], v[248:251], v[196:199], v[102:105]\n"
      "v_exp_f32_e64 v212, v212\n"
      "v_mfma_f32_16x16x32_bf16 v[180:183], v[34:37], v[44:47], v[180:183]\n"
      "v_cvt_pk_bf16_f32 v62, v184, v185\n"
      "v_cvt_pk_bf16_f32 v63, v186, v187\n"
      "v_cvt_pk_bf16_f32 v64, v212, 0\n"
      "v_bitop3_b32 v160, v160, v161, s4 bitop3:0x7e\n"
      "v_bitop3_b32 v162, v162, v163, s4 bitop3:0x7e\n"
      "v_mfma_f32_16x16x32_bf16 v[184:187], v[66:69], v[144:147], 0\n"
      "ds_read_b128 v[188:191], v119 offset:30720\n"
      "v_or_b32 v160, v160, v162\n"
      "v_mfma_f32_16x16x32_bf16 v[184:187], v[66:69], v[62:65], v[184:187]\n"
      "v_cmp_ne_u32 vcc, 0, v160\n"
      "v_max3_f32 v202, v164, v165, v166\n"
      "v_mfma_f32_16x16x32_bf16 v[248:251], v[236:239], v[144:147], 0\n"
      "s_mov_b64 s[48:49], vcc\n"
      "v_max3_f32 v203, v167, v192, v193\n"
      "v_max3_f32 v202, v202, v194, v195\n"
      "v_mfma_f32_16x16x32_bf16 v[248:251], v[50:53], v[62:65], v[248:251]\n"
      "s_cbranch_vccnz L_flg_90_%=\n"
      "L_fb_91_%=:\n"
      "v_max3_f32 v203, v203, v148, v149\n"
      "v_mfma_f32_16x16x32_bf16 v[160:163], v[244:247], v[144:147], 0\n"
      "v_max3_f32 v202, v202, v150, v151\n"
      "v_max3_f32 v202, v202, v203, v200\n"
      "v_mfma_f32_16x16x32_bf16 v[160:163], v[54:57], v[62:65], v[160:163]\n"
      "v_mov_b32 v203, v202\n"
      "v_mfma_f32_16x16x32_bf16 v[228:231], v[240:243], v[144:147], 0\n"
      "s_nop 0\n"
      "v_permlane16_swap_b32_e64 v202, v203\n"
      "v_mfma_f32_16x16x32_bf16 v[228:231], v[58:61], v[62:65], v[228:231]\n"
      "v_max_f32 v202, v202, v203\n"
      "v_mov_b32 v203, v202\n"
      "v_mfma_f32_16x16x32_bf16 v[144:147], v[140:143], v[196:199], v[106:109]\n"
      "s_nop 0\n"
      "v_permlane32_swap_b32 v202, v203\n"
      "v_mfma_f32_16x16x32_bf16 v[144:147], v[40:43], v[44:47], v[144:147]\n"
      "v_max_f32 v202, v202, v203\n"
      "v_sub_f32 v164, v164, v202\n"
      "v_sub_f32 v165, v165, v202\n"
      "v_sub_f32 v166, v166, v202\n"
      "v_sub_f32 v167, v167, v202\n"
      "v_sub_f32 v192, v192, v202\n"
      "v_sub_f32 v193, v193, v202\n"
      "v_sub_f32 v194, v194, v202\n"
      "v_sub_f32 v195, v195, v202\n"
      "v_sub_f32 v148, v148, v202\n"
      "v_sub_f32 v149, v149, v202\n"
      "v_sub_f32 v150, v150, v202\n"
      "v_sub_f32 v151, v151, v202\n"
      "v_sub_f32 v200, v200, v202\n"
      "v_exp_f32 v164, v164\n"
      "v_exp_f32 v165, v165\n"
      "v_exp_f32 v166, v166\n"
      "v_cvt_pk_bf16_f32 v164, v164, v165\n"
      "v_exp_f32 v167, v167\n"
      "v_mfma_f32_16x16x32_bf16 v[140:143], v[136:139], v[196:199], v[74:77]\n"
      "s_waitcnt lgkmcnt(0)\n"
      "v_exp_f32 v192, v192\n"
      "v_mfma_f32_16x16x32_bf16 v[140:143], v[216:219], v[156:159], v[140:143]\n"
      "global_store_dwordx4 v116, v[188:191], s[54:55] offset:256 sc1\n"
      "v_mfma_f32_16x16x32_bf16 v[140:143], v[152:155], v[44:47], v[140:143]\n"
      "v_cvt_pk_bf16_f32 v165, v166, v167\n"
      "v_exp_f32 v193, v193\n"
      "v_mfma_f32_16x16x32_bf16 v[188:191], v[156:159], v[196:199], v[110:113]\n"
      "v_exp_f32 v194, v194\n"
      "v_mfma_f32_16x16x32_bf16 v[188:191], v[46:49], v[44:47], v[188:191]\n"
      "v_cvt_pk_bf16_f32 v166, v192, v193\n"
      "v_exp_f32 v195, v195\n"
      "v_exp_f32 v148, v148\n"
      "v_cvt_pk_bf16_f32 v167, v194, v195\n"
      "v_mfma_f32_16x16x32_bf16 v[192:195], v[216:219], v[136:139], v[76:79]\n"
      "v_exp_f32 v149, v149\n"
      "v_exp_f32 v150, v150\n"
      "v_exp_f32 v151, v151\n"
      "v_exp_f32 v200, v200\n"
      "v_cvt_pk_bf16_f32 v62, v148, v149\n"
      "v_cvt_pk_bf16_f32 v63, v150, v151\n"
      "v_mfma_f32_16x16x16_bf16 v[192:195], v[154:155], v[152:153], v[192:195]\n"
      "v_cvt_pk_bf16_f32 v64, v200, 0\n"
      "v_rcp_f32_e64 v184, v184\n"
      "v_max3_f32 v142, v180, v181, v182\n"
      "v_mfma_f32_16x16x32_bf16 v[196:199], v[66:69], v[164:167], 0\n"
      "ds_read_b128 v[156:159], v120 offset:18432\n"
      "v_mul_f32 v248, v184, v248\n"
      "v_mfma_f32_16x16x32_bf16 v[196:199], v[66:69], v[62:65], v[196:199]\n"
      "v_mul_f32 v249, v184, v249\n"
      "v_cvt_pk_bf16_f32 v248, v248, v249\n"
      "v_mfma_f32_16x16x32_bf16 v[136:139], v[236:239], v[164:167], 0\n"
      "v_mul_f32 v250, v184, v250\n"
      "v_mul_f32 v251, v184, v251\n"
      "v_mfma_f32_16x16x32_bf16 v[136:139], v[50:53], v[62:65], v[136:139]\n"
      "v_cvt_pk_bf16_f32 v249, v250, v251\n"
      "v_mul_f32 v160, v184, v160\n"
      "v_mul_f32 v161, v184, v161\n"
      "v_cvt_pk_bf16_f32 v250, v160, v161\n"
      "v_mfma_f32_16x16x32_bf16 v[148:151], v[244:247], v[164:167], 0\n"
      "v_mul_f32 v162, v184, v162\n"
      "v_mul_f32 v163, v184, v163\n"
      "v_mfma_f32_16x16x32_bf16 v[148:151], v[54:57], v[62:65], v[148:151]\n"
      "v_cvt_pk_bf16_f32 v251, v162, v163\n"
      "v_mul_f32 v228, v184, v228\n"
      "v_mul_f32 v229, v184, v229\n"
      "v_cvt_pk_bf16_f32 v228, v228, v229\n"
      "v_mul_f32 v230, v184, v230\n"
      "v_mul_f32 v231, v184, v231\n"
      "v_mfma_f32_16x16x32_bf16 v[202:205], v[240:243], v[164:167], 0\n"
      "v_cvt_pk_bf16_f32 v229, v230, v231\n"
      "v_max3_f32 v200, v183, v144, v145\n"
      "v_mfma_f32_16x16x32_bf16 v[202:205], v[58:61], v[62:65], v[202:205]\n"
      "ds_write_b128 v124, v[248:251]\n"
      "v_max3_f32 v142, v142, v146, v147\n"
      "v_max3_f32 v200, v200, v188, v189\n"
      "v_max3_f32 v142, v142, v190, v191\n"
      "v_max3_f32 v142, v142, v200, v140\n"
      "v_mov_b32 v200, v142\n"
      "s_waitcnt lgkmcnt(1)\n"
      "s_nop 0\n"
      "v_permlane16_swap_b32 v142, v200\n"
      "v_max_f32 v142, v142, v200\n"
      "v_mov_b32 v200, v142\n"
      "s_nop 1\n"
      "v_permlane32_swap_b32 v142, v200\n"
      "v_max_f32 v142, v142, v200\n"
      "v_sub_f32 v180, v180, v142\n"
      "v_sub_f32 v181, v181, v142\n"
      "v_sub_f32 v182, v182, v142\n"
      "v_sub_f32 v183, v183, v142\n"
      "v_sub_f32 v144, v144, v142\n"
      "v_sub_f32 v145, v145, v142\n"
      "v_sub_f32 v146, v146, v142\n"
      "v_sub_f32 v147, v147, v142\n"
      "v_sub_f32 v188, v188, v142\n"
      "v_sub_f32 v189, v189, v142\n"
      "v_sub_f32 v190, v190, v142\n"
      "v_sub_f32 v191, v191, v142\n"
      "v_sub_f32 v140, v140, v142\n"
      "v_exp_f32 v180, v180\n"
      "v_exp_f32 v181, v181\n"
      "v_exp_f32_e64 v182, v182\n"
      "v_cvt_pk_bf16_f32 v180, v180, v181\n"
      "v_exp_f32 v183, v183\n"
      "v_exp_f32 v144, v144\n"
      "v_cvt_pk_bf16_f32 v181, v182, v183\n"
      "v_exp_f32 v145, v145\n"
      "v_exp_f32 v146, v146\n"
      "v_cvt_pk_bf16_f32 v182, v144, v145\n"
      "v_exp_f32 v147, v147\n"
      "v_exp_f32 v188, v188\n"
      "ds_write_b64 v125, v[228:229]\n"
      "v_cvt_pk_bf16_f32 v183, v146, v147\n"
      "v_exp_f32 v189, v189\n"
      "v_exp_f32 v190, v190\n"
      "v_exp_f32 v191, v191\n"
      "v_exp_f32 v140, v140\n"
      "v_cvt_pk_bf16_f32 v62, v188, v189\n"
      "v_cvt_pk_bf16_f32 v63, v190, v191\n"
      "v_cvt_pk_bf16_f32 v64, v140, 0\n"
      "v_rcp_f32_e64 v196, v196\n"
      "v_mfma_f32_16x16x32_bf16 v[152:155], v[66:69], v[180:183], 0\n"
      "global_store_dwordx4 v117, v[156:159], s[54:55] sc1\n"
      "v_mfma_f32_16x16x32_bf16 v[152:155], v[66:69], v[62:65], v[152:155]\n"
      "ds_read_b128 v[144:147], v123 offset:18688\n"
      "v_mul_f32_e64 v136, v196, v136\n"
      "v_mfma_f32_16x16x32_bf16 v[156:159], v[236:239], v[180:183], 0\n"
      "ds_read_b128 v[228:231], v123 offset:21760\n"
      "v_mul_f32_e64 v137, v196, v137\n"
      "v_mfma_f32_16x16x32_bf16 v[156:159], v[50:53], v[62:65], v[156:159]\n"
      "ds_read_b128 v[248:251], v123 offset:24832\n"
      "v_cvt_pk_bf16_f32 v136, v136, v137\n"
      "v_mfma_f32_16x16x32_bf16 v[160:163], v[244:247], v[180:183], 0\n"
      "ds_read_b128 v[216:219], v121 offset:18432\n"
      "v_mul_f32_e64 v138, v196, v138\n"
      "v_mfma_f32_16x16x32_bf16 v[160:163], v[54:57], v[62:65], v[160:163]\n"
      "ds_read_b128 v[208:211], v123 offset:27904\n"
      "v_mul_f32 v139, v196, v139\n"
      "v_cvt_pk_bf16_f32 v137, v138, v139\n"
      "v_mul_f32 v148, v196, v148\n"
      "v_mul_f32_e64 v149, v196, v149\n"
      "v_cvt_pk_bf16_f32 v138, v148, v149\n"
      "v_mfma_f32_16x16x32_bf16 v[164:167], v[240:243], v[180:183], 0\n"
      "ds_read_b128 v[220:223], v123 offset:30976\n"
      "v_mul_f32_e64 v150, v196, v150\n"
      "v_mfma_f32_16x16x32_bf16 v[164:167], v[58:61], v[62:65], v[164:167]\n"
      "v_mul_f32_e64 v151, v196, v151\n"
      "v_cvt_pk_bf16_f32 v139, v150, v151\n"
      "v_mul_f32_e64 v202, v196, v202\n"
      "v_mul_f32_e64 v203, v196, v203\n"
      "v_cvt_pk_bf16_f32 v202, v202, v203\n"
      "v_mul_f32_e64 v204, v196, v204\n"
      "v_mul_f32_e64 v205, v196, v205\n"
      "v_cvt_pk_bf16_f32 v203, v204, v205\n"
      "s_waitcnt lgkmcnt(4)\n"
      "v_mfma_f32_16x16x32_bf16 v[180:183], a[216:219], v[144:147], 0\n"
      "ds_read_b128 v[196:199], v123 offset:34048\n"
      "v_cndmask_b32 v213, v213, v201, s[42:43]\n"
      "v_mfma_f32_16x16x32_bf16 v[180:183], a[228:231], v[228:231], v[180:183]\n"
      "ds_write_b128 v124, v[136:139] offset:256\n"
      "s_waitcnt lgkmcnt(3)\n"
      "v_mfma_f32_16x16x32_bf16 v[184:187], a[220:223], v[144:147], 0\n"
      "ds_read_b128 v[136:139], v123 offset:18944\n"
      "v_cndmask_b32 v213, v213, v141, s[44:45]\n"
      "v_mfma_f32_16x16x32_bf16 v[184:187], a[232:235], v[228:231], v[184:187]\n"
      "ds_write_b64 v125, v[202:203] offset:256\n"
      "v_mfma_f32_16x16x32_bf16 v[188:191], a[224:227], v[144:147], 0\n"
      "ds_read_b128 v[140:143], v123 offset:22016\n"
      "v_cndmask_b32 v213, v213, v193, s[46:47]\n"
      "v_mfma_f32_16x16x32_bf16 v[188:191], a[236:239], v[228:231], v[188:191]\n"
      "ds_read_b128 v[144:147], v123 offset:25088\n"
      "v_max_f32_dpp v193, v213, v213 row_ror:8 row_mask:0xf bank_mask:0xf\n"
      "v_mfma_f32_16x16x32_bf16 v[180:183], a[240:243], v[248:251], v[180:183]\n"
      "s_nop 0\n"
      "v_max_f32_dpp v193, v193, v193 row_ror:4 row_mask:0xf bank_mask:0xf\n"
      "v_mfma_f32_16x16x32_bf16 v[180:183], a[252:255], v[208:211], v[180:183]\n"
      "s_waitcnt lgkmcnt(5)\n"
      "v_max_f32_dpp v193, v193, v193 row_ror:2 row_mask:0xf bank_mask:0xf\n"
      "v_mfma_f32_16x16x32_bf16 v[184:187], a[244:247], v[248:251], v[184:187]\n"
      "s_nop 0\n"
      "v_max_f32_dpp v193, v193, v193 row_ror:1 row_mask:0xf bank_mask:0xf\n"
      "v_mfma_f32_16x16x32_bf16 v[184:187], v[0:3], v[208:211], v[184:187]\n"
      "s_mov_b32 exec_hi, 0\n"
      "global_store_dwordx4 v118, v[216:219], s[54:55] sc1\n"
      "s_mov_b32 exec_hi, -1\n"
      "v_mfma_f32_16x16x32_bf16 v[188:191], a[248:251], v[248:251], v[188:191]\n"
      "ds_read_b128 v[148:151], v123 offset:28160\n"
      "v_max_f32_dpp v193, v193, v193 row_bcast:15 row_mask:0xa bank_mask:0xf\n"
      "v_mfma_f32_16x16x32_bf16 v[188:191], v[4:7], v[208:211], v[188:191]\n"
      "s_add_u32 s28, s27, 2\n"
      "v_max_f32_dpp v193, v193, v193 row_bcast:31 row_mask:0xc bank_mask:0xf\n"
      "v_mfma_f32_16x16x32_bf16 v[180:183], v[8:11], v[220:223], v[180:183]\n"
      "s_waitcnt lgkmcnt(2)\n"
      "v_readlane_b32 s31, v193, 63\n"
      "s_min_u32 m0, s28, s3\n"
      "v_mfma_f32_16x16x32_bf16 v[180:183], v[20:23], v[196:199], v[180:183]\n"
      "s_movrels_b32 s9, s80\n"
      "v_subrev_f32 v213, s31, v213\n"
      "v_exp_f32 v213, v213\n"
      "v_mfma_f32_16x16x32_bf16 v[184:187], v[12:15], v[220:223], v[184:187]\n"
      "s_lshr_b32 s8, s9, 8\n"
      "v_cvt_pk_bf16_f32 v213, v213, v213\n"
      "s_and_b32 s9, s9, 0xff\n"
      "v_mfma_f32_16x16x32_bf16 v[184:187], v[24:27], v[196:199], v[184:187]\n"
      "ds_write_b16 v130, v213\n"
      "v_mfma_f32_16x16x32_bf16 v[188:191], v[16:19], v[220:223], v[188:191]\n"
      "ds_read_b128 v[220:223], v131\n"
      "v_fma_f32 v213, v180, v180, s39\n"
      "v_mfma_f32_16x16x32_bf16 v[188:191], v[28:31], v[196:199], v[188:191]\n"
      "ds_read_b128 v[208:211], v131 offset:64\n"
      "v_fmac_f32_e64 v213, v181, v181\n"
      "v_mfma_f32_16x16x32_bf16 v[192:195], a[216:219], v[136:139], 0\n"
      "s_waitcnt lgkmcnt(3)\n"
      "v_fmac_f32 v213, v182, v182\n"
      "v_fmac_f32_e64 v213, v183, v183\n"
      "v_mfma_f32_16x16x32_bf16 v[192:195], a[228:231], v[140:143], v[192:195]\n"
      "s_mul_i32 s10, s8, 73\n"
      "v_fmac_f32 v213, v184, v184\n"
      "v_fmac_f32 v213, v185, v185\n"
      "v_mfma_f32_16x16x32_bf16 v[196:199], a[220:223], v[136:139], 0\n"
      "s_add_u32 s10, s10, s9\n"
      "v_fmac_f32 v213, v186, v186\n"
      "v_fmac_f32 v213, v187, v187\n"
      "v_mfma_f32_16x16x32_bf16 v[196:199], a[232:235], v[140:143], v[196:199]\n"
      "s_mul_i32 s28, s10, 4802\n"
      "v_fmac_f32 v213, v188, v188\n"
      "v_fmac_f32_e64 v213, v189, v189\n"
      "v_mfma_f32_16x16x32_bf16 v[200:203], a[224:227], v[136:139], 0\n"
      "ds_read_b128 v[216:219], v123 offset:31232\n"
      "v_fmac_f32_e64 v213, v190, v190\n"
      "v_mfma_f32_16x16x32_bf16 v[200:203], a[236:239], v[140:143], v[200:203]\n"
      "ds_read_b128 v[248:251], v123 offset:34304\n"
      "v_fmac_f32 v213, v191, v191\n"
      "v_mfma_f32_16x16x32_bf16 v[192:195], a[240:243], v[144:147], v[192:195]\n"
      "s_add_u32 s56, s18, s28\n"
      "s_addc_u32 s57, s19, 0\n"
      "s_and_b32 s56, s56, -16\n"
      "v_mfma_f32_16x16x32_bf16 v[192:195], a[252:255], v[148:151], v[192:195]\n"
      "s_waitcnt lgkmcnt(2)\n"
      "s_sub_u32 s58, s56, 110592\n"
      "v_mfma_f32_16x16x32_bf16 v[196:199], a[244:247], v[144:147], v[196:199]\n"
      "s_subb_u32 s59, s57, 0\n"
      "v_permlane16_swap_b32 v214, v213\n"
      "v_mfma_f32_16x16x32_bf16 v[196:199], v[0:3], v[148:151], v[196:199]\n"
      "s_sub_u32 s56, s56, 73728\n"
      "v_add_f32 v214, v214, v213\n"
      "v_mfma_f32_16x16x32_bf16 v[200:203], a[248:251], v[144:147], v[200:203]\n"
      "s_subb_u32 s57, s57, 0\n"
      "s_mul_i32 s28, s8, 1373568\n"
      "s_mul_i32 s29, s9, 2688\n"
      "v_mfma_f32_16x16x32_bf16 v[200:203], v[4:7], v[148:151], v[200:203]\n"
      "s_add_u32 s28, s28, s29\n"
      "s_cmp_eq_u32 s8, 72\n"
      "v_mfma_f32_16x16x32_bf16 v[228:231], v[220:223], v[66:69], 0\n"
      "s_cselect_b32 s8, 0, 100270464\n"
      "s_cmp_eq_u32 s9, 72\n"
      "s_cselect_b32 s9, 0, 196224\n"
      "v_mfma_f32_16x16x32_bf16 v[228:231], v[208:211], v[66:69], v[228:231]\n"
      "s_waitcnt lgkmcnt(0)\n"
      "s_add_u32 s10, s8, s9\n"
      "v_mfma_f32_16x16x32_bf16 v[192:195], v[8:11], v[216:219], v[192:195]\n"
      "s_add_u32 s28, s28, s10\n"
      "s_add_u32 s52, s16, s28\n"
      "s_addc_u32 s53, s17, 0\n"
      "v_mfma_f32_16x16x32_bf16 v[192:195], v[20:23], v[248:251], v[192:195]\n"
      "s_add_u32 s27, s27, 1\n"
      "v_mov_b32_dpp v228, v228 quad_perm:[0,0,0,0] row_mask:0xf bank_mask:0xf\n"
      "v_rcp_f32 v228, v228\n"
      "v_mfma_f32_16x16x32_bf16 v[196:199], v[12:15], v[216:219], v[196:199]\n"
      "s_cmp_ge_u32 s27, s26\n"
      "s_cselect_b32 s28, 1, 0\n"
      "s_xor_b32 s29, s10, s11\n"
      "v_mfma_f32_16x16x32_bf16 v[196:199], v[24:27], v[248:251], v[196:199]\n"
      "ds_read_b128 v[136:139], v114 offset:18432\n"
      "v_fma_f32 v148, v192, v192, s39\n"
      "v_mfma_f32_16x16x32_bf16 v[200:203], v[16:19], v[216:219], v[200:203]\n"
      "ds_read_b128 v[140:143], v115 offset:18432\n"
      "v_fmac_f32 v148, v193, v193\n"
      "v_mfma_f32_16x16x32_bf16 v[200:203], v[28:31], v[248:251], v[200:203]\n"
      "s_or_b32 s28, s28, s29\n"
      "v_fmac_f32 v148, v194, v194\n"
      "v_fmac_f32 v148, v195, v195\n"
      "v_mfma_f32_16x16x32_bf16 v[248:251], v[220:223], v[236:239], 0\n"
      "v_fmac_f32 v148, v196, v196\n"
      "v_fmac_f32 v148, v197, v197\n"
      "v_mfma_f32_16x16x32_bf16 v[248:251], v[208:211], v[50:53], v[248:251]\n"
      "v_fmac_f32 v148, v198, v198\n"
      "v_fmac_f32 v148, v199, v199\n"
      "v_mfma_f32_16x16x32_bf16 v[144:147], v[220:223], v[244:247], 0\n"
      "v_fmac_f32 v148, v200, v200\n"
      "v_fmac_f32 v148, v201, v201\n"
      "v_mfma_f32_16x16x32_bf16 v[144:147], v[208:211], v[54:57], v[144:147]\n"
      "v_fmac_f32 v148, v202, v202\n"
      "v_fmac_f32 v148, v203, v203\n"
      "v_mfma_f32_16x16x32_bf16 v[244:247], v[220:223], v[240:243], 0\n"
      "v_mov_b32 v149, v148\n"
      "v_mfma_f32_16x16x32_bf16 v[244:247], v[208:211], v[58:61], v[244:247]\n"
      "s_nop 0\n"
      "v_permlane16_swap_b32 v148, v149\n"
      "v_add_f32 v148, v148, v149\n"
      "v_cndmask_b32 v248, v248, v144, s[42:43]\n"
      "s_waitcnt lgkmcnt(0)\n"
      "v_mfma_f32_16x16x32_bf16 v[144:147], a[0:3], v[136:139], 0\n"
      "v_permlane32_swap_b32 v214, v148\n"
      "v_mfma_f32_16x16x32_bf16 v[144:147], a[36:39], v[140:143], v[144:147]\n"
      "v_add_f32 v214, v214, v148\n"
      "v_cndmask_b32 v248, v248, v244, s[44:45]\n"
      "v_mfma_f32_16x16x32_bf16 v[148:151], a[4:7], v[136:139], 0\n"
      "v_mul_f32 v248, v228, v248\n"
      "v_cvt_pk_bf16_f32 v248, v248, v248\n"
      "v_mfma_f32_16x16x32_bf16 v[148:151], a[40:43], v[140:143], v[148:151]\n"
      "ds_write_b32 v129, v214\n"
      "ds_write_b16 v135, v248\n"
      "s_branch L_join_30_%=\n"
      "L_flg_90_%=:\n"
      "v_cmp_gt_i32 vcc, 0, v160\n"
      "s_cbranch_vccz L_fb_91_%=\n"
      "s_sleep 1\n"
      "ds_read_b128 v[160:163], v128 offset:3072\n"
      "s_waitcnt lgkmcnt(0)\n"
      "v_bitop3_b32 v160, v160, v161, s4 bitop3:0x7e\n"
      "v_bitop3_b32 v162, v162, v163, s4 bitop3:0x7e\n"
      "v_or_b32 v160, v160, v162\n"
      "v_cmp_ne_u32 s[48:49], 0, v160\n"
      "s_branch L_flg_90_%=\n"
      "L_gen_29_%=:\n"
      "s_waitcnt lgkmcnt(0)\n"
      "s_nop 7\n"
      "s_nop 7\n"
      "v_mbcnt_lo_u32_b32 v215, -1, 0\n"
      "v_mbcnt_hi_u32_b32 v215, -1, v215\n"
      "v_and_b32 v227, 15, v215\n"
      "v_lshrrev_b32 v226, 4, v215\n"
      "v_mul_u32_u24 v252, 0x62, v227\n"
      "v_lshl_add_u32 v252, v226, 3, v252\n"
      "v_add_u32 v252, s30, v252\n"
      "v_and_b32 v215, -4, v252\n"
      "v_and_b32 v252, 2, v252\n"
      "v_lshlrev_b32 v226, 2, v226\n"
      "v_sub_u32_e64 v227, v227, v226\n"
      "v_and_b32_e64 v226, 1, v227\n"
      "v_lshlrev_b32_e64 v226, 4, v226\n"
      "v_lshrrev_b32_e64 v227, 1, v227\n"
      "v_cmp_eq_u32 vcc, 0, v227\n"
      "s_movk_i32 s31, 0x3fb9\n"
      "v_lshlrev_b32 v226, v226, s31\n"
      "ds_read2_b32 v[192:193], v215 offset0:0 offset1:1\n"
      "ds_read2_b32 v[166:167], v215 offset0:8 offset1:9\n"
      "ds_read2_b32 v[194:195], v215 offset0:16 offset1:17\n"
      "ds_read2_b32 v[162:163], v215 offset0:2 offset1:10\n"
      "ds_read2_b32 v[188:189], v215 offset0:18 offset1:24\n"
      "v_add_u32 v215, 1568, v215\n"
      "ds_read2_b32 v[202:203], v215 offset0:0 offset1:1\n"
      "ds_read2_b32 v[160:161], v215 offset0:8 offset1:9\n"
      "ds_read2_b32 v[148:149], v215 offset0:16 offset1:17\n"
      "ds_read2_b32 v[190:191], v215 offset0:2 offset1:10\n"
      "ds_read2_b32 v[164:165], v215 offset0:18 offset1:24\n"
      "v_add_u32 v215, 1568, v215\n"
      "ds_read2_b32 v[150:151], v215 offset0:0 offset1:1\n"
      "ds_read2_b32 v[234:235], v215 offset0:8 offset1:9\n"
      "ds_read2_b32 v[204:205], v215 offset0:16 offset1:17\n"
      "ds_read2_b32 v[200:201], v215 offset0:2 offset1:10\n"
      "ds_read2_b32 v[206:207], v215 offset0:18 offset1:24\n"
      "v_cndmask_b32 v232, 0, v226, vcc\n"
      "v_cmp_eq_u32 vcc, 1, v227\n"
      "s_waitcnt lgkmcnt(13)\n"
      "v_alignbyte_b32 v192, v193, v192, v252\n"
      "v_cndmask_b32 v233, 0, v226, vcc\n"
      "v_mfma_f32_16x16x32_bf16 v[224:227], v[248:251], v[180:183], v[90:93]\n"
      "s_waitcnt lgkmcnt(11)\n"
      "v_alignbyte_b32 v193, v162, v193, v252\n"
      "v_mfma_f32_16x16x32_bf16 v[224:227], v[34:37], v[38:41], v[224:227]\n"
      "v_alignbyte_b32 v166, v167, v166, v252\n"
      "v_alignbyte_b32 v167, v163, v167, v252\n"
      "v_mfma_f32_16x16x32_bf16 v[220:223], v[140:143], v[180:183], v[94:97]\n"
      "s_waitcnt lgkmcnt(8)\n"
      "v_alignbyte_b32 v194, v195, v194, v252\n"
      "v_alignbyte_b32 v195, v188, v195, v252\n"
      "v_mfma_f32_16x16x32_bf16 v[220:223], v[40:43], v[38:41], v[220:223]\n"
      "v_alignbyte_b32 v189, v189, v189, v252\n"
      "v_lshlrev_b32_e64 v189, 16, v189\n"
      "v_mfma_f32_16x16x32_bf16 v[208:211], v[136:139], v[180:183], v[72:75]\n"
      "s_waitcnt lgkmcnt(5)\n"
      "v_fmac_f32 v212, s37, v189\n"
      "v_alignbyte_b32 v202, v203, v202, v252\n"
      "v_mfma_f32_16x16x32_bf16 v[208:211], v[216:219], v[140:143], v[208:211]\n"
      "v_alignbyte_b32 v203, v190, v203, v252\n"
      "v_alignbyte_b32 v160, v161, v160, v252\n"
      "v_mfma_f32_16x16x32_bf16 v[208:211], v[152:155], v[38:41], v[208:211]\n"
      "v_alignbyte_b32 v161, v191, v161, v252\n"
      "v_alignbyte_b32 v148, v149, v148, v252\n"
      "v_mfma_f32_16x16x32_bf16 v[188:191], v[156:159], v[180:183], v[98:101]\n"
      "v_alignbyte_b32 v149, v164, v149, v252\n"
      "v_alignbyte_b32 v165, v165, v165, v252\n"
      "v_mfma_f32_16x16x32_bf16 v[188:191], v[46:49], v[38:41], v[188:191]\n"
      "v_lshlrev_b32_e64 v165, 16, v165\n"
      "v_mfma_f32_16x16x16_bf16 v[144:147], v[232:233], v[192:193], v[144:147]\n"
      "v_mfma_f32_16x16x16_bf16 v[228:231], v[232:233], v[166:167], v[228:231]\n"
      "s_nop 0\n"
      "v_mfma_f32_16x16x16_bf16 v[184:187], v[232:233], v[194:195], v[184:187]\n"
      "ds_read_b128 v[180:183], v128 offset:3072\n"
      "s_nop 4\n"
      "v_max3_f32 v162, v144, v145, v146\n"
      "v_max3_f32 v163, v147, v228, v229\n"
      "v_mfma_f32_16x16x16_bf16 v[224:227], v[232:233], v[202:203], v[224:227]\n"
      "v_max3_f32 v162, v162, v230, v231\n"
      "v_max3_f32 v163, v163, v184, v185\n"
      "v_mfma_f32_16x16x16_bf16 v[220:223], v[232:233], v[160:161], v[220:223]\n"
      "v_max3_f32 v162, v162, v186, v187\n"
      "v_max3_f32 v162, v162, v163, v212\n"
      "v_mfma_f32_16x16x16_bf16 v[188:191], v[232:233], v[148:149], v[188:191]\n"
      "v_mov_b32 v203, v162\n"
      "v_fmac_f32 v208, s37, v165\n"
      "s_nop 0\n"
      "v_permlane16_swap_b32 v162, v203\n"
      "v_max_f32 v162, v162, v203\n"
      "v_mov_b32_e64 v161, v162\n"
      "s_waitcnt lgkmcnt(1)\n"
      "v_alignbyte_b32 v150, v151, v150, v252\n"
      "v_permlane32_swap_b32 v162, v161\n"
      "v_max_f32 v162, v162, v161\n"
      "v_sub_f32 v144, v144, v162\n"
      "v_sub_f32 v145, v145, v162\n"
      "v_sub_f32 v146, v146, v162\n"
      "v_sub_f32 v147, v147, v162\n"
      "v_sub_f32 v228, v228, v162\n"
      "v_sub_f32 v229, v229, v162\n"
      "v_sub_f32 v230, v230, v162\n"
      "v_sub_f32 v231, v231, v162\n"
      "v_sub_f32 v184, v184, v162\n"
      "v_sub_f32 v185, v185, v162\n"
      "v_sub_f32 v186, v186, v162\n"
      "v_sub_f32 v187, v187, v162\n"
      "v_sub_f32 v212, v212, v162\n"
      "v_exp_f32 v144, v144\n"
      "v_exp_f32 v145, v145\n"
      "v_exp_f32 v146, v146\n"
      "v_cvt_pk_bf16_f32 v144, v144, v145\n"
      "v_exp_f32 v147, v147\n"
      "v_exp_f32 v228, v228\n"
      "v_cvt_pk_bf16_f32 v145, v146, v147\n"
      "v_exp_f32 v229, v229\n"
      "v_exp_f32 v230, v230\n"
      "v_cvt_pk_bf16_f32 v146, v228, v229\n"
      "v_exp_f32 v231, v231\n"
      "v_exp_f32 v184, v184\n"
      "v_cvt_pk_bf16_f32 v147, v230, v231\n"
      "v_exp_f32 v185, v185\n"
      "v_exp_f32 v186, v186\n"
      "v_exp_f32 v187, v187\n"
      "v_exp_f32 v212, v212\n"
      "v_cvt_pk_bf16_f32 v62, v184, v185\n"
      "v_cvt_pk_bf16_f32 v63, v186, v187\n"
      "v_cvt_pk_bf16_f32 v64, v212, 0\n"
      "v_max3_f32 v202, v224, v225, v226\n"
      "v_max3_f32 v203, v227, v220, v221\n"
      "v_mfma_f32_16x16x32_bf16 v[160:163], v[66:69], v[144:147], 0\n"
      "v_alignbyte_b32 v151, v200, v151, v252\n"
      "v_max3_f32 v202, v202, v222, v223\n"
      "v_mfma_f32_16x16x32_bf16 v[160:163], v[66:69], v[62:65], v[160:163]\n"
      "v_alignbyte_b32 v234, v235, v234, v252\n"
      "v_alignbyte_b32 v235, v201, v235, v252\n"
      "v_mfma_f32_16x16x32_bf16 v[184:187], v[236:239], v[144:147], 0\n"
      "v_alignbyte_b32 v204, v205, v204, v252\n"
      "v_max3_f32 v203, v203, v188, v189\n"
      "v_mfma_f32_16x16x32_bf16 v[184:187], v[50:53], v[62:65], v[184:187]\n"
      "v_alignbyte_b32 v205, v206, v205, v252\n"
      "v_alignbyte_b32 v207, v207, v207, v252\n"
      "v_lshlrev_b32 v207, 16, v207\n"
      "v_max3_f32 v202, v202, v190, v191\n"
      "v_max3_f32 v202, v202, v203, v208\n"
      "v_mfma_f32_16x16x32_bf16 v[164:167], v[244:247], v[144:147], 0\n"
      "s_waitcnt lgkmcnt(0)\n"
      "v_bitop3_b32 v180, v180, v181, s4 bitop3:0x7e\n"
      "v_bitop3_b32 v182, v182, v183, s4 bitop3:0x7e\n"
      "v_mfma_f32_16x16x32_bf16 v[164:167], v[54:57], v[62:65], v[164:167]\n"
      "v_or_b32 v180, v180, v182\n"
      "v_cmp_ne_u32 vcc, 0, v180\n"
      "s_nop 0\n"
      "s_mov_b64 s[48:49], vcc\n"
      "s_cbranch_vccnz L_flg_96_%=\n"
      "L_fb_97_%=:\n"
      "v_mov_b32 v203, v202\n"
      "s_nop 1\n"
      "v_permlane16_swap_b32 v202, v203\n"
      "v_max_f32 v202, v202, v203\n"
      "v_mov_b32 v203, v202\n"
      "s_nop 1\n"
      "v_permlane32_swap_b32 v202, v203\n"
      "v_max_f32 v202, v202, v203\n"
      "v_sub_f32 v224, v224, v202\n"
      "v_sub_f32 v225, v225, v202\n"
      "v_sub_f32 v226, v226, v202\n"
      "v_sub_f32 v227, v227, v202\n"
      "v_sub_f32 v220, v220, v202\n"
      "v_sub_f32 v221, v221, v202\n"
      "v_sub_f32 v222, v222, v202\n"
      "v_sub_f32 v223, v223, v202\n"
      "v_sub_f32 v188, v188, v202\n"
      "v_sub_f32 v189, v189, v202\n"
      "v_sub_f32 v190, v190, v202\n"
      "v_sub_f32 v191, v191, v202\n"
      "v_sub_f32 v208, v208, v202\n"
      "v_exp_f32 v224, v224\n"
      "v_exp_f32 v225, v225\n"
      "v_exp_f32 v226, v226\n"
      "v_cvt_pk_bf16_f32 v224, v224, v225\n"
      "v_exp_f32 v227, v227\n"
      "v_exp_f32_e64 v220, v220\n"
      "v_cvt_pk_bf16_f32 v225, v226, v227\n"
      "v_exp_f32 v221, v221\n"
      "v_exp_f32 v222, v222\n"
      "v_cvt_pk_bf16_f32 v226, v220, v221\n"
      "v_exp_f32 v223, v223\n"
      "v_exp_f32 v188, v188\n"
      "v_cvt_pk_bf16_f32 v227, v222, v223\n"
      "v_mfma_f32_16x16x32_bf16 v[192:195], v[240:243], v[144:147], 0\n"
      "ds_read_b128 v[200:203], v119 offset:30720\n"
      "v_mfma_f32_16x16x32_bf16 v[192:195], v[58:61], v[62:65], v[192:195]\n"
      "v_exp_f32 v189, v189\n"
      "v_mfma_f32_16x16x32_bf16 v[144:147], v[248:251], v[196:199], v[102:105]\n"
      "v_exp_f32 v190, v190\n"
      "v_mfma_f32_16x16x32_bf16 v[144:147], v[34:37], v[44:47], v[144:147]\n"
      "v_exp_f32 v191, v191\n"
      "v_mfma_f32_16x16x32_bf16 v[180:183], v[140:143], v[196:199], v[106:109]\n"
      "v_exp_f32 v208, v208\n"
      "v_mfma_f32_16x16x32_bf16 v[180:183], v[40:43], v[44:47], v[180:183]\n"
      "v_cvt_pk_bf16_f32 v62, v188, v189\n"
      "v_cvt_pk_bf16_f32 v63, v190, v191\n"
      "v_mfma_f32_16x16x16_bf16 v[144:147], v[232:233], v[150:151], v[144:147]\n"
      "v_cvt_pk_bf16_f32 v64, v208, 0\n"
      "v_rcp_f32 v160, v160\n"
      "v_mfma_f32_16x16x32_bf16 v[140:143], v[136:139], v[196:199], v[74:77]\n"
      "v_mul_f32 v184, v160, v184\n"
      "v_mul_f32 v185, v160, v185\n"
      "v_mfma_f32_16x16x32_bf16 v[140:143], v[216:219], v[156:159], v[140:143]\n"
      "s_waitcnt lgkmcnt(0)\n"
      "v_cvt_pk_bf16_f32 v184, v184, v185\n"
      "v_mul_f32_e64 v186, v160, v186\n"
      "v_mfma_f32_16x16x32_bf16 v[140:143], v[152:155], v[44:47], v[140:143]\n"
      "global_store_dwordx4 v116, v[200:203], s[54:55] offset:256 sc1\n"
      "v_mfma_f32_16x16x32_bf16 v[188:191], v[156:159], v[196:199], v[110:113]\n"
      "v_mul_f32_e64 v187, v160, v187\n"
      "v_cvt_pk_bf16_f32 v185, v186, v187\n"
      "v_mfma_f32_16x16x32_bf16 v[188:191], v[46:49], v[44:47], v[188:191]\n"
      "v_mul_f32 v164, v160, v164\n"
      "v_mul_f32 v165, v160, v165\n"
      "v_mfma_f32_16x16x32_bf16 v[196:199], v[216:219], v[136:139], v[76:79]\n"
      "v_fmac_f32 v140, s37, v207\n"
      "v_cvt_pk_bf16_f32 v186, v164, v165\n"
      "v_mfma_f32_16x16x16_bf16 v[180:183], v[232:233], v[234:235], v[180:183]\n"
      "ds_read_b128 v[156:159], v120 offset:18432\n"
      "v_mul_f32 v166, v160, v166\n"
      "v_mfma_f32_16x16x16_bf16 v[188:191], v[232:233], v[204:205], v[188:191]\n"
      "v_mul_f32_e64 v167, v160, v167\n"
      "v_cvt_pk_bf16_f32 v187, v166, v167\n"
      "v_mfma_f32_16x16x16_bf16 v[196:199], v[154:155], v[152:153], v[196:199]\n"
      "ds_write_b128 v124, v[184:187]\n"
      "v_mfma_f32_16x16x32_bf16 v[200:203], v[66:69], v[224:227], 0\n"
      "v_mul_f32 v192, v160, v192\n"
      "v_mul_f32 v193, v160, v193\n"
      "v_mfma_f32_16x16x32_bf16 v[200:203], v[66:69], v[62:65], v[200:203]\n"
      "v_cvt_pk_bf16_f32 v192, v192, v193\n"
      "v_mul_f32 v194, v160, v194\n"
      "v_mfma_f32_16x16x32_bf16 v[184:187], v[236:239], v[224:227], 0\n"
      "v_mul_f32_e64 v195, v160, v195\n"
      "v_cvt_pk_bf16_f32 v193, v194, v195\n"
      "v_mfma_f32_16x16x32_bf16 v[184:187], v[50:53], v[62:65], v[184:187]\n"
      "ds_write_b64 v125, v[192:193]\n"
      "v_mfma_f32_16x16x32_bf16 v[164:167], v[244:247], v[224:227], 0\n"
      "s_waitcnt lgkmcnt(2)\n"
      "v_max3_f32 v143, v144, v145, v146\n"
      "v_mfma_f32_16x16x32_bf16 v[164:167], v[54:57], v[62:65], v[164:167]\n"
      "global_store_dwordx4 v117, v[156:159], s[54:55] sc1\n"
      "v_mfma_f32_16x16x32_bf16 v[192:195], v[240:243], v[224:227], 0\n"
      "v_max3_f32 v142, v147, v180, v181\n"
      "v_max3_f32 v143, v143, v182, v183\n"
      "v_mfma_f32_16x16x32_bf16 v[192:195], v[58:61], v[62:65], v[192:195]\n"
      "v_max3_f32 v142, v142, v188, v189\n"
      "v_max3_f32 v143, v143, v190, v191\n"
      "v_max3_f32 v143, v143, v142, v140\n"
      "v_mov_b32 v142, v143\n"
      "s_nop 1\n"
      "v_permlane16_swap_b32 v143, v142\n"
      "v_max_f32 v143, v143, v142\n"
      "v_mov_b32 v142, v143\n"
      "v_rcp_f32 v200, v200\n"
      "s_nop 0\n"
      "v_permlane32_swap_b32 v143, v142\n"
      "v_max_f32 v143, v143, v142\n"
      "v_sub_f32 v144, v144, v143\n"
      "v_sub_f32 v145, v145, v143\n"
      "v_sub_f32 v146, v146, v143\n"
      "v_sub_f32 v147, v147, v143\n"
      "v_sub_f32 v180, v180, v143\n"
      "v_sub_f32 v181, v181, v143\n"
      "v_sub_f32 v182, v182, v143\n"
      "v_sub_f32 v183, v183, v143\n"
      "v_sub_f32 v188, v188, v143\n"
      "v_sub_f32 v189, v189, v143\n"
      "v_sub_f32 v190, v190, v143\n"
      "v_sub_f32 v191, v191, v143\n"
      "v_sub_f32 v140, v140, v143\n"
      "v_exp_f32 v144, v144\n"
      "v_exp_f32 v145, v145\n"
      "v_exp_f32 v146, v146\n"
      "v_cvt_pk_bf16_f32 v144, v144, v145\n"
      "v_exp_f32 v147, v147\n"
      "v_exp_f32 v180, v180\n"
      "v_cvt_pk_bf16_f32 v145, v146, v147\n"
      "v_exp_f32 v181, v181\n"
      "v_exp_f32 v182, v182\n"
      "v_cvt_pk_bf16_f32 v146, v180, v181\n"
      "v_exp_f32 v183, v183\n"
      "v_exp_f32 v188, v188\n"
      "v_cvt_pk_bf16_f32 v147, v182, v183\n"
      "v_exp_f32 v189, v189\n"
      "v_exp_f32 v190, v190\n"
      "v_exp_f32 v191, v191\n"
      "v_exp_f32_e64 v140, v140\n"
      "v_cvt_pk_bf16_f32 v62, v188, v189\n"
      "v_cvt_pk_bf16_f32 v63, v190, v191\n"
      "v_cvt_pk_bf16_f32 v64, v140, 0\n"
      "v_mul_f32 v184, v200, v184\n"
      "v_mul_f32 v185, v200, v185\n"
      "v_mfma_f32_16x16x32_bf16 v[152:155], v[66:69], v[144:147], 0\n"
      "ds_read_b128 v[136:139], v123 offset:18688\n"
      "v_cvt_pk_bf16_f32 v184, v184, v185\n"
      "v_mfma_f32_16x16x32_bf16 v[152:155], v[66:69], v[62:65], v[152:155]\n"
      "ds_read_b128 v[202:205], v123 offset:21760\n"
      "v_mul_f32 v186, v200, v186\n"
      "v_mfma_f32_16x16x32_bf16 v[156:159], v[236:239], v[144:147], 0\n"
      "ds_read_b128 v[148:151], v121 offset:18432\n"
      "v_mul_f32 v187, v200, v187\n"
      "v_mfma_f32_16x16x32_bf16 v[156:159], v[50:53], v[62:65], v[156:159]\n"
      "ds_read_b128 v[216:219], v123 offset:24832\n"
      "v_cvt_pk_bf16_f32 v185, v186, v187\n"
      "v_mfma_f32_16x16x32_bf16 v[160:163], v[244:247], v[144:147], 0\n"
      "ds_read_b128 v[232:235], v123 offset:27904\n"
      "v_mul_f32 v164, v200, v164\n"
      "v_mfma_f32_16x16x32_bf16 v[160:163], v[54:57], v[62:65], v[160:163]\n"
      "v_mul_f32 v165, v200, v165\n"
      "v_cvt_pk_bf16_f32 v186, v164, v165\n"
      "v_mul_f32 v166, v200, v166\n"
      "v_mul_f32 v167, v200, v167\n"
      "v_cvt_pk_bf16_f32 v187, v166, v167\n"
      "v_mfma_f32_16x16x32_bf16 v[164:167], v[240:243], v[144:147], 0\n"
      "ds_read_b128 v[144:147], v123 offset:30976\n"
      "v_mul_f32 v192, v200, v192\n"
      "v_mfma_f32_16x16x32_bf16 v[164:167], v[58:61], v[62:65], v[164:167]\n"
      "ds_write_b128 v124, v[184:187] offset:256\n"
      "s_waitcnt lgkmcnt(5)\n"
      "v_mfma_f32_16x16x32_bf16 v[180:183], a[216:219], v[136:139], 0\n"
      "ds_read_b128 v[224:227], v123 offset:34048\n"
      "v_mul_f32 v193, v200, v193\n"
      "v_mfma_f32_16x16x32_bf16 v[180:183], a[228:231], v[202:205], v[180:183]\n"
      "s_waitcnt lgkmcnt(5)\n"
      "v_cvt_pk_bf16_f32 v192, v192, v193\n"
      "v_mul_f32 v194, v200, v194\n"
      "v_mfma_f32_16x16x32_bf16 v[184:187], a[220:223], v[136:139], 0\n"
      "v_mul_f32 v195, v200, v195\n"
      "s_waitcnt lgkmcnt(3)\n"
      "v_mfma_f32_16x16x32_bf16 v[184:187], a[232:235], v[202:205], v[184:187]\n"
      "s_mov_b32 exec_hi, 0\n"
      "global_store_dwordx4 v118, v[148:151], s[54:55] sc1\n"
      "s_mov_b32 exec_hi, -1\n"
      "v_mfma_f32_16x16x32_bf16 v[188:191], a[224:227], v[136:139], 0\n"
      "s_add_u32 s28, s27, 2\n"
      "v_cvt_pk_bf16_f32 v193, v194, v195\n"
      "v_cndmask_b32 v213, v213, v209, s[42:43]\n"
      "v_mfma_f32_16x16x32_bf16 v[188:191], a[236:239], v[202:205], v[188:191]\n"
      "ds_write_b64 v125, v[192:193] offset:256\n"
      "v_mfma_f32_16x16x32_bf16 v[180:183], a[240:243], v[216:219], v[180:183]\n"
      "ds_read_b128 v[136:139], v123 offset:18944\n"
      "v_cndmask_b32 v213, v213, v141, s[44:45]\n"
      "v_mfma_f32_16x16x32_bf16 v[180:183], a[252:255], v[232:235], v[180:183]\n"
      "ds_read_b128 v[140:143], v123 offset:22016\n"
      "s_waitcnt lgkmcnt(3)\n"
      "v_mfma_f32_16x16x32_bf16 v[184:187], a[244:247], v[216:219], v[184:187]\n"
      "s_min_u32 m0, s28, s3\n"
      "v_cndmask_b32 v213, v213, v197, s[46:47]\n"
      "v_mbcnt_lo_u32_b32 v215, -1, 0\n"
      "v_mfma_f32_16x16x32_bf16 v[184:187], v[0:3], v[232:235], v[184:187]\n"
      "s_movrels_b32 s9, s80\n"
      "v_mbcnt_hi_u32_b32 v215, -1, v215\n"
      "v_min_u32 v215, 48, v215\n"
      "v_mfma_f32_16x16x32_bf16 v[188:191], a[248:251], v[216:219], v[188:191]\n"
      "s_lshr_b32 s8, s9, 8\n"
      "v_lshlrev_b32 v215, 1, v215\n"
      "v_add_u32_e64 v215, s30, v215\n"
      "v_mfma_f32_16x16x32_bf16 v[188:191], v[4:7], v[232:235], v[188:191]\n"
      "ds_read_u16 v197, v215 offset:4704\n"
      "v_mfma_f32_16x16x32_bf16 v[180:183], v[8:11], v[144:147], v[180:183]\n"
      "ds_read_b128 v[148:151], v123 offset:25088\n"
      "s_and_b32 s9, s9, 0xff\n"
      "v_mfma_f32_16x16x32_bf16 v[180:183], v[20:23], v[224:227], v[180:183]\n"
      "ds_read_b128 v[232:235], v123 offset:28160\n"
      "v_mfma_f32_16x16x32_bf16 v[184:187], v[12:15], v[144:147], v[184:187]\n"
      "s_mul_i32 s10, s8, 73\n"
      "s_add_u32 s10, s10, s9\n"
      "s_mul_i32 s28, s10, 4802\n"
      "v_mfma_f32_16x16x32_bf16 v[184:187], v[24:27], v[224:227], v[184:187]\n"
      "s_waitcnt lgkmcnt(3)\n"
      "s_add_u32 s56, s18, s28\n"
      "v_mfma_f32_16x16x32_bf16 v[188:191], v[16:19], v[144:147], v[188:191]\n"
      "s_addc_u32 s57, s19, 0\n"
      "s_and_b32 s56, s56, -16\n"
      "s_sub_u32 s58, s56, 110592\n"
      "v_mfma_f32_16x16x32_bf16 v[188:191], v[28:31], v[224:227], v[188:191]\n"
      "s_subb_u32 s59, s57, 0\n"
      "s_sub_u32 s56, s56, 73728\n"
      "v_mfma_f32_16x16x32_bf16 v[192:195], a[216:219], v[136:139], 0\n"
      "s_subb_u32 s57, s57, 0\n"
      "s_mul_i32 s28, s8, 1373568\n"
      "s_mul_i32 s29, s9, 2688\n"
      "v_mfma_f32_16x16x32_bf16 v[192:195], a[228:231], v[140:143], v[192:195]\n"
      "s_waitcnt lgkmcnt(2)\n"
      "v_lshlrev_b32_e64 v197, 16, v197\n"
      "v_fmac_f32_e64 v213, s37, v197\n"
      "v_mfma_f32_16x16x32_bf16 v[196:199], a[220:223], v[136:139], 0\n"
      "s_add_u32 s28, s28, s29\n"
      "v_max_f32_dpp v215, v213, v213 row_ror:8 row_mask:0xf bank_mask:0xf\n"
      "v_fma_f32 v212, v180, v180, s39\n"
      "v_mfma_f32_16x16x32_bf16 v[196:199], a[232:235], v[140:143], v[196:199]\n"
      "s_waitcnt lgkmcnt(0)\n"
      "v_max_f32_dpp v215, v215, v215 row_ror:4 row_mask:0xf bank_mask:0xf\n"
      "v_fmac_f32_e64 v212, v181, v181\n"
      "v_mfma_f32_16x16x32_bf16 v[200:203], a[224:227], v[136:139], 0\n"
      "ds_read_b128 v[144:147], v123 offset:31232\n"
      "v_max_f32_dpp v215, v215, v215 row_ror:2 row_mask:0xf bank_mask:0xf\n"
      "v_mfma_f32_16x16x32_bf16 v[200:203], a[236:239], v[140:143], v[200:203]\n"
      "ds_read_b128 v[220:223], v123 offset:34304\n"
      "v_max_f32_dpp v215, v215, v215 row_ror:1 row_mask:0xf bank_mask:0xf\n"
      "v_mfma_f32_16x16x32_bf16 v[192:195], a[240:243], v[148:151], v[192:195]\n"
      "s_cmp_eq_u32 s8, 72\n"
      "v_max_f32_dpp v215, v215, v215 row_bcast:15 row_mask:0xa bank_mask:0xf\n"
      "v_fmac_f32_e64 v212, v182, v182\n"
      "v_mfma_f32_16x16x32_bf16 v[192:195], a[252:255], v[232:235], v[192:195]\n"
      "s_cselect_b32 s8, 0, 100270464\n"
      "v_max_f32_dpp v215, v215, v215 row_bcast:31 row_mask:0xc bank_mask:0xf\n"
      "v_fmac_f32_e64 v212, v183, v183\n"
      "v_mfma_f32_16x16x32_bf16 v[196:199], a[244:247], v[148:151], v[196:199]\n"
      "s_cmp_eq_u32 s9, 72\n"
      "v_readlane_b32 s31, v215, 63\n"
      "v_fmac_f32_e64 v212, v184, v184\n"
      "v_mfma_f32_16x16x32_bf16 v[196:199], v[0:3], v[232:235], v[196:199]\n"
      "s_cselect_b32 s9, 0, 196224\n"
      "v_subrev_f32 v213, s31, v213\n"
      "v_exp_f32 v213, v213\n"
      "v_mfma_f32_16x16x32_bf16 v[200:203], a[248:251], v[148:151], v[200:203]\n"
      "s_add_u32 s10, s8, s9\n"
      "v_cvt_pk_bf16_f32 v213, v213, v213\n"
      "v_fmac_f32 v212, v185, v185\n"
      "v_mfma_f32_16x16x32_bf16 v[200:203], v[4:7], v[232:235], v[200:203]\n"
      "ds_write_b16 v130, v213\n"
      "v_fmac_f32 v212, v186, v186\n"
      "v_fmac_f32 v212, v187, v187\n"
      "v_fmac_f32 v212, v188, v188\n"
      "v_fmac_f32 v212, v189, v189\n"
      "v_fmac_f32 v212, v190, v190\n"
      "s_waitcnt lgkmcnt(1)\n"
      "v_mfma_f32_16x16x32_bf16 v[192:195], v[8:11], v[144:147], v[192:195]\n"
      "ds_read_b128 v[216:219], v131\n"
      "v_fmac_f32 v212, v191, v191\n"
      "v_mfma_f32_16x16x32_bf16 v[192:195], v[20:23], v[220:223], v[192:195]\n"
      "ds_read_b128 v[148:151], v131 offset:64\n"
      "v_permlane16_swap_b32 v214, v212\n"
      "v_mfma_f32_16x16x32_bf16 v[196:199], v[12:15], v[144:147], v[196:199]\n"
      "s_add_u32 s28, s28, s10\n"
      "v_add_f32_e64 v214, v214, v212\n"
      "s_add_u32 s52, s16, s28\n"
      "v_mfma_f32_16x16x32_bf16 v[196:199], v[24:27], v[220:223], v[196:199]\n"
      "ds_read_b128 v[136:139], v114 offset:18432\n"
      "v_fma_f32 v227, v192, v192, s39\n"
      "v_mfma_f32_16x16x32_bf16 v[200:203], v[16:19], v[144:147], v[200:203]\n"
      "ds_read_b128 v[140:143], v115 offset:18432\n"
      "v_fmac_f32 v227, v193, v193\n"
      "v_mfma_f32_16x16x32_bf16 v[200:203], v[28:31], v[220:223], v[200:203]\n"
      "s_addc_u32 s53, s17, 0\n"
      "v_fmac_f32 v227, v194, v194\n"
      "v_fmac_f32 v227, v195, v195\n"
      "v_fmac_f32 v227, v196, v196\n"
      "v_fmac_f32 v227, v197, v197\n"
      "v_fmac_f32 v227, v198, v198\n"
      "v_fmac_f32 v227, v199, v199\n"
      "s_nop 0\n"
      "v_fmac_f32 v227, v200, v200\n"
      "v_fmac_f32 v227, v201, v201\n"
      "v_fmac_f32 v227, v202, v202\n"
      "v_fmac_f32 v227, v203, v203\n"
      "v_mov_b32 v220, v227\n"
      "s_add_u32 s27, s27, 1\n"
      "s_nop 0\n"
      "v_permlane16_swap_b32 v227, v220\n"
      "s_waitcnt lgkmcnt(2)\n"
      "v_mfma_f32_16x16x32_bf16 v[232:235], v[216:219], v[66:69], 0\n"
      "s_cmp_ge_u32 s27, s26\n"
      "v_add_f32 v227, v227, v220\n"
      "s_cselect_b32 s28, 1, 0\n"
      "v_mfma_f32_16x16x32_bf16 v[232:235], v[148:151], v[66:69], v[232:235]\n"
      "s_xor_b32 s29, s10, s11\n"
      "v_permlane32_swap_b32 v214, v227\n"
      "v_mfma_f32_16x16x32_bf16 v[220:223], v[216:219], v[236:239], 0\n"
      "s_or_b32 s28, s28, s29\n"
      "v_add_f32 v214, v214, v227\n"
      "s_waitcnt lgkmcnt(0)\n"
      "v_mfma_f32_16x16x32_bf16 v[220:223], v[148:151], v[50:53], v[220:223]\n"
      "ds_write_b32 v129, v214\n"
      "v_mfma_f32_16x16x32_bf16 v[236:239], v[216:219], v[244:247], 0\n"
      "v_mov_b32_dpp v232, v232 quad_perm:[0,0,0,0] row_mask:0xf bank_mask:0xf\n"
      "v_mfma_f32_16x16x32_bf16 v[236:239], v[148:151], v[54:57], v[236:239]\n"
      "v_rcp_f32_e64 v232, v232\n"
      "v_mfma_f32_16x16x32_bf16 v[224:227], v[216:219], v[240:243], 0\n"
      "v_mfma_f32_16x16x32_bf16 v[224:227], v[148:151], v[58:61], v[224:227]\n"
      "v_mfma_f32_16x16x32_bf16 v[144:147], a[0:3], v[136:139], 0\n"
      "v_mfma_f32_16x16x32_bf16 v[144:147], a[36:39], v[140:143], v[144:147]\n"
      "v_mfma_f32_16x16x32_bf16 v[148:151], a[4:7], v[136:139], 0\n"
      "s_nop 1\n"
      "v_cndmask_b32 v220, v220, v236, s[42:43]\n"
      "v_mfma_f32_16x16x32_bf16 v[148:151], a[40:43], v[140:143], v[148:151]\n"
      "s_nop 0\n"
      "v_cndmask_b32 v220, v220, v224, s[44:45]\n"
      "v_mul_f32 v220, v232, v220\n"
      "v_cvt_pk_bf16_f32 v220, v220, v220\n"
      "ds_write_b16 v135, v220\n"
      "s_branch L_join_30_%=\n"
      "L_flg_96_%=:\n"
      "v_cmp_gt_i32 vcc, 0, v180\n"
      "s_cbranch_vccz L_fb_97_%=\n"
      "s_sleep 1\n"
      "ds_read_b128 v[180:183], v128 offset:3072\n"
      "s_waitcnt lgkmcnt(0)\n"
      "v_bitop3_b32 v180, v180, v181, s4 bitop3:0x7e\n"
      "v_bitop3_b32 v182, v182, v183, s4 bitop3:0x7e\n"
      "v_or_b32 v180, v180, v182\n"
      "v_cmp_ne_u32 s[48:49], 0, v180\n"
      "s_branch L_flg_96_%=\n"
      "L_edge_1_%=:\n"
      "s_mov_b32 s11, s10\n"
      "s_sub_u32 s28, s60, s8\n"
      "s_sub_u32 s29, s61, s9\n"
      "s_mov_b32 s60, s8\n"
      "s_mov_b32 s61, s9\n"
      "s_mov_b64 exec, s[64:65]\n"
      "v_add_u32 v116, s28, v116\n"
      "s_mov_b64 exec, s[70:71]\n"
      "v_add_u32 v116, s29, v116\n"
      "s_mov_b64 exec, s[66:67]\n"
      "v_add_u32 v117, s28, v117\n"
      "s_mov_b64 exec, s[72:73]\n"
      "v_add_u32 v117, s29, v117\n"
      "s_mov_b64 exec, s[68:69]\n"
      "v_add_u32 v118, s28, v118\n"
      "s_mov_b64 exec, s[74:75]\n"
      "v_add_u32 v118, s29, v118\n"
      "s_mov_b64 exec, -1\n"
      "s_nop 4\n"
      "s_branch L_eb_2_%=\n"
      "L_flg_3_%=:\n"
      "v_cmp_gt_i32 vcc, 0, v152\n"
      "s_cbranch_vccz L_fb_4_%=\n"
      "s_sleep 1\n"
      "ds_read_b128 v[152:155], v128 offset:2048\n"
      "s_waitcnt lgkmcnt(0)\n"
      "v_bitop3_b32 v152, v152, v153, s4 bitop3:0x7e\n"
      "v_bitop3_b32 v154, v154, v155, s4 bitop3:0x7e\n"
      "v_or_b32 v152, v152, v154\n"
      "v_cmp_ne_u32 s[48:49], 0, v152\n"
      "s_branch L_flg_3_%=\n"
      "L_edge_6_%=:\n"
      "s_mov_b32 s11, s10\n"
      "s_sub_u32 s28, s60, s8\n"
      "s_sub_u32 s29, s61, s9\n"
      "s_mov_b32 s60, s8\n"
      "s_mov_b32 s61, s9\n"
      "s_mov_b64 exec, s[64:65]\n"
      "v_add_u32 v116, s28, v116\n"
      "s_mov_b64 exec, s[70:71]\n"
      "v_add_u32 v116, s29, v116\n"
      "s_mov_b64 exec, s[66:67]\n"
      "v_add_u32 v117, s28, v117\n"
      "s_mov_b64 exec, s[72:73]\n"
      "v_add_u32 v117, s29, v117\n"
      "s_mov_b64 exec, s[68:69]\n"
      "v_add_u32 v118, s28, v118\n"
      "s_mov_b64 exec, s[74:75]\n"
      "v_add_u32 v118, s29, v118\n"
      "s_mov_b64 exec, -1\n"
      "s_nop 4\n"
      "s_branch L_eb_7_%=\n"
      "L_flg_12_%=:\n"
      "v_cmp_gt_i32 vcc, 0, v156\n"
      "s_cbranch_vccz L_fb_13_%=\n"
      "s_sleep 1\n"
      "ds_read_b128 v[156:159], v128 offset:3072\n"
      "s_waitcnt lgkmcnt(0)\n"
      "v_bitop3_b32 v156, v156, v157, s4 bitop3:0x7e\n"
      "v_bitop3_b32 v158, v158, v159, s4 bitop3:0x7e\n"
      "v_or_b32 v156, v156, v158\n"
      "v_cmp_ne_u32 s[48:49], 0, v156\n"
      "s_branch L_flg_12_%=\n"
      "L_edge_15_%=:\n"
      "s_mov_b32 s11, s10\n"
      "s_sub_u32 s28, s60, s8\n"
      "s_sub_u32 s29, s61, s9\n"
      "s_mov_b32 s60, s8\n"
      "s_mov_b32 s61, s9\n"
      "s_mov_b64 exec, s[64:65]\n"
      "v_add_u32 v116, s28, v116\n"
      "s_mov_b64 exec, s[70:71]\n"
      "v_add_u32 v116, s29, v116\n"
      "s_mov_b64 exec, s[66:67]\n"
      "v_add_u32 v117, s28, v117\n"
      "s_mov_b64 exec, s[72:73]\n"
      "v_add_u32 v117, s29, v117\n"
      "s_mov_b64 exec, s[68:69]\n"
      "v_add_u32 v118, s28, v118\n"
      "s_mov_b64 exec, s[74:75]\n"
      "v_add_u32 v118, s29, v118\n"
      "s_mov_b64 exec, -1\n"
      "s_nop 4\n"
      "s_branch L_eb_16_%=\n"
      "L_edge_17_%=:\n"
      "s_mov_b32 s11, s10\n"
      "s_sub_u32 s28, s60, s8\n"
      "s_sub_u32 s29, s61, s9\n"
      "s_mov_b32 s60, s8\n"
      "s_mov_b32 s61, s9\n"
      "s_mov_b64 exec, s[64:65]\n"
      "v_add_u32 v116, s28, v116\n"
      "s_mov_b64 exec, s[70:71]\n"
      "v_add_u32 v116, s29, v116\n"
      "s_mov_b64 exec, s[66:67]\n"
      "v_add_u32 v117, s28, v117\n"
      "s_mov_b64 exec, s[72:73]\n"
      "v_add_u32 v117, s29, v117\n"
      "s_mov_b64 exec, s[68:69]\n"
      "v_add_u32 v118, s28, v118\n"
      "s_mov_b64 exec, s[74:75]\n"
      "v_add_u32 v118, s29, v118\n"
      "s_mov_b64 exec, -1\n"
      "s_nop 4\n"
      "s_branch L_eb_18_%=\n"
      "L_flg_23_%=:\n"
      "v_cmp_gt_i32 vcc, 0, v204\n"
      "s_cbranch_vccz L_fb_24_%=\n"
      "s_sleep 1\n"
      "ds_read_b128 v[204:207], v128 offset:2048\n"
      "s_waitcnt lgkmcnt(0)\n"
      "v_bitop3_b32 v204, v204, v205, s4 bitop3:0x7e\n"
      "v_bitop3_b32 v206, v206, v207, s4 bitop3:0x7e\n"
      "v_or_b32 v204, v204, v206\n"
      "v_cmp_ne_u32 s[48:49], 0, v204\n"
      "s_branch L_flg_23_%=\n"
      "L_edge_26_%=:\n"
      "s_mov_b32 s11, s10\n"
      "s_sub_u32 s28, s60, s8\n"
      "s_sub_u32 s29, s61, s9\n"
      "s_mov_b32 s60, s8\n"
      "s_mov_b32 s61, s9\n"
      "s_mov_b64 exec, s[64:65]\n"
      "v_add_u32 v116, s28, v116\n"
      "s_mov_b64 exec, s[70:71]\n"
      "v_add_u32 v116, s29, v116\n"
      "s_mov_b64 exec, s[66:67]\n"
      "v_add_u32 v117, s28, v117\n"
      "s_mov_b64 exec, s[72:73]\n"
      "v_add_u32 v117, s29, v117\n"
      "s_mov_b64 exec, s[68:69]\n"
      "v_add_u32 v118, s28, v118\n"
      "s_mov_b64 exec, s[74:75]\n"
      "v_add_u32 v118, s29, v118\n"
      "s_mov_b64 exec, -1\n"
      "s_nop 4\n"
      "s_branch L_eb_27_%=\n"
      "L_flg_32_%=:\n"
      "v_cmp_gt_i32 vcc, 0, v148\n"
      "s_cbranch_vccz L_fb_33_%=\n"
      "s_sleep 1\n"
      "ds_read_b128 v[148:151], v128 offset:3072\n"
      "s_waitcnt lgkmcnt(0)\n"
      "v_bitop3_b32 v148, v148, v149, s4 bitop3:0x7e\n"
      "v_bitop3_b32 v150, v150, v151, s4 bitop3:0x7e\n"
      "v_or_b32 v148, v148, v150\n"
      "v_cmp_ne_u32 s[48:49], 0, v148\n"
      "s_branch L_flg_32_%=\n"
      "L_edge_35_%=:\n"
      "s_mov_b32 s11, s10\n"
      "s_sub_u32 s28, s60, s8\n"
      "s_sub_u32 s29, s61, s9\n"
      "s_mov_b32 s60, s8\n"
      "s_mov_b32 s61, s9\n"
      "s_mov_b64 exec, s[64:65]\n"
      "v_add_u32 v116, s28, v116\n"
      "s_mov_b64 exec, s[70:71]\n"
      "v_add_u32 v116, s29, v116\n"
      "s_mov_b64 exec, s[66:67]\n"
      "v_add_u32 v117, s28, v117\n"
      "s_mov_b64 exec, s[72:73]\n"
      "v_add_u32 v117, s29, v117\n"
      "s_mov_b64 exec, s[68:69]\n"
      "v_add_u32 v118, s28, v118\n"
      "s_mov_b64 exec, s[74:75]\n"
      "v_add_u32 v118, s29, v118\n"
      "s_mov_b64 exec, -1\n"
      "s_nop 4\n"
      "s_branch L_eb_36_%=\n"
      "L_edge_37_%=:\n"
      "s_mov_b32 s11, s10\n"
      "s_sub_u32 s28, s60, s8\n"
      "s_sub_u32 s29, s61, s9\n"
      "s_mov_b32 s60, s8\n"
      "s_mov_b32 s61, s9\n"
      "s_mov_b64 exec, s[64:65]\n"
      "v_add_u32 v116, s28, v116\n"
      "s_mov_b64 exec, s[70:71]\n"
      "v_add_u32 v116, s29, v116\n"
      "s_mov_b64 exec, s[66:67]\n"
      "v_add_u32 v117, s28, v117\n"
      "s_mov_b64 exec, s[72:73]\n"
      "v_add_u32 v117, s29, v117\n"
      "s_mov_b64 exec, s[68:69]\n"
      "v_add_u32 v118, s28, v118\n"
      "s_mov_b64 exec, s[74:75]\n"
      "v_add_u32 v118, s29, v118\n"
      "s_mov_b64 exec, -1\n"
      "s_nop 4\n"
      "s_branch L_eb_38_%=\n"
      "L_edge_39_%=:\n"
      "s_mov_b32 s11, s10\n"
      "s_sub_u32 s28, s60, s8\n"
      "s_sub_u32 s29, s61, s9\n"
      "s_mov_b32 s60, s8\n"
      "s_mov_b32 s61, s9\n"
      "s_mov_b64 exec, s[64:65]\n"
      "v_add_u32 v116, s28, v116\n"
      "s_mov_b64 exec, s[70:71]\n"
      "v_add_u32 v116, s29, v116\n"
      "s_mov_b64 exec, s[66:67]\n"
      "v_add_u32 v117, s28, v117\n"
      "s_mov_b64 exec, s[72:73]\n"
      "v_add_u32 v117, s29, v117\n"
      "s_mov_b64 exec, s[68:69]\n"
      "v_add_u32 v118, s28, v118\n"
      "s_mov_b64 exec, s[74:75]\n"
      "v_add_u32 v118, s29, v118\n"
      "s_mov_b64 exec, -1\n"
      "s_nop 4\n"
      "s_branch L_eb_40_%=\n"
      "L_edge_42_%=:\n"
      "s_mov_b32 s11, s10\n"
      "s_sub_u32 s28, s60, s8\n"
      "s_sub_u32 s29, s61, s9\n"
      "s_mov_b32 s60, s8\n"
      "s_mov_b32 s61, s9\n"
      "s_mov_b64 exec, s[64:65]\n"
      "v_add_u32 v116, s28, v116\n"
      "s_mov_b64 exec, s[70:71]\n"
      "v_add_u32 v116, s29, v116\n"
      "s_mov_b64 exec, s[66:67]\n"
      "v_add_u32 v117, s28, v117\n"
      "s_mov_b64 exec, s[72:73]\n"
      "v_add_u32 v117, s29, v117\n"
      "s_mov_b64 exec, s[68:69]\n"
      "v_add_u32 v118, s28, v118\n"
      "s_mov_b64 exec, s[74:75]\n"
      "v_add_u32 v118, s29, v118\n"
      "s_mov_b64 exec, -1\n"
      "s_nop 4\n"
      "s_branch L_eb_43_%=\n"
      "L_edge_44_%=:\n"
      "s_mov_b32 s11, s10\n"
      "s_sub_u32 s28, s60, s8\n"
      "s_sub_u32 s29, s61, s9\n"
      "s_mov_b32 s60, s8\n"
      "s_mov_b32 s61, s9\n"
      "s_mov_b64 exec, s[64:65]\n"
      "v_add_u32 v116, s28, v116\n"
      "s_mov_b64 exec, s[70:71]\n"
      "v_add_u32 v116, s29, v116\n"
      "s_mov_b64 exec, s[66:67]\n"
      "v_add_u32 v117, s28, v117\n"
      "s_mov_b64 exec, s[72:73]\n"
      "v_add_u32 v117, s29, v117\n"
      "s_mov_b64 exec, s[68:69]\n"
      "v_add_u32 v118, s28, v118\n"
      "s_mov_b64 exec, s[74:75]\n"
      "v_add_u32 v118, s29, v118\n"
      "s_mov_b64 exec, -1\n"
      "s_nop 4\n"
      "s_branch L_eb_45_%=\n"
      "L_edge_46_%=:\n"
      "s_mov_b32 s11, s10\n"
      "s_sub_u32 s28, s60, s8\n"
      "s_sub_u32 s29, s61, s9\n"
      "s_mov_b32 s60, s8\n"
      "s_mov_b32 s61, s9\n"
      "s_mov_b64 exec, s[64:65]\n"
      "v_add_u32 v116, s28, v116\n"
      "s_mov_b64 exec, s[70:71]\n"
      "v_add_u32 v116, s29, v116\n"
      "s_mov_b64 exec, s[66:67]\n"
      "v_add_u32 v117, s28, v117\n"
      "s_mov_b64 exec, s[72:73]\n"
      "v_add_u32 v117, s29, v117\n"
      "s_mov_b64 exec, s[68:69]\n"
      "v_add_u32 v118, s28, v118\n"
      "s_mov_b64 exec, s[74:75]\n"
      "v_add_u32 v118, s29, v118\n"
      "s_mov_b64 exec, -1\n"
      "s_nop 4\n"
      "s_branch L_eb_47_%=\n"
      "L_cend_%=:\n"
      :
      : "s"(__builtin_amdgcn_kernarg_segment_ptr()), "s"(wave), "s"(blockIdx.x), "s"(CT_TAB), "s"(lds0)
      : "v0","v1","v2","v3","v4","v5","v6","v7","v8","v9","v10","v11","v12","v13","v14","v15","v16","v17","v18","v19","v20","v21","v22","v23","v24","v25","v26","v27","v28","v29","v30","v31","v32","v33","v34","v35","v36","v37","v38","v39","v40","v41","v42","v43","v44","v45","v46","v47","v48","v49","v50","v51","v52","v53","v54","v55","v56","v57","v58","v59","v60","v61","v62","v63","v64","v65","v66","v67","v68","v69","v70","v71","v72","v73","v74","v75","v76","v77","v78","v79","v80","v81","v82","v83","v84","v85","v86","v87","v88","v89","v90","v91","v92","v93","v94","v95","v96","v97","v98","v99","v100","v101","v102","v103","v104","v105","v106","v107","v108","v109","v110","v111","v112","v113","v114","v115","v116","v117","v118","v119","v120","v121","v122","v123","v124","v125","v126","v127","v128","v129","v130","v131","v132","v133","v134","v135","v136","v137","v138","v139","v140","v141","v142","v143","v144","v145","v146","v147","v148","v149","v150","v151","v152","v153","v154","v155","v156","v157","v158","v159","v160","v161","v162","v163","v164","v165","v166","v167","v168","v169","v170","v171","v172","v173","v174","v175","v176","v177","v178","v179","v180","v181","v182","v183","v184","v185","v186","v187","v188","v189","v190","v191","v192","v193","v194","v195","v196","v197","v198","v199","v200","v201","v202","v203","v204","v205","v206","v207","v208","v209","v210","v211","v212","v213","v214","v215","v216","v217","v218","v219","v220","v221","v222","v223","v224","v225","v226","v227","v228","v229","v230","v231","v232","v233","v234","v235","v236","v237","v238","v239","v240","v241","v242","v243","v244","v245","v246","v247","v248","v249","v250","v251","v252","v253","v254","v255","a0","a1","a2","a3","a4","a5","a6","a7","a8","a9","a10","a11","a12","a13","a14","a15","a16","a17","a18","a19","a20","a21","a22","a23","a24","a25","a26","a27","a28","a29","a30","a31","a32","a33","a34","a35","a36","a37","a38","a39","a40","a41","a42","a43","a44","a45","a46","a47","a48","a49","a50","a51","a52","a53","a54","a55","a56","a57","a58","a59","a60","a61","a62","a63","a64","a65","a66","a67","a68","a69","a70","a71","a72","a73","a74","a75","a76","a77","a78","a79","a80","a81","a82","a83","a84","a85","a86","a87","a88","a89","a90","a91","a92","a93","a94","a95","a96","a97","a98","a99","a100","a101","a102","a103","a104","a105","a106","a107","a108","a109","a110","a111","a112","a113","a114","a115","a116","a117","a118","a119","a120","a121","a122","a123","a124","a125","a126","a127","a128","a129","a130","a131","a132","a133","a134","a135","a136","a137","a138","a139","a140","a141","a142","a143","a144","a145","a146","a147","a148","a149","a150","a151","a152","a153","a154","a155","a156","a157","a158","a159","a160","a161","a162","a163","a164","a165","a166","a167","a168","a169","a170","a171","a172","a173","a174","a175","a176","a177","a178","a179","a180","a181","a182","a183","a184","a185","a186","a187","a188","a189","a190","a191","a192","a193","a194","a195","a196","a197","a198","a199","a200","a201","a202","a203","a204","a205","a206","a207","a208","a209","a210","a211","a212","a213","a214","a215","a216","a217","a218","a219","a220","a221","a222","a223","a224","a225","a226","a227","a228","a229","a230","a231","a232","a233","a234","a235","a236","a237","a238","a239","a240","a241","a242","a243","a244","a245","a246","a247","a248","a249","a250","a251","a252","a253","a254","a255","s2","s3","s4","s5","s6","s7","s8","s9","s10","s11","s12","s13","s16","s17","s18","s19","s20","s21","s22","s23","s24","s25","s26","s27","s28","s29","s30","s31","s34","s35","s36","s37","s38","s39","s40","s41","s42","s43","s44","s45","s46","s47","s48","s49","s50","s51","s52","s53","s54","s55","s60","s61","s62","s63","s64","s65","s66","s67","s68","s69","s70","s71","s72","s73","s74","s75","s76","s77","s78","s79","s80","s81","s82","s83","s84","s85","s86","s87","s88","s89","s90","s91","s92","s93","s94","s95","s96","s97","s98","s99","s100","s101", "vcc", "scc", "memory");
}

}

extern "C" void kernel_launch(void* const* d_in, const int* in_sizes, int n_in, void* d_out, int out_size, void* d_ws,
                              size_t ws_size, hipStream_t stream) {
  const uint16_t* x = (const uint16_t*)d_in[0];
  const uint16_t* wqkv = (const uint16_t*)d_in[1];
  const uint16_t* wo = (const uint16_t*)d_in[2];
  const uint16_t* ls = (const uint16_t*)d_in[3];
  const uint16_t* w1 = (const uint16_t*)d_in[4];
  const uint16_t* w2 = (const uint16_t*)d_in[5];
  const uint16_t* rc = (const uint16_t*)d_in[6];
  const int* ridx = (const int*)d_in[7];
  const uint16_t* mask = (const uint16_t*)d_in[8];
  swin::swin_main<<<256, 256, 0, stream>>>(x, mask, (char*)d_ws, (uint16_t*)d_out, wqkv, wo, ls, w1, w2, rc, ridx);
}
```

```cpp
#include <hip/hip_runtime.h>
#include <hip/hip_bf16.h>
#include <stdint.h>

namespace swin {

constexpr unsigned LDS_BYTES = 163840u;

__device__ __attribute__((aligned(256))) const unsigned CT_TAB[26704] = {0,64,0,0xc0000,144,208,16,0xc0010,288,352,32,0xc0020,432,496,48,0xc0030,576,512,64,0xc0040,720,656,80,0xc0050,864,800,96,0xc0060,1008,944,112,0xc0070,1024,1088,400,0xc0190,1168,1232,384,0xc0180,1312,1376,432,0xc01b0,1456,1520,416,0xc01a0,1600,1536,464,0xc01d0,1744,1680,448,0xc01c0,1888,1824,496,0xc01f0,2032,1968,480,0xc01e0,16,80,800,0xc0320,128,192,816,0xc0330,304,368,768,0xc0300,416,480,784,0xc0310,592,528,864,0xc0360,704,640,880,0xc0370,880,816,832,0xc0340,992,928,848,0xc0350,1040,1104,1200,0xef8b0,1152,1216,1184,0xef8a0,1328,1392,1168,0xef890,1440,1504,1152,0xef880,1616,1552,1264,0xef8f0,1728,1664,1248,0xef8e0,1904,1840,1232,0xef8d0,2016,1952,1216,0xef8c0,32,96,1600,0xefa40,176,240,1616,0xefa50,256,320,1632,0xefa60,400,464,1648,0xefa70,608,544,1536,0xefa00,752,688,1552,0xefa10,832,768,1568,0xefa20,976,912,1584,0xefa30,1056,1120,2000,0xefbd0,1200,1264,1984,0xefbc0,1280,1344,2032,0xefbf0,1424,1488,2016,0xefbe0,1632,1568,1936,0xefb90,1776,1712,1920,0xefb80,1856,1792,1968,0xefbb0,2000,1936,1952,0xefba0,48,112,2400,0xefd60,160,224,2416,0xefd70,272,336,2368,0xefd40,384,448,2384,0xefd50,624,560,2336,0xefd20,736,672,2352,0xefd30,848,784,2304,0xefd00,960,896,2320,0xefd10,1072,1136,0x2fef0,0xefef0,1184,1248,0x2fee0,0xefee0,1296,1360,0x2fed0,0xefed0,1408,1472,0x2fec0,0xefec0,1648,1584,0x2feb0,0xefeb0,1760,1696,0x2fea0,0xefea0,1872,1808,0x2fe90,0xefe90,1984,1920,0x2fe80,0xefe80,0xc0100,73728,77824,90112,0xc0110,73744,77840,90128,0xc0120,73760,77856,90144,0xc0130,73776,77872,90160,0xc0140,73792,77888,90176,0xc0150,73808,77904,90192,0xc0160,73824,77920,90208,0xc0170,73840,77936,90224,0xc0290,73856,77952,90240,0xc0280,73872,77968,90256,0xc02b0,73888,77984,90272,0xc02a0,73904,78000,90288,0xc02d0,73920,78016,90304,0xc02c0,73936,78032,90320,0xc02f0,73952,78048,90336,0xc02e0,73968,78064,90352,0xc0420,73984,78080,90368,0xc0430,74000,78096,90384,0xc0400,74016,78112,90400,0xc0410,74032,78128,90416,0xc0460,74048,78144,90432,0xc0470,74064,78160,90448,0xc0440,74080,78176,90464,0xc0450,74096,78192,90480,0xef9b0,74112,78208,90496,0xef9a0,74128,78224,90512,0xef990,74144,78240,90528,0xef980,74160,78256,90544,0xef9f0,74176,78272,90560,0xef9e0,74192,78288,90576,0xef9d0,74208,78304,90592,0xef9c0,74224,78320,90608,0xc0100,74240,78336,90624,0xc0110,74256,78352,90640,0xc0120,74272,78368,90656,0xc0130,74288,78384,90672,0xc0140,74304,78400,90688,0xc0150,74320,78416,90704,0xc0160,74336,78432,90720,0xc0170,74352,78448,90736,0xc0290,74368,78464,90752,0xc0280,74384,78480,90768,0xc02b0,74400,78496,90784,0xc02a0,74416,78512,90800,0xc02d0,74432,78528,90816,0xc02c0,74448,78544,90832,0xc02f0,74464,78560,90848,0xc02e0,74480,78576,90864,0xc0420,74496,78592,90880,0xc0430,74512,78608,90896,0xc0400,74528,78624,90912,0xc0410,74544,78640,90928,0xc0460,74560,78656,90944,0xc0470,74576,78672,90960,0xc0440,74592,78688,90976,0xc0450,74608,78704,90992,0xef9b0,74624,78720,91008,0xef9a0,74640,78736,91024,0xef990,74656,78752,91040,0xef980,74672,78768,91056,0xef9f0,74688,78784,91072,0xef9e0,74704,78800,91088,0xef9d0,74720,78816,91104,0xef9c0,74736,78832,91120,0x1c000,36864,36864,49152,0x1c004,36880,36880,49168,0x1c008,36896,36896,49184,0x1c00c,36912,36912,49200,0x1c010,36928,36928,49216,0x1c014,36944,36944,49232,0x1c018,36960,36960,49248,0x1c01c,36976,36976,49264,0x1c020,36992,36992,49280,0x1c024,37008,37008,49296,0x1c028,37024,37024,49312,0x1c02c,37040,37040,49328,0x1c030,37056,37056,49344,0x1c034,37072,37072,49360,0x1c038,37088,37088,49376,0x1c03c,37104,37104,49392,0x1c040,37632,37632,49920,0x1c044,37648,37648,49936,0x1c048,37664,37664,49952,0x1c04c,37680,37680,49968,0x1c050,37696,37696,49984,0x1c054,37712,37712,50000,0x1c058,37728,37728,50016,0x1c05c,37744,37744,50032,0x1c060,37760,37760,50048,0x1c064,37776,37776,50064,0x1c068,37792,37792,50080,0x1c06c,37808,37808,50096,0x1c070,37824,37824,50112,0x1c074,37840,37840,50128,0x1c078,37856,37856,50144,0x1c07c,37872,37872,50160,0x1c080,38400,38400,50688,0x1c084,38416,38416,50704,0x1c088,38432,38432,50720,0x1c08c,38448,38448,50736,0x1c090,38464,38464,50752,0x1c094,38480,38480,50768,0x1c098,38496,38496,50784,0x1c09c,38512,38512,50800,0x1c0a0,38528,38528,50816,0x1c0a4,38544,38544,50832,0x1c0a8,38560,38560,50848,0x1c0ac,38576,38576,50864,0x1c0b0,38592,38592,50880,0x1c0b4,38608,38608,50896,0x1c0b8,38624,38624,50912,0x1c0bc,38640,38640,50928,0x1c0c0,39168,39168,51456,0x1c0c4,39184,39184,51472,0x1c0c8,39200,39200,51488,0x1c0cc,39216,39216,51504,0x1c0d0,39232,39232,51520,0x1c0d4,39248,39248,51536,0x1c0d8,39264,39264,51552,0x1c0dc,39280,39280,51568,0x1c0e0,39296,39296,51584,0x1c0e4,39312,39312,51600,0x1c0e8,39328,39328,51616,0x1c0ec,39344,39344,51632,0x1c0f0,39360,39360,51648,0x1c0f4,39376,39376,51664,0x1c0f8,39392,39392,51680,0x1c0fc,39408,39408,51696,73728,73792,0x1d600,0x1d600,73872,73936,0x1d610,0x1d610,74016,74080,0x1d620,0x1d620,74160,74224,0x1d630,0x1d630,74304,74240,0x1d640,0x1d640,74448,74384,0x1d650,0x1d650,74592,74528,0x1d660,0x1d660,74736,74672,0x1d670,0x1d670,74752,74816,0x1d680,0x1d680,74896,74960,0x1d690,0x1d690,75040,75104,0x1d6a0,0x1d6a0,75184,75248,0x1d6b0,0x1d6b0,75328,75264,0x1d6c0,0x1d6c0,75472,75408,0x1d6d0,0x1d6d0,75616,75552,0x1d6e0,0x1d6e0,75760,75696,0x1d6f0,0x1d6f0,73744,73800,0x1d700,0x1d700,73856,73944,0x1d710,0x1d710,74032,74088,0x1d720,0x1d720,74144,74232,0x1d730,0x1d730,74320,74248,0x1d740,0x1d740,74432,74392,0x1d750,0x1d750,74608,74536,0x1d760,0x1d760,74720,74680,0x1d770,0x1d770,74768,74824,0x1d780,0x1d780,74880,74968,0x1d790,0x1d790,75056,75112,0x1d7a0,0x1d7a0,75168,75256,0x1d7b0,0x1d7b0,75344,75272,0x1d7c0,0x1d7c0,75456,75416,0x1d7d0,0x1d7d0,75632,75560,0x1d7e0,0x1d7e0,75744,75704,0x1d7f0,0x1d7f0,73760,73808,0x1d800,0x1d800,73904,73920,0x1d810,0x1d810,73984,74096,0x1d820,0x1d820,74128,74208,0x1d830,0x1d830,74336,74256,0x1d840,0x1d840,74480,74368,0x1d850,0x1d850,74560,74544,0x1d860,0x1d860,74704,74656,0x1d870,0x1d870,74784,74832,0x1d880,0x1d880,74928,74944,0x1d890,0x1d890,75008,75120,0x1d8a0,0x1d8a0,75152,75232,0x1d8b0,0x1d8b0,75360,75280,0x1d8c0,0x1d8c0,75504,75392,0x1d8d0,0x1d8d0,75584,75568,0x1d8e0,0x1d8e0,75728,75680,0x1d8f0,0x1d8f0,73776,73816,0x1d900,0x1d900,73888,73928,0x1d910,0x1d910,74000,74104,0x1d920,0x1d920,74112,74216,0x1d930,0x1d930,74352,74264,0x1d940,0x1d940,74464,74376,0x1d950,0x1d950,74576,74552,0x1d960,0x1d960,74688,74664,0x1d970,0x1d970,74800,74840,0x1d980,0x1d980,74912,74952,0x1d990,0x1d990,75024,75128,0x1d9a0,0x1d9a0,75136,75240,0x1d9b0,0x1d9b0,75376,75288,0x1d9c0,0x1d9c0,75488,75400,0x1d9d0,0x1d9d0,75600,75576,0x1d9e0,0x1d9e0,75712,75688,0x1d9f0,0x1d9f0,0x1e600,0x1e600,0x1e800,0x100000,0x1e602,0x100000,0x100000,0x1e800,0x1e604,0x100000,0x100000,0x100000,0x1e606,0x100000,0x100000,0x100000,0x1e610,0x1e600,0x100000,0x100000,0x1e612,0x100000,0x100000,0x1e800,0x1e614,0x100000,0x100000,0x100000,0x1e616,0x100000,0x100000,0x100000,0x1e620,0x1e600,0x100000,0x100000,0x1e622,0x100000,0x100000,0x1e800,0x1e624,0x100000,0x100000,0x100000,0x1e626,0x100000,0x100000,0x100000,0x1e630,0x1e600,0x100000,0x100000,0x1e632,0x100000,0x100000,0x1e800,0x1e634,0x100000,0x100000,0x100000,0x1e636,0x100000,0x100000,0x100000,0x1e608,0x1e610,0x1e830,0x100000,0x1e60a,0x100000,0x100000,0x1e830,0x1e60c,0x100000,0x100000,0x100000,0x1e60e,0x100000,0x100000,0x100000,0x1e618,0x1e610,0x100000,0x100000,0x1e61a,0x100000,0x100000,0x1e830,0x1e61c,0x100000,0x100000,0x100000,0x1e61e,0x100000,0x100000,0x100000,0x1e628,0x1e610,0x100000,0x100000,0x1e62a,0x100000,0x100000,0x1e830,0x1e62c,0x100000,0x100000,0x100000,0x1e62e,0x100000,0x100000,0x100000,0x1e638,0x1e610,0x100000,0x100000,0x1e63a,0x100000,0x100000,0x1e830,0x1e63c,0x100000,0x100000,0x100000,0x1e63e,0x100000,0x100000,0x100000,0x1e640,0x1e620,0x1e860,0x100000,0x1e642,0x100000,0x100000,0x1e860,0x1e644,0x100000,0x100000,0x100000,0x1e646,0x100000,0x100000,0x100000,0x1e650,0x1e620,0x100000,0x100000,0x1e652,0x100000,0x100000,0x1e860,0x1e654,0x100000,0x100000,0x100000,0x1e656,0x100000,0x100000,0x100000,0x1e660,0x1e620,0x100000,0x100000,0x1e662,0x100000,0x100000,0x1e860,0x1e664,0x100000,0x100000,0x100000,0x1e666,0x100000,0x100000,0x100000,0x1e670,0x1e620,0x100000,0x100000,0x1e672,0x100000,0x100000,0x1e860,0x1e674,0x100000,0x100000,0x100000,0x1e676,0x100000,0x100000,0x100000,0x1e648,0x1e630,0x1e890,0x100000,0x1e64a,0x100000,0x100000,0x1e890,0x1e64c,0x100000,0x100000,0x100000,0x1e64e,0x100000,0x100000,0x100000,0x1e658,0x1e630,0x100000,0x100000,0x1e65a,0x100000,0x100000,0x1e890,0x1e65c,0x100000,0x100000,0x100000,0x1e65e,0x100000,0x100000,0x100000,0x1e668,0x1e630,0x100000,0x100000,0x1e66a,0x100000,0x100000,0x1e890,0x1e66c,0x100000,0x100000,0x100000,0x1e66e,0x100000,0x100000,0x100000,0x1e678,0x1e630,0x100000,0x100000,0x1e67a,0x100000,0x100000,0x1e890,0x1e67c,0x100000,0x100000,0x100000,0x1e67e,0x100000,0x100000,0x100000,0x1eb00,0x24c70,36864,36928,0x1eb02,0x24c72,36880,36936,0x1eb04,0x24c74,36896,36944,0x1eb06,0x24c76,36912,36952,0x1eb08,0x24dd0,37248,37312,0x1eb0a,0x24dd2,37264,37320,0x1eb0c,0x24dd4,37280,37328,0x1eb0e,0x24dd6,37296,37336,0x1eb10,0x24f30,37664,37728,0x1eb12,0x24f32,37680,37736,0x1eb14,0x24f34,37632,37744,0x1eb16,0x24f36,37648,37752,0x1eb18,0x25090,38048,38112,0x1eb1a,0x25092,38064,38120,0x1eb1c,0x25094,38016,38128,0x1eb1e,0x25096,38032,38136,0x1eb00,0x24c78,38464,38400,0x1eb02,0x24c7a,38480,38408,0x1eb04,0x24c7c,38496,38416,0x1eb06,0x24c7e,38512,38424,0x1eb08,0x24dd8,38848,38784,0x1eb0a,0x24dda,38864,38792,0x1eb0c,0x24ddc,38880,38800,0x1eb0e,0x24dde,38896,38808,0x1eb10,0x24f38,39264,39200,0x1eb12,0x24f3a,39280,39208,0x1eb14,0x24f3c,39232,39216,0x1eb16,0x24f3e,39248,39224,0x1eb18,0x25098,39648,39584,0x1eb1a,0x2509a,39664,39592,0x1eb1c,0x2509c,39616,39600,0x1eb1e,0x2509e,39632,39608,0x1eb00,0x26270,39936,40000,0x1eb02,0x26272,39952,40008,0x1eb04,0x26274,39968,40016,0x1eb06,0x26276,39984,40024,0x1eb08,0x263d0,40320,40384,0x1eb0a,0x263d2,40336,40392,0x1eb0c,0x263d4,40352,40400,0x1eb0e,0x263d6,40368,40408,0x1eb10,0x26530,40736,40800,0x1eb12,0x26532,40752,40808,0x1eb14,0x26534,40704,40816,0x1eb16,0x26536,40720,40824,0x1eb18,0x26690,41120,41184,0x1eb1a,0x26692,41136,41192,0x1eb1c,0x26694,41088,41200,0x1eb1e,0x26696,41104,41208,0x1eb00,0x100000,41536,41472,0x1eb02,0x100000,41552,41480,0x1eb04,0x100000,41568,41488,0x1eb06,0x100000,41584,41496,0x1eb08,0x100000,41920,41856,0x1eb0a,0x100000,41936,41864,0x1eb0c,0x100000,41952,41872,0x1eb0e,0x100000,41968,41880,0x1eb10,0x100000,42336,42272,0x1eb12,0x100000,42352,42280,0x1eb14,0x100000,42304,42288,0x1eb16,0x100000,42320,42296,0x1eb18,0x100000,42720,42656,0x1eb1a,0x100000,42736,42664,0x1eb1c,0x100000,42688,42672,0x1eb1e,0x100000,42704,42680,36864,36896,36928,36864,36872,36904,36936,36880,36880,36912,36944,36896,36888,36920,36952,36912,38016,38048,38080,36928,38024,38056,38088,36944,38032,38064,38096,36960,38040,38072,38104,36976,39200,39168,39264,36992,39208,39176,39272,37008,39216,39184,39280,37024,39224,39192,39288,37040,40352,40320,40416,37056,40360,40328,40424,37072,40368,40336,40432,37088,40376,40344,40440,37104,46144,46176,46080,37120,46152,46184,46088,37136,46160,46192,46096,37152,46168,46200,46104,37168,47296,47328,47232,37184,47304,47336,47240,37200,47312,47344,47248,37216,47320,47352,47256,37232,48480,48448,48416,37248,48488,48456,48424,37264,48496,48464,48432,37280,48504,48472,48440,37296,49632,49600,49568,37312,49640,49608,49576,37328,49648,49616,49584,37344,49656,49624,49592,37360,55296,55328,55360,37376,55304,55336,55368,37392,55312,55344,55376,37408,55320,55352,55384,37424,56448,56480,56512,37440,56456,56488,56520,37456,56464,56496,56528,37472,56472,56504,56536,37488,57632,57600,57696,37504,57640,57608,57704,37520,57648,57616,57712,37536,57656,57624,57720,37552,58784,58752,58848,37568,58792,58760,58856,37584,58800,58768,58864,37600,58808,58776,58872,37616,64576,64608,64512,37632,64584,64616,64520,37648,64592,64624,64528,37664,64600,64632,64536,37680,65728,65760,65664,37696,65736,65768,65672,37712,65744,65776,65680,37728,65752,65784,65688,37744,66912,66880,66848,37760,66920,66888,66856,37776,66928,66896,66864,37792,66936,66904,66872,37808,68064,68032,68000,37824,68072,68040,68008,37840,68080,68048,68016,37856,68088,68056,68024,37872,0x1e800,0x1e800,0x20cc0,0,0x1e8c4,0x1e8c4,0x20cc4,16,0x1e988,0x1e988,0x20cc8,32,0x1ea4c,0x1ea4c,0x20ccc,48,0x1eb10,0x1eb10,0x20cd0,64,0x1ebd4,0x1ebd4,0x20cd4,80,0x1ec98,0x1ec98,0x20cd8,96,0x1ed5c,0x1ed5c,0x20cdc,112,0x1ee20,0x1ee20,0x20ce0,384,0x1eee4,0x1eee4,0x20ce4,400,0x1efa8,0x1efa8,0x20ce8,416,0x1f06c,0x1f06c,0x20cec,432,0x1f130,0x1f130,0x20cf0,448,0x1f1f4,0x1f1f4,0x20cf4,464,0x1f2b8,0x1f2b8,0x20cf8,480,0x1f37c,0x1f37c,0x20cfc,496,0x1e810,0x1e800,0x20cc0,768,0x1e8d4,0x1e8c4,0x20cc4,784,0x1e998,0x1e988,0x20cc8,800,0x1ea5c,0x1ea4c,0x20ccc,816,0x1eb20,0x1eb10,0x20cd0,832,0x1ebe4,0x1ebd4,0x20cd4,848,0x1eca8,0x1ec98,0x20cd8,864,0x1ed6c,0x1ed5c,0x20cdc,880,0x1ee30,0x1ee20,0x20ce0,1152,0x1eef4,0x1eee4,0x20ce4,1168,0x1efb8,0x1efa8,0x20ce8,1184,0x1f07c,0x1f06c,0x20cec,1200,0x1f140,0x1f130,0x20cf0,1216,0x1f204,0x1f1f4,0x20cf4,1232,0x1f2c8,0x1f2b8,0x20cf8,1248,0x1f38c,0x1f37c,0x20cfc,1264,0x1e820,0x1e800,0x20cc0,1536,0x1e8e4,0x1e8c4,0x20cc4,1552,0x1e9a8,0x1e988,0x20cc8,1568,0x1ea6c,0x1ea4c,0x20ccc,1584,0x1eb30,0x1eb10,0x20cd0,1600,0x1ebf4,0x1ebd4,0x20cd4,1616,0x1ecb8,0x1ec98,0x20cd8,1632,0x1ed7c,0x1ed5c,0x20cdc,1648,0x1ee40,0x1ee20,0x20ce0,1920,0x1ef04,0x1eee4,0x20ce4,1936,0x1efc8,0x1efa8,0x20ce8,1952,0x1f08c,0x1f06c,0x20cec,1968,0x1f150,0x1f130,0x20cf0,1984,0x1f214,0x1f1f4,0x20cf4,2000,0x1f2d8,0x1f2b8,0x20cf8,2016,0x1f39c,0x1f37c,0x20cfc,2032,0x1e830,0x1e800,0x20cc0,2304,0x1e8f4,0x1e8c4,0x20cc4,2320,0x1e9b8,0x1e988,0x20cc8,2336,0x1ea7c,0x1ea4c,0x20ccc,2352,0x1eb40,0x1eb10,0x20cd0,2368,0x1ec04,0x1ebd4,0x20cd4,2384,0x1ecc8,0x1ec98,0x20cd8,2400,0x1ed8c,0x1ed5c,0x20cdc,2416,0x1ee50,0x1ee20,0x20ce0,2688,0x1ef14,0x1eee4,0x20ce4,2704,0x1efd8,0x1efa8,0x20ce8,2720,0x1f09c,0x1f06c,0x20cec,2736,0x1f160,0x1f130,0x20cf0,2752,0x1f224,0x1f1f4,0x20cf4,2768,0x1f2e8,0x1f2b8,0x20cf8,2784,0x1f3ac,0x1f37c,0x20cfc,2800,36864,0x26d70,0x1e800,0x1eb00,36880,0x26d74,0x1ec90,0x1ef90,36896,0x26d78,0x1f120,0x1f420,36912,0x26d7c,0x1f5b0,0x1f8b0,36928,0x26d80,0x1fa40,0x1fd40,36944,0x26d84,0x1fed0,0x201d0,36960,0x26d88,0x20360,0x20660,36976,0x26d8c,0x207f0,0x20af0,37248,0x26d90,0x20c80,0x20f80,37264,0x26d94,0x21110,0x21410,37280,0x26d98,0x215a0,0x218a0,37296,0x26d9c,0x21a30,0x21d30,37312,0x26da0,0x21ec0,0x221c0,37328,0x26da4,0x22350,0x22650,37344,0x26da8,0x227e0,0x22ae0,37360,0x26dac,0x22c70,0x22f70,37664,0x26db0,0x1e830,0x1eb08,37680,0x26db4,0x1ecc0,0x1ef98,37632,0x26db8,0x1f150,0x1f428,37648,0x26dbc,0x1f5e0,0x1f8b8,37728,0x26dc0,0x1fa70,0x1fd48,37744,0x26dc4,0x1ff00,0x201d8,37696,0x26dc8,0x20390,0x20668,37712,0x26dcc,0x20820,0x20af8,38048,0x26dd0,0x20cb0,0x20f88,38064,0x26dd4,0x21140,0x21418,38016,0x26dd8,0x215d0,0x218a8,38032,0x26ddc,0x21a60,0x21d38,38112,0x26de0,0x21ef0,0x221c8,38128,0x26de4,0x22380,0x22658,38080,0x26de8,0x22810,0x22ae8,38096,0x26dec,0x22ca0,0x22f78,38464,0x26df0,0x1e860,0x1eb10,38480,0x26df4,0x1ecf0,0x1efa0,38496,0x26df8,0x1f180,0x1f430,38512,0x26dfc,0x1f610,0x1f8c0,38400,0x26e00,0x1faa0,0x1fd50,38416,0x26e04,0x1ff30,0x201e0,38432,0x26e08,0x203c0,0x20670,38448,0x26e0c,0x20850,0x20b00,38848,0x26e10,0x20ce0,0x20f90,38864,0x26e14,0x21170,0x21420,38880,0x26e18,0x21600,0x218b0,38896,0x26e1c,0x21a90,0x21d40,38784,0x26e20,0x21f20,0x221d0,38800,0x26e24,0x223b0,0x22660,38816,0x26e28,0x22840,0x22af0,38832,0x26e2c,0x22cd0,0x22f80,39264,0x26e30,0x1e890,0x1eb18,39280,0x26e34,0x1ed20,0x1efa8,39232,0x26e38,0x1f1b0,0x1f438,39248,0x26e3c,0x1f640,0x1f8c8,39200,0x26e40,0x1fad0,0x1fd58,39216,0x26e44,0x1ff60,0x201e8,39168,0x26e48,0x203f0,0x20678,39184,0x26e4c,0x20880,0x20b08,39648,0x26e50,0x20d10,0x20f98,39664,0x26e54,0x211a0,0x21428,39616,0x26e58,0x21630,0x218b8,39632,0x26e5c,0x21ac0,0x21d48,39584,0x26e60,0x21f50,0x221d8,39600,0x26e64,0x223e0,0x22668,39552,0x26e68,0x22870,0x22af8,39568,0x26e6c,0x22d00,0x22f88,0x3f803f80,0x3f803f80,16256,0,0,0x3f803f80,0x3f800000,0,0,0x3f803f80,0,16256,0,0x3f803f80,0,0x3f800000,0x3f803f80,0,0,0,0,0,0,0,0,0,0,0,0,0,0,0,0x3f803f80,0,0,0,0,0,0,0,0,0,0,0,0,0,0,0,0x3f803f80,0,0,0,0,0,0,0,0,0,0,0,0,0,0,0,0x3f803f80,0,0,0,0,0,0,0,0,0,0,0,0,0,0,0,0x3f803f80,0x3f803f80,16256,0,0,0x3f803f80,0x3f800000,0,0,0x3f803f80,0,16256,0,0x3f803f80,0,0x3f800000,0x3f803f80,0,0,0,0,0,0,0,0,0,0,0,0,0,0,0,0x3f803f80,0,0,0,0,0,0,0,0,0,0,0,0,0,0,0,0x3f803f80,0,0,0,0,0,0,0,0,0,0,0,0,0,0,0,0x3f803f80,0,0,0,0,0,0,0,0,0,0,0,0,0,0,0,0x3f803f80,0x3f803f80,16256,0,0,0x3f803f80,0x3f800000,0,0,0x3f803f80,0,16256,0,0x3f803f80,0,0x3f800000,0x3f803f80,0,0,0,0,0,0,0,0,0,0,0,0,0,0,0,0x3f803f80,0,0,0,0,0,0,0,0,0,0,0,0,0,0,0,0x3f803f80,0,0,0,0,0,0,0,0,0,0,0,0,0,0,0,0x3f803f80,0,0,0,0,0,0,0,0,0,0,0,0,0,0,0,0x3f803f80,0x3f803f80,16256,0,0,0x3f803f80,0x3f800000,0,0,0x3f803f80,0,16256,0,0x3f803f80,0,0x3f800000,0,64,0x30000,0xf0000,144,208,0x30010,0xf0010,288,352,0x30020,0xf0020,432,496,0x30030,0xf0030,576,512,0x30040,0xf0040,720,656,0x30050,0xf0050,864,800,0x30060,0xf0060,1008,944,0x30070,0xf0070,1024,1088,0x30190,0xf0190,1168,1232,0x30180,0xf0180,1312,1376,0x301b0,0xf01b0,1456,1520,0x301a0,0xf01a0,1600,1536,0x301d0,0xf01d0,1744,1680,0x301c0,0xf01c0,1888,1824,0x301f0,0xf01f0,2032,1968,0x301e0,0xf01e0,16,80,0x30320,0x11f720,128,192,0x30330,0x11f730,304,368,0x30300,0x11f700,416,480,0x30310,0x11f710,592,528,0x30360,0x11f760,704,640,0x30370,0x11f770,880,816,0x30340,0x11f740,992,928,0x30350,0x11f750,1040,1104,0x304b0,0x11f8b0,1152,1216,0x304a0,0x11f8a0,1328,1392,0x30490,0x11f890,1440,1504,0x30480,0x11f880,1616,1552,0x304f0,0x11f8f0,1728,1664,0x304e0,0x11f8e0,1904,1840,0x304d0,0x11f8d0,2016,1952,0x304c0,0x11f8c0,32,96,0x30640,0x11fa40,176,240,0x30650,0x11fa50,256,320,0x30660,0x11fa60,400,464,0x30670,0x11fa70,608,544,0x30600,0x11fa00,752,688,0x30610,0x11fa10,832,768,0x30620,0x11fa20,976,912,0x30630,0x11fa30,1056,1120,0x307d0,0x11fbd0,1200,1264,0x307c0,0x11fbc0,1280,1344,0x307f0,0x11fbf0,1424,1488,0x307e0,0x11fbe0,1632,1568,0x30790,0x11fb90,1776,1712,0x30780,0x11fb80,1856,1792,0x307b0,0x11fbb0,2000,1936,0x307a0,0x11fba0,48,112,0x5fd60,0x11fd60,160,224,0x5fd70,0x11fd70,272,336,0x5fd40,0x11fd40,384,448,0x5fd50,0x11fd50,624,560,0x5fd20,0x11fd20,736,672,0x5fd30,0x11fd30,848,784,0x5fd00,0x11fd00,960,896,0x5fd10,0x11fd10,1072,1136,0x5fef0,0x11fef0,1184,1248,0x5fee0,0x11fee0,1296,1360,0x5fed0,0x11fed0,1408,1472,0x5fec0,0x11fec0,1648,1584,0x5feb0,0x11feb0,1760,1696,0x5fea0,0x11fea0,1872,1808,0x5fe90,0x11fe90,1984,1920,0x5fe80,0x11fe80,0xefb40,74752,78848,90624,0xefb50,74768,78864,90640,0xefb60,74784,78880,90656,0xefb70,74800,78896,90672,0xefb00,74816,78912,90688,0xefb10,74832,78928,90704,0xefb20,74848,78944,90720,0xefb30,74864,78960,90736,0xefcd0,74880,78976,90752,0xefcc0,74896,78992,90768,0xefcf0,74912,79008,90784,0xefce0,74928,79024,90800,0xefc90,74944,79040,90816,0xefc80,74960,79056,90832,0xefcb0,74976,79072,90848,0xefca0,74992,79088,90864,0xefe60,75008,79104,90880,0xefe70,75024,79120,90896,0xefe40,75040,79136,90912,0xefe50,75056,79152,90928,0xefe20,75072,79168,90944,0xefe30,75088,79184,90960,0xefe00,75104,79200,90976,0xefe10,75120,79216,90992,0xefff0,75136,79232,91008,0xeffe0,75152,79248,91024,0xeffd0,75168,79264,91040,0xeffc0,75184,79280,91056,0xeffb0,75200,79296,91072,0xeffa0,75216,79312,91088,0xeff90,75232,79328,91104,0xeff80,75248,79344,91120,0xefb40,75264,79360,91136,0xefb50,75280,79376,91152,0xefb60,75296,79392,91168,0xefb70,75312,79408,91184,0xefb00,75328,79424,91200,0xefb10,75344,79440,91216,0xefb20,75360,79456,91232,0xefb30,75376,79472,91248,0xefcd0,75392,79488,91264,0xefcc0,75408,79504,91280,0xefcf0,75424,79520,91296,0xefce0,75440,79536,91312,0xefc90,75456,79552,91328,0xefc80,75472,79568,91344,0xefcb0,75488,79584,91360,0xefca0,75504,79600,91376,0xefe60,75520,79616,91392,0xefe70,75536,79632,91408,0xefe40,75552,79648,91424,0xefe50,75568,79664,91440,0xefe20,75584,79680,91456,0xefe30,75600,79696,91472,0xefe00,75616,79712,91488,0xefe10,75632,79728,91504,0xefff0,75648,79744,91520,0xeffe0,75664,79760,91536,0xeffd0,75680,79776,91552,0xeffc0,75696,79792,91568,0xeffb0,75712,79808,91584,0xeffa0,75728,79824,91600,0xeff90,75744,79840,91616,0xeff80,75760,79856,91632,0x1c0a0,36864,39936,49160,0x1c0a4,36880,39952,49176,0x1c0a8,36896,39968,49192,0x1c0ac,36912,39984,49208,0x1c0b0,36928,40000,49224,0x1c0b4,36944,40016,49240,0x1c0b8,36960,40032,49256,0x1c0bc,36976,40048,49272,0x1c0c0,36992,40064,49288,0x1c0c4,37008,40080,49304,0x1c0c8,37024,40096,49320,0x1c0cc,37040,40112,49336,0x1c0d0,37056,40128,49352,0x1c0d4,37072,40144,49368,0x1c0d8,37088,40160,49384,0x1c0dc,37104,40176,49400,0x1c0e0,37632,40704,49928,0x1c0e4,37648,40720,49944,0x1c0e8,37664,40736,49960,0x1c0ec,37680,40752,49976,0x1c0f0,37696,40768,49992,0x1c0f4,37712,40784,50008,0x1c0f8,37728,40800,50024,0x1c0fc,37744,40816,50040,0x1c100,37760,40832,50056,0x1c104,37776,40848,50072,0x1c108,37792,40864,50088,0x1c10c,37808,40880,50104,0x1c110,37824,40896,50120,0x1c114,37840,40912,50136,0x1c118,37856,40928,50152,0x1c11c,37872,40944,50168,0x1c120,38400,41472,50696,0x1c124,38416,41488,50712,0x1c128,38432,41504,50728,0x1c12c,38448,41520,50744,0x1c130,38464,41536,50760,0x1c134,38480,41552,50776,0x1c138,38496,41568,50792,0x1c13c,38512,41584,50808,0x1c140,38528,41600,50824,0x1c144,38544,41616,50840,0x1c148,38560,41632,50856,0x1c14c,38576,41648,50872,0x1c150,38592,41664,50888,0x1c154,38608,41680,50904,0x1c158,38624,41696,50920,0x1c15c,38640,41712,50936,0x1c160,39168,42240,51464,0x1c164,39184,42256,51480,0x1c168,39200,42272,51496,0x1c16c,39216,42288,51512,0x1c170,39232,42304,51528,0x1c174,39248,42320,51544,0x1c178,39264,42336,51560,0x1c17c,39280,42352,51576,0x1c180,39296,42368,51592,0x1c184,39312,42384,51608,0x1c188,39328,42400,51624,0x1c18c,39344,42416,51640,0x1c190,39360,42432,51656,0x1c194,39376,42448,51672,0x1c198,39392,42464,51688,0x1c19c,39408,42480,51704,73824,79904,0x1d600,0x1d604,73968,80048,0x1d610,0x1d614,74048,80128,0x1d620,0x1d624,74192,80272,0x1d630,0x1d634,74272,80480,0x1d640,0x1d644,74416,80624,0x1d650,0x1d654,74496,80704,0x1d660,0x1d664,74640,80848,0x1d670,0x1d674,74848,80928,0x1d680,0x1d684,74992,81072,0x1d690,0x1d694,75072,81152,0x1d6a0,0x1d6a4,75216,81296,0x1d6b0,0x1d6b4,75296,81504,0x1d6c0,0x1d6c4,75440,81648,0x1d6d0,0x1d6d4,75520,81728,0x1d6e0,0x1d6e4,75664,81872,0x1d6f0,0x1d6f4,73840,79912,0x1d700,0x1d704,73952,80056,0x1d710,0x1d714,74064,80136,0x1d720,0x1d724,74176,80280,0x1d730,0x1d734,74288,80488,0x1d740,0x1d744,74400,80632,0x1d750,0x1d754,74512,80712,0x1d760,0x1d764,74624,80856,0x1d770,0x1d774,74864,80936,0x1d780,0x1d784,74976,81080,0x1d790,0x1d794,75088,81160,0x1d7a0,0x1d7a4,75200,81304,0x1d7b0,0x1d7b4,75312,81512,0x1d7c0,0x1d7c4,75424,81656,0x1d7d0,0x1d7d4,75536,81736,0x1d7e0,0x1d7e4,75648,81880,0x1d7f0,0x1d7f4,79872,79920,0x1d800,0x1d804,80016,80032,0x1d810,0x1d814,80160,80144,0x1d820,0x1d824,80304,80256,0x1d830,0x1d834,80448,80496,0x1d840,0x1d844,80592,80608,0x1d850,0x1d854,80736,80720,0x1d860,0x1d864,80880,80832,0x1d870,0x1d874,80896,80944,0x1d880,0x1d884,81040,81056,0x1d890,0x1d894,81184,81168,0x1d8a0,0x1d8a4,81328,81280,0x1d8b0,0x1d8b4,81472,81520,0x1d8c0,0x1d8c4,81616,81632,0x1d8d0,0x1d8d4,81760,81744,0x1d8e0,0x1d8e4,81904,81856,0x1d8f0,0x1d8f4,79888,79928,0x1d900,0x1d904,80000,80040,0x1d910,0x1d914,80176,80152,0x1d920,0x1d924,80288,80264,0x1d930,0x1d934,80464,80504,0x1d940,0x1d944,80576,80616,0x1d950,0x1d954,80752,80728,0x1d960,0x1d964,80864,80840,0x1d970,0x1d974,80912,80952,0x1d980,0x1d984,81024,81064,0x1d990,0x1d994,81200,81176,0x1d9a0,0x1d9a4,81312,81288,0x1d9b0,0x1d9b4,81488,81528,0x1d9c0,0x1d9c4,81600,81640,0x1d9d0,0x1d9d4,81776,81752,0x1d9e0,0x1d9e4,81888,81864,0x1d9f0,0x1d9f4,0x1e680,0x1e680,0x1e8c0,0x100000,0x1e682,0x100000,0x100000,0x1e8c0,0x1e684,0x100000,0x100000,0x100000,0x1e686,0x100000,0x100000,0x100000,0x1e690,0x1e680,0x100000,0x100000,0x1e692,0x100000,0x100000,0x1e8c0,0x1e694,0x100000,0x100000,0x100000,0x1e696,0x100000,0x100000,0x100000,0x1e6a0,0x1e680,0x100000,0x100000,0x1e6a2,0x100000,0x100000,0x1e8c0,0x1e6a4,0x100000,0x100000,0x100000,0x1e6a6,0x100000,0x100000,0x100000,0x1e6b0,0x1e680,0x100000,0x100000,0x1e6b2,0x100000,0x100000,0x1e8c0,0x1e6b4,0x100000,0x100000,0x100000,0x1e6b6,0x100000,0x100000,0x100000,0x1e688,0x1e690,0x1e8f0,0x100000,0x1e68a,0x100000,0x100000,0x1e8f0,0x1e68c,0x100000,0x100000,0x100000,0x1e68e,0x100000,0x100000,0x100000,0x1e698,0x1e690,0x100000,0x100000,0x1e69a,0x100000,0x100000,0x1e8f0,0x1e69c,0x100000,0x100000,0x100000,0x1e69e,0x100000,0x100000,0x100000,0x1e6a8,0x1e690,0x100000,0x100000,0x1e6aa,0x100000,0x100000,0x1e8f0,0x1e6ac,0x100000,0x100000,0x100000,0x1e6ae,0x100000,0x100000,0x100000,0x1e6b8,0x1e690,0x100000,0x100000,0x1e6ba,0x100000,0x100000,0x1e8f0,0x1e6bc,0x100000,0x100000,0x100000,0x1e6be,0x100000,0x100000,0x100000,0x1e6c0,0x1e6a0,0x1e920,0x100000,0x1e6c2,0x100000,0x100000,0x1e920,0x1e6c4,0x100000,0x100000,0x100000,0x1e6c6,0x100000,0x100000,0x100000,0x1e6d0,0x1e6a0,0x100000,0x100000,0x1e6d2,0x100000,0x100000,0x1e920,0x1e6d4,0x100000,0x100000,0x100000,0x1e6d6,0x100000,0x100000,0x100000,0x1e6e0,0x1e6a0,0x100000,0x100000,0x1e6e2,0x100000,0x100000,0x1e920,0x1e6e4,0x100000,0x100000,0x100000,0x1e6e6,0x100000,0x100000,0x100000,0x1e6f0,0x1e6a0,0x100000,0x100000,0x1e6f2,0x100000,0x100000,0x1e920,0x1e6f4,0x100000,0x100000,0x100000,0x1e6f6,0x100000,0x100000,0x100000,0x1e6c8,0x1e6b0,0x1e950,0x100000,0x1e6ca,0x100000,0x100000,0x1e950,0x1e6cc,0x100000,0x100000,0x100000,0x1e6ce,0x100000,0x100000,0x100000,0x1e6d8,0x1e6b0,0x100000,0x100000,0x1e6da,0x100000,0x100000,0x1e950,0x1e6dc,0x100000,0x100000,0x100000,0x1e6de,0x100000,0x100000,0x100000,0x1e6e8,0x1e6b0,0x100000,0x100000,0x1e6ea,0x100000,0x100000,0x1e950,0x1e6ec,0x100000,0x100000,0x100000,0x1e6ee,0x100000,0x100000,0x100000,0x1e6f8,0x1e6b0,0x100000,0x100000,0x1e6fa,0x100000,0x100000,0x1e950,0x1e6fc,0x100000,0x100000,0x100000,0x1e6fe,0x100000,0x100000,0x100000,0x1eb60,0x251f0,36960,37024,0x1eb62,0x251f2,36976,37032,0x1eb64,0x251f4,36992,37040,0x1eb66,0x251f6,37008,37048,0x1eb68,0x25350,37344,37408,0x1eb6a,0x25352,37360,37416,0x1eb6c,0x25354,37376,37424,0x1eb6e,0x25356,37392,37432,0x1eb70,0x254b0,37696,37760,0x1eb72,0x254b2,37712,37768,0x1eb74,0x254b4,37792,37776,0x1eb76,0x254b6,37808,37784,0x1eb78,0x25610,38080,38144,0x1eb7a,0x25612,38096,38152,0x1eb7c,0x25614,38176,38160,0x1eb7e,0x25616,38192,38168,0x1eb60,0x251f8,38432,38624,0x1eb62,0x251fa,38448,38632,0x1eb64,0x251fc,38592,38640,0x1eb66,0x251fe,38608,38648,0x1eb68,0x25358,38816,39008,0x1eb6a,0x2535a,38832,39016,0x1eb6c,0x2535c,38976,39024,0x1eb6e,0x2535e,38992,39032,0x1eb70,0x254b8,39168,39360,0x1eb72,0x254ba,39184,39368,0x1eb74,0x254bc,39392,39376,0x1eb76,0x254be,39408,39384,0x1eb78,0x25618,39552,39744,0x1eb7a,0x2561a,39568,39752,0x1eb7c,0x2561c,39776,39760,0x1eb7e,0x2561e,39792,39768,0x1eb60,0x26278,40032,40096,0x1eb62,0x2627a,40048,40104,0x1eb64,0x2627c,40064,40112,0x1eb66,0x2627e,40080,40120,0x1eb68,0x263d8,40416,40480,0x1eb6a,0x263da,40432,40488,0x1eb6c,0x263dc,40448,40496,0x1eb6e,0x263de,40464,40504,0x1eb70,0x26538,40768,40832,0x1eb72,0x2653a,40784,40840,0x1eb74,0x2653c,40864,40848,0x1eb76,0x2653e,40880,40856,0x1eb78,0x26698,41152,41216,0x1eb7a,0x2669a,41168,41224,0x1eb7c,0x2669c,41248,41232,0x1eb7e,0x2669e,41264,41240,0x1eb60,0x100000,41504,41696,0x1eb62,0x100000,41520,41704,0x1eb64,0x100000,41664,41712,0x1eb66,0x100000,41680,41720,0x1eb68,0x100000,41888,42080,0x1eb6a,0x100000,41904,42088,0x1eb6c,0x100000,42048,42096,0x1eb6e,0x100000,42064,42104,0x1eb70,0x100000,42240,42432,0x1eb72,0x100000,42256,42440,0x1eb74,0x100000,42464,42448,0x1eb76,0x100000,42480,42456,0x1eb78,0x100000,42624,42816,0x1eb7a,0x100000,42640,42824,0x1eb7c,0x100000,42848,42832,0x1eb7e,0x100000,42864,42840,36960,36992,37024,46080,36968,37000,37032,46096,36976,37008,37040,46112,36984,37016,37048,46128,38112,38144,38176,46144,38120,38152,38184,46160,38128,38160,38192,46176,38136,38168,38200,46192,39232,39328,39296,46208,39240,39336,39304,46224,39248,39344,39312,46240,39256,39352,39320,46256,40384,40480,40448,46272,40392,40488,40456,46288,40400,40496,40464,46304,40408,40504,40472,46320,46112,46272,46304,46336,46120,46280,46312,46352,46128,46288,46320,46368,46136,46296,46328,46384,47264,47424,47456,46400,47272,47432,47464,46416,47280,47440,47472,46432,47288,47448,47480,46448,48384,48608,48576,46464,48392,48616,48584,46480,48400,48624,48592,46496,48408,48632,48600,46512,49536,49760,49728,46528,49544,49768,49736,46544,49552,49776,49744,46560,49560,49784,49752,46576,55392,55424,55456,46592,55400,55432,55464,46608,55408,55440,55472,46624,55416,55448,55480,46640,56544,56576,56608,46656,56552,56584,56616,46672,56560,56592,56624,46688,56568,56600,56632,46704,57664,57760,57728,46720,57672,57768,57736,46736,57680,57776,57744,46752,57688,57784,57752,46768,58816,58912,58880,46784,58824,58920,58888,46800,58832,58928,58896,46816,58840,58936,58904,46832,64544,64704,64736,46848,64552,64712,64744,46864,64560,64720,64752,46880,64568,64728,64760,46896,65696,65856,65888,46912,65704,65864,65896,46928,65712,65872,65904,46944,65720,65880,65912,46960,66816,67040,67008,46976,66824,67048,67016,46992,66832,67056,67024,47008,66840,67064,67032,47024,67968,68192,68160,47040,67976,68200,68168,47056,67984,68208,68176,47072,67992,68216,68184,47088,0x1e800,0x1e800,0x20cc0,18432,0x1e8c4,0x1e8c4,0x20cc4,18448,0x1e988,0x1e988,0x20cc8,18464,0x1ea4c,0x1ea4c,0x20ccc,18480,0x1eb10,0x1eb10,0x20cd0,18496,0x1ebd4,0x1ebd4,0x20cd4,18512,0x1ec98,0x1ec98,0x20cd8,18528,0x1ed5c,0x1ed5c,0x20cdc,18544,0x1ee20,0x1ee20,0x20ce0,18816,0x1eee4,0x1eee4,0x20ce4,18832,0x1efa8,0x1efa8,0x20ce8,18848,0x1f06c,0x1f06c,0x20cec,18864,0x1f130,0x1f130,0x20cf0,18880,0x1f1f4,0x1f1f4,0x20cf4,18896,0x1f2b8,0x1f2b8,0x20cf8,18912,0x1f37c,0x1f37c,0x20cfc,18928,0x1e810,0x1e800,0x20cc0,19200,0x1e8d4,0x1e8c4,0x20cc4,19216,0x1e998,0x1e988,0x20cc8,19232,0x1ea5c,0x1ea4c,0x20ccc,19248,0x1eb20,0x1eb10,0x20cd0,19264,0x1ebe4,0x1ebd4,0x20cd4,19280,0x1eca8,0x1ec98,0x20cd8,19296,0x1ed6c,0x1ed5c,0x20cdc,19312,0x1ee30,0x1ee20,0x20ce0,19584,0x1eef4,0x1eee4,0x20ce4,19600,0x1efb8,0x1efa8,0x20ce8,19616,0x1f07c,0x1f06c,0x20cec,19632,0x1f140,0x1f130,0x20cf0,19648,0x1f204,0x1f1f4,0x20cf4,19664,0x1f2c8,0x1f2b8,0x20cf8,19680,0x1f38c,0x1f37c,0x20cfc,19696,0x1e820,0x1e800,0x20cc0,19968,0x1e8e4,0x1e8c4,0x20cc4,19984,0x1e9a8,0x1e988,0x20cc8,20000,0x1ea6c,0x1ea4c,0x20ccc,20016,0x1eb30,0x1eb10,0x20cd0,20032,0x1ebf4,0x1ebd4,0x20cd4,20048,0x1ecb8,0x1ec98,0x20cd8,20064,0x1ed7c,0x1ed5c,0x20cdc,20080,0x1ee40,0x1ee20,0x20ce0,20352,0x1ef04,0x1eee4,0x20ce4,20368,0x1efc8,0x1efa8,0x20ce8,20384,0x1f08c,0x1f06c,0x20cec,20400,0x1f150,0x1f130,0x20cf0,20416,0x1f214,0x1f1f4,0x20cf4,20432,0x1f2d8,0x1f2b8,0x20cf8,20448,0x1f39c,0x1f37c,0x20cfc,20464,0x1e830,0x1e800,0x20cc0,20736,0x1e8f4,0x1e8c4,0x20cc4,20752,0x1e9b8,0x1e988,0x20cc8,20768,0x1ea7c,0x1ea4c,0x20ccc,20784,0x1eb40,0x1eb10,0x20cd0,20800,0x1ec04,0x1ebd4,0x20cd4,20816,0x1ecc8,0x1ec98,0x20cd8,20832,0x1ed8c,0x1ed5c,0x20cdc,20848,0x1ee50,0x1ee20,0x20ce0,21120,0x1ef14,0x1eee4,0x20ce4,21136,0x1efd8,0x1efa8,0x20ce8,21152,0x1f09c,0x1f06c,0x20cec,21168,0x1f160,0x1f130,0x20cf0,21184,0x1f224,0x1f1f4,0x20cf4,21200,0x1f2e8,0x1f2b8,0x20cf8,21216,0x1f3ac,0x1f37c,0x20cfc,21232,55296,0x26e70,0x1e8c0,0x1eb60,55312,0x26e74,0x1ed50,0x1eff0,55328,0x26e78,0x1f1e0,0x1f480,55344,0x26e7c,0x1f670,0x1f910,55360,0x26e80,0x1fb00,0x1fda0,55376,0x26e84,0x1ff90,0x20230,55392,0x26e88,0x20420,0x206c0,55408,0x26e8c,0x208b0,0x20b50,55680,0x26e90,0x20d40,0x20fe0,55696,0x26e94,0x211d0,0x21470,55712,0x26e98,0x21660,0x21900,55728,0x26e9c,0x21af0,0x21d90,55744,0x26ea0,0x21f80,0x22220,55760,0x26ea4,0x22410,0x226b0,55776,0x26ea8,0x228a0,0x22b40,55792,0x26eac,0x22d30,0x22fd0,56096,0x26eb0,0x1e8f0,0x1eb68,56112,0x26eb4,0x1ed80,0x1eff8,56064,0x26eb8,0x1f210,0x1f488,56080,0x26ebc,0x1f6a0,0x1f918,56160,0x26ec0,0x1fb30,0x1fda8,56176,0x26ec4,0x1ffc0,0x20238,56128,0x26ec8,0x20450,0x206c8,56144,0x26ecc,0x208e0,0x20b58,56480,0x26ed0,0x20d70,0x20fe8,56496,0x26ed4,0x21200,0x21478,56448,0x26ed8,0x21690,0x21908,56464,0x26edc,0x21b20,0x21d98,56544,0x26ee0,0x21fb0,0x22228,56560,0x26ee4,0x22440,0x226b8,56512,0x26ee8,0x228d0,0x22b48,56528,0x26eec,0x22d60,0x22fd8,56896,0x26ef0,0x1e920,0x1eb70,56912,0x26ef4,0x1edb0,0x1f000,56928,0x26ef8,0x1f240,0x1f490,56944,0x26efc,0x1f6d0,0x1f920,56832,0x26f00,0x1fb60,0x1fdb0,56848,0x26f04,0x1fff0,0x20240,56864,0x26f08,0x20480,0x206d0,56880,0x26f0c,0x20910,0x20b60,57280,0x26f10,0x20da0,0x20ff0,57296,0x26f14,0x21230,0x21480,57312,0x26f18,0x216c0,0x21910,57328,0x26f1c,0x21b50,0x21da0,57216,0x26f20,0x21fe0,0x22230,57232,0x26f24,0x22470,0x226c0,57248,0x26f28,0x22900,0x22b50,57264,0x26f2c,0x22d90,0x22fe0,57696,0x26f30,0x1e950,0x1eb78,57712,0x26f34,0x1ede0,0x1f008,57664,0x26f38,0x1f270,0x1f498,57680,0x26f3c,0x1f700,0x1f928,57632,0x26f40,0x1fb90,0x1fdb8,57648,0x26f44,0x20020,0x20248,57600,0x26f48,0x204b0,0x206d8,57616,0x26f4c,0x20940,0x20b68,58080,0x26f50,0x20dd0,0x20ff8,58096,0x26f54,0x21260,0x21488,58048,0x26f58,0x216f0,0x21918,58064,0x26f5c,0x21b80,0x21da8,58016,0x26f60,0x22010,0x22238,58032,0x26f64,0x224a0,0x226c8,57984,0x26f68,0x22930,0x22b58,58000,0x26f6c,0x22dc0,0x22fe8,0x3f803f80,0x3f803f80,16256,0,0,0x3f803f80,0x3f800000,0,0,0x3f803f80,0,16256,0,0x3f803f80,0,0x3f800000,0x3f803f80,0,0,0,0,0,0,0,0,0,0,0,0,0,0,0,0x3f803f80,0,0,0,0,0,0,0,0,0,0,0,0,0,0,0,0x3f803f80,0,0,0,0,0,0,0,0,0,0,0,0,0,0,0,0x3f803f80,0,0,0,0,0,0,0,0,0,0,0,0,0,0,0,0x3f803f80,0x3f803f80,16256,0,0,0x3f803f80,0x3f800000,0,0,0x3f803f80,0,16256,0,0x3f803f80,0,0x3f800000,0x3f803f80,0,0,0,0,0,0,0,0,0,0,0,0,0,0,0,0x3f803f80,0,0,0,0,0,0,0,0,0,0,0,0,0,0,0,0x3f803f80,0,0,0,0,0,0,0,0,0,0,0,0,0,0,0,0x3f803f80,0,0,0,0,0,0,0,0,0,0,0,0,0,0,0,0x3f803f80,0x3f803f80,16256,0,0,0x3f803f80,0x3f800000,0,0,0x3f803f80,0,16256,0,0x3f803f80,0,0x3f800000,0x3f803f80,0,0,0,0,0,0,0,0,0,0,0,0,0,0,0,0x3f803f80,0,0,0,0,0,0,0,0,0,0,0,0,0,0,0,0x3f803f80,0,0,0,0,0,0,0,0,0,0,0,0,0,0,0,0x3f803f80,0,0,0,0,0,0,0,0,0,0,0,0,0,0,0,0x3f803f80,0x3f803f80,16256,0,0,0x3f803f80,0x3f800000,0,0,0x3f803f80,0,16256,0,0x3f803f80,0,0x3f800000,0,64,0x60000,0xc0080,144,208,0x60010,0xc0090,288,352,0x60020,0xc00a0,432,496,0x60030,0xc00b0,576,512,0x60040,0xc00c0,720,656,0x60050,0xc00d0,864,800,0x60060,0xc00e0,1008,944,0x60070,0xc00f0,1024,1088,0x60190,0xc0210,1168,1232,0x60180,0xc0200,1312,1376,0x601b0,0xc0230,1456,1520,0x601a0,0xc0220,1600,1536,0x601d0,0xc0250,1744,1680,0x601c0,0xc0240,1888,1824,0x601f0,0xc0270,2032,1968,0x601e0,0xc0260,16,80,0x60320,0xc03a0,128,192,0x60330,0xc03b0,304,368,0x60300,0xc0380,416,480,0x60310,0xc0390,592,528,0x60360,0xc03e0,704,640,0x60370,0xc03f0,880,816,0x60340,0xc03c0,992,928,0x60350,0xc03d0,1040,1104,0x604b0,0xef930,1152,1216,0x604a0,0xef920,1328,1392,0x60490,0xef910,1440,1504,0x60480,0xef900,1616,1552,0x604f0,0xef970,1728,1664,0x604e0,0xef960,1904,1840,0x604d0,0xef950,2016,1952,0x604c0,0xef940,32,96,0x60640,0xefac0,176,240,0x60650,0xefad0,256,320,0x60660,0xefae0,400,464,0x60670,0xefaf0,608,544,0x60600,0xefa80,752,688,0x60610,0xefa90,832,768,0x60620,0xefaa0,976,912,0x60630,0xefab0,1056,1120,0x8fbd0,0xefc50,1200,1264,0x8fbc0,0xefc40,1280,1344,0x8fbf0,0xefc70,1424,1488,0x8fbe0,0xefc60,1632,1568,0x8fb90,0xefc10,1776,1712,0x8fb80,0xefc00,1856,1792,0x8fbb0,0xefc30,2000,1936,0x8fba0,0xefc20,48,112,0x8fd60,0xefde0,160,224,0x8fd70,0xefdf0,272,336,0x8fd40,0xefdc0,384,448,0x8fd50,0xefdd0,624,560,0x8fd20,0xefda0,736,672,0x8fd30,0xefdb0,848,784,0x8fd00,0xefd80,960,896,0x8fd10,0xefd90,1072,1136,0x8fef0,0xeff70,1184,1248,0x8fee0,0xeff60,1296,1360,0x8fed0,0xeff50,1408,1472,0x8fec0,0xeff40,1648,1584,0x8feb0,0xeff30,1760,1696,0x8fea0,0xeff20,1872,1808,0x8fe90,0xeff10,1984,1920,0x8fe80,0xeff00,0xf0100,75776,83968,91136,0xf0110,75792,83984,91152,0xf0120,75808,84000,91168,0xf0130,75824,84016,91184,0xf0140,75840,84032,91200,0xf0150,75856,84048,91216,0xf0160,75872,84064,91232,0xf0170,75888,84080,91248,0xf0290,75904,84096,91264,0xf0280,75920,84112,91280,0xf02b0,75936,84128,91296,0xf02a0,75952,84144,91312,0xf02d0,75968,84160,91328,0xf02c0,75984,84176,91344,0xf02f0,76000,84192,91360,0xf02e0,76016,84208,91376,0x11f820,76032,84224,91392,0x11f830,76048,84240,91408,0x11f800,76064,84256,91424,0x11f810,76080,84272,91440,0x11f860,76096,84288,91456,0x11f870,76112,84304,91472,0x11f840,76128,84320,91488,0x11f850,76144,84336,91504,0x11f9b0,76160,84352,91520,0x11f9a0,76176,84368,91536,0x11f990,76192,84384,91552,0x11f980,76208,84400,91568,0x11f9f0,76224,84416,91584,0x11f9e0,76240,84432,91600,0x11f9d0,76256,84448,91616,0x11f9c0,76272,84464,91632,0xf0100,76288,84480,91648,0xf0110,76304,84496,91664,0xf0120,76320,84512,91680,0xf0130,76336,84528,91696,0xf0140,76352,84544,91712,0xf0150,76368,84560,91728,0xf0160,76384,84576,91744,0xf0170,76400,84592,91760,0xf0290,76416,84608,91776,0xf0280,76432,84624,91792,0xf02b0,76448,84640,91808,0xf02a0,76464,84656,91824,0xf02d0,76480,84672,91840,0xf02c0,76496,84688,91856,0xf02f0,76512,84704,91872,0xf02e0,76528,84720,91888,0x11f820,76544,84736,91904,0x11f830,76560,84752,91920,0x11f800,76576,84768,91936,0x11f810,76592,84784,91952,0x11f860,76608,84800,91968,0x11f870,76624,84816,91984,0x11f840,76640,84832,92000,0x11f850,76656,84848,92016,0x11f9b0,76672,84864,92032,0x11f9a0,76688,84880,92048,0x11f990,76704,84896,92064,0x11f980,76720,84912,92080,0x11f9f0,76736,84928,92096,0x11f9e0,76752,84944,92112,0x11f9d0,76768,84960,92128,0x11f9c0,76784,84976,92144,0x1c140,36864,43008,52224,0x1c144,36880,43024,52240,0x1c148,36896,43040,52256,0x1c14c,36912,43056,52272,0x1c150,36928,43072,52288,0x1c154,36944,43088,52304,0x1c158,36960,43104,52320,0x1c15c,36976,43120,52336,0x1c160,36992,43136,52352,0x1c164,37008,43152,52368,0x1c168,37024,43168,52384,0x1c16c,37040,43184,52400,0x1c170,37056,43200,52416,0x1c174,37072,43216,52432,0x1c178,37088,43232,52448,0x1c17c,37104,43248,52464,0x1c180,37632,43776,52992,0x1c184,37648,43792,53008,0x1c188,37664,43808,53024,0x1c18c,37680,43824,53040,0x1c190,37696,43840,53056,0x1c194,37712,43856,53072,0x1c198,37728,43872,53088,0x1c19c,37744,43888,53104,0x1c1a0,37760,43904,53120,0x1c1a4,37776,43920,53136,0x1c1a8,37792,43936,53152,0x1c1ac,37808,43952,53168,0x1c1b0,37824,43968,53184,0x1c1b4,37840,43984,53200,0x1c1b8,37856,44000,53216,0x1c1bc,37872,44016,53232,0x1c1c0,38400,44544,53760,0x1c1c4,38416,44560,53776,0x1c1c8,38432,44576,53792,0x1c1cc,38448,44592,53808,0x1c1d0,38464,44608,53824,0x1c1d4,38480,44624,53840,0x1c1d8,38496,44640,53856,0x1c1dc,38512,44656,53872,0x1c1e0,38528,44672,53888,0x1c1e4,38544,44688,53904,0x1c1e8,38560,44704,53920,0x1c1ec,38576,44720,53936,0x1c1f0,38592,44736,53952,0x1c1f4,38608,44752,53968,0x1c1f8,38624,44768,53984,0x1c1fc,38640,44784,54000,0x1c200,39168,45312,54528,0x1c204,39184,45328,54544,0x1c208,39200,45344,54560,0x1c20c,39216,45360,54576,0x1c210,39232,45376,54592,0x1c214,39248,45392,54608,0x1c218,39264,45408,54624,0x1c21c,39280,45424,54640,0x1c220,39296,45440,54656,0x1c224,39312,45456,54672,0x1c228,39328,45472,54688,0x1c22c,39344,45488,54704,0x1c230,39360,45504,54720,0x1c234,39376,45520,54736,0x1c238,39392,45536,54752,0x1c23c,39408,45552,54768,79936,86016,0x1d600,0x1d608,80080,86160,0x1d610,0x1d618,80224,86304,0x1d620,0x1d628,80368,86448,0x1d630,0x1d638,80384,86592,0x1d640,0x1d648,80528,86736,0x1d650,0x1d658,80672,86880,0x1d660,0x1d668,80816,87024,0x1d670,0x1d678,80960,87040,0x1d680,0x1d688,81104,87184,0x1d690,0x1d698,81248,87328,0x1d6a0,0x1d6a8,81392,87472,0x1d6b0,0x1d6b8,81408,87616,0x1d6c0,0x1d6c8,81552,87760,0x1d6d0,0x1d6d8,81696,87904,0x1d6e0,0x1d6e8,81840,88048,0x1d6f0,0x1d6f8,79952,86024,0x1d700,0x1d708,80064,86168,0x1d710,0x1d718,80240,86312,0x1d720,0x1d728,80352,86456,0x1d730,0x1d738,80400,86600,0x1d740,0x1d748,80512,86744,0x1d750,0x1d758,80688,86888,0x1d760,0x1d768,80800,87032,0x1d770,0x1d778,80976,87048,0x1d780,0x1d788,81088,87192,0x1d790,0x1d798,81264,87336,0x1d7a0,0x1d7a8,81376,87480,0x1d7b0,0x1d7b8,81424,87624,0x1d7c0,0x1d7c8,81536,87768,0x1d7d0,0x1d7d8,81712,87912,0x1d7e0,0x1d7e8,81824,88056,0x1d7f0,0x1d7f8,79968,86032,0x1d800,0x1d808,80112,86144,0x1d810,0x1d818,80192,86320,0x1d820,0x1d828,80336,86432,0x1d830,0x1d838,80416,86608,0x1d840,0x1d848,80560,86720,0x1d850,0x1d858,80640,86896,0x1d860,0x1d868,80784,87008,0x1d870,0x1d878,80992,87056,0x1d880,0x1d888,81136,87168,0x1d890,0x1d898,81216,87344,0x1d8a0,0x1d8a8,81360,87456,0x1d8b0,0x1d8b8,81440,87632,0x1d8c0,0x1d8c8,81584,87744,0x1d8d0,0x1d8d8,81664,87920,0x1d8e0,0x1d8e8,81808,88032,0x1d8f0,0x1d8f8,79984,86040,0x1d900,0x1d908,80096,86152,0x1d910,0x1d918,80208,86328,0x1d920,0x1d928,80320,86440,0x1d930,0x1d938,80432,86616,0x1d940,0x1d948,80544,86728,0x1d950,0x1d958,80656,86904,0x1d960,0x1d968,80768,87016,0x1d970,0x1d978,81008,87064,0x1d980,0x1d988,81120,87176,0x1d990,0x1d998,81232,87352,0x1d9a0,0x1d9a8,81344,87464,0x1d9b0,0x1d9b8,81456,87640,0x1d9c0,0x1d9c8,81568,87752,0x1d9d0,0x1d9d8,81680,87928,0x1d9e0,0x1d9e8,81792,88040,0x1d9f0,0x1d9f8,0x1e700,0x1e700,0x1e980,0x100000,0x1e702,0x100000,0x100000,0x1e980,0x1e704,0x100000,0x100000,0x100000,0x1e706,0x100000,0x100000,0x100000,0x1e710,0x1e700,0x100000,0x100000,0x1e712,0x100000,0x100000,0x1e980,0x1e714,0x100000,0x100000,0x100000,0x1e716,0x100000,0x100000,0x100000,0x1e720,0x1e700,0x100000,0x100000,0x1e722,0x100000,0x100000,0x1e980,0x1e724,0x100000,0x100000,0x100000,0x1e726,0x100000,0x100000,0x100000,0x1e730,0x1e700,0x100000,0x100000,0x1e732,0x100000,0x100000,0x1e980,0x1e734,0x100000,0x100000,0x100000,0x1e736,0x100000,0x100000,0x100000,0x1e708,0x1e710,0x1e9b0,0x100000,0x1e70a,0x100000,0x100000,0x1e9b0,0x1e70c,0x100000,0x100000,0x100000,0x1e70e,0x100000,0x100000,0x100000,0x1e718,0x1e710,0x100000,0x100000,0x1e71a,0x100000,0x100000,0x1e9b0,0x1e71c,0x100000,0x100000,0x100000,0x1e71e,0x100000,0x100000,0x100000,0x1e728,0x1e710,0x100000,0x100000,0x1e72a,0x100000,0x100000,0x1e9b0,0x1e72c,0x100000,0x100000,0x100000,0x1e72e,0x100000,0x100000,0x100000,0x1e738,0x1e710,0x100000,0x100000,0x1e73a,0x100000,0x100000,0x1e9b0,0x1e73c,0x100000,0x100000,0x100000,0x1e73e,0x100000,0x100000,0x100000,0x1e740,0x1e720,0x1e9e0,0x100000,0x1e742,0x100000,0x100000,0x1e9e0,0x1e744,0x100000,0x100000,0x100000,0x1e746,0x100000,0x100000,0x100000,0x1e750,0x1e720,0x100000,0x100000,0x1e752,0x100000,0x100000,0x1e9e0,0x1e754,0x100000,0x100000,0x100000,0x1e756,0x100000,0x100000,0x100000,0x1e760,0x1e720,0x100000,0x100000,0x1e762,0x100000,0x100000,0x1e9e0,0x1e764,0x100000,0x100000,0x100000,0x1e766,0x100000,0x100000,0x100000,0x1e770,0x1e720,0x100000,0x100000,0x1e772,0x100000,0x100000,0x1e9e0,0x1e774,0x100000,0x100000,0x100000,0x1e776,0x100000,0x100000,0x100000,0x1e748,0x1e730,0x1ea10,0x100000,0x1e74a,0x100000,0x100000,0x1ea10,0x1e74c,0x100000,0x100000,0x100000,0x1e74e,0x100000,0x100000,0x100000,0x1e758,0x1e730,0x100000,0x100000,0x1e75a,0x100000,0x100000,0x1ea10,0x1e75c,0x100000,0x100000,0x100000,0x1e75e,0x100000,0x100000,0x100000,0x1e768,0x1e730,0x100000,0x100000,0x1e76a,0x100000,0x100000,0x1ea10,0x1e76c,0x100000,0x100000,0x100000,0x1e76e,0x100000,0x100000,0x100000,0x1e778,0x1e730,0x100000,0x100000,0x1e77a,0x100000,0x100000,0x1ea10,0x1e77c,0x100000,0x100000,0x100000,0x1e77e,0x100000,0x100000,0x100000,0x1ebc0,0x25770,37056,37120,0x1ebc2,0x25772,37072,37128,0x1ebc4,0x25774,37088,37136,0x1ebc6,0x25776,37104,37144,0x1ebc8,0x258d0,37440,37504,0x1ebca,0x258d2,37456,37512,0x1ebcc,0x258d4,37472,37520,0x1ebce,0x258d6,37488,37528,0x1ebd0,0x25a30,37856,37920,0x1ebd2,0x25a32,37872,37928,0x1ebd4,0x25a34,37824,37936,0x1ebd6,0x25a36,37840,37944,0x1ebd8,0x25b90,38240,38304,0x1ebda,0x25b92,38256,38312,0x1ebdc,0x25b94,38208,38320,0x1ebde,0x25b96,38224,38328,0x1ebc0,0x25778,38528,38720,0x1ebc2,0x2577a,38544,38728,0x1ebc4,0x2577c,38560,38736,0x1ebc6,0x2577e,38576,38744,0x1ebc8,0x258d8,38912,39104,0x1ebca,0x258da,38928,39112,0x1ebcc,0x258dc,38944,39120,0x1ebce,0x258de,38960,39128,0x1ebd0,0x25a38,39328,39520,0x1ebd2,0x25a3a,39344,39528,0x1ebd4,0x25a3c,39296,39536,0x1ebd6,0x25a3e,39312,39544,0x1ebd8,0x25b98,39712,39904,0x1ebda,0x25b9a,39728,39912,0x1ebdc,0x25b9c,39680,39920,0x1ebde,0x25b9e,39696,39928,0x1ebc0,0x267f0,40128,40192,0x1ebc2,0x267f2,40144,40200,0x1ebc4,0x267f4,40160,40208,0x1ebc6,0x267f6,40176,40216,0x1ebc8,0x26950,40512,40576,0x1ebca,0x26952,40528,40584,0x1ebcc,0x26954,40544,40592,0x1ebce,0x26956,40560,40600,0x1ebd0,0x26ab0,40928,40992,0x1ebd2,0x26ab2,40944,41000,0x1ebd4,0x26ab4,40896,41008,0x1ebd6,0x26ab6,40912,41016,0x1ebd8,0x26c10,41312,41376,0x1ebda,0x26c12,41328,41384,0x1ebdc,0x26c14,41280,41392,0x1ebde,0x26c16,41296,41400,0x1ebc0,0x100000,41600,41792,0x1ebc2,0x100000,41616,41800,0x1ebc4,0x100000,41632,41808,0x1ebc6,0x100000,41648,41816,0x1ebc8,0x100000,41984,42176,0x1ebca,0x100000,42000,42184,0x1ebcc,0x100000,42016,42192,0x1ebce,0x100000,42032,42200,0x1ebd0,0x100000,42400,42592,0x1ebd2,0x100000,42416,42600,0x1ebd4,0x100000,42368,42608,0x1ebd6,0x100000,42384,42616,0x1ebd8,0x100000,42784,42976,0x1ebda,0x100000,42800,42984,0x1ebdc,0x100000,42752,42992,0x1ebde,0x100000,42768,43000,37056,37088,37120,55296,37064,37096,37128,55312,37072,37104,37136,55328,37080,37112,37144,55344,38208,38240,38272,55360,38216,38248,38280,55376,38224,38256,38288,55392,38232,38264,38296,55408,39392,39360,39456,55424,39400,39368,39464,55440,39408,39376,39472,55456,39416,39384,39480,55472,40544,40512,40608,55488,40552,40520,40616,55504,40560,40528,40624,55520,40568,40536,40632,55536,46208,46240,46400,55552,46216,46248,46408,55568,46224,46256,46416,55584,46232,46264,46424,55600,47360,47392,47552,55616,47368,47400,47560,55632,47376,47408,47568,55648,47384,47416,47576,55664,48544,48512,48736,55680,48552,48520,48744,55696,48560,48528,48752,55712,48568,48536,48760,55728,49696,49664,49888,55744,49704,49672,49896,55760,49712,49680,49904,55776,49720,49688,49912,55792,55488,55520,55552,55808,55496,55528,55560,55824,55504,55536,55568,55840,55512,55544,55576,55856,56640,56672,56704,55872,56648,56680,56712,55888,56656,56688,56720,55904,56664,56696,56728,55920,57824,57792,57888,55936,57832,57800,57896,55952,57840,57808,57904,55968,57848,57816,57912,55984,58976,58944,59040,56000,58984,58952,59048,56016,58992,58960,59056,56032,59000,58968,59064,56048,64640,64672,64832,56064,64648,64680,64840,56080,64656,64688,64848,56096,64664,64696,64856,56112,65792,65824,65984,56128,65800,65832,65992,56144,65808,65840,66000,56160,65816,65848,66008,56176,66976,66944,67168,56192,66984,66952,67176,56208,66992,66960,67184,56224,67000,66968,67192,56240,68128,68096,68320,56256,68136,68104,68328,56272,68144,68112,68336,56288,68152,68120,68344,56304,0x1e800,0x1e800,0x20cc0,36864,0x1e8c4,0x1e8c4,0x20cc4,36880,0x1e988,0x1e988,0x20cc8,36896,0x1ea4c,0x1ea4c,0x20ccc,36912,0x1eb10,0x1eb10,0x20cd0,36928,0x1ebd4,0x1ebd4,0x20cd4,36944,0x1ec98,0x1ec98,0x20cd8,36960,0x1ed5c,0x1ed5c,0x20cdc,36976,0x1ee20,0x1ee20,0x20ce0,37248,0x1eee4,0x1eee4,0x20ce4,37264,0x1efa8,0x1efa8,0x20ce8,37280,0x1f06c,0x1f06c,0x20cec,37296,0x1f130,0x1f130,0x20cf0,37312,0x1f1f4,0x1f1f4,0x20cf4,37328,0x1f2b8,0x1f2b8,0x20cf8,37344,0x1f37c,0x1f37c,0x20cfc,37360,0x1e810,0x1e800,0x20cc0,37632,0x1e8d4,0x1e8c4,0x20cc4,37648,0x1e998,0x1e988,0x20cc8,37664,0x1ea5c,0x1ea4c,0x20ccc,37680,0x1eb20,0x1eb10,0x20cd0,37696,0x1ebe4,0x1ebd4,0x20cd4,37712,0x1eca8,0x1ec98,0x20cd8,37728,0x1ed6c,0x1ed5c,0x20cdc,37744,0x1ee30,0x1ee20,0x20ce0,38016,0x1eef4,0x1eee4,0x20ce4,38032,0x1efb8,0x1efa8,0x20ce8,38048,0x1f07c,0x1f06c,0x20cec,38064,0x1f140,0x1f130,0x20cf0,38080,0x1f204,0x1f1f4,0x20cf4,38096,0x1f2c8,0x1f2b8,0x20cf8,38112,0x1f38c,0x1f37c,0x20cfc,38128,0x1e820,0x1e800,0x20cc0,38400,0x1e8e4,0x1e8c4,0x20cc4,38416,0x1e9a8,0x1e988,0x20cc8,38432,0x1ea6c,0x1ea4c,0x20ccc,38448,0x1eb30,0x1eb10,0x20cd0,38464,0x1ebf4,0x1ebd4,0x20cd4,38480,0x1ecb8,0x1ec98,0x20cd8,38496,0x1ed7c,0x1ed5c,0x20cdc,38512,0x1ee40,0x1ee20,0x20ce0,38784,0x1ef04,0x1eee4,0x20ce4,38800,0x1efc8,0x1efa8,0x20ce8,38816,0x1f08c,0x1f06c,0x20cec,38832,0x1f150,0x1f130,0x20cf0,38848,0x1f214,0x1f1f4,0x20cf4,38864,0x1f2d8,0x1f2b8,0x20cf8,38880,0x1f39c,0x1f37c,0x20cfc,38896,0x1e830,0x1e800,0x20cc0,39168,0x1e8f4,0x1e8c4,0x20cc4,39184,0x1e9b8,0x1e988,0x20cc8,39200,0x1ea7c,0x1ea4c,0x20ccc,39216,0x1eb40,0x1eb10,0x20cd0,39232,0x1ec04,0x1ebd4,0x20cd4,39248,0x1ecc8,0x1ec98,0x20cd8,39264,0x1ed8c,0x1ed5c,0x20cdc,39280,0x1ee50,0x1ee20,0x20ce0,39552,0x1ef14,0x1eee4,0x20ce4,39568,0x1efd8,0x1efa8,0x20ce8,39584,0x1f09c,0x1f06c,0x20cec,39600,0x1f160,0x1f130,0x20cf0,39616,0x1f224,0x1f1f4,0x20cf4,39632,0x1f2e8,0x1f2b8,0x20cf8,39648,0x1f3ac,0x1f37c,0x20cfc,39664,73728,0x26f70,0x1e980,0x1ebc0,73744,0x26f74,0x1ee10,0x1f050,73760,0x26f78,0x1f2a0,0x1f4e0,73776,0x26f7c,0x1f730,0x1f970,73792,0x26f80,0x1fbc0,0x1fe00,73808,0x26f84,0x20050,0x20290,73824,0x26f88,0x204e0,0x20720,73840,0x26f8c,0x20970,0x20bb0,74112,0x26f90,0x20e00,0x21040,74128,0x26f94,0x21290,0x214d0,74144,0x26f98,0x21720,0x21960,74160,0x26f9c,0x21bb0,0x21df0,74176,0x26fa0,0x22040,0x22280,74192,0x26fa4,0x224d0,0x22710,74208,0x26fa8,0x22960,0x22ba0,74224,0x26fac,0x22df0,0x23030,74528,0x26fb0,0x1e9b0,0x1ebc8,74544,0x26fb4,0x1ee40,0x1f058,74496,0x26fb8,0x1f2d0,0x1f4e8,74512,0x26fbc,0x1f760,0x1f978,74592,0x26fc0,0x1fbf0,0x1fe08,74608,0x26fc4,0x20080,0x20298,74560,0x26fc8,0x20510,0x20728,74576,0x26fcc,0x209a0,0x20bb8,74912,0x26fd0,0x20e30,0x21048,74928,0x26fd4,0x212c0,0x214d8,74880,0x26fd8,0x21750,0x21968,74896,0x26fdc,0x21be0,0x21df8,74976,0x26fe0,0x22070,0x22288,74992,0x26fe4,0x22500,0x22718,74944,0x26fe8,0x22990,0x22ba8,74960,0x26fec,0x22e20,0x23038,75328,0x26ff0,0x1e9e0,0x1ebd0,75344,0x26ff4,0x1ee70,0x1f060,75360,0x26ff8,0x1f300,0x1f4f0,75376,0x26ffc,0x1f790,0x1f980,75264,0x27000,0x1fc20,0x1fe10,75280,0x27004,0x200b0,0x202a0,75296,0x27008,0x20540,0x20730,75312,0x2700c,0x209d0,0x20bc0,75712,0x27010,0x20e60,0x21050,75728,0x27014,0x212f0,0x214e0,75744,0x27018,0x21780,0x21970,75760,0x2701c,0x21c10,0x21e00,75648,0x27020,0x220a0,0x22290,75664,0x27024,0x22530,0x22720,75680,0x27028,0x229c0,0x22bb0,75696,0x2702c,0x22e50,0x23040,76128,0x27030,0x1ea10,0x1ebd8,76144,0x27034,0x1eea0,0x1f068,76096,0x27038,0x1f330,0x1f4f8,76112,0x2703c,0x1f7c0,0x1f988,76064,0x27040,0x1fc50,0x1fe18,76080,0x27044,0x200e0,0x202a8,76032,0x27048,0x20570,0x20738,76048,0x2704c,0x20a00,0x20bc8,76512,0x27050,0x20e90,0x21058,76528,0x27054,0x21320,0x214e8,76480,0x27058,0x217b0,0x21978,76496,0x2705c,0x21c40,0x21e08,76448,0x27060,0x220d0,0x22298,76464,0x27064,0x22560,0x22728,76416,0x27068,0x229f0,0x22bb8,76432,0x2706c,0x22e80,0x23048,0x3f803f80,0x3f803f80,16256,0,0,0x3f803f80,0x3f800000,0,0,0x3f803f80,0,16256,0,0x3f803f80,0,0x3f800000,0x3f803f80,0,0,0,0,0,0,0,0,0,0,0,0,0,0,0,0x3f803f80,0,0,0,0,0,0,0,0,0,0,0,0,0,0,0,0x3f803f80,0,0,0,0,0,0,0,0,0,0,0,0,0,0,0,0x3f803f80,0,0,0,0,0,0,0,0,0,0,0,0,0,0,0,0x3f803f80,0x3f803f80,16256,0,0,0x3f803f80,0x3f800000,0,0,0x3f803f80,0,16256,0,0x3f803f80,0,0x3f800000,0x3f803f80,0,0,0,0,0,0,0,0,0,0,0,0,0,0,0,0x3f803f80,0,0,0,0,0,0,0,0,0,0,0,0,0,0,0,0x3f803f80,0,0,0,0,0,0,0,0,0,0,0,0,0,0,0,0x3f803f80,0,0,0,0,0,0,0,0,0,0,0,0,0,0,0,0x3f803f80,0x3f803f80,16256,0,0,0x3f803f80,0x3f800000,0,0,0x3f803f80,0,16256,0,0x3f803f80,0,0x3f800000,0x3f803f80,0,0,0,0,0,0,0,0,0,0,0,0,0,0,0,0x3f803f80,0,0,0,0,0,0,0,0,0,0,0,0,0,0,0,0x3f803f80,0,0,0,0,0,0,0,0,0,0,0,0,0,0,0,0x3f803f80,0,0,0,0,0,0,0,0,0,0,0,0,0,0,0,0x3f803f80,0x3f803f80,16256,0,0,0x3f803f80,0x3f800000,0,0,0x3f803f80,0,16256,0,0x3f803f80,0,0x3f800000,0,64,0x90000,0xf0080,144,208,0x90010,0xf0090,288,352,0x90020,0xf00a0,432,496,0x90030,0xf00b0,576,512,0x90040,0xf00c0,720,656,0x90050,0xf00d0,864,800,0x90060,0xf00e0,1008,944,0x90070,0xf00f0,1024,1088,0x90190,0xf0210,1168,1232,0x90180,0xf0200,1312,1376,0x901b0,0xf0230,1456,1520,0x901a0,0xf0220,1600,1536,0x901d0,0xf0250,1744,1680,0x901c0,0xf0240,1888,1824,0x901f0,0xf0270,2032,1968,0x901e0,0xf0260,16,80,0x90320,0x11f7a0,128,192,0x90330,0x11f7b0,304,368,0x90300,0x11f780,416,480,0x90310,0x11f790,592,528,0x90360,0x11f7e0,704,640,0x90370,0x11f7f0,880,816,0x90340,0x11f7c0,992,928,0x90350,0x11f7d0,1040,1104,0x904b0,0x11f930,1152,1216,0x904a0,0x11f920,1328,1392,0x90490,0x11f910,1440,1504,0x90480,0x11f900,1616,1552,0x904f0,0x11f970,1728,1664,0x904e0,0x11f960,1904,1840,0x904d0,0x11f950,2016,1952,0x904c0,0x11f940,32,96,0xbfa40,0x11fac0,176,240,0xbfa50,0x11fad0,256,320,0xbfa60,0x11fae0,400,464,0xbfa70,0x11faf0,608,544,0xbfa00,0x11fa80,752,688,0xbfa10,0x11fa90,832,768,0xbfa20,0x11faa0,976,912,0xbfa30,0x11fab0,1056,1120,0xbfbd0,0x11fc50,1200,1264,0xbfbc0,0x11fc40,1280,1344,0xbfbf0,0x11fc70,1424,1488,0xbfbe0,0x11fc60,1632,1568,0xbfb90,0x11fc10,1776,1712,0xbfb80,0x11fc00,1856,1792,0xbfbb0,0x11fc30,2000,1936,0xbfba0,0x11fc20,48,112,0xbfd60,0x11fde0,160,224,0xbfd70,0x11fdf0,272,336,0xbfd40,0x11fdc0,384,448,0xbfd50,0x11fdd0,624,560,0xbfd20,0x11fda0,736,672,0xbfd30,0x11fdb0,848,784,0xbfd00,0x11fd80,960,896,0xbfd10,0x11fd90,1072,1136,0xbfef0,0x11ff70,1184,1248,0xbfee0,0x11ff60,1296,1360,0xbfed0,0x11ff50,1408,1472,0xbfec0,0x11ff40,1648,1584,0xbfeb0,0x11ff30,1760,1696,0xbfea0,0x11ff20,1872,1808,0xbfe90,0x11ff10,1984,1920,0xbfe80,0x11ff00,0x11fb40,76800,84992,91648,0x11fb50,76816,85008,91664,0x11fb60,76832,85024,91680,0x11fb70,76848,85040,91696,0x11fb00,76864,85056,91712,0x11fb10,76880,85072,91728,0x11fb20,76896,85088,91744,0x11fb30,76912,85104,91760,0x11fcd0,76928,85120,91776,0x11fcc0,76944,85136,91792,0x11fcf0,76960,85152,91808,0x11fce0,76976,85168,91824,0x11fc90,76992,85184,91840,0x11fc80,77008,85200,91856,0x11fcb0,77024,85216,91872,0x11fca0,77040,85232,91888,0x11fe60,77056,85248,91904,0x11fe70,77072,85264,91920,0x11fe40,77088,85280,91936,0x11fe50,77104,85296,91952,0x11fe20,77120,85312,91968,0x11fe30,77136,85328,91984,0x11fe00,77152,85344,92000,0x11fe10,77168,85360,92016,0x11fff0,77184,85376,92032,0x11ffe0,77200,85392,92048,0x11ffd0,77216,85408,92064,0x11ffc0,77232,85424,92080,0x11ffb0,77248,85440,92096,0x11ffa0,77264,85456,92112,0x11ff90,77280,85472,92128,0x11ff80,77296,85488,92144,0x11fb40,77312,85504,92160,0x11fb50,77328,85520,92176,0x11fb60,77344,85536,92192,0x11fb70,77360,85552,92208,0x11fb00,77376,85568,92224,0x11fb10,77392,85584,92240,0x11fb20,77408,85600,92256,0x11fb30,77424,85616,92272,0x11fcd0,77440,85632,92288,0x11fcc0,77456,85648,92304,0x11fcf0,77472,85664,92320,0x11fce0,77488,85680,92336,0x11fc90,77504,85696,92352,0x11fc80,77520,85712,92368,0x11fcb0,77536,85728,92384,0x11fca0,77552,85744,92400,0x11fe60,77568,85760,92416,0x11fe70,77584,85776,92432,0x11fe40,77600,85792,92448,0x11fe50,77616,85808,92464,0x11fe20,77632,85824,92480,0x11fe30,77648,85840,92496,0x11fe00,77664,85856,92512,0x11fe10,77680,85872,92528,0x11fff0,77696,85888,92544,0x11ffe0,77712,85904,92560,0x11ffd0,77728,85920,92576,0x11ffc0,77744,85936,92592,0x11ffb0,77760,85952,92608,0x11ffa0,77776,85968,92624,0x11ff90,77792,85984,92640,0x11ff80,77808,86000,92656,0x1c1d0,36864,46080,52232,0x1c1d4,36880,46096,52248,0x1c1d8,36896,46112,52264,0x1c1dc,36912,46128,52280,0x1c1e0,36928,46144,52296,0x1c1e4,36944,46160,52312,0x1c1e8,36960,46176,52328,0x1c1ec,36976,46192,52344,0x1c1f0,36992,46208,52360,0x1c1f4,37008,46224,52376,0x1c1f8,37024,46240,52392,0x1c1fc,37040,46256,52408,0x1c200,37056,46272,52424,0x1c204,37072,46288,52440,0x1c208,37088,46304,52456,0x1c20c,37104,46320,52472,0x1c210,37632,46848,53000,0x1c214,37648,46864,53016,0x1c218,37664,46880,53032,0x1c21c,37680,46896,53048,0x1c220,37696,46912,53064,0x1c224,37712,46928,53080,0x1c228,37728,46944,53096,0x1c22c,37744,46960,53112,0x1c230,37760,46976,53128,0x1c234,37776,46992,53144,0x1c238,37792,47008,53160,0x1c23c,37808,47024,53176,0x1c240,37824,47040,53192,0x1c244,37840,47056,53208,0x1c248,37856,47072,53224,0x1c24c,37872,47088,53240,0x1c250,38400,47616,53768,0x1c254,38416,47632,53784,0x1c258,38432,47648,53800,0x1c25c,38448,47664,53816,0x1c260,38464,47680,53832,0x1c264,38480,47696,53848,0x1c268,38496,47712,53864,0x1c26c,38512,47728,53880,0x1c270,38528,47744,53896,0x1c274,38544,47760,53912,0x1c278,38560,47776,53928,0x1c27c,38576,47792,53944,0x1c280,38592,47808,53960,0x1c284,38608,47824,53976,0x1c288,38624,47840,53992,0x1c28c,38640,47856,54008,0x1c290,39168,48384,54536,0x1c294,39184,48400,54552,0x1c298,39200,48416,54568,0x1c29c,39216,48432,54584,0x1c2a0,39232,48448,54600,0x1c2a4,39248,48464,54616,0x1c2a8,39264,48480,54632,0x1c2ac,39280,48496,54648,0x1c2b0,39296,48512,54664,0x1c2b4,39312,48528,54680,0x1c2b8,39328,48544,54696,0x1c2bc,39344,48560,54712,0x1c2c0,39360,48576,54728,0x1c2c4,39376,48592,54744,0x1c2c8,39392,48608,54760,0x1c2cc,39408,48624,54776,86048,86112,0x1d600,0x1d60c,86192,86256,0x1d610,0x1d61c,86272,86336,0x1d620,0x1d62c,86416,86480,0x1d630,0x1d63c,86624,86560,0x1d640,0x1d64c,86768,86704,0x1d650,0x1d65c,86848,86784,0x1d660,0x1d66c,86992,86928,0x1d670,0x1d67c,87072,87136,0x1d680,0x1d68c,87216,87280,0x1d690,0x1d69c,87296,87360,0x1d6a0,0x1d6ac,87440,87504,0x1d6b0,0x1d6bc,87648,87584,0x1d6c0,0x1d6cc,87792,87728,0x1d6d0,0x1d6dc,87872,87808,0x1d6e0,0x1d6ec,88016,87952,0x1d6f0,0x1d6fc,86064,86120,0x1d700,0x1d70c,86176,86264,0x1d710,0x1d71c,86288,86344,0x1d720,0x1d72c,86400,86488,0x1d730,0x1d73c,86640,86568,0x1d740,0x1d74c,86752,86712,0x1d750,0x1d75c,86864,86792,0x1d760,0x1d76c,86976,86936,0x1d770,0x1d77c,87088,87144,0x1d780,0x1d78c,87200,87288,0x1d790,0x1d79c,87312,87368,0x1d7a0,0x1d7ac,87424,87512,0x1d7b0,0x1d7bc,87664,87592,0x1d7c0,0x1d7cc,87776,87736,0x1d7d0,0x1d7dc,87888,87816,0x1d7e0,0x1d7ec,88000,87960,0x1d7f0,0x1d7fc,86080,86128,0x1d800,0x1d80c,86224,86240,0x1d810,0x1d81c,86368,86352,0x1d820,0x1d82c,86512,86464,0x1d830,0x1d83c,86528,86576,0x1d840,0x1d84c,86672,86688,0x1d850,0x1d85c,86816,86800,0x1d860,0x1d86c,86960,86912,0x1d870,0x1d87c,87104,87152,0x1d880,0x1d88c,87248,87264,0x1d890,0x1d89c,87392,87376,0x1d8a0,0x1d8ac,87536,87488,0x1d8b0,0x1d8bc,87552,87600,0x1d8c0,0x1d8cc,87696,87712,0x1d8d0,0x1d8dc,87840,87824,0x1d8e0,0x1d8ec,87984,87936,0x1d8f0,0x1d8fc,86096,86136,0x1d900,0x1d90c,86208,86248,0x1d910,0x1d91c,86384,86360,0x1d920,0x1d92c,86496,86472,0x1d930,0x1d93c,86544,86584,0x1d940,0x1d94c,86656,86696,0x1d950,0x1d95c,86832,86808,0x1d960,0x1d96c,86944,86920,0x1d970,0x1d97c,87120,87160,0x1d980,0x1d98c,87232,87272,0x1d990,0x1d99c,87408,87384,0x1d9a0,0x1d9ac,87520,87496,0x1d9b0,0x1d9bc,87568,87608,0x1d9c0,0x1d9cc,87680,87720,0x1d9d0,0x1d9dc,87856,87832,0x1d9e0,0x1d9ec,87968,87944,0x1d9f0,0x1d9fc,0x1e780,0x1e780,0x1ea40,0x100000,0x1e782,0x100000,0x100000,0x1ea40,0x1e784,0x100000,0x100000,0x100000,0x1e786,0x100000,0x100000,0x100000,0x1e790,0x1e780,0x100000,0x100000,0x1e792,0x100000,0x100000,0x1ea40,0x1e794,0x100000,0x100000,0x100000,0x1e796,0x100000,0x100000,0x100000,0x1e7a0,0x1e780,0x100000,0x100000,0x1e7a2,0x100000,0x100000,0x1ea40,0x1e7a4,0x100000,0x100000,0x100000,0x1e7a6,0x100000,0x100000,0x100000,0x1e7b0,0x1e780,0x100000,0x100000,0x1e7b2,0x100000,0x100000,0x1ea40,0x1e7b4,0x100000,0x100000,0x100000,0x1e7b6,0x100000,0x100000,0x100000,0x1e788,0x1e790,0x1ea70,0x100000,0x1e78a,0x100000,0x100000,0x1ea70,0x1e78c,0x100000,0x100000,0x100000,0x1e78e,0x100000,0x100000,0x100000,0x1e798,0x1e790,0x100000,0x100000,0x1e79a,0x100000,0x100000,0x1ea70,0x1e79c,0x100000,0x100000,0x100000,0x1e79e,0x100000,0x100000,0x100000,0x1e7a8,0x1e790,0x100000,0x100000,0x1e7aa,0x100000,0x100000,0x1ea70,0x1e7ac,0x100000,0x100000,0x100000,0x1e7ae,0x100000,0x100000,0x100000,0x1e7b8,0x1e790,0x100000,0x100000,0x1e7ba,0x100000,0x100000,0x1ea70,0x1e7bc,0x100000,0x100000,0x100000,0x1e7be,0x100000,0x100000,0x100000,0x1e7c0,0x1e7a0,0x1eaa0,0x100000,0x1e7c2,0x100000,0x100000,0x1eaa0,0x1e7c4,0x100000,0x100000,0x100000,0x1e7c6,0x100000,0x100000,0x100000,0x1e7d0,0x1e7a0,0x100000,0x100000,0x1e7d2,0x100000,0x100000,0x1eaa0,0x1e7d4,0x100000,0x100000,0x100000,0x1e7d6,0x100000,0x100000,0x100000,0x1e7e0,0x1e7a0,0x100000,0x100000,0x1e7e2,0x100000,0x100000,0x1eaa0,0x1e7e4,0x100000,0x100000,0x100000,0x1e7e6,0x100000,0x100000,0x100000,0x1e7f0,0x1e7a0,0x100000,0x100000,0x1e7f2,0x100000,0x100000,0x1eaa0,0x1e7f4,0x100000,0x100000,0x100000,0x1e7f6,0x100000,0x100000,0x100000,0x1e7c8,0x1e7b0,0x1ead0,0x100000,0x1e7ca,0x100000,0x100000,0x1ead0,0x1e7cc,0x100000,0x100000,0x100000,0x1e7ce,0x100000,0x100000,0x100000,0x1e7d8,0x1e7b0,0x100000,0x100000,0x1e7da,0x100000,0x100000,0x1ead0,0x1e7dc,0x100000,0x100000,0x100000,0x1e7de,0x100000,0x100000,0x100000,0x1e7e8,0x1e7b0,0x100000,0x100000,0x1e7ea,0x100000,0x100000,0x1ead0,0x1e7ec,0x100000,0x100000,0x100000,0x1e7ee,0x100000,0x100000,0x100000,0x1e7f8,0x1e7b0,0x100000,0x100000,0x1e7fa,0x100000,0x100000,0x1ead0,0x1e7fc,0x100000,0x100000,0x100000,0x1e7fe,0x100000,0x100000,0x100000,0x1ec20,0x25cf0,37152,37216,0x1ec22,0x25cf2,37168,37224,0x1ec24,0x25cf4,37184,37232,0x1ec26,0x25cf6,37200,37240,0x1ec28,0x25e50,37536,37600,0x1ec2a,0x25e52,37552,37608,0x1ec2c,0x25e54,37568,37616,0x1ec2e,0x25e56,37584,37624,0x1ec30,0x25fb0,37888,37952,0x1ec32,0x25fb2,37904,37960,0x1ec34,0x25fb4,37984,37968,0x1ec36,0x25fb6,38000,37976,0x1ec38,0x26110,38272,38336,0x1ec3a,0x26112,38288,38344,0x1ec3c,0x26114,38368,38352,0x1ec3e,0x26116,38384,38360,0x1ec20,0x25cf8,38752,38688,0x1ec22,0x25cfa,38768,38696,0x1ec24,0x25cfc,38656,38704,0x1ec26,0x25cfe,38672,38712,0x1ec28,0x25e58,39136,39072,0x1ec2a,0x25e5a,39152,39080,0x1ec2c,0x25e5c,39040,39088,0x1ec2e,0x25e5e,39056,39096,0x1ec30,0x25fb8,39488,39424,0x1ec32,0x25fba,39504,39432,0x1ec34,0x25fbc,39456,39440,0x1ec36,0x25fbe,39472,39448,0x1ec38,0x26118,39872,39808,0x1ec3a,0x2611a,39888,39816,0x1ec3c,0x2611c,39840,39824,0x1ec3e,0x2611e,39856,39832,0x1ec20,0x267f8,40224,40288,0x1ec22,0x267fa,40240,40296,0x1ec24,0x267fc,40256,40304,0x1ec26,0x267fe,40272,40312,0x1ec28,0x26958,40608,40672,0x1ec2a,0x2695a,40624,40680,0x1ec2c,0x2695c,40640,40688,0x1ec2e,0x2695e,40656,40696,0x1ec30,0x26ab8,40960,41024,0x1ec32,0x26aba,40976,41032,0x1ec34,0x26abc,41056,41040,0x1ec36,0x26abe,41072,41048,0x1ec38,0x26c18,41344,41408,0x1ec3a,0x26c1a,41360,41416,0x1ec3c,0x26c1c,41440,41424,0x1ec3e,0x26c1e,41456,41432,0x1ec20,0x100000,41824,41760,0x1ec22,0x100000,41840,41768,0x1ec24,0x100000,41728,41776,0x1ec26,0x100000,41744,41784,0x1ec28,0x100000,42208,42144,0x1ec2a,0x100000,42224,42152,0x1ec2c,0x100000,42112,42160,0x1ec2e,0x100000,42128,42168,0x1ec30,0x100000,42560,42496,0x1ec32,0x100000,42576,42504,0x1ec34,0x100000,42528,42512,0x1ec36,0x100000,42544,42520,0x1ec38,0x100000,42944,42880,0x1ec3a,0x100000,42960,42888,0x1ec3c,0x100000,42912,42896,0x1ec3e,0x100000,42928,42904,37152,37184,37216,64512,37160,37192,37224,64528,37168,37200,37232,64544,37176,37208,37240,64560,38304,38336,38368,64576,38312,38344,38376,64592,38320,38352,38384,64608,38328,38360,38392,64624,39424,39520,39488,64640,39432,39528,39496,64656,39440,39536,39504,64672,39448,39544,39512,64688,40576,40672,40640,64704,40584,40680,40648,64720,40592,40688,40656,64736,40600,40696,40664,64752,46432,46336,46368,64768,46440,46344,46376,64784,46448,46352,46384,64800,46456,46360,46392,64816,47584,47488,47520,64832,47592,47496,47528,64848,47600,47504,47536,64864,47608,47512,47544,64880,48704,48672,48640,64896,48712,48680,48648,64912,48720,48688,48656,64928,48728,48696,48664,64944,49856,49824,49792,64960,49864,49832,49800,64976,49872,49840,49808,64992,49880,49848,49816,65008,55584,55616,55648,65024,55592,55624,55656,65040,55600,55632,55664,65056,55608,55640,55672,65072,56736,56768,56800,65088,56744,56776,56808,65104,56752,56784,56816,65120,56760,56792,56824,65136,57856,57952,57920,65152,57864,57960,57928,65168,57872,57968,57936,65184,57880,57976,57944,65200,59008,59104,59072,65216,59016,59112,59080,65232,59024,59120,59088,65248,59032,59128,59096,65264,64864,64768,64800,65280,64872,64776,64808,65296,64880,64784,64816,65312,64888,64792,64824,65328,66016,65920,65952,65344,66024,65928,65960,65360,66032,65936,65968,65376,66040,65944,65976,65392,67136,67104,67072,65408,67144,67112,67080,65424,67152,67120,67088,65440,67160,67128,67096,65456,68288,68256,68224,65472,68296,68264,68232,65488,68304,68272,68240,65504,68312,68280,68248,65520,0x1e800,0x1e800,0x20cc0,55296,0x1e8c4,0x1e8c4,0x20cc4,55312,0x1e988,0x1e988,0x20cc8,55328,0x1ea4c,0x1ea4c,0x20ccc,55344,0x1eb10,0x1eb10,0x20cd0,55360,0x1ebd4,0x1ebd4,0x20cd4,55376,0x1ec98,0x1ec98,0x20cd8,55392,0x1ed5c,0x1ed5c,0x20cdc,55408,0x1ee20,0x1ee20,0x20ce0,55680,0x1eee4,0x1eee4,0x20ce4,55696,0x1efa8,0x1efa8,0x20ce8,55712,0x1f06c,0x1f06c,0x20cec,55728,0x1f130,0x1f130,0x20cf0,55744,0x1f1f4,0x1f1f4,0x20cf4,55760,0x1f2b8,0x1f2b8,0x20cf8,55776,0x1f37c,0x1f37c,0x20cfc,55792,0x1e810,0x1e800,0x20cc0,56064,0x1e8d4,0x1e8c4,0x20cc4,56080,0x1e998,0x1e988,0x20cc8,56096,0x1ea5c,0x1ea4c,0x20ccc,56112,0x1eb20,0x1eb10,0x20cd0,56128,0x1ebe4,0x1ebd4,0x20cd4,56144,0x1eca8,0x1ec98,0x20cd8,56160,0x1ed6c,0x1ed5c,0x20cdc,56176,0x1ee30,0x1ee20,0x20ce0,56448,0x1eef4,0x1eee4,0x20ce4,56464,0x1efb8,0x1efa8,0x20ce8,56480,0x1f07c,0x1f06c,0x20cec,56496,0x1f140,0x1f130,0x20cf0,56512,0x1f204,0x1f1f4,0x20cf4,56528,0x1f2c8,0x1f2b8,0x20cf8,56544,0x1f38c,0x1f37c,0x20cfc,56560,0x1e820,0x1e800,0x20cc0,56832,0x1e8e4,0x1e8c4,0x20cc4,56848,0x1e9a8,0x1e988,0x20cc8,56864,0x1ea6c,0x1ea4c,0x20ccc,56880,0x1eb30,0x1eb10,0x20cd0,56896,0x1ebf4,0x1ebd4,0x20cd4,56912,0x1ecb8,0x1ec98,0x20cd8,56928,0x1ed7c,0x1ed5c,0x20cdc,56944,0x1ee40,0x1ee20,0x20ce0,57216,0x1ef04,0x1eee4,0x20ce4,57232,0x1efc8,0x1efa8,0x20ce8,57248,0x1f08c,0x1f06c,0x20cec,57264,0x1f150,0x1f130,0x20cf0,57280,0x1f214,0x1f1f4,0x20cf4,57296,0x1f2d8,0x1f2b8,0x20cf8,57312,0x1f39c,0x1f37c,0x20cfc,57328,0x1e830,0x1e800,0x20cc0,57600,0x1e8f4,0x1e8c4,0x20cc4,57616,0x1e9b8,0x1e988,0x20cc8,57632,0x1ea7c,0x1ea4c,0x20ccc,57648,0x1eb40,0x1eb10,0x20cd0,57664,0x1ec04,0x1ebd4,0x20cd4,57680,0x1ecc8,0x1ec98,0x20cd8,57696,0x1ed8c,0x1ed5c,0x20cdc,57712,0x1ee50,0x1ee20,0x20ce0,57984,0x1ef14,0x1eee4,0x20ce4,58000,0x1efd8,0x1efa8,0x20ce8,58016,0x1f09c,0x1f06c,0x20cec,58032,0x1f160,0x1f130,0x20cf0,58048,0x1f224,0x1f1f4,0x20cf4,58064,0x1f2e8,0x1f2b8,0x20cf8,58080,0x1f3ac,0x1f37c,0x20cfc,58096,92160,0x27070,0x1ea40,0x1ec20,92176,0x27074,0x1eed0,0x1f0b0,92192,0x27078,0x1f360,0x1f540,92208,0x2707c,0x1f7f0,0x1f9d0,92224,0x27080,0x1fc80,0x1fe60,92240,0x27084,0x20110,0x202f0,92256,0x27088,0x205a0,0x20780,92272,0x2708c,0x20a30,0x20c10,92544,0x27090,0x20ec0,0x210a0,92560,0x27094,0x21350,0x21530,92576,0x27098,0x217e0,0x219c0,92592,0x2709c,0x21c70,0x21e50,92608,0x270a0,0x22100,0x222e0,92624,0x270a4,0x22590,0x22770,92640,0x270a8,0x22a20,0x22c00,92656,0x270ac,0x22eb0,0x23090,92960,0x270b0,0x1ea70,0x1ec28,92976,0x270b4,0x1ef00,0x1f0b8,92928,0x270b8,0x1f390,0x1f548,92944,0x270bc,0x1f820,0x1f9d8,93024,0x270c0,0x1fcb0,0x1fe68,93040,0x270c4,0x20140,0x202f8,92992,0x270c8,0x205d0,0x20788,93008,0x270cc,0x20a60,0x20c18,93344,0x270d0,0x20ef0,0x210a8,93360,0x270d4,0x21380,0x21538,93312,0x270d8,0x21810,0x219c8,93328,0x270dc,0x21ca0,0x21e58,93408,0x270e0,0x22130,0x222e8,93424,0x270e4,0x225c0,0x22778,93376,0x270e8,0x22a50,0x22c08,93392,0x270ec,0x22ee0,0x23098,93760,0x270f0,0x1eaa0,0x1ec30,93776,0x270f4,0x1ef30,0x1f0c0,93792,0x270f8,0x1f3c0,0x1f550,93808,0x270fc,0x1f850,0x1f9e0,93696,0x27100,0x1fce0,0x1fe70,93712,0x27104,0x20170,0x20300,93728,0x27108,0x20600,0x20790,93744,0x2710c,0x20a90,0x20c20,94144,0x27110,0x20f20,0x210b0,94160,0x27114,0x213b0,0x21540,94176,0x27118,0x21840,0x219d0,94192,0x2711c,0x21cd0,0x21e60,94080,0x27120,0x22160,0x222f0,94096,0x27124,0x225f0,0x22780,94112,0x27128,0x22a80,0x22c10,94128,0x2712c,0x22f10,0x230a0,94560,0x27130,0x1ead0,0x1ec38,94576,0x27134,0x1ef60,0x1f0c8,94528,0x27138,0x1f3f0,0x1f558,94544,0x2713c,0x1f880,0x1f9e8,94496,0x27140,0x1fd10,0x1fe78,94512,0x27144,0x201a0,0x20308,94464,0x27148,0x20630,0x20798,94480,0x2714c,0x20ac0,0x20c28,94944,0x27150,0x20f50,0x210b8,94960,0x27154,0x213e0,0x21548,94912,0x27158,0x21870,0x219d8,94928,0x2715c,0x21d00,0x21e68,94880,0x27160,0x22190,0x222f8,94896,0x27164,0x22620,0x22788,94848,0x27168,0x22ab0,0x22c18,94864,0x2716c,0x22f40,0x230a8,0x3f803f80,0x3f803f80,16256,0,0,0x3f803f80,0x3f800000,0,0,0x3f803f80,0,16256,0,0x3f803f80,0,0x3f800000,0x3f803f80,0,0,0,0,0,0,0,0,0,0,0,0,0,0,0,0x3f803f80,0,0,0,0,0,0,0,0,0,0,0,0,0,0,0,0x3f803f80,0,0,0,0,0,0,0,0,0,0,0,0,0,0,0,0x3f803f80,0,0,0,0,0,0,0,0,0,0,0,0,0,0,0,0x3f803f80,0x3f803f80,16256,0,0,0x3f803f80,0x3f800000,0,0,0x3f803f80,0,16256,0,0x3f803f80,0,0x3f800000,0x3f803f80,0,0,0,0,0,0,0,0,0,0,0,0,0,0,0,0x3f803f80,0,0,0,0,0,0,0,0,0,0,0,0,0,0,0,0x3f803f80,0,0,0,0,0,0,0,0,0,0,0,0,0,0,0,0x3f803f80,0,0,0,0,0,0,0,0,0,0,0,0,0,0,0,0x3f803f80,0x3f803f80,16256,0,0,0x3f803f80,0x3f800000,0,0,0x3f803f80,0,16256,0,0x3f803f80,0,0x3f800000,0x3f803f80,0,0,0,0,0,0,0,0,0,0,0,0,0,0,0,0x3f803f80,0,0,0,0,0,0,0,0,0,0,0,0,0,0,0,0x3f803f80,0,0,0,0,0,0,0,0,0,0,0,0,0,0,0,0x3f803f80,0,0,0,0,0,0,0,0,0,0,0,0,0,0,0,0x3f803f80,0x3f803f80,16256,0,0,0x3f803f80,0x3f800000,0,0,0x3f803f80,0,16256,0,0x3f803f80,0,0x3f800000,0xffffffff,0xffffffff,0,0,0,0,0xffffffff,0xff000000,0xff000000,0xffffff,0xff000000,0xff000000,0,4096,16384,0x1c000,0xffffffff,0xffffffff,0,0,0,0,0xffffff,0xffff0000,0xffff0000,65535,0xffffff,0xffffff,1024,5120,16896,0x1c0a0,0xffffffff,0xffffffff,0,0,0,0,65535,0xffffff00,0xff000000,0xffffff,0xffff0000,0xffff0000,2048,10240,17408,0x1c140,0xffffffff,0,0,0,0,0,255,0xffffffff,0xffff0000,65535,65535,65535,3072,11264,17920,0x1c1d0,0,0x3fb8aa3b,0x2a8cbccc,0x38fba882,0x7fff7fff,0x415db3d7,0xffff0000,0,0,65535,0,0xffff0000,0,0,0,0,0,808,1800,2608,3600,4408,5400,6208,7200,8192,9000,9992,10800,11792,12600,18432,14385,18447,16170,18462,18462,18462,18462,18462,20,19,0,0,0,0,0,0,0x1b0000,0x5b8480,0xae0a80,0xee8f00,0x1411500,0x1819980,0x1d41f80,0x214a400,0x2672a00,0x2b9b000,0x2fa3480,0x34cba80,0x38d3f00,0x3dfc500,0x4204980,0x60a80,0x4b2b680,0x6a800,0x5452380,0x74580,0x74580,0x74580,0x74580,0x74580,0x74580,0x74580,0x74580,0x74580,0x74580,0x74580,0x74580,0x74580,1,809,1801,2609,3601,4409,5401,6209,7201,8193,9001,9993,10801,11793,12601,13592,14386,15377,16171,18463,18463,18463,18463,18463,20,19,0,0,0,0,0,0,0x1b0a80,0x5b8f00,0xae1500,0xee9980,0x1411f80,0x181a400,0x1d42a00,0x214ae80,0x2673480,0x2b9ba80,0x2fa3f00,0x34cc500,0x38d4980,0x3dfcf80,0x4205400,0x472cf80,0x4b2c100,0x5053c80,0x5452e00,0x75000,0x75000,0x75000,0x75000,0x75000,0x75000,0x75000,0x75000,0x75000,0x75000,0x75000,0x75000,0x75000,2,810,1802,2610,3602,4410,5402,6210,7202,8194,9002,9994,10802,11794,12602,13593,14387,15378,16172,17162,17671,18216,18216,18216,22,21,0,0,0,0,0,0,0x1b1500,0x5b9980,0xae1f80,0xeea400,0x1412a00,0x181ae80,0x1d43480,0x214b900,0x2673f00,0x2b9c500,0x2fa4980,0x34ccf80,0x38d5400,0x3dfda00,0x4205e80,0x472da00,0x4b2cb80,0x5054700,0x5453880,0x597a980,0x5c17500,0x5ecba80,0x5ecba80,0x5ecba80,0x5ecba80,0x5ecba80,0x5ecba80,0x5ecba80,0x5ecba80,0x5ecba80,0x5ecba80,0x5ecba80,3,811,1803,2611,3603,4411,5403,6211,7203,8195,9003,9995,10803,11795,12603,13594,14388,15379,16173,17163,17672,17672,17672,17672,21,20,0,0,0,0,0,0,0x1b1f80,0x5ba400,0xae2a00,0xeeae80,0x1413480,0x181b900,0x1d43f00,0x214c380,0x2674980,0x2b9cf80,0x2fa5400,0x34cda00,0x38d5e80,0x3dfe480,0x4206900,0x472e480,0x4b2d600,0x5055180,0x5454300,0x597b400,0x5c17f80,0x5c17f80,0x5c17f80,0x5c17f80,0x5c17f80,0x5c17f80,0x5c17f80,0x5c17f80,0x5c17f80,0x5c17f80,0x5c17f80,0x5c17f80,4,812,1804,2612,3604,4412,5404,6212,7204,8196,9004,9996,10804,11796,12604,13595,14389,15380,16174,18464,17673,17673,17673,17673,21,20,0,0,0,0,0,0,0x1b2a00,0x5bae80,0xae3480,0xeeb900,0x1413f00,0x181c380,0x1d44980,0x214ce00,0x2675400,0x2b9da00,0x2fa5e80,0x34ce480,0x38d6900,0x3dfef00,0x4207380,0x472ef00,0x4b2e080,0x5055c00,0x5454d80,0x75a80,0x5c18a00,0x5c18a00,0x5c18a00,0x5c18a00,0x5c18a00,0x5c18a00,0x5c18a00,0x5c18a00,0x5c18a00,0x5c18a00,0x5c18a00,0x5c18a00,5,813,1805,2613,3605,4413,5405,6213,7205,8197,9005,9997,10805,11797,12605,13596,14390,15381,16175,18465,18465,18465,18465,18465,20,19,0,0,0,0,0,0,0x1b3480,0x5bb900,0xae3f00,0xeec380,0x1414980,0x181ce00,0x1d45400,0x214d880,0x2675e80,0x2b9e480,0x2fa6900,0x34cef00,0x38d7380,0x3dff980,0x4207e00,0x472f980,0x4b2eb00,0x5056680,0x5455800,0x76500,0x76500,0x76500,0x76500,0x76500,0x76500,0x76500,0x76500,0x76500,0x76500,0x76500,0x76500,0x76500,6,814,1806,2614,3606,4414,5406,6214,7206,8198,9006,9998,10806,11798,12606,13597,14391,15382,16176,18466,17674,17674,17674,17674,21,20,0,0,0,0,0,0,0x1b3f00,0x5bc380,0xae4980,0xeece00,0x1415400,0x181d880,0x1d45e80,0x214e300,0x2676900,0x2b9ef00,0x2fa7380,0x34cf980,0x38d7e00,0x3e00400,0x4208880,0x4730400,0x4b2f580,0x5057100,0x5456280,0x76f80,0x5c19480,0x5c19480,0x5c19480,0x5c19480,0x5c19480,0x5c19480,0x5c19480,0x5c19480,0x5c19480,0x5c19480,0x5c19480,0x5c19480,7,815,1807,2615,3607,4415,5407,6215,7207,8199,9007,9999,10807,11799,12607,13598,14392,15383,16177,17164,17675,17675,17675,17675,21,20,0,0,0,0,0,0,0x1b4980,0x5bce00,0xae5400,0xeed880,0x1415e80,0x181e300,0x1d46900,0x214ed80,0x2677380,0x2b9f980,0x2fa7e00,0x34d0400,0x38d8880,0x3e00e80,0x4209300,0x4730e80,0x4b30000,0x5057b80,0x5456d00,0x597be80,0x5c19f00,0x5c19f00,0x5c19f00,0x5c19f00,0x5c19f00,0x5c19f00,0x5c19f00,0x5c19f00,0x5c19f00,0x5c19f00,0x5c19f00,0x5c19f00,8,816,1808,2616,3608,4416,5408,6400,7208,8200,9008,10000,10808,11800,12608,18433,14393,18448,16178,18467,18467,18467,18467,18467,20,19,0,0,0,0,0,0,0x1b5400,0x5bd880,0xae5e80,0xeee300,0x1416900,0x181ed80,0x1d47380,0x226f980,0x2677e00,0x2ba0400,0x2fa8880,0x34d0e80,0x38d9300,0x3e01900,0x4209d80,0x61500,0x4b30a80,0x6b280,0x5457780,0x77a00,0x77a00,0x77a00,0x77a00,0x77a00,0x77a00,0x77a00,0x77a00,0x77a00,0x77a00,0x77a00,0x77a00,0x77a00,9,817,1809,2617,3609,4417,5409,6401,7209,8201,9009,10001,10809,11801,12609,13599,14394,15384,16179,18468,18468,18468,18468,18468,20,19,0,0,0,0,0,0,0x1b5e80,0x5be300,0xae6900,0xeeed80,0x1417380,0x181f800,0x1d47e00,0x2270400,0x2678880,0x2ba0e80,0x2fa9300,0x34d1900,0x38d9d80,0x3e02380,0x420a800,0x4731900,0x4b31500,0x5058600,0x5458200,0x78480,0x78480,0x78480,0x78480,0x78480,0x78480,0x78480,0x78480,0x78480,0x78480,0x78480,0x78480,0x78480,10,818,1810,2618,3610,4418,5410,6402,7210,8202,9010,10002,10810,11802,12610,13600,14395,15385,16180,17165,17676,18217,18217,18217,22,21,0,0,0,0,0,0,0x1b6900,0x5bed80,0xae7380,0xeef800,0x1417e00,0x1820280,0x1d48880,0x2270e80,0x2679300,0x2ba1900,0x2fa9d80,0x34d2380,0x38da800,0x3e02e00,0x420b280,0x4732380,0x4b31f80,0x5059080,0x5458c80,0x597c900,0x5c1a980,0x5ecc500,0x5ecc500,0x5ecc500,0x5ecc500,0x5ecc500,0x5ecc500,0x5ecc500,0x5ecc500,0x5ecc500,0x5ecc500,0x5ecc500,11,819,1811,2619,3611,4419,5411,6403,7211,8203,9011,10003,10811,11803,12611,13601,14396,15386,16181,17166,17677,17677,17677,17677,21,20,0,0,0,0,0,0,0x1b7380,0x5bf800,0xae7e00,0xef0280,0x1418880,0x1820d00,0x1d49300,0x2271900,0x2679d80,0x2ba2380,0x2faa800,0x34d2e00,0x38db280,0x3e03880,0x420bd00,0x4732e00,0x4b32a00,0x5059b00,0x5459700,0x597d380,0x5c1b400,0x5c1b400,0x5c1b400,0x5c1b400,0x5c1b400,0x5c1b400,0x5c1b400,0x5c1b400,0x5c1b400,0x5c1b400,0x5c1b400,0x5c1b400,12,820,1812,2620,3612,4420,5412,6404,7212,8204,9012,10004,10812,11804,12612,13602,14397,15387,16182,18469,17678,17678,17678,17678,21,20,0,0,0,0,0,0,0x1b7e00,0x5c0280,0xae8880,0xef0d00,0x1419300,0x1821780,0x1d49d80,0x2272380,0x267a800,0x2ba2e00,0x2fab280,0x34d3880,0x38dbd00,0x3e04300,0x420c780,0x4733880,0x4b33480,0x505a580,0x545a180,0x78f00,0x5c1be80,0x5c1be80,0x5c1be80,0x5c1be80,0x5c1be80,0x5c1be80,0x5c1be80,0x5c1be80,0x5c1be80,0x5c1be80,0x5c1be80,0x5c1be80,13,821,1813,2621,3613,4421,5413,6405,7213,8205,9013,10005,10813,11805,12613,13603,14398,15388,16183,18470,18470,18470,18470,18470,20,19,0,0,0,0,0,0,0x1b8880,0x5c0d00,0xae9300,0xef1780,0x1419d80,0x1822200,0x1d4a800,0x2272e00,0x267b280,0x2ba3880,0x2fabd00,0x34d4300,0x38dc780,0x3e04d80,0x420d200,0x4734300,0x4b33f00,0x505b000,0x545ac00,0x79980,0x79980,0x79980,0x79980,0x79980,0x79980,0x79980,0x79980,0x79980,0x79980,0x79980,0x79980,0x79980,14,822,1814,2622,3614,4422,5414,6406,7214,8206,9014,10006,10814,11806,12614,13604,14399,15389,16184,18471,17679,17679,17679,17679,21,20,0,0,0,0,0,0,0x1b9300,0x5c1780,0xae9d80,0xef2200,0x141a800,0x1822c80,0x1d4b280,0x2273880,0x267bd00,0x2ba4300,0x2fac780,0x34d4d80,0x38dd200,0x3e05800,0x420dc80,0x4734d80,0x4b34980,0x505ba80,0x545b680,0x7a400,0x5c1c900,0x5c1c900,0x5c1c900,0x5c1c900,0x5c1c900,0x5c1c900,0x5c1c900,0x5c1c900,0x5c1c900,0x5c1c900,0x5c1c900,0x5c1c900,15,823,1815,2623,3615,4423,5415,6407,7215,8207,9015,10007,10815,11807,12615,13605,14400,15390,16185,17167,17680,17680,17680,17680,21,20,0,0,0,0,0,0,0x1b9d80,0x5c2200,0xaea800,0xef2c80,0x141b280,0x1823700,0x1d4bd00,0x2274300,0x267c780,0x2ba4d80,0x2fad200,0x34d5800,0x38ddc80,0x3e06280,0x420e700,0x4735800,0x4b35400,0x505c500,0x545c100,0x597de00,0x5c1d380,0x5c1d380,0x5c1d380,0x5c1d380,0x5c1d380,0x5c1d380,0x5c1d380,0x5c1d380,0x5c1d380,0x5c1d380,0x5c1d380,0x5c1d380,16,824,1816,2624,3616,4608,5416,6408,7216,8208,9016,10008,10816,11808,12800,18434,14401,18449,16186,18472,18472,18472,18472,18472,20,19,0,0,0,0,0,0,0x1ba800,0x5c2c80,0xaeb280,0xef3700,0x141bd00,0x1944300,0x1d4c780,0x2274d80,0x267d200,0x2ba5800,0x2fadc80,0x34d6280,0x38de700,0x3e06d00,0x432f300,0x61f80,0x4b35e80,0x6bd00,0x545cb80,0x7ae80,0x7ae80,0x7ae80,0x7ae80,0x7ae80,0x7ae80,0x7ae80,0x7ae80,0x7ae80,0x7ae80,0x7ae80,0x7ae80,0x7ae80,17,825,1817,2625,3617,4609,5417,6409,7217,8209,9017,10009,10817,11809,12801,13606,14402,15391,16187,18473,18473,18473,18473,18473,20,19,0,0,0,0,0,0,0x1bb280,0x5c3700,0xaebd00,0xef4180,0x141c780,0x1944d80,0x1d4d200,0x2275800,0x267dc80,0x2ba6280,0x2fae700,0x34d6d00,0x38df180,0x3e07780,0x432fd80,0x4736280,0x4b36900,0x505cf80,0x545d600,0x7b900,0x7b900,0x7b900,0x7b900,0x7b900,0x7b900,0x7b900,0x7b900,0x7b900,0x7b900,0x7b900,0x7b900,0x7b900,18,826,1818,2626,3618,4610,5418,6410,7218,8210,9018,10010,10818,11810,12802,13607,14403,15392,16188,17168,17681,18218,18218,18218,22,21,0,0,0,0,0,0,0x1bbd00,0x5c4180,0xaec780,0xef4c00,0x141d200,0x1945800,0x1d4dc80,0x2276280,0x267e700,0x2ba6d00,0x2faf180,0x34d7780,0x38dfc00,0x3e08200,0x4330800,0x4736d00,0x4b37380,0x505da00,0x545e080,0x597e880,0x5c1de00,0x5eccf80,0x5eccf80,0x5eccf80,0x5eccf80,0x5eccf80,0x5eccf80,0x5eccf80,0x5eccf80,0x5eccf80,0x5eccf80,0x5eccf80,19,827,1819,2627,3619,4611,5419,6411,7219,8211,9019,10011,10819,11811,12803,13608,14404,15393,16189,17169,17682,17682,17682,17682,21,20,0,0,0,0,0,0,0x1bc780,0x5c4c00,0xaed200,0xef5680,0x141dc80,0x1946280,0x1d4e700,0x2276d00,0x267f180,0x2ba7780,0x2fafc00,0x34d8200,0x38e0680,0x3e08c80,0x4331280,0x4737780,0x4b37e00,0x505e480,0x545eb00,0x597f300,0x5c1e880,0x5c1e880,0x5c1e880,0x5c1e880,0x5c1e880,0x5c1e880,0x5c1e880,0x5c1e880,0x5c1e880,0x5c1e880,0x5c1e880,0x5c1e880,20,828,1820,2628,3620,4612,5420,6412,7220,8212,9020,10012,10820,11812,12804,13609,14405,15394,16190,18474,17683,17683,17683,17683,21,20,0,0,0,0,0,0,0x1bd200,0x5c5680,0xaedc80,0xef6100,0x141e700,0x1946d00,0x1d4f180,0x2277780,0x267fc00,0x2ba8200,0x2fb0680,0x34d8c80,0x38e1100,0x3e09700,0x4331d00,0x4738200,0x4b38880,0x505ef00,0x545f580,0x7c380,0x5c1f300,0x5c1f300,0x5c1f300,0x5c1f300,0x5c1f300,0x5c1f300,0x5c1f300,0x5c1f300,0x5c1f300,0x5c1f300,0x5c1f300,0x5c1f300,21,829,1821,2629,3621,4613,5421,6413,7221,8213,9021,10013,10821,11813,12805,13610,14406,15395,16191,18475,18475,18475,18475,18475,20,19,0,0,0,0,0,0,0x1bdc80,0x5c6100,0xaee700,0xef6b80,0x141f180,0x1947780,0x1d4fc00,0x2278200,0x2680680,0x2ba8c80,0x2fb1100,0x34d9700,0x38e1b80,0x3e0a180,0x4332780,0x4738c80,0x4b39300,0x505f980,0x5460000,0x7ce00,0x7ce00,0x7ce00,0x7ce00,0x7ce00,0x7ce00,0x7ce00,0x7ce00,0x7ce00,0x7ce00,0x7ce00,0x7ce00,0x7ce00,22,830,1822,2630,3622,4614,5422,6414,7222,8214,9022,10014,10822,11814,12806,13611,14407,15396,16192,18476,17684,17684,17684,17684,21,20,0,0,0,0,0,0,0x1be700,0x5c6b80,0xaef180,0xef7600,0x141fc00,0x1948200,0x1d50680,0x2278c80,0x2681100,0x2ba9700,0x2fb1b80,0x34da180,0x38e2600,0x3e0ac00,0x4333200,0x4739700,0x4b39d80,0x5060400,0x5460a80,0x7d880,0x5c1fd80,0x5c1fd80,0x5c1fd80,0x5c1fd80,0x5c1fd80,0x5c1fd80,0x5c1fd80,0x5c1fd80,0x5c1fd80,0x5c1fd80,0x5c1fd80,0x5c1fd80,23,831,1823,2631,3623,4615,5423,6415,7223,8215,9023,10015,10823,11815,12807,13612,14592,15397,16193,17170,17685,17685,17685,17685,21,20,0,0,0,0,0,0,0x1bf180,0x5c7600,0xaefc00,0xef8080,0x1420680,0x1948c80,0x1d51100,0x2279700,0x2681b80,0x2baa180,0x2fb2600,0x34dac00,0x38e3080,0x3e0b680,0x4333c80,0x473a180,0x4c5a980,0x5060e80,0x5461500,0x597fd80,0x5c20800,0x5c20800,0x5c20800,0x5c20800,0x5c20800,0x5c20800,0x5c20800,0x5c20800,0x5c20800,0x5c20800,0x5c20800,0x5c20800,24,832,1824,2816,3624,4616,5424,6416,7224,8216,9024,10016,11008,11816,12808,18435,14593,18450,16194,18477,18477,18477,18477,18477,20,19,0,0,0,0,0,0,0x1bfc00,0x5c8080,0xaf0680,0x1018c80,0x1421100,0x1949700,0x1d51b80,0x227a180,0x2682600,0x2baac00,0x2fb3080,0x34db680,0x3a03c80,0x3e0c100,0x4334700,0x62a00,0x4c5b400,0x6c780,0x5461f80,0x7e300,0x7e300,0x7e300,0x7e300,0x7e300,0x7e300,0x7e300,0x7e300,0x7e300,0x7e300,0x7e300,0x7e300,0x7e300,25,833,1825,2817,3625,4617,5425,6417,7225,8217,9025,10017,11009,11817,12809,13613,14594,15398,16195,18478,18478,18478,18478,18478,20,19,0,0,0,0,0,0,0x1c0680,0x5c8b00,0xaf1100,0x1019700,0x1421b80,0x194a180,0x1d52600,0x227ac00,0x2683080,0x2bab680,0x2fb3b00,0x34dc100,0x3a04700,0x3e0cb80,0x4335180,0x473ac00,0x4c5be80,0x5061900,0x5462a00,0x7ed80,0x7ed80,0x7ed80,0x7ed80,0x7ed80,0x7ed80,0x7ed80,0x7ed80,0x7ed80,0x7ed80,0x7ed80,0x7ed80,0x7ed80,26,834,1826,2818,3626,4618,5426,6418,7226,8218,9026,10018,11010,11818,12810,13614,14595,15399,16196,17171,17686,18219,18219,18219,22,21,0,0,0,0,0,0,0x1c1100,0x5c9580,0xaf1b80,0x101a180,0x1422600,0x194ac00,0x1d53080,0x227b680,0x2683b00,0x2bac100,0x2fb4580,0x34dcb80,0x3a05180,0x3e0d600,0x4335c00,0x473b680,0x4c5c900,0x5062380,0x5463480,0x5980800,0x5c21280,0x5ecda00,0x5ecda00,0x5ecda00,0x5ecda00,0x5ecda00,0x5ecda00,0x5ecda00,0x5ecda00,0x5ecda00,0x5ecda00,0x5ecda00,27,835,1827,2819,3627,4619,5427,6419,7227,8219,9027,10019,11011,11819,12811,13615,14596,15400,16197,17172,17687,17687,17687,17687,21,20,0,0,0,0,0,0,0x1c1b80,0x5ca000,0xaf2600,0x101ac00,0x1423080,0x194b680,0x1d53b00,0x227c100,0x2684580,0x2bacb80,0x2fb5000,0x34dd600,0x3a05c00,0x3e0e080,0x4336680,0x473c100,0x4c5d380,0x5062e00,0x5463f00,0x5981280,0x5c21d00,0x5c21d00,0x5c21d00,0x5c21d00,0x5c21d00,0x5c21d00,0x5c21d00,0x5c21d00,0x5c21d00,0x5c21d00,0x5c21d00,0x5c21d00,28,836,1828,2820,3628,4620,5428,6420,7228,8220,9028,10020,11012,11820,12812,13616,14597,15401,16198,18479,17688,17688,17688,17688,21,20,0,0,0,0,0,0,0x1c2600,0x5caa80,0xaf3080,0x101b680,0x1423b00,0x194c100,0x1d54580,0x227cb80,0x2685000,0x2bad600,0x2fb5a80,0x34de080,0x3a06680,0x3e0eb00,0x4337100,0x473cb80,0x4c5de00,0x5063880,0x5464980,0x7f800,0x5c22780,0x5c22780,0x5c22780,0x5c22780,0x5c22780,0x5c22780,0x5c22780,0x5c22780,0x5c22780,0x5c22780,0x5c22780,0x5c22780,29,837,1829,2821,3629,4621,5429,6421,7229,8221,9029,10021,11013,11821,12813,13617,14598,15402,16199,18480,18480,18480,18480,18480,20,19,0,0,0,0,0,0,0x1c3080,0x5cb500,0xaf3b00,0x101c100,0x1424580,0x194cb80,0x1d55000,0x227d600,0x2685a80,0x2bae080,0x2fb6500,0x34deb00,0x3a07100,0x3e0f580,0x4337b80,0x473d600,0x4c5e880,0x5064300,0x5465400,0x80280,0x80280,0x80280,0x80280,0x80280,0x80280,0x80280,0x80280,0x80280,0x80280,0x80280,0x80280,0x80280,30,838,1830,2822,3630,4622,5430,6422,7230,8222,9030,10022,11014,11822,12814,13618,14599,15403,16384,18481,17689,17689,17689,17689,21,20,0,0,0,0,0,0,0x1c3b00,0x5cbf80,0xaf4580,0x101cb80,0x1425000,0x194d600,0x1d55a80,0x227e080,0x2686500,0x2baeb00,0x2fb6f80,0x34df580,0x3a07b80,0x3e10000,0x4338600,0x473e080,0x4c5f300,0x5064d80,0x5586000,0x80d00,0x5c23200,0x5c23200,0x5c23200,0x5c23200,0x5c23200,0x5c23200,0x5c23200,0x5c23200,0x5c23200,0x5c23200,0x5c23200,0x5c23200,31,839,1831,2823,3631,4623,5431,6423,7231,8223,9031,10023,11015,11823,12815,13619,14600,15404,16385,17173,17690,17690,17690,17690,21,20,0,0,0,0,0,0,0x1c4580,0x5cca00,0xaf5000,0x101d600,0x1425a80,0x194e080,0x1d56500,0x227eb00,0x2686f80,0x2baf580,0x2fb7a00,0x34e0000,0x3a08600,0x3e10a80,0x4339080,0x473eb00,0x4c5fd80,0x5065800,0x5586a80,0x5981d00,0x5c23c80,0x5c23c80,0x5c23c80,0x5c23c80,0x5c23c80,0x5c23c80,0x5c23c80,0x5c23c80,0x5c23c80,0x5c23c80,0x5c23c80,0x5c23c80,32,1024,1832,2824,3632,4624,5432,6424,7232,8224,9216,10024,11016,11824,12816,18436,14601,18451,16386,18482,18482,18482,18482,18482,20,19,0,0,0,0,0,0,0x1c5000,0x6ed600,0xaf5a80,0x101e080,0x1426500,0x194eb00,0x1d56f80,0x227f580,0x2687a00,0x2bb0000,0x30d8600,0x34e0a80,0x3a09080,0x3e11500,0x4339b00,0x63480,0x4c60800,0x6d200,0x5587500,0x81780,0x81780,0x81780,0x81780,0x81780,0x81780,0x81780,0x81780,0x81780,0x81780,0x81780,0x81780,0x81780,33,1025,1833,2825,3633,4625,5433,6425,7233,8225,9217,10025,11017,11825,12817,13620,14602,15405,16387,18483,18483,18483,18483,18483,20,19,0,0,0,0,0,0,0x1c5a80,0x6ee080,0xaf6500,0x101eb00,0x1426f80,0x194f580,0x1d57a00,0x2280000,0x2688480,0x2bb0a80,0x30d9080,0x34e1500,0x3a09b00,0x3e11f80,0x433a580,0x473f580,0x4c61280,0x5066280,0x5587f80,0x82200,0x82200,0x82200,0x82200,0x82200,0x82200,0x82200,0x82200,0x82200,0x82200,0x82200,0x82200,0x82200,34,1026,1834,2826,3634,4626,5434,6426,7234,8226,9218,10026,11018,11826,12818,13621,14603,15406,16388,17174,17691,18220,18220,18220,22,21,0,0,0,0,0,0,0x1c6500,0x6eeb00,0xaf6f80,0x101f580,0x1427a00,0x1950000,0x1d58480,0x2280a80,0x2688f00,0x2bb1500,0x30d9b00,0x34e1f80,0x3a0a580,0x3e12a00,0x433b000,0x4740000,0x4c61d00,0x5066d00,0x5588a00,0x5982780,0x5c24700,0x5ece480,0x5ece480,0x5ece480,0x5ece480,0x5ece480,0x5ece480,0x5ece480,0x5ece480,0x5ece480,0x5ece480,0x5ece480,35,1027,1835,2827,3635,4627,5435,6427,7235,8227,9219,10027,11019,11827,12819,13622,14604,15407,16389,17175,17692,17692,17692,17692,21,20,0,0,0,0,0,0,0x1c6f80,0x6ef580,0xaf7a00,0x1020000,0x1428480,0x1950a80,0x1d58f00,0x2281500,0x2689980,0x2bb1f80,0x30da580,0x34e2a00,0x3a0b000,0x3e13480,0x433ba80,0x4740a80,0x4c62780,0x5067780,0x5589480,0x5983200,0x5c25180,0x5c25180,0x5c25180,0x5c25180,0x5c25180,0x5c25180,0x5c25180,0x5c25180,0x5c25180,0x5c25180,0x5c25180,0x5c25180,36,1028,1836,2828,3636,4628,5436,6428,7236,8228,9220,10028,11020,11828,12820,13623,14605,15408,16390,17176,17693,17693,17693,17693,21,20,0,0,0,0,0,0,0x1c7a00,0x6f0000,0xaf8480,0x1020a80,0x1428f00,0x1951500,0x1d59980,0x2281f80,0x268a400,0x2bb2a00,0x30db000,0x34e3480,0x3a0ba80,0x3e13f00,0x433c500,0x4741500,0x4c63200,0x5068200,0x5589f00,0x5983c80,0x5c25c00,0x5c25c00,0x5c25c00,0x5c25c00,0x5c25c00,0x5c25c00,0x5c25c00,0x5c25c00,0x5c25c00,0x5c25c00,0x5c25c00,0x5c25c00,37,1029,1837,2829,3637,4629,5437,6429,7237,8229,9221,10029,11021,11829,12821,13624,14606,15409,16391,18484,18484,18484,18484,18484,20,19,0,0,0,0,0,0,0x1c8480,0x6f0a80,0xaf8f00,0x1021500,0x1429980,0x1951f80,0x1d5a400,0x2282a00,0x268ae80,0x2bb3480,0x30dba80,0x34e3f00,0x3a0c500,0x3e14980,0x433cf80,0x4741f80,0x4c63c80,0x5068c80,0x558a980,0x82c80,0x82c80,0x82c80,0x82c80,0x82c80,0x82c80,0x82c80,0x82c80,0x82c80,0x82c80,0x82c80,0x82c80,0x82c80,38,1030,1838,2830,3638,4630,5438,6430,7238,8230,9222,10030,11022,11830,12822,13625,14607,15410,16392,18485,17694,17694,17694,17694,21,20,0,0,0,0,0,0,0x1c8f00,0x6f1500,0xaf9980,0x1021f80,0x142a400,0x1952a00,0x1d5ae80,0x2283480,0x268b900,0x2bb3f00,0x30dc500,0x34e4980,0x3a0cf80,0x3e15400,0x433da00,0x4742a00,0x4c64700,0x5069700,0x558b400,0x83700,0x5c26680,0x5c26680,0x5c26680,0x5c26680,0x5c26680,0x5c26680,0x5c26680,0x5c26680,0x5c26680,0x5c26680,0x5c26680,0x5c26680,39,1031,1839,2831,3639,4631,5439,6431,7239,8231,9223,10031,11023,11831,12823,13626,14608,15411,16393,17177,17695,17695,17695,17695,21,20,0,0,0,0,0,0,0x1c9980,0x6f1f80,0xafa400,0x1022a00,0x142ae80,0x1953480,0x1d5b900,0x2283f00,0x268c380,0x2bb4980,0x30dcf80,0x34e5400,0x3a0da00,0x3e15e80,0x433e480,0x4743480,0x4c65180,0x506a180,0x558be80,0x5984700,0x5c27100,0x5c27100,0x5c27100,0x5c27100,0x5c27100,0x5c27100,0x5c27100,0x5c27100,0x5c27100,0x5c27100,0x5c27100,0x5c27100,40,1032,1840,2832,3640,4632,5440,6432,7424,8232,9224,10032,11024,11832,12824,18437,14609,18452,16394,18486,18486,18486,18486,18486,20,19,0,0,0,0,0,0,0x1ca400,0x6f2a00,0xafae80,0x1023480,0x142b900,0x1953f00,0x1d5c380,0x2284980,0x27acf80,0x2bb5400,0x30dda00,0x34e5e80,0x3a0e480,0x3e16900,0x433ef00,0x63f00,0x4c65c00,0x6dc80,0x558c900,0x84180,0x84180,0x84180,0x84180,0x84180,0x84180,0x84180,0x84180,0x84180,0x84180,0x84180,0x84180,0x84180,41,1033,1841,2833,3641,4633,5441,6433,7425,8233,9225,10033,11025,11833,12825,13627,14610,15412,16395,18487,18487,18487,18487,18487,20,19,0,0,0,0,0,0,0x1cae80,0x6f3480,0xafb900,0x1023f00,0x142c380,0x1954980,0x1d5ce00,0x2285400,0x27ada00,0x2bb5e80,0x30de480,0x34e6900,0x3a0ef00,0x3e17380,0x433f980,0x4743f00,0x4c66680,0x506ac00,0x558d380,0x84c00,0x84c00,0x84c00,0x84c00,0x84c00,0x84c00,0x84c00,0x84c00,0x84c00,0x84c00,0x84c00,0x84c00,0x84c00,42,1034,1842,2834,3642,4634,5442,6434,7426,8234,9226,10034,11026,11834,12826,13628,14611,15413,16396,17178,17696,18221,18221,18221,22,21,0,0,0,0,0,0,0x1cb900,0x6f3f00,0xafc380,0x1024980,0x142ce00,0x1955400,0x1d5d880,0x2285e80,0x27ae480,0x2bb6900,0x30def00,0x34e7380,0x3a0f980,0x3e17e00,0x4340400,0x4744980,0x4c67100,0x506b680,0x558de00,0x5985180,0x5c27b80,0x5ecef00,0x5ecef00,0x5ecef00,0x5ecef00,0x5ecef00,0x5ecef00,0x5ecef00,0x5ecef00,0x5ecef00,0x5ecef00,0x5ecef00,43,1035,1843,2835,3643,4635,5443,6435,7427,8235,9227,10035,11027,11835,12827,13629,14612,15414,16397,17179,17697,17697,17697,17697,21,20,0,0,0,0,0,0,0x1cc380,0x6f4980,0xafce00,0x1025400,0x142d880,0x1955e80,0x1d5e300,0x2286900,0x27aef00,0x2bb7380,0x30df980,0x34e7e00,0x3a10400,0x3e18880,0x4340e80,0x4745400,0x4c67b80,0x506c100,0x558e880,0x5985c00,0x5c28600,0x5c28600,0x5c28600,0x5c28600,0x5c28600,0x5c28600,0x5c28600,0x5c28600,0x5c28600,0x5c28600,0x5c28600,0x5c28600,44,1036,1844,2836,3644,4636,5444,6436,7428,8236,9228,10036,11028,11836,12828,13630,14613,15415,16398,17180,17698,17698,17698,17698,21,20,0,0,0,0,0,0,0x1cce00,0x6f5400,0xafd880,0x1025e80,0x142e300,0x1956900,0x1d5ed80,0x2287380,0x27af980,0x2bb7e00,0x30e0400,0x34e8880,0x3a10e80,0x3e19300,0x4341900,0x4745e80,0x4c68600,0x506cb80,0x558f300,0x5986680,0x5c29080,0x5c29080,0x5c29080,0x5c29080,0x5c29080,0x5c29080,0x5c29080,0x5c29080,0x5c29080,0x5c29080,0x5c29080,0x5c29080,45,1037,1845,2837,3645,4637,5445,6437,7429,8237,9229,10037,11029,11837,12829,13631,14614,15416,16399,18488,18488,18488,18488,18488,20,19,0,0,0,0,0,0,0x1cd880,0x6f5e80,0xafe300,0x1026900,0x142ed80,0x1957380,0x1d5f800,0x2287e00,0x27b0400,0x2bb8880,0x30e0e80,0x34e9300,0x3a11900,0x3e19d80,0x4342380,0x4746900,0x4c69080,0x506d600,0x558fd80,0x85680,0x85680,0x85680,0x85680,0x85680,0x85680,0x85680,0x85680,0x85680,0x85680,0x85680,0x85680,0x85680,46,1038,1846,2838,3646,4638,5446,6438,7430,8238,9230,10038,11030,11838,12830,13632,14615,15417,16400,18489,17699,17699,17699,17699,21,20,0,0,0,0,0,0,0x1ce300,0x6f6900,0xafed80,0x1027380,0x142f800,0x1957e00,0x1d60280,0x2288880,0x27b0e80,0x2bb9300,0x30e1900,0x34e9d80,0x3a12380,0x3e1a800,0x4342e00,0x4747380,0x4c69b00,0x506e080,0x5590800,0x86100,0x5c29b00,0x5c29b00,0x5c29b00,0x5c29b00,0x5c29b00,0x5c29b00,0x5c29b00,0x5c29b00,0x5c29b00,0x5c29b00,0x5c29b00,0x5c29b00,47,1039,1847,2839,3647,4639,5447,6439,7431,8239,9231,10039,11031,11839,12831,13633,14616,15418,16401,17181,17700,17700,17700,17700,21,20,0,0,0,0,0,0,0x1ced80,0x6f7380,0xaff800,0x1027e00,0x1430280,0x1958880,0x1d60d00,0x2289300,0x27b1900,0x2bb9d80,0x30e2380,0x34ea800,0x3a12e00,0x3e1b280,0x4343880,0x4747e00,0x4c6a580,0x506eb00,0x5591280,0x5987100,0x5c2a580,0x5c2a580,0x5c2a580,0x5c2a580,0x5c2a580,0x5c2a580,0x5c2a580,0x5c2a580,0x5c2a580,0x5c2a580,0x5c2a580,0x5c2a580,48,1040,1848,2840,3648,4640,5632,6440,7432,8240,9232,10040,11032,11840,12832,18438,14617,18453,16402,18490,18490,18490,18490,18490,20,19,0,0,0,0,0,0,0x1cf800,0x6f7e00,0xb00280,0x1028880,0x1430d00,0x1959300,0x1e81900,0x2289d80,0x27b2380,0x2bba800,0x30e2e00,0x34eb280,0x3a13880,0x3e1bd00,0x4344300,0x64980,0x4c6b000,0x6e700,0x5591d00,0x86b80,0x86b80,0x86b80,0x86b80,0x86b80,0x86b80,0x86b80,0x86b80,0x86b80,0x86b80,0x86b80,0x86b80,0x86b80,49,1041,1849,2841,3649,4641,5633,6441,7433,8241,9233,10041,11033,11841,12833,13634,14618,15419,16403,18491,18491,18491,18491,18491,20,19,0,0,0,0,0,0,0x1d0280,0x6f8880,0xb00d00,0x1029300,0x1431780,0x1959d80,0x1e82380,0x228a800,0x27b2e00,0x2bbb280,0x30e3880,0x34ebd00,0x3a14300,0x3e1c780,0x4344d80,0x4748880,0x4c6ba80,0x506f580,0x5592780,0x87600,0x87600,0x87600,0x87600,0x87600,0x87600,0x87600,0x87600,0x87600,0x87600,0x87600,0x87600,0x87600,50,1042,1850,2842,3650,4642,5634,6442,7434,8242,9234,10042,11034,11842,12834,13635,14619,15420,16404,17182,17701,18222,18222,18222,22,21,0,0,0,0,0,0,0x1d0d00,0x6f9300,0xb01780,0x1029d80,0x1432200,0x195a800,0x1e82e00,0x228b280,0x27b3880,0x2bbbd00,0x30e4300,0x34ec780,0x3a14d80,0x3e1d200,0x4345800,0x4749300,0x4c6c500,0x5070000,0x5593200,0x5987b80,0x5c2b000,0x5ecf980,0x5ecf980,0x5ecf980,0x5ecf980,0x5ecf980,0x5ecf980,0x5ecf980,0x5ecf980,0x5ecf980,0x5ecf980,0x5ecf980,51,1043,1851,2843,3651,4643,5635,6443,7435,8243,9235,10043,11035,11843,12835,13636,14620,15421,16405,17183,17702,17702,17702,17702,21,20,0,0,0,0,0,0,0x1d1780,0x6f9d80,0xb02200,0x102a800,0x1432c80,0x195b280,0x1e83880,0x228bd00,0x27b4300,0x2bbc780,0x30e4d80,0x34ed200,0x3a15800,0x3e1dc80,0x4346280,0x4749d80,0x4c6cf80,0x5070a80,0x5593c80,0x5988600,0x5c2ba80,0x5c2ba80,0x5c2ba80,0x5c2ba80,0x5c2ba80,0x5c2ba80,0x5c2ba80,0x5c2ba80,0x5c2ba80,0x5c2ba80,0x5c2ba80,0x5c2ba80,52,1044,1852,2844,3652,4644,5636,6444,7436,8244,9236,10044,11036,11844,12836,13637,14621,15422,16406,17184,17703,17703,17703,17703,21,20,0,0,0,0,0,0,0x1d2200,0x6fa800,0xb02c80,0x102b280,0x1433700,0x195bd00,0x1e84300,0x228c780,0x27b4d80,0x2bbd200,0x30e5800,0x34edc80,0x3a16280,0x3e1e700,0x4346d00,0x474a800,0x4c6da00,0x5071500,0x5594700,0x5989080,0x5c2c500,0x5c2c500,0x5c2c500,0x5c2c500,0x5c2c500,0x5c2c500,0x5c2c500,0x5c2c500,0x5c2c500,0x5c2c500,0x5c2c500,0x5c2c500,53,1045,1853,2845,3653,4645,5637,6445,7437,8245,9237,10045,11037,11845,12837,13638,14622,15423,16407,18492,18492,18492,18492,18492,20,19,0,0,0,0,0,0,0x1d2c80,0x6fb280,0xb03700,0x102bd00,0x1434180,0x195c780,0x1e84d80,0x228d200,0x27b5800,0x2bbdc80,0x30e6280,0x34ee700,0x3a16d00,0x3e1f180,0x4347780,0x474b280,0x4c6e480,0x5071f80,0x5595180,0x88080,0x88080,0x88080,0x88080,0x88080,0x88080,0x88080,0x88080,0x88080,0x88080,0x88080,0x88080,0x88080,54,1046,1854,2846,3654,4646,5638,6446,7438,8246,9238,10046,11038,11846,12838,13639,14623,15424,16408,18493,17704,17704,17704,17704,21,20,0,0,0,0,0,0,0x1d3700,0x6fbd00,0xb04180,0x102c780,0x1434c00,0x195d200,0x1e85800,0x228dc80,0x27b6280,0x2bbe700,0x30e6d00,0x34ef180,0x3a17780,0x3e1fc00,0x4348200,0x474bd00,0x4c6ef00,0x5072a00,0x5595c00,0x88b00,0x5c2cf80,0x5c2cf80,0x5c2cf80,0x5c2cf80,0x5c2cf80,0x5c2cf80,0x5c2cf80,0x5c2cf80,0x5c2cf80,0x5c2cf80,0x5c2cf80,0x5c2cf80,55,1047,1855,2847,3655,4647,5639,6447,7439,8247,9239,10047,11039,11847,12839,13824,14624,15425,16409,17185,17705,17705,17705,17705,21,20,0,0,0,0,0,0,0x1d4180,0x6fc780,0xb04c00,0x102d200,0x1435680,0x195dc80,0x1e86280,0x228e700,0x27b6d00,0x2bbf180,0x30e7780,0x34efc00,0x3a18200,0x3e20680,0x4348c80,0x486c900,0x4c6f980,0x5073480,0x5596680,0x5989b00,0x5c2da00,0x5c2da00,0x5c2da00,0x5c2da00,0x5c2da00,0x5c2da00,0x5c2da00,0x5c2da00,0x5c2da00,0x5c2da00,0x5c2da00,0x5c2da00,56,1048,1856,2848,3840,4648,5640,6448,7440,8248,9240,10048,11040,12032,12840,18439,14625,18454,16410,18494,18494,18494,18494,18494,20,19,0,0,0,0,0,0,0x1d4c00,0x6fd200,0xb05680,0x102dc80,0x1556280,0x195e700,0x1e86d00,0x228f180,0x27b7780,0x2bbfc00,0x30e8200,0x34f0680,0x3a18c80,0x3f41280,0x4349700,0x65400,0x4c70400,0x6f180,0x5597100,0x89580,0x89580,0x89580,0x89580,0x89580,0x89580,0x89580,0x89580,0x89580,0x89580,0x89580,0x89580,0x89580,57,1049,1857,2849,3841,4649,5641,6449,7441,8249,9241,10049,11041,12033,12841,13825,14626,15426,16411,18495,18495,18495,18495,18495,20,19,0,0,0,0,0,0,0x1d5680,0x6fdc80,0xb06100,0x102e700,0x1556d00,0x195f180,0x1e87780,0x228fc00,0x27b8200,0x2bc0680,0x30e8c80,0x34f1100,0x3a19700,0x3f41d00,0x434a180,0x486d380,0x4c70e80,0x5073f00,0x5597b80,0x8a000,0x8a000,0x8a000,0x8a000,0x8a000,0x8a000,0x8a000,0x8a000,0x8a000,0x8a000,0x8a000,0x8a000,0x8a000,58,1050,1858,2850,3842,4650,5642,6450,7442,8250,9242,10050,11042,12034,12842,13826,14627,15427,16412,17186,17706,18223,18223,18223,22,21,0,0,0,0,0,0,0x1d6100,0x6fe700,0xb06b80,0x102f180,0x1557780,0x195fc00,0x1e88200,0x2290680,0x27b8c80,0x2bc1100,0x30e9700,0x34f1b80,0x3a1a180,0x3f42780,0x434ac00,0x486de00,0x4c71900,0x5074980,0x5598600,0x598a580,0x5c2e480,0x5ed0400,0x5ed0400,0x5ed0400,0x5ed0400,0x5ed0400,0x5ed0400,0x5ed0400,0x5ed0400,0x5ed0400,0x5ed0400,0x5ed0400,59,1051,1859,2851,3843,4651,5643,6451,7443,8251,9243,10051,11043,12035,12843,13827,14628,15428,16413,17187,17707,17707,17707,17707,21,20,0,0,0,0,0,0,0x1d6b80,0x6ff180,0xb07600,0x102fc00,0x1558200,0x1960680,0x1e88c80,0x2291100,0x27b9700,0x2bc1b80,0x30ea180,0x34f2600,0x3a1ac00,0x3f43200,0x434b680,0x486e880,0x4c72380,0x5075400,0x5599080,0x598b000,0x5c2ef00,0x5c2ef00,0x5c2ef00,0x5c2ef00,0x5c2ef00,0x5c2ef00,0x5c2ef00,0x5c2ef00,0x5c2ef00,0x5c2ef00,0x5c2ef00,0x5c2ef00,60,1052,1860,2852,3844,4652,5644,6452,7444,8252,9244,10052,11044,12036,12844,13828,14629,15429,16414,17188,17708,17708,17708,17708,21,20,0,0,0,0,0,0,0x1d7600,0x6ffc00,0xb08080,0x1030680,0x1558c80,0x1961100,0x1e89700,0x2291b80,0x27ba180,0x2bc2600,0x30eac00,0x34f3080,0x3a1b680,0x3f43c80,0x434c100,0x486f300,0x4c72e00,0x5075e80,0x5599b00,0x598ba80,0x5c2f980,0x5c2f980,0x5c2f980,0x5c2f980,0x5c2f980,0x5c2f980,0x5c2f980,0x5c2f980,0x5c2f980,0x5c2f980,0x5c2f980,0x5c2f980,61,1053,1861,2853,3845,4653,5645,6453,7445,8253,9245,10053,11045,12037,12845,13829,14630,15430,16415,18496,18496,18496,18496,18496,20,19,0,0,0,0,0,0,0x1d8080,0x700680,0xb08b00,0x1031100,0x1559700,0x1961b80,0x1e8a180,0x2292600,0x27bac00,0x2bc3080,0x30eb680,0x34f3b00,0x3a1c100,0x3f44700,0x434cb80,0x486fd80,0x4c73880,0x5076900,0x559a580,0x8aa80,0x8aa80,0x8aa80,0x8aa80,0x8aa80,0x8aa80,0x8aa80,0x8aa80,0x8aa80,0x8aa80,0x8aa80,0x8aa80,0x8aa80,62,1054,1862,2854,3846,4654,5646,6454,7446,8254,9246,10054,11046,12038,12846,13830,14631,15431,16416,18497,17709,17709,17709,17709,21,20,0,0,0,0,0,0,0x1d8b00,0x701100,0xb09580,0x1031b80,0x155a180,0x1962600,0x1e8ac00,0x2293080,0x27bb680,0x2bc3b00,0x30ec100,0x34f4580,0x3a1cb80,0x3f45180,0x434d600,0x4870800,0x4c74300,0x5077380,0x559b000,0x8b500,0x5c30400,0x5c30400,0x5c30400,0x5c30400,0x5c30400,0x5c30400,0x5c30400,0x5c30400,0x5c30400,0x5c30400,0x5c30400,0x5c30400,63,1055,1863,2855,3847,4655,5647,6455,7447,8255,9247,10055,11047,12039,12847,13831,14632,15616,16417,17189,17710,17710,17710,17710,21,20,0,0,0,0,0,0,0x1d9580,0x701b80,0xb0a000,0x1032600,0x155ac00,0x1963080,0x1e8b680,0x2293b00,0x27bc100,0x2bc4580,0x30ecb80,0x34f5000,0x3a1d600,0x3f45c00,0x434e080,0x4871280,0x4c74d80,0x5197f80,0x559ba80,0x598c500,0x5c30e80,0x5c30e80,0x5c30e80,0x5c30e80,0x5c30e80,0x5c30e80,0x5c30e80,0x5c30e80,0x5c30e80,0x5c30e80,0x5c30e80,0x5c30e80,64,1056,2048,2856,3848,4656,5648,6456,7448,8256,9248,10240,11048,12040,12848,18440,14633,18455,16418,18498,18498,18498,18498,18498,20,19,0,0,0,0,0,0,0x1da000,0x702600,0xc2ac00,0x1033080,0x155b680,0x1963b00,0x1e8c100,0x2294580,0x27bcb80,0x2bc5000,0x30ed600,0x3615c00,0x3a1e080,0x3f46680,0x434eb00,0x65e80,0x4c75800,0x6fc00,0x559c500,0x8bf80,0x8bf80,0x8bf80,0x8bf80,0x8bf80,0x8bf80,0x8bf80,0x8bf80,0x8bf80,0x8bf80,0x8bf80,0x8bf80,0x8bf80,65,1057,2049,2857,3849,4657,5649,6457,7449,8257,9249,10241,11049,12041,12849,13832,14634,15617,16419,18499,18499,18499,18499,18499,20,19,0,0,0,0,0,0,0x1daa80,0x703080,0xc2b680,0x1033b00,0x155c100,0x1964580,0x1e8cb80,0x2295000,0x27bd600,0x2bc5a80,0x30ee080,0x3616680,0x3a1eb00,0x3f47100,0x434f580,0x4871d00,0x4c76280,0x5198a00,0x559cf80,0x8ca00,0x8ca00,0x8ca00,0x8ca00,0x8ca00,0x8ca00,0x8ca00,0x8ca00,0x8ca00,0x8ca00,0x8ca00,0x8ca00,0x8ca00,66,1058,2050,2858,3850,4658,5650,6458,7450,8258,9250,10242,11050,12042,12850,13833,14635,15618,16420,17190,17711,18224,18224,18224,22,21,0,0,0,0,0,0,0x1db500,0x703b00,0xc2c100,0x1034580,0x155cb80,0x1965000,0x1e8d600,0x2295a80,0x27be080,0x2bc6500,0x30eeb00,0x3617100,0x3a1f580,0x3f47b80,0x4350000,0x4872780,0x4c76d00,0x5199480,0x559da00,0x598cf80,0x5c31900,0x5ed0e80,0x5ed0e80,0x5ed0e80,0x5ed0e80,0x5ed0e80,0x5ed0e80,0x5ed0e80,0x5ed0e80,0x5ed0e80,0x5ed0e80,0x5ed0e80,67,1059,2051,2859,3851,4659,5651,6459,7451,8259,9251,10243,11051,12043,12851,13834,14636,15619,16421,17191,17712,17712,17712,17712,21,20,0,0,0,0,0,0,0x1dbf80,0x704580,0xc2cb80,0x1035000,0x155d600,0x1965a80,0x1e8e080,0x2296500,0x27beb00,0x2bc6f80,0x30ef580,0x3617b80,0x3a20000,0x3f48600,0x4350a80,0x4873200,0x4c77780,0x5199f00,0x559e480,0x598da00,0x5c32380,0x5c32380,0x5c32380,0x5c32380,0x5c32380,0x5c32380,0x5c32380,0x5c32380,0x5c32380,0x5c32380,0x5c32380,0x5c32380,68,1060,2052,2860,3852,4660,5652,6460,7452,8260,9252,10244,11052,12044,12852,13835,14637,15620,16422,17192,17713,17713,17713,17713,21,20,0,0,0,0,0,0,0x1dca00,0x705000,0xc2d600,0x1035a80,0x155e080,0x1966500,0x1e8eb00,0x2296f80,0x27bf580,0x2bc7a00,0x30f0000,0x3618600,0x3a20a80,0x3f49080,0x4351500,0x4873c80,0x4c78200,0x519a980,0x559ef00,0x598e480,0x5c32e00,0x5c32e00,0x5c32e00,0x5c32e00,0x5c32e00,0x5c32e00,0x5c32e00,0x5c32e00,0x5c32e00,0x5c32e00,0x5c32e00,0x5c32e00,69,1061,2053,2861,3853,4661,5653,6461,7453,8261,9253,10245,11053,12045,12853,13836,14638,15621,16423,18500,18500,18500,18500,18500,20,19,0,0,0,0,0,0,0x1dd480,0x705a80,0xc2e080,0x1036500,0x155eb00,0x1966f80,0x1e8f580,0x2297a00,0x27c0000,0x2bc8480,0x30f0a80,0x3619080,0x3a21500,0x3f49b00,0x4351f80,0x4874700,0x4c78c80,0x519b400,0x559f980,0x8d480,0x8d480,0x8d480,0x8d480,0x8d480,0x8d480,0x8d480,0x8d480,0x8d480,0x8d480,0x8d480,0x8d480,0x8d480,70,1062,2054,2862,3854,4662,5654,6462,7454,8262,9254,10246,11054,12046,12854,13837,14639,15622,16424,18501,17714,17714,17714,17714,21,20,0,0,0,0,0,0,0x1ddf00,0x706500,0xc2eb00,0x1036f80,0x155f580,0x1967a00,0x1e90000,0x2298480,0x27c0a80,0x2bc8f00,0x30f1500,0x3619b00,0x3a21f80,0x3f4a580,0x4352a00,0x4875180,0x4c79700,0x519be80,0x55a0400,0x8df00,0x5c33880,0x5c33880,0x5c33880,0x5c33880,0x5c33880,0x5c33880,0x5c33880,0x5c33880,0x5c33880,0x5c33880,0x5c33880,0x5c33880,71,1063,2055,2863,3855,4663,5655,6463,7455,8263,9255,10247,11055,12047,12855,13838,14640,15623,16425,17193,17715,17715,17715,17715,21,20,0,0,0,0,0,0,0x1de980,0x706f80,0xc2f580,0x1037a00,0x1560000,0x1968480,0x1e90a80,0x2298f00,0x27c1500,0x2bc9980,0x30f1f80,0x361a580,0x3a22a00,0x3f4b000,0x4353480,0x4875c00,0x4c7a180,0x519c900,0x55a0e80,0x598ef00,0x5c34300,0x5c34300,0x5c34300,0x5c34300,0x5c34300,0x5c34300,0x5c34300,0x5c34300,0x5c34300,0x5c34300,0x5c34300,0x5c34300,256,1064,2056,2864,3856,4664,5656,6464,7456,8448,9256,10248,11056,12048,12856,18441,14641,18456,16426,18502,18502,18502,18502,18502,20,19,0,0,0,0,0,0,0x2ff580,0x707a00,0xc30000,0x1038480,0x1560a80,0x1968f00,0x1e91500,0x2299980,0x27c1f80,0x2cea580,0x30f2a00,0x361b000,0x3a23480,0x3f4ba80,0x4353f00,0x66900,0x4c7ac00,0x70680,0x55a1900,0x8e980,0x8e980,0x8e980,0x8e980,0x8e980,0x8e980,0x8e980,0x8e980,0x8e980,0x8e980,0x8e980,0x8e980,0x8e980,257,1065,2057,2865,3857,4665,5657,6465,7457,8449,9257,10249,11057,12049,12857,13839,14642,15624,16427,18503,18503,18503,18503,18503,20,19,0,0,0,0,0,0,0x300000,0x708480,0xc30a80,0x1038f00,0x1561500,0x1969980,0x1e91f80,0x229a400,0x27c2a00,0x2ceb000,0x30f3480,0x361ba80,0x3a23f00,0x3f4c500,0x4354980,0x4876680,0x4c7b680,0x519d380,0x55a2380,0x8f400,0x8f400,0x8f400,0x8f400,0x8f400,0x8f400,0x8f400,0x8f400,0x8f400,0x8f400,0x8f400,0x8f400,0x8f400,258,1066,2058,2866,3858,4666,5658,6466,7458,8450,9258,10250,11058,12050,12858,13840,14643,15625,16428,17194,17716,18225,18225,18225,22,21,0,0,0,0,0,0,0x300a80,0x708f00,0xc31500,0x1039980,0x1561f80,0x196a400,0x1e92a00,0x229ae80,0x27c3480,0x2ceba80,0x30f3f00,0x361c500,0x3a24980,0x3f4cf80,0x4355400,0x4877100,0x4c7c100,0x519de00,0x55a2e00,0x598f980,0x5c34d80,0x5ed1900,0x5ed1900,0x5ed1900,0x5ed1900,0x5ed1900,0x5ed1900,0x5ed1900,0x5ed1900,0x5ed1900,0x5ed1900,0x5ed1900,259,1067,2059,2867,3859,4667,5659,6467,7459,8451,9259,10251,11059,12051,12859,13841,14644,15626,16429,17195,17717,17717,17717,17717,21,20,0,0,0,0,0,0,0x301500,0x709980,0xc31f80,0x103a400,0x1562a00,0x196ae80,0x1e93480,0x229b900,0x27c3f00,0x2cec500,0x30f4980,0x361cf80,0x3a25400,0x3f4da00,0x4355e80,0x4877b80,0x4c7cb80,0x519e880,0x55a3880,0x5990400,0x5c35800,0x5c35800,0x5c35800,0x5c35800,0x5c35800,0x5c35800,0x5c35800,0x5c35800,0x5c35800,0x5c35800,0x5c35800,0x5c35800,260,1068,2060,2868,3860,4668,5660,6468,7460,8452,9260,10252,11060,12052,12860,13842,14645,15627,16430,17196,17718,17718,17718,17718,21,20,0,0,0,0,0,0,0x301f80,0x70a400,0xc32a00,0x103ae80,0x1563480,0x196b900,0x1e93f00,0x229c380,0x27c4980,0x2cecf80,0x30f5400,0x361da00,0x3a25e80,0x3f4e480,0x4356900,0x4878600,0x4c7d600,0x519f300,0x55a4300,0x5990e80,0x5c36280,0x5c36280,0x5c36280,0x5c36280,0x5c36280,0x5c36280,0x5c36280,0x5c36280,0x5c36280,0x5c36280,0x5c36280,0x5c36280,261,1069,2061,2869,3861,4669,5661,6469,7461,8453,9261,10253,11061,12053,12861,13843,14646,15628,16431,18504,18504,18504,18504,18504,20,19,0,0,0,0,0,0,0x302a00,0x70ae80,0xc33480,0x103b900,0x1563f00,0x196c380,0x1e94980,0x229ce00,0x27c5400,0x2ceda00,0x30f5e80,0x361e480,0x3a26900,0x3f4ef00,0x4357380,0x4879080,0x4c7e080,0x519fd80,0x55a4d80,0x60000,0x60000,0x60000,0x60000,0x60000,0x60000,0x60000,0x60000,0x60000,0x60000,0x60000,0x60000,0x60000,262,1070,2062,2870,3862,4670,5662,6470,7462,8454,9262,10254,11062,12054,12862,13844,14647,15629,16432,72,17719,17719,17719,17719,21,20,0,0,0,0,0,0,0x303480,0x70b900,0xc33f00,0x103c380,0x1564980,0x196ce00,0x1e95400,0x229d880,0x27c5e80,0x2cee480,0x30f6900,0x361ef00,0x3a27380,0x3f4f980,0x4357e00,0x4879b00,0x4c7eb00,0x51a0800,0x55a5800,0x1af580,0x5c36d00,0x5c36d00,0x5c36d00,0x5c36d00,0x5c36d00,0x5c36d00,0x5c36d00,0x5c36d00,0x5c36d00,0x5c36d00,0x5c36d00,0x5c36d00,263,1071,2063,2871,3863,4671,5663,6471,7463,8455,9263,10255,11063,12055,12863,13845,14648,15630,16433,17197,17720,17720,17720,17720,21,20,0,0,0,0,0,0,0x303f00,0x70c380,0xc34980,0x103ce00,0x1565400,0x196d880,0x1e95e80,0x229e300,0x27c6900,0x2ceef00,0x30f7380,0x361f980,0x3a27e00,0x3f50400,0x4358880,0x487a580,0x4c7f580,0x51a1280,0x55a6280,0x5991900,0x5c37780,0x5c37780,0x5c37780,0x5c37780,0x5c37780,0x5c37780,0x5c37780,0x5c37780,0x5c37780,0x5c37780,0x5c37780,0x5c37780,264,1072,2064,2872,3864,4672,5664,6656,7464,8456,9264,10256,11064,12056,12864,18442,14649,18457,16434,328,328,328,328,328,20,19,0,0,0,0,0,0,0x304980,0x70ce00,0xc35400,0x103d880,0x1565e80,0x196e300,0x1e96900,0x23bef00,0x27c7380,0x2cef980,0x30f7e00,0x3620400,0x3a28880,0x3f50e80,0x4359300,0x67380,0x4c80000,0x71100,0x55a6d00,0x2feb00,0x2feb00,0x2feb00,0x2feb00,0x2feb00,0x2feb00,0x2feb00,0x2feb00,0x2feb00,0x2feb00,0x2feb00,0x2feb00,0x2feb00,265,1073,2065,2873,3865,4673,5665,6657,7465,8457,9265,10257,11065,12057,12865,13846,14650,15631,16435,584,584,584,584,584,20,19,0,0,0,0,0,0,0x305400,0x70d880,0xc35e80,0x103e300,0x1566900,0x196ed80,0x1e97380,0x23bf980,0x27c7e00,0x2cf0400,0x30f8880,0x3620e80,0x3a29300,0x3f51900,0x4359d80,0x487b000,0x4c80a80,0x51a1d00,0x55a7780,0x44e080,0x44e080,0x44e080,0x44e080,0x44e080,0x44e080,0x44e080,0x44e080,0x44e080,0x44e080,0x44e080,0x44e080,0x44e080,266,1074,2066,2874,3866,4674,5666,6658,7466,8458,9266,10258,11066,12058,12866,13847,14651,15632,16436,17198,17721,18226,18226,18226,22,21,0,0,0,0,0,0,0x305e80,0x70e300,0xc36900,0x103ed80,0x1567380,0x196f800,0x1e97e00,0x23c0400,0x27c8880,0x2cf0e80,0x30f9300,0x3621900,0x3a29d80,0x3f52380,0x435a800,0x487ba80,0x4c81500,0x51a2780,0x55a8200,0x5992380,0x5c38200,0x5ed2380,0x5ed2380,0x5ed2380,0x5ed2380,0x5ed2380,0x5ed2380,0x5ed2380,0x5ed2380,0x5ed2380,0x5ed2380,0x5ed2380,267,1075,2067,2875,3867,4675,5667,6659,7467,8459,9267,10259,11067,12059,12867,13848,14652,15633,16437,17199,17722,17722,17722,17722,21,20,0,0,0,0,0,0,0x306900,0x70ed80,0xc37380,0x103f800,0x1567e00,0x1970280,0x1e98880,0x23c0e80,0x27c9300,0x2cf1900,0x30f9d80,0x3622380,0x3a2a800,0x3f52e00,0x435b280,0x487c500,0x4c81f80,0x51a3200,0x55a8c80,0x5992e00,0x5c38c80,0x5c38c80,0x5c38c80,0x5c38c80,0x5c38c80,0x5c38c80,0x5c38c80,0x5c38c80,0x5c38c80,0x5c38c80,0x5c38c80,0x5c38c80,268,1076,2068,2876,3868,4676,5668,6660,7468,8460,9268,10260,11068,12060,12868,13849,14653,15634,16438,17200,17723,17723,17723,17723,21,20,0,0,0,0,0,0,0x307380,0x70f800,0xc37e00,0x1040280,0x1568880,0x1970d00,0x1e99300,0x23c1900,0x27c9d80,0x2cf2380,0x30fa800,0x3622e00,0x3a2b280,0x3f53880,0x435bd00,0x487cf80,0x4c82a00,0x51a3c80,0x55a9700,0x5993880,0x5c39700,0x5c39700,0x5c39700,0x5c39700,0x5c39700,0x5c39700,0x5c39700,0x5c39700,0x5c39700,0x5c39700,0x5c39700,0x5c39700,269,1077,2069,2877,3869,4677,5669,6661,7469,8461,9269,10261,11069,12061,12869,13850,14654,15635,16439,840,840,840,840,840,20,19,0,0,0,0,0,0,0x307e00,0x710280,0xc38880,0x1040d00,0x1569300,0x1971780,0x1e99d80,0x23c2380,0x27ca800,0x2cf2e00,0x30fb280,0x3623880,0x3a2bd00,0x3f54300,0x435c780,0x487da00,0x4c83480,0x51a4700,0x55aa180,0x59d600,0x59d600,0x59d600,0x59d600,0x59d600,0x59d600,0x59d600,0x59d600,0x59d600,0x59d600,0x59d600,0x59d600,0x59d600,270,1078,2070,2878,3870,4678,5670,6662,7470,8462,9270,10262,11070,12062,12870,13851,14655,15636,16440,1096,17724,17724,17724,17724,21,20,0,0,0,0,0,0,0x308880,0x710d00,0xc39300,0x1041780,0x1569d80,0x1972200,0x1e9a800,0x23c2e00,0x27cb280,0x2cf3880,0x30fbd00,0x3624300,0x3a2c780,0x3f54d80,0x435d200,0x487e480,0x4c83f00,0x51a5180,0x55aac00,0x6ecb80,0x5c3a180,0x5c3a180,0x5c3a180,0x5c3a180,0x5c3a180,0x5c3a180,0x5c3a180,0x5c3a180,0x5c3a180,0x5c3a180,0x5c3a180,0x5c3a180,271,1079,2071,2879,3871,4679,5671,6663,7471,8463,9271,10263,11071,12063,12871,13852,14656,15637,16441,17201,17725,17725,17725,17725,21,20,0,0,0,0,0,0,0x309300,0x711780,0xc39d80,0x1042200,0x156a800,0x1972c80,0x1e9b280,0x23c3880,0x27cbd00,0x2cf4300,0x30fc780,0x3624d80,0x3a2d200,0x3f55800,0x435dc80,0x487ef00,0x4c84980,0x51a5c00,0x55ab680,0x5994300,0x5c3ac00,0x5c3ac00,0x5c3ac00,0x5c3ac00,0x5c3ac00,0x5c3ac00,0x5c3ac00,0x5c3ac00,0x5c3ac00,0x5c3ac00,0x5c3ac00,0x5c3ac00,272,1080,2072,2880,3872,4864,5672,6664,7472,8464,9272,10264,11072,12064,13056,18443,14657,18458,16442,1352,1352,1352,1352,1352,20,19,0,0,0,0,0,0,0x309d80,0x712200,0xc3a800,0x1042c80,0x156b280,0x1a93880,0x1e9bd00,0x23c4300,0x27cc780,0x2cf4d80,0x30fd200,0x3625800,0x3a2dc80,0x3f56280,0x447e880,0x67e00,0x4c85400,0x71b80,0x55ac100,0x83c100,0x83c100,0x83c100,0x83c100,0x83c100,0x83c100,0x83c100,0x83c100,0x83c100,0x83c100,0x83c100,0x83c100,0x83c100,273,1081,2073,2881,3873,4865,5673,6665,7473,8465,9273,10265,11073,12065,13057,13853,14658,15638,16443,1608,1608,1608,1608,1608,20,19,0,0,0,0,0,0,0x30a800,0x712c80,0xc3b280,0x1043700,0x156bd00,0x1a94300,0x1e9c780,0x23c4d80,0x27cd200,0x2cf5800,0x30fdc80,0x3626280,0x3a2e700,0x3f56d00,0x447f300,0x487f980,0x4c85e80,0x51a6680,0x55acb80,0x98b680,0x98b680,0x98b680,0x98b680,0x98b680,0x98b680,0x98b680,0x98b680,0x98b680,0x98b680,0x98b680,0x98b680,0x98b680,274,1082,2074,2882,3874,4866,5674,6666,7474,8466,9274,10266,11074,12066,13058,13854,14659,15639,16444,17202,17726,18227,18227,18227,22,21,0,0,0,0,0,0,0x30b280,0x713700,0xc3bd00,0x1044180,0x156c780,0x1a94d80,0x1e9d200,0x23c5800,0x27cdc80,0x2cf6280,0x30fe700,0x3626d00,0x3a2f180,0x3f57780,0x447fd80,0x4880400,0x4c86900,0x51a7100,0x55ad600,0x5994d80,0x5c3b680,0x5ed2e00,0x5ed2e00,0x5ed2e00,0x5ed2e00,0x5ed2e00,0x5ed2e00,0x5ed2e00,0x5ed2e00,0x5ed2e00,0x5ed2e00,0x5ed2e00,275,1083,2075,2883,3875,4867,5675,6667,7475,8467,9275,10267,11075,12067,13059,13855,14660,15640,16445,17203,17727,17727,17727,17727,21,20,0,0,0,0,0,0,0x30bd00,0x714180,0xc3c780,0x1044c00,0x156d200,0x1a95800,0x1e9dc80,0x23c6280,0x27ce700,0x2cf6d00,0x30ff180,0x3627780,0x3a2fc00,0x3f58200,0x4480800,0x4880e80,0x4c87380,0x51a7b80,0x55ae080,0x5995800,0x5c3c100,0x5c3c100,0x5c3c100,0x5c3c100,0x5c3c100,0x5c3c100,0x5c3c100,0x5c3c100,0x5c3c100,0x5c3c100,0x5c3c100,0x5c3c100,276,1084,2076,2884,3876,4868,5676,6668,7476,8468,9276,10268,11076,12068,13060,13856,14661,15641,16446,17204,17728,17728,17728,17728,21,20,0,0,0,0,0,0,0x30c780,0x714c00,0xc3d200,0x1045680,0x156dc80,0x1a96280,0x1e9e700,0x23c6d00,0x27cf180,0x2cf7780,0x30ffc00,0x3628200,0x3a30680,0x3f58c80,0x4481280,0x4881900,0x4c87e00,0x51a8600,0x55aeb00,0x5996280,0x5c3cb80,0x5c3cb80,0x5c3cb80,0x5c3cb80,0x5c3cb80,0x5c3cb80,0x5c3cb80,0x5c3cb80,0x5c3cb80,0x5c3cb80,0x5c3cb80,0x5c3cb80,277,1085,2077,2885,3877,4869,5677,6669,7477,8469,9277,10269,11077,12069,13061,13857,14662,15642,16447,1864,1864,1864,1864,1864,20,19,0,0,0,0,0,0,0x30d200,0x715680,0xc3dc80,0x1046100,0x156e700,0x1a96d00,0x1e9f180,0x23c7780,0x27cfc00,0x2cf8200,0x3100680,0x3628c80,0x3a31100,0x3f59700,0x4481d00,0x4882380,0x4c88880,0x51a9080,0x55af580,0xadac00,0xadac00,0xadac00,0xadac00,0xadac00,0xadac00,0xadac00,0xadac00,0xadac00,0xadac00,0xadac00,0xadac00,0xadac00,278,1086,2078,2886,3878,4870,5678,6670,7478,8470,9278,10270,11078,12070,13062,13858,14663,15643,16448,2120,17729,17729,17729,17729,21,20,0,0,0,0,0,0,0x30dc80,0x716100,0xc3e700,0x1046b80,0x156f180,0x1a97780,0x1e9fc00,0x23c8200,0x27d0680,0x2cf8c80,0x3101100,0x3629700,0x3a31b80,0x3f5a180,0x4482780,0x4882e00,0x4c89300,0x51a9b00,0x55b0000,0xc2a180,0x5c3d600,0x5c3d600,0x5c3d600,0x5c3d600,0x5c3d600,0x5c3d600,0x5c3d600,0x5c3d600,0x5c3d600,0x5c3d600,0x5c3d600,0x5c3d600,279,1087,2079,2887,3879,4871,5679,6671,7479,8471,9279,10271,11079,12071,13063,13859,14848,15644,16449,17205,17730,17730,17730,17730,21,20,0,0,0,0,0,0,0x30e700,0x716b80,0xc3f180,0x1047600,0x156fc00,0x1a98200,0x1ea0680,0x23c8c80,0x27d1100,0x2cf9700,0x3101b80,0x362a180,0x3a32600,0x3f5ac00,0x4483200,0x4883880,0x4da9f00,0x51aa580,0x55b0a80,0x5996d00,0x5c3e080,0x5c3e080,0x5c3e080,0x5c3e080,0x5c3e080,0x5c3e080,0x5c3e080,0x5c3e080,0x5c3e080,0x5c3e080,0x5c3e080,0x5c3e080,280,1088,2080,3072,3880,4872,5680,6672,7480,8472,9280,10272,11264,12072,13064,18444,14849,18459,16450,2376,2376,2376,2376,2376,20,19,0,0,0,0,0,0,0x30f180,0x717600,0xc3fc00,0x1168200,0x1570680,0x1a98c80,0x1ea1100,0x23c9700,0x27d1b80,0x2cfa180,0x3102600,0x362ac00,0x3b53200,0x3f5b680,0x4483c80,0x68880,0x4daa980,0x72600,0x55b1500,0xd79700,0xd79700,0xd79700,0xd79700,0xd79700,0xd79700,0xd79700,0xd79700,0xd79700,0xd79700,0xd79700,0xd79700,0xd79700,281,1089,2081,3073,3881,4873,5681,6673,7481,8473,9281,10273,11265,12073,13065,13860,14850,15645,16451,2632,2632,2632,2632,2632,20,19,0,0,0,0,0,0,0x30fc00,0x718080,0xc40680,0x1168c80,0x1571100,0x1a99700,0x1ea1b80,0x23ca180,0x27d2600,0x2cfac00,0x3103080,0x362b680,0x3b53c80,0x3f5c100,0x4484700,0x4884300,0x4dab400,0x51ab000,0x55b1f80,0xec8c80,0xec8c80,0xec8c80,0xec8c80,0xec8c80,0xec8c80,0xec8c80,0xec8c80,0xec8c80,0xec8c80,0xec8c80,0xec8c80,0xec8c80,282,1090,2082,3074,3882,4874,5682,6674,7482,8474,9282,10274,11266,12074,13066,13861,14851,15646,16452,17206,17731,18228,18228,18228,22,21,0,0,0,0,0,0,0x310680,0x718b00,0xc41100,0x1169700,0x1571b80,0x1a9a180,0x1ea2600,0x23cac00,0x27d3080,0x2cfb680,0x3103b00,0x362c100,0x3b54700,0x3f5cb80,0x4485180,0x4884d80,0x4dabe80,0x51aba80,0x55b2a00,0x5997780,0x5c3eb00,0x5ed3880,0x5ed3880,0x5ed3880,0x5ed3880,0x5ed3880,0x5ed3880,0x5ed3880,0x5ed3880,0x5ed3880,0x5ed3880,0x5ed3880,283,1091,2083,3075,3883,4875,5683,6675,7483,8475,9283,10275,11267,12075,13067,13862,14852,15647,16453,17207,17732,17732,17732,17732,21,20,0,0,0,0,0,0,0x311100,0x719580,0xc41b80,0x116a180,0x1572600,0x1a9ac00,0x1ea3080,0x23cb680,0x27d3b00,0x2cfc100,0x3104580,0x362cb80,0x3b55180,0x3f5d600,0x4485c00,0x4885800,0x4dac900,0x51ac500,0x55b3480,0x5998200,0x5c3f580,0x5c3f580,0x5c3f580,0x5c3f580,0x5c3f580,0x5c3f580,0x5c3f580,0x5c3f580,0x5c3f580,0x5c3f580,0x5c3f580,0x5c3f580,284,1092,2084,3076,3884,4876,5684,6676,7484,8476,9284,10276,11268,12076,13068,13863,14853,15648,16454,17208,17733,17733,17733,17733,21,20,0,0,0,0,0,0,0x311b80,0x71a000,0xc42600,0x116ac00,0x1573080,0x1a9b680,0x1ea3b00,0x23cc100,0x27d4580,0x2cfcb80,0x3105000,0x362d600,0x3b55c00,0x3f5e080,0x4486680,0x4886280,0x4dad380,0x51acf80,0x55b3f00,0x5998c80,0x5c40000,0x5c40000,0x5c40000,0x5c40000,0x5c40000,0x5c40000,0x5c40000,0x5c40000,0x5c40000,0x5c40000,0x5c40000,0x5c40000,285,1093,2085,3077,3885,4877,5685,6677,7485,8477,9285,10277,11269,12077,13069,13864,14854,15649,16455,2888,2888,2888,2888,2888,20,19,0,0,0,0,0,0,0x312600,0x71aa80,0xc43080,0x116b680,0x1573b00,0x1a9c100,0x1ea4580,0x23ccb80,0x27d5000,0x2cfd600,0x3105a80,0x362e080,0x3b56680,0x3f5eb00,0x4487100,0x4886d00,0x4dade00,0x51ada00,0x55b4980,0x1018200,0x1018200,0x1018200,0x1018200,0x1018200,0x1018200,0x1018200,0x1018200,0x1018200,0x1018200,0x1018200,0x1018200,0x1018200,286,1094,2086,3078,3886,4878,5686,6678,7486,8478,9286,10278,11270,12078,13070,13865,14855,15650,16640,3144,17734,17734,17734,17734,21,20,0,0,0,0,0,0,0x313080,0x71b500,0xc43b00,0x116c100,0x1574580,0x1a9cb80,0x1ea5000,0x23cd600,0x27d5a80,0x2cfe080,0x3106500,0x362eb00,0x3b57100,0x3f5f580,0x4487b80,0x4887780,0x4dae880,0x51ae480,0x56d5580,0x1167780,0x5c40a80,0x5c40a80,0x5c40a80,0x5c40a80,0x5c40a80,0x5c40a80,0x5c40a80,0x5c40a80,0x5c40a80,0x5c40a80,0x5c40a80,0x5c40a80,287,1095,2087,3079,3887,4879,5687,6679,7487,8479,9287,10279,11271,12079,13071,13866,14856,15651,16641,17209,17735,17735,17735,17735,21,20,0,0,0,0,0,0,0x313b00,0x71bf80,0xc44580,0x116cb80,0x1575000,0x1a9d600,0x1ea5a80,0x23ce080,0x27d6500,0x2cfeb00,0x3106f80,0x362f580,0x3b57b80,0x3f60000,0x4488600,0x4888200,0x4daf300,0x51aef00,0x56d6000,0x5999700,0x5c41500,0x5c41500,0x5c41500,0x5c41500,0x5c41500,0x5c41500,0x5c41500,0x5c41500,0x5c41500,0x5c41500,0x5c41500,0x5c41500,288,1280,2088,3080,3888,4880,5688,6680,7488,8480,9472,10280,11272,12080,13072,18445,14857,18460,16642,3400,3400,3400,3400,3400,20,19,0,0,0,0,0,0,0x314580,0x83cb80,0xc45000,0x116d600,0x1575a80,0x1a9e080,0x1ea6500,0x23ceb00,0x27d6f80,0x2cff580,0x3227b80,0x3630000,0x3b58600,0x3f60a80,0x4489080,0x69300,0x4dafd80,0x73080,0x56d6a80,0x12b6d00,0x12b6d00,0x12b6d00,0x12b6d00,0x12b6d00,0x12b6d00,0x12b6d00,0x12b6d00,0x12b6d00,0x12b6d00,0x12b6d00,0x12b6d00,0x12b6d00,289,1281,2089,3081,3889,4881,5689,6681,7489,8481,9473,10281,11273,12081,13073,13867,14858,15652,16643,3656,3656,3656,3656,3656,20,19,0,0,0,0,0,0,0x315000,0x83d600,0xc45a80,0x116e080,0x1576500,0x1a9eb00,0x1ea6f80,0x23cf580,0x27d7a00,0x2d00000,0x3228600,0x3630a80,0x3b59080,0x3f61500,0x4489b00,0x4888c80,0x4db0800,0x51af980,0x56d7500,0x1406280,0x1406280,0x1406280,0x1406280,0x1406280,0x1406280,0x1406280,0x1406280,0x1406280,0x1406280,0x1406280,0x1406280,0x1406280,290,1282,2090,3082,3890,4882,5690,6682,7490,8482,9474,10282,11274,12082,13074,13868,14859,15653,16644,17210,17920,18229,18229,18229,22,21,0,0,0,0,0,0,0x315a80,0x83e080,0xc46500,0x116eb00,0x1576f80,0x1a9f580,0x1ea7a00,0x23d0000,0x27d8480,0x2d00a80,0x3229080,0x3631500,0x3b59b00,0x3f61f80,0x448a580,0x4889700,0x4db1280,0x51b0400,0x56d7f80,0x599a180,0x5d62100,0x5ed4300,0x5ed4300,0x5ed4300,0x5ed4300,0x5ed4300,0x5ed4300,0x5ed4300,0x5ed4300,0x5ed4300,0x5ed4300,0x5ed4300,291,1283,2091,3083,3891,4883,5691,6683,7491,8483,9475,10283,11275,12083,13075,13869,14860,15654,16645,17211,17921,17921,17921,17921,21,20,0,0,0,0,0,0,0x316500,0x83eb00,0xc46f80,0x116f580,0x1577a00,0x1aa0000,0x1ea8480,0x23d0a80,0x27d8f00,0x2d01500,0x3229b00,0x3631f80,0x3b5a580,0x3f62a00,0x448b000,0x488a180,0x4db1d00,0x51b0e80,0x56d8a00,0x599ac00,0x5d62b80,0x5d62b80,0x5d62b80,0x5d62b80,0x5d62b80,0x5d62b80,0x5d62b80,0x5d62b80,0x5d62b80,0x5d62b80,0x5d62b80,0x5d62b80,292,1284,2092,3084,3892,4884,5692,6684,7492,8484,9476,10284,11276,12084,13076,13870,14861,15655,16646,17212,17922,17922,17922,17922,21,20,0,0,0,0,0,0,0x316f80,0x83f580,0xc47a00,0x1170000,0x1578480,0x1aa0a80,0x1ea8f00,0x23d1500,0x27d9980,0x2d01f80,0x322a580,0x3632a00,0x3b5b000,0x3f63480,0x448ba80,0x488ac00,0x4db2780,0x51b1900,0x56d9480,0x599b680,0x5d63600,0x5d63600,0x5d63600,0x5d63600,0x5d63600,0x5d63600,0x5d63600,0x5d63600,0x5d63600,0x5d63600,0x5d63600,0x5d63600,293,1285,2093,3085,3893,4885,5693,6685,7493,8485,9477,10285,11277,12085,13077,13871,14862,15656,16647,3912,3912,3912,3912,3912,20,19,0,0,0,0,0,0,0x317a00,0x840000,0xc48480,0x1170a80,0x1578f00,0x1aa1500,0x1ea9980,0x23d1f80,0x27da400,0x2d02a00,0x322b000,0x3633480,0x3b5ba80,0x3f63f00,0x448c500,0x488b680,0x4db3200,0x51b2380,0x56d9f00,0x1555800,0x1555800,0x1555800,0x1555800,0x1555800,0x1555800,0x1555800,0x1555800,0x1555800,0x1555800,0x1555800,0x1555800,0x1555800,294,1286,2094,3086,3894,4886,5694,6686,7494,8486,9478,10286,11278,12086,13078,13872,14863,15657,16648,4168,17923,17923,17923,17923,21,20,0,0,0,0,0,0,0x318480,0x840a80,0xc48f00,0x1171500,0x1579980,0x1aa1f80,0x1eaa400,0x23d2a00,0x27dae80,0x2d03480,0x322ba80,0x3633f00,0x3b5c500,0x3f64980,0x448cf80,0x488c100,0x4db3c80,0x51b2e00,0x56da980,0x16a4d80,0x5d64080,0x5d64080,0x5d64080,0x5d64080,0x5d64080,0x5d64080,0x5d64080,0x5d64080,0x5d64080,0x5d64080,0x5d64080,0x5d64080,295,1287,2095,3087,3895,4887,5695,6687,7495,8487,9479,10287,11279,12087,13079,13873,14864,15658,16649,17213,17924,17924,17924,17924,21,20,0,0,0,0,0,0,0x318f00,0x841500,0xc49980,0x1171f80,0x157a400,0x1aa2a00,0x1eaae80,0x23d3480,0x27db900,0x2d03f00,0x322c500,0x3634980,0x3b5cf80,0x3f65400,0x448da00,0x488cb80,0x4db4700,0x51b3880,0x56db400,0x599c100,0x5d64b00,0x5d64b00,0x5d64b00,0x5d64b00,0x5d64b00,0x5d64b00,0x5d64b00,0x5d64b00,0x5d64b00,0x5d64b00,0x5d64b00,0x5d64b00,296,1288,2096,3088,3896,4888,5696,6688,7680,8488,9480,10288,11280,12088,13080,18446,14865,18461,16650,4424,4424,4424,4424,4424,20,19,0,0,0,0,0,0,0x319980,0x841f80,0xc4a400,0x1172a00,0x157ae80,0x1aa3480,0x1eab900,0x23d3f00,0x28fc500,0x2d04980,0x322cf80,0x3635400,0x3b5da00,0x3f65e80,0x448e480,0x69d80,0x4db5180,0x73b00,0x56dbe80,0x17f4300,0x17f4300,0x17f4300,0x17f4300,0x17f4300,0x17f4300,0x17f4300,0x17f4300,0x17f4300,0x17f4300,0x17f4300,0x17f4300,0x17f4300,297,1289,2097,3089,3897,4889,5697,6689,7681,8489,9481,10289,11281,12089,13081,13874,14866,15659,16651,4680,4680,4680,4680,4680,20,19,0,0,0,0,0,0,0x31a400,0x842a00,0xc4ae80,0x1173480,0x157b900,0x1aa3f00,0x1eac380,0x23d4980,0x28fcf80,0x2d05400,0x322da00,0x3635e80,0x3b5e480,0x3f66900,0x448ef00,0x488d600,0x4db5c00,0x51b4300,0x56dc900,0x1943880,0x1943880,0x1943880,0x1943880,0x1943880,0x1943880,0x1943880,0x1943880,0x1943880,0x1943880,0x1943880,0x1943880,0x1943880,298,1290,2098,3090,3898,4890,5698,6690,7682,8490,9482,10290,11282,12090,13082,13875,14867,15660,16652,17214,17925,18230,18230,18230,22,21,0,0,0,0,0,0,0x31ae80,0x843480,0xc4b900,0x1173f00,0x157c380,0x1aa4980,0x1eace00,0x23d5400,0x28fda00,0x2d05e80,0x322e480,0x3636900,0x3b5ef00,0x3f67380,0x448f980,0x488e080,0x4db6680,0x51b4d80,0x56dd380,0x599cb80,0x5d65580,0x5ed4d80,0x5ed4d80,0x5ed4d80,0x5ed4d80,0x5ed4d80,0x5ed4d80,0x5ed4d80,0x5ed4d80,0x5ed4d80,0x5ed4d80,0x5ed4d80,299,1291,2099,3091,3899,4891,5699,6691,7683,8491,9483,10291,11283,12091,13083,13876,14868,15661,16653,17215,17926,17926,17926,17926,21,20,0,0,0,0,0,0,0x31b900,0x843f00,0xc4c380,0x1174980,0x157ce00,0x1aa5400,0x1ead880,0x23d5e80,0x28fe480,0x2d06900,0x322ef00,0x3637380,0x3b5f980,0x3f67e00,0x4490400,0x488eb00,0x4db7100,0x51b5800,0x56dde00,0x599d600,0x5d66000,0x5d66000,0x5d66000,0x5d66000,0x5d66000,0x5d66000,0x5d66000,0x5d66000,0x5d66000,0x5d66000,0x5d66000,0x5d66000,300,1292,2100,3092,3900,4892,5700,6692,7684,8492,9484,10292,11284,12092,13084,13877,14869,15662,16654,17216,17927,17927,17927,17927,21,20,0,0,0,0,0,0,0x31c380,0x844980,0xc4ce00,0x1175400,0x157d880,0x1aa5e80,0x1eae300,0x23d6900,0x28fef00,0x2d07380,0x322f980,0x3637e00,0x3b60400,0x3f68880,0x4490e80,0x488f580,0x4db7b80,0x51b6280,0x56de880,0x599e080,0x5d66a80,0x5d66a80,0x5d66a80,0x5d66a80,0x5d66a80,0x5d66a80,0x5d66a80,0x5d66a80,0x5d66a80,0x5d66a80,0x5d66a80,0x5d66a80,301,1293,2101,3093,3901,4893,5701,6693,7685,8493,9485,10293,11285,12093,13085,13878,14870,15663,16655,4936,4936,4936,4936,4936,20,19,0,0,0,0,0,0,0x31ce00,0x845400,0xc4d880,0x1175e80,0x157e300,0x1aa6900,0x1eaed80,0x23d7380,0x28ff980,0x2d07e00,0x3230400,0x3638880,0x3b60e80,0x3f69300,0x4491900,0x4890000,0x4db8600,0x51b6d00,0x56df300,0x1a92e00,0x1a92e00,0x1a92e00,0x1a92e00,0x1a92e00,0x1a92e00,0x1a92e00,0x1a92e00,0x1a92e00,0x1a92e00,0x1a92e00,0x1a92e00,0x1a92e00,302,1294,2102,3094,3902,4894,5702,6694,7686,8494,9486,10294,11286,12094,13086,13879,14871,15664,16656,5192,17928,17928,17928,17928,21,20,0,0,0,0,0,0,0x31d880,0x845e80,0xc4e300,0x1176900,0x157ed80,0x1aa7380,0x1eaf800,0x23d7e00,0x2900400,0x2d08880,0x3230e80,0x3639300,0x3b61900,0x3f69d80,0x4492380,0x4890a80,0x4db9080,0x51b7780,0x56dfd80,0x1be2380,0x5d67500,0x5d67500,0x5d67500,0x5d67500,0x5d67500,0x5d67500,0x5d67500,0x5d67500,0x5d67500,0x5d67500,0x5d67500,0x5d67500,303,1295,2103,3095,3903,4895,5703,6695,7687,8495,9487,10295,11287,12095,13087,13880,14872,15665,16657,17217,17929,17929,17929,17929,21,20,0,0,0,0,0,0,0x31e300,0x846900,0xc4ed80,0x1177380,0x157f800,0x1aa7e00,0x1eb0280,0x23d8880,0x2900e80,0x2d09300,0x3231900,0x3639d80,0x3b62380,0x3f6a800,0x4492e00,0x4891500,0x4db9b00,0x51b8200,0x56e0800,0x599eb00,0x5d67f80,0x5d67f80,0x5d67f80,0x5d67f80,0x5d67f80,0x5d67f80,0x5d67f80,0x5d67f80,0x5d67f80,0x5d67f80,0x5d67f80,0x5d67f80,304,1296,2104,3096,3904,4896,5888,6696,7688,8496,9488,10296,11288,12096,13088,13881,14873,15666,16658,17218,17930,17930,17930,17930,21,20,0,0,0,0,0,0,0x31ed80,0x847380,0xc4f800,0x1177e00,0x1580280,0x1aa8880,0x1fd0e80,0x23d9300,0x2901900,0x2d09d80,0x3232380,0x363a800,0x3b62e00,0x3f6b280,0x4493880,0x4891f80,0x4dba580,0x51b8c80,0x56e1280,0x599f580,0x5d68a00,0x5d68a00,0x5d68a00,0x5d68a00,0x5d68a00,0x5d68a00,0x5d68a00,0x5d68a00,0x5d68a00,0x5d68a00,0x5d68a00,0x5d68a00,305,1297,2105,3097,3905,4897,5889,6697,7689,8497,9489,10297,11289,12097,13089,13882,14874,15667,16659,5448,5448,5448,5448,5448,20,19,0,0,0,0,0,0,0x31f800,0x847e00,0xc50280,0x1178880,0x1580d00,0x1aa9300,0x1fd1900,0x23d9d80,0x2902380,0x2d0a800,0x3232e00,0x363b280,0x3b63880,0x3f6bd00,0x4494300,0x4892a00,0x4dbb000,0x51b9700,0x56e1d00,0x1d31900,0x1d31900,0x1d31900,0x1d31900,0x1d31900,0x1d31900,0x1d31900,0x1d31900,0x1d31900,0x1d31900,0x1d31900,0x1d31900,0x1d31900,306,1298,2106,3098,3906,4898,5890,6698,7690,8498,9490,10298,11290,12098,13090,13883,14875,15668,16660,17219,17931,18231,18231,18231,22,21,0,0,0,0,0,0,0x320280,0x848880,0xc50d00,0x1179300,0x1581780,0x1aa9d80,0x1fd2380,0x23da800,0x2902e00,0x2d0b280,0x3233880,0x363bd00,0x3b64300,0x3f6c780,0x4494d80,0x4893480,0x4dbba80,0x51ba180,0x56e2780,0x59a0000,0x5d69480,0x5ed5800,0x5ed5800,0x5ed5800,0x5ed5800,0x5ed5800,0x5ed5800,0x5ed5800,0x5ed5800,0x5ed5800,0x5ed5800,0x5ed5800,307,1299,2107,3099,3907,4899,5891,6699,7691,8499,9491,10299,11291,12099,13091,13884,14876,15669,16661,17220,17932,17932,17932,17932,21,20,0,0,0,0,0,0,0x320d00,0x849300,0xc51780,0x1179d80,0x1582200,0x1aaa800,0x1fd2e00,0x23db280,0x2903880,0x2d0bd00,0x3234300,0x363c780,0x3b64d80,0x3f6d200,0x4495800,0x4893f00,0x4dbc500,0x51bac00,0x56e3200,0x59a0a80,0x5d69f00,0x5d69f00,0x5d69f00,0x5d69f00,0x5d69f00,0x5d69f00,0x5d69f00,0x5d69f00,0x5d69f00,0x5d69f00,0x5d69f00,0x5d69f00,308,1300,2108,3100,3908,4900,5892,6700,7692,8500,9492,10300,11292,12100,13092,13885,14877,15670,16662,17221,17933,17933,17933,17933,21,20,0,0,0,0,0,0,0x321780,0x849d80,0xc52200,0x117a800,0x1582c80,0x1aab280,0x1fd3880,0x23dbd00,0x2904300,0x2d0c780,0x3234d80,0x363d200,0x3b65800,0x3f6dc80,0x4496280,0x4894980,0x4dbcf80,0x51bb680,0x56e3c80,0x59a1500,0x5d6a980,0x5d6a980,0x5d6a980,0x5d6a980,0x5d6a980,0x5d6a980,0x5d6a980,0x5d6a980,0x5d6a980,0x5d6a980,0x5d6a980,0x5d6a980,309,1301,2109,3101,3909,4901,5893,6701,7693,8501,9493,10301,11293,12101,13093,13886,14878,15671,16663,5704,5704,5704,5704,5704,20,19,0,0,0,0,0,0,0x322200,0x84a800,0xc52c80,0x117b280,0x1583700,0x1aabd00,0x1fd4300,0x23dc780,0x2904d80,0x2d0d200,0x3235800,0x363dc80,0x3b66280,0x3f6e700,0x4496d00,0x4895400,0x4dbda00,0x51bc100,0x56e4700,0x1e80e80,0x1e80e80,0x1e80e80,0x1e80e80,0x1e80e80,0x1e80e80,0x1e80e80,0x1e80e80,0x1e80e80,0x1e80e80,0x1e80e80,0x1e80e80,0x1e80e80,310,1302,2110,3102,3910,4902,5894,6702,7694,8502,9494,10302,11294,12102,13094,13887,14879,15672,16664,5960,17934,17934,17934,17934,21,20,0,0,0,0,0,0,0x322c80,0x84b280,0xc53700,0x117bd00,0x1584180,0x1aac780,0x1fd4d80,0x23dd200,0x2905800,0x2d0dc80,0x3236280,0x363e700,0x3b66d00,0x3f6f180,0x4497780,0x4895e80,0x4dbe480,0x51bcb80,0x56e5180,0x1fd0400,0x5d6b400,0x5d6b400,0x5d6b400,0x5d6b400,0x5d6b400,0x5d6b400,0x5d6b400,0x5d6b400,0x5d6b400,0x5d6b400,0x5d6b400,0x5d6b400,311,1303,2111,3103,3911,4903,5895,6703,7695,8503,9495,10303,11295,12103,13095,13888,14880,15673,16665,17222,17935,17935,17935,17935,21,20,0,0,0,0,0,0,0x323700,0x84bd00,0xc54180,0x117c780,0x1584c00,0x1aad200,0x1fd5800,0x23ddc80,0x2906280,0x2d0e700,0x3236d00,0x363f180,0x3b67780,0x3f6fc00,0x4498200,0x4896900,0x4dbef00,0x51bd600,0x56e5c00,0x59a1f80,0x5d6be80,0x5d6be80,0x5d6be80,0x5d6be80,0x5d6be80,0x5d6be80,0x5d6be80,0x5d6be80,0x5d6be80,0x5d6be80,0x5d6be80,0x5d6be80,312,1304,2112,3104,4096,4904,5896,6704,7696,8504,9496,10304,11296,12288,13096,13889,14881,15674,16666,17223,17936,17936,17936,17936,21,20,0,0,0,0,0,0,0x324180,0x84c780,0xc54c00,0x117d200,0x16a5800,0x1aadc80,0x1fd6280,0x23de700,0x2906d00,0x2d0f180,0x3237780,0x363fc00,0x3b68200,0x4090800,0x4498c80,0x4897380,0x4dbf980,0x51be080,0x56e6680,0x59a2a00,0x5d6c900,0x5d6c900,0x5d6c900,0x5d6c900,0x5d6c900,0x5d6c900,0x5d6c900,0x5d6c900,0x5d6c900,0x5d6c900,0x5d6c900,0x5d6c900,313,1305,2113,3105,4097,4905,5897,6705,7697,8505,9497,10305,11297,12289,13097,13890,14882,15675,16667,6216,6216,6216,6216,6216,20,19,0,0,0,0,0,0,0x324c00,0x84d200,0xc55680,0x117dc80,0x16a6280,0x1aae700,0x1fd6d00,0x23df180,0x2907780,0x2d0fc00,0x3238200,0x3640680,0x3b68c80,0x4091280,0x4499700,0x4897e00,0x4dc0400,0x51beb00,0x56e7100,0x211f980,0x211f980,0x211f980,0x211f980,0x211f980,0x211f980,0x211f980,0x211f980,0x211f980,0x211f980,0x211f980,0x211f980,0x211f980,314,1306,2114,3106,4098,4906,5898,6706,7698,8506,9498,10306,11298,12290,13098,13891,14883,15676,16668,17408,17937,18232,18232,18232,22,21,0,0,0,0,0,0,0x325680,0x84dc80,0xc56100,0x117e700,0x16a6d00,0x1aaf180,0x1fd7780,0x23dfc00,0x2908200,0x2d10680,0x3238c80,0x3641100,0x3b69700,0x4091d00,0x449a180,0x4898880,0x4dc0e80,0x51bf580,0x56e7b80,0x5ac3600,0x5d6d380,0x5ed6280,0x5ed6280,0x5ed6280,0x5ed6280,0x5ed6280,0x5ed6280,0x5ed6280,0x5ed6280,0x5ed6280,0x5ed6280,0x5ed6280,315,1307,2115,3107,4099,4907,5899,6707,7699,8507,9499,10307,11299,12291,13099,13892,14884,15677,16669,17409,17938,17938,17938,17938,21,20,0,0,0,0,0,0,0x326100,0x84e700,0xc56b80,0x117f180,0x16a7780,0x1aafc00,0x1fd8200,0x23e0680,0x2908c80,0x2d11100,0x3239700,0x3641b80,0x3b6a180,0x4092780,0x449ac00,0x4899300,0x4dc1900,0x51c0000,0x56e8600,0x5ac4080,0x5d6de00,0x5d6de00,0x5d6de00,0x5d6de00,0x5d6de00,0x5d6de00,0x5d6de00,0x5d6de00,0x5d6de00,0x5d6de00,0x5d6de00,0x5d6de00,316,1308,2116,3108,4100,4908,5900,6708,7700,8508,9500,10308,11300,12292,13100,13893,14885,15678,16670,17410,17939,17939,17939,17939,21,20,0,0,0,0,0,0,0x326b80,0x84f180,0xc57600,0x117fc00,0x16a8200,0x1ab0680,0x1fd8c80,0x23e1100,0x2909700,0x2d11b80,0x323a180,0x3642600,0x3b6ac00,0x4093200,0x449b680,0x4899d80,0x4dc2380,0x51c0a80,0x56e9080,0x5ac4b00,0x5d6e880,0x5d6e880,0x5d6e880,0x5d6e880,0x5d6e880,0x5d6e880,0x5d6e880,0x5d6e880,0x5d6e880,0x5d6e880,0x5d6e880,0x5d6e880,317,1309,2117,3109,4101,4909,5901,6709,7701,8509,9501,10309,11301,12293,13101,13894,14886,15679,16671,6472,6472,6472,6472,6472,20,19,0,0,0,0,0,0,0x327600,0x84fc00,0xc58080,0x1180680,0x16a8c80,0x1ab1100,0x1fd9700,0x23e1b80,0x290a180,0x2d12600,0x323ac00,0x3643080,0x3b6b680,0x4093c80,0x449c100,0x489a800,0x4dc2e00,0x51c1500,0x56e9b00,0x226ef00,0x226ef00,0x226ef00,0x226ef00,0x226ef00,0x226ef00,0x226ef00,0x226ef00,0x226ef00,0x226ef00,0x226ef00,0x226ef00,0x226ef00,318,1310,2118,3110,4102,4910,5902,6710,7702,8510,9502,10310,11302,12294,13102,13895,14887,15680,16672,6728,17940,17940,17940,17940,21,20,0,0,0,0,0,0,0x328080,0x850680,0xc58b00,0x1181100,0x16a9700,0x1ab1b80,0x1fda180,0x23e2600,0x290ac00,0x2d13080,0x323b680,0x3643b00,0x3b6c100,0x4094700,0x449cb80,0x489b280,0x4dc3880,0x51c1f80,0x56ea580,0x23be480,0x5d6f300,0x5d6f300,0x5d6f300,0x5d6f300,0x5d6f300,0x5d6f300,0x5d6f300,0x5d6f300,0x5d6f300,0x5d6f300,0x5d6f300,0x5d6f300,319,1311,2119,3111,4103,4911,5903,6711,7703,8511,9503,10311,11303,12295,13103,14080,14888,15681,16673,17411,17941,17941,17941,17941,21,20,0,0,0,0,0,0,0x328b00,0x851100,0xc59580,0x1181b80,0x16aa180,0x1ab2600,0x1fdac00,0x23e3080,0x290b680,0x2d13b00,0x323c100,0x3644580,0x3b6cb80,0x4095180,0x449d600,0x49bbe80,0x4dc4300,0x51c2a00,0x56eb000,0x5ac5580,0x5d6fd80,0x5d6fd80,0x5d6fd80,0x5d6fd80,0x5d6fd80,0x5d6fd80,0x5d6fd80,0x5d6fd80,0x5d6fd80,0x5d6fd80,0x5d6fd80,0x5d6fd80,320,1312,2304,3112,4104,4912,5904,6712,7704,8512,9504,10496,11304,12296,13104,14081,14889,15682,16674,17412,17942,17942,17942,17942,21,20,0,0,0,0,0,0,0x329580,0x851b80,0xd7a180,0x1182600,0x16aac00,0x1ab3080,0x1fdb680,0x23e3b00,0x290c100,0x2d14580,0x323cb80,0x3765180,0x3b6d600,0x4095c00,0x449e080,0x49bc900,0x4dc4d80,0x51c3480,0x56eba80,0x5ac6000,0x5d70800,0x5d70800,0x5d70800,0x5d70800,0x5d70800,0x5d70800,0x5d70800,0x5d70800,0x5d70800,0x5d70800,0x5d70800,0x5d70800,321,1313,2305,3113,4105,4913,5905,6713,7705,8513,9505,10497,11305,12297,13105,14082,14890,15683,16675,6984,6984,6984,6984,6984,20,19,0,0,0,0,0,0,0x32a000,0x852600,0xd7ac00,0x1183080,0x16ab680,0x1ab3b00,0x1fdc100,0x23e4580,0x290cb80,0x2d15000,0x323d600,0x3765c00,0x3b6e080,0x4096680,0x449eb00,0x49bd380,0x4dc5800,0x51c3f00,0x56ec500,0x250da00,0x250da00,0x250da00,0x250da00,0x250da00,0x250da00,0x250da00,0x250da00,0x250da00,0x250da00,0x250da00,0x250da00,0x250da00,322,1314,2306,3114,4106,4914,5906,6714,7706,8514,9506,10498,11306,12298,13106,14083,14891,15684,16676,17413,17943,18233,18233,18233,22,21,0,0,0,0,0,0,0x32aa80,0x853080,0xd7b680,0x1183b00,0x16ac100,0x1ab4580,0x1fdcb80,0x23e5000,0x290d600,0x2d15a80,0x323e080,0x3766680,0x3b6eb00,0x4097100,0x449f580,0x49bde00,0x4dc6280,0x51c4980,0x56ecf80,0x5ac6a80,0x5d71280,0x5ed6d00,0x5ed6d00,0x5ed6d00,0x5ed6d00,0x5ed6d00,0x5ed6d00,0x5ed6d00,0x5ed6d00,0x5ed6d00,0x5ed6d00,0x5ed6d00,323,1315,2307,3115,4107,4915,5907,6715,7707,8515,9507,10499,11307,12299,13107,14084,14892,15685,16677,17414,17944,17944,17944,17944,21,20,0,0,0,0,0,0,0x32b500,0x853b00,0xd7c100,0x1184580,0x16acb80,0x1ab5000,0x1fdd600,0x23e5a80,0x290e080,0x2d16500,0x323eb00,0x3767100,0x3b6f580,0x4097b80,0x44a0000,0x49be880,0x4dc6d00,0x51c5400,0x56eda00,0x5ac7500,0x5d71d00,0x5d71d00,0x5d71d00,0x5d71d00,0x5d71d00,0x5d71d00,0x5d71d00,0x5d71d00,0x5d71d00,0x5d71d00,0x5d71d00,0x5d71d00,324,1316,2308,3116,4108,4916,5908,6716,7708,8516,9508,10500,11308,12300,13108,14085,14893,15686,16678,17415,17945,17945,17945,17945,21,20,0,0,0,0,0,0,0x32bf80,0x854580,0xd7cb80,0x1185000,0x16ad600,0x1ab5a80,0x1fde080,0x23e6500,0x290eb00,0x2d16f80,0x323f580,0x3767b80,0x3b70000,0x4098600,0x44a0a80,0x49bf300,0x4dc7780,0x51c5e80,0x56ee480,0x5ac7f80,0x5d72780,0x5d72780,0x5d72780,0x5d72780,0x5d72780,0x5d72780,0x5d72780,0x5d72780,0x5d72780,0x5d72780,0x5d72780,0x5d72780,325,1317,2309,3117,4109,4917,5909,6717,7709,8517,9509,10501,11309,12301,13109,14086,14894,15687,16679,7240,7240,7240,7240,7240,20,19,0,0,0,0,0,0,0x32ca00,0x855000,0xd7d600,0x1185a80,0x16ae080,0x1ab6500,0x1fdeb00,0x23e6f80,0x290f580,0x2d17a00,0x3240000,0x3768600,0x3b70a80,0x4099080,0x44a1500,0x49bfd80,0x4dc8200,0x51c6900,0x56eef00,0x265cf80,0x265cf80,0x265cf80,0x265cf80,0x265cf80,0x265cf80,0x265cf80,0x265cf80,0x265cf80,0x265cf80,0x265cf80,0x265cf80,0x265cf80,326,1318,2310,3118,4110,4918,5910,6718,7710,8518,9510,10502,11310,12302,13110,14087,14895,15872,16680,7496,17946,17946,17946,17946,21,20,0,0,0,0,0,0,0x32d480,0x855a80,0xd7e080,0x1186500,0x16aeb00,0x1ab6f80,0x1fdf580,0x23e7a00,0x2910000,0x2d18480,0x3240a80,0x3769080,0x3b71500,0x4099b00,0x44a1f80,0x49c0800,0x4dc8c80,0x52e7500,0x56ef980,0x27ac500,0x5d73200,0x5d73200,0x5d73200,0x5d73200,0x5d73200,0x5d73200,0x5d73200,0x5d73200,0x5d73200,0x5d73200,0x5d73200,0x5d73200,327,1319,2311,3119,4111,4919,5911,6719,7711,8519,9511,10503,11311,12303,13111,14088,14896,15873,16681,17416,17947,17947,17947,17947,21,20,0,0,0,0,0,0,0x32df00,0x856500,0xd7eb00,0x1186f80,0x16af580,0x1ab7a00,0x1fe0000,0x23e8480,0x2910a80,0x2d18f00,0x3241500,0x3769b00,0x3b71f80,0x409a580,0x44a2a00,0x49c1280,0x4dc9700,0x52e7f80,0x56f0400,0x5ac8a00,0x5d73c80,0x5d73c80,0x5d73c80,0x5d73c80,0x5d73c80,0x5d73c80,0x5d73c80,0x5d73c80,0x5d73c80,0x5d73c80,0x5d73c80,0x5d73c80,512,1320,2312,3120,4112,4920,5912,6720,7712,8704,9512,10504,11312,12304,13112,14089,14897,15874,16682,17417,17948,17948,17948,17948,21,20,0,0,0,0,0,0,0x44eb00,0x856f80,0xd7f580,0x1187a00,0x16b0000,0x1ab8480,0x1fe0a80,0x23e8f00,0x2911500,0x2e39b00,0x3241f80,0x376a580,0x3b72a00,0x409b000,0x44a3480,0x49c1d00,0x4dca180,0x52e8a00,0x56f0e80,0x5ac9480,0x5d74700,0x5d74700,0x5d74700,0x5d74700,0x5d74700,0x5d74700,0x5d74700,0x5d74700,0x5d74700,0x5d74700,0x5d74700,0x5d74700,513,1321,2313,3121,4113,4921,5913,6721,7713,8705,9513,10505,11313,12305,13113,14090,14898,15875,16683,7752,7752,7752,7752,7752,20,19,0,0,0,0,0,0,0x44f580,0x857a00,0xd80000,0x1188480,0x16b0a80,0x1ab8f00,0x1fe1500,0x23e9980,0x2911f80,0x2e3a580,0x3242a00,0x376b000,0x3b73480,0x409ba80,0x44a3f00,0x49c2780,0x4dcac00,0x52e9480,0x56f1900,0x28fba80,0x28fba80,0x28fba80,0x28fba80,0x28fba80,0x28fba80,0x28fba80,0x28fba80,0x28fba80,0x28fba80,0x28fba80,0x28fba80,0x28fba80,514,1322,2314,3122,4114,4922,5914,6722,7714,8706,9514,10506,11314,12306,13114,14091,14899,15876,16684,17418,17949,18234,18234,18234,22,21,0,0,0,0,0,0,0x450000,0x858480,0xd80a80,0x1188f00,0x16b1500,0x1ab9980,0x1fe1f80,0x23ea400,0x2912a00,0x2e3b000,0x3243480,0x376ba80,0x3b73f00,0x409c500,0x44a4980,0x49c3200,0x4dcb680,0x52e9f00,0x56f2380,0x5ac9f00,0x5d75180,0x5ed7780,0x5ed7780,0x5ed7780,0x5ed7780,0x5ed7780,0x5ed7780,0x5ed7780,0x5ed7780,0x5ed7780,0x5ed7780,0x5ed7780,515,1323,2315,3123,4115,4923,5915,6723,7715,8707,9515,10507,11315,12307,13115,14092,14900,15877,16685,17419,17950,17950,17950,17950,21,20,0,0,0,0,0,0,0x450a80,0x858f00,0xd81500,0x1189980,0x16b1f80,0x1aba400,0x1fe2a00,0x23eae80,0x2913480,0x2e3ba80,0x3243f00,0x376c500,0x3b74980,0x409cf80,0x44a5400,0x49c3c80,0x4dcc100,0x52ea980,0x56f2e00,0x5aca980,0x5d75c00,0x5d75c00,0x5d75c00,0x5d75c00,0x5d75c00,0x5d75c00,0x5d75c00,0x5d75c00,0x5d75c00,0x5d75c00,0x5d75c00,0x5d75c00,516,1324,2316,3124,4116,4924,5916,6724,7716,8708,9516,10508,11316,12308,13116,14093,14901,15878,16686,17420,17951,17951,17951,17951,21,20,0,0,0,0,0,0,0x451500,0x859980,0xd81f80,0x118a400,0x16b2a00,0x1abae80,0x1fe3480,0x23eb900,0x2913f00,0x2e3c500,0x3244980,0x376cf80,0x3b75400,0x409da00,0x44a5e80,0x49c4700,0x4dccb80,0x52eb400,0x56f3880,0x5acb400,0x5d76680,0x5d76680,0x5d76680,0x5d76680,0x5d76680,0x5d76680,0x5d76680,0x5d76680,0x5d76680,0x5d76680,0x5d76680,0x5d76680,517,1325,2317,3125,4117,4925,5917,6725,7717,8709,9517,10509,11317,12309,13117,14094,14902,15879,16687,8008,8008,8008,8008,8008,20,19,0,0,0,0,0,0,0x451f80,0x85a400,0xd82a00,0x118ae80,0x16b3480,0x1abb900,0x1fe3f00,0x23ec380,0x2914980,0x2e3cf80,0x3245400,0x376da00,0x3b75e80,0x409e480,0x44a6900,0x49c5180,0x4dcd600,0x52ebe80,0x56f4300,0x2a4b000,0x2a4b000,0x2a4b000,0x2a4b000,0x2a4b000,0x2a4b000,0x2a4b000,0x2a4b000,0x2a4b000,0x2a4b000,0x2a4b000,0x2a4b000,0x2a4b000,518,1326,2318,3126,4118,4926,5918,6726,7718,8710,9518,10510,11318,12310,13118,14095,14903,15880,16688,8264,17952,17952,17952,17952,21,20,0,0,0,0,0,0,0x452a00,0x85ae80,0xd83480,0x118b900,0x16b3f00,0x1abc380,0x1fe4980,0x23ece00,0x2915400,0x2e3da00,0x3245e80,0x376e480,0x3b76900,0x409ef00,0x44a7380,0x49c5c00,0x4dce080,0x52ec900,0x56f4d80,0x2b9a580,0x5d77100,0x5d77100,0x5d77100,0x5d77100,0x5d77100,0x5d77100,0x5d77100,0x5d77100,0x5d77100,0x5d77100,0x5d77100,0x5d77100,519,1327,2319,3127,4119,4927,5919,6727,7719,8711,9519,10511,11319,12311,13119,14096,14904,15881,16689,17421,17953,17953,17953,17953,21,20,0,0,0,0,0,0,0x453480,0x85b900,0xd83f00,0x118c380,0x16b4980,0x1abce00,0x1fe5400,0x23ed880,0x2915e80,0x2e3e480,0x3246900,0x376ef00,0x3b77380,0x409f980,0x44a7e00,0x49c6680,0x4dceb00,0x52ed380,0x56f5800,0x5acbe80,0x5d77b80,0x5d77b80,0x5d77b80,0x5d77b80,0x5d77b80,0x5d77b80,0x5d77b80,0x5d77b80,0x5d77b80,0x5d77b80,0x5d77b80,0x5d77b80,520,1328,2320,3128,4120,4928,5920,6912,7720,8712,9520,10512,11320,12312,13120,14097,14905,15882,16690,17422,17954,17954,17954,17954,21,20,0,0,0,0,0,0,0x453f00,0x85c380,0xd84980,0x118ce00,0x16b5400,0x1abd880,0x1fe5e80,0x250e480,0x2916900,0x2e3ef00,0x3247380,0x376f980,0x3b77e00,0x40a0400,0x44a8880,0x49c7100,0x4dcf580,0x52ede00,0x56f6280,0x5acc900,0x5d78600,0x5d78600,0x5d78600,0x5d78600,0x5d78600,0x5d78600,0x5d78600,0x5d78600,0x5d78600,0x5d78600,0x5d78600,0x5d78600,521,1329,2321,3129,4121,4929,5921,6913,7721,8713,9521,10513,11321,12313,13121,14098,14906,15883,16691,8520,8520,8520,8520,8520,20,19,0,0,0,0,0,0,0x454980,0x85ce00,0xd85400,0x118d880,0x16b5e80,0x1abe300,0x1fe6900,0x250ef00,0x2917380,0x2e3f980,0x3247e00,0x3770400,0x3b78880,0x40a0e80,0x44a9300,0x49c7b80,0x4dd0000,0x52ee880,0x56f6d00,0x2ce9b00,0x2ce9b00,0x2ce9b00,0x2ce9b00,0x2ce9b00,0x2ce9b00,0x2ce9b00,0x2ce9b00,0x2ce9b00,0x2ce9b00,0x2ce9b00,0x2ce9b00,0x2ce9b00,522,1330,2322,3130,4122,4930,5922,6914,7722,8714,9522,10514,11322,12314,13122,14099,14907,15884,16692,17423,17955,18235,18235,18235,22,21,0,0,0,0,0,0,0x455400,0x85d880,0xd85e80,0x118e300,0x16b6900,0x1abed80,0x1fe7380,0x250f980,0x2917e00,0x2e40400,0x3248880,0x3770e80,0x3b79300,0x40a1900,0x44a9d80,0x49c8600,0x4dd0a80,0x52ef300,0x56f7780,0x5acd380,0x5d79080,0x5ed8200,0x5ed8200,0x5ed8200,0x5ed8200,0x5ed8200,0x5ed8200,0x5ed8200,0x5ed8200,0x5ed8200,0x5ed8200,0x5ed8200,523,1331,2323,3131,4123,4931,5923,6915,7723,8715,9523,10515,11323,12315,13123,14100,14908,15885,16693,17424,17956,17956,17956,17956,21,20,0,0,0,0,0,0,0x455e80,0x85e300,0xd86900,0x118ed80,0x16b7380,0x1abf800,0x1fe7e00,0x2510400,0x2918880,0x2e40e80,0x3249300,0x3771900,0x3b79d80,0x40a2380,0x44aa800,0x49c9080,0x4dd1500,0x52efd80,0x56f8200,0x5acde00,0x5d79b00,0x5d79b00,0x5d79b00,0x5d79b00,0x5d79b00,0x5d79b00,0x5d79b00,0x5d79b00,0x5d79b00,0x5d79b00,0x5d79b00,0x5d79b00,524,1332,2324,3132,4124,4932,5924,6916,7724,8716,9524,10516,11324,12316,13124,14101,14909,15886,16694,17425,17957,17957,17957,17957,21,20,0,0,0,0,0,0,0x456900,0x85ed80,0xd87380,0x118f800,0x16b7e00,0x1ac0280,0x1fe8880,0x2510e80,0x2919300,0x2e41900,0x3249d80,0x3772380,0x3b7a800,0x40a2e00,0x44ab280,0x49c9b00,0x4dd1f80,0x52f0800,0x56f8c80,0x5ace880,0x5d7a580,0x5d7a580,0x5d7a580,0x5d7a580,0x5d7a580,0x5d7a580,0x5d7a580,0x5d7a580,0x5d7a580,0x5d7a580,0x5d7a580,0x5d7a580,525,1333,2325,3133,4125,4933,5925,6917,7725,8717,9525,10517,11325,12317,13125,14102,14910,15887,16695,8776,8776,8776,8776,8776,20,19,0,0,0,0,0,0,0x457380,0x85f800,0xd87e00,0x1190280,0x16b8880,0x1ac0d00,0x1fe9300,0x2511900,0x2919d80,0x2e42380,0x324a800,0x3772e00,0x3b7b280,0x40a3880,0x44abd00,0x49ca580,0x4dd2a00,0x52f1280,0x56f9700,0x2e39080,0x2e39080,0x2e39080,0x2e39080,0x2e39080,0x2e39080,0x2e39080,0x2e39080,0x2e39080,0x2e39080,0x2e39080,0x2e39080,0x2e39080,526,1334,2326,3134,4126,4934,5926,6918,7726,8718,9526,10518,11326,12318,13126,14103,14911,15888,16696,9032,17958,17958,17958,17958,21,20,0,0,0,0,0,0,0x457e00,0x860280,0xd88880,0x1190d00,0x16b9300,0x1ac1780,0x1fe9d80,0x2512380,0x291a800,0x2e42e00,0x324b280,0x3773880,0x3b7bd00,0x40a4300,0x44ac780,0x49cb000,0x4dd3480,0x52f1d00,0x56fa180,0x2f88600,0x5d7b000,0x5d7b000,0x5d7b000,0x5d7b000,0x5d7b000,0x5d7b000,0x5d7b000,0x5d7b000,0x5d7b000,0x5d7b000,0x5d7b000,0x5d7b000,527,1335,2327,3135,4127,4935,5927,6919,7727,8719,9527,10519,11327,12319,13127,14104,14912,15889,16697,17426,17959,17959,17959,17959,21,20,0,0,0,0,0,0,0x458880,0x860d00,0xd89300,0x1191780,0x16b9d80,0x1ac2200,0x1fea800,0x2512e00,0x291b280,0x2e43880,0x324bd00,0x3774300,0x3b7c780,0x40a4d80,0x44ad200,0x49cba80,0x4dd3f00,0x52f2780,0x56fac00,0x5acf300,0x5d7ba80,0x5d7ba80,0x5d7ba80,0x5d7ba80,0x5d7ba80,0x5d7ba80,0x5d7ba80,0x5d7ba80,0x5d7ba80,0x5d7ba80,0x5d7ba80,0x5d7ba80,528,1336,2328,3136,4128,5120,5928,6920,7728,8720,9528,10520,11328,12320,13312,14105,14913,15890,16698,17427,17960,17960,17960,17960,21,20,0,0,0,0,0,0,0x459300,0x861780,0xd89d80,0x1192200,0x16ba800,0x1be2e00,0x1feb280,0x2513880,0x291bd00,0x2e44300,0x324c780,0x3774d80,0x3b7d200,0x40a5800,0x45cde00,0x49cc500,0x4dd4980,0x52f3200,0x56fb680,0x5acfd80,0x5d7c500,0x5d7c500,0x5d7c500,0x5d7c500,0x5d7c500,0x5d7c500,0x5d7c500,0x5d7c500,0x5d7c500,0x5d7c500,0x5d7c500,0x5d7c500,529,1337,2329,3137,4129,5121,5929,6921,7729,8721,9529,10521,11329,12321,13313,14106,14914,15891,16699,9288,9288,9288,9288,9288,20,19,0,0,0,0,0,0,0x459d80,0x862200,0xd8a800,0x1192c80,0x16bb280,0x1be3880,0x1febd00,0x2514300,0x291c780,0x2e44d80,0x324d200,0x3775800,0x3b7dc80,0x40a6280,0x45ce880,0x49ccf80,0x4dd5400,0x52f3c80,0x56fc100,0x30d7b80,0x30d7b80,0x30d7b80,0x30d7b80,0x30d7b80,0x30d7b80,0x30d7b80,0x30d7b80,0x30d7b80,0x30d7b80,0x30d7b80,0x30d7b80,0x30d7b80,530,1338,2330,3138,4130,5122,5930,6922,7730,8722,9530,10522,11330,12322,13314,14107,14915,15892,16700,17428,17961,18236,18236,18236,22,21,0,0,0,0,0,0,0x45a800,0x862c80,0xd8b280,0x1193700,0x16bbd00,0x1be4300,0x1fec780,0x2514d80,0x291d200,0x2e45800,0x324dc80,0x3776280,0x3b7e700,0x40a6d00,0x45cf300,0x49cda00,0x4dd5e80,0x52f4700,0x56fcb80,0x5ad0800,0x5d7cf80,0x5ed8c80,0x5ed8c80,0x5ed8c80,0x5ed8c80,0x5ed8c80,0x5ed8c80,0x5ed8c80,0x5ed8c80,0x5ed8c80,0x5ed8c80,0x5ed8c80,531,1339,2331,3139,4131,5123,5931,6923,7731,8723,9531,10523,11331,12323,13315,14108,14916,15893,16701,17429,17962,17962,17962,17962,21,20,0,0,0,0,0,0,0x45b280,0x863700,0xd8bd00,0x1194180,0x16bc780,0x1be4d80,0x1fed200,0x2515800,0x291dc80,0x2e46280,0x324e700,0x3776d00,0x3b7f180,0x40a7780,0x45cfd80,0x49ce480,0x4dd6900,0x52f5180,0x56fd600,0x5ad1280,0x5d7da00,0x5d7da00,0x5d7da00,0x5d7da00,0x5d7da00,0x5d7da00,0x5d7da00,0x5d7da00,0x5d7da00,0x5d7da00,0x5d7da00,0x5d7da00,532,1340,2332,3140,4132,5124,5932,6924,7732,8724,9532,10524,11332,12324,13316,14109,14917,15894,16702,17430,17963,17963,17963,17963,21,20,0,0,0,0,0,0,0x45bd00,0x864180,0xd8c780,0x1194c00,0x16bd200,0x1be5800,0x1fedc80,0x2516280,0x291e700,0x2e46d00,0x324f180,0x3777780,0x3b7fc00,0x40a8200,0x45d0800,0x49cef00,0x4dd7380,0x52f5c00,0x56fe080,0x5ad1d00,0x5d7e480,0x5d7e480,0x5d7e480,0x5d7e480,0x5d7e480,0x5d7e480,0x5d7e480,0x5d7e480,0x5d7e480,0x5d7e480,0x5d7e480,0x5d7e480,533,1341,2333,3141,4133,5125,5933,6925,7733,8725,9533,10525,11333,12325,13317,14110,14918,15895,16703,9544,9544,9544,9544,9544,20,19,0,0,0,0,0,0,0x45c780,0x864c00,0xd8d200,0x1195680,0x16bdc80,0x1be6280,0x1fee700,0x2516d00,0x291f180,0x2e47780,0x324fc00,0x3778200,0x3b80680,0x40a8c80,0x45d1280,0x49cf980,0x4dd7e00,0x52f6680,0x56feb00,0x3227100,0x3227100,0x3227100,0x3227100,0x3227100,0x3227100,0x3227100,0x3227100,0x3227100,0x3227100,0x3227100,0x3227100,0x3227100,534,1342,2334,3142,4134,5126,5934,6926,7734,8726,9534,10526,11334,12326,13318,14111,14919,15896,16704,9800,17964,17964,17964,17964,21,20,0,0,0,0,0,0,0x45d200,0x865680,0xd8dc80,0x1196100,0x16be700,0x1be6d00,0x1fef180,0x2517780,0x291fc00,0x2e48200,0x3250680,0x3778c80,0x3b81100,0x40a9700,0x45d1d00,0x49d0400,0x4dd8880,0x52f7100,0x56ff580,0x3376680,0x5d7ef00,0x5d7ef00,0x5d7ef00,0x5d7ef00,0x5d7ef00,0x5d7ef00,0x5d7ef00,0x5d7ef00,0x5d7ef00,0x5d7ef00,0x5d7ef00,0x5d7ef00,535,1343,2335,3143,4135,5127,5935,6927,7735,8727,9535,10527,11335,12327,13319,14112,15104,15897,16705,17431,17965,17965,17965,17965,21,20,0,0,0,0,0,0,0x45dc80,0x866100,0xd8e700,0x1196b80,0x16bf180,0x1be7780,0x1fefc00,0x2518200,0x2920680,0x2e48c80,0x3251100,0x3779700,0x3b81b80,0x40aa180,0x45d2780,0x49d0e80,0x4ef9480,0x52f7b80,0x5700000,0x5ad2780,0x5d7f980,0x5d7f980,0x5d7f980,0x5d7f980,0x5d7f980,0x5d7f980,0x5d7f980,0x5d7f980,0x5d7f980,0x5d7f980,0x5d7f980,0x5d7f980,536,1344,2336,3328,4136,5128,5936,6928,7736,8728,9536,10528,11520,12328,13320,14113,15105,15898,16706,17432,17966,17966,17966,17966,21,20,0,0,0,0,0,0,0x45e700,0x866b80,0xd8f180,0x12b7780,0x16bfc00,0x1be8200,0x1ff0680,0x2518c80,0x2921100,0x2e49700,0x3251b80,0x377a180,0x3ca2780,0x40aac00,0x45d3200,0x49d1900,0x4ef9f00,0x52f8600,0x5700a80,0x5ad3200,0x5d80400,0x5d80400,0x5d80400,0x5d80400,0x5d80400,0x5d80400,0x5d80400,0x5d80400,0x5d80400,0x5d80400,0x5d80400,0x5d80400,537,1345,2337,3329,4137,5129,5937,6929,7737,8729,9537,10529,11521,12329,13321,14114,15106,15899,16707,10056,10056,10056,10056,10056,20,19,0,0,0,0,0,0,0x45f180,0x867600,0xd8fc00,0x12b8200,0x16c0680,0x1be8c80,0x1ff1100,0x2519700,0x2921b80,0x2e4a180,0x3252600,0x377ac00,0x3ca3200,0x40ab680,0x45d3c80,0x49d2380,0x4efa980,0x52f9080,0x5701500,0x34c5c00,0x34c5c00,0x34c5c00,0x34c5c00,0x34c5c00,0x34c5c00,0x34c5c00,0x34c5c00,0x34c5c00,0x34c5c00,0x34c5c00,0x34c5c00,0x34c5c00,538,1346,2338,3330,4138,5130,5938,6930,7738,8730,9538,10530,11522,12330,13322,14115,15107,15900,16708,17433,17967,18237,18237,18237,22,21,0,0,0,0,0,0,0x45fc00,0x868080,0xd90680,0x12b8c80,0x16c1100,0x1be9700,0x1ff1b80,0x251a180,0x2922600,0x2e4ac00,0x3253080,0x377b680,0x3ca3c80,0x40ac100,0x45d4700,0x49d2e00,0x4efb400,0x52f9b00,0x5701f80,0x5ad3c80,0x5d80e80,0x5ed9700,0x5ed9700,0x5ed9700,0x5ed9700,0x5ed9700,0x5ed9700,0x5ed9700,0x5ed9700,0x5ed9700,0x5ed9700,0x5ed9700,539,1347,2339,3331,4139,5131,5939,6931,7739,8731,9539,10531,11523,12331,13323,14116,15108,15901,16709,17434,17968,17968,17968,17968,21,20,0,0,0,0,0,0,0x460680,0x868b00,0xd91100,0x12b9700,0x16c1b80,0x1bea180,0x1ff2600,0x251ac00,0x2923080,0x2e4b680,0x3253b00,0x377c100,0x3ca4700,0x40acb80,0x45d5180,0x49d3880,0x4efbe80,0x52fa580,0x5702a00,0x5ad4700,0x5d81900,0x5d81900,0x5d81900,0x5d81900,0x5d81900,0x5d81900,0x5d81900,0x5d81900,0x5d81900,0x5d81900,0x5d81900,0x5d81900,540,1348,2340,3332,4140,5132,5940,6932,7740,8732,9540,10532,11524,12332,13324,14117,15109,15902,16710,17435,17969,17969,17969,17969,21,20,0,0,0,0,0,0,0x461100,0x869580,0xd91b80,0x12ba180,0x16c2600,0x1beac00,0x1ff3080,0x251b680,0x2923b00,0x2e4c100,0x3254580,0x377cb80,0x3ca5180,0x40ad600,0x45d5c00,0x49d4300,0x4efc900,0x52fb000,0x5703480,0x5ad5180,0x5d82380,0x5d82380,0x5d82380,0x5d82380,0x5d82380,0x5d82380,0x5d82380,0x5d82380,0x5d82380,0x5d82380,0x5d82380,0x5d82380,541,1349,2341,3333,4141,5133,5941,6933,7741,8733,9541,10533,11525,12333,13325,14118,15110,15903,16711,10312,10312,10312,10312,10312,20,19,0,0,0,0,0,0,0x461b80,0x86a000,0xd92600,0x12bac00,0x16c3080,0x1beb680,0x1ff3b00,0x251c100,0x2924580,0x2e4cb80,0x3255000,0x377d600,0x3ca5c00,0x40ae080,0x45d6680,0x49d4d80,0x4efd380,0x52fba80,0x5703f00,0x3615180,0x3615180,0x3615180,0x3615180,0x3615180,0x3615180,0x3615180,0x3615180,0x3615180,0x3615180,0x3615180,0x3615180,0x3615180,542,1350,2342,3334,4142,5134,5942,6934,7742,8734,9542,10534,11526,12334,13326,14119,15111,15904,16896,10568,17970,17970,17970,17970,21,20,0,0,0,0,0,0,0x462600,0x86aa80,0xd93080,0x12bb680,0x16c3b00,0x1bec100,0x1ff4580,0x251cb80,0x2925000,0x2e4d600,0x3255a80,0x377e080,0x3ca6680,0x40aeb00,0x45d7100,0x49d5800,0x4efde00,0x52fc500,0x5824b00,0x3764700,0x5d82e00,0x5d82e00,0x5d82e00,0x5d82e00,0x5d82e00,0x5d82e00,0x5d82e00,0x5d82e00,0x5d82e00,0x5d82e00,0x5d82e00,0x5d82e00,543,1351,2343,3335,4143,5135,5943,6935,7743,8735,9543,10535,11527,12335,13327,14120,15112,15905,16897,17436,17971,17971,17971,17971,21,20,0,0,0,0,0,0,0x463080,0x86b500,0xd93b00,0x12bc100,0x16c4580,0x1becb80,0x1ff5000,0x251d600,0x2925a80,0x2e4e080,0x3256500,0x377eb00,0x3ca7100,0x40af580,0x45d7b80,0x49d6280,0x4efe880,0x52fcf80,0x5825580,0x5ad5c00,0x5d83880,0x5d83880,0x5d83880,0x5d83880,0x5d83880,0x5d83880,0x5d83880,0x5d83880,0x5d83880,0x5d83880,0x5d83880,0x5d83880,544,1536,2344,3336,4144,5136,5944,6936,7744,8736,9728,10536,11528,12336,13328,14121,15113,15906,16898,17437,17972,17972,17972,17972,21,20,0,0,0,0,0,0,0x463b00,0x98c100,0xd94580,0x12bcb80,0x16c5000,0x1bed600,0x1ff5a80,0x251e080,0x2926500,0x2e4eb00,0x3377100,0x377f580,0x3ca7b80,0x40b0000,0x45d8600,0x49d6d00,0x4eff300,0x52fda00,0x5826000,0x5ad6680,0x5d84300,0x5d84300,0x5d84300,0x5d84300,0x5d84300,0x5d84300,0x5d84300,0x5d84300,0x5d84300,0x5d84300,0x5d84300,0x5d84300,545,1537,2345,3337,4145,5137,5945,6937,7745,8737,9729,10537,11529,12337,13329,14122,15114,15907,16899,10824,10824,10824,10824,10824,20,19,0,0,0,0,0,0,0x464580,0x98cb80,0xd95000,0x12bd600,0x16c5a80,0x1bee080,0x1ff6500,0x251eb00,0x2926f80,0x2e4f580,0x3377b80,0x3780000,0x3ca8600,0x40b0a80,0x45d9080,0x49d7780,0x4effd80,0x52fe480,0x5826a80,0x38b3c80,0x38b3c80,0x38b3c80,0x38b3c80,0x38b3c80,0x38b3c80,0x38b3c80,0x38b3c80,0x38b3c80,0x38b3c80,0x38b3c80,0x38b3c80,0x38b3c80,546,1538,2346,3338,4146,5138,5946,6938,7746,8738,9730,10538,11530,12338,13330,14123,15115,15908,16900,17438,17973,18238,18238,18238,22,21,0,0,0,0,0,0,0x465000,0x98d600,0xd95a80,0x12be080,0x16c6500,0x1beeb00,0x1ff6f80,0x251f580,0x2927a00,0x2e50000,0x3378600,0x3780a80,0x3ca9080,0x40b1500,0x45d9b00,0x49d8200,0x4f00800,0x52fef00,0x5827500,0x5ad7100,0x5d84d80,0x5eda180,0x5eda180,0x5eda180,0x5eda180,0x5eda180,0x5eda180,0x5eda180,0x5eda180,0x5eda180,0x5eda180,0x5eda180,547,1539,2347,3339,4147,5139,5947,6939,7747,8739,9731,10539,11531,12339,13331,14124,15116,15909,16901,17439,17974,17974,17974,17974,21,20,0,0,0,0,0,0,0x465a80,0x98e080,0xd96500,0x12beb00,0x16c6f80,0x1bef580,0x1ff7a00,0x2520000,0x2928480,0x2e50a80,0x3379080,0x3781500,0x3ca9b00,0x40b1f80,0x45da580,0x49d8c80,0x4f01280,0x52ff980,0x5827f80,0x5ad7b80,0x5d85800,0x5d85800,0x5d85800,0x5d85800,0x5d85800,0x5d85800,0x5d85800,0x5d85800,0x5d85800,0x5d85800,0x5d85800,0x5d85800,548,1540,2348,3340,4148,5140,5948,6940,7748,8740,9732,10540,11532,12340,13332,14125,15117,15910,16902,17440,17975,17975,17975,17975,21,20,0,0,0,0,0,0,0x466500,0x98eb00,0xd96f80,0x12bf580,0x16c7a00,0x1bf0000,0x1ff8480,0x2520a80,0x2928f00,0x2e51500,0x3379b00,0x3781f80,0x3caa580,0x40b2a00,0x45db000,0x49d9700,0x4f01d00,0x5300400,0x5828a00,0x5ad8600,0x5d86280,0x5d86280,0x5d86280,0x5d86280,0x5d86280,0x5d86280,0x5d86280,0x5d86280,0x5d86280,0x5d86280,0x5d86280,0x5d86280,549,1541,2349,3341,4149,5141,5949,6941,7749,8741,9733,10541,11533,12341,13333,14126,15118,15911,16903,11080,11080,11080,11080,11080,20,19,0,0,0,0,0,0,0x466f80,0x98f580,0xd97a00,0x12c0000,0x16c8480,0x1bf0a80,0x1ff8f00,0x2521500,0x2929980,0x2e51f80,0x337a580,0x3782a00,0x3cab000,0x40b3480,0x45dba80,0x49da180,0x4f02780,0x5300e80,0x5829480,0x3a03200,0x3a03200,0x3a03200,0x3a03200,0x3a03200,0x3a03200,0x3a03200,0x3a03200,0x3a03200,0x3a03200,0x3a03200,0x3a03200,0x3a03200,550,1542,2350,3342,4150,5142,5950,6942,7750,8742,9734,10542,11534,12342,13334,14127,15119,15912,16904,11336,17976,17976,17976,17976,21,20,0,0,0,0,0,0,0x467a00,0x990000,0xd98480,0x12c0a80,0x16c8f00,0x1bf1500,0x1ff9980,0x2521f80,0x292a400,0x2e52a00,0x337b000,0x3783480,0x3caba80,0x40b3f00,0x45dc500,0x49dac00,0x4f03200,0x5301900,0x5829f00,0x3b52780,0x5d86d00,0x5d86d00,0x5d86d00,0x5d86d00,0x5d86d00,0x5d86d00,0x5d86d00,0x5d86d00,0x5d86d00,0x5d86d00,0x5d86d00,0x5d86d00,551,1543,2351,3343,4151,5143,5951,6943,7751,8743,9735,10543,11535,12343,13335,14128,15120,15913,16905,17441,17977,17977,17977,17977,21,20,0,0,0,0,0,0,0x468480,0x990a80,0xd98f00,0x12c1500,0x16c9980,0x1bf1f80,0x1ffa400,0x2522a00,0x292ae80,0x2e53480,0x337ba80,0x3783f00,0x3cac500,0x40b4980,0x45dcf80,0x49db680,0x4f03c80,0x5302380,0x582a980,0x5ad9080,0x5d87780,0x5d87780,0x5d87780,0x5d87780,0x5d87780,0x5d87780,0x5d87780,0x5d87780,0x5d87780,0x5d87780,0x5d87780,0x5d87780,552,1544,2352,3344,4152,5144,5952,6944,7936,8744,9736,10544,11536,12344,13336,14129,15121,15914,16906,17442,17978,17978,17978,17978,21,20,0,0,0,0,0,0,0x468f00,0x991500,0xd99980,0x12c1f80,0x16ca400,0x1bf2a00,0x1ffae80,0x2523480,0x2a4ba80,0x2e53f00,0x337c500,0x3784980,0x3cacf80,0x40b5400,0x45dda00,0x49dc100,0x4f04700,0x5302e00,0x582b400,0x5ad9b00,0x5d88200,0x5d88200,0x5d88200,0x5d88200,0x5d88200,0x5d88200,0x5d88200,0x5d88200,0x5d88200,0x5d88200,0x5d88200,0x5d88200,553,1545,2353,3345,4153,5145,5953,6945,7937,8745,9737,10545,11537,12345,13337,14130,15122,15915,16907,11592,11592,11592,11592,11592,20,19,0,0,0,0,0,0,0x469980,0x991f80,0xd9a400,0x12c2a00,0x16cae80,0x1bf3480,0x1ffb900,0x2523f00,0x2a4c500,0x2e54980,0x337cf80,0x3785400,0x3cada00,0x40b5e80,0x45de480,0x49dcb80,0x4f05180,0x5303880,0x582be80,0x3ca1d00,0x3ca1d00,0x3ca1d00,0x3ca1d00,0x3ca1d00,0x3ca1d00,0x3ca1d00,0x3ca1d00,0x3ca1d00,0x3ca1d00,0x3ca1d00,0x3ca1d00,0x3ca1d00,554,1546,2354,3346,4154,5146,5954,6946,7938,8746,9738,10546,11538,12346,13338,14131,15123,15916,16908,17443,17979,18239,18239,18239,22,21,0,0,0,0,0,0,0x46a400,0x992a00,0xd9ae80,0x12c3480,0x16cb900,0x1bf3f00,0x1ffc380,0x2524980,0x2a4cf80,0x2e55400,0x337da00,0x3785e80,0x3cae480,0x40b6900,0x45def00,0x49dd600,0x4f05c00,0x5304300,0x582c900,0x5ada580,0x5d88c80,0x5edac00,0x5edac00,0x5edac00,0x5edac00,0x5edac00,0x5edac00,0x5edac00,0x5edac00,0x5edac00,0x5edac00,0x5edac00,555,1547,2355,3347,4155,5147,5955,6947,7939,8747,9739,10547,11539,12347,13339,14132,15124,15917,16909,17444,17980,17980,17980,17980,21,20,0,0,0,0,0,0,0x46ae80,0x993480,0xd9b900,0x12c3f00,0x16cc380,0x1bf4980,0x1ffce00,0x2525400,0x2a4da00,0x2e55e80,0x337e480,0x3786900,0x3caef00,0x40b7380,0x45df980,0x49de080,0x4f06680,0x5304d80,0x582d380,0x5adb000,0x5d89700,0x5d89700,0x5d89700,0x5d89700,0x5d89700,0x5d89700,0x5d89700,0x5d89700,0x5d89700,0x5d89700,0x5d89700,0x5d89700,556,1548,2356,3348,4156,5148,5956,6948,7940,8748,9740,10548,11540,12348,13340,14133,15125,15918,16910,17445,17981,17981,17981,17981,21,20,0,0,0,0,0,0,0x46b900,0x993f00,0xd9c380,0x12c4980,0x16cce00,0x1bf5400,0x1ffd880,0x2525e80,0x2a4e480,0x2e56900,0x337ef00,0x3787380,0x3caf980,0x40b7e00,0x45e0400,0x49deb00,0x4f07100,0x5305800,0x582de00,0x5adba80,0x5d8a180,0x5d8a180,0x5d8a180,0x5d8a180,0x5d8a180,0x5d8a180,0x5d8a180,0x5d8a180,0x5d8a180,0x5d8a180,0x5d8a180,0x5d8a180,557,1549,2357,3349,4157,5149,5957,6949,7941,8749,9741,10549,11541,12349,13341,14134,15126,15919,16911,11848,11848,11848,11848,11848,20,19,0,0,0,0,0,0,0x46c380,0x994980,0xd9ce00,0x12c5400,0x16cd880,0x1bf5e80,0x1ffe300,0x2526900,0x2a4ef00,0x2e57380,0x337f980,0x3787e00,0x3cb0400,0x40b8880,0x45e0e80,0x49df580,0x4f07b80,0x5306280,0x582e880,0x3df1280,0x3df1280,0x3df1280,0x3df1280,0x3df1280,0x3df1280,0x3df1280,0x3df1280,0x3df1280,0x3df1280,0x3df1280,0x3df1280,0x3df1280,558,1550,2358,3350,4158,5150,5958,6950,7942,8750,9742,10550,11542,12350,13342,14135,15127,15920,16912,12104,17982,17982,17982,17982,21,20,0,0,0,0,0,0,0x46ce00,0x995400,0xd9d880,0x12c5e80,0x16ce300,0x1bf6900,0x1ffed80,0x2527380,0x2a4f980,0x2e57e00,0x3380400,0x3788880,0x3cb0e80,0x40b9300,0x45e1900,0x49e0000,0x4f08600,0x5306d00,0x582f300,0x3f40800,0x5d8ac00,0x5d8ac00,0x5d8ac00,0x5d8ac00,0x5d8ac00,0x5d8ac00,0x5d8ac00,0x5d8ac00,0x5d8ac00,0x5d8ac00,0x5d8ac00,0x5d8ac00,559,1551,2359,3351,4159,5151,5959,6951,7943,8751,9743,10551,11543,12351,13343,14136,15128,15921,16913,17446,17983,17983,17983,17983,21,20,0,0,0,0,0,0,0x46d880,0x995e80,0xd9e300,0x12c6900,0x16ced80,0x1bf7380,0x1fff800,0x2527e00,0x2a50400,0x2e58880,0x3380e80,0x3789300,0x3cb1900,0x40b9d80,0x45e2380,0x49e0a80,0x4f09080,0x5307780,0x582fd80,0x5adc500,0x5d8b680,0x5d8b680,0x5d8b680,0x5d8b680,0x5d8b680,0x5d8b680,0x5d8b680,0x5d8b680,0x5d8b680,0x5d8b680,0x5d8b680,0x5d8b680,560,1552,2360,3352,4160,5152,6144,6952,7944,8752,9744,10552,11544,12352,13344,14137,15129,15922,16914,17447,17984,17984,17984,17984,21,20,0,0,0,0,0,0,0x46e300,0x996900,0xd9ed80,0x12c7380,0x16cf800,0x1bf7e00,0x2120400,0x2528880,0x2a50e80,0x2e59300,0x3381900,0x3789d80,0x3cb2380,0x40ba800,0x45e2e00,0x49e1500,0x4f09b00,0x5308200,0x5830800,0x5adcf80,0x5d8c100,0x5d8c100,0x5d8c100,0x5d8c100,0x5d8c100,0x5d8c100,0x5d8c100,0x5d8c100,0x5d8c100,0x5d8c100,0x5d8c100,0x5d8c100,561,1553,2361,3353,4161,5153,6145,6953,7945,8753,9745,10553,11545,12353,13345,14138,15130,15923,16915,12360,12360,12360,12360,12360,20,19,0,0,0,0,0,0,0x46ed80,0x997380,0xd9f800,0x12c7e00,0x16d0280,0x1bf8880,0x2120e80,0x2529300,0x2a51900,0x2e59d80,0x3382380,0x378a800,0x3cb2e00,0x40bb280,0x45e3880,0x49e1f80,0x4f0a580,0x5308c80,0x5831280,0x408fd80,0x408fd80,0x408fd80,0x408fd80,0x408fd80,0x408fd80,0x408fd80,0x408fd80,0x408fd80,0x408fd80,0x408fd80,0x408fd80,0x408fd80,562,1554,2362,3354,4162,5154,6146,6954,7946,8754,9746,10554,11546,12354,13346,14139,15131,15924,16916,17448,17985,18240,18240,18240,22,21,0,0,0,0,0,0,0x46f800,0x997e00,0xda0280,0x12c8880,0x16d0d00,0x1bf9300,0x2121900,0x2529d80,0x2a52380,0x2e5a800,0x3382e00,0x378b280,0x3cb3880,0x40bbd00,0x45e4300,0x49e2a00,0x4f0b000,0x5309700,0x5831d00,0x5adda00,0x5d8cb80,0x5edb680,0x5edb680,0x5edb680,0x5edb680,0x5edb680,0x5edb680,0x5edb680,0x5edb680,0x5edb680,0x5edb680,0x5edb680,563,1555,2363,3355,4163,5155,6147,6955,7947,8755,9747,10555,11547,12355,13347,14140,15132,15925,16917,17449,17986,17986,17986,17986,21,20,0,0,0,0,0,0,0x470280,0x998880,0xda0d00,0x12c9300,0x16d1780,0x1bf9d80,0x2122380,0x252a800,0x2a52e00,0x2e5b280,0x3383880,0x378bd00,0x3cb4300,0x40bc780,0x45e4d80,0x49e3480,0x4f0ba80,0x530a180,0x5832780,0x5ade480,0x5d8d600,0x5d8d600,0x5d8d600,0x5d8d600,0x5d8d600,0x5d8d600,0x5d8d600,0x5d8d600,0x5d8d600,0x5d8d600,0x5d8d600,0x5d8d600,564,1556,2364,3356,4164,5156,6148,6956,7948,8756,9748,10556,11548,12356,13348,14141,15133,15926,16918,17450,17987,17987,17987,17987,21,20,0,0,0,0,0,0,0x470d00,0x999300,0xda1780,0x12c9d80,0x16d2200,0x1bfa800,0x2122e00,0x252b280,0x2a53880,0x2e5bd00,0x3384300,0x378c780,0x3cb4d80,0x40bd200,0x45e5800,0x49e3f00,0x4f0c500,0x530ac00,0x5833200,0x5adef00,0x5d8e080,0x5d8e080,0x5d8e080,0x5d8e080,0x5d8e080,0x5d8e080,0x5d8e080,0x5d8e080,0x5d8e080,0x5d8e080,0x5d8e080,0x5d8e080,565,1557,2365,3357,4165,5157,6149,6957,7949,8757,9749,10557,11549,12357,13349,14142,15134,15927,16919,12616,12616,12616,12616,12616,20,19,0,0,0,0,0,0,0x471780,0x999d80,0xda2200,0x12ca800,0x16d2c80,0x1bfb280,0x2123880,0x252bd00,0x2a54300,0x2e5c780,0x3384d80,0x378d200,0x3cb5800,0x40bdc80,0x45e6280,0x49e4980,0x4f0cf80,0x530b680,0x5833c80,0x41df300,0x41df300,0x41df300,0x41df300,0x41df300,0x41df300,0x41df300,0x41df300,0x41df300,0x41df300,0x41df300,0x41df300,0x41df300,566,1558,2366,3358,4166,5158,6150,6958,7950,8758,9750,10558,11550,12358,13350,14143,15135,15928,16920,12872,17988,17988,17988,17988,21,20,0,0,0,0,0,0,0x472200,0x99a800,0xda2c80,0x12cb280,0x16d3700,0x1bfbd00,0x2124300,0x252c780,0x2a54d80,0x2e5d200,0x3385800,0x378dc80,0x3cb6280,0x40be700,0x45e6d00,0x49e5400,0x4f0da00,0x530c100,0x5834700,0x432e880,0x5d8eb00,0x5d8eb00,0x5d8eb00,0x5d8eb00,0x5d8eb00,0x5d8eb00,0x5d8eb00,0x5d8eb00,0x5d8eb00,0x5d8eb00,0x5d8eb00,0x5d8eb00,567,1559,2367,3359,4167,5159,6151,6959,7951,8759,9751,10559,11551,12359,13351,14144,15136,15929,16921,17451,17989,17989,17989,17989,21,20,0,0,0,0,0,0,0x472c80,0x99b280,0xda3700,0x12cbd00,0x16d4180,0x1bfc780,0x2124d80,0x252d200,0x2a55800,0x2e5dc80,0x3386280,0x378e700,0x3cb6d00,0x40bf180,0x45e7780,0x49e5e80,0x4f0e480,0x530cb80,0x5835180,0x5adf980,0x5d8f580,0x5d8f580,0x5d8f580,0x5d8f580,0x5d8f580,0x5d8f580,0x5d8f580,0x5d8f580,0x5d8f580,0x5d8f580,0x5d8f580,0x5d8f580,568,1560,2368,3360,4352,5160,6152,6960,7952,8760,9752,10560,11552,12544,13352,14145,15137,15930,16922,17452,17990,17990,17990,17990,21,20,0,0,0,0,0,0,0x473700,0x99bd00,0xda4180,0x12cc780,0x17f4d80,0x1bfd200,0x2125800,0x252dc80,0x2a56280,0x2e5e700,0x3386d00,0x378f180,0x3cb7780,0x41dfd80,0x45e8200,0x49e6900,0x4f0ef00,0x530d600,0x5835c00,0x5ae0400,0x5d90000,0x5d90000,0x5d90000,0x5d90000,0x5d90000,0x5d90000,0x5d90000,0x5d90000,0x5d90000,0x5d90000,0x5d90000,0x5d90000,569,1561,2369,3361,4353,5161,6153,6961,7953,8761,9753,10561,11553,12545,13353,14146,15138,15931,16923,13128,13128,13128,13128,13128,20,19,0,0,0,0,0,0,0x474180,0x99c780,0xda4c00,0x12cd200,0x17f5800,0x1bfdc80,0x2126280,0x252e700,0x2a56d00,0x2e5f180,0x3387780,0x378fc00,0x3cb8200,0x41e0800,0x45e8c80,0x49e7380,0x4f0f980,0x530e080,0x5836680,0x447de00,0x447de00,0x447de00,0x447de00,0x447de00,0x447de00,0x447de00,0x447de00,0x447de00,0x447de00,0x447de00,0x447de00,0x447de00,570,1562,2370,3362,4354,5162,6154,6962,7954,8762,9754,10562,11554,12546,13354,14147,15139,15932,16924,17453,17991,18241,18241,18241,22,21,0,0,0,0,0,0,0x474c00,0x99d200,0xda5680,0x12cdc80,0x17f6280,0x1bfe700,0x2126d00,0x252f180,0x2a57780,0x2e5fc00,0x3388200,0x3790680,0x3cb8c80,0x41e1280,0x45e9700,0x49e7e00,0x4f10400,0x530eb00,0x5837100,0x5ae0e80,0x5d90a80,0x5edc100,0x5edc100,0x5edc100,0x5edc100,0x5edc100,0x5edc100,0x5edc100,0x5edc100,0x5edc100,0x5edc100,0x5edc100,571,1563,2371,3363,4355,5163,6155,6963,7955,8763,9755,10563,11555,12547,13355,14148,15140,15933,16925,17454,18176,18176,18176,18176,21,20,0,0,0,0,0,0,0x475680,0x99dc80,0xda6100,0x12ce700,0x17f6d00,0x1bff180,0x2127780,0x252fc00,0x2a58200,0x2e60680,0x3388c80,0x3791100,0x3cb9700,0x41e1d00,0x45ea180,0x49e8880,0x4f10e80,0x530f580,0x5837b80,0x5ae1900,0x5eb1680,0x5eb1680,0x5eb1680,0x5eb1680,0x5eb1680,0x5eb1680,0x5eb1680,0x5eb1680,0x5eb1680,0x5eb1680,0x5eb1680,0x5eb1680,572,1564,2372,3364,4356,5164,6156,6964,7956,8764,9756,10564,11556,12548,13356,14149,15141,15934,16926,17455,18177,18177,18177,18177,21,20,0,0,0,0,0,0,0x476100,0x99e700,0xda6b80,0x12cf180,0x17f7780,0x1bffc00,0x2128200,0x2530680,0x2a58c80,0x2e61100,0x3389700,0x3791b80,0x3cba180,0x41e2780,0x45eac00,0x49e9300,0x4f11900,0x5310000,0x5838600,0x5ae2380,0x5eb2100,0x5eb2100,0x5eb2100,0x5eb2100,0x5eb2100,0x5eb2100,0x5eb2100,0x5eb2100,0x5eb2100,0x5eb2100,0x5eb2100,0x5eb2100,573,1565,2373,3365,4357,5165,6157,6965,7957,8765,9757,10565,11557,12549,13357,14150,15142,15935,16927,13384,13384,13384,13384,13384,20,19,0,0,0,0,0,0,0x476b80,0x99f180,0xda7600,0x12cfc00,0x17f8200,0x1c00680,0x2128c80,0x2531100,0x2a59700,0x2e61b80,0x338a180,0x3792600,0x3cbac00,0x41e3200,0x45eb680,0x49e9d80,0x4f12380,0x5310a80,0x5839080,0x45cd380,0x45cd380,0x45cd380,0x45cd380,0x45cd380,0x45cd380,0x45cd380,0x45cd380,0x45cd380,0x45cd380,0x45cd380,0x45cd380,0x45cd380,574,1566,2374,3366,4358,5166,6158,6966,7958,8766,9758,10566,11558,12550,13358,14151,15143,15936,16928,13640,18178,18178,18178,18178,21,20,0,0,0,0,0,0,0x477600,0x99fc00,0xda8080,0x12d0680,0x17f8c80,0x1c01100,0x2129700,0x2531b80,0x2a5a180,0x2e62600,0x338ac00,0x3793080,0x3cbb680,0x41e3c80,0x45ec100,0x49ea800,0x4f12e00,0x5311500,0x5839b00,0x471c900,0x5eb2b80,0x5eb2b80,0x5eb2b80,0x5eb2b80,0x5eb2b80,0x5eb2b80,0x5eb2b80,0x5eb2b80,0x5eb2b80,0x5eb2b80,0x5eb2b80,0x5eb2b80,575,1567,2375,3367,4359,5167,6159,6967,7959,8767,9759,10567,11559,12551,13359,14336,15144,15937,16929,17456,18179,18179,18179,18179,21,20,0,0,0,0,0,0,0x478080,0x9a0680,0xda8b00,0x12d1100,0x17f9700,0x1c01b80,0x212a180,0x2532600,0x2a5ac00,0x2e63080,0x338b680,0x3793b00,0x3cbc100,0x41e4700,0x45ecb80,0x4b0b400,0x4f13880,0x5311f80,0x583a580,0x5ae2e00,0x5eb3600,0x5eb3600,0x5eb3600,0x5eb3600,0x5eb3600,0x5eb3600,0x5eb3600,0x5eb3600,0x5eb3600,0x5eb3600,0x5eb3600,0x5eb3600,576,1568,2560,3368,4360,5168,6160,6968,7960,8768,9760,10752,11560,12552,13360,14337,15145,15938,16930,17457,18180,18180,18180,18180,21,20,0,0,0,0,0,0,0x478b00,0x9a1100,0xec9700,0x12d1b80,0x17fa180,0x1c02600,0x212ac00,0x2533080,0x2a5b680,0x2e63b00,0x338c100,0x38b4700,0x3cbcb80,0x41e5180,0x45ed600,0x4b0be80,0x4f14300,0x5312a00,0x583b000,0x5ae3880,0x5eb4080,0x5eb4080,0x5eb4080,0x5eb4080,0x5eb4080,0x5eb4080,0x5eb4080,0x5eb4080,0x5eb4080,0x5eb4080,0x5eb4080,0x5eb4080,577,1569,2561,3369,4361,5169,6161,6969,7961,8769,9761,10753,11561,12553,13361,14338,15146,15939,16931,13896,13896,13896,13896,13896,20,19,0,0,0,0,0,0,0x479580,0x9a1b80,0xeca180,0x12d2600,0x17fac00,0x1c03080,0x212b680,0x2533b00,0x2a5c100,0x2e64580,0x338cb80,0x38b5180,0x3cbd600,0x41e5c00,0x45ee080,0x4b0c900,0x4f14d80,0x5313480,0x583ba80,0x486be80,0x486be80,0x486be80,0x486be80,0x486be80,0x486be80,0x486be80,0x486be80,0x486be80,0x486be80,0x486be80,0x486be80,0x486be80,578,1570,2562,3370,4362,5170,6162,6970,7962,8770,9762,10754,11562,12554,13362,14339,15147,15940,16932,17458,18181,18242,18242,18242,22,21,0,0,0,0,0,0,0x47a000,0x9a2600,0xecac00,0x12d3080,0x17fb680,0x1c03b00,0x212c100,0x2534580,0x2a5cb80,0x2e65000,0x338d600,0x38b5c00,0x3cbe080,0x41e6680,0x45eeb00,0x4b0d380,0x4f15800,0x5313f00,0x583c500,0x5ae4300,0x5eb4b00,0x5edcb80,0x5edcb80,0x5edcb80,0x5edcb80,0x5edcb80,0x5edcb80,0x5edcb80,0x5edcb80,0x5edcb80,0x5edcb80,0x5edcb80,579,1571,2563,3371,4363,5171,6163,6971,7963,8771,9763,10755,11563,12555,13363,14340,15148,15941,16933,17459,18182,18182,18182,18182,21,20,0,0,0,0,0,0,0x47aa80,0x9a3080,0xecb680,0x12d3b00,0x17fc100,0x1c04580,0x212cb80,0x2535000,0x2a5d600,0x2e65a80,0x338e080,0x38b6680,0x3cbeb00,0x41e7100,0x45ef580,0x4b0de00,0x4f16280,0x5314980,0x583cf80,0x5ae4d80,0x5eb5580,0x5eb5580,0x5eb5580,0x5eb5580,0x5eb5580,0x5eb5580,0x5eb5580,0x5eb5580,0x5eb5580,0x5eb5580,0x5eb5580,0x5eb5580,580,1572,2564,3372,4364,5172,6164,6972,7964,8772,9764,10756,11564,12556,13364,14341,15149,15942,16934,17460,18183,18183,18183,18183,21,20,0,0,0,0,0,0,0x47b500,0x9a3b00,0xecc100,0x12d4580,0x17fcb80,0x1c05000,0x212d600,0x2535a80,0x2a5e080,0x2e66500,0x338eb00,0x38b7100,0x3cbf580,0x41e7b80,0x45f0000,0x4b0e880,0x4f16d00,0x5315400,0x583da00,0x5ae5800,0x5eb6000,0x5eb6000,0x5eb6000,0x5eb6000,0x5eb6000,0x5eb6000,0x5eb6000,0x5eb6000,0x5eb6000,0x5eb6000,0x5eb6000,0x5eb6000,581,1573,2565,3373,4365,5173,6165,6973,7965,8773,9765,10757,11565,12557,13365,14342,15150,15943,16935,14152,14152,14152,14152,14152,20,19,0,0,0,0,0,0,0x47bf80,0x9a4580,0xeccb80,0x12d5000,0x17fd600,0x1c05a80,0x212e080,0x2536500,0x2a5eb00,0x2e66f80,0x338f580,0x38b7b80,0x3cc0000,0x41e8600,0x45f0a80,0x4b0f300,0x4f17780,0x5315e80,0x583e480,0x49bb400,0x49bb400,0x49bb400,0x49bb400,0x49bb400,0x49bb400,0x49bb400,0x49bb400,0x49bb400,0x49bb400,0x49bb400,0x49bb400,0x49bb400,582,1574,2566,3374,4366,5174,6166,6974,7966,8774,9766,10758,11566,12558,13366,14343,15151,16128,16936,14408,18184,18184,18184,18184,21,20,0,0,0,0,0,0,0x47ca00,0x9a5000,0xecd600,0x12d5a80,0x17fe080,0x1c06500,0x212eb00,0x2536f80,0x2a5f580,0x2e67a00,0x3390000,0x38b8600,0x3cc0a80,0x41e9080,0x45f1500,0x4b0fd80,0x4f18200,0x5436a80,0x583ef00,0x4b0a980,0x5eb6a80,0x5eb6a80,0x5eb6a80,0x5eb6a80,0x5eb6a80,0x5eb6a80,0x5eb6a80,0x5eb6a80,0x5eb6a80,0x5eb6a80,0x5eb6a80,0x5eb6a80,583,1575,2567,3375,4367,5175,6167,6975,7967,8775,9767,10759,11567,12559,13367,14344,15152,16129,16937,17461,18185,18185,18185,18185,21,20,0,0,0,0,0,0,0x47d480,0x9a5a80,0xece080,0x12d6500,0x17feb00,0x1c06f80,0x212f580,0x2537a00,0x2a60000,0x2e68480,0x3390a80,0x38b9080,0x3cc1500,0x41e9b00,0x45f1f80,0x4b10800,0x4f18c80,0x5437500,0x583f980,0x5ae6280,0x5eb7500,0x5eb7500,0x5eb7500,0x5eb7500,0x5eb7500,0x5eb7500,0x5eb7500,0x5eb7500,0x5eb7500,0x5eb7500,0x5eb7500,0x5eb7500,768,1576,2568,3376,4368,5176,6168,6976,7968,8960,9768,10760,11568,12560,13368,14345,15153,16130,16938,17462,18186,18186,18186,18186,21,20,0,0,0,0,0,0,0x59e080,0x9a6500,0xeceb00,0x12d6f80,0x17ff580,0x1c07a00,0x2130000,0x2538480,0x2a60a80,0x2f89080,0x3391500,0x38b9b00,0x3cc1f80,0x41ea580,0x45f2a00,0x4b11280,0x4f19700,0x5437f80,0x5840400,0x5ae6d00,0x5eb7f80,0x5eb7f80,0x5eb7f80,0x5eb7f80,0x5eb7f80,0x5eb7f80,0x5eb7f80,0x5eb7f80,0x5eb7f80,0x5eb7f80,0x5eb7f80,0x5eb7f80,769,1577,2569,3377,4369,5177,6169,6977,7969,8961,9769,10761,11569,12561,13369,14346,15154,16131,16939,14664,14664,14664,14664,14664,20,19,0,0,0,0,0,0,0x59eb00,0x9a6f80,0xecf580,0x12d7a00,0x1800000,0x1c08480,0x2130a80,0x2538f00,0x2a61500,0x2f89b00,0x3391f80,0x38ba580,0x3cc2a00,0x41eb000,0x45f3480,0x4b11d00,0x4f1a180,0x5438a00,0x5840e80,0x4c59f00,0x4c59f00,0x4c59f00,0x4c59f00,0x4c59f00,0x4c59f00,0x4c59f00,0x4c59f00,0x4c59f00,0x4c59f00,0x4c59f00,0x4c59f00,0x4c59f00,770,1578,2570,3378,4370,5178,6170,6978,7970,8962,9770,10762,11570,12562,13370,14347,15155,16132,16940,17463,18187,18243,18243,18243,22,21,0,0,0,0,0,0,0x59f580,0x9a7a00,0xed0000,0x12d8480,0x1800a80,0x1c08f00,0x2131500,0x2539980,0x2a61f80,0x2f8a580,0x3392a00,0x38bb000,0x3cc3480,0x41eba80,0x45f3f00,0x4b12780,0x4f1ac00,0x5439480,0x5841900,0x5ae7780,0x5eb8a00,0x5edd600,0x5edd600,0x5edd600,0x5edd600,0x5edd600,0x5edd600,0x5edd600,0x5edd600,0x5edd600,0x5edd600,0x5edd600,771,1579,2571,3379,4371,5179,6171,6979,7971,8963,9771,10763,11571,12563,13371,14348,15156,16133,16941,17464,18188,18188,18188,18188,21,20,0,0,0,0,0,0,0x5a0000,0x9a8480,0xed0a80,0x12d8f00,0x1801500,0x1c09980,0x2131f80,0x253a400,0x2a62a00,0x2f8b000,0x3393480,0x38bba80,0x3cc3f00,0x41ec500,0x45f4980,0x4b13200,0x4f1b680,0x5439f00,0x5842380,0x5ae8200,0x5eb9480,0x5eb9480,0x5eb9480,0x5eb9480,0x5eb9480,0x5eb9480,0x5eb9480,0x5eb9480,0x5eb9480,0x5eb9480,0x5eb9480,0x5eb9480,772,1580,2572,3380,4372,5180,6172,6980,7972,8964,9772,10764,11572,12564,13372,14349,15157,16134,16942,17465,18189,18189,18189,18189,21,20,0,0,0,0,0,0,0x5a0a80,0x9a8f00,0xed1500,0x12d9980,0x1801f80,0x1c0a400,0x2132a00,0x253ae80,0x2a63480,0x2f8ba80,0x3393f00,0x38bc500,0x3cc4980,0x41ecf80,0x45f5400,0x4b13c80,0x4f1c100,0x543a980,0x5842e00,0x5ae8c80,0x5eb9f00,0x5eb9f00,0x5eb9f00,0x5eb9f00,0x5eb9f00,0x5eb9f00,0x5eb9f00,0x5eb9f00,0x5eb9f00,0x5eb9f00,0x5eb9f00,0x5eb9f00,773,1581,2573,3381,4373,5181,6173,6981,7973,8965,9773,10765,11573,12565,13373,14350,15158,16135,16943,14920,14920,14920,14920,14920,20,19,0,0,0,0,0,0,0x5a1500,0x9a9980,0xed1f80,0x12da400,0x1802a00,0x1c0ae80,0x2133480,0x253b900,0x2a63f00,0x2f8c500,0x3394980,0x38bcf80,0x3cc5400,0x41eda00,0x45f5e80,0x4b14700,0x4f1cb80,0x543b400,0x5843880,0x4da9480,0x4da9480,0x4da9480,0x4da9480,0x4da9480,0x4da9480,0x4da9480,0x4da9480,0x4da9480,0x4da9480,0x4da9480,0x4da9480,0x4da9480,774,1582,2574,3382,4374,5182,6174,6982,7974,8966,9774,10766,11574,12566,13374,14351,15159,16136,16944,15176,18190,18190,18190,18190,21,20,0,0,0,0,0,0,0x5a1f80,0x9aa400,0xed2a00,0x12dae80,0x1803480,0x1c0b900,0x2133f00,0x253c380,0x2a64980,0x2f8cf80,0x3395400,0x38bda00,0x3cc5e80,0x41ee480,0x45f6900,0x4b15180,0x4f1d600,0x543be80,0x5844300,0x4ef8a00,0x5eba980,0x5eba980,0x5eba980,0x5eba980,0x5eba980,0x5eba980,0x5eba980,0x5eba980,0x5eba980,0x5eba980,0x5eba980,0x5eba980,775,1583,2575,3383,4375,5183,6175,6983,7975,8967,9775,10767,11575,12567,13375,14352,15160,16137,16945,17466,18191,18191,18191,18191,21,20,0,0,0,0,0,0,0x5a2a00,0x9aae80,0xed3480,0x12db900,0x1803f00,0x1c0c380,0x2134980,0x253ce00,0x2a65400,0x2f8da00,0x3395e80,0x38be480,0x3cc6900,0x41eef00,0x45f7380,0x4b15c00,0x4f1e080,0x543c900,0x5844d80,0x5ae9700,0x5ebb400,0x5ebb400,0x5ebb400,0x5ebb400,0x5ebb400,0x5ebb400,0x5ebb400,0x5ebb400,0x5ebb400,0x5ebb400,0x5ebb400,0x5ebb400,776,1584,2576,3384,4376,5184,6176,7168,7976,8968,9776,10768,11576,12568,13376,14353,15161,16138,16946,17467,18192,18192,18192,18192,21,20,0,0,0,0,0,0,0x5a3480,0x9ab900,0xed3f00,0x12dc380,0x1804980,0x1c0ce00,0x2135400,0x265da00,0x2a65e80,0x2f8e480,0x3396900,0x38bef00,0x3cc7380,0x41ef980,0x45f7e00,0x4b16680,0x4f1eb00,0x543d380,0x5845800,0x5aea180,0x5ebbe80,0x5ebbe80,0x5ebbe80,0x5ebbe80,0x5ebbe80,0x5ebbe80,0x5ebbe80,0x5ebbe80,0x5ebbe80,0x5ebbe80,0x5ebbe80,0x5ebbe80,777,1585,2577,3385,4377,5185,6177,7169,7977,8969,9777,10769,11577,12569,13377,14354,15162,16139,16947,15432,15432,15432,15432,15432,20,19,0,0,0,0,0,0,0x5a3f00,0x9ac380,0xed4980,0x12dce00,0x1805400,0x1c0d880,0x2135e80,0x265e480,0x2a66900,0x2f8ef00,0x3397380,0x38bf980,0x3cc7e00,0x41f0400,0x45f8880,0x4b17100,0x4f1f580,0x543de00,0x5846280,0x5047f80,0x5047f80,0x5047f80,0x5047f80,0x5047f80,0x5047f80,0x5047f80,0x5047f80,0x5047f80,0x5047f80,0x5047f80,0x5047f80,0x5047f80,778,1586,2578,3386,4378,5186,6178,7170,7978,8970,9778,10770,11578,12570,13378,14355,15163,16140,16948,17468,18193,18244,18244,18244,22,21,0,0,0,0,0,0,0x5a4980,0x9ace00,0xed5400,0x12dd880,0x1805e80,0x1c0e300,0x2136900,0x265ef00,0x2a67380,0x2f8f980,0x3397e00,0x38c0400,0x3cc8880,0x41f0e80,0x45f9300,0x4b17b80,0x4f20000,0x543e880,0x5846d00,0x5aeac00,0x5ebc900,0x5ede080,0x5ede080,0x5ede080,0x5ede080,0x5ede080,0x5ede080,0x5ede080,0x5ede080,0x5ede080,0x5ede080,0x5ede080,779,1587,2579,3387,4379,5187,6179,7171,7979,8971,9779,10771,11579,12571,13379,14356,15164,16141,16949,17469,18194,18194,18194,18194,21,20,0,0,0,0,0,0,0x5a5400,0x9ad880,0xed5e80,0x12de300,0x1806900,0x1c0ed80,0x2137380,0x265f980,0x2a67e00,0x2f90400,0x3398880,0x38c0e80,0x3cc9300,0x41f1900,0x45f9d80,0x4b18600,0x4f20a80,0x543f300,0x5847780,0x5aeb680,0x5ebd380,0x5ebd380,0x5ebd380,0x5ebd380,0x5ebd380,0x5ebd380,0x5ebd380,0x5ebd380,0x5ebd380,0x5ebd380,0x5ebd380,0x5ebd380,780,1588,2580,3388,4380,5188,6180,7172,7980,8972,9780,10772,11580,12572,13380,14357,15165,16142,16950,17470,18195,18195,18195,18195,21,20,0,0,0,0,0,0,0x5a5e80,0x9ae300,0xed6900,0x12ded80,0x1807380,0x1c0f800,0x2137e00,0x2660400,0x2a68880,0x2f90e80,0x3399300,0x38c1900,0x3cc9d80,0x41f2380,0x45fa800,0x4b19080,0x4f21500,0x543fd80,0x5848200,0x5aec100,0x5ebde00,0x5ebde00,0x5ebde00,0x5ebde00,0x5ebde00,0x5ebde00,0x5ebde00,0x5ebde00,0x5ebde00,0x5ebde00,0x5ebde00,0x5ebde00,781,1589,2581,3389,4381,5189,6181,7173,7981,8973,9781,10773,11581,12573,13381,14358,15166,16143,16951,15688,15688,15688,15688,15688,20,19,0,0,0,0,0,0,0x5a6900,0x9aed80,0xed7380,0x12df800,0x1807e00,0x1c10280,0x2138880,0x2660e80,0x2a69300,0x2f91900,0x3399d80,0x38c2380,0x3cca800,0x41f2e00,0x45fb280,0x4b19b00,0x4f21f80,0x5440800,0x5848c80,0x5197500,0x5197500,0x5197500,0x5197500,0x5197500,0x5197500,0x5197500,0x5197500,0x5197500,0x5197500,0x5197500,0x5197500,0x5197500,782,1590,2582,3390,4382,5190,6182,7174,7982,8974,9782,10774,11582,12574,13382,14359,15167,16144,16952,15944,18196,18196,18196,18196,21,20,0,0,0,0,0,0,0x5a7380,0x9af800,0xed7e00,0x12e0280,0x1808880,0x1c10d00,0x2139300,0x2661900,0x2a69d80,0x2f92380,0x339a800,0x38c2e00,0x3ccb280,0x41f3880,0x45fbd00,0x4b1a580,0x4f22a00,0x5441280,0x5849700,0x52e6a80,0x5ebe880,0x5ebe880,0x5ebe880,0x5ebe880,0x5ebe880,0x5ebe880,0x5ebe880,0x5ebe880,0x5ebe880,0x5ebe880,0x5ebe880,0x5ebe880,783,1591,2583,3391,4383,5191,6183,7175,7983,8975,9783,10775,11583,12575,13383,14360,15168,16145,16953,17471,18197,18197,18197,18197,21,20,0,0,0,0,0,0,0x5a7e00,0x9b0280,0xed8880,0x12e0d00,0x1809300,0x1c11780,0x2139d80,0x2662380,0x2a6a800,0x2f92e00,0x339b280,0x38c3880,0x3ccbd00,0x41f4300,0x45fc780,0x4b1b000,0x4f23480,0x5441d00,0x584a180,0x5aecb80,0x5ebf300,0x5ebf300,0x5ebf300,0x5ebf300,0x5ebf300,0x5ebf300,0x5ebf300,0x5ebf300,0x5ebf300,0x5ebf300,0x5ebf300,0x5ebf300,784,1592,2584,3392,4384,5376,6184,7176,7984,8976,9784,10776,11584,12576,13568,14361,15169,16146,16954,17472,18198,18198,18198,18198,21,20,0,0,0,0,0,0,0x5a8880,0x9b0d00,0xed9300,0x12e1780,0x1809d80,0x1d32380,0x213a800,0x2662e00,0x2a6b280,0x2f93880,0x339bd00,0x38c4300,0x3ccc780,0x41f4d80,0x471d380,0x4b1ba80,0x4f23f00,0x5442780,0x584ac00,0x5aed600,0x5ebfd80,0x5ebfd80,0x5ebfd80,0x5ebfd80,0x5ebfd80,0x5ebfd80,0x5ebfd80,0x5ebfd80,0x5ebfd80,0x5ebfd80,0x5ebfd80,0x5ebfd80,785,1593,2585,3393,4385,5377,6185,7177,7985,8977,9785,10777,11585,12577,13569,14362,15170,16147,16955,16200,16200,16200,16200,16200,20,19,0,0,0,0,0,0,0x5a9300,0x9b1780,0xed9d80,0x12e2200,0x180a800,0x1d32e00,0x213b280,0x2663880,0x2a6bd00,0x2f94300,0x339c780,0x38c4d80,0x3ccd200,0x41f5800,0x471de00,0x4b1c500,0x4f24980,0x5443200,0x584b680,0x5436000,0x5436000,0x5436000,0x5436000,0x5436000,0x5436000,0x5436000,0x5436000,0x5436000,0x5436000,0x5436000,0x5436000,0x5436000,786,1594,2586,3394,4386,5378,6186,7178,7986,8978,9786,10778,11586,12578,13570,14363,15171,16148,16956,17473,18199,18245,18245,18245,22,21,0,0,0,0,0,0,0x5a9d80,0x9b2200,0xeda800,0x12e2c80,0x180b280,0x1d33880,0x213bd00,0x2664300,0x2a6c780,0x2f94d80,0x339d200,0x38c5800,0x3ccdc80,0x41f6280,0x471e880,0x4b1cf80,0x4f25400,0x5443c80,0x584c100,0x5aee080,0x5ec0800,0x5edeb00,0x5edeb00,0x5edeb00,0x5edeb00,0x5edeb00,0x5edeb00,0x5edeb00,0x5edeb00,0x5edeb00,0x5edeb00,0x5edeb00,787,1595,2587,3395,4387,5379,6187,7179,7987,8979,9787,10779,11587,12579,13571,14364,15172,16149,16957,17474,18200,18200,18200,18200,21,20,0,0,0,0,0,0,0x5aa800,0x9b2c80,0xedb280,0x12e3700,0x180bd00,0x1d34300,0x213c780,0x2664d80,0x2a6d200,0x2f95800,0x339dc80,0x38c6280,0x3cce700,0x41f6d00,0x471f300,0x4b1da00,0x4f25e80,0x5444700,0x584cb80,0x5aeeb00,0x5ec1280,0x5ec1280,0x5ec1280,0x5ec1280,0x5ec1280,0x5ec1280,0x5ec1280,0x5ec1280,0x5ec1280,0x5ec1280,0x5ec1280,0x5ec1280,788,1596,2588,3396,4388,5380,6188,7180,7988,8980,9788,10780,11588,12580,13572,14365,15173,16150,16958,17475,18201,18201,18201,18201,21,20,0,0,0,0,0,0,0x5ab280,0x9b3700,0xedbd00,0x12e4180,0x180c780,0x1d34d80,0x213d200,0x2665800,0x2a6dc80,0x2f96280,0x339e700,0x38c6d00,0x3ccf180,0x41f7780,0x471fd80,0x4b1e480,0x4f26900,0x5445180,0x584d600,0x5aef580,0x5ec1d00,0x5ec1d00,0x5ec1d00,0x5ec1d00,0x5ec1d00,0x5ec1d00,0x5ec1d00,0x5ec1d00,0x5ec1d00,0x5ec1d00,0x5ec1d00,0x5ec1d00,789,1597,2589,3397,4389,5381,6189,7181,7989,8981,9789,10781,11589,12581,13573,14366,15174,16151,16959,16456,16456,16456,16456,16456,20,19,0,0,0,0,0,0,0x5abd00,0x9b4180,0xedc780,0x12e4c00,0x180d200,0x1d35800,0x213dc80,0x2666280,0x2a6e700,0x2f96d00,0x339f180,0x38c7780,0x3ccfc00,0x41f8200,0x4720800,0x4b1ef00,0x4f27380,0x5445c00,0x584e080,0x5585580,0x5585580,0x5585580,0x5585580,0x5585580,0x5585580,0x5585580,0x5585580,0x5585580,0x5585580,0x5585580,0x5585580,0x5585580,790,1598,2590,3398,4390,5382,6190,7182,7990,8982,9790,10782,11590,12582,13574,14367,15175,16152,16960,16712,18202,18202,18202,18202,21,20,0,0,0,0,0,0,0x5ac780,0x9b4c00,0xedd200,0x12e5680,0x180dc80,0x1d36280,0x213e700,0x2666d00,0x2a6f180,0x2f97780,0x339fc00,0x38c8200,0x3cd0680,0x41f8c80,0x4721280,0x4b1f980,0x4f27e00,0x5446680,0x584eb00,0x56d4b00,0x5ec2780,0x5ec2780,0x5ec2780,0x5ec2780,0x5ec2780,0x5ec2780,0x5ec2780,0x5ec2780,0x5ec2780,0x5ec2780,0x5ec2780,0x5ec2780,791,1599,2591,3399,4391,5383,6191,7183,7991,8983,9791,10783,11591,12583,13575,14368,15360,16153,16961,17476,18203,18203,18203,18203,21,20,0,0,0,0,0,0,0x5ad200,0x9b5680,0xeddc80,0x12e6100,0x180e700,0x1d36d00,0x213f180,0x2667780,0x2a6fc00,0x2f98200,0x33a0680,0x38c8c80,0x3cd1100,0x41f9700,0x4721d00,0x4b20400,0x5048a00,0x5447100,0x584f580,0x5af0000,0x5ec3200,0x5ec3200,0x5ec3200,0x5ec3200,0x5ec3200,0x5ec3200,0x5ec3200,0x5ec3200,0x5ec3200,0x5ec3200,0x5ec3200,0x5ec3200,792,1600,2592,3584,4392,5384,6192,7184,7992,8984,9792,10784,11776,12584,13576,14369,15361,16154,16962,17477,18204,18204,18204,18204,21,20,0,0,0,0,0,0,0x5adc80,0x9b6100,0xede700,0x1406d00,0x180f180,0x1d37780,0x213fc00,0x2668200,0x2a70680,0x2f98c80,0x33a1100,0x38c9700,0x3df1d00,0x41fa180,0x4722780,0x4b20e80,0x5049480,0x5447b80,0x5850000,0x5af0a80,0x5ec3c80,0x5ec3c80,0x5ec3c80,0x5ec3c80,0x5ec3c80,0x5ec3c80,0x5ec3c80,0x5ec3c80,0x5ec3c80,0x5ec3c80,0x5ec3c80,0x5ec3c80,793,1601,2593,3585,4393,5385,6193,7185,7993,8985,9793,10785,11777,12585,13577,14370,15362,16155,16963,16968,16968,16968,16968,16968,20,19,0,0,0,0,0,0,0x5ae700,0x9b6b80,0xedf180,0x1407780,0x180fc00,0x1d38200,0x2140680,0x2668c80,0x2a71100,0x2f99700,0x33a1b80,0x38ca180,0x3df2780,0x41fac00,0x4723200,0x4b21900,0x5049f00,0x5448600,0x5850a80,0x5824080,0x5824080,0x5824080,0x5824080,0x5824080,0x5824080,0x5824080,0x5824080,0x5824080,0x5824080,0x5824080,0x5824080,0x5824080,794,1602,2594,3586,4394,5386,6194,7186,7994,8986,9794,10786,11778,12586,13578,14371,15363,16156,16964,17478,18205,18246,18246,18246,22,21,0,0,0,0,0,0,0x5af180,0x9b7600,0xedfc00,0x1408200,0x1810680,0x1d38c80,0x2141100,0x2669700,0x2a71b80,0x2f9a180,0x33a2600,0x38cac00,0x3df3200,0x41fb680,0x4723c80,0x4b22380,0x504a980,0x5449080,0x5851500,0x5af1500,0x5ec4700,0x5edf580,0x5edf580,0x5edf580,0x5edf580,0x5edf580,0x5edf580,0x5edf580,0x5edf580,0x5edf580,0x5edf580,0x5edf580,795,1603,2595,3587,4395,5387,6195,7187,7995,8987,9795,10787,11779,12587,13579,14372,15364,16157,16965,17479,18206,18206,18206,18206,21,20,0,0,0,0,0,0,0x5afc00,0x9b8080,0xee0680,0x1408c80,0x1811100,0x1d39700,0x2141b80,0x266a180,0x2a72600,0x2f9ac00,0x33a3080,0x38cb680,0x3df3c80,0x41fc100,0x4724700,0x4b22e00,0x504b400,0x5449b00,0x5851f80,0x5af1f80,0x5ec5180,0x5ec5180,0x5ec5180,0x5ec5180,0x5ec5180,0x5ec5180,0x5ec5180,0x5ec5180,0x5ec5180,0x5ec5180,0x5ec5180,0x5ec5180,796,1604,2596,3588,4396,5388,6196,7188,7996,8988,9796,10788,11780,12588,13580,14373,15365,16158,16966,17664,18207,18207,18207,18207,21,20,0,0,0,0,0,0,0x5b0680,0x9b8b00,0xee1100,0x1409700,0x1811b80,0x1d3a180,0x2142600,0x266ac00,0x2a73080,0x2f9b680,0x33a3b00,0x38cc100,0x3df4700,0x41fcb80,0x4725180,0x4b23880,0x504be80,0x544a580,0x5852a00,0x5c12b80,0x5ec5c00,0x5ec5c00,0x5ec5c00,0x5ec5c00,0x5ec5c00,0x5ec5c00,0x5ec5c00,0x5ec5c00,0x5ec5c00,0x5ec5c00,0x5ec5c00,0x5ec5c00,797,1605,2597,3589,4397,5389,6197,7189,7997,8989,9797,10789,11781,12589,13581,14374,15366,16159,16967,17224,17224,17224,17224,17224,20,19,0,0,0,0,0,0,0x5b1100,0x9b9580,0xee1b80,0x140a180,0x1812600,0x1d3ac00,0x2143080,0x266b680,0x2a73b00,0x2f9c100,0x33a4580,0x38ccb80,0x3df5180,0x41fd600,0x4725c00,0x4b24300,0x504c900,0x544b000,0x5853480,0x5973600,0x5973600,0x5973600,0x5973600,0x5973600,0x5973600,0x5973600,0x5973600,0x5973600,0x5973600,0x5973600,0x5973600,0x5973600,798,1606,2598,3590,4398,5390,6198,7190,7998,8990,9798,10790,11782,12590,13582,14375,15367,16160,17152,17480,18208,18208,18208,18208,21,20,0,0,0,0,0,0,0x5b1b80,0x9ba000,0xee2600,0x140ac00,0x1813080,0x1d3b680,0x2143b00,0x266c100,0x2a74580,0x2f9cb80,0x33a5000,0x38cd600,0x3df5c00,0x41fe080,0x4726680,0x4b24d80,0x504d380,0x544ba80,0x5974080,0x5ac2b80,0x5ec6680,0x5ec6680,0x5ec6680,0x5ec6680,0x5ec6680,0x5ec6680,0x5ec6680,0x5ec6680,0x5ec6680,0x5ec6680,0x5ec6680,0x5ec6680,799,1607,2599,3591,4399,5391,6199,7191,7999,8991,9799,10791,11783,12591,13583,14376,15368,16161,17153,17665,18209,18209,18209,18209,21,20,0,0,0,0,0,0,0x5b2600,0x9baa80,0xee3080,0x140b680,0x1813b00,0x1d3c100,0x2144580,0x266cb80,0x2a75000,0x2f9d600,0x33a5a80,0x38ce080,0x3df6680,0x41feb00,0x4727100,0x4b25800,0x504de00,0x544c500,0x5974b00,0x5c13600,0x5ec7100,0x5ec7100,0x5ec7100,0x5ec7100,0x5ec7100,0x5ec7100,0x5ec7100,0x5ec7100,0x5ec7100,0x5ec7100,0x5ec7100,0x5ec7100,800,1792,2600,3592,4400,5392,6200,7192,8000,8992,9984,10792,11784,12592,13584,14377,15369,16162,17154,17666,18210,18210,18210,18210,21,20,0,0,0,0,0,0,0x5b3080,0xadb680,0xee3b00,0x140c100,0x1814580,0x1d3cb80,0x2145000,0x266d600,0x2a75a80,0x2f9e080,0x34c6680,0x38ceb00,0x3df7100,0x41ff580,0x4727b80,0x4b26280,0x504e880,0x544cf80,0x5975580,0x5c14080,0x5ec7b80,0x5ec7b80,0x5ec7b80,0x5ec7b80,0x5ec7b80,0x5ec7b80,0x5ec7b80,0x5ec7b80,0x5ec7b80,0x5ec7b80,0x5ec7b80,0x5ec7b80,801,1793,2601,3593,4401,5393,6201,7193,8001,8993,9985,10793,11785,12593,13585,14378,15370,16163,17155,17736,17736,17736,17736,17736,20,19,0,0,0,0,0,0,0x5b3b00,0xadc100,0xee4580,0x140cb80,0x1815000,0x1d3d600,0x2145a80,0x266e080,0x2a76500,0x2f9eb00,0x34c7100,0x38cf580,0x3df7b80,0x4200000,0x4728600,0x4b26d00,0x504f300,0x544da00,0x5976000,0x5c12100,0x5c12100,0x5c12100,0x5c12100,0x5c12100,0x5c12100,0x5c12100,0x5c12100,0x5c12100,0x5c12100,0x5c12100,0x5c12100,0x5c12100,802,1794,2602,3594,4402,5394,6202,7194,8002,8994,9986,10794,11786,12594,13586,14379,15371,16164,17156,17667,18211,18247,18247,18247,22,21,0,0,0,0,0,0,0x5b4580,0xadcb80,0xee5000,0x140d600,0x1815a80,0x1d3e080,0x2146500,0x266eb00,0x2a76f80,0x2f9f580,0x34c7b80,0x38d0000,0x3df8600,0x4200a80,0x4729080,0x4b27780,0x504fd80,0x544e480,0x5976a80,0x5c14b00,0x5ec8600,0x5ee0000,0x5ee0000,0x5ee0000,0x5ee0000,0x5ee0000,0x5ee0000,0x5ee0000,0x5ee0000,0x5ee0000,0x5ee0000,0x5ee0000,803,1795,2603,3595,4403,5395,6203,7195,8003,8995,9987,10795,11787,12595,13587,14380,15372,16165,17157,17668,18212,18212,18212,18212,21,20,0,0,0,0,0,0,0x5b5000,0xadd600,0xee5a80,0x140e080,0x1816500,0x1d3eb00,0x2146f80,0x266f580,0x2a77a00,0x2fa0000,0x34c8600,0x38d0a80,0x3df9080,0x4201500,0x4729b00,0x4b28200,0x5050800,0x544ef00,0x5977500,0x5c15580,0x5ec9080,0x5ec9080,0x5ec9080,0x5ec9080,0x5ec9080,0x5ec9080,0x5ec9080,0x5ec9080,0x5ec9080,0x5ec9080,0x5ec9080,0x5ec9080,804,1796,2604,3596,4404,5396,6204,7196,8004,8996,9988,10796,11788,12596,13588,14381,15373,16166,17158,17669,18213,18213,18213,18213,21,20,0,0,0,0,0,0,0x5b5a80,0xade080,0xee6500,0x140eb00,0x1816f80,0x1d3f580,0x2147a00,0x2670000,0x2a78480,0x2fa0a80,0x34c9080,0x38d1500,0x3df9b00,0x4201f80,0x472a580,0x4b28c80,0x5051280,0x544f980,0x5977f80,0x5c16000,0x5ec9b00,0x5ec9b00,0x5ec9b00,0x5ec9b00,0x5ec9b00,0x5ec9b00,0x5ec9b00,0x5ec9b00,0x5ec9b00,0x5ec9b00,0x5ec9b00,0x5ec9b00,805,1797,2605,3597,4405,5397,6205,7197,8005,8997,9989,10797,11789,12597,13589,14382,15374,16167,17159,17992,17992,17992,17992,17992,20,19,0,0,0,0,0,0,0x5b6500,0xadeb00,0xee6f80,0x140f580,0x1817a00,0x1d40000,0x2148480,0x2670a80,0x2a78f00,0x2fa1500,0x34c9b00,0x38d1f80,0x3dfa580,0x4202a00,0x472b000,0x4b29700,0x5051d00,0x5450400,0x5978a00,0x5d61680,0x5d61680,0x5d61680,0x5d61680,0x5d61680,0x5d61680,0x5d61680,0x5d61680,0x5d61680,0x5d61680,0x5d61680,0x5d61680,0x5d61680,806,1798,2606,3598,4406,5398,6206,7198,8006,8998,9990,10798,11790,12598,13590,14383,15375,16168,17160,18248,18214,18214,18214,18214,21,20,0,0,0,0,0,0,0x5b6f80,0xadf580,0xee7a00,0x1410000,0x1818480,0x1d40a80,0x2148f00,0x2671500,0x2a79980,0x2fa1f80,0x34ca580,0x38d2a00,0x3dfb000,0x4203480,0x472ba80,0x4b2a180,0x5052780,0x5450e80,0x5979480,0x5eb0c00,0x5eca580,0x5eca580,0x5eca580,0x5eca580,0x5eca580,0x5eca580,0x5eca580,0x5eca580,0x5eca580,0x5eca580,0x5eca580,0x5eca580,807,1799,2607,3599,4407,5399,6207,7199,8007,8999,9991,10799,11791,12599,13591,14384,15376,16169,17161,17670,18215,18215,18215,18215,21,20,0,0,0,0,0,0,0x5b7a00,0xae0000,0xee8480,0x1410a80,0x1818f00,0x1d41500,0x2149980,0x2671f80,0x2a7a400,0x2fa2a00,0x34cb000,0x38d3480,0x3dfba80,0x4203f00,0x472c500,0x4b2ac00,0x5053200,0x5451900,0x5979f00,0x5c16a80,0x5ecb000,0x5ecb000,0x5ecb000,0x5ecb000,0x5ecb000,0x5ecb000,0x5ecb000,0x5ecb000,0x5ecb000,0x5ecb000,0x5ecb000,0x5ecb000};

__global__ __launch_bounds__(256, 1) void swin_main(const uint16_t* x, const uint16_t* mask, char* ws, uint16_t* out,
                                                    const uint16_t* wqkv, const uint16_t* wo, const uint16_t* ls,
                                                    const uint16_t* w1, const uint16_t* w2, const uint16_t* rc,
                                                    const int* ridx) {
  __shared__ __attribute__((aligned(16))) char smem[LDS_BYTES];
  const int wave = __builtin_amdgcn_readfirstlane(threadIdx.x >> 6);
  const unsigned lds0 = (unsigned)(size_t)smem;
  asm volatile(
      ".p2align 5\n"
      "s_load_dwordx8 s[16:23], %0, 0\n"
      "s_load_dwordx4 s[4:7], %0, 32\n"
      "s_load_dwordx8 s[80:87], %0, 48\n"
      "s_load_dwordx2 s[90:91], %0, 80\n"
      "s_mov_b32 s24, %1\n"
      "s_mov_b32 s25, %2\n"
      "s_mov_b64 s[60:61], %3\n"
      "s_getpc_b64 s[12:13]\n"
      "L_ipf0_%=:\n"
      "v_mbcnt_lo_u32_b32 v136, -1, 0\n"
      "v_mbcnt_hi_u32_b32 v136, -1, v136\n"
      "s_lshr_b32 s28, s25, 3\n"
      "s_lshl_b32 s29, s28, 2\n"
      "s_add_u32 s29, s29, s24\n"
      "s_mul_i32 s29, s29, 14\n"
      "v_min_u32_e64 v137, 13, v136\n"
      "v_add_lshl_u32 v137, v137, s29, 7\n"
      "v_min_u32 v137, 221056, v137\n"
      "v_lshlrev_b32_e64 v138, 4, v136\n"
      "s_lshl_b32 s29, s24, 6\n"
      "s_add_u32 s54, s60, s29\n"
      "s_addc_u32 s55, s61, 0\n"
      "s_and_b32 s29, s29, 64\n"
      "v_xor_b32_e64 v139, s29, v138\n"
      "s_mul_i32 s30, s24, 9216\n"
      "v_add_u32_e64 v139, s30, v139\n"
      "s_add_u32 s2, s30, 36864\n"
      "v_xor_b32 v140, 32, v139\n"
      "s_mov_b64 s[34:35], 0xffff\n"
      "v_cndmask_b32 v141, v140, v139, s[34:35]\n"
      "s_lshl_b64 s[34:35], s[34:35], 48\n"
      "v_cndmask_b32 v142, v139, v140, s[34:35]\n"
      "s_lshl_b64 s[34:35], -1, 32\n"
      "v_cndmask_b32 v143, v140, v139, s[34:35]\n"
      "s_waitcnt lgkmcnt(0)\n"
      "global_load_dword v31, v137, s[4:5]\n"
      "s_add_u32 m0, s2, 0\n"
      "s_nop 0\n"
      "global_load_lds_dwordx4 v139, s[4:5]\n"
      "global_load_lds_dwordx4 v139, s[4:5] offset:1024\n"
      "global_load_lds_dwordx4 v141, s[4:5] offset:2048\n"
      "global_load_lds_dwordx4 v140, s[4:5] offset:3072\n"
      "s_add_u32 s4, s4, 4096\n"
      "s_addc_u32 s5, s5, 0\n"
      "s_add_u32 m0, s2, 4096\n"
      "s_nop 0\n"
      "global_load_lds_dwordx4 v143, s[4:5]\n"
      "global_load_lds_dwordx4 v139, s[4:5] offset:1024\n"
      "global_load_lds_dwordx4 v142, s[4:5] offset:2048\n"
      "global_load_lds_dwordx4 v140, s[4:5] offset:3072\n"
      "s_add_u32 s4, s4, 4096\n"
      "s_addc_u32 s5, s5, 0\n"
      "s_add_u32 m0, s2, 8192\n"
      "s_nop 0\n"
      "global_load_lds_dwordx4 v140, s[4:5]\n"
      "s_add_u32 s4, s4, 28672\n"
      "s_addc_u32 s5, s5, 0\n"
      "s_load_dwordx16 s[36:51], s[82:83], 0\n"
      "s_load_dwordx2 s[8:9], s[80:81], 0\n"
      "s_load_dwordx16 s[64:79], s[84:85], 0\n"
      "s_lshl_b32 s29, s24, 8\n"
      "v_lshl_add_u32 v144, v136, 2, s29\n"
      "v_min_u32 v144, 672, v144\n"
      "v_add_u32 v145, 150640, v144\n"
      "s_lshl_b32 s29, s28, 2\n"
      "s_add_u32 s29, s29, s24\n"
      "s_mov_b64 s[34:35], s[16:17]\n"
      "s_mov_b32 s30, s29\n"
      "s_sub_u32 s31, s29, 48\n"
      "s_cmp_ge_u32 s29, 48\n"
      "s_cselect_b64 s[34:35], s[22:23], s[34:35]\n"
      "s_cselect_b32 s30, s31, s30\n"
      "s_sub_u32 s31, s29, 96\n"
      "s_cmp_ge_u32 s29, 96\n"
      "s_cselect_b64 s[34:35], s[18:19], s[34:35]\n"
      "s_cselect_b32 s30, s31, s30\n"
      "s_sub_u32 s31, s29, 54\n"
      "s_cmp_ge_u32 s29, 109\n"
      "s_cselect_b64 s[34:35], s[20:21], s[34:35]\n"
      "s_cselect_b32 s30, s31, s30\n"
      "s_lshl_b32 s30, s30, 21\n"
      "s_load_dword s100, s[34:35], s30\n"
      "s_and_b32 s29, s29, 7\n"
      "s_cmp_eq_u32 s29, 0\n"
      "s_cselect_b64 s[34:35], s[6:7], s[34:35]\n"
      "s_cmp_eq_u32 s29, 1\n"
      "s_cselect_b64 s[34:35], s[90:91], s[34:35]\n"
      "s_cmp_eq_u32 s29, 2\n"
      "s_cselect_b64 s[34:35], s[86:87], s[34:35]\n"
      "s_cmp_eq_u32 s29, 3\n"
      "s_cselect_b64 s[34:35], s[60:61], s[34:35]\n"
      "s_cmp_eq_u32 s29, 4\n"
      "s_cselect_b64 s[34:35], s[12:13], s[34:35]\n"
      "s_load_dword s100, s[34:35], 0\n"
      "s_add_u32 m0, s2, 36864\n"
      "s_nop 0\n"
      "global_load_lds_dwordx4 v139, s[4:5]\n"
      "global_load_lds_dwordx4 v139, s[4:5] offset:1024\n"
      "global_load_lds_dwordx4 v141, s[4:5] offset:2048\n"
      "global_load_lds_dwordx4 v140, s[4:5] offset:3072\n"
      "s_add_u32 s4, s4, 4096\n"
      "s_addc_u32 s5, s5, 0\n"
      "s_add_u32 m0, s2, 40960\n"
      "s_nop 0\n"
      "global_load_lds_dwordx4 v143, s[4:5]\n"
      "global_load_lds_dwordx4 v139, s[4:5] offset:1024\n"
      "global_load_lds_dwordx4 v142, s[4:5] offset:2048\n"
      "global_load_lds_dwordx4 v140, s[4:5] offset:3072\n"
      "s_add_u32 s4, s4, 4096\n"
      "s_addc_u32 s5, s5, 0\n"
      "s_add_u32 m0, s2, 45056\n"
      "s_nop 0\n"
      "global_load_lds_dwordx4 v140, s[4:5]\n"
      "s_add_u32 s4, s4, 32768\n"
      "s_addc_u32 s5, s5, 0\n"
      "v_add_u32 v146, 4096, v140\n"
      "v_add_u32 v147, s2, v138\n"
      "v_lshrrev_b32 v148, 2, v136\n"
      "global_load_dwordx4 a[72:75], v139, s[4:5] offset:-4096\n"
      "v_and_b32 v149, 3, v148\n"
      "v_lshrrev_b32_e64 v150, 4, v136\n"
      "global_load_dwordx4 a[76:79], v139, s[4:5] offset:-3072\n"
      "v_lshl_add_u32 v149, v150, 3, v149\n"
      "v_mul_u32_u24 v149, 1152, v149\n"
      "global_load_dwordx4 a[80:83], v141, s[4:5] offset:-2048\n"
      "v_and_b32_e64 v150, 1, v136\n"
      "v_lshl_add_u32 v149, v150, 3, v149\n"
      "global_load_dwordx4 a[84:87], v140, s[4:5] offset:-1024\n"
      "v_add_u32 v149, 36864, v149\n"
      "v_and_b32_e64 v148, 6, v148\n"
      "global_load_dwordx4 a[88:91], v143, s[4:5]\n"
      "v_bfe_u32 v150, v136, 1, 1\n"
      "s_mul_i32 s29, s24, 6\n"
      "global_load_dwordx4 a[92:95], v139, s[4:5] offset:1024\n"
      "v_add_u32 v150, s29, v150\n"
      "v_xor_b32_e64 v151, v150, v148\n"
      "global_load_dwordx4 a[96:99], v142, s[4:5] offset:2048\n"
      "v_lshl_add_u32 v151, v151, 4, v149\n"
      "v_add_u32_e64 v150, 2, v150\n"
      "v_xor_b32_e64 v152, v150, v148\n"
      "global_load_dwordx4 a[100:103], v140, s[4:5] offset:3072\n"
      "v_lshl_add_u32 v152, v152, 4, v149\n"
      "v_add_u32_e64 v150, 2, v150\n"
      "global_load_dwordx4 a[104:107], v146, s[4:5]\n"
      "v_xor_b32_e64 v153, v150, v148\n"
      "v_lshl_add_u32 v153, v153, 4, v149\n"
      "s_movk_i32 s29, 18\n"
      "s_mov_b32 s30, 73600\n"
      "s_cmp_eq_u32 s24, 1\n"
      "v_add_u32 v139, 36864, v139\n"
      "v_add_u32 v140, 36864, v140\n"
      "v_add_u32 v141, 36864, v141\n"
      "v_add_u32 v143, 36864, v143\n"
      "v_add_u32 v142, 36864, v142\n"
      "v_add_u32 v146, 36864, v146\n"
      "global_load_dwordx4 a[108:111], v139, s[4:5] offset:-4096\n"
      "s_cselect_b64 s[12:13], s[6:7], s[12:13]\n"
      "s_cmp_eq_u32 s24, 2\n"
      "s_cselect_b32 s29, 16, s29\n"
      "s_cselect_b32 s30, L_cend_%=-L_ipf0_%=-128, s30\n"
      "s_cmp_eq_u32 s24, 3\n"
      "global_load_dwordx4 a[112:115], v139, s[4:5] offset:-3072\n"
      "s_cselect_b64 s[12:13], s[60:61], s[12:13]\n"
      "s_cselect_b32 s29, 10, s29\n"
      "s_cselect_b32 s30, 40832, s30\n"
      "global_load_dwordx4 a[116:119], v141, s[4:5] offset:-2048\n"
      "s_cmp_eq_u32 s24, 0\n"
      "s_cselect_b64 s[12:13], s[90:91], s[12:13]\n"
      "global_load_dwordx4 a[120:123], v140, s[4:5] offset:-1024\n"
      "s_cselect_b32 s29, 3, s29\n"
      "s_cselect_b32 s30, 9476, s30\n"
      "global_load_dwordx4 a[124:127], v143, s[4:5]\n"
      "s_mul_i32 s28, s28, s29\n"
      "s_sub_u32 s29, s29, 1\n"
      "global_load_dwordx4 a[128:131], v139, s[4:5] offset:1024\n"
      "v_min_u32 v137, s29, v136\n"
      "v_add_lshl_u32 v137, v137, s28, 7\n"
      "global_load_dwordx4 a[132:135], v142, s[4:5] offset:2048\n"
      "v_min_u32_e64 v137, s30, v137\n"
      "s_mul_i32 s28, s24, 10240\n"
      "global_load_dwordx4 a[136:139], v140, s[4:5] offset:3072\n"
      "s_add_u32 s28, s28, 4096\n"
      "v_add_u32 v154, s28, v138\n"
      "global_load_dwordx4 a[140:143], v146, s[4:5]\n"
      "v_add_u32 v155, 8192, v154\n"
      "global_load_dword v144, v144, s[86:87]\n"
      "s_mov_b64 s[56:57], s[90:91]\n"
      "s_load_dwordx16 s[84:99], s[84:85], 64\n"
      "s_lshl_b32 s29, s25, 8\n"
      "s_add_u32 s52, s60, s29\n"
      "s_addc_u32 s53, s61, 0\n"
      "s_lshl_b32 s29, s24, 5\n"
      "v_lshrrev_b32_e64 v149, 3, v136\n"
      "v_lshl_add_u32 v148, v149, 2, s29\n"
      "v_add_u32 v148, 41408, v148\n"
      "global_load_dword v148, v148, s[52:53]\n"
      "global_load_dword v30, v137, s[12:13]\n"
      "v_xor_b32 v149, v149, v136\n"
      "v_and_b32 v149, 7, v149\n"
      "s_waitcnt vmcnt(21)\n"
      "s_barrier\n"
      "ds_read_b64_tr_b16 a[0:1], v151\n"
      "ds_read_b64_tr_b16 a[2:3], v151 offset:4608\n"
      "ds_read_b64_tr_b16 a[4:5], v152\n"
      "ds_read_b64_tr_b16 a[6:7], v152 offset:4608\n"
      "ds_read_b64_tr_b16 a[8:9], v153\n"
      "ds_read_b64_tr_b16 a[10:11], v153 offset:4608\n"
      "ds_read_b64_tr_b16 a[12:13], v151 offset:384\n"
      "ds_read_b64_tr_b16 a[14:15], v151 offset:4992\n"
      "ds_read_b64_tr_b16 a[16:17], v152 offset:384\n"
      "v_add_u32 v139, 36864, v139\n"
      "v_add_u32 v140, 36864, v140\n"
      "v_add_u32 v141, 36864, v141\n"
      "v_add_u32 v143, 36864, v143\n"
      "v_add_u32 v142, 36864, v142\n"
      "v_add_u32 v146, 36864, v146\n"
      "global_load_dwordx4 a[144:147], v139, s[4:5] offset:-4096\n"
      "ds_read_b64_tr_b16 a[18:19], v152 offset:4992\n"
      "ds_read_b64_tr_b16 a[20:21], v153 offset:384\n"
      "ds_read_b64_tr_b16 a[22:23], v153 offset:4992\n"
      "ds_read_b64_tr_b16 a[24:25], v151 offset:768\n"
      "ds_read_b64_tr_b16 a[26:27], v151 offset:5376\n"
      "ds_read_b64_tr_b16 a[28:29], v152 offset:768\n"
      "ds_read_b64_tr_b16 a[30:31], v152 offset:5376\n"
      "ds_read_b64_tr_b16 a[32:33], v153 offset:768\n"
      "ds_read_b64_tr_b16 a[34:35], v153 offset:5376\n"
      "ds_read_b64_tr_b16 a[36:37], v151 offset:36864\n"
      "ds_read_b64_tr_b16 a[38:39], v151 offset:41472\n"
      "global_load_dwordx4 a[148:151], v139, s[4:5] offset:-3072\n"
      "ds_read_b64_tr_b16 a[40:41], v152 offset:36864\n"
      "ds_read_b64_tr_b16 a[42:43], v152 offset:41472\n"
      "ds_read_b64_tr_b16 a[44:45], v153 offset:36864\n"
      "ds_read_b64_tr_b16 a[46:47], v153 offset:41472\n"
      "global_load_dwordx4 a[152:155], v141, s[4:5] offset:-2048\n"
      "ds_read_b64_tr_b16 a[48:49], v151 offset:37248\n"
      "ds_read_b64_tr_b16 a[50:51], v151 offset:41856\n"
      "ds_read_b64_tr_b16 a[52:53], v152 offset:37248\n"
      "ds_read_b64_tr_b16 a[54:55], v152 offset:41856\n"
      "global_load_dwordx4 a[156:159], v140, s[4:5] offset:-1024\n"
      "ds_read_b64_tr_b16 a[56:57], v153 offset:37248\n"
      "ds_read_b64_tr_b16 a[58:59], v153 offset:41856\n"
      "ds_read_b64_tr_b16 a[60:61], v151 offset:37632\n"
      "ds_read_b64_tr_b16 a[62:63], v151 offset:42240\n"
      "global_load_dwordx4 a[160:163], v143, s[4:5]\n"
      "ds_read_b64_tr_b16 a[64:65], v152 offset:37632\n"
      "ds_read_b64_tr_b16 a[66:67], v152 offset:42240\n"
      "ds_read_b64_tr_b16 a[68:69], v153 offset:37632\n"
      "ds_read_b64_tr_b16 a[70:71], v153 offset:42240\n"
      "s_waitcnt lgkmcnt(0)\n"
      "s_pack_ll_b32_b16 s28, s36, s44\n"
      "s_waitcnt vmcnt(7)\n"
      "v_dot2_f32_bf16 v150, s28, v144, 0\n"
      "s_pack_hh_b32_b16 s29, s36, s44\n"
      "v_dot2_f32_bf16 v136, s29, v144, 0\n"
      "s_pack_ll_b32_b16 s30, s37, s45\n"
      "v_dot2_f32_bf16 v137, s30, v144, 0\n"
      "s_pack_hh_b32_b16 s31, s37, s45\n"
      "v_dot2_f32_bf16 v156, s31, v144, 0\n"
      "s_pack_ll_b32_b16 s28, s38, s46\n"
      "v_dot2_f32_bf16 v157, s28, v144, 0\n"
      "s_pack_hh_b32_b16 s29, s38, s46\n"
      "v_dot2_f32_bf16 v158, s29, v144, 0\n"
      "s_pack_ll_b32_b16 s30, s39, s47\n"
      "v_dot2_f32_bf16 v159, s30, v144, 0\n"
      "s_pack_hh_b32_b16 s31, s39, s47\n"
      "v_dot2_f32_bf16 v160, s31, v144, 0\n"
      "s_pack_ll_b32_b16 s28, s40, s48\n"
      "v_dot2_f32_bf16 v161, s28, v144, 0\n"
      "s_waitcnt vmcnt(6)\n"
      "v_lshl_add_u32 v148, v149, 4, v148\n"
      "s_lshl_b32 s10, s24, 10\n"
      "s_add_u32 m0, s10, 18432\n"
      "s_nop 0\n"
      "global_load_lds_dwordx4 v148, s[16:17]\n"
      "s_add_u32 m0, s10, 24448\n"
      "s_nop 0\n"
      "global_load_lds_dwordx4 v148, s[16:17] offset:128\n"
      "s_add_u32 m0, s10, 30464\n"
      "s_nop 0\n"
      "global_load_lds_dwordx4 v148, s[16:17] offset:256\n"
      "v_add_u32_e64 v149, s10, v138\n"
      "s_add_u32 m0, s10, 124928\n"
      "s_nop 0\n"
      "global_load_lds_dwordx4 v149, s[56:57]\n"
      "v_add_u32 v149, 4096, v149\n"
      "s_add_u32 m0, s10, 129024\n"
      "s_nop 0\n"
      "global_load_lds_dwordx4 v149, s[56:57]\n"
      "v_add_u32 v149, 4096, v149\n"
      "v_min_u32 v149, 9600, v149\n"
      "s_add_u32 m0, s10, 133120\n"
      "s_nop 0\n"
      "global_load_lds_dwordx4 v149, s[56:57]\n"
      "s_pack_hh_b32_b16 s29, s40, s48\n"
      "v_dot2_f32_bf16 v138, s29, v144, 0\n"
      "s_pack_ll_b32_b16 s30, s41, s49\n"
      "v_dot2_f32_bf16 v149, s30, v144, 0\n"
      "s_pack_hh_b32_b16 s31, s41, s49\n"
      "v_dot2_f32_bf16 v162, s31, v144, 0\n"
      "s_pack_ll_b32_b16 s28, s42, s50\n"
      "v_dot2_f32_bf16 v163, s28, v144, 0\n"
      "s_pack_hh_b32_b16 s29, s42, s50\n"
      "v_dot2_f32_bf16 v164, s29, v144, 0\n"
      "s_pack_ll_b32_b16 s30, s43, s51\n"
      "v_dot2_f32_bf16 v168, s30, v144, 0\n"
      "s_pack_hh_b32_b16 s31, s43, s51\n"
      "v_dot2_f32_bf16 v165, s31, v144, 0\n"
      "s_load_dwordx8 s[36:43], s[60:61], 41216\n"
      "s_load_dwordx4 s[44:47], s[60:61], 41248\n"
      "s_mov_b64 s[48:49], s[60:61]\n"
      "v_max_f32 v150, 0, v150\n"
      "v_max_f32 v136, 0, v136\n"
      "v_cvt_pk_bf16_f32 v150, v150, v136\n"
      "v_max_f32 v137, 0, v137\n"
      "v_max_f32 v156, 0, v156\n"
      "global_load_dwordx4 v[114:117], v154, s[60:61] offset:-4096\n"
      "v_cvt_pk_bf16_f32 v137, v137, v156\n"
      "v_max_f32 v157, 0, v157\n"
      "v_max_f32_e64 v158, 0, v158\n"
      "v_cvt_pk_bf16_f32 v157, v157, v158\n"
      "v_max_f32 v159, 0, v159\n"
      "global_load_dwordx4 a[164:167], v139, s[4:5] offset:1024\n"
      "v_max_f32 v160, 0, v160\n"
      "v_cvt_pk_bf16_f32 v159, v159, v160\n"
      "v_max_f32 v161, 0, v161\n"
      "v_max_f32 v138, 0, v138\n"
      "global_load_dwordx4 a[168:171], v142, s[4:5] offset:2048\n"
      "v_cvt_pk_bf16_f32 v161, v161, v138\n"
      "v_max_f32 v149, 0, v149\n"
      "v_max_f32 v162, 0, v162\n"
      "v_cvt_pk_bf16_f32 v149, v149, v162\n"
      "global_load_dwordx4 a[172:175], v140, s[4:5] offset:3072\n"
      "v_max_f32 v163, 0, v163\n"
      "v_max_f32 v164, 0, v164\n"
      "v_cvt_pk_bf16_f32 v163, v163, v164\n"
      "v_max_f32 v168, 0, v168\n"
      "global_load_dwordx4 a[176:179], v146, s[4:5]\n"
      "v_max_f32 v165, 0, v165\n"
      "v_cvt_pk_bf16_f32 v168, v168, v165\n"
      "s_waitcnt lgkmcnt(0)\n"
      "s_barrier\n"
      "s_pack_ll_b32_b16 s28, s64, s66\n"
      "v_dot2_f32_bf16 v164, s28, v150, 0\n"
      "s_pack_hh_b32_b16 s29, s64, s66\n"
      "v_dot2_f32_bf16 v165, s29, v150, 0\n"
      "s_pack_ll_b32_b16 s30, s65, s67\n"
      "v_dot2_f32_bf16 v166, s30, v150, 0\n"
      "s_pack_hh_b32_b16 s31, s65, s67\n"
      "v_dot2_f32_bf16 v167, s31, v150, 0\n"
      "s_pack_ll_b32_b16 s28, s68, s70\n"
      "v_dot2_f32_bf16 v164, s28, v137, v164\n"
      "s_pack_hh_b32_b16 s29, s68, s70\n"
      "v_dot2_f32_bf16 v165, s29, v137, v165\n"
      "s_pack_ll_b32_b16 s30, s69, s71\n"
      "v_dot2_f32_bf16 v166, s30, v137, v166\n"
      "s_pack_hh_b32_b16 s31, s69, s71\n"
      "v_dot2_f32_bf16 v167, s31, v137, v167\n"
      "v_add_u32 v139, 36864, v139\n"
      "v_add_u32 v140, 36864, v140\n"
      "v_add_u32 v141, 36864, v141\n"
      "v_add_u32 v143, 36864, v143\n"
      "v_add_u32 v142, 36864, v142\n"
      "v_add_u32 v146, 36864, v146\n"
      "global_load_dwordx4 a[180:183], v139, s[4:5] offset:-4096\n"
      "s_pack_ll_b32_b16 s28, s72, s74\n"
      "v_dot2_f32_bf16 v164, s28, v157, v164\n"
      "s_pack_hh_b32_b16 s29, s72, s74\n"
      "v_dot2_f32_bf16 v165, s29, v157, v165\n"
      "s_pack_ll_b32_b16 s30, s73, s75\n"
      "v_dot2_f32_bf16 v166, s30, v157, v166\n"
      "s_pack_hh_b32_b16 s31, s73, s75\n"
      "v_dot2_f32_bf16 v167, s31, v157, v167\n"
      "s_pack_ll_b32_b16 s28, s76, s78\n"
      "v_dot2_f32_bf16 v164, s28, v159, v164\n"
      "s_pack_hh_b32_b16 s29, s76, s78\n"
      "v_dot2_f32_bf16 v165, s29, v159, v165\n"
      "s_pack_ll_b32_b16 s30, s77, s79\n"
      "v_dot2_f32_bf16 v166, s30, v159, v166\n"
      "s_pack_hh_b32_b16 s31, s77, s79\n"
      "v_dot2_f32_bf16 v167, s31, v159, v167\n"
      "s_load_dwordx16 s[64:79], s[54:55], 40960\n"
      "s_lshl_b32 s28, s24, 4\n"
      "global_load_dwordx4 a[184:187], v139, s[4:5] offset:-3072\n"
      "s_lshr_b64 s[8:9], s[8:9], s28\n"
      "s_lshl_b32 s8, s8, 16\n"
      "v_mov_b32 v150, s8\n"
      "v_max_f32 v150, 0xc2c80000, v150\n"
      "v_min_f32 v150, 0x40930000, v150\n"
      "v_mul_f32 v150, 0x3fb8aa3b, v150\n"
      "v_exp_f32 v150, v150\n"
      "s_nop 0\n"
      "v_cvt_pk_bf16_f32 v150, v150, v150\n"
      "global_load_dwordx4 a[188:191], v141, s[4:5] offset:-2048\n"
      "v_lshlrev_b32_e64 v150, 16, v150\n"
      "s_pack_ll_b32_b16 s28, s84, s86\n"
      "v_dot2_f32_bf16 v164, s28, v161, v164\n"
      "s_pack_hh_b32_b16 s29, s84, s86\n"
      "v_dot2_f32_bf16 v165, s29, v161, v165\n"
      "s_pack_ll_b32_b16 s30, s85, s87\n"
      "v_dot2_f32_bf16 v166, s30, v161, v166\n"
      "global_load_dwordx4 a[192:195], v140, s[4:5] offset:-1024\n"
      "s_pack_hh_b32_b16 s31, s85, s87\n"
      "v_dot2_f32_bf16 v167, s31, v161, v167\n"
      "s_pack_ll_b32_b16 s28, s88, s90\n"
      "v_dot2_f32_bf16 v164, s28, v149, v164\n"
      "s_pack_hh_b32_b16 s29, s88, s90\n"
      "v_dot2_f32_bf16 v165, s29, v149, v165\n"
      "s_pack_ll_b32_b16 s30, s89, s91\n"
      "v_dot2_f32_bf16 v166, s30, v149, v166\n"
      "global_load_dwordx4 a[196:199], v143, s[4:5]\n"
      "s_pack_hh_b32_b16 s31, s89, s91\n"
      "v_dot2_f32_bf16 v167, s31, v149, v167\n"
      "s_pack_ll_b32_b16 s28, s92, s94\n"
      "v_dot2_f32_bf16 v164, s28, v163, v164\n"
      "s_pack_hh_b32_b16 s29, s92, s94\n"
      "v_dot2_f32_bf16 v165, s29, v163, v165\n"
      "s_pack_ll_b32_b16 s30, s93, s95\n"
      "v_dot2_f32_bf16 v166, s30, v163, v166\n"
      "global_load_dwordx4 a[200:203], v139, s[4:5] offset:1024\n"
      "s_pack_hh_b32_b16 s31, s93, s95\n"
      "v_dot2_f32_bf16 v167, s31, v163, v167\n"
      "s_pack_ll_b32_b16 s28, s96, s98\n"
      "v_dot2_f32_bf16 v164, s28, v168, v164\n"
      "s_pack_hh_b32_b16 s29, s96, s98\n"
      "v_dot2_f32_bf16 v165, s29, v168, v165\n"
      "s_pack_ll_b32_b16 s30, s97, s99\n"
      "global_load_dwordx4 a[204:207], v142, s[4:5] offset:2048\n"
      "v_dot2_f32_bf16 v166, s30, v168, v166\n"
      "s_pack_hh_b32_b16 s31, s97, s99\n"
      "v_dot2_f32_bf16 v167, s31, v168, v167\n"
      "s_load_dwordx16 s[80:95], s[52:53], 41280\n"
      "s_load_dwordx4 s[96:99], s[52:53], 41344\n"
      "s_load_dwordx2 s[100:101], s[52:53], 41360\n"
      "s_load_dword s26, s[52:53], 41376\n"
      "s_load_dword s3, s[52:53], 41380\n"
      "global_load_dwordx4 a[208:211], v140, s[4:5] offset:3072\n"
      "ds_write_b128 v147, a[72:75]\n"
      "ds_write_b128 v147, a[76:79] offset:1024\n"
      "ds_write_b128 v147, a[80:83] offset:2048\n"
      "ds_write_b128 v147, a[84:87] offset:3072\n"
      "ds_write_b128 v147, a[88:91] offset:4096\n"
      "ds_write_b128 v147, a[92:95] offset:5120\n"
      "ds_write_b128 v147, a[96:99] offset:6144\n"
      "global_load_dwordx4 a[212:215], v146, s[4:5]\n"
      "ds_write_b128 v147, a[100:103] offset:7168\n"
      "ds_write_b128 v147, a[104:107] offset:8192\n"
      "ds_write_b128 v147, a[108:111] offset:36864\n"
      "ds_write_b128 v147, a[112:115] offset:37888\n"
      "ds_write_b128 v147, a[116:119] offset:38912\n"
      "ds_write_b128 v147, a[120:123] offset:39936\n"
      "ds_write_b128 v147, a[124:127] offset:40960\n"
      "ds_write_b128 v147, a[128:131] offset:41984\n"
      "global_load_dwordx4 v[140:143], v154, s[60:61] offset:3072\n"
      "ds_write_b128 v147, a[132:135] offset:43008\n"
      "ds_write_b128 v147, a[136:139] offset:44032\n"
      "ds_write_b128 v147, a[140:143] offset:45056\n"
      "s_waitcnt vmcnt(15)\n"
      "s_waitcnt lgkmcnt(0)\n"
      "s_barrier\n"
      "L_sched_%=:\n"
      "ds_read_b64_tr_b16 a[76:77], v152\n"
      "s_waitcnt vmcnt(14)\n"
      "ds_read_b128 v[136:139], v114 offset:18432\n"
      "ds_read_b128 v[156:159], v115 offset:18432\n"
      "ds_read_b64_tr_b16 a[82:83], v153 offset:4608\n"
      "ds_read_b64_tr_b16 a[88:89], v152 offset:384\n"
      "v_cvt_pk_bf16_f32 v167, v166, v167\n"
      "v_cmp_nge_f32 vcc, 0x42480000, v150\n"
      "v_lshlrev_b32 v166, 16, v167\n"
      "s_waitcnt vmcnt(0)\n"
      "v_add_u32 v149, 3072, v143\n"
      "v_cvt_pk_bf16_f32 v165, v164, v165\n"
      "v_lshlrev_b32 v164, 16, v165\n"
      "s_mov_b64 s[50:51], vcc\n"
      "ds_read_b128 v[160:163], v114 offset:20480\n"
      "ds_read_b128 v[168:171], v115 offset:20480\n"
      "ds_read_b64_tr_b16 a[80:81], v153\n"
      "v_and_b32 v167, 0xffff0000, v167\n"
      "v_mul_f32 v167, 0xbfb8aa3b, v167\n"
      "v_and_b32 v165, 0xffff0000, v165\n"
      "v_mul_f32 v166, 0xbfb8aa3b, v166\n"
      "v_mul_f32 v164, 0xbfb8aa3b, v164\n"
      "v_mul_f32 v165, 0xbfb8aa3b, v165\n"
      "v_exp_f32 v167, v167\n"
      "v_exp_f32 v166, v166\n"
      "v_exp_f32 v164, v164\n"
      "v_exp_f32 v165, v165\n"
      "s_waitcnt lgkmcnt(5)\n"
      "v_mfma_f32_16x16x32_bf16 v[172:175], a[0:3], v[136:139], 0\n"
      "ds_read_b64_tr_b16 a[90:91], v152 offset:4992\n"
      "v_add_f32 v164, 1.0, v164\n"
      "v_mfma_f32_16x16x32_bf16 v[172:175], a[36:39], v[156:159], v[172:175]\n"
      "global_load_dwordx4 a[216:219], v149, s[6:7] offset:-3072\n"
      "v_mfma_f32_16x16x32_bf16 v[176:179], a[4:7], v[136:139], 0\n"
      "ds_read_b64_tr_b16 a[94:95], v153 offset:4992\n"
      "v_add_f32_e64 v165, 1.0, v165\n"
      "v_mfma_f32_16x16x32_bf16 v[176:179], a[40:43], v[156:159], v[176:179]\n"
      "ds_read_b64_tr_b16 a[100:101], v152 offset:768\n"
      "ds_read_b64_tr_b16 a[84:85], v151 offset:384\n"
      "ds_read_b64_tr_b16 a[96:97], v151 offset:768\n"
      "s_waitcnt lgkmcnt(5)\n"
      "v_mfma_f32_16x16x32_bf16 v[180:183], a[8:11], v[136:139], 0\n"
      "ds_read_b64_tr_b16 a[98:99], v151 offset:5376\n"
      "v_add_f32 v166, 1.0, v166\n"
      "v_mfma_f32_16x16x32_bf16 v[180:183], a[44:47], v[156:159], v[180:183]\n"
      "ds_read_b64_tr_b16 a[92:93], v153 offset:384\n"
      "ds_read_b64_tr_b16 a[86:87], v151 offset:4992\n"
      "v_mfma_f32_16x16x32_bf16 v[184:187], a[12:15], v[136:139], 0\n"
      "ds_read_b64_tr_b16 a[102:103], v152 offset:5376\n"
      "v_mul_f32 v150, 0x3fb8aa3b, v150\n"
      "v_mfma_f32_16x16x32_bf16 v[184:187], a[48:51], v[156:159], v[184:187]\n"
      "global_load_dwordx4 a[220:223], v149, s[6:7] offset:-2944\n"
      "v_rcp_f32_e64 v165, v165\n"
      "v_mfma_f32_16x16x32_bf16 v[188:191], a[16:19], v[136:139], 0\n"
      "ds_read_b64_tr_b16 a[78:79], v152 offset:4608\n"
      "v_add_f32_e64 v167, 1.0, v167\n"
      "v_mfma_f32_16x16x32_bf16 v[188:191], a[52:55], v[156:159], v[188:191]\n"
      "ds_read_b64_tr_b16 a[104:105], v153 offset:768\n"
      "v_rcp_f32_e64 v164, v164\n"
      "v_mfma_f32_16x16x32_bf16 v[192:195], a[20:23], v[136:139], 0\n"
      "ds_read_b64_tr_b16 a[74:75], v151 offset:4608\n"
      "ds_read_b64_tr_b16 a[72:73], v151\n"
      "v_mfma_f32_16x16x32_bf16 v[192:195], a[56:59], v[156:159], v[192:195]\n"
      "ds_read_b64_tr_b16 a[106:107], v153 offset:5376\n"
      "ds_read_b64_tr_b16 a[110:111], v151 offset:41472\n"
      "v_mfma_f32_16x16x32_bf16 v[196:199], a[24:27], v[136:139], 0\n"
      "ds_read_b64_tr_b16 a[112:113], v152 offset:36864\n"
      "v_cvt_pk_bf16_f32 v165, v164, v165\n"
      "v_mfma_f32_16x16x32_bf16 v[196:199], a[60:63], v[156:159], v[196:199]\n"
      "ds_read_b64_tr_b16 a[114:115], v152 offset:41472\n"
      "v_rcp_f32 v166, v166\n"
      "v_mfma_f32_16x16x32_bf16 v[200:203], a[28:31], v[136:139], 0\n"
      "ds_read_b64_tr_b16 a[116:117], v153 offset:36864\n"
      "v_lshlrev_b32 v164, 16, v165\n"
      "v_mfma_f32_16x16x32_bf16 v[200:203], a[64:67], v[156:159], v[200:203]\n"
      "ds_read_b64_tr_b16 a[118:119], v153 offset:41472\n"
      "v_and_b32 v165, 0xffff0000, v165\n"
      "v_mfma_f32_16x16x32_bf16 v[204:207], a[32:35], v[136:139], 0\n"
      "global_load_dwordx4 a[224:227], v149, s[6:7] offset:-2816\n"
      "v_add_f32 v164, -0.5, v164\n"
      "v_mfma_f32_16x16x32_bf16 v[204:207], a[68:71], v[156:159], v[204:207]\n"
      "ds_read_b64_tr_b16 a[108:109], v151 offset:36864\n"
      "v_rcp_f32 v167, v167\n"
      "v_mfma_f32_16x16x32_bf16 v[136:139], a[0:3], v[160:163], 0\n"
      "ds_read_b64_tr_b16 a[122:123], v151 offset:41856\n"
      "v_cvt_pk_bf16_f32 v167, v166, v167\n"
      "v_mfma_f32_16x16x32_bf16 v[136:139], a[36:39], v[168:171], v[136:139]\n"
      "ds_read_b64_tr_b16 a[120:121], v151 offset:37248\n"
      "ds_read_b64_tr_b16 a[124:125], v152 offset:37248\n"
      "v_mfma_f32_16x16x32_bf16 v[156:159], a[4:7], v[160:163], 0\n"
      "ds_read_b64_tr_b16 a[126:127], v152 offset:41856\n"
      "v_lshlrev_b32 v166, 16, v167\n"
      "v_mfma_f32_16x16x32_bf16 v[156:159], a[40:43], v[168:171], v[156:159]\n"
      "ds_read_b64_tr_b16 a[128:129], v153 offset:37248\n"
      "v_add_f32 v165, -0.5, v165\n"
      "v_mfma_f32_16x16x32_bf16 v[208:211], a[8:11], v[160:163], 0\n"
      "ds_read_b64_tr_b16 a[130:131], v153 offset:41856\n"
      "v_mul_f32 v164, 0x41b8aa3b, v164\n"
      "v_mfma_f32_16x16x32_bf16 v[208:211], a[44:47], v[168:171], v[208:211]\n"
      "global_load_dwordx4 a[228:231], v149, s[6:7]\n"
      "v_mfma_f32_16x16x32_bf16 v[212:215], a[12:15], v[160:163], 0\n"
      "ds_read_b64_tr_b16 a[134:135], v151 offset:42240\n"
      "v_add_f32_e64 v166, -0.5, v166\n"
      "v_mfma_f32_16x16x32_bf16 v[212:215], a[48:51], v[168:171], v[212:215]\n"
      "global_load_dwordx4 a[232:235], v149, s[6:7] offset:128\n"
      "v_mfma_f32_16x16x32_bf16 v[216:219], a[16:19], v[160:163], 0\n"
      "ds_read_b64_tr_b16 a[136:137], v152 offset:37632\n"
      "v_mul_f32 v166, 0x41b8aa3b, v166\n"
      "v_mfma_f32_16x16x32_bf16 v[220:223], a[20:23], v[160:163], 0\n"
      "ds_read_b64_tr_b16 a[138:139], v152 offset:42240\n"
      "v_and_b32 v167, 0xffff0000, v167\n"
      "v_mfma_f32_16x16x32_bf16 v[220:223], a[56:59], v[168:171], v[220:223]\n"
      "ds_read_b64_tr_b16 a[140:141], v153 offset:37632\n"
      "v_add_f32_e64 v167, -0.5, v167\n"
      "v_mul_f32 v165, 0x41b8aa3b, v165\n"
      "v_mul_f32 v167, 0x41b8aa3b, v167\n"
      "v_mfma_f32_16x16x32_bf16 v[224:227], a[24:27], v[160:163], 0\n"
      "ds_read_b64_tr_b16 a[132:133], v151 offset:37632\n"
      "v_mfma_f32_16x16x32_bf16 v[216:219], a[52:55], v[168:171], v[216:219]\n"
      "global_load_dwordx4 a[236:239], v149, s[6:7] offset:256\n"
      "v_add_u32 v149, 9216, v143\n"
      "v_mfma_f32_16x16x32_bf16 v[228:231], a[28:31], v[160:163], 0\n"
      "global_load_dwordx4 a[240:243], v149, s[6:7] offset:-3072\n"
      "ds_write_b32 v145, v165 offset:768\n"
      "ds_write_b32 v145, v166 offset:1536\n"
      "v_mfma_f32_16x16x32_bf16 v[228:231], a[64:67], v[168:171], v[228:231]\n"
      "ds_write_b32 v145, v167 offset:2304\n"
      "v_mfma_f32_16x16x32_bf16 v[224:227], a[60:63], v[168:171], v[224:227]\n"
      "ds_write_b32 v145, v164\n"
      "v_mfma_f32_16x16x32_bf16 v[232:235], a[32:35], v[160:163], 0\n"
      "ds_read_b64_tr_b16 a[142:143], v153 offset:42240\n"
      "v_mfma_f32_16x16x32_bf16 v[232:235], a[68:71], v[168:171], v[232:235]\n"
      "global_load_dwordx4 a[244:247], v149, s[6:7] offset:-2944\n"
      "global_load_dwordx4 a[248:251], v149, s[6:7] offset:-2816\n"
      "s_waitcnt lgkmcnt(0)\n"
      "s_barrier\n"
      "ds_read_b128 v[164:167], v115 offset:24576\n"
      "v_add_u32 v145, 3136, v140\n"
      "s_mov_b32 s11, 100466688\n"
      "ds_read_b128 v[160:163], v114 offset:24576\n"
      "ds_read2_b32 v[96:97], v145 offset0:18 offset1:19\n"
      "v_mov_b32 v42, 0\n"
      "s_sub_u32 s16, s16, 99876864\n"
      "s_subb_u32 s17, s17, 0\n"
      "ds_read2_b32 v[82:83], v140 offset0:16 offset1:17\n"
      "v_mov_b32 v48, 0\n"
      "v_mov_b32 v37, 0\n"
      "v_mov_b32 v57, 0\n"
      "v_readfirstlane_b32 s36, v150\n"
      "s_sub_u32 s22, s22, 99876864\n"
      "ds_read_b128 v[168:171], v114 offset:26624\n"
      "ds_read2_b32 v[80:81], v140 offset0:2 offset1:3\n"
      "global_load_dwordx4 v[118:121], v154, s[48:49] offset:-3072\n"
      "ds_read2_b32 v[86:87], v140 offset0:32 offset1:33\n"
      "s_subb_u32 s23, s23, 0\n"
      "ds_read2_b32 v[92:93], v145 offset0:2 offset1:3\n"
      "ds_read_b128 v[236:239], v115 offset:26624\n"
      "v_mov_b32 v36, 0\n"
      "v_mov_b32_e64 v53, 0\n"
      "s_add_u32 s12, s20, 16777216\n"
      "s_addc_u32 s13, s21, 0\n"
      "s_waitcnt lgkmcnt(7)\n"
      "v_mfma_f32_16x16x32_bf16 v[172:175], a[72:75], v[160:163], v[172:175]\n"
      "ds_read2_b32 v[94:95], v145 offset0:16 offset1:17\n"
      "s_mul_i32 s2, s24, 768\n"
      "v_mfma_f32_16x16x32_bf16 v[172:175], a[108:111], v[164:167], v[172:175]\n"
      "ds_read2_b32 v[88:89], v140 offset0:34 offset1:35\n"
      "v_mov_b32 v150, 134528\n"
      "v_mfma_f32_16x16x32_bf16 v[176:179], a[76:79], v[160:163], v[176:179]\n"
      "ds_read2_b32 v[90:91], v145 offset0:0 offset1:1\n"
      "v_mov_b32_e64 v65, 0\n"
      "v_mfma_f32_16x16x32_bf16 v[176:179], a[112:115], v[164:167], v[176:179]\n"
      "global_load_dwordx4 v[122:125], v154, s[48:49] offset:-2048\n"
      "v_mfma_f32_16x16x32_bf16 v[180:183], a[80:83], v[160:163], v[180:183]\n"
      "ds_read2_b32 v[100:101], v145 offset0:34 offset1:35\n"
      "v_add_u32 v146, 6272, v140\n"
      "v_mfma_f32_16x16x32_bf16 v[180:183], a[116:119], v[164:167], v[180:183]\n"
      "ds_read2_b32 v[98:99], v145 offset0:32 offset1:33\n"
      "v_mov_b32 v43, 0\n"
      "v_mfma_f32_16x16x32_bf16 v[184:187], a[84:87], v[160:163], v[184:187]\n"
      "ds_read2_b32 v[104:105], v146 offset0:2 offset1:3\n"
      "v_mov_b32 v61, 0\n"
      "v_mfma_f32_16x16x32_bf16 v[184:187], a[120:123], v[164:167], v[184:187]\n"
      "ds_read_b32 v70, v141 offset:192\n"
      "ds_read2_b32 v[102:103], v146 offset0:0 offset1:1\n"
      "v_mfma_f32_16x16x32_bf16 v[188:191], a[88:91], v[160:163], v[188:191]\n"
      "ds_read2_b32 v[106:107], v146 offset0:16 offset1:17\n"
      "v_mov_b32 v49, 0\n"
      "v_mfma_f32_16x16x32_bf16 v[188:191], a[124:127], v[164:167], v[188:191]\n"
      "ds_read2_b32 v[110:111], v146 offset0:32 offset1:33\n"
      "ds_read2_b32 v[84:85], v140 offset0:18 offset1:19\n"
      "v_mfma_f32_16x16x32_bf16 v[192:195], a[92:95], v[160:163], v[192:195]\n"
      "s_add_u32 s2, s2, 150640\n"
      "ds_read2_b32 v[112:113], v146 offset0:34 offset1:35\n"
      "v_mfma_f32_16x16x32_bf16 v[192:195], a[128:131], v[164:167], v[192:195]\n"
      "ds_read_b32 v77, v150\n"
      "ds_read_b32 v72, v141 offset:3328\n"
      "v_mfma_f32_16x16x32_bf16 v[196:199], a[96:99], v[160:163], v[196:199]\n"
      "global_load_dwordx4 v[240:243], v155, s[48:49] offset:-3072\n"
      "v_mfma_f32_16x16x32_bf16 v[196:199], a[132:135], v[164:167], v[196:199]\n"
      "ds_read2_b32 v[78:79], v140 offset0:0 offset1:1\n"
      "ds_read2_b32 v[108:109], v146 offset0:18 offset1:19\n"
      "v_mfma_f32_16x16x32_bf16 v[200:203], a[100:103], v[160:163], v[200:203]\n"
      "s_waitcnt lgkmcnt(10)\n"
      "ds_read_b32 v74, v141 offset:6464\n"
      "v_mfma_f32_16x16x32_bf16 v[200:203], a[136:139], v[164:167], v[200:203]\n"
      "ds_read_b32 v75, v142 offset:128\n"
      "ds_read_b32 v71, v142\n"
      "v_mfma_f32_16x16x32_bf16 v[204:207], a[104:107], v[160:163], v[204:207]\n"
      "ds_read_b32 v73, v142 offset:64\n"
      "v_mfma_f32_16x16x32_bf16 v[204:207], a[140:143], v[164:167], v[204:207]\n"
      "ds_write_b128 v147, a[204:207] offset:43008\n"
      "v_mfma_f32_16x16x32_bf16 v[136:139], a[72:75], v[168:171], v[136:139]\n"
      "ds_write_b128 v147, a[156:159] offset:3072\n"
      "v_mfma_f32_16x16x32_bf16 v[136:139], a[108:111], v[236:239], v[136:139]\n"
      "ds_write_b128 v147, a[144:147]\n"
      "v_mfma_f32_16x16x32_bf16 v[156:159], a[76:79], v[168:171], v[156:159]\n"
      "ds_write_b128 v147, a[164:167] offset:5120\n"
      "v_mfma_f32_16x16x32_bf16 v[156:159], a[112:115], v[236:239], v[156:159]\n"
      "ds_write_b128 v147, a[148:151] offset:1024\n"
      "v_mfma_f32_16x16x32_bf16 v[208:211], a[80:83], v[168:171], v[208:211]\n"
      "global_load_dwordx4 v[160:163], v155, s[48:49] offset:-4096\n"
      "v_mfma_f32_16x16x32_bf16 v[208:211], a[116:119], v[236:239], v[208:211]\n"
      "ds_write_b128 v147, a[152:155] offset:2048\n"
      "v_mfma_f32_16x16x32_bf16 v[212:215], a[84:87], v[168:171], v[212:215]\n"
      "ds_write_b128 v147, a[208:211] offset:44032\n"
      "v_mfma_f32_16x16x32_bf16 v[212:215], a[120:123], v[236:239], v[212:215]\n"
      "ds_write_b128 v147, a[184:187] offset:37888\n"
      "v_mfma_f32_16x16x32_bf16 v[216:219], a[88:91], v[168:171], v[216:219]\n"
      "ds_write_b128 v147, a[160:163] offset:4096\n"
      "v_mfma_f32_16x16x32_bf16 v[220:223], a[92:95], v[168:171], v[220:223]\n"
      "ds_write_b128 v147, a[172:175] offset:7168\n"
      "v_mfma_f32_16x16x32_bf16 v[224:227], a[96:99], v[168:171], v[224:227]\n"
      "ds_write_b128 v147, a[200:203] offset:41984\n"
      "v_mfma_f32_16x16x32_bf16 v[228:231], a[100:103], v[168:171], v[228:231]\n"
      "ds_write_b128 v147, a[168:171] offset:6144\n"
      "v_mfma_f32_16x16x32_bf16 v[224:227], a[132:135], v[236:239], v[224:227]\n"
      "ds_write_b128 v147, a[212:215] offset:45056\n"
      "v_mfma_f32_16x16x32_bf16 v[232:235], a[104:107], v[168:171], v[232:235]\n"
      "ds_write_b128 v147, a[188:191] offset:38912\n"
      "v_mfma_f32_16x16x32_bf16 v[228:231], a[136:139], v[236:239], v[228:231]\n"
      "ds_write_b128 v147, a[196:199] offset:40960\n"
      "v_mfma_f32_16x16x32_bf16 v[220:223], a[128:131], v[236:239], v[220:223]\n"
      "ds_write_b128 v147, a[192:195] offset:39936\n"
      "v_mfma_f32_16x16x32_bf16 v[216:219], a[124:127], v[236:239], v[216:219]\n"
      "ds_write_b128 v147, a[180:183] offset:36864\n"
      "v_mfma_f32_16x16x32_bf16 v[232:235], a[140:143], v[236:239], v[232:235]\n"
      "ds_write_b128 v147, a[176:179] offset:8192\n"
      "s_waitcnt lgkmcnt(0)\n"
      "s_barrier\n"
      "s_waitcnt vmcnt(1)\n"
      "v_mov_b32 v144, v241\n"
      "v_mov_b32_e64 v145, v241\n"
      "v_lshl_add_u32 v79, v79, 2, s2\n"
      "v_mov_b32_e64 v146, v241\n"
      "v_lshl_add_u32 v82, v82, 2, s2\n"
      "s_mov_b32 s60, 100270464\n"
      "v_lshl_add_u32 v81, v81, 2, s2\n"
      "s_mov_b32 s61, 196224\n"
      "v_mov_b32 v67, v240\n"
      "v_lshl_add_u32 v78, v78, 2, s2\n"
      "v_mov_b32_e64 v69, v240\n"
      "s_lshr_b32 s8, s80, 8\n"
      "s_and_b32 s9, s80, 0xff\n"
      "s_mul_i32 s10, s8, 73\n"
      "v_mov_b32_e64 v147, v241\n"
      "v_lshl_add_u32 v83, v83, 2, s2\n"
      "v_mov_b32 v68, v240\n"
      "s_add_u32 s10, s10, s9\n"
      "ds_read_b64_tr_b16 a[196:197], v152 offset:37248\n"
      "s_mul_i32 s28, s10, 4802\n"
      "v_mov_b32_e64 v66, v240\n"
      "s_add_u32 s56, s18, s28\n"
      "s_addc_u32 s57, s19, 0\n"
      "ds_read_b64_tr_b16 a[164:165], v153 offset:384\n"
      "s_and_b32 s56, s56, -16\n"
      "v_lshl_add_u32 v86, v86, 2, s2\n"
      "v_lshl_add_u32 v85, v85, 2, s2\n"
      "s_sub_u32 s58, s56, 110592\n"
      "ds_read_b64_tr_b16 a[188:189], v153 offset:36864\n"
      "s_subb_u32 s59, s57, 0\n"
      "ds_read_b64_tr_b16 a[198:199], v152 offset:41856\n"
      "ds_read_b64_tr_b16 a[148:149], v152\n"
      "v_lshl_add_u32 v88, v88, 2, s2\n"
      "s_sub_u32 s56, s56, 73728\n"
      "ds_read_b64_tr_b16 a[146:147], v151 offset:4608\n"
      "ds_read_b64_tr_b16 a[186:187], v152 offset:41472\n"
      "v_lshl_add_u32 v80, v80, 2, s2\n"
      "v_mov_b32 v240, 0\n"
      "v_add_u32 v148, 99876864, v148\n"
      "s_subb_u32 s57, s57, 0\n"
      "ds_read_b128 v[236:239], v115 offset:32768\n"
      "ds_read_b64_tr_b16 a[158:159], v151 offset:4992\n"
      "ds_read_b64_tr_b16 a[170:171], v151 offset:5376\n"
      "ds_read_b64_tr_b16 a[190:191], v153 offset:41472\n"
      "v_mov_b32_e64 v241, 0\n"
      "s_mul_i32 s28, s8, 1373568\n"
      "s_mul_i32 s29, s9, 2688\n"
      "ds_read_b64_tr_b16 a[182:183], v151 offset:41472\n"
      "ds_read_b64_tr_b16 a[152:153], v153\n"
      "ds_read_b32 v79, v79\n"
      "ds_read_b64_tr_b16 a[202:203], v153 offset:41856\n"
      "ds_read_b32 v82, v82\n"
      "ds_read_b64_tr_b16 a[184:185], v152 offset:36864\n"
      "s_add_u32 s28, s28, s29\n"
      "ds_read_b64_tr_b16 a[168:169], v151 offset:768\n"
      "ds_read_b64_tr_b16 a[180:181], v151 offset:36864\n"
      "s_cmp_eq_u32 s8, 72\n"
      "ds_read_b64_tr_b16 a[166:167], v153 offset:4992\n"
      "v_lshl_add_u32 v84, v84, 2, s2\n"
      "s_cselect_b32 s8, 0, 100270464\n"
      "ds_read_b64_tr_b16 a[206:207], v151 offset:42240\n"
      "ds_read_b64_tr_b16 a[144:145], v151\n"
      "s_waitcnt vmcnt(0)\n"
      "ds_write_b32 v161, v148\n"
      "ds_read_b64_tr_b16 a[160:161], v152 offset:384\n"
      "ds_read_b64_tr_b16 a[208:209], v152 offset:37632\n"
      "ds_read_b128 v[164:167], v114 offset:30720\n"
      "ds_read_b64_tr_b16 a[194:195], v151 offset:41856\n"
      "ds_read_b64_tr_b16 a[200:201], v153 offset:37248\n"
      "ds_read_b64_tr_b16 a[214:215], v153 offset:42240\n"
      "v_lshl_add_u32 v87, v87, 2, s2\n"
      "s_cmp_eq_u32 s9, 72\n"
      "s_cselect_b32 s9, 0, 196224\n"
      "ds_read_b64_tr_b16 a[174:175], v152 offset:5376\n"
      "s_add_u32 s10, s8, s9\n"
      "ds_read_b64_tr_b16 a[178:179], v153 offset:5376\n"
      "ds_read_b32 v85, v85\n"
      "s_add_u32 s28, s28, s10\n"
      "s_add_u32 s52, s16, s28\n"
      "ds_read_b128 v[168:171], v115 offset:30720\n"
      "ds_read_b64_tr_b16 a[154:155], v153 offset:4608\n"
      "ds_read_b64_tr_b16 a[150:151], v152 offset:4608\n"
      "ds_read_b32 v81, v81\n"
      "ds_read_b64_tr_b16 a[176:177], v153 offset:768\n"
      "ds_read_b64_tr_b16 a[210:211], v152 offset:42240\n"
      "ds_read_b32 v83, v83\n"
      "ds_read_b64_tr_b16 a[192:193], v151 offset:37248\n"
      "ds_read_b64_tr_b16 a[172:173], v152 offset:768\n"
      "ds_read_b64_tr_b16 a[212:213], v153 offset:37632\n"
      "ds_read_b64_tr_b16 a[204:205], v151 offset:37632\n"
      "s_addc_u32 s53, s17, 0\n"
      "ds_read_b32 v78, v78\n"
      "s_cmp_lg_u32 s10, s11\n"
      "ds_read_b64_tr_b16 a[156:157], v151 offset:384\n"
      "ds_read_b64_tr_b16 a[162:163], v152 offset:4992\n"
      "ds_read_b128 v[150:153], v114 offset:32768\n"
      "v_lshl_add_u32 v89, v89, 2, s2\n"
      "s_cbranch_scc1 L_edge_1_%=\n"
      "L_eb_2_%=:\n"
      "s_add_u32 m0, s76, 0\n"
      "s_waitcnt lgkmcnt(12)\n"
      "global_load_lds_dwordx4 v116, s[52:53]\n"
      "s_add_u32 m0, s76, 6016\n"
      "v_mfma_f32_16x16x32_bf16 v[172:175], a[144:147], v[164:167], v[172:175]\n"
      "global_load_lds_dwordx4 v116, s[52:53] offset:128\n"
      "v_lshl_add_u32 v95, v95, 2, s2\n"
      "v_mfma_f32_16x16x32_bf16 v[176:179], a[148:151], v[164:167], v[176:179]\n"
      "ds_read_b32 v89, v89\n"
      "v_lshl_add_u32 v99, v99, 2, s2\n"
      "v_lshl_add_u32 v92, v92, 2, s2\n"
      "v_lshl_add_u32 v91, v91, 2, s2\n"
      "v_mov_b32 v245, 0\n"
      "v_mov_b32_e64 v244, 0\n"
      "v_mfma_f32_16x16x32_bf16 v[172:175], a[180:183], v[168:171], v[172:175]\n"
      "ds_read_b32 v87, v87\n"
      "v_lshl_add_u32 v75, v75, 2, s2\n"
      "v_mfma_f32_16x16x32_bf16 v[180:183], a[152:155], v[164:167], v[180:183]\n"
      "s_waitcnt lgkmcnt(3)\n"
      "ds_read_b32 v80, v80\n"
      "v_mfma_f32_16x16x32_bf16 v[180:183], a[188:191], v[168:171], v[180:183]\n"
      "ds_read_b32 v84, v84\n"
      "s_add_u32 m0, s76, 12032\n"
      "v_lshl_add_u32 v93, v93, 2, s2\n"
      "v_lshl_add_u32 v94, v94, 2, s2\n"
      "v_mfma_f32_16x16x32_bf16 v[176:179], a[184:187], v[168:171], v[176:179]\n"
      "global_load_lds_dwordx4 v116, s[52:53] offset:256\n"
      "v_lshl_add_u32 v103, v103, 2, s2\n"
      "s_add_u32 m0, s77, 0\n"
      "v_lshl_add_u32 v90, v90, 2, s2\n"
      "v_lshl_add_u32 v72, v72, 2, s2\n"
      "v_mfma_f32_16x16x32_bf16 v[184:187], a[156:159], v[164:167], v[184:187]\n"
      "ds_read_b32 v86, v86\n"
      "v_lshl_add_u32 v100, v100, 2, s2\n"
      "v_mfma_f32_16x16x32_bf16 v[184:187], a[192:195], v[168:171], v[184:187]\n"
      "global_load_lds_dwordx4 v117, s[52:53]\n"
      "s_add_u32 m0, s78, 0\n"
      "v_lshl_add_u32 v96, v96, 2, s2\n"
      "s_mov_b32 exec_hi, 0\n"
      "global_load_lds_dwordx4 v118, s[52:53]\n"
      "s_mov_b32 exec_hi, -1\n"
      "ds_read_b32 v94, v94\n"
      "v_mfma_f32_16x16x32_bf16 v[188:191], a[160:163], v[164:167], v[188:191]\n"
      "s_add_u32 m0, s76, 110592\n"
      "ds_read_b32 v90, v90\n"
      "v_mfma_f32_16x16x32_bf16 v[188:191], a[196:199], v[168:171], v[188:191]\n"
      "global_load_lds_dwordx4 v119, s[56:57]\n"
      "v_lshl_add_u32 v113, v113, 2, s2\n"
      "v_lshl_add_u32 v112, v112, 2, s2\n"
      "s_add_u32 m0, s79, 0\n"
      "ds_read_b32 v91, v91\n"
      "global_load_lds_dword v122, s[58:59]\n"
      "v_mfma_f32_16x16x32_bf16 v[192:195], a[164:167], v[164:167], v[192:195]\n"
      "ds_read_b32 v88, v88\n"
      "v_lshl_add_u32 v101, v101, 2, s2\n"
      "v_mfma_f32_16x16x32_bf16 v[192:195], a[200:203], v[168:171], v[192:195]\n"
      "global_load_dwordx4 v[126:129], v154, s[48:49] offset:-1024\n"
      "v_lshl_add_u32 v109, v109, 2, s2\n"
      "v_mfma_f32_16x16x32_bf16 v[196:199], a[168:171], v[164:167], v[196:199]\n"
      "s_waitcnt lgkmcnt(4)\n"
      "global_load_dwordx4 v[130:133], v154, s[48:49]\n"
      "global_load_dwordx4 v[248:251], v154, s[48:49] offset:1024\n"
      "v_lshl_add_u32 v77, v77, 2, s2\n"
      "v_lshl_add_u32 v98, v98, 2, s2\n"
      "v_lshl_add_u32 v104, v104, 2, s2\n"
      "v_mfma_f32_16x16x32_bf16 v[200:203], a[172:175], v[164:167], v[200:203]\n"
      "global_load_dwordx4 a[252:255], v149, s[6:7]\n"
      "v_lshl_add_u32 v73, v73, 2, s2\n"
      "v_lshl_add_u32 v97, v97, 2, s2\n"
      "v_lshl_add_u32 v110, v110, 2, s2\n"
      "v_lshl_add_u32 v102, v102, 2, s2\n"
      "v_mfma_f32_16x16x32_bf16 v[204:207], a[176:179], v[164:167], v[204:207]\n"
      "ds_read_b32 v92, v92\n"
      "v_lshl_add_u32 v105, v105, 2, s2\n"
      "v_mfma_f32_16x16x32_bf16 v[204:207], a[212:215], v[168:171], v[204:207]\n"
      "ds_read_b32 v93, v93\n"
      "ds_read_b32 v98, v98\n"
      "v_lshl_add_u32 v108, v108, 2, s2\n"
      "v_mfma_f32_16x16x32_bf16 v[196:199], a[204:207], v[168:171], v[196:199]\n"
      "ds_read_b32 v100, v100\n"
      "v_lshl_add_u32 v107, v107, 2, s2\n"
      "v_mfma_f32_16x16x32_bf16 v[200:203], a[208:211], v[168:171], v[200:203]\n"
      "ds_read_b32 v107, v107\n"
      "global_load_dwordx4 v[0:3], v149, s[6:7] offset:128\n"
      "v_mfma_f32_16x16x32_bf16 v[136:139], a[144:147], v[150:153], v[136:139]\n"
      "ds_read_b32 v96, v96\n"
      "v_fma_f32 v148, v172, v172, s38\n"
      "v_mfma_f32_16x16x32_bf16 v[136:139], a[180:183], v[236:239], v[136:139]\n"
      "global_load_dwordx4 v[4:7], v149, s[6:7] offset:256\n"
      "v_fma_f32 v161, v184, v184, s38\n"
      "v_lshl_add_u32 v106, v106, 2, s2\n"
      "v_fmac_f32 v148, v173, v173\n"
      "v_lshl_add_u32 v111, v111, 2, s2\n"
      "v_fmac_f32 v161, v185, v185\n"
      "v_mfma_f32_16x16x32_bf16 v[156:159], a[148:151], v[150:153], v[156:159]\n"
      "ds_read_b32 v97, v97\n"
      "v_add_u32 v149, 15360, v143\n"
      "v_mfma_f32_16x16x32_bf16 v[156:159], a[184:187], v[236:239], v[156:159]\n"
      "ds_read_b32 v103, v103\n"
      "ds_read_b32 v95, v95\n"
      "v_lshl_add_u32 v71, v71, 2, s2\n"
      "v_fmac_f32 v161, v186, v186\n"
      "v_fmac_f32 v161, v187, v187\n"
      "v_fmac_f32_e64 v148, v174, v174\n"
      "v_mfma_f32_16x16x32_bf16 v[208:211], a[152:155], v[150:153], v[208:211]\n"
      "ds_read_b32 v102, v102\n"
      "v_fmac_f32 v148, v175, v175\n"
      "v_mfma_f32_16x16x32_bf16 v[208:211], a[188:191], v[236:239], v[208:211]\n"
      "global_load_dwordx4 v[8:11], v149, s[6:7] offset:-3072\n"
      "v_fmac_f32 v148, v176, v176\n"
      "v_fmac_f32 v161, v188, v188\n"
      "v_fmac_f32 v161, v189, v189\n"
      "v_fmac_f32 v161, v190, v190\n"
      "v_fmac_f32 v148, v177, v177\n"
      "v_lshl_add_u32 v74, v74, 2, s2\n"
      "v_fmac_f32 v161, v191, v191\n"
      "v_fmac_f32 v148, v178, v178\n"
      "v_fmac_f32 v161, v192, v192\n"
      "v_fmac_f32 v148, v179, v179\n"
      "v_fmac_f32 v161, v193, v193\n"
      "v_fmac_f32 v161, v194, v194\n"
      "v_lshl_add_u32 v70, v70, 2, s2\n"
      "v_fmac_f32 v148, v180, v180\n"
      "v_fmac_f32 v148, v181, v181\n"
      "v_fmac_f32 v148, v182, v182\n"
      "v_fmac_f32 v161, v195, v195\n"
      "v_fmac_f32_e64 v148, v183, v183\n"
      "v_mfma_f32_16x16x32_bf16 v[212:215], a[156:159], v[150:153], v[212:215]\n"
      "ds_read_b32 v104, v104\n"
      "ds_read_b32 v109, v109\n"
      "ds_read_b32 v74, v74\n"
      "v_permlane16_swap_b32 v148, v161\n"
      "v_add_f32 v148, v148, v161\n"
      "v_mov_b32_e64 v161, v148\n"
      "v_mfma_f32_16x16x32_bf16 v[212:215], a[192:195], v[236:239], v[212:215]\n"
      "global_load_dwordx4 v[12:15], v149, s[6:7] offset:-2944\n"
      "ds_read_b32 v106, v106\n"
      "ds_read_b32 v105, v105\n"
      "v_permlane32_swap_b32 v148, v161\n"
      "v_add_f32 v148, v148, v161\n"
      "v_rsq_f32 v148, v148\n"
      "ds_read_b32 v111, v111\n"
      "v_mov_b32 v161, v148\n"
      "v_mfma_f32_16x16x32_bf16 v[216:219], a[160:163], v[150:153], v[216:219]\n"
      "ds_read_b32 v101, v101\n"
      "v_permlane16_swap_b32 v148, v161\n"
      "v_mul_f32 v187, v161, v187\n"
      "v_mul_f32 v185, v161, v185\n"
      "v_mul_f32 v190, v161, v190\n"
      "v_mul_f32 v192, v161, v192\n"
      "v_mul_f32 v191, v161, v191\n"
      "v_mul_f32_e64 v184, v161, v184\n"
      "v_mfma_f32_16x16x32_bf16 v[216:219], a[196:199], v[236:239], v[216:219]\n"
      "global_load_dwordx4 v[16:19], v149, s[6:7] offset:-2816\n"
      "ds_read_b32 v108, v108\n"
      "global_load_dwordx4 v[20:23], v149, s[6:7]\n"
      "v_mul_f32 v193, v161, v193\n"
      "v_cvt_pk_bf16_f32 v184, v184, v185\n"
      "v_mul_f32 v148, s36, v148\n"
      "v_mul_f32 v189, v161, v189\n"
      "v_mul_f32 v188, v161, v188\n"
      "v_mul_f32_e64 v186, v161, v186\n"
      "v_cvt_pk_bf16_f32 v185, v186, v187\n"
      "v_cvt_pk_bf16_f32 v186, v188, v189\n"
      "v_mfma_f32_16x16x32_bf16 v[220:223], a[164:167], v[150:153], v[220:223]\n"
      "ds_read_b32 v70, v70\n"
      "v_cvt_pk_bf16_f32 v187, v190, v191\n"
      "v_mfma_f32_16x16x32_bf16 v[220:223], a[200:203], v[236:239], v[220:223]\n"
      "ds_read_b32 v77, v77\n"
      "ds_read_b32 v99, v99\n"
      "ds_read_b32 v72, v72\n"
      "global_load_dwordx4 v[24:27], v149, s[6:7] offset:128\n"
      "ds_read_b32 v113, v113\n"
      "ds_read_b32 v71, v71\n"
      "ds_read_b32 v73, v73\n"
      "ds_read_b32 v112, v112\n"
      "v_mul_f32 v173, v148, v173\n"
      "v_mul_f32 v179, v148, v179\n"
      "v_mul_f32 v178, v148, v178\n"
      "v_mul_f32 v195, v161, v195\n"
      "v_mul_f32 v172, v148, v172\n"
      "v_cvt_pk_bf16_f32 v172, v172, v173\n"
      "v_mul_f32 v174, v148, v174\n"
      "v_mfma_f32_16x16x32_bf16 v[224:227], a[168:171], v[150:153], v[224:227]\n"
      "ds_read_b32 v110, v110\n"
      "v_mul_f32 v176, v148, v176\n"
      "v_mfma_f32_16x16x32_bf16 v[224:227], a[204:207], v[236:239], v[224:227]\n"
      "ds_read_b32 v75, v75\n"
      "v_mul_f32 v177, v148, v177\n"
      "v_mul_f32 v180, v148, v180\n"
      "v_mul_f32 v194, v161, v194\n"
      "v_mul_f32_e64 v183, v148, v183\n"
      "v_cvt_pk_bf16_f32 v34, v192, v193\n"
      "v_mul_f32_e64 v182, v148, v182\n"
      "v_mul_f32 v181, v148, v181\n"
      "v_mfma_f32_16x16x32_bf16 v[232:235], a[176:179], v[150:153], v[232:235]\n"
      "global_load_dwordx4 v[28:31], v149, s[6:7] offset:256\n"
      "s_waitcnt vmcnt(12)\n"
      "v_mul_f32_e64 v175, v148, v175\n"
      "v_cvt_pk_bf16_f32 v32, v180, v181\n"
      "v_mfma_f32_16x16x32_bf16 v[228:231], a[172:175], v[150:153], v[228:231]\n"
      "ds_read_b32 v161, v122\n"
      "ds_read_b128 v[148:151], v119 offset:36864\n"
      "v_cvt_pk_bf16_f32 v173, v174, v175\n"
      "s_waitcnt lgkmcnt(4)\n"
      "s_waitcnt vmcnt(9)\n"
      "v_add_u32 v140, 36864, v251\n"
      "v_cvt_pk_bf16_f32 v35, v194, v195\n"
      "v_mov_b32 v135, v249\n"
      "v_mov_b32_e64 v134, v248\n"
      "v_cvt_pk_bf16_f32 v33, v182, v183\n"
      "s_mov_b32 exec_lo, 0xffff0000\n"
      "s_mov_b32 exec_hi, 0xffffffff\n"
      "v_mov_b32 v70, 0xf149f2ca\n"
      "v_mov_b32 v72, 0xf149f2ca\n"
      "v_mov_b32 v74, 0xf149f2ca\n"
      "s_mov_b64 exec, -1\n"
      "v_mov_b32 v141, -1\n"
      "v_cvt_pk_bf16_f32 v174, v176, v177\n"
      "s_mov_b32 exec_lo, 0x0\n"
      "s_mov_b32 exec_hi, 0xfffe0000\n"
      "v_mov_b32 v77, 0xf149f2ca\n"
      "s_mov_b64 exec, -1\n"
      "ds_write_b32 v129, v141 offset:1024\n"
      "v_cvt_pk_bf16_f32 v175, v178, v179\n"
      "s_mov_b32 s4, 0\n"
      "v_mfma_f32_16x16x32_bf16 v[232:235], a[212:215], v[236:239], v[232:235]\n"
      "ds_write_b32 v129, v141 offset:3072\n"
      "ds_write_b32 v129, v141\n"
      "s_waitcnt lgkmcnt(3)\n"
      "v_bitop3_b32 v161, v161, v151, s40 bitop3:0xa8\n"
      "v_or3_b32 v148, v148, v149, v150\n"
      "v_and_b32 v148, s40, v148\n"
      "v_or3_b32 v148, v148, v161, s4\n"
      "v_mfma_f32_16x16x32_bf16 v[228:231], a[208:211], v[236:239], v[228:231]\n"
      "s_mov_b32 s5, 0\n"
      "ds_write_b32 v129, v148 offset:2048\n"
      "v_add_u32 v143, 36864, v250\n"
      "s_waitcnt lgkmcnt(0)\n"
      "s_barrier\n"
      "v_fma_f32 v161, v136, v136, s38\n"
      "v_fma_f32 v141, v212, v212, s38\n"
      "v_fmac_f32 v161, v137, v137\n"
      "v_fmac_f32 v141, v213, v213\n"
      "v_cvt_pk_bf16_f32 v204, v204, v205\n"
      "v_fmac_f32 v161, v138, v138\n"
      "v_fmac_f32 v141, v214, v214\n"
      "v_fmac_f32 v161, v139, v139\n"
      "v_fmac_f32 v141, v215, v215\n"
      "v_fmac_f32 v161, v156, v156\n"
      "v_fmac_f32 v161, v157, v157\n"
      "v_fmac_f32 v161, v158, v158\n"
      "v_fmac_f32 v141, v216, v216\n"
      "v_fmac_f32 v161, v159, v159\n"
      "v_fmac_f32 v141, v217, v217\n"
      "v_fmac_f32 v141, v218, v218\n"
      "v_fmac_f32 v141, v219, v219\n"
      "v_fmac_f32 v161, v208, v208\n"
      "v_fmac_f32 v161, v209, v209\n"
      "v_cvt_pk_bf16_f32 v196, v196, v197\n"
      "v_fmac_f32 v141, v220, v220\n"
      "v_fmac_f32 v141, v221, v221\n"
      "v_fmac_f32 v141, v222, v222\n"
      "v_fmac_f32 v161, v210, v210\n"
      "v_fmac_f32 v141, v223, v223\n"
      "v_fmac_f32 v161, v211, v211\n"
      "v_cvt_pk_bf16_f32 v200, v200, v201\n"
      "v_cvt_pk_bf16_f32 v197, v198, v199\n"
      "v_cvt_pk_bf16_f32 v201, v202, v203\n"
      "v_permlane16_swap_b32 v161, v141\n"
      "v_add_f32 v161, v161, v141\n"
      "v_mov_b32 v141, v161\n"
      "s_mov_b32 s27, 0\n"
      "ds_read_b128 v[148:151], v114\n"
      "v_permlane32_swap_b32 v161, v141\n"
      "v_add_f32 v161, v161, v141\n"
      "v_rsq_f32 v161, v161\n"
      "v_cvt_pk_bf16_f32 v205, v206, v207\n"
      "v_mov_b32_e64 v141, v161\n"
      "s_add_u32 s28, s27, 1\n"
      "ds_read_b128 v[180:183], v128 offset:2048\n"
      "ds_read_b128 v[152:155], v115\n"
      "v_permlane16_swap_b32 v161, v141\n"
      "v_mul_f32 v219, v141, v219\n"
      "v_mul_f32 v220, v141, v220\n"
      "s_min_u32 m0, s28, s3\n"
      "v_mul_f32 v161, s36, v161\n"
      "v_mul_f32 v223, v141, v223\n"
      "v_mul_f32 v216, v141, v216\n"
      "s_movrels_b32 s9, s80\n"
      "v_mul_f32 v159, v161, v159\n"
      "v_mul_f32 v212, v141, v212\n"
      "s_lshr_b32 s8, s9, 8\n"
      "v_mul_f32 v136, v161, v136\n"
      "v_mul_f32 v215, v141, v215\n"
      "v_mul_f32 v156, v161, v156\n"
      "v_mul_f32_e64 v213, v141, v213\n"
      "v_mul_f32_e64 v137, v161, v137\n"
      "v_mul_f32_e64 v214, v141, v214\n"
      "v_mul_f32_e64 v218, v141, v218\n"
      "v_cvt_pk_bf16_f32 v212, v212, v213\n"
      "s_and_b32 s9, s9, 0xff\n"
      "ds_write_b128 v162, v[172:175]\n"
      "v_cvt_pk_bf16_f32 v213, v214, v215\n"
      "v_mul_f32 v222, v141, v222\n"
      "v_cvt_pk_bf16_f32 v136, v136, v137\n"
      "v_cvt_pk_bf16_f32 v215, v218, v219\n"
      "s_mul_i32 s10, s8, 73\n"
      "s_waitcnt lgkmcnt(1)\n"
      "ds_write_b128 v162, v[32:35] offset:16\n"
      "v_cvt_pk_bf16_f32 v41, v222, v223\n"
      "v_mul_f32 v139, v161, v139\n"
      "s_add_u32 s10, s10, s9\n"
      "ds_write_b128 v162, v[184:187] offset:32\n"
      "s_mul_i32 s28, s10, 4802\n"
      "v_mul_f32_e64 v221, v141, v221\n"
      "v_mfma_f32_16x16x32_bf16 v[184:187], a[0:3], v[148:151], 0\n"
      "ds_read_b128 v[172:175], v114 offset:6144\n"
      "v_mul_f32 v157, v161, v157\n"
      "v_mfma_f32_16x16x32_bf16 v[184:187], a[36:39], v[152:155], v[184:187]\n"
      "ds_write_b64 v163, v[196:197]\n"
      "v_mfma_f32_16x16x32_bf16 v[196:199], a[4:7], v[148:151], 0\n"
      "s_add_u32 s56, s18, s28\n"
      "ds_read_b128 v[164:167], v115 offset:6144\n"
      "v_mfma_f32_16x16x32_bf16 v[196:199], a[40:43], v[152:155], v[196:199]\n"
      "ds_write_b64 v163, v[200:201] offset:32\n"
      "v_mfma_f32_16x16x32_bf16 v[200:203], a[8:11], v[148:151], 0\n"
      "s_addc_u32 s57, s19, 0\n"
      "s_and_b32 s56, s56, -16\n"
      "s_sub_u32 s58, s56, 110592\n"
      "v_mfma_f32_16x16x32_bf16 v[200:203], a[44:47], v[152:155], v[200:203]\n"
      "ds_write_b64 v163, v[204:205] offset:64\n"
      "v_mul_f32 v138, v161, v138\n"
      "v_cvt_pk_bf16_f32 v137, v138, v139\n"
      "v_mfma_f32_16x16x32_bf16 v[176:179], a[12:15], v[148:151], 0\n"
      "s_subb_u32 s59, s57, 0\n"
      "v_mul_f32_e64 v158, v161, v158\n"
      "v_cvt_pk_bf16_f32 v138, v156, v157\n"
      "v_mfma_f32_16x16x32_bf16 v[176:179], a[48:51], v[152:155], v[176:179]\n"
      "s_sub_u32 s56, s56, 73728\n"
      "v_mul_f32_e64 v217, v141, v217\n"
      "v_cvt_pk_bf16_f32 v139, v158, v159\n"
      "v_cvt_pk_bf16_f32 v214, v216, v217\n"
      "v_mfma_f32_16x16x32_bf16 v[156:159], a[16:19], v[148:151], 0\n"
      "s_subb_u32 s57, s57, 0\n"
      "v_mul_f32 v209, v161, v209\n"
      "v_mul_f32 v208, v161, v208\n"
      "v_mfma_f32_16x16x32_bf16 v[156:159], a[52:55], v[152:155], v[156:159]\n"
      "s_waitcnt lgkmcnt(2)\n"
      "ds_read_b128 v[192:195], v114 offset:12288\n"
      "v_mfma_f32_16x16x32_bf16 v[188:191], a[20:23], v[148:151], 0\n"
      "v_cvt_pk_bf16_f32 v38, v208, v209\n"
      "v_cvt_pk_bf16_f32 v40, v220, v221\n"
      "v_mfma_f32_16x16x32_bf16 v[188:191], a[56:59], v[152:155], v[188:191]\n"
      "ds_read_b128 v[168:171], v115 offset:12288\n"
      "v_mul_f32 v211, v161, v211\n"
      "v_mfma_f32_16x16x32_bf16 v[220:223], v[148:151], a[24:27], 0\n"
      "s_mul_i32 s28, s8, 1373568\n"
      "v_cvt_pk_bf16_f32 v224, v224, v225\n"
      "v_mul_f32 v210, v161, v210\n"
      "v_mfma_f32_16x16x32_bf16 v[220:223], v[152:155], a[60:63], v[220:223]\n"
      "s_mul_i32 s29, s9, 2688\n"
      "v_cvt_pk_bf16_f32 v228, v228, v229\n"
      "v_cvt_pk_bf16_f32 v39, v210, v211\n"
      "v_mfma_f32_16x16x32_bf16 v[208:211], v[148:151], a[28:31], 0\n"
      "v_cvt_pk_bf16_f32 v232, v232, v233\n"
      "v_cvt_pk_bf16_f32 v229, v230, v231\n"
      "v_mfma_f32_16x16x32_bf16 v[208:211], v[152:155], a[64:67], v[208:211]\n"
      "s_add_u32 s28, s28, s29\n"
      "s_cmp_eq_u32 s8, 72\n"
      "s_cselect_b32 s8, 0, 100270464\n"
      "v_mfma_f32_16x16x32_bf16 v[216:219], v[148:151], a[32:35], 0\n"
      "s_cmp_eq_u32 s9, 72\n"
      "s_cselect_b32 s9, 0, 196224\n"
      "v_cvt_pk_bf16_f32 v225, v226, v227\n"
      "v_mfma_f32_16x16x32_bf16 v[216:219], v[152:155], a[68:71], v[216:219]\n"
      "s_add_u32 s10, s8, s9\n"
      "v_bitop3_b32 v182, v182, v183, s4 bitop3:0x7e\n"
      "s_add_u32 s28, s28, s10\n"
      "v_mfma_f32_16x16x32_bf16 v[184:187], a[72:75], v[172:175], v[184:187]\n"
      "v_bitop3_b32 v180, v180, v181, s4 bitop3:0x7e\n"
      "v_mfma_f32_16x16x32_bf16 v[184:187], a[108:111], v[164:167], v[184:187]\n"
      "s_add_u32 s52, s16, s28\n"
      "s_waitcnt lgkmcnt(0)\n"
      "s_addc_u32 s53, s17, 0\n"
      "v_mfma_f32_16x16x32_bf16 v[156:159], a[88:91], v[172:175], v[156:159]\n"
      "s_cmp_lg_u32 s10, s11\n"
      "v_or_b32 v180, v180, v182\n"
      "v_cvt_pk_bf16_f32 v233, v234, v235\n"
      "v_mfma_f32_16x16x32_bf16 v[196:199], a[76:79], v[172:175], v[196:199]\n"
      "s_cbranch_scc1 L_edge_6_%=\n"
      "L_eb_7_%=:\n"
      "v_mfma_f32_16x16x32_bf16 v[196:199], a[112:115], v[164:167], v[196:199]\n"
      "v_cmp_ne_u32 vcc, 0, v180\n"
      "s_add_u32 m0, s76, 18432\n"
      "v_mfma_f32_16x16x32_bf16 v[200:203], a[80:83], v[172:175], v[200:203]\n"
      "global_load_lds_dwordx4 v116, s[52:53]\n"
      "v_mfma_f32_16x16x32_bf16 v[200:203], a[116:119], v[164:167], v[200:203]\n"
      "s_mov_b32 exec_lo, 0x003f003f\n"
      "s_mov_b32 exec_hi, 0x003f003f\n"
      "ds_write_b128 v162, v[136:139] offset:18688\n"
      "ds_write_b128 v162, v[38:41] offset:18704\n"
      "ds_write_b128 v162, v[212:215] offset:18720\n"
      "ds_write_b64 v163, v[224:225] offset:18688\n"
      "ds_write_b64 v163, v[228:229] offset:18720\n"
      "ds_write_b64 v163, v[232:233] offset:18752\n"
      "s_mov_b64 exec, -1\n"
      "v_mfma_f32_16x16x32_bf16 v[176:179], a[84:87], v[172:175], v[176:179]\n"
      "ds_read_b128 v[152:155], v115 offset:2048\n"
      "v_mfma_f32_16x16x32_bf16 v[176:179], a[120:123], v[164:167], v[176:179]\n"
      "s_mov_b64 s[48:49], vcc\n"
      "s_add_u32 m0, s76, 24448\n"
      "v_mfma_f32_16x16x32_bf16 v[156:159], a[124:127], v[164:167], v[156:159]\n"
      "global_load_lds_dwordx4 v116, s[52:53] offset:128\n"
      "v_mfma_f32_16x16x32_bf16 v[188:191], a[92:95], v[172:175], v[188:191]\n"
      "ds_read_b128 v[136:139], v114 offset:2048\n"
      "v_mfma_f32_16x16x32_bf16 v[188:191], a[128:131], v[164:167], v[188:191]\n"
      "s_nop 0\n"
      "v_mfma_f32_16x16x32_bf16 v[148:151], v[144:147], a[216:219], 0\n"
      "s_nop 0\n"
      "v_mfma_f32_16x16x32_bf16 v[148:151], v[144:147], a[220:223], v[148:151]\n"
      "s_nop 0\n"
      "v_mfma_f32_16x16x32_bf16 v[148:151], v[144:147], a[224:227], v[148:151]\n"
      "s_cbranch_vccnz L_flg_3_%=\n"
      "L_fb_4_%=:\n"
      "v_mfma_f32_16x16x32_bf16 v[220:223], v[172:175], a[96:99], v[220:223]\n"
      "s_add_u32 m0, s76, 30464\n"
      "v_mfma_f32_16x16x32_bf16 v[220:223], v[164:167], a[132:135], v[220:223]\n"
      "global_load_lds_dwordx4 v116, s[52:53] offset:256\n"
      "v_mfma_f32_16x16x32_bf16 v[208:211], v[172:175], a[100:103], v[208:211]\n"
      "s_nop 1\n"
      "v_add_f32_dpp v148, v148, v148 quad_perm:[1,0,3,2] row_mask:0xf bank_mask:0xf\n"
      "v_mfma_f32_16x16x32_bf16 v[208:211], v[164:167], a[136:139], v[208:211]\n"
      "s_nop 0\n"
      "v_add_f32_dpp v148, v148, v148 quad_perm:[2,3,0,1] row_mask:0xf bank_mask:0xf\n"
      "v_mfma_f32_16x16x32_bf16 v[216:219], v[172:175], a[104:107], v[216:219]\n"
      "s_nop 0\n"
      "v_add_f32_dpp v148, v148, v148 row_half_mirror row_mask:0xf bank_mask:0xf\n"
      "v_mfma_f32_16x16x32_bf16 v[216:219], v[164:167], a[140:143], v[216:219]\n"
      "s_nop 0\n"
      "v_mfma_f32_16x16x32_bf16 v[184:187], a[144:147], v[192:195], v[184:187]\n"
      "v_mul_f32 v150, 0xbbaaaaab, v148\n"
      "v_mul_f32 v149, 0xbbaaaaab, v148\n"
      "v_mfma_f32_16x16x32_bf16 v[184:187], a[180:183], v[168:171], v[184:187]\n"
      "v_mul_f32 v151, 0xbbaaaaab, v148\n"
      "v_mul_f32 v148, 0xbbaaaaab, v148\n"
      "v_mfma_f32_16x16x32_bf16 v[196:199], a[148:151], v[192:195], v[196:199]\n"
      "v_mfma_f32_16x16x32_bf16 v[196:199], a[184:187], v[168:171], v[196:199]\n"
      "v_mfma_f32_16x16x32_bf16 v[164:167], v[242:245], a[216:219], v[148:151]\n"
      "s_nop 0\n"
      "v_mfma_f32_16x16x32_bf16 v[172:175], v[240:243], a[216:219], v[148:151]\n"
      "s_waitcnt lgkmcnt(0)\n"
      "v_mfma_f32_16x16x32_bf16 v[180:183], v[242:245], a[220:223], v[148:151]\n"
      "s_nop 0\n"
      "v_fma_f32 v141, v184, v184, s38\n"
      "s_add_u32 m0, s77, 18432\n"
      "v_mfma_f32_16x16x32_bf16 v[212:215], v[240:243], a[220:223], v[148:151]\n"
      "s_nop 1\n"
      "v_cvt_pk_bf16_f32 v164, v164, v165\n"
      "v_mfma_f32_16x16x32_bf16 v[232:235], v[242:245], a[224:227], v[148:151]\n"
      "v_cvt_pk_bf16_f32 v165, v166, v167\n"
      "v_cvt_pk_bf16_f32 v166, v172, v173\n"
      "v_mfma_f32_16x16x32_bf16 v[228:231], v[240:243], a[224:227], v[148:151]\n"
      "v_cvt_pk_bf16_f32 v167, v174, v175\n"
      "v_cvt_pk_bf16_f32 v180, v180, v181\n"
      "v_mfma_f32_16x16x32_bf16 v[200:203], a[152:155], v[192:195], v[200:203]\n"
      "v_cvt_pk_bf16_f32 v181, v182, v183\n"
      "v_cvt_pk_bf16_f32 v182, v212, v213\n"
      "v_mfma_f32_16x16x32_bf16 v[200:203], a[188:191], v[168:171], v[200:203]\n"
      "ds_write_b128 v160, v[164:167]\n"
      "v_mfma_f32_16x16x32_bf16 v[148:151], v[144:147], a[228:231], 0\n"
      "global_load_lds_dwordx4 v117, s[52:53]\n"
      "v_mfma_f32_16x16x32_bf16 v[148:151], v[144:147], a[232:235], v[148:151]\n"
      "s_add_u32 m0, s78, 18432\n"
      "v_cvt_pk_bf16_f32 v232, v232, v233\n"
      "v_cvt_pk_bf16_f32 v183, v214, v215\n"
      "v_mfma_f32_16x16x32_bf16 v[176:179], a[156:159], v[192:195], v[176:179]\n"
      "v_cvt_pk_bf16_f32 v233, v234, v235\n"
      "v_cvt_pk_bf16_f32 v234, v228, v229\n"
      "v_mfma_f32_16x16x32_bf16 v[176:179], a[192:195], v[168:171], v[176:179]\n"
      "ds_write_b128 v160, v[180:183] offset:128\n"
      "v_mfma_f32_16x16x32_bf16 v[148:151], v[144:147], a[236:239], v[148:151]\n"
      "v_fmac_f32 v141, v185, v185\n"
      "v_cvt_pk_bf16_f32 v235, v230, v231\n"
      "v_mfma_f32_16x16x32_bf16 v[156:159], a[160:163], v[192:195], v[156:159]\n"
      "v_fmac_f32 v141, v186, v186\n"
      "v_fmac_f32_e64 v141, v187, v187\n"
      "v_mfma_f32_16x16x32_bf16 v[156:159], a[196:199], v[168:171], v[156:159]\n"
      "ds_write_b128 v160, v[232:235] offset:256\n"
      "v_mfma_f32_16x16x32_bf16 v[188:191], a[164:167], v[192:195], v[188:191]\n"
      "v_add_f32_dpp v148, v148, v148 quad_perm:[1,0,3,2] row_mask:0xf bank_mask:0xf\n"
      "v_fma_f32 v142, v176, v176, s38\n"
      "v_mfma_f32_16x16x32_bf16 v[188:191], a[200:203], v[168:171], v[188:191]\n"
      "v_add_f32_dpp v148, v148, v148 quad_perm:[2,3,0,1] row_mask:0xf bank_mask:0xf\n"
      "v_fmac_f32_e64 v142, v177, v177\n"
      "v_mfma_f32_16x16x32_bf16 v[220:223], v[192:195], a[168:171], v[220:223]\n"
      "v_add_f32_dpp v148, v148, v148 row_half_mirror row_mask:0xf bank_mask:0xf\n"
      "v_mul_f32 v151, 0xbbaaaaab, v148\n"
      "v_mfma_f32_16x16x32_bf16 v[220:223], v[168:171], a[204:207], v[220:223]\n"
      "v_mul_f32 v149, 0xbbaaaaab, v148\n"
      "v_mul_f32 v150, 0xbbaaaaab, v148\n"
      "v_mfma_f32_16x16x32_bf16 v[208:211], v[192:195], a[172:175], v[208:211]\n"
      "v_fmac_f32_e64 v142, v178, v178\n"
      "v_mul_f32 v148, 0xbbaaaaab, v148\n"
      "v_mfma_f32_16x16x32_bf16 v[208:211], v[168:171], a[208:211], v[208:211]\n"
      "v_fmac_f32 v141, v196, v196\n"
      "v_fmac_f32 v142, v179, v179\n"
      "v_mfma_f32_16x16x32_bf16 v[164:167], v[242:245], a[228:231], v[148:151]\n"
      "v_fmac_f32 v141, v197, v197\n"
      "v_fmac_f32 v142, v156, v156\n"
      "v_mfma_f32_16x16x32_bf16 v[180:183], v[240:243], a[228:231], v[148:151]\n"
      "ds_read_b128 v[172:175], v114 offset:8192\n"
      "v_fmac_f32_e64 v142, v157, v157\n"
      "v_mfma_f32_16x16x32_bf16 v[232:235], v[242:245], a[232:235], v[148:151]\n"
      "v_fmac_f32 v141, v198, v198\n"
      "v_fmac_f32 v142, v158, v158\n"
      "v_mfma_f32_16x16x32_bf16 v[228:231], v[240:243], a[232:235], v[148:151]\n"
      "v_cvt_pk_bf16_f32 v164, v164, v165\n"
      "v_cvt_pk_bf16_f32 v165, v166, v167\n"
      "v_mfma_f32_16x16x32_bf16 v[212:215], v[242:245], a[236:239], v[148:151]\n"
      "v_cvt_pk_bf16_f32 v166, v180, v181\n"
      "v_cvt_pk_bf16_f32 v167, v182, v183\n"
      "v_mfma_f32_16x16x32_bf16 v[180:183], v[240:243], a[236:239], v[148:151]\n"
      "ds_write_b128 v160, v[164:167] offset:3072\n"
      "v_mfma_f32_16x16x32_bf16 v[216:219], v[192:195], a[176:179], v[216:219]\n"
      "ds_read_b128 v[164:167], v115 offset:8192\n"
      "v_cvt_pk_bf16_f32 v232, v232, v233\n"
      "v_mfma_f32_16x16x32_bf16 v[216:219], v[168:171], a[212:215], v[216:219]\n"
      "v_cvt_pk_bf16_f32 v233, v234, v235\n"
      "v_cvt_pk_bf16_f32 v234, v228, v229\n"
      "v_mfma_f32_16x16x32_bf16 v[148:151], a[0:3], v[136:139], 0\n"
      "v_cvt_pk_bf16_f32 v212, v212, v213\n"
      "v_cvt_pk_bf16_f32 v235, v230, v231\n"
      "v_mfma_f32_16x16x32_bf16 v[148:151], a[36:39], v[152:155], v[148:151]\n"
      "ds_write_b128 v160, v[232:235] offset:3200\n"
      "v_mfma_f32_16x16x32_bf16 v[168:171], a[4:7], v[136:139], 0\n"
      "v_cvt_pk_bf16_f32 v213, v214, v215\n"
      "v_cvt_pk_bf16_f32 v214, v180, v181\n"
      "v_mfma_f32_16x16x32_bf16 v[168:171], a[40:43], v[152:155], v[168:171]\n"
      "s_mov_b32 exec_hi, 0\n"
      "global_load_lds_dwordx4 v118, s[52:53]\n"
      "s_mov_b32 exec_hi, -1\n"
      "v_mfma_f32_16x16x32_bf16 v[192:195], a[8:11], v[136:139], 0\n"
      "v_cvt_pk_bf16_f32 v215, v182, v183\n"
      "v_fmac_f32 v141, v199, v199\n"
      "v_mfma_f32_16x16x32_bf16 v[192:195], a[44:47], v[152:155], v[192:195]\n"
      "v_fmac_f32 v142, v159, v159\n"
      "v_fmac_f32_e64 v141, v200, v200\n"
      "v_mfma_f32_16x16x32_bf16 v[180:183], a[12:15], v[136:139], 0\n"
      "v_fmac_f32 v141, v201, v201\n"
      "v_fmac_f32 v141, v202, v202\n"
      "v_mfma_f32_16x16x32_bf16 v[180:183], a[48:51], v[152:155], v[180:183]\n"
      "ds_write_b128 v160, v[212:215] offset:3328\n"
      "v_mfma_f32_16x16x32_bf16 v[212:215], a[16:19], v[136:139], 0\n"
      "v_fmac_f32 v141, v203, v203\n"
      "v_fmac_f32 v142, v188, v188\n"
      "v_mfma_f32_16x16x32_bf16 v[212:215], a[52:55], v[152:155], v[212:215]\n"
      "v_fmac_f32 v142, v189, v189\n"
      "v_fmac_f32 v142, v190, v190\n"
      "v_mfma_f32_16x16x32_bf16 v[232:235], a[20:23], v[136:139], 0\n"
      "s_waitcnt lgkmcnt(1)\n"
      "v_fmac_f32 v142, v191, v191\n"
      "v_mfma_f32_16x16x32_bf16 v[232:235], a[56:59], v[152:155], v[232:235]\n"
      "s_nop 0\n"
      "v_permlane16_swap_b32 v141, v142\n"
      "v_mfma_f32_16x16x32_bf16 v[228:231], v[136:139], a[24:27], 0\n"
      "v_add_f32 v141, v141, v142\n"
      "v_mov_b32 v142, v141\n"
      "v_mfma_f32_16x16x32_bf16 v[228:231], v[152:155], a[60:63], v[228:231]\n"
      "s_nop 0\n"
      "v_permlane32_swap_b32 v141, v142\n"
      "v_mfma_f32_16x16x32_bf16 v[224:227], v[136:139], a[28:31], 0\n"
      "v_add_f32 v141, v141, v142\n"
      "v_mfma_f32_16x16x32_bf16 v[224:227], v[152:155], a[64:67], v[224:227]\n"
      "v_rsq_f32 v141, v141\n"
      "v_mfma_f32_16x16x32_bf16 v[148:151], a[72:75], v[172:175], v[148:151]\n"
      "v_mov_b32 v142, v141\n"
      "v_mfma_f32_16x16x32_bf16 v[148:151], a[108:111], v[164:167], v[148:151]\n"
      "s_add_u32 m0, s76, 115456\n"
      "v_permlane16_swap_b32 v141, v142\n"
      "v_mfma_f32_16x16x32_bf16 v[168:171], a[76:79], v[172:175], v[168:171]\n"
      "global_load_lds_dwordx4 v119, s[56:57]\n"
      "v_mfma_f32_16x16x32_bf16 v[168:171], a[112:115], v[164:167], v[168:171]\n"
      "v_mul_f32 v176, v142, v176\n"
      "v_mul_f32 v177, v142, v177\n"
      "v_mfma_f32_16x16x32_bf16 v[192:195], a[80:83], v[172:175], v[192:195]\n"
      "v_cvt_pk_bf16_f32 v176, v176, v177\n"
      "v_mul_f32 v178, v142, v178\n"
      "v_mfma_f32_16x16x32_bf16 v[192:195], a[116:119], v[164:167], v[192:195]\n"
      "v_mul_f32 v179, v142, v179\n"
      "v_cvt_pk_bf16_f32 v177, v178, v179\n"
      "v_mfma_f32_16x16x32_bf16 v[180:183], a[84:87], v[172:175], v[180:183]\n"
      "v_mul_f32 v157, v142, v157\n"
      "v_mfma_f32_16x16x32_bf16 v[180:183], a[120:123], v[164:167], v[180:183]\n"
      "s_add_u32 m0, s79, 4864\n"
      "v_mul_f32 v158, v142, v158\n"
      "v_mul_f32 v156, v142, v156\n"
      "v_mfma_f32_16x16x32_bf16 v[204:207], v[136:139], a[32:35], 0\n"
      "ds_read_b128 v[136:139], v114 offset:14336\n"
      "v_mul_f32 v159, v142, v159\n"
      "v_mfma_f32_16x16x32_bf16 v[204:207], v[152:155], a[68:71], v[204:207]\n"
      "v_cvt_pk_bf16_f32 v178, v156, v157\n"
      "v_cvt_pk_bf16_f32 v179, v158, v159\n"
      "v_mfma_f32_16x16x32_bf16 v[212:215], a[88:91], v[172:175], v[212:215]\n"
      "v_mul_f32 v188, v142, v188\n"
      "v_mul_f32 v189, v142, v189\n"
      "v_mfma_f32_16x16x32_bf16 v[212:215], a[124:127], v[164:167], v[212:215]\n"
      "ds_read_b128 v[152:155], v115 offset:14336\n"
      "v_mul_f32 v190, v142, v190\n"
      "v_mfma_f32_16x16x32_bf16 v[156:159], v[144:147], a[240:243], 0\n"
      "v_mul_f32 v141, s36, v141\n"
      "v_mul_f32 v191, v142, v191\n"
      "v_mfma_f32_16x16x32_bf16 v[156:159], v[144:147], a[244:247], v[156:159]\n"
      "v_mfma_f32_16x16x32_bf16 v[156:159], v[144:147], a[248:251], v[156:159]\n"
      "v_mul_f32 v184, v141, v184\n"
      "v_mul_f32_e64 v186, v141, v186\n"
      "v_mfma_f32_16x16x32_bf16 v[232:235], a[92:95], v[172:175], v[232:235]\n"
      "v_mul_f32 v185, v141, v185\n"
      "v_cvt_pk_bf16_f32 v184, v184, v185\n"
      "v_mfma_f32_16x16x32_bf16 v[232:235], a[128:131], v[164:167], v[232:235]\n"
      "v_mul_f32 v196, v141, v196\n"
      "v_mfma_f32_16x16x32_bf16 v[228:231], v[172:175], a[96:99], v[228:231]\n"
      "v_mul_f32 v202, v141, v202\n"
      "v_mul_f32 v187, v141, v187\n"
      "v_mfma_f32_16x16x32_bf16 v[228:231], v[164:167], a[132:135], v[228:231]\n"
      "v_add_f32_dpp v156, v156, v156 quad_perm:[1,0,3,2] row_mask:0xf bank_mask:0xf\n"
      "v_cvt_pk_bf16_f32 v35, v190, v191\n"
      "v_mfma_f32_16x16x32_bf16 v[224:227], v[172:175], a[100:103], v[224:227]\n"
      "v_mul_f32 v203, v141, v203\n"
      "v_mul_f32 v198, v141, v198\n"
      "v_mfma_f32_16x16x32_bf16 v[224:227], v[164:167], a[136:139], v[224:227]\n"
      "s_waitcnt lgkmcnt(0)\n"
      "v_mul_f32 v201, v141, v201\n"
      "v_mfma_f32_16x16x32_bf16 v[204:207], v[172:175], a[104:107], v[204:207]\n"
      "v_mul_f32_e64 v199, v141, v199\n"
      "v_mul_f32_e64 v200, v141, v200\n"
      "v_mfma_f32_16x16x32_bf16 v[204:207], v[164:167], a[140:143], v[204:207]\n"
      "global_load_lds_dword v122, s[58:59]\n"
      "v_mfma_f32_16x16x32_bf16 v[148:151], a[144:147], v[136:139], v[148:151]\n"
      "v_cvt_pk_bf16_f32 v34, v188, v189\n"
      "v_cvt_pk_bf16_f32 v185, v186, v187\n"
      "v_mfma_f32_16x16x32_bf16 v[148:151], a[180:183], v[152:155], v[148:151]\n"
      "v_mul_f32_e64 v197, v141, v197\n"
      "v_mfma_f32_16x16x32_bf16 v[168:171], a[148:151], v[136:139], v[168:171]\n"
      "s_nop 0\n"
      "v_add_f32_dpp v156, v156, v156 quad_perm:[2,3,0,1] row_mask:0xf bank_mask:0xf\n"
      "v_mfma_f32_16x16x32_bf16 v[168:171], a[184:187], v[152:155], v[168:171]\n"
      "s_nop 0\n"
      "v_add_f32_dpp v156, v156, v156 row_half_mirror row_mask:0xf bank_mask:0xf\n"
      "v_mul_f32 v159, 0xbbaaaaab, v156\n"
      "s_waitcnt vmcnt(15)\n"
      "v_mfma_f32_16x16x32_bf16 v[164:167], v[144:147], a[252:255], 0\n"
      "s_nop 0\n"
      "v_cvt_pk_bf16_f32 v33, v202, v203\n"
      "s_waitcnt vmcnt(14)\n"
      "v_mfma_f32_16x16x32_bf16 v[164:167], v[144:147], v[0:3], v[164:167]\n"
      "s_nop 0\n"
      "v_cvt_pk_bf16_f32 v187, v198, v199\n"
      "v_cvt_pk_bf16_f32 v186, v196, v197\n"
      "s_waitcnt vmcnt(13)\n"
      "v_mfma_f32_16x16x32_bf16 v[164:167], v[144:147], v[4:7], v[164:167]\n"
      "s_nop 0\n"
      "v_mul_f32 v157, 0xbbaaaaab, v156\n"
      "v_mfma_f32_16x16x32_bf16 v[192:195], a[152:155], v[136:139], v[192:195]\n"
      "s_nop 0\n"
      "v_mul_f32 v158, 0xbbaaaaab, v156\n"
      "v_mul_f32 v156, 0xbbaaaaab, v156\n"
      "v_mfma_f32_16x16x32_bf16 v[192:195], a[188:191], v[152:155], v[192:195]\n"
      "s_nop 0\n"
      "v_mfma_f32_16x16x32_bf16 v[196:199], v[242:245], a[240:243], v[156:159]\n"
      "v_mfma_f32_16x16x32_bf16 v[188:191], v[240:243], a[240:243], v[156:159]\n"
      "v_mfma_f32_16x16x32_bf16 v[180:183], a[156:159], v[136:139], v[180:183]\n"
      "ds_read_b128 v[172:175], v115 offset:4096\n"
      "v_cvt_pk_bf16_f32 v32, v200, v201\n"
      "v_mfma_f32_16x16x32_bf16 v[180:183], a[192:195], v[152:155], v[180:183]\n"
      "ds_read_b128 v[200:203], v114 offset:4096\n"
      "v_mfma_f32_16x16x32_bf16 v[212:215], a[160:163], v[136:139], v[212:215]\n"
      "s_nop 0\n"
      "v_cvt_pk_bf16_f32 v196, v196, v197\n"
      "v_mfma_f32_16x16x32_bf16 v[212:215], a[196:199], v[152:155], v[212:215]\n"
      "s_nop 0\n"
      "v_add_f32_dpp v164, v164, v164 quad_perm:[1,0,3,2] row_mask:0xf bank_mask:0xf\n"
      "v_cvt_pk_bf16_f32 v197, v198, v199\n"
      "v_mfma_f32_16x16x32_bf16 v[232:235], a[164:167], v[136:139], v[232:235]\n"
      "v_cvt_pk_bf16_f32 v199, v190, v191\n"
      "v_mfma_f32_16x16x32_bf16 v[232:235], a[200:203], v[152:155], v[232:235]\n"
      "v_cvt_pk_bf16_f32 v198, v188, v189\n"
      "v_cvt_pk_bf16_f32 v220, v220, v221\n"
      "v_mfma_f32_16x16x32_bf16 v[188:191], v[242:245], a[244:247], v[156:159]\n"
      "ds_write_b128 v160, v[196:199] offset:6144\n"
      "v_mfma_f32_16x16x32_bf16 v[196:199], v[240:243], a[244:247], v[156:159]\n"
      "v_cvt_pk_bf16_f32 v221, v222, v223\n"
      "v_add_f32_dpp v164, v164, v164 quad_perm:[2,3,0,1] row_mask:0xf bank_mask:0xf\n"
      "v_mfma_f32_16x16x32_bf16 v[228:231], v[136:139], a[168:171], v[228:231]\n"
      "s_nop 0\n"
      "v_add_f32_dpp v164, v164, v164 row_half_mirror row_mask:0xf bank_mask:0xf\n"
      "v_mul_f32 v167, 0xbbaaaaab, v164\n"
      "v_mfma_f32_16x16x32_bf16 v[228:231], v[152:155], a[204:207], v[228:231]\n"
      "s_waitcnt lgkmcnt(1)\n"
      "v_cvt_pk_bf16_f32 v188, v188, v189\n"
      "v_cvt_pk_bf16_f32 v189, v190, v191\n"
      "v_mfma_f32_16x16x32_bf16 v[224:227], v[136:139], a[172:175], v[224:227]\n"
      "v_cvt_pk_bf16_f32 v190, v196, v197\n"
      "v_cvt_pk_bf16_f32 v191, v198, v199\n"
      "v_mfma_f32_16x16x32_bf16 v[224:227], v[152:155], a[208:211], v[224:227]\n"
      "ds_write_b128 v160, v[188:191] offset:6272\n"
      "v_mfma_f32_16x16x32_bf16 v[188:191], v[242:245], a[248:251], v[156:159]\n"
      "v_mul_f32 v166, 0xbbaaaaab, v164\n"
      "v_mul_f32 v165, 0xbbaaaaab, v164\n"
      "v_mfma_f32_16x16x32_bf16 v[196:199], v[240:243], a[248:251], v[156:159]\n"
      "v_mul_f32 v164, 0xbbaaaaab, v164\n"
      "v_cvt_pk_bf16_f32 v222, v228, v229\n"
      "v_mfma_f32_16x16x32_bf16 v[204:207], v[136:139], a[176:179], v[204:207]\n"
      "v_cvt_pk_bf16_f32 v223, v230, v231\n"
      "v_mfma_f32_16x16x32_bf16 v[204:207], v[152:155], a[212:215], v[204:207]\n"
      "v_cvt_pk_bf16_f32 v188, v188, v189\n"
      "v_cvt_pk_bf16_f32 v189, v190, v191\n"
      "v_mfma_f32_16x16x32_bf16 v[136:139], v[242:245], a[252:255], v[164:167]\n"
      "v_cvt_pk_bf16_f32 v191, v198, v199\n"
      "v_cvt_pk_bf16_f32 v190, v196, v197\n"
      "v_mfma_f32_16x16x32_bf16 v[152:155], v[240:243], a[252:255], v[164:167]\n"
      "ds_write_b128 v160, v[188:191] offset:6400\n"
      "v_mfma_f32_16x16x32_bf16 v[156:159], v[242:245], v[0:3], v[164:167]\n"
      "v_cvt_pk_bf16_f32 v208, v208, v209\n"
      "v_cvt_pk_bf16_f32 v209, v210, v211\n"
      "v_mfma_f32_16x16x32_bf16 v[188:191], v[240:243], v[0:3], v[164:167]\n"
      "v_cvt_pk_bf16_f32 v136, v136, v137\n"
      "v_cvt_pk_bf16_f32 v137, v138, v139\n"
      "v_mfma_f32_16x16x32_bf16 v[196:199], v[242:245], v[4:7], v[164:167]\n"
      "v_cvt_pk_bf16_f32 v138, v152, v153\n"
      "v_cvt_pk_bf16_f32 v139, v154, v155\n"
      "v_mfma_f32_16x16x32_bf16 v[152:155], v[240:243], v[4:7], v[164:167]\n"
      "ds_write_b128 v160, v[136:139] offset:9216\n"
      "v_mfma_f32_16x16x32_bf16 v[136:139], a[0:3], v[200:203], 0\n"
      "ds_read_b128 v[228:231], v114 offset:10240\n"
      "v_cvt_pk_bf16_f32 v156, v156, v157\n"
      "v_mfma_f32_16x16x32_bf16 v[136:139], a[36:39], v[172:175], v[136:139]\n"
      "ds_read_b128 v[164:167], v115 offset:10240\n"
      "v_cvt_pk_bf16_f32 v157, v158, v159\n"
      "v_cvt_pk_bf16_f32 v158, v188, v189\n"
      "v_cvt_pk_bf16_f32 v159, v190, v191\n"
      "v_mfma_f32_16x16x32_bf16 v[188:191], a[4:7], v[200:203], 0\n"
      "v_cvt_pk_bf16_f32 v196, v196, v197\n"
      "v_cvt_pk_bf16_f32 v197, v198, v199\n"
      "v_mfma_f32_16x16x32_bf16 v[188:191], a[40:43], v[172:175], v[188:191]\n"
      "ds_write_b128 v160, v[156:159] offset:9344\n"
      "v_mfma_f32_16x16x32_bf16 v[156:159], a[8:11], v[200:203], 0\n"
      "v_cvt_pk_bf16_f32 v199, v154, v155\n"
      "v_cvt_pk_bf16_f32 v198, v152, v153\n"
      "v_mfma_f32_16x16x32_bf16 v[156:159], a[44:47], v[172:175], v[156:159]\n"
      "ds_write_b128 v160, v[196:199] offset:9472\n"
      "v_mfma_f32_16x16x32_bf16 v[152:155], a[12:15], v[200:203], 0\n"
      "v_cvt_pk_bf16_f32 v211, v226, v227\n"
      "v_cvt_pk_bf16_f32 v210, v224, v225\n"
      "v_mfma_f32_16x16x32_bf16 v[152:155], a[48:51], v[172:175], v[152:155]\n"
      "s_nop 0\n"
      "v_cvt_pk_bf16_f32 v216, v216, v217\n"
      "v_cvt_pk_bf16_f32 v217, v218, v219\n"
      "v_mfma_f32_16x16x32_bf16 v[196:199], a[16:19], v[200:203], 0\n"
      "s_waitcnt lgkmcnt(2)\n"
      "v_cvt_pk_bf16_f32 v219, v206, v207\n"
      "v_cvt_pk_bf16_f32 v218, v204, v205\n"
      "v_mfma_f32_16x16x32_bf16 v[196:199], a[52:55], v[172:175], v[196:199]\n"
      "v_fma_f32 v141, v148, v148, s38\n"
      "v_fma_f32 v142, v180, v180, s38\n"
      "v_mfma_f32_16x16x32_bf16 v[204:207], a[20:23], v[200:203], 0\n"
      "v_fmac_f32 v141, v149, v149\n"
      "v_fmac_f32 v142, v181, v181\n"
      "v_mfma_f32_16x16x32_bf16 v[204:207], a[56:59], v[172:175], v[204:207]\n"
      "v_fmac_f32 v141, v150, v150\n"
      "v_fmac_f32 v142, v182, v182\n"
      "v_mfma_f32_16x16x32_bf16 v[136:139], a[72:75], v[228:231], v[136:139]\n"
      "v_fmac_f32 v141, v151, v151\n"
      "v_fmac_f32 v142, v183, v183\n"
      "v_mfma_f32_16x16x32_bf16 v[136:139], a[108:111], v[164:167], v[136:139]\n"
      "v_fmac_f32 v141, v168, v168\n"
      "v_fmac_f32 v142, v212, v212\n"
      "v_mfma_f32_16x16x32_bf16 v[188:191], a[76:79], v[228:231], v[188:191]\n"
      "v_fmac_f32 v141, v169, v169\n"
      "v_fmac_f32 v142, v213, v213\n"
      "v_mfma_f32_16x16x32_bf16 v[188:191], a[112:115], v[164:167], v[188:191]\n"
      "v_fmac_f32 v141, v170, v170\n"
      "v_fmac_f32 v142, v214, v214\n"
      "v_mfma_f32_16x16x32_bf16 v[156:159], a[80:83], v[228:231], v[156:159]\n"
      "v_fmac_f32 v141, v171, v171\n"
      "v_fmac_f32 v142, v215, v215\n"
      "v_mfma_f32_16x16x32_bf16 v[156:159], a[116:119], v[164:167], v[156:159]\n"
      "v_fmac_f32 v141, v192, v192\n"
      "v_fmac_f32 v142, v232, v232\n"
      "v_mfma_f32_16x16x32_bf16 v[152:155], a[84:87], v[228:231], v[152:155]\n"
      "v_fmac_f32 v141, v193, v193\n"
      "v_fmac_f32 v142, v233, v233\n"
      "v_mfma_f32_16x16x32_bf16 v[152:155], a[120:123], v[164:167], v[152:155]\n"
      "v_fmac_f32 v141, v194, v194\n"
      "v_fmac_f32 v142, v234, v234\n"
      "v_mfma_f32_16x16x32_bf16 v[196:199], a[88:91], v[228:231], v[196:199]\n"
      "v_fmac_f32 v141, v195, v195\n"
      "v_fmac_f32 v142, v235, v235\n"
      "v_mfma_f32_16x16x32_bf16 v[196:199], a[124:127], v[164:167], v[196:199]\n"
      "s_nop 0\n"
      "v_permlane16_swap_b32 v141, v142\n"
      "v_mfma_f32_16x16x32_bf16 v[204:207], a[92:95], v[228:231], v[204:207]\n"
      "v_add_f32 v141, v141, v142\n"
      "v_mov_b32 v142, v141\n"
      "v_mfma_f32_16x16x32_bf16 v[204:207], a[128:131], v[164:167], v[204:207]\n"
      "s_nop 0\n"
      "v_permlane32_swap_b32 v141, v142\n"
      "v_mfma_f32_16x16x32_bf16 v[224:227], v[200:203], a[24:27], 0\n"
      "v_add_f32 v141, v141, v142\n"
      "v_mfma_f32_16x16x32_bf16 v[224:227], v[172:175], a[60:63], v[224:227]\n"
      "v_rsq_f32 v141, v141\n"
      "v_mfma_f32_16x16x32_bf16 v[224:227], v[228:231], a[96:99], v[224:227]\n"
      "v_mov_b32 v142, v141\n"
      "v_mfma_f32_16x16x32_bf16 v[224:227], v[164:167], a[132:135], v[224:227]\n"
      "s_nop 0\n"
      "v_permlane16_swap_b32_e64 v141, v142\n"
      "v_mfma_f32_16x16x32_bf16 v[236:239], v[200:203], a[28:31], 0\n"
      "v_mul_f32 v181, v142, v181\n"
      "v_mul_f32 v180, v142, v180\n"
      "v_mfma_f32_16x16x32_bf16 v[236:239], v[172:175], a[64:67], v[236:239]\n"
      "v_mul_f32_e64 v182, v142, v182\n"
      "v_cvt_pk_bf16_f32 v180, v180, v181\n"
      "v_mfma_f32_16x16x32_bf16 v[236:239], v[228:231], a[100:103], v[236:239]\n"
      "v_mul_f32_e64 v183, v142, v183\n"
      "v_cvt_pk_bf16_f32 v181, v182, v183\n"
      "v_mfma_f32_16x16x32_bf16 v[236:239], v[164:167], a[136:139], v[236:239]\n"
      "v_mul_f32 v212, v142, v212\n"
      "v_mul_f32 v213, v142, v213\n"
      "v_mfma_f32_16x16x32_bf16 v[252:255], v[200:203], a[32:35], 0\n"
      "v_cvt_pk_bf16_f32 v182, v212, v213\n"
      "v_mul_f32 v214, v142, v214\n"
      "v_mfma_f32_16x16x32_bf16 v[252:255], v[172:175], a[68:71], v[252:255]\n"
      "v_mul_f32 v215, v142, v215\n"
      "v_cvt_pk_bf16_f32 v183, v214, v215\n"
      "v_mfma_f32_16x16x32_bf16 v[252:255], v[228:231], a[104:107], v[252:255]\n"
      "ds_read_b128 v[172:175], v114 offset:16384\n"
      "v_mul_f32 v232, v142, v232\n"
      "v_mfma_f32_16x16x32_bf16 v[252:255], v[164:167], a[140:143], v[252:255]\n"
      "ds_read_b128 v[164:167], v115 offset:16384\n"
      "v_mul_f32 v233, v142, v233\n"
      "v_cvt_pk_bf16_f32 v40, v232, v233\n"
      "s_waitcnt vmcnt(12)\n"
      "v_mfma_f32_16x16x32_bf16 v[200:203], v[144:147], v[8:11], 0\n"
      "v_mul_f32 v235, v142, v235\n"
      "v_mul_f32 v234, v142, v234\n"
      "s_waitcnt vmcnt(11)\n"
      "v_mfma_f32_16x16x32_bf16 v[200:203], v[144:147], v[12:15], v[200:203]\n"
      "v_cvt_pk_bf16_f32 v41, v234, v235\n"
      "v_mul_f32 v141, s36, v141\n"
      "s_waitcnt vmcnt(10)\n"
      "v_mfma_f32_16x16x32_bf16 v[200:203], v[144:147], v[16:19], v[200:203]\n"
      "v_mul_f32 v168, v141, v168\n"
      "v_mul_f32 v149, v141, v149\n"
      "v_mul_f32 v171, v141, v171\n"
      "v_mul_f32 v148, v141, v148\n"
      "v_mul_f32 v170, v141, v170\n"
      "v_mul_f32_e64 v169, v141, v169\n"
      "s_waitcnt vmcnt(9)\n"
      "v_mfma_f32_16x16x32_bf16 v[232:235], v[144:147], v[20:23], 0\n"
      "v_mul_f32 v150, v141, v150\n"
      "v_cvt_pk_bf16_f32 v148, v148, v149\n"
      "s_waitcnt vmcnt(8)\n"
      "v_mfma_f32_16x16x32_bf16 v[232:235], v[144:147], v[24:27], v[232:235]\n"
      "v_mul_f32 v151, v141, v151\n"
      "v_cvt_pk_bf16_f32 v149, v150, v151\n"
      "s_waitcnt vmcnt(7)\n"
      "v_mfma_f32_16x16x32_bf16 v[232:235], v[144:147], v[28:31], v[232:235]\n"
      "v_cvt_pk_bf16_f32 v151, v170, v171\n"
      "v_cvt_pk_bf16_f32 v150, v168, v169\n"
      "s_waitcnt lgkmcnt(0)\n"
      "v_mfma_f32_16x16x32_bf16 v[136:139], a[144:147], v[172:175], v[136:139]\n"
      "v_mul_f32 v192, v141, v192\n"
      "v_mul_f32_e64 v193, v141, v193\n"
      "v_mfma_f32_16x16x32_bf16 v[136:139], a[180:183], v[164:167], v[136:139]\n"
      "v_cvt_pk_bf16_f32 v38, v192, v193\n"
      "v_mul_f32_e64 v195, v141, v195\n"
      "v_mfma_f32_16x16x32_bf16 v[188:191], a[148:151], v[172:175], v[188:191]\n"
      "v_mul_f32 v194, v141, v194\n"
      "v_add_f32_dpp v200, v200, v200 quad_perm:[1,0,3,2] row_mask:0xf bank_mask:0xf\n"
      "v_mfma_f32_16x16x32_bf16 v[188:191], a[184:187], v[164:167], v[188:191]\n"
      "s_nop 0\n"
      "v_add_f32_dpp v200, v200, v200 quad_perm:[2,3,0,1] row_mask:0xf bank_mask:0xf\n"
      "v_add_f32_dpp v232, v232, v232 quad_perm:[1,0,3,2] row_mask:0xf bank_mask:0xf\n"
      "v_mfma_f32_16x16x32_bf16 v[156:159], a[152:155], v[172:175], v[156:159]\n"
      "v_add_f32_dpp v200, v200, v200 row_half_mirror row_mask:0xf bank_mask:0xf\n"
      "v_mfma_f32_16x16x32_bf16 v[156:159], a[188:191], v[164:167], v[156:159]\n"
      "v_mul_f32 v203, 0xbbaaaaab, v200\n"
      "v_mul_f32 v201, 0xbbaaaaab, v200\n"
      "v_mfma_f32_16x16x32_bf16 v[152:155], a[156:159], v[172:175], v[152:155]\n"
      "v_mul_f32 v202, 0xbbaaaaab, v200\n"
      "v_mul_f32 v200, 0xbbaaaaab, v200\n"
      "v_mfma_f32_16x16x32_bf16 v[152:155], a[192:195], v[164:167], v[152:155]\n"
      "v_add_f32_dpp v232, v232, v232 quad_perm:[2,3,0,1] row_mask:0xf bank_mask:0xf\n"
      "v_cvt_pk_bf16_f32 v39, v194, v195\n"
      "v_mfma_f32_16x16x32_bf16 v[236:239], v[172:175], a[172:175], v[236:239]\n"
      "v_add_f32_dpp v232, v232, v232 row_half_mirror row_mask:0xf bank_mask:0xf\n"
      "v_mfma_f32_16x16x32_bf16 v[144:147], v[242:245], v[8:11], v[200:203]\n"
      "v_mul_f32 v234, 0xbbaaaaab, v232\n"
      "v_mul_f32 v233, 0xbbaaaaab, v232\n"
      "v_mfma_f32_16x16x32_bf16 v[192:195], v[240:243], v[8:11], v[200:203]\n"
      "ds_read_u16 v52, v134\n"
      "v_mfma_f32_16x16x32_bf16 v[168:171], v[242:245], v[12:15], v[200:203]\n"
      "v_mul_f32 v235, 0xbbaaaaab, v232\n"
      "v_mul_f32 v232, 0xbbaaaaab, v232\n"
      "v_mfma_f32_16x16x32_bf16 v[196:199], a[160:163], v[172:175], v[196:199]\n"
      "v_cvt_pk_bf16_f32 v144, v144, v145\n"
      "v_mfma_f32_16x16x32_bf16 v[196:199], a[196:199], v[164:167], v[196:199]\n"
      "ds_read_u16 v60, v134 offset:64\n"
      "v_cvt_pk_bf16_f32 v145, v146, v147\n"
      "v_mfma_f32_16x16x32_bf16 v[204:207], a[164:167], v[172:175], v[204:207]\n"
      "v_cvt_pk_bf16_f32 v147, v194, v195\n"
      "v_cvt_pk_bf16_f32 v146, v192, v193\n"
      "v_mfma_f32_16x16x32_bf16 v[204:207], a[200:203], v[164:167], v[204:207]\n"
      "ds_write_b128 v160, v[144:147] offset:12288\n"
      "v_mfma_f32_16x16x32_bf16 v[144:147], v[240:243], v[12:15], v[200:203]\n"
      "v_cvt_pk_bf16_f32 v168, v168, v169\n"
      "v_mfma_f32_16x16x32_bf16 v[224:227], v[172:175], a[168:171], v[224:227]\n"
      "v_mfma_f32_16x16x32_bf16 v[224:227], v[164:167], a[204:207], v[224:227]\n"
      "v_mfma_f32_16x16x32_bf16 v[192:195], v[242:245], v[16:19], v[200:203]\n"
      "ds_read_u16 v56, v134 offset:32\n"
      "v_cvt_pk_bf16_f32 v169, v170, v171\n"
      "v_mfma_f32_16x16x32_bf16 v[252:255], v[172:175], a[176:179], v[252:255]\n"
      "v_mfma_f32_16x16x32_bf16 v[252:255], v[164:167], a[212:215], v[252:255]\n"
      "v_cvt_pk_bf16_f32 v170, v144, v145\n"
      "v_cvt_pk_bf16_f32 v171, v146, v147\n"
      "v_mfma_f32_16x16x32_bf16 v[144:147], v[240:243], v[16:19], v[200:203]\n"
      "v_mfma_f32_16x16x32_bf16 v[236:239], v[164:167], a[208:211], v[236:239]\n"
      "ds_write_b128 v160, v[168:171] offset:12416\n"
      "v_mfma_f32_16x16x32_bf16 v[164:167], v[242:245], v[20:23], v[232:235]\n"
      "s_nop 0\n"
      "v_cvt_pk_bf16_f32 v192, v192, v193\n"
      "v_cvt_pk_bf16_f32 v193, v194, v195\n"
      "v_mfma_f32_16x16x32_bf16 v[200:203], v[240:243], v[24:27], v[232:235]\n"
      "s_nop 1\n"
      "v_cvt_pk_bf16_f32 v194, v144, v145\n"
      "v_mfma_f32_16x16x32_bf16 v[168:171], v[240:243], v[20:23], v[232:235]\n"
      "s_nop 0\n"
      "v_cvt_pk_bf16_f32 v164, v164, v165\n"
      "v_cvt_pk_bf16_f32 v195, v146, v147\n"
      "v_mfma_f32_16x16x32_bf16 v[228:231], v[242:245], v[28:31], v[232:235]\n"
      "ds_write_b128 v160, v[192:195] offset:12544\n"
      "v_cvt_pk_bf16_f32 v165, v166, v167\n"
      "v_mfma_f32_16x16x32_bf16 v[172:175], v[242:245], v[24:27], v[232:235]\n"
      "s_waitcnt vmcnt(0)\n"
      "v_cvt_pk_bf16_f32 v166, v168, v169\n"
      "v_cvt_pk_bf16_f32 v167, v170, v171\n"
      "v_mfma_f32_16x16x32_bf16 v[192:195], v[240:243], v[28:31], v[232:235]\n"
      "ds_write_b128 v160, v[164:167] offset:15360\n"
      "ds_read_b128 v[164:167], v119 offset:41728\n"
      "ds_read_b32 v141, v122 offset:4864\n"
      "v_cvt_pk_bf16_f32 v228, v228, v229\n"
      "v_cvt_pk_bf16_f32 v172, v172, v173\n"
      "v_cvt_pk_bf16_f32 v229, v230, v231\n"
      "v_cvt_pk_bf16_f32 v173, v174, v175\n"
      "v_cvt_pk_bf16_f32 v174, v200, v201\n"
      "v_cvt_pk_bf16_f32 v230, v192, v193\n"
      "v_cvt_pk_bf16_f32 v175, v202, v203\n"
      "v_cvt_pk_bf16_f32 v231, v194, v195\n"
      "ds_read_b64 v[212:213], v132 offset:24\n"
      "ds_write_b128 v160, v[172:175] offset:15488\n"
      "ds_read_b128 v[144:147], v132 offset:32\n"
      "ds_write_b128 v160, v[228:231] offset:15616\n"
      "ds_read_b128 v[240:243], v133\n"
      "ds_read_b64 v[214:215], v133 offset:16\n"
      "s_waitcnt lgkmcnt(7)\n"
      "v_or3_b32 v164, v164, v165, v166\n"
      "s_waitcnt lgkmcnt(6)\n"
      "v_and_b32_e64 v164, s40, v164\n"
      "v_bitop3_b32 v141, v141, v167, s40 bitop3:0xa8\n"
      "v_or3_b32 v164, v164, v141, s4\n"
      "ds_write_b32 v129, v164 offset:3072\n"
      "s_waitcnt lgkmcnt(0)\n"
      "s_barrier\n"
      "v_mfma_f32_16x16x32_bf16 v[164:167], v[176:179], v[184:187], v[78:81]\n"
      "ds_read_b64_tr_b16 a[216:217], v250\n"
      "v_fma_f32 v141, v136, v136, s38\n"
      "v_mfma_f32_16x16x32_bf16 v[164:167], v[34:37], v[32:35], v[164:167]\n"
      "ds_read_b64_tr_b16 a[220:221], v250 offset:8\n"
      "v_fma_f32 v142, v152, v152, s38\n"
      "v_mfma_f32_16x16x32_bf16 v[192:195], v[180:183], v[184:187], v[82:85]\n"
      "ds_read_b64_tr_b16 a[224:225], v251\n"
      "v_fmac_f32 v141, v137, v137\n"
      "v_mfma_f32_16x16x32_bf16 v[192:195], v[40:43], v[32:35], v[192:195]\n"
      "ds_read_b64_tr_b16 a[218:219], v250 offset:6144\n"
      "v_fmac_f32 v142, v153, v153\n"
      "v_mfma_f32_16x16x32_bf16 v[172:175], v[144:147], v[184:187], v[70:73]\n"
      "ds_read_b64_tr_b16 a[222:223], v250 offset:6152\n"
      "v_fmac_f32 v141, v138, v138\n"
      "v_mfma_f32_16x16x32_bf16 v[172:175], v[240:243], v[176:179], v[172:175]\n"
      "ds_read_b64_tr_b16 a[226:227], v251 offset:6144\n"
      "v_fmac_f32 v142, v154, v154\n"
      "v_mfma_f32_16x16x32_bf16 v[172:175], v[212:215], v[32:35], v[172:175]\n"
      "ds_read_b64_tr_b16 a[228:229], v250 offset:18432\n"
      "v_fmac_f32 v141, v139, v139\n"
      "v_fmac_f32 v142, v155, v155\n"
      "v_fmac_f32 v141, v188, v188\n"
      "v_fmac_f32 v142, v196, v196\n"
      "v_fmac_f32 v141, v189, v189\n"
      "v_fmac_f32 v142, v197, v197\n"
      "v_fmac_f32 v141, v190, v190\n"
      "v_fmac_f32 v142, v198, v198\n"
      "v_fmac_f32 v141, v191, v191\n"
      "v_fmac_f32 v142, v199, v199\n"
      "v_fmac_f32 v141, v156, v156\n"
      "v_fmac_f32 v142, v204, v204\n"
      "v_fmac_f32 v141, v157, v157\n"
      "v_fmac_f32 v142, v205, v205\n"
      "v_fmac_f32 v141, v158, v158\n"
      "v_fmac_f32 v142, v206, v206\n"
      "v_fmac_f32 v141, v159, v159\n"
      "v_fmac_f32 v142, v207, v207\n"
      "s_nop 1\n"
      "v_permlane16_swap_b32 v141, v142\n"
      "v_add_f32 v141, v141, v142\n"
      "v_mov_b32 v142, v141\n"
      "s_nop 1\n"
      "v_permlane32_swap_b32 v141, v142\n"
      "v_add_f32 v141, v141, v142\n"
      "v_rsq_f32 v141, v141\n"
      "v_cvt_pk_bf16_f32 v50, v224, v225\n"
      "v_mov_b32_e64 v142, v141\n"
      "v_cvt_pk_bf16_f32 v51, v226, v227\n"
      "s_nop 0\n"
      "v_permlane16_swap_b32 v141, v142\n"
      "v_mul_f32 v152, v142, v152\n"
      "v_mul_f32 v153, v142, v153\n"
      "v_cvt_pk_bf16_f32 v152, v152, v153\n"
      "v_mul_f32 v154, v142, v154\n"
      "v_mul_f32 v155, v142, v155\n"
      "v_cvt_pk_bf16_f32 v153, v154, v155\n"
      "v_mul_f32 v196, v142, v196\n"
      "v_mul_f32 v197, v142, v197\n"
      "v_cvt_pk_bf16_f32 v154, v196, v197\n"
      "v_mul_f32 v198, v142, v198\n"
      "v_mul_f32 v199, v142, v199\n"
      "v_cvt_pk_bf16_f32 v155, v198, v199\n"
      "v_mul_f32 v204, v142, v204\n"
      "v_mul_f32 v205, v142, v205\n"
      "v_cvt_pk_bf16_f32 v46, v204, v205\n"
      "v_mul_f32 v206, v142, v206\n"
      "v_mul_f32 v207, v142, v207\n"
      "v_cvt_pk_bf16_f32 v47, v206, v207\n"
      "v_mul_f32 v141, s36, v141\n"
      "v_mul_f32 v136, v141, v136\n"
      "v_mfma_f32_16x16x32_bf16 v[196:199], v[152:155], v[184:187], v[86:89]\n"
      "ds_read_b64_tr_b16 a[232:233], v250 offset:18440\n"
      "v_mul_f32_e64 v137, v141, v137\n"
      "v_mfma_f32_16x16x32_bf16 v[196:199], v[46:49], v[32:35], v[196:199]\n"
      "ds_read_b64_tr_b16 a[236:237], v251 offset:18432\n"
      "ds_read_b64_tr_b16 a[230:231], v250 offset:24576\n"
      "ds_read_b64_tr_b16 a[234:235], v250 offset:24584\n"
      "ds_read_b64_tr_b16 a[238:239], v251 offset:24576\n"
      "ds_read_b64_tr_b16 a[240:241], v143\n"
      "ds_read_b64_tr_b16 a[244:245], v143 offset:8\n"
      "ds_read_b64_tr_b16 a[248:249], v140\n"
      "ds_read_b64_tr_b16 a[242:243], v143 offset:6144\n"
      "ds_read_b64_tr_b16 a[246:247], v143 offset:6152\n"
      "ds_read_b64_tr_b16 a[250:251], v140 offset:6144\n"
      "ds_read_b64_tr_b16 a[252:253], v143 offset:18432\n"
      "ds_read_b64_tr_b16 v[0:1], v143 offset:18440\n"
      "ds_read_b64_tr_b16 v[4:5], v140 offset:18432\n"
      "ds_read_b64_tr_b16 a[254:255], v143 offset:24576\n"
      "ds_read_b64_tr_b16 v[2:3], v143 offset:24584\n"
      "ds_read_b64_tr_b16 v[6:7], v140 offset:24576\n"
      "ds_read_b64_tr_b16 v[8:9], v250 offset:12288\n"
      "ds_read_b64_tr_b16 v[12:13], v250 offset:12296\n"
      "ds_read_b64_tr_b16 v[16:17], v251 offset:12288\n"
      "ds_read_b64_tr_b16 v[10:11], v250 offset:30720\n"
      "ds_read_b64_tr_b16 v[14:15], v250 offset:30728\n"
      "ds_read_b64_tr_b16 v[18:19], v251 offset:30720\n"
      "ds_read_b64_tr_b16 v[20:21], v143 offset:12288\n"
      "ds_read_b64_tr_b16 v[24:25], v143 offset:12296\n"
      "ds_read_b64_tr_b16 v[28:29], v140 offset:12288\n"
      "ds_read_b64_tr_b16 v[22:23], v143 offset:30720\n"
      "ds_read_b64_tr_b16 v[26:27], v143 offset:30728\n"
      "ds_read_b64_tr_b16 v[30:31], v140 offset:30720\n"
      "v_cvt_pk_bf16_f32 v136, v136, v137\n"
      "v_mul_f32 v138, v141, v138\n"
      "v_mul_f32 v139, v141, v139\n"
      "v_cvt_pk_bf16_f32 v137, v138, v139\n"
      "v_mul_f32 v188, v141, v188\n"
      "v_mul_f32 v189, v141, v189\n"
      "v_cvt_pk_bf16_f32 v138, v188, v189\n"
      "v_mul_f32 v190, v141, v190\n"
      "v_mul_f32 v191, v141, v191\n"
      "v_cvt_pk_bf16_f32 v139, v190, v191\n"
      "v_mul_f32 v156, v141, v156\n"
      "v_mul_f32 v157, v141, v157\n"
      "v_cvt_pk_bf16_f32 v44, v156, v157\n"
      "v_mul_f32 v158, v141, v158\n"
      "v_mul_f32 v159, v141, v159\n"
      "v_cvt_pk_bf16_f32 v45, v158, v159\n"
      "v_cvt_pk_bf16_f32 v54, v236, v237\n"
      "v_cvt_pk_bf16_f32 v55, v238, v239\n"
      "v_cvt_pk_bf16_f32 v58, v252, v253\n"
      "v_cvt_pk_bf16_f32 v59, v254, v255\n"
      "s_waitcnt lgkmcnt(0)\n"
      "s_barrier\n"
      "s_or_b64 s[34:35], s[48:49], s[50:51]\n"
      "s_cbranch_scc1 L_gen_8_%=\n"
      "L_fast_11_%=:\n"
      "v_mfma_f32_16x16x32_bf16 v[156:159], v[176:179], v[148:151], v[90:93]\n"
      "v_exp_f32 v193, v193\n"
      "v_mfma_f32_16x16x32_bf16 v[156:159], v[34:37], v[38:41], v[156:159]\n"
      "v_exp_f32 v164, v164\n"
      "v_exp_f32 v172, v172\n"
      "v_exp_f32 v192, v192\n"
      "v_mfma_f32_16x16x32_bf16 v[188:191], v[180:183], v[148:151], v[94:97]\n"
      "v_exp_f32 v165, v165\n"
      "v_mfma_f32_16x16x32_bf16 v[188:191], v[40:43], v[38:41], v[188:191]\n"
      "ds_read_b128 v[140:143], v128 offset:3072\n"
      "v_exp_f32 v166, v166\n"
      "v_mfma_f32_16x16x32_bf16 v[184:187], v[144:147], v[148:151], v[72:75]\n"
      "v_exp_f32 v197, v197\n"
      "v_mfma_f32_16x16x32_bf16 v[184:187], v[240:243], v[180:183], v[184:187]\n"
      "v_exp_f32 v167, v167\n"
      "v_mfma_f32_16x16x32_bf16 v[184:187], v[212:215], v[38:41], v[184:187]\n"
      "v_exp_f32_e64 v195, v195\n"
      "v_mfma_f32_16x16x32_bf16 v[160:163], v[152:155], v[148:151], v[98:101]\n"
      "v_cvt_pk_bf16_f32 v164, v164, v165\n"
      "v_cvt_pk_bf16_f32 v165, v166, v167\n"
      "v_mfma_f32_16x16x32_bf16 v[160:163], v[46:49], v[38:41], v[160:163]\n"
      "v_exp_f32 v190, v190\n"
      "v_mfma_f32_16x16x32_bf16 v[200:203], v[176:179], v[136:139], v[102:105]\n"
      "v_exp_f32 v194, v194\n"
      "v_mfma_f32_16x16x32_bf16 v[200:203], v[34:37], v[44:47], v[200:203]\n"
      "v_exp_f32 v189, v189\n"
      "v_mfma_f32_16x16x32_bf16 v[176:179], v[180:183], v[136:139], v[106:109]\n"
      "v_exp_f32 v196, v196\n"
      "v_mfma_f32_16x16x32_bf16 v[176:179], v[40:43], v[44:47], v[176:179]\n"
      "v_cvt_pk_bf16_f32 v167, v194, v195\n"
      "v_cvt_pk_bf16_f32 v166, v192, v193\n"
      "v_mfma_f32_16x16x32_bf16 v[192:195], v[144:147], v[136:139], v[74:77]\n"
      "v_exp_f32 v198, v198\n"
      "v_mfma_f32_16x16x32_bf16 v[192:195], v[240:243], v[152:155], v[192:195]\n"
      "v_exp_f32 v199, v199\n"
      "v_mfma_f32_16x16x32_bf16 v[192:195], v[212:215], v[44:47], v[192:195]\n"
      "s_waitcnt lgkmcnt(0)\n"
      "v_exp_f32 v157, v157\n"
      "v_mfma_f32_16x16x32_bf16 v[180:183], v[152:155], v[136:139], v[110:113]\n"
      "v_cvt_pk_bf16_f32 v63, v198, v199\n"
      "v_cvt_pk_bf16_f32 v62, v196, v197\n"
      "v_mfma_f32_16x16x32_bf16 v[180:183], v[46:49], v[44:47], v[180:183]\n"
      "v_cvt_pk_bf16_f32 v64, v172, 0\n"
      "v_bitop3_b32 v140, v140, v141, s4 bitop3:0x7e\n"
      "v_exp_f32 v162, v162\n"
      "v_bitop3_b32 v142, v142, v143, s4 bitop3:0x7e\n"
      "v_mfma_f32_16x16x32_bf16 v[148:151], v[240:243], v[144:147], v[76:79]\n"
      "v_exp_f32 v159, v159\n"
      "v_or_b32 v140, v140, v142\n"
      "v_exp_f32 v191, v191\n"
      "v_mfma_f32_16x16x32_bf16 v[144:147], v[66:69], v[164:167], 0\n"
      "v_cndmask_b32 v173, v173, v185, s[42:43]\n"
      "v_cmp_ne_u32 vcc, 0, v140\n"
      "v_mfma_f32_16x16x32_bf16 v[144:147], v[66:69], v[62:65], v[144:147]\n"
      "s_mov_b64 s[48:49], vcc\n"
      "v_exp_f32 v163, v163\n"
      "v_exp_f32 v184, v184\n"
      "v_mfma_f32_16x16x32_bf16 v[152:155], v[220:223], v[164:167], 0\n"
      "s_cbranch_vccnz L_flg_12_%=\n"
      "L_fb_13_%=:\n"
      "v_mfma_f32_16x16x32_bf16 v[152:155], v[50:53], v[62:65], v[152:155]\n"
      "s_add_u32 s28, s27, 2\n"
      "v_exp_f32 v161, v161\n"
      "v_exp_f32 v160, v160\n"
      "v_exp_f32 v156, v156\n"
      "v_mfma_f32_16x16x32_bf16 v[196:199], v[208:211], v[164:167], 0\n"
      "s_min_u32 m0, s28, s3\n"
      "v_exp_f32_e64 v158, v158\n"
      "v_mfma_f32_16x16x32_bf16 v[196:199], v[54:57], v[62:65], v[196:199]\n"
      "s_movrels_b32 s9, s80\n"
      "v_rcp_f32 v144, v144\n"
      "v_mfma_f32_16x16x32_bf16 v[168:171], v[216:219], v[164:167], 0\n"
      "s_lshr_b32 s8, s9, 8\n"
      "v_exp_f32 v188, v188\n"
      "v_mfma_f32_16x16x32_bf16 v[168:171], v[58:61], v[62:65], v[168:171]\n"
      "s_and_b32 s9, s9, 0xff\n"
      "v_cvt_pk_bf16_f32 v64, v184, 0\n"
      "v_cvt_pk_bf16_f32 v156, v156, v157\n"
      "v_cvt_pk_bf16_f32 v157, v158, v159\n"
      "v_cvt_pk_bf16_f32 v62, v160, v161\n"
      "v_cvt_pk_bf16_f32 v158, v188, v189\n"
      "v_cvt_pk_bf16_f32 v159, v190, v191\n"
      "v_cndmask_b32 v173, v173, v193, s[44:45]\n"
      "v_mfma_f32_16x16x16_bf16 v[148:151], v[214:215], v[212:213], v[148:151]\n"
      "s_mul_i32 s10, s8, 73\n"
      "v_cvt_pk_bf16_f32 v63, v162, v163\n"
      "v_exp_f32 v181, v181\n"
      "v_mfma_f32_16x16x32_bf16 v[188:191], v[66:69], v[156:159], 0\n"
      "s_add_u32 s10, s10, s9\n"
      "v_mul_f32 v153, v144, v153\n"
      "v_mul_f32 v152, v144, v152\n"
      "v_mfma_f32_16x16x32_bf16 v[188:191], v[66:69], v[62:65], v[188:191]\n"
      "ds_read_b128 v[136:139], v114 offset:18432\n"
      "v_cvt_pk_bf16_f32 v152, v152, v153\n"
      "v_mul_f32 v170, v144, v170\n"
      "v_mul_f32 v155, v144, v155\n"
      "v_mfma_f32_16x16x32_bf16 v[184:187], v[220:223], v[156:159], 0\n"
      "ds_read_b128 v[140:143], v115 offset:18432\n"
      "v_mul_f32_e64 v154, v144, v154\n"
      "v_mfma_f32_16x16x32_bf16 v[184:187], v[50:53], v[62:65], v[184:187]\n"
      "s_mul_i32 s28, s10, 4802\n"
      "v_cndmask_b32 v173, v173, v149, s[46:47]\n"
      "v_exp_f32 v192, v192\n"
      "v_mfma_f32_16x16x32_bf16 v[212:215], v[208:211], v[156:159], 0\n"
      "s_add_u32 s56, s18, s28\n"
      "v_mul_f32 v196, v144, v196\n"
      "v_mul_f32 v197, v144, v197\n"
      "v_mfma_f32_16x16x32_bf16 v[212:215], v[54:57], v[62:65], v[212:215]\n"
      "s_addc_u32 s57, s19, 0\n"
      "v_mul_f32 v198, v144, v198\n"
      "v_exp_f32 v182, v182\n"
      "v_exp_f32 v203, v203\n"
      "v_cvt_pk_bf16_f32 v153, v154, v155\n"
      "v_exp_f32 v177, v177\n"
      "v_mul_f32 v168, v144, v168\n"
      "v_cvt_pk_bf16_f32 v154, v196, v197\n"
      "v_mul_f32 v169, v144, v169\n"
      "v_mfma_f32_16x16x32_bf16 v[194:197], v[216:219], v[156:159], 0\n"
      "s_and_b32 s56, s56, -16\n"
      "v_mul_f32 v199, v144, v199\n"
      "v_cvt_pk_bf16_f32 v155, v198, v199\n"
      "v_mfma_f32_16x16x32_bf16 v[194:197], v[58:61], v[62:65], v[194:197]\n"
      "ds_write_b128 v124, v[152:155]\n"
      "v_exp_f32 v179, v179\n"
      "v_exp_f32 v180, v180\n"
      "v_mul_f32 v171, v144, v171\n"
      "s_waitcnt lgkmcnt(1)\n"
      "v_cvt_pk_bf16_f32 v168, v168, v169\n"
      "v_exp_f32 v183, v183\n"
      "v_exp_f32 v201, v201\n"
      "v_cvt_pk_bf16_f32 v169, v170, v171\n"
      "v_exp_f32 v176, v176\n"
      "v_exp_f32 v200, v200\n"
      "v_cvt_pk_bf16_f32 v62, v180, v181\n"
      "v_exp_f32 v202, v202\n"
      "v_mfma_f32_16x16x32_bf16 v[144:147], a[0:3], v[136:139], 0\n"
      "s_sub_u32 s58, s56, 110592\n"
      "v_exp_f32 v173, v173\n"
      "v_mfma_f32_16x16x32_bf16 v[144:147], a[36:39], v[140:143], v[144:147]\n"
      "ds_write_b64 v125, v[168:169]\n"
      "v_exp_f32 v178, v178\n"
      "v_mfma_f32_16x16x32_bf16 v[148:151], a[4:7], v[136:139], 0\n"
      "s_subb_u32 s59, s57, 0\n"
      "v_cvt_pk_bf16_f32 v173, v173, v173\n"
      "v_mov_b32 v193, 0\n"
      "v_mfma_f32_16x16x32_bf16 v[148:151], a[40:43], v[140:143], v[148:151]\n"
      "ds_write_b16 v130, v173\n"
      "v_cvt_pk_bf16_f32 v200, v200, v201\n"
      "v_cvt_pk_bf16_f32 v201, v202, v203\n"
      "v_cvt_pk_bf16_f32 v64, v192, 0\n"
      "v_cvt_pk_bf16_f32 v203, v178, v179\n"
      "v_cvt_pk_bf16_f32 v202, v176, v177\n"
      "v_rcp_f32_e64 v188, v188\n"
      "v_cvt_pk_bf16_f32 v63, v182, v183\n"
      "v_mfma_f32_16x16x32_bf16 v[152:155], v[66:69], v[200:203], 0\n"
      "ds_read_b128 v[180:183], v131\n"
      "v_mul_f32_e64 v184, v188, v184\n"
      "v_mfma_f32_16x16x32_bf16 v[152:155], v[66:69], v[62:65], v[152:155]\n"
      "ds_read_b128 v[176:179], v131 offset:64\n"
      "v_mul_f32 v214, v188, v214\n"
      "v_mul_f32 v187, v188, v187\n"
      "v_mul_f32_e64 v185, v188, v185\n"
      "v_mfma_f32_16x16x32_bf16 v[156:159], v[220:223], v[200:203], 0\n"
      "s_sub_u32 s56, s56, 73728\n"
      "v_mul_f32 v186, v188, v186\n"
      "v_cvt_pk_bf16_f32 v184, v184, v185\n"
      "v_mfma_f32_16x16x32_bf16 v[156:159], v[50:53], v[62:65], v[156:159]\n"
      "s_subb_u32 s57, s57, 0\n"
      "v_mul_f32 v195, v188, v195\n"
      "v_cvt_pk_bf16_f32 v185, v186, v187\n"
      "v_mfma_f32_16x16x32_bf16 v[160:163], v[208:211], v[200:203], 0\n"
      "s_mul_i32 s28, s8, 1373568\n"
      "v_mul_f32 v212, v188, v212\n"
      "v_mul_f32 v213, v188, v213\n"
      "v_mfma_f32_16x16x32_bf16 v[160:163], v[54:57], v[62:65], v[160:163]\n"
      "s_mul_i32 s29, s9, 2688\n"
      "v_mul_f32 v194, v188, v194\n"
      "v_cvt_pk_bf16_f32 v186, v212, v213\n"
      "v_mfma_f32_16x16x32_bf16 v[164:167], v[216:219], v[200:203], 0\n"
      "s_add_u32 s28, s28, s29\n"
      "v_mul_f32_e64 v215, v188, v215\n"
      "v_cvt_pk_bf16_f32 v187, v214, v215\n"
      "v_mfma_f32_16x16x32_bf16 v[164:167], v[58:61], v[62:65], v[164:167]\n"
      "ds_write_b128 v124, v[184:187] offset:256\n"
      "s_waitcnt lgkmcnt(1)\n"
      "v_mfma_f32_16x16x32_bf16 v[184:187], v[180:183], v[66:69], 0\n"
      "s_cmp_eq_u32 s8, 72\n"
      "v_cvt_pk_bf16_f32 v194, v194, v195\n"
      "v_mul_f32_e64 v197, v188, v197\n"
      "v_mfma_f32_16x16x32_bf16 v[184:187], v[176:179], v[66:69], v[184:187]\n"
      "s_cselect_b32 s8, 0, 100270464\n"
      "v_mul_f32 v196, v188, v196\n"
      "s_cmp_eq_u32 s9, 72\n"
      "v_mfma_f32_16x16x32_bf16 v[200:203], v[180:183], v[220:223], 0\n"
      "s_cselect_b32 s9, 0, 196224\n"
      "s_add_u32 s10, s8, s9\n"
      "v_cvt_pk_bf16_f32 v195, v196, v197\n"
      "v_mfma_f32_16x16x32_bf16 v[200:203], v[176:179], v[50:53], v[200:203]\n"
      "ds_write_b64 v125, v[194:195] offset:256\n"
      "v_mfma_f32_16x16x32_bf16 v[196:199], v[180:183], v[208:211], 0\n"
      "s_add_u32 s28, s28, s10\n"
      "s_add_u32 s52, s16, s28\n"
      "v_mov_b32_dpp v184, v184 quad_perm:[0,0,0,0] row_mask:0xf bank_mask:0xf\n"
      "v_mfma_f32_16x16x32_bf16 v[196:199], v[176:179], v[54:57], v[196:199]\n"
      "ds_write_b32 v129, v193\n"
      "v_mfma_f32_16x16x32_bf16 v[208:211], v[180:183], v[216:219], 0\n"
      "s_addc_u32 s53, s17, 0\n"
      "v_rcp_f32 v184, v184\n"
      "v_mfma_f32_16x16x32_bf16 v[208:211], v[176:179], v[58:61], v[208:211]\n"
      "s_cmp_lg_u32 s10, s11\n"
      "s_nop 1\n"
      "v_cndmask_b32 v200, v200, v196, s[42:43]\n"
      "s_nop 3\n"
      "v_cndmask_b32 v200, v200, v208, s[44:45]\n"
      "v_mul_f32 v200, v184, v200\n"
      "v_cvt_pk_bf16_f32 v200, v200, v200\n"
      "s_cbranch_scc1 L_edge_15_%=\n"
      "L_eb_16_%=:\n"
      "ds_write_b16 v135, v200\n"
      "L_join_10_%=:\n"
      "s_mov_b32 s27, 1\n"
      ".p2align 6\n"
      "L_loop_%=:\n"
      "v_mfma_f32_16x16x32_bf16 v[208:211], a[8:11], v[136:139], 0\n"
      "s_add_u32 m0, s76, 0\n"
      "ds_read_b128 v[216:219], v114 offset:24576\n"
      "v_mfma_f32_16x16x32_bf16 v[208:211], a[44:47], v[140:143], v[208:211]\n"
      "ds_read_b128 v[220:223], v115 offset:24576\n"
      "v_rcp_f32 v152, v152\n"
      "v_mfma_f32_16x16x32_bf16 v[212:215], a[12:15], v[136:139], 0\n"
      "v_mul_f32 v157, v152, v157\n"
      "v_mul_f32 v156, v152, v156\n"
      "v_mfma_f32_16x16x32_bf16 v[212:215], a[48:51], v[140:143], v[212:215]\n"
      "ds_read_b128 v[240:243], v114 offset:30720\n"
      "v_cvt_pk_bf16_f32 v156, v156, v157\n"
      "v_mul_f32 v162, v152, v162\n"
      "v_mfma_f32_16x16x32_bf16 v[252:255], a[16:19], v[136:139], 0\n"
      "v_mul_f32 v159, v152, v159\n"
      "v_mul_f32_e64 v158, v152, v158\n"
      "v_mfma_f32_16x16x32_bf16 v[252:255], a[52:55], v[140:143], v[252:255]\n"
      "s_waitcnt lgkmcnt(3)\n"
      "s_barrier\n"
      "v_mfma_f32_16x16x32_bf16 v[236:239], a[20:23], v[136:139], 0\n"
      "ds_read_b128 v[248:251], v128\n"
      "v_cvt_pk_bf16_f32 v157, v158, v159\n"
      "v_mfma_f32_16x16x32_bf16 v[236:239], a[56:59], v[140:143], v[236:239]\n"
      "global_load_lds_dwordx4 v116, s[52:53]\n"
      "v_mfma_f32_16x16x32_bf16 v[204:207], v[136:139], a[24:27], 0\n"
      "s_waitcnt lgkmcnt(1)\n"
      "v_mul_f32 v160, v152, v160\n"
      "v_mul_f32 v161, v152, v161\n"
      "v_mfma_f32_16x16x32_bf16 v[204:207], v[140:143], a[60:63], v[204:207]\n"
      "v_mul_f32 v163, v152, v163\n"
      "v_cvt_pk_bf16_f32 v158, v160, v161\n"
      "v_mfma_f32_16x16x32_bf16 v[224:227], v[136:139], a[28:31], 0\n"
      "v_cvt_pk_bf16_f32 v159, v162, v163\n"
      "v_mul_f32_e64 v164, v152, v164\n"
      "v_mfma_f32_16x16x32_bf16 v[224:227], v[140:143], a[64:67], v[224:227]\n"
      "ds_write_b128 v124, v[156:159] offset:512\n"
      "v_mfma_f32_16x16x32_bf16 v[160:163], v[136:139], a[32:35], 0\n"
      "ds_read_b128 v[136:139], v115 offset:30720\n"
      "v_mul_f32 v165, v152, v165\n"
      "v_mfma_f32_16x16x32_bf16 v[160:163], v[140:143], a[68:71], v[160:163]\n"
      "s_waitcnt lgkmcnt(2)\n"
      "v_cvt_pk_bf16_f32 v164, v164, v165\n"
      "v_mfma_f32_16x16x32_bf16 v[144:147], a[72:75], v[216:219], v[144:147]\n"
      "s_add_u32 m0, s76, 6016\n"
      "v_mul_f32 v166, v152, v166\n"
      "v_mul_f32 v167, v152, v167\n"
      "v_mfma_f32_16x16x32_bf16 v[144:147], a[108:111], v[220:223], v[144:147]\n"
      "global_load_lds_dwordx4 v116, s[52:53] offset:128\n"
      "v_mfma_f32_16x16x32_bf16 v[148:151], a[76:79], v[216:219], v[148:151]\n"
      "v_cvt_pk_bf16_f32 v165, v166, v167\n"
      "v_add_f32 v248, |v248|, |v249|\n"
      "v_mfma_f32_16x16x32_bf16 v[148:151], a[112:115], v[220:223], v[148:151]\n"
      "ds_write_b64 v125, v[164:165] offset:512\n"
      "v_mfma_f32_16x16x32_bf16 v[208:211], a[80:83], v[216:219], v[208:211]\n"
      "v_add_f32 v250, |v250|, |v251|\n"
      "v_add_f32_e64 v248, v248, v250\n"
      "v_mfma_f32_16x16x32_bf16 v[208:211], a[116:119], v[220:223], v[208:211]\n"
      "s_add_u32 m0, s76, 12032\n"
      "v_rsq_f32 v248, v248\n"
      "v_mfma_f32_16x16x32_bf16 v[212:215], a[84:87], v[216:219], v[212:215]\n"
      "v_mul_f32 v248, s41, v248\n"
      "v_mov_b32 v152, v248\n"
      "v_mfma_f32_16x16x32_bf16 v[212:215], a[120:123], v[220:223], v[212:215]\n"
      "s_nop 0\n"
      "v_permlane32_swap_b32_e64 v248, v152\n"
      "v_mfma_f32_16x16x32_bf16 v[252:255], a[88:91], v[216:219], v[252:255]\n"
      "ds_read_b128 v[140:143], v114 offset:20480\n"
      "v_mov_b32_e64 v153, v248\n"
      "v_mfma_f32_16x16x32_bf16 v[252:255], a[124:127], v[220:223], v[252:255]\n"
      "global_load_lds_dwordx4 v116, s[52:53] offset:256\n"
      "v_mfma_f32_16x16x32_bf16 v[236:239], a[92:95], v[216:219], v[236:239]\n"
      "s_waitcnt lgkmcnt(2)\n"
      "v_permlane16_swap_b32 v248, v153\n"
      "v_mfma_f32_16x16x32_bf16 v[236:239], a[128:131], v[220:223], v[236:239]\n"
      "ds_read_b128 v[156:159], v115 offset:20480\n"
      "v_mul_f32 v168, v248, v168\n"
      "v_mfma_f32_16x16x32_bf16 v[204:207], v[216:219], a[96:99], v[204:207]\n"
      "v_mul_f32 v169, v248, v169\n"
      "v_cvt_pk_bf16_f32 v168, v168, v169\n"
      "v_mfma_f32_16x16x32_bf16 v[204:207], v[220:223], a[132:135], v[204:207]\n"
      "v_mul_f32 v171, v248, v171\n"
      "v_mul_f32 v170, v248, v170\n"
      "v_mfma_f32_16x16x32_bf16 v[224:227], v[216:219], a[100:103], v[224:227]\n"
      "v_mul_f32_e64 v172, v248, v172\n"
      "v_cvt_pk_bf16_f32 v169, v170, v171\n"
      "v_mfma_f32_16x16x32_bf16 v[224:227], v[220:223], a[136:139], v[224:227]\n"
      "s_add_u32 m0, s77, 0\n"
      "v_mul_f32 v173, v248, v173\n"
      "v_cvt_pk_bf16_f32 v170, v172, v173\n"
      "v_mfma_f32_16x16x32_bf16 v[160:163], v[216:219], a[104:107], v[160:163]\n"
      "v_mul_f32 v174, v248, v174\n"
      "v_mul_f32 v175, v248, v175\n"
      "v_mfma_f32_16x16x32_bf16 v[160:163], v[220:223], a[140:143], v[160:163]\n"
      "v_cvt_pk_bf16_f32 v171, v174, v175\n"
      "v_mul_f32_e64 v176, v248, v176\n"
      "v_mfma_f32_16x16x32_bf16 v[144:147], a[144:147], v[240:243], v[144:147]\n"
      "global_load_lds_dwordx4 v117, s[52:53]\n"
      "v_mfma_f32_16x16x32_bf16 v[144:147], a[180:183], v[136:139], v[144:147]\n"
      "v_mul_f32_e64 v177, v248, v177\n"
      "v_cvt_pk_bf16_f32 v176, v176, v177\n"
      "v_mfma_f32_16x16x32_bf16 v[148:151], a[148:151], v[240:243], v[148:151]\n"
      "v_mul_f32 v178, v248, v178\n"
      "v_mul_f32 v179, v248, v179\n"
      "v_mfma_f32_16x16x32_bf16 v[148:151], a[184:187], v[136:139], v[148:151]\n"
      "ds_write_b128 v126, v[168:171] offset:18432\n"
      "v_mfma_f32_16x16x32_bf16 v[208:211], a[152:155], v[240:243], v[208:211]\n"
      "s_waitcnt lgkmcnt(1)\n"
      "v_cvt_pk_bf16_f32 v177, v178, v179\n"
      "v_mul_f32 v180, v153, v180\n"
      "v_mfma_f32_16x16x32_bf16 v[208:211], a[188:191], v[136:139], v[208:211]\n"
      "v_mul_f32 v181, v153, v181\n"
      "v_cvt_pk_bf16_f32 v180, v180, v181\n"
      "v_mfma_f32_16x16x32_bf16 v[212:215], a[156:159], v[240:243], v[212:215]\n"
      "v_mul_f32 v183, v153, v183\n"
      "v_mul_f32 v182, v153, v182\n"
      "v_mfma_f32_16x16x32_bf16 v[212:215], a[192:195], v[136:139], v[212:215]\n"
      "s_add_u32 m0, s78, 0\n"
      "v_cvt_pk_bf16_f32 v181, v182, v183\n"
      "v_mul_f32_e64 v184, v153, v184\n"
      "v_mfma_f32_16x16x32_bf16 v[252:255], a[160:163], v[240:243], v[252:255]\n"
      "ds_read_b128 v[168:171], v114 offset:26624\n"
      "v_mul_f32_e64 v185, v153, v185\n"
      "v_mfma_f32_16x16x32_bf16 v[252:255], a[196:199], v[136:139], v[252:255]\n"
      "ds_read_b128 v[172:175], v115 offset:26624\n"
      "v_cvt_pk_bf16_f32 v182, v184, v185\n"
      "v_mfma_f32_16x16x32_bf16 v[236:239], a[164:167], v[240:243], v[236:239]\n"
      "v_mul_f32 v186, v153, v186\n"
      "v_mul_f32 v187, v153, v187\n"
      "v_mfma_f32_16x16x32_bf16 v[236:239], a[200:203], v[136:139], v[236:239]\n"
      "s_mov_b32 exec_hi, 0\n"
      "global_load_lds_dwordx4 v118, s[52:53]\n"
      "s_mov_b32 exec_hi, -1\n"
      "v_mfma_f32_16x16x32_bf16 v[204:207], v[240:243], a[168:171], v[204:207]\n"
      "v_cvt_pk_bf16_f32 v183, v186, v187\n"
      "v_mul_f32_e64 v188, v153, v188\n"
      "v_mfma_f32_16x16x32_bf16 v[204:207], v[136:139], a[204:207], v[204:207]\n"
      "ds_write_b128 v126, v[180:183] offset:20480\n"
      "v_mfma_f32_16x16x32_bf16 v[224:227], v[240:243], a[172:175], v[224:227]\n"
      "v_mul_f32_e64 v189, v153, v189\n"
      "v_cvt_pk_bf16_f32 v188, v188, v189\n"
      "v_mfma_f32_16x16x32_bf16 v[224:227], v[136:139], a[208:211], v[224:227]\n"
      "s_add_u32 m0, s76, 110592\n"
      "v_mul_f32 v191, v153, v191\n"
      "v_mul_f32 v190, v153, v190\n"
      "v_mfma_f32_16x16x32_bf16 v[160:163], v[240:243], a[176:179], v[160:163]\n"
      "s_waitcnt lgkmcnt(1)\n"
      "v_mul_f32 v192, v152, v192\n"
      "v_cvt_pk_bf16_f32 v189, v190, v191\n"
      "v_mfma_f32_16x16x32_bf16 v[160:163], v[136:139], a[212:215], v[160:163]\n"
      "ds_write2st64_b64 v127, v[176:177], v[188:189] offset0:36 offset1:40\n"
      "v_mfma_f32_16x16x32_bf16 v[180:183], a[0:3], v[140:143], 0\n"
      "global_load_lds_dwordx4 v119, s[56:57]\n"
      "v_mfma_f32_16x16x32_bf16 v[180:183], a[36:39], v[156:159], v[180:183]\n"
      "s_add_u32 m0, s79, 0\n"
      "v_mul_f32 v193, v152, v193\n"
      "v_cvt_pk_bf16_f32 v192, v192, v193\n"
      "v_mfma_f32_16x16x32_bf16 v[176:179], a[4:7], v[140:143], 0\n"
      "s_sub_u32 s28, s27, 3\n"
      "v_mul_f32 v195, v152, v195\n"
      "v_mul_f32 v194, v152, v194\n"
      "v_mfma_f32_16x16x32_bf16 v[176:179], a[40:43], v[156:159], v[176:179]\n"
      "v_mul_f32 v196, v152, v196\n"
      "v_cvt_pk_bf16_f32 v193, v194, v195\n"
      "v_mfma_f32_16x16x32_bf16 v[184:187], a[8:11], v[140:143], 0\n"
      "v_mul_f32_e64 v197, v152, v197\n"
      "v_cvt_pk_bf16_f32 v194, v196, v197\n"
      "v_mfma_f32_16x16x32_bf16 v[184:187], a[44:47], v[156:159], v[184:187]\n"
      "s_cselect_b64 s[54:55], s[12:13], s[22:23]\n"
      "v_mul_f32 v198, v152, v198\n"
      "v_mul_f32_e64 v199, v152, v199\n"
      "v_mfma_f32_16x16x32_bf16 v[136:139], a[12:15], v[140:143], 0\n"
      "ds_read_b128 v[188:191], v114 offset:32768\n"
      "v_cvt_pk_bf16_f32 v195, v198, v199\n"
      "v_mfma_f32_16x16x32_bf16 v[136:139], a[48:51], v[156:159], v[136:139]\n"
      "ds_write_b128 v126, v[192:195] offset:22528\n"
      "v_mfma_f32_16x16x32_bf16 v[192:195], a[16:19], v[140:143], 0\n"
      "global_load_lds_dword v122, s[58:59]\n"
      "v_mfma_f32_16x16x32_bf16 v[192:195], a[52:55], v[156:159], v[192:195]\n"
      "ds_read_b128 v[196:199], v115 offset:32768\n"
      "v_mul_f32 v200, v152, v200\n"
      "v_mfma_f32_16x16x32_bf16 v[164:167], a[20:23], v[140:143], 0\n"
      "s_max_i32 m0, s28, 0\n"
      "v_mul_f32_e64 v201, v152, v201\n"
      "v_cvt_pk_bf16_f32 v200, v200, v201\n"
      "v_mfma_f32_16x16x32_bf16 v[164:167], a[56:59], v[156:159], v[164:167]\n"
      "s_movrels_b32 s9, s80\n"
      "v_mul_f32 v202, v152, v202\n"
      "v_mul_f32 v203, v152, v203\n"
      "v_mfma_f32_16x16x32_bf16 v[152:155], v[140:143], a[24:27], 0\n"
      "s_lshr_b32 s8, s9, 8\n"
      "v_fma_f32 v248, v144, v144, s38\n"
      "v_cvt_pk_bf16_f32 v201, v202, v203\n"
      "v_mfma_f32_16x16x32_bf16 v[152:155], v[156:159], a[60:63], v[152:155]\n"
      "ds_write_b64 v127, v[200:201] offset:22528\n"
      "v_mfma_f32_16x16x32_bf16 v[200:203], v[140:143], a[28:31], 0\n"
      "s_and_b32 s9, s9, 0xff\n"
      "v_fmac_f32_e64 v248, v145, v145\n"
      "v_fma_f32 v250, v212, v212, s38\n"
      "v_mfma_f32_16x16x32_bf16 v[200:203], v[156:159], a[64:67], v[200:203]\n"
      "s_mul_i32 s28, s8, 1373568\n"
      "v_fmac_f32 v248, v146, v146\n"
      "v_fmac_f32 v250, v213, v213\n"
      "v_mfma_f32_16x16x32_bf16 v[240:243], v[140:143], a[32:35], 0\n"
      "s_mul_i32 s29, s9, 2688\n"
      "v_fmac_f32 v250, v214, v214\n"
      "v_fmac_f32 v248, v147, v147\n"
      "v_mfma_f32_16x16x32_bf16 v[240:243], v[156:159], a[68:71], v[240:243]\n"
      "s_waitcnt lgkmcnt(1)\n"
      "v_fmac_f32 v248, v148, v148\n"
      "v_fmac_f32 v250, v215, v215\n"
      "v_mfma_f32_16x16x32_bf16 v[180:183], a[72:75], v[168:171], v[180:183]\n"
      "s_add_u32 s28, s28, s29\n"
      "v_fmac_f32 v250, v252, v252\n"
      "v_fmac_f32 v248, v149, v149\n"
      "v_mfma_f32_16x16x32_bf16 v[180:183], a[108:111], v[172:175], v[180:183]\n"
      "s_cmp_eq_u32 s8, 72\n"
      "v_fmac_f32 v248, v150, v150\n"
      "v_fmac_f32 v250, v253, v253\n"
      "v_mfma_f32_16x16x32_bf16 v[176:179], a[76:79], v[168:171], v[176:179]\n"
      "s_cselect_b32 s8, 0, 100270464\n"
      "v_fmac_f32 v248, v151, v151\n"
      "v_fmac_f32 v250, v254, v254\n"
      "v_mfma_f32_16x16x32_bf16 v[176:179], a[112:115], v[172:175], v[176:179]\n"
      "s_cmp_eq_u32 s9, 72\n"
      "v_fmac_f32 v248, v208, v208\n"
      "v_fmac_f32 v250, v255, v255\n"
      "v_mfma_f32_16x16x32_bf16 v[184:187], a[80:83], v[168:171], v[184:187]\n"
      "ds_read_b128 v[140:143], v114 offset:22528\n"
      "v_fmac_f32_e64 v250, v236, v236\n"
      "v_mfma_f32_16x16x32_bf16 v[184:187], a[116:119], v[172:175], v[184:187]\n"
      "s_cselect_b32 s9, 0, 196224\n"
      "v_fmac_f32 v248, v209, v209\n"
      "v_fmac_f32 v250, v237, v237\n"
      "v_mfma_f32_16x16x32_bf16 v[136:139], a[84:87], v[168:171], v[136:139]\n"
      "ds_read_b128 v[156:159], v115 offset:22528\n"
      "v_fmac_f32 v248, v210, v210\n"
      "v_mfma_f32_16x16x32_bf16 v[136:139], a[120:123], v[172:175], v[136:139]\n"
      "s_add_u32 s10, s8, s9\n"
      "v_fmac_f32 v250, v238, v238\n"
      "v_fmac_f32 v248, v211, v211\n"
      "v_mfma_f32_16x16x32_bf16 v[192:195], a[88:91], v[168:171], v[192:195]\n"
      "s_add_u32 s28, s28, s10\n"
      "v_cvt_pk_bf16_f32 v204, v204, v205\n"
      "v_fmac_f32 v250, v239, v239\n"
      "v_mfma_f32_16x16x32_bf16 v[192:195], a[124:127], v[172:175], v[192:195]\n"
      "s_add_u32 s54, s54, s28\n"
      "v_permlane16_swap_b32 v248, v250\n"
      "v_mfma_f32_16x16x32_bf16 v[164:167], a[92:95], v[168:171], v[164:167]\n"
      "s_addc_u32 s55, s55, 0\n"
      "v_add_f32 v248, v248, v250\n"
      "v_mov_b32 v250, v248\n"
      "v_mfma_f32_16x16x32_bf16 v[164:167], a[128:131], v[172:175], v[164:167]\n"
      "s_cmp_lg_u32 s10, s11\n"
      "v_permlane32_swap_b32 v248, v250\n"
      "v_add_f32 v248, v248, v250\n"
      "v_mfma_f32_16x16x32_bf16 v[152:155], v[168:171], a[96:99], v[152:155]\n"
      "s_cbranch_scc1 L_edge_17_%=\n"
      "L_eb_18_%=:\n"
      "v_mfma_f32_16x16x32_bf16 v[152:155], v[172:175], a[132:135], v[152:155]\n"
      "s_mov_b32 s5, s4\n"
      "v_rsq_f32 v248, v248\n"
      "v_mfma_f32_16x16x32_bf16 v[200:203], v[168:171], a[100:103], v[200:203]\n"
      "s_waitcnt lgkmcnt(0)\n"
      "v_cvt_pk_bf16_f32 v205, v206, v207\n"
      "v_mov_b32_e64 v250, v248\n"
      "v_mfma_f32_16x16x32_bf16 v[200:203], v[172:175], a[136:139], v[200:203]\n"
      "s_add_u32 s4, s27, 1\n"
      "v_permlane16_swap_b32 v248, v250\n"
      "v_mfma_f32_16x16x32_bf16 v[240:243], v[168:171], a[104:107], v[240:243]\n"
      "v_mul_f32 v212, v250, v212\n"
      "v_mul_f32 v213, v250, v213\n"
      "v_mfma_f32_16x16x32_bf16 v[240:243], v[172:175], a[140:143], v[240:243]\n"
      "v_cvt_pk_bf16_f32 v212, v212, v213\n"
      "v_mul_f32 v214, v250, v214\n"
      "v_mfma_f32_16x16x32_bf16 v[180:183], a[144:147], v[188:191], v[180:183]\n"
      "v_mul_f32 v215, v250, v215\n"
      "v_cvt_pk_bf16_f32 v213, v214, v215\n"
      "v_mfma_f32_16x16x32_bf16 v[180:183], a[180:183], v[196:199], v[180:183]\n"
      "ds_read_b128 v[168:171], v119\n"
      "v_mul_f32_e64 v252, v250, v252\n"
      "v_mfma_f32_16x16x32_bf16 v[176:179], a[148:151], v[188:191], v[176:179]\n"
      "s_lshl_b32 s4, s4, 30\n"
      "v_mul_f32 v253, v250, v253\n"
      "v_cvt_pk_bf16_f32 v214, v252, v253\n"
      "v_mfma_f32_16x16x32_bf16 v[176:179], a[184:187], v[196:199], v[176:179]\n"
      "v_mul_f32 v254, v250, v254\n"
      "v_mul_f32 v255, v250, v255\n"
      "v_mfma_f32_16x16x32_bf16 v[184:187], a[152:155], v[188:191], v[184:187]\n"
      "v_cvt_pk_bf16_f32 v215, v254, v255\n"
      "v_mul_f32_e64 v236, v250, v236\n"
      "v_mfma_f32_16x16x32_bf16 v[184:187], a[188:191], v[196:199], v[184:187]\n"
      "s_and_b32 s4, s4, 0x80000000\n"
      "v_mul_f32_e64 v237, v250, v237\n"
      "v_cvt_pk_bf16_f32 v34, v236, v237\n"
      "v_mfma_f32_16x16x32_bf16 v[136:139], a[156:159], v[188:191], v[136:139]\n"
      "ds_read_b128 v[172:175], v114 offset:28672\n"
      "v_mul_f32 v238, v250, v238\n"
      "v_mfma_f32_16x16x32_bf16 v[136:139], a[192:195], v[196:199], v[136:139]\n"
      "v_mul_f32 v239, v250, v239\n"
      "v_cvt_pk_bf16_f32 v35, v238, v239\n"
      "v_mfma_f32_16x16x32_bf16 v[192:195], a[160:163], v[188:191], v[192:195]\n"
      "ds_read_b128 v[236:239], v115 offset:28672\n"
      "v_mul_f32_e64 v248, s36, v248\n"
      "v_mfma_f32_16x16x32_bf16 v[192:195], a[196:199], v[196:199], v[192:195]\n"
      "s_waitcnt lgkmcnt(2)\n"
      "v_mul_f32 v145, v248, v145\n"
      "v_mul_f32_e64 v144, v248, v144\n"
      "v_mfma_f32_16x16x32_bf16 v[164:167], a[164:167], v[188:191], v[164:167]\n"
      "global_store_dwordx4 v116, v[168:171], s[54:55] sc1\n"
      "v_mfma_f32_16x16x32_bf16 v[164:167], a[200:203], v[196:199], v[164:167]\n"
      "v_mul_f32_e64 v146, v248, v146\n"
      "v_cvt_pk_bf16_f32 v144, v144, v145\n"
      "v_mfma_f32_16x16x32_bf16 v[152:155], v[188:191], a[168:171], v[152:155]\n"
      "v_mul_f32 v147, v248, v147\n"
      "v_cvt_pk_bf16_f32 v145, v146, v147\n"
      "v_mfma_f32_16x16x32_bf16 v[152:155], v[196:199], a[204:207], v[152:155]\n"
      "v_mul_f32 v148, v248, v148\n"
      "v_mul_f32 v149, v248, v149\n"
      "v_mfma_f32_16x16x32_bf16 v[200:203], v[188:191], a[172:175], v[200:203]\n"
      "v_mul_f32 v150, v248, v150\n"
      "v_cvt_pk_bf16_f32 v146, v148, v149\n"
      "v_mfma_f32_16x16x32_bf16 v[200:203], v[196:199], a[208:211], v[200:203]\n"
      "v_mul_f32 v151, v248, v151\n"
      "v_cvt_pk_bf16_f32 v147, v150, v151\n"
      "v_mfma_f32_16x16x32_bf16 v[240:243], v[188:191], a[176:179], v[240:243]\n"
      "v_mul_f32 v209, v248, v209\n"
      "v_mul_f32_e64 v208, v248, v208\n"
      "v_mfma_f32_16x16x32_bf16 v[240:243], v[196:199], a[212:215], v[240:243]\n"
      "v_mul_f32 v210, v248, v210\n"
      "v_cvt_pk_bf16_f32 v32, v208, v209\n"
      "v_mfma_f32_16x16x32_bf16 v[196:199], a[0:3], v[140:143], 0\n"
      "v_mul_f32 v211, v248, v211\n"
      "v_cvt_pk_bf16_f32 v33, v210, v211\n"
      "v_mfma_f32_16x16x32_bf16 v[196:199], a[36:39], v[156:159], v[196:199]\n"
      "v_cvt_pk_bf16_f32 v206, v152, v153\n"
      "v_cvt_pk_bf16_f32 v207, v154, v155\n"
      "v_mfma_f32_16x16x32_bf16 v[188:191], a[4:7], v[140:143], 0\n"
      "v_cvt_pk_bf16_f32 v224, v224, v225\n"
      "v_cvt_pk_bf16_f32 v225, v226, v227\n"
      "v_mfma_f32_16x16x32_bf16 v[188:191], a[40:43], v[156:159], v[188:191]\n"
      "ds_read_b128 v[168:171], v119 offset:6144\n"
      "v_cvt_pk_bf16_f32 v226, v200, v201\n"
      "v_mfma_f32_16x16x32_bf16 v[152:155], a[8:11], v[140:143], 0\n"
      "v_cvt_pk_bf16_f32 v227, v202, v203\n"
      "v_cvt_pk_bf16_f32 v160, v160, v161\n"
      "v_mfma_f32_16x16x32_bf16 v[152:155], a[44:47], v[156:159], v[152:155]\n"
      "v_cvt_pk_bf16_f32 v161, v162, v163\n"
      "v_cvt_pk_bf16_f32 v162, v240, v241\n"
      "v_mfma_f32_16x16x32_bf16 v[200:203], a[12:15], v[140:143], 0\n"
      "v_fma_f32 v216, v180, v180, s38\n"
      "v_cvt_pk_bf16_f32 v163, v242, v243\n"
      "v_mfma_f32_16x16x32_bf16 v[200:203], a[48:51], v[156:159], v[200:203]\n"
      "v_fmac_f32 v216, v181, v181\n"
      "v_fma_f32 v217, v136, v136, s38\n"
      "v_mfma_f32_16x16x32_bf16 v[148:151], a[16:19], v[140:143], 0\n"
      "v_fmac_f32 v216, v182, v182\n"
      "v_fmac_f32_e64 v217, v137, v137\n"
      "v_mfma_f32_16x16x32_bf16 v[148:151], a[52:55], v[156:159], v[148:151]\n"
      "v_fmac_f32 v217, v138, v138\n"
      "v_fmac_f32 v216, v183, v183\n"
      "v_mfma_f32_16x16x32_bf16 v[240:243], a[20:23], v[140:143], 0\n"
      "v_fmac_f32 v216, v176, v176\n"
      "v_fmac_f32 v217, v139, v139\n"
      "v_mfma_f32_16x16x32_bf16 v[240:243], a[56:59], v[156:159], v[240:243]\n"
      "s_waitcnt lgkmcnt(0)\n"
      "v_fmac_f32 v217, v192, v192\n"
      "v_fmac_f32_e64 v216, v177, v177\n"
      "v_mfma_f32_16x16x32_bf16 v[208:211], v[140:143], a[24:27], 0\n"
      "global_store_dwordx4 v116, v[168:171], s[54:55] offset:128 sc1\n"
      "v_mfma_f32_16x16x32_bf16 v[208:211], v[156:159], a[60:63], v[208:211]\n"
      "ds_read_b128 v[252:255], v114 offset:34816\n"
      "v_fmac_f32_e64 v217, v193, v193\n"
      "v_mfma_f32_16x16x32_bf16 v[248:251], v[140:143], a[28:31], 0\n"
      "v_fmac_f32 v216, v178, v178\n"
      "v_fmac_f32 v217, v194, v194\n"
      "v_mfma_f32_16x16x32_bf16 v[248:251], v[156:159], a[64:67], v[248:251]\n"
      "v_fmac_f32 v216, v179, v179\n"
      "v_fmac_f32 v217, v195, v195\n"
      "v_mfma_f32_16x16x32_bf16 v[220:223], v[140:143], a[32:35], 0\n"
      "ds_read_b128 v[168:171], v115 offset:34816\n"
      "v_fmac_f32 v216, v184, v184\n"
      "v_mfma_f32_16x16x32_bf16 v[220:223], v[156:159], a[68:71], v[220:223]\n"
      "v_fmac_f32 v217, v164, v164\n"
      "v_fmac_f32 v216, v185, v185\n"
      "v_mfma_f32_16x16x32_bf16 v[196:199], a[72:75], v[172:175], v[196:199]\n"
      "v_fmac_f32 v217, v165, v165\n"
      "v_fmac_f32_e64 v217, v166, v166\n"
      "v_mfma_f32_16x16x32_bf16 v[196:199], a[108:111], v[236:239], v[196:199]\n"
      "ds_read_b128 v[140:143], v132 offset:1200\n"
      "v_fmac_f32 v216, v186, v186\n"
      "v_mfma_f32_16x16x32_bf16 v[188:191], a[76:79], v[172:175], v[188:191]\n"
      "v_fmac_f32 v217, v167, v167\n"
      "v_fmac_f32_e64 v216, v187, v187\n"
      "v_mfma_f32_16x16x32_bf16 v[188:191], a[112:115], v[236:239], v[188:191]\n"
      "s_nop 0\n"
      "v_permlane16_swap_b32 v216, v217\n"
      "v_mfma_f32_16x16x32_bf16 v[152:155], a[80:83], v[172:175], v[152:155]\n"
      "v_add_f32 v216, v216, v217\n"
      "v_mov_b32 v217, v216\n"
      "v_mfma_f32_16x16x32_bf16 v[152:155], a[116:119], v[236:239], v[152:155]\n"
      "s_nop 0\n"
      "v_permlane32_swap_b32 v216, v217\n"
      "v_mfma_f32_16x16x32_bf16 v[200:203], a[84:87], v[172:175], v[200:203]\n"
      "v_add_f32_e64 v216, v216, v217\n"
      "v_mfma_f32_16x16x32_bf16 v[200:203], a[120:123], v[236:239], v[200:203]\n"
      "v_rsq_f32 v216, v216\n"
      "v_mfma_f32_16x16x32_bf16 v[148:151], a[88:91], v[172:175], v[148:151]\n"
      "v_mov_b32 v217, v216\n"
      "v_mfma_f32_16x16x32_bf16 v[148:151], a[124:127], v[236:239], v[148:151]\n"
      "s_nop 0\n"
      "v_permlane16_swap_b32 v216, v217\n"
      "v_mfma_f32_16x16x32_bf16 v[240:243], a[92:95], v[172:175], v[240:243]\n"
      "s_waitcnt lgkmcnt(0)\n"
      "v_mul_f32 v192, v217, v192\n"
      "v_mul_f32_e64 v136, v217, v136\n"
      "v_mfma_f32_16x16x32_bf16 v[240:243], a[128:131], v[236:239], v[240:243]\n"
      "ds_read_b64 v[156:157], v132 offset:1192\n"
      "v_mul_f32 v137, v217, v137\n"
      "v_mfma_f32_16x16x32_bf16 v[208:211], v[172:175], a[96:99], v[208:211]\n"
      "v_mul_f32 v138, v217, v138\n"
      "v_cvt_pk_bf16_f32 v136, v136, v137\n"
      "v_mfma_f32_16x16x32_bf16 v[208:211], v[236:239], a[132:135], v[208:211]\n"
      "v_mul_f32 v194, v217, v194\n"
      "v_mul_f32 v139, v217, v139\n"
      "v_mfma_f32_16x16x32_bf16 v[248:251], v[172:175], a[100:103], v[248:251]\n"
      "ds_read_u16 v52, v134 offset:1168\n"
      "v_cvt_pk_bf16_f32 v137, v138, v139\n"
      "v_mfma_f32_16x16x32_bf16 v[248:251], v[236:239], a[136:139], v[248:251]\n"
      "ds_read_u16 v56, v134 offset:1200\n"
      "v_mul_f32_e64 v193, v217, v193\n"
      "v_mfma_f32_16x16x32_bf16 v[220:223], v[172:175], a[104:107], v[220:223]\n"
      "ds_read_b128 v[228:231], v133 offset:1168\n"
      "v_cvt_pk_bf16_f32 v138, v192, v193\n"
      "v_mfma_f32_16x16x32_bf16 v[220:223], v[236:239], a[140:143], v[220:223]\n"
      "v_mul_f32 v164, v217, v164\n"
      "v_mul_f32 v195, v217, v195\n"
      "v_mfma_f32_16x16x32_bf16 v[196:199], a[144:147], v[252:255], v[196:199]\n"
      "ds_read_u16 v60, v134 offset:1232\n"
      "v_mfma_f32_16x16x32_bf16 v[196:199], a[180:183], v[168:171], v[196:199]\n"
      "ds_read_b128 v[236:239], v123\n"
      "v_cvt_pk_bf16_f32 v139, v194, v195\n"
      "v_mfma_f32_16x16x32_bf16 v[188:191], a[148:151], v[252:255], v[188:191]\n"
      "ds_read_b64 v[158:159], v133 offset:1184\n"
      "v_mul_f32 v165, v217, v165\n"
      "v_mfma_f32_16x16x32_bf16 v[188:191], a[184:187], v[168:171], v[188:191]\n"
      "v_mul_f32 v166, v217, v166\n"
      "v_cvt_pk_bf16_f32 v40, v164, v165\n"
      "v_mfma_f32_16x16x32_bf16 v[152:155], a[152:155], v[252:255], v[152:155]\n"
      "ds_read_b128 v[192:195], v123 offset:3072\n"
      "v_mul_f32 v167, v217, v167\n"
      "v_mfma_f32_16x16x32_bf16 v[152:155], a[188:191], v[168:171], v[152:155]\n"
      "v_mul_f32 v216, s36, v216\n"
      "v_cvt_pk_bf16_f32 v41, v166, v167\n"
      "v_mfma_f32_16x16x32_bf16 v[200:203], a[156:159], v[252:255], v[200:203]\n"
      "v_mul_f32 v180, v216, v180\n"
      "v_mul_f32 v181, v216, v181\n"
      "v_mfma_f32_16x16x32_bf16 v[200:203], a[192:195], v[168:171], v[200:203]\n"
      "v_mul_f32 v182, v216, v182\n"
      "v_cvt_pk_bf16_f32 v180, v180, v181\n"
      "v_mfma_f32_16x16x32_bf16 v[148:151], a[160:163], v[252:255], v[148:151]\n"
      "v_mul_f32 v183, v216, v183\n"
      "v_cvt_pk_bf16_f32 v181, v182, v183\n"
      "v_mfma_f32_16x16x32_bf16 v[148:151], a[196:199], v[168:171], v[148:151]\n"
      "v_mul_f32 v179, v216, v179\n"
      "v_mul_f32 v176, v216, v176\n"
      "v_mfma_f32_16x16x32_bf16 v[240:243], a[164:167], v[252:255], v[240:243]\n"
      "v_mul_f32_e64 v177, v216, v177\n"
      "v_cvt_pk_bf16_f32 v182, v176, v177\n"
      "v_mfma_f32_16x16x32_bf16 v[240:243], a[200:203], v[168:171], v[240:243]\n"
      "ds_read_b128 v[164:167], v123 offset:6144\n"
      "v_mfma_f32_16x16x32_bf16 v[208:211], v[252:255], a[168:171], v[208:211]\n"
      "v_mul_f32 v178, v216, v178\n"
      "v_mfma_f32_16x16x32_bf16 v[208:211], v[168:171], a[204:207], v[208:211]\n"
      "ds_read_b128 v[232:235], v123 offset:9216\n"
      "v_mul_f32 v186, v216, v186\n"
      "v_mfma_f32_16x16x32_bf16 v[248:251], v[252:255], a[172:175], v[248:251]\n"
      "v_mul_f32 v187, v216, v187\n"
      "v_mul_f32 v184, v216, v184\n"
      "v_mfma_f32_16x16x32_bf16 v[248:251], v[168:171], a[208:211], v[248:251]\n"
      "s_waitcnt lgkmcnt(2)\n"
      "v_mul_f32 v185, v216, v185\n"
      "v_cvt_pk_bf16_f32 v39, v186, v187\n"
      "v_mfma_f32_16x16x32_bf16 v[220:223], v[252:255], a[176:179], v[220:223]\n"
      "v_fma_f32 v252, v196, v196, s38\n"
      "v_cvt_pk_bf16_f32 v183, v178, v179\n"
      "v_mfma_f32_16x16x32_bf16 v[220:223], v[168:171], a[212:215], v[220:223]\n"
      "v_fma_f32 v253, v200, v200, s38\n"
      "v_fmac_f32 v252, v197, v197\n"
      "v_mfma_f32_16x16x32_bf16 v[168:171], a[216:219], v[236:239], 0\n"
      "v_fmac_f32 v252, v198, v198\n"
      "v_fmac_f32 v253, v201, v201\n"
      "v_mfma_f32_16x16x32_bf16 v[168:171], a[228:231], v[192:195], v[168:171]\n"
      "v_cvt_pk_bf16_f32 v38, v184, v185\n"
      "v_fmac_f32 v253, v202, v202\n"
      "v_mfma_f32_16x16x32_bf16 v[172:175], a[220:223], v[236:239], 0\n"
      "v_fmac_f32 v252, v199, v199\n"
      "v_fmac_f32 v253, v203, v203\n"
      "v_mfma_f32_16x16x32_bf16 v[172:175], a[232:235], v[192:195], v[172:175]\n"
      "v_fmac_f32 v252, v188, v188\n"
      "v_fmac_f32 v253, v148, v148\n"
      "v_mfma_f32_16x16x32_bf16 v[176:179], a[224:227], v[236:239], 0\n"
      "s_or_b64 s[34:35], s[48:49], s[50:51]\n"
      "v_fmac_f32 v252, v189, v189\n"
      "v_fmac_f32 v253, v149, v149\n"
      "v_mfma_f32_16x16x32_bf16 v[176:179], a[236:239], v[192:195], v[176:179]\n"
      "s_waitcnt lgkmcnt(0)\n"
      "v_fmac_f32 v253, v150, v150\n"
      "v_fmac_f32 v252, v190, v190\n"
      "v_mfma_f32_16x16x32_bf16 v[168:171], a[240:243], v[164:167], v[168:171]\n"
      "ds_read_b128 v[184:187], v123 offset:12288\n"
      "v_fmac_f32 v252, v191, v191\n"
      "v_mfma_f32_16x16x32_bf16 v[168:171], a[252:255], v[232:235], v[168:171]\n"
      "ds_read_b128 v[192:195], v123 offset:15360\n"
      "v_fmac_f32 v253, v151, v151\n"
      "v_mfma_f32_16x16x32_bf16 v[172:175], a[244:247], v[164:167], v[172:175]\n"
      "v_fmac_f32 v253, v240, v240\n"
      "v_fmac_f32 v252, v152, v152\n"
      "v_mfma_f32_16x16x32_bf16 v[172:175], v[0:3], v[232:235], v[172:175]\n"
      "v_fmac_f32 v253, v241, v241\n"
      "v_fmac_f32 v252, v153, v153\n"
      "v_mfma_f32_16x16x32_bf16 v[176:179], a[248:251], v[164:167], v[176:179]\n"
      "v_fmac_f32 v253, v242, v242\n"
      "v_fmac_f32 v252, v154, v154\n"
      "v_mfma_f32_16x16x32_bf16 v[176:179], v[4:7], v[232:235], v[176:179]\n"
      "v_fmac_f32 v253, v243, v243\n"
      "v_fmac_f32 v252, v155, v155\n"
      "v_mfma_f32_16x16x32_bf16 v[164:167], v[212:215], v[144:147], v[78:81]\n"
      "s_nop 0\n"
      "v_permlane16_swap_b32 v252, v253\n"
      "v_mfma_f32_16x16x32_bf16 v[164:167], v[34:37], v[32:35], v[164:167]\n"
      "v_add_f32 v252, v252, v253\n"
      "v_mov_b32 v253, v252\n"
      "v_add_u32 v132, 2336, v132\n"
      "v_add_u32 v133, 2336, v133\n"
      "v_permlane32_swap_b32 v252, v253\n"
      "v_add_f32 v252, v252, v253\n"
      "v_rsq_f32 v252, v252\n"
      "s_waitcnt lgkmcnt(0)\n"
      "v_mfma_f32_16x16x32_bf16 v[168:171], v[8:11], v[184:187], v[168:171]\n"
      "v_add_u32 v134, 2336, v134\n"
      "v_mov_b32 v253, v252\n"
      "v_mfma_f32_16x16x32_bf16 v[168:171], v[20:23], v[192:195], v[168:171]\n"
      "s_nop 0\n"
      "v_permlane16_swap_b32 v252, v253\n"
      "v_mul_f32 v202, v253, v202\n"
      "v_mfma_f32_16x16x32_bf16 v[172:175], v[12:15], v[184:187], v[172:175]\n"
      "v_mul_f32 v200, v253, v200\n"
      "v_mul_f32 v201, v253, v201\n"
      "v_mfma_f32_16x16x32_bf16 v[172:175], v[24:27], v[192:195], v[172:175]\n"
      "v_mul_f32 v148, v253, v148\n"
      "v_cvt_pk_bf16_f32 v200, v200, v201\n"
      "v_mfma_f32_16x16x32_bf16 v[176:179], v[16:19], v[184:187], v[176:179]\n"
      "v_mul_f32 v203, v253, v203\n"
      "v_cvt_pk_bf16_f32 v201, v202, v203\n"
      "v_mfma_f32_16x16x32_bf16 v[176:179], v[28:31], v[192:195], v[176:179]\n"
      "v_mul_f32 v149, v253, v149\n"
      "v_mul_f32 v151, v253, v151\n"
      "v_mul_f32 v243, v253, v243\n"
      "v_mfma_f32_16x16x32_bf16 v[184:187], v[136:139], v[144:147], v[82:85]\n"
      "v_mul_f32 v150, v253, v150\n"
      "v_cvt_pk_bf16_f32 v202, v148, v149\n"
      "v_mfma_f32_16x16x32_bf16 v[184:187], v[40:43], v[32:35], v[184:187]\n"
      "v_mul_f32_e64 v252, s36, v252\n"
      "v_cvt_pk_bf16_f32 v203, v150, v151\n"
      "v_mfma_f32_16x16x32_bf16 v[148:151], v[140:143], v[144:147], v[70:73]\n"
      "v_mul_f32 v240, v253, v240\n"
      "v_mul_f32 v241, v253, v241\n"
      "v_mfma_f32_16x16x32_bf16 v[148:151], v[228:231], v[212:215], v[148:151]\n"
      "v_mul_f32_e64 v242, v253, v242\n"
      "v_cvt_pk_bf16_f32 v46, v240, v241\n"
      "v_mfma_f32_16x16x32_bf16 v[148:151], v[156:159], v[32:35], v[148:151]\n"
      "v_mul_f32 v154, v252, v154\n"
      "v_mul_f32 v152, v252, v152\n"
      "v_mul_f32_e64 v196, v252, v196\n"
      "v_cvt_pk_bf16_f32 v47, v242, v243\n"
      "v_mfma_f32_16x16x32_bf16 v[192:195], v[200:203], v[144:147], v[86:89]\n"
      "s_waitcnt vmcnt(2)\n"
      "v_mul_f32 v197, v252, v197\n"
      "v_cvt_pk_bf16_f32 v196, v196, v197\n"
      "v_mfma_f32_16x16x32_bf16 v[192:195], v[46:49], v[32:35], v[192:195]\n"
      "ds_read_b32 v150, v122\n"
      "ds_read_b128 v[144:147], v119 offset:36864\n"
      "v_mul_f32_e64 v190, v252, v190\n"
      "v_fma_f32 v151, v168, v168, s39\n"
      "v_cvt_pk_bf16_f32 v58, v220, v221\n"
      "v_mul_f32 v199, v252, v199\n"
      "v_cvt_pk_bf16_f32 v54, v248, v249\n"
      "v_mul_f32 v155, v252, v155\n"
      "v_cvt_pk_bf16_f32 v51, v210, v211\n"
      "v_cvt_pk_bf16_f32 v55, v250, v251\n"
      "v_mul_f32 v153, v252, v153\n"
      "v_mul_f32 v198, v252, v198\n"
      "v_fmac_f32 v151, v169, v169\n"
      "v_mul_f32 v188, v252, v188\n"
      "v_mul_f32 v189, v252, v189\n"
      "v_cvt_pk_bf16_f32 v197, v198, v199\n"
      "v_fmac_f32 v151, v170, v170\n"
      "v_fmac_f32_e64 v151, v171, v171\n"
      "v_cvt_pk_bf16_f32 v59, v222, v223\n"
      "v_cvt_pk_bf16_f32 v50, v208, v209\n"
      "v_fmac_f32 v151, v172, v172\n"
      "v_cvt_pk_bf16_f32 v44, v152, v153\n"
      "v_fmac_f32 v151, v173, v173\n"
      "v_mul_f32 v191, v252, v191\n"
      "v_fmac_f32 v151, v174, v174\n"
      "v_fmac_f32 v151, v175, v175\n"
      "v_cvt_pk_bf16_f32 v45, v154, v155\n"
      "v_cvt_pk_bf16_f32 v198, v188, v189\n"
      "v_cvt_pk_bf16_f32 v199, v190, v191\n"
      "v_fmac_f32 v151, v176, v176\n"
      "v_fmac_f32 v151, v177, v177\n"
      "v_fmac_f32_e64 v151, v178, v178\n"
      "v_fmac_f32_e64 v151, v179, v179\n"
      "s_waitcnt lgkmcnt(0)\n"
      "v_or3_b32 v144, v144, v145, v146\n"
      "v_and_b32 v144, s40, v144\n"
      "v_bitop3_b32 v150, v150, v147, s40 bitop3:0xa8\n"
      "v_or3_b32 v144, v144, v150, s4\n"
      "ds_write_b32 v129, v144 offset:2048\n"
      "s_cbranch_scc1 L_gen_19_%=\n"
      "L_fast_22_%=:\n"
      "v_mfma_f32_16x16x32_bf16 v[152:155], v[212:215], v[180:183], v[90:93]\n"
      "ds_read_b128 v[188:191], v119 offset:12288\n"
      "v_exp_f32 v184, v184\n"
      "v_exp_f32 v165, v165\n"
      "v_exp_f32_e64 v167, v167\n"
      "v_mfma_f32_16x16x32_bf16 v[152:155], v[34:37], v[38:41], v[152:155]\n"
      "ds_read_b128 v[144:147], v123 offset:256\n"
      "v_exp_f32_e64 v164, v164\n"
      "v_mfma_f32_16x16x32_bf16 v[220:223], v[136:139], v[180:183], v[94:97]\n"
      "ds_read_b128 v[248:251], v123 offset:3328\n"
      "v_exp_f32_e64 v187, v187\n"
      "v_mfma_f32_16x16x32_bf16 v[240:243], v[140:143], v[180:183], v[72:75]\n"
      "ds_read_b128 v[252:255], v123 offset:6400\n"
      "v_cvt_pk_bf16_f32 v164, v164, v165\n"
      "v_mfma_f32_16x16x32_bf16 v[240:243], v[228:231], v[136:139], v[240:243]\n"
      "v_exp_f32_e64 v166, v166\n"
      "v_mfma_f32_16x16x32_bf16 v[240:243], v[156:159], v[38:41], v[240:243]\n"
      "ds_read_b128 v[208:211], v128 offset:2048\n"
      "v_exp_f32 v186, v186\n"
      "v_mfma_f32_16x16x32_bf16 v[220:223], v[40:43], v[38:41], v[220:223]\n"
      "ds_read_b128 v[236:239], v123 offset:9472\n"
      "v_exp_f32 v193, v193\n"
      "v_exp_f32 v195, v195\n"
      "v_exp_f32 v185, v185\n"
      "v_exp_f32 v194, v194\n"
      "v_cvt_pk_bf16_f32 v165, v166, v167\n"
      "v_mfma_f32_16x16x32_bf16 v[232:235], v[200:203], v[180:183], v[98:101]\n"
      "s_waitcnt lgkmcnt(4)\n"
      "v_exp_f32 v192, v192\n"
      "v_mfma_f32_16x16x32_bf16 v[232:235], v[46:49], v[38:41], v[232:235]\n"
      "s_waitcnt lgkmcnt(3)\n"
      "v_cvt_pk_bf16_f32 v166, v184, v185\n"
      "v_cvt_pk_bf16_f32 v167, v186, v187\n"
      "v_mfma_f32_16x16x32_bf16 v[180:183], a[216:219], v[144:147], 0\n"
      "global_store_dwordx4 v116, v[188:191], s[54:55] offset:256 sc1\n"
      "v_cvt_pk_bf16_f32 v62, v192, v193\n"
      "v_cvt_pk_bf16_f32 v63, v194, v195\n"
      "v_mfma_f32_16x16x32_bf16 v[180:183], a[228:231], v[248:251], v[180:183]\n"
      "v_exp_f32 v148, v148\n"
      "v_mfma_f32_16x16x32_bf16 v[184:187], a[220:223], v[144:147], 0\n"
      "s_waitcnt lgkmcnt(1)\n"
      "v_bitop3_b32 v208, v208, v209, s4 bitop3:0x7e\n"
      "v_bitop3_b32 v210, v210, v211, s4 bitop3:0x7e\n"
      "v_mfma_f32_16x16x32_bf16 v[184:187], a[232:235], v[248:251], v[184:187]\n"
      "s_waitcnt lgkmcnt(0)\n"
      "v_or_b32 v208, v208, v210\n"
      "v_exp_f32_e64 v152, v152\n"
      "v_cmp_ne_u32 vcc, 0, v208\n"
      "v_mfma_f32_16x16x32_bf16 v[188:191], a[224:227], v[144:147], 0\n"
      "s_mov_b64 s[48:49], vcc\n"
      "ds_read_b128 v[192:195], v123 offset:12544\n"
      "v_mfma_f32_16x16x32_bf16 v[188:191], a[236:239], v[248:251], v[188:191]\n"
      "ds_read_b128 v[144:147], v123 offset:15616\n"
      "v_exp_f32 v220, v220\n"
      "v_mfma_f32_16x16x32_bf16 v[180:183], a[240:243], v[252:255], v[180:183]\n"
      "s_cbranch_vccnz L_flg_23_%=\n"
      "L_fb_24_%=:\n"
      "v_mfma_f32_16x16x32_bf16 v[180:183], a[252:255], v[236:239], v[180:183]\n"
      "ds_read_b128 v[208:211], v120\n"
      "v_exp_f32 v221, v221\n"
      "v_mfma_f32_16x16x32_bf16 v[184:187], a[244:247], v[252:255], v[184:187]\n"
      "v_exp_f32 v154, v154\n"
      "v_mfma_f32_16x16x32_bf16 v[184:187], v[0:3], v[236:239], v[184:187]\n"
      "v_exp_f32 v153, v153\n"
      "v_exp_f32 v155, v155\n"
      "v_cvt_pk_bf16_f32 v64, v148, 0\n"
      "v_cvt_pk_bf16_f32 v152, v152, v153\n"
      "v_mfma_f32_16x16x32_bf16 v[188:191], a[248:251], v[252:255], v[188:191]\n"
      "ds_read_b128 v[252:255], v123 offset:512\n"
      "v_cvt_pk_bf16_f32 v153, v154, v155\n"
      "v_mfma_f32_16x16x32_bf16 v[188:191], v[4:7], v[236:239], v[188:191]\n"
      "ds_read_b128 v[236:239], v123 offset:3584\n"
      "s_waitcnt lgkmcnt(3)\n"
      "v_cvt_pk_bf16_f32 v154, v220, v221\n"
      "v_mfma_f32_16x16x32_bf16 v[180:183], v[8:11], v[192:195], v[180:183]\n"
      "v_exp_f32 v222, v222\n"
      "v_mfma_f32_16x16x32_bf16 v[180:183], v[20:23], v[144:147], v[180:183]\n"
      "v_exp_f32 v223, v223\n"
      "v_mfma_f32_16x16x32_bf16 v[184:187], v[12:15], v[192:195], v[184:187]\n"
      "s_waitcnt lgkmcnt(2)\n"
      "v_exp_f32 v232, v232\n"
      "v_mfma_f32_16x16x32_bf16 v[184:187], v[24:27], v[144:147], v[184:187]\n"
      "global_store_dwordx4 v117, v[208:211], s[54:55] sc1\n"
      "v_mfma_f32_16x16x32_bf16 v[188:191], v[16:19], v[192:195], v[188:191]\n"
      "s_nop 0\n"
      "v_fma_f32 v148, v180, v180, s39\n"
      "v_cvt_pk_bf16_f32 v155, v222, v223\n"
      "v_mfma_f32_16x16x32_bf16 v[188:191], v[28:31], v[144:147], v[188:191]\n"
      "v_exp_f32 v233, v233\n"
      "v_mfma_f32_16x16x32_bf16 v[192:195], v[66:69], v[164:167], 0\n"
      "v_exp_f32 v234, v234\n"
      "v_mfma_f32_16x16x32_bf16 v[192:195], v[66:69], v[62:65], v[192:195]\n"
      "v_exp_f32 v235, v235\n"
      "v_mfma_f32_16x16x32_bf16 v[144:147], v[204:207], v[164:167], 0\n"
      "s_waitcnt lgkmcnt(0)\n"
      "v_exp_f32 v240, v240\n"
      "v_mfma_f32_16x16x32_bf16 v[144:147], v[50:53], v[62:65], v[144:147]\n"
      "v_fmac_f32 v148, v181, v181\n"
      "v_fmac_f32 v148, v182, v182\n"
      "v_mfma_f32_16x16x32_bf16 v[208:211], v[224:227], v[164:167], 0\n"
      "v_fmac_f32 v148, v183, v183\n"
      "v_fmac_f32 v148, v184, v184\n"
      "v_mfma_f32_16x16x32_bf16 v[208:211], v[54:57], v[62:65], v[208:211]\n"
      "v_fmac_f32 v148, v185, v185\n"
      "v_fmac_f32 v148, v186, v186\n"
      "v_mfma_f32_16x16x32_bf16 v[220:223], v[160:163], v[164:167], 0\n"
      "v_fmac_f32 v148, v187, v187\n"
      "v_fmac_f32 v148, v188, v188\n"
      "v_mfma_f32_16x16x32_bf16 v[220:223], v[58:61], v[62:65], v[220:223]\n"
      "v_fmac_f32 v148, v189, v189\n"
      "v_fmac_f32 v148, v190, v190\n"
      "v_mfma_f32_16x16x32_bf16 v[164:167], v[212:215], v[196:199], v[102:105]\n"
      "v_fmac_f32 v148, v191, v191\n"
      "v_cvt_pk_bf16_f32 v62, v232, v233\n"
      "v_mfma_f32_16x16x32_bf16 v[164:167], v[34:37], v[44:47], v[164:167]\n"
      "ds_read_b128 v[212:215], v121\n"
      "v_cvt_pk_bf16_f32 v63, v234, v235\n"
      "v_mfma_f32_16x16x32_bf16 v[232:235], v[136:139], v[196:199], v[106:109]\n"
      "v_cndmask_b32 v149, v149, v241, s[42:43]\n"
      "v_cvt_pk_bf16_f32 v64, v240, 0\n"
      "v_mfma_f32_16x16x32_bf16 v[232:235], v[40:43], v[44:47], v[232:235]\n"
      "v_rcp_f32 v192, v192\n"
      "v_mfma_f32_16x16x32_bf16 v[136:139], v[140:143], v[196:199], v[74:77]\n"
      "v_mul_f32 v145, v192, v145\n"
      "v_mul_f32 v144, v192, v144\n"
      "v_mfma_f32_16x16x32_bf16 v[136:139], v[228:231], v[200:203], v[136:139]\n"
      "v_mul_f32 v146, v192, v146\n"
      "v_cvt_pk_bf16_f32 v144, v144, v145\n"
      "v_mfma_f32_16x16x32_bf16 v[136:139], v[156:159], v[44:47], v[136:139]\n"
      "v_mul_f32 v147, v192, v147\n"
      "v_cvt_pk_bf16_f32 v145, v146, v147\n"
      "v_mfma_f32_16x16x32_bf16 v[240:243], v[200:203], v[196:199], v[110:113]\n"
      "ds_read_b128 v[248:251], v123 offset:6656\n"
      "v_mul_f32 v208, v192, v208\n"
      "v_mfma_f32_16x16x32_bf16 v[240:243], v[46:49], v[44:47], v[240:243]\n"
      "v_mul_f32_e64 v209, v192, v209\n"
      "v_cvt_pk_bf16_f32 v146, v208, v209\n"
      "v_mfma_f32_16x16x32_bf16 v[196:199], v[228:231], v[140:143], v[76:79]\n"
      "ds_read_b128 v[140:143], v123 offset:9728\n"
      "v_mul_f32 v210, v192, v210\n"
      "v_mfma_f32_16x16x32_bf16 v[228:231], v[66:69], v[152:155], 0\n"
      "ds_read_b128 v[216:219], v123 offset:12800\n"
      "v_mul_f32 v211, v192, v211\n"
      "v_mfma_f32_16x16x32_bf16 v[228:231], v[66:69], v[62:65], v[228:231]\n"
      "s_waitcnt lgkmcnt(3)\n"
      "v_cvt_pk_bf16_f32 v147, v210, v211\n"
      "v_mul_f32 v220, v192, v220\n"
      "v_mfma_f32_16x16x16_bf16 v[196:199], v[158:159], v[156:157], v[196:199]\n"
      "ds_write_b128 v124, v[144:147] offset:18432\n"
      "v_mfma_f32_16x16x32_bf16 v[144:147], v[204:207], v[152:155], 0\n"
      "v_mul_f32_e64 v221, v192, v221\n"
      "v_cvt_pk_bf16_f32 v220, v220, v221\n"
      "v_mfma_f32_16x16x32_bf16 v[144:147], v[50:53], v[62:65], v[144:147]\n"
      "s_mov_b32 exec_hi, 0\n"
      "global_store_dwordx4 v118, v[212:215], s[54:55] sc1\n"
      "s_mov_b32 exec_hi, -1\n"
      "v_mfma_f32_16x16x32_bf16 v[156:159], v[224:227], v[152:155], 0\n"
      "s_add_u32 s28, s27, 2\n"
      "v_mul_f32 v222, v192, v222\n"
      "v_mul_f32 v223, v192, v223\n"
      "v_mfma_f32_16x16x32_bf16 v[156:159], v[54:57], v[62:65], v[156:159]\n"
      "s_waitcnt lgkmcnt(2)\n"
      "v_cndmask_b32 v149, v149, v137, s[44:45]\n"
      "v_cvt_pk_bf16_f32 v221, v222, v223\n"
      "v_mfma_f32_16x16x32_bf16 v[212:215], v[160:163], v[152:155], 0\n"
      "s_min_u32 m0, s28, s3\n"
      "v_cndmask_b32 v149, v149, v197, s[46:47]\n"
      "s_movrels_b32 s9, s80\n"
      "v_mfma_f32_16x16x32_bf16 v[212:215], v[58:61], v[62:65], v[212:215]\n"
      "ds_write_b64 v125, v[220:221] offset:18432\n"
      "v_mfma_f32_16x16x32_bf16 v[192:195], a[216:219], v[252:255], 0\n"
      "s_lshr_b32 s8, s9, 8\n"
      "v_exp_f32 v149, v149\n"
      "v_mfma_f32_16x16x32_bf16 v[192:195], a[228:231], v[236:239], v[192:195]\n"
      "s_and_b32 s9, s9, 0xff\n"
      "v_cvt_pk_bf16_f32 v149, v149, v149\n"
      "v_exp_f32 v164, v164\n"
      "v_mfma_f32_16x16x32_bf16 v[196:199], a[220:223], v[252:255], 0\n"
      "s_mul_i32 s10, s8, 73\n"
      "v_exp_f32 v165, v165\n"
      "v_mfma_f32_16x16x32_bf16 v[196:199], a[232:235], v[236:239], v[196:199]\n"
      "ds_write_b16 v130, v149\n"
      "v_mfma_f32_16x16x32_bf16 v[200:203], a[224:227], v[252:255], 0\n"
      "ds_read_b128 v[252:255], v131\n"
      "v_cvt_pk_bf16_f32 v164, v164, v165\n"
      "v_mfma_f32_16x16x32_bf16 v[200:203], a[236:239], v[236:239], v[200:203]\n"
      "ds_read_b128 v[236:239], v131 offset:64\n"
      "v_exp_f32 v166, v166\n"
      "v_mfma_f32_16x16x32_bf16 v[192:195], a[240:243], v[248:251], v[192:195]\n"
      "ds_read_b128 v[152:155], v123 offset:15872\n"
      "s_add_u32 s10, s10, s9\n"
      "v_mfma_f32_16x16x32_bf16 v[192:195], a[252:255], v[140:143], v[192:195]\n"
      "ds_read_b128 v[220:223], v114\n"
      "v_exp_f32_e64 v167, v167\n"
      "v_mfma_f32_16x16x32_bf16 v[196:199], a[244:247], v[248:251], v[196:199]\n"
      "s_mul_i32 s28, s10, 4802\n"
      "v_exp_f32 v232, v232\n"
      "v_mfma_f32_16x16x32_bf16 v[196:199], v[0:3], v[140:143], v[196:199]\n"
      "s_add_u32 s56, s18, s28\n"
      "v_cvt_pk_bf16_f32 v165, v166, v167\n"
      "v_exp_f32_e64 v233, v233\n"
      "v_mfma_f32_16x16x32_bf16 v[200:203], a[248:251], v[248:251], v[200:203]\n"
      "ds_read_b128 v[248:251], v115\n"
      "v_cvt_pk_bf16_f32 v166, v232, v233\n"
      "v_mfma_f32_16x16x32_bf16 v[200:203], v[4:7], v[140:143], v[200:203]\n"
      "s_addc_u32 s57, s19, 0\n"
      "v_exp_f32 v240, v240\n"
      "v_exp_f32 v235, v235\n"
      "v_exp_f32_e64 v234, v234\n"
      "s_waitcnt lgkmcnt(3)\n"
      "v_mfma_f32_16x16x32_bf16 v[208:211], v[252:255], v[66:69], 0\n"
      "s_and_b32 s56, s56, -16\n"
      "v_cvt_pk_bf16_f32 v167, v234, v235\n"
      "s_waitcnt lgkmcnt(2)\n"
      "v_mfma_f32_16x16x32_bf16 v[208:211], v[236:239], v[66:69], v[208:211]\n"
      "s_sub_u32 s58, s56, 110592\n"
      "v_exp_f32 v241, v241\n"
      "v_mfma_f32_16x16x32_bf16 v[192:195], v[8:11], v[216:219], v[192:195]\n"
      "s_subb_u32 s59, s57, 0\n"
      "v_exp_f32_e64 v242, v242\n"
      "v_mfma_f32_16x16x32_bf16 v[192:195], v[20:23], v[152:155], v[192:195]\n"
      "s_sub_u32 s56, s56, 73728\n"
      "v_exp_f32 v243, v243\n"
      "v_mfma_f32_16x16x32_bf16 v[196:199], v[12:15], v[216:219], v[196:199]\n"
      "s_subb_u32 s57, s57, 0\n"
      "v_exp_f32 v136, v136\n"
      "v_mfma_f32_16x16x32_bf16 v[196:199], v[24:27], v[152:155], v[196:199]\n"
      "s_waitcnt lgkmcnt(0)\n"
      "v_cvt_pk_bf16_f32 v63, v242, v243\n"
      "v_cvt_pk_bf16_f32 v62, v240, v241\n"
      "v_mfma_f32_16x16x32_bf16 v[200:203], v[16:19], v[216:219], v[200:203]\n"
      "s_mul_i32 s28, s8, 1373568\n"
      "v_cvt_pk_bf16_f32 v64, v136, 0\n"
      "v_fma_f32 v149, v192, v192, s39\n"
      "v_mfma_f32_16x16x32_bf16 v[200:203], v[28:31], v[152:155], v[200:203]\n"
      "s_mul_i32 s29, s9, 2688\n"
      "v_rcp_f32 v228, v228\n"
      "v_mfma_f32_16x16x32_bf16 v[136:139], v[66:69], v[164:167], 0\n"
      "s_add_u32 s28, s28, s29\n"
      "v_mul_f32 v144, v228, v144\n"
      "v_mul_f32 v145, v228, v145\n"
      "v_mfma_f32_16x16x32_bf16 v[136:139], v[66:69], v[62:65], v[136:139]\n"
      "s_cmp_eq_u32 s8, 72\n"
      "v_mul_f32_e64 v146, v228, v146\n"
      "v_cvt_pk_bf16_f32 v144, v144, v145\n"
      "v_mfma_f32_16x16x32_bf16 v[140:143], v[204:207], v[164:167], 0\n"
      "s_cselect_b32 s8, 0, 100270464\n"
      "v_mul_f32_e64 v147, v228, v147\n"
      "v_cvt_pk_bf16_f32 v145, v146, v147\n"
      "v_mfma_f32_16x16x32_bf16 v[140:143], v[50:53], v[62:65], v[140:143]\n"
      "s_cmp_eq_u32 s9, 72\n"
      "v_mul_f32 v157, v228, v157\n"
      "v_mul_f32 v156, v228, v156\n"
      "v_mfma_f32_16x16x32_bf16 v[152:155], v[224:227], v[164:167], 0\n"
      "s_cselect_b32 s9, 0, 196224\n"
      "v_mul_f32_e64 v158, v228, v158\n"
      "v_cvt_pk_bf16_f32 v146, v156, v157\n"
      "v_mfma_f32_16x16x32_bf16 v[152:155], v[54:57], v[62:65], v[152:155]\n"
      "s_add_u32 s10, s8, s9\n"
      "v_mul_f32 v159, v228, v159\n"
      "v_cvt_pk_bf16_f32 v147, v158, v159\n"
      "v_mfma_f32_16x16x32_bf16 v[156:159], v[160:163], v[164:167], 0\n"
      "s_add_u32 s28, s28, s10\n"
      "v_mul_f32 v213, v228, v213\n"
      "v_mul_f32_e64 v212, v228, v212\n"
      "v_mfma_f32_16x16x32_bf16 v[156:159], v[58:61], v[62:65], v[156:159]\n"
      "ds_write_b128 v124, v[144:147] offset:18688\n"
      "v_mfma_f32_16x16x32_bf16 v[164:167], v[252:255], v[204:207], 0\n"
      "s_add_u32 s52, s16, s28\n"
      "v_cvt_pk_bf16_f32 v212, v212, v213\n"
      "v_mul_f32 v214, v228, v214\n"
      "v_mfma_f32_16x16x32_bf16 v[164:167], v[236:239], v[50:53], v[164:167]\n"
      "s_addc_u32 s53, s17, 0\n"
      "v_mul_f32 v215, v228, v215\n"
      "v_cvt_pk_bf16_f32 v213, v214, v215\n"
      "v_mfma_f32_16x16x32_bf16 v[144:147], v[252:255], v[224:227], 0\n"
      "s_add_u32 s27, s27, 1\n"
      "v_fmac_f32 v149, v193, v193\n"
      "v_fmac_f32 v149, v194, v194\n"
      "v_mfma_f32_16x16x32_bf16 v[144:147], v[236:239], v[54:57], v[144:147]\n"
      "ds_write_b64 v125, v[212:213] offset:18688\n"
      "v_mfma_f32_16x16x32_bf16 v[212:215], v[252:255], v[160:163], 0\n"
      "s_cmp_ge_u32 s27, s26\n"
      "v_fmac_f32 v149, v195, v195\n"
      "v_fmac_f32 v149, v196, v196\n"
      "v_mfma_f32_16x16x32_bf16 v[212:215], v[236:239], v[58:61], v[212:215]\n"
      "v_fmac_f32 v149, v197, v197\n"
      "v_fmac_f32 v149, v198, v198\n"
      "v_fmac_f32 v149, v199, v199\n"
      "v_fmac_f32 v149, v200, v200\n"
      "v_fmac_f32 v149, v201, v201\n"
      "v_permlane16_swap_b32 v151, v148\n"
      "v_mov_b32_dpp v208, v208 quad_perm:[0,0,0,0] row_mask:0xf bank_mask:0xf\n"
      "v_add_f32 v151, v151, v148\n"
      "v_rcp_f32 v208, v208\n"
      "v_fmac_f32_e64 v149, v202, v202\n"
      "v_mfma_f32_16x16x32_bf16 v[236:239], a[4:7], v[220:223], 0\n"
      "s_cselect_b32 s28, 1, 0\n"
      "v_fmac_f32 v149, v203, v203\n"
      "v_mov_b32 v150, v149\n"
      "v_mfma_f32_16x16x32_bf16 v[236:239], a[40:43], v[248:251], v[236:239]\n"
      "s_xor_b32 s29, s10, s11\n"
      "v_permlane16_swap_b32 v149, v150\n"
      "v_cndmask_b32 v164, v164, v144, s[42:43]\n"
      "v_mfma_f32_16x16x32_bf16 v[160:163], a[0:3], v[220:223], 0\n"
      "s_or_b32 s28, s28, s29\n"
      "v_add_f32_e64 v149, v149, v150\n"
      "v_cndmask_b32 v164, v164, v212, s[44:45]\n"
      "v_mul_f32_e64 v164, v208, v164\n"
      "v_permlane32_swap_b32_e64 v151, v149\n"
      "v_cvt_pk_bf16_f32 v164, v164, v164\n"
      "v_add_f32 v151, v151, v149\n"
      "v_mfma_f32_16x16x32_bf16 v[160:163], a[36:39], v[248:251], v[160:163]\n"
      "ds_write_b32 v129, v151 offset:1024\n"
      "ds_write_b16 v135, v164 offset:16\n"
      "L_join_21_%=:\n"
      "s_cbranch_scc1 L_tail1_%=\n"
      "L_mid_%=:\n"
      "v_mfma_f32_16x16x32_bf16 v[164:167], a[8:11], v[220:223], 0\n"
      "ds_read_b128 v[144:147], v114 offset:6144\n"
      "s_add_u32 m0, s76, 18432\n"
      "v_mfma_f32_16x16x32_bf16 v[164:167], a[44:47], v[248:251], v[164:167]\n"
      "ds_read_b128 v[148:151], v115 offset:6144\n"
      "v_rcp_f32 v136, v136\n"
      "v_mfma_f32_16x16x32_bf16 v[212:215], a[12:15], v[220:223], 0\n"
      "v_mul_f32 v140, v136, v140\n"
      "v_mul_f32_e64 v141, v136, v141\n"
      "v_mfma_f32_16x16x32_bf16 v[212:215], a[48:51], v[248:251], v[212:215]\n"
      "ds_read_b128 v[252:255], v114 offset:12288\n"
      "v_cvt_pk_bf16_f32 v140, v140, v141\n"
      "v_mfma_f32_16x16x32_bf16 v[224:227], a[16:19], v[220:223], 0\n"
      "v_mul_f32 v143, v136, v143\n"
      "v_mul_f32 v142, v136, v142\n"
      "v_mfma_f32_16x16x32_bf16 v[224:227], a[52:55], v[248:251], v[224:227]\n"
      "s_waitcnt lgkmcnt(3)\n"
      "s_barrier\n"
      "v_mfma_f32_16x16x32_bf16 v[204:207], a[20:23], v[220:223], 0\n"
      "ds_read_b128 v[208:211], v128 offset:1024\n"
      "v_cvt_pk_bf16_f32 v141, v142, v143\n"
      "v_mfma_f32_16x16x32_bf16 v[204:207], a[56:59], v[248:251], v[204:207]\n"
      "global_load_lds_dwordx4 v116, s[52:53]\n"
      "v_mfma_f32_16x16x32_bf16 v[216:219], v[220:223], a[24:27], 0\n"
      "v_mul_f32_e64 v153, v136, v153\n"
      "v_mul_f32_e64 v152, v136, v152\n"
      "v_mfma_f32_16x16x32_bf16 v[216:219], v[248:251], a[60:63], v[216:219]\n"
      "v_mul_f32_e64 v154, v136, v154\n"
      "v_cvt_pk_bf16_f32 v142, v152, v153\n"
      "v_mfma_f32_16x16x32_bf16 v[240:243], v[220:223], a[28:31], 0\n"
      "v_mul_f32_e64 v155, v136, v155\n"
      "v_cvt_pk_bf16_f32 v143, v154, v155\n"
      "v_mfma_f32_16x16x32_bf16 v[240:243], v[248:251], a[64:67], v[240:243]\n"
      "ds_write_b128 v124, v[140:143] offset:18944\n"
      "v_mfma_f32_16x16x32_bf16 v[232:235], v[220:223], a[32:35], 0\n"
      "ds_read_b128 v[140:143], v115 offset:12288\n"
      "v_mul_f32 v156, v136, v156\n"
      "v_mfma_f32_16x16x32_bf16 v[232:235], v[248:251], a[68:71], v[232:235]\n"
      "s_waitcnt lgkmcnt(3)\n"
      "v_mul_f32_e64 v157, v136, v157\n"
      "v_cvt_pk_bf16_f32 v156, v156, v157\n"
      "v_mfma_f32_16x16x32_bf16 v[160:163], a[72:75], v[144:147], v[160:163]\n"
      "s_add_u32 m0, s76, 24448\n"
      "v_mul_f32_e64 v158, v136, v158\n"
      "v_mul_f32 v159, v136, v159\n"
      "v_mfma_f32_16x16x32_bf16 v[160:163], a[108:111], v[148:151], v[160:163]\n"
      "global_load_lds_dwordx4 v116, s[52:53] offset:128\n"
      "v_mfma_f32_16x16x32_bf16 v[236:239], a[76:79], v[144:147], v[236:239]\n"
      "s_waitcnt lgkmcnt(2)\n"
      "v_add_f32 v208, |v208|, |v209|\n"
      "v_cvt_pk_bf16_f32 v157, v158, v159\n"
      "v_mfma_f32_16x16x32_bf16 v[236:239], a[112:115], v[148:151], v[236:239]\n"
      "ds_write_b64 v125, v[156:157] offset:18944\n"
      "v_mfma_f32_16x16x32_bf16 v[164:167], a[80:83], v[144:147], v[164:167]\n"
      "s_add_u32 m0, s76, 30464\n"
      "v_add_f32 v210, |v210|, |v211|\n"
      "v_add_f32_e64 v208, v208, v210\n"
      "v_mfma_f32_16x16x32_bf16 v[164:167], a[116:119], v[148:151], v[164:167]\n"
      "v_rsq_f32 v208, v208\n"
      "v_mfma_f32_16x16x32_bf16 v[212:215], a[84:87], v[144:147], v[212:215]\n"
      "v_mul_f32 v208, s41, v208\n"
      "v_mov_b32_e64 v156, v208\n"
      "v_mfma_f32_16x16x32_bf16 v[212:215], a[120:123], v[148:151], v[212:215]\n"
      "s_nop 0\n"
      "v_permlane32_swap_b32 v208, v156\n"
      "v_mfma_f32_16x16x32_bf16 v[224:227], a[88:91], v[144:147], v[224:227]\n"
      "ds_read_b128 v[136:139], v114 offset:2048\n"
      "v_mov_b32_e64 v157, v208\n"
      "v_mfma_f32_16x16x32_bf16 v[224:227], a[124:127], v[148:151], v[224:227]\n"
      "global_load_lds_dwordx4 v116, s[52:53] offset:256\n"
      "v_mfma_f32_16x16x32_bf16 v[204:207], a[92:95], v[144:147], v[204:207]\n"
      "s_add_u32 m0, s77, 18432\n"
      "v_permlane16_swap_b32 v208, v157\n"
      "v_mfma_f32_16x16x32_bf16 v[204:207], a[128:131], v[148:151], v[204:207]\n"
      "ds_read_b128 v[152:155], v115 offset:2048\n"
      "v_mul_f32 v168, v208, v168\n"
      "v_mfma_f32_16x16x32_bf16 v[216:219], v[144:147], a[96:99], v[216:219]\n"
      "s_waitcnt lgkmcnt(2)\n"
      "v_mul_f32 v169, v208, v169\n"
      "v_cvt_pk_bf16_f32 v168, v168, v169\n"
      "v_mfma_f32_16x16x32_bf16 v[216:219], v[148:151], a[132:135], v[216:219]\n"
      "v_mul_f32 v170, v208, v170\n"
      "v_mul_f32 v171, v208, v171\n"
      "v_mfma_f32_16x16x32_bf16 v[240:243], v[144:147], a[100:103], v[240:243]\n"
      "v_cvt_pk_bf16_f32 v169, v170, v171\n"
      "v_mul_f32_e64 v172, v208, v172\n"
      "v_mfma_f32_16x16x32_bf16 v[240:243], v[148:151], a[136:139], v[240:243]\n"
      "v_mul_f32 v173, v208, v173\n"
      "v_cvt_pk_bf16_f32 v170, v172, v173\n"
      "v_mfma_f32_16x16x32_bf16 v[232:235], v[144:147], a[104:107], v[232:235]\n"
      "v_mul_f32 v175, v208, v175\n"
      "v_mul_f32 v174, v208, v174\n"
      "v_mfma_f32_16x16x32_bf16 v[232:235], v[148:151], a[140:143], v[232:235]\n"
      "v_cvt_pk_bf16_f32 v171, v174, v175\n"
      "v_mul_f32 v176, v208, v176\n"
      "v_mfma_f32_16x16x32_bf16 v[160:163], a[144:147], v[252:255], v[160:163]\n"
      "global_load_lds_dwordx4 v117, s[52:53]\n"
      "v_mfma_f32_16x16x32_bf16 v[160:163], a[180:183], v[140:143], v[160:163]\n"
      "v_mul_f32 v177, v208, v177\n"
      "v_cvt_pk_bf16_f32 v176, v176, v177\n"
      "v_mfma_f32_16x16x32_bf16 v[236:239], a[148:151], v[252:255], v[236:239]\n"
      "s_waitcnt lgkmcnt(0)\n"
      "v_mul_f32 v179, v208, v179\n"
      "v_mul_f32 v178, v208, v178\n"
      "v_mfma_f32_16x16x32_bf16 v[236:239], a[184:187], v[140:143], v[236:239]\n"
      "ds_write_b128 v126, v[168:171]\n"
      "v_mfma_f32_16x16x32_bf16 v[164:167], a[152:155], v[252:255], v[164:167]\n"
      "v_cvt_pk_bf16_f32 v177, v178, v179\n"
      "v_mul_f32 v180, v157, v180\n"
      "v_mfma_f32_16x16x32_bf16 v[164:167], a[188:191], v[140:143], v[164:167]\n"
      "v_mul_f32 v181, v157, v181\n"
      "v_cvt_pk_bf16_f32 v180, v180, v181\n"
      "v_mfma_f32_16x16x32_bf16 v[212:215], a[156:159], v[252:255], v[212:215]\n"
      "s_add_u32 m0, s78, 18432\n"
      "v_mul_f32 v183, v157, v183\n"
      "v_mul_f32 v182, v157, v182\n"
      "v_mfma_f32_16x16x32_bf16 v[212:215], a[192:195], v[140:143], v[212:215]\n"
      "v_mul_f32_e64 v184, v157, v184\n"
      "v_cvt_pk_bf16_f32 v181, v182, v183\n"
      "v_mfma_f32_16x16x32_bf16 v[224:227], a[160:163], v[252:255], v[224:227]\n"
      "ds_read_b128 v[168:171], v114 offset:8192\n"
      "v_mul_f32_e64 v185, v157, v185\n"
      "v_mfma_f32_16x16x32_bf16 v[224:227], a[196:199], v[140:143], v[224:227]\n"
      "ds_read_b128 v[172:175], v115 offset:8192\n"
      "v_cvt_pk_bf16_f32 v182, v184, v185\n"
      "v_mfma_f32_16x16x32_bf16 v[204:207], a[164:167], v[252:255], v[204:207]\n"
      "v_mul_f32 v187, v157, v187\n"
      "v_mul_f32 v186, v157, v186\n"
      "v_mfma_f32_16x16x32_bf16 v[204:207], a[200:203], v[140:143], v[204:207]\n"
      "s_mov_b32 exec_hi, 0\n"
      "global_load_lds_dwordx4 v118, s[52:53]\n"
      "s_mov_b32 exec_hi, -1\n"
      "v_mfma_f32_16x16x32_bf16 v[216:219], v[252:255], a[168:171], v[216:219]\n"
      "v_cvt_pk_bf16_f32 v183, v186, v187\n"
      "v_mul_f32 v188, v157, v188\n"
      "v_mfma_f32_16x16x32_bf16 v[216:219], v[140:143], a[204:207], v[216:219]\n"
      "ds_write_b128 v126, v[180:183] offset:2048\n"
      "v_mfma_f32_16x16x32_bf16 v[240:243], v[252:255], a[172:175], v[240:243]\n"
      "v_mul_f32 v189, v157, v189\n"
      "v_cvt_pk_bf16_f32 v188, v188, v189\n"
      "v_mfma_f32_16x16x32_bf16 v[240:243], v[140:143], a[208:211], v[240:243]\n"
      "s_add_u32 m0, s76, 115456\n"
      "v_mul_f32 v190, v157, v190\n"
      "v_mul_f32 v191, v157, v191\n"
      "v_mfma_f32_16x16x32_bf16 v[232:235], v[252:255], a[176:179], v[232:235]\n"
      "s_waitcnt lgkmcnt(1)\n"
      "v_mul_f32 v192, v156, v192\n"
      "v_cvt_pk_bf16_f32 v189, v190, v191\n"
      "v_mfma_f32_16x16x32_bf16 v[232:235], v[140:143], a[212:215], v[232:235]\n"
      "ds_write2st64_b64 v127, v[176:177], v[188:189] offset0:0 offset1:4\n"
      "v_mfma_f32_16x16x32_bf16 v[180:183], a[0:3], v[136:139], 0\n"
      "global_load_lds_dwordx4 v119, s[56:57]\n"
      "v_mfma_f32_16x16x32_bf16 v[180:183], a[36:39], v[152:155], v[180:183]\n"
      "s_add_u32 m0, s79, 4864\n"
      "v_mul_f32 v193, v156, v193\n"
      "v_cvt_pk_bf16_f32 v192, v192, v193\n"
      "v_mfma_f32_16x16x32_bf16 v[176:179], a[4:7], v[136:139], 0\n"
      "s_sub_u32 s28, s27, 3\n"
      "v_mul_f32 v195, v156, v195\n"
      "v_mul_f32 v194, v156, v194\n"
      "v_mfma_f32_16x16x32_bf16 v[176:179], a[40:43], v[152:155], v[176:179]\n"
      "v_cvt_pk_bf16_f32 v193, v194, v195\n"
      "v_mul_f32_e64 v196, v156, v196\n"
      "v_mfma_f32_16x16x32_bf16 v[184:187], a[8:11], v[136:139], 0\n"
      "s_cselect_b64 s[54:55], s[12:13], s[22:23]\n"
      "v_mul_f32 v197, v156, v197\n"
      "v_cvt_pk_bf16_f32 v194, v196, v197\n"
      "v_mfma_f32_16x16x32_bf16 v[184:187], a[44:47], v[152:155], v[184:187]\n"
      "v_mul_f32 v198, v156, v198\n"
      "v_mul_f32 v199, v156, v199\n"
      "v_mfma_f32_16x16x32_bf16 v[140:143], a[12:15], v[136:139], 0\n"
      "ds_read_b128 v[188:191], v114 offset:14336\n"
      "v_cvt_pk_bf16_f32 v195, v198, v199\n"
      "v_mfma_f32_16x16x32_bf16 v[140:143], a[48:51], v[152:155], v[140:143]\n"
      "ds_write_b128 v126, v[192:195] offset:4096\n"
      "v_mfma_f32_16x16x32_bf16 v[192:195], a[16:19], v[136:139], 0\n"
      "global_load_lds_dword v122, s[58:59]\n"
      "v_mfma_f32_16x16x32_bf16 v[192:195], a[52:55], v[152:155], v[192:195]\n"
      "ds_read_b128 v[196:199], v115 offset:14336\n"
      "v_mul_f32 v200, v156, v200\n"
      "v_mfma_f32_16x16x32_bf16 v[144:147], a[20:23], v[136:139], 0\n"
      "s_max_i32 m0, s28, 0\n"
      "v_mul_f32 v201, v156, v201\n"
      "v_cvt_pk_bf16_f32 v200, v200, v201\n"
      "v_mfma_f32_16x16x32_bf16 v[144:147], a[56:59], v[152:155], v[144:147]\n"
      "s_movrels_b32 s9, s80\n"
      "v_mul_f32_e64 v202, v156, v202\n"
      "v_mul_f32 v203, v156, v203\n"
      "v_mfma_f32_16x16x32_bf16 v[156:159], v[136:139], a[24:27], 0\n"
      "s_lshr_b32 s8, s9, 8\n"
      "v_cvt_pk_bf16_f32 v201, v202, v203\n"
      "v_fma_f32 v252, v160, v160, s38\n"
      "v_mfma_f32_16x16x32_bf16 v[156:159], v[152:155], a[60:63], v[156:159]\n"
      "ds_write_b64 v127, v[200:201] offset:4096\n"
      "v_mfma_f32_16x16x32_bf16 v[200:203], v[136:139], a[28:31], 0\n"
      "s_and_b32 s9, s9, 0xff\n"
      "v_fma_f32 v253, v212, v212, s38\n"
      "v_fmac_f32 v252, v161, v161\n"
      "v_mfma_f32_16x16x32_bf16 v[200:203], v[152:155], a[64:67], v[200:203]\n"
      "s_mul_i32 s28, s8, 1373568\n"
      "v_fmac_f32 v253, v213, v213\n"
      "v_fmac_f32 v252, v162, v162\n"
      "v_mfma_f32_16x16x32_bf16 v[148:151], v[136:139], a[32:35], 0\n"
      "s_mul_i32 s29, s9, 2688\n"
      "v_fmac_f32 v252, v163, v163\n"
      "v_fmac_f32_e64 v253, v214, v214\n"
      "v_mfma_f32_16x16x32_bf16 v[148:151], v[152:155], a[68:71], v[148:151]\n"
      "s_waitcnt lgkmcnt(1)\n"
      "v_fmac_f32 v252, v236, v236\n"
      "v_fmac_f32 v253, v215, v215\n"
      "v_mfma_f32_16x16x32_bf16 v[180:183], a[72:75], v[168:171], v[180:183]\n"
      "s_add_u32 s28, s28, s29\n"
      "v_fmac_f32 v252, v237, v237\n"
      "v_fmac_f32 v253, v224, v224\n"
      "v_mfma_f32_16x16x32_bf16 v[180:183], a[108:111], v[172:175], v[180:183]\n"
      "s_cmp_eq_u32 s8, 72\n"
      "v_fmac_f32 v252, v238, v238\n"
      "v_fmac_f32 v253, v225, v225\n"
      "v_mfma_f32_16x16x32_bf16 v[176:179], a[76:79], v[168:171], v[176:179]\n"
      "s_cselect_b32 s8, 0, 100270464\n"
      "v_fmac_f32 v252, v239, v239\n"
      "v_fmac_f32 v253, v226, v226\n"
      "v_mfma_f32_16x16x32_bf16 v[176:179], a[112:115], v[172:175], v[176:179]\n"
      "s_cmp_eq_u32 s9, 72\n"
      "v_fmac_f32 v252, v164, v164\n"
      "v_fmac_f32 v253, v227, v227\n"
      "v_mfma_f32_16x16x32_bf16 v[184:187], a[80:83], v[168:171], v[184:187]\n"
      "ds_read_b128 v[136:139], v114 offset:4096\n"
      "v_fmac_f32 v253, v204, v204\n"
      "v_mfma_f32_16x16x32_bf16 v[184:187], a[116:119], v[172:175], v[184:187]\n"
      "s_cselect_b32 s9, 0, 196224\n"
      "v_fmac_f32 v252, v165, v165\n"
      "v_fmac_f32_e64 v253, v205, v205\n"
      "v_mfma_f32_16x16x32_bf16 v[140:143], a[84:87], v[168:171], v[140:143]\n"
      "ds_read_b128 v[152:155], v115 offset:4096\n"
      "v_fmac_f32 v252, v166, v166\n"
      "v_mfma_f32_16x16x32_bf16 v[140:143], a[120:123], v[172:175], v[140:143]\n"
      "s_add_u32 s10, s8, s9\n"
      "v_fmac_f32 v253, v206, v206\n"
      "v_fmac_f32 v252, v167, v167\n"
      "v_mfma_f32_16x16x32_bf16 v[192:195], a[88:91], v[168:171], v[192:195]\n"
      "s_add_u32 s28, s28, s10\n"
      "v_fmac_f32 v253, v207, v207\n"
      "v_cvt_pk_bf16_f32 v216, v216, v217\n"
      "v_mfma_f32_16x16x32_bf16 v[192:195], a[124:127], v[172:175], v[192:195]\n"
      "s_add_u32 s54, s54, s28\n"
      "v_permlane16_swap_b32 v252, v253\n"
      "v_mfma_f32_16x16x32_bf16 v[144:147], a[92:95], v[168:171], v[144:147]\n"
      "s_addc_u32 s55, s55, 0\n"
      "v_add_f32 v252, v252, v253\n"
      "v_mov_b32 v253, v252\n"
      "v_mfma_f32_16x16x32_bf16 v[144:147], a[128:131], v[172:175], v[144:147]\n"
      "s_cmp_lg_u32 s10, s11\n"
      "v_permlane32_swap_b32 v252, v253\n"
      "v_mfma_f32_16x16x32_bf16 v[156:159], v[168:171], a[96:99], v[156:159]\n"
      "s_cbranch_scc1 L_edge_26_%=\n"
      "L_eb_27_%=:\n"
      "v_add_f32 v252, v252, v253\n"
      "v_mfma_f32_16x16x32_bf16 v[156:159], v[172:175], a[132:135], v[156:159]\n"
      "v_rsq_f32_e64 v252, v252\n"
      "v_mfma_f32_16x16x32_bf16 v[200:203], v[168:171], a[100:103], v[200:203]\n"
      "s_mov_b32 s5, s4\n"
      "v_cvt_pk_bf16_f32 v217, v218, v219\n"
      "v_mov_b32 v253, v252\n"
      "v_mfma_f32_16x16x32_bf16 v[200:203], v[172:175], a[136:139], v[200:203]\n"
      "s_add_u32 s4, s27, 1\n"
      "v_permlane16_swap_b32_e64 v252, v253\n"
      "v_mfma_f32_16x16x32_bf16 v[148:151], v[168:171], a[104:107], v[148:151]\n"
      "s_lshl_b32 s4, s4, 30\n"
      "v_mul_f32 v213, v253, v213\n"
      "v_mul_f32 v212, v253, v212\n"
      "v_mfma_f32_16x16x32_bf16 v[148:151], v[172:175], a[140:143], v[148:151]\n"
      "v_mul_f32 v214, v253, v214\n"
      "v_cvt_pk_bf16_f32 v212, v212, v213\n"
      "v_mfma_f32_16x16x32_bf16 v[180:183], a[144:147], v[188:191], v[180:183]\n"
      "v_mul_f32_e64 v215, v253, v215\n"
      "v_cvt_pk_bf16_f32 v213, v214, v215\n"
      "v_mfma_f32_16x16x32_bf16 v[180:183], a[180:183], v[196:199], v[180:183]\n"
      "ds_read_b128 v[168:171], v119 offset:18432\n"
      "v_mul_f32 v224, v253, v224\n"
      "v_mfma_f32_16x16x32_bf16 v[176:179], a[148:151], v[188:191], v[176:179]\n"
      "v_mul_f32 v225, v253, v225\n"
      "v_cvt_pk_bf16_f32 v214, v224, v225\n"
      "v_mfma_f32_16x16x32_bf16 v[176:179], a[184:187], v[196:199], v[176:179]\n"
      "s_and_b32 s4, s4, 0x80000000\n"
      "v_mul_f32 v226, v253, v226\n"
      "v_mul_f32 v227, v253, v227\n"
      "v_mfma_f32_16x16x32_bf16 v[184:187], a[152:155], v[188:191], v[184:187]\n"
      "v_mul_f32 v204, v253, v204\n"
      "v_cvt_pk_bf16_f32 v215, v226, v227\n"
      "v_mfma_f32_16x16x32_bf16 v[184:187], a[188:191], v[196:199], v[184:187]\n"
      "v_mul_f32 v205, v253, v205\n"
      "v_cvt_pk_bf16_f32 v34, v204, v205\n"
      "v_mfma_f32_16x16x32_bf16 v[140:143], a[156:159], v[188:191], v[140:143]\n"
      "v_mul_f32 v207, v253, v207\n"
      "v_mul_f32 v206, v253, v206\n"
      "v_mfma_f32_16x16x32_bf16 v[140:143], a[192:195], v[196:199], v[140:143]\n"
      "ds_read_b128 v[172:175], v114 offset:10240\n"
      "v_cvt_pk_bf16_f32 v35, v206, v207\n"
      "v_mfma_f32_16x16x32_bf16 v[192:195], a[160:163], v[188:191], v[192:195]\n"
      "ds_read_b128 v[204:207], v115 offset:10240\n"
      "v_mul_f32 v252, s36, v252\n"
      "v_mfma_f32_16x16x32_bf16 v[192:195], a[196:199], v[196:199], v[192:195]\n"
      "s_waitcnt lgkmcnt(2)\n"
      "v_mul_f32 v161, v252, v161\n"
      "v_mul_f32 v160, v252, v160\n"
      "v_mfma_f32_16x16x32_bf16 v[144:147], a[164:167], v[188:191], v[144:147]\n"
      "global_store_dwordx4 v116, v[168:171], s[54:55] sc1\n"
      "v_mfma_f32_16x16x32_bf16 v[144:147], a[200:203], v[196:199], v[144:147]\n"
      "v_mul_f32_e64 v162, v252, v162\n"
      "v_cvt_pk_bf16_f32 v160, v160, v161\n"
      "v_mfma_f32_16x16x32_bf16 v[156:159], v[188:191], a[168:171], v[156:159]\n"
      "v_mul_f32 v163, v252, v163\n"
      "v_cvt_pk_bf16_f32 v161, v162, v163\n"
      "v_mfma_f32_16x16x32_bf16 v[156:159], v[196:199], a[204:207], v[156:159]\n"
      "v_mul_f32 v236, v252, v236\n"
      "v_mul_f32 v237, v252, v237\n"
      "v_mfma_f32_16x16x32_bf16 v[200:203], v[188:191], a[172:175], v[200:203]\n"
      "v_mul_f32 v238, v252, v238\n"
      "v_cvt_pk_bf16_f32 v162, v236, v237\n"
      "v_mfma_f32_16x16x32_bf16 v[200:203], v[196:199], a[208:211], v[200:203]\n"
      "v_mul_f32 v239, v252, v239\n"
      "v_cvt_pk_bf16_f32 v163, v238, v239\n"
      "v_mfma_f32_16x16x32_bf16 v[148:151], v[188:191], a[176:179], v[148:151]\n"
      "s_waitcnt lgkmcnt(0)\n"
      "v_mul_f32 v164, v252, v164\n"
      "v_mul_f32 v165, v252, v165\n"
      "v_mfma_f32_16x16x32_bf16 v[148:151], v[196:199], a[212:215], v[148:151]\n"
      "v_mul_f32 v166, v252, v166\n"
      "v_cvt_pk_bf16_f32 v32, v164, v165\n"
      "v_mfma_f32_16x16x32_bf16 v[196:199], a[0:3], v[136:139], 0\n"
      "v_mul_f32 v167, v252, v167\n"
      "v_cvt_pk_bf16_f32 v33, v166, v167\n"
      "v_mfma_f32_16x16x32_bf16 v[196:199], a[36:39], v[152:155], v[196:199]\n"
      "v_cvt_pk_bf16_f32 v218, v156, v157\n"
      "v_cvt_pk_bf16_f32 v219, v158, v159\n"
      "v_mfma_f32_16x16x32_bf16 v[188:191], a[4:7], v[136:139], 0\n"
      "v_cvt_pk_bf16_f32 v240, v240, v241\n"
      "v_cvt_pk_bf16_f32 v241, v242, v243\n"
      "v_mfma_f32_16x16x32_bf16 v[188:191], a[40:43], v[152:155], v[188:191]\n"
      "v_cvt_pk_bf16_f32 v243, v202, v203\n"
      "v_cvt_pk_bf16_f32 v242, v200, v201\n"
      "v_mfma_f32_16x16x32_bf16 v[200:203], a[8:11], v[136:139], 0\n"
      "ds_read_b128 v[168:171], v119 offset:24576\n"
      "v_cvt_pk_bf16_f32 v232, v232, v233\n"
      "v_mfma_f32_16x16x32_bf16 v[200:203], a[44:47], v[152:155], v[200:203]\n"
      "v_cvt_pk_bf16_f32 v233, v234, v235\n"
      "v_cvt_pk_bf16_f32 v234, v148, v149\n"
      "v_mfma_f32_16x16x32_bf16 v[156:159], a[12:15], v[136:139], 0\n"
      "v_cvt_pk_bf16_f32 v235, v150, v151\n"
      "v_fma_f32 v220, v180, v180, s38\n"
      "v_mfma_f32_16x16x32_bf16 v[156:159], a[48:51], v[152:155], v[156:159]\n"
      "v_fmac_f32 v220, v181, v181\n"
      "v_fma_f32 v221, v140, v140, s38\n"
      "v_mfma_f32_16x16x32_bf16 v[164:167], a[16:19], v[136:139], 0\n"
      "v_fmac_f32 v221, v141, v141\n"
      "v_fmac_f32_e64 v220, v182, v182\n"
      "v_mfma_f32_16x16x32_bf16 v[164:167], a[52:55], v[152:155], v[164:167]\n"
      "v_fmac_f32 v221, v142, v142\n"
      "v_fmac_f32 v220, v183, v183\n"
      "v_mfma_f32_16x16x32_bf16 v[148:151], a[20:23], v[136:139], 0\n"
      "v_fmac_f32 v220, v176, v176\n"
      "v_fmac_f32 v221, v143, v143\n"
      "v_mfma_f32_16x16x32_bf16 v[148:151], a[56:59], v[152:155], v[148:151]\n"
      "v_fmac_f32 v220, v177, v177\n"
      "v_fmac_f32 v221, v192, v192\n"
      "v_mfma_f32_16x16x32_bf16 v[236:239], v[136:139], a[24:27], 0\n"
      "s_waitcnt lgkmcnt(0)\n"
      "v_fmac_f32 v221, v193, v193\n"
      "v_mfma_f32_16x16x32_bf16 v[236:239], v[152:155], a[60:63], v[236:239]\n"
      "global_store_dwordx4 v116, v[168:171], s[54:55] offset:128 sc1\n"
      "v_mfma_f32_16x16x32_bf16 v[224:227], v[136:139], a[28:31], 0\n"
      "ds_read_b128 v[252:255], v114 offset:16384\n"
      "v_fmac_f32 v220, v178, v178\n"
      "v_mfma_f32_16x16x32_bf16 v[224:227], v[152:155], a[64:67], v[224:227]\n"
      "v_fmac_f32 v220, v179, v179\n"
      "v_fmac_f32 v221, v194, v194\n"
      "v_mfma_f32_16x16x32_bf16 v[208:211], v[136:139], a[32:35], 0\n"
      "ds_read_b128 v[168:171], v115 offset:16384\n"
      "v_fmac_f32 v221, v195, v195\n"
      "v_mfma_f32_16x16x32_bf16 v[208:211], v[152:155], a[68:71], v[208:211]\n"
      "v_fmac_f32 v220, v184, v184\n"
      "v_fmac_f32 v221, v144, v144\n"
      "v_mfma_f32_16x16x32_bf16 v[196:199], a[72:75], v[172:175], v[196:199]\n"
      "v_fmac_f32 v220, v185, v185\n"
      "v_fmac_f32 v221, v145, v145\n"
      "v_mfma_f32_16x16x32_bf16 v[196:199], a[108:111], v[204:207], v[196:199]\n"
      "v_fmac_f32 v220, v186, v186\n"
      "v_fmac_f32 v221, v146, v146\n"
      "v_mfma_f32_16x16x32_bf16 v[188:191], a[76:79], v[172:175], v[188:191]\n"
      "v_fmac_f32 v220, v187, v187\n"
      "v_fmac_f32 v221, v147, v147\n"
      "v_mfma_f32_16x16x32_bf16 v[188:191], a[112:115], v[204:207], v[188:191]\n"
      "s_nop 0\n"
      "v_permlane16_swap_b32 v220, v221\n"
      "v_mfma_f32_16x16x32_bf16 v[200:203], a[80:83], v[172:175], v[200:203]\n"
      "v_add_f32 v220, v220, v221\n"
      "v_mov_b32 v221, v220\n"
      "v_mfma_f32_16x16x32_bf16 v[200:203], a[116:119], v[204:207], v[200:203]\n"
      "s_nop 0\n"
      "v_permlane32_swap_b32 v220, v221\n"
      "v_mfma_f32_16x16x32_bf16 v[156:159], a[84:87], v[172:175], v[156:159]\n"
      "v_add_f32 v220, v220, v221\n"
      "v_mfma_f32_16x16x32_bf16 v[156:159], a[120:123], v[204:207], v[156:159]\n"
      "s_waitcnt lgkmcnt(0)\n"
      "v_rsq_f32 v220, v220\n"
      "v_mfma_f32_16x16x32_bf16 v[164:167], a[88:91], v[172:175], v[164:167]\n"
      "v_mov_b32 v221, v220\n"
      "v_add_u32 v135, 32, v135\n"
      "v_mfma_f32_16x16x32_bf16 v[164:167], a[124:127], v[204:207], v[164:167]\n"
      "v_permlane16_swap_b32 v220, v221\n"
      "v_mfma_f32_16x16x32_bf16 v[148:151], a[92:95], v[172:175], v[148:151]\n"
      "v_mul_f32 v140, v221, v140\n"
      "v_mul_f32 v141, v221, v141\n"
      "v_mfma_f32_16x16x32_bf16 v[148:151], a[128:131], v[204:207], v[148:151]\n"
      "ds_read_b128 v[136:139], v132 offset:32\n"
      "v_cvt_pk_bf16_f32 v140, v140, v141\n"
      "v_mfma_f32_16x16x32_bf16 v[236:239], v[172:175], a[96:99], v[236:239]\n"
      "v_mul_f32 v142, v221, v142\n"
      "v_mul_f32 v143, v221, v143\n"
      "v_mfma_f32_16x16x32_bf16 v[236:239], v[204:207], a[132:135], v[236:239]\n"
      "ds_read_b64 v[152:153], v132 offset:24\n"
      "v_cvt_pk_bf16_f32 v141, v142, v143\n"
      "v_mfma_f32_16x16x32_bf16 v[224:227], v[172:175], a[100:103], v[224:227]\n"
      "ds_read_u16 v52, v134\n"
      "v_mul_f32 v192, v221, v192\n"
      "v_mfma_f32_16x16x32_bf16 v[224:227], v[204:207], a[136:139], v[224:227]\n"
      "v_mul_f32 v194, v221, v194\n"
      "v_mul_f32_e64 v193, v221, v193\n"
      "v_mfma_f32_16x16x32_bf16 v[208:211], v[172:175], a[104:107], v[208:211]\n"
      "ds_read_b128 v[228:231], v133\n"
      "v_cvt_pk_bf16_f32 v142, v192, v193\n"
      "v_mfma_f32_16x16x32_bf16 v[208:211], v[204:207], a[140:143], v[208:211]\n"
      "ds_read_b64 v[154:155], v133 offset:16\n"
      "v_mfma_f32_16x16x32_bf16 v[196:199], a[144:147], v[252:255], v[196:199]\n"
      "ds_read_u16 v60, v134 offset:64\n"
      "v_mul_f32_e64 v195, v221, v195\n"
      "v_mfma_f32_16x16x32_bf16 v[196:199], a[180:183], v[168:171], v[196:199]\n"
      "ds_read_b128 v[204:207], v123 offset:18432\n"
      "v_cvt_pk_bf16_f32 v143, v194, v195\n"
      "v_mfma_f32_16x16x32_bf16 v[188:191], a[148:151], v[252:255], v[188:191]\n"
      "v_mul_f32 v144, v221, v144\n"
      "v_mul_f32 v145, v221, v145\n"
      "v_mfma_f32_16x16x32_bf16 v[188:191], a[184:187], v[168:171], v[188:191]\n"
      "ds_read_b128 v[192:195], v123 offset:21504\n"
      "v_mul_f32_e64 v146, v221, v146\n"
      "v_mfma_f32_16x16x32_bf16 v[200:203], a[152:155], v[252:255], v[200:203]\n"
      "ds_read_u16 v56, v134 offset:32\n"
      "v_cvt_pk_bf16_f32 v40, v144, v145\n"
      "v_mfma_f32_16x16x32_bf16 v[200:203], a[188:191], v[168:171], v[200:203]\n"
      "v_mul_f32 v220, s36, v220\n"
      "v_mul_f32 v181, v220, v181\n"
      "v_mfma_f32_16x16x32_bf16 v[156:159], a[156:159], v[252:255], v[156:159]\n"
      "v_mul_f32 v147, v221, v147\n"
      "v_mul_f32 v182, v220, v182\n"
      "v_mfma_f32_16x16x32_bf16 v[156:159], a[192:195], v[168:171], v[156:159]\n"
      "v_mul_f32 v180, v220, v180\n"
      "v_cvt_pk_bf16_f32 v180, v180, v181\n"
      "v_mfma_f32_16x16x32_bf16 v[164:167], a[160:163], v[252:255], v[164:167]\n"
      "v_mul_f32 v178, v220, v178\n"
      "v_mul_f32 v183, v220, v183\n"
      "v_mfma_f32_16x16x32_bf16 v[164:167], a[196:199], v[168:171], v[164:167]\n"
      "v_cvt_pk_bf16_f32 v41, v146, v147\n"
      "v_mul_f32 v176, v220, v176\n"
      "v_mfma_f32_16x16x32_bf16 v[148:151], a[164:167], v[252:255], v[148:151]\n"
      "ds_read_b128 v[144:147], v123 offset:24576\n"
      "v_cvt_pk_bf16_f32 v181, v182, v183\n"
      "v_mfma_f32_16x16x32_bf16 v[148:151], a[200:203], v[168:171], v[148:151]\n"
      "v_mul_f32 v177, v220, v177\n"
      "v_mul_f32 v179, v220, v179\n"
      "v_mfma_f32_16x16x32_bf16 v[236:239], v[252:255], a[168:171], v[236:239]\n"
      "s_waitcnt lgkmcnt(2)\n"
      "v_cvt_pk_bf16_f32 v182, v176, v177\n"
      "v_mul_f32 v184, v220, v184\n"
      "v_mfma_f32_16x16x32_bf16 v[236:239], v[168:171], a[204:207], v[236:239]\n"
      "ds_read_b128 v[244:247], v123 offset:27648\n"
      "v_mul_f32 v187, v220, v187\n"
      "v_mfma_f32_16x16x32_bf16 v[224:227], v[252:255], a[172:175], v[224:227]\n"
      "v_mul_f32 v185, v220, v185\n"
      "v_cvt_pk_bf16_f32 v38, v184, v185\n"
      "v_mfma_f32_16x16x32_bf16 v[224:227], v[168:171], a[208:211], v[224:227]\n"
      "s_or_b64 s[34:35], s[48:49], s[50:51]\n"
      "v_mul_f32 v186, v220, v186\n"
      "v_mfma_f32_16x16x32_bf16 v[208:211], v[252:255], a[176:179], v[208:211]\n"
      "v_cvt_pk_bf16_f32 v183, v178, v179\n"
      "v_cvt_pk_bf16_f32 v39, v186, v187\n"
      "v_mfma_f32_16x16x32_bf16 v[208:211], v[168:171], a[212:215], v[208:211]\n"
      "v_fma_f32 v253, v156, v156, s38\n"
      "v_fma_f32 v252, v196, v196, s38\n"
      "v_mfma_f32_16x16x32_bf16 v[168:171], a[216:219], v[204:207], 0\n"
      "v_fmac_f32 v252, v197, v197\n"
      "v_fmac_f32 v253, v157, v157\n"
      "v_mfma_f32_16x16x32_bf16 v[168:171], a[228:231], v[192:195], v[168:171]\n"
      "v_fmac_f32 v252, v198, v198\n"
      "v_fmac_f32 v253, v158, v158\n"
      "v_mfma_f32_16x16x32_bf16 v[172:175], a[220:223], v[204:207], 0\n"
      "v_fmac_f32 v253, v159, v159\n"
      "v_fmac_f32 v252, v199, v199\n"
      "v_mfma_f32_16x16x32_bf16 v[172:175], a[232:235], v[192:195], v[172:175]\n"
      "v_fmac_f32 v253, v164, v164\n"
      "v_fmac_f32 v252, v188, v188\n"
      "v_mfma_f32_16x16x32_bf16 v[176:179], a[224:227], v[204:207], 0\n"
      "v_fmac_f32 v253, v165, v165\n"
      "v_fmac_f32 v252, v189, v189\n"
      "v_mfma_f32_16x16x32_bf16 v[176:179], a[236:239], v[192:195], v[176:179]\n"
      "s_waitcnt lgkmcnt(0)\n"
      "v_fmac_f32 v252, v190, v190\n"
      "v_fmac_f32 v253, v166, v166\n"
      "v_mfma_f32_16x16x32_bf16 v[168:171], a[240:243], v[144:147], v[168:171]\n"
      "ds_read_b128 v[184:187], v123 offset:30720\n"
      "v_fmac_f32 v252, v191, v191\n"
      "v_mfma_f32_16x16x32_bf16 v[168:171], a[252:255], v[244:247], v[168:171]\n"
      "ds_read_b128 v[192:195], v123 offset:33792\n"
      "v_fmac_f32 v253, v167, v167\n"
      "v_mfma_f32_16x16x32_bf16 v[172:175], a[244:247], v[144:147], v[172:175]\n"
      "v_fmac_f32 v253, v148, v148\n"
      "v_fmac_f32_e64 v252, v200, v200\n"
      "v_mfma_f32_16x16x32_bf16 v[172:175], v[0:3], v[244:247], v[172:175]\n"
      "v_fmac_f32 v253, v149, v149\n"
      "v_fmac_f32 v252, v201, v201\n"
      "v_mfma_f32_16x16x32_bf16 v[176:179], a[248:251], v[144:147], v[176:179]\n"
      "v_fmac_f32 v253, v150, v150\n"
      "v_fmac_f32 v252, v202, v202\n"
      "v_mfma_f32_16x16x32_bf16 v[176:179], v[4:7], v[244:247], v[176:179]\n"
      "v_cvt_pk_bf16_f32 v50, v236, v237\n"
      "v_fmac_f32 v253, v151, v151\n"
      "v_fmac_f32 v252, v203, v203\n"
      "v_mfma_f32_16x16x32_bf16 v[144:147], v[212:215], v[160:163], v[78:81]\n"
      "s_nop 0\n"
      "v_permlane16_swap_b32 v252, v253\n"
      "v_mfma_f32_16x16x32_bf16 v[144:147], v[34:37], v[32:35], v[144:147]\n"
      "v_add_f32 v252, v252, v253\n"
      "v_mov_b32 v253, v252\n"
      "v_mfma_f32_16x16x32_bf16 v[244:247], v[140:143], v[160:163], v[82:85]\n"
      "s_nop 0\n"
      "v_permlane32_swap_b32 v252, v253\n"
      "v_mfma_f32_16x16x32_bf16 v[244:247], v[40:43], v[32:35], v[244:247]\n"
      "s_waitcnt lgkmcnt(0)\n"
      "v_add_f32 v252, v252, v253\n"
      "v_rsq_f32_e64 v252, v252\n"
      "v_mfma_f32_16x16x32_bf16 v[168:171], v[8:11], v[184:187], v[168:171]\n"
      "v_cvt_pk_bf16_f32 v51, v238, v239\n"
      "v_mov_b32 v253, v252\n"
      "v_mfma_f32_16x16x32_bf16 v[168:171], v[20:23], v[192:195], v[168:171]\n"
      "s_nop 0\n"
      "v_permlane16_swap_b32 v252, v253\n"
      "v_mfma_f32_16x16x32_bf16 v[172:175], v[12:15], v[184:187], v[172:175]\n"
      "v_mul_f32 v157, v253, v157\n"
      "v_mul_f32 v156, v253, v156\n"
      "v_mfma_f32_16x16x32_bf16 v[172:175], v[24:27], v[192:195], v[172:175]\n"
      "v_mul_f32 v148, v253, v148\n"
      "v_cvt_pk_bf16_f32 v156, v156, v157\n"
      "v_mul_f32 v158, v253, v158\n"
      "v_mul_f32 v151, v253, v151\n"
      "v_mul_f32 v165, v253, v165\n"
      "v_mul_f32 v252, s36, v252\n"
      "v_mul_f32 v164, v253, v164\n"
      "v_mul_f32 v199, v252, v199\n"
      "v_mfma_f32_16x16x32_bf16 v[176:179], v[16:19], v[184:187], v[176:179]\n"
      "v_mul_f32 v159, v253, v159\n"
      "v_cvt_pk_bf16_f32 v157, v158, v159\n"
      "v_mfma_f32_16x16x32_bf16 v[176:179], v[28:31], v[192:195], v[176:179]\n"
      "v_mul_f32 v149, v253, v149\n"
      "v_mul_f32 v167, v253, v167\n"
      "v_mul_f32 v150, v253, v150\n"
      "v_mul_f32_e64 v166, v253, v166\n"
      "v_cvt_pk_bf16_f32 v159, v166, v167\n"
      "v_cvt_pk_bf16_f32 v47, v150, v151\n"
      "v_cvt_pk_bf16_f32 v158, v164, v165\n"
      "v_cvt_pk_bf16_f32 v46, v148, v149\n"
      "v_mul_f32 v196, v252, v196\n"
      "v_mfma_f32_16x16x32_bf16 v[184:187], v[156:159], v[160:163], v[86:89]\n"
      "s_waitcnt vmcnt(2)\n"
      "v_mul_f32 v197, v252, v197\n"
      "v_cvt_pk_bf16_f32 v196, v196, v197\n"
      "v_mfma_f32_16x16x32_bf16 v[184:187], v[46:49], v[32:35], v[184:187]\n"
      "ds_read_b128 v[192:195], v119 offset:41728\n"
      "v_mul_f32 v202, v252, v202\n"
      "v_mfma_f32_16x16x32_bf16 v[236:239], v[136:139], v[160:163], v[70:73]\n"
      "ds_read_b32 v160, v122 offset:4864\n"
      "v_cvt_pk_bf16_f32 v54, v224, v225\n"
      "v_mfma_f32_16x16x32_bf16 v[236:239], v[228:231], v[212:215], v[236:239]\n"
      "v_mul_f32 v189, v252, v189\n"
      "v_mul_f32 v188, v252, v188\n"
      "v_mfma_f32_16x16x32_bf16 v[236:239], v[152:155], v[32:35], v[236:239]\n"
      "v_cvt_pk_bf16_f32 v59, v210, v211\n"
      "v_mul_f32 v198, v252, v198\n"
      "v_mul_f32 v190, v252, v190\n"
      "v_cvt_pk_bf16_f32 v55, v226, v227\n"
      "v_cvt_pk_bf16_f32 v197, v198, v199\n"
      "v_mul_f32 v203, v252, v203\n"
      "v_mul_f32 v191, v252, v191\n"
      "v_cvt_pk_bf16_f32 v199, v190, v191\n"
      "v_fma_f32 v238, v168, v168, s39\n"
      "v_fmac_f32 v238, v169, v169\n"
      "v_cvt_pk_bf16_f32 v198, v188, v189\n"
      "v_fmac_f32 v238, v170, v170\n"
      "v_fmac_f32_e64 v238, v171, v171\n"
      "v_cvt_pk_bf16_f32 v45, v202, v203\n"
      "v_mul_f32 v201, v252, v201\n"
      "v_fmac_f32 v238, v172, v172\n"
      "v_fmac_f32 v238, v173, v173\n"
      "v_fmac_f32 v238, v174, v174\n"
      "v_fmac_f32 v238, v175, v175\n"
      "v_fmac_f32 v238, v176, v176\n"
      "v_mul_f32_e64 v200, v252, v200\n"
      "v_fmac_f32_e64 v238, v177, v177\n"
      "v_fmac_f32_e64 v238, v178, v178\n"
      "v_cvt_pk_bf16_f32 v58, v208, v209\n"
      "v_fmac_f32 v238, v179, v179\n"
      "s_waitcnt lgkmcnt(0)\n"
      "v_cvt_pk_bf16_f32 v44, v200, v201\n"
      "v_bitop3_b32 v160, v160, v195, s40 bitop3:0xa8\n"
      "v_or3_b32 v192, v192, v193, v194\n"
      "v_and_b32 v192, s40, v192\n"
      "v_or3_b32 v192, v192, v160, s4\n"
      "ds_write_b32 v129, v192 offset:3072\n"
      "s_cbranch_scc1 L_gen_28_%=\n"
      "L_fast_31_%=:\n"
      "v_mfma_f32_16x16x32_bf16 v[192:195], v[212:215], v[180:183], v[90:93]\n"
      "ds_read_b128 v[188:191], v119 offset:30720\n"
      "v_exp_f32 v145, v145\n"
      "v_exp_f32 v187, v187\n"
      "v_exp_f32_e64 v144, v144\n"
      "v_mfma_f32_16x16x32_bf16 v[192:195], v[34:37], v[38:41], v[192:195]\n"
      "ds_read_b128 v[200:203], v123 offset:18688\n"
      "v_exp_f32 v185, v185\n"
      "v_mfma_f32_16x16x32_bf16 v[160:163], v[140:143], v[180:183], v[94:97]\n"
      "ds_read_b128 v[164:167], v123 offset:21760\n"
      "v_exp_f32 v245, v245\n"
      "v_mfma_f32_16x16x32_bf16 v[208:211], v[136:139], v[180:183], v[72:75]\n"
      "ds_read_b128 v[224:227], v123 offset:24832\n"
      "v_cvt_pk_bf16_f32 v144, v144, v145\n"
      "v_mfma_f32_16x16x32_bf16 v[208:211], v[228:231], v[140:143], v[208:211]\n"
      "v_exp_f32 v146, v146\n"
      "v_mfma_f32_16x16x32_bf16 v[208:211], v[152:155], v[38:41], v[208:211]\n"
      "ds_read_b128 v[148:151], v128 offset:3072\n"
      "v_exp_f32 v184, v184\n"
      "v_exp_f32_e64 v147, v147\n"
      "v_mfma_f32_16x16x32_bf16 v[160:163], v[40:43], v[38:41], v[160:163]\n"
      "ds_read_b128 v[204:207], v123 offset:27904\n"
      "v_exp_f32 v244, v244\n"
      "v_exp_f32 v236, v236\n"
      "v_cvt_pk_bf16_f32 v145, v146, v147\n"
      "v_cvt_pk_bf16_f32 v146, v244, v245\n"
      "v_mfma_f32_16x16x32_bf16 v[252:255], v[156:159], v[180:183], v[98:101]\n"
      "v_exp_f32 v186, v186\n"
      "v_mfma_f32_16x16x32_bf16 v[252:255], v[46:49], v[38:41], v[252:255]\n"
      "s_waitcnt lgkmcnt(3)\n"
      "v_exp_f32 v247, v247\n"
      "v_cvt_pk_bf16_f32 v63, v186, v187\n"
      "v_cvt_pk_bf16_f32 v62, v184, v185\n"
      "v_mfma_f32_16x16x32_bf16 v[180:183], a[216:219], v[200:203], 0\n"
      "v_exp_f32 v246, v246\n"
      "v_mfma_f32_16x16x32_bf16 v[180:183], a[228:231], v[164:167], v[180:183]\n"
      "global_store_dwordx4 v116, v[188:191], s[54:55] offset:256 sc1\n"
      "v_cvt_pk_bf16_f32 v64, v236, 0\n"
      "v_mfma_f32_16x16x32_bf16 v[184:187], a[220:223], v[200:203], 0\n"
      "s_waitcnt lgkmcnt(1)\n"
      "v_bitop3_b32 v148, v148, v149, s4 bitop3:0x7e\n"
      "v_bitop3_b32 v150, v150, v151, s4 bitop3:0x7e\n"
      "v_mfma_f32_16x16x32_bf16 v[184:187], a[232:235], v[164:167], v[184:187]\n"
      "s_waitcnt lgkmcnt(0)\n"
      "v_or_b32_e64 v148, v148, v150\n"
      "v_cmp_ne_u32 vcc, 0, v148\n"
      "v_mfma_f32_16x16x32_bf16 v[188:191], a[224:227], v[200:203], 0\n"
      "ds_read_b128 v[200:203], v123 offset:30976\n"
      "s_mov_b64 s[48:49], vcc\n"
      "v_mfma_f32_16x16x32_bf16 v[188:191], a[236:239], v[164:167], v[188:191]\n"
      "ds_read_b128 v[164:167], v123 offset:34048\n"
      "v_exp_f32 v192, v192\n"
      "v_mfma_f32_16x16x32_bf16 v[180:183], a[240:243], v[224:227], v[180:183]\n"
      "s_cbranch_vccnz L_flg_32_%=\n"
      "L_fb_33_%=:\n"
      "v_mfma_f32_16x16x32_bf16 v[180:183], a[252:255], v[204:207], v[180:183]\n"
      "ds_read_b128 v[148:151], v120 offset:18432\n"
      "v_exp_f32 v195, v195\n"
      "v_exp_f32 v193, v193\n"
      "v_mfma_f32_16x16x32_bf16 v[184:187], a[244:247], v[224:227], v[184:187]\n"
      "v_exp_f32 v194, v194\n"
      "v_mfma_f32_16x16x32_bf16 v[184:187], v[0:3], v[204:207], v[184:187]\n"
      "v_cvt_pk_bf16_f32 v192, v192, v193\n"
      "v_exp_f32 v161, v161\n"
      "v_exp_f32_e64 v160, v160\n"
      "v_mfma_f32_16x16x32_bf16 v[188:191], a[248:251], v[224:227], v[188:191]\n"
      "ds_read_b128 v[224:227], v123 offset:18944\n"
      "v_cvt_pk_bf16_f32 v193, v194, v195\n"
      "v_mfma_f32_16x16x32_bf16 v[188:191], v[4:7], v[204:207], v[188:191]\n"
      "ds_read_b128 v[204:207], v123 offset:22016\n"
      "s_waitcnt lgkmcnt(3)\n"
      "v_mfma_f32_16x16x32_bf16 v[180:183], v[8:11], v[200:203], v[180:183]\n"
      "v_exp_f32 v162, v162\n"
      "v_mfma_f32_16x16x32_bf16 v[180:183], v[20:23], v[164:167], v[180:183]\n"
      "v_cvt_pk_bf16_f32 v194, v160, v161\n"
      "v_exp_f32 v163, v163\n"
      "v_mfma_f32_16x16x32_bf16 v[184:187], v[12:15], v[200:203], v[184:187]\n"
      "s_waitcnt lgkmcnt(2)\n"
      "v_exp_f32_e64 v252, v252\n"
      "v_mfma_f32_16x16x32_bf16 v[184:187], v[24:27], v[164:167], v[184:187]\n"
      "global_store_dwordx4 v117, v[148:151], s[54:55] sc1\n"
      "v_cvt_pk_bf16_f32 v147, v246, v247\n"
      "v_mfma_f32_16x16x32_bf16 v[188:191], v[16:19], v[200:203], v[188:191]\n"
      "v_fma_f32 v236, v180, v180, s39\n"
      "v_cvt_pk_bf16_f32 v195, v162, v163\n"
      "v_mfma_f32_16x16x32_bf16 v[188:191], v[28:31], v[164:167], v[188:191]\n"
      "v_exp_f32_e64 v253, v253\n"
      "v_mfma_f32_16x16x32_bf16 v[200:203], v[66:69], v[144:147], 0\n"
      "v_exp_f32 v254, v254\n"
      "v_mfma_f32_16x16x32_bf16 v[200:203], v[66:69], v[62:65], v[200:203]\n"
      "v_exp_f32 v255, v255\n"
      "v_mfma_f32_16x16x32_bf16 v[160:163], v[216:219], v[144:147], 0\n"
      "v_exp_f32 v208, v208\n"
      "v_mfma_f32_16x16x32_bf16 v[160:163], v[50:53], v[62:65], v[160:163]\n"
      "s_waitcnt lgkmcnt(0)\n"
      "v_fmac_f32 v236, v181, v181\n"
      "v_fmac_f32 v236, v182, v182\n"
      "v_mfma_f32_16x16x32_bf16 v[164:167], v[240:243], v[144:147], 0\n"
      "v_fmac_f32 v236, v183, v183\n"
      "v_fmac_f32 v236, v184, v184\n"
      "v_mfma_f32_16x16x32_bf16 v[164:167], v[54:57], v[62:65], v[164:167]\n"
      "v_fmac_f32 v236, v185, v185\n"
      "v_fmac_f32 v236, v186, v186\n"
      "v_mfma_f32_16x16x32_bf16 v[148:151], v[232:235], v[144:147], 0\n"
      "v_fmac_f32 v236, v187, v187\n"
      "v_fmac_f32 v236, v188, v188\n"
      "v_mfma_f32_16x16x32_bf16 v[148:151], v[58:61], v[62:65], v[148:151]\n"
      "v_fmac_f32 v236, v189, v189\n"
      "v_fmac_f32 v236, v190, v190\n"
      "v_mfma_f32_16x16x32_bf16 v[144:147], v[212:215], v[196:199], v[102:105]\n"
      "v_cvt_pk_bf16_f32 v62, v252, v253\n"
      "v_fmac_f32_e64 v236, v191, v191\n"
      "v_mfma_f32_16x16x32_bf16 v[144:147], v[34:37], v[44:47], v[144:147]\n"
      "ds_read_b128 v[212:215], v121 offset:18432\n"
      "v_cvt_pk_bf16_f32 v63, v254, v255\n"
      "v_mfma_f32_16x16x32_bf16 v[252:255], v[140:143], v[196:199], v[106:109]\n"
      "v_cndmask_b32 v237, v237, v209, s[42:43]\n"
      "v_cvt_pk_bf16_f32 v64, v208, 0\n"
      "v_mfma_f32_16x16x32_bf16 v[252:255], v[40:43], v[44:47], v[252:255]\n"
      "v_rcp_f32 v200, v200\n"
      "v_mfma_f32_16x16x32_bf16 v[208:211], v[136:139], v[196:199], v[74:77]\n"
      "v_mul_f32 v161, v200, v161\n"
      "v_mul_f32_e64 v160, v200, v160\n"
      "v_mfma_f32_16x16x32_bf16 v[208:211], v[228:231], v[156:159], v[208:211]\n"
      "v_cvt_pk_bf16_f32 v160, v160, v161\n"
      "v_mul_f32 v162, v200, v162\n"
      "v_mfma_f32_16x16x32_bf16 v[208:211], v[152:155], v[44:47], v[208:211]\n"
      "v_mul_f32 v163, v200, v163\n"
      "v_cvt_pk_bf16_f32 v161, v162, v163\n"
      "v_mfma_f32_16x16x32_bf16 v[244:247], v[156:159], v[196:199], v[110:113]\n"
      "ds_read_b128 v[140:143], v123 offset:25088\n"
      "v_mul_f32 v164, v200, v164\n"
      "v_mfma_f32_16x16x32_bf16 v[244:247], v[46:49], v[44:47], v[244:247]\n"
      "v_mul_f32 v165, v200, v165\n"
      "v_cvt_pk_bf16_f32 v162, v164, v165\n"
      "v_mfma_f32_16x16x32_bf16 v[196:199], v[228:231], v[136:139], v[76:79]\n"
      "ds_read_b128 v[156:159], v123 offset:28160\n"
      "v_mul_f32 v166, v200, v166\n"
      "v_mfma_f32_16x16x32_bf16 v[228:231], v[66:69], v[192:195], 0\n"
      "ds_read_b128 v[220:223], v123 offset:31232\n"
      "v_mul_f32 v167, v200, v167\n"
      "v_mfma_f32_16x16x32_bf16 v[228:231], v[66:69], v[62:65], v[228:231]\n"
      "v_mul_f32_e64 v148, v200, v148\n"
      "v_cvt_pk_bf16_f32 v163, v166, v167\n"
      "v_mfma_f32_16x16x16_bf16 v[196:199], v[154:155], v[152:153], v[196:199]\n"
      "ds_write_b128 v124, v[160:163]\n"
      "v_mfma_f32_16x16x32_bf16 v[248:251], v[216:219], v[192:195], 0\n"
      "s_waitcnt lgkmcnt(4)\n"
      "v_mul_f32 v149, v200, v149\n"
      "v_cvt_pk_bf16_f32 v148, v148, v149\n"
      "v_mfma_f32_16x16x32_bf16 v[248:251], v[50:53], v[62:65], v[248:251]\n"
      "s_mov_b32 exec_hi, 0\n"
      "global_store_dwordx4 v118, v[212:215], s[54:55] sc1\n"
      "s_mov_b32 exec_hi, -1\n"
      "v_mfma_f32_16x16x32_bf16 v[164:167], v[240:243], v[192:195], 0\n"
      "s_add_u32 s28, s27, 2\n"
      "v_mul_f32 v151, v200, v151\n"
      "v_mul_f32 v150, v200, v150\n"
      "v_mfma_f32_16x16x32_bf16 v[164:167], v[54:57], v[62:65], v[164:167]\n"
      "s_waitcnt lgkmcnt(2)\n"
      "v_cvt_pk_bf16_f32 v149, v150, v151\n"
      "v_cndmask_b32 v237, v237, v209, s[44:45]\n"
      "v_mfma_f32_16x16x32_bf16 v[212:215], v[232:235], v[192:195], 0\n"
      "s_min_u32 m0, s28, s3\n"
      "v_cndmask_b32 v237, v237, v197, s[46:47]\n"
      "s_movrels_b32 s9, s80\n"
      "v_mfma_f32_16x16x32_bf16 v[212:215], v[58:61], v[62:65], v[212:215]\n"
      "ds_write_b64 v125, v[148:149]\n"
      "v_mfma_f32_16x16x32_bf16 v[192:195], a[216:219], v[224:227], 0\n"
      "s_lshr_b32 s8, s9, 8\n"
      "v_exp_f32 v237, v237\n"
      "v_mfma_f32_16x16x32_bf16 v[192:195], a[228:231], v[204:207], v[192:195]\n"
      "s_and_b32 s9, s9, 0xff\n"
      "v_exp_f32_e64 v144, v144\n"
      "v_cvt_pk_bf16_f32 v237, v237, v237\n"
      "v_mfma_f32_16x16x32_bf16 v[196:199], a[220:223], v[224:227], 0\n"
      "s_mul_i32 s10, s8, 73\n"
      "v_exp_f32_e64 v145, v145\n"
      "v_mfma_f32_16x16x32_bf16 v[196:199], a[232:235], v[204:207], v[196:199]\n"
      "ds_write_b16 v130, v237\n"
      "v_mfma_f32_16x16x32_bf16 v[200:203], a[224:227], v[224:227], 0\n"
      "ds_read_b128 v[148:151], v131\n"
      "v_cvt_pk_bf16_f32 v144, v144, v145\n"
      "v_mfma_f32_16x16x32_bf16 v[200:203], a[236:239], v[204:207], v[200:203]\n"
      "ds_read_b128 v[204:207], v131 offset:64\n"
      "v_exp_f32 v146, v146\n"
      "v_mfma_f32_16x16x32_bf16 v[192:195], a[240:243], v[140:143], v[192:195]\n"
      "ds_read_b128 v[152:155], v123 offset:34304\n"
      "s_add_u32 s10, s10, s9\n"
      "v_mfma_f32_16x16x32_bf16 v[192:195], a[252:255], v[156:159], v[192:195]\n"
      "ds_read_b128 v[136:139], v114 offset:18432\n"
      "v_exp_f32_e64 v147, v147\n"
      "v_mfma_f32_16x16x32_bf16 v[196:199], a[244:247], v[140:143], v[196:199]\n"
      "s_mul_i32 s28, s10, 4802\n"
      "v_exp_f32 v252, v252\n"
      "v_mfma_f32_16x16x32_bf16 v[196:199], v[0:3], v[156:159], v[196:199]\n"
      "s_add_u32 s56, s18, s28\n"
      "v_cvt_pk_bf16_f32 v145, v146, v147\n"
      "v_exp_f32_e64 v253, v253\n"
      "v_mfma_f32_16x16x32_bf16 v[200:203], a[248:251], v[140:143], v[200:203]\n"
      "ds_read_b128 v[140:143], v115 offset:18432\n"
      "v_cvt_pk_bf16_f32 v146, v252, v253\n"
      "v_mfma_f32_16x16x32_bf16 v[200:203], v[4:7], v[156:159], v[200:203]\n"
      "s_addc_u32 s57, s19, 0\n"
      "v_exp_f32 v254, v254\n"
      "v_exp_f32 v255, v255\n"
      "v_exp_f32_e64 v244, v244\n"
      "s_waitcnt lgkmcnt(3)\n"
      "v_mfma_f32_16x16x32_bf16 v[224:227], v[148:151], v[66:69], 0\n"
      "s_and_b32 s56, s56, -16\n"
      "v_cvt_pk_bf16_f32 v147, v254, v255\n"
      "s_waitcnt lgkmcnt(2)\n"
      "v_mfma_f32_16x16x32_bf16 v[224:227], v[204:207], v[66:69], v[224:227]\n"
      "s_sub_u32 s58, s56, 110592\n"
      "v_exp_f32 v245, v245\n"
      "v_mfma_f32_16x16x32_bf16 v[192:195], v[8:11], v[220:223], v[192:195]\n"
      "s_subb_u32 s59, s57, 0\n"
      "v_exp_f32_e64 v246, v246\n"
      "v_mfma_f32_16x16x32_bf16 v[192:195], v[20:23], v[152:155], v[192:195]\n"
      "s_sub_u32 s56, s56, 73728\n"
      "v_exp_f32 v247, v247\n"
      "v_mfma_f32_16x16x32_bf16 v[196:199], v[12:15], v[220:223], v[196:199]\n"
      "s_subb_u32 s57, s57, 0\n"
      "v_exp_f32 v208, v208\n"
      "v_mfma_f32_16x16x32_bf16 v[196:199], v[24:27], v[152:155], v[196:199]\n"
      "s_waitcnt lgkmcnt(0)\n"
      "v_cvt_pk_bf16_f32 v62, v244, v245\n"
      "v_cvt_pk_bf16_f32 v63, v246, v247\n"
      "v_mfma_f32_16x16x32_bf16 v[200:203], v[16:19], v[220:223], v[200:203]\n"
      "s_mul_i32 s28, s8, 1373568\n"
      "v_fma_f32 v237, v192, v192, s39\n"
      "v_cvt_pk_bf16_f32 v64, v208, 0\n"
      "v_mfma_f32_16x16x32_bf16 v[200:203], v[28:31], v[152:155], v[200:203]\n"
      "s_mul_i32 s29, s9, 2688\n"
      "v_rcp_f32 v228, v228\n"
      "v_mfma_f32_16x16x32_bf16 v[152:155], v[66:69], v[144:147], 0\n"
      "s_add_u32 s28, s28, s29\n"
      "v_mul_f32 v248, v228, v248\n"
      "v_mul_f32 v249, v228, v249\n"
      "v_mfma_f32_16x16x32_bf16 v[152:155], v[66:69], v[62:65], v[152:155]\n"
      "s_cmp_eq_u32 s8, 72\n"
      "v_mul_f32 v250, v228, v250\n"
      "v_mul_f32 v165, v228, v165\n"
      "v_cvt_pk_bf16_f32 v248, v248, v249\n"
      "v_mfma_f32_16x16x32_bf16 v[156:159], v[216:219], v[144:147], 0\n"
      "s_cselect_b32 s8, 0, 100270464\n"
      "v_mul_f32_e64 v251, v228, v251\n"
      "v_cvt_pk_bf16_f32 v249, v250, v251\n"
      "v_mfma_f32_16x16x32_bf16 v[156:159], v[50:53], v[62:65], v[156:159]\n"
      "s_cmp_eq_u32 s9, 72\n"
      "v_mul_f32 v215, v228, v215\n"
      "v_mul_f32 v164, v228, v164\n"
      "v_mfma_f32_16x16x32_bf16 v[160:163], v[240:243], v[144:147], 0\n"
      "s_cselect_b32 s9, 0, 196224\n"
      "v_cvt_pk_bf16_f32 v250, v164, v165\n"
      "v_mul_f32_e64 v166, v228, v166\n"
      "v_mfma_f32_16x16x32_bf16 v[160:163], v[54:57], v[62:65], v[160:163]\n"
      "s_add_u32 s10, s8, s9\n"
      "v_mul_f32 v167, v228, v167\n"
      "v_cvt_pk_bf16_f32 v251, v166, v167\n"
      "v_mfma_f32_16x16x32_bf16 v[164:167], v[232:235], v[144:147], 0\n"
      "s_add_u32 s28, s28, s10\n"
      "v_mul_f32 v213, v228, v213\n"
      "v_mul_f32_e64 v212, v228, v212\n"
      "v_mfma_f32_16x16x32_bf16 v[164:167], v[58:61], v[62:65], v[164:167]\n"
      "ds_write_b128 v124, v[248:251] offset:256\n"
      "v_mfma_f32_16x16x32_bf16 v[248:251], v[148:151], v[216:219], 0\n"
      "s_add_u32 s52, s16, s28\n"
      "v_cvt_pk_bf16_f32 v212, v212, v213\n"
      "v_mul_f32 v214, v228, v214\n"
      "v_mfma_f32_16x16x32_bf16 v[248:251], v[204:207], v[50:53], v[248:251]\n"
      "s_addc_u32 s53, s17, 0\n"
      "v_mov_b32_dpp v224, v224 quad_perm:[0,0,0,0] row_mask:0xf bank_mask:0xf\n"
      "v_cvt_pk_bf16_f32 v213, v214, v215\n"
      "v_mfma_f32_16x16x32_bf16 v[216:219], v[148:151], v[240:243], 0\n"
      "s_add_u32 s27, s27, 1\n"
      "v_fmac_f32 v237, v193, v193\n"
      "v_fmac_f32 v237, v194, v194\n"
      "v_mfma_f32_16x16x32_bf16 v[216:219], v[204:207], v[54:57], v[216:219]\n"
      "ds_write_b64 v125, v[212:213] offset:256\n"
      "v_mfma_f32_16x16x32_bf16 v[212:215], v[148:151], v[232:235], 0\n"
      "s_cmp_ge_u32 s27, s26\n"
      "v_fmac_f32 v237, v195, v195\n"
      "v_fmac_f32 v237, v196, v196\n"
      "v_mfma_f32_16x16x32_bf16 v[212:215], v[204:207], v[58:61], v[212:215]\n"
      "s_cselect_b32 s28, 1, 0\n"
      "v_fmac_f32 v237, v197, v197\n"
      "v_fmac_f32 v237, v198, v198\n"
      "v_fmac_f32 v237, v199, v199\n"
      "v_permlane16_swap_b32 v238, v236\n"
      "v_fmac_f32 v237, v200, v200\n"
      "v_add_f32 v238, v238, v236\n"
      "v_fmac_f32 v237, v201, v201\n"
      "v_fmac_f32 v237, v202, v202\n"
      "v_cndmask_b32 v248, v248, v216, s[42:43]\n"
      "v_rcp_f32 v224, v224\n"
      "v_mfma_f32_16x16x32_bf16 v[148:151], a[4:7], v[136:139], 0\n"
      "s_xor_b32 s29, s10, s11\n"
      "v_fmac_f32 v237, v203, v203\n"
      "v_mov_b32 v239, v237\n"
      "v_mfma_f32_16x16x32_bf16 v[148:151], a[40:43], v[140:143], v[148:151]\n"
      "s_or_b32 s28, s28, s29\n"
      "v_permlane16_swap_b32 v237, v239\n"
      "v_cndmask_b32 v248, v248, v212, s[44:45]\n"
      "v_mul_f32 v248, v224, v248\n"
      "v_add_f32 v237, v237, v239\n"
      "v_mfma_f32_16x16x32_bf16 v[144:147], a[0:3], v[136:139], 0\n"
      "s_nop 0\n"
      "v_permlane32_swap_b32 v238, v237\n"
      "v_cvt_pk_bf16_f32 v248, v248, v248\n"
      "v_add_f32_e64 v238, v238, v237\n"
      "v_mfma_f32_16x16x32_bf16 v[144:147], a[36:39], v[140:143], v[144:147]\n"
      "ds_write_b16 v135, v248\n"
      "ds_write_b32 v129, v238\n"
      "L_join_30_%=:\n"
      "s_cbranch_scc0 L_loop_%=\n"
      "L_tail0_%=:\n"
      "s_mov_b32 s11, s10\n"
      "s_sub_u32 s28, s60, s8\n"
      "s_sub_u32 s29, s61, s9\n"
      "s_mov_b32 s60, s8\n"
      "s_mov_b32 s61, s9\n"
      "s_mov_b64 exec, s[64:65]\n"
      "v_add_u32 v116, s28, v116\n"
      "s_mov_b64 exec, s[70:71]\n"
      "v_add_u32 v116, s29, v116\n"
      "s_mov_b64 exec, s[66:67]\n"
      "v_add_u32 v117, s28, v117\n"
      "s_mov_b64 exec, s[72:73]\n"
      "v_add_u32 v117, s29, v117\n"
      "s_mov_b64 exec, s[68:69]\n"
      "v_add_u32_e64 v118, s28, v118\n"
      "s_mov_b64 exec, s[74:75]\n"
      "v_add_u32_e64 v118, s29, v118\n"
      "s_mov_b64 exec, -1\n"
      "s_nop 4\n"
      "s_cmp_lt_u32 s27, s26\n"
      "s_cbranch_scc1 L_loop_%=\n"
      "s_nop 7\n"
      "s_nop 7\n"
      "v_mbcnt_lo_u32_b32 v144, -1, 0\n"
      "v_mbcnt_hi_u32_b32 v144, -1, v144\n"
      "v_rcp_f32_e64 v152, v152\n"
      "v_and_b32_e64 v153, 15, v144\n"
      "v_pk_mul_f32 v[160:161], v[160:161], v[152:153] op_sel_hi:[1,0]\n"
      "v_pk_mul_f32 v[164:165], v[164:165], v[152:153] op_sel_hi:[1,0]\n"
      "v_cvt_pk_bf16_f32 v164, v164, v165\n"
      "v_pk_mul_f32 v[166:167], v[166:167], v[152:153] op_sel_hi:[1,0]\n"
      "v_cvt_pk_bf16_f32 v165, v166, v167\n"
      "v_pk_mul_f32 v[162:163], v[162:163], v[152:153] op_sel_hi:[1,0]\n"
      "v_lshrrev_b32_e64 v145, 4, v144\n"
      "v_pk_mul_f32 v[156:157], v[156:157], v[152:153] op_sel_hi:[1,0]\n"
      "v_mul_u32_u24 v145, 352, v145\n"
      "v_cvt_pk_bf16_f32 v156, v156, v157\n"
      "v_pk_mul_f32 v[158:159], v[158:159], v[152:153] op_sel_hi:[1,0]\n"
      "v_cvt_pk_bf16_f32 v157, v158, v159\n"
      "v_lshl_add_u32 v145, v153, 4, v145\n"
      "v_cvt_pk_bf16_f32 v159, v162, v163\n"
      "v_cvt_pk_bf16_f32 v158, v160, v161\n"
      "v_add_u32 v145, 150640, v145\n"
      "ds_write_b64 v125, v[164:165] offset:512\n"
      "ds_write_b128 v124, v[156:159] offset:512\n"
      "s_waitcnt lgkmcnt(0)\n"
      "s_barrier\n"
      "ds_read_b128 v[140:143], v123 offset:3072\n"
      "ds_read_b128 v[152:155], v123\n"
      "ds_read_b128 v[156:159], v128\n"
      "s_sub_u32 s28, s27, 3\n"
      "s_cselect_b64 s[54:55], s[12:13], s[22:23]\n"
      "s_max_i32 m0, s28, 0\n"
      "ds_read_b128 v[136:139], v119\n"
      "s_movrels_b32 s9, s80\n"
      "s_lshr_b32 s8, s9, 8\n"
      "s_and_b32 s9, s9, 0xff\n"
      "s_mul_i32 s28, s8, 1373568\n"
      "s_mul_i32 s29, s9, 2688\n"
      "ds_read_b128 v[212:215], v123 offset:6144\n"
      "ds_read_b128 v[164:167], v119 offset:6144\n"
      "s_add_u32 s28, s28, s29\n"
      "ds_read_b128 v[160:163], v119 offset:12288\n"
      "ds_read_b128 v[148:151], v120\n"
      "s_cmp_eq_u32 s8, 72\n"
      "s_cselect_b32 s8, 0, 100270464\n"
      "s_cmp_eq_u32 s9, 72\n"
      "s_cselect_b32 s9, 0, 196224\n"
      "ds_read_b128 v[236:239], v121\n"
      "s_add_u32 s10, s8, s9\n"
      "s_add_u32 s28, s28, s10\n"
      "s_add_u32 s54, s54, s28\n"
      "s_waitcnt lgkmcnt(7)\n"
      "v_mfma_f32_16x16x32_bf16 v[216:219], a[216:219], v[152:155], 0\n"
      "ds_read_b128 v[248:251], v123 offset:9216\n"
      "s_waitcnt lgkmcnt(7)\n"
      "v_mfma_f32_16x16x32_bf16 v[216:219], a[228:231], v[140:143], v[216:219]\n"
      "ds_read_b128 v[204:207], v123 offset:12288\n"
      "v_add_f32 v158, |v158|, |v159|\n"
      "v_add_f32 v156, |v156|, |v157|\n"
      "v_mfma_f32_16x16x32_bf16 v[232:235], a[220:223], v[152:155], 0\n"
      "ds_read_b128 v[240:243], v123 offset:15360\n"
      "v_add_f32 v156, v156, v158\n"
      "v_mfma_f32_16x16x32_bf16 v[232:235], a[232:235], v[140:143], v[232:235]\n"
      "s_addc_u32 s55, s55, 0\n"
      "v_rsq_f32 v156, v156\n"
      "v_mfma_f32_16x16x32_bf16 v[220:223], a[224:227], v[152:155], 0\n"
      "ds_read_b128 v[152:155], v123 offset:3328\n"
      "v_mul_f32 v156, s41, v156\n"
      "v_mov_b32 v158, v156\n"
      "v_mfma_f32_16x16x32_bf16 v[220:223], a[236:239], v[140:143], v[220:223]\n"
      "ds_read_b128 v[140:143], v123 offset:6400\n"
      "v_permlane32_swap_b32_e64 v156, v158\n"
      "v_mov_b32_e64 v159, v156\n"
      "s_cmp_lg_u32 s10, s11\n"
      "s_waitcnt lgkmcnt(5)\n"
      "ds_read_b128 v[224:227], v123 offset:256\n"
      "v_permlane16_swap_b32_e64 v156, v159\n"
      "s_waitcnt lgkmcnt(5)\n"
      "v_mfma_f32_16x16x32_bf16 v[216:219], a[240:243], v[212:215], v[216:219]\n"
      "s_cbranch_scc1 L_edge_35_%=\n"
      "L_eb_36_%=:\n"
      "v_mfma_f32_16x16x32_bf16 v[216:219], a[252:255], v[248:251], v[216:219]\n"
      "global_store_dwordx4 v116, v[136:139], s[54:55] sc1\n"
      "v_mfma_f32_16x16x32_bf16 v[232:235], a[244:247], v[212:215], v[232:235]\n"
      "ds_read_b128 v[244:247], v123 offset:9472\n"
      "v_mul_f32 v168, v156, v168\n"
      "v_mfma_f32_16x16x32_bf16 v[232:235], v[0:3], v[248:251], v[232:235]\n"
      "v_mul_f32 v169, v156, v169\n"
      "v_mul_f32_e64 v170, v156, v170\n"
      "v_mfma_f32_16x16x32_bf16 v[220:223], a[248:251], v[212:215], v[220:223]\n"
      "ds_read_b128 v[136:139], v123 offset:12544\n"
      "v_cvt_pk_bf16_f32 v168, v168, v169\n"
      "v_mfma_f32_16x16x32_bf16 v[220:223], v[4:7], v[248:251], v[220:223]\n"
      "s_waitcnt lgkmcnt(3)\n"
      "ds_read_b128 v[248:251], v123 offset:15616\n"
      "v_mfma_f32_16x16x32_bf16 v[216:219], v[8:11], v[204:207], v[216:219]\n"
      "v_mul_f32 v171, v156, v171\n"
      "v_cvt_pk_bf16_f32 v169, v170, v171\n"
      "v_mfma_f32_16x16x32_bf16 v[216:219], v[20:23], v[240:243], v[216:219]\n"
      "s_waitcnt lgkmcnt(3)\n"
      "v_mul_f32 v173, v156, v173\n"
      "v_mul_f32_e64 v172, v156, v172\n"
      "v_mfma_f32_16x16x32_bf16 v[232:235], v[12:15], v[204:207], v[232:235]\n"
      "v_mul_f32 v174, v156, v174\n"
      "v_mul_f32 v175, v156, v175\n"
      "v_mfma_f32_16x16x32_bf16 v[232:235], v[24:27], v[240:243], v[232:235]\n"
      "global_store_dwordx4 v116, v[164:167], s[54:55] offset:128 sc1\n"
      "v_mfma_f32_16x16x32_bf16 v[220:223], v[16:19], v[204:207], v[220:223]\n"
      "v_mul_f32 v177, v156, v177\n"
      "v_cvt_pk_bf16_f32 v170, v172, v173\n"
      "v_mfma_f32_16x16x32_bf16 v[220:223], v[28:31], v[240:243], v[220:223]\n"
      "v_mul_f32 v176, v156, v176\n"
      "v_cvt_pk_bf16_f32 v171, v174, v175\n"
      "v_mfma_f32_16x16x32_bf16 v[172:175], a[216:219], v[224:227], 0\n"
      "ds_read_b128 v[240:243], v123 offset:512\n"
      "v_mul_f32_e64 v178, v156, v178\n"
      "v_mfma_f32_16x16x32_bf16 v[172:175], a[228:231], v[152:155], v[172:175]\n"
      "ds_write_b128 v126, v[168:171] offset:18432\n"
      "v_mfma_f32_16x16x32_bf16 v[168:171], a[220:223], v[224:227], 0\n"
      "ds_read_b128 v[164:167], v123 offset:3584\n"
      "v_cvt_pk_bf16_f32 v176, v176, v177\n"
      "v_mfma_f32_16x16x32_bf16 v[168:171], a[232:235], v[152:155], v[168:171]\n"
      "s_waitcnt lgkmcnt(3)\n"
      "v_mul_f32 v179, v156, v179\n"
      "v_mfma_f32_16x16x32_bf16 v[204:207], a[224:227], v[224:227], 0\n"
      "s_lshl_b32 s29, s24, 8\n"
      "v_cvt_pk_bf16_f32 v177, v178, v179\n"
      "v_mul_f32 v180, v159, v180\n"
      "v_mfma_f32_16x16x32_bf16 v[204:207], a[236:239], v[152:155], v[204:207]\n"
      "ds_write_b64 v127, v[176:177] offset:18432\n"
      "v_mfma_f32_16x16x32_bf16 v[172:175], a[240:243], v[140:143], v[172:175]\n"
      "ds_read_b128 v[152:155], v123 offset:6656\n"
      "v_mul_f32_e64 v181, v159, v181\n"
      "v_mfma_f32_16x16x32_bf16 v[172:175], a[252:255], v[244:247], v[172:175]\n"
      "ds_read_b128 v[176:179], v123 offset:9728\n"
      "v_cvt_pk_bf16_f32 v180, v180, v181\n"
      "v_mfma_f32_16x16x32_bf16 v[168:171], a[244:247], v[140:143], v[168:171]\n"
      "v_mul_f32 v183, v159, v183\n"
      "v_mul_f32 v182, v159, v182\n"
      "v_mfma_f32_16x16x32_bf16 v[168:171], v[0:3], v[244:247], v[168:171]\n"
      "s_add_u32 s29, s29, 159088\n"
      "v_cvt_pk_bf16_f32 v181, v182, v183\n"
      "v_mul_f32_e64 v184, v159, v184\n"
      "v_mfma_f32_16x16x32_bf16 v[204:207], a[248:251], v[140:143], v[204:207]\n"
      "v_mul_f32 v185, v159, v185\n"
      "v_cvt_pk_bf16_f32 v182, v184, v185\n"
      "v_mfma_f32_16x16x32_bf16 v[204:207], v[4:7], v[244:247], v[204:207]\n"
      "s_waitcnt lgkmcnt(3)\n"
      "v_mul_f32 v187, v159, v187\n"
      "v_mul_f32 v186, v159, v186\n"
      "v_mfma_f32_16x16x32_bf16 v[172:175], v[8:11], v[136:139], v[172:175]\n"
      "v_mul_f32_e64 v188, v159, v188\n"
      "v_cvt_pk_bf16_f32 v183, v186, v187\n"
      "v_mfma_f32_16x16x32_bf16 v[172:175], v[20:23], v[248:251], v[172:175]\n"
      "ds_write_b128 v126, v[180:183] offset:20480\n"
      "v_mfma_f32_16x16x32_bf16 v[168:171], v[12:15], v[136:139], v[168:171]\n"
      "ds_read_b128 v[180:183], v123 offset:12800\n"
      "v_mul_f32_e64 v189, v159, v189\n"
      "v_mfma_f32_16x16x32_bf16 v[168:171], v[24:27], v[248:251], v[168:171]\n"
      "ds_read_b128 v[184:187], v123 offset:15872\n"
      "v_cvt_pk_bf16_f32 v188, v188, v189\n"
      "v_mfma_f32_16x16x32_bf16 v[204:207], v[16:19], v[136:139], v[204:207]\n"
      "v_mul_f32 v191, v159, v191\n"
      "v_mul_f32 v190, v159, v190\n"
      "v_mfma_f32_16x16x32_bf16 v[204:207], v[28:31], v[248:251], v[204:207]\n"
      "v_fma_f32 v159, v216, v216, s39\n"
      "v_cvt_pk_bf16_f32 v189, v190, v191\n"
      "v_mfma_f32_16x16x32_bf16 v[140:143], a[216:219], v[240:243], 0\n"
      "global_store_dwordx4 v116, v[160:163], s[54:55] offset:256 sc1\n"
      "v_mfma_f32_16x16x32_bf16 v[140:143], a[228:231], v[164:167], v[140:143]\n"
      "ds_write_b64 v127, v[188:189] offset:20480\n"
      "v_mfma_f32_16x16x32_bf16 v[188:191], a[220:223], v[240:243], 0\n"
      "ds_read_b128 v[136:139], v145\n"
      "v_fmac_f32_e64 v159, v217, v217\n"
      "v_mfma_f32_16x16x32_bf16 v[188:191], a[232:235], v[164:167], v[188:191]\n"
      "ds_read_b128 v[160:163], v145 offset:1408\n"
      "v_fmac_f32 v159, v218, v218\n"
      "v_mfma_f32_16x16x32_bf16 v[248:251], a[224:227], v[240:243], 0\n"
      "v_fmac_f32 v159, v219, v219\n"
      "v_fmac_f32 v159, v232, v232\n"
      "v_mfma_f32_16x16x32_bf16 v[248:251], a[236:239], v[164:167], v[248:251]\n"
      "s_waitcnt lgkmcnt(3)\n"
      "v_fmac_f32 v159, v233, v233\n"
      "v_fmac_f32 v159, v234, v234\n"
      "v_mfma_f32_16x16x32_bf16 v[140:143], a[240:243], v[152:155], v[140:143]\n"
      "v_fmac_f32 v159, v235, v235\n"
      "v_fmac_f32 v159, v220, v220\n"
      "v_mfma_f32_16x16x32_bf16 v[140:143], a[252:255], v[176:179], v[140:143]\n"
      "v_fmac_f32 v159, v221, v221\n"
      "v_fmac_f32 v159, v222, v222\n"
      "v_mfma_f32_16x16x32_bf16 v[188:191], a[244:247], v[152:155], v[188:191]\n"
      "global_store_dwordx4 v117, v[148:151], s[54:55] sc1\n"
      "v_mfma_f32_16x16x32_bf16 v[188:191], v[0:3], v[176:179], v[188:191]\n"
      "v_mul_f32 v192, v158, v192\n"
      "v_fmac_f32 v159, v223, v223\n"
      "v_mfma_f32_16x16x32_bf16 v[248:251], a[248:251], v[152:155], v[248:251]\n"
      "ds_read_b128 v[152:155], v145 offset:2816\n"
      "v_mul_f32 v193, v158, v193\n"
      "v_mfma_f32_16x16x32_bf16 v[248:251], v[4:7], v[176:179], v[248:251]\n"
      "v_cvt_pk_bf16_f32 v192, v192, v193\n"
      "v_mul_f32 v194, v158, v194\n"
      "v_mfma_f32_16x16x32_bf16 v[140:143], v[8:11], v[180:183], v[140:143]\n"
      "ds_read_b128 v[176:179], v145 offset:4224\n"
      "v_mul_f32 v195, v158, v195\n"
      "v_mfma_f32_16x16x32_bf16 v[140:143], v[20:23], v[184:187], v[140:143]\n"
      "v_mul_f32 v196, v158, v196\n"
      "v_cvt_pk_bf16_f32 v193, v194, v195\n"
      "v_mfma_f32_16x16x32_bf16 v[188:191], v[12:15], v[180:183], v[188:191]\n"
      "v_mul_f32_e64 v197, v158, v197\n"
      "v_cvt_pk_bf16_f32 v194, v196, v197\n"
      "v_mfma_f32_16x16x32_bf16 v[188:191], v[24:27], v[184:187], v[188:191]\n"
      "ds_read_b128 v[148:151], v145 offset:5632\n"
      "v_mul_f32 v198, v158, v198\n"
      "v_mfma_f32_16x16x32_bf16 v[248:251], v[16:19], v[180:183], v[248:251]\n"
      "s_waitcnt lgkmcnt(3)\n"
      "v_mul_f32_e64 v199, v158, v199\n"
      "v_cvt_pk_bf16_f32 v195, v198, v199\n"
      "v_mfma_f32_16x16x32_bf16 v[248:251], v[28:31], v[184:187], v[248:251]\n"
      "ds_write_b128 v126, v[192:195] offset:22528\n"
      "v_mfma_f32_16x16x32_bf16 v[192:195], a[216:219], v[136:139], 0\n"
      "ds_read_b128 v[184:187], v145 offset:7040\n"
      "v_mul_f32 v201, v158, v201\n"
      "v_mfma_f32_16x16x32_bf16 v[192:195], a[228:231], v[160:163], v[192:195]\n"
      "v_mul_f32 v200, v158, v200\n"
      "v_mul_f32 v202, v158, v202\n"
      "v_mfma_f32_16x16x32_bf16 v[180:183], a[220:223], v[136:139], 0\n"
      "v_cvt_pk_bf16_f32 v200, v200, v201\n"
      "s_waitcnt lgkmcnt(3)\n"
      "v_mfma_f32_16x16x32_bf16 v[180:183], a[232:235], v[160:163], v[180:183]\n"
      "ds_read_b128 v[196:199], v145 offset:256\n"
      "v_mul_f32_e64 v203, v158, v203\n"
      "v_mfma_f32_16x16x32_bf16 v[164:167], a[224:227], v[136:139], 0\n"
      "v_fma_f32 v158, v172, v172, s39\n"
      "v_cvt_pk_bf16_f32 v201, v202, v203\n"
      "v_mfma_f32_16x16x32_bf16 v[164:167], a[236:239], v[160:163], v[164:167]\n"
      "ds_write_b64 v127, v[200:201] offset:22528\n"
      "v_mfma_f32_16x16x32_bf16 v[192:195], a[240:243], v[152:155], v[192:195]\n"
      "ds_read_b128 v[200:203], v145 offset:1664\n"
      "v_fmac_f32 v158, v173, v173\n"
      "v_mfma_f32_16x16x32_bf16 v[192:195], a[252:255], v[176:179], v[192:195]\n"
      "v_fmac_f32 v158, v174, v174\n"
      "v_fmac_f32_e64 v158, v175, v175\n"
      "v_mfma_f32_16x16x32_bf16 v[180:183], a[244:247], v[152:155], v[180:183]\n"
      "s_waitcnt lgkmcnt(3)\n"
      "v_fmac_f32 v158, v168, v168\n"
      "v_fmac_f32_e64 v158, v169, v169\n"
      "v_mfma_f32_16x16x32_bf16 v[180:183], v[0:3], v[176:179], v[180:183]\n"
      "ds_read_b128 v[136:139], v145 offset:3072\n"
      "v_fmac_f32 v158, v170, v170\n"
      "v_mfma_f32_16x16x32_bf16 v[164:167], a[248:251], v[152:155], v[164:167]\n"
      "ds_read_b128 v[152:155], v145 offset:4480\n"
      "v_fmac_f32 v158, v171, v171\n"
      "v_mfma_f32_16x16x32_bf16 v[164:167], v[4:7], v[176:179], v[164:167]\n"
      "v_fmac_f32 v158, v204, v204\n"
      "v_fmac_f32 v158, v205, v205\n"
      "v_mfma_f32_16x16x32_bf16 v[192:195], v[8:11], v[148:151], v[192:195]\n"
      "v_fmac_f32 v158, v206, v206\n"
      "v_fmac_f32 v158, v207, v207\n"
      "v_mfma_f32_16x16x32_bf16 v[192:195], v[20:23], v[184:187], v[192:195]\n"
      "ds_read_b128 v[176:179], v145 offset:5888\n"
      "v_lshl_add_u32 v156, v144, 2, s29\n"
      "v_mfma_f32_16x16x32_bf16 v[180:183], v[12:15], v[148:151], v[180:183]\n"
      "s_waitcnt lgkmcnt(3)\n"
      "v_lshrrev_b32 v157, 3, v144\n"
      "v_mfma_f32_16x16x32_bf16 v[180:183], v[24:27], v[184:187], v[180:183]\n"
      "ds_read_b32 v156, v156\n"
      "v_fma_f32 v144, v140, v140, s39\n"
      "v_mfma_f32_16x16x32_bf16 v[164:167], v[16:19], v[148:151], v[164:167]\n"
      "ds_read_b128 v[148:151], v145 offset:7296\n"
      "v_mfma_f32_16x16x32_bf16 v[164:167], v[28:31], v[184:187], v[164:167]\n"
      "s_lshl_b32 s29, s24, 3\n"
      "v_fmac_f32 v144, v141, v141\n"
      "v_add_u32 v157, s29, v157\n"
      "v_mfma_f32_16x16x32_bf16 v[184:187], a[216:219], v[196:199], 0\n"
      "v_cmp_gt_u32 s[48:49], s26, v157\n"
      "v_fmac_f32 v144, v142, v142\n"
      "v_mfma_f32_16x16x32_bf16 v[184:187], a[228:231], v[200:203], v[184:187]\n"
      "s_mov_b32 exec_hi, 0\n"
      "global_store_dwordx4 v118, v[236:239], s[54:55] sc1\n"
      "s_mov_b32 exec_hi, -1\n"
      "v_mfma_f32_16x16x32_bf16 v[160:163], a[220:223], v[196:199], 0\n"
      "s_waitcnt lgkmcnt(3)\n"
      "v_fmac_f32 v144, v143, v143\n"
      "v_fmac_f32 v144, v188, v188\n"
      "v_mfma_f32_16x16x32_bf16 v[160:163], a[232:235], v[200:203], v[160:163]\n"
      "v_fmac_f32 v144, v189, v189\n"
      "v_fmac_f32 v144, v190, v190\n"
      "v_mfma_f32_16x16x32_bf16 v[236:239], a[224:227], v[196:199], 0\n"
      "v_fmac_f32 v144, v191, v191\n"
      "v_fmac_f32 v144, v248, v248\n"
      "v_mfma_f32_16x16x32_bf16 v[236:239], a[236:239], v[200:203], v[236:239]\n"
      "v_fmac_f32 v144, v249, v249\n"
      "v_fmac_f32 v144, v250, v250\n"
      "v_mfma_f32_16x16x32_bf16 v[184:187], a[240:243], v[136:139], v[184:187]\n"
      "v_fmac_f32 v144, v251, v251\n"
      "v_mov_b32 v157, v144\n"
      "v_mfma_f32_16x16x32_bf16 v[184:187], a[252:255], v[152:155], v[184:187]\n"
      "v_permlane16_swap_b32 v159, v158\n"
      "v_mfma_f32_16x16x32_bf16 v[160:163], a[244:247], v[136:139], v[160:163]\n"
      "v_fma_f32 v200, v192, v192, s39\n"
      "v_add_f32 v159, v159, v158\n"
      "v_mfma_f32_16x16x32_bf16 v[160:163], v[0:3], v[152:155], v[160:163]\n"
      "s_waitcnt lgkmcnt(0)\n"
      "v_permlane16_swap_b32 v144, v157\n"
      "v_mfma_f32_16x16x32_bf16 v[236:239], a[248:251], v[136:139], v[236:239]\n"
      "v_add_f32 v144, v144, v157\n"
      "v_mfma_f32_16x16x32_bf16 v[236:239], v[4:7], v[152:155], v[236:239]\n"
      "s_nop 0\n"
      "v_permlane32_swap_b32 v159, v144\n"
      "v_mfma_f32_16x16x32_bf16 v[184:187], v[8:11], v[176:179], v[184:187]\n"
      "v_add_f32 v159, v159, v144\n"
      "v_fmac_f32 v200, v193, v193\n"
      "v_mfma_f32_16x16x32_bf16 v[184:187], v[20:23], v[148:151], v[184:187]\n"
      "ds_write_b32 v129, v159 offset:1024\n"
      "v_mfma_f32_16x16x32_bf16 v[160:163], v[12:15], v[176:179], v[160:163]\n"
      "v_fmac_f32 v200, v194, v194\n"
      "v_fmac_f32 v200, v195, v195\n"
      "v_mfma_f32_16x16x32_bf16 v[160:163], v[24:27], v[148:151], v[160:163]\n"
      "v_fmac_f32 v200, v180, v180\n"
      "v_mfma_f32_16x16x32_bf16 v[236:239], v[16:19], v[176:179], v[236:239]\n"
      "v_fmac_f32 v200, v181, v181\n"
      "v_fmac_f32_e64 v200, v182, v182\n"
      "v_mfma_f32_16x16x32_bf16 v[236:239], v[28:31], v[148:151], v[236:239]\n"
      "v_fmac_f32 v200, v183, v183\n"
      "v_fmac_f32 v200, v164, v164\n"
      "v_fmac_f32 v200, v165, v165\n"
      "v_fmac_f32 v200, v166, v166\n"
      "v_fmac_f32_e64 v200, v167, v167\n"
      "v_pk_mul_f32 v[158:159], v[184:185], v[184:185]\n"
      "v_pk_fma_f32 v[158:159], v[186:187], v[186:187], v[158:159]\n"
      "v_pk_fma_f32 v[158:159], v[160:161], v[160:161], v[158:159]\n"
      "v_pk_fma_f32 v[158:159], v[162:163], v[162:163], v[158:159]\n"
      "v_pk_fma_f32 v[158:159], v[236:237], v[236:237], v[158:159]\n"
      "v_pk_fma_f32 v[158:159], v[238:239], v[238:239], v[158:159]\n"
      "v_add_f32 v158, s39, v158\n"
      "v_add_f32 v157, v158, v159\n"
      "v_mov_b32 v201, v157\n"
      "v_mov_b32 v202, v201\n"
      "v_permlane16_swap_b32 v200, v157\n"
      "v_add_f32_e64 v200, v200, v157\n"
      "v_permlane16_swap_b32_e64 v201, v202\n"
      "v_add_f32_e64 v201, v201, v202\n"
      "s_nop 1\n"
      "v_permlane32_swap_b32_e64 v200, v201\n"
      "v_add_f32_e64 v200, v200, v201\n"
      "ds_write_b32 v129, v200 offset:2048\n"
      "s_waitcnt lgkmcnt(0)\n"
      "s_barrier\n"
      "s_add_u32 s27, s27, 1\n"
      "ds_read_b128 v[152:155], v128 offset:2048\n"
      "ds_read_b128 v[176:179], v128 offset:1024\n"
      "s_sub_u32 s28, s27, 3\n"
      "s_cselect_b64 s[54:55], s[12:13], s[22:23]\n"
      "s_max_i32 m0, s28, 0\n"
      "ds_read_b128 v[200:203], v119 offset:18432\n"
      "s_movrels_b32 s9, s80\n"
      "ds_read_b128 v[136:139], v119 offset:30720\n"
      "s_lshr_b32 s8, s9, 8\n"
      "s_and_b32 s9, s9, 0xff\n"
      "s_mul_i32 s28, s8, 1373568\n"
      "s_mul_i32 s29, s9, 2688\n"
      "s_add_u32 s28, s28, s29\n"
      "s_cmp_eq_u32 s8, 72\n"
      "ds_read_b128 v[196:199], v119 offset:24576\n"
      "s_cselect_b32 s8, 0, 100270464\n"
      "s_cmp_eq_u32 s9, 72\n"
      "s_cselect_b32 s9, 0, 196224\n"
      "s_add_u32 s10, s8, s9\n"
      "s_add_u32 s28, s28, s10\n"
      "s_add_u32 s54, s54, s28\n"
      "s_addc_u32 s55, s55, 0\n"
      "ds_read_b128 v[148:151], v120 offset:18432\n"
      "s_cmp_lg_u32 s10, s11\n"
      "ds_read_b128 v[144:147], v121 offset:18432\n"
      "s_cbranch_scc1 L_edge_37_%=\n"
      "L_eb_38_%=:\n"
      "s_waitcnt lgkmcnt(5)\n"
      "v_add_f32 v178, |v178|, |v179|\n"
      "v_add_f32 v176, |v176|, |v177|\n"
      "v_add_f32 v176, v176, v178\n"
      "v_rsq_f32 v176, v176\n"
      "v_add_f32 v154, |v154|, |v155|\n"
      "v_mul_f32 v176, s41, v176\n"
      "v_mov_b32 v178, v176\n"
      "v_add_f32 v152, |v152|, |v153|\n"
      "v_add_f32 v152, v152, v154\n"
      "v_permlane32_swap_b32 v176, v178\n"
      "v_mov_b32 v155, v176\n"
      "v_rsq_f32 v152, v152\n"
      "v_pk_mul_f32 v[248:249], v[248:249], v[178:179] op_sel_hi:[1,0]\n"
      "v_permlane16_swap_b32_e64 v176, v155\n"
      "v_pk_mul_f32 v[206:207], v[206:207], v[154:155] op_sel:[0,1]\n"
      "v_pk_mul_f32 v[172:173], v[172:173], v[154:155] op_sel:[0,1]\n"
      "v_pk_mul_f32 v[216:217], v[216:217], v[176:177] op_sel_hi:[1,0]\n"
      "v_pk_mul_f32 v[168:169], v[168:169], v[154:155] op_sel:[0,1]\n"
      "v_pk_mul_f32 v[188:189], v[188:189], v[178:179] op_sel_hi:[1,0]\n"
      "v_pk_mul_f32 v[250:251], v[250:251], v[178:179] op_sel_hi:[1,0]\n"
      "v_pk_mul_f32 v[204:205], v[204:205], v[154:155] op_sel:[0,1]\n"
      "v_pk_mul_f32 v[232:233], v[232:233], v[176:177] op_sel_hi:[1,0]\n"
      "v_cvt_pk_bf16_f32 v216, v216, v217\n"
      "v_pk_mul_f32 v[174:175], v[174:175], v[154:155] op_sel:[0,1]\n"
      "v_mul_f32 v152, s41, v152\n"
      "v_pk_mul_f32 v[218:219], v[218:219], v[176:177] op_sel_hi:[1,0]\n"
      "v_pk_mul_f32 v[170:171], v[170:171], v[154:155] op_sel:[0,1]\n"
      "v_mov_b32 v155, v152\n"
      "v_pk_mul_f32 v[142:143], v[142:143], v[178:179] op_sel_hi:[1,0]\n"
      "s_waitcnt lgkmcnt(0)\n"
      "v_cvt_pk_bf16_f32 v204, v204, v205\n"
      "v_pk_mul_f32 v[220:221], v[220:221], v[176:177] op_sel_hi:[1,0]\n"
      "v_permlane32_swap_b32 v152, v155\n"
      "v_mov_b32_e64 v155, v152\n"
      "v_pk_mul_f32 v[222:223], v[222:223], v[176:177] op_sel_hi:[1,0]\n"
      "v_pk_mul_f32 v[140:141], v[140:141], v[178:179] op_sel_hi:[1,0]\n"
      "v_permlane16_swap_b32_e64 v152, v155\n"
      "v_pk_mul_f32 v[234:235], v[234:235], v[176:177] op_sel_hi:[1,0]\n"
      "v_subrev_u32 v153, 73728, v127\n"
      "v_pk_mul_f32 v[164:165], v[164:165], v[152:153] op_sel_hi:[1,0]\n"
      "v_cvt_pk_bf16_f32 v172, v172, v173\n"
      "v_cvt_pk_bf16_f32 v173, v174, v175\n"
      "v_pk_mul_f32 v[236:237], v[236:237], v[154:155] op_sel:[0,1]\n"
      "v_cvt_pk_bf16_f32 v140, v140, v141\n"
      "v_pk_mul_f32 v[180:181], v[180:181], v[152:153] op_sel_hi:[1,0]\n"
      "v_cvt_pk_bf16_f32 v164, v164, v165\n"
      "v_pk_mul_f32 v[184:185], v[184:185], v[154:155] op_sel:[0,1]\n"
      "v_cvt_pk_bf16_f32 v184, v184, v185\n"
      "v_pk_mul_f32 v[182:183], v[182:183], v[152:153] op_sel_hi:[1,0]\n"
      "v_cvt_pk_bf16_f32 v141, v142, v143\n"
      "v_pk_mul_f32 v[192:193], v[192:193], v[152:153] op_sel_hi:[1,0]\n"
      "v_cvt_pk_bf16_f32 v220, v220, v221\n"
      "v_cvt_pk_bf16_f32 v221, v222, v223\n"
      "v_pk_mul_f32 v[166:167], v[166:167], v[152:153] op_sel_hi:[1,0]\n"
      "v_pk_mul_f32 v[194:195], v[194:195], v[152:153] op_sel_hi:[1,0]\n"
      "global_store_dwordx4 v116, v[200:203], s[54:55] sc1\n"
      "ds_write_b64 v127, v[220:221]\n"
      "v_cvt_pk_bf16_f32 v236, v236, v237\n"
      "v_cvt_pk_bf16_f32 v205, v206, v207\n"
      "v_pk_mul_f32 v[160:161], v[160:161], v[154:155] op_sel:[0,1]\n"
      "v_cvt_pk_bf16_f32 v142, v188, v189\n"
      "v_cvt_pk_bf16_f32 v248, v248, v249\n"
      "v_pk_mul_f32 v[162:163], v[162:163], v[154:155] op_sel:[0,1]\n"
      "v_cvt_pk_bf16_f32 v249, v250, v251\n"
      "v_cvt_pk_bf16_f32 v165, v166, v167\n"
      "v_pk_mul_f32 v[190:191], v[190:191], v[178:179] op_sel_hi:[1,0]\n"
      "v_cvt_pk_bf16_f32 v192, v192, v193\n"
      "v_cvt_pk_bf16_f32 v193, v194, v195\n"
      "v_cvt_pk_bf16_f32 v217, v218, v219\n"
      "v_subrev_u32 v154, 73728, v126\n"
      "v_pk_mul_f32 v[186:187], v[186:187], v[154:155] op_sel:[0,1]\n"
      "v_pk_mul_f32 v[238:239], v[238:239], v[154:155] op_sel:[0,1]\n"
      "v_cvt_pk_bf16_f32 v218, v232, v233\n"
      "v_cvt_pk_bf16_f32 v175, v170, v171\n"
      "v_cvt_pk_bf16_f32 v143, v190, v191\n"
      "v_cvt_pk_bf16_f32 v194, v180, v181\n"
      "v_cvt_pk_bf16_f32 v174, v168, v169\n"
      "v_cvt_pk_bf16_f32 v195, v182, v183\n"
      "v_cvt_pk_bf16_f32 v219, v234, v235\n"
      "global_store_dwordx4 v116, v[196:199], s[54:55] offset:128 sc1\n"
      "ds_write_b64 v127, v[204:205] offset:2048\n"
      "ds_write_b128 v154, v[192:195]\n"
      "ds_write_b128 v126, v[172:175] offset:2048\n"
      "ds_write_b128 v126, v[216:219]\n"
      "ds_write_b128 v126, v[140:143] offset:4096\n"
      "global_store_dwordx4 v116, v[136:139], s[54:55] offset:256 sc1\n"
      "global_store_dwordx4 v117, v[148:151], s[54:55] sc1\n"
      "v_cvt_pk_bf16_f32 v185, v186, v187\n"
      "v_cvt_pk_bf16_f32 v237, v238, v239\n"
      "v_cvt_pk_bf16_f32 v186, v160, v161\n"
      "v_cvt_pk_bf16_f32 v187, v162, v163\n"
      "ds_write_b64 v153, v[164:165]\n"
      "ds_write_b128 v154, v[184:187] offset:2048\n"
      "ds_write_b64 v127, v[248:249] offset:4096\n"
      "ds_write_b64 v153, v[236:237] offset:2048\n"
      "s_mov_b32 exec_hi, 0\n"
      "global_store_dwordx4 v118, v[144:147], s[54:55] sc1\n"
      "s_mov_b32 exec_hi, -1\n"
      "s_waitcnt lgkmcnt(0)\n"
      "s_barrier\n"
      "ds_read_b128 v[192:195], v119 offset:6144\n"
      "ds_read_b128 v[184:187], v119\n"
      "s_add_u32 s27, s27, 1\n"
      "v_subrev_u32 v157, 73728, v119\n"
      "s_sub_u32 s28, s27, 3\n"
      "ds_read_b128 v[196:199], v157\n"
      "ds_read_b128 v[188:191], v121\n"
      "ds_read_b128 v[152:155], v157 offset:6144\n"
      "s_cselect_b64 s[54:55], s[12:13], s[22:23]\n"
      "ds_read_b128 v[172:175], v157 offset:12288\n"
      "ds_read_b128 v[140:143], v120\n"
      "s_max_i32 m0, s28, 0\n"
      "ds_read_b128 v[180:183], v119 offset:12288\n"
      "s_movrels_b32 s9, s80\n"
      "s_lshr_b32 s8, s9, 8\n"
      "s_and_b32 s9, s9, 0xff\n"
      "s_mul_i32 s28, s8, 1373568\n"
      "s_mul_i32 s29, s9, 2688\n"
      "s_add_u32 s28, s28, s29\n"
      "s_cmp_eq_u32 s8, 72\n"
      "s_cselect_b32 s8, 0, 100270464\n"
      "s_cmp_eq_u32 s9, 72\n"
      "s_cselect_b32 s9, 0, 196224\n"
      "s_add_u32 s10, s8, s9\n"
      "s_add_u32 s28, s28, s10\n"
      "s_add_u32 s54, s54, s28\n"
      "s_addc_u32 s55, s55, 0\n"
      "s_cmp_lg_u32 s10, s11\n"
      "s_waitcnt lgkmcnt(6)\n"
      "s_cbranch_scc1 L_edge_39_%=\n"
      "L_eb_40_%=:\n"
      "global_store_dwordx4 v116, v[184:187], s[54:55] sc1\n"
      "global_store_dwordx4 v116, v[192:195], s[54:55] offset:128 sc1\n"
      "s_waitcnt lgkmcnt(2)\n"
      "s_waitcnt lgkmcnt(1)\n"
      "s_waitcnt lgkmcnt(0)\n"
      "global_store_dwordx4 v116, v[180:183], s[54:55] offset:256 sc1\n"
      "global_store_dwordx4 v117, v[140:143], s[54:55] sc1\n"
      "s_mov_b32 exec_hi, 0\n"
      "global_store_dwordx4 v118, v[188:191], s[54:55] sc1\n"
      "s_mov_b32 exec_hi, -1\n"
      "s_mov_b64 exec, s[48:49]\n"
      "s_cbranch_execz L_np_41_%=\n"
      "global_store_dwordx4 v156, v[196:199], s[22:23] sc1\n"
      "global_store_dwordx4 v156, v[152:155], s[22:23] offset:128 sc1\n"
      "global_store_dwordx4 v156, v[172:175], s[22:23] offset:256 sc1\n"
      "L_np_41_%=:\n"
      "s_mov_b64 exec, -1\n"
      "s_nop 0\n"
      "s_endpgm\n"
      "L_tail1_%=:\n"
      "s_mov_b32 s11, s10\n"
      "s_sub_u32 s28, s60, s8\n"
      "s_sub_u32 s29, s61, s9\n"
      "s_mov_b32 s60, s8\n"
      "s_mov_b32 s61, s9\n"
      "s_mov_b64 exec, s[64:65]\n"
      "v_add_u32 v116, s28, v116\n"
      "s_mov_b64 exec, s[70:71]\n"
      "v_add_u32 v116, s29, v116\n"
      "s_mov_b64 exec, s[66:67]\n"
      "v_add_u32 v117, s28, v117\n"
      "s_mov_b64 exec, s[72:73]\n"
      "v_add_u32 v117, s29, v117\n"
      "s_mov_b64 exec, s[68:69]\n"
      "v_add_u32 v118, s28, v118\n"
      "s_mov_b64 exec, s[74:75]\n"
      "v_add_u32_e64 v118, s29, v118\n"
      "s_mov_b64 exec, -1\n"
      "s_nop 4\n"
      "s_cmp_lt_u32 s27, s26\n"
      "s_cbranch_scc1 L_mid_%=\n"
      "s_nop 7\n"
      "s_nop 7\n"
      "v_mbcnt_lo_u32_b32 v144, -1, 0\n"
      "v_mbcnt_hi_u32_b32 v144, -1, v144\n"
      "v_rcp_f32_e64 v136, v136\n"
      "v_and_b32_e64 v137, 15, v144\n"
      "v_pk_mul_f32 v[152:153], v[152:153], v[136:137] op_sel_hi:[1,0]\n"
      "v_pk_mul_f32 v[156:157], v[156:157], v[136:137] op_sel_hi:[1,0]\n"
      "v_cvt_pk_bf16_f32 v156, v156, v157\n"
      "v_pk_mul_f32 v[158:159], v[158:159], v[136:137] op_sel_hi:[1,0]\n"
      "v_cvt_pk_bf16_f32 v157, v158, v159\n"
      "v_pk_mul_f32 v[154:155], v[154:155], v[136:137] op_sel_hi:[1,0]\n"
      "v_lshrrev_b32_e64 v145, 4, v144\n"
      "v_pk_mul_f32 v[140:141], v[140:141], v[136:137] op_sel_hi:[1,0]\n"
      "v_mul_u32_u24 v145, 352, v145\n"
      "v_cvt_pk_bf16_f32 v140, v140, v141\n"
      "v_pk_mul_f32 v[142:143], v[142:143], v[136:137] op_sel_hi:[1,0]\n"
      "v_cvt_pk_bf16_f32 v141, v142, v143\n"
      "v_lshl_add_u32 v145, v137, 4, v145\n"
      "v_cvt_pk_bf16_f32 v143, v154, v155\n"
      "v_cvt_pk_bf16_f32 v142, v152, v153\n"
      "v_add_u32 v145, 150640, v145\n"
      "ds_write_b64 v125, v[156:157] offset:18944\n"
      "ds_write_b128 v124, v[140:143] offset:18944\n"
      "s_waitcnt lgkmcnt(0)\n"
      "s_barrier\n"
      "s_sub_u32 s28, s27, 3\n"
      "ds_read_b128 v[140:143], v123 offset:18432\n"
      "ds_read_b128 v[152:155], v123 offset:21504\n"
      "ds_read_b128 v[156:159], v128 offset:1024\n"
      "ds_read_b128 v[236:239], v119 offset:24576\n"
      "ds_read_b128 v[212:215], v123 offset:18688\n"
      "s_cselect_b64 s[54:55], s[12:13], s[22:23]\n"
      "s_max_i32 m0, s28, 0\n"
      "ds_read_b128 v[136:139], v119 offset:18432\n"
      "ds_read_b128 v[220:223], v123 offset:27648\n"
      "s_movrels_b32 s9, s80\n"
      "ds_read_b128 v[248:251], v123 offset:24576\n"
      "s_lshr_b32 s8, s9, 8\n"
      "s_and_b32 s9, s9, 0xff\n"
      "s_mul_i32 s28, s8, 1373568\n"
      "ds_read_b128 v[148:151], v120 offset:18432\n"
      "s_mul_i32 s29, s9, 2688\n"
      "s_add_u32 s28, s28, s29\n"
      "s_cmp_eq_u32 s8, 72\n"
      "ds_read_b128 v[160:163], v119 offset:30720\n"
      "s_cselect_b32 s8, 0, 100270464\n"
      "ds_read_b128 v[164:167], v121 offset:18432\n"
      "s_cmp_eq_u32 s9, 72\n"
      "s_cselect_b32 s9, 0, 196224\n"
      "s_add_u32 s10, s8, s9\n"
      "s_add_u32 s28, s28, s10\n"
      "s_waitcnt lgkmcnt(9)\n"
      "v_mfma_f32_16x16x32_bf16 v[204:207], a[216:219], v[140:143], 0\n"
      "s_add_u32 s54, s54, s28\n"
      "s_waitcnt lgkmcnt(8)\n"
      "v_add_f32 v158, |v158|, |v159|\n"
      "v_mfma_f32_16x16x32_bf16 v[204:207], a[228:231], v[152:155], v[204:207]\n"
      "ds_read_b128 v[216:219], v123 offset:30720\n"
      "v_add_f32 v156, |v156|, |v157|\n"
      "v_mfma_f32_16x16x32_bf16 v[232:235], a[220:223], v[140:143], 0\n"
      "ds_read_b128 v[208:211], v123 offset:33792\n"
      "v_add_f32 v156, v156, v158\n"
      "v_mfma_f32_16x16x32_bf16 v[232:235], a[232:235], v[152:155], v[232:235]\n"
      "s_addc_u32 s55, s55, 0\n"
      "v_rsq_f32 v156, v156\n"
      "v_mfma_f32_16x16x32_bf16 v[224:227], a[224:227], v[140:143], 0\n"
      "s_waitcnt lgkmcnt(5)\n"
      "v_mul_f32 v156, s41, v156\n"
      "v_mov_b32_e64 v146, v156\n"
      "v_mfma_f32_16x16x32_bf16 v[224:227], a[236:239], v[152:155], v[224:227]\n"
      "ds_read_b128 v[140:143], v123 offset:21760\n"
      "v_permlane32_swap_b32 v156, v146\n"
      "v_mov_b32 v147, v156\n"
      "v_mfma_f32_16x16x32_bf16 v[232:235], a[244:247], v[248:251], v[232:235]\n"
      "s_cmp_lg_u32 s10, s11\n"
      "v_permlane16_swap_b32 v156, v147\n"
      "v_mfma_f32_16x16x32_bf16 v[204:207], a[240:243], v[248:251], v[204:207]\n"
      "s_cbranch_scc1 L_edge_42_%=\n"
      "L_eb_43_%=:\n"
      "v_mul_f32 v171, v156, v171\n"
      "v_mfma_f32_16x16x32_bf16 v[204:207], a[252:255], v[220:223], v[204:207]\n"
      "global_store_dwordx4 v116, v[136:139], s[54:55] sc1\n"
      "v_mfma_f32_16x16x32_bf16 v[232:235], v[0:3], v[220:223], v[232:235]\n"
      "v_mul_f32 v169, v156, v169\n"
      "v_mul_f32 v168, v156, v168\n"
      "v_mfma_f32_16x16x32_bf16 v[224:227], a[248:251], v[248:251], v[224:227]\n"
      "ds_read_b128 v[248:251], v123 offset:30976\n"
      "v_mul_f32 v175, v156, v175\n"
      "v_mfma_f32_16x16x32_bf16 v[224:227], v[4:7], v[220:223], v[224:227]\n"
      "ds_read_b128 v[220:223], v123 offset:34048\n"
      "s_waitcnt lgkmcnt(3)\n"
      "v_cvt_pk_bf16_f32 v168, v168, v169\n"
      "v_mfma_f32_16x16x32_bf16 v[204:207], v[8:11], v[216:219], v[204:207]\n"
      "ds_read_b128 v[152:155], v123 offset:24832\n"
      "v_mul_f32_e64 v170, v156, v170\n"
      "v_mfma_f32_16x16x32_bf16 v[204:207], v[20:23], v[208:211], v[204:207]\n"
      "ds_read_b128 v[228:231], v123 offset:27904\n"
      "v_cvt_pk_bf16_f32 v169, v170, v171\n"
      "v_mfma_f32_16x16x32_bf16 v[232:235], v[12:15], v[216:219], v[232:235]\n"
      "v_mul_f32 v174, v156, v174\n"
      "v_mul_f32 v173, v156, v173\n"
      "v_mfma_f32_16x16x32_bf16 v[232:235], v[24:27], v[208:211], v[232:235]\n"
      "global_store_dwordx4 v116, v[236:239], s[54:55] offset:128 sc1\n"
      "v_mfma_f32_16x16x32_bf16 v[224:227], v[16:19], v[216:219], v[224:227]\n"
      "s_waitcnt lgkmcnt(4)\n"
      "v_mul_f32 v172, v156, v172\n"
      "v_mul_f32_e64 v176, v156, v176\n"
      "v_mfma_f32_16x16x32_bf16 v[224:227], v[28:31], v[208:211], v[224:227]\n"
      "v_cvt_pk_bf16_f32 v171, v174, v175\n"
      "v_cvt_pk_bf16_f32 v170, v172, v173\n"
      "v_mfma_f32_16x16x32_bf16 v[208:211], a[216:219], v[212:215], 0\n"
      "ds_read_b128 v[236:239], v123 offset:18944\n"
      "v_mul_f32_e64 v177, v156, v177\n"
      "v_mfma_f32_16x16x32_bf16 v[208:211], a[228:231], v[140:143], v[208:211]\n"
      "ds_write_b128 v126, v[168:171]\n"
      "v_mfma_f32_16x16x32_bf16 v[168:171], a[220:223], v[212:215], 0\n"
      "ds_read_b128 v[172:175], v123 offset:22016\n"
      "v_cvt_pk_bf16_f32 v176, v176, v177\n"
      "v_mfma_f32_16x16x32_bf16 v[168:171], a[232:235], v[140:143], v[168:171]\n"
      "s_waitcnt lgkmcnt(3)\n"
      "v_mul_f32 v178, v156, v178\n"
      "v_mul_f32_e64 v179, v156, v179\n"
      "v_mfma_f32_16x16x32_bf16 v[216:219], a[224:227], v[212:215], 0\n"
      "v_cvt_pk_bf16_f32 v177, v178, v179\n"
      "v_mul_f32_e64 v180, v147, v180\n"
      "v_mfma_f32_16x16x32_bf16 v[216:219], a[236:239], v[140:143], v[216:219]\n"
      "ds_write_b64 v127, v[176:177]\n"
      "v_mfma_f32_16x16x32_bf16 v[208:211], a[240:243], v[152:155], v[208:211]\n"
      "ds_read_b128 v[140:143], v123 offset:25088\n"
      "v_mul_f32_e64 v181, v147, v181\n"
      "v_mfma_f32_16x16x32_bf16 v[208:211], a[252:255], v[228:231], v[208:211]\n"
      "ds_read_b128 v[176:179], v123 offset:28160\n"
      "v_cvt_pk_bf16_f32 v180, v180, v181\n"
      "v_mfma_f32_16x16x32_bf16 v[168:171], a[244:247], v[152:155], v[168:171]\n"
      "s_lshl_b32 s29, s24, 8\n"
      "v_mul_f32 v182, v147, v182\n"
      "v_mul_f32 v183, v147, v183\n"
      "v_mfma_f32_16x16x32_bf16 v[168:171], v[0:3], v[228:231], v[168:171]\n"
      "v_cvt_pk_bf16_f32 v181, v182, v183\n"
      "v_mul_f32 v184, v147, v184\n"
      "v_mfma_f32_16x16x32_bf16 v[216:219], a[248:251], v[152:155], v[216:219]\n"
      "v_mul_f32 v185, v147, v185\n"
      "v_cvt_pk_bf16_f32 v182, v184, v185\n"
      "v_mfma_f32_16x16x32_bf16 v[216:219], v[4:7], v[228:231], v[216:219]\n"
      "v_mul_f32 v187, v147, v187\n"
      "v_mul_f32 v186, v147, v186\n"
      "v_mfma_f32_16x16x32_bf16 v[208:211], v[8:11], v[248:251], v[208:211]\n"
      "v_mul_f32 v188, v147, v188\n"
      "v_cvt_pk_bf16_f32 v183, v186, v187\n"
      "v_mfma_f32_16x16x32_bf16 v[208:211], v[20:23], v[220:223], v[208:211]\n"
      "ds_write_b128 v126, v[180:183] offset:2048\n"
      "v_mfma_f32_16x16x32_bf16 v[168:171], v[12:15], v[248:251], v[168:171]\n"
      "ds_read_b128 v[180:183], v123 offset:31232\n"
      "v_mul_f32_e64 v189, v147, v189\n"
      "v_mfma_f32_16x16x32_bf16 v[168:171], v[24:27], v[220:223], v[168:171]\n"
      "ds_read_b128 v[184:187], v123 offset:34304\n"
      "v_cvt_pk_bf16_f32 v188, v188, v189\n"
      "v_mfma_f32_16x16x32_bf16 v[216:219], v[16:19], v[248:251], v[216:219]\n"
      "s_waitcnt lgkmcnt(3)\n"
      "v_mul_f32 v190, v147, v190\n"
      "v_mul_f32_e64 v191, v147, v191\n"
      "v_mfma_f32_16x16x32_bf16 v[216:219], v[28:31], v[220:223], v[216:219]\n"
      "s_add_u32 s29, s29, 159088\n"
      "v_cvt_pk_bf16_f32 v189, v190, v191\n"
      "v_fma_f32 v147, v204, v204, s39\n"
      "v_mfma_f32_16x16x32_bf16 v[220:223], a[216:219], v[236:239], 0\n"
      "global_store_dwordx4 v116, v[160:163], s[54:55] offset:256 sc1\n"
      "v_mfma_f32_16x16x32_bf16 v[220:223], a[228:231], v[172:175], v[220:223]\n"
      "ds_write_b64 v127, v[188:189] offset:2048\n"
      "v_mfma_f32_16x16x32_bf16 v[188:191], a[220:223], v[236:239], 0\n"
      "ds_read_b128 v[160:163], v145\n"
      "v_fmac_f32 v147, v205, v205\n"
      "v_mfma_f32_16x16x32_bf16 v[188:191], a[232:235], v[172:175], v[188:191]\n"
      "ds_read_b128 v[248:251], v145 offset:1408\n"
      "v_fmac_f32 v147, v206, v206\n"
      "v_mfma_f32_16x16x32_bf16 v[228:231], a[224:227], v[236:239], 0\n"
      "v_fmac_f32 v147, v207, v207\n"
      "v_fmac_f32 v147, v232, v232\n"
      "v_mfma_f32_16x16x32_bf16 v[228:231], a[236:239], v[172:175], v[228:231]\n"
      "s_waitcnt lgkmcnt(3)\n"
      "v_fmac_f32 v147, v233, v233\n"
      "v_fmac_f32_e64 v147, v234, v234\n"
      "v_mfma_f32_16x16x32_bf16 v[220:223], a[240:243], v[140:143], v[220:223]\n"
      "v_fmac_f32 v147, v235, v235\n"
      "v_fmac_f32 v147, v224, v224\n"
      "v_mfma_f32_16x16x32_bf16 v[220:223], a[252:255], v[176:179], v[220:223]\n"
      "v_fmac_f32 v147, v225, v225\n"
      "v_fmac_f32 v147, v226, v226\n"
      "v_mfma_f32_16x16x32_bf16 v[188:191], a[244:247], v[140:143], v[188:191]\n"
      "global_store_dwordx4 v117, v[148:151], s[54:55] sc1\n"
      "v_mfma_f32_16x16x32_bf16 v[188:191], v[0:3], v[176:179], v[188:191]\n"
      "v_fmac_f32 v147, v227, v227\n"
      "v_mul_f32 v192, v146, v192\n"
      "v_mfma_f32_16x16x32_bf16 v[228:231], a[248:251], v[140:143], v[228:231]\n"
      "ds_read_b128 v[140:143], v145 offset:2816\n"
      "v_mul_f32 v193, v146, v193\n"
      "v_mfma_f32_16x16x32_bf16 v[228:231], v[4:7], v[176:179], v[228:231]\n"
      "v_mul_f32 v194, v146, v194\n"
      "v_cvt_pk_bf16_f32 v192, v192, v193\n"
      "v_mfma_f32_16x16x32_bf16 v[220:223], v[8:11], v[180:183], v[220:223]\n"
      "ds_read_b128 v[176:179], v145 offset:4224\n"
      "v_mul_f32_e64 v195, v146, v195\n"
      "v_mfma_f32_16x16x32_bf16 v[220:223], v[20:23], v[184:187], v[220:223]\n"
      "v_cvt_pk_bf16_f32 v193, v194, v195\n"
      "v_mul_f32 v196, v146, v196\n"
      "v_mfma_f32_16x16x32_bf16 v[188:191], v[12:15], v[180:183], v[188:191]\n"
      "v_mul_f32 v197, v146, v197\n"
      "v_cvt_pk_bf16_f32 v194, v196, v197\n"
      "v_mfma_f32_16x16x32_bf16 v[188:191], v[24:27], v[184:187], v[188:191]\n"
      "ds_read_b128 v[148:151], v145 offset:5632\n"
      "v_mul_f32_e64 v198, v146, v198\n"
      "v_mfma_f32_16x16x32_bf16 v[228:231], v[16:19], v[180:183], v[228:231]\n"
      "s_waitcnt lgkmcnt(3)\n"
      "v_mul_f32 v199, v146, v199\n"
      "v_cvt_pk_bf16_f32 v195, v198, v199\n"
      "v_mfma_f32_16x16x32_bf16 v[228:231], v[28:31], v[184:187], v[228:231]\n"
      "ds_write_b128 v126, v[192:195] offset:4096\n"
      "v_mfma_f32_16x16x32_bf16 v[192:195], a[216:219], v[160:163], 0\n"
      "ds_read_b128 v[184:187], v145 offset:7040\n"
      "v_mul_f32_e64 v202, v146, v202\n"
      "v_mfma_f32_16x16x32_bf16 v[192:195], a[228:231], v[248:251], v[192:195]\n"
      "v_mul_f32 v200, v146, v200\n"
      "v_mul_f32 v201, v146, v201\n"
      "v_mfma_f32_16x16x32_bf16 v[180:183], a[220:223], v[160:163], 0\n"
      "v_cvt_pk_bf16_f32 v200, v200, v201\n"
      "v_mul_f32 v203, v146, v203\n"
      "v_mfma_f32_16x16x32_bf16 v[180:183], a[232:235], v[248:251], v[180:183]\n"
      "ds_read_b128 v[196:199], v145 offset:256\n"
      "v_fma_f32 v146, v208, v208, s39\n"
      "v_mfma_f32_16x16x32_bf16 v[172:175], a[224:227], v[160:163], 0\n"
      "s_waitcnt lgkmcnt(4)\n"
      "v_cvt_pk_bf16_f32 v201, v202, v203\n"
      "v_mfma_f32_16x16x32_bf16 v[172:175], a[236:239], v[248:251], v[172:175]\n"
      "ds_write_b64 v127, v[200:201] offset:4096\n"
      "v_mfma_f32_16x16x32_bf16 v[192:195], a[240:243], v[140:143], v[192:195]\n"
      "ds_read_b128 v[248:251], v145 offset:1664\n"
      "v_fmac_f32 v146, v209, v209\n"
      "v_mfma_f32_16x16x32_bf16 v[192:195], a[252:255], v[176:179], v[192:195]\n"
      "v_fmac_f32 v146, v210, v210\n"
      "v_fmac_f32 v146, v211, v211\n"
      "v_mfma_f32_16x16x32_bf16 v[180:183], a[244:247], v[140:143], v[180:183]\n"
      "s_waitcnt lgkmcnt(3)\n"
      "v_fmac_f32 v146, v168, v168\n"
      "v_fmac_f32 v146, v169, v169\n"
      "v_mfma_f32_16x16x32_bf16 v[180:183], v[0:3], v[176:179], v[180:183]\n"
      "ds_read_b128 v[200:203], v145 offset:3072\n"
      "v_fmac_f32 v146, v170, v170\n"
      "v_mfma_f32_16x16x32_bf16 v[172:175], a[248:251], v[140:143], v[172:175]\n"
      "ds_read_b128 v[140:143], v145 offset:4480\n"
      "v_fmac_f32 v146, v171, v171\n"
      "v_mfma_f32_16x16x32_bf16 v[172:175], v[4:7], v[176:179], v[172:175]\n"
      "v_fmac_f32 v146, v216, v216\n"
      "v_fmac_f32 v146, v217, v217\n"
      "v_mfma_f32_16x16x32_bf16 v[192:195], v[8:11], v[148:151], v[192:195]\n"
      "v_fmac_f32 v146, v218, v218\n"
      "v_fmac_f32 v146, v219, v219\n"
      "v_mfma_f32_16x16x32_bf16 v[192:195], v[20:23], v[184:187], v[192:195]\n"
      "ds_read_b128 v[176:179], v145 offset:5888\n"
      "v_lshl_add_u32 v236, v144, 2, s29\n"
      "v_mfma_f32_16x16x32_bf16 v[180:183], v[12:15], v[148:151], v[180:183]\n"
      "v_lshrrev_b32 v160, 3, v144\n"
      "s_waitcnt lgkmcnt(3)\n"
      "v_mfma_f32_16x16x32_bf16 v[180:183], v[24:27], v[184:187], v[180:183]\n"
      "ds_read_b32 v236, v236\n"
      "v_fma_f32 v144, v220, v220, s39\n"
      "v_mfma_f32_16x16x32_bf16 v[172:175], v[16:19], v[148:151], v[172:175]\n"
      "ds_read_b128 v[148:151], v145 offset:7296\n"
      "v_fmac_f32 v144, v221, v221\n"
      "v_mfma_f32_16x16x32_bf16 v[172:175], v[28:31], v[184:187], v[172:175]\n"
      "s_lshl_b32 s29, s24, 3\n"
      "v_add_u32 v160, s29, v160\n"
      "v_fmac_f32 v144, v222, v222\n"
      "v_mfma_f32_16x16x32_bf16 v[184:187], a[216:219], v[196:199], 0\n"
      "v_fmac_f32_e64 v144, v223, v223\n"
      "v_cmp_gt_u32 s[48:49], s26, v160\n"
      "v_mfma_f32_16x16x32_bf16 v[184:187], a[228:231], v[248:251], v[184:187]\n"
      "s_mov_b32 exec_hi, 0\n"
      "global_store_dwordx4 v118, v[164:167], s[54:55] sc1\n"
      "s_mov_b32 exec_hi, -1\n"
      "v_mfma_f32_16x16x32_bf16 v[160:163], a[220:223], v[196:199], 0\n"
      "v_fmac_f32 v144, v188, v188\n"
      "s_waitcnt lgkmcnt(3)\n"
      "v_mfma_f32_16x16x32_bf16 v[160:163], a[232:235], v[248:251], v[160:163]\n"
      "v_fmac_f32 v144, v189, v189\n"
      "v_fmac_f32 v144, v190, v190\n"
      "v_mfma_f32_16x16x32_bf16 v[164:167], a[224:227], v[196:199], 0\n"
      "v_fmac_f32 v144, v191, v191\n"
      "v_fmac_f32 v144, v228, v228\n"
      "v_mfma_f32_16x16x32_bf16 v[164:167], a[236:239], v[248:251], v[164:167]\n"
      "v_fmac_f32 v144, v229, v229\n"
      "v_fmac_f32 v144, v230, v230\n"
      "v_mfma_f32_16x16x32_bf16 v[184:187], a[240:243], v[200:203], v[184:187]\n"
      "s_waitcnt lgkmcnt(0)\n"
      "v_fmac_f32 v144, v231, v231\n"
      "v_mfma_f32_16x16x32_bf16 v[184:187], a[252:255], v[140:143], v[184:187]\n"
      "v_permlane16_swap_b32 v147, v146\n"
      "v_mfma_f32_16x16x32_bf16 v[160:163], a[244:247], v[200:203], v[160:163]\n"
      "v_mov_b32 v238, v144\n"
      "v_add_f32 v147, v147, v146\n"
      "v_mfma_f32_16x16x32_bf16 v[160:163], v[0:3], v[140:143], v[160:163]\n"
      "v_permlane16_swap_b32 v144, v238\n"
      "v_mfma_f32_16x16x32_bf16 v[164:167], a[248:251], v[200:203], v[164:167]\n"
      "v_add_f32_e64 v144, v144, v238\n"
      "v_fma_f32 v146, v192, v192, s39\n"
      "v_mfma_f32_16x16x32_bf16 v[164:167], v[4:7], v[140:143], v[164:167]\n"
      "v_permlane32_swap_b32 v147, v144\n"
      "v_mfma_f32_16x16x32_bf16 v[184:187], v[8:11], v[176:179], v[184:187]\n"
      "v_fmac_f32 v146, v193, v193\n"
      "v_add_f32 v147, v147, v144\n"
      "v_mfma_f32_16x16x32_bf16 v[184:187], v[20:23], v[148:151], v[184:187]\n"
      "ds_write_b32 v129, v147\n"
      "v_mfma_f32_16x16x32_bf16 v[160:163], v[12:15], v[176:179], v[160:163]\n"
      "v_fmac_f32 v146, v194, v194\n"
      "v_fmac_f32 v146, v195, v195\n"
      "v_mfma_f32_16x16x32_bf16 v[160:163], v[24:27], v[148:151], v[160:163]\n"
      "v_fmac_f32 v146, v180, v180\n"
      "v_fmac_f32 v146, v181, v181\n"
      "v_mfma_f32_16x16x32_bf16 v[164:167], v[16:19], v[176:179], v[164:167]\n"
      "v_fmac_f32 v146, v182, v182\n"
      "v_fmac_f32 v146, v183, v183\n"
      "v_mfma_f32_16x16x32_bf16 v[164:167], v[28:31], v[148:151], v[164:167]\n"
      "v_fmac_f32 v146, v172, v172\n"
      "v_fmac_f32 v146, v173, v173\n"
      "v_fmac_f32 v146, v174, v174\n"
      "v_fmac_f32_e64 v146, v175, v175\n"
      "v_pk_mul_f32 v[238:239], v[184:185], v[184:185]\n"
      "v_pk_fma_f32 v[238:239], v[186:187], v[186:187], v[238:239]\n"
      "v_pk_fma_f32 v[238:239], v[160:161], v[160:161], v[238:239]\n"
      "v_pk_fma_f32 v[238:239], v[162:163], v[162:163], v[238:239]\n"
      "v_pk_fma_f32 v[238:239], v[164:165], v[164:165], v[238:239]\n"
      "v_pk_fma_f32 v[238:239], v[166:167], v[166:167], v[238:239]\n"
      "v_add_f32 v238, s39, v238\n"
      "v_add_f32 v147, v238, v239\n"
      "v_mov_b32 v144, v147\n"
      "v_mov_b32 v145, v144\n"
      "v_permlane16_swap_b32 v146, v147\n"
      "v_add_f32_e64 v146, v146, v147\n"
      "v_permlane16_swap_b32_e64 v144, v145\n"
      "v_add_f32_e64 v144, v144, v145\n"
      "s_nop 1\n"
      "v_permlane32_swap_b32_e64 v146, v144\n"
      "v_add_f32_e64 v146, v146, v144\n"
      "ds_write_b32 v129, v146 offset:2048\n"
      "s_waitcnt lgkmcnt(0)\n"
      "s_barrier\n"
      "s_add_u32 s27, s27, 1\n"
      "ds_read_b128 v[176:179], v128 offset:2048\n"
      "ds_read_b128 v[148:151], v128\n"
      "s_sub_u32 s28, s27, 3\n"
      "s_cselect_b64 s[54:55], s[12:13], s[22:23]\n"
      "s_max_i32 m0, s28, 0\n"
      "ds_read_b128 v[144:147], v119\n"
      "s_movrels_b32 s9, s80\n"
      "ds_read_b128 v[200:203], v119 offset:12288\n"
      "s_lshr_b32 s8, s9, 8\n"
      "s_and_b32 s9, s9, 0xff\n"
      "s_mul_i32 s28, s8, 1373568\n"
      "s_mul_i32 s29, s9, 2688\n"
      "s_add_u32 s28, s28, s29\n"
      "s_cmp_eq_u32 s8, 72\n"
      "ds_read_b128 v[140:143], v119 offset:6144\n"
      "s_cselect_b32 s8, 0, 100270464\n"
      "s_cmp_eq_u32 s9, 72\n"
      "s_cselect_b32 s9, 0, 196224\n"
      "s_add_u32 s10, s8, s9\n"
      "s_add_u32 s28, s28, s10\n"
      "s_add_u32 s54, s54, s28\n"
      "s_addc_u32 s55, s55, 0\n"
      "ds_read_b128 v[248:251], v120\n"
      "s_cmp_lg_u32 s10, s11\n"
      "ds_read_b128 v[196:199], v121\n"
      "s_cbranch_scc1 L_edge_44_%=\n"
      "L_eb_45_%=:\n"
      "s_waitcnt lgkmcnt(5)\n"
      "v_add_f32 v150, |v150|, |v151|\n"
      "v_add_f32 v148, |v148|, |v149|\n"
      "v_add_f32 v148, v148, v150\n"
      "v_rsq_f32 v148, v148\n"
      "v_add_f32 v178, |v178|, |v179|\n"
      "v_mul_f32 v148, s41, v148\n"
      "v_mov_b32 v150, v148\n"
      "v_add_f32 v176, |v176|, |v177|\n"
      "v_add_f32 v176, v176, v178\n"
      "v_permlane32_swap_b32 v148, v150\n"
      "v_mov_b32 v179, v148\n"
      "v_rsq_f32 v176, v176\n"
      "v_pk_mul_f32 v[228:229], v[228:229], v[150:151] op_sel_hi:[1,0]\n"
      "v_permlane16_swap_b32_e64 v148, v179\n"
      "v_pk_mul_f32 v[218:219], v[218:219], v[178:179] op_sel:[0,1]\n"
      "v_pk_mul_f32 v[208:209], v[208:209], v[178:179] op_sel:[0,1]\n"
      "v_pk_mul_f32 v[204:205], v[204:205], v[148:149] op_sel_hi:[1,0]\n"
      "v_pk_mul_f32 v[168:169], v[168:169], v[178:179] op_sel:[0,1]\n"
      "v_pk_mul_f32 v[188:189], v[188:189], v[150:151] op_sel_hi:[1,0]\n"
      "v_pk_mul_f32 v[230:231], v[230:231], v[150:151] op_sel_hi:[1,0]\n"
      "v_pk_mul_f32 v[216:217], v[216:217], v[178:179] op_sel:[0,1]\n"
      "v_pk_mul_f32 v[232:233], v[232:233], v[148:149] op_sel_hi:[1,0]\n"
      "v_cvt_pk_bf16_f32 v204, v204, v205\n"
      "v_pk_mul_f32 v[210:211], v[210:211], v[178:179] op_sel:[0,1]\n"
      "v_mul_f32 v176, s41, v176\n"
      "v_pk_mul_f32 v[206:207], v[206:207], v[148:149] op_sel_hi:[1,0]\n"
      "v_pk_mul_f32 v[170:171], v[170:171], v[178:179] op_sel:[0,1]\n"
      "v_mov_b32 v179, v176\n"
      "v_pk_mul_f32 v[222:223], v[222:223], v[150:151] op_sel_hi:[1,0]\n"
      "s_waitcnt lgkmcnt(0)\n"
      "v_cvt_pk_bf16_f32 v216, v216, v217\n"
      "v_pk_mul_f32 v[224:225], v[224:225], v[148:149] op_sel_hi:[1,0]\n"
      "v_permlane32_swap_b32 v176, v179\n"
      "v_mov_b32_e64 v179, v176\n"
      "v_pk_mul_f32 v[226:227], v[226:227], v[148:149] op_sel_hi:[1,0]\n"
      "v_pk_mul_f32 v[220:221], v[220:221], v[150:151] op_sel_hi:[1,0]\n"
      "v_permlane16_swap_b32_e64 v176, v179\n"
      "v_pk_mul_f32 v[234:235], v[234:235], v[148:149] op_sel_hi:[1,0]\n"
      "v_subrev_u32 v177, 73728, v127\n"
      "v_pk_mul_f32 v[172:173], v[172:173], v[176:177] op_sel_hi:[1,0]\n"
      "v_cvt_pk_bf16_f32 v208, v208, v209\n"
      "v_cvt_pk_bf16_f32 v209, v210, v211\n"
      "v_pk_mul_f32 v[164:165], v[164:165], v[178:179] op_sel:[0,1]\n"
      "v_cvt_pk_bf16_f32 v220, v220, v221\n"
      "v_pk_mul_f32 v[180:181], v[180:181], v[176:177] op_sel_hi:[1,0]\n"
      "v_cvt_pk_bf16_f32 v172, v172, v173\n"
      "v_pk_mul_f32 v[184:185], v[184:185], v[178:179] op_sel:[0,1]\n"
      "v_cvt_pk_bf16_f32 v184, v184, v185\n"
      "v_pk_mul_f32 v[182:183], v[182:183], v[176:177] op_sel_hi:[1,0]\n"
      "v_cvt_pk_bf16_f32 v221, v222, v223\n"
      "v_pk_mul_f32 v[192:193], v[192:193], v[176:177] op_sel_hi:[1,0]\n"
      "v_cvt_pk_bf16_f32 v224, v224, v225\n"
      "v_cvt_pk_bf16_f32 v225, v226, v227\n"
      "v_pk_mul_f32 v[174:175], v[174:175], v[176:177] op_sel_hi:[1,0]\n"
      "v_pk_mul_f32 v[194:195], v[194:195], v[176:177] op_sel_hi:[1,0]\n"
      "global_store_dwordx4 v116, v[144:147], s[54:55] sc1\n"
      "ds_write_b64 v127, v[224:225] offset:18432\n"
      "v_cvt_pk_bf16_f32 v164, v164, v165\n"
      "v_cvt_pk_bf16_f32 v217, v218, v219\n"
      "v_pk_mul_f32 v[160:161], v[160:161], v[178:179] op_sel:[0,1]\n"
      "v_cvt_pk_bf16_f32 v222, v188, v189\n"
      "v_cvt_pk_bf16_f32 v228, v228, v229\n"
      "v_pk_mul_f32 v[162:163], v[162:163], v[178:179] op_sel:[0,1]\n"
      "v_cvt_pk_bf16_f32 v229, v230, v231\n"
      "v_cvt_pk_bf16_f32 v173, v174, v175\n"
      "v_pk_mul_f32 v[190:191], v[190:191], v[150:151] op_sel_hi:[1,0]\n"
      "v_cvt_pk_bf16_f32 v192, v192, v193\n"
      "v_cvt_pk_bf16_f32 v193, v194, v195\n"
      "v_cvt_pk_bf16_f32 v205, v206, v207\n"
      "v_subrev_u32 v178, 73728, v126\n"
      "v_pk_mul_f32 v[186:187], v[186:187], v[178:179] op_sel:[0,1]\n"
      "v_pk_mul_f32 v[166:167], v[166:167], v[178:179] op_sel:[0,1]\n"
      "v_cvt_pk_bf16_f32 v206, v232, v233\n"
      "v_cvt_pk_bf16_f32 v211, v170, v171\n"
      "v_cvt_pk_bf16_f32 v223, v190, v191\n"
      "v_cvt_pk_bf16_f32 v194, v180, v181\n"
      "v_cvt_pk_bf16_f32 v210, v168, v169\n"
      "v_cvt_pk_bf16_f32 v195, v182, v183\n"
      "v_cvt_pk_bf16_f32 v207, v234, v235\n"
      "global_store_dwordx4 v116, v[140:143], s[54:55] offset:128 sc1\n"
      "ds_write_b64 v127, v[216:217] offset:20480\n"
      "ds_write_b128 v178, v[192:195] offset:18432\n"
      "ds_write_b128 v126, v[208:211] offset:20480\n"
      "ds_write_b128 v126, v[204:207] offset:18432\n"
      "ds_write_b128 v126, v[220:223] offset:22528\n"
      "global_store_dwordx4 v116, v[200:203], s[54:55] offset:256 sc1\n"
      "global_store_dwordx4 v117, v[248:251], s[54:55] sc1\n"
      "v_cvt_pk_bf16_f32 v185, v186, v187\n"
      "v_cvt_pk_bf16_f32 v165, v166, v167\n"
      "v_cvt_pk_bf16_f32 v186, v160, v161\n"
      "v_cvt_pk_bf16_f32 v187, v162, v163\n"
      "ds_write_b64 v177, v[172:173] offset:18432\n"
      "ds_write_b128 v178, v[184:187] offset:20480\n"
      "ds_write_b64 v127, v[228:229] offset:22528\n"
      "ds_write_b64 v177, v[164:165] offset:20480\n"
      "s_mov_b32 exec_hi, 0\n"
      "global_store_dwordx4 v118, v[196:199], s[54:55] sc1\n"
      "s_mov_b32 exec_hi, -1\n"
      "s_waitcnt lgkmcnt(0)\n"
      "s_barrier\n"
      "ds_read_b128 v[164:167], v119 offset:24576\n"
      "ds_read_b128 v[196:199], v119 offset:18432\n"
      "s_add_u32 s27, s27, 1\n"
      "v_subrev_u32 v238, 73728, v119\n"
      "s_sub_u32 s28, s27, 3\n"
      "ds_read_b128 v[172:175], v238 offset:18432\n"
      "ds_read_b128 v[248:251], v121 offset:18432\n"
      "ds_read_b128 v[192:195], v238 offset:24576\n"
      "s_cselect_b64 s[54:55], s[12:13], s[22:23]\n"
      "ds_read_b128 v[180:183], v238 offset:30720\n"
      "ds_read_b128 v[160:163], v120 offset:18432\n"
      "s_max_i32 m0, s28, 0\n"
      "ds_read_b128 v[184:187], v119 offset:30720\n"
      "s_movrels_b32 s9, s80\n"
      "s_lshr_b32 s8, s9, 8\n"
      "s_and_b32 s9, s9, 0xff\n"
      "s_mul_i32 s28, s8, 1373568\n"
      "s_mul_i32 s29, s9, 2688\n"
      "s_add_u32 s28, s28, s29\n"
      "s_cmp_eq_u32 s8, 72\n"
      "s_cselect_b32 s8, 0, 100270464\n"
      "s_cmp_eq_u32 s9, 72\n"
      "s_cselect_b32 s9, 0, 196224\n"
      "s_add_u32 s10, s8, s9\n"
      "s_add_u32 s28, s28, s10\n"
      "s_add_u32 s54, s54, s28\n"
      "s_addc_u32 s55, s55, 0\n"
      "s_cmp_lg_u32 s10, s11\n"
      "s_waitcnt lgkmcnt(6)\n"
      "s_cbranch_scc1 L_edge_46_%=\n"
      "L_eb_47_%=:\n"
      "global_store_dwordx4 v116, v[196:199], s[54:55] sc1\n"
      "global_store_dwordx4 v116, v[164:167], s[54:55] offset:128 sc1\n"
      "s_waitcnt lgkmcnt(2)\n"
      "s_waitcnt lgkmcnt(1)\n"
      "s_waitcnt lgkmcnt(0)\n"
      "global_store_dwordx4 v116, v[184:187], s[54:55] offset:256 sc1\n"
      "global_store_dwordx4 v117, v[160:163], s[54:55] sc1\n"
      "s_mov_b32 exec_hi, 0\n"
      "global_store_dwordx4 v118, v[248:251], s[54:55] sc1\n"
      "s_mov_b32 exec_hi, -1\n"
      "s_mov_b64 exec, s[48:49]\n"
      "s_cbranch_execz L_np_48_%=\n"
      "global_store_dwordx4 v236, v[172:175], s[22:23] sc1\n"
      "global_store_dwordx4 v236, v[192:195], s[22:23] offset:128 sc1\n"
      "global_store_dwordx4 v236, v[180:183], s[22:23] offset:256 sc1\n"
      "L_np_48_%=:\n"
      "s_mov_b64 exec, -1\n"
      "s_nop 0\n"
      "s_endpgm\n"
      "L_gen_8_%=:\n"
      "s_waitcnt lgkmcnt(0)\n"
      "s_nop 7\n"
      "s_nop 7\n"
      "s_cmp_eq_u64 s[48:49], 0\n"
      "s_cbranch_scc1 L_go_49_%=\n"
      "s_mov_b32 m0, s27\n"
      "s_nop 0\n"
      "s_movrels_b32 s9, s80\n"
      "s_lshr_b32 s8, s9, 8\n"
      "s_and_b32 s9, s9, 0xff\n"
      "s_mul_i32 s10, s8, 73\n"
      "s_add_u32 s10, s10, s9\n"
      "s_mul_i32 s30, s10, 4802\n"
      "s_add_u32 s30, s30, s18\n"
      "s_and_b32 s29, s30, 15\n"
      "s_add_u32 s30, s29, 110592\n"
      "s_lshl_b64 s[34:35], s[48:49], 4\n"
      "s_lshr_b64 s[34:35], s[34:35], 5\n"
      "s_cbranch_scc1 L_gen_9_%=\n"
      "v_mbcnt_lo_u32_b32 v156, -1, 0\n"
      "v_mbcnt_hi_u32_b32 v156, -1, v156\n"
      "v_lshlrev_b32 v157, 1, v156\n"
      "v_add_u32 v158, 4802, v157\n"
      "v_cmp_gt_u32 vcc, s29, v157\n"
      "s_nop 1\n"
      "v_cndmask_b32 v157, v158, v157, vcc\n"
      "v_add_u32 v157, 110592, v157\n"
      "v_mov_b32 v158, 0\n"
      "s_mov_b64 exec, 0x7f\n"
      "ds_write_b16 v157, v158\n"
      "s_mov_b64 exec, -1\n"
      "v_lshlrev_b32 v157, 4, v156\n"
      "v_add_u32 v157, 110592, v157\n"
      "v_min_u32 v156, 111296, v157\n"
      "ds_read_b128 v[160:163], v157\n"
      "ds_read_b128 v[140:143], v157 offset:1024\n"
      "ds_read_b128 v[168:171], v157 offset:2048\n"
      "ds_read_b128 v[184:187], v157 offset:3072\n"
      "ds_read_b128 v[188:191], v156 offset:4096\n"
      "s_waitcnt lgkmcnt(4)\n"
      "v_or3_b32 v160, v160, v161, v162\n"
      "s_waitcnt lgkmcnt(3)\n"
      "v_or3_b32 v160, v160, v163, v140\n"
      "v_or3_b32 v160, v160, v141, v142\n"
      "s_waitcnt lgkmcnt(2)\n"
      "v_or3_b32 v160, v160, v143, v168\n"
      "v_or3_b32 v160, v160, v169, v170\n"
      "s_waitcnt lgkmcnt(1)\n"
      "v_or3_b32 v160, v160, v171, v184\n"
      "v_or3_b32 v160, v160, v185, v186\n"
      "s_waitcnt lgkmcnt(0)\n"
      "v_or3_b32 v160, v160, v187, v188\n"
      "v_or3_b32 v160, v160, v189, v190\n"
      "v_bitop3_b32 v160, v160, v191, s40 bitop3:0xa8\n"
      "v_cmp_ne_u32 s[48:49], 0, v160\n"
      "s_cmp_lg_u64 s[48:49], 0\n"
      "s_cbranch_scc1 L_gen_9_%=\n"
      "s_cmp_eq_u64 s[50:51], 0\n"
      "s_cbranch_scc1 L_fast_11_%=\n"
      "L_go_49_%=:\n"
      "v_mfma_f32_16x16x32_bf16 v[160:163], v[176:179], v[148:151], v[90:93]\n"
      "ds_read_b128 v[156:159], v128 offset:3072\n"
      "v_max3_f32 v143, v164, v165, v166\n"
      "v_mfma_f32_16x16x32_bf16 v[160:163], v[34:37], v[38:41], v[160:163]\n"
      "v_max3_f32 v141, v167, v192, v193\n"
      "v_max3_f32 v143, v143, v194, v195\n"
      "v_mfma_f32_16x16x32_bf16 v[188:191], v[180:183], v[148:151], v[94:97]\n"
      "s_nop 0\n"
      "v_max3_f32 v141, v141, v196, v197\n"
      "v_max3_f32 v143, v143, v198, v199\n"
      "v_mfma_f32_16x16x32_bf16 v[188:191], v[40:43], v[38:41], v[188:191]\n"
      "v_max3_f32 v143, v143, v141, v172\n"
      "v_mov_b32 v141, v143\n"
      "v_mfma_f32_16x16x32_bf16 v[184:187], v[144:147], v[148:151], v[72:75]\n"
      "s_nop 0\n"
      "v_permlane16_swap_b32 v143, v141\n"
      "v_mfma_f32_16x16x32_bf16 v[184:187], v[240:243], v[180:183], v[184:187]\n"
      "v_max_f32 v143, v143, v141\n"
      "v_mov_b32 v141, v143\n"
      "v_mfma_f32_16x16x32_bf16 v[184:187], v[212:215], v[38:41], v[184:187]\n"
      "s_nop 0\n"
      "v_permlane32_swap_b32 v143, v141\n"
      "v_mfma_f32_16x16x32_bf16 v[168:171], v[152:155], v[148:151], v[98:101]\n"
      "v_max_f32 v143, v143, v141\n"
      "v_sub_f32 v164, v164, v143\n"
      "v_mfma_f32_16x16x32_bf16 v[168:171], v[46:49], v[38:41], v[168:171]\n"
      "v_sub_f32 v165, v165, v143\n"
      "v_sub_f32 v166, v166, v143\n"
      "v_mfma_f32_16x16x32_bf16 v[148:151], v[176:179], v[136:139], v[102:105]\n"
      "v_sub_f32 v167, v167, v143\n"
      "v_sub_f32 v192, v192, v143\n"
      "v_mfma_f32_16x16x32_bf16 v[148:151], v[34:37], v[44:47], v[148:151]\n"
      "v_sub_f32 v193, v193, v143\n"
      "v_sub_f32 v194, v194, v143\n"
      "v_mfma_f32_16x16x32_bf16 v[176:179], v[180:183], v[136:139], v[106:109]\n"
      "s_waitcnt lgkmcnt(0)\n"
      "v_sub_f32 v195, v195, v143\n"
      "v_sub_f32 v196, v196, v143\n"
      "v_mfma_f32_16x16x32_bf16 v[176:179], v[40:43], v[44:47], v[176:179]\n"
      "v_sub_f32 v197, v197, v143\n"
      "v_sub_f32 v198, v198, v143\n"
      "v_mfma_f32_16x16x32_bf16 v[180:183], v[144:147], v[136:139], v[74:77]\n"
      "v_sub_f32 v199, v199, v143\n"
      "v_sub_f32 v172, v172, v143\n"
      "v_mfma_f32_16x16x32_bf16 v[180:183], v[240:243], v[152:155], v[180:183]\n"
      "v_exp_f32 v164, v164\n"
      "v_mfma_f32_16x16x32_bf16 v[180:183], v[212:215], v[44:47], v[180:183]\n"
      "v_exp_f32 v165, v165\n"
      "v_mfma_f32_16x16x32_bf16 v[140:143], v[152:155], v[136:139], v[110:113]\n"
      "v_exp_f32 v166, v166\n"
      "v_mfma_f32_16x16x32_bf16 v[140:143], v[46:49], v[44:47], v[140:143]\n"
      "v_cvt_pk_bf16_f32 v164, v164, v165\n"
      "v_exp_f32 v167, v167\n"
      "v_mfma_f32_16x16x32_bf16 v[136:139], v[240:243], v[144:147], v[76:79]\n"
      "v_exp_f32 v192, v192\n"
      "v_cvt_pk_bf16_f32 v165, v166, v167\n"
      "v_exp_f32 v193, v193\n"
      "v_exp_f32_e64 v194, v194\n"
      "v_cvt_pk_bf16_f32 v166, v192, v193\n"
      "v_exp_f32 v195, v195\n"
      "v_mfma_f32_16x16x16_bf16 v[136:139], v[214:215], v[212:213], v[136:139]\n"
      "v_exp_f32 v196, v196\n"
      "v_cvt_pk_bf16_f32 v167, v194, v195\n"
      "v_exp_f32 v197, v197\n"
      "v_exp_f32 v198, v198\n"
      "v_exp_f32 v199, v199\n"
      "v_exp_f32 v172, v172\n"
      "v_cvt_pk_bf16_f32 v62, v196, v197\n"
      "v_cvt_pk_bf16_f32 v63, v198, v199\n"
      "v_cvt_pk_bf16_f32 v64, v172, 0\n"
      "v_bitop3_b32 v156, v156, v157, s4 bitop3:0x7e\n"
      "v_bitop3_b32 v158, v158, v159, s4 bitop3:0x7e\n"
      "v_mfma_f32_16x16x32_bf16 v[152:155], v[66:69], v[164:167], 0\n"
      "v_or_b32 v156, v156, v158\n"
      "v_cmp_ne_u32 vcc, 0, v156\n"
      "v_mfma_f32_16x16x32_bf16 v[152:155], v[66:69], v[62:65], v[152:155]\n"
      "s_mov_b64 s[48:49], vcc\n"
      "v_max3_f32 v136, v160, v161, v162\n"
      "v_max3_f32 v138, v163, v188, v189\n"
      "v_mfma_f32_16x16x32_bf16 v[144:147], v[220:223], v[164:167], 0\n"
      "s_cbranch_vccnz L_flg_50_%=\n"
      "L_fb_51_%=:\n"
      "v_mfma_f32_16x16x32_bf16 v[144:147], v[50:53], v[62:65], v[144:147]\n"
      "v_max3_f32 v136, v136, v190, v191\n"
      "v_max3_f32 v138, v138, v168, v169\n"
      "v_mfma_f32_16x16x32_bf16 v[156:159], v[208:211], v[164:167], 0\n"
      "v_max3_f32 v136, v136, v170, v171\n"
      "v_max3_f32 v136, v136, v138, v184\n"
      "v_mfma_f32_16x16x32_bf16 v[156:159], v[54:57], v[62:65], v[156:159]\n"
      "v_mov_b32 v138, v136\n"
      "v_mfma_f32_16x16x32_bf16 v[196:199], v[216:219], v[164:167], 0\n"
      "s_nop 0\n"
      "v_permlane16_swap_b32_e64 v136, v138\n"
      "v_mfma_f32_16x16x32_bf16 v[196:199], v[58:61], v[62:65], v[196:199]\n"
      "v_max_f32 v136, v136, v138\n"
      "v_mov_b32 v138, v136\n"
      "v_rcp_f32 v152, v152\n"
      "s_nop 0\n"
      "v_permlane32_swap_b32 v136, v138\n"
      "v_max_f32 v136, v136, v138\n"
      "v_sub_f32 v160, v160, v136\n"
      "v_sub_f32 v161, v161, v136\n"
      "v_sub_f32 v162, v162, v136\n"
      "v_sub_f32 v163, v163, v136\n"
      "v_sub_f32 v188, v188, v136\n"
      "v_sub_f32 v189, v189, v136\n"
      "v_sub_f32 v190, v190, v136\n"
      "v_sub_f32 v191, v191, v136\n"
      "v_sub_f32 v168, v168, v136\n"
      "v_sub_f32 v169, v169, v136\n"
      "v_sub_f32 v170, v170, v136\n"
      "v_sub_f32 v171, v171, v136\n"
      "v_sub_f32 v184, v184, v136\n"
      "v_exp_f32 v160, v160\n"
      "v_exp_f32 v161, v161\n"
      "v_exp_f32 v162, v162\n"
      "v_cvt_pk_bf16_f32 v160, v160, v161\n"
      "v_exp_f32 v163, v163\n"
      "v_exp_f32 v188, v188\n"
      "v_cvt_pk_bf16_f32 v161, v162, v163\n"
      "v_exp_f32 v189, v189\n"
      "v_exp_f32 v190, v190\n"
      "v_cvt_pk_bf16_f32 v162, v188, v189\n"
      "v_exp_f32 v191, v191\n"
      "v_exp_f32 v168, v168\n"
      "v_cvt_pk_bf16_f32 v163, v190, v191\n"
      "v_exp_f32 v169, v169\n"
      "v_exp_f32 v170, v170\n"
      "v_exp_f32 v171, v171\n"
      "v_exp_f32 v184, v184\n"
      "v_cvt_pk_bf16_f32 v62, v168, v169\n"
      "v_cvt_pk_bf16_f32 v63, v170, v171\n"
      "v_cvt_pk_bf16_f32 v64, v184, 0\n"
      "v_mul_f32 v144, v152, v144\n"
      "v_mul_f32 v145, v152, v145\n"
      "v_mfma_f32_16x16x32_bf16 v[168:171], v[66:69], v[160:163], 0\n"
      "s_add_u32 s28, s27, 2\n"
      "v_cvt_pk_bf16_f32 v144, v144, v145\n"
      "v_mul_f32 v146, v152, v146\n"
      "v_mfma_f32_16x16x32_bf16 v[168:171], v[66:69], v[62:65], v[168:171]\n"
      "s_min_u32 m0, s28, s3\n"
      "v_mul_f32 v147, v152, v147\n"
      "v_cvt_pk_bf16_f32 v145, v146, v147\n"
      "v_mfma_f32_16x16x32_bf16 v[188:191], v[220:223], v[160:163], 0\n"
      "s_movrels_b32 s9, s80\n"
      "v_mul_f32 v156, v152, v156\n"
      "v_mul_f32 v157, v152, v157\n"
      "v_mfma_f32_16x16x32_bf16 v[188:191], v[50:53], v[62:65], v[188:191]\n"
      "s_lshr_b32 s8, s9, 8\n"
      "v_cvt_pk_bf16_f32 v146, v156, v157\n"
      "v_mul_f32 v158, v152, v158\n"
      "v_mfma_f32_16x16x32_bf16 v[212:215], v[208:211], v[160:163], 0\n"
      "s_and_b32 s9, s9, 0xff\n"
      "v_mul_f32 v159, v152, v159\n"
      "v_cvt_pk_bf16_f32 v147, v158, v159\n"
      "v_mfma_f32_16x16x32_bf16 v[212:215], v[54:57], v[62:65], v[212:215]\n"
      "ds_write_b128 v124, v[144:147]\n"
      "v_mfma_f32_16x16x32_bf16 v[144:147], v[216:219], v[160:163], 0\n"
      "s_mul_i32 s10, s8, 73\n"
      "v_mul_f32 v196, v152, v196\n"
      "v_mul_f32 v197, v152, v197\n"
      "v_mfma_f32_16x16x32_bf16 v[144:147], v[58:61], v[62:65], v[144:147]\n"
      "s_add_u32 s10, s10, s9\n"
      "v_cvt_pk_bf16_f32 v196, v196, v197\n"
      "v_mul_f32 v198, v152, v198\n"
      "v_mul_f32_e64 v199, v152, v199\n"
      "v_cvt_pk_bf16_f32 v197, v198, v199\n"
      "v_max3_f32 v136, v148, v149, v150\n"
      "v_max3_f32 v138, v151, v176, v177\n"
      "v_max3_f32 v136, v136, v178, v179\n"
      "v_max3_f32 v138, v138, v140, v141\n"
      "v_max3_f32 v136, v136, v142, v143\n"
      "v_max3_f32 v136, v136, v138, v180\n"
      "v_mov_b32 v138, v136\n"
      "s_mul_i32 s28, s10, 4802\n"
      "s_nop 0\n"
      "v_permlane16_swap_b32 v136, v138\n"
      "v_max_f32 v136, v136, v138\n"
      "v_mov_b32 v138, v136\n"
      "s_add_u32 s56, s18, s28\n"
      "s_nop 0\n"
      "v_permlane32_swap_b32 v136, v138\n"
      "v_max_f32 v136, v136, v138\n"
      "v_sub_f32 v148, v148, v136\n"
      "v_sub_f32 v149, v149, v136\n"
      "v_sub_f32 v150, v150, v136\n"
      "v_sub_f32 v151, v151, v136\n"
      "v_sub_f32 v176, v176, v136\n"
      "v_sub_f32 v177, v177, v136\n"
      "v_sub_f32 v178, v178, v136\n"
      "v_sub_f32 v179, v179, v136\n"
      "v_sub_f32 v140, v140, v136\n"
      "v_sub_f32 v141, v141, v136\n"
      "v_sub_f32 v142, v142, v136\n"
      "v_sub_f32 v143, v143, v136\n"
      "v_sub_f32 v180, v180, v136\n"
      "v_exp_f32 v148, v148\n"
      "v_exp_f32 v149, v149\n"
      "v_exp_f32 v150, v150\n"
      "v_cvt_pk_bf16_f32 v148, v148, v149\n"
      "v_exp_f32 v151, v151\n"
      "v_exp_f32_e64 v176, v176\n"
      "v_cvt_pk_bf16_f32 v149, v150, v151\n"
      "s_addc_u32 s57, s19, 0\n"
      "ds_write_b64 v125, v[196:197]\n"
      "v_exp_f32 v177, v177\n"
      "v_exp_f32_e64 v178, v178\n"
      "v_cvt_pk_bf16_f32 v150, v176, v177\n"
      "v_exp_f32 v179, v179\n"
      "v_exp_f32 v140, v140\n"
      "v_cvt_pk_bf16_f32 v151, v178, v179\n"
      "v_exp_f32 v141, v141\n"
      "v_exp_f32 v142, v142\n"
      "v_exp_f32_e64 v143, v143\n"
      "v_exp_f32_e64 v180, v180\n"
      "v_cvt_pk_bf16_f32 v62, v140, v141\n"
      "v_cvt_pk_bf16_f32 v63, v142, v143\n"
      "v_cvt_pk_bf16_f32 v64, v180, 0\n"
      "v_cndmask_b32 v173, v173, v185, s[42:43]\n"
      "v_cndmask_b32 v173, v173, v181, s[44:45]\n"
      "v_mfma_f32_16x16x32_bf16 v[152:155], v[66:69], v[148:151], 0\n"
      "s_and_b32 s56, s56, -16\n"
      "v_cndmask_b32 v173, v173, v137, s[46:47]\n"
      "s_sub_u32 s58, s56, 110592\n"
      "v_mfma_f32_16x16x32_bf16 v[152:155], v[66:69], v[62:65], v[152:155]\n"
      "s_subb_u32 s59, s57, 0\n"
      "v_max_f32_dpp v137, v173, v173 row_ror:8 row_mask:0xf bank_mask:0xf\n"
      "v_rcp_f32_e64 v168, v168\n"
      "v_mfma_f32_16x16x32_bf16 v[156:159], v[220:223], v[148:151], 0\n"
      "s_sub_u32 s56, s56, 73728\n"
      "v_max_f32_dpp v137, v137, v137 row_ror:4 row_mask:0xf bank_mask:0xf\n"
      "v_mul_f32 v188, v168, v188\n"
      "v_mfma_f32_16x16x32_bf16 v[156:159], v[50:53], v[62:65], v[156:159]\n"
      "s_subb_u32 s57, s57, 0\n"
      "v_max_f32_dpp v137, v137, v137 row_ror:2 row_mask:0xf bank_mask:0xf\n"
      "v_mul_f32_e64 v189, v168, v189\n"
      "v_mfma_f32_16x16x32_bf16 v[160:163], v[208:211], v[148:151], 0\n"
      "s_mul_i32 s28, s8, 1373568\n"
      "v_max_f32_dpp v137, v137, v137 row_ror:1 row_mask:0xf bank_mask:0xf\n"
      "v_cvt_pk_bf16_f32 v188, v188, v189\n"
      "v_mfma_f32_16x16x32_bf16 v[160:163], v[54:57], v[62:65], v[160:163]\n"
      "s_mul_i32 s29, s9, 2688\n"
      "v_max_f32_dpp v137, v137, v137 row_bcast:15 row_mask:0xa bank_mask:0xf\n"
      "v_mul_f32 v190, v168, v190\n"
      "v_mfma_f32_16x16x32_bf16 v[164:167], v[216:219], v[148:151], 0\n"
      "s_add_u32 s28, s28, s29\n"
      "v_max_f32_dpp v137, v137, v137 row_bcast:31 row_mask:0xc bank_mask:0xf\n"
      "v_mul_f32_e64 v191, v168, v191\n"
      "v_mfma_f32_16x16x32_bf16 v[164:167], v[58:61], v[62:65], v[164:167]\n"
      "s_cmp_eq_u32 s8, 72\n"
      "v_readlane_b32 s31, v137, 63\n"
      "v_cvt_pk_bf16_f32 v189, v190, v191\n"
      "v_mul_f32 v212, v168, v212\n"
      "v_subrev_f32 v173, s31, v173\n"
      "v_exp_f32 v173, v173\n"
      "v_mul_f32 v213, v168, v213\n"
      "v_cvt_pk_bf16_f32 v173, v173, v173\n"
      "v_cvt_pk_bf16_f32 v190, v212, v213\n"
      "v_mul_f32 v214, v168, v214\n"
      "v_mul_f32 v215, v168, v215\n"
      "v_cvt_pk_bf16_f32 v191, v214, v215\n"
      "v_mul_f32 v144, v168, v144\n"
      "v_mul_f32 v145, v168, v145\n"
      "v_cvt_pk_bf16_f32 v144, v144, v145\n"
      "v_mul_f32 v146, v168, v146\n"
      "v_mul_f32 v147, v168, v147\n"
      "v_cvt_pk_bf16_f32 v145, v146, v147\n"
      "s_cselect_b32 s8, 0, 100270464\n"
      "ds_write_b16 v130, v173\n"
      "v_mov_b32_e64 v146, 0\n"
      "s_cmp_eq_u32 s9, 72\n"
      "ds_read_b128 v[148:151], v131\n"
      "ds_read_b128 v[212:215], v131 offset:64\n"
      "ds_write_b128 v124, v[188:191] offset:256\n"
      "ds_write_b64 v125, v[144:145] offset:256\n"
      "ds_write_b32 v129, v146\n"
      "ds_read_b128 v[136:139], v114 offset:18432\n"
      "ds_read_b128 v[140:143], v115 offset:18432\n"
      "s_cselect_b32 s9, 0, 196224\n"
      "s_add_u32 s10, s8, s9\n"
      "s_add_u32 s28, s28, s10\n"
      "s_add_u32 s52, s16, s28\n"
      "s_addc_u32 s53, s17, 0\n"
      "s_cmp_lg_u32 s10, s11\n"
      "s_cbranch_scc1 L_edge_53_%=\n"
      "L_eb_54_%=:\n"
      "s_waitcnt lgkmcnt(5)\n"
      "v_mfma_f32_16x16x32_bf16 v[188:191], v[148:151], v[66:69], 0\n"
      "v_mfma_f32_16x16x32_bf16 v[188:191], v[212:215], v[66:69], v[188:191]\n"
      "v_mfma_f32_16x16x32_bf16 v[176:179], v[148:151], v[220:223], 0\n"
      "v_mfma_f32_16x16x32_bf16 v[176:179], v[212:215], v[50:53], v[176:179]\n"
      "v_mfma_f32_16x16x32_bf16 v[220:223], v[148:151], v[208:211], 0\n"
      "s_waitcnt lgkmcnt(0)\n"
      "v_mfma_f32_16x16x32_bf16 v[220:223], v[212:215], v[54:57], v[220:223]\n"
      "v_mfma_f32_16x16x32_bf16 v[208:211], v[148:151], v[216:219], 0\n"
      "s_nop 1\n"
      "v_mov_b32_dpp v188, v188 quad_perm:[0,0,0,0] row_mask:0xf bank_mask:0xf\n"
      "v_mfma_f32_16x16x32_bf16 v[208:211], v[212:215], v[58:61], v[208:211]\n"
      "v_rcp_f32 v188, v188\n"
      "v_mfma_f32_16x16x32_bf16 v[144:147], a[0:3], v[136:139], 0\n"
      "s_nop 0\n"
      "v_cndmask_b32 v176, v176, v220, s[42:43]\n"
      "v_mfma_f32_16x16x32_bf16 v[144:147], a[36:39], v[140:143], v[144:147]\n"
      "s_nop 0\n"
      "v_mfma_f32_16x16x32_bf16 v[148:151], a[4:7], v[136:139], 0\n"
      "s_nop 1\n"
      "v_cndmask_b32 v176, v176, v208, s[44:45]\n"
      "v_mfma_f32_16x16x32_bf16 v[148:151], a[40:43], v[140:143], v[148:151]\n"
      "v_mul_f32 v176, v188, v176\n"
      "v_cvt_pk_bf16_f32 v176, v176, v176\n"
      "ds_write_b16 v135, v176\n"
      "s_branch L_join_10_%=\n"
      "L_flg_50_%=:\n"
      "v_cmp_gt_i32 vcc, 0, v156\n"
      "s_cbranch_vccz L_fb_51_%=\n"
      "s_sleep 1\n"
      "ds_read_b128 v[156:159], v128 offset:3072\n"
      "s_waitcnt lgkmcnt(0)\n"
      "v_bitop3_b32 v156, v156, v157, s4 bitop3:0x7e\n"
      "v_bitop3_b32 v158, v158, v159, s4 bitop3:0x7e\n"
      "v_or_b32 v156, v156, v158\n"
      "v_cmp_ne_u32 s[48:49], 0, v156\n"
      "s_branch L_flg_50_%=\n"
      "L_edge_53_%=:\n"
      "s_mov_b32 s11, s10\n"
      "s_sub_u32 s28, s60, s8\n"
      "s_sub_u32 s29, s61, s9\n"
      "s_mov_b32 s60, s8\n"
      "s_mov_b32 s61, s9\n"
      "s_mov_b64 exec, s[64:65]\n"
      "v_add_u32 v116, s28, v116\n"
      "s_mov_b64 exec, s[70:71]\n"
      "v_add_u32 v116, s29, v116\n"
      "s_mov_b64 exec, s[66:67]\n"
      "v_add_u32 v117, s28, v117\n"
      "s_mov_b64 exec, s[72:73]\n"
      "v_add_u32 v117, s29, v117\n"
      "s_mov_b64 exec, s[68:69]\n"
      "v_add_u32 v118, s28, v118\n"
      "s_mov_b64 exec, s[74:75]\n"
      "v_add_u32_e64 v118, s29, v118\n"
      "s_mov_b64 exec, -1\n"
      "s_nop 4\n"
      "s_branch L_eb_54_%=\n"
      "L_gen_9_%=:\n"
      "s_waitcnt lgkmcnt(0)\n"
      "s_nop 7\n"
      "s_nop 7\n"
      "v_mfma_f32_16x16x32_bf16 v[156:159], v[176:179], v[148:151], v[90:93]\n"
      "s_movk_i32 s31, 0x3fb9\n"
      "v_mbcnt_lo_u32_b32 v160, -1, 0\n"
      "v_mbcnt_hi_u32_b32 v160, -1, v160\n"
      "v_mfma_f32_16x16x32_bf16 v[156:159], v[34:37], v[38:41], v[156:159]\n"
      "v_and_b32 v161, 15, v160\n"
      "v_lshrrev_b32 v162, 4, v160\n"
      "v_mfma_f32_16x16x32_bf16 v[140:143], v[180:183], v[148:151], v[94:97]\n"
      "v_mul_u32_u24 v163, 0x62, v161\n"
      "v_lshl_add_u32 v163, v162, 3, v163\n"
      "v_mfma_f32_16x16x32_bf16 v[140:143], v[40:43], v[38:41], v[140:143]\n"
      "v_add_u32 v163, s30, v163\n"
      "v_and_b32 v160, -4, v163\n"
      "v_and_b32 v163, 2, v163\n"
      "v_lshlrev_b32 v162, 2, v162\n"
      "v_sub_u32 v161, v161, v162\n"
      "v_and_b32 v162, 1, v161\n"
      "v_lshlrev_b32_e64 v162, 4, v162\n"
      "v_lshlrev_b32 v162, v162, s31\n"
      "v_lshrrev_b32_e64 v161, 1, v161\n"
      "v_cmp_eq_u32 vcc, 0, v161\n"
      "s_nop 1\n"
      "v_cndmask_b32 v174, 0, v162, vcc\n"
      "v_cmp_eq_u32 vcc, 1, v161\n"
      "s_nop 1\n"
      "v_cndmask_b32 v175, 0, v162, vcc\n"
      "v_mfma_f32_16x16x32_bf16 v[168:171], v[144:147], v[148:151], v[72:75]\n"
      "ds_read2_b32 v[184:185], v160 offset0:0 offset1:1\n"
      "v_mfma_f32_16x16x32_bf16 v[168:171], v[240:243], v[180:183], v[168:171]\n"
      "ds_read2_b32 v[186:187], v160 offset0:8 offset1:9\n"
      "v_mfma_f32_16x16x32_bf16 v[168:171], v[212:215], v[38:41], v[168:171]\n"
      "ds_read2_b32 v[188:189], v160 offset0:16 offset1:17\n"
      "ds_read2_b32 v[190:191], v160 offset0:2 offset1:10\n"
      "ds_read2_b32 v[200:201], v160 offset0:18 offset1:24\n"
      "v_add_u32 v160, 1568, v160\n"
      "ds_read2_b32 v[202:203], v160 offset0:0 offset1:1\n"
      "ds_read2_b32 v[204:205], v160 offset0:8 offset1:9\n"
      "ds_read2_b32 v[206:207], v160 offset0:16 offset1:17\n"
      "ds_read2_b32 v[224:225], v160 offset0:2 offset1:10\n"
      "ds_read2_b32 v[226:227], v160 offset0:18 offset1:24\n"
      "v_add_u32 v160, 1568, v160\n"
      "ds_read2_b32 v[170:171], v160 offset0:0 offset1:1\n"
      "ds_read2_b32 v[228:229], v160 offset0:8 offset1:9\n"
      "ds_read2_b32 v[230:231], v160 offset0:16 offset1:17\n"
      "ds_read2_b32 v[232:233], v160 offset0:2 offset1:10\n"
      "ds_read2_b32 v[234:235], v160 offset0:18 offset1:24\n"
      "s_waitcnt lgkmcnt(13)\n"
      "v_alignbyte_b32 v184, v185, v184, v163\n"
      "v_alignbyte_b32 v186, v187, v186, v163\n"
      "s_waitcnt lgkmcnt(12)\n"
      "v_alignbyte_b32 v188, v189, v188, v163\n"
      "s_waitcnt lgkmcnt(11)\n"
      "v_alignbyte_b32 v185, v190, v185, v163\n"
      "v_alignbyte_b32 v187, v191, v187, v163\n"
      "v_mfma_f32_16x16x32_bf16 v[236:239], v[152:155], v[148:151], v[98:101]\n"
      "s_waitcnt lgkmcnt(10)\n"
      "v_alignbyte_b32 v189, v200, v189, v163\n"
      "v_alignbyte_b32 v201, v201, v201, v163\n"
      "v_mfma_f32_16x16x32_bf16 v[236:239], v[46:49], v[38:41], v[236:239]\n"
      "s_waitcnt lgkmcnt(9)\n"
      "v_lshlrev_b32 v201, 16, v201\n"
      "v_fmac_f32_e64 v172, s37, v201\n"
      "v_mfma_f32_16x16x16_bf16 v[164:167], v[174:175], v[184:185], v[164:167]\n"
      "ds_read_b128 v[148:151], v128 offset:3072\n"
      "v_alignbyte_b32 v202, v203, v202, v163\n"
      "v_mfma_f32_16x16x16_bf16 v[192:195], v[174:175], v[186:187], v[192:195]\n"
      "s_waitcnt lgkmcnt(6)\n"
      "v_alignbyte_b32 v203, v224, v203, v163\n"
      "v_mfma_f32_16x16x16_bf16 v[196:199], v[174:175], v[188:189], v[196:199]\n"
      "s_nop 0\n"
      "v_alignbyte_b32 v204, v205, v204, v163\n"
      "v_alignbyte_b32 v205, v225, v205, v163\n"
      "v_mfma_f32_16x16x32_bf16 v[184:187], v[176:179], v[136:139], v[102:105]\n"
      "v_alignbyte_b32 v206, v207, v206, v163\n"
      "v_max3_f32 v162, v164, v165, v166\n"
      "v_mfma_f32_16x16x32_bf16 v[184:187], v[34:37], v[44:47], v[184:187]\n"
      "v_alignbyte_b32 v207, v226, v207, v163\n"
      "v_alignbyte_b32 v227, v227, v227, v163\n"
      "v_mfma_f32_16x16x32_bf16 v[176:179], v[180:183], v[136:139], v[106:109]\n"
      "s_waitcnt lgkmcnt(1)\n"
      "v_lshlrev_b32 v227, 16, v227\n"
      "v_alignbyte_b32 v170, v171, v170, v163\n"
      "v_mfma_f32_16x16x32_bf16 v[176:179], v[40:43], v[44:47], v[176:179]\n"
      "v_alignbyte_b32 v171, v232, v171, v163\n"
      "v_max3_f32 v161, v167, v192, v193\n"
      "v_mfma_f32_16x16x32_bf16 v[180:183], v[144:147], v[136:139], v[74:77]\n"
      "v_alignbyte_b32 v228, v229, v228, v163\n"
      "v_alignbyte_b32 v229, v233, v229, v163\n"
      "v_mfma_f32_16x16x32_bf16 v[180:183], v[240:243], v[152:155], v[180:183]\n"
      "v_alignbyte_b32 v230, v231, v230, v163\n"
      "v_max3_f32 v162, v162, v194, v195\n"
      "v_mfma_f32_16x16x32_bf16 v[180:183], v[212:215], v[44:47], v[180:183]\n"
      "v_alignbyte_b32 v231, v234, v231, v163\n"
      "v_alignbyte_b32 v235, v235, v235, v163\n"
      "v_mfma_f32_16x16x32_bf16 v[188:191], v[152:155], v[136:139], v[110:113]\n"
      "v_lshlrev_b32_e64 v235, 16, v235\n"
      "v_max3_f32 v161, v161, v196, v197\n"
      "v_mfma_f32_16x16x32_bf16 v[188:191], v[46:49], v[44:47], v[188:191]\n"
      "v_max3_f32 v162, v162, v198, v199\n"
      "v_max3_f32 v162, v162, v161, v172\n"
      "v_mov_b32 v161, v162\n"
      "s_nop 1\n"
      "v_permlane16_swap_b32 v162, v161\n"
      "v_max_f32 v162, v162, v161\n"
      "v_mfma_f32_16x16x16_bf16 v[156:159], v[174:175], v[202:203], v[156:159]\n"
      "v_mov_b32 v161, v162\n"
      "v_mfma_f32_16x16x32_bf16 v[136:139], v[240:243], v[144:147], v[76:79]\n"
      "s_nop 0\n"
      "v_permlane32_swap_b32 v162, v161\n"
      "v_mfma_f32_16x16x16_bf16 v[140:143], v[174:175], v[204:205], v[140:143]\n"
      "v_max_f32 v162, v162, v161\n"
      "v_sub_f32 v164, v164, v162\n"
      "v_mfma_f32_16x16x16_bf16 v[236:239], v[174:175], v[206:207], v[236:239]\n"
      "v_sub_f32 v165, v165, v162\n"
      "v_sub_f32 v166, v166, v162\n"
      "v_mfma_f32_16x16x16_bf16 v[136:139], v[214:215], v[212:213], v[136:139]\n"
      "s_waitcnt lgkmcnt(0)\n"
      "v_sub_f32 v167, v167, v162\n"
      "v_sub_f32 v192, v192, v162\n"
      "v_sub_f32 v193, v193, v162\n"
      "v_sub_f32 v194, v194, v162\n"
      "v_sub_f32 v195, v195, v162\n"
      "v_sub_f32 v196, v196, v162\n"
      "v_sub_f32 v197, v197, v162\n"
      "v_sub_f32 v198, v198, v162\n"
      "v_sub_f32 v199, v199, v162\n"
      "v_sub_f32 v172, v172, v162\n"
      "v_exp_f32 v164, v164\n"
      "v_exp_f32 v165, v165\n"
      "v_exp_f32 v166, v166\n"
      "v_cvt_pk_bf16_f32 v164, v164, v165\n"
      "v_exp_f32 v167, v167\n"
      "v_exp_f32 v192, v192\n"
      "v_cvt_pk_bf16_f32 v165, v166, v167\n"
      "v_exp_f32 v193, v193\n"
      "v_exp_f32 v194, v194\n"
      "v_cvt_pk_bf16_f32 v166, v192, v193\n"
      "v_exp_f32 v195, v195\n"
      "v_exp_f32 v196, v196\n"
      "v_cvt_pk_bf16_f32 v167, v194, v195\n"
      "v_mfma_f32_16x16x16_bf16 v[184:187], v[174:175], v[170:171], v[184:187]\n"
      "v_exp_f32 v197, v197\n"
      "v_mfma_f32_16x16x16_bf16 v[176:179], v[174:175], v[228:229], v[176:179]\n"
      "v_exp_f32 v198, v198\n"
      "v_mfma_f32_16x16x16_bf16 v[188:191], v[174:175], v[230:231], v[188:191]\n"
      "v_exp_f32 v199, v199\n"
      "v_exp_f32_e64 v172, v172\n"
      "v_fmac_f32_e64 v168, s37, v227\n"
      "v_cvt_pk_bf16_f32 v62, v196, v197\n"
      "v_cvt_pk_bf16_f32 v63, v198, v199\n"
      "v_cvt_pk_bf16_f32 v64, v172, 0\n"
      "v_bitop3_b32 v148, v148, v149, s4 bitop3:0x7e\n"
      "v_bitop3_b32 v150, v150, v151, s4 bitop3:0x7e\n"
      "v_mfma_f32_16x16x32_bf16 v[152:155], v[66:69], v[164:167], 0\n"
      "v_or_b32 v148, v148, v150\n"
      "v_cmp_ne_u32 vcc, 0, v148\n"
      "v_mfma_f32_16x16x32_bf16 v[152:155], v[66:69], v[62:65], v[152:155]\n"
      "s_mov_b64 s[48:49], vcc\n"
      "v_max3_f32 v162, v156, v157, v158\n"
      "v_max3_f32 v161, v159, v140, v141\n"
      "v_mfma_f32_16x16x32_bf16 v[144:147], v[220:223], v[164:167], 0\n"
      "s_cbranch_vccnz L_flg_61_%=\n"
      "L_fb_62_%=:\n"
      "v_mfma_f32_16x16x32_bf16 v[144:147], v[50:53], v[62:65], v[144:147]\n"
      "v_max3_f32 v162, v162, v142, v143\n"
      "v_max3_f32 v161, v161, v236, v237\n"
      "v_mfma_f32_16x16x32_bf16 v[148:151], v[208:211], v[164:167], 0\n"
      "v_max3_f32 v162, v162, v238, v239\n"
      "v_max3_f32 v162, v162, v161, v168\n"
      "v_mfma_f32_16x16x32_bf16 v[148:151], v[54:57], v[62:65], v[148:151]\n"
      "v_mov_b32 v161, v162\n"
      "v_fmac_f32 v180, s37, v235\n"
      "v_mfma_f32_16x16x32_bf16 v[196:199], v[216:219], v[164:167], 0\n"
      "v_permlane16_swap_b32 v162, v161\n"
      "v_mfma_f32_16x16x32_bf16 v[196:199], v[58:61], v[62:65], v[196:199]\n"
      "v_max_f32 v162, v162, v161\n"
      "v_mov_b32 v161, v162\n"
      "s_nop 1\n"
      "v_permlane32_swap_b32 v162, v161\n"
      "v_max_f32 v162, v162, v161\n"
      "v_sub_f32 v156, v156, v162\n"
      "v_sub_f32 v157, v157, v162\n"
      "v_sub_f32 v158, v158, v162\n"
      "v_sub_f32 v159, v159, v162\n"
      "v_sub_f32 v140, v140, v162\n"
      "v_sub_f32 v141, v141, v162\n"
      "v_sub_f32 v142, v142, v162\n"
      "v_sub_f32 v143, v143, v162\n"
      "v_sub_f32 v236, v236, v162\n"
      "v_sub_f32 v237, v237, v162\n"
      "v_sub_f32 v238, v238, v162\n"
      "v_sub_f32 v239, v239, v162\n"
      "v_sub_f32 v168, v168, v162\n"
      "v_exp_f32 v156, v156\n"
      "v_exp_f32 v157, v157\n"
      "v_exp_f32 v158, v158\n"
      "v_cvt_pk_bf16_f32 v156, v156, v157\n"
      "v_exp_f32 v159, v159\n"
      "v_exp_f32 v140, v140\n"
      "v_cvt_pk_bf16_f32 v157, v158, v159\n"
      "v_exp_f32 v141, v141\n"
      "v_exp_f32 v142, v142\n"
      "v_cvt_pk_bf16_f32 v158, v140, v141\n"
      "v_exp_f32 v143, v143\n"
      "v_exp_f32 v236, v236\n"
      "v_cvt_pk_bf16_f32 v159, v142, v143\n"
      "v_exp_f32 v237, v237\n"
      "v_exp_f32 v238, v238\n"
      "v_exp_f32 v239, v239\n"
      "v_exp_f32 v168, v168\n"
      "v_cvt_pk_bf16_f32 v62, v236, v237\n"
      "v_cvt_pk_bf16_f32 v63, v238, v239\n"
      "v_cvt_pk_bf16_f32 v64, v168, 0\n"
      "v_rcp_f32 v152, v152\n"
      "v_max3_f32 v153, v184, v185, v186\n"
      "v_mfma_f32_16x16x32_bf16 v[140:143], v[66:69], v[156:159], 0\n"
      "v_mul_f32 v144, v152, v144\n"
      "v_mul_f32 v145, v152, v145\n"
      "v_mfma_f32_16x16x32_bf16 v[140:143], v[66:69], v[62:65], v[140:143]\n"
      "s_add_u32 s28, s27, 2\n"
      "v_cvt_pk_bf16_f32 v144, v144, v145\n"
      "v_mul_f32_e64 v146, v152, v146\n"
      "v_mfma_f32_16x16x32_bf16 v[236:239], v[220:223], v[156:159], 0\n"
      "s_min_u32 m0, s28, s3\n"
      "v_mul_f32 v147, v152, v147\n"
      "v_cvt_pk_bf16_f32 v145, v146, v147\n"
      "v_mfma_f32_16x16x32_bf16 v[236:239], v[50:53], v[62:65], v[236:239]\n"
      "s_movrels_b32 s9, s80\n"
      "v_mul_f32 v148, v152, v148\n"
      "v_mul_f32 v149, v152, v149\n"
      "v_mfma_f32_16x16x32_bf16 v[228:231], v[208:211], v[156:159], 0\n"
      "s_lshr_b32 s8, s9, 8\n"
      "v_cvt_pk_bf16_f32 v146, v148, v149\n"
      "v_mul_f32 v150, v152, v150\n"
      "v_mfma_f32_16x16x32_bf16 v[228:231], v[54:57], v[62:65], v[228:231]\n"
      "s_and_b32 s9, s9, 0xff\n"
      "v_mul_f32 v151, v152, v151\n"
      "v_cvt_pk_bf16_f32 v147, v150, v151\n"
      "v_mfma_f32_16x16x32_bf16 v[148:151], v[216:219], v[156:159], 0\n"
      "s_mul_i32 s10, s8, 73\n"
      "v_mul_f32 v196, v152, v196\n"
      "v_mul_f32 v197, v152, v197\n"
      "v_mfma_f32_16x16x32_bf16 v[148:151], v[58:61], v[62:65], v[148:151]\n"
      "ds_write_b128 v124, v[144:147]\n"
      "v_cvt_pk_bf16_f32 v196, v196, v197\n"
      "v_mul_f32 v198, v152, v198\n"
      "v_mul_f32 v199, v152, v199\n"
      "v_cvt_pk_bf16_f32 v197, v198, v199\n"
      "v_max3_f32 v152, v187, v176, v177\n"
      "v_max3_f32 v153, v153, v178, v179\n"
      "v_max3_f32 v152, v152, v188, v189\n"
      "v_max3_f32 v153, v153, v190, v191\n"
      "v_max3_f32 v153, v153, v152, v180\n"
      "v_mov_b32 v152, v153\n"
      "s_add_u32 s10, s10, s9\n"
      "s_nop 0\n"
      "v_permlane16_swap_b32 v153, v152\n"
      "v_max_f32 v153, v153, v152\n"
      "v_mov_b32 v152, v153\n"
      "s_mul_i32 s28, s10, 4802\n"
      "s_nop 0\n"
      "v_permlane32_swap_b32 v153, v152\n"
      "v_max_f32 v153, v153, v152\n"
      "v_sub_f32 v184, v184, v153\n"
      "v_sub_f32 v185, v185, v153\n"
      "v_sub_f32 v186, v186, v153\n"
      "v_sub_f32 v187, v187, v153\n"
      "v_sub_f32 v176, v176, v153\n"
      "v_sub_f32 v177, v177, v153\n"
      "v_sub_f32 v178, v178, v153\n"
      "v_sub_f32 v179, v179, v153\n"
      "v_sub_f32 v188, v188, v153\n"
      "v_sub_f32 v189, v189, v153\n"
      "v_sub_f32 v190, v190, v153\n"
      "v_sub_f32 v191, v191, v153\n"
      "v_sub_f32 v180, v180, v153\n"
      "v_exp_f32 v184, v184\n"
      "v_exp_f32 v185, v185\n"
      "v_exp_f32 v186, v186\n"
      "v_cvt_pk_bf16_f32 v184, v184, v185\n"
      "v_exp_f32 v187, v187\n"
      "v_exp_f32 v176, v176\n"
      "v_cvt_pk_bf16_f32 v185, v186, v187\n"
      "s_add_u32 s56, s18, s28\n"
      "ds_write_b64 v125, v[196:197]\n"
      "v_exp_f32 v177, v177\n"
      "v_exp_f32 v178, v178\n"
      "v_cvt_pk_bf16_f32 v186, v176, v177\n"
      "v_exp_f32 v179, v179\n"
      "v_exp_f32 v188, v188\n"
      "v_cvt_pk_bf16_f32 v187, v178, v179\n"
      "v_exp_f32 v189, v189\n"
      "v_exp_f32 v190, v190\n"
      "v_exp_f32_e64 v191, v191\n"
      "v_exp_f32_e64 v180, v180\n"
      "v_cvt_pk_bf16_f32 v62, v188, v189\n"
      "v_cvt_pk_bf16_f32 v63, v190, v191\n"
      "v_cvt_pk_bf16_f32 v64, v180, 0\n"
      "v_cndmask_b32 v173, v173, v169, s[42:43]\n"
      "v_cndmask_b32 v173, v173, v181, s[44:45]\n"
      "v_mfma_f32_16x16x32_bf16 v[152:155], v[66:69], v[184:187], 0\n"
      "s_addc_u32 s57, s19, 0\n"
      "v_cndmask_b32 v173, v173, v137, s[46:47]\n"
      "v_mbcnt_lo_u32_b32 v160, -1, 0\n"
      "v_mfma_f32_16x16x32_bf16 v[152:155], v[66:69], v[62:65], v[152:155]\n"
      "s_and_b32 s56, s56, -16\n"
      "v_mbcnt_hi_u32_b32 v160, -1, v160\n"
      "v_min_u32 v160, 48, v160\n"
      "v_mfma_f32_16x16x32_bf16 v[156:159], v[220:223], v[184:187], 0\n"
      "s_sub_u32 s58, s56, 110592\n"
      "v_lshlrev_b32 v160, 1, v160\n"
      "v_add_u32 v160, s30, v160\n"
      "v_mfma_f32_16x16x32_bf16 v[156:159], v[50:53], v[62:65], v[156:159]\n"
      "ds_read_u16 v141, v160 offset:4704\n"
      "v_rcp_f32 v140, v140\n"
      "v_mfma_f32_16x16x32_bf16 v[160:163], v[208:211], v[184:187], 0\n"
      "s_subb_u32 s59, s57, 0\n"
      "v_mul_f32 v236, v140, v236\n"
      "v_mul_f32_e64 v237, v140, v237\n"
      "v_mfma_f32_16x16x32_bf16 v[160:163], v[54:57], v[62:65], v[160:163]\n"
      "s_sub_u32 s56, s56, 73728\n"
      "v_cvt_pk_bf16_f32 v236, v236, v237\n"
      "v_mul_f32 v238, v140, v238\n"
      "v_mfma_f32_16x16x32_bf16 v[164:167], v[216:219], v[184:187], 0\n"
      "s_subb_u32 s57, s57, 0\n"
      "v_mul_f32_e64 v239, v140, v239\n"
      "v_cvt_pk_bf16_f32 v237, v238, v239\n"
      "v_mfma_f32_16x16x32_bf16 v[164:167], v[58:61], v[62:65], v[164:167]\n"
      "s_mul_i32 s28, s8, 1373568\n"
      "v_mul_f32 v228, v140, v228\n"
      "v_mul_f32 v229, v140, v229\n"
      "v_cvt_pk_bf16_f32 v238, v228, v229\n"
      "v_mul_f32 v230, v140, v230\n"
      "v_mul_f32 v231, v140, v231\n"
      "v_cvt_pk_bf16_f32 v239, v230, v231\n"
      "v_mul_f32 v148, v140, v148\n"
      "v_mul_f32 v149, v140, v149\n"
      "v_cvt_pk_bf16_f32 v148, v148, v149\n"
      "v_mul_f32 v150, v140, v150\n"
      "s_mul_i32 s29, s9, 2688\n"
      "ds_write_b128 v124, v[236:239] offset:256\n"
      "s_waitcnt lgkmcnt(1)\n"
      "v_lshlrev_b32 v141, 16, v141\n"
      "v_fmac_f32 v173, s37, v141\n"
      "v_mul_f32 v151, v140, v151\n"
      "v_cvt_pk_bf16_f32 v149, v150, v151\n"
      "v_max_f32_dpp v150, v173, v173 row_ror:8 row_mask:0xf bank_mask:0xf\n"
      "s_add_u32 s28, s28, s29\n"
      "s_nop 0\n"
      "v_max_f32_dpp v150, v150, v150 row_ror:4 row_mask:0xf bank_mask:0xf\n"
      "s_cmp_eq_u32 s8, 72\n"
      "s_nop 0\n"
      "v_max_f32_dpp v150, v150, v150 row_ror:2 row_mask:0xf bank_mask:0xf\n"
      "s_cselect_b32 s8, 0, 100270464\n"
      "s_nop 0\n"
      "v_max_f32_dpp v150, v150, v150 row_ror:1 row_mask:0xf bank_mask:0xf\n"
      "s_cmp_eq_u32 s9, 72\n"
      "s_nop 0\n"
      "v_max_f32_dpp v150, v150, v150 row_bcast:15 row_mask:0xa bank_mask:0xf\n"
      "s_cselect_b32 s9, 0, 196224\n"
      "s_nop 0\n"
      "v_max_f32_dpp v150, v150, v150 row_bcast:31 row_mask:0xc bank_mask:0xf\n"
      "s_nop 0\n"
      "v_readlane_b32 s31, v150, 63\n"
      "s_add_u32 s10, s8, s9\n"
      "s_nop 0\n"
      "v_subrev_f32 v173, s31, v173\n"
      "v_exp_f32_e64 v173, v173\n"
      "s_nop 0\n"
      "v_cvt_pk_bf16_f32 v173, v173, v173\n"
      "s_add_u32 s28, s28, s10\n"
      "ds_write_b16 v130, v173\n"
      "v_mov_b32_e64 v150, 0\n"
      "s_add_u32 s52, s16, s28\n"
      "ds_read_b128 v[144:147], v131\n"
      "ds_read_b128 v[236:239], v131 offset:64\n"
      "ds_write_b64 v125, v[148:149] offset:256\n"
      "ds_write_b32 v129, v150\n"
      "ds_read_b128 v[136:139], v114 offset:18432\n"
      "ds_read_b128 v[140:143], v115 offset:18432\n"
      "s_addc_u32 s53, s17, 0\n"
      "s_cmp_lg_u32 s10, s11\n"
      "s_cbranch_scc1 L_edge_64_%=\n"
      "L_eb_65_%=:\n"
      "s_waitcnt lgkmcnt(4)\n"
      "v_mfma_f32_16x16x32_bf16 v[228:231], v[144:147], v[66:69], 0\n"
      "v_mfma_f32_16x16x32_bf16 v[228:231], v[236:239], v[66:69], v[228:231]\n"
      "s_nop 0\n"
      "v_mfma_f32_16x16x32_bf16 v[184:187], v[144:147], v[220:223], 0\n"
      "s_waitcnt lgkmcnt(0)\n"
      "v_mfma_f32_16x16x32_bf16 v[184:187], v[236:239], v[50:53], v[184:187]\n"
      "v_mfma_f32_16x16x32_bf16 v[220:223], v[144:147], v[208:211], 0\n"
      "v_mfma_f32_16x16x32_bf16 v[220:223], v[236:239], v[54:57], v[220:223]\n"
      "v_mfma_f32_16x16x32_bf16 v[208:211], v[144:147], v[216:219], 0\n"
      "s_nop 1\n"
      "v_mov_b32_dpp v228, v228 quad_perm:[0,0,0,0] row_mask:0xf bank_mask:0xf\n"
      "v_mfma_f32_16x16x32_bf16 v[208:211], v[236:239], v[58:61], v[208:211]\n"
      "v_rcp_f32 v228, v228\n"
      "v_mfma_f32_16x16x32_bf16 v[144:147], a[0:3], v[136:139], 0\n"
      "s_nop 0\n"
      "v_cndmask_b32 v184, v184, v220, s[42:43]\n"
      "v_mfma_f32_16x16x32_bf16 v[144:147], a[36:39], v[140:143], v[144:147]\n"
      "v_mfma_f32_16x16x32_bf16 v[148:151], a[4:7], v[136:139], 0\n"
      "s_nop 1\n"
      "v_cndmask_b32 v184, v184, v208, s[44:45]\n"
      "v_mfma_f32_16x16x32_bf16 v[148:151], a[40:43], v[140:143], v[148:151]\n"
      "v_mul_f32_e64 v184, v228, v184\n"
      "v_cvt_pk_bf16_f32 v184, v184, v184\n"
      "ds_write_b16 v135, v184\n"
      "s_branch L_join_10_%=\n"
      "L_flg_61_%=:\n"
      "v_cmp_gt_i32 vcc, 0, v148\n"
      "s_cbranch_vccz L_fb_62_%=\n"
      "s_sleep 1\n"
      "ds_read_b128 v[148:151], v128 offset:3072\n"
      "s_waitcnt lgkmcnt(0)\n"
      "v_bitop3_b32 v148, v148, v149, s4 bitop3:0x7e\n"
      "v_bitop3_b32 v150, v150, v151, s4 bitop3:0x7e\n"
      "v_or_b32 v148, v148, v150\n"
      "v_cmp_ne_u32 s[48:49], 0, v148\n"
      "s_branch L_flg_61_%=\n"
      "L_edge_64_%=:\n"
      "s_mov_b32 s11, s10\n"
      "s_sub_u32 s28, s60, s8\n"
      "s_sub_u32 s29, s61, s9\n"
      "s_mov_b32 s60, s8\n"
      "s_mov_b32 s61, s9\n"
      "s_mov_b64 exec, s[64:65]\n"
      "v_add_u32 v116, s28, v116\n"
      "s_mov_b64 exec, s[70:71]\n"
      "v_add_u32 v116, s29, v116\n"
      "s_mov_b64 exec, s[66:67]\n"
      "v_add_u32 v117, s28, v117\n"
      "s_mov_b64 exec, s[72:73]\n"
      "v_add_u32_e64 v117, s29, v117\n"
      "s_mov_b64 exec, s[68:69]\n"
      "v_add_u32 v118, s28, v118\n"
      "s_mov_b64 exec, s[74:75]\n"
      "v_add_u32_e64 v118, s29, v118\n"
      "s_mov_b64 exec, -1\n"
      "s_nop 4\n"
      "s_branch L_eb_65_%=\n"
      "L_gen_19_%=:\n"
      "s_waitcnt lgkmcnt(0)\n"
      "s_nop 7\n"
      "s_nop 7\n"
      "s_cmp_eq_u64 s[48:49], 0\n"
      "s_cbranch_scc1 L_go_75_%=\n"
      "s_mov_b32 m0, s27\n"
      "s_nop 0\n"
      "s_movrels_b32 s9, s80\n"
      "s_lshr_b32 s8, s9, 8\n"
      "s_and_b32 s9, s9, 0xff\n"
      "s_mul_i32 s10, s8, 73\n"
      "s_add_u32 s10, s10, s9\n"
      "s_mul_i32 s30, s10, 4802\n"
      "s_add_u32 s30, s30, s18\n"
      "s_and_b32 s29, s30, 15\n"
      "s_add_u32 s30, s29, 115456\n"
      "s_lshl_b64 s[34:35], s[48:49], 4\n"
      "s_lshr_b64 s[34:35], s[34:35], 5\n"
      "s_cbranch_scc1 L_gen_20_%=\n"
      "v_mbcnt_lo_u32_b32 v152, -1, 0\n"
      "v_mbcnt_hi_u32_b32 v152, -1, v152\n"
      "v_lshlrev_b32 v153, 1, v152\n"
      "v_add_u32 v154, 4802, v153\n"
      "v_cmp_gt_u32 vcc, s29, v153\n"
      "s_nop 1\n"
      "v_cndmask_b32 v153, v154, v153, vcc\n"
      "v_add_u32 v153, 115456, v153\n"
      "v_mov_b32 v154, 0\n"
      "s_mov_b64 exec, 0x7f\n"
      "ds_write_b16 v153, v154\n"
      "s_mov_b64 exec, -1\n"
      "v_lshlrev_b32 v153, 4, v152\n"
      "v_add_u32 v153, 115456, v153\n"
      "v_min_u32 v152, 116160, v153\n"
      "ds_read_b128 v[188:191], v153\n"
      "ds_read_b128 v[220:223], v153 offset:1024\n"
      "ds_read_b128 v[248:251], v153 offset:2048\n"
      "ds_read_b128 v[236:239], v153 offset:3072\n"
      "ds_read_b128 v[144:147], v152 offset:4096\n"
      "s_waitcnt lgkmcnt(4)\n"
      "v_or3_b32 v188, v188, v189, v190\n"
      "s_waitcnt lgkmcnt(3)\n"
      "v_or3_b32 v188, v188, v191, v220\n"
      "v_or3_b32 v188, v188, v221, v222\n"
      "s_waitcnt lgkmcnt(2)\n"
      "v_or3_b32 v188, v188, v223, v248\n"
      "v_or3_b32 v188, v188, v249, v250\n"
      "s_waitcnt lgkmcnt(1)\n"
      "v_or3_b32 v188, v188, v251, v236\n"
      "v_or3_b32 v188, v188, v237, v238\n"
      "s_waitcnt lgkmcnt(0)\n"
      "v_or3_b32 v188, v188, v239, v144\n"
      "v_or3_b32 v188, v188, v145, v146\n"
      "v_bitop3_b32 v188, v188, v147, s40 bitop3:0xa8\n"
      "v_cmp_ne_u32 s[48:49], 0, v188\n"
      "s_cmp_lg_u64 s[48:49], 0\n"
      "s_cbranch_scc1 L_gen_20_%=\n"
      "s_cmp_eq_u64 s[50:51], 0\n"
      "s_cbranch_scc1 L_fast_22_%=\n"
      "L_go_75_%=:\n"
      "v_mfma_f32_16x16x32_bf16 v[152:155], v[212:215], v[180:183], v[90:93]\n"
      "ds_read_b128 v[188:191], v128 offset:2048\n"
      "v_max3_f32 v223, v164, v165, v166\n"
      "v_mfma_f32_16x16x32_bf16 v[152:155], v[34:37], v[38:41], v[152:155]\n"
      "v_max3_f32 v221, v167, v184, v185\n"
      "v_max3_f32 v223, v223, v186, v187\n"
      "v_mfma_f32_16x16x32_bf16 v[248:251], v[136:139], v[180:183], v[94:97]\n"
      "v_max3_f32 v221, v221, v192, v193\n"
      "v_max3_f32 v223, v223, v194, v195\n"
      "v_mfma_f32_16x16x32_bf16 v[248:251], v[40:43], v[38:41], v[248:251]\n"
      "v_max3_f32 v223, v223, v221, v148\n"
      "v_mov_b32 v221, v223\n"
      "v_mfma_f32_16x16x32_bf16 v[236:239], v[140:143], v[180:183], v[72:75]\n"
      "s_nop 0\n"
      "v_permlane16_swap_b32 v223, v221\n"
      "v_mfma_f32_16x16x32_bf16 v[236:239], v[228:231], v[136:139], v[236:239]\n"
      "v_max_f32 v223, v223, v221\n"
      "v_mov_b32 v221, v223\n"
      "v_mfma_f32_16x16x32_bf16 v[236:239], v[156:159], v[38:41], v[236:239]\n"
      "s_nop 0\n"
      "v_permlane32_swap_b32 v223, v221\n"
      "v_mfma_f32_16x16x32_bf16 v[144:147], v[200:203], v[180:183], v[98:101]\n"
      "v_max_f32 v223, v223, v221\n"
      "v_sub_f32 v164, v164, v223\n"
      "v_mfma_f32_16x16x32_bf16 v[144:147], v[46:49], v[38:41], v[144:147]\n"
      "s_waitcnt lgkmcnt(0)\n"
      "v_sub_f32 v165, v165, v223\n"
      "v_sub_f32 v166, v166, v223\n"
      "v_sub_f32 v167, v167, v223\n"
      "v_sub_f32 v184, v184, v223\n"
      "v_sub_f32 v185, v185, v223\n"
      "v_sub_f32 v186, v186, v223\n"
      "v_sub_f32 v187, v187, v223\n"
      "v_sub_f32 v192, v192, v223\n"
      "v_sub_f32 v193, v193, v223\n"
      "v_sub_f32 v194, v194, v223\n"
      "v_sub_f32 v195, v195, v223\n"
      "v_sub_f32 v148, v148, v223\n"
      "v_exp_f32 v164, v164\n"
      "v_exp_f32 v165, v165\n"
      "v_exp_f32_e64 v166, v166\n"
      "v_cvt_pk_bf16_f32 v164, v164, v165\n"
      "v_exp_f32 v167, v167\n"
      "v_exp_f32 v184, v184\n"
      "v_cvt_pk_bf16_f32 v165, v166, v167\n"
      "v_exp_f32 v185, v185\n"
      "v_exp_f32 v186, v186\n"
      "v_cvt_pk_bf16_f32 v166, v184, v185\n"
      "v_exp_f32 v187, v187\n"
      "v_exp_f32 v192, v192\n"
      "v_cvt_pk_bf16_f32 v167, v186, v187\n"
      "v_exp_f32 v193, v193\n"
      "v_exp_f32 v194, v194\n"
      "v_exp_f32 v195, v195\n"
      "v_mfma_f32_16x16x32_bf16 v[184:187], v[212:215], v[196:199], v[102:105]\n"
      "v_exp_f32 v148, v148\n"
      "v_mfma_f32_16x16x32_bf16 v[184:187], v[34:37], v[44:47], v[184:187]\n"
      "v_cvt_pk_bf16_f32 v62, v192, v193\n"
      "v_cvt_pk_bf16_f32 v63, v194, v195\n"
      "v_cvt_pk_bf16_f32 v64, v148, 0\n"
      "v_bitop3_b32 v188, v188, v189, s4 bitop3:0x7e\n"
      "v_bitop3_b32 v190, v190, v191, s4 bitop3:0x7e\n"
      "v_mfma_f32_16x16x32_bf16 v[180:183], v[66:69], v[164:167], 0\n"
      "ds_read_b128 v[192:195], v119 offset:12288\n"
      "v_or_b32 v188, v188, v190\n"
      "v_mfma_f32_16x16x32_bf16 v[180:183], v[66:69], v[62:65], v[180:183]\n"
      "v_cmp_ne_u32 vcc, 0, v188\n"
      "v_max3_f32 v238, v152, v153, v154\n"
      "v_mfma_f32_16x16x32_bf16 v[220:223], v[204:207], v[164:167], 0\n"
      "s_mov_b64 s[48:49], vcc\n"
      "v_max3_f32 v239, v155, v248, v249\n"
      "v_max3_f32 v238, v238, v250, v251\n"
      "v_mfma_f32_16x16x32_bf16 v[220:223], v[50:53], v[62:65], v[220:223]\n"
      "s_cbranch_vccnz L_flg_76_%=\n"
      "L_fb_77_%=:\n"
      "v_max3_f32 v239, v239, v144, v145\n"
      "v_mfma_f32_16x16x32_bf16 v[188:191], v[224:227], v[164:167], 0\n"
      "v_max3_f32 v238, v238, v146, v147\n"
      "v_max3_f32 v238, v238, v239, v236\n"
      "v_mfma_f32_16x16x32_bf16 v[188:191], v[54:57], v[62:65], v[188:191]\n"
      "v_mov_b32_e64 v239, v238\n"
      "v_mfma_f32_16x16x32_bf16 v[212:215], v[160:163], v[164:167], 0\n"
      "s_nop 0\n"
      "v_permlane16_swap_b32 v238, v239\n"
      "v_mfma_f32_16x16x32_bf16 v[212:215], v[58:61], v[62:65], v[212:215]\n"
      "v_max_f32 v238, v238, v239\n"
      "v_mov_b32 v239, v238\n"
      "v_mfma_f32_16x16x32_bf16 v[164:167], v[136:139], v[196:199], v[106:109]\n"
      "s_nop 0\n"
      "v_permlane32_swap_b32 v238, v239\n"
      "v_mfma_f32_16x16x32_bf16 v[164:167], v[40:43], v[44:47], v[164:167]\n"
      "v_max_f32 v238, v238, v239\n"
      "v_sub_f32 v152, v152, v238\n"
      "v_sub_f32 v153, v153, v238\n"
      "v_sub_f32 v154, v154, v238\n"
      "v_sub_f32 v155, v155, v238\n"
      "v_sub_f32 v248, v248, v238\n"
      "v_sub_f32 v249, v249, v238\n"
      "v_sub_f32 v250, v250, v238\n"
      "v_sub_f32 v251, v251, v238\n"
      "v_sub_f32 v144, v144, v238\n"
      "v_sub_f32 v145, v145, v238\n"
      "v_sub_f32 v146, v146, v238\n"
      "v_sub_f32 v147, v147, v238\n"
      "v_sub_f32 v236, v236, v238\n"
      "v_exp_f32 v152, v152\n"
      "v_exp_f32 v153, v153\n"
      "v_exp_f32_e64 v154, v154\n"
      "v_cvt_pk_bf16_f32 v152, v152, v153\n"
      "v_exp_f32 v155, v155\n"
      "v_mfma_f32_16x16x32_bf16 v[238:241], v[140:143], v[196:199], v[74:77]\n"
      "s_waitcnt lgkmcnt(0)\n"
      "v_exp_f32_e64 v248, v248\n"
      "v_mfma_f32_16x16x32_bf16 v[238:241], v[228:231], v[200:203], v[238:241]\n"
      "global_store_dwordx4 v116, v[192:195], s[54:55] offset:256 sc1\n"
      "v_mfma_f32_16x16x32_bf16 v[238:241], v[156:159], v[44:47], v[238:241]\n"
      "v_cvt_pk_bf16_f32 v153, v154, v155\n"
      "v_exp_f32 v249, v249\n"
      "v_mfma_f32_16x16x32_bf16 v[136:139], v[200:203], v[196:199], v[110:113]\n"
      "v_exp_f32 v250, v250\n"
      "v_mfma_f32_16x16x32_bf16 v[136:139], v[46:49], v[44:47], v[136:139]\n"
      "v_cvt_pk_bf16_f32 v154, v248, v249\n"
      "v_exp_f32 v251, v251\n"
      "v_exp_f32 v144, v144\n"
      "v_cvt_pk_bf16_f32 v155, v250, v251\n"
      "v_mfma_f32_16x16x32_bf16 v[192:195], v[228:231], v[140:143], v[76:79]\n"
      "v_exp_f32 v145, v145\n"
      "v_exp_f32 v146, v146\n"
      "v_exp_f32 v147, v147\n"
      "v_exp_f32 v236, v236\n"
      "v_cvt_pk_bf16_f32 v62, v144, v145\n"
      "v_cvt_pk_bf16_f32 v63, v146, v147\n"
      "v_mfma_f32_16x16x16_bf16 v[192:195], v[158:159], v[156:157], v[192:195]\n"
      "v_cvt_pk_bf16_f32 v64, v236, 0\n"
      "v_rcp_f32_e64 v180, v180\n"
      "v_max3_f32 v236, v184, v185, v186\n"
      "v_mfma_f32_16x16x32_bf16 v[196:199], v[66:69], v[152:155], 0\n"
      "ds_read_b128 v[140:143], v120\n"
      "v_mul_f32 v220, v180, v220\n"
      "v_mfma_f32_16x16x32_bf16 v[196:199], v[66:69], v[62:65], v[196:199]\n"
      "v_mul_f32 v221, v180, v221\n"
      "v_cvt_pk_bf16_f32 v220, v220, v221\n"
      "v_mfma_f32_16x16x32_bf16 v[200:203], v[204:207], v[152:155], 0\n"
      "v_mul_f32 v222, v180, v222\n"
      "v_mul_f32 v223, v180, v223\n"
      "v_mfma_f32_16x16x32_bf16 v[200:203], v[50:53], v[62:65], v[200:203]\n"
      "v_cvt_pk_bf16_f32 v221, v222, v223\n"
      "v_mul_f32 v188, v180, v188\n"
      "v_mul_f32 v189, v180, v189\n"
      "v_cvt_pk_bf16_f32 v222, v188, v189\n"
      "v_mfma_f32_16x16x32_bf16 v[248:251], v[224:227], v[152:155], 0\n"
      "v_mul_f32 v190, v180, v190\n"
      "v_mul_f32 v191, v180, v191\n"
      "v_mfma_f32_16x16x32_bf16 v[248:251], v[54:57], v[62:65], v[248:251]\n"
      "v_cvt_pk_bf16_f32 v223, v190, v191\n"
      "v_mul_f32 v212, v180, v212\n"
      "v_mul_f32 v213, v180, v213\n"
      "v_cvt_pk_bf16_f32 v212, v212, v213\n"
      "v_mul_f32 v214, v180, v214\n"
      "v_mul_f32 v215, v180, v215\n"
      "v_mfma_f32_16x16x32_bf16 v[188:191], v[160:163], v[152:155], 0\n"
      "v_cvt_pk_bf16_f32 v213, v214, v215\n"
      "v_max3_f32 v192, v187, v164, v165\n"
      "v_mfma_f32_16x16x32_bf16 v[188:191], v[58:61], v[62:65], v[188:191]\n"
      "ds_write_b128 v124, v[220:223] offset:18432\n"
      "v_max3_f32 v236, v236, v166, v167\n"
      "v_max3_f32 v192, v192, v136, v137\n"
      "v_max3_f32 v236, v236, v138, v139\n"
      "v_max3_f32 v236, v236, v192, v238\n"
      "v_mov_b32 v192, v236\n"
      "s_waitcnt lgkmcnt(1)\n"
      "s_nop 0\n"
      "v_permlane16_swap_b32 v236, v192\n"
      "v_max_f32 v236, v236, v192\n"
      "v_mov_b32 v192, v236\n"
      "s_nop 1\n"
      "v_permlane32_swap_b32 v236, v192\n"
      "v_max_f32 v236, v236, v192\n"
      "v_sub_f32 v184, v184, v236\n"
      "v_sub_f32 v185, v185, v236\n"
      "v_sub_f32 v186, v186, v236\n"
      "v_sub_f32 v187, v187, v236\n"
      "v_sub_f32 v164, v164, v236\n"
      "v_sub_f32 v165, v165, v236\n"
      "v_sub_f32 v166, v166, v236\n"
      "v_sub_f32 v167, v167, v236\n"
      "v_sub_f32 v136, v136, v236\n"
      "v_sub_f32 v137, v137, v236\n"
      "v_sub_f32 v138, v138, v236\n"
      "v_sub_f32 v139, v139, v236\n"
      "v_sub_f32 v238, v238, v236\n"
      "v_exp_f32 v184, v184\n"
      "v_exp_f32 v185, v185\n"
      "v_exp_f32 v186, v186\n"
      "v_cvt_pk_bf16_f32 v184, v184, v185\n"
      "v_exp_f32 v187, v187\n"
      "v_exp_f32 v164, v164\n"
      "v_cvt_pk_bf16_f32 v185, v186, v187\n"
      "v_exp_f32 v165, v165\n"
      "v_exp_f32 v166, v166\n"
      "v_cvt_pk_bf16_f32 v186, v164, v165\n"
      "v_exp_f32 v167, v167\n"
      "v_exp_f32 v136, v136\n"
      "ds_write_b64 v125, v[212:213] offset:18432\n"
      "v_cvt_pk_bf16_f32 v187, v166, v167\n"
      "v_exp_f32 v137, v137\n"
      "v_exp_f32 v138, v138\n"
      "v_exp_f32 v139, v139\n"
      "v_exp_f32 v238, v238\n"
      "v_cvt_pk_bf16_f32 v62, v136, v137\n"
      "v_cvt_pk_bf16_f32 v63, v138, v139\n"
      "v_cvt_pk_bf16_f32 v64, v238, 0\n"
      "v_rcp_f32 v196, v196\n"
      "v_mfma_f32_16x16x32_bf16 v[136:139], v[66:69], v[184:187], 0\n"
      "global_store_dwordx4 v117, v[140:143], s[54:55] sc1\n"
      "v_mfma_f32_16x16x32_bf16 v[136:139], v[66:69], v[62:65], v[136:139]\n"
      "ds_read_b128 v[220:223], v123 offset:256\n"
      "v_mul_f32 v200, v196, v200\n"
      "v_mfma_f32_16x16x32_bf16 v[140:143], v[204:207], v[184:187], 0\n"
      "ds_read_b128 v[164:167], v123 offset:3328\n"
      "v_mul_f32 v201, v196, v201\n"
      "v_mfma_f32_16x16x32_bf16 v[140:143], v[50:53], v[62:65], v[140:143]\n"
      "ds_read_b128 v[212:215], v123 offset:6400\n"
      "v_cvt_pk_bf16_f32 v200, v200, v201\n"
      "v_mfma_f32_16x16x32_bf16 v[152:155], v[224:227], v[184:187], 0\n"
      "ds_read_b128 v[144:147], v121\n"
      "v_mul_f32 v202, v196, v202\n"
      "v_mfma_f32_16x16x32_bf16 v[152:155], v[54:57], v[62:65], v[152:155]\n"
      "ds_read_b128 v[228:231], v123 offset:9472\n"
      "v_mul_f32 v203, v196, v203\n"
      "v_mfma_f32_16x16x32_bf16 v[156:159], v[160:163], v[184:187], 0\n"
      "ds_read_b128 v[240:243], v123 offset:12544\n"
      "v_cvt_pk_bf16_f32 v201, v202, v203\n"
      "v_mfma_f32_16x16x32_bf16 v[156:159], v[58:61], v[62:65], v[156:159]\n"
      "v_mul_f32 v248, v196, v248\n"
      "v_mul_f32 v249, v196, v249\n"
      "v_cvt_pk_bf16_f32 v202, v248, v249\n"
      "v_mul_f32 v250, v196, v250\n"
      "v_mul_f32 v251, v196, v251\n"
      "v_cvt_pk_bf16_f32 v203, v250, v251\n"
      "v_mul_f32 v188, v196, v188\n"
      "v_mul_f32 v189, v196, v189\n"
      "v_cvt_pk_bf16_f32 v188, v188, v189\n"
      "v_mul_f32 v190, v196, v190\n"
      "v_mul_f32 v191, v196, v191\n"
      "v_cvt_pk_bf16_f32 v189, v190, v191\n"
      "s_waitcnt lgkmcnt(4)\n"
      "v_mfma_f32_16x16x32_bf16 v[180:183], a[216:219], v[220:223], 0\n"
      "ds_read_b128 v[196:199], v123 offset:15616\n"
      "v_cndmask_b32 v149, v149, v237, s[42:43]\n"
      "v_mfma_f32_16x16x32_bf16 v[180:183], a[228:231], v[164:167], v[180:183]\n"
      "ds_write_b128 v124, v[200:203] offset:18688\n"
      "s_waitcnt lgkmcnt(3)\n"
      "v_mfma_f32_16x16x32_bf16 v[184:187], a[220:223], v[220:223], 0\n"
      "ds_read_b128 v[248:251], v123 offset:512\n"
      "v_cndmask_b32 v149, v149, v239, s[44:45]\n"
      "v_mfma_f32_16x16x32_bf16 v[184:187], a[232:235], v[164:167], v[184:187]\n"
      "ds_write_b64 v125, v[188:189] offset:18688\n"
      "v_mfma_f32_16x16x32_bf16 v[188:191], a[224:227], v[220:223], 0\n"
      "ds_read_b128 v[220:223], v123 offset:3584\n"
      "v_cndmask_b32 v149, v149, v193, s[46:47]\n"
      "v_mfma_f32_16x16x32_bf16 v[188:191], a[236:239], v[164:167], v[188:191]\n"
      "ds_read_b128 v[236:239], v123 offset:6656\n"
      "v_max_f32_dpp v193, v149, v149 row_ror:8 row_mask:0xf bank_mask:0xf\n"
      "v_mfma_f32_16x16x32_bf16 v[180:183], a[240:243], v[212:215], v[180:183]\n"
      "ds_read_b128 v[164:167], v123 offset:9728\n"
      "v_max_f32_dpp v193, v193, v193 row_ror:4 row_mask:0xf bank_mask:0xf\n"
      "v_mfma_f32_16x16x32_bf16 v[180:183], a[252:255], v[228:231], v[180:183]\n"
      "s_waitcnt lgkmcnt(6)\n"
      "v_max_f32_dpp v193, v193, v193 row_ror:2 row_mask:0xf bank_mask:0xf\n"
      "v_mfma_f32_16x16x32_bf16 v[184:187], a[244:247], v[212:215], v[184:187]\n"
      "s_nop 0\n"
      "v_max_f32_dpp v193, v193, v193 row_ror:1 row_mask:0xf bank_mask:0xf\n"
      "v_mfma_f32_16x16x32_bf16 v[184:187], v[0:3], v[228:231], v[184:187]\n"
      "s_mov_b32 exec_hi, 0\n"
      "global_store_dwordx4 v118, v[144:147], s[54:55] sc1\n"
      "s_mov_b32 exec_hi, -1\n"
      "v_mfma_f32_16x16x32_bf16 v[188:191], a[248:251], v[212:215], v[188:191]\n"
      "ds_read_b128 v[212:215], v123 offset:12800\n"
      "v_max_f32_dpp v193, v193, v193 row_bcast:15 row_mask:0xa bank_mask:0xf\n"
      "v_mfma_f32_16x16x32_bf16 v[188:191], v[4:7], v[228:231], v[188:191]\n"
      "ds_read_b128 v[228:231], v123 offset:15872\n"
      "v_max_f32_dpp v193, v193, v193 row_bcast:31 row_mask:0xc bank_mask:0xf\n"
      "s_waitcnt lgkmcnt(4)\n"
      "v_mfma_f32_16x16x32_bf16 v[180:183], v[8:11], v[240:243], v[180:183]\n"
      "s_add_u32 s28, s27, 2\n"
      "v_readlane_b32 s31, v193, 63\n"
      "s_min_u32 m0, s28, s3\n"
      "v_mfma_f32_16x16x32_bf16 v[180:183], v[20:23], v[196:199], v[180:183]\n"
      "s_movrels_b32 s9, s80\n"
      "v_subrev_f32 v149, s31, v149\n"
      "v_exp_f32 v149, v149\n"
      "v_mfma_f32_16x16x32_bf16 v[184:187], v[12:15], v[240:243], v[184:187]\n"
      "s_waitcnt lgkmcnt(2)\n"
      "v_cvt_pk_bf16_f32 v149, v149, v149\n"
      "s_lshr_b32 s8, s9, 8\n"
      "v_mfma_f32_16x16x32_bf16 v[184:187], v[24:27], v[196:199], v[184:187]\n"
      "ds_write_b16 v130, v149\n"
      "v_mfma_f32_16x16x32_bf16 v[188:191], v[16:19], v[240:243], v[188:191]\n"
      "ds_read_b128 v[240:243], v131\n"
      "v_fma_f32 v149, v180, v180, s39\n"
      "v_mfma_f32_16x16x32_bf16 v[188:191], v[28:31], v[196:199], v[188:191]\n"
      "ds_read_b128 v[144:147], v131 offset:64\n"
      "v_fmac_f32 v149, v181, v181\n"
      "v_mfma_f32_16x16x32_bf16 v[192:195], a[216:219], v[248:251], 0\n"
      "s_and_b32 s9, s9, 0xff\n"
      "v_fmac_f32 v149, v182, v182\n"
      "v_fmac_f32 v149, v183, v183\n"
      "v_mfma_f32_16x16x32_bf16 v[192:195], a[228:231], v[220:223], v[192:195]\n"
      "s_waitcnt lgkmcnt(3)\n"
      "v_fmac_f32 v149, v184, v184\n"
      "v_fmac_f32 v149, v185, v185\n"
      "v_mfma_f32_16x16x32_bf16 v[196:199], a[220:223], v[248:251], 0\n"
      "s_mul_i32 s10, s8, 73\n"
      "v_fmac_f32 v149, v186, v186\n"
      "v_fmac_f32 v149, v187, v187\n"
      "v_mfma_f32_16x16x32_bf16 v[196:199], a[232:235], v[220:223], v[196:199]\n"
      "s_add_u32 s10, s10, s9\n"
      "v_fmac_f32 v149, v188, v188\n"
      "v_fmac_f32_e64 v149, v189, v189\n"
      "v_mfma_f32_16x16x32_bf16 v[200:203], a[224:227], v[248:251], 0\n"
      "s_mul_i32 s28, s10, 4802\n"
      "v_fmac_f32_e64 v149, v190, v190\n"
      "v_fmac_f32 v149, v191, v191\n"
      "v_mfma_f32_16x16x32_bf16 v[200:203], a[236:239], v[220:223], v[200:203]\n"
      "s_add_u32 s56, s18, s28\n"
      "s_addc_u32 s57, s19, 0\n"
      "v_mfma_f32_16x16x32_bf16 v[192:195], a[240:243], v[236:239], v[192:195]\n"
      "s_and_b32 s56, s56, -16\n"
      "s_sub_u32 s58, s56, 110592\n"
      "s_subb_u32 s59, s57, 0\n"
      "v_mfma_f32_16x16x32_bf16 v[192:195], a[252:255], v[164:167], v[192:195]\n"
      "s_waitcnt lgkmcnt(0)\n"
      "s_sub_u32 s56, s56, 73728\n"
      "v_mfma_f32_16x16x32_bf16 v[196:199], a[244:247], v[236:239], v[196:199]\n"
      "s_subb_u32 s57, s57, 0\n"
      "v_permlane16_swap_b32 v151, v149\n"
      "v_mfma_f32_16x16x32_bf16 v[196:199], v[0:3], v[164:167], v[196:199]\n"
      "s_mul_i32 s28, s8, 1373568\n"
      "v_add_f32 v151, v151, v149\n"
      "v_mfma_f32_16x16x32_bf16 v[200:203], a[248:251], v[236:239], v[200:203]\n"
      "s_mul_i32 s29, s9, 2688\n"
      "s_add_u32 s28, s28, s29\n"
      "s_cmp_eq_u32 s8, 72\n"
      "v_mfma_f32_16x16x32_bf16 v[200:203], v[4:7], v[164:167], v[200:203]\n"
      "s_cselect_b32 s8, 0, 100270464\n"
      "s_cmp_eq_u32 s9, 72\n"
      "v_mfma_f32_16x16x32_bf16 v[192:195], v[8:11], v[212:215], v[192:195]\n"
      "s_cselect_b32 s9, 0, 196224\n"
      "s_add_u32 s10, s8, s9\n"
      "s_add_u32 s28, s28, s10\n"
      "v_mfma_f32_16x16x32_bf16 v[192:195], v[20:23], v[228:231], v[192:195]\n"
      "s_add_u32 s52, s16, s28\n"
      "s_addc_u32 s53, s17, 0\n"
      "v_mfma_f32_16x16x32_bf16 v[196:199], v[12:15], v[212:215], v[196:199]\n"
      "s_add_u32 s27, s27, 1\n"
      "s_cmp_ge_u32 s27, s26\n"
      "s_cselect_b32 s28, 1, 0\n"
      "v_mfma_f32_16x16x32_bf16 v[196:199], v[24:27], v[228:231], v[196:199]\n"
      "ds_read_b128 v[220:223], v114\n"
      "v_fma_f32 v236, v192, v192, s39\n"
      "v_mfma_f32_16x16x32_bf16 v[200:203], v[16:19], v[212:215], v[200:203]\n"
      "ds_read_b128 v[248:251], v115\n"
      "v_fmac_f32 v236, v193, v193\n"
      "v_mfma_f32_16x16x32_bf16 v[200:203], v[28:31], v[228:231], v[200:203]\n"
      "s_xor_b32 s29, s10, s11\n"
      "v_fmac_f32 v236, v194, v194\n"
      "v_fmac_f32 v236, v195, v195\n"
      "v_mfma_f32_16x16x32_bf16 v[228:231], v[240:243], v[66:69], 0\n"
      "s_or_b32 s28, s28, s29\n"
      "v_fmac_f32 v236, v196, v196\n"
      "v_fmac_f32 v236, v197, v197\n"
      "v_mfma_f32_16x16x32_bf16 v[228:231], v[144:147], v[66:69], v[228:231]\n"
      "v_fmac_f32 v236, v198, v198\n"
      "v_fmac_f32 v236, v199, v199\n"
      "v_mfma_f32_16x16x32_bf16 v[212:215], v[240:243], v[204:207], 0\n"
      "v_fmac_f32 v236, v200, v200\n"
      "v_fmac_f32 v236, v201, v201\n"
      "v_mfma_f32_16x16x32_bf16 v[212:215], v[144:147], v[50:53], v[212:215]\n"
      "v_fmac_f32 v236, v202, v202\n"
      "v_fmac_f32 v236, v203, v203\n"
      "v_mfma_f32_16x16x32_bf16 v[204:207], v[240:243], v[224:227], 0\n"
      "v_mov_b32 v237, v236\n"
      "v_mov_b32_dpp v228, v228 quad_perm:[0,0,0,0] row_mask:0xf bank_mask:0xf\n"
      "v_mfma_f32_16x16x32_bf16 v[204:207], v[144:147], v[54:57], v[204:207]\n"
      "v_permlane16_swap_b32 v236, v237\n"
      "v_mfma_f32_16x16x32_bf16 v[224:227], v[240:243], v[160:163], 0\n"
      "s_waitcnt lgkmcnt(0)\n"
      "v_add_f32 v236, v236, v237\n"
      "v_mfma_f32_16x16x32_bf16 v[224:227], v[144:147], v[58:61], v[224:227]\n"
      "s_nop 0\n"
      "v_permlane32_swap_b32 v151, v236\n"
      "v_mfma_f32_16x16x32_bf16 v[160:163], a[0:3], v[220:223], 0\n"
      "v_add_f32_e64 v151, v151, v236\n"
      "v_cndmask_b32 v212, v212, v204, s[42:43]\n"
      "v_mfma_f32_16x16x32_bf16 v[160:163], a[36:39], v[248:251], v[160:163]\n"
      "ds_write_b32 v129, v151 offset:1024\n"
      "v_mfma_f32_16x16x32_bf16 v[236:239], a[4:7], v[220:223], 0\n"
      "v_cndmask_b32 v212, v212, v224, s[44:45]\n"
      "v_mfma_f32_16x16x32_bf16 v[236:239], a[40:43], v[248:251], v[236:239]\n"
      "v_rcp_f32 v228, v228\n"
      "s_nop 0\n"
      "v_mul_f32 v212, v228, v212\n"
      "v_cvt_pk_bf16_f32 v212, v212, v212\n"
      "ds_write_b16 v135, v212 offset:16\n"
      "s_branch L_join_21_%=\n"
      "L_flg_76_%=:\n"
      "v_cmp_gt_i32 vcc, 0, v188\n"
      "s_cbranch_vccz L_fb_77_%=\n"
      "s_sleep 1\n"
      "ds_read_b128 v[188:191], v128 offset:2048\n"
      "s_waitcnt lgkmcnt(0)\n"
      "v_bitop3_b32 v188, v188, v189, s4 bitop3:0x7e\n"
      "v_bitop3_b32 v190, v190, v191, s4 bitop3:0x7e\n"
      "v_or_b32_e64 v188, v188, v190\n"
      "v_cmp_ne_u32 s[48:49], 0, v188\n"
      "s_branch L_flg_76_%=\n"
      "L_gen_20_%=:\n"
      "s_waitcnt lgkmcnt(0)\n"
      "s_nop 7\n"
      "s_nop 7\n"
      "v_mbcnt_lo_u32_b32 v223, -1, 0\n"
      "v_mbcnt_hi_u32_b32 v223, -1, v223\n"
      "v_and_b32 v152, 15, v223\n"
      "v_lshrrev_b32 v220, 4, v223\n"
      "v_mul_u32_u24 v221, 0x62, v152\n"
      "v_lshl_add_u32 v221, v220, 3, v221\n"
      "v_add_u32 v221, s30, v221\n"
      "v_and_b32 v223, -4, v221\n"
      "v_and_b32 v221, 2, v221\n"
      "v_lshlrev_b32 v220, 2, v220\n"
      "v_sub_u32 v152, v152, v220\n"
      "v_and_b32 v220, 1, v152\n"
      "v_lshlrev_b32_e64 v220, 4, v220\n"
      "v_lshrrev_b32_e64 v152, 1, v152\n"
      "v_cmp_eq_u32 vcc, 0, v152\n"
      "s_movk_i32 s31, 0x3fb9\n"
      "v_lshlrev_b32 v220, v220, s31\n"
      "ds_read2_b32 v[188:189], v223 offset0:0 offset1:1\n"
      "ds_read2_b32 v[190:191], v223 offset0:8 offset1:9\n"
      "ds_read2_b32 v[236:237], v223 offset0:16 offset1:17\n"
      "ds_read2_b32 v[248:249], v223 offset0:2 offset1:10\n"
      "ds_read2_b32 v[154:155], v223 offset0:18 offset1:24\n"
      "v_add_u32 v223, 1568, v223\n"
      "ds_read2_b32 v[238:239], v223 offset0:0 offset1:1\n"
      "ds_read2_b32 v[218:219], v223 offset0:8 offset1:9\n"
      "ds_read2_b32 v[144:145], v223 offset0:16 offset1:17\n"
      "ds_read2_b32 v[210:211], v223 offset0:2 offset1:10\n"
      "ds_read2_b32 v[216:217], v223 offset0:18 offset1:24\n"
      "v_add_u32 v223, 1568, v223\n"
      "ds_read2_b32 v[250:251], v223 offset0:0 offset1:1\n"
      "ds_read2_b32 v[242:243], v223 offset0:8 offset1:9\n"
      "ds_read2_b32 v[146:147], v223 offset0:16 offset1:17\n"
      "ds_read2_b32 v[244:245], v223 offset0:2 offset1:10\n"
      "ds_read2_b32 v[208:209], v223 offset0:18 offset1:24\n"
      "v_cndmask_b32 v240, 0, v220, vcc\n"
      "v_cmp_eq_u32 vcc, 1, v152\n"
      "s_waitcnt lgkmcnt(13)\n"
      "v_alignbyte_b32 v188, v189, v188, v221\n"
      "v_cndmask_b32 v241, 0, v220, vcc\n"
      "v_mfma_f32_16x16x32_bf16 v[232:235], v[212:215], v[180:183], v[90:93]\n"
      "s_waitcnt lgkmcnt(11)\n"
      "v_alignbyte_b32 v189, v248, v189, v221\n"
      "v_mfma_f32_16x16x32_bf16 v[232:235], v[34:37], v[38:41], v[232:235]\n"
      "v_alignbyte_b32 v190, v191, v190, v221\n"
      "v_alignbyte_b32 v191, v249, v191, v221\n"
      "v_mfma_f32_16x16x32_bf16 v[246:249], v[136:139], v[180:183], v[94:97]\n"
      "s_waitcnt lgkmcnt(8)\n"
      "v_alignbyte_b32 v236, v237, v236, v221\n"
      "v_alignbyte_b32 v237, v154, v237, v221\n"
      "v_mfma_f32_16x16x32_bf16 v[246:249], v[40:43], v[38:41], v[246:249]\n"
      "v_alignbyte_b32 v155, v155, v155, v221\n"
      "v_lshlrev_b32_e64 v155, 16, v155\n"
      "v_mfma_f32_16x16x32_bf16 v[252:255], v[140:143], v[180:183], v[72:75]\n"
      "s_waitcnt lgkmcnt(5)\n"
      "v_fmac_f32 v148, s37, v155\n"
      "v_alignbyte_b32 v238, v239, v238, v221\n"
      "v_mfma_f32_16x16x32_bf16 v[252:255], v[228:231], v[136:139], v[252:255]\n"
      "v_alignbyte_b32 v239, v210, v239, v221\n"
      "v_alignbyte_b32 v218, v219, v218, v221\n"
      "v_mfma_f32_16x16x32_bf16 v[252:255], v[156:159], v[38:41], v[252:255]\n"
      "v_alignbyte_b32 v219, v211, v219, v221\n"
      "v_alignbyte_b32 v144, v145, v144, v221\n"
      "v_mfma_f32_16x16x32_bf16 v[152:155], v[200:203], v[180:183], v[98:101]\n"
      "v_alignbyte_b32 v145, v216, v145, v221\n"
      "v_alignbyte_b32 v217, v217, v217, v221\n"
      "v_mfma_f32_16x16x32_bf16 v[152:155], v[46:49], v[38:41], v[152:155]\n"
      "v_lshlrev_b32_e64 v217, 16, v217\n"
      "v_mfma_f32_16x16x16_bf16 v[164:167], v[240:241], v[188:189], v[164:167]\n"
      "v_mfma_f32_16x16x16_bf16 v[184:187], v[240:241], v[190:191], v[184:187]\n"
      "s_nop 0\n"
      "v_mfma_f32_16x16x16_bf16 v[192:195], v[240:241], v[236:237], v[192:195]\n"
      "ds_read_b128 v[188:191], v128 offset:2048\n"
      "s_nop 4\n"
      "v_max3_f32 v236, v164, v165, v166\n"
      "v_max3_f32 v222, v167, v184, v185\n"
      "v_mfma_f32_16x16x16_bf16 v[232:235], v[240:241], v[238:239], v[232:235]\n"
      "v_max3_f32 v236, v236, v186, v187\n"
      "v_max3_f32 v222, v222, v192, v193\n"
      "v_mfma_f32_16x16x16_bf16 v[246:249], v[240:241], v[218:219], v[246:249]\n"
      "v_max3_f32 v236, v236, v194, v195\n"
      "v_max3_f32 v236, v236, v222, v148\n"
      "v_mfma_f32_16x16x16_bf16 v[152:155], v[240:241], v[144:145], v[152:155]\n"
      "v_mov_b32 v222, v236\n"
      "v_fmac_f32 v252, s37, v217\n"
      "s_nop 0\n"
      "v_permlane16_swap_b32 v236, v222\n"
      "v_max_f32 v236, v236, v222\n"
      "v_mov_b32_e64 v222, v236\n"
      "s_waitcnt lgkmcnt(1)\n"
      "v_alignbyte_b32 v250, v251, v250, v221\n"
      "v_permlane32_swap_b32 v236, v222\n"
      "v_max_f32 v236, v236, v222\n"
      "v_sub_f32 v164, v164, v236\n"
      "v_sub_f32 v165, v165, v236\n"
      "v_sub_f32 v166, v166, v236\n"
      "v_sub_f32 v167, v167, v236\n"
      "v_sub_f32 v184, v184, v236\n"
      "v_sub_f32 v185, v185, v236\n"
      "v_sub_f32 v186, v186, v236\n"
      "v_sub_f32 v187, v187, v236\n"
      "v_sub_f32 v192, v192, v236\n"
      "v_sub_f32 v193, v193, v236\n"
      "v_sub_f32 v194, v194, v236\n"
      "v_sub_f32 v195, v195, v236\n"
      "v_sub_f32 v148, v148, v236\n"
      "v_exp_f32 v164, v164\n"
      "v_exp_f32 v165, v165\n"
      "v_exp_f32 v166, v166\n"
      "v_cvt_pk_bf16_f32 v164, v164, v165\n"
      "v_exp_f32 v167, v167\n"
      "v_exp_f32 v184, v184\n"
      "v_cvt_pk_bf16_f32 v165, v166, v167\n"
      "v_exp_f32 v185, v185\n"
      "v_exp_f32 v186, v186\n"
      "v_cvt_pk_bf16_f32 v166, v184, v185\n"
      "v_exp_f32 v187, v187\n"
      "v_exp_f32 v192, v192\n"
      "v_cvt_pk_bf16_f32 v167, v186, v187\n"
      "v_exp_f32 v193, v193\n"
      "v_exp_f32 v194, v194\n"
      "v_exp_f32 v195, v195\n"
      "v_exp_f32 v148, v148\n"
      "v_cvt_pk_bf16_f32 v62, v192, v193\n"
      "v_cvt_pk_bf16_f32 v63, v194, v195\n"
      "v_cvt_pk_bf16_f32 v64, v148, 0\n"
      "v_max3_f32 v222, v232, v233, v234\n"
      "v_max3_f32 v220, v235, v246, v247\n"
      "v_max3_f32 v222, v222, v248, v249\n"
      "v_max3_f32 v220, v220, v152, v153\n"
      "v_max3_f32 v222, v222, v154, v155\n"
      "v_max3_f32 v222, v222, v220, v252\n"
      "v_mov_b32 v220, v222\n"
      "v_alignbyte_b32 v251, v244, v251, v221\n"
      "s_nop 0\n"
      "v_permlane16_swap_b32 v222, v220\n"
      "v_max_f32 v222, v222, v220\n"
      "v_mov_b32_e64 v220, v222\n"
      "v_alignbyte_b32 v242, v243, v242, v221\n"
      "v_alignbyte_b32 v243, v245, v243, v221\n"
      "v_permlane32_swap_b32 v222, v220\n"
      "v_max_f32 v222, v222, v220\n"
      "v_sub_f32 v232, v232, v222\n"
      "v_sub_f32 v233, v233, v222\n"
      "v_sub_f32 v234, v234, v222\n"
      "v_sub_f32 v235, v235, v222\n"
      "v_sub_f32 v246, v246, v222\n"
      "v_sub_f32 v247, v247, v222\n"
      "v_sub_f32 v248, v248, v222\n"
      "v_sub_f32 v249, v249, v222\n"
      "v_alignbyte_b32 v146, v147, v146, v221\n"
      "v_sub_f32 v152, v152, v222\n"
      "v_sub_f32_e64 v153, v153, v222\n"
      "v_alignbyte_b32 v147, v208, v147, v221\n"
      "v_mfma_f32_16x16x32_bf16 v[184:187], v[66:69], v[164:167], 0\n"
      "s_waitcnt lgkmcnt(0)\n"
      "v_alignbyte_b32 v209, v209, v209, v221\n"
      "v_lshlrev_b32_e64 v209, 16, v209\n"
      "v_mfma_f32_16x16x32_bf16 v[184:187], v[66:69], v[62:65], v[184:187]\n"
      "v_bitop3_b32 v188, v188, v189, s4 bitop3:0x7e\n"
      "v_bitop3_b32 v190, v190, v191, s4 bitop3:0x7e\n"
      "v_mfma_f32_16x16x32_bf16 v[180:183], v[204:207], v[164:167], 0\n"
      "v_or_b32 v188, v188, v190\n"
      "v_cmp_ne_u32 vcc, 0, v188\n"
      "v_mfma_f32_16x16x32_bf16 v[180:183], v[50:53], v[62:65], v[180:183]\n"
      "s_mov_b64 s[48:49], vcc\n"
      "v_sub_f32 v154, v154, v222\n"
      "v_sub_f32 v155, v155, v222\n"
      "v_sub_f32 v252, v252, v222\n"
      "v_exp_f32 v232, v232\n"
      "v_exp_f32 v233, v233\n"
      "v_exp_f32 v234, v234\n"
      "s_cbranch_vccnz L_flg_82_%=\n"
      "L_fb_83_%=:\n"
      "v_mfma_f32_16x16x32_bf16 v[236:239], v[224:227], v[164:167], 0\n"
      "v_cvt_pk_bf16_f32 v232, v232, v233\n"
      "v_mfma_f32_16x16x32_bf16 v[236:239], v[54:57], v[62:65], v[236:239]\n"
      "ds_read_b128 v[188:191], v119 offset:12288\n"
      "v_exp_f32 v235, v235\n"
      "v_mfma_f32_16x16x32_bf16 v[192:195], v[160:163], v[164:167], 0\n"
      "v_exp_f32 v246, v246\n"
      "v_mfma_f32_16x16x32_bf16 v[192:195], v[58:61], v[62:65], v[192:195]\n"
      "v_cvt_pk_bf16_f32 v233, v234, v235\n"
      "v_exp_f32 v247, v247\n"
      "v_mfma_f32_16x16x32_bf16 v[218:221], v[212:215], v[196:199], v[102:105]\n"
      "v_exp_f32 v248, v248\n"
      "v_mfma_f32_16x16x32_bf16 v[218:221], v[34:37], v[44:47], v[218:221]\n"
      "v_cvt_pk_bf16_f32 v234, v246, v247\n"
      "v_exp_f32 v249, v249\n"
      "v_mfma_f32_16x16x32_bf16 v[212:215], v[136:139], v[196:199], v[106:109]\n"
      "v_exp_f32 v152, v152\n"
      "v_mfma_f32_16x16x32_bf16 v[212:215], v[40:43], v[44:47], v[212:215]\n"
      "v_cvt_pk_bf16_f32 v235, v248, v249\n"
      "v_exp_f32 v153, v153\n"
      "v_mfma_f32_16x16x16_bf16 v[218:221], v[240:241], v[250:251], v[218:221]\n"
      "s_waitcnt lgkmcnt(0)\n"
      "v_exp_f32 v154, v154\n"
      "v_exp_f32 v155, v155\n"
      "v_mfma_f32_16x16x16_bf16 v[212:215], v[240:241], v[242:243], v[212:215]\n"
      "global_store_dwordx4 v116, v[188:191], s[54:55] offset:256 sc1\n"
      "v_mfma_f32_16x16x32_bf16 v[248:251], v[140:143], v[196:199], v[74:77]\n"
      "v_exp_f32_e64 v252, v252\n"
      "v_mfma_f32_16x16x32_bf16 v[248:251], v[228:231], v[200:203], v[248:251]\n"
      "v_cvt_pk_bf16_f32 v62, v152, v153\n"
      "v_cvt_pk_bf16_f32 v63, v154, v155\n"
      "v_mfma_f32_16x16x32_bf16 v[248:251], v[156:159], v[44:47], v[248:251]\n"
      "v_cvt_pk_bf16_f32 v64, v252, 0\n"
      "v_rcp_f32 v184, v184\n"
      "v_mfma_f32_16x16x32_bf16 v[136:139], v[200:203], v[196:199], v[110:113]\n"
      "v_mul_f32 v180, v184, v180\n"
      "v_mul_f32_e64 v181, v184, v181\n"
      "v_mfma_f32_16x16x32_bf16 v[136:139], v[46:49], v[44:47], v[136:139]\n"
      "v_cvt_pk_bf16_f32 v180, v180, v181\n"
      "v_mul_f32 v182, v184, v182\n"
      "v_mfma_f32_16x16x32_bf16 v[188:191], v[228:231], v[140:143], v[76:79]\n"
      "ds_read_b128 v[140:143], v120\n"
      "v_fmac_f32 v248, s37, v209\n"
      "v_mul_f32 v183, v184, v183\n"
      "v_mfma_f32_16x16x16_bf16 v[136:139], v[240:241], v[146:147], v[136:139]\n"
      "v_cvt_pk_bf16_f32 v181, v182, v183\n"
      "v_mul_f32 v236, v184, v236\n"
      "v_mfma_f32_16x16x16_bf16 v[188:191], v[158:159], v[156:157], v[188:191]\n"
      "v_mul_f32 v237, v184, v237\n"
      "v_cvt_pk_bf16_f32 v182, v236, v237\n"
      "v_mfma_f32_16x16x32_bf16 v[200:203], v[66:69], v[232:235], 0\n"
      "v_mul_f32 v238, v184, v238\n"
      "v_mul_f32_e64 v239, v184, v239\n"
      "v_mfma_f32_16x16x32_bf16 v[200:203], v[66:69], v[62:65], v[200:203]\n"
      "v_cvt_pk_bf16_f32 v183, v238, v239\n"
      "v_mul_f32 v192, v184, v192\n"
      "v_mfma_f32_16x16x32_bf16 v[196:199], v[204:207], v[232:235], 0\n"
      "v_mul_f32 v193, v184, v193\n"
      "v_cvt_pk_bf16_f32 v192, v192, v193\n"
      "v_mfma_f32_16x16x32_bf16 v[196:199], v[50:53], v[62:65], v[196:199]\n"
      "ds_write_b128 v124, v[180:183] offset:18432\n"
      "v_mfma_f32_16x16x32_bf16 v[180:183], v[224:227], v[232:235], 0\n"
      "s_waitcnt lgkmcnt(1)\n"
      "v_mul_f32 v194, v184, v194\n"
      "v_mfma_f32_16x16x32_bf16 v[180:183], v[54:57], v[62:65], v[180:183]\n"
      "global_store_dwordx4 v117, v[140:143], s[54:55] sc1\n"
      "v_mfma_f32_16x16x32_bf16 v[236:239], v[160:163], v[232:235], 0\n"
      "v_mul_f32_e64 v195, v184, v195\n"
      "v_cvt_pk_bf16_f32 v193, v194, v195\n"
      "v_mfma_f32_16x16x32_bf16 v[236:239], v[58:61], v[62:65], v[236:239]\n"
      "ds_write_b64 v125, v[192:193] offset:18432\n"
      "v_max3_f32 v250, v218, v219, v220\n"
      "v_max3_f32 v222, v221, v212, v213\n"
      "v_max3_f32 v250, v250, v214, v215\n"
      "v_max3_f32 v222, v222, v136, v137\n"
      "v_max3_f32 v250, v250, v138, v139\n"
      "v_max3_f32 v250, v250, v222, v248\n"
      "v_mov_b32 v222, v250\n"
      "s_nop 1\n"
      "v_permlane16_swap_b32 v250, v222\n"
      "v_max_f32 v250, v250, v222\n"
      "v_mov_b32 v222, v250\n"
      "v_rcp_f32 v200, v200\n"
      "s_nop 0\n"
      "v_permlane32_swap_b32 v250, v222\n"
      "v_max_f32 v250, v250, v222\n"
      "v_sub_f32 v218, v218, v250\n"
      "v_sub_f32 v219, v219, v250\n"
      "v_sub_f32 v220, v220, v250\n"
      "v_sub_f32 v221, v221, v250\n"
      "v_sub_f32 v212, v212, v250\n"
      "v_sub_f32 v213, v213, v250\n"
      "v_sub_f32 v214, v214, v250\n"
      "v_sub_f32 v215, v215, v250\n"
      "v_sub_f32 v136, v136, v250\n"
      "v_sub_f32 v137, v137, v250\n"
      "v_sub_f32 v138, v138, v250\n"
      "v_sub_f32 v139, v139, v250\n"
      "v_sub_f32 v248, v248, v250\n"
      "v_exp_f32 v218, v218\n"
      "v_exp_f32 v219, v219\n"
      "v_exp_f32 v220, v220\n"
      "v_cvt_pk_bf16_f32 v218, v218, v219\n"
      "v_exp_f32 v221, v221\n"
      "v_exp_f32 v212, v212\n"
      "v_cvt_pk_bf16_f32 v219, v220, v221\n"
      "v_exp_f32 v213, v213\n"
      "v_exp_f32 v214, v214\n"
      "v_cvt_pk_bf16_f32 v220, v212, v213\n"
      "v_exp_f32 v215, v215\n"
      "v_exp_f32 v136, v136\n"
      "v_cvt_pk_bf16_f32 v221, v214, v215\n"
      "v_exp_f32 v137, v137\n"
      "v_exp_f32 v138, v138\n"
      "v_exp_f32 v139, v139\n"
      "v_exp_f32_e64 v248, v248\n"
      "v_cvt_pk_bf16_f32 v62, v136, v137\n"
      "v_cvt_pk_bf16_f32 v63, v138, v139\n"
      "v_cvt_pk_bf16_f32 v64, v248, 0\n"
      "v_mul_f32 v196, v200, v196\n"
      "v_mul_f32 v197, v200, v197\n"
      "v_mfma_f32_16x16x32_bf16 v[136:139], v[66:69], v[218:221], 0\n"
      "ds_read_b128 v[192:195], v123 offset:256\n"
      "v_cvt_pk_bf16_f32 v196, v196, v197\n"
      "v_mfma_f32_16x16x32_bf16 v[136:139], v[66:69], v[62:65], v[136:139]\n"
      "ds_read_b128 v[144:147], v123 offset:3328\n"
      "v_mul_f32 v198, v200, v198\n"
      "v_mfma_f32_16x16x32_bf16 v[140:143], v[204:207], v[218:221], 0\n"
      "ds_read_b128 v[240:243], v121\n"
      "v_mul_f32 v199, v200, v199\n"
      "v_mfma_f32_16x16x32_bf16 v[140:143], v[50:53], v[62:65], v[140:143]\n"
      "ds_read_b128 v[228:231], v123 offset:6400\n"
      "v_cvt_pk_bf16_f32 v197, v198, v199\n"
      "v_mfma_f32_16x16x32_bf16 v[152:155], v[224:227], v[218:221], 0\n"
      "ds_read_b128 v[232:235], v123 offset:9472\n"
      "v_mul_f32 v180, v200, v180\n"
      "v_mfma_f32_16x16x32_bf16 v[152:155], v[54:57], v[62:65], v[152:155]\n"
      "v_mul_f32 v181, v200, v181\n"
      "v_cvt_pk_bf16_f32 v198, v180, v181\n"
      "v_mfma_f32_16x16x32_bf16 v[156:159], v[160:163], v[218:221], 0\n"
      "ds_read_b128 v[218:221], v123 offset:12544\n"
      "v_mul_f32 v182, v200, v182\n"
      "v_mfma_f32_16x16x32_bf16 v[156:159], v[58:61], v[62:65], v[156:159]\n"
      "v_mul_f32 v183, v200, v183\n"
      "v_cvt_pk_bf16_f32 v199, v182, v183\n"
      "v_mul_f32 v236, v200, v236\n"
      "v_mul_f32 v237, v200, v237\n"
      "v_cvt_pk_bf16_f32 v236, v236, v237\n"
      "v_mul_f32 v238, v200, v238\n"
      "v_mul_f32 v239, v200, v239\n"
      "v_cvt_pk_bf16_f32 v237, v238, v239\n"
      "s_waitcnt lgkmcnt(4)\n"
      "v_mfma_f32_16x16x32_bf16 v[180:183], a[216:219], v[192:195], 0\n"
      "ds_read_b128 v[200:203], v123 offset:15616\n"
      "v_cndmask_b32 v149, v149, v253, s[42:43]\n"
      "v_mfma_f32_16x16x32_bf16 v[180:183], a[228:231], v[144:147], v[180:183]\n"
      "ds_write_b128 v124, v[196:199] offset:18688\n"
      "s_waitcnt lgkmcnt(3)\n"
      "v_mfma_f32_16x16x32_bf16 v[184:187], a[220:223], v[192:195], 0\n"
      "v_cndmask_b32 v149, v149, v249, s[44:45]\n"
      "v_cndmask_b32 v149, v149, v189, s[46:47]\n"
      "v_mfma_f32_16x16x32_bf16 v[184:187], a[232:235], v[144:147], v[184:187]\n"
      "ds_write_b64 v125, v[236:237] offset:18688\n"
      "v_mfma_f32_16x16x32_bf16 v[188:191], a[224:227], v[192:195], 0\n"
      "ds_read_b128 v[248:251], v123 offset:512\n"
      "v_mbcnt_lo_u32_b32 v223, -1, 0\n"
      "v_mfma_f32_16x16x32_bf16 v[188:191], a[236:239], v[144:147], v[188:191]\n"
      "ds_read_b128 v[236:239], v123 offset:3584\n"
      "v_mbcnt_hi_u32_b32 v223, -1, v223\n"
      "v_mfma_f32_16x16x32_bf16 v[180:183], a[240:243], v[228:231], v[180:183]\n"
      "v_min_u32 v223, 48, v223\n"
      "v_lshlrev_b32 v223, 1, v223\n"
      "v_mfma_f32_16x16x32_bf16 v[180:183], a[252:255], v[232:235], v[180:183]\n"
      "s_waitcnt lgkmcnt(4)\n"
      "v_add_u32 v223, s30, v223\n"
      "v_mfma_f32_16x16x32_bf16 v[184:187], a[244:247], v[228:231], v[184:187]\n"
      "ds_read_u16 v222, v223 offset:4704\n"
      "v_mfma_f32_16x16x32_bf16 v[184:187], v[0:3], v[232:235], v[184:187]\n"
      "ds_read_b128 v[244:247], v123 offset:6656\n"
      "v_mfma_f32_16x16x32_bf16 v[188:191], a[248:251], v[228:231], v[188:191]\n"
      "ds_read_b128 v[212:215], v123 offset:9728\n"
      "v_mfma_f32_16x16x32_bf16 v[188:191], v[4:7], v[232:235], v[188:191]\n"
      "s_mov_b32 exec_hi, 0\n"
      "global_store_dwordx4 v118, v[240:243], s[54:55] sc1\n"
      "s_mov_b32 exec_hi, -1\n"
      "v_mfma_f32_16x16x32_bf16 v[180:183], v[8:11], v[218:221], v[180:183]\n"
      "s_add_u32 s28, s27, 2\n"
      "s_min_u32 m0, s28, s3\n"
      "v_mfma_f32_16x16x32_bf16 v[180:183], v[20:23], v[200:203], v[180:183]\n"
      "s_waitcnt lgkmcnt(3)\n"
      "s_movrels_b32 s9, s80\n"
      "v_mfma_f32_16x16x32_bf16 v[184:187], v[12:15], v[218:221], v[184:187]\n"
      "s_lshr_b32 s8, s9, 8\n"
      "s_and_b32 s9, s9, 0xff\n"
      "s_mul_i32 s10, s8, 73\n"
      "v_mfma_f32_16x16x32_bf16 v[184:187], v[24:27], v[200:203], v[184:187]\n"
      "s_add_u32 s10, s10, s9\n"
      "s_mul_i32 s28, s10, 4802\n"
      "v_mfma_f32_16x16x32_bf16 v[188:191], v[16:19], v[218:221], v[188:191]\n"
      "s_waitcnt lgkmcnt(2)\n"
      "v_lshlrev_b32 v222, 16, v222\n"
      "v_fmac_f32_e64 v149, s37, v222\n"
      "v_mfma_f32_16x16x32_bf16 v[188:191], v[28:31], v[200:203], v[188:191]\n"
      "ds_read_b128 v[164:167], v123 offset:12800\n"
      "v_max_f32_dpp v220, v149, v149 row_ror:8 row_mask:0xf bank_mask:0xf\n"
      "v_mfma_f32_16x16x32_bf16 v[192:195], a[216:219], v[248:251], 0\n"
      "ds_read_b128 v[240:243], v123 offset:15872\n"
      "v_max_f32_dpp v220, v220, v220 row_ror:4 row_mask:0xf bank_mask:0xf\n"
      "v_mfma_f32_16x16x32_bf16 v[192:195], a[228:231], v[236:239], v[192:195]\n"
      "s_waitcnt lgkmcnt(2)\n"
      "v_max_f32_dpp v220, v220, v220 row_ror:2 row_mask:0xf bank_mask:0xf\n"
      "v_fma_f32 v145, v180, v180, s39\n"
      "v_mfma_f32_16x16x32_bf16 v[196:199], a[220:223], v[248:251], 0\n"
      "s_add_u32 s56, s18, s28\n"
      "v_max_f32_dpp v220, v220, v220 row_ror:1 row_mask:0xf bank_mask:0xf\n"
      "v_fmac_f32 v145, v181, v181\n"
      "v_mfma_f32_16x16x32_bf16 v[196:199], a[232:235], v[236:239], v[196:199]\n"
      "s_addc_u32 s57, s19, 0\n"
      "v_max_f32_dpp v220, v220, v220 row_bcast:15 row_mask:0xa bank_mask:0xf\n"
      "v_fmac_f32_e64 v145, v182, v182\n"
      "v_mfma_f32_16x16x32_bf16 v[200:203], a[224:227], v[248:251], 0\n"
      "s_and_b32 s56, s56, -16\n"
      "v_max_f32_dpp v220, v220, v220 row_bcast:31 row_mask:0xc bank_mask:0xf\n"
      "v_fmac_f32 v145, v183, v183\n"
      "v_mfma_f32_16x16x32_bf16 v[200:203], a[236:239], v[236:239], v[200:203]\n"
      "s_sub_u32 s58, s56, 110592\n"
      "v_readlane_b32 s31, v220, 63\n"
      "v_fmac_f32_e64 v145, v184, v184\n"
      "v_mfma_f32_16x16x32_bf16 v[192:195], a[240:243], v[244:247], v[192:195]\n"
      "s_subb_u32 s59, s57, 0\n"
      "v_subrev_f32 v149, s31, v149\n"
      "v_fmac_f32_e64 v145, v185, v185\n"
      "v_mfma_f32_16x16x32_bf16 v[192:195], a[252:255], v[212:215], v[192:195]\n"
      "s_sub_u32 s56, s56, 73728\n"
      "v_exp_f32 v149, v149\n"
      "v_mfma_f32_16x16x32_bf16 v[196:199], a[244:247], v[244:247], v[196:199]\n"
      "s_waitcnt lgkmcnt(0)\n"
      "v_cvt_pk_bf16_f32 v149, v149, v149\n"
      "v_mfma_f32_16x16x32_bf16 v[196:199], v[0:3], v[212:215], v[196:199]\n"
      "ds_write_b16 v130, v149\n"
      "v_mfma_f32_16x16x32_bf16 v[200:203], a[248:251], v[244:247], v[200:203]\n"
      "ds_read_b128 v[228:231], v131\n"
      "v_fmac_f32_e64 v145, v186, v186\n"
      "v_mfma_f32_16x16x32_bf16 v[200:203], v[4:7], v[212:215], v[200:203]\n"
      "ds_read_b128 v[236:239], v131 offset:64\n"
      "v_fmac_f32 v145, v187, v187\n"
      "v_mfma_f32_16x16x32_bf16 v[192:195], v[8:11], v[164:167], v[192:195]\n"
      "s_subb_u32 s57, s57, 0\n"
      "v_fmac_f32 v145, v188, v188\n"
      "v_fmac_f32 v145, v189, v189\n"
      "v_mfma_f32_16x16x32_bf16 v[192:195], v[20:23], v[240:243], v[192:195]\n"
      "s_mul_i32 s28, s8, 1373568\n"
      "v_fmac_f32 v145, v190, v190\n"
      "v_fmac_f32 v145, v191, v191\n"
      "v_mfma_f32_16x16x32_bf16 v[196:199], v[12:15], v[164:167], v[196:199]\n"
      "s_mul_i32 s29, s9, 2688\n"
      "v_permlane16_swap_b32_e64 v151, v145\n"
      "v_mfma_f32_16x16x32_bf16 v[196:199], v[24:27], v[240:243], v[196:199]\n"
      "ds_read_b128 v[220:223], v114\n"
      "v_fma_f32 v148, v192, v192, s39\n"
      "v_mfma_f32_16x16x32_bf16 v[200:203], v[16:19], v[164:167], v[200:203]\n"
      "ds_read_b128 v[248:251], v115\n"
      "v_fmac_f32 v148, v193, v193\n"
      "v_mfma_f32_16x16x32_bf16 v[200:203], v[28:31], v[240:243], v[200:203]\n"
      "s_add_u32 s28, s28, s29\n"
      "v_fmac_f32 v148, v194, v194\n"
      "v_fmac_f32 v148, v195, v195\n"
      "v_fmac_f32 v148, v196, v196\n"
      "v_fmac_f32 v148, v197, v197\n"
      "v_fmac_f32 v148, v198, v198\n"
      "v_fmac_f32 v148, v199, v199\n"
      "v_add_f32 v151, v151, v145\n"
      "v_fmac_f32_e64 v148, v200, v200\n"
      "s_waitcnt lgkmcnt(2)\n"
      "v_mfma_f32_16x16x32_bf16 v[208:211], v[228:231], v[66:69], 0\n"
      "s_cmp_eq_u32 s8, 72\n"
      "v_fmac_f32 v148, v201, v201\n"
      "v_fmac_f32 v148, v202, v202\n"
      "v_mfma_f32_16x16x32_bf16 v[208:211], v[236:239], v[66:69], v[208:211]\n"
      "s_cselect_b32 s8, 0, 100270464\n"
      "v_fmac_f32 v148, v203, v203\n"
      "v_mov_b32 v245, v148\n"
      "v_mfma_f32_16x16x32_bf16 v[144:147], v[228:231], v[204:207], 0\n"
      "s_cmp_eq_u32 s9, 72\n"
      "v_permlane16_swap_b32_e64 v148, v245\n"
      "v_mfma_f32_16x16x32_bf16 v[144:147], v[236:239], v[50:53], v[144:147]\n"
      "s_cselect_b32 s9, 0, 196224\n"
      "v_add_f32_e64 v148, v148, v245\n"
      "v_mov_b32_dpp v208, v208 quad_perm:[0,0,0,0] row_mask:0xf bank_mask:0xf\n"
      "v_mfma_f32_16x16x32_bf16 v[212:215], v[228:231], v[224:227], 0\n"
      "s_add_u32 s10, s8, s9\n"
      "v_permlane32_swap_b32 v151, v148\n"
      "v_mfma_f32_16x16x32_bf16 v[212:215], v[236:239], v[54:57], v[212:215]\n"
      "s_waitcnt lgkmcnt(0)\n"
      "v_add_f32 v151, v151, v148\n"
      "v_rcp_f32 v208, v208\n"
      "v_mfma_f32_16x16x32_bf16 v[252:255], v[228:231], v[160:163], 0\n"
      "s_add_u32 s28, s28, s10\n"
      "s_add_u32 s52, s16, s28\n"
      "s_addc_u32 s53, s17, 0\n"
      "v_mfma_f32_16x16x32_bf16 v[252:255], v[236:239], v[58:61], v[252:255]\n"
      "ds_write_b32 v129, v151 offset:1024\n"
      "v_mfma_f32_16x16x32_bf16 v[160:163], a[0:3], v[220:223], 0\n"
      "s_add_u32 s27, s27, 1\n"
      "v_cndmask_b32 v144, v144, v212, s[42:43]\n"
      "s_cmp_ge_u32 s27, s26\n"
      "v_mfma_f32_16x16x32_bf16 v[160:163], a[36:39], v[248:251], v[160:163]\n"
      "s_cselect_b32 s28, 1, 0\n"
      "s_nop 0\n"
      "v_cndmask_b32 v144, v144, v252, s[44:45]\n"
      "v_mfma_f32_16x16x32_bf16 v[236:239], a[4:7], v[220:223], 0\n"
      "s_xor_b32 s29, s10, s11\n"
      "v_mul_f32 v144, v208, v144\n"
      "v_cvt_pk_bf16_f32 v144, v144, v144\n"
      "v_mfma_f32_16x16x32_bf16 v[236:239], a[40:43], v[248:251], v[236:239]\n"
      "ds_write_b16 v135, v144 offset:16\n"
      "s_or_b32 s28, s28, s29\n"
      "s_branch L_join_21_%=\n"
      "L_flg_82_%=:\n"
      "v_cmp_gt_i32 vcc, 0, v188\n"
      "s_cbranch_vccz L_fb_83_%=\n"
      "s_sleep 1\n"
      "ds_read_b128 v[188:191], v128 offset:2048\n"
      "s_waitcnt lgkmcnt(0)\n"
      "v_bitop3_b32 v188, v188, v189, s4 bitop3:0x7e\n"
      "v_bitop3_b32 v190, v190, v191, s4 bitop3:0x7e\n"
      "v_or_b32 v188, v188, v190\n"
      "v_cmp_ne_u32 s[48:49], 0, v188\n"
      "s_branch L_flg_82_%=\n"
      "L_gen_28_%=:\n"
      "s_waitcnt lgkmcnt(0)\n"
      "s_nop 7\n"
      "s_nop 7\n"
      "s_cmp_eq_u64 s[48:49], 0\n"
      "s_cbranch_scc1 L_go_89_%=\n"
      "s_mov_b32 m0, s27\n"
      "s_nop 0\n"
      "s_movrels_b32 s9, s80\n"
      "s_lshr_b32 s8, s9, 8\n"
      "s_and_b32 s9, s9, 0xff\n"
      "s_mul_i32 s10, s8, 73\n"
      "s_add_u32 s10, s10, s9\n"
      "s_mul_i32 s30, s10, 4802\n"
      "s_add_u32 s30, s30, s18\n"
      "s_and_b32 s29, s30, 15\n"
      "s_add_u32 s30, s29, 110592\n"
      "s_lshl_b64 s[34:35], s[48:49], 4\n"
      "s_lshr_b64 s[34:35], s[34:35], 5\n"
      "s_cbranch_scc1 L_gen_29_%=\n"
      "v_mbcnt_lo_u32_b32 v160, -1, 0\n"
      "v_mbcnt_hi_u32_b32 v160, -1, v160\n"
      "v_lshlrev_b32 v161, 1, v160\n"
      "v_add_u32 v162, 4802, v161\n"
      "v_cmp_gt_u32 vcc, s29, v161\n"
      "s_nop 1\n"
      "v_cndmask_b32 v161, v162, v161, vcc\n"
      "v_add_u32 v161, 110592, v161\n"
      "v_mov_b32 v162, 0\n"
      "s_mov_b64 exec, 0x7f\n"
      "ds_write_b16 v161, v162\n"
      "s_mov_b64 exec, -1\n"
      "v_lshlrev_b32 v161, 4, v160\n"
      "v_add_u32 v161, 110592, v161\n"
      "v_min_u32 v160, 111296, v161\n"
      "ds_read_b128 v[164:167], v161\n"
      "ds_read_b128 v[188:191], v161 offset:1024\n"
      "ds_read_b128 v[192:195], v161 offset:2048\n"
      "ds_read_b128 v[200:203], v161 offset:3072\n"
      "ds_read_b128 v[148:151], v160 offset:4096\n"
      "s_waitcnt lgkmcnt(4)\n"
      "v_or3_b32 v164, v164, v165, v166\n"
      "s_waitcnt lgkmcnt(3)\n"
      "v_or3_b32 v164, v164, v167, v188\n"
      "v_or3_b32 v164, v164, v189, v190\n"
      "s_waitcnt lgkmcnt(2)\n"
      "v_or3_b32 v164, v164, v191, v192\n"
      "v_or3_b32 v164, v164, v193, v194\n"
      "s_waitcnt lgkmcnt(1)\n"
      "v_or3_b32 v164, v164, v195, v200\n"
      "v_or3_b32 v164, v164, v201, v202\n"
      "s_waitcnt lgkmcnt(0)\n"
      "v_or3_b32 v164, v164, v203, v148\n"
      "v_or3_b32 v164, v164, v149, v150\n"
      "v_bitop3_b32 v164, v164, v151, s40 bitop3:0xa8\n"
      "v_cmp_ne_u32 s[48:49], 0, v164\n"
      "s_cmp_lg_u64 s[48:49], 0\n"
      "s_cbranch_scc1 L_gen_29_%=\n"
      "s_cmp_eq_u64 s[50:51], 0\n"
      "s_cbranch_scc1 L_fast_31_%=\n"
      "L_go_89_%=:\n"
      "v_mfma_f32_16x16x32_bf16 v[164:167], v[212:215], v[180:183], v[90:93]\n"
      "ds_read_b128 v[160:163], v128 offset:3072\n"
      "v_max3_f32 v191, v144, v145, v146\n"
      "v_mfma_f32_16x16x32_bf16 v[164:167], v[34:37], v[38:41], v[164:167]\n"
      "s_nop 0\n"
      "v_max3_f32 v189, v147, v244, v245\n"
      "v_max3_f32 v191, v191, v246, v247\n"
      "v_mfma_f32_16x16x32_bf16 v[192:195], v[140:143], v[180:183], v[94:97]\n"
      "v_max3_f32 v189, v189, v184, v185\n"
      "v_max3_f32 v191, v191, v186, v187\n"
      "v_mfma_f32_16x16x32_bf16 v[192:195], v[40:43], v[38:41], v[192:195]\n"
      "v_max3_f32 v191, v191, v189, v236\n"
      "v_mov_b32_e64 v189, v191\n"
      "v_mfma_f32_16x16x32_bf16 v[200:203], v[136:139], v[180:183], v[72:75]\n"
      "s_nop 0\n"
      "v_permlane16_swap_b32 v191, v189\n"
      "v_mfma_f32_16x16x32_bf16 v[200:203], v[228:231], v[140:143], v[200:203]\n"
      "v_max_f32 v191, v191, v189\n"
      "v_mov_b32 v189, v191\n"
      "v_mfma_f32_16x16x32_bf16 v[200:203], v[152:155], v[38:41], v[200:203]\n"
      "s_nop 0\n"
      "v_permlane32_swap_b32 v191, v189\n"
      "v_mfma_f32_16x16x32_bf16 v[148:151], v[156:159], v[180:183], v[98:101]\n"
      "v_max_f32 v191, v191, v189\n"
      "v_sub_f32 v144, v144, v191\n"
      "v_mfma_f32_16x16x32_bf16 v[148:151], v[46:49], v[38:41], v[148:151]\n"
      "s_waitcnt lgkmcnt(0)\n"
      "v_sub_f32 v145, v145, v191\n"
      "v_sub_f32 v146, v146, v191\n"
      "v_sub_f32 v147, v147, v191\n"
      "v_sub_f32 v244, v244, v191\n"
      "v_sub_f32 v245, v245, v191\n"
      "v_sub_f32 v246, v246, v191\n"
      "v_sub_f32 v247, v247, v191\n"
      "v_sub_f32 v184, v184, v191\n"
      "v_sub_f32 v185, v185, v191\n"
      "v_sub_f32 v186, v186, v191\n"
      "v_sub_f32 v187, v187, v191\n"
      "v_sub_f32 v236, v236, v191\n"
      "v_exp_f32 v144, v144\n"
      "v_exp_f32 v145, v145\n"
      "v_exp_f32 v146, v146\n"
      "v_cvt_pk_bf16_f32 v144, v144, v145\n"
      "v_exp_f32 v147, v147\n"
      "v_exp_f32 v244, v244\n"
      "v_cvt_pk_bf16_f32 v145, v146, v147\n"
      "v_exp_f32 v245, v245\n"
      "v_exp_f32 v246, v246\n"
      "v_cvt_pk_bf16_f32 v146, v244, v245\n"
      "v_exp_f32 v247, v247\n"
      "v_exp_f32 v184, v184\n"
      "v_cvt_pk_bf16_f32 v147, v246, v247\n"
      "v_exp_f32 v185, v185\n"
      "v_exp_f32 v186, v186\n"
      "v_exp_f32_e64 v187, v187\n"
      "v_mfma_f32_16x16x32_bf16 v[180:183], v[212:215], v[196:199], v[102:105]\n"
      "v_exp_f32_e64 v236, v236\n"
      "v_mfma_f32_16x16x32_bf16 v[180:183], v[34:37], v[44:47], v[180:183]\n"
      "v_cvt_pk_bf16_f32 v62, v184, v185\n"
      "v_cvt_pk_bf16_f32 v63, v186, v187\n"
      "v_cvt_pk_bf16_f32 v64, v236, 0\n"
      "v_bitop3_b32 v160, v160, v161, s4 bitop3:0x7e\n"
      "v_bitop3_b32 v162, v162, v163, s4 bitop3:0x7e\n"
      "v_mfma_f32_16x16x32_bf16 v[184:187], v[66:69], v[144:147], 0\n"
      "ds_read_b128 v[188:191], v119 offset:30720\n"
      "v_or_b32 v160, v160, v162\n"
      "v_mfma_f32_16x16x32_bf16 v[184:187], v[66:69], v[62:65], v[184:187]\n"
      "v_cmp_ne_u32 vcc, 0, v160\n"
      "v_max3_f32 v202, v164, v165, v166\n"
      "v_mfma_f32_16x16x32_bf16 v[212:215], v[216:219], v[144:147], 0\n"
      "s_mov_b64 s[48:49], vcc\n"
      "v_max3_f32 v203, v167, v192, v193\n"
      "v_max3_f32 v202, v202, v194, v195\n"
      "v_mfma_f32_16x16x32_bf16 v[212:215], v[50:53], v[62:65], v[212:215]\n"
      "s_cbranch_vccnz L_flg_90_%=\n"
      "L_fb_91_%=:\n"
      "v_max3_f32 v203, v203, v148, v149\n"
      "v_mfma_f32_16x16x32_bf16 v[160:163], v[240:243], v[144:147], 0\n"
      "v_max3_f32 v202, v202, v150, v151\n"
      "v_max3_f32 v202, v202, v203, v200\n"
      "v_mfma_f32_16x16x32_bf16 v[160:163], v[54:57], v[62:65], v[160:163]\n"
      "v_mov_b32 v203, v202\n"
      "v_mfma_f32_16x16x32_bf16 v[244:247], v[232:235], v[144:147], 0\n"
      "s_nop 0\n"
      "v_permlane16_swap_b32_e64 v202, v203\n"
      "v_mfma_f32_16x16x32_bf16 v[244:247], v[58:61], v[62:65], v[244:247]\n"
      "v_max_f32 v202, v202, v203\n"
      "v_mov_b32 v203, v202\n"
      "v_mfma_f32_16x16x32_bf16 v[144:147], v[140:143], v[196:199], v[106:109]\n"
      "s_nop 0\n"
      "v_permlane32_swap_b32 v202, v203\n"
      "v_mfma_f32_16x16x32_bf16 v[144:147], v[40:43], v[44:47], v[144:147]\n"
      "v_max_f32 v202, v202, v203\n"
      "v_sub_f32 v164, v164, v202\n"
      "v_sub_f32 v165, v165, v202\n"
      "v_sub_f32 v166, v166, v202\n"
      "v_sub_f32 v167, v167, v202\n"
      "v_sub_f32 v192, v192, v202\n"
      "v_sub_f32 v193, v193, v202\n"
      "v_sub_f32 v194, v194, v202\n"
      "v_sub_f32 v195, v195, v202\n"
      "v_sub_f32 v148, v148, v202\n"
      "v_sub_f32 v149, v149, v202\n"
      "v_sub_f32 v150, v150, v202\n"
      "v_sub_f32 v151, v151, v202\n"
      "v_sub_f32 v200, v200, v202\n"
      "v_exp_f32 v164, v164\n"
      "v_exp_f32 v165, v165\n"
      "v_exp_f32 v166, v166\n"
      "v_cvt_pk_bf16_f32 v164, v164, v165\n"
      "v_exp_f32 v167, v167\n"
      "v_mfma_f32_16x16x32_bf16 v[140:143], v[136:139], v[196:199], v[74:77]\n"
      "s_waitcnt lgkmcnt(0)\n"
      "v_exp_f32 v192, v192\n"
      "v_mfma_f32_16x16x32_bf16 v[140:143], v[228:231], v[156:159], v[140:143]\n"
      "global_store_dwordx4 v116, v[188:191], s[54:55] offset:256 sc1\n"
      "v_mfma_f32_16x16x32_bf16 v[140:143], v[152:155], v[44:47], v[140:143]\n"
      "v_cvt_pk_bf16_f32 v165, v166, v167\n"
      "v_exp_f32 v193, v193\n"
      "v_mfma_f32_16x16x32_bf16 v[188:191], v[156:159], v[196:199], v[110:113]\n"
      "v_exp_f32 v194, v194\n"
      "v_mfma_f32_16x16x32_bf16 v[188:191], v[46:49], v[44:47], v[188:191]\n"
      "v_cvt_pk_bf16_f32 v166, v192, v193\n"
      "v_exp_f32 v195, v195\n"
      "v_exp_f32 v148, v148\n"
      "v_cvt_pk_bf16_f32 v167, v194, v195\n"
      "v_mfma_f32_16x16x32_bf16 v[192:195], v[228:231], v[136:139], v[76:79]\n"
      "v_exp_f32 v149, v149\n"
      "v_exp_f32 v150, v150\n"
      "v_exp_f32 v151, v151\n"
      "v_exp_f32 v200, v200\n"
      "v_cvt_pk_bf16_f32 v62, v148, v149\n"
      "v_cvt_pk_bf16_f32 v63, v150, v151\n"
      "v_mfma_f32_16x16x16_bf16 v[192:195], v[154:155], v[152:153], v[192:195]\n"
      "v_cvt_pk_bf16_f32 v64, v200, 0\n"
      "v_rcp_f32_e64 v184, v184\n"
      "v_max3_f32 v142, v180, v181, v182\n"
      "v_mfma_f32_16x16x32_bf16 v[196:199], v[66:69], v[164:167], 0\n"
      "ds_read_b128 v[156:159], v120 offset:18432\n"
      "v_mul_f32 v212, v184, v212\n"
      "v_mfma_f32_16x16x32_bf16 v[196:199], v[66:69], v[62:65], v[196:199]\n"
      "v_mul_f32 v213, v184, v213\n"
      "v_cvt_pk_bf16_f32 v212, v212, v213\n"
      "v_mfma_f32_16x16x32_bf16 v[136:139], v[216:219], v[164:167], 0\n"
      "v_mul_f32 v214, v184, v214\n"
      "v_mul_f32 v215, v184, v215\n"
      "v_mfma_f32_16x16x32_bf16 v[136:139], v[50:53], v[62:65], v[136:139]\n"
      "v_cvt_pk_bf16_f32 v213, v214, v215\n"
      "v_mul_f32 v160, v184, v160\n"
      "v_mul_f32 v161, v184, v161\n"
      "v_cvt_pk_bf16_f32 v214, v160, v161\n"
      "v_mfma_f32_16x16x32_bf16 v[148:151], v[240:243], v[164:167], 0\n"
      "v_mul_f32 v162, v184, v162\n"
      "v_mul_f32 v163, v184, v163\n"
      "v_mfma_f32_16x16x32_bf16 v[148:151], v[54:57], v[62:65], v[148:151]\n"
      "v_cvt_pk_bf16_f32 v215, v162, v163\n"
      "v_mul_f32 v244, v184, v244\n"
      "v_mul_f32 v245, v184, v245\n"
      "v_cvt_pk_bf16_f32 v244, v244, v245\n"
      "v_mul_f32 v246, v184, v246\n"
      "v_mul_f32 v247, v184, v247\n"
      "v_mfma_f32_16x16x32_bf16 v[202:205], v[232:235], v[164:167], 0\n"
      "v_cvt_pk_bf16_f32 v245, v246, v247\n"
      "v_max3_f32 v200, v183, v144, v145\n"
      "v_mfma_f32_16x16x32_bf16 v[202:205], v[58:61], v[62:65], v[202:205]\n"
      "ds_write_b128 v124, v[212:215]\n"
      "v_max3_f32 v142, v142, v146, v147\n"
      "v_max3_f32 v200, v200, v188, v189\n"
      "v_max3_f32 v142, v142, v190, v191\n"
      "v_max3_f32 v142, v142, v200, v140\n"
      "v_mov_b32 v200, v142\n"
      "s_waitcnt lgkmcnt(1)\n"
      "s_nop 0\n"
      "v_permlane16_swap_b32 v142, v200\n"
      "v_max_f32 v142, v142, v200\n"
      "v_mov_b32 v200, v142\n"
      "s_nop 1\n"
      "v_permlane32_swap_b32 v142, v200\n"
      "v_max_f32 v142, v142, v200\n"
      "v_sub_f32 v180, v180, v142\n"
      "v_sub_f32 v181, v181, v142\n"
      "v_sub_f32 v182, v182, v142\n"
      "v_sub_f32 v183, v183, v142\n"
      "v_sub_f32 v144, v144, v142\n"
      "v_sub_f32 v145, v145, v142\n"
      "v_sub_f32 v146, v146, v142\n"
      "v_sub_f32 v147, v147, v142\n"
      "v_sub_f32 v188, v188, v142\n"
      "v_sub_f32 v189, v189, v142\n"
      "v_sub_f32 v190, v190, v142\n"
      "v_sub_f32 v191, v191, v142\n"
      "v_sub_f32 v140, v140, v142\n"
      "v_exp_f32 v180, v180\n"
      "v_exp_f32 v181, v181\n"
      "v_exp_f32_e64 v182, v182\n"
      "v_cvt_pk_bf16_f32 v180, v180, v181\n"
      "v_exp_f32 v183, v183\n"
      "v_exp_f32 v144, v144\n"
      "v_cvt_pk_bf16_f32 v181, v182, v183\n"
      "v_exp_f32 v145, v145\n"
      "v_exp_f32 v146, v146\n"
      "v_cvt_pk_bf16_f32 v182, v144, v145\n"
      "v_exp_f32 v147, v147\n"
      "v_exp_f32 v188, v188\n"
      "ds_write_b64 v125, v[244:245]\n"
      "v_cvt_pk_bf16_f32 v183, v146, v147\n"
      "v_exp_f32 v189, v189\n"
      "v_exp_f32 v190, v190\n"
      "v_exp_f32 v191, v191\n"
      "v_exp_f32 v140, v140\n"
      "v_cvt_pk_bf16_f32 v62, v188, v189\n"
      "v_cvt_pk_bf16_f32 v63, v190, v191\n"
      "v_cvt_pk_bf16_f32 v64, v140, 0\n"
      "v_rcp_f32_e64 v196, v196\n"
      "v_mfma_f32_16x16x32_bf16 v[152:155], v[66:69], v[180:183], 0\n"
      "global_store_dwordx4 v117, v[156:159], s[54:55] sc1\n"
      "v_mfma_f32_16x16x32_bf16 v[152:155], v[66:69], v[62:65], v[152:155]\n"
      "ds_read_b128 v[144:147], v123 offset:18688\n"
      "v_mul_f32_e64 v136, v196, v136\n"
      "v_mfma_f32_16x16x32_bf16 v[156:159], v[216:219], v[180:183], 0\n"
      "ds_read_b128 v[244:247], v123 offset:21760\n"
      "v_mul_f32_e64 v137, v196, v137\n"
      "v_mfma_f32_16x16x32_bf16 v[156:159], v[50:53], v[62:65], v[156:159]\n"
      "ds_read_b128 v[212:215], v123 offset:24832\n"
      "v_cvt_pk_bf16_f32 v136, v136, v137\n"
      "v_mfma_f32_16x16x32_bf16 v[160:163], v[240:243], v[180:183], 0\n"
      "ds_read_b128 v[228:231], v121 offset:18432\n"
      "v_mul_f32_e64 v138, v196, v138\n"
      "v_mfma_f32_16x16x32_bf16 v[160:163], v[54:57], v[62:65], v[160:163]\n"
      "ds_read_b128 v[208:211], v123 offset:27904\n"
      "v_mul_f32 v139, v196, v139\n"
      "v_cvt_pk_bf16_f32 v137, v138, v139\n"
      "v_mul_f32 v148, v196, v148\n"
      "v_mul_f32_e64 v149, v196, v149\n"
      "v_cvt_pk_bf16_f32 v138, v148, v149\n"
      "v_mfma_f32_16x16x32_bf16 v[164:167], v[232:235], v[180:183], 0\n"
      "ds_read_b128 v[220:223], v123 offset:30976\n"
      "v_mul_f32_e64 v150, v196, v150\n"
      "v_mfma_f32_16x16x32_bf16 v[164:167], v[58:61], v[62:65], v[164:167]\n"
      "v_mul_f32_e64 v151, v196, v151\n"
      "v_cvt_pk_bf16_f32 v139, v150, v151\n"
      "v_mul_f32_e64 v202, v196, v202\n"
      "v_mul_f32_e64 v203, v196, v203\n"
      "v_cvt_pk_bf16_f32 v202, v202, v203\n"
      "v_mul_f32_e64 v204, v196, v204\n"
      "v_mul_f32_e64 v205, v196, v205\n"
      "v_cvt_pk_bf16_f32 v203, v204, v205\n"
      "s_waitcnt lgkmcnt(4)\n"
      "v_mfma_f32_16x16x32_bf16 v[180:183], a[216:219], v[144:147], 0\n"
      "ds_read_b128 v[196:199], v123 offset:34048\n"
      "v_cndmask_b32 v237, v237, v201, s[42:43]\n"
      "v_mfma_f32_16x16x32_bf16 v[180:183], a[228:231], v[244:247], v[180:183]\n"
      "ds_write_b128 v124, v[136:139] offset:256\n"
      "s_waitcnt lgkmcnt(3)\n"
      "v_mfma_f32_16x16x32_bf16 v[184:187], a[220:223], v[144:147], 0\n"
      "ds_read_b128 v[136:139], v123 offset:18944\n"
      "v_cndmask_b32 v237, v237, v141, s[44:45]\n"
      "v_mfma_f32_16x16x32_bf16 v[184:187], a[232:235], v[244:247], v[184:187]\n"
      "ds_write_b64 v125, v[202:203] offset:256\n"
      "v_mfma_f32_16x16x32_bf16 v[188:191], a[224:227], v[144:147], 0\n"
      "ds_read_b128 v[140:143], v123 offset:22016\n"
      "v_cndmask_b32 v237, v237, v193, s[46:47]\n"
      "v_mfma_f32_16x16x32_bf16 v[188:191], a[236:239], v[244:247], v[188:191]\n"
      "ds_read_b128 v[144:147], v123 offset:25088\n"
      "v_max_f32_dpp v193, v237, v237 row_ror:8 row_mask:0xf bank_mask:0xf\n"
      "v_mfma_f32_16x16x32_bf16 v[180:183], a[240:243], v[212:215], v[180:183]\n"
      "s_nop 0\n"
      "v_max_f32_dpp v193, v193, v193 row_ror:4 row_mask:0xf bank_mask:0xf\n"
      "v_mfma_f32_16x16x32_bf16 v[180:183], a[252:255], v[208:211], v[180:183]\n"
      "s_waitcnt lgkmcnt(5)\n"
      "v_max_f32_dpp v193, v193, v193 row_ror:2 row_mask:0xf bank_mask:0xf\n"
      "v_mfma_f32_16x16x32_bf16 v[184:187], a[244:247], v[212:215], v[184:187]\n"
      "s_nop 0\n"
      "v_max_f32_dpp v193, v193, v193 row_ror:1 row_mask:0xf bank_mask:0xf\n"
      "v_mfma_f32_16x16x32_bf16 v[184:187], v[0:3], v[208:211], v[184:187]\n"
      "s_mov_b32 exec_hi, 0\n"
      "global_store_dwordx4 v118, v[228:231], s[54:55] sc1\n"
      "s_mov_b32 exec_hi, -1\n"
      "v_mfma_f32_16x16x32_bf16 v[188:191], a[248:251], v[212:215], v[188:191]\n"
      "ds_read_b128 v[148:151], v123 offset:28160\n"
      "v_max_f32_dpp v193, v193, v193 row_bcast:15 row_mask:0xa bank_mask:0xf\n"
      "v_mfma_f32_16x16x32_bf16 v[188:191], v[4:7], v[208:211], v[188:191]\n"
      "s_add_u32 s28, s27, 2\n"
      "v_max_f32_dpp v193, v193, v193 row_bcast:31 row_mask:0xc bank_mask:0xf\n"
      "v_mfma_f32_16x16x32_bf16 v[180:183], v[8:11], v[220:223], v[180:183]\n"
      "s_waitcnt lgkmcnt(2)\n"
      "v_readlane_b32 s31, v193, 63\n"
      "s_min_u32 m0, s28, s3\n"
      "v_mfma_f32_16x16x32_bf16 v[180:183], v[20:23], v[196:199], v[180:183]\n"
      "s_movrels_b32 s9, s80\n"
      "v_subrev_f32 v237, s31, v237\n"
      "v_exp_f32 v237, v237\n"
      "v_mfma_f32_16x16x32_bf16 v[184:187], v[12:15], v[220:223], v[184:187]\n"
      "s_lshr_b32 s8, s9, 8\n"
      "v_cvt_pk_bf16_f32 v237, v237, v237\n"
      "s_and_b32 s9, s9, 0xff\n"
      "v_mfma_f32_16x16x32_bf16 v[184:187], v[24:27], v[196:199], v[184:187]\n"
      "ds_write_b16 v130, v237\n"
      "v_mfma_f32_16x16x32_bf16 v[188:191], v[16:19], v[220:223], v[188:191]\n"
      "ds_read_b128 v[220:223], v131\n"
      "v_fma_f32 v237, v180, v180, s39\n"
      "v_mfma_f32_16x16x32_bf16 v[188:191], v[28:31], v[196:199], v[188:191]\n"
      "ds_read_b128 v[208:211], v131 offset:64\n"
      "v_fmac_f32_e64 v237, v181, v181\n"
      "v_mfma_f32_16x16x32_bf16 v[192:195], a[216:219], v[136:139], 0\n"
      "s_waitcnt lgkmcnt(3)\n"
      "v_fmac_f32 v237, v182, v182\n"
      "v_fmac_f32_e64 v237, v183, v183\n"
      "v_mfma_f32_16x16x32_bf16 v[192:195], a[228:231], v[140:143], v[192:195]\n"
      "s_mul_i32 s10, s8, 73\n"
      "v_fmac_f32 v237, v184, v184\n"
      "v_fmac_f32 v237, v185, v185\n"
      "v_mfma_f32_16x16x32_bf16 v[196:199], a[220:223], v[136:139], 0\n"
      "s_add_u32 s10, s10, s9\n"
      "v_fmac_f32 v237, v186, v186\n"
      "v_fmac_f32 v237, v187, v187\n"
      "v_mfma_f32_16x16x32_bf16 v[196:199], a[232:235], v[140:143], v[196:199]\n"
      "s_mul_i32 s28, s10, 4802\n"
      "v_fmac_f32 v237, v188, v188\n"
      "v_fmac_f32_e64 v237, v189, v189\n"
      "v_mfma_f32_16x16x32_bf16 v[200:203], a[224:227], v[136:139], 0\n"
      "ds_read_b128 v[228:231], v123 offset:31232\n"
      "v_fmac_f32_e64 v237, v190, v190\n"
      "v_mfma_f32_16x16x32_bf16 v[200:203], a[236:239], v[140:143], v[200:203]\n"
      "ds_read_b128 v[212:215], v123 offset:34304\n"
      "v_fmac_f32 v237, v191, v191\n"
      "v_mfma_f32_16x16x32_bf16 v[192:195], a[240:243], v[144:147], v[192:195]\n"
      "s_add_u32 s56, s18, s28\n"
      "s_addc_u32 s57, s19, 0\n"
      "s_and_b32 s56, s56, -16\n"
      "v_mfma_f32_16x16x32_bf16 v[192:195], a[252:255], v[148:151], v[192:195]\n"
      "s_waitcnt lgkmcnt(2)\n"
      "s_sub_u32 s58, s56, 110592\n"
      "v_mfma_f32_16x16x32_bf16 v[196:199], a[244:247], v[144:147], v[196:199]\n"
      "s_subb_u32 s59, s57, 0\n"
      "v_permlane16_swap_b32 v238, v237\n"
      "v_mfma_f32_16x16x32_bf16 v[196:199], v[0:3], v[148:151], v[196:199]\n"
      "s_sub_u32 s56, s56, 73728\n"
      "v_add_f32 v238, v238, v237\n"
      "v_mfma_f32_16x16x32_bf16 v[200:203], a[248:251], v[144:147], v[200:203]\n"
      "s_subb_u32 s57, s57, 0\n"
      "s_mul_i32 s28, s8, 1373568\n"
      "s_mul_i32 s29, s9, 2688\n"
      "v_mfma_f32_16x16x32_bf16 v[200:203], v[4:7], v[148:151], v[200:203]\n"
      "s_add_u32 s28, s28, s29\n"
      "s_cmp_eq_u32 s8, 72\n"
      "v_mfma_f32_16x16x32_bf16 v[244:247], v[220:223], v[66:69], 0\n"
      "s_cselect_b32 s8, 0, 100270464\n"
      "s_cmp_eq_u32 s9, 72\n"
      "s_cselect_b32 s9, 0, 196224\n"
      "v_mfma_f32_16x16x32_bf16 v[244:247], v[208:211], v[66:69], v[244:247]\n"
      "s_waitcnt lgkmcnt(0)\n"
      "s_add_u32 s10, s8, s9\n"
      "v_mfma_f32_16x16x32_bf16 v[192:195], v[8:11], v[228:231], v[192:195]\n"
      "s_add_u32 s28, s28, s10\n"
      "s_add_u32 s52, s16, s28\n"
      "s_addc_u32 s53, s17, 0\n"
      "v_mfma_f32_16x16x32_bf16 v[192:195], v[20:23], v[212:215], v[192:195]\n"
      "s_add_u32 s27, s27, 1\n"
      "v_mov_b32_dpp v244, v244 quad_perm:[0,0,0,0] row_mask:0xf bank_mask:0xf\n"
      "v_rcp_f32 v244, v244\n"
      "v_mfma_f32_16x16x32_bf16 v[196:199], v[12:15], v[228:231], v[196:199]\n"
      "s_cmp_ge_u32 s27, s26\n"
      "s_cselect_b32 s28, 1, 0\n"
      "s_xor_b32 s29, s10, s11\n"
      "v_mfma_f32_16x16x32_bf16 v[196:199], v[24:27], v[212:215], v[196:199]\n"
      "ds_read_b128 v[136:139], v114 offset:18432\n"
      "v_fma_f32 v148, v192, v192, s39\n"
      "v_mfma_f32_16x16x32_bf16 v[200:203], v[16:19], v[228:231], v[200:203]\n"
      "ds_read_b128 v[140:143], v115 offset:18432\n"
      "v_fmac_f32 v148, v193, v193\n"
      "v_mfma_f32_16x16x32_bf16 v[200:203], v[28:31], v[212:215], v[200:203]\n"
      "s_or_b32 s28, s28, s29\n"
      "v_fmac_f32 v148, v194, v194\n"
      "v_fmac_f32 v148, v195, v195\n"
      "v_mfma_f32_16x16x32_bf16 v[212:215], v[220:223], v[216:219], 0\n"
      "v_fmac_f32 v148, v196, v196\n"
      "v_fmac_f32 v148, v197, v197\n"
      "v_mfma_f32_16x16x32_bf16 v[212:215], v[208:211], v[50:53], v[212:215]\n"
      "v_fmac_f32 v148, v198, v198\n"
      "v_fmac_f32 v148, v199, v199\n"
      "v_mfma_f32_16x16x32_bf16 v[144:147], v[220:223], v[240:243], 0\n"
      "v_fmac_f32 v148, v200, v200\n"
      "v_fmac_f32 v148, v201, v201\n"
      "v_mfma_f32_16x16x32_bf16 v[144:147], v[208:211], v[54:57], v[144:147]\n"
      "v_fmac_f32 v148, v202, v202\n"
      "v_fmac_f32 v148, v203, v203\n"
      "v_mfma_f32_16x16x32_bf16 v[240:243], v[220:223], v[232:235], 0\n"
      "v_mov_b32 v149, v148\n"
      "v_mfma_f32_16x16x32_bf16 v[240:243], v[208:211], v[58:61], v[240:243]\n"
      "s_nop 0\n"
      "v_permlane16_swap_b32 v148, v149\n"
      "v_add_f32 v148, v148, v149\n"
      "v_cndmask_b32 v212, v212, v144, s[42:43]\n"
      "s_waitcnt lgkmcnt(0)\n"
      "v_mfma_f32_16x16x32_bf16 v[144:147], a[0:3], v[136:139], 0\n"
      "v_permlane32_swap_b32 v238, v148\n"
      "v_mfma_f32_16x16x32_bf16 v[144:147], a[36:39], v[140:143], v[144:147]\n"
      "v_add_f32 v238, v238, v148\n"
      "v_cndmask_b32 v212, v212, v240, s[44:45]\n"
      "v_mfma_f32_16x16x32_bf16 v[148:151], a[4:7], v[136:139], 0\n"
      "v_mul_f32 v212, v244, v212\n"
      "v_cvt_pk_bf16_f32 v212, v212, v212\n"
      "v_mfma_f32_16x16x32_bf16 v[148:151], a[40:43], v[140:143], v[148:151]\n"
      "ds_write_b32 v129, v238\n"
      "ds_write_b16 v135, v212\n"
      "s_branch L_join_30_%=\n"
      "L_flg_90_%=:\n"
      "v_cmp_gt_i32 vcc, 0, v160\n"
      "s_cbranch_vccz L_fb_91_%=\n"
      "s_sleep 1\n"
      "ds_read_b128 v[160:163], v128 offset:3072\n"
      "s_waitcnt lgkmcnt(0)\n"
      "v_bitop3_b32 v160, v160, v161, s4 bitop3:0x7e\n"
      "v_bitop3_b32 v162, v162, v163, s4 bitop3:0x7e\n"
      "v_or_b32 v160, v160, v162\n"
      "v_cmp_ne_u32 s[48:49], 0, v160\n"
      "s_branch L_flg_90_%=\n"
      "L_gen_29_%=:\n"
      "s_waitcnt lgkmcnt(0)\n"
      "s_nop 7\n"
      "s_nop 7\n"
      "v_mbcnt_lo_u32_b32 v239, -1, 0\n"
      "v_mbcnt_hi_u32_b32 v239, -1, v239\n"
      "v_and_b32 v226, 15, v239\n"
      "v_lshrrev_b32 v225, 4, v239\n"
      "v_mul_u32_u24 v211, 0x62, v226\n"
      "v_lshl_add_u32 v211, v225, 3, v211\n"
      "v_add_u32 v211, s30, v211\n"
      "v_and_b32 v239, -4, v211\n"
      "v_and_b32 v211, 2, v211\n"
      "v_lshlrev_b32 v225, 2, v225\n"
      "v_sub_u32_e64 v226, v226, v225\n"
      "v_and_b32_e64 v225, 1, v226\n"
      "v_lshlrev_b32_e64 v225, 4, v225\n"
      "v_lshrrev_b32_e64 v226, 1, v226\n"
      "v_cmp_eq_u32 vcc, 0, v226\n"
      "s_movk_i32 s31, 0x3fb9\n"
      "v_lshlrev_b32 v225, v225, s31\n"
      "ds_read2_b32 v[192:193], v239 offset0:0 offset1:1\n"
      "ds_read2_b32 v[166:167], v239 offset0:8 offset1:9\n"
      "ds_read2_b32 v[194:195], v239 offset0:16 offset1:17\n"
      "ds_read2_b32 v[162:163], v239 offset0:2 offset1:10\n"
      "ds_read2_b32 v[188:189], v239 offset0:18 offset1:24\n"
      "v_add_u32 v239, 1568, v239\n"
      "ds_read2_b32 v[202:203], v239 offset0:0 offset1:1\n"
      "ds_read2_b32 v[160:161], v239 offset0:8 offset1:9\n"
      "ds_read2_b32 v[148:149], v239 offset0:16 offset1:17\n"
      "ds_read2_b32 v[190:191], v239 offset0:2 offset1:10\n"
      "ds_read2_b32 v[164:165], v239 offset0:18 offset1:24\n"
      "v_add_u32 v239, 1568, v239\n"
      "ds_read2_b32 v[150:151], v239 offset0:0 offset1:1\n"
      "ds_read2_b32 v[248:249], v239 offset0:8 offset1:9\n"
      "ds_read2_b32 v[204:205], v239 offset0:16 offset1:17\n"
      "ds_read2_b32 v[200:201], v239 offset0:2 offset1:10\n"
      "ds_read2_b32 v[206:207], v239 offset0:18 offset1:24\n"
      "v_cndmask_b32 v250, 0, v225, vcc\n"
      "v_cmp_eq_u32 vcc, 1, v226\n"
      "s_waitcnt lgkmcnt(13)\n"
      "v_alignbyte_b32 v192, v193, v192, v211\n"
      "v_cndmask_b32 v251, 0, v225, vcc\n"
      "v_mfma_f32_16x16x32_bf16 v[224:227], v[212:215], v[180:183], v[90:93]\n"
      "s_waitcnt lgkmcnt(11)\n"
      "v_alignbyte_b32 v193, v162, v193, v211\n"
      "v_mfma_f32_16x16x32_bf16 v[224:227], v[34:37], v[38:41], v[224:227]\n"
      "v_alignbyte_b32 v166, v167, v166, v211\n"
      "v_alignbyte_b32 v167, v163, v167, v211\n"
      "v_mfma_f32_16x16x32_bf16 v[252:255], v[140:143], v[180:183], v[94:97]\n"
      "s_waitcnt lgkmcnt(8)\n"
      "v_alignbyte_b32 v194, v195, v194, v211\n"
      "v_alignbyte_b32 v195, v188, v195, v211\n"
      "v_mfma_f32_16x16x32_bf16 v[252:255], v[40:43], v[38:41], v[252:255]\n"
      "v_alignbyte_b32 v189, v189, v189, v211\n"
      "v_lshlrev_b32_e64 v189, 16, v189\n"
      "v_mfma_f32_16x16x32_bf16 v[220:223], v[136:139], v[180:183], v[72:75]\n"
      "s_waitcnt lgkmcnt(5)\n"
      "v_fmac_f32 v236, s37, v189\n"
      "v_alignbyte_b32 v202, v203, v202, v211\n"
      "v_mfma_f32_16x16x32_bf16 v[220:223], v[228:231], v[140:143], v[220:223]\n"
      "v_alignbyte_b32 v203, v190, v203, v211\n"
      "v_alignbyte_b32 v160, v161, v160, v211\n"
      "v_mfma_f32_16x16x32_bf16 v[220:223], v[152:155], v[38:41], v[220:223]\n"
      "v_alignbyte_b32 v161, v191, v161, v211\n"
      "v_alignbyte_b32 v148, v149, v148, v211\n"
      "v_mfma_f32_16x16x32_bf16 v[188:191], v[156:159], v[180:183], v[98:101]\n"
      "v_alignbyte_b32 v149, v164, v149, v211\n"
      "v_alignbyte_b32 v165, v165, v165, v211\n"
      "v_mfma_f32_16x16x32_bf16 v[188:191], v[46:49], v[38:41], v[188:191]\n"
      "v_lshlrev_b32_e64 v165, 16, v165\n"
      "v_mfma_f32_16x16x16_bf16 v[144:147], v[250:251], v[192:193], v[144:147]\n"
      "v_mfma_f32_16x16x16_bf16 v[244:247], v[250:251], v[166:167], v[244:247]\n"
      "s_nop 0\n"
      "v_mfma_f32_16x16x16_bf16 v[184:187], v[250:251], v[194:195], v[184:187]\n"
      "ds_read_b128 v[180:183], v128 offset:3072\n"
      "s_nop 4\n"
      "v_max3_f32 v163, v144, v145, v146\n"
      "v_max3_f32 v162, v147, v244, v245\n"
      "v_mfma_f32_16x16x16_bf16 v[224:227], v[250:251], v[202:203], v[224:227]\n"
      "v_max3_f32 v163, v163, v246, v247\n"
      "v_max3_f32 v162, v162, v184, v185\n"
      "v_mfma_f32_16x16x16_bf16 v[252:255], v[250:251], v[160:161], v[252:255]\n"
      "v_max3_f32 v163, v163, v186, v187\n"
      "v_max3_f32 v163, v163, v162, v236\n"
      "v_mfma_f32_16x16x16_bf16 v[188:191], v[250:251], v[148:149], v[188:191]\n"
      "v_mov_b32 v203, v163\n"
      "v_fmac_f32 v220, s37, v165\n"
      "s_nop 0\n"
      "v_permlane16_swap_b32 v163, v203\n"
      "v_max_f32 v163, v163, v203\n"
      "v_mov_b32_e64 v160, v163\n"
      "s_waitcnt lgkmcnt(1)\n"
      "v_alignbyte_b32 v150, v151, v150, v211\n"
      "v_permlane32_swap_b32 v163, v160\n"
      "v_max_f32 v163, v163, v160\n"
      "v_sub_f32 v144, v144, v163\n"
      "v_sub_f32 v145, v145, v163\n"
      "v_sub_f32 v146, v146, v163\n"
      "v_sub_f32 v147, v147, v163\n"
      "v_sub_f32 v244, v244, v163\n"
      "v_sub_f32 v245, v245, v163\n"
      "v_sub_f32 v246, v246, v163\n"
      "v_sub_f32 v247, v247, v163\n"
      "v_sub_f32 v184, v184, v163\n"
      "v_sub_f32 v185, v185, v163\n"
      "v_sub_f32 v186, v186, v163\n"
      "v_sub_f32 v187, v187, v163\n"
      "v_sub_f32 v236, v236, v163\n"
      "v_exp_f32 v144, v144\n"
      "v_exp_f32 v145, v145\n"
      "v_exp_f32 v146, v146\n"
      "v_cvt_pk_bf16_f32 v144, v144, v145\n"
      "v_exp_f32 v147, v147\n"
      "v_exp_f32 v244, v244\n"
      "v_cvt_pk_bf16_f32 v145, v146, v147\n"
      "v_exp_f32 v245, v245\n"
      "v_exp_f32 v246, v246\n"
      "v_cvt_pk_bf16_f32 v146, v244, v245\n"
      "v_exp_f32 v247, v247\n"
      "v_exp_f32 v184, v184\n"
      "v_cvt_pk_bf16_f32 v147, v246, v247\n"
      "v_exp_f32 v185, v185\n"
      "v_exp_f32 v186, v186\n"
      "v_exp_f32 v187, v187\n"
      "v_exp_f32 v236, v236\n"
      "v_cvt_pk_bf16_f32 v62, v184, v185\n"
      "v_cvt_pk_bf16_f32 v63, v186, v187\n"
      "v_cvt_pk_bf16_f32 v64, v236, 0\n"
      "v_max3_f32 v202, v224, v225, v226\n"
      "v_max3_f32 v203, v227, v252, v253\n"
      "v_mfma_f32_16x16x32_bf16 v[160:163], v[66:69], v[144:147], 0\n"
      "v_alignbyte_b32 v151, v200, v151, v211\n"
      "v_max3_f32 v202, v202, v254, v255\n"
      "v_mfma_f32_16x16x32_bf16 v[160:163], v[66:69], v[62:65], v[160:163]\n"
      "v_alignbyte_b32 v248, v249, v248, v211\n"
      "v_alignbyte_b32 v249, v201, v249, v211\n"
      "v_mfma_f32_16x16x32_bf16 v[164:167], v[216:219], v[144:147], 0\n"
      "v_alignbyte_b32 v204, v205, v204, v211\n"
      "v_max3_f32 v203, v203, v188, v189\n"
      "v_mfma_f32_16x16x32_bf16 v[164:167], v[50:53], v[62:65], v[164:167]\n"
      "v_alignbyte_b32 v205, v206, v205, v211\n"
      "v_alignbyte_b32 v207, v207, v207, v211\n"
      "v_lshlrev_b32 v207, 16, v207\n"
      "v_max3_f32 v202, v202, v190, v191\n"
      "v_max3_f32 v202, v202, v203, v220\n"
      "v_mfma_f32_16x16x32_bf16 v[184:187], v[240:243], v[144:147], 0\n"
      "s_waitcnt lgkmcnt(0)\n"
      "v_bitop3_b32 v180, v180, v181, s4 bitop3:0x7e\n"
      "v_bitop3_b32 v182, v182, v183, s4 bitop3:0x7e\n"
      "v_mfma_f32_16x16x32_bf16 v[184:187], v[54:57], v[62:65], v[184:187]\n"
      "v_or_b32 v180, v180, v182\n"
      "v_cmp_ne_u32 vcc, 0, v180\n"
      "s_nop 0\n"
      "s_mov_b64 s[48:49], vcc\n"
      "s_cbranch_vccnz L_flg_96_%=\n"
      "L_fb_97_%=:\n"
      "v_mov_b32 v203, v202\n"
      "s_nop 1\n"
      "v_permlane16_swap_b32 v202, v203\n"
      "v_max_f32 v202, v202, v203\n"
      "v_mov_b32 v203, v202\n"
      "s_nop 1\n"
      "v_permlane32_swap_b32 v202, v203\n"
      "v_max_f32 v202, v202, v203\n"
      "v_sub_f32 v224, v224, v202\n"
      "v_sub_f32 v225, v225, v202\n"
      "v_sub_f32 v226, v226, v202\n"
      "v_sub_f32 v227, v227, v202\n"
      "v_sub_f32 v252, v252, v202\n"
      "v_sub_f32 v253, v253, v202\n"
      "v_sub_f32 v254, v254, v202\n"
      "v_sub_f32 v255, v255, v202\n"
      "v_sub_f32 v188, v188, v202\n"
      "v_sub_f32 v189, v189, v202\n"
      "v_sub_f32 v190, v190, v202\n"
      "v_sub_f32 v191, v191, v202\n"
      "v_sub_f32 v220, v220, v202\n"
      "v_exp_f32 v224, v224\n"
      "v_exp_f32 v225, v225\n"
      "v_exp_f32 v226, v226\n"
      "v_cvt_pk_bf16_f32 v224, v224, v225\n"
      "v_exp_f32 v227, v227\n"
      "v_exp_f32_e64 v252, v252\n"
      "v_cvt_pk_bf16_f32 v225, v226, v227\n"
      "v_exp_f32 v253, v253\n"
      "v_exp_f32 v254, v254\n"
      "v_cvt_pk_bf16_f32 v226, v252, v253\n"
      "v_exp_f32 v255, v255\n"
      "v_exp_f32 v188, v188\n"
      "v_cvt_pk_bf16_f32 v227, v254, v255\n"
      "v_mfma_f32_16x16x32_bf16 v[192:195], v[232:235], v[144:147], 0\n"
      "ds_read_b128 v[200:203], v119 offset:30720\n"
      "v_mfma_f32_16x16x32_bf16 v[192:195], v[58:61], v[62:65], v[192:195]\n"
      "v_exp_f32 v189, v189\n"
      "v_mfma_f32_16x16x32_bf16 v[144:147], v[212:215], v[196:199], v[102:105]\n"
      "v_exp_f32 v190, v190\n"
      "v_mfma_f32_16x16x32_bf16 v[144:147], v[34:37], v[44:47], v[144:147]\n"
      "v_exp_f32 v191, v191\n"
      "v_mfma_f32_16x16x32_bf16 v[180:183], v[140:143], v[196:199], v[106:109]\n"
      "v_exp_f32 v220, v220\n"
      "v_mfma_f32_16x16x32_bf16 v[180:183], v[40:43], v[44:47], v[180:183]\n"
      "v_cvt_pk_bf16_f32 v62, v188, v189\n"
      "v_cvt_pk_bf16_f32 v63, v190, v191\n"
      "v_mfma_f32_16x16x16_bf16 v[144:147], v[250:251], v[150:151], v[144:147]\n"
      "v_cvt_pk_bf16_f32 v64, v220, 0\n"
      "v_rcp_f32 v160, v160\n"
      "v_mfma_f32_16x16x32_bf16 v[140:143], v[136:139], v[196:199], v[74:77]\n"
      "v_mul_f32 v164, v160, v164\n"
      "v_mul_f32 v165, v160, v165\n"
      "v_mfma_f32_16x16x32_bf16 v[140:143], v[228:231], v[156:159], v[140:143]\n"
      "s_waitcnt lgkmcnt(0)\n"
      "v_cvt_pk_bf16_f32 v164, v164, v165\n"
      "v_mul_f32_e64 v166, v160, v166\n"
      "v_mfma_f32_16x16x32_bf16 v[140:143], v[152:155], v[44:47], v[140:143]\n"
      "global_store_dwordx4 v116, v[200:203], s[54:55] offset:256 sc1\n"
      "v_mfma_f32_16x16x32_bf16 v[188:191], v[156:159], v[196:199], v[110:113]\n"
      "v_mul_f32_e64 v167, v160, v167\n"
      "v_cvt_pk_bf16_f32 v165, v166, v167\n"
      "v_mfma_f32_16x16x32_bf16 v[188:191], v[46:49], v[44:47], v[188:191]\n"
      "v_mul_f32 v184, v160, v184\n"
      "v_mul_f32 v185, v160, v185\n"
      "v_mfma_f32_16x16x32_bf16 v[196:199], v[228:231], v[136:139], v[76:79]\n"
      "v_fmac_f32 v140, s37, v207\n"
      "v_cvt_pk_bf16_f32 v166, v184, v185\n"
      "v_mfma_f32_16x16x16_bf16 v[180:183], v[250:251], v[248:249], v[180:183]\n"
      "ds_read_b128 v[156:159], v120 offset:18432\n"
      "v_mul_f32 v186, v160, v186\n"
      "v_mfma_f32_16x16x16_bf16 v[188:191], v[250:251], v[204:205], v[188:191]\n"
      "v_mul_f32_e64 v187, v160, v187\n"
      "v_cvt_pk_bf16_f32 v167, v186, v187\n"
      "v_mfma_f32_16x16x16_bf16 v[196:199], v[154:155], v[152:153], v[196:199]\n"
      "ds_write_b128 v124, v[164:167]\n"
      "v_mfma_f32_16x16x32_bf16 v[200:203], v[66:69], v[224:227], 0\n"
      "v_mul_f32 v192, v160, v192\n"
      "v_mul_f32 v193, v160, v193\n"
      "v_mfma_f32_16x16x32_bf16 v[200:203], v[66:69], v[62:65], v[200:203]\n"
      "v_cvt_pk_bf16_f32 v192, v192, v193\n"
      "v_mul_f32 v194, v160, v194\n"
      "v_mfma_f32_16x16x32_bf16 v[184:187], v[216:219], v[224:227], 0\n"
      "v_mul_f32_e64 v195, v160, v195\n"
      "v_cvt_pk_bf16_f32 v193, v194, v195\n"
      "v_mfma_f32_16x16x32_bf16 v[184:187], v[50:53], v[62:65], v[184:187]\n"
      "ds_write_b64 v125, v[192:193]\n"
      "v_mfma_f32_16x16x32_bf16 v[164:167], v[240:243], v[224:227], 0\n"
      "s_waitcnt lgkmcnt(2)\n"
      "v_max3_f32 v142, v144, v145, v146\n"
      "v_mfma_f32_16x16x32_bf16 v[164:167], v[54:57], v[62:65], v[164:167]\n"
      "global_store_dwordx4 v117, v[156:159], s[54:55] sc1\n"
      "v_mfma_f32_16x16x32_bf16 v[192:195], v[232:235], v[224:227], 0\n"
      "v_max3_f32 v143, v147, v180, v181\n"
      "v_max3_f32 v142, v142, v182, v183\n"
      "v_mfma_f32_16x16x32_bf16 v[192:195], v[58:61], v[62:65], v[192:195]\n"
      "v_max3_f32 v143, v143, v188, v189\n"
      "v_max3_f32 v142, v142, v190, v191\n"
      "v_max3_f32 v142, v142, v143, v140\n"
      "v_mov_b32 v143, v142\n"
      "s_nop 1\n"
      "v_permlane16_swap_b32 v142, v143\n"
      "v_max_f32 v142, v142, v143\n"
      "v_mov_b32 v143, v142\n"
      "v_rcp_f32 v200, v200\n"
      "s_nop 0\n"
      "v_permlane32_swap_b32 v142, v143\n"
      "v_max_f32 v142, v142, v143\n"
      "v_sub_f32 v144, v144, v142\n"
      "v_sub_f32 v145, v145, v142\n"
      "v_sub_f32 v146, v146, v142\n"
      "v_sub_f32 v147, v147, v142\n"
      "v_sub_f32 v180, v180, v142\n"
      "v_sub_f32 v181, v181, v142\n"
      "v_sub_f32 v182, v182, v142\n"
      "v_sub_f32 v183, v183, v142\n"
      "v_sub_f32 v188, v188, v142\n"
      "v_sub_f32 v189, v189, v142\n"
      "v_sub_f32 v190, v190, v142\n"
      "v_sub_f32 v191, v191, v142\n"
      "v_sub_f32 v140, v140, v142\n"
      "v_exp_f32 v144, v144\n"
      "v_exp_f32 v145, v145\n"
      "v_exp_f32 v146, v146\n"
      "v_cvt_pk_bf16_f32 v144, v144, v145\n"
      "v_exp_f32 v147, v147\n"
      "v_exp_f32 v180, v180\n"
      "v_cvt_pk_bf16_f32 v145, v146, v147\n"
      "v_exp_f32 v181, v181\n"
      "v_exp_f32 v182, v182\n"
      "v_cvt_pk_bf16_f32 v146, v180, v181\n"
      "v_exp_f32 v183, v183\n"
      "v_exp_f32 v188, v188\n"
      "v_cvt_pk_bf16_f32 v147, v182, v183\n"
      "v_exp_f32 v189, v189\n"
      "v_exp_f32 v190, v190\n"
      "v_exp_f32 v191, v191\n"
      "v_exp_f32_e64 v140, v140\n"
      "v_cvt_pk_bf16_f32 v62, v188, v189\n"
      "v_cvt_pk_bf16_f32 v63, v190, v191\n"
      "v_cvt_pk_bf16_f32 v64, v140, 0\n"
      "v_mul_f32 v184, v200, v184\n"
      "v_mul_f32 v185, v200, v185\n"
      "v_mfma_f32_16x16x32_bf16 v[152:155], v[66:69], v[144:147], 0\n"
      "ds_read_b128 v[136:139], v123 offset:18688\n"
      "v_cvt_pk_bf16_f32 v184, v184, v185\n"
      "v_mfma_f32_16x16x32_bf16 v[152:155], v[66:69], v[62:65], v[152:155]\n"
      "ds_read_b128 v[202:205], v123 offset:21760\n"
      "v_mul_f32 v186, v200, v186\n"
      "v_mfma_f32_16x16x32_bf16 v[156:159], v[216:219], v[144:147], 0\n"
      "ds_read_b128 v[148:151], v121 offset:18432\n"
      "v_mul_f32 v187, v200, v187\n"
      "v_mfma_f32_16x16x32_bf16 v[156:159], v[50:53], v[62:65], v[156:159]\n"
      "ds_read_b128 v[212:215], v123 offset:24832\n"
      "v_cvt_pk_bf16_f32 v185, v186, v187\n"
      "v_mfma_f32_16x16x32_bf16 v[160:163], v[240:243], v[144:147], 0\n"
      "ds_read_b128 v[228:231], v123 offset:27904\n"
      "v_mul_f32 v164, v200, v164\n"
      "v_mfma_f32_16x16x32_bf16 v[160:163], v[54:57], v[62:65], v[160:163]\n"
      "v_mul_f32 v165, v200, v165\n"
      "v_cvt_pk_bf16_f32 v186, v164, v165\n"
      "v_mul_f32 v166, v200, v166\n"
      "v_mul_f32 v167, v200, v167\n"
      "v_cvt_pk_bf16_f32 v187, v166, v167\n"
      "v_mfma_f32_16x16x32_bf16 v[164:167], v[232:235], v[144:147], 0\n"
      "ds_read_b128 v[144:147], v123 offset:30976\n"
      "v_mul_f32 v192, v200, v192\n"
      "v_mfma_f32_16x16x32_bf16 v[164:167], v[58:61], v[62:65], v[164:167]\n"
      "ds_write_b128 v124, v[184:187] offset:256\n"
      "s_waitcnt lgkmcnt(5)\n"
      "v_mfma_f32_16x16x32_bf16 v[180:183], a[216:219], v[136:139], 0\n"
      "ds_read_b128 v[252:255], v123 offset:34048\n"
      "v_mul_f32 v193, v200, v193\n"
      "v_mfma_f32_16x16x32_bf16 v[180:183], a[228:231], v[202:205], v[180:183]\n"
      "s_waitcnt lgkmcnt(5)\n"
      "v_cvt_pk_bf16_f32 v192, v192, v193\n"
      "v_mul_f32 v194, v200, v194\n"
      "v_mfma_f32_16x16x32_bf16 v[184:187], a[220:223], v[136:139], 0\n"
      "v_mul_f32 v195, v200, v195\n"
      "s_waitcnt lgkmcnt(3)\n"
      "v_mfma_f32_16x16x32_bf16 v[184:187], a[232:235], v[202:205], v[184:187]\n"
      "s_mov_b32 exec_hi, 0\n"
      "global_store_dwordx4 v118, v[148:151], s[54:55] sc1\n"
      "s_mov_b32 exec_hi, -1\n"
      "v_mfma_f32_16x16x32_bf16 v[188:191], a[224:227], v[136:139], 0\n"
      "s_add_u32 s28, s27, 2\n"
      "v_cvt_pk_bf16_f32 v193, v194, v195\n"
      "v_cndmask_b32 v237, v237, v221, s[42:43]\n"
      "v_mfma_f32_16x16x32_bf16 v[188:191], a[236:239], v[202:205], v[188:191]\n"
      "ds_write_b64 v125, v[192:193] offset:256\n"
      "v_mfma_f32_16x16x32_bf16 v[180:183], a[240:243], v[212:215], v[180:183]\n"
      "ds_read_b128 v[136:139], v123 offset:18944\n"
      "v_cndmask_b32 v237, v237, v141, s[44:45]\n"
      "v_mfma_f32_16x16x32_bf16 v[180:183], a[252:255], v[228:231], v[180:183]\n"
      "ds_read_b128 v[140:143], v123 offset:22016\n"
      "s_waitcnt lgkmcnt(3)\n"
      "v_mfma_f32_16x16x32_bf16 v[184:187], a[244:247], v[212:215], v[184:187]\n"
      "s_min_u32 m0, s28, s3\n"
      "v_cndmask_b32 v237, v237, v197, s[46:47]\n"
      "v_mbcnt_lo_u32_b32 v239, -1, 0\n"
      "v_mfma_f32_16x16x32_bf16 v[184:187], v[0:3], v[228:231], v[184:187]\n"
      "s_movrels_b32 s9, s80\n"
      "v_mbcnt_hi_u32_b32 v239, -1, v239\n"
      "v_min_u32 v239, 48, v239\n"
      "v_mfma_f32_16x16x32_bf16 v[188:191], a[248:251], v[212:215], v[188:191]\n"
      "s_lshr_b32 s8, s9, 8\n"
      "v_lshlrev_b32 v239, 1, v239\n"
      "v_add_u32_e64 v239, s30, v239\n"
      "v_mfma_f32_16x16x32_bf16 v[188:191], v[4:7], v[228:231], v[188:191]\n"
      "ds_read_u16 v197, v239 offset:4704\n"
      "v_mfma_f32_16x16x32_bf16 v[180:183], v[8:11], v[144:147], v[180:183]\n"
      "ds_read_b128 v[148:151], v123 offset:25088\n"
      "s_and_b32 s9, s9, 0xff\n"
      "v_mfma_f32_16x16x32_bf16 v[180:183], v[20:23], v[252:255], v[180:183]\n"
      "ds_read_b128 v[228:231], v123 offset:28160\n"
      "v_mfma_f32_16x16x32_bf16 v[184:187], v[12:15], v[144:147], v[184:187]\n"
      "s_mul_i32 s10, s8, 73\n"
      "s_add_u32 s10, s10, s9\n"
      "s_mul_i32 s28, s10, 4802\n"
      "v_mfma_f32_16x16x32_bf16 v[184:187], v[24:27], v[252:255], v[184:187]\n"
      "s_waitcnt lgkmcnt(3)\n"
      "s_add_u32 s56, s18, s28\n"
      "v_mfma_f32_16x16x32_bf16 v[188:191], v[16:19], v[144:147], v[188:191]\n"
      "s_addc_u32 s57, s19, 0\n"
      "s_and_b32 s56, s56, -16\n"
      "s_sub_u32 s58, s56, 110592\n"
      "v_mfma_f32_16x16x32_bf16 v[188:191], v[28:31], v[252:255], v[188:191]\n"
      "s_subb_u32 s59, s57, 0\n"
      "s_sub_u32 s56, s56, 73728\n"
      "v_mfma_f32_16x16x32_bf16 v[192:195], a[216:219], v[136:139], 0\n"
      "s_subb_u32 s57, s57, 0\n"
      "s_mul_i32 s28, s8, 1373568\n"
      "s_mul_i32 s29, s9, 2688\n"
      "v_mfma_f32_16x16x32_bf16 v[192:195], a[228:231], v[140:143], v[192:195]\n"
      "s_waitcnt lgkmcnt(2)\n"
      "v_lshlrev_b32_e64 v197, 16, v197\n"
      "v_fmac_f32_e64 v237, s37, v197\n"
      "v_mfma_f32_16x16x32_bf16 v[196:199], a[220:223], v[136:139], 0\n"
      "s_add_u32 s28, s28, s29\n"
      "v_max_f32_dpp v239, v237, v237 row_ror:8 row_mask:0xf bank_mask:0xf\n"
      "v_fma_f32 v236, v180, v180, s39\n"
      "v_mfma_f32_16x16x32_bf16 v[196:199], a[232:235], v[140:143], v[196:199]\n"
      "s_waitcnt lgkmcnt(0)\n"
      "v_max_f32_dpp v239, v239, v239 row_ror:4 row_mask:0xf bank_mask:0xf\n"
      "v_fmac_f32_e64 v236, v181, v181\n"
      "v_mfma_f32_16x16x32_bf16 v[200:203], a[224:227], v[136:139], 0\n"
      "ds_read_b128 v[144:147], v123 offset:31232\n"
      "v_max_f32_dpp v239, v239, v239 row_ror:2 row_mask:0xf bank_mask:0xf\n"
      "v_mfma_f32_16x16x32_bf16 v[200:203], a[236:239], v[140:143], v[200:203]\n"
      "ds_read_b128 v[204:207], v123 offset:34304\n"
      "v_max_f32_dpp v239, v239, v239 row_ror:1 row_mask:0xf bank_mask:0xf\n"
      "v_mfma_f32_16x16x32_bf16 v[192:195], a[240:243], v[148:151], v[192:195]\n"
      "s_cmp_eq_u32 s8, 72\n"
      "v_max_f32_dpp v239, v239, v239 row_bcast:15 row_mask:0xa bank_mask:0xf\n"
      "v_fmac_f32_e64 v236, v182, v182\n"
      "v_mfma_f32_16x16x32_bf16 v[192:195], a[252:255], v[228:231], v[192:195]\n"
      "s_cselect_b32 s8, 0, 100270464\n"
      "v_max_f32_dpp v239, v239, v239 row_bcast:31 row_mask:0xc bank_mask:0xf\n"
      "v_fmac_f32_e64 v236, v183, v183\n"
      "v_mfma_f32_16x16x32_bf16 v[196:199], a[244:247], v[148:151], v[196:199]\n"
      "s_cmp_eq_u32 s9, 72\n"
      "v_readlane_b32 s31, v239, 63\n"
      "v_fmac_f32_e64 v236, v184, v184\n"
      "v_mfma_f32_16x16x32_bf16 v[196:199], v[0:3], v[228:231], v[196:199]\n"
      "s_cselect_b32 s9, 0, 196224\n"
      "v_subrev_f32 v237, s31, v237\n"
      "v_exp_f32 v237, v237\n"
      "v_mfma_f32_16x16x32_bf16 v[200:203], a[248:251], v[148:151], v[200:203]\n"
      "s_add_u32 s10, s8, s9\n"
      "v_cvt_pk_bf16_f32 v237, v237, v237\n"
      "v_fmac_f32 v236, v185, v185\n"
      "v_mfma_f32_16x16x32_bf16 v[200:203], v[4:7], v[228:231], v[200:203]\n"
      "ds_write_b16 v130, v237\n"
      "v_fmac_f32 v236, v186, v186\n"
      "v_fmac_f32 v236, v187, v187\n"
      "v_fmac_f32 v236, v188, v188\n"
      "v_fmac_f32 v236, v189, v189\n"
      "v_fmac_f32 v236, v190, v190\n"
      "s_waitcnt lgkmcnt(1)\n"
      "v_mfma_f32_16x16x32_bf16 v[192:195], v[8:11], v[144:147], v[192:195]\n"
      "ds_read_b128 v[212:215], v131\n"
      "v_fmac_f32 v236, v191, v191\n"
      "v_mfma_f32_16x16x32_bf16 v[192:195], v[20:23], v[204:207], v[192:195]\n"
      "ds_read_b128 v[148:151], v131 offset:64\n"
      "v_permlane16_swap_b32 v238, v236\n"
      "v_mfma_f32_16x16x32_bf16 v[196:199], v[12:15], v[144:147], v[196:199]\n"
      "s_add_u32 s28, s28, s10\n"
      "v_add_f32_e64 v238, v238, v236\n"
      "s_add_u32 s52, s16, s28\n"
      "v_mfma_f32_16x16x32_bf16 v[196:199], v[24:27], v[204:207], v[196:199]\n"
      "ds_read_b128 v[136:139], v114 offset:18432\n"
      "v_fma_f32 v237, v192, v192, s39\n"
      "v_mfma_f32_16x16x32_bf16 v[200:203], v[16:19], v[144:147], v[200:203]\n"
      "ds_read_b128 v[140:143], v115 offset:18432\n"
      "v_fmac_f32 v237, v193, v193\n"
      "v_mfma_f32_16x16x32_bf16 v[200:203], v[28:31], v[204:207], v[200:203]\n"
      "s_addc_u32 s53, s17, 0\n"
      "v_fmac_f32 v237, v194, v194\n"
      "v_fmac_f32 v237, v195, v195\n"
      "v_fmac_f32 v237, v196, v196\n"
      "v_fmac_f32 v237, v197, v197\n"
      "v_fmac_f32 v237, v198, v198\n"
      "v_fmac_f32 v237, v199, v199\n"
      "s_nop 0\n"
      "v_fmac_f32 v237, v200, v200\n"
      "v_fmac_f32 v237, v201, v201\n"
      "v_fmac_f32 v237, v202, v202\n"
      "v_fmac_f32 v237, v203, v203\n"
      "v_mov_b32 v236, v237\n"
      "s_add_u32 s27, s27, 1\n"
      "s_nop 0\n"
      "v_permlane16_swap_b32 v237, v236\n"
      "s_waitcnt lgkmcnt(2)\n"
      "v_mfma_f32_16x16x32_bf16 v[204:207], v[212:215], v[66:69], 0\n"
      "s_cmp_ge_u32 s27, s26\n"
      "v_add_f32 v237, v237, v236\n"
      "s_cselect_b32 s28, 1, 0\n"
      "v_mfma_f32_16x16x32_bf16 v[204:207], v[148:151], v[66:69], v[204:207]\n"
      "s_xor_b32 s29, s10, s11\n"
      "v_permlane32_swap_b32 v238, v237\n"
      "v_mfma_f32_16x16x32_bf16 v[220:223], v[212:215], v[216:219], 0\n"
      "s_or_b32 s28, s28, s29\n"
      "v_add_f32 v238, v238, v237\n"
      "s_waitcnt lgkmcnt(0)\n"
      "v_mfma_f32_16x16x32_bf16 v[220:223], v[148:151], v[50:53], v[220:223]\n"
      "ds_write_b32 v129, v238\n"
      "v_mfma_f32_16x16x32_bf16 v[252:255], v[212:215], v[240:243], 0\n"
      "v_mov_b32_dpp v204, v204 quad_perm:[0,0,0,0] row_mask:0xf bank_mask:0xf\n"
      "v_mfma_f32_16x16x32_bf16 v[252:255], v[148:151], v[54:57], v[252:255]\n"
      "v_rcp_f32_e64 v204, v204\n"
      "v_mfma_f32_16x16x32_bf16 v[236:239], v[212:215], v[232:235], 0\n"
      "v_mfma_f32_16x16x32_bf16 v[236:239], v[148:151], v[58:61], v[236:239]\n"
      "v_mfma_f32_16x16x32_bf16 v[144:147], a[0:3], v[136:139], 0\n"
      "v_mfma_f32_16x16x32_bf16 v[144:147], a[36:39], v[140:143], v[144:147]\n"
      "v_mfma_f32_16x16x32_bf16 v[148:151], a[4:7], v[136:139], 0\n"
      "s_nop 1\n"
      "v_cndmask_b32 v220, v220, v252, s[42:43]\n"
      "v_mfma_f32_16x16x32_bf16 v[148:151], a[40:43], v[140:143], v[148:151]\n"
      "s_nop 0\n"
      "v_cndmask_b32 v220, v220, v236, s[44:45]\n"
      "v_mul_f32 v220, v204, v220\n"
      "v_cvt_pk_bf16_f32 v220, v220, v220\n"
      "ds_write_b16 v135, v220\n"
      "s_branch L_join_30_%=\n"
      "L_flg_96_%=:\n"
      "v_cmp_gt_i32 vcc, 0, v180\n"
      "s_cbranch_vccz L_fb_97_%=\n"
      "s_sleep 1\n"
      "ds_read_b128 v[180:183], v128 offset:3072\n"
      "s_waitcnt lgkmcnt(0)\n"
      "v_bitop3_b32 v180, v180, v181, s4 bitop3:0x7e\n"
      "v_bitop3_b32 v182, v182, v183, s4 bitop3:0x7e\n"
      "v_or_b32 v180, v180, v182\n"
      "v_cmp_ne_u32 s[48:49], 0, v180\n"
      "s_branch L_flg_96_%=\n"
      "L_edge_1_%=:\n"
      "s_mov_b32 s11, s10\n"
      "s_sub_u32 s28, s60, s8\n"
      "s_sub_u32 s29, s61, s9\n"
      "s_mov_b32 s60, s8\n"
      "s_mov_b32 s61, s9\n"
      "s_mov_b64 exec, s[64:65]\n"
      "v_add_u32 v116, s28, v116\n"
      "s_mov_b64 exec, s[70:71]\n"
      "v_add_u32 v116, s29, v116\n"
      "s_mov_b64 exec, s[66:67]\n"
      "v_add_u32 v117, s28, v117\n"
      "s_mov_b64 exec, s[72:73]\n"
      "v_add_u32 v117, s29, v117\n"
      "s_mov_b64 exec, s[68:69]\n"
      "v_add_u32 v118, s28, v118\n"
      "s_mov_b64 exec, s[74:75]\n"
      "v_add_u32 v118, s29, v118\n"
      "s_mov_b64 exec, -1\n"
      "s_nop 4\n"
      "s_branch L_eb_2_%=\n"
      "L_flg_3_%=:\n"
      "v_cmp_gt_i32 vcc, 0, v180\n"
      "s_cbranch_vccz L_fb_4_%=\n"
      "s_sleep 1\n"
      "ds_read_b128 v[180:183], v128 offset:2048\n"
      "s_waitcnt lgkmcnt(0)\n"
      "v_bitop3_b32 v180, v180, v181, s4 bitop3:0x7e\n"
      "v_bitop3_b32 v182, v182, v183, s4 bitop3:0x7e\n"
      "v_or_b32 v180, v180, v182\n"
      "v_cmp_ne_u32 s[48:49], 0, v180\n"
      "s_branch L_flg_3_%=\n"
      "L_edge_6_%=:\n"
      "s_mov_b32 s11, s10\n"
      "s_sub_u32 s28, s60, s8\n"
      "s_sub_u32 s29, s61, s9\n"
      "s_mov_b32 s60, s8\n"
      "s_mov_b32 s61, s9\n"
      "s_mov_b64 exec, s[64:65]\n"
      "v_add_u32 v116, s28, v116\n"
      "s_mov_b64 exec, s[70:71]\n"
      "v_add_u32 v116, s29, v116\n"
      "s_mov_b64 exec, s[66:67]\n"
      "v_add_u32 v117, s28, v117\n"
      "s_mov_b64 exec, s[72:73]\n"
      "v_add_u32 v117, s29, v117\n"
      "s_mov_b64 exec, s[68:69]\n"
      "v_add_u32 v118, s28, v118\n"
      "s_mov_b64 exec, s[74:75]\n"
      "v_add_u32 v118, s29, v118\n"
      "s_mov_b64 exec, -1\n"
      "s_nop 4\n"
      "s_branch L_eb_7_%=\n"
      "L_flg_12_%=:\n"
      "v_cmp_gt_i32 vcc, 0, v140\n"
      "s_cbranch_vccz L_fb_13_%=\n"
      "s_sleep 1\n"
      "ds_read_b128 v[140:143], v128 offset:3072\n"
      "s_waitcnt lgkmcnt(0)\n"
      "v_bitop3_b32 v140, v140, v141, s4 bitop3:0x7e\n"
      "v_bitop3_b32 v142, v142, v143, s4 bitop3:0x7e\n"
      "v_or_b32 v140, v140, v142\n"
      "v_cmp_ne_u32 s[48:49], 0, v140\n"
      "s_branch L_flg_12_%=\n"
      "L_edge_15_%=:\n"
      "s_mov_b32 s11, s10\n"
      "s_sub_u32 s28, s60, s8\n"
      "s_sub_u32 s29, s61, s9\n"
      "s_mov_b32 s60, s8\n"
      "s_mov_b32 s61, s9\n"
      "s_mov_b64 exec, s[64:65]\n"
      "v_add_u32 v116, s28, v116\n"
      "s_mov_b64 exec, s[70:71]\n"
      "v_add_u32 v116, s29, v116\n"
      "s_mov_b64 exec, s[66:67]\n"
      "v_add_u32 v117, s28, v117\n"
      "s_mov_b64 exec, s[72:73]\n"
      "v_add_u32 v117, s29, v117\n"
      "s_mov_b64 exec, s[68:69]\n"
      "v_add_u32 v118, s28, v118\n"
      "s_mov_b64 exec, s[74:75]\n"
      "v_add_u32 v118, s29, v118\n"
      "s_mov_b64 exec, -1\n"
      "s_nop 4\n"
      "s_branch L_eb_16_%=\n"
      "L_edge_17_%=:\n"
      "s_mov_b32 s11, s10\n"
      "s_sub_u32 s28, s60, s8\n"
      "s_sub_u32 s29, s61, s9\n"
      "s_mov_b32 s60, s8\n"
      "s_mov_b32 s61, s9\n"
      "s_mov_b64 exec, s[64:65]\n"
      "v_add_u32 v116, s28, v116\n"
      "s_mov_b64 exec, s[70:71]\n"
      "v_add_u32 v116, s29, v116\n"
      "s_mov_b64 exec, s[66:67]\n"
      "v_add_u32 v117, s28, v117\n"
      "s_mov_b64 exec, s[72:73]\n"
      "v_add_u32 v117, s29, v117\n"
      "s_mov_b64 exec, s[68:69]\n"
      "v_add_u32 v118, s28, v118\n"
      "s_mov_b64 exec, s[74:75]\n"
      "v_add_u32 v118, s29, v118\n"
      "s_mov_b64 exec, -1\n"
      "s_nop 4\n"
      "s_branch L_eb_18_%=\n"
      "L_flg_23_%=:\n"
      "v_cmp_gt_i32 vcc, 0, v208\n"
      "s_cbranch_vccz L_fb_24_%=\n"
      "s_sleep 1\n"
      "ds_read_b128 v[208:211], v128 offset:2048\n"
      "s_waitcnt lgkmcnt(0)\n"
      "v_bitop3_b32 v208, v208, v209, s4 bitop3:0x7e\n"
      "v_bitop3_b32 v210, v210, v211, s4 bitop3:0x7e\n"
      "v_or_b32 v208, v208, v210\n"
      "v_cmp_ne_u32 s[48:49], 0, v208\n"
      "s_branch L_flg_23_%=\n"
      "L_edge_26_%=:\n"
      "s_mov_b32 s11, s10\n"
      "s_sub_u32 s28, s60, s8\n"
      "s_sub_u32 s29, s61, s9\n"
      "s_mov_b32 s60, s8\n"
      "s_mov_b32 s61, s9\n"
      "s_mov_b64 exec, s[64:65]\n"
      "v_add_u32 v116, s28, v116\n"
      "s_mov_b64 exec, s[70:71]\n"
      "v_add_u32 v116, s29, v116\n"
      "s_mov_b64 exec, s[66:67]\n"
      "v_add_u32 v117, s28, v117\n"
      "s_mov_b64 exec, s[72:73]\n"
      "v_add_u32 v117, s29, v117\n"
      "s_mov_b64 exec, s[68:69]\n"
      "v_add_u32 v118, s28, v118\n"
      "s_mov_b64 exec, s[74:75]\n"
      "v_add_u32 v118, s29, v118\n"
      "s_mov_b64 exec, -1\n"
      "s_nop 4\n"
      "s_branch L_eb_27_%=\n"
      "L_flg_32_%=:\n"
      "v_cmp_gt_i32 vcc, 0, v148\n"
      "s_cbranch_vccz L_fb_33_%=\n"
      "s_sleep 1\n"
      "ds_read_b128 v[148:151], v128 offset:3072\n"
      "s_waitcnt lgkmcnt(0)\n"
      "v_bitop3_b32 v148, v148, v149, s4 bitop3:0x7e\n"
      "v_bitop3_b32 v150, v150, v151, s4 bitop3:0x7e\n"
      "v_or_b32 v148, v148, v150\n"
      "v_cmp_ne_u32 s[48:49], 0, v148\n"
      "s_branch L_flg_32_%=\n"
      "L_edge_35_%=:\n"
      "s_mov_b32 s11, s10\n"
      "s_sub_u32 s28, s60, s8\n"
      "s_sub_u32 s29, s61, s9\n"
      "s_mov_b32 s60, s8\n"
      "s_mov_b32 s61, s9\n"
      "s_mov_b64 exec, s[64:65]\n"
      "v_add_u32 v116, s28, v116\n"
      "s_mov_b64 exec, s[70:71]\n"
      "v_add_u32 v116, s29, v116\n"
      "s_mov_b64 exec, s[66:67]\n"
      "v_add_u32 v117, s28, v117\n"
      "s_mov_b64 exec, s[72:73]\n"
      "v_add_u32 v117, s29, v117\n"
      "s_mov_b64 exec, s[68:69]\n"
      "v_add_u32 v118, s28, v118\n"
      "s_mov_b64 exec, s[74:75]\n"
      "v_add_u32 v118, s29, v118\n"
      "s_mov_b64 exec, -1\n"
      "s_nop 4\n"
      "s_branch L_eb_36_%=\n"
      "L_edge_37_%=:\n"
      "s_mov_b32 s11, s10\n"
      "s_sub_u32 s28, s60, s8\n"
      "s_sub_u32 s29, s61, s9\n"
      "s_mov_b32 s60, s8\n"
      "s_mov_b32 s61, s9\n"
      "s_mov_b64 exec, s[64:65]\n"
      "v_add_u32 v116, s28, v116\n"
      "s_mov_b64 exec, s[70:71]\n"
      "v_add_u32 v116, s29, v116\n"
      "s_mov_b64 exec, s[66:67]\n"
      "v_add_u32 v117, s28, v117\n"
      "s_mov_b64 exec, s[72:73]\n"
      "v_add_u32 v117, s29, v117\n"
      "s_mov_b64 exec, s[68:69]\n"
      "v_add_u32 v118, s28, v118\n"
      "s_mov_b64 exec, s[74:75]\n"
      "v_add_u32 v118, s29, v118\n"
      "s_mov_b64 exec, -1\n"
      "s_nop 4\n"
      "s_branch L_eb_38_%=\n"
      "L_edge_39_%=:\n"
      "s_mov_b32 s11, s10\n"
      "s_sub_u32 s28, s60, s8\n"
      "s_sub_u32 s29, s61, s9\n"
      "s_mov_b32 s60, s8\n"
      "s_mov_b32 s61, s9\n"
      "s_mov_b64 exec, s[64:65]\n"
      "v_add_u32 v116, s28, v116\n"
      "s_mov_b64 exec, s[70:71]\n"
      "v_add_u32 v116, s29, v116\n"
      "s_mov_b64 exec, s[66:67]\n"
      "v_add_u32 v117, s28, v117\n"
      "s_mov_b64 exec, s[72:73]\n"
      "v_add_u32 v117, s29, v117\n"
      "s_mov_b64 exec, s[68:69]\n"
      "v_add_u32 v118, s28, v118\n"
      "s_mov_b64 exec, s[74:75]\n"
      "v_add_u32 v118, s29, v118\n"
      "s_mov_b64 exec, -1\n"
      "s_nop 4\n"
      "s_branch L_eb_40_%=\n"
      "L_edge_42_%=:\n"
      "s_mov_b32 s11, s10\n"
      "s_sub_u32 s28, s60, s8\n"
      "s_sub_u32 s29, s61, s9\n"
      "s_mov_b32 s60, s8\n"
      "s_mov_b32 s61, s9\n"
      "s_mov_b64 exec, s[64:65]\n"
      "v_add_u32 v116, s28, v116\n"
      "s_mov_b64 exec, s[70:71]\n"
      "v_add_u32 v116, s29, v116\n"
      "s_mov_b64 exec, s[66:67]\n"
      "v_add_u32 v117, s28, v117\n"
      "s_mov_b64 exec, s[72:73]\n"
      "v_add_u32 v117, s29, v117\n"
      "s_mov_b64 exec, s[68:69]\n"
      "v_add_u32 v118, s28, v118\n"
      "s_mov_b64 exec, s[74:75]\n"
      "v_add_u32 v118, s29, v118\n"
      "s_mov_b64 exec, -1\n"
      "s_nop 4\n"
      "s_branch L_eb_43_%=\n"
      "L_edge_44_%=:\n"
      "s_mov_b32 s11, s10\n"
      "s_sub_u32 s28, s60, s8\n"
      "s_sub_u32 s29, s61, s9\n"
      "s_mov_b32 s60, s8\n"
      "s_mov_b32 s61, s9\n"
      "s_mov_b64 exec, s[64:65]\n"
      "v_add_u32 v116, s28, v116\n"
      "s_mov_b64 exec, s[70:71]\n"
      "v_add_u32 v116, s29, v116\n"
      "s_mov_b64 exec, s[66:67]\n"
      "v_add_u32 v117, s28, v117\n"
      "s_mov_b64 exec, s[72:73]\n"
      "v_add_u32 v117, s29, v117\n"
      "s_mov_b64 exec, s[68:69]\n"
      "v_add_u32 v118, s28, v118\n"
      "s_mov_b64 exec, s[74:75]\n"
      "v_add_u32 v118, s29, v118\n"
      "s_mov_b64 exec, -1\n"
      "s_nop 4\n"
      "s_branch L_eb_45_%=\n"
      "L_edge_46_%=:\n"
      "s_mov_b32 s11, s10\n"
      "s_sub_u32 s28, s60, s8\n"
      "s_sub_u32 s29, s61, s9\n"
      "s_mov_b32 s60, s8\n"
      "s_mov_b32 s61, s9\n"
      "s_mov_b64 exec, s[64:65]\n"
      "v_add_u32 v116, s28, v116\n"
      "s_mov_b64 exec, s[70:71]\n"
      "v_add_u32 v116, s29, v116\n"
      "s_mov_b64 exec, s[66:67]\n"
      "v_add_u32 v117, s28, v117\n"
      "s_mov_b64 exec, s[72:73]\n"
      "v_add_u32 v117, s29, v117\n"
      "s_mov_b64 exec, s[68:69]\n"
      "v_add_u32 v118, s28, v118\n"
      "s_mov_b64 exec, s[74:75]\n"
      "v_add_u32 v118, s29, v118\n"
      "s_mov_b64 exec, -1\n"
      "s_nop 4\n"
      "s_branch L_eb_47_%=\n"
      "L_cend_%=:\n"
      :
      : "s"(__builtin_amdgcn_kernarg_segment_ptr()), "s"(wave), "s"(blockIdx.x), "s"(CT_TAB), "s"(lds0)
      : "v0","v1","v2","v3","v4","v5","v6","v7","v8","v9","v10","v11","v12","v13","v14","v15","v16","v17","v18","v19","v20","v21","v22","v23","v24","v25","v26","v27","v28","v29","v30","v31","v32","v33","v34","v35","v36","v37","v38","v39","v40","v41","v42","v43","v44","v45","v46","v47","v48","v49","v50","v51","v52","v53","v54","v55","v56","v57","v58","v59","v60","v61","v62","v63","v64","v65","v66","v67","v68","v69","v70","v71","v72","v73","v74","v75","v76","v77","v78","v79","v80","v81","v82","v83","v84","v85","v86","v87","v88","v89","v90","v91","v92","v93","v94","v95","v96","v97","v98","v99","v100","v101","v102","v103","v104","v105","v106","v107","v108","v109","v110","v111","v112","v113","v114","v115","v116","v117","v118","v119","v120","v121","v122","v123","v124","v125","v126","v127","v128","v129","v130","v131","v132","v133","v134","v135","v136","v137","v138","v139","v140","v141","v142","v143","v144","v145","v146","v147","v148","v149","v150","v151","v152","v153","v154","v155","v156","v157","v158","v159","v160","v161","v162","v163","v164","v165","v166","v167","v168","v169","v170","v171","v172","v173","v174","v175","v176","v177","v178","v179","v180","v181","v182","v183","v184","v185","v186","v187","v188","v189","v190","v191","v192","v193","v194","v195","v196","v197","v198","v199","v200","v201","v202","v203","v204","v205","v206","v207","v208","v209","v210","v211","v212","v213","v214","v215","v216","v217","v218","v219","v220","v221","v222","v223","v224","v225","v226","v227","v228","v229","v230","v231","v232","v233","v234","v235","v236","v237","v238","v239","v240","v241","v242","v243","v244","v245","v246","v247","v248","v249","v250","v251","v252","v253","v254","v255","a0","a1","a2","a3","a4","a5","a6","a7","a8","a9","a10","a11","a12","a13","a14","a15","a16","a17","a18","a19","a20","a21","a22","a23","a24","a25","a26","a27","a28","a29","a30","a31","a32","a33","a34","a35","a36","a37","a38","a39","a40","a41","a42","a43","a44","a45","a46","a47","a48","a49","a50","a51","a52","a53","a54","a55","a56","a57","a58","a59","a60","a61","a62","a63","a64","a65","a66","a67","a68","a69","a70","a71","a72","a73","a74","a75","a76","a77","a78","a79","a80","a81","a82","a83","a84","a85","a86","a87","a88","a89","a90","a91","a92","a93","a94","a95","a96","a97","a98","a99","a100","a101","a102","a103","a104","a105","a106","a107","a108","a109","a110","a111","a112","a113","a114","a115","a116","a117","a118","a119","a120","a121","a122","a123","a124","a125","a126","a127","a128","a129","a130","a131","a132","a133","a134","a135","a136","a137","a138","a139","a140","a141","a142","a143","a144","a145","a146","a147","a148","a149","a150","a151","a152","a153","a154","a155","a156","a157","a158","a159","a160","a161","a162","a163","a164","a165","a166","a167","a168","a169","a170","a171","a172","a173","a174","a175","a176","a177","a178","a179","a180","a181","a182","a183","a184","a185","a186","a187","a188","a189","a190","a191","a192","a193","a194","a195","a196","a197","a198","a199","a200","a201","a202","a203","a204","a205","a206","a207","a208","a209","a210","a211","a212","a213","a214","a215","a216","a217","a218","a219","a220","a221","a222","a223","a224","a225","a226","a227","a228","a229","a230","a231","a232","a233","a234","a235","a236","a237","a238","a239","a240","a241","a242","a243","a244","a245","a246","a247","a248","a249","a250","a251","a252","a253","a254","a255","s2","s3","s4","s5","s6","s7","s8","s9","s10","s11","s12","s13","s16","s17","s18","s19","s20","s21","s22","s23","s24","s25","s26","s27","s28","s29","s30","s31","s34","s35","s36","s37","s38","s39","s40","s41","s42","s43","s44","s45","s46","s47","s48","s49","s50","s51","s52","s53","s54","s55","s60","s61","s62","s63","s64","s65","s66","s67","s68","s69","s70","s71","s72","s73","s74","s75","s76","s77","s78","s79","s80","s81","s82","s83","s84","s85","s86","s87","s88","s89","s90","s91","s92","s93","s94","s95","s96","s97","s98","s99","s100","s101", "vcc", "scc", "memory");
}

}

extern "C" void kernel_launch(void* const* d_in, const int* in_sizes, int n_in, void* d_out, int out_size, void* d_ws,
                              size_t ws_size, hipStream_t stream) {
  const uint16_t* x = (const uint16_t*)d_in[0];
  const uint16_t* wqkv = (const uint16_t*)d_in[1];
  const uint16_t* wo = (const uint16_t*)d_in[2];
  const uint16_t* ls = (const uint16_t*)d_in[3];
  const uint16_t* w1 = (const uint16_t*)d_in[4];
  const uint16_t* w2 = (const uint16_t*)d_in[5];
  const uint16_t* rc = (const uint16_t*)d_in[6];
  const int* ridx = (const int*)d_in[7];
  const uint16_t* mask = (const uint16_t*)d_in[8];
  swin::swin_main<<<256, 256, 0, stream>>>(x, mask, (char*)d_ws, (uint16_t*)d_out, wqkv, wo, ls, w1, w2, rc, ridx);
}
```

```cpp
#include <hip/hip_runtime.h>
#include <hip/hip_bf16.h>
#include <stdint.h>

namespace swin {

constexpr unsigned LDS_BYTES = 163840u;

__device__ __attribute__((aligned(256))) const unsigned CT_TAB[26704] = {0,64,0,0xc0000,144,208,16,0xc0010,288,352,32,0xc0020,432,496,48,0xc0030,576,512,64,0xc0040,720,656,80,0xc0050,864,800,96,0xc0060,1008,944,112,0xc0070,1024,1088,400,0xc0190,1168,1232,384,0xc0180,1312,1376,432,0xc01b0,1456,1520,416,0xc01a0,1600,1536,464,0xc01d0,1744,1680,448,0xc01c0,1888,1824,496,0xc01f0,2032,1968,480,0xc01e0,16,80,800,0xc0320,128,192,816,0xc0330,304,368,768,0xc0300,416,480,784,0xc0310,592,528,864,0xc0360,704,640,880,0xc0370,880,816,832,0xc0340,992,928,848,0xc0350,1040,1104,1200,0xef8b0,1152,1216,1184,0xef8a0,1328,1392,1168,0xef890,1440,1504,1152,0xef880,1616,1552,1264,0xef8f0,1728,1664,1248,0xef8e0,1904,1840,1232,0xef8d0,2016,1952,1216,0xef8c0,32,96,1600,0xefa40,176,240,1616,0xefa50,256,320,1632,0xefa60,400,464,1648,0xefa70,608,544,1536,0xefa00,752,688,1552,0xefa10,832,768,1568,0xefa20,976,912,1584,0xefa30,1056,1120,2000,0xefbd0,1200,1264,1984,0xefbc0,1280,1344,2032,0xefbf0,1424,1488,2016,0xefbe0,1632,1568,1936,0xefb90,1776,1712,1920,0xefb80,1856,1792,1968,0xefbb0,2000,1936,1952,0xefba0,48,112,2400,0xefd60,160,224,2416,0xefd70,272,336,2368,0xefd40,384,448,2384,0xefd50,624,560,2336,0xefd20,736,672,2352,0xefd30,848,784,2304,0xefd00,960,896,2320,0xefd10,1072,1136,0x2fef0,0xefef0,1184,1248,0x2fee0,0xefee0,1296,1360,0x2fed0,0xefed0,1408,1472,0x2fec0,0xefec0,1648,1584,0x2feb0,0xefeb0,1760,1696,0x2fea0,0xefea0,1872,1808,0x2fe90,0xefe90,1984,1920,0x2fe80,0xefe80,0xc0100,73728,77824,90112,0xc0110,73744,77840,90128,0xc0120,73760,77856,90144,0xc0130,73776,77872,90160,0xc0140,73792,77888,90176,0xc0150,73808,77904,90192,0xc0160,73824,77920,90208,0xc0170,73840,77936,90224,0xc0290,73856,77952,90240,0xc0280,73872,77968,90256,0xc02b0,73888,77984,90272,0xc02a0,73904,78000,90288,0xc02d0,73920,78016,90304,0xc02c0,73936,78032,90320,0xc02f0,73952,78048,90336,0xc02e0,73968,78064,90352,0xc0420,73984,78080,90368,0xc0430,74000,78096,90384,0xc0400,74016,78112,90400,0xc0410,74032,78128,90416,0xc0460,74048,78144,90432,0xc0470,74064,78160,90448,0xc0440,74080,78176,90464,0xc0450,74096,78192,90480,0xef9b0,74112,78208,90496,0xef9a0,74128,78224,90512,0xef990,74144,78240,90528,0xef980,74160,78256,90544,0xef9f0,74176,78272,90560,0xef9e0,74192,78288,90576,0xef9d0,74208,78304,90592,0xef9c0,74224,78320,90608,0xc0100,74240,78336,90624,0xc0110,74256,78352,90640,0xc0120,74272,78368,90656,0xc0130,74288,78384,90672,0xc0140,74304,78400,90688,0xc0150,74320,78416,90704,0xc0160,74336,78432,90720,0xc0170,74352,78448,90736,0xc0290,74368,78464,90752,0xc0280,74384,78480,90768,0xc02b0,74400,78496,90784,0xc02a0,74416,78512,90800,0xc02d0,74432,78528,90816,0xc02c0,74448,78544,90832,0xc02f0,74464,78560,90848,0xc02e0,74480,78576,90864,0xc0420,74496,78592,90880,0xc0430,74512,78608,90896,0xc0400,74528,78624,90912,0xc0410,74544,78640,90928,0xc0460,74560,78656,90944,0xc0470,74576,78672,90960,0xc0440,74592,78688,90976,0xc0450,74608,78704,90992,0xef9b0,74624,78720,91008,0xef9a0,74640,78736,91024,0xef990,74656,78752,91040,0xef980,74672,78768,91056,0xef9f0,74688,78784,91072,0xef9e0,74704,78800,91088,0xef9d0,74720,78816,91104,0xef9c0,74736,78832,91120,0x1c000,36864,36864,49152,0x1c004,36880,36880,49168,0x1c008,36896,36896,49184,0x1c00c,36912,36912,49200,0x1c010,36928,36928,49216,0x1c014,36944,36944,49232,0x1c018,36960,36960,49248,0x1c01c,36976,36976,49264,0x1c020,36992,36992,49280,0x1c024,37008,37008,49296,0x1c028,37024,37024,49312,0x1c02c,37040,37040,49328,0x1c030,37056,37056,49344,0x1c034,37072,37072,49360,0x1c038,37088,37088,49376,0x1c03c,37104,37104,49392,0x1c040,37120,37120,49408,0x1c044,37136,37136,49424,0x1c048,37152,37152,49440,0x1c04c,37168,37168,49456,0x1c050,37184,37184,49472,0x1c054,37200,37200,49488,0x1c058,37216,37216,49504,0x1c05c,37232,37232,49520,0x1c060,37248,37248,49536,0x1c064,37264,37264,49552,0x1c068,37280,37280,49568,0x1c06c,37296,37296,49584,0x1c070,37312,37312,49600,0x1c074,37328,37328,49616,0x1c078,37344,37344,49632,0x1c07c,37360,37360,49648,0x1c080,37376,37376,49664,0x1c084,37392,37392,49680,0x1c088,37408,37408,49696,0x1c08c,37424,37424,49712,0x1c090,37440,37440,49728,0x1c094,37456,37456,49744,0x1c098,37472,37472,49760,0x1c09c,37488,37488,49776,0x1c0a0,37504,37504,49792,0x1c0a4,37520,37520,49808,0x1c0a8,37536,37536,49824,0x1c0ac,37552,37552,49840,0x1c0b0,37568,37568,49856,0x1c0b4,37584,37584,49872,0x1c0b8,37600,37600,49888,0x1c0bc,37616,37616,49904,0x1c0c0,37632,37632,49920,0x1c0c4,37648,37648,49936,0x1c0c8,37664,37664,49952,0x1c0cc,37680,37680,49968,0x1c0d0,37696,37696,49984,0x1c0d4,37712,37712,50000,0x1c0d8,37728,37728,50016,0x1c0dc,37744,37744,50032,0x1c0e0,37760,37760,50048,0x1c0e4,37776,37776,50064,0x1c0e8,37792,37792,50080,0x1c0ec,37808,37808,50096,0x1c0f0,37824,37824,50112,0x1c0f4,37840,37840,50128,0x1c0f8,37856,37856,50144,0x1c0fc,37872,37872,50160,73728,73792,0x1d600,0x1d600,73872,73936,0x1d610,0x1d610,74016,74080,0x1d620,0x1d620,74160,74224,0x1d630,0x1d630,74304,74240,0x1d640,0x1d640,74448,74384,0x1d650,0x1d650,74592,74528,0x1d660,0x1d660,74736,74672,0x1d670,0x1d670,74752,74816,0x1d680,0x1d680,74896,74960,0x1d690,0x1d690,75040,75104,0x1d6a0,0x1d6a0,75184,75248,0x1d6b0,0x1d6b0,75328,75264,0x1d6c0,0x1d6c0,75472,75408,0x1d6d0,0x1d6d0,75616,75552,0x1d6e0,0x1d6e0,75760,75696,0x1d6f0,0x1d6f0,73744,73800,0x1d700,0x1d700,73856,73944,0x1d710,0x1d710,74032,74088,0x1d720,0x1d720,74144,74232,0x1d730,0x1d730,74320,74248,0x1d740,0x1d740,74432,74392,0x1d750,0x1d750,74608,74536,0x1d760,0x1d760,74720,74680,0x1d770,0x1d770,74768,74824,0x1d780,0x1d780,74880,74968,0x1d790,0x1d790,75056,75112,0x1d7a0,0x1d7a0,75168,75256,0x1d7b0,0x1d7b0,75344,75272,0x1d7c0,0x1d7c0,75456,75416,0x1d7d0,0x1d7d0,75632,75560,0x1d7e0,0x1d7e0,75744,75704,0x1d7f0,0x1d7f0,73760,73808,0x1d800,0x1d800,73904,73920,0x1d810,0x1d810,73984,74096,0x1d820,0x1d820,74128,74208,0x1d830,0x1d830,74336,74256,0x1d840,0x1d840,74480,74368,0x1d850,0x1d850,74560,74544,0x1d860,0x1d860,74704,74656,0x1d870,0x1d870,74784,74832,0x1d880,0x1d880,74928,74944,0x1d890,0x1d890,75008,75120,0x1d8a0,0x1d8a0,75152,75232,0x1d8b0,0x1d8b0,75360,75280,0x1d8c0,0x1d8c0,75504,75392,0x1d8d0,0x1d8d0,75584,75568,0x1d8e0,0x1d8e0,75728,75680,0x1d8f0,0x1d8f0,73776,73816,0x1d900,0x1d900,73888,73928,0x1d910,0x1d910,74000,74104,0x1d920,0x1d920,74112,74216,0x1d930,0x1d930,74352,74264,0x1d940,0x1d940,74464,74376,0x1d950,0x1d950,74576,74552,0x1d960,0x1d960,74688,74664,0x1d970,0x1d970,74800,74840,0x1d980,0x1d980,74912,74952,0x1d990,0x1d990,75024,75128,0x1d9a0,0x1d9a0,75136,75240,0x1d9b0,0x1d9b0,75376,75288,0x1d9c0,0x1d9c0,75488,75400,0x1d9d0,0x1d9d0,75600,75576,0x1d9e0,0x1d9e0,75712,75688,0x1d9f0,0x1d9f0,0x1e600,0x1e600,0x1e800,0x100000,0x1e602,0x100000,0x100000,0x1e800,0x1e604,0x100000,0x100000,0x100000,0x1e606,0x100000,0x100000,0x100000,0x1e610,0x1e600,0x100000,0x100000,0x1e612,0x100000,0x100000,0x1e800,0x1e614,0x100000,0x100000,0x100000,0x1e616,0x100000,0x100000,0x100000,0x1e620,0x1e600,0x100000,0x100000,0x1e622,0x100000,0x100000,0x1e800,0x1e624,0x100000,0x100000,0x100000,0x1e626,0x100000,0x100000,0x100000,0x1e630,0x1e600,0x100000,0x100000,0x1e632,0x100000,0x100000,0x1e800,0x1e634,0x100000,0x100000,0x100000,0x1e636,0x100000,0x100000,0x100000,0x1e608,0x1e610,0x1e830,0x100000,0x1e60a,0x100000,0x100000,0x1e830,0x1e60c,0x100000,0x100000,0x100000,0x1e60e,0x100000,0x100000,0x100000,0x1e618,0x1e610,0x100000,0x100000,0x1e61a,0x100000,0x100000,0x1e830,0x1e61c,0x100000,0x100000,0x100000,0x1e61e,0x100000,0x100000,0x100000,0x1e628,0x1e610,0x100000,0x100000,0x1e62a,0x100000,0x100000,0x1e830,0x1e62c,0x100000,0x100000,0x100000,0x1e62e,0x100000,0x100000,0x100000,0x1e638,0x1e610,0x100000,0x100000,0x1e63a,0x100000,0x100000,0x1e830,0x1e63c,0x100000,0x100000,0x100000,0x1e63e,0x100000,0x100000,0x100000,0x1e640,0x1e620,0x1e860,0x100000,0x1e642,0x100000,0x100000,0x1e860,0x1e644,0x100000,0x100000,0x100000,0x1e646,0x100000,0x100000,0x100000,0x1e650,0x1e620,0x100000,0x100000,0x1e652,0x100000,0x100000,0x1e860,0x1e654,0x100000,0x100000,0x100000,0x1e656,0x100000,0x100000,0x100000,0x1e660,0x1e620,0x100000,0x100000,0x1e662,0x100000,0x100000,0x1e860,0x1e664,0x100000,0x100000,0x100000,0x1e666,0x100000,0x100000,0x100000,0x1e670,0x1e620,0x100000,0x100000,0x1e672,0x100000,0x100000,0x1e860,0x1e674,0x100000,0x100000,0x100000,0x1e676,0x100000,0x100000,0x100000,0x1e648,0x1e630,0x1e890,0x100000,0x1e64a,0x100000,0x100000,0x1e890,0x1e64c,0x100000,0x100000,0x100000,0x1e64e,0x100000,0x100000,0x100000,0x1e658,0x1e630,0x100000,0x100000,0x1e65a,0x100000,0x100000,0x1e890,0x1e65c,0x100000,0x100000,0x100000,0x1e65e,0x100000,0x100000,0x100000,0x1e668,0x1e630,0x100000,0x100000,0x1e66a,0x100000,0x100000,0x1e890,0x1e66c,0x100000,0x100000,0x100000,0x1e66e,0x100000,0x100000,0x100000,0x1e678,0x1e630,0x100000,0x100000,0x1e67a,0x100000,0x100000,0x1e890,0x1e67c,0x100000,0x100000,0x100000,0x1e67e,0x100000,0x100000,0x100000,0x1eb00,0x24c70,36864,36928,0x1eb02,0x24c72,36880,36936,0x1eb04,0x24c74,36896,36944,0x1eb06,0x24c76,36912,36952,0x1eb08,0x24dd0,37248,37312,0x1eb0a,0x24dd2,37264,37320,0x1eb0c,0x24dd4,37280,37328,0x1eb0e,0x24dd6,37296,37336,0x1eb10,0x24f30,37664,37728,0x1eb12,0x24f32,37680,37736,0x1eb14,0x24f34,37632,37744,0x1eb16,0x24f36,37648,37752,0x1eb18,0x25090,38048,38112,0x1eb1a,0x25092,38064,38120,0x1eb1c,0x25094,38016,38128,0x1eb1e,0x25096,38032,38136,0x1eb00,0x24c78,38464,38400,0x1eb02,0x24c7a,38480,38408,0x1eb04,0x24c7c,38496,38416,0x1eb06,0x24c7e,38512,38424,0x1eb08,0x24dd8,38848,38784,0x1eb0a,0x24dda,38864,38792,0x1eb0c,0x24ddc,38880,38800,0x1eb0e,0x24dde,38896,38808,0x1eb10,0x24f38,39264,39200,0x1eb12,0x24f3a,39280,39208,0x1eb14,0x24f3c,39232,39216,0x1eb16,0x24f3e,39248,39224,0x1eb18,0x25098,39648,39584,0x1eb1a,0x2509a,39664,39592,0x1eb1c,0x2509c,39616,39600,0x1eb1e,0x2509e,39632,39608,0x1eb00,0x26270,39936,40000,0x1eb02,0x26272,39952,40008,0x1eb04,0x26274,39968,40016,0x1eb06,0x26276,39984,40024,0x1eb08,0x263d0,40320,40384,0x1eb0a,0x263d2,40336,40392,0x1eb0c,0x263d4,40352,40400,0x1eb0e,0x263d6,40368,40408,0x1eb10,0x26530,40736,40800,0x1eb12,0x26532,40752,40808,0x1eb14,0x26534,40704,40816,0x1eb16,0x26536,40720,40824,0x1eb18,0x26690,41120,41184,0x1eb1a,0x26692,41136,41192,0x1eb1c,0x26694,41088,41200,0x1eb1e,0x26696,41104,41208,0x1eb00,0x100000,41536,41472,0x1eb02,0x100000,41552,41480,0x1eb04,0x100000,41568,41488,0x1eb06,0x100000,41584,41496,0x1eb08,0x100000,41920,41856,0x1eb0a,0x100000,41936,41864,0x1eb0c,0x100000,41952,41872,0x1eb0e,0x100000,41968,41880,0x1eb10,0x100000,42336,42272,0x1eb12,0x100000,42352,42280,0x1eb14,0x100000,42304,42288,0x1eb16,0x100000,42320,42296,0x1eb18,0x100000,42720,42656,0x1eb1a,0x100000,42736,42664,0x1eb1c,0x100000,42688,42672,0x1eb1e,0x100000,42704,42680,36864,36896,36928,36864,36872,36904,36936,36880,36880,36912,36944,36896,36888,36920,36952,36912,38016,38048,38080,36928,38024,38056,38088,36944,38032,38064,38096,36960,38040,38072,38104,36976,39200,39168,39264,36992,39208,39176,39272,37008,39216,39184,39280,37024,39224,39192,39288,37040,40352,40320,40416,37056,40360,40328,40424,37072,40368,40336,40432,37088,40376,40344,40440,37104,46144,46176,46080,37120,46152,46184,46088,37136,46160,46192,46096,37152,46168,46200,46104,37168,47296,47328,47232,37184,47304,47336,47240,37200,47312,47344,47248,37216,47320,47352,47256,37232,48480,48448,48416,37248,48488,48456,48424,37264,48496,48464,48432,37280,48504,48472,48440,37296,49632,49600,49568,37312,49640,49608,49576,37328,49648,49616,49584,37344,49656,49624,49592,37360,55296,55328,55360,37376,55304,55336,55368,37392,55312,55344,55376,37408,55320,55352,55384,37424,56448,56480,56512,37440,56456,56488,56520,37456,56464,56496,56528,37472,56472,56504,56536,37488,57632,57600,57696,37504,57640,57608,57704,37520,57648,57616,57712,37536,57656,57624,57720,37552,58784,58752,58848,37568,58792,58760,58856,37584,58800,58768,58864,37600,58808,58776,58872,37616,64576,64608,64512,37632,64584,64616,64520,37648,64592,64624,64528,37664,64600,64632,64536,37680,65728,65760,65664,37696,65736,65768,65672,37712,65744,65776,65680,37728,65752,65784,65688,37744,66912,66880,66848,37760,66920,66888,66856,37776,66928,66896,66864,37792,66936,66904,66872,37808,68064,68032,68000,37824,68072,68040,68008,37840,68080,68048,68016,37856,68088,68056,68024,37872,0x1e800,0x1e800,0x20cc0,0,0x1e8c4,0x1e8c4,0x20cc4,16,0x1e988,0x1e988,0x20cc8,32,0x1ea4c,0x1ea4c,0x20ccc,48,0x1eb10,0x1eb10,0x20cd0,64,0x1ebd4,0x1ebd4,0x20cd4,80,0x1ec98,0x1ec98,0x20cd8,96,0x1ed5c,0x1ed5c,0x20cdc,112,0x1ee20,0x1ee20,0x20ce0,384,0x1eee4,0x1eee4,0x20ce4,400,0x1efa8,0x1efa8,0x20ce8,416,0x1f06c,0x1f06c,0x20cec,432,0x1f130,0x1f130,0x20cf0,448,0x1f1f4,0x1f1f4,0x20cf4,464,0x1f2b8,0x1f2b8,0x20cf8,480,0x1f37c,0x1f37c,0x20cfc,496,0x1e810,0x1e800,0x20cc0,768,0x1e8d4,0x1e8c4,0x20cc4,784,0x1e998,0x1e988,0x20cc8,800,0x1ea5c,0x1ea4c,0x20ccc,816,0x1eb20,0x1eb10,0x20cd0,832,0x1ebe4,0x1ebd4,0x20cd4,848,0x1eca8,0x1ec98,0x20cd8,864,0x1ed6c,0x1ed5c,0x20cdc,880,0x1ee30,0x1ee20,0x20ce0,1152,0x1eef4,0x1eee4,0x20ce4,1168,0x1efb8,0x1efa8,0x20ce8,1184,0x1f07c,0x1f06c,0x20cec,1200,0x1f140,0x1f130,0x20cf0,1216,0x1f204,0x1f1f4,0x20cf4,1232,0x1f2c8,0x1f2b8,0x20cf8,1248,0x1f38c,0x1f37c,0x20cfc,1264,0x1e820,0x1e800,0x20cc0,1536,0x1e8e4,0x1e8c4,0x20cc4,1552,0x1e9a8,0x1e988,0x20cc8,1568,0x1ea6c,0x1ea4c,0x20ccc,1584,0x1eb30,0x1eb10,0x20cd0,1600,0x1ebf4,0x1ebd4,0x20cd4,1616,0x1ecb8,0x1ec98,0x20cd8,1632,0x1ed7c,0x1ed5c,0x20cdc,1648,0x1ee40,0x1ee20,0x20ce0,1920,0x1ef04,0x1eee4,0x20ce4,1936,0x1efc8,0x1efa8,0x20ce8,1952,0x1f08c,0x1f06c,0x20cec,1968,0x1f150,0x1f130,0x20cf0,1984,0x1f214,0x1f1f4,0x20cf4,2000,0x1f2d8,0x1f2b8,0x20cf8,2016,0x1f39c,0x1f37c,0x20cfc,2032,0x1e830,0x1e800,0x20cc0,2304,0x1e8f4,0x1e8c4,0x20cc4,2320,0x1e9b8,0x1e988,0x20cc8,2336,0x1ea7c,0x1ea4c,0x20ccc,2352,0x1eb40,0x1eb10,0x20cd0,2368,0x1ec04,0x1ebd4,0x20cd4,2384,0x1ecc8,0x1ec98,0x20cd8,2400,0x1ed8c,0x1ed5c,0x20cdc,2416,0x1ee50,0x1ee20,0x20ce0,2688,0x1ef14,0x1eee4,0x20ce4,2704,0x1efd8,0x1efa8,0x20ce8,2720,0x1f09c,0x1f06c,0x20cec,2736,0x1f160,0x1f130,0x20cf0,2752,0x1f224,0x1f1f4,0x20cf4,2768,0x1f2e8,0x1f2b8,0x20cf8,2784,0x1f3ac,0x1f37c,0x20cfc,2800,36864,0x26d70,0x1e800,0x1eb00,36880,0x26d74,0x1ec90,0x1ef90,36896,0x26d78,0x1f120,0x1f420,36912,0x26d7c,0x1f5b0,0x1f8b0,36928,0x26d80,0x1fa40,0x1fd40,36944,0x26d84,0x1fed0,0x201d0,36960,0x26d88,0x20360,0x20660,36976,0x26d8c,0x207f0,0x20af0,37248,0x26d90,0x20c80,0x20f80,37264,0x26d94,0x21110,0x21410,37280,0x26d98,0x215a0,0x218a0,37296,0x26d9c,0x21a30,0x21d30,37312,0x26da0,0x21ec0,0x221c0,37328,0x26da4,0x22350,0x22650,37344,0x26da8,0x227e0,0x22ae0,37360,0x26dac,0x22c70,0x22f70,37664,0x26db0,0x1e830,0x1eb08,37680,0x26db4,0x1ecc0,0x1ef98,37632,0x26db8,0x1f150,0x1f428,37648,0x26dbc,0x1f5e0,0x1f8b8,37728,0x26dc0,0x1fa70,0x1fd48,37744,0x26dc4,0x1ff00,0x201d8,37696,0x26dc8,0x20390,0x20668,37712,0x26dcc,0x20820,0x20af8,38048,0x26dd0,0x20cb0,0x20f88,38064,0x26dd4,0x21140,0x21418,38016,0x26dd8,0x215d0,0x218a8,38032,0x26ddc,0x21a60,0x21d38,38112,0x26de0,0x21ef0,0x221c8,38128,0x26de4,0x22380,0x22658,38080,0x26de8,0x22810,0x22ae8,38096,0x26dec,0x22ca0,0x22f78,38464,0x26df0,0x1e860,0x1eb10,38480,0x26df4,0x1ecf0,0x1efa0,38496,0x26df8,0x1f180,0x1f430,38512,0x26dfc,0x1f610,0x1f8c0,38400,0x26e00,0x1faa0,0x1fd50,38416,0x26e04,0x1ff30,0x201e0,38432,0x26e08,0x203c0,0x20670,38448,0x26e0c,0x20850,0x20b00,38848,0x26e10,0x20ce0,0x20f90,38864,0x26e14,0x21170,0x21420,38880,0x26e18,0x21600,0x218b0,38896,0x26e1c,0x21a90,0x21d40,38784,0x26e20,0x21f20,0x221d0,38800,0x26e24,0x223b0,0x22660,38816,0x26e28,0x22840,0x22af0,38832,0x26e2c,0x22cd0,0x22f80,39264,0x26e30,0x1e890,0x1eb18,39280,0x26e34,0x1ed20,0x1efa8,39232,0x26e38,0x1f1b0,0x1f438,39248,0x26e3c,0x1f640,0x1f8c8,39200,0x26e40,0x1fad0,0x1fd58,39216,0x26e44,0x1ff60,0x201e8,39168,0x26e48,0x203f0,0x20678,39184,0x26e4c,0x20880,0x20b08,39648,0x26e50,0x20d10,0x20f98,39664,0x26e54,0x211a0,0x21428,39616,0x26e58,0x21630,0x218b8,39632,0x26e5c,0x21ac0,0x21d48,39584,0x26e60,0x21f50,0x221d8,39600,0x26e64,0x223e0,0x22668,39552,0x26e68,0x22870,0x22af8,39568,0x26e6c,0x22d00,0x22f88,0x3f803f80,0x3f803f80,16256,0,0,0x3f803f80,0x3f800000,0,0,0x3f803f80,0,16256,0,0x3f803f80,0,0x3f800000,0x3f803f80,0,0,0,0,0,0,0,0,0,0,0,0,0,0,0,0x3f803f80,0,0,0,0,0,0,0,0,0,0,0,0,0,0,0,0x3f803f80,0,0,0,0,0,0,0,0,0,0,0,0,0,0,0,0x3f803f80,0,0,0,0,0,0,0,0,0,0,0,0,0,0,0,0x3f803f80,0x3f803f80,16256,0,0,0x3f803f80,0x3f800000,0,0,0x3f803f80,0,16256,0,0x3f803f80,0,0x3f800000,0x3f803f80,0,0,0,0,0,0,0,0,0,0,0,0,0,0,0,0x3f803f80,0,0,0,0,0,0,0,0,0,0,0,0,0,0,0,0x3f803f80,0,0,0,0,0,0,0,0,0,0,0,0,0,0,0,0x3f803f80,0,0,0,0,0,0,0,0,0,0,0,0,0,0,0,0x3f803f80,0x3f803f80,16256,0,0,0x3f803f80,0x3f800000,0,0,0x3f803f80,0,16256,0,0x3f803f80,0,0x3f800000,0x3f803f80,0,0,0,0,0,0,0,0,0,0,0,0,0,0,0,0x3f803f80,0,0,0,0,0,0,0,0,0,0,0,0,0,0,0,0x3f803f80,0,0,0,0,0,0,0,0,0,0,0,0,0,0,0,0x3f803f80,0,0,0,0,0,0,0,0,0,0,0,0,0,0,0,0x3f803f80,0x3f803f80,16256,0,0,0x3f803f80,0x3f800000,0,0,0x3f803f80,0,16256,0,0x3f803f80,0,0x3f800000,0,64,0x30000,0xf0000,144,208,0x30010,0xf0010,288,352,0x30020,0xf0020,432,496,0x30030,0xf0030,576,512,0x30040,0xf0040,720,656,0x30050,0xf0050,864,800,0x30060,0xf0060,1008,944,0x30070,0xf0070,1024,1088,0x30190,0xf0190,1168,1232,0x30180,0xf0180,1312,1376,0x301b0,0xf01b0,1456,1520,0x301a0,0xf01a0,1600,1536,0x301d0,0xf01d0,1744,1680,0x301c0,0xf01c0,1888,1824,0x301f0,0xf01f0,2032,1968,0x301e0,0xf01e0,16,80,0x30320,0x11f720,128,192,0x30330,0x11f730,304,368,0x30300,0x11f700,416,480,0x30310,0x11f710,592,528,0x30360,0x11f760,704,640,0x30370,0x11f770,880,816,0x30340,0x11f740,992,928,0x30350,0x11f750,1040,1104,0x304b0,0x11f8b0,1152,1216,0x304a0,0x11f8a0,1328,1392,0x30490,0x11f890,1440,1504,0x30480,0x11f880,1616,1552,0x304f0,0x11f8f0,1728,1664,0x304e0,0x11f8e0,1904,1840,0x304d0,0x11f8d0,2016,1952,0x304c0,0x11f8c0,32,96,0x30640,0x11fa40,176,240,0x30650,0x11fa50,256,320,0x30660,0x11fa60,400,464,0x30670,0x11fa70,608,544,0x30600,0x11fa00,752,688,0x30610,0x11fa10,832,768,0x30620,0x11fa20,976,912,0x30630,0x11fa30,1056,1120,0x307d0,0x11fbd0,1200,1264,0x307c0,0x11fbc0,1280,1344,0x307f0,0x11fbf0,1424,1488,0x307e0,0x11fbe0,1632,1568,0x30790,0x11fb90,1776,1712,0x30780,0x11fb80,1856,1792,0x307b0,0x11fbb0,2000,1936,0x307a0,0x11fba0,48,112,0x5fd60,0x11fd60,160,224,0x5fd70,0x11fd70,272,336,0x5fd40,0x11fd40,384,448,0x5fd50,0x11fd50,624,560,0x5fd20,0x11fd20,736,672,0x5fd30,0x11fd30,848,784,0x5fd00,0x11fd00,960,896,0x5fd10,0x11fd10,1072,1136,0x5fef0,0x11fef0,1184,1248,0x5fee0,0x11fee0,1296,1360,0x5fed0,0x11fed0,1408,1472,0x5fec0,0x11fec0,1648,1584,0x5feb0,0x11feb0,1760,1696,0x5fea0,0x11fea0,1872,1808,0x5fe90,0x11fe90,1984,1920,0x5fe80,0x11fe80,0xefb40,74752,78848,90624,0xefb50,74768,78864,90640,0xefb60,74784,78880,90656,0xefb70,74800,78896,90672,0xefb00,74816,78912,90688,0xefb10,74832,78928,90704,0xefb20,74848,78944,90720,0xefb30,74864,78960,90736,0xefcd0,74880,78976,90752,0xefcc0,74896,78992,90768,0xefcf0,74912,79008,90784,0xefce0,74928,79024,90800,0xefc90,74944,79040,90816,0xefc80,74960,79056,90832,0xefcb0,74976,79072,90848,0xefca0,74992,79088,90864,0xefe60,75008,79104,90880,0xefe70,75024,79120,90896,0xefe40,75040,79136,90912,0xefe50,75056,79152,90928,0xefe20,75072,79168,90944,0xefe30,75088,79184,90960,0xefe00,75104,79200,90976,0xefe10,75120,79216,90992,0xefff0,75136,79232,91008,0xeffe0,75152,79248,91024,0xeffd0,75168,79264,91040,0xeffc0,75184,79280,91056,0xeffb0,75200,79296,91072,0xeffa0,75216,79312,91088,0xeff90,75232,79328,91104,0xeff80,75248,79344,91120,0xefb40,75264,79360,91136,0xefb50,75280,79376,91152,0xefb60,75296,79392,91168,0xefb70,75312,79408,91184,0xefb00,75328,79424,91200,0xefb10,75344,79440,91216,0xefb20,75360,79456,91232,0xefb30,75376,79472,91248,0xefcd0,75392,79488,91264,0xefcc0,75408,79504,91280,0xefcf0,75424,79520,91296,0xefce0,75440,79536,91312,0xefc90,75456,79552,91328,0xefc80,75472,79568,91344,0xefcb0,75488,79584,91360,0xefca0,75504,79600,91376,0xefe60,75520,79616,91392,0xefe70,75536,79632,91408,0xefe40,75552,79648,91424,0xefe50,75568,79664,91440,0xefe20,75584,79680,91456,0xefe30,75600,79696,91472,0xefe00,75616,79712,91488,0xefe10,75632,79728,91504,0xefff0,75648,79744,91520,0xeffe0,75664,79760,91536,0xeffd0,75680,79776,91552,0xeffc0,75696,79792,91568,0xeffb0,75712,79808,91584,0xeffa0,75728,79824,91600,0xeff90,75744,79840,91616,0xeff80,75760,79856,91632,0x1c0a0,36864,39936,49160,0x1c0a4,36880,39952,49176,0x1c0a8,36896,39968,49192,0x1c0ac,36912,39984,49208,0x1c0b0,36928,40000,49224,0x1c0b4,36944,40016,49240,0x1c0b8,36960,40032,49256,0x1c0bc,36976,40048,49272,0x1c0c0,36992,40064,49288,0x1c0c4,37008,40080,49304,0x1c0c8,37024,40096,49320,0x1c0cc,37040,40112,49336,0x1c0d0,37056,40128,49352,0x1c0d4,37072,40144,49368,0x1c0d8,37088,40160,49384,0x1c0dc,37104,40176,49400,0x1c0e0,37120,40192,49416,0x1c0e4,37136,40208,49432,0x1c0e8,37152,40224,49448,0x1c0ec,37168,40240,49464,0x1c0f0,37184,40256,49480,0x1c0f4,37200,40272,49496,0x1c0f8,37216,40288,49512,0x1c0fc,37232,40304,49528,0x1c100,37248,40320,49544,0x1c104,37264,40336,49560,0x1c108,37280,40352,49576,0x1c10c,37296,40368,49592,0x1c110,37312,40384,49608,0x1c114,37328,40400,49624,0x1c118,37344,40416,49640,0x1c11c,37360,40432,49656,0x1c120,37376,40448,49672,0x1c124,37392,40464,49688,0x1c128,37408,40480,49704,0x1c12c,37424,40496,49720,0x1c130,37440,40512,49736,0x1c134,37456,40528,49752,0x1c138,37472,40544,49768,0x1c13c,37488,40560,49784,0x1c140,37504,40576,49800,0x1c144,37520,40592,49816,0x1c148,37536,40608,49832,0x1c14c,37552,40624,49848,0x1c150,37568,40640,49864,0x1c154,37584,40656,49880,0x1c158,37600,40672,49896,0x1c15c,37616,40688,49912,0x1c160,37632,40704,49928,0x1c164,37648,40720,49944,0x1c168,37664,40736,49960,0x1c16c,37680,40752,49976,0x1c170,37696,40768,49992,0x1c174,37712,40784,50008,0x1c178,37728,40800,50024,0x1c17c,37744,40816,50040,0x1c180,37760,40832,50056,0x1c184,37776,40848,50072,0x1c188,37792,40864,50088,0x1c18c,37808,40880,50104,0x1c190,37824,40896,50120,0x1c194,37840,40912,50136,0x1c198,37856,40928,50152,0x1c19c,37872,40944,50168,73824,79904,0x1d600,0x1d604,73968,80048,0x1d610,0x1d614,74048,80128,0x1d620,0x1d624,74192,80272,0x1d630,0x1d634,74272,80480,0x1d640,0x1d644,74416,80624,0x1d650,0x1d654,74496,80704,0x1d660,0x1d664,74640,80848,0x1d670,0x1d674,74848,80928,0x1d680,0x1d684,74992,81072,0x1d690,0x1d694,75072,81152,0x1d6a0,0x1d6a4,75216,81296,0x1d6b0,0x1d6b4,75296,81504,0x1d6c0,0x1d6c4,75440,81648,0x1d6d0,0x1d6d4,75520,81728,0x1d6e0,0x1d6e4,75664,81872,0x1d6f0,0x1d6f4,73840,79912,0x1d700,0x1d704,73952,80056,0x1d710,0x1d714,74064,80136,0x1d720,0x1d724,74176,80280,0x1d730,0x1d734,74288,80488,0x1d740,0x1d744,74400,80632,0x1d750,0x1d754,74512,80712,0x1d760,0x1d764,74624,80856,0x1d770,0x1d774,74864,80936,0x1d780,0x1d784,74976,81080,0x1d790,0x1d794,75088,81160,0x1d7a0,0x1d7a4,75200,81304,0x1d7b0,0x1d7b4,75312,81512,0x1d7c0,0x1d7c4,75424,81656,0x1d7d0,0x1d7d4,75536,81736,0x1d7e0,0x1d7e4,75648,81880,0x1d7f0,0x1d7f4,79872,79920,0x1d800,0x1d804,80016,80032,0x1d810,0x1d814,80160,80144,0x1d820,0x1d824,80304,80256,0x1d830,0x1d834,80448,80496,0x1d840,0x1d844,80592,80608,0x1d850,0x1d854,80736,80720,0x1d860,0x1d864,80880,80832,0x1d870,0x1d874,80896,80944,0x1d880,0x1d884,81040,81056,0x1d890,0x1d894,81184,81168,0x1d8a0,0x1d8a4,81328,81280,0x1d8b0,0x1d8b4,81472,81520,0x1d8c0,0x1d8c4,81616,81632,0x1d8d0,0x1d8d4,81760,81744,0x1d8e0,0x1d8e4,81904,81856,0x1d8f0,0x1d8f4,79888,79928,0x1d900,0x1d904,80000,80040,0x1d910,0x1d914,80176,80152,0x1d920,0x1d924,80288,80264,0x1d930,0x1d934,80464,80504,0x1d940,0x1d944,80576,80616,0x1d950,0x1d954,80752,80728,0x1d960,0x1d964,80864,80840,0x1d970,0x1d974,80912,80952,0x1d980,0x1d984,81024,81064,0x1d990,0x1d994,81200,81176,0x1d9a0,0x1d9a4,81312,81288,0x1d9b0,0x1d9b4,81488,81528,0x1d9c0,0x1d9c4,81600,81640,0x1d9d0,0x1d9d4,81776,81752,0x1d9e0,0x1d9e4,81888,81864,0x1d9f0,0x1d9f4,0x1e680,0x1e680,0x1e8c0,0x100000,0x1e682,0x100000,0x100000,0x1e8c0,0x1e684,0x100000,0x100000,0x100000,0x1e686,0x100000,0x100000,0x100000,0x1e690,0x1e680,0x100000,0x100000,0x1e692,0x100000,0x100000,0x1e8c0,0x1e694,0x100000,0x100000,0x100000,0x1e696,0x100000,0x100000,0x100000,0x1e6a0,0x1e680,0x100000,0x100000,0x1e6a2,0x100000,0x100000,0x1e8c0,0x1e6a4,0x100000,0x100000,0x100000,0x1e6a6,0x100000,0x100000,0x100000,0x1e6b0,0x1e680,0x100000,0x100000,0x1e6b2,0x100000,0x100000,0x1e8c0,0x1e6b4,0x100000,0x100000,0x100000,0x1e6b6,0x100000,0x100000,0x100000,0x1e688,0x1e690,0x1e8f0,0x100000,0x1e68a,0x100000,0x100000,0x1e8f0,0x1e68c,0x100000,0x100000,0x100000,0x1e68e,0x100000,0x100000,0x100000,0x1e698,0x1e690,0x100000,0x100000,0x1e69a,0x100000,0x100000,0x1e8f0,0x1e69c,0x100000,0x100000,0x100000,0x1e69e,0x100000,0x100000,0x100000,0x1e6a8,0x1e690,0x100000,0x100000,0x1e6aa,0x100000,0x100000,0x1e8f0,0x1e6ac,0x100000,0x100000,0x100000,0x1e6ae,0x100000,0x100000,0x100000,0x1e6b8,0x1e690,0x100000,0x100000,0x1e6ba,0x100000,0x100000,0x1e8f0,0x1e6bc,0x100000,0x100000,0x100000,0x1e6be,0x100000,0x100000,0x100000,0x1e6c0,0x1e6a0,0x1e920,0x100000,0x1e6c2,0x100000,0x100000,0x1e920,0x1e6c4,0x100000,0x100000,0x100000,0x1e6c6,0x100000,0x100000,0x100000,0x1e6d0,0x1e6a0,0x100000,0x100000,0x1e6d2,0x100000,0x100000,0x1e920,0x1e6d4,0x100000,0x100000,0x100000,0x1e6d6,0x100000,0x100000,0x100000,0x1e6e0,0x1e6a0,0x100000,0x100000,0x1e6e2,0x100000,0x100000,0x1e920,0x1e6e4,0x100000,0x100000,0x100000,0x1e6e6,0x100000,0x100000,0x100000,0x1e6f0,0x1e6a0,0x100000,0x100000,0x1e6f2,0x100000,0x100000,0x1e920,0x1e6f4,0x100000,0x100000,0x100000,0x1e6f6,0x100000,0x100000,0x100000,0x1e6c8,0x1e6b0,0x1e950,0x100000,0x1e6ca,0x100000,0x100000,0x1e950,0x1e6cc,0x100000,0x100000,0x100000,0x1e6ce,0x100000,0x100000,0x100000,0x1e6d8,0x1e6b0,0x100000,0x100000,0x1e6da,0x100000,0x100000,0x1e950,0x1e6dc,0x100000,0x100000,0x100000,0x1e6de,0x100000,0x100000,0x100000,0x1e6e8,0x1e6b0,0x100000,0x100000,0x1e6ea,0x100000,0x100000,0x1e950,0x1e6ec,0x100000,0x100000,0x100000,0x1e6ee,0x100000,0x100000,0x100000,0x1e6f8,0x1e6b0,0x100000,0x100000,0x1e6fa,0x100000,0x100000,0x1e950,0x1e6fc,0x100000,0x100000,0x100000,0x1e6fe,0x100000,0x100000,0x100000,0x1eb60,0x251f0,36960,37024,0x1eb62,0x251f2,36976,37032,0x1eb64,0x251f4,36992,37040,0x1eb66,0x251f6,37008,37048,0x1eb68,0x25350,37344,37408,0x1eb6a,0x25352,37360,37416,0x1eb6c,0x25354,37376,37424,0x1eb6e,0x25356,37392,37432,0x1eb70,0x254b0,37696,37760,0x1eb72,0x254b2,37712,37768,0x1eb74,0x254b4,37792,37776,0x1eb76,0x254b6,37808,37784,0x1eb78,0x25610,38080,38144,0x1eb7a,0x25612,38096,38152,0x1eb7c,0x25614,38176,38160,0x1eb7e,0x25616,38192,38168,0x1eb60,0x251f8,38432,38624,0x1eb62,0x251fa,38448,38632,0x1eb64,0x251fc,38592,38640,0x1eb66,0x251fe,38608,38648,0x1eb68,0x25358,38816,39008,0x1eb6a,0x2535a,38832,39016,0x1eb6c,0x2535c,38976,39024,0x1eb6e,0x2535e,38992,39032,0x1eb70,0x254b8,39168,39360,0x1eb72,0x254ba,39184,39368,0x1eb74,0x254bc,39392,39376,0x1eb76,0x254be,39408,39384,0x1eb78,0x25618,39552,39744,0x1eb7a,0x2561a,39568,39752,0x1eb7c,0x2561c,39776,39760,0x1eb7e,0x2561e,39792,39768,0x1eb60,0x26278,40032,40096,0x1eb62,0x2627a,40048,40104,0x1eb64,0x2627c,40064,40112,0x1eb66,0x2627e,40080,40120,0x1eb68,0x263d8,40416,40480,0x1eb6a,0x263da,40432,40488,0x1eb6c,0x263dc,40448,40496,0x1eb6e,0x263de,40464,40504,0x1eb70,0x26538,40768,40832,0x1eb72,0x2653a,40784,40840,0x1eb74,0x2653c,40864,40848,0x1eb76,0x2653e,40880,40856,0x1eb78,0x26698,41152,41216,0x1eb7a,0x2669a,41168,41224,0x1eb7c,0x2669c,41248,41232,0x1eb7e,0x2669e,41264,41240,0x1eb60,0x100000,41504,41696,0x1eb62,0x100000,41520,41704,0x1eb64,0x100000,41664,41712,0x1eb66,0x100000,41680,41720,0x1eb68,0x100000,41888,42080,0x1eb6a,0x100000,41904,42088,0x1eb6c,0x100000,42048,42096,0x1eb6e,0x100000,42064,42104,0x1eb70,0x100000,42240,42432,0x1eb72,0x100000,42256,42440,0x1eb74,0x100000,42464,42448,0x1eb76,0x100000,42480,42456,0x1eb78,0x100000,42624,42816,0x1eb7a,0x100000,42640,42824,0x1eb7c,0x100000,42848,42832,0x1eb7e,0x100000,42864,42840,36960,36992,37024,46080,36968,37000,37032,46096,36976,37008,37040,46112,36984,37016,37048,46128,38112,38144,38176,46144,38120,38152,38184,46160,38128,38160,38192,46176,38136,38168,38200,46192,39232,39328,39296,46208,39240,39336,39304,46224,39248,39344,39312,46240,39256,39352,39320,46256,40384,40480,40448,46272,40392,40488,40456,46288,40400,40496,40464,46304,40408,40504,40472,46320,46112,46272,46304,46336,46120,46280,46312,46352,46128,46288,46320,46368,46136,46296,46328,46384,47264,47424,47456,46400,47272,47432,47464,46416,47280,47440,47472,46432,47288,47448,47480,46448,48384,48608,48576,46464,48392,48616,48584,46480,48400,48624,48592,46496,48408,48632,48600,46512,49536,49760,49728,46528,49544,49768,49736,46544,49552,49776,49744,46560,49560,49784,49752,46576,55392,55424,55456,46592,55400,55432,55464,46608,55408,55440,55472,46624,55416,55448,55480,46640,56544,56576,56608,46656,56552,56584,56616,46672,56560,56592,56624,46688,56568,56600,56632,46704,57664,57760,57728,46720,57672,57768,57736,46736,57680,57776,57744,46752,57688,57784,57752,46768,58816,58912,58880,46784,58824,58920,58888,46800,58832,58928,58896,46816,58840,58936,58904,46832,64544,64704,64736,46848,64552,64712,64744,46864,64560,64720,64752,46880,64568,64728,64760,46896,65696,65856,65888,46912,65704,65864,65896,46928,65712,65872,65904,46944,65720,65880,65912,46960,66816,67040,67008,46976,66824,67048,67016,46992,66832,67056,67024,47008,66840,67064,67032,47024,67968,68192,68160,47040,67976,68200,68168,47056,67984,68208,68176,47072,67992,68216,68184,47088,0x1e800,0x1e800,0x20cc0,18432,0x1e8c4,0x1e8c4,0x20cc4,18448,0x1e988,0x1e988,0x20cc8,18464,0x1ea4c,0x1ea4c,0x20ccc,18480,0x1eb10,0x1eb10,0x20cd0,18496,0x1ebd4,0x1ebd4,0x20cd4,18512,0x1ec98,0x1ec98,0x20cd8,18528,0x1ed5c,0x1ed5c,0x20cdc,18544,0x1ee20,0x1ee20,0x20ce0,18816,0x1eee4,0x1eee4,0x20ce4,18832,0x1efa8,0x1efa8,0x20ce8,18848,0x1f06c,0x1f06c,0x20cec,18864,0x1f130,0x1f130,0x20cf0,18880,0x1f1f4,0x1f1f4,0x20cf4,18896,0x1f2b8,0x1f2b8,0x20cf8,18912,0x1f37c,0x1f37c,0x20cfc,18928,0x1e810,0x1e800,0x20cc0,19200,0x1e8d4,0x1e8c4,0x20cc4,19216,0x1e998,0x1e988,0x20cc8,19232,0x1ea5c,0x1ea4c,0x20ccc,19248,0x1eb20,0x1eb10,0x20cd0,19264,0x1ebe4,0x1ebd4,0x20cd4,19280,0x1eca8,0x1ec98,0x20cd8,19296,0x1ed6c,0x1ed5c,0x20cdc,19312,0x1ee30,0x1ee20,0x20ce0,19584,0x1eef4,0x1eee4,0x20ce4,19600,0x1efb8,0x1efa8,0x20ce8,19616,0x1f07c,0x1f06c,0x20cec,19632,0x1f140,0x1f130,0x20cf0,19648,0x1f204,0x1f1f4,0x20cf4,19664,0x1f2c8,0x1f2b8,0x20cf8,19680,0x1f38c,0x1f37c,0x20cfc,19696,0x1e820,0x1e800,0x20cc0,19968,0x1e8e4,0x1e8c4,0x20cc4,19984,0x1e9a8,0x1e988,0x20cc8,20000,0x1ea6c,0x1ea4c,0x20ccc,20016,0x1eb30,0x1eb10,0x20cd0,20032,0x1ebf4,0x1ebd4,0x20cd4,20048,0x1ecb8,0x1ec98,0x20cd8,20064,0x1ed7c,0x1ed5c,0x20cdc,20080,0x1ee40,0x1ee20,0x20ce0,20352,0x1ef04,0x1eee4,0x20ce4,20368,0x1efc8,0x1efa8,0x20ce8,20384,0x1f08c,0x1f06c,0x20cec,20400,0x1f150,0x1f130,0x20cf0,20416,0x1f214,0x1f1f4,0x20cf4,20432,0x1f2d8,0x1f2b8,0x20cf8,20448,0x1f39c,0x1f37c,0x20cfc,20464,0x1e830,0x1e800,0x20cc0,20736,0x1e8f4,0x1e8c4,0x20cc4,20752,0x1e9b8,0x1e988,0x20cc8,20768,0x1ea7c,0x1ea4c,0x20ccc,20784,0x1eb40,0x1eb10,0x20cd0,20800,0x1ec04,0x1ebd4,0x20cd4,20816,0x1ecc8,0x1ec98,0x20cd8,20832,0x1ed8c,0x1ed5c,0x20cdc,20848,0x1ee50,0x1ee20,0x20ce0,21120,0x1ef14,0x1eee4,0x20ce4,21136,0x1efd8,0x1efa8,0x20ce8,21152,0x1f09c,0x1f06c,0x20cec,21168,0x1f160,0x1f130,0x20cf0,21184,0x1f224,0x1f1f4,0x20cf4,21200,0x1f2e8,0x1f2b8,0x20cf8,21216,0x1f3ac,0x1f37c,0x20cfc,21232,55296,0x26e70,0x1e8c0,0x1eb60,55312,0x26e74,0x1ed50,0x1eff0,55328,0x26e78,0x1f1e0,0x1f480,55344,0x26e7c,0x1f670,0x1f910,55360,0x26e80,0x1fb00,0x1fda0,55376,0x26e84,0x1ff90,0x20230,55392,0x26e88,0x20420,0x206c0,55408,0x26e8c,0x208b0,0x20b50,55680,0x26e90,0x20d40,0x20fe0,55696,0x26e94,0x211d0,0x21470,55712,0x26e98,0x21660,0x21900,55728,0x26e9c,0x21af0,0x21d90,55744,0x26ea0,0x21f80,0x22220,55760,0x26ea4,0x22410,0x226b0,55776,0x26ea8,0x228a0,0x22b40,55792,0x26eac,0x22d30,0x22fd0,56096,0x26eb0,0x1e8f0,0x1eb68,56112,0x26eb4,0x1ed80,0x1eff8,56064,0x26eb8,0x1f210,0x1f488,56080,0x26ebc,0x1f6a0,0x1f918,56160,0x26ec0,0x1fb30,0x1fda8,56176,0x26ec4,0x1ffc0,0x20238,56128,0x26ec8,0x20450,0x206c8,56144,0x26ecc,0x208e0,0x20b58,56480,0x26ed0,0x20d70,0x20fe8,56496,0x26ed4,0x21200,0x21478,56448,0x26ed8,0x21690,0x21908,56464,0x26edc,0x21b20,0x21d98,56544,0x26ee0,0x21fb0,0x22228,56560,0x26ee4,0x22440,0x226b8,56512,0x26ee8,0x228d0,0x22b48,56528,0x26eec,0x22d60,0x22fd8,56896,0x26ef0,0x1e920,0x1eb70,56912,0x26ef4,0x1edb0,0x1f000,56928,0x26ef8,0x1f240,0x1f490,56944,0x26efc,0x1f6d0,0x1f920,56832,0x26f00,0x1fb60,0x1fdb0,56848,0x26f04,0x1fff0,0x20240,56864,0x26f08,0x20480,0x206d0,56880,0x26f0c,0x20910,0x20b60,57280,0x26f10,0x20da0,0x20ff0,57296,0x26f14,0x21230,0x21480,57312,0x26f18,0x216c0,0x21910,57328,0x26f1c,0x21b50,0x21da0,57216,0x26f20,0x21fe0,0x22230,57232,0x26f24,0x22470,0x226c0,57248,0x26f28,0x22900,0x22b50,57264,0x26f2c,0x22d90,0x22fe0,57696,0x26f30,0x1e950,0x1eb78,57712,0x26f34,0x1ede0,0x1f008,57664,0x26f38,0x1f270,0x1f498,57680,0x26f3c,0x1f700,0x1f928,57632,0x26f40,0x1fb90,0x1fdb8,57648,0x26f44,0x20020,0x20248,57600,0x26f48,0x204b0,0x206d8,57616,0x26f4c,0x20940,0x20b68,58080,0x26f50,0x20dd0,0x20ff8,58096,0x26f54,0x21260,0x21488,58048,0x26f58,0x216f0,0x21918,58064,0x26f5c,0x21b80,0x21da8,58016,0x26f60,0x22010,0x22238,58032,0x26f64,0x224a0,0x226c8,57984,0x26f68,0x22930,0x22b58,58000,0x26f6c,0x22dc0,0x22fe8,0x3f803f80,0x3f803f80,16256,0,0,0x3f803f80,0x3f800000,0,0,0x3f803f80,0,16256,0,0x3f803f80,0,0x3f800000,0x3f803f80,0,0,0,0,0,0,0,0,0,0,0,0,0,0,0,0x3f803f80,0,0,0,0,0,0,0,0,0,0,0,0,0,0,0,0x3f803f80,0,0,0,0,0,0,0,0,0,0,0,0,0,0,0,0x3f803f80,0,0,0,0,0,0,0,0,0,0,0,0,0,0,0,0x3f803f80,0x3f803f80,16256,0,0,0x3f803f80,0x3f800000,0,0,0x3f803f80,0,16256,0,0x3f803f80,0,0x3f800000,0x3f803f80,0,0,0,0,0,0,0,0,0,0,0,0,0,0,0,0x3f803f80,0,0,0,0,0,0,0,0,0,0,0,0,0,0,0,0x3f803f80,0,0,0,0,0,0,0,0,0,0,0,0,0,0,0,0x3f803f80,0,0,0,0,0,0,0,0,0,0,0,0,0,0,0,0x3f803f80,0x3f803f80,16256,0,0,0x3f803f80,0x3f800000,0,0,0x3f803f80,0,16256,0,0x3f803f80,0,0x3f800000,0x3f803f80,0,0,0,0,0,0,0,0,0,0,0,0,0,0,0,0x3f803f80,0,0,0,0,0,0,0,0,0,0,0,0,0,0,0,0x3f803f80,0,0,0,0,0,0,0,0,0,0,0,0,0,0,0,0x3f803f80,0,0,0,0,0,0,0,0,0,0,0,0,0,0,0,0x3f803f80,0x3f803f80,16256,0,0,0x3f803f80,0x3f800000,0,0,0x3f803f80,0,16256,0,0x3f803f80,0,0x3f800000,0,64,0x60000,0xc0080,144,208,0x60010,0xc0090,288,352,0x60020,0xc00a0,432,496,0x60030,0xc00b0,576,512,0x60040,0xc00c0,720,656,0x60050,0xc00d0,864,800,0x60060,0xc00e0,1008,944,0x60070,0xc00f0,1024,1088,0x60190,0xc0210,1168,1232,0x60180,0xc0200,1312,1376,0x601b0,0xc0230,1456,1520,0x601a0,0xc0220,1600,1536,0x601d0,0xc0250,1744,1680,0x601c0,0xc0240,1888,1824,0x601f0,0xc0270,2032,1968,0x601e0,0xc0260,16,80,0x60320,0xc03a0,128,192,0x60330,0xc03b0,304,368,0x60300,0xc0380,416,480,0x60310,0xc0390,592,528,0x60360,0xc03e0,704,640,0x60370,0xc03f0,880,816,0x60340,0xc03c0,992,928,0x60350,0xc03d0,1040,1104,0x604b0,0xef930,1152,1216,0x604a0,0xef920,1328,1392,0x60490,0xef910,1440,1504,0x60480,0xef900,1616,1552,0x604f0,0xef970,1728,1664,0x604e0,0xef960,1904,1840,0x604d0,0xef950,2016,1952,0x604c0,0xef940,32,96,0x60640,0xefac0,176,240,0x60650,0xefad0,256,320,0x60660,0xefae0,400,464,0x60670,0xefaf0,608,544,0x60600,0xefa80,752,688,0x60610,0xefa90,832,768,0x60620,0xefaa0,976,912,0x60630,0xefab0,1056,1120,0x8fbd0,0xefc50,1200,1264,0x8fbc0,0xefc40,1280,1344,0x8fbf0,0xefc70,1424,1488,0x8fbe0,0xefc60,1632,1568,0x8fb90,0xefc10,1776,1712,0x8fb80,0xefc00,1856,1792,0x8fbb0,0xefc30,2000,1936,0x8fba0,0xefc20,48,112,0x8fd60,0xefde0,160,224,0x8fd70,0xefdf0,272,336,0x8fd40,0xefdc0,384,448,0x8fd50,0xefdd0,624,560,0x8fd20,0xefda0,736,672,0x8fd30,0xefdb0,848,784,0x8fd00,0xefd80,960,896,0x8fd10,0xefd90,1072,1136,0x8fef0,0xeff70,1184,1248,0x8fee0,0xeff60,1296,1360,0x8fed0,0xeff50,1408,1472,0x8fec0,0xeff40,1648,1584,0x8feb0,0xeff30,1760,1696,0x8fea0,0xeff20,1872,1808,0x8fe90,0xeff10,1984,1920,0x8fe80,0xeff00,0xf0100,75776,83968,91136,0xf0110,75792,83984,91152,0xf0120,75808,84000,91168,0xf0130,75824,84016,91184,0xf0140,75840,84032,91200,0xf0150,75856,84048,91216,0xf0160,75872,84064,91232,0xf0170,75888,84080,91248,0xf0290,75904,84096,91264,0xf0280,75920,84112,91280,0xf02b0,75936,84128,91296,0xf02a0,75952,84144,91312,0xf02d0,75968,84160,91328,0xf02c0,75984,84176,91344,0xf02f0,76000,84192,91360,0xf02e0,76016,84208,91376,0x11f820,76032,84224,91392,0x11f830,76048,84240,91408,0x11f800,76064,84256,91424,0x11f810,76080,84272,91440,0x11f860,76096,84288,91456,0x11f870,76112,84304,91472,0x11f840,76128,84320,91488,0x11f850,76144,84336,91504,0x11f9b0,76160,84352,91520,0x11f9a0,76176,84368,91536,0x11f990,76192,84384,91552,0x11f980,76208,84400,91568,0x11f9f0,76224,84416,91584,0x11f9e0,76240,84432,91600,0x11f9d0,76256,84448,91616,0x11f9c0,76272,84464,91632,0xf0100,76288,84480,91648,0xf0110,76304,84496,91664,0xf0120,76320,84512,91680,0xf0130,76336,84528,91696,0xf0140,76352,84544,91712,0xf0150,76368,84560,91728,0xf0160,76384,84576,91744,0xf0170,76400,84592,91760,0xf0290,76416,84608,91776,0xf0280,76432,84624,91792,0xf02b0,76448,84640,91808,0xf02a0,76464,84656,91824,0xf02d0,76480,84672,91840,0xf02c0,76496,84688,91856,0xf02f0,76512,84704,91872,0xf02e0,76528,84720,91888,0x11f820,76544,84736,91904,0x11f830,76560,84752,91920,0x11f800,76576,84768,91936,0x11f810,76592,84784,91952,0x11f860,76608,84800,91968,0x11f870,76624,84816,91984,0x11f840,76640,84832,92000,0x11f850,76656,84848,92016,0x11f9b0,76672,84864,92032,0x11f9a0,76688,84880,92048,0x11f990,76704,84896,92064,0x11f980,76720,84912,92080,0x11f9f0,76736,84928,92096,0x11f9e0,76752,84944,92112,0x11f9d0,76768,84960,92128,0x11f9c0,76784,84976,92144,0x1c140,36864,43008,52224,0x1c144,36880,43024,52240,0x1c148,36896,43040,52256,0x1c14c,36912,43056,52272,0x1c150,36928,43072,52288,0x1c154,36944,43088,52304,0x1c158,36960,43104,52320,0x1c15c,36976,43120,52336,0x1c160,36992,43136,52352,0x1c164,37008,43152,52368,0x1c168,37024,43168,52384,0x1c16c,37040,43184,52400,0x1c170,37056,43200,52416,0x1c174,37072,43216,52432,0x1c178,37088,43232,52448,0x1c17c,37104,43248,52464,0x1c180,37120,43264,52480,0x1c184,37136,43280,52496,0x1c188,37152,43296,52512,0x1c18c,37168,43312,52528,0x1c190,37184,43328,52544,0x1c194,37200,43344,52560,0x1c198,37216,43360,52576,0x1c19c,37232,43376,52592,0x1c1a0,37248,43392,52608,0x1c1a4,37264,43408,52624,0x1c1a8,37280,43424,52640,0x1c1ac,37296,43440,52656,0x1c1b0,37312,43456,52672,0x1c1b4,37328,43472,52688,0x1c1b8,37344,43488,52704,0x1c1bc,37360,43504,52720,0x1c1c0,37376,43520,52736,0x1c1c4,37392,43536,52752,0x1c1c8,37408,43552,52768,0x1c1cc,37424,43568,52784,0x1c1d0,37440,43584,52800,0x1c1d4,37456,43600,52816,0x1c1d8,37472,43616,52832,0x1c1dc,37488,43632,52848,0x1c1e0,37504,43648,52864,0x1c1e4,37520,43664,52880,0x1c1e8,37536,43680,52896,0x1c1ec,37552,43696,52912,0x1c1f0,37568,43712,52928,0x1c1f4,37584,43728,52944,0x1c1f8,37600,43744,52960,0x1c1fc,37616,43760,52976,0x1c200,37632,43776,52992,0x1c204,37648,43792,53008,0x1c208,37664,43808,53024,0x1c20c,37680,43824,53040,0x1c210,37696,43840,53056,0x1c214,37712,43856,53072,0x1c218,37728,43872,53088,0x1c21c,37744,43888,53104,0x1c220,37760,43904,53120,0x1c224,37776,43920,53136,0x1c228,37792,43936,53152,0x1c22c,37808,43952,53168,0x1c230,37824,43968,53184,0x1c234,37840,43984,53200,0x1c238,37856,44000,53216,0x1c23c,37872,44016,53232,79936,86016,0x1d600,0x1d608,80080,86160,0x1d610,0x1d618,80224,86304,0x1d620,0x1d628,80368,86448,0x1d630,0x1d638,80384,86592,0x1d640,0x1d648,80528,86736,0x1d650,0x1d658,80672,86880,0x1d660,0x1d668,80816,87024,0x1d670,0x1d678,80960,87040,0x1d680,0x1d688,81104,87184,0x1d690,0x1d698,81248,87328,0x1d6a0,0x1d6a8,81392,87472,0x1d6b0,0x1d6b8,81408,87616,0x1d6c0,0x1d6c8,81552,87760,0x1d6d0,0x1d6d8,81696,87904,0x1d6e0,0x1d6e8,81840,88048,0x1d6f0,0x1d6f8,79952,86024,0x1d700,0x1d708,80064,86168,0x1d710,0x1d718,80240,86312,0x1d720,0x1d728,80352,86456,0x1d730,0x1d738,80400,86600,0x1d740,0x1d748,80512,86744,0x1d750,0x1d758,80688,86888,0x1d760,0x1d768,80800,87032,0x1d770,0x1d778,80976,87048,0x1d780,0x1d788,81088,87192,0x1d790,0x1d798,81264,87336,0x1d7a0,0x1d7a8,81376,87480,0x1d7b0,0x1d7b8,81424,87624,0x1d7c0,0x1d7c8,81536,87768,0x1d7d0,0x1d7d8,81712,87912,0x1d7e0,0x1d7e8,81824,88056,0x1d7f0,0x1d7f8,79968,86032,0x1d800,0x1d808,80112,86144,0x1d810,0x1d818,80192,86320,0x1d820,0x1d828,80336,86432,0x1d830,0x1d838,80416,86608,0x1d840,0x1d848,80560,86720,0x1d850,0x1d858,80640,86896,0x1d860,0x1d868,80784,87008,0x1d870,0x1d878,80992,87056,0x1d880,0x1d888,81136,87168,0x1d890,0x1d898,81216,87344,0x1d8a0,0x1d8a8,81360,87456,0x1d8b0,0x1d8b8,81440,87632,0x1d8c0,0x1d8c8,81584,87744,0x1d8d0,0x1d8d8,81664,87920,0x1d8e0,0x1d8e8,81808,88032,0x1d8f0,0x1d8f8,79984,86040,0x1d900,0x1d908,80096,86152,0x1d910,0x1d918,80208,86328,0x1d920,0x1d928,80320,86440,0x1d930,0x1d938,80432,86616,0x1d940,0x1d948,80544,86728,0x1d950,0x1d958,80656,86904,0x1d960,0x1d968,80768,87016,0x1d970,0x1d978,81008,87064,0x1d980,0x1d988,81120,87176,0x1d990,0x1d998,81232,87352,0x1d9a0,0x1d9a8,81344,87464,0x1d9b0,0x1d9b8,81456,87640,0x1d9c0,0x1d9c8,81568,87752,0x1d9d0,0x1d9d8,81680,87928,0x1d9e0,0x1d9e8,81792,88040,0x1d9f0,0x1d9f8,0x1e700,0x1e700,0x1e980,0x100000,0x1e702,0x100000,0x100000,0x1e980,0x1e704,0x100000,0x100000,0x100000,0x1e706,0x100000,0x100000,0x100000,0x1e710,0x1e700,0x100000,0x100000,0x1e712,0x100000,0x100000,0x1e980,0x1e714,0x100000,0x100000,0x100000,0x1e716,0x100000,0x100000,0x100000,0x1e720,0x1e700,0x100000,0x100000,0x1e722,0x100000,0x100000,0x1e980,0x1e724,0x100000,0x100000,0x100000,0x1e726,0x100000,0x100000,0x100000,0x1e730,0x1e700,0x100000,0x100000,0x1e732,0x100000,0x100000,0x1e980,0x1e734,0x100000,0x100000,0x100000,0x1e736,0x100000,0x100000,0x100000,0x1e708,0x1e710,0x1e9b0,0x100000,0x1e70a,0x100000,0x100000,0x1e9b0,0x1e70c,0x100000,0x100000,0x100000,0x1e70e,0x100000,0x100000,0x100000,0x1e718,0x1e710,0x100000,0x100000,0x1e71a,0x100000,0x100000,0x1e9b0,0x1e71c,0x100000,0x100000,0x100000,0x1e71e,0x100000,0x100000,0x100000,0x1e728,0x1e710,0x100000,0x100000,0x1e72a,0x100000,0x100000,0x1e9b0,0x1e72c,0x100000,0x100000,0x100000,0x1e72e,0x100000,0x100000,0x100000,0x1e738,0x1e710,0x100000,0x100000,0x1e73a,0x100000,0x100000,0x1e9b0,0x1e73c,0x100000,0x100000,0x100000,0x1e73e,0x100000,0x100000,0x100000,0x1e740,0x1e720,0x1e9e0,0x100000,0x1e742,0x100000,0x100000,0x1e9e0,0x1e744,0x100000,0x100000,0x100000,0x1e746,0x100000,0x100000,0x100000,0x1e750,0x1e720,0x100000,0x100000,0x1e752,0x100000,0x100000,0x1e9e0,0x1e754,0x100000,0x100000,0x100000,0x1e756,0x100000,0x100000,0x100000,0x1e760,0x1e720,0x100000,0x100000,0x1e762,0x100000,0x100000,0x1e9e0,0x1e764,0x100000,0x100000,0x100000,0x1e766,0x100000,0x100000,0x100000,0x1e770,0x1e720,0x100000,0x100000,0x1e772,0x100000,0x100000,0x1e9e0,0x1e774,0x100000,0x100000,0x100000,0x1e776,0x100000,0x100000,0x100000,0x1e748,0x1e730,0x1ea10,0x100000,0x1e74a,0x100000,0x100000,0x1ea10,0x1e74c,0x100000,0x100000,0x100000,0x1e74e,0x100000,0x100000,0x100000,0x1e758,0x1e730,0x100000,0x100000,0x1e75a,0x100000,0x100000,0x1ea10,0x1e75c,0x100000,0x100000,0x100000,0x1e75e,0x100000,0x100000,0x100000,0x1e768,0x1e730,0x100000,0x100000,0x1e76a,0x100000,0x100000,0x1ea10,0x1e76c,0x100000,0x100000,0x100000,0x1e76e,0x100000,0x100000,0x100000,0x1e778,0x1e730,0x100000,0x100000,0x1e77a,0x100000,0x100000,0x1ea10,0x1e77c,0x100000,0x100000,0x100000,0x1e77e,0x100000,0x100000,0x100000,0x1ebc0,0x25770,37056,37120,0x1ebc2,0x25772,37072,37128,0x1ebc4,0x25774,37088,37136,0x1ebc6,0x25776,37104,37144,0x1ebc8,0x258d0,37440,37504,0x1ebca,0x258d2,37456,37512,0x1ebcc,0x258d4,37472,37520,0x1ebce,0x258d6,37488,37528,0x1ebd0,0x25a30,37856,37920,0x1ebd2,0x25a32,37872,37928,0x1ebd4,0x25a34,37824,37936,0x1ebd6,0x25a36,37840,37944,0x1ebd8,0x25b90,38240,38304,0x1ebda,0x25b92,38256,38312,0x1ebdc,0x25b94,38208,38320,0x1ebde,0x25b96,38224,38328,0x1ebc0,0x25778,38528,38720,0x1ebc2,0x2577a,38544,38728,0x1ebc4,0x2577c,38560,38736,0x1ebc6,0x2577e,38576,38744,0x1ebc8,0x258d8,38912,39104,0x1ebca,0x258da,38928,39112,0x1ebcc,0x258dc,38944,39120,0x1ebce,0x258de,38960,39128,0x1ebd0,0x25a38,39328,39520,0x1ebd2,0x25a3a,39344,39528,0x1ebd4,0x25a3c,39296,39536,0x1ebd6,0x25a3e,39312,39544,0x1ebd8,0x25b98,39712,39904,0x1ebda,0x25b9a,39728,39912,0x1ebdc,0x25b9c,39680,39920,0x1ebde,0x25b9e,39696,39928,0x1ebc0,0x267f0,40128,40192,0x1ebc2,0x267f2,40144,40200,0x1ebc4,0x267f4,40160,40208,0x1ebc6,0x267f6,40176,40216,0x1ebc8,0x26950,40512,40576,0x1ebca,0x26952,40528,40584,0x1ebcc,0x26954,40544,40592,0x1ebce,0x26956,40560,40600,0x1ebd0,0x26ab0,40928,40992,0x1ebd2,0x26ab2,40944,41000,0x1ebd4,0x26ab4,40896,41008,0x1ebd6,0x26ab6,40912,41016,0x1ebd8,0x26c10,41312,41376,0x1ebda,0x26c12,41328,41384,0x1ebdc,0x26c14,41280,41392,0x1ebde,0x26c16,41296,41400,0x1ebc0,0x100000,41600,41792,0x1ebc2,0x100000,41616,41800,0x1ebc4,0x100000,41632,41808,0x1ebc6,0x100000,41648,41816,0x1ebc8,0x100000,41984,42176,0x1ebca,0x100000,42000,42184,0x1ebcc,0x100000,42016,42192,0x1ebce,0x100000,42032,42200,0x1ebd0,0x100000,42400,42592,0x1ebd2,0x100000,42416,42600,0x1ebd4,0x100000,42368,42608,0x1ebd6,0x100000,42384,42616,0x1ebd8,0x100000,42784,42976,0x1ebda,0x100000,42800,42984,0x1ebdc,0x100000,42752,42992,0x1ebde,0x100000,42768,43000,37056,37088,37120,55296,37064,37096,37128,55312,37072,37104,37136,55328,37080,37112,37144,55344,38208,38240,38272,55360,38216,38248,38280,55376,38224,38256,38288,55392,38232,38264,38296,55408,39392,39360,39456,55424,39400,39368,39464,55440,39408,39376,39472,55456,39416,39384,39480,55472,40544,40512,40608,55488,40552,40520,40616,55504,40560,40528,40624,55520,40568,40536,40632,55536,46208,46240,46400,55552,46216,46248,46408,55568,46224,46256,46416,55584,46232,46264,46424,55600,47360,47392,47552,55616,47368,47400,47560,55632,47376,47408,47568,55648,47384,47416,47576,55664,48544,48512,48736,55680,48552,48520,48744,55696,48560,48528,48752,55712,48568,48536,48760,55728,49696,49664,49888,55744,49704,49672,49896,55760,49712,49680,49904,55776,49720,49688,49912,55792,55488,55520,55552,55808,55496,55528,55560,55824,55504,55536,55568,55840,55512,55544,55576,55856,56640,56672,56704,55872,56648,56680,56712,55888,56656,56688,56720,55904,56664,56696,56728,55920,57824,57792,57888,55936,57832,57800,57896,55952,57840,57808,57904,55968,57848,57816,57912,55984,58976,58944,59040,56000,58984,58952,59048,56016,58992,58960,59056,56032,59000,58968,59064,56048,64640,64672,64832,56064,64648,64680,64840,56080,64656,64688,64848,56096,64664,64696,64856,56112,65792,65824,65984,56128,65800,65832,65992,56144,65808,65840,66000,56160,65816,65848,66008,56176,66976,66944,67168,56192,66984,66952,67176,56208,66992,66960,67184,56224,67000,66968,67192,56240,68128,68096,68320,56256,68136,68104,68328,56272,68144,68112,68336,56288,68152,68120,68344,56304,0x1e800,0x1e800,0x20cc0,36864,0x1e8c4,0x1e8c4,0x20cc4,36880,0x1e988,0x1e988,0x20cc8,36896,0x1ea4c,0x1ea4c,0x20ccc,36912,0x1eb10,0x1eb10,0x20cd0,36928,0x1ebd4,0x1ebd4,0x20cd4,36944,0x1ec98,0x1ec98,0x20cd8,36960,0x1ed5c,0x1ed5c,0x20cdc,36976,0x1ee20,0x1ee20,0x20ce0,37248,0x1eee4,0x1eee4,0x20ce4,37264,0x1efa8,0x1efa8,0x20ce8,37280,0x1f06c,0x1f06c,0x20cec,37296,0x1f130,0x1f130,0x20cf0,37312,0x1f1f4,0x1f1f4,0x20cf4,37328,0x1f2b8,0x1f2b8,0x20cf8,37344,0x1f37c,0x1f37c,0x20cfc,37360,0x1e810,0x1e800,0x20cc0,37632,0x1e8d4,0x1e8c4,0x20cc4,37648,0x1e998,0x1e988,0x20cc8,37664,0x1ea5c,0x1ea4c,0x20ccc,37680,0x1eb20,0x1eb10,0x20cd0,37696,0x1ebe4,0x1ebd4,0x20cd4,37712,0x1eca8,0x1ec98,0x20cd8,37728,0x1ed6c,0x1ed5c,0x20cdc,37744,0x1ee30,0x1ee20,0x20ce0,38016,0x1eef4,0x1eee4,0x20ce4,38032,0x1efb8,0x1efa8,0x20ce8,38048,0x1f07c,0x1f06c,0x20cec,38064,0x1f140,0x1f130,0x20cf0,38080,0x1f204,0x1f1f4,0x20cf4,38096,0x1f2c8,0x1f2b8,0x20cf8,38112,0x1f38c,0x1f37c,0x20cfc,38128,0x1e820,0x1e800,0x20cc0,38400,0x1e8e4,0x1e8c4,0x20cc4,38416,0x1e9a8,0x1e988,0x20cc8,38432,0x1ea6c,0x1ea4c,0x20ccc,38448,0x1eb30,0x1eb10,0x20cd0,38464,0x1ebf4,0x1ebd4,0x20cd4,38480,0x1ecb8,0x1ec98,0x20cd8,38496,0x1ed7c,0x1ed5c,0x20cdc,38512,0x1ee40,0x1ee20,0x20ce0,38784,0x1ef04,0x1eee4,0x20ce4,38800,0x1efc8,0x1efa8,0x20ce8,38816,0x1f08c,0x1f06c,0x20cec,38832,0x1f150,0x1f130,0x20cf0,38848,0x1f214,0x1f1f4,0x20cf4,38864,0x1f2d8,0x1f2b8,0x20cf8,38880,0x1f39c,0x1f37c,0x20cfc,38896,0x1e830,0x1e800,0x20cc0,39168,0x1e8f4,0x1e8c4,0x20cc4,39184,0x1e9b8,0x1e988,0x20cc8,39200,0x1ea7c,0x1ea4c,0x20ccc,39216,0x1eb40,0x1eb10,0x20cd0,39232,0x1ec04,0x1ebd4,0x20cd4,39248,0x1ecc8,0x1ec98,0x20cd8,39264,0x1ed8c,0x1ed5c,0x20cdc,39280,0x1ee50,0x1ee20,0x20ce0,39552,0x1ef14,0x1eee4,0x20ce4,39568,0x1efd8,0x1efa8,0x20ce8,39584,0x1f09c,0x1f06c,0x20cec,39600,0x1f160,0x1f130,0x20cf0,39616,0x1f224,0x1f1f4,0x20cf4,39632,0x1f2e8,0x1f2b8,0x20cf8,39648,0x1f3ac,0x1f37c,0x20cfc,39664,73728,0x26f70,0x1e980,0x1ebc0,73744,0x26f74,0x1ee10,0x1f050,73760,0x26f78,0x1f2a0,0x1f4e0,73776,0x26f7c,0x1f730,0x1f970,73792,0x26f80,0x1fbc0,0x1fe00,73808,0x26f84,0x20050,0x20290,73824,0x26f88,0x204e0,0x20720,73840,0x26f8c,0x20970,0x20bb0,74112,0x26f90,0x20e00,0x21040,74128,0x26f94,0x21290,0x214d0,74144,0x26f98,0x21720,0x21960,74160,0x26f9c,0x21bb0,0x21df0,74176,0x26fa0,0x22040,0x22280,74192,0x26fa4,0x224d0,0x22710,74208,0x26fa8,0x22960,0x22ba0,74224,0x26fac,0x22df0,0x23030,74528,0x26fb0,0x1e9b0,0x1ebc8,74544,0x26fb4,0x1ee40,0x1f058,74496,0x26fb8,0x1f2d0,0x1f4e8,74512,0x26fbc,0x1f760,0x1f978,74592,0x26fc0,0x1fbf0,0x1fe08,74608,0x26fc4,0x20080,0x20298,74560,0x26fc8,0x20510,0x20728,74576,0x26fcc,0x209a0,0x20bb8,74912,0x26fd0,0x20e30,0x21048,74928,0x26fd4,0x212c0,0x214d8,74880,0x26fd8,0x21750,0x21968,74896,0x26fdc,0x21be0,0x21df8,74976,0x26fe0,0x22070,0x22288,74992,0x26fe4,0x22500,0x22718,74944,0x26fe8,0x22990,0x22ba8,74960,0x26fec,0x22e20,0x23038,75328,0x26ff0,0x1e9e0,0x1ebd0,75344,0x26ff4,0x1ee70,0x1f060,75360,0x26ff8,0x1f300,0x1f4f0,75376,0x26ffc,0x1f790,0x1f980,75264,0x27000,0x1fc20,0x1fe10,75280,0x27004,0x200b0,0x202a0,75296,0x27008,0x20540,0x20730,75312,0x2700c,0x209d0,0x20bc0,75712,0x27010,0x20e60,0x21050,75728,0x27014,0x212f0,0x214e0,75744,0x27018,0x21780,0x21970,75760,0x2701c,0x21c10,0x21e00,75648,0x27020,0x220a0,0x22290,75664,0x27024,0x22530,0x22720,75680,0x27028,0x229c0,0x22bb0,75696,0x2702c,0x22e50,0x23040,76128,0x27030,0x1ea10,0x1ebd8,76144,0x27034,0x1eea0,0x1f068,76096,0x27038,0x1f330,0x1f4f8,76112,0x2703c,0x1f7c0,0x1f988,76064,0x27040,0x1fc50,0x1fe18,76080,0x27044,0x200e0,0x202a8,76032,0x27048,0x20570,0x20738,76048,0x2704c,0x20a00,0x20bc8,76512,0x27050,0x20e90,0x21058,76528,0x27054,0x21320,0x214e8,76480,0x27058,0x217b0,0x21978,76496,0x2705c,0x21c40,0x21e08,76448,0x27060,0x220d0,0x22298,76464,0x27064,0x22560,0x22728,76416,0x27068,0x229f0,0x22bb8,76432,0x2706c,0x22e80,0x23048,0x3f803f80,0x3f803f80,16256,0,0,0x3f803f80,0x3f800000,0,0,0x3f803f80,0,16256,0,0x3f803f80,0,0x3f800000,0x3f803f80,0,0,0,0,0,0,0,0,0,0,0,0,0,0,0,0x3f803f80,0,0,0,0,0,0,0,0,0,0,0,0,0,0,0,0x3f803f80,0,0,0,0,0,0,0,0,0,0,0,0,0,0,0,0x3f803f80,0,0,0,0,0,0,0,0,0,0,0,0,0,0,0,0x3f803f80,0x3f803f80,16256,0,0,0x3f803f80,0x3f800000,0,0,0x3f803f80,0,16256,0,0x3f803f80,0,0x3f800000,0x3f803f80,0,0,0,0,0,0,0,0,0,0,0,0,0,0,0,0x3f803f80,0,0,0,0,0,0,0,0,0,0,0,0,0,0,0,0x3f803f80,0,0,0,0,0,0,0,0,0,0,0,0,0,0,0,0x3f803f80,0,0,0,0,0,0,0,0,0,0,0,0,0,0,0,0x3f803f80,0x3f803f80,16256,0,0,0x3f803f80,0x3f800000,0,0,0x3f803f80,0,16256,0,0x3f803f80,0,0x3f800000,0x3f803f80,0,0,0,0,0,0,0,0,0,0,0,0,0,0,0,0x3f803f80,0,0,0,0,0,0,0,0,0,0,0,0,0,0,0,0x3f803f80,0,0,0,0,0,0,0,0,0,0,0,0,0,0,0,0x3f803f80,0,0,0,0,0,0,0,0,0,0,0,0,0,0,0,0x3f803f80,0x3f803f80,16256,0,0,0x3f803f80,0x3f800000,0,0,0x3f803f80,0,16256,0,0x3f803f80,0,0x3f800000,0,64,0x90000,0xf0080,144,208,0x90010,0xf0090,288,352,0x90020,0xf00a0,432,496,0x90030,0xf00b0,576,512,0x90040,0xf00c0,720,656,0x90050,0xf00d0,864,800,0x90060,0xf00e0,1008,944,0x90070,0xf00f0,1024,1088,0x90190,0xf0210,1168,1232,0x90180,0xf0200,1312,1376,0x901b0,0xf0230,1456,1520,0x901a0,0xf0220,1600,1536,0x901d0,0xf0250,1744,1680,0x901c0,0xf0240,1888,1824,0x901f0,0xf0270,2032,1968,0x901e0,0xf0260,16,80,0x90320,0x11f7a0,128,192,0x90330,0x11f7b0,304,368,0x90300,0x11f780,416,480,0x90310,0x11f790,592,528,0x90360,0x11f7e0,704,640,0x90370,0x11f7f0,880,816,0x90340,0x11f7c0,992,928,0x90350,0x11f7d0,1040,1104,0x904b0,0x11f930,1152,1216,0x904a0,0x11f920,1328,1392,0x90490,0x11f910,1440,1504,0x90480,0x11f900,1616,1552,0x904f0,0x11f970,1728,1664,0x904e0,0x11f960,1904,1840,0x904d0,0x11f950,2016,1952,0x904c0,0x11f940,32,96,0xbfa40,0x11fac0,176,240,0xbfa50,0x11fad0,256,320,0xbfa60,0x11fae0,400,464,0xbfa70,0x11faf0,608,544,0xbfa00,0x11fa80,752,688,0xbfa10,0x11fa90,832,768,0xbfa20,0x11faa0,976,912,0xbfa30,0x11fab0,1056,1120,0xbfbd0,0x11fc50,1200,1264,0xbfbc0,0x11fc40,1280,1344,0xbfbf0,0x11fc70,1424,1488,0xbfbe0,0x11fc60,1632,1568,0xbfb90,0x11fc10,1776,1712,0xbfb80,0x11fc00,1856,1792,0xbfbb0,0x11fc30,2000,1936,0xbfba0,0x11fc20,48,112,0xbfd60,0x11fde0,160,224,0xbfd70,0x11fdf0,272,336,0xbfd40,0x11fdc0,384,448,0xbfd50,0x11fdd0,624,560,0xbfd20,0x11fda0,736,672,0xbfd30,0x11fdb0,848,784,0xbfd00,0x11fd80,960,896,0xbfd10,0x11fd90,1072,1136,0xbfef0,0x11ff70,1184,1248,0xbfee0,0x11ff60,1296,1360,0xbfed0,0x11ff50,1408,1472,0xbfec0,0x11ff40,1648,1584,0xbfeb0,0x11ff30,1760,1696,0xbfea0,0x11ff20,1872,1808,0xbfe90,0x11ff10,1984,1920,0xbfe80,0x11ff00,0x11fb40,76800,84992,91648,0x11fb50,76816,85008,91664,0x11fb60,76832,85024,91680,0x11fb70,76848,85040,91696,0x11fb00,76864,85056,91712,0x11fb10,76880,85072,91728,0x11fb20,76896,85088,91744,0x11fb30,76912,85104,91760,0x11fcd0,76928,85120,91776,0x11fcc0,76944,85136,91792,0x11fcf0,76960,85152,91808,0x11fce0,76976,85168,91824,0x11fc90,76992,85184,91840,0x11fc80,77008,85200,91856,0x11fcb0,77024,85216,91872,0x11fca0,77040,85232,91888,0x11fe60,77056,85248,91904,0x11fe70,77072,85264,91920,0x11fe40,77088,85280,91936,0x11fe50,77104,85296,91952,0x11fe20,77120,85312,91968,0x11fe30,77136,85328,91984,0x11fe00,77152,85344,92000,0x11fe10,77168,85360,92016,0x11fff0,77184,85376,92032,0x11ffe0,77200,85392,92048,0x11ffd0,77216,85408,92064,0x11ffc0,77232,85424,92080,0x11ffb0,77248,85440,92096,0x11ffa0,77264,85456,92112,0x11ff90,77280,85472,92128,0x11ff80,77296,85488,92144,0x11fb40,77312,85504,92160,0x11fb50,77328,85520,92176,0x11fb60,77344,85536,92192,0x11fb70,77360,85552,92208,0x11fb00,77376,85568,92224,0x11fb10,77392,85584,92240,0x11fb20,77408,85600,92256,0x11fb30,77424,85616,92272,0x11fcd0,77440,85632,92288,0x11fcc0,77456,85648,92304,0x11fcf0,77472,85664,92320,0x11fce0,77488,85680,92336,0x11fc90,77504,85696,92352,0x11fc80,77520,85712,92368,0x11fcb0,77536,85728,92384,0x11fca0,77552,85744,92400,0x11fe60,77568,85760,92416,0x11fe70,77584,85776,92432,0x11fe40,77600,85792,92448,0x11fe50,77616,85808,92464,0x11fe20,77632,85824,92480,0x11fe30,77648,85840,92496,0x11fe00,77664,85856,92512,0x11fe10,77680,85872,92528,0x11fff0,77696,85888,92544,0x11ffe0,77712,85904,92560,0x11ffd0,77728,85920,92576,0x11ffc0,77744,85936,92592,0x11ffb0,77760,85952,92608,0x11ffa0,77776,85968,92624,0x11ff90,77792,85984,92640,0x11ff80,77808,86000,92656,0x1c1d0,36864,46080,52232,0x1c1d4,36880,46096,52248,0x1c1d8,36896,46112,52264,0x1c1dc,36912,46128,52280,0x1c1e0,36928,46144,52296,0x1c1e4,36944,46160,52312,0x1c1e8,36960,46176,52328,0x1c1ec,36976,46192,52344,0x1c1f0,36992,46208,52360,0x1c1f4,37008,46224,52376,0x1c1f8,37024,46240,52392,0x1c1fc,37040,46256,52408,0x1c200,37056,46272,52424,0x1c204,37072,46288,52440,0x1c208,37088,46304,52456,0x1c20c,37104,46320,52472,0x1c210,37120,46336,52488,0x1c214,37136,46352,52504,0x1c218,37152,46368,52520,0x1c21c,37168,46384,52536,0x1c220,37184,46400,52552,0x1c224,37200,46416,52568,0x1c228,37216,46432,52584,0x1c22c,37232,46448,52600,0x1c230,37248,46464,52616,0x1c234,37264,46480,52632,0x1c238,37280,46496,52648,0x1c23c,37296,46512,52664,0x1c240,37312,46528,52680,0x1c244,37328,46544,52696,0x1c248,37344,46560,52712,0x1c24c,37360,46576,52728,0x1c250,37376,46592,52744,0x1c254,37392,46608,52760,0x1c258,37408,46624,52776,0x1c25c,37424,46640,52792,0x1c260,37440,46656,52808,0x1c264,37456,46672,52824,0x1c268,37472,46688,52840,0x1c26c,37488,46704,52856,0x1c270,37504,46720,52872,0x1c274,37520,46736,52888,0x1c278,37536,46752,52904,0x1c27c,37552,46768,52920,0x1c280,37568,46784,52936,0x1c284,37584,46800,52952,0x1c288,37600,46816,52968,0x1c28c,37616,46832,52984,0x1c290,37632,46848,53000,0x1c294,37648,46864,53016,0x1c298,37664,46880,53032,0x1c29c,37680,46896,53048,0x1c2a0,37696,46912,53064,0x1c2a4,37712,46928,53080,0x1c2a8,37728,46944,53096,0x1c2ac,37744,46960,53112,0x1c2b0,37760,46976,53128,0x1c2b4,37776,46992,53144,0x1c2b8,37792,47008,53160,0x1c2bc,37808,47024,53176,0x1c2c0,37824,47040,53192,0x1c2c4,37840,47056,53208,0x1c2c8,37856,47072,53224,0x1c2cc,37872,47088,53240,86048,86112,0x1d600,0x1d60c,86192,86256,0x1d610,0x1d61c,86272,86336,0x1d620,0x1d62c,86416,86480,0x1d630,0x1d63c,86624,86560,0x1d640,0x1d64c,86768,86704,0x1d650,0x1d65c,86848,86784,0x1d660,0x1d66c,86992,86928,0x1d670,0x1d67c,87072,87136,0x1d680,0x1d68c,87216,87280,0x1d690,0x1d69c,87296,87360,0x1d6a0,0x1d6ac,87440,87504,0x1d6b0,0x1d6bc,87648,87584,0x1d6c0,0x1d6cc,87792,87728,0x1d6d0,0x1d6dc,87872,87808,0x1d6e0,0x1d6ec,88016,87952,0x1d6f0,0x1d6fc,86064,86120,0x1d700,0x1d70c,86176,86264,0x1d710,0x1d71c,86288,86344,0x1d720,0x1d72c,86400,86488,0x1d730,0x1d73c,86640,86568,0x1d740,0x1d74c,86752,86712,0x1d750,0x1d75c,86864,86792,0x1d760,0x1d76c,86976,86936,0x1d770,0x1d77c,87088,87144,0x1d780,0x1d78c,87200,87288,0x1d790,0x1d79c,87312,87368,0x1d7a0,0x1d7ac,87424,87512,0x1d7b0,0x1d7bc,87664,87592,0x1d7c0,0x1d7cc,87776,87736,0x1d7d0,0x1d7dc,87888,87816,0x1d7e0,0x1d7ec,88000,87960,0x1d7f0,0x1d7fc,86080,86128,0x1d800,0x1d80c,86224,86240,0x1d810,0x1d81c,86368,86352,0x1d820,0x1d82c,86512,86464,0x1d830,0x1d83c,86528,86576,0x1d840,0x1d84c,86672,86688,0x1d850,0x1d85c,86816,86800,0x1d860,0x1d86c,86960,86912,0x1d870,0x1d87c,87104,87152,0x1d880,0x1d88c,87248,87264,0x1d890,0x1d89c,87392,87376,0x1d8a0,0x1d8ac,87536,87488,0x1d8b0,0x1d8bc,87552,87600,0x1d8c0,0x1d8cc,87696,87712,0x1d8d0,0x1d8dc,87840,87824,0x1d8e0,0x1d8ec,87984,87936,0x1d8f0,0x1d8fc,86096,86136,0x1d900,0x1d90c,86208,86248,0x1d910,0x1d91c,86384,86360,0x1d920,0x1d92c,86496,86472,0x1d930,0x1d93c,86544,86584,0x1d940,0x1d94c,86656,86696,0x1d950,0x1d95c,86832,86808,0x1d960,0x1d96c,86944,86920,0x1d970,0x1d97c,87120,87160,0x1d980,0x1d98c,87232,87272,0x1d990,0x1d99c,87408,87384,0x1d9a0,0x1d9ac,87520,87496,0x1d9b0,0x1d9bc,87568,87608,0x1d9c0,0x1d9cc,87680,87720,0x1d9d0,0x1d9dc,87856,87832,0x1d9e0,0x1d9ec,87968,87944,0x1d9f0,0x1d9fc,0x1e780,0x1e780,0x1ea40,0x100000,0x1e782,0x100000,0x100000,0x1ea40,0x1e784,0x100000,0x100000,0x100000,0x1e786,0x100000,0x100000,0x100000,0x1e790,0x1e780,0x100000,0x100000,0x1e792,0x100000,0x100000,0x1ea40,0x1e794,0x100000,0x100000,0x100000,0x1e796,0x100000,0x100000,0x100000,0x1e7a0,0x1e780,0x100000,0x100000,0x1e7a2,0x100000,0x100000,0x1ea40,0x1e7a4,0x100000,0x100000,0x100000,0x1e7a6,0x100000,0x100000,0x100000,0x1e7b0,0x1e780,0x100000,0x100000,0x1e7b2,0x100000,0x100000,0x1ea40,0x1e7b4,0x100000,0x100000,0x100000,0x1e7b6,0x100000,0x100000,0x100000,0x1e788,0x1e790,0x1ea70,0x100000,0x1e78a,0x100000,0x100000,0x1ea70,0x1e78c,0x100000,0x100000,0x100000,0x1e78e,0x100000,0x100000,0x100000,0x1e798,0x1e790,0x100000,0x100000,0x1e79a,0x100000,0x100000,0x1ea70,0x1e79c,0x100000,0x100000,0x100000,0x1e79e,0x100000,0x100000,0x100000,0x1e7a8,0x1e790,0x100000,0x100000,0x1e7aa,0x100000,0x100000,0x1ea70,0x1e7ac,0x100000,0x100000,0x100000,0x1e7ae,0x100000,0x100000,0x100000,0x1e7b8,0x1e790,0x100000,0x100000,0x1e7ba,0x100000,0x100000,0x1ea70,0x1e7bc,0x100000,0x100000,0x100000,0x1e7be,0x100000,0x100000,0x100000,0x1e7c0,0x1e7a0,0x1eaa0,0x100000,0x1e7c2,0x100000,0x100000,0x1eaa0,0x1e7c4,0x100000,0x100000,0x100000,0x1e7c6,0x100000,0x100000,0x100000,0x1e7d0,0x1e7a0,0x100000,0x100000,0x1e7d2,0x100000,0x100000,0x1eaa0,0x1e7d4,0x100000,0x100000,0x100000,0x1e7d6,0x100000,0x100000,0x100000,0x1e7e0,0x1e7a0,0x100000,0x100000,0x1e7e2,0x100000,0x100000,0x1eaa0,0x1e7e4,0x100000,0x100000,0x100000,0x1e7e6,0x100000,0x100000,0x100000,0x1e7f0,0x1e7a0,0x100000,0x100000,0x1e7f2,0x100000,0x100000,0x1eaa0,0x1e7f4,0x100000,0x100000,0x100000,0x1e7f6,0x100000,0x100000,0x100000,0x1e7c8,0x1e7b0,0x1ead0,0x100000,0x1e7ca,0x100000,0x100000,0x1ead0,0x1e7cc,0x100000,0x100000,0x100000,0x1e7ce,0x100000,0x100000,0x100000,0x1e7d8,0x1e7b0,0x100000,0x100000,0x1e7da,0x100000,0x100000,0x1ead0,0x1e7dc,0x100000,0x100000,0x100000,0x1e7de,0x100000,0x100000,0x100000,0x1e7e8,0x1e7b0,0x100000,0x100000,0x1e7ea,0x100000,0x100000,0x1ead0,0x1e7ec,0x100000,0x100000,0x100000,0x1e7ee,0x100000,0x100000,0x100000,0x1e7f8,0x1e7b0,0x100000,0x100000,0x1e7fa,0x100000,0x100000,0x1ead0,0x1e7fc,0x100000,0x100000,0x100000,0x1e7fe,0x100000,0x100000,0x100000,0x1ec20,0x25cf0,37152,37216,0x1ec22,0x25cf2,37168,37224,0x1ec24,0x25cf4,37184,37232,0x1ec26,0x25cf6,37200,37240,0x1ec28,0x25e50,37536,37600,0x1ec2a,0x25e52,37552,37608,0x1ec2c,0x25e54,37568,37616,0x1ec2e,0x25e56,37584,37624,0x1ec30,0x25fb0,37888,37952,0x1ec32,0x25fb2,37904,37960,0x1ec34,0x25fb4,37984,37968,0x1ec36,0x25fb6,38000,37976,0x1ec38,0x26110,38272,38336,0x1ec3a,0x26112,38288,38344,0x1ec3c,0x26114,38368,38352,0x1ec3e,0x26116,38384,38360,0x1ec20,0x25cf8,38752,38688,0x1ec22,0x25cfa,38768,38696,0x1ec24,0x25cfc,38656,38704,0x1ec26,0x25cfe,38672,38712,0x1ec28,0x25e58,39136,39072,0x1ec2a,0x25e5a,39152,39080,0x1ec2c,0x25e5c,39040,39088,0x1ec2e,0x25e5e,39056,39096,0x1ec30,0x25fb8,39488,39424,0x1ec32,0x25fba,39504,39432,0x1ec34,0x25fbc,39456,39440,0x1ec36,0x25fbe,39472,39448,0x1ec38,0x26118,39872,39808,0x1ec3a,0x2611a,39888,39816,0x1ec3c,0x2611c,39840,39824,0x1ec3e,0x2611e,39856,39832,0x1ec20,0x267f8,40224,40288,0x1ec22,0x267fa,40240,40296,0x1ec24,0x267fc,40256,40304,0x1ec26,0x267fe,40272,40312,0x1ec28,0x26958,40608,40672,0x1ec2a,0x2695a,40624,40680,0x1ec2c,0x2695c,40640,40688,0x1ec2e,0x2695e,40656,40696,0x1ec30,0x26ab8,40960,41024,0x1ec32,0x26aba,40976,41032,0x1ec34,0x26abc,41056,41040,0x1ec36,0x26abe,41072,41048,0x1ec38,0x26c18,41344,41408,0x1ec3a,0x26c1a,41360,41416,0x1ec3c,0x26c1c,41440,41424,0x1ec3e,0x26c1e,41456,41432,0x1ec20,0x100000,41824,41760,0x1ec22,0x100000,41840,41768,0x1ec24,0x100000,41728,41776,0x1ec26,0x100000,41744,41784,0x1ec28,0x100000,42208,42144,0x1ec2a,0x100000,42224,42152,0x1ec2c,0x100000,42112,42160,0x1ec2e,0x100000,42128,42168,0x1ec30,0x100000,42560,42496,0x1ec32,0x100000,42576,42504,0x1ec34,0x100000,42528,42512,0x1ec36,0x100000,42544,42520,0x1ec38,0x100000,42944,42880,0x1ec3a,0x100000,42960,42888,0x1ec3c,0x100000,42912,42896,0x1ec3e,0x100000,42928,42904,37152,37184,37216,64512,37160,37192,37224,64528,37168,37200,37232,64544,37176,37208,37240,64560,38304,38336,38368,64576,38312,38344,38376,64592,38320,38352,38384,64608,38328,38360,38392,64624,39424,39520,39488,64640,39432,39528,39496,64656,39440,39536,39504,64672,39448,39544,39512,64688,40576,40672,40640,64704,40584,40680,40648,64720,40592,40688,40656,64736,40600,40696,40664,64752,46432,46336,46368,64768,46440,46344,46376,64784,46448,46352,46384,64800,46456,46360,46392,64816,47584,47488,47520,64832,47592,47496,47528,64848,47600,47504,47536,64864,47608,47512,47544,64880,48704,48672,48640,64896,48712,48680,48648,64912,48720,48688,48656,64928,48728,48696,48664,64944,49856,49824,49792,64960,49864,49832,49800,64976,49872,49840,49808,64992,49880,49848,49816,65008,55584,55616,55648,65024,55592,55624,55656,65040,55600,55632,55664,65056,55608,55640,55672,65072,56736,56768,56800,65088,56744,56776,56808,65104,56752,56784,56816,65120,56760,56792,56824,65136,57856,57952,57920,65152,57864,57960,57928,65168,57872,57968,57936,65184,57880,57976,57944,65200,59008,59104,59072,65216,59016,59112,59080,65232,59024,59120,59088,65248,59032,59128,59096,65264,64864,64768,64800,65280,64872,64776,64808,65296,64880,64784,64816,65312,64888,64792,64824,65328,66016,65920,65952,65344,66024,65928,65960,65360,66032,65936,65968,65376,66040,65944,65976,65392,67136,67104,67072,65408,67144,67112,67080,65424,67152,67120,67088,65440,67160,67128,67096,65456,68288,68256,68224,65472,68296,68264,68232,65488,68304,68272,68240,65504,68312,68280,68248,65520,0x1e800,0x1e800,0x20cc0,55296,0x1e8c4,0x1e8c4,0x20cc4,55312,0x1e988,0x1e988,0x20cc8,55328,0x1ea4c,0x1ea4c,0x20ccc,55344,0x1eb10,0x1eb10,0x20cd0,55360,0x1ebd4,0x1ebd4,0x20cd4,55376,0x1ec98,0x1ec98,0x20cd8,55392,0x1ed5c,0x1ed5c,0x20cdc,55408,0x1ee20,0x1ee20,0x20ce0,55680,0x1eee4,0x1eee4,0x20ce4,55696,0x1efa8,0x1efa8,0x20ce8,55712,0x1f06c,0x1f06c,0x20cec,55728,0x1f130,0x1f130,0x20cf0,55744,0x1f1f4,0x1f1f4,0x20cf4,55760,0x1f2b8,0x1f2b8,0x20cf8,55776,0x1f37c,0x1f37c,0x20cfc,55792,0x1e810,0x1e800,0x20cc0,56064,0x1e8d4,0x1e8c4,0x20cc4,56080,0x1e998,0x1e988,0x20cc8,56096,0x1ea5c,0x1ea4c,0x20ccc,56112,0x1eb20,0x1eb10,0x20cd0,56128,0x1ebe4,0x1ebd4,0x20cd4,56144,0x1eca8,0x1ec98,0x20cd8,56160,0x1ed6c,0x1ed5c,0x20cdc,56176,0x1ee30,0x1ee20,0x20ce0,56448,0x1eef4,0x1eee4,0x20ce4,56464,0x1efb8,0x1efa8,0x20ce8,56480,0x1f07c,0x1f06c,0x20cec,56496,0x1f140,0x1f130,0x20cf0,56512,0x1f204,0x1f1f4,0x20cf4,56528,0x1f2c8,0x1f2b8,0x20cf8,56544,0x1f38c,0x1f37c,0x20cfc,56560,0x1e820,0x1e800,0x20cc0,56832,0x1e8e4,0x1e8c4,0x20cc4,56848,0x1e9a8,0x1e988,0x20cc8,56864,0x1ea6c,0x1ea4c,0x20ccc,56880,0x1eb30,0x1eb10,0x20cd0,56896,0x1ebf4,0x1ebd4,0x20cd4,56912,0x1ecb8,0x1ec98,0x20cd8,56928,0x1ed7c,0x1ed5c,0x20cdc,56944,0x1ee40,0x1ee20,0x20ce0,57216,0x1ef04,0x1eee4,0x20ce4,57232,0x1efc8,0x1efa8,0x20ce8,57248,0x1f08c,0x1f06c,0x20cec,57264,0x1f150,0x1f130,0x20cf0,57280,0x1f214,0x1f1f4,0x20cf4,57296,0x1f2d8,0x1f2b8,0x20cf8,57312,0x1f39c,0x1f37c,0x20cfc,57328,0x1e830,0x1e800,0x20cc0,57600,0x1e8f4,0x1e8c4,0x20cc4,57616,0x1e9b8,0x1e988,0x20cc8,57632,0x1ea7c,0x1ea4c,0x20ccc,57648,0x1eb40,0x1eb10,0x20cd0,57664,0x1ec04,0x1ebd4,0x20cd4,57680,0x1ecc8,0x1ec98,0x20cd8,57696,0x1ed8c,0x1ed5c,0x20cdc,57712,0x1ee50,0x1ee20,0x20ce0,57984,0x1ef14,0x1eee4,0x20ce4,58000,0x1efd8,0x1efa8,0x20ce8,58016,0x1f09c,0x1f06c,0x20cec,58032,0x1f160,0x1f130,0x20cf0,58048,0x1f224,0x1f1f4,0x20cf4,58064,0x1f2e8,0x1f2b8,0x20cf8,58080,0x1f3ac,0x1f37c,0x20cfc,58096,92160,0x27070,0x1ea40,0x1ec20,92176,0x27074,0x1eed0,0x1f0b0,92192,0x27078,0x1f360,0x1f540,92208,0x2707c,0x1f7f0,0x1f9d0,92224,0x27080,0x1fc80,0x1fe60,92240,0x27084,0x20110,0x202f0,92256,0x27088,0x205a0,0x20780,92272,0x2708c,0x20a30,0x20c10,92544,0x27090,0x20ec0,0x210a0,92560,0x27094,0x21350,0x21530,92576,0x27098,0x217e0,0x219c0,92592,0x2709c,0x21c70,0x21e50,92608,0x270a0,0x22100,0x222e0,92624,0x270a4,0x22590,0x22770,92640,0x270a8,0x22a20,0x22c00,92656,0x270ac,0x22eb0,0x23090,92960,0x270b0,0x1ea70,0x1ec28,92976,0x270b4,0x1ef00,0x1f0b8,92928,0x270b8,0x1f390,0x1f548,92944,0x270bc,0x1f820,0x1f9d8,93024,0x270c0,0x1fcb0,0x1fe68,93040,0x270c4,0x20140,0x202f8,92992,0x270c8,0x205d0,0x20788,93008,0x270cc,0x20a60,0x20c18,93344,0x270d0,0x20ef0,0x210a8,93360,0x270d4,0x21380,0x21538,93312,0x270d8,0x21810,0x219c8,93328,0x270dc,0x21ca0,0x21e58,93408,0x270e0,0x22130,0x222e8,93424,0x270e4,0x225c0,0x22778,93376,0x270e8,0x22a50,0x22c08,93392,0x270ec,0x22ee0,0x23098,93760,0x270f0,0x1eaa0,0x1ec30,93776,0x270f4,0x1ef30,0x1f0c0,93792,0x270f8,0x1f3c0,0x1f550,93808,0x270fc,0x1f850,0x1f9e0,93696,0x27100,0x1fce0,0x1fe70,93712,0x27104,0x20170,0x20300,93728,0x27108,0x20600,0x20790,93744,0x2710c,0x20a90,0x20c20,94144,0x27110,0x20f20,0x210b0,94160,0x27114,0x213b0,0x21540,94176,0x27118,0x21840,0x219d0,94192,0x2711c,0x21cd0,0x21e60,94080,0x27120,0x22160,0x222f0,94096,0x27124,0x225f0,0x22780,94112,0x27128,0x22a80,0x22c10,94128,0x2712c,0x22f10,0x230a0,94560,0x27130,0x1ead0,0x1ec38,94576,0x27134,0x1ef60,0x1f0c8,94528,0x27138,0x1f3f0,0x1f558,94544,0x2713c,0x1f880,0x1f9e8,94496,0x27140,0x1fd10,0x1fe78,94512,0x27144,0x201a0,0x20308,94464,0x27148,0x20630,0x20798,94480,0x2714c,0x20ac0,0x20c28,94944,0x27150,0x20f50,0x210b8,94960,0x27154,0x213e0,0x21548,94912,0x27158,0x21870,0x219d8,94928,0x2715c,0x21d00,0x21e68,94880,0x27160,0x22190,0x222f8,94896,0x27164,0x22620,0x22788,94848,0x27168,0x22ab0,0x22c18,94864,0x2716c,0x22f40,0x230a8,0x3f803f80,0x3f803f80,16256,0,0,0x3f803f80,0x3f800000,0,0,0x3f803f80,0,16256,0,0x3f803f80,0,0x3f800000,0x3f803f80,0,0,0,0,0,0,0,0,0,0,0,0,0,0,0,0x3f803f80,0,0,0,0,0,0,0,0,0,0,0,0,0,0,0,0x3f803f80,0,0,0,0,0,0,0,0,0,0,0,0,0,0,0,0x3f803f80,0,0,0,0,0,0,0,0,0,0,0,0,0,0,0,0x3f803f80,0x3f803f80,16256,0,0,0x3f803f80,0x3f800000,0,0,0x3f803f80,0,16256,0,0x3f803f80,0,0x3f800000,0x3f803f80,0,0,0,0,0,0,0,0,0,0,0,0,0,0,0,0x3f803f80,0,0,0,0,0,0,0,0,0,0,0,0,0,0,0,0x3f803f80,0,0,0,0,0,0,0,0,0,0,0,0,0,0,0,0x3f803f80,0,0,0,0,0,0,0,0,0,0,0,0,0,0,0,0x3f803f80,0x3f803f80,16256,0,0,0x3f803f80,0x3f800000,0,0,0x3f803f80,0,16256,0,0x3f803f80,0,0x3f800000,0x3f803f80,0,0,0,0,0,0,0,0,0,0,0,0,0,0,0,0x3f803f80,0,0,0,0,0,0,0,0,0,0,0,0,0,0,0,0x3f803f80,0,0,0,0,0,0,0,0,0,0,0,0,0,0,0,0x3f803f80,0,0,0,0,0,0,0,0,0,0,0,0,0,0,0,0x3f803f80,0x3f803f80,16256,0,0,0x3f803f80,0x3f800000,0,0,0x3f803f80,0,16256,0,0x3f803f80,0,0x3f800000,0xffffffff,0xffffffff,0,0,0,0,0xffffffff,0xff000000,0xff000000,0xffffff,0xff000000,0xff000000,0,4096,16384,0x1c000,0xffffffff,0xffffffff,0,0,0,0,0xffffff,0xffff0000,0xffff0000,65535,0xffffff,0xffffff,1024,5120,16896,0x1c0a0,0xffffffff,0xffffffff,0,0,0,0,65535,0xffffff00,0xff000000,0xffffff,0xffff0000,0xffff0000,2048,10240,17408,0x1c140,0xffffffff,0,0,0,0,0,255,0xffffffff,0xffff0000,65535,65535,65535,3072,11264,17920,0x1c1d0,0,0x3fb8aa3b,0x2a8cbccc,0x38fba882,0x7fff7fff,0x415db3d7,0xffff0000,0,0,65535,0,0xffff0000,0,0,0,0,0,808,1800,2608,3600,4408,5400,6208,7200,8192,9000,9992,10800,11792,12600,18432,14385,18447,16170,18462,18462,18462,18462,18462,20,19,0,0,0,0,0,0,0x1b0000,0x5b8480,0xae0a80,0xee8f00,0x1411500,0x1819980,0x1d41f80,0x214a400,0x2672a00,0x2b9b000,0x2fa3480,0x34cba80,0x38d3f00,0x3dfc500,0x4204980,0x60a80,0x4b2b680,0x6a800,0x5452380,0x74580,0x74580,0x74580,0x74580,0x74580,0x74580,0x74580,0x74580,0x74580,0x74580,0x74580,0x74580,0x74580,1,809,1801,2609,3601,4409,5401,6209,7201,8193,9001,9993,10801,11793,12601,13592,14386,15377,16171,18463,18463,18463,18463,18463,20,19,0,0,0,0,0,0,0x1b0a80,0x5b8f00,0xae1500,0xee9980,0x1411f80,0x181a400,0x1d42a00,0x214ae80,0x2673480,0x2b9ba80,0x2fa3f00,0x34cc500,0x38d4980,0x3dfcf80,0x4205400,0x472cf80,0x4b2c100,0x5053c80,0x5452e00,0x75000,0x75000,0x75000,0x75000,0x75000,0x75000,0x75000,0x75000,0x75000,0x75000,0x75000,0x75000,0x75000,2,810,1802,2610,3602,4410,5402,6210,7202,8194,9002,9994,10802,11794,12602,13593,14387,15378,16172,17162,17671,18216,18216,18216,22,21,0,0,0,0,0,0,0x1b1500,0x5b9980,0xae1f80,0xeea400,0x1412a00,0x181ae80,0x1d43480,0x214b900,0x2673f00,0x2b9c500,0x2fa4980,0x34ccf80,0x38d5400,0x3dfda00,0x4205e80,0x472da00,0x4b2cb80,0x5054700,0x5453880,0x597a980,0x5c17500,0x5ecba80,0x5ecba80,0x5ecba80,0x5ecba80,0x5ecba80,0x5ecba80,0x5ecba80,0x5ecba80,0x5ecba80,0x5ecba80,0x5ecba80,3,811,1803,2611,3603,4411,5403,6211,7203,8195,9003,9995,10803,11795,12603,13594,14388,15379,16173,17163,17672,17672,17672,17672,21,20,0,0,0,0,0,0,0x1b1f80,0x5ba400,0xae2a00,0xeeae80,0x1413480,0x181b900,0x1d43f00,0x214c380,0x2674980,0x2b9cf80,0x2fa5400,0x34cda00,0x38d5e80,0x3dfe480,0x4206900,0x472e480,0x4b2d600,0x5055180,0x5454300,0x597b400,0x5c17f80,0x5c17f80,0x5c17f80,0x5c17f80,0x5c17f80,0x5c17f80,0x5c17f80,0x5c17f80,0x5c17f80,0x5c17f80,0x5c17f80,0x5c17f80,4,812,1804,2612,3604,4412,5404,6212,7204,8196,9004,9996,10804,11796,12604,13595,14389,15380,16174,18464,17673,17673,17673,17673,21,20,0,0,0,0,0,0,0x1b2a00,0x5bae80,0xae3480,0xeeb900,0x1413f00,0x181c380,0x1d44980,0x214ce00,0x2675400,0x2b9da00,0x2fa5e80,0x34ce480,0x38d6900,0x3dfef00,0x4207380,0x472ef00,0x4b2e080,0x5055c00,0x5454d80,0x75a80,0x5c18a00,0x5c18a00,0x5c18a00,0x5c18a00,0x5c18a00,0x5c18a00,0x5c18a00,0x5c18a00,0x5c18a00,0x5c18a00,0x5c18a00,0x5c18a00,5,813,1805,2613,3605,4413,5405,6213,7205,8197,9005,9997,10805,11797,12605,13596,14390,15381,16175,18465,18465,18465,18465,18465,20,19,0,0,0,0,0,0,0x1b3480,0x5bb900,0xae3f00,0xeec380,0x1414980,0x181ce00,0x1d45400,0x214d880,0x2675e80,0x2b9e480,0x2fa6900,0x34cef00,0x38d7380,0x3dff980,0x4207e00,0x472f980,0x4b2eb00,0x5056680,0x5455800,0x76500,0x76500,0x76500,0x76500,0x76500,0x76500,0x76500,0x76500,0x76500,0x76500,0x76500,0x76500,0x76500,6,814,1806,2614,3606,4414,5406,6214,7206,8198,9006,9998,10806,11798,12606,13597,14391,15382,16176,18466,17674,17674,17674,17674,21,20,0,0,0,0,0,0,0x1b3f00,0x5bc380,0xae4980,0xeece00,0x1415400,0x181d880,0x1d45e80,0x214e300,0x2676900,0x2b9ef00,0x2fa7380,0x34cf980,0x38d7e00,0x3e00400,0x4208880,0x4730400,0x4b2f580,0x5057100,0x5456280,0x76f80,0x5c19480,0x5c19480,0x5c19480,0x5c19480,0x5c19480,0x5c19480,0x5c19480,0x5c19480,0x5c19480,0x5c19480,0x5c19480,0x5c19480,7,815,1807,2615,3607,4415,5407,6215,7207,8199,9007,9999,10807,11799,12607,13598,14392,15383,16177,17164,17675,17675,17675,17675,21,20,0,0,0,0,0,0,0x1b4980,0x5bce00,0xae5400,0xeed880,0x1415e80,0x181e300,0x1d46900,0x214ed80,0x2677380,0x2b9f980,0x2fa7e00,0x34d0400,0x38d8880,0x3e00e80,0x4209300,0x4730e80,0x4b30000,0x5057b80,0x5456d00,0x597be80,0x5c19f00,0x5c19f00,0x5c19f00,0x5c19f00,0x5c19f00,0x5c19f00,0x5c19f00,0x5c19f00,0x5c19f00,0x5c19f00,0x5c19f00,0x5c19f00,8,816,1808,2616,3608,4416,5408,6400,7208,8200,9008,10000,10808,11800,12608,18433,14393,18448,16178,18467,18467,18467,18467,18467,20,19,0,0,0,0,0,0,0x1b5400,0x5bd880,0xae5e80,0xeee300,0x1416900,0x181ed80,0x1d47380,0x226f980,0x2677e00,0x2ba0400,0x2fa8880,0x34d0e80,0x38d9300,0x3e01900,0x4209d80,0x61500,0x4b30a80,0x6b280,0x5457780,0x77a00,0x77a00,0x77a00,0x77a00,0x77a00,0x77a00,0x77a00,0x77a00,0x77a00,0x77a00,0x77a00,0x77a00,0x77a00,9,817,1809,2617,3609,4417,5409,6401,7209,8201,9009,10001,10809,11801,12609,13599,14394,15384,16179,18468,18468,18468,18468,18468,20,19,0,0,0,0,0,0,0x1b5e80,0x5be300,0xae6900,0xeeed80,0x1417380,0x181f800,0x1d47e00,0x2270400,0x2678880,0x2ba0e80,0x2fa9300,0x34d1900,0x38d9d80,0x3e02380,0x420a800,0x4731900,0x4b31500,0x5058600,0x5458200,0x78480,0x78480,0x78480,0x78480,0x78480,0x78480,0x78480,0x78480,0x78480,0x78480,0x78480,0x78480,0x78480,10,818,1810,2618,3610,4418,5410,6402,7210,8202,9010,10002,10810,11802,12610,13600,14395,15385,16180,17165,17676,18217,18217,18217,22,21,0,0,0,0,0,0,0x1b6900,0x5bed80,0xae7380,0xeef800,0x1417e00,0x1820280,0x1d48880,0x2270e80,0x2679300,0x2ba1900,0x2fa9d80,0x34d2380,0x38da800,0x3e02e00,0x420b280,0x4732380,0x4b31f80,0x5059080,0x5458c80,0x597c900,0x5c1a980,0x5ecc500,0x5ecc500,0x5ecc500,0x5ecc500,0x5ecc500,0x5ecc500,0x5ecc500,0x5ecc500,0x5ecc500,0x5ecc500,0x5ecc500,11,819,1811,2619,3611,4419,5411,6403,7211,8203,9011,10003,10811,11803,12611,13601,14396,15386,16181,17166,17677,17677,17677,17677,21,20,0,0,0,0,0,0,0x1b7380,0x5bf800,0xae7e00,0xef0280,0x1418880,0x1820d00,0x1d49300,0x2271900,0x2679d80,0x2ba2380,0x2faa800,0x34d2e00,0x38db280,0x3e03880,0x420bd00,0x4732e00,0x4b32a00,0x5059b00,0x5459700,0x597d380,0x5c1b400,0x5c1b400,0x5c1b400,0x5c1b400,0x5c1b400,0x5c1b400,0x5c1b400,0x5c1b400,0x5c1b400,0x5c1b400,0x5c1b400,0x5c1b400,12,820,1812,2620,3612,4420,5412,6404,7212,8204,9012,10004,10812,11804,12612,13602,14397,15387,16182,18469,17678,17678,17678,17678,21,20,0,0,0,0,0,0,0x1b7e00,0x5c0280,0xae8880,0xef0d00,0x1419300,0x1821780,0x1d49d80,0x2272380,0x267a800,0x2ba2e00,0x2fab280,0x34d3880,0x38dbd00,0x3e04300,0x420c780,0x4733880,0x4b33480,0x505a580,0x545a180,0x78f00,0x5c1be80,0x5c1be80,0x5c1be80,0x5c1be80,0x5c1be80,0x5c1be80,0x5c1be80,0x5c1be80,0x5c1be80,0x5c1be80,0x5c1be80,0x5c1be80,13,821,1813,2621,3613,4421,5413,6405,7213,8205,9013,10005,10813,11805,12613,13603,14398,15388,16183,18470,18470,18470,18470,18470,20,19,0,0,0,0,0,0,0x1b8880,0x5c0d00,0xae9300,0xef1780,0x1419d80,0x1822200,0x1d4a800,0x2272e00,0x267b280,0x2ba3880,0x2fabd00,0x34d4300,0x38dc780,0x3e04d80,0x420d200,0x4734300,0x4b33f00,0x505b000,0x545ac00,0x79980,0x79980,0x79980,0x79980,0x79980,0x79980,0x79980,0x79980,0x79980,0x79980,0x79980,0x79980,0x79980,14,822,1814,2622,3614,4422,5414,6406,7214,8206,9014,10006,10814,11806,12614,13604,14399,15389,16184,18471,17679,17679,17679,17679,21,20,0,0,0,0,0,0,0x1b9300,0x5c1780,0xae9d80,0xef2200,0x141a800,0x1822c80,0x1d4b280,0x2273880,0x267bd00,0x2ba4300,0x2fac780,0x34d4d80,0x38dd200,0x3e05800,0x420dc80,0x4734d80,0x4b34980,0x505ba80,0x545b680,0x7a400,0x5c1c900,0x5c1c900,0x5c1c900,0x5c1c900,0x5c1c900,0x5c1c900,0x5c1c900,0x5c1c900,0x5c1c900,0x5c1c900,0x5c1c900,0x5c1c900,15,823,1815,2623,3615,4423,5415,6407,7215,8207,9015,10007,10815,11807,12615,13605,14400,15390,16185,17167,17680,17680,17680,17680,21,20,0,0,0,0,0,0,0x1b9d80,0x5c2200,0xaea800,0xef2c80,0x141b280,0x1823700,0x1d4bd00,0x2274300,0x267c780,0x2ba4d80,0x2fad200,0x34d5800,0x38ddc80,0x3e06280,0x420e700,0x4735800,0x4b35400,0x505c500,0x545c100,0x597de00,0x5c1d380,0x5c1d380,0x5c1d380,0x5c1d380,0x5c1d380,0x5c1d380,0x5c1d380,0x5c1d380,0x5c1d380,0x5c1d380,0x5c1d380,0x5c1d380,16,824,1816,2624,3616,4608,5416,6408,7216,8208,9016,10008,10816,11808,12800,18434,14401,18449,16186,18472,18472,18472,18472,18472,20,19,0,0,0,0,0,0,0x1ba800,0x5c2c80,0xaeb280,0xef3700,0x141bd00,0x1944300,0x1d4c780,0x2274d80,0x267d200,0x2ba5800,0x2fadc80,0x34d6280,0x38de700,0x3e06d00,0x432f300,0x61f80,0x4b35e80,0x6bd00,0x545cb80,0x7ae80,0x7ae80,0x7ae80,0x7ae80,0x7ae80,0x7ae80,0x7ae80,0x7ae80,0x7ae80,0x7ae80,0x7ae80,0x7ae80,0x7ae80,17,825,1817,2625,3617,4609,5417,6409,7217,8209,9017,10009,10817,11809,12801,13606,14402,15391,16187,18473,18473,18473,18473,18473,20,19,0,0,0,0,0,0,0x1bb280,0x5c3700,0xaebd00,0xef4180,0x141c780,0x1944d80,0x1d4d200,0x2275800,0x267dc80,0x2ba6280,0x2fae700,0x34d6d00,0x38df180,0x3e07780,0x432fd80,0x4736280,0x4b36900,0x505cf80,0x545d600,0x7b900,0x7b900,0x7b900,0x7b900,0x7b900,0x7b900,0x7b900,0x7b900,0x7b900,0x7b900,0x7b900,0x7b900,0x7b900,18,826,1818,2626,3618,4610,5418,6410,7218,8210,9018,10010,10818,11810,12802,13607,14403,15392,16188,17168,17681,18218,18218,18218,22,21,0,0,0,0,0,0,0x1bbd00,0x5c4180,0xaec780,0xef4c00,0x141d200,0x1945800,0x1d4dc80,0x2276280,0x267e700,0x2ba6d00,0x2faf180,0x34d7780,0x38dfc00,0x3e08200,0x4330800,0x4736d00,0x4b37380,0x505da00,0x545e080,0x597e880,0x5c1de00,0x5eccf80,0x5eccf80,0x5eccf80,0x5eccf80,0x5eccf80,0x5eccf80,0x5eccf80,0x5eccf80,0x5eccf80,0x5eccf80,0x5eccf80,19,827,1819,2627,3619,4611,5419,6411,7219,8211,9019,10011,10819,11811,12803,13608,14404,15393,16189,17169,17682,17682,17682,17682,21,20,0,0,0,0,0,0,0x1bc780,0x5c4c00,0xaed200,0xef5680,0x141dc80,0x1946280,0x1d4e700,0x2276d00,0x267f180,0x2ba7780,0x2fafc00,0x34d8200,0x38e0680,0x3e08c80,0x4331280,0x4737780,0x4b37e00,0x505e480,0x545eb00,0x597f300,0x5c1e880,0x5c1e880,0x5c1e880,0x5c1e880,0x5c1e880,0x5c1e880,0x5c1e880,0x5c1e880,0x5c1e880,0x5c1e880,0x5c1e880,0x5c1e880,20,828,1820,2628,3620,4612,5420,6412,7220,8212,9020,10012,10820,11812,12804,13609,14405,15394,16190,18474,17683,17683,17683,17683,21,20,0,0,0,0,0,0,0x1bd200,0x5c5680,0xaedc80,0xef6100,0x141e700,0x1946d00,0x1d4f180,0x2277780,0x267fc00,0x2ba8200,0x2fb0680,0x34d8c80,0x38e1100,0x3e09700,0x4331d00,0x4738200,0x4b38880,0x505ef00,0x545f580,0x7c380,0x5c1f300,0x5c1f300,0x5c1f300,0x5c1f300,0x5c1f300,0x5c1f300,0x5c1f300,0x5c1f300,0x5c1f300,0x5c1f300,0x5c1f300,0x5c1f300,21,829,1821,2629,3621,4613,5421,6413,7221,8213,9021,10013,10821,11813,12805,13610,14406,15395,16191,18475,18475,18475,18475,18475,20,19,0,0,0,0,0,0,0x1bdc80,0x5c6100,0xaee700,0xef6b80,0x141f180,0x1947780,0x1d4fc00,0x2278200,0x2680680,0x2ba8c80,0x2fb1100,0x34d9700,0x38e1b80,0x3e0a180,0x4332780,0x4738c80,0x4b39300,0x505f980,0x5460000,0x7ce00,0x7ce00,0x7ce00,0x7ce00,0x7ce00,0x7ce00,0x7ce00,0x7ce00,0x7ce00,0x7ce00,0x7ce00,0x7ce00,0x7ce00,22,830,1822,2630,3622,4614,5422,6414,7222,8214,9022,10014,10822,11814,12806,13611,14407,15396,16192,18476,17684,17684,17684,17684,21,20,0,0,0,0,0,0,0x1be700,0x5c6b80,0xaef180,0xef7600,0x141fc00,0x1948200,0x1d50680,0x2278c80,0x2681100,0x2ba9700,0x2fb1b80,0x34da180,0x38e2600,0x3e0ac00,0x4333200,0x4739700,0x4b39d80,0x5060400,0x5460a80,0x7d880,0x5c1fd80,0x5c1fd80,0x5c1fd80,0x5c1fd80,0x5c1fd80,0x5c1fd80,0x5c1fd80,0x5c1fd80,0x5c1fd80,0x5c1fd80,0x5c1fd80,0x5c1fd80,23,831,1823,2631,3623,4615,5423,6415,7223,8215,9023,10015,10823,11815,12807,13612,14592,15397,16193,17170,17685,17685,17685,17685,21,20,0,0,0,0,0,0,0x1bf180,0x5c7600,0xaefc00,0xef8080,0x1420680,0x1948c80,0x1d51100,0x2279700,0x2681b80,0x2baa180,0x2fb2600,0x34dac00,0x38e3080,0x3e0b680,0x4333c80,0x473a180,0x4c5a980,0x5060e80,0x5461500,0x597fd80,0x5c20800,0x5c20800,0x5c20800,0x5c20800,0x5c20800,0x5c20800,0x5c20800,0x5c20800,0x5c20800,0x5c20800,0x5c20800,0x5c20800,24,832,1824,2816,3624,4616,5424,6416,7224,8216,9024,10016,11008,11816,12808,18435,14593,18450,16194,18477,18477,18477,18477,18477,20,19,0,0,0,0,0,0,0x1bfc00,0x5c8080,0xaf0680,0x1018c80,0x1421100,0x1949700,0x1d51b80,0x227a180,0x2682600,0x2baac00,0x2fb3080,0x34db680,0x3a03c80,0x3e0c100,0x4334700,0x62a00,0x4c5b400,0x6c780,0x5461f80,0x7e300,0x7e300,0x7e300,0x7e300,0x7e300,0x7e300,0x7e300,0x7e300,0x7e300,0x7e300,0x7e300,0x7e300,0x7e300,25,833,1825,2817,3625,4617,5425,6417,7225,8217,9025,10017,11009,11817,12809,13613,14594,15398,16195,18478,18478,18478,18478,18478,20,19,0,0,0,0,0,0,0x1c0680,0x5c8b00,0xaf1100,0x1019700,0x1421b80,0x194a180,0x1d52600,0x227ac00,0x2683080,0x2bab680,0x2fb3b00,0x34dc100,0x3a04700,0x3e0cb80,0x4335180,0x473ac00,0x4c5be80,0x5061900,0x5462a00,0x7ed80,0x7ed80,0x7ed80,0x7ed80,0x7ed80,0x7ed80,0x7ed80,0x7ed80,0x7ed80,0x7ed80,0x7ed80,0x7ed80,0x7ed80,26,834,1826,2818,3626,4618,5426,6418,7226,8218,9026,10018,11010,11818,12810,13614,14595,15399,16196,17171,17686,18219,18219,18219,22,21,0,0,0,0,0,0,0x1c1100,0x5c9580,0xaf1b80,0x101a180,0x1422600,0x194ac00,0x1d53080,0x227b680,0x2683b00,0x2bac100,0x2fb4580,0x34dcb80,0x3a05180,0x3e0d600,0x4335c00,0x473b680,0x4c5c900,0x5062380,0x5463480,0x5980800,0x5c21280,0x5ecda00,0x5ecda00,0x5ecda00,0x5ecda00,0x5ecda00,0x5ecda00,0x5ecda00,0x5ecda00,0x5ecda00,0x5ecda00,0x5ecda00,27,835,1827,2819,3627,4619,5427,6419,7227,8219,9027,10019,11011,11819,12811,13615,14596,15400,16197,17172,17687,17687,17687,17687,21,20,0,0,0,0,0,0,0x1c1b80,0x5ca000,0xaf2600,0x101ac00,0x1423080,0x194b680,0x1d53b00,0x227c100,0x2684580,0x2bacb80,0x2fb5000,0x34dd600,0x3a05c00,0x3e0e080,0x4336680,0x473c100,0x4c5d380,0x5062e00,0x5463f00,0x5981280,0x5c21d00,0x5c21d00,0x5c21d00,0x5c21d00,0x5c21d00,0x5c21d00,0x5c21d00,0x5c21d00,0x5c21d00,0x5c21d00,0x5c21d00,0x5c21d00,28,836,1828,2820,3628,4620,5428,6420,7228,8220,9028,10020,11012,11820,12812,13616,14597,15401,16198,18479,17688,17688,17688,17688,21,20,0,0,0,0,0,0,0x1c2600,0x5caa80,0xaf3080,0x101b680,0x1423b00,0x194c100,0x1d54580,0x227cb80,0x2685000,0x2bad600,0x2fb5a80,0x34de080,0x3a06680,0x3e0eb00,0x4337100,0x473cb80,0x4c5de00,0x5063880,0x5464980,0x7f800,0x5c22780,0x5c22780,0x5c22780,0x5c22780,0x5c22780,0x5c22780,0x5c22780,0x5c22780,0x5c22780,0x5c22780,0x5c22780,0x5c22780,29,837,1829,2821,3629,4621,5429,6421,7229,8221,9029,10021,11013,11821,12813,13617,14598,15402,16199,18480,18480,18480,18480,18480,20,19,0,0,0,0,0,0,0x1c3080,0x5cb500,0xaf3b00,0x101c100,0x1424580,0x194cb80,0x1d55000,0x227d600,0x2685a80,0x2bae080,0x2fb6500,0x34deb00,0x3a07100,0x3e0f580,0x4337b80,0x473d600,0x4c5e880,0x5064300,0x5465400,0x80280,0x80280,0x80280,0x80280,0x80280,0x80280,0x80280,0x80280,0x80280,0x80280,0x80280,0x80280,0x80280,30,838,1830,2822,3630,4622,5430,6422,7230,8222,9030,10022,11014,11822,12814,13618,14599,15403,16384,18481,17689,17689,17689,17689,21,20,0,0,0,0,0,0,0x1c3b00,0x5cbf80,0xaf4580,0x101cb80,0x1425000,0x194d600,0x1d55a80,0x227e080,0x2686500,0x2baeb00,0x2fb6f80,0x34df580,0x3a07b80,0x3e10000,0x4338600,0x473e080,0x4c5f300,0x5064d80,0x5586000,0x80d00,0x5c23200,0x5c23200,0x5c23200,0x5c23200,0x5c23200,0x5c23200,0x5c23200,0x5c23200,0x5c23200,0x5c23200,0x5c23200,0x5c23200,31,839,1831,2823,3631,4623,5431,6423,7231,8223,9031,10023,11015,11823,12815,13619,14600,15404,16385,17173,17690,17690,17690,17690,21,20,0,0,0,0,0,0,0x1c4580,0x5cca00,0xaf5000,0x101d600,0x1425a80,0x194e080,0x1d56500,0x227eb00,0x2686f80,0x2baf580,0x2fb7a00,0x34e0000,0x3a08600,0x3e10a80,0x4339080,0x473eb00,0x4c5fd80,0x5065800,0x5586a80,0x5981d00,0x5c23c80,0x5c23c80,0x5c23c80,0x5c23c80,0x5c23c80,0x5c23c80,0x5c23c80,0x5c23c80,0x5c23c80,0x5c23c80,0x5c23c80,0x5c23c80,32,1024,1832,2824,3632,4624,5432,6424,7232,8224,9216,10024,11016,11824,12816,18436,14601,18451,16386,18482,18482,18482,18482,18482,20,19,0,0,0,0,0,0,0x1c5000,0x6ed600,0xaf5a80,0x101e080,0x1426500,0x194eb00,0x1d56f80,0x227f580,0x2687a00,0x2bb0000,0x30d8600,0x34e0a80,0x3a09080,0x3e11500,0x4339b00,0x63480,0x4c60800,0x6d200,0x5587500,0x81780,0x81780,0x81780,0x81780,0x81780,0x81780,0x81780,0x81780,0x81780,0x81780,0x81780,0x81780,0x81780,33,1025,1833,2825,3633,4625,5433,6425,7233,8225,9217,10025,11017,11825,12817,13620,14602,15405,16387,18483,18483,18483,18483,18483,20,19,0,0,0,0,0,0,0x1c5a80,0x6ee080,0xaf6500,0x101eb00,0x1426f80,0x194f580,0x1d57a00,0x2280000,0x2688480,0x2bb0a80,0x30d9080,0x34e1500,0x3a09b00,0x3e11f80,0x433a580,0x473f580,0x4c61280,0x5066280,0x5587f80,0x82200,0x82200,0x82200,0x82200,0x82200,0x82200,0x82200,0x82200,0x82200,0x82200,0x82200,0x82200,0x82200,34,1026,1834,2826,3634,4626,5434,6426,7234,8226,9218,10026,11018,11826,12818,13621,14603,15406,16388,17174,17691,18220,18220,18220,22,21,0,0,0,0,0,0,0x1c6500,0x6eeb00,0xaf6f80,0x101f580,0x1427a00,0x1950000,0x1d58480,0x2280a80,0x2688f00,0x2bb1500,0x30d9b00,0x34e1f80,0x3a0a580,0x3e12a00,0x433b000,0x4740000,0x4c61d00,0x5066d00,0x5588a00,0x5982780,0x5c24700,0x5ece480,0x5ece480,0x5ece480,0x5ece480,0x5ece480,0x5ece480,0x5ece480,0x5ece480,0x5ece480,0x5ece480,0x5ece480,35,1027,1835,2827,3635,4627,5435,6427,7235,8227,9219,10027,11019,11827,12819,13622,14604,15407,16389,17175,17692,17692,17692,17692,21,20,0,0,0,0,0,0,0x1c6f80,0x6ef580,0xaf7a00,0x1020000,0x1428480,0x1950a80,0x1d58f00,0x2281500,0x2689980,0x2bb1f80,0x30da580,0x34e2a00,0x3a0b000,0x3e13480,0x433ba80,0x4740a80,0x4c62780,0x5067780,0x5589480,0x5983200,0x5c25180,0x5c25180,0x5c25180,0x5c25180,0x5c25180,0x5c25180,0x5c25180,0x5c25180,0x5c25180,0x5c25180,0x5c25180,0x5c25180,36,1028,1836,2828,3636,4628,5436,6428,7236,8228,9220,10028,11020,11828,12820,13623,14605,15408,16390,17176,17693,17693,17693,17693,21,20,0,0,0,0,0,0,0x1c7a00,0x6f0000,0xaf8480,0x1020a80,0x1428f00,0x1951500,0x1d59980,0x2281f80,0x268a400,0x2bb2a00,0x30db000,0x34e3480,0x3a0ba80,0x3e13f00,0x433c500,0x4741500,0x4c63200,0x5068200,0x5589f00,0x5983c80,0x5c25c00,0x5c25c00,0x5c25c00,0x5c25c00,0x5c25c00,0x5c25c00,0x5c25c00,0x5c25c00,0x5c25c00,0x5c25c00,0x5c25c00,0x5c25c00,37,1029,1837,2829,3637,4629,5437,6429,7237,8229,9221,10029,11021,11829,12821,13624,14606,15409,16391,18484,18484,18484,18484,18484,20,19,0,0,0,0,0,0,0x1c8480,0x6f0a80,0xaf8f00,0x1021500,0x1429980,0x1951f80,0x1d5a400,0x2282a00,0x268ae80,0x2bb3480,0x30dba80,0x34e3f00,0x3a0c500,0x3e14980,0x433cf80,0x4741f80,0x4c63c80,0x5068c80,0x558a980,0x82c80,0x82c80,0x82c80,0x82c80,0x82c80,0x82c80,0x82c80,0x82c80,0x82c80,0x82c80,0x82c80,0x82c80,0x82c80,38,1030,1838,2830,3638,4630,5438,6430,7238,8230,9222,10030,11022,11830,12822,13625,14607,15410,16392,18485,17694,17694,17694,17694,21,20,0,0,0,0,0,0,0x1c8f00,0x6f1500,0xaf9980,0x1021f80,0x142a400,0x1952a00,0x1d5ae80,0x2283480,0x268b900,0x2bb3f00,0x30dc500,0x34e4980,0x3a0cf80,0x3e15400,0x433da00,0x4742a00,0x4c64700,0x5069700,0x558b400,0x83700,0x5c26680,0x5c26680,0x5c26680,0x5c26680,0x5c26680,0x5c26680,0x5c26680,0x5c26680,0x5c26680,0x5c26680,0x5c26680,0x5c26680,39,1031,1839,2831,3639,4631,5439,6431,7239,8231,9223,10031,11023,11831,12823,13626,14608,15411,16393,17177,17695,17695,17695,17695,21,20,0,0,0,0,0,0,0x1c9980,0x6f1f80,0xafa400,0x1022a00,0x142ae80,0x1953480,0x1d5b900,0x2283f00,0x268c380,0x2bb4980,0x30dcf80,0x34e5400,0x3a0da00,0x3e15e80,0x433e480,0x4743480,0x4c65180,0x506a180,0x558be80,0x5984700,0x5c27100,0x5c27100,0x5c27100,0x5c27100,0x5c27100,0x5c27100,0x5c27100,0x5c27100,0x5c27100,0x5c27100,0x5c27100,0x5c27100,40,1032,1840,2832,3640,4632,5440,6432,7424,8232,9224,10032,11024,11832,12824,18437,14609,18452,16394,18486,18486,18486,18486,18486,20,19,0,0,0,0,0,0,0x1ca400,0x6f2a00,0xafae80,0x1023480,0x142b900,0x1953f00,0x1d5c380,0x2284980,0x27acf80,0x2bb5400,0x30dda00,0x34e5e80,0x3a0e480,0x3e16900,0x433ef00,0x63f00,0x4c65c00,0x6dc80,0x558c900,0x84180,0x84180,0x84180,0x84180,0x84180,0x84180,0x84180,0x84180,0x84180,0x84180,0x84180,0x84180,0x84180,41,1033,1841,2833,3641,4633,5441,6433,7425,8233,9225,10033,11025,11833,12825,13627,14610,15412,16395,18487,18487,18487,18487,18487,20,19,0,0,0,0,0,0,0x1cae80,0x6f3480,0xafb900,0x1023f00,0x142c380,0x1954980,0x1d5ce00,0x2285400,0x27ada00,0x2bb5e80,0x30de480,0x34e6900,0x3a0ef00,0x3e17380,0x433f980,0x4743f00,0x4c66680,0x506ac00,0x558d380,0x84c00,0x84c00,0x84c00,0x84c00,0x84c00,0x84c00,0x84c00,0x84c00,0x84c00,0x84c00,0x84c00,0x84c00,0x84c00,42,1034,1842,2834,3642,4634,5442,6434,7426,8234,9226,10034,11026,11834,12826,13628,14611,15413,16396,17178,17696,18221,18221,18221,22,21,0,0,0,0,0,0,0x1cb900,0x6f3f00,0xafc380,0x1024980,0x142ce00,0x1955400,0x1d5d880,0x2285e80,0x27ae480,0x2bb6900,0x30def00,0x34e7380,0x3a0f980,0x3e17e00,0x4340400,0x4744980,0x4c67100,0x506b680,0x558de00,0x5985180,0x5c27b80,0x5ecef00,0x5ecef00,0x5ecef00,0x5ecef00,0x5ecef00,0x5ecef00,0x5ecef00,0x5ecef00,0x5ecef00,0x5ecef00,0x5ecef00,43,1035,1843,2835,3643,4635,5443,6435,7427,8235,9227,10035,11027,11835,12827,13629,14612,15414,16397,17179,17697,17697,17697,17697,21,20,0,0,0,0,0,0,0x1cc380,0x6f4980,0xafce00,0x1025400,0x142d880,0x1955e80,0x1d5e300,0x2286900,0x27aef00,0x2bb7380,0x30df980,0x34e7e00,0x3a10400,0x3e18880,0x4340e80,0x4745400,0x4c67b80,0x506c100,0x558e880,0x5985c00,0x5c28600,0x5c28600,0x5c28600,0x5c28600,0x5c28600,0x5c28600,0x5c28600,0x5c28600,0x5c28600,0x5c28600,0x5c28600,0x5c28600,44,1036,1844,2836,3644,4636,5444,6436,7428,8236,9228,10036,11028,11836,12828,13630,14613,15415,16398,17180,17698,17698,17698,17698,21,20,0,0,0,0,0,0,0x1cce00,0x6f5400,0xafd880,0x1025e80,0x142e300,0x1956900,0x1d5ed80,0x2287380,0x27af980,0x2bb7e00,0x30e0400,0x34e8880,0x3a10e80,0x3e19300,0x4341900,0x4745e80,0x4c68600,0x506cb80,0x558f300,0x5986680,0x5c29080,0x5c29080,0x5c29080,0x5c29080,0x5c29080,0x5c29080,0x5c29080,0x5c29080,0x5c29080,0x5c29080,0x5c29080,0x5c29080,45,1037,1845,2837,3645,4637,5445,6437,7429,8237,9229,10037,11029,11837,12829,13631,14614,15416,16399,18488,18488,18488,18488,18488,20,19,0,0,0,0,0,0,0x1cd880,0x6f5e80,0xafe300,0x1026900,0x142ed80,0x1957380,0x1d5f800,0x2287e00,0x27b0400,0x2bb8880,0x30e0e80,0x34e9300,0x3a11900,0x3e19d80,0x4342380,0x4746900,0x4c69080,0x506d600,0x558fd80,0x85680,0x85680,0x85680,0x85680,0x85680,0x85680,0x85680,0x85680,0x85680,0x85680,0x85680,0x85680,0x85680,46,1038,1846,2838,3646,4638,5446,6438,7430,8238,9230,10038,11030,11838,12830,13632,14615,15417,16400,18489,17699,17699,17699,17699,21,20,0,0,0,0,0,0,0x1ce300,0x6f6900,0xafed80,0x1027380,0x142f800,0x1957e00,0x1d60280,0x2288880,0x27b0e80,0x2bb9300,0x30e1900,0x34e9d80,0x3a12380,0x3e1a800,0x4342e00,0x4747380,0x4c69b00,0x506e080,0x5590800,0x86100,0x5c29b00,0x5c29b00,0x5c29b00,0x5c29b00,0x5c29b00,0x5c29b00,0x5c29b00,0x5c29b00,0x5c29b00,0x5c29b00,0x5c29b00,0x5c29b00,47,1039,1847,2839,3647,4639,5447,6439,7431,8239,9231,10039,11031,11839,12831,13633,14616,15418,16401,17181,17700,17700,17700,17700,21,20,0,0,0,0,0,0,0x1ced80,0x6f7380,0xaff800,0x1027e00,0x1430280,0x1958880,0x1d60d00,0x2289300,0x27b1900,0x2bb9d80,0x30e2380,0x34ea800,0x3a12e00,0x3e1b280,0x4343880,0x4747e00,0x4c6a580,0x506eb00,0x5591280,0x5987100,0x5c2a580,0x5c2a580,0x5c2a580,0x5c2a580,0x5c2a580,0x5c2a580,0x5c2a580,0x5c2a580,0x5c2a580,0x5c2a580,0x5c2a580,0x5c2a580,48,1040,1848,2840,3648,4640,5632,6440,7432,8240,9232,10040,11032,11840,12832,18438,14617,18453,16402,18490,18490,18490,18490,18490,20,19,0,0,0,0,0,0,0x1cf800,0x6f7e00,0xb00280,0x1028880,0x1430d00,0x1959300,0x1e81900,0x2289d80,0x27b2380,0x2bba800,0x30e2e00,0x34eb280,0x3a13880,0x3e1bd00,0x4344300,0x64980,0x4c6b000,0x6e700,0x5591d00,0x86b80,0x86b80,0x86b80,0x86b80,0x86b80,0x86b80,0x86b80,0x86b80,0x86b80,0x86b80,0x86b80,0x86b80,0x86b80,49,1041,1849,2841,3649,4641,5633,6441,7433,8241,9233,10041,11033,11841,12833,13634,14618,15419,16403,18491,18491,18491,18491,18491,20,19,0,0,0,0,0,0,0x1d0280,0x6f8880,0xb00d00,0x1029300,0x1431780,0x1959d80,0x1e82380,0x228a800,0x27b2e00,0x2bbb280,0x30e3880,0x34ebd00,0x3a14300,0x3e1c780,0x4344d80,0x4748880,0x4c6ba80,0x506f580,0x5592780,0x87600,0x87600,0x87600,0x87600,0x87600,0x87600,0x87600,0x87600,0x87600,0x87600,0x87600,0x87600,0x87600,50,1042,1850,2842,3650,4642,5634,6442,7434,8242,9234,10042,11034,11842,12834,13635,14619,15420,16404,17182,17701,18222,18222,18222,22,21,0,0,0,0,0,0,0x1d0d00,0x6f9300,0xb01780,0x1029d80,0x1432200,0x195a800,0x1e82e00,0x228b280,0x27b3880,0x2bbbd00,0x30e4300,0x34ec780,0x3a14d80,0x3e1d200,0x4345800,0x4749300,0x4c6c500,0x5070000,0x5593200,0x5987b80,0x5c2b000,0x5ecf980,0x5ecf980,0x5ecf980,0x5ecf980,0x5ecf980,0x5ecf980,0x5ecf980,0x5ecf980,0x5ecf980,0x5ecf980,0x5ecf980,51,1043,1851,2843,3651,4643,5635,6443,7435,8243,9235,10043,11035,11843,12835,13636,14620,15421,16405,17183,17702,17702,17702,17702,21,20,0,0,0,0,0,0,0x1d1780,0x6f9d80,0xb02200,0x102a800,0x1432c80,0x195b280,0x1e83880,0x228bd00,0x27b4300,0x2bbc780,0x30e4d80,0x34ed200,0x3a15800,0x3e1dc80,0x4346280,0x4749d80,0x4c6cf80,0x5070a80,0x5593c80,0x5988600,0x5c2ba80,0x5c2ba80,0x5c2ba80,0x5c2ba80,0x5c2ba80,0x5c2ba80,0x5c2ba80,0x5c2ba80,0x5c2ba80,0x5c2ba80,0x5c2ba80,0x5c2ba80,52,1044,1852,2844,3652,4644,5636,6444,7436,8244,9236,10044,11036,11844,12836,13637,14621,15422,16406,17184,17703,17703,17703,17703,21,20,0,0,0,0,0,0,0x1d2200,0x6fa800,0xb02c80,0x102b280,0x1433700,0x195bd00,0x1e84300,0x228c780,0x27b4d80,0x2bbd200,0x30e5800,0x34edc80,0x3a16280,0x3e1e700,0x4346d00,0x474a800,0x4c6da00,0x5071500,0x5594700,0x5989080,0x5c2c500,0x5c2c500,0x5c2c500,0x5c2c500,0x5c2c500,0x5c2c500,0x5c2c500,0x5c2c500,0x5c2c500,0x5c2c500,0x5c2c500,0x5c2c500,53,1045,1853,2845,3653,4645,5637,6445,7437,8245,9237,10045,11037,11845,12837,13638,14622,15423,16407,18492,18492,18492,18492,18492,20,19,0,0,0,0,0,0,0x1d2c80,0x6fb280,0xb03700,0x102bd00,0x1434180,0x195c780,0x1e84d80,0x228d200,0x27b5800,0x2bbdc80,0x30e6280,0x34ee700,0x3a16d00,0x3e1f180,0x4347780,0x474b280,0x4c6e480,0x5071f80,0x5595180,0x88080,0x88080,0x88080,0x88080,0x88080,0x88080,0x88080,0x88080,0x88080,0x88080,0x88080,0x88080,0x88080,54,1046,1854,2846,3654,4646,5638,6446,7438,8246,9238,10046,11038,11846,12838,13639,14623,15424,16408,18493,17704,17704,17704,17704,21,20,0,0,0,0,0,0,0x1d3700,0x6fbd00,0xb04180,0x102c780,0x1434c00,0x195d200,0x1e85800,0x228dc80,0x27b6280,0x2bbe700,0x30e6d00,0x34ef180,0x3a17780,0x3e1fc00,0x4348200,0x474bd00,0x4c6ef00,0x5072a00,0x5595c00,0x88b00,0x5c2cf80,0x5c2cf80,0x5c2cf80,0x5c2cf80,0x5c2cf80,0x5c2cf80,0x5c2cf80,0x5c2cf80,0x5c2cf80,0x5c2cf80,0x5c2cf80,0x5c2cf80,55,1047,1855,2847,3655,4647,5639,6447,7439,8247,9239,10047,11039,11847,12839,13824,14624,15425,16409,17185,17705,17705,17705,17705,21,20,0,0,0,0,0,0,0x1d4180,0x6fc780,0xb04c00,0x102d200,0x1435680,0x195dc80,0x1e86280,0x228e700,0x27b6d00,0x2bbf180,0x30e7780,0x34efc00,0x3a18200,0x3e20680,0x4348c80,0x486c900,0x4c6f980,0x5073480,0x5596680,0x5989b00,0x5c2da00,0x5c2da00,0x5c2da00,0x5c2da00,0x5c2da00,0x5c2da00,0x5c2da00,0x5c2da00,0x5c2da00,0x5c2da00,0x5c2da00,0x5c2da00,56,1048,1856,2848,3840,4648,5640,6448,7440,8248,9240,10048,11040,12032,12840,18439,14625,18454,16410,18494,18494,18494,18494,18494,20,19,0,0,0,0,0,0,0x1d4c00,0x6fd200,0xb05680,0x102dc80,0x1556280,0x195e700,0x1e86d00,0x228f180,0x27b7780,0x2bbfc00,0x30e8200,0x34f0680,0x3a18c80,0x3f41280,0x4349700,0x65400,0x4c70400,0x6f180,0x5597100,0x89580,0x89580,0x89580,0x89580,0x89580,0x89580,0x89580,0x89580,0x89580,0x89580,0x89580,0x89580,0x89580,57,1049,1857,2849,3841,4649,5641,6449,7441,8249,9241,10049,11041,12033,12841,13825,14626,15426,16411,18495,18495,18495,18495,18495,20,19,0,0,0,0,0,0,0x1d5680,0x6fdc80,0xb06100,0x102e700,0x1556d00,0x195f180,0x1e87780,0x228fc00,0x27b8200,0x2bc0680,0x30e8c80,0x34f1100,0x3a19700,0x3f41d00,0x434a180,0x486d380,0x4c70e80,0x5073f00,0x5597b80,0x8a000,0x8a000,0x8a000,0x8a000,0x8a000,0x8a000,0x8a000,0x8a000,0x8a000,0x8a000,0x8a000,0x8a000,0x8a000,58,1050,1858,2850,3842,4650,5642,6450,7442,8250,9242,10050,11042,12034,12842,13826,14627,15427,16412,17186,17706,18223,18223,18223,22,21,0,0,0,0,0,0,0x1d6100,0x6fe700,0xb06b80,0x102f180,0x1557780,0x195fc00,0x1e88200,0x2290680,0x27b8c80,0x2bc1100,0x30e9700,0x34f1b80,0x3a1a180,0x3f42780,0x434ac00,0x486de00,0x4c71900,0x5074980,0x5598600,0x598a580,0x5c2e480,0x5ed0400,0x5ed0400,0x5ed0400,0x5ed0400,0x5ed0400,0x5ed0400,0x5ed0400,0x5ed0400,0x5ed0400,0x5ed0400,0x5ed0400,59,1051,1859,2851,3843,4651,5643,6451,7443,8251,9243,10051,11043,12035,12843,13827,14628,15428,16413,17187,17707,17707,17707,17707,21,20,0,0,0,0,0,0,0x1d6b80,0x6ff180,0xb07600,0x102fc00,0x1558200,0x1960680,0x1e88c80,0x2291100,0x27b9700,0x2bc1b80,0x30ea180,0x34f2600,0x3a1ac00,0x3f43200,0x434b680,0x486e880,0x4c72380,0x5075400,0x5599080,0x598b000,0x5c2ef00,0x5c2ef00,0x5c2ef00,0x5c2ef00,0x5c2ef00,0x5c2ef00,0x5c2ef00,0x5c2ef00,0x5c2ef00,0x5c2ef00,0x5c2ef00,0x5c2ef00,60,1052,1860,2852,3844,4652,5644,6452,7444,8252,9244,10052,11044,12036,12844,13828,14629,15429,16414,17188,17708,17708,17708,17708,21,20,0,0,0,0,0,0,0x1d7600,0x6ffc00,0xb08080,0x1030680,0x1558c80,0x1961100,0x1e89700,0x2291b80,0x27ba180,0x2bc2600,0x30eac00,0x34f3080,0x3a1b680,0x3f43c80,0x434c100,0x486f300,0x4c72e00,0x5075e80,0x5599b00,0x598ba80,0x5c2f980,0x5c2f980,0x5c2f980,0x5c2f980,0x5c2f980,0x5c2f980,0x5c2f980,0x5c2f980,0x5c2f980,0x5c2f980,0x5c2f980,0x5c2f980,61,1053,1861,2853,3845,4653,5645,6453,7445,8253,9245,10053,11045,12037,12845,13829,14630,15430,16415,18496,18496,18496,18496,18496,20,19,0,0,0,0,0,0,0x1d8080,0x700680,0xb08b00,0x1031100,0x1559700,0x1961b80,0x1e8a180,0x2292600,0x27bac00,0x2bc3080,0x30eb680,0x34f3b00,0x3a1c100,0x3f44700,0x434cb80,0x486fd80,0x4c73880,0x5076900,0x559a580,0x8aa80,0x8aa80,0x8aa80,0x8aa80,0x8aa80,0x8aa80,0x8aa80,0x8aa80,0x8aa80,0x8aa80,0x8aa80,0x8aa80,0x8aa80,62,1054,1862,2854,3846,4654,5646,6454,7446,8254,9246,10054,11046,12038,12846,13830,14631,15431,16416,18497,17709,17709,17709,17709,21,20,0,0,0,0,0,0,0x1d8b00,0x701100,0xb09580,0x1031b80,0x155a180,0x1962600,0x1e8ac00,0x2293080,0x27bb680,0x2bc3b00,0x30ec100,0x34f4580,0x3a1cb80,0x3f45180,0x434d600,0x4870800,0x4c74300,0x5077380,0x559b000,0x8b500,0x5c30400,0x5c30400,0x5c30400,0x5c30400,0x5c30400,0x5c30400,0x5c30400,0x5c30400,0x5c30400,0x5c30400,0x5c30400,0x5c30400,63,1055,1863,2855,3847,4655,5647,6455,7447,8255,9247,10055,11047,12039,12847,13831,14632,15616,16417,17189,17710,17710,17710,17710,21,20,0,0,0,0,0,0,0x1d9580,0x701b80,0xb0a000,0x1032600,0x155ac00,0x1963080,0x1e8b680,0x2293b00,0x27bc100,0x2bc4580,0x30ecb80,0x34f5000,0x3a1d600,0x3f45c00,0x434e080,0x4871280,0x4c74d80,0x5197f80,0x559ba80,0x598c500,0x5c30e80,0x5c30e80,0x5c30e80,0x5c30e80,0x5c30e80,0x5c30e80,0x5c30e80,0x5c30e80,0x5c30e80,0x5c30e80,0x5c30e80,0x5c30e80,64,1056,2048,2856,3848,4656,5648,6456,7448,8256,9248,10240,11048,12040,12848,18440,14633,18455,16418,18498,18498,18498,18498,18498,20,19,0,0,0,0,0,0,0x1da000,0x702600,0xc2ac00,0x1033080,0x155b680,0x1963b00,0x1e8c100,0x2294580,0x27bcb80,0x2bc5000,0x30ed600,0x3615c00,0x3a1e080,0x3f46680,0x434eb00,0x65e80,0x4c75800,0x6fc00,0x559c500,0x8bf80,0x8bf80,0x8bf80,0x8bf80,0x8bf80,0x8bf80,0x8bf80,0x8bf80,0x8bf80,0x8bf80,0x8bf80,0x8bf80,0x8bf80,65,1057,2049,2857,3849,4657,5649,6457,7449,8257,9249,10241,11049,12041,12849,13832,14634,15617,16419,18499,18499,18499,18499,18499,20,19,0,0,0,0,0,0,0x1daa80,0x703080,0xc2b680,0x1033b00,0x155c100,0x1964580,0x1e8cb80,0x2295000,0x27bd600,0x2bc5a80,0x30ee080,0x3616680,0x3a1eb00,0x3f47100,0x434f580,0x4871d00,0x4c76280,0x5198a00,0x559cf80,0x8ca00,0x8ca00,0x8ca00,0x8ca00,0x8ca00,0x8ca00,0x8ca00,0x8ca00,0x8ca00,0x8ca00,0x8ca00,0x8ca00,0x8ca00,66,1058,2050,2858,3850,4658,5650,6458,7450,8258,9250,10242,11050,12042,12850,13833,14635,15618,16420,17190,17711,18224,18224,18224,22,21,0,0,0,0,0,0,0x1db500,0x703b00,0xc2c100,0x1034580,0x155cb80,0x1965000,0x1e8d600,0x2295a80,0x27be080,0x2bc6500,0x30eeb00,0x3617100,0x3a1f580,0x3f47b80,0x4350000,0x4872780,0x4c76d00,0x5199480,0x559da00,0x598cf80,0x5c31900,0x5ed0e80,0x5ed0e80,0x5ed0e80,0x5ed0e80,0x5ed0e80,0x5ed0e80,0x5ed0e80,0x5ed0e80,0x5ed0e80,0x5ed0e80,0x5ed0e80,67,1059,2051,2859,3851,4659,5651,6459,7451,8259,9251,10243,11051,12043,12851,13834,14636,15619,16421,17191,17712,17712,17712,17712,21,20,0,0,0,0,0,0,0x1dbf80,0x704580,0xc2cb80,0x1035000,0x155d600,0x1965a80,0x1e8e080,0x2296500,0x27beb00,0x2bc6f80,0x30ef580,0x3617b80,0x3a20000,0x3f48600,0x4350a80,0x4873200,0x4c77780,0x5199f00,0x559e480,0x598da00,0x5c32380,0x5c32380,0x5c32380,0x5c32380,0x5c32380,0x5c32380,0x5c32380,0x5c32380,0x5c32380,0x5c32380,0x5c32380,0x5c32380,68,1060,2052,2860,3852,4660,5652,6460,7452,8260,9252,10244,11052,12044,12852,13835,14637,15620,16422,17192,17713,17713,17713,17713,21,20,0,0,0,0,0,0,0x1dca00,0x705000,0xc2d600,0x1035a80,0x155e080,0x1966500,0x1e8eb00,0x2296f80,0x27bf580,0x2bc7a00,0x30f0000,0x3618600,0x3a20a80,0x3f49080,0x4351500,0x4873c80,0x4c78200,0x519a980,0x559ef00,0x598e480,0x5c32e00,0x5c32e00,0x5c32e00,0x5c32e00,0x5c32e00,0x5c32e00,0x5c32e00,0x5c32e00,0x5c32e00,0x5c32e00,0x5c32e00,0x5c32e00,69,1061,2053,2861,3853,4661,5653,6461,7453,8261,9253,10245,11053,12045,12853,13836,14638,15621,16423,18500,18500,18500,18500,18500,20,19,0,0,0,0,0,0,0x1dd480,0x705a80,0xc2e080,0x1036500,0x155eb00,0x1966f80,0x1e8f580,0x2297a00,0x27c0000,0x2bc8480,0x30f0a80,0x3619080,0x3a21500,0x3f49b00,0x4351f80,0x4874700,0x4c78c80,0x519b400,0x559f980,0x8d480,0x8d480,0x8d480,0x8d480,0x8d480,0x8d480,0x8d480,0x8d480,0x8d480,0x8d480,0x8d480,0x8d480,0x8d480,70,1062,2054,2862,3854,4662,5654,6462,7454,8262,9254,10246,11054,12046,12854,13837,14639,15622,16424,18501,17714,17714,17714,17714,21,20,0,0,0,0,0,0,0x1ddf00,0x706500,0xc2eb00,0x1036f80,0x155f580,0x1967a00,0x1e90000,0x2298480,0x27c0a80,0x2bc8f00,0x30f1500,0x3619b00,0x3a21f80,0x3f4a580,0x4352a00,0x4875180,0x4c79700,0x519be80,0x55a0400,0x8df00,0x5c33880,0x5c33880,0x5c33880,0x5c33880,0x5c33880,0x5c33880,0x5c33880,0x5c33880,0x5c33880,0x5c33880,0x5c33880,0x5c33880,71,1063,2055,2863,3855,4663,5655,6463,7455,8263,9255,10247,11055,12047,12855,13838,14640,15623,16425,17193,17715,17715,17715,17715,21,20,0,0,0,0,0,0,0x1de980,0x706f80,0xc2f580,0x1037a00,0x1560000,0x1968480,0x1e90a80,0x2298f00,0x27c1500,0x2bc9980,0x30f1f80,0x361a580,0x3a22a00,0x3f4b000,0x4353480,0x4875c00,0x4c7a180,0x519c900,0x55a0e80,0x598ef00,0x5c34300,0x5c34300,0x5c34300,0x5c34300,0x5c34300,0x5c34300,0x5c34300,0x5c34300,0x5c34300,0x5c34300,0x5c34300,0x5c34300,256,1064,2056,2864,3856,4664,5656,6464,7456,8448,9256,10248,11056,12048,12856,18441,14641,18456,16426,18502,18502,18502,18502,18502,20,19,0,0,0,0,0,0,0x2ff580,0x707a00,0xc30000,0x1038480,0x1560a80,0x1968f00,0x1e91500,0x2299980,0x27c1f80,0x2cea580,0x30f2a00,0x361b000,0x3a23480,0x3f4ba80,0x4353f00,0x66900,0x4c7ac00,0x70680,0x55a1900,0x8e980,0x8e980,0x8e980,0x8e980,0x8e980,0x8e980,0x8e980,0x8e980,0x8e980,0x8e980,0x8e980,0x8e980,0x8e980,257,1065,2057,2865,3857,4665,5657,6465,7457,8449,9257,10249,11057,12049,12857,13839,14642,15624,16427,18503,18503,18503,18503,18503,20,19,0,0,0,0,0,0,0x300000,0x708480,0xc30a80,0x1038f00,0x1561500,0x1969980,0x1e91f80,0x229a400,0x27c2a00,0x2ceb000,0x30f3480,0x361ba80,0x3a23f00,0x3f4c500,0x4354980,0x4876680,0x4c7b680,0x519d380,0x55a2380,0x8f400,0x8f400,0x8f400,0x8f400,0x8f400,0x8f400,0x8f400,0x8f400,0x8f400,0x8f400,0x8f400,0x8f400,0x8f400,258,1066,2058,2866,3858,4666,5658,6466,7458,8450,9258,10250,11058,12050,12858,13840,14643,15625,16428,17194,17716,18225,18225,18225,22,21,0,0,0,0,0,0,0x300a80,0x708f00,0xc31500,0x1039980,0x1561f80,0x196a400,0x1e92a00,0x229ae80,0x27c3480,0x2ceba80,0x30f3f00,0x361c500,0x3a24980,0x3f4cf80,0x4355400,0x4877100,0x4c7c100,0x519de00,0x55a2e00,0x598f980,0x5c34d80,0x5ed1900,0x5ed1900,0x5ed1900,0x5ed1900,0x5ed1900,0x5ed1900,0x5ed1900,0x5ed1900,0x5ed1900,0x5ed1900,0x5ed1900,259,1067,2059,2867,3859,4667,5659,6467,7459,8451,9259,10251,11059,12051,12859,13841,14644,15626,16429,17195,17717,17717,17717,17717,21,20,0,0,0,0,0,0,0x301500,0x709980,0xc31f80,0x103a400,0x1562a00,0x196ae80,0x1e93480,0x229b900,0x27c3f00,0x2cec500,0x30f4980,0x361cf80,0x3a25400,0x3f4da00,0x4355e80,0x4877b80,0x4c7cb80,0x519e880,0x55a3880,0x5990400,0x5c35800,0x5c35800,0x5c35800,0x5c35800,0x5c35800,0x5c35800,0x5c35800,0x5c35800,0x5c35800,0x5c35800,0x5c35800,0x5c35800,260,1068,2060,2868,3860,4668,5660,6468,7460,8452,9260,10252,11060,12052,12860,13842,14645,15627,16430,17196,17718,17718,17718,17718,21,20,0,0,0,0,0,0,0x301f80,0x70a400,0xc32a00,0x103ae80,0x1563480,0x196b900,0x1e93f00,0x229c380,0x27c4980,0x2cecf80,0x30f5400,0x361da00,0x3a25e80,0x3f4e480,0x4356900,0x4878600,0x4c7d600,0x519f300,0x55a4300,0x5990e80,0x5c36280,0x5c36280,0x5c36280,0x5c36280,0x5c36280,0x5c36280,0x5c36280,0x5c36280,0x5c36280,0x5c36280,0x5c36280,0x5c36280,261,1069,2061,2869,3861,4669,5661,6469,7461,8453,9261,10253,11061,12053,12861,13843,14646,15628,16431,18504,18504,18504,18504,18504,20,19,0,0,0,0,0,0,0x302a00,0x70ae80,0xc33480,0x103b900,0x1563f00,0x196c380,0x1e94980,0x229ce00,0x27c5400,0x2ceda00,0x30f5e80,0x361e480,0x3a26900,0x3f4ef00,0x4357380,0x4879080,0x4c7e080,0x519fd80,0x55a4d80,0x60000,0x60000,0x60000,0x60000,0x60000,0x60000,0x60000,0x60000,0x60000,0x60000,0x60000,0x60000,0x60000,262,1070,2062,2870,3862,4670,5662,6470,7462,8454,9262,10254,11062,12054,12862,13844,14647,15629,16432,72,17719,17719,17719,17719,21,20,0,0,0,0,0,0,0x303480,0x70b900,0xc33f00,0x103c380,0x1564980,0x196ce00,0x1e95400,0x229d880,0x27c5e80,0x2cee480,0x30f6900,0x361ef00,0x3a27380,0x3f4f980,0x4357e00,0x4879b00,0x4c7eb00,0x51a0800,0x55a5800,0x1af580,0x5c36d00,0x5c36d00,0x5c36d00,0x5c36d00,0x5c36d00,0x5c36d00,0x5c36d00,0x5c36d00,0x5c36d00,0x5c36d00,0x5c36d00,0x5c36d00,263,1071,2063,2871,3863,4671,5663,6471,7463,8455,9263,10255,11063,12055,12863,13845,14648,15630,16433,17197,17720,17720,17720,17720,21,20,0,0,0,0,0,0,0x303f00,0x70c380,0xc34980,0x103ce00,0x1565400,0x196d880,0x1e95e80,0x229e300,0x27c6900,0x2ceef00,0x30f7380,0x361f980,0x3a27e00,0x3f50400,0x4358880,0x487a580,0x4c7f580,0x51a1280,0x55a6280,0x5991900,0x5c37780,0x5c37780,0x5c37780,0x5c37780,0x5c37780,0x5c37780,0x5c37780,0x5c37780,0x5c37780,0x5c37780,0x5c37780,0x5c37780,264,1072,2064,2872,3864,4672,5664,6656,7464,8456,9264,10256,11064,12056,12864,18442,14649,18457,16434,328,328,328,328,328,20,19,0,0,0,0,0,0,0x304980,0x70ce00,0xc35400,0x103d880,0x1565e80,0x196e300,0x1e96900,0x23bef00,0x27c7380,0x2cef980,0x30f7e00,0x3620400,0x3a28880,0x3f50e80,0x4359300,0x67380,0x4c80000,0x71100,0x55a6d00,0x2feb00,0x2feb00,0x2feb00,0x2feb00,0x2feb00,0x2feb00,0x2feb00,0x2feb00,0x2feb00,0x2feb00,0x2feb00,0x2feb00,0x2feb00,265,1073,2065,2873,3865,4673,5665,6657,7465,8457,9265,10257,11065,12057,12865,13846,14650,15631,16435,584,584,584,584,584,20,19,0,0,0,0,0,0,0x305400,0x70d880,0xc35e80,0x103e300,0x1566900,0x196ed80,0x1e97380,0x23bf980,0x27c7e00,0x2cf0400,0x30f8880,0x3620e80,0x3a29300,0x3f51900,0x4359d80,0x487b000,0x4c80a80,0x51a1d00,0x55a7780,0x44e080,0x44e080,0x44e080,0x44e080,0x44e080,0x44e080,0x44e080,0x44e080,0x44e080,0x44e080,0x44e080,0x44e080,0x44e080,266,1074,2066,2874,3866,4674,5666,6658,7466,8458,9266,10258,11066,12058,12866,13847,14651,15632,16436,17198,17721,18226,18226,18226,22,21,0,0,0,0,0,0,0x305e80,0x70e300,0xc36900,0x103ed80,0x1567380,0x196f800,0x1e97e00,0x23c0400,0x27c8880,0x2cf0e80,0x30f9300,0x3621900,0x3a29d80,0x3f52380,0x435a800,0x487ba80,0x4c81500,0x51a2780,0x55a8200,0x5992380,0x5c38200,0x5ed2380,0x5ed2380,0x5ed2380,0x5ed2380,0x5ed2380,0x5ed2380,0x5ed2380,0x5ed2380,0x5ed2380,0x5ed2380,0x5ed2380,267,1075,2067,2875,3867,4675,5667,6659,7467,8459,9267,10259,11067,12059,12867,13848,14652,15633,16437,17199,17722,17722,17722,17722,21,20,0,0,0,0,0,0,0x306900,0x70ed80,0xc37380,0x103f800,0x1567e00,0x1970280,0x1e98880,0x23c0e80,0x27c9300,0x2cf1900,0x30f9d80,0x3622380,0x3a2a800,0x3f52e00,0x435b280,0x487c500,0x4c81f80,0x51a3200,0x55a8c80,0x5992e00,0x5c38c80,0x5c38c80,0x5c38c80,0x5c38c80,0x5c38c80,0x5c38c80,0x5c38c80,0x5c38c80,0x5c38c80,0x5c38c80,0x5c38c80,0x5c38c80,268,1076,2068,2876,3868,4676,5668,6660,7468,8460,9268,10260,11068,12060,12868,13849,14653,15634,16438,17200,17723,17723,17723,17723,21,20,0,0,0,0,0,0,0x307380,0x70f800,0xc37e00,0x1040280,0x1568880,0x1970d00,0x1e99300,0x23c1900,0x27c9d80,0x2cf2380,0x30fa800,0x3622e00,0x3a2b280,0x3f53880,0x435bd00,0x487cf80,0x4c82a00,0x51a3c80,0x55a9700,0x5993880,0x5c39700,0x5c39700,0x5c39700,0x5c39700,0x5c39700,0x5c39700,0x5c39700,0x5c39700,0x5c39700,0x5c39700,0x5c39700,0x5c39700,269,1077,2069,2877,3869,4677,5669,6661,7469,8461,9269,10261,11069,12061,12869,13850,14654,15635,16439,840,840,840,840,840,20,19,0,0,0,0,0,0,0x307e00,0x710280,0xc38880,0x1040d00,0x1569300,0x1971780,0x1e99d80,0x23c2380,0x27ca800,0x2cf2e00,0x30fb280,0x3623880,0x3a2bd00,0x3f54300,0x435c780,0x487da00,0x4c83480,0x51a4700,0x55aa180,0x59d600,0x59d600,0x59d600,0x59d600,0x59d600,0x59d600,0x59d600,0x59d600,0x59d600,0x59d600,0x59d600,0x59d600,0x59d600,270,1078,2070,2878,3870,4678,5670,6662,7470,8462,9270,10262,11070,12062,12870,13851,14655,15636,16440,1096,17724,17724,17724,17724,21,20,0,0,0,0,0,0,0x308880,0x710d00,0xc39300,0x1041780,0x1569d80,0x1972200,0x1e9a800,0x23c2e00,0x27cb280,0x2cf3880,0x30fbd00,0x3624300,0x3a2c780,0x3f54d80,0x435d200,0x487e480,0x4c83f00,0x51a5180,0x55aac00,0x6ecb80,0x5c3a180,0x5c3a180,0x5c3a180,0x5c3a180,0x5c3a180,0x5c3a180,0x5c3a180,0x5c3a180,0x5c3a180,0x5c3a180,0x5c3a180,0x5c3a180,271,1079,2071,2879,3871,4679,5671,6663,7471,8463,9271,10263,11071,12063,12871,13852,14656,15637,16441,17201,17725,17725,17725,17725,21,20,0,0,0,0,0,0,0x309300,0x711780,0xc39d80,0x1042200,0x156a800,0x1972c80,0x1e9b280,0x23c3880,0x27cbd00,0x2cf4300,0x30fc780,0x3624d80,0x3a2d200,0x3f55800,0x435dc80,0x487ef00,0x4c84980,0x51a5c00,0x55ab680,0x5994300,0x5c3ac00,0x5c3ac00,0x5c3ac00,0x5c3ac00,0x5c3ac00,0x5c3ac00,0x5c3ac00,0x5c3ac00,0x5c3ac00,0x5c3ac00,0x5c3ac00,0x5c3ac00,272,1080,2072,2880,3872,4864,5672,6664,7472,8464,9272,10264,11072,12064,13056,18443,14657,18458,16442,1352,1352,1352,1352,1352,20,19,0,0,0,0,0,0,0x309d80,0x712200,0xc3a800,0x1042c80,0x156b280,0x1a93880,0x1e9bd00,0x23c4300,0x27cc780,0x2cf4d80,0x30fd200,0x3625800,0x3a2dc80,0x3f56280,0x447e880,0x67e00,0x4c85400,0x71b80,0x55ac100,0x83c100,0x83c100,0x83c100,0x83c100,0x83c100,0x83c100,0x83c100,0x83c100,0x83c100,0x83c100,0x83c100,0x83c100,0x83c100,273,1081,2073,2881,3873,4865,5673,6665,7473,8465,9273,10265,11073,12065,13057,13853,14658,15638,16443,1608,1608,1608,1608,1608,20,19,0,0,0,0,0,0,0x30a800,0x712c80,0xc3b280,0x1043700,0x156bd00,0x1a94300,0x1e9c780,0x23c4d80,0x27cd200,0x2cf5800,0x30fdc80,0x3626280,0x3a2e700,0x3f56d00,0x447f300,0x487f980,0x4c85e80,0x51a6680,0x55acb80,0x98b680,0x98b680,0x98b680,0x98b680,0x98b680,0x98b680,0x98b680,0x98b680,0x98b680,0x98b680,0x98b680,0x98b680,0x98b680,274,1082,2074,2882,3874,4866,5674,6666,7474,8466,9274,10266,11074,12066,13058,13854,14659,15639,16444,17202,17726,18227,18227,18227,22,21,0,0,0,0,0,0,0x30b280,0x713700,0xc3bd00,0x1044180,0x156c780,0x1a94d80,0x1e9d200,0x23c5800,0x27cdc80,0x2cf6280,0x30fe700,0x3626d00,0x3a2f180,0x3f57780,0x447fd80,0x4880400,0x4c86900,0x51a7100,0x55ad600,0x5994d80,0x5c3b680,0x5ed2e00,0x5ed2e00,0x5ed2e00,0x5ed2e00,0x5ed2e00,0x5ed2e00,0x5ed2e00,0x5ed2e00,0x5ed2e00,0x5ed2e00,0x5ed2e00,275,1083,2075,2883,3875,4867,5675,6667,7475,8467,9275,10267,11075,12067,13059,13855,14660,15640,16445,17203,17727,17727,17727,17727,21,20,0,0,0,0,0,0,0x30bd00,0x714180,0xc3c780,0x1044c00,0x156d200,0x1a95800,0x1e9dc80,0x23c6280,0x27ce700,0x2cf6d00,0x30ff180,0x3627780,0x3a2fc00,0x3f58200,0x4480800,0x4880e80,0x4c87380,0x51a7b80,0x55ae080,0x5995800,0x5c3c100,0x5c3c100,0x5c3c100,0x5c3c100,0x5c3c100,0x5c3c100,0x5c3c100,0x5c3c100,0x5c3c100,0x5c3c100,0x5c3c100,0x5c3c100,276,1084,2076,2884,3876,4868,5676,6668,7476,8468,9276,10268,11076,12068,13060,13856,14661,15641,16446,17204,17728,17728,17728,17728,21,20,0,0,0,0,0,0,0x30c780,0x714c00,0xc3d200,0x1045680,0x156dc80,0x1a96280,0x1e9e700,0x23c6d00,0x27cf180,0x2cf7780,0x30ffc00,0x3628200,0x3a30680,0x3f58c80,0x4481280,0x4881900,0x4c87e00,0x51a8600,0x55aeb00,0x5996280,0x5c3cb80,0x5c3cb80,0x5c3cb80,0x5c3cb80,0x5c3cb80,0x5c3cb80,0x5c3cb80,0x5c3cb80,0x5c3cb80,0x5c3cb80,0x5c3cb80,0x5c3cb80,277,1085,2077,2885,3877,4869,5677,6669,7477,8469,9277,10269,11077,12069,13061,13857,14662,15642,16447,1864,1864,1864,1864,1864,20,19,0,0,0,0,0,0,0x30d200,0x715680,0xc3dc80,0x1046100,0x156e700,0x1a96d00,0x1e9f180,0x23c7780,0x27cfc00,0x2cf8200,0x3100680,0x3628c80,0x3a31100,0x3f59700,0x4481d00,0x4882380,0x4c88880,0x51a9080,0x55af580,0xadac00,0xadac00,0xadac00,0xadac00,0xadac00,0xadac00,0xadac00,0xadac00,0xadac00,0xadac00,0xadac00,0xadac00,0xadac00,278,1086,2078,2886,3878,4870,5678,6670,7478,8470,9278,10270,11078,12070,13062,13858,14663,15643,16448,2120,17729,17729,17729,17729,21,20,0,0,0,0,0,0,0x30dc80,0x716100,0xc3e700,0x1046b80,0x156f180,0x1a97780,0x1e9fc00,0x23c8200,0x27d0680,0x2cf8c80,0x3101100,0x3629700,0x3a31b80,0x3f5a180,0x4482780,0x4882e00,0x4c89300,0x51a9b00,0x55b0000,0xc2a180,0x5c3d600,0x5c3d600,0x5c3d600,0x5c3d600,0x5c3d600,0x5c3d600,0x5c3d600,0x5c3d600,0x5c3d600,0x5c3d600,0x5c3d600,0x5c3d600,279,1087,2079,2887,3879,4871,5679,6671,7479,8471,9279,10271,11079,12071,13063,13859,14848,15644,16449,17205,17730,17730,17730,17730,21,20,0,0,0,0,0,0,0x30e700,0x716b80,0xc3f180,0x1047600,0x156fc00,0x1a98200,0x1ea0680,0x23c8c80,0x27d1100,0x2cf9700,0x3101b80,0x362a180,0x3a32600,0x3f5ac00,0x4483200,0x4883880,0x4da9f00,0x51aa580,0x55b0a80,0x5996d00,0x5c3e080,0x5c3e080,0x5c3e080,0x5c3e080,0x5c3e080,0x5c3e080,0x5c3e080,0x5c3e080,0x5c3e080,0x5c3e080,0x5c3e080,0x5c3e080,280,1088,2080,3072,3880,4872,5680,6672,7480,8472,9280,10272,11264,12072,13064,18444,14849,18459,16450,2376,2376,2376,2376,2376,20,19,0,0,0,0,0,0,0x30f180,0x717600,0xc3fc00,0x1168200,0x1570680,0x1a98c80,0x1ea1100,0x23c9700,0x27d1b80,0x2cfa180,0x3102600,0x362ac00,0x3b53200,0x3f5b680,0x4483c80,0x68880,0x4daa980,0x72600,0x55b1500,0xd79700,0xd79700,0xd79700,0xd79700,0xd79700,0xd79700,0xd79700,0xd79700,0xd79700,0xd79700,0xd79700,0xd79700,0xd79700,281,1089,2081,3073,3881,4873,5681,6673,7481,8473,9281,10273,11265,12073,13065,13860,14850,15645,16451,2632,2632,2632,2632,2632,20,19,0,0,0,0,0,0,0x30fc00,0x718080,0xc40680,0x1168c80,0x1571100,0x1a99700,0x1ea1b80,0x23ca180,0x27d2600,0x2cfac00,0x3103080,0x362b680,0x3b53c80,0x3f5c100,0x4484700,0x4884300,0x4dab400,0x51ab000,0x55b1f80,0xec8c80,0xec8c80,0xec8c80,0xec8c80,0xec8c80,0xec8c80,0xec8c80,0xec8c80,0xec8c80,0xec8c80,0xec8c80,0xec8c80,0xec8c80,282,1090,2082,3074,3882,4874,5682,6674,7482,8474,9282,10274,11266,12074,13066,13861,14851,15646,16452,17206,17731,18228,18228,18228,22,21,0,0,0,0,0,0,0x310680,0x718b00,0xc41100,0x1169700,0x1571b80,0x1a9a180,0x1ea2600,0x23cac00,0x27d3080,0x2cfb680,0x3103b00,0x362c100,0x3b54700,0x3f5cb80,0x4485180,0x4884d80,0x4dabe80,0x51aba80,0x55b2a00,0x5997780,0x5c3eb00,0x5ed3880,0x5ed3880,0x5ed3880,0x5ed3880,0x5ed3880,0x5ed3880,0x5ed3880,0x5ed3880,0x5ed3880,0x5ed3880,0x5ed3880,283,1091,2083,3075,3883,4875,5683,6675,7483,8475,9283,10275,11267,12075,13067,13862,14852,15647,16453,17207,17732,17732,17732,17732,21,20,0,0,0,0,0,0,0x311100,0x719580,0xc41b80,0x116a180,0x1572600,0x1a9ac00,0x1ea3080,0x23cb680,0x27d3b00,0x2cfc100,0x3104580,0x362cb80,0x3b55180,0x3f5d600,0x4485c00,0x4885800,0x4dac900,0x51ac500,0x55b3480,0x5998200,0x5c3f580,0x5c3f580,0x5c3f580,0x5c3f580,0x5c3f580,0x5c3f580,0x5c3f580,0x5c3f580,0x5c3f580,0x5c3f580,0x5c3f580,0x5c3f580,284,1092,2084,3076,3884,4876,5684,6676,7484,8476,9284,10276,11268,12076,13068,13863,14853,15648,16454,17208,17733,17733,17733,17733,21,20,0,0,0,0,0,0,0x311b80,0x71a000,0xc42600,0x116ac00,0x1573080,0x1a9b680,0x1ea3b00,0x23cc100,0x27d4580,0x2cfcb80,0x3105000,0x362d600,0x3b55c00,0x3f5e080,0x4486680,0x4886280,0x4dad380,0x51acf80,0x55b3f00,0x5998c80,0x5c40000,0x5c40000,0x5c40000,0x5c40000,0x5c40000,0x5c40000,0x5c40000,0x5c40000,0x5c40000,0x5c40000,0x5c40000,0x5c40000,285,1093,2085,3077,3885,4877,5685,6677,7485,8477,9285,10277,11269,12077,13069,13864,14854,15649,16455,2888,2888,2888,2888,2888,20,19,0,0,0,0,0,0,0x312600,0x71aa80,0xc43080,0x116b680,0x1573b00,0x1a9c100,0x1ea4580,0x23ccb80,0x27d5000,0x2cfd600,0x3105a80,0x362e080,0x3b56680,0x3f5eb00,0x4487100,0x4886d00,0x4dade00,0x51ada00,0x55b4980,0x1018200,0x1018200,0x1018200,0x1018200,0x1018200,0x1018200,0x1018200,0x1018200,0x1018200,0x1018200,0x1018200,0x1018200,0x1018200,286,1094,2086,3078,3886,4878,5686,6678,7486,8478,9286,10278,11270,12078,13070,13865,14855,15650,16640,3144,17734,17734,17734,17734,21,20,0,0,0,0,0,0,0x313080,0x71b500,0xc43b00,0x116c100,0x1574580,0x1a9cb80,0x1ea5000,0x23cd600,0x27d5a80,0x2cfe080,0x3106500,0x362eb00,0x3b57100,0x3f5f580,0x4487b80,0x4887780,0x4dae880,0x51ae480,0x56d5580,0x1167780,0x5c40a80,0x5c40a80,0x5c40a80,0x5c40a80,0x5c40a80,0x5c40a80,0x5c40a80,0x5c40a80,0x5c40a80,0x5c40a80,0x5c40a80,0x5c40a80,287,1095,2087,3079,3887,4879,5687,6679,7487,8479,9287,10279,11271,12079,13071,13866,14856,15651,16641,17209,17735,17735,17735,17735,21,20,0,0,0,0,0,0,0x313b00,0x71bf80,0xc44580,0x116cb80,0x1575000,0x1a9d600,0x1ea5a80,0x23ce080,0x27d6500,0x2cfeb00,0x3106f80,0x362f580,0x3b57b80,0x3f60000,0x4488600,0x4888200,0x4daf300,0x51aef00,0x56d6000,0x5999700,0x5c41500,0x5c41500,0x5c41500,0x5c41500,0x5c41500,0x5c41500,0x5c41500,0x5c41500,0x5c41500,0x5c41500,0x5c41500,0x5c41500,288,1280,2088,3080,3888,4880,5688,6680,7488,8480,9472,10280,11272,12080,13072,18445,14857,18460,16642,3400,3400,3400,3400,3400,20,19,0,0,0,0,0,0,0x314580,0x83cb80,0xc45000,0x116d600,0x1575a80,0x1a9e080,0x1ea6500,0x23ceb00,0x27d6f80,0x2cff580,0x3227b80,0x3630000,0x3b58600,0x3f60a80,0x4489080,0x69300,0x4dafd80,0x73080,0x56d6a80,0x12b6d00,0x12b6d00,0x12b6d00,0x12b6d00,0x12b6d00,0x12b6d00,0x12b6d00,0x12b6d00,0x12b6d00,0x12b6d00,0x12b6d00,0x12b6d00,0x12b6d00,289,1281,2089,3081,3889,4881,5689,6681,7489,8481,9473,10281,11273,12081,13073,13867,14858,15652,16643,3656,3656,3656,3656,3656,20,19,0,0,0,0,0,0,0x315000,0x83d600,0xc45a80,0x116e080,0x1576500,0x1a9eb00,0x1ea6f80,0x23cf580,0x27d7a00,0x2d00000,0x3228600,0x3630a80,0x3b59080,0x3f61500,0x4489b00,0x4888c80,0x4db0800,0x51af980,0x56d7500,0x1406280,0x1406280,0x1406280,0x1406280,0x1406280,0x1406280,0x1406280,0x1406280,0x1406280,0x1406280,0x1406280,0x1406280,0x1406280,290,1282,2090,3082,3890,4882,5690,6682,7490,8482,9474,10282,11274,12082,13074,13868,14859,15653,16644,17210,17920,18229,18229,18229,22,21,0,0,0,0,0,0,0x315a80,0x83e080,0xc46500,0x116eb00,0x1576f80,0x1a9f580,0x1ea7a00,0x23d0000,0x27d8480,0x2d00a80,0x3229080,0x3631500,0x3b59b00,0x3f61f80,0x448a580,0x4889700,0x4db1280,0x51b0400,0x56d7f80,0x599a180,0x5d62100,0x5ed4300,0x5ed4300,0x5ed4300,0x5ed4300,0x5ed4300,0x5ed4300,0x5ed4300,0x5ed4300,0x5ed4300,0x5ed4300,0x5ed4300,291,1283,2091,3083,3891,4883,5691,6683,7491,8483,9475,10283,11275,12083,13075,13869,14860,15654,16645,17211,17921,17921,17921,17921,21,20,0,0,0,0,0,0,0x316500,0x83eb00,0xc46f80,0x116f580,0x1577a00,0x1aa0000,0x1ea8480,0x23d0a80,0x27d8f00,0x2d01500,0x3229b00,0x3631f80,0x3b5a580,0x3f62a00,0x448b000,0x488a180,0x4db1d00,0x51b0e80,0x56d8a00,0x599ac00,0x5d62b80,0x5d62b80,0x5d62b80,0x5d62b80,0x5d62b80,0x5d62b80,0x5d62b80,0x5d62b80,0x5d62b80,0x5d62b80,0x5d62b80,0x5d62b80,292,1284,2092,3084,3892,4884,5692,6684,7492,8484,9476,10284,11276,12084,13076,13870,14861,15655,16646,17212,17922,17922,17922,17922,21,20,0,0,0,0,0,0,0x316f80,0x83f580,0xc47a00,0x1170000,0x1578480,0x1aa0a80,0x1ea8f00,0x23d1500,0x27d9980,0x2d01f80,0x322a580,0x3632a00,0x3b5b000,0x3f63480,0x448ba80,0x488ac00,0x4db2780,0x51b1900,0x56d9480,0x599b680,0x5d63600,0x5d63600,0x5d63600,0x5d63600,0x5d63600,0x5d63600,0x5d63600,0x5d63600,0x5d63600,0x5d63600,0x5d63600,0x5d63600,293,1285,2093,3085,3893,4885,5693,6685,7493,8485,9477,10285,11277,12085,13077,13871,14862,15656,16647,3912,3912,3912,3912,3912,20,19,0,0,0,0,0,0,0x317a00,0x840000,0xc48480,0x1170a80,0x1578f00,0x1aa1500,0x1ea9980,0x23d1f80,0x27da400,0x2d02a00,0x322b000,0x3633480,0x3b5ba80,0x3f63f00,0x448c500,0x488b680,0x4db3200,0x51b2380,0x56d9f00,0x1555800,0x1555800,0x1555800,0x1555800,0x1555800,0x1555800,0x1555800,0x1555800,0x1555800,0x1555800,0x1555800,0x1555800,0x1555800,294,1286,2094,3086,3894,4886,5694,6686,7494,8486,9478,10286,11278,12086,13078,13872,14863,15657,16648,4168,17923,17923,17923,17923,21,20,0,0,0,0,0,0,0x318480,0x840a80,0xc48f00,0x1171500,0x1579980,0x1aa1f80,0x1eaa400,0x23d2a00,0x27dae80,0x2d03480,0x322ba80,0x3633f00,0x3b5c500,0x3f64980,0x448cf80,0x488c100,0x4db3c80,0x51b2e00,0x56da980,0x16a4d80,0x5d64080,0x5d64080,0x5d64080,0x5d64080,0x5d64080,0x5d64080,0x5d64080,0x5d64080,0x5d64080,0x5d64080,0x5d64080,0x5d64080,295,1287,2095,3087,3895,4887,5695,6687,7495,8487,9479,10287,11279,12087,13079,13873,14864,15658,16649,17213,17924,17924,17924,17924,21,20,0,0,0,0,0,0,0x318f00,0x841500,0xc49980,0x1171f80,0x157a400,0x1aa2a00,0x1eaae80,0x23d3480,0x27db900,0x2d03f00,0x322c500,0x3634980,0x3b5cf80,0x3f65400,0x448da00,0x488cb80,0x4db4700,0x51b3880,0x56db400,0x599c100,0x5d64b00,0x5d64b00,0x5d64b00,0x5d64b00,0x5d64b00,0x5d64b00,0x5d64b00,0x5d64b00,0x5d64b00,0x5d64b00,0x5d64b00,0x5d64b00,296,1288,2096,3088,3896,4888,5696,6688,7680,8488,9480,10288,11280,12088,13080,18446,14865,18461,16650,4424,4424,4424,4424,4424,20,19,0,0,0,0,0,0,0x319980,0x841f80,0xc4a400,0x1172a00,0x157ae80,0x1aa3480,0x1eab900,0x23d3f00,0x28fc500,0x2d04980,0x322cf80,0x3635400,0x3b5da00,0x3f65e80,0x448e480,0x69d80,0x4db5180,0x73b00,0x56dbe80,0x17f4300,0x17f4300,0x17f4300,0x17f4300,0x17f4300,0x17f4300,0x17f4300,0x17f4300,0x17f4300,0x17f4300,0x17f4300,0x17f4300,0x17f4300,297,1289,2097,3089,3897,4889,5697,6689,7681,8489,9481,10289,11281,12089,13081,13874,14866,15659,16651,4680,4680,4680,4680,4680,20,19,0,0,0,0,0,0,0x31a400,0x842a00,0xc4ae80,0x1173480,0x157b900,0x1aa3f00,0x1eac380,0x23d4980,0x28fcf80,0x2d05400,0x322da00,0x3635e80,0x3b5e480,0x3f66900,0x448ef00,0x488d600,0x4db5c00,0x51b4300,0x56dc900,0x1943880,0x1943880,0x1943880,0x1943880,0x1943880,0x1943880,0x1943880,0x1943880,0x1943880,0x1943880,0x1943880,0x1943880,0x1943880,298,1290,2098,3090,3898,4890,5698,6690,7682,8490,9482,10290,11282,12090,13082,13875,14867,15660,16652,17214,17925,18230,18230,18230,22,21,0,0,0,0,0,0,0x31ae80,0x843480,0xc4b900,0x1173f00,0x157c380,0x1aa4980,0x1eace00,0x23d5400,0x28fda00,0x2d05e80,0x322e480,0x3636900,0x3b5ef00,0x3f67380,0x448f980,0x488e080,0x4db6680,0x51b4d80,0x56dd380,0x599cb80,0x5d65580,0x5ed4d80,0x5ed4d80,0x5ed4d80,0x5ed4d80,0x5ed4d80,0x5ed4d80,0x5ed4d80,0x5ed4d80,0x5ed4d80,0x5ed4d80,0x5ed4d80,299,1291,2099,3091,3899,4891,5699,6691,7683,8491,9483,10291,11283,12091,13083,13876,14868,15661,16653,17215,17926,17926,17926,17926,21,20,0,0,0,0,0,0,0x31b900,0x843f00,0xc4c380,0x1174980,0x157ce00,0x1aa5400,0x1ead880,0x23d5e80,0x28fe480,0x2d06900,0x322ef00,0x3637380,0x3b5f980,0x3f67e00,0x4490400,0x488eb00,0x4db7100,0x51b5800,0x56dde00,0x599d600,0x5d66000,0x5d66000,0x5d66000,0x5d66000,0x5d66000,0x5d66000,0x5d66000,0x5d66000,0x5d66000,0x5d66000,0x5d66000,0x5d66000,300,1292,2100,3092,3900,4892,5700,6692,7684,8492,9484,10292,11284,12092,13084,13877,14869,15662,16654,17216,17927,17927,17927,17927,21,20,0,0,0,0,0,0,0x31c380,0x844980,0xc4ce00,0x1175400,0x157d880,0x1aa5e80,0x1eae300,0x23d6900,0x28fef00,0x2d07380,0x322f980,0x3637e00,0x3b60400,0x3f68880,0x4490e80,0x488f580,0x4db7b80,0x51b6280,0x56de880,0x599e080,0x5d66a80,0x5d66a80,0x5d66a80,0x5d66a80,0x5d66a80,0x5d66a80,0x5d66a80,0x5d66a80,0x5d66a80,0x5d66a80,0x5d66a80,0x5d66a80,301,1293,2101,3093,3901,4893,5701,6693,7685,8493,9485,10293,11285,12093,13085,13878,14870,15663,16655,4936,4936,4936,4936,4936,20,19,0,0,0,0,0,0,0x31ce00,0x845400,0xc4d880,0x1175e80,0x157e300,0x1aa6900,0x1eaed80,0x23d7380,0x28ff980,0x2d07e00,0x3230400,0x3638880,0x3b60e80,0x3f69300,0x4491900,0x4890000,0x4db8600,0x51b6d00,0x56df300,0x1a92e00,0x1a92e00,0x1a92e00,0x1a92e00,0x1a92e00,0x1a92e00,0x1a92e00,0x1a92e00,0x1a92e00,0x1a92e00,0x1a92e00,0x1a92e00,0x1a92e00,302,1294,2102,3094,3902,4894,5702,6694,7686,8494,9486,10294,11286,12094,13086,13879,14871,15664,16656,5192,17928,17928,17928,17928,21,20,0,0,0,0,0,0,0x31d880,0x845e80,0xc4e300,0x1176900,0x157ed80,0x1aa7380,0x1eaf800,0x23d7e00,0x2900400,0x2d08880,0x3230e80,0x3639300,0x3b61900,0x3f69d80,0x4492380,0x4890a80,0x4db9080,0x51b7780,0x56dfd80,0x1be2380,0x5d67500,0x5d67500,0x5d67500,0x5d67500,0x5d67500,0x5d67500,0x5d67500,0x5d67500,0x5d67500,0x5d67500,0x5d67500,0x5d67500,303,1295,2103,3095,3903,4895,5703,6695,7687,8495,9487,10295,11287,12095,13087,13880,14872,15665,16657,17217,17929,17929,17929,17929,21,20,0,0,0,0,0,0,0x31e300,0x846900,0xc4ed80,0x1177380,0x157f800,0x1aa7e00,0x1eb0280,0x23d8880,0x2900e80,0x2d09300,0x3231900,0x3639d80,0x3b62380,0x3f6a800,0x4492e00,0x4891500,0x4db9b00,0x51b8200,0x56e0800,0x599eb00,0x5d67f80,0x5d67f80,0x5d67f80,0x5d67f80,0x5d67f80,0x5d67f80,0x5d67f80,0x5d67f80,0x5d67f80,0x5d67f80,0x5d67f80,0x5d67f80,304,1296,2104,3096,3904,4896,5888,6696,7688,8496,9488,10296,11288,12096,13088,13881,14873,15666,16658,17218,17930,17930,17930,17930,21,20,0,0,0,0,0,0,0x31ed80,0x847380,0xc4f800,0x1177e00,0x1580280,0x1aa8880,0x1fd0e80,0x23d9300,0x2901900,0x2d09d80,0x3232380,0x363a800,0x3b62e00,0x3f6b280,0x4493880,0x4891f80,0x4dba580,0x51b8c80,0x56e1280,0x599f580,0x5d68a00,0x5d68a00,0x5d68a00,0x5d68a00,0x5d68a00,0x5d68a00,0x5d68a00,0x5d68a00,0x5d68a00,0x5d68a00,0x5d68a00,0x5d68a00,305,1297,2105,3097,3905,4897,5889,6697,7689,8497,9489,10297,11289,12097,13089,13882,14874,15667,16659,5448,5448,5448,5448,5448,20,19,0,0,0,0,0,0,0x31f800,0x847e00,0xc50280,0x1178880,0x1580d00,0x1aa9300,0x1fd1900,0x23d9d80,0x2902380,0x2d0a800,0x3232e00,0x363b280,0x3b63880,0x3f6bd00,0x4494300,0x4892a00,0x4dbb000,0x51b9700,0x56e1d00,0x1d31900,0x1d31900,0x1d31900,0x1d31900,0x1d31900,0x1d31900,0x1d31900,0x1d31900,0x1d31900,0x1d31900,0x1d31900,0x1d31900,0x1d31900,306,1298,2106,3098,3906,4898,5890,6698,7690,8498,9490,10298,11290,12098,13090,13883,14875,15668,16660,17219,17931,18231,18231,18231,22,21,0,0,0,0,0,0,0x320280,0x848880,0xc50d00,0x1179300,0x1581780,0x1aa9d80,0x1fd2380,0x23da800,0x2902e00,0x2d0b280,0x3233880,0x363bd00,0x3b64300,0x3f6c780,0x4494d80,0x4893480,0x4dbba80,0x51ba180,0x56e2780,0x59a0000,0x5d69480,0x5ed5800,0x5ed5800,0x5ed5800,0x5ed5800,0x5ed5800,0x5ed5800,0x5ed5800,0x5ed5800,0x5ed5800,0x5ed5800,0x5ed5800,307,1299,2107,3099,3907,4899,5891,6699,7691,8499,9491,10299,11291,12099,13091,13884,14876,15669,16661,17220,17932,17932,17932,17932,21,20,0,0,0,0,0,0,0x320d00,0x849300,0xc51780,0x1179d80,0x1582200,0x1aaa800,0x1fd2e00,0x23db280,0x2903880,0x2d0bd00,0x3234300,0x363c780,0x3b64d80,0x3f6d200,0x4495800,0x4893f00,0x4dbc500,0x51bac00,0x56e3200,0x59a0a80,0x5d69f00,0x5d69f00,0x5d69f00,0x5d69f00,0x5d69f00,0x5d69f00,0x5d69f00,0x5d69f00,0x5d69f00,0x5d69f00,0x5d69f00,0x5d69f00,308,1300,2108,3100,3908,4900,5892,6700,7692,8500,9492,10300,11292,12100,13092,13885,14877,15670,16662,17221,17933,17933,17933,17933,21,20,0,0,0,0,0,0,0x321780,0x849d80,0xc52200,0x117a800,0x1582c80,0x1aab280,0x1fd3880,0x23dbd00,0x2904300,0x2d0c780,0x3234d80,0x363d200,0x3b65800,0x3f6dc80,0x4496280,0x4894980,0x4dbcf80,0x51bb680,0x56e3c80,0x59a1500,0x5d6a980,0x5d6a980,0x5d6a980,0x5d6a980,0x5d6a980,0x5d6a980,0x5d6a980,0x5d6a980,0x5d6a980,0x5d6a980,0x5d6a980,0x5d6a980,309,1301,2109,3101,3909,4901,5893,6701,7693,8501,9493,10301,11293,12101,13093,13886,14878,15671,16663,5704,5704,5704,5704,5704,20,19,0,0,0,0,0,0,0x322200,0x84a800,0xc52c80,0x117b280,0x1583700,0x1aabd00,0x1fd4300,0x23dc780,0x2904d80,0x2d0d200,0x3235800,0x363dc80,0x3b66280,0x3f6e700,0x4496d00,0x4895400,0x4dbda00,0x51bc100,0x56e4700,0x1e80e80,0x1e80e80,0x1e80e80,0x1e80e80,0x1e80e80,0x1e80e80,0x1e80e80,0x1e80e80,0x1e80e80,0x1e80e80,0x1e80e80,0x1e80e80,0x1e80e80,310,1302,2110,3102,3910,4902,5894,6702,7694,8502,9494,10302,11294,12102,13094,13887,14879,15672,16664,5960,17934,17934,17934,17934,21,20,0,0,0,0,0,0,0x322c80,0x84b280,0xc53700,0x117bd00,0x1584180,0x1aac780,0x1fd4d80,0x23dd200,0x2905800,0x2d0dc80,0x3236280,0x363e700,0x3b66d00,0x3f6f180,0x4497780,0x4895e80,0x4dbe480,0x51bcb80,0x56e5180,0x1fd0400,0x5d6b400,0x5d6b400,0x5d6b400,0x5d6b400,0x5d6b400,0x5d6b400,0x5d6b400,0x5d6b400,0x5d6b400,0x5d6b400,0x5d6b400,0x5d6b400,311,1303,2111,3103,3911,4903,5895,6703,7695,8503,9495,10303,11295,12103,13095,13888,14880,15673,16665,17222,17935,17935,17935,17935,21,20,0,0,0,0,0,0,0x323700,0x84bd00,0xc54180,0x117c780,0x1584c00,0x1aad200,0x1fd5800,0x23ddc80,0x2906280,0x2d0e700,0x3236d00,0x363f180,0x3b67780,0x3f6fc00,0x4498200,0x4896900,0x4dbef00,0x51bd600,0x56e5c00,0x59a1f80,0x5d6be80,0x5d6be80,0x5d6be80,0x5d6be80,0x5d6be80,0x5d6be80,0x5d6be80,0x5d6be80,0x5d6be80,0x5d6be80,0x5d6be80,0x5d6be80,312,1304,2112,3104,4096,4904,5896,6704,7696,8504,9496,10304,11296,12288,13096,13889,14881,15674,16666,17223,17936,17936,17936,17936,21,20,0,0,0,0,0,0,0x324180,0x84c780,0xc54c00,0x117d200,0x16a5800,0x1aadc80,0x1fd6280,0x23de700,0x2906d00,0x2d0f180,0x3237780,0x363fc00,0x3b68200,0x4090800,0x4498c80,0x4897380,0x4dbf980,0x51be080,0x56e6680,0x59a2a00,0x5d6c900,0x5d6c900,0x5d6c900,0x5d6c900,0x5d6c900,0x5d6c900,0x5d6c900,0x5d6c900,0x5d6c900,0x5d6c900,0x5d6c900,0x5d6c900,313,1305,2113,3105,4097,4905,5897,6705,7697,8505,9497,10305,11297,12289,13097,13890,14882,15675,16667,6216,6216,6216,6216,6216,20,19,0,0,0,0,0,0,0x324c00,0x84d200,0xc55680,0x117dc80,0x16a6280,0x1aae700,0x1fd6d00,0x23df180,0x2907780,0x2d0fc00,0x3238200,0x3640680,0x3b68c80,0x4091280,0x4499700,0x4897e00,0x4dc0400,0x51beb00,0x56e7100,0x211f980,0x211f980,0x211f980,0x211f980,0x211f980,0x211f980,0x211f980,0x211f980,0x211f980,0x211f980,0x211f980,0x211f980,0x211f980,314,1306,2114,3106,4098,4906,5898,6706,7698,8506,9498,10306,11298,12290,13098,13891,14883,15676,16668,17408,17937,18232,18232,18232,22,21,0,0,0,0,0,0,0x325680,0x84dc80,0xc56100,0x117e700,0x16a6d00,0x1aaf180,0x1fd7780,0x23dfc00,0x2908200,0x2d10680,0x3238c80,0x3641100,0x3b69700,0x4091d00,0x449a180,0x4898880,0x4dc0e80,0x51bf580,0x56e7b80,0x5ac3600,0x5d6d380,0x5ed6280,0x5ed6280,0x5ed6280,0x5ed6280,0x5ed6280,0x5ed6280,0x5ed6280,0x5ed6280,0x5ed6280,0x5ed6280,0x5ed6280,315,1307,2115,3107,4099,4907,5899,6707,7699,8507,9499,10307,11299,12291,13099,13892,14884,15677,16669,17409,17938,17938,17938,17938,21,20,0,0,0,0,0,0,0x326100,0x84e700,0xc56b80,0x117f180,0x16a7780,0x1aafc00,0x1fd8200,0x23e0680,0x2908c80,0x2d11100,0x3239700,0x3641b80,0x3b6a180,0x4092780,0x449ac00,0x4899300,0x4dc1900,0x51c0000,0x56e8600,0x5ac4080,0x5d6de00,0x5d6de00,0x5d6de00,0x5d6de00,0x5d6de00,0x5d6de00,0x5d6de00,0x5d6de00,0x5d6de00,0x5d6de00,0x5d6de00,0x5d6de00,316,1308,2116,3108,4100,4908,5900,6708,7700,8508,9500,10308,11300,12292,13100,13893,14885,15678,16670,17410,17939,17939,17939,17939,21,20,0,0,0,0,0,0,0x326b80,0x84f180,0xc57600,0x117fc00,0x16a8200,0x1ab0680,0x1fd8c80,0x23e1100,0x2909700,0x2d11b80,0x323a180,0x3642600,0x3b6ac00,0x4093200,0x449b680,0x4899d80,0x4dc2380,0x51c0a80,0x56e9080,0x5ac4b00,0x5d6e880,0x5d6e880,0x5d6e880,0x5d6e880,0x5d6e880,0x5d6e880,0x5d6e880,0x5d6e880,0x5d6e880,0x5d6e880,0x5d6e880,0x5d6e880,317,1309,2117,3109,4101,4909,5901,6709,7701,8509,9501,10309,11301,12293,13101,13894,14886,15679,16671,6472,6472,6472,6472,6472,20,19,0,0,0,0,0,0,0x327600,0x84fc00,0xc58080,0x1180680,0x16a8c80,0x1ab1100,0x1fd9700,0x23e1b80,0x290a180,0x2d12600,0x323ac00,0x3643080,0x3b6b680,0x4093c80,0x449c100,0x489a800,0x4dc2e00,0x51c1500,0x56e9b00,0x226ef00,0x226ef00,0x226ef00,0x226ef00,0x226ef00,0x226ef00,0x226ef00,0x226ef00,0x226ef00,0x226ef00,0x226ef00,0x226ef00,0x226ef00,318,1310,2118,3110,4102,4910,5902,6710,7702,8510,9502,10310,11302,12294,13102,13895,14887,15680,16672,6728,17940,17940,17940,17940,21,20,0,0,0,0,0,0,0x328080,0x850680,0xc58b00,0x1181100,0x16a9700,0x1ab1b80,0x1fda180,0x23e2600,0x290ac00,0x2d13080,0x323b680,0x3643b00,0x3b6c100,0x4094700,0x449cb80,0x489b280,0x4dc3880,0x51c1f80,0x56ea580,0x23be480,0x5d6f300,0x5d6f300,0x5d6f300,0x5d6f300,0x5d6f300,0x5d6f300,0x5d6f300,0x5d6f300,0x5d6f300,0x5d6f300,0x5d6f300,0x5d6f300,319,1311,2119,3111,4103,4911,5903,6711,7703,8511,9503,10311,11303,12295,13103,14080,14888,15681,16673,17411,17941,17941,17941,17941,21,20,0,0,0,0,0,0,0x328b00,0x851100,0xc59580,0x1181b80,0x16aa180,0x1ab2600,0x1fdac00,0x23e3080,0x290b680,0x2d13b00,0x323c100,0x3644580,0x3b6cb80,0x4095180,0x449d600,0x49bbe80,0x4dc4300,0x51c2a00,0x56eb000,0x5ac5580,0x5d6fd80,0x5d6fd80,0x5d6fd80,0x5d6fd80,0x5d6fd80,0x5d6fd80,0x5d6fd80,0x5d6fd80,0x5d6fd80,0x5d6fd80,0x5d6fd80,0x5d6fd80,320,1312,2304,3112,4104,4912,5904,6712,7704,8512,9504,10496,11304,12296,13104,14081,14889,15682,16674,17412,17942,17942,17942,17942,21,20,0,0,0,0,0,0,0x329580,0x851b80,0xd7a180,0x1182600,0x16aac00,0x1ab3080,0x1fdb680,0x23e3b00,0x290c100,0x2d14580,0x323cb80,0x3765180,0x3b6d600,0x4095c00,0x449e080,0x49bc900,0x4dc4d80,0x51c3480,0x56eba80,0x5ac6000,0x5d70800,0x5d70800,0x5d70800,0x5d70800,0x5d70800,0x5d70800,0x5d70800,0x5d70800,0x5d70800,0x5d70800,0x5d70800,0x5d70800,321,1313,2305,3113,4105,4913,5905,6713,7705,8513,9505,10497,11305,12297,13105,14082,14890,15683,16675,6984,6984,6984,6984,6984,20,19,0,0,0,0,0,0,0x32a000,0x852600,0xd7ac00,0x1183080,0x16ab680,0x1ab3b00,0x1fdc100,0x23e4580,0x290cb80,0x2d15000,0x323d600,0x3765c00,0x3b6e080,0x4096680,0x449eb00,0x49bd380,0x4dc5800,0x51c3f00,0x56ec500,0x250da00,0x250da00,0x250da00,0x250da00,0x250da00,0x250da00,0x250da00,0x250da00,0x250da00,0x250da00,0x250da00,0x250da00,0x250da00,322,1314,2306,3114,4106,4914,5906,6714,7706,8514,9506,10498,11306,12298,13106,14083,14891,15684,16676,17413,17943,18233,18233,18233,22,21,0,0,0,0,0,0,0x32aa80,0x853080,0xd7b680,0x1183b00,0x16ac100,0x1ab4580,0x1fdcb80,0x23e5000,0x290d600,0x2d15a80,0x323e080,0x3766680,0x3b6eb00,0x4097100,0x449f580,0x49bde00,0x4dc6280,0x51c4980,0x56ecf80,0x5ac6a80,0x5d71280,0x5ed6d00,0x5ed6d00,0x5ed6d00,0x5ed6d00,0x5ed6d00,0x5ed6d00,0x5ed6d00,0x5ed6d00,0x5ed6d00,0x5ed6d00,0x5ed6d00,323,1315,2307,3115,4107,4915,5907,6715,7707,8515,9507,10499,11307,12299,13107,14084,14892,15685,16677,17414,17944,17944,17944,17944,21,20,0,0,0,0,0,0,0x32b500,0x853b00,0xd7c100,0x1184580,0x16acb80,0x1ab5000,0x1fdd600,0x23e5a80,0x290e080,0x2d16500,0x323eb00,0x3767100,0x3b6f580,0x4097b80,0x44a0000,0x49be880,0x4dc6d00,0x51c5400,0x56eda00,0x5ac7500,0x5d71d00,0x5d71d00,0x5d71d00,0x5d71d00,0x5d71d00,0x5d71d00,0x5d71d00,0x5d71d00,0x5d71d00,0x5d71d00,0x5d71d00,0x5d71d00,324,1316,2308,3116,4108,4916,5908,6716,7708,8516,9508,10500,11308,12300,13108,14085,14893,15686,16678,17415,17945,17945,17945,17945,21,20,0,0,0,0,0,0,0x32bf80,0x854580,0xd7cb80,0x1185000,0x16ad600,0x1ab5a80,0x1fde080,0x23e6500,0x290eb00,0x2d16f80,0x323f580,0x3767b80,0x3b70000,0x4098600,0x44a0a80,0x49bf300,0x4dc7780,0x51c5e80,0x56ee480,0x5ac7f80,0x5d72780,0x5d72780,0x5d72780,0x5d72780,0x5d72780,0x5d72780,0x5d72780,0x5d72780,0x5d72780,0x5d72780,0x5d72780,0x5d72780,325,1317,2309,3117,4109,4917,5909,6717,7709,8517,9509,10501,11309,12301,13109,14086,14894,15687,16679,7240,7240,7240,7240,7240,20,19,0,0,0,0,0,0,0x32ca00,0x855000,0xd7d600,0x1185a80,0x16ae080,0x1ab6500,0x1fdeb00,0x23e6f80,0x290f580,0x2d17a00,0x3240000,0x3768600,0x3b70a80,0x4099080,0x44a1500,0x49bfd80,0x4dc8200,0x51c6900,0x56eef00,0x265cf80,0x265cf80,0x265cf80,0x265cf80,0x265cf80,0x265cf80,0x265cf80,0x265cf80,0x265cf80,0x265cf80,0x265cf80,0x265cf80,0x265cf80,326,1318,2310,3118,4110,4918,5910,6718,7710,8518,9510,10502,11310,12302,13110,14087,14895,15872,16680,7496,17946,17946,17946,17946,21,20,0,0,0,0,0,0,0x32d480,0x855a80,0xd7e080,0x1186500,0x16aeb00,0x1ab6f80,0x1fdf580,0x23e7a00,0x2910000,0x2d18480,0x3240a80,0x3769080,0x3b71500,0x4099b00,0x44a1f80,0x49c0800,0x4dc8c80,0x52e7500,0x56ef980,0x27ac500,0x5d73200,0x5d73200,0x5d73200,0x5d73200,0x5d73200,0x5d73200,0x5d73200,0x5d73200,0x5d73200,0x5d73200,0x5d73200,0x5d73200,327,1319,2311,3119,4111,4919,5911,6719,7711,8519,9511,10503,11311,12303,13111,14088,14896,15873,16681,17416,17947,17947,17947,17947,21,20,0,0,0,0,0,0,0x32df00,0x856500,0xd7eb00,0x1186f80,0x16af580,0x1ab7a00,0x1fe0000,0x23e8480,0x2910a80,0x2d18f00,0x3241500,0x3769b00,0x3b71f80,0x409a580,0x44a2a00,0x49c1280,0x4dc9700,0x52e7f80,0x56f0400,0x5ac8a00,0x5d73c80,0x5d73c80,0x5d73c80,0x5d73c80,0x5d73c80,0x5d73c80,0x5d73c80,0x5d73c80,0x5d73c80,0x5d73c80,0x5d73c80,0x5d73c80,512,1320,2312,3120,4112,4920,5912,6720,7712,8704,9512,10504,11312,12304,13112,14089,14897,15874,16682,17417,17948,17948,17948,17948,21,20,0,0,0,0,0,0,0x44eb00,0x856f80,0xd7f580,0x1187a00,0x16b0000,0x1ab8480,0x1fe0a80,0x23e8f00,0x2911500,0x2e39b00,0x3241f80,0x376a580,0x3b72a00,0x409b000,0x44a3480,0x49c1d00,0x4dca180,0x52e8a00,0x56f0e80,0x5ac9480,0x5d74700,0x5d74700,0x5d74700,0x5d74700,0x5d74700,0x5d74700,0x5d74700,0x5d74700,0x5d74700,0x5d74700,0x5d74700,0x5d74700,513,1321,2313,3121,4113,4921,5913,6721,7713,8705,9513,10505,11313,12305,13113,14090,14898,15875,16683,7752,7752,7752,7752,7752,20,19,0,0,0,0,0,0,0x44f580,0x857a00,0xd80000,0x1188480,0x16b0a80,0x1ab8f00,0x1fe1500,0x23e9980,0x2911f80,0x2e3a580,0x3242a00,0x376b000,0x3b73480,0x409ba80,0x44a3f00,0x49c2780,0x4dcac00,0x52e9480,0x56f1900,0x28fba80,0x28fba80,0x28fba80,0x28fba80,0x28fba80,0x28fba80,0x28fba80,0x28fba80,0x28fba80,0x28fba80,0x28fba80,0x28fba80,0x28fba80,514,1322,2314,3122,4114,4922,5914,6722,7714,8706,9514,10506,11314,12306,13114,14091,14899,15876,16684,17418,17949,18234,18234,18234,22,21,0,0,0,0,0,0,0x450000,0x858480,0xd80a80,0x1188f00,0x16b1500,0x1ab9980,0x1fe1f80,0x23ea400,0x2912a00,0x2e3b000,0x3243480,0x376ba80,0x3b73f00,0x409c500,0x44a4980,0x49c3200,0x4dcb680,0x52e9f00,0x56f2380,0x5ac9f00,0x5d75180,0x5ed7780,0x5ed7780,0x5ed7780,0x5ed7780,0x5ed7780,0x5ed7780,0x5ed7780,0x5ed7780,0x5ed7780,0x5ed7780,0x5ed7780,515,1323,2315,3123,4115,4923,5915,6723,7715,8707,9515,10507,11315,12307,13115,14092,14900,15877,16685,17419,17950,17950,17950,17950,21,20,0,0,0,0,0,0,0x450a80,0x858f00,0xd81500,0x1189980,0x16b1f80,0x1aba400,0x1fe2a00,0x23eae80,0x2913480,0x2e3ba80,0x3243f00,0x376c500,0x3b74980,0x409cf80,0x44a5400,0x49c3c80,0x4dcc100,0x52ea980,0x56f2e00,0x5aca980,0x5d75c00,0x5d75c00,0x5d75c00,0x5d75c00,0x5d75c00,0x5d75c00,0x5d75c00,0x5d75c00,0x5d75c00,0x5d75c00,0x5d75c00,0x5d75c00,516,1324,2316,3124,4116,4924,5916,6724,7716,8708,9516,10508,11316,12308,13116,14093,14901,15878,16686,17420,17951,17951,17951,17951,21,20,0,0,0,0,0,0,0x451500,0x859980,0xd81f80,0x118a400,0x16b2a00,0x1abae80,0x1fe3480,0x23eb900,0x2913f00,0x2e3c500,0x3244980,0x376cf80,0x3b75400,0x409da00,0x44a5e80,0x49c4700,0x4dccb80,0x52eb400,0x56f3880,0x5acb400,0x5d76680,0x5d76680,0x5d76680,0x5d76680,0x5d76680,0x5d76680,0x5d76680,0x5d76680,0x5d76680,0x5d76680,0x5d76680,0x5d76680,517,1325,2317,3125,4117,4925,5917,6725,7717,8709,9517,10509,11317,12309,13117,14094,14902,15879,16687,8008,8008,8008,8008,8008,20,19,0,0,0,0,0,0,0x451f80,0x85a400,0xd82a00,0x118ae80,0x16b3480,0x1abb900,0x1fe3f00,0x23ec380,0x2914980,0x2e3cf80,0x3245400,0x376da00,0x3b75e80,0x409e480,0x44a6900,0x49c5180,0x4dcd600,0x52ebe80,0x56f4300,0x2a4b000,0x2a4b000,0x2a4b000,0x2a4b000,0x2a4b000,0x2a4b000,0x2a4b000,0x2a4b000,0x2a4b000,0x2a4b000,0x2a4b000,0x2a4b000,0x2a4b000,518,1326,2318,3126,4118,4926,5918,6726,7718,8710,9518,10510,11318,12310,13118,14095,14903,15880,16688,8264,17952,17952,17952,17952,21,20,0,0,0,0,0,0,0x452a00,0x85ae80,0xd83480,0x118b900,0x16b3f00,0x1abc380,0x1fe4980,0x23ece00,0x2915400,0x2e3da00,0x3245e80,0x376e480,0x3b76900,0x409ef00,0x44a7380,0x49c5c00,0x4dce080,0x52ec900,0x56f4d80,0x2b9a580,0x5d77100,0x5d77100,0x5d77100,0x5d77100,0x5d77100,0x5d77100,0x5d77100,0x5d77100,0x5d77100,0x5d77100,0x5d77100,0x5d77100,519,1327,2319,3127,4119,4927,5919,6727,7719,8711,9519,10511,11319,12311,13119,14096,14904,15881,16689,17421,17953,17953,17953,17953,21,20,0,0,0,0,0,0,0x453480,0x85b900,0xd83f00,0x118c380,0x16b4980,0x1abce00,0x1fe5400,0x23ed880,0x2915e80,0x2e3e480,0x3246900,0x376ef00,0x3b77380,0x409f980,0x44a7e00,0x49c6680,0x4dceb00,0x52ed380,0x56f5800,0x5acbe80,0x5d77b80,0x5d77b80,0x5d77b80,0x5d77b80,0x5d77b80,0x5d77b80,0x5d77b80,0x5d77b80,0x5d77b80,0x5d77b80,0x5d77b80,0x5d77b80,520,1328,2320,3128,4120,4928,5920,6912,7720,8712,9520,10512,11320,12312,13120,14097,14905,15882,16690,17422,17954,17954,17954,17954,21,20,0,0,0,0,0,0,0x453f00,0x85c380,0xd84980,0x118ce00,0x16b5400,0x1abd880,0x1fe5e80,0x250e480,0x2916900,0x2e3ef00,0x3247380,0x376f980,0x3b77e00,0x40a0400,0x44a8880,0x49c7100,0x4dcf580,0x52ede00,0x56f6280,0x5acc900,0x5d78600,0x5d78600,0x5d78600,0x5d78600,0x5d78600,0x5d78600,0x5d78600,0x5d78600,0x5d78600,0x5d78600,0x5d78600,0x5d78600,521,1329,2321,3129,4121,4929,5921,6913,7721,8713,9521,10513,11321,12313,13121,14098,14906,15883,16691,8520,8520,8520,8520,8520,20,19,0,0,0,0,0,0,0x454980,0x85ce00,0xd85400,0x118d880,0x16b5e80,0x1abe300,0x1fe6900,0x250ef00,0x2917380,0x2e3f980,0x3247e00,0x3770400,0x3b78880,0x40a0e80,0x44a9300,0x49c7b80,0x4dd0000,0x52ee880,0x56f6d00,0x2ce9b00,0x2ce9b00,0x2ce9b00,0x2ce9b00,0x2ce9b00,0x2ce9b00,0x2ce9b00,0x2ce9b00,0x2ce9b00,0x2ce9b00,0x2ce9b00,0x2ce9b00,0x2ce9b00,522,1330,2322,3130,4122,4930,5922,6914,7722,8714,9522,10514,11322,12314,13122,14099,14907,15884,16692,17423,17955,18235,18235,18235,22,21,0,0,0,0,0,0,0x455400,0x85d880,0xd85e80,0x118e300,0x16b6900,0x1abed80,0x1fe7380,0x250f980,0x2917e00,0x2e40400,0x3248880,0x3770e80,0x3b79300,0x40a1900,0x44a9d80,0x49c8600,0x4dd0a80,0x52ef300,0x56f7780,0x5acd380,0x5d79080,0x5ed8200,0x5ed8200,0x5ed8200,0x5ed8200,0x5ed8200,0x5ed8200,0x5ed8200,0x5ed8200,0x5ed8200,0x5ed8200,0x5ed8200,523,1331,2323,3131,4123,4931,5923,6915,7723,8715,9523,10515,11323,12315,13123,14100,14908,15885,16693,17424,17956,17956,17956,17956,21,20,0,0,0,0,0,0,0x455e80,0x85e300,0xd86900,0x118ed80,0x16b7380,0x1abf800,0x1fe7e00,0x2510400,0x2918880,0x2e40e80,0x3249300,0x3771900,0x3b79d80,0x40a2380,0x44aa800,0x49c9080,0x4dd1500,0x52efd80,0x56f8200,0x5acde00,0x5d79b00,0x5d79b00,0x5d79b00,0x5d79b00,0x5d79b00,0x5d79b00,0x5d79b00,0x5d79b00,0x5d79b00,0x5d79b00,0x5d79b00,0x5d79b00,524,1332,2324,3132,4124,4932,5924,6916,7724,8716,9524,10516,11324,12316,13124,14101,14909,15886,16694,17425,17957,17957,17957,17957,21,20,0,0,0,0,0,0,0x456900,0x85ed80,0xd87380,0x118f800,0x16b7e00,0x1ac0280,0x1fe8880,0x2510e80,0x2919300,0x2e41900,0x3249d80,0x3772380,0x3b7a800,0x40a2e00,0x44ab280,0x49c9b00,0x4dd1f80,0x52f0800,0x56f8c80,0x5ace880,0x5d7a580,0x5d7a580,0x5d7a580,0x5d7a580,0x5d7a580,0x5d7a580,0x5d7a580,0x5d7a580,0x5d7a580,0x5d7a580,0x5d7a580,0x5d7a580,525,1333,2325,3133,4125,4933,5925,6917,7725,8717,9525,10517,11325,12317,13125,14102,14910,15887,16695,8776,8776,8776,8776,8776,20,19,0,0,0,0,0,0,0x457380,0x85f800,0xd87e00,0x1190280,0x16b8880,0x1ac0d00,0x1fe9300,0x2511900,0x2919d80,0x2e42380,0x324a800,0x3772e00,0x3b7b280,0x40a3880,0x44abd00,0x49ca580,0x4dd2a00,0x52f1280,0x56f9700,0x2e39080,0x2e39080,0x2e39080,0x2e39080,0x2e39080,0x2e39080,0x2e39080,0x2e39080,0x2e39080,0x2e39080,0x2e39080,0x2e39080,0x2e39080,526,1334,2326,3134,4126,4934,5926,6918,7726,8718,9526,10518,11326,12318,13126,14103,14911,15888,16696,9032,17958,17958,17958,17958,21,20,0,0,0,0,0,0,0x457e00,0x860280,0xd88880,0x1190d00,0x16b9300,0x1ac1780,0x1fe9d80,0x2512380,0x291a800,0x2e42e00,0x324b280,0x3773880,0x3b7bd00,0x40a4300,0x44ac780,0x49cb000,0x4dd3480,0x52f1d00,0x56fa180,0x2f88600,0x5d7b000,0x5d7b000,0x5d7b000,0x5d7b000,0x5d7b000,0x5d7b000,0x5d7b000,0x5d7b000,0x5d7b000,0x5d7b000,0x5d7b000,0x5d7b000,527,1335,2327,3135,4127,4935,5927,6919,7727,8719,9527,10519,11327,12319,13127,14104,14912,15889,16697,17426,17959,17959,17959,17959,21,20,0,0,0,0,0,0,0x458880,0x860d00,0xd89300,0x1191780,0x16b9d80,0x1ac2200,0x1fea800,0x2512e00,0x291b280,0x2e43880,0x324bd00,0x3774300,0x3b7c780,0x40a4d80,0x44ad200,0x49cba80,0x4dd3f00,0x52f2780,0x56fac00,0x5acf300,0x5d7ba80,0x5d7ba80,0x5d7ba80,0x5d7ba80,0x5d7ba80,0x5d7ba80,0x5d7ba80,0x5d7ba80,0x5d7ba80,0x5d7ba80,0x5d7ba80,0x5d7ba80,528,1336,2328,3136,4128,5120,5928,6920,7728,8720,9528,10520,11328,12320,13312,14105,14913,15890,16698,17427,17960,17960,17960,17960,21,20,0,0,0,0,0,0,0x459300,0x861780,0xd89d80,0x1192200,0x16ba800,0x1be2e00,0x1feb280,0x2513880,0x291bd00,0x2e44300,0x324c780,0x3774d80,0x3b7d200,0x40a5800,0x45cde00,0x49cc500,0x4dd4980,0x52f3200,0x56fb680,0x5acfd80,0x5d7c500,0x5d7c500,0x5d7c500,0x5d7c500,0x5d7c500,0x5d7c500,0x5d7c500,0x5d7c500,0x5d7c500,0x5d7c500,0x5d7c500,0x5d7c500,529,1337,2329,3137,4129,5121,5929,6921,7729,8721,9529,10521,11329,12321,13313,14106,14914,15891,16699,9288,9288,9288,9288,9288,20,19,0,0,0,0,0,0,0x459d80,0x862200,0xd8a800,0x1192c80,0x16bb280,0x1be3880,0x1febd00,0x2514300,0x291c780,0x2e44d80,0x324d200,0x3775800,0x3b7dc80,0x40a6280,0x45ce880,0x49ccf80,0x4dd5400,0x52f3c80,0x56fc100,0x30d7b80,0x30d7b80,0x30d7b80,0x30d7b80,0x30d7b80,0x30d7b80,0x30d7b80,0x30d7b80,0x30d7b80,0x30d7b80,0x30d7b80,0x30d7b80,0x30d7b80,530,1338,2330,3138,4130,5122,5930,6922,7730,8722,9530,10522,11330,12322,13314,14107,14915,15892,16700,17428,17961,18236,18236,18236,22,21,0,0,0,0,0,0,0x45a800,0x862c80,0xd8b280,0x1193700,0x16bbd00,0x1be4300,0x1fec780,0x2514d80,0x291d200,0x2e45800,0x324dc80,0x3776280,0x3b7e700,0x40a6d00,0x45cf300,0x49cda00,0x4dd5e80,0x52f4700,0x56fcb80,0x5ad0800,0x5d7cf80,0x5ed8c80,0x5ed8c80,0x5ed8c80,0x5ed8c80,0x5ed8c80,0x5ed8c80,0x5ed8c80,0x5ed8c80,0x5ed8c80,0x5ed8c80,0x5ed8c80,531,1339,2331,3139,4131,5123,5931,6923,7731,8723,9531,10523,11331,12323,13315,14108,14916,15893,16701,17429,17962,17962,17962,17962,21,20,0,0,0,0,0,0,0x45b280,0x863700,0xd8bd00,0x1194180,0x16bc780,0x1be4d80,0x1fed200,0x2515800,0x291dc80,0x2e46280,0x324e700,0x3776d00,0x3b7f180,0x40a7780,0x45cfd80,0x49ce480,0x4dd6900,0x52f5180,0x56fd600,0x5ad1280,0x5d7da00,0x5d7da00,0x5d7da00,0x5d7da00,0x5d7da00,0x5d7da00,0x5d7da00,0x5d7da00,0x5d7da00,0x5d7da00,0x5d7da00,0x5d7da00,532,1340,2332,3140,4132,5124,5932,6924,7732,8724,9532,10524,11332,12324,13316,14109,14917,15894,16702,17430,17963,17963,17963,17963,21,20,0,0,0,0,0,0,0x45bd00,0x864180,0xd8c780,0x1194c00,0x16bd200,0x1be5800,0x1fedc80,0x2516280,0x291e700,0x2e46d00,0x324f180,0x3777780,0x3b7fc00,0x40a8200,0x45d0800,0x49cef00,0x4dd7380,0x52f5c00,0x56fe080,0x5ad1d00,0x5d7e480,0x5d7e480,0x5d7e480,0x5d7e480,0x5d7e480,0x5d7e480,0x5d7e480,0x5d7e480,0x5d7e480,0x5d7e480,0x5d7e480,0x5d7e480,533,1341,2333,3141,4133,5125,5933,6925,7733,8725,9533,10525,11333,12325,13317,14110,14918,15895,16703,9544,9544,9544,9544,9544,20,19,0,0,0,0,0,0,0x45c780,0x864c00,0xd8d200,0x1195680,0x16bdc80,0x1be6280,0x1fee700,0x2516d00,0x291f180,0x2e47780,0x324fc00,0x3778200,0x3b80680,0x40a8c80,0x45d1280,0x49cf980,0x4dd7e00,0x52f6680,0x56feb00,0x3227100,0x3227100,0x3227100,0x3227100,0x3227100,0x3227100,0x3227100,0x3227100,0x3227100,0x3227100,0x3227100,0x3227100,0x3227100,534,1342,2334,3142,4134,5126,5934,6926,7734,8726,9534,10526,11334,12326,13318,14111,14919,15896,16704,9800,17964,17964,17964,17964,21,20,0,0,0,0,0,0,0x45d200,0x865680,0xd8dc80,0x1196100,0x16be700,0x1be6d00,0x1fef180,0x2517780,0x291fc00,0x2e48200,0x3250680,0x3778c80,0x3b81100,0x40a9700,0x45d1d00,0x49d0400,0x4dd8880,0x52f7100,0x56ff580,0x3376680,0x5d7ef00,0x5d7ef00,0x5d7ef00,0x5d7ef00,0x5d7ef00,0x5d7ef00,0x5d7ef00,0x5d7ef00,0x5d7ef00,0x5d7ef00,0x5d7ef00,0x5d7ef00,535,1343,2335,3143,4135,5127,5935,6927,7735,8727,9535,10527,11335,12327,13319,14112,15104,15897,16705,17431,17965,17965,17965,17965,21,20,0,0,0,0,0,0,0x45dc80,0x866100,0xd8e700,0x1196b80,0x16bf180,0x1be7780,0x1fefc00,0x2518200,0x2920680,0x2e48c80,0x3251100,0x3779700,0x3b81b80,0x40aa180,0x45d2780,0x49d0e80,0x4ef9480,0x52f7b80,0x5700000,0x5ad2780,0x5d7f980,0x5d7f980,0x5d7f980,0x5d7f980,0x5d7f980,0x5d7f980,0x5d7f980,0x5d7f980,0x5d7f980,0x5d7f980,0x5d7f980,0x5d7f980,536,1344,2336,3328,4136,5128,5936,6928,7736,8728,9536,10528,11520,12328,13320,14113,15105,15898,16706,17432,17966,17966,17966,17966,21,20,0,0,0,0,0,0,0x45e700,0x866b80,0xd8f180,0x12b7780,0x16bfc00,0x1be8200,0x1ff0680,0x2518c80,0x2921100,0x2e49700,0x3251b80,0x377a180,0x3ca2780,0x40aac00,0x45d3200,0x49d1900,0x4ef9f00,0x52f8600,0x5700a80,0x5ad3200,0x5d80400,0x5d80400,0x5d80400,0x5d80400,0x5d80400,0x5d80400,0x5d80400,0x5d80400,0x5d80400,0x5d80400,0x5d80400,0x5d80400,537,1345,2337,3329,4137,5129,5937,6929,7737,8729,9537,10529,11521,12329,13321,14114,15106,15899,16707,10056,10056,10056,10056,10056,20,19,0,0,0,0,0,0,0x45f180,0x867600,0xd8fc00,0x12b8200,0x16c0680,0x1be8c80,0x1ff1100,0x2519700,0x2921b80,0x2e4a180,0x3252600,0x377ac00,0x3ca3200,0x40ab680,0x45d3c80,0x49d2380,0x4efa980,0x52f9080,0x5701500,0x34c5c00,0x34c5c00,0x34c5c00,0x34c5c00,0x34c5c00,0x34c5c00,0x34c5c00,0x34c5c00,0x34c5c00,0x34c5c00,0x34c5c00,0x34c5c00,0x34c5c00,538,1346,2338,3330,4138,5130,5938,6930,7738,8730,9538,10530,11522,12330,13322,14115,15107,15900,16708,17433,17967,18237,18237,18237,22,21,0,0,0,0,0,0,0x45fc00,0x868080,0xd90680,0x12b8c80,0x16c1100,0x1be9700,0x1ff1b80,0x251a180,0x2922600,0x2e4ac00,0x3253080,0x377b680,0x3ca3c80,0x40ac100,0x45d4700,0x49d2e00,0x4efb400,0x52f9b00,0x5701f80,0x5ad3c80,0x5d80e80,0x5ed9700,0x5ed9700,0x5ed9700,0x5ed9700,0x5ed9700,0x5ed9700,0x5ed9700,0x5ed9700,0x5ed9700,0x5ed9700,0x5ed9700,539,1347,2339,3331,4139,5131,5939,6931,7739,8731,9539,10531,11523,12331,13323,14116,15108,15901,16709,17434,17968,17968,17968,17968,21,20,0,0,0,0,0,0,0x460680,0x868b00,0xd91100,0x12b9700,0x16c1b80,0x1bea180,0x1ff2600,0x251ac00,0x2923080,0x2e4b680,0x3253b00,0x377c100,0x3ca4700,0x40acb80,0x45d5180,0x49d3880,0x4efbe80,0x52fa580,0x5702a00,0x5ad4700,0x5d81900,0x5d81900,0x5d81900,0x5d81900,0x5d81900,0x5d81900,0x5d81900,0x5d81900,0x5d81900,0x5d81900,0x5d81900,0x5d81900,540,1348,2340,3332,4140,5132,5940,6932,7740,8732,9540,10532,11524,12332,13324,14117,15109,15902,16710,17435,17969,17969,17969,17969,21,20,0,0,0,0,0,0,0x461100,0x869580,0xd91b80,0x12ba180,0x16c2600,0x1beac00,0x1ff3080,0x251b680,0x2923b00,0x2e4c100,0x3254580,0x377cb80,0x3ca5180,0x40ad600,0x45d5c00,0x49d4300,0x4efc900,0x52fb000,0x5703480,0x5ad5180,0x5d82380,0x5d82380,0x5d82380,0x5d82380,0x5d82380,0x5d82380,0x5d82380,0x5d82380,0x5d82380,0x5d82380,0x5d82380,0x5d82380,541,1349,2341,3333,4141,5133,5941,6933,7741,8733,9541,10533,11525,12333,13325,14118,15110,15903,16711,10312,10312,10312,10312,10312,20,19,0,0,0,0,0,0,0x461b80,0x86a000,0xd92600,0x12bac00,0x16c3080,0x1beb680,0x1ff3b00,0x251c100,0x2924580,0x2e4cb80,0x3255000,0x377d600,0x3ca5c00,0x40ae080,0x45d6680,0x49d4d80,0x4efd380,0x52fba80,0x5703f00,0x3615180,0x3615180,0x3615180,0x3615180,0x3615180,0x3615180,0x3615180,0x3615180,0x3615180,0x3615180,0x3615180,0x3615180,0x3615180,542,1350,2342,3334,4142,5134,5942,6934,7742,8734,9542,10534,11526,12334,13326,14119,15111,15904,16896,10568,17970,17970,17970,17970,21,20,0,0,0,0,0,0,0x462600,0x86aa80,0xd93080,0x12bb680,0x16c3b00,0x1bec100,0x1ff4580,0x251cb80,0x2925000,0x2e4d600,0x3255a80,0x377e080,0x3ca6680,0x40aeb00,0x45d7100,0x49d5800,0x4efde00,0x52fc500,0x5824b00,0x3764700,0x5d82e00,0x5d82e00,0x5d82e00,0x5d82e00,0x5d82e00,0x5d82e00,0x5d82e00,0x5d82e00,0x5d82e00,0x5d82e00,0x5d82e00,0x5d82e00,543,1351,2343,3335,4143,5135,5943,6935,7743,8735,9543,10535,11527,12335,13327,14120,15112,15905,16897,17436,17971,17971,17971,17971,21,20,0,0,0,0,0,0,0x463080,0x86b500,0xd93b00,0x12bc100,0x16c4580,0x1becb80,0x1ff5000,0x251d600,0x2925a80,0x2e4e080,0x3256500,0x377eb00,0x3ca7100,0x40af580,0x45d7b80,0x49d6280,0x4efe880,0x52fcf80,0x5825580,0x5ad5c00,0x5d83880,0x5d83880,0x5d83880,0x5d83880,0x5d83880,0x5d83880,0x5d83880,0x5d83880,0x5d83880,0x5d83880,0x5d83880,0x5d83880,544,1536,2344,3336,4144,5136,5944,6936,7744,8736,9728,10536,11528,12336,13328,14121,15113,15906,16898,17437,17972,17972,17972,17972,21,20,0,0,0,0,0,0,0x463b00,0x98c100,0xd94580,0x12bcb80,0x16c5000,0x1bed600,0x1ff5a80,0x251e080,0x2926500,0x2e4eb00,0x3377100,0x377f580,0x3ca7b80,0x40b0000,0x45d8600,0x49d6d00,0x4eff300,0x52fda00,0x5826000,0x5ad6680,0x5d84300,0x5d84300,0x5d84300,0x5d84300,0x5d84300,0x5d84300,0x5d84300,0x5d84300,0x5d84300,0x5d84300,0x5d84300,0x5d84300,545,1537,2345,3337,4145,5137,5945,6937,7745,8737,9729,10537,11529,12337,13329,14122,15114,15907,16899,10824,10824,10824,10824,10824,20,19,0,0,0,0,0,0,0x464580,0x98cb80,0xd95000,0x12bd600,0x16c5a80,0x1bee080,0x1ff6500,0x251eb00,0x2926f80,0x2e4f580,0x3377b80,0x3780000,0x3ca8600,0x40b0a80,0x45d9080,0x49d7780,0x4effd80,0x52fe480,0x5826a80,0x38b3c80,0x38b3c80,0x38b3c80,0x38b3c80,0x38b3c80,0x38b3c80,0x38b3c80,0x38b3c80,0x38b3c80,0x38b3c80,0x38b3c80,0x38b3c80,0x38b3c80,546,1538,2346,3338,4146,5138,5946,6938,7746,8738,9730,10538,11530,12338,13330,14123,15115,15908,16900,17438,17973,18238,18238,18238,22,21,0,0,0,0,0,0,0x465000,0x98d600,0xd95a80,0x12be080,0x16c6500,0x1beeb00,0x1ff6f80,0x251f580,0x2927a00,0x2e50000,0x3378600,0x3780a80,0x3ca9080,0x40b1500,0x45d9b00,0x49d8200,0x4f00800,0x52fef00,0x5827500,0x5ad7100,0x5d84d80,0x5eda180,0x5eda180,0x5eda180,0x5eda180,0x5eda180,0x5eda180,0x5eda180,0x5eda180,0x5eda180,0x5eda180,0x5eda180,547,1539,2347,3339,4147,5139,5947,6939,7747,8739,9731,10539,11531,12339,13331,14124,15116,15909,16901,17439,17974,17974,17974,17974,21,20,0,0,0,0,0,0,0x465a80,0x98e080,0xd96500,0x12beb00,0x16c6f80,0x1bef580,0x1ff7a00,0x2520000,0x2928480,0x2e50a80,0x3379080,0x3781500,0x3ca9b00,0x40b1f80,0x45da580,0x49d8c80,0x4f01280,0x52ff980,0x5827f80,0x5ad7b80,0x5d85800,0x5d85800,0x5d85800,0x5d85800,0x5d85800,0x5d85800,0x5d85800,0x5d85800,0x5d85800,0x5d85800,0x5d85800,0x5d85800,548,1540,2348,3340,4148,5140,5948,6940,7748,8740,9732,10540,11532,12340,13332,14125,15117,15910,16902,17440,17975,17975,17975,17975,21,20,0,0,0,0,0,0,0x466500,0x98eb00,0xd96f80,0x12bf580,0x16c7a00,0x1bf0000,0x1ff8480,0x2520a80,0x2928f00,0x2e51500,0x3379b00,0x3781f80,0x3caa580,0x40b2a00,0x45db000,0x49d9700,0x4f01d00,0x5300400,0x5828a00,0x5ad8600,0x5d86280,0x5d86280,0x5d86280,0x5d86280,0x5d86280,0x5d86280,0x5d86280,0x5d86280,0x5d86280,0x5d86280,0x5d86280,0x5d86280,549,1541,2349,3341,4149,5141,5949,6941,7749,8741,9733,10541,11533,12341,13333,14126,15118,15911,16903,11080,11080,11080,11080,11080,20,19,0,0,0,0,0,0,0x466f80,0x98f580,0xd97a00,0x12c0000,0x16c8480,0x1bf0a80,0x1ff8f00,0x2521500,0x2929980,0x2e51f80,0x337a580,0x3782a00,0x3cab000,0x40b3480,0x45dba80,0x49da180,0x4f02780,0x5300e80,0x5829480,0x3a03200,0x3a03200,0x3a03200,0x3a03200,0x3a03200,0x3a03200,0x3a03200,0x3a03200,0x3a03200,0x3a03200,0x3a03200,0x3a03200,0x3a03200,550,1542,2350,3342,4150,5142,5950,6942,7750,8742,9734,10542,11534,12342,13334,14127,15119,15912,16904,11336,17976,17976,17976,17976,21,20,0,0,0,0,0,0,0x467a00,0x990000,0xd98480,0x12c0a80,0x16c8f00,0x1bf1500,0x1ff9980,0x2521f80,0x292a400,0x2e52a00,0x337b000,0x3783480,0x3caba80,0x40b3f00,0x45dc500,0x49dac00,0x4f03200,0x5301900,0x5829f00,0x3b52780,0x5d86d00,0x5d86d00,0x5d86d00,0x5d86d00,0x5d86d00,0x5d86d00,0x5d86d00,0x5d86d00,0x5d86d00,0x5d86d00,0x5d86d00,0x5d86d00,551,1543,2351,3343,4151,5143,5951,6943,7751,8743,9735,10543,11535,12343,13335,14128,15120,15913,16905,17441,17977,17977,17977,17977,21,20,0,0,0,0,0,0,0x468480,0x990a80,0xd98f00,0x12c1500,0x16c9980,0x1bf1f80,0x1ffa400,0x2522a00,0x292ae80,0x2e53480,0x337ba80,0x3783f00,0x3cac500,0x40b4980,0x45dcf80,0x49db680,0x4f03c80,0x5302380,0x582a980,0x5ad9080,0x5d87780,0x5d87780,0x5d87780,0x5d87780,0x5d87780,0x5d87780,0x5d87780,0x5d87780,0x5d87780,0x5d87780,0x5d87780,0x5d87780,552,1544,2352,3344,4152,5144,5952,6944,7936,8744,9736,10544,11536,12344,13336,14129,15121,15914,16906,17442,17978,17978,17978,17978,21,20,0,0,0,0,0,0,0x468f00,0x991500,0xd99980,0x12c1f80,0x16ca400,0x1bf2a00,0x1ffae80,0x2523480,0x2a4ba80,0x2e53f00,0x337c500,0x3784980,0x3cacf80,0x40b5400,0x45dda00,0x49dc100,0x4f04700,0x5302e00,0x582b400,0x5ad9b00,0x5d88200,0x5d88200,0x5d88200,0x5d88200,0x5d88200,0x5d88200,0x5d88200,0x5d88200,0x5d88200,0x5d88200,0x5d88200,0x5d88200,553,1545,2353,3345,4153,5145,5953,6945,7937,8745,9737,10545,11537,12345,13337,14130,15122,15915,16907,11592,11592,11592,11592,11592,20,19,0,0,0,0,0,0,0x469980,0x991f80,0xd9a400,0x12c2a00,0x16cae80,0x1bf3480,0x1ffb900,0x2523f00,0x2a4c500,0x2e54980,0x337cf80,0x3785400,0x3cada00,0x40b5e80,0x45de480,0x49dcb80,0x4f05180,0x5303880,0x582be80,0x3ca1d00,0x3ca1d00,0x3ca1d00,0x3ca1d00,0x3ca1d00,0x3ca1d00,0x3ca1d00,0x3ca1d00,0x3ca1d00,0x3ca1d00,0x3ca1d00,0x3ca1d00,0x3ca1d00,554,1546,2354,3346,4154,5146,5954,6946,7938,8746,9738,10546,11538,12346,13338,14131,15123,15916,16908,17443,17979,18239,18239,18239,22,21,0,0,0,0,0,0,0x46a400,0x992a00,0xd9ae80,0x12c3480,0x16cb900,0x1bf3f00,0x1ffc380,0x2524980,0x2a4cf80,0x2e55400,0x337da00,0x3785e80,0x3cae480,0x40b6900,0x45def00,0x49dd600,0x4f05c00,0x5304300,0x582c900,0x5ada580,0x5d88c80,0x5edac00,0x5edac00,0x5edac00,0x5edac00,0x5edac00,0x5edac00,0x5edac00,0x5edac00,0x5edac00,0x5edac00,0x5edac00,555,1547,2355,3347,4155,5147,5955,6947,7939,8747,9739,10547,11539,12347,13339,14132,15124,15917,16909,17444,17980,17980,17980,17980,21,20,0,0,0,0,0,0,0x46ae80,0x993480,0xd9b900,0x12c3f00,0x16cc380,0x1bf4980,0x1ffce00,0x2525400,0x2a4da00,0x2e55e80,0x337e480,0x3786900,0x3caef00,0x40b7380,0x45df980,0x49de080,0x4f06680,0x5304d80,0x582d380,0x5adb000,0x5d89700,0x5d89700,0x5d89700,0x5d89700,0x5d89700,0x5d89700,0x5d89700,0x5d89700,0x5d89700,0x5d89700,0x5d89700,0x5d89700,556,1548,2356,3348,4156,5148,5956,6948,7940,8748,9740,10548,11540,12348,13340,14133,15125,15918,16910,17445,17981,17981,17981,17981,21,20,0,0,0,0,0,0,0x46b900,0x993f00,0xd9c380,0x12c4980,0x16cce00,0x1bf5400,0x1ffd880,0x2525e80,0x2a4e480,0x2e56900,0x337ef00,0x3787380,0x3caf980,0x40b7e00,0x45e0400,0x49deb00,0x4f07100,0x5305800,0x582de00,0x5adba80,0x5d8a180,0x5d8a180,0x5d8a180,0x5d8a180,0x5d8a180,0x5d8a180,0x5d8a180,0x5d8a180,0x5d8a180,0x5d8a180,0x5d8a180,0x5d8a180,557,1549,2357,3349,4157,5149,5957,6949,7941,8749,9741,10549,11541,12349,13341,14134,15126,15919,16911,11848,11848,11848,11848,11848,20,19,0,0,0,0,0,0,0x46c380,0x994980,0xd9ce00,0x12c5400,0x16cd880,0x1bf5e80,0x1ffe300,0x2526900,0x2a4ef00,0x2e57380,0x337f980,0x3787e00,0x3cb0400,0x40b8880,0x45e0e80,0x49df580,0x4f07b80,0x5306280,0x582e880,0x3df1280,0x3df1280,0x3df1280,0x3df1280,0x3df1280,0x3df1280,0x3df1280,0x3df1280,0x3df1280,0x3df1280,0x3df1280,0x3df1280,0x3df1280,558,1550,2358,3350,4158,5150,5958,6950,7942,8750,9742,10550,11542,12350,13342,14135,15127,15920,16912,12104,17982,17982,17982,17982,21,20,0,0,0,0,0,0,0x46ce00,0x995400,0xd9d880,0x12c5e80,0x16ce300,0x1bf6900,0x1ffed80,0x2527380,0x2a4f980,0x2e57e00,0x3380400,0x3788880,0x3cb0e80,0x40b9300,0x45e1900,0x49e0000,0x4f08600,0x5306d00,0x582f300,0x3f40800,0x5d8ac00,0x5d8ac00,0x5d8ac00,0x5d8ac00,0x5d8ac00,0x5d8ac00,0x5d8ac00,0x5d8ac00,0x5d8ac00,0x5d8ac00,0x5d8ac00,0x5d8ac00,559,1551,2359,3351,4159,5151,5959,6951,7943,8751,9743,10551,11543,12351,13343,14136,15128,15921,16913,17446,17983,17983,17983,17983,21,20,0,0,0,0,0,0,0x46d880,0x995e80,0xd9e300,0x12c6900,0x16ced80,0x1bf7380,0x1fff800,0x2527e00,0x2a50400,0x2e58880,0x3380e80,0x3789300,0x3cb1900,0x40b9d80,0x45e2380,0x49e0a80,0x4f09080,0x5307780,0x582fd80,0x5adc500,0x5d8b680,0x5d8b680,0x5d8b680,0x5d8b680,0x5d8b680,0x5d8b680,0x5d8b680,0x5d8b680,0x5d8b680,0x5d8b680,0x5d8b680,0x5d8b680,560,1552,2360,3352,4160,5152,6144,6952,7944,8752,9744,10552,11544,12352,13344,14137,15129,15922,16914,17447,17984,17984,17984,17984,21,20,0,0,0,0,0,0,0x46e300,0x996900,0xd9ed80,0x12c7380,0x16cf800,0x1bf7e00,0x2120400,0x2528880,0x2a50e80,0x2e59300,0x3381900,0x3789d80,0x3cb2380,0x40ba800,0x45e2e00,0x49e1500,0x4f09b00,0x5308200,0x5830800,0x5adcf80,0x5d8c100,0x5d8c100,0x5d8c100,0x5d8c100,0x5d8c100,0x5d8c100,0x5d8c100,0x5d8c100,0x5d8c100,0x5d8c100,0x5d8c100,0x5d8c100,561,1553,2361,3353,4161,5153,6145,6953,7945,8753,9745,10553,11545,12353,13345,14138,15130,15923,16915,12360,12360,12360,12360,12360,20,19,0,0,0,0,0,0,0x46ed80,0x997380,0xd9f800,0x12c7e00,0x16d0280,0x1bf8880,0x2120e80,0x2529300,0x2a51900,0x2e59d80,0x3382380,0x378a800,0x3cb2e00,0x40bb280,0x45e3880,0x49e1f80,0x4f0a580,0x5308c80,0x5831280,0x408fd80,0x408fd80,0x408fd80,0x408fd80,0x408fd80,0x408fd80,0x408fd80,0x408fd80,0x408fd80,0x408fd80,0x408fd80,0x408fd80,0x408fd80,562,1554,2362,3354,4162,5154,6146,6954,7946,8754,9746,10554,11546,12354,13346,14139,15131,15924,16916,17448,17985,18240,18240,18240,22,21,0,0,0,0,0,0,0x46f800,0x997e00,0xda0280,0x12c8880,0x16d0d00,0x1bf9300,0x2121900,0x2529d80,0x2a52380,0x2e5a800,0x3382e00,0x378b280,0x3cb3880,0x40bbd00,0x45e4300,0x49e2a00,0x4f0b000,0x5309700,0x5831d00,0x5adda00,0x5d8cb80,0x5edb680,0x5edb680,0x5edb680,0x5edb680,0x5edb680,0x5edb680,0x5edb680,0x5edb680,0x5edb680,0x5edb680,0x5edb680,563,1555,2363,3355,4163,5155,6147,6955,7947,8755,9747,10555,11547,12355,13347,14140,15132,15925,16917,17449,17986,17986,17986,17986,21,20,0,0,0,0,0,0,0x470280,0x998880,0xda0d00,0x12c9300,0x16d1780,0x1bf9d80,0x2122380,0x252a800,0x2a52e00,0x2e5b280,0x3383880,0x378bd00,0x3cb4300,0x40bc780,0x45e4d80,0x49e3480,0x4f0ba80,0x530a180,0x5832780,0x5ade480,0x5d8d600,0x5d8d600,0x5d8d600,0x5d8d600,0x5d8d600,0x5d8d600,0x5d8d600,0x5d8d600,0x5d8d600,0x5d8d600,0x5d8d600,0x5d8d600,564,1556,2364,3356,4164,5156,6148,6956,7948,8756,9748,10556,11548,12356,13348,14141,15133,15926,16918,17450,17987,17987,17987,17987,21,20,0,0,0,0,0,0,0x470d00,0x999300,0xda1780,0x12c9d80,0x16d2200,0x1bfa800,0x2122e00,0x252b280,0x2a53880,0x2e5bd00,0x3384300,0x378c780,0x3cb4d80,0x40bd200,0x45e5800,0x49e3f00,0x4f0c500,0x530ac00,0x5833200,0x5adef00,0x5d8e080,0x5d8e080,0x5d8e080,0x5d8e080,0x5d8e080,0x5d8e080,0x5d8e080,0x5d8e080,0x5d8e080,0x5d8e080,0x5d8e080,0x5d8e080,565,1557,2365,3357,4165,5157,6149,6957,7949,8757,9749,10557,11549,12357,13349,14142,15134,15927,16919,12616,12616,12616,12616,12616,20,19,0,0,0,0,0,0,0x471780,0x999d80,0xda2200,0x12ca800,0x16d2c80,0x1bfb280,0x2123880,0x252bd00,0x2a54300,0x2e5c780,0x3384d80,0x378d200,0x3cb5800,0x40bdc80,0x45e6280,0x49e4980,0x4f0cf80,0x530b680,0x5833c80,0x41df300,0x41df300,0x41df300,0x41df300,0x41df300,0x41df300,0x41df300,0x41df300,0x41df300,0x41df300,0x41df300,0x41df300,0x41df300,566,1558,2366,3358,4166,5158,6150,6958,7950,8758,9750,10558,11550,12358,13350,14143,15135,15928,16920,12872,17988,17988,17988,17988,21,20,0,0,0,0,0,0,0x472200,0x99a800,0xda2c80,0x12cb280,0x16d3700,0x1bfbd00,0x2124300,0x252c780,0x2a54d80,0x2e5d200,0x3385800,0x378dc80,0x3cb6280,0x40be700,0x45e6d00,0x49e5400,0x4f0da00,0x530c100,0x5834700,0x432e880,0x5d8eb00,0x5d8eb00,0x5d8eb00,0x5d8eb00,0x5d8eb00,0x5d8eb00,0x5d8eb00,0x5d8eb00,0x5d8eb00,0x5d8eb00,0x5d8eb00,0x5d8eb00,567,1559,2367,3359,4167,5159,6151,6959,7951,8759,9751,10559,11551,12359,13351,14144,15136,15929,16921,17451,17989,17989,17989,17989,21,20,0,0,0,0,0,0,0x472c80,0x99b280,0xda3700,0x12cbd00,0x16d4180,0x1bfc780,0x2124d80,0x252d200,0x2a55800,0x2e5dc80,0x3386280,0x378e700,0x3cb6d00,0x40bf180,0x45e7780,0x49e5e80,0x4f0e480,0x530cb80,0x5835180,0x5adf980,0x5d8f580,0x5d8f580,0x5d8f580,0x5d8f580,0x5d8f580,0x5d8f580,0x5d8f580,0x5d8f580,0x5d8f580,0x5d8f580,0x5d8f580,0x5d8f580,568,1560,2368,3360,4352,5160,6152,6960,7952,8760,9752,10560,11552,12544,13352,14145,15137,15930,16922,17452,17990,17990,17990,17990,21,20,0,0,0,0,0,0,0x473700,0x99bd00,0xda4180,0x12cc780,0x17f4d80,0x1bfd200,0x2125800,0x252dc80,0x2a56280,0x2e5e700,0x3386d00,0x378f180,0x3cb7780,0x41dfd80,0x45e8200,0x49e6900,0x4f0ef00,0x530d600,0x5835c00,0x5ae0400,0x5d90000,0x5d90000,0x5d90000,0x5d90000,0x5d90000,0x5d90000,0x5d90000,0x5d90000,0x5d90000,0x5d90000,0x5d90000,0x5d90000,569,1561,2369,3361,4353,5161,6153,6961,7953,8761,9753,10561,11553,12545,13353,14146,15138,15931,16923,13128,13128,13128,13128,13128,20,19,0,0,0,0,0,0,0x474180,0x99c780,0xda4c00,0x12cd200,0x17f5800,0x1bfdc80,0x2126280,0x252e700,0x2a56d00,0x2e5f180,0x3387780,0x378fc00,0x3cb8200,0x41e0800,0x45e8c80,0x49e7380,0x4f0f980,0x530e080,0x5836680,0x447de00,0x447de00,0x447de00,0x447de00,0x447de00,0x447de00,0x447de00,0x447de00,0x447de00,0x447de00,0x447de00,0x447de00,0x447de00,570,1562,2370,3362,4354,5162,6154,6962,7954,8762,9754,10562,11554,12546,13354,14147,15139,15932,16924,17453,17991,18241,18241,18241,22,21,0,0,0,0,0,0,0x474c00,0x99d200,0xda5680,0x12cdc80,0x17f6280,0x1bfe700,0x2126d00,0x252f180,0x2a57780,0x2e5fc00,0x3388200,0x3790680,0x3cb8c80,0x41e1280,0x45e9700,0x49e7e00,0x4f10400,0x530eb00,0x5837100,0x5ae0e80,0x5d90a80,0x5edc100,0x5edc100,0x5edc100,0x5edc100,0x5edc100,0x5edc100,0x5edc100,0x5edc100,0x5edc100,0x5edc100,0x5edc100,571,1563,2371,3363,4355,5163,6155,6963,7955,8763,9755,10563,11555,12547,13355,14148,15140,15933,16925,17454,18176,18176,18176,18176,21,20,0,0,0,0,0,0,0x475680,0x99dc80,0xda6100,0x12ce700,0x17f6d00,0x1bff180,0x2127780,0x252fc00,0x2a58200,0x2e60680,0x3388c80,0x3791100,0x3cb9700,0x41e1d00,0x45ea180,0x49e8880,0x4f10e80,0x530f580,0x5837b80,0x5ae1900,0x5eb1680,0x5eb1680,0x5eb1680,0x5eb1680,0x5eb1680,0x5eb1680,0x5eb1680,0x5eb1680,0x5eb1680,0x5eb1680,0x5eb1680,0x5eb1680,572,1564,2372,3364,4356,5164,6156,6964,7956,8764,9756,10564,11556,12548,13356,14149,15141,15934,16926,17455,18177,18177,18177,18177,21,20,0,0,0,0,0,0,0x476100,0x99e700,0xda6b80,0x12cf180,0x17f7780,0x1bffc00,0x2128200,0x2530680,0x2a58c80,0x2e61100,0x3389700,0x3791b80,0x3cba180,0x41e2780,0x45eac00,0x49e9300,0x4f11900,0x5310000,0x5838600,0x5ae2380,0x5eb2100,0x5eb2100,0x5eb2100,0x5eb2100,0x5eb2100,0x5eb2100,0x5eb2100,0x5eb2100,0x5eb2100,0x5eb2100,0x5eb2100,0x5eb2100,573,1565,2373,3365,4357,5165,6157,6965,7957,8765,9757,10565,11557,12549,13357,14150,15142,15935,16927,13384,13384,13384,13384,13384,20,19,0,0,0,0,0,0,0x476b80,0x99f180,0xda7600,0x12cfc00,0x17f8200,0x1c00680,0x2128c80,0x2531100,0x2a59700,0x2e61b80,0x338a180,0x3792600,0x3cbac00,0x41e3200,0x45eb680,0x49e9d80,0x4f12380,0x5310a80,0x5839080,0x45cd380,0x45cd380,0x45cd380,0x45cd380,0x45cd380,0x45cd380,0x45cd380,0x45cd380,0x45cd380,0x45cd380,0x45cd380,0x45cd380,0x45cd380,574,1566,2374,3366,4358,5166,6158,6966,7958,8766,9758,10566,11558,12550,13358,14151,15143,15936,16928,13640,18178,18178,18178,18178,21,20,0,0,0,0,0,0,0x477600,0x99fc00,0xda8080,0x12d0680,0x17f8c80,0x1c01100,0x2129700,0x2531b80,0x2a5a180,0x2e62600,0x338ac00,0x3793080,0x3cbb680,0x41e3c80,0x45ec100,0x49ea800,0x4f12e00,0x5311500,0x5839b00,0x471c900,0x5eb2b80,0x5eb2b80,0x5eb2b80,0x5eb2b80,0x5eb2b80,0x5eb2b80,0x5eb2b80,0x5eb2b80,0x5eb2b80,0x5eb2b80,0x5eb2b80,0x5eb2b80,575,1567,2375,3367,4359,5167,6159,6967,7959,8767,9759,10567,11559,12551,13359,14336,15144,15937,16929,17456,18179,18179,18179,18179,21,20,0,0,0,0,0,0,0x478080,0x9a0680,0xda8b00,0x12d1100,0x17f9700,0x1c01b80,0x212a180,0x2532600,0x2a5ac00,0x2e63080,0x338b680,0x3793b00,0x3cbc100,0x41e4700,0x45ecb80,0x4b0b400,0x4f13880,0x5311f80,0x583a580,0x5ae2e00,0x5eb3600,0x5eb3600,0x5eb3600,0x5eb3600,0x5eb3600,0x5eb3600,0x5eb3600,0x5eb3600,0x5eb3600,0x5eb3600,0x5eb3600,0x5eb3600,576,1568,2560,3368,4360,5168,6160,6968,7960,8768,9760,10752,11560,12552,13360,14337,15145,15938,16930,17457,18180,18180,18180,18180,21,20,0,0,0,0,0,0,0x478b00,0x9a1100,0xec9700,0x12d1b80,0x17fa180,0x1c02600,0x212ac00,0x2533080,0x2a5b680,0x2e63b00,0x338c100,0x38b4700,0x3cbcb80,0x41e5180,0x45ed600,0x4b0be80,0x4f14300,0x5312a00,0x583b000,0x5ae3880,0x5eb4080,0x5eb4080,0x5eb4080,0x5eb4080,0x5eb4080,0x5eb4080,0x5eb4080,0x5eb4080,0x5eb4080,0x5eb4080,0x5eb4080,0x5eb4080,577,1569,2561,3369,4361,5169,6161,6969,7961,8769,9761,10753,11561,12553,13361,14338,15146,15939,16931,13896,13896,13896,13896,13896,20,19,0,0,0,0,0,0,0x479580,0x9a1b80,0xeca180,0x12d2600,0x17fac00,0x1c03080,0x212b680,0x2533b00,0x2a5c100,0x2e64580,0x338cb80,0x38b5180,0x3cbd600,0x41e5c00,0x45ee080,0x4b0c900,0x4f14d80,0x5313480,0x583ba80,0x486be80,0x486be80,0x486be80,0x486be80,0x486be80,0x486be80,0x486be80,0x486be80,0x486be80,0x486be80,0x486be80,0x486be80,0x486be80,578,1570,2562,3370,4362,5170,6162,6970,7962,8770,9762,10754,11562,12554,13362,14339,15147,15940,16932,17458,18181,18242,18242,18242,22,21,0,0,0,0,0,0,0x47a000,0x9a2600,0xecac00,0x12d3080,0x17fb680,0x1c03b00,0x212c100,0x2534580,0x2a5cb80,0x2e65000,0x338d600,0x38b5c00,0x3cbe080,0x41e6680,0x45eeb00,0x4b0d380,0x4f15800,0x5313f00,0x583c500,0x5ae4300,0x5eb4b00,0x5edcb80,0x5edcb80,0x5edcb80,0x5edcb80,0x5edcb80,0x5edcb80,0x5edcb80,0x5edcb80,0x5edcb80,0x5edcb80,0x5edcb80,579,1571,2563,3371,4363,5171,6163,6971,7963,8771,9763,10755,11563,12555,13363,14340,15148,15941,16933,17459,18182,18182,18182,18182,21,20,0,0,0,0,0,0,0x47aa80,0x9a3080,0xecb680,0x12d3b00,0x17fc100,0x1c04580,0x212cb80,0x2535000,0x2a5d600,0x2e65a80,0x338e080,0x38b6680,0x3cbeb00,0x41e7100,0x45ef580,0x4b0de00,0x4f16280,0x5314980,0x583cf80,0x5ae4d80,0x5eb5580,0x5eb5580,0x5eb5580,0x5eb5580,0x5eb5580,0x5eb5580,0x5eb5580,0x5eb5580,0x5eb5580,0x5eb5580,0x5eb5580,0x5eb5580,580,1572,2564,3372,4364,5172,6164,6972,7964,8772,9764,10756,11564,12556,13364,14341,15149,15942,16934,17460,18183,18183,18183,18183,21,20,0,0,0,0,0,0,0x47b500,0x9a3b00,0xecc100,0x12d4580,0x17fcb80,0x1c05000,0x212d600,0x2535a80,0x2a5e080,0x2e66500,0x338eb00,0x38b7100,0x3cbf580,0x41e7b80,0x45f0000,0x4b0e880,0x4f16d00,0x5315400,0x583da00,0x5ae5800,0x5eb6000,0x5eb6000,0x5eb6000,0x5eb6000,0x5eb6000,0x5eb6000,0x5eb6000,0x5eb6000,0x5eb6000,0x5eb6000,0x5eb6000,0x5eb6000,581,1573,2565,3373,4365,5173,6165,6973,7965,8773,9765,10757,11565,12557,13365,14342,15150,15943,16935,14152,14152,14152,14152,14152,20,19,0,0,0,0,0,0,0x47bf80,0x9a4580,0xeccb80,0x12d5000,0x17fd600,0x1c05a80,0x212e080,0x2536500,0x2a5eb00,0x2e66f80,0x338f580,0x38b7b80,0x3cc0000,0x41e8600,0x45f0a80,0x4b0f300,0x4f17780,0x5315e80,0x583e480,0x49bb400,0x49bb400,0x49bb400,0x49bb400,0x49bb400,0x49bb400,0x49bb400,0x49bb400,0x49bb400,0x49bb400,0x49bb400,0x49bb400,0x49bb400,582,1574,2566,3374,4366,5174,6166,6974,7966,8774,9766,10758,11566,12558,13366,14343,15151,16128,16936,14408,18184,18184,18184,18184,21,20,0,0,0,0,0,0,0x47ca00,0x9a5000,0xecd600,0x12d5a80,0x17fe080,0x1c06500,0x212eb00,0x2536f80,0x2a5f580,0x2e67a00,0x3390000,0x38b8600,0x3cc0a80,0x41e9080,0x45f1500,0x4b0fd80,0x4f18200,0x5436a80,0x583ef00,0x4b0a980,0x5eb6a80,0x5eb6a80,0x5eb6a80,0x5eb6a80,0x5eb6a80,0x5eb6a80,0x5eb6a80,0x5eb6a80,0x5eb6a80,0x5eb6a80,0x5eb6a80,0x5eb6a80,583,1575,2567,3375,4367,5175,6167,6975,7967,8775,9767,10759,11567,12559,13367,14344,15152,16129,16937,17461,18185,18185,18185,18185,21,20,0,0,0,0,0,0,0x47d480,0x9a5a80,0xece080,0x12d6500,0x17feb00,0x1c06f80,0x212f580,0x2537a00,0x2a60000,0x2e68480,0x3390a80,0x38b9080,0x3cc1500,0x41e9b00,0x45f1f80,0x4b10800,0x4f18c80,0x5437500,0x583f980,0x5ae6280,0x5eb7500,0x5eb7500,0x5eb7500,0x5eb7500,0x5eb7500,0x5eb7500,0x5eb7500,0x5eb7500,0x5eb7500,0x5eb7500,0x5eb7500,0x5eb7500,768,1576,2568,3376,4368,5176,6168,6976,7968,8960,9768,10760,11568,12560,13368,14345,15153,16130,16938,17462,18186,18186,18186,18186,21,20,0,0,0,0,0,0,0x59e080,0x9a6500,0xeceb00,0x12d6f80,0x17ff580,0x1c07a00,0x2130000,0x2538480,0x2a60a80,0x2f89080,0x3391500,0x38b9b00,0x3cc1f80,0x41ea580,0x45f2a00,0x4b11280,0x4f19700,0x5437f80,0x5840400,0x5ae6d00,0x5eb7f80,0x5eb7f80,0x5eb7f80,0x5eb7f80,0x5eb7f80,0x5eb7f80,0x5eb7f80,0x5eb7f80,0x5eb7f80,0x5eb7f80,0x5eb7f80,0x5eb7f80,769,1577,2569,3377,4369,5177,6169,6977,7969,8961,9769,10761,11569,12561,13369,14346,15154,16131,16939,14664,14664,14664,14664,14664,20,19,0,0,0,0,0,0,0x59eb00,0x9a6f80,0xecf580,0x12d7a00,0x1800000,0x1c08480,0x2130a80,0x2538f00,0x2a61500,0x2f89b00,0x3391f80,0x38ba580,0x3cc2a00,0x41eb000,0x45f3480,0x4b11d00,0x4f1a180,0x5438a00,0x5840e80,0x4c59f00,0x4c59f00,0x4c59f00,0x4c59f00,0x4c59f00,0x4c59f00,0x4c59f00,0x4c59f00,0x4c59f00,0x4c59f00,0x4c59f00,0x4c59f00,0x4c59f00,770,1578,2570,3378,4370,5178,6170,6978,7970,8962,9770,10762,11570,12562,13370,14347,15155,16132,16940,17463,18187,18243,18243,18243,22,21,0,0,0,0,0,0,0x59f580,0x9a7a00,0xed0000,0x12d8480,0x1800a80,0x1c08f00,0x2131500,0x2539980,0x2a61f80,0x2f8a580,0x3392a00,0x38bb000,0x3cc3480,0x41eba80,0x45f3f00,0x4b12780,0x4f1ac00,0x5439480,0x5841900,0x5ae7780,0x5eb8a00,0x5edd600,0x5edd600,0x5edd600,0x5edd600,0x5edd600,0x5edd600,0x5edd600,0x5edd600,0x5edd600,0x5edd600,0x5edd600,771,1579,2571,3379,4371,5179,6171,6979,7971,8963,9771,10763,11571,12563,13371,14348,15156,16133,16941,17464,18188,18188,18188,18188,21,20,0,0,0,0,0,0,0x5a0000,0x9a8480,0xed0a80,0x12d8f00,0x1801500,0x1c09980,0x2131f80,0x253a400,0x2a62a00,0x2f8b000,0x3393480,0x38bba80,0x3cc3f00,0x41ec500,0x45f4980,0x4b13200,0x4f1b680,0x5439f00,0x5842380,0x5ae8200,0x5eb9480,0x5eb9480,0x5eb9480,0x5eb9480,0x5eb9480,0x5eb9480,0x5eb9480,0x5eb9480,0x5eb9480,0x5eb9480,0x5eb9480,0x5eb9480,772,1580,2572,3380,4372,5180,6172,6980,7972,8964,9772,10764,11572,12564,13372,14349,15157,16134,16942,17465,18189,18189,18189,18189,21,20,0,0,0,0,0,0,0x5a0a80,0x9a8f00,0xed1500,0x12d9980,0x1801f80,0x1c0a400,0x2132a00,0x253ae80,0x2a63480,0x2f8ba80,0x3393f00,0x38bc500,0x3cc4980,0x41ecf80,0x45f5400,0x4b13c80,0x4f1c100,0x543a980,0x5842e00,0x5ae8c80,0x5eb9f00,0x5eb9f00,0x5eb9f00,0x5eb9f00,0x5eb9f00,0x5eb9f00,0x5eb9f00,0x5eb9f00,0x5eb9f00,0x5eb9f00,0x5eb9f00,0x5eb9f00,773,1581,2573,3381,4373,5181,6173,6981,7973,8965,9773,10765,11573,12565,13373,14350,15158,16135,16943,14920,14920,14920,14920,14920,20,19,0,0,0,0,0,0,0x5a1500,0x9a9980,0xed1f80,0x12da400,0x1802a00,0x1c0ae80,0x2133480,0x253b900,0x2a63f00,0x2f8c500,0x3394980,0x38bcf80,0x3cc5400,0x41eda00,0x45f5e80,0x4b14700,0x4f1cb80,0x543b400,0x5843880,0x4da9480,0x4da9480,0x4da9480,0x4da9480,0x4da9480,0x4da9480,0x4da9480,0x4da9480,0x4da9480,0x4da9480,0x4da9480,0x4da9480,0x4da9480,774,1582,2574,3382,4374,5182,6174,6982,7974,8966,9774,10766,11574,12566,13374,14351,15159,16136,16944,15176,18190,18190,18190,18190,21,20,0,0,0,0,0,0,0x5a1f80,0x9aa400,0xed2a00,0x12dae80,0x1803480,0x1c0b900,0x2133f00,0x253c380,0x2a64980,0x2f8cf80,0x3395400,0x38bda00,0x3cc5e80,0x41ee480,0x45f6900,0x4b15180,0x4f1d600,0x543be80,0x5844300,0x4ef8a00,0x5eba980,0x5eba980,0x5eba980,0x5eba980,0x5eba980,0x5eba980,0x5eba980,0x5eba980,0x5eba980,0x5eba980,0x5eba980,0x5eba980,775,1583,2575,3383,4375,5183,6175,6983,7975,8967,9775,10767,11575,12567,13375,14352,15160,16137,16945,17466,18191,18191,18191,18191,21,20,0,0,0,0,0,0,0x5a2a00,0x9aae80,0xed3480,0x12db900,0x1803f00,0x1c0c380,0x2134980,0x253ce00,0x2a65400,0x2f8da00,0x3395e80,0x38be480,0x3cc6900,0x41eef00,0x45f7380,0x4b15c00,0x4f1e080,0x543c900,0x5844d80,0x5ae9700,0x5ebb400,0x5ebb400,0x5ebb400,0x5ebb400,0x5ebb400,0x5ebb400,0x5ebb400,0x5ebb400,0x5ebb400,0x5ebb400,0x5ebb400,0x5ebb400,776,1584,2576,3384,4376,5184,6176,7168,7976,8968,9776,10768,11576,12568,13376,14353,15161,16138,16946,17467,18192,18192,18192,18192,21,20,0,0,0,0,0,0,0x5a3480,0x9ab900,0xed3f00,0x12dc380,0x1804980,0x1c0ce00,0x2135400,0x265da00,0x2a65e80,0x2f8e480,0x3396900,0x38bef00,0x3cc7380,0x41ef980,0x45f7e00,0x4b16680,0x4f1eb00,0x543d380,0x5845800,0x5aea180,0x5ebbe80,0x5ebbe80,0x5ebbe80,0x5ebbe80,0x5ebbe80,0x5ebbe80,0x5ebbe80,0x5ebbe80,0x5ebbe80,0x5ebbe80,0x5ebbe80,0x5ebbe80,777,1585,2577,3385,4377,5185,6177,7169,7977,8969,9777,10769,11577,12569,13377,14354,15162,16139,16947,15432,15432,15432,15432,15432,20,19,0,0,0,0,0,0,0x5a3f00,0x9ac380,0xed4980,0x12dce00,0x1805400,0x1c0d880,0x2135e80,0x265e480,0x2a66900,0x2f8ef00,0x3397380,0x38bf980,0x3cc7e00,0x41f0400,0x45f8880,0x4b17100,0x4f1f580,0x543de00,0x5846280,0x5047f80,0x5047f80,0x5047f80,0x5047f80,0x5047f80,0x5047f80,0x5047f80,0x5047f80,0x5047f80,0x5047f80,0x5047f80,0x5047f80,0x5047f80,778,1586,2578,3386,4378,5186,6178,7170,7978,8970,9778,10770,11578,12570,13378,14355,15163,16140,16948,17468,18193,18244,18244,18244,22,21,0,0,0,0,0,0,0x5a4980,0x9ace00,0xed5400,0x12dd880,0x1805e80,0x1c0e300,0x2136900,0x265ef00,0x2a67380,0x2f8f980,0x3397e00,0x38c0400,0x3cc8880,0x41f0e80,0x45f9300,0x4b17b80,0x4f20000,0x543e880,0x5846d00,0x5aeac00,0x5ebc900,0x5ede080,0x5ede080,0x5ede080,0x5ede080,0x5ede080,0x5ede080,0x5ede080,0x5ede080,0x5ede080,0x5ede080,0x5ede080,779,1587,2579,3387,4379,5187,6179,7171,7979,8971,9779,10771,11579,12571,13379,14356,15164,16141,16949,17469,18194,18194,18194,18194,21,20,0,0,0,0,0,0,0x5a5400,0x9ad880,0xed5e80,0x12de300,0x1806900,0x1c0ed80,0x2137380,0x265f980,0x2a67e00,0x2f90400,0x3398880,0x38c0e80,0x3cc9300,0x41f1900,0x45f9d80,0x4b18600,0x4f20a80,0x543f300,0x5847780,0x5aeb680,0x5ebd380,0x5ebd380,0x5ebd380,0x5ebd380,0x5ebd380,0x5ebd380,0x5ebd380,0x5ebd380,0x5ebd380,0x5ebd380,0x5ebd380,0x5ebd380,780,1588,2580,3388,4380,5188,6180,7172,7980,8972,9780,10772,11580,12572,13380,14357,15165,16142,16950,17470,18195,18195,18195,18195,21,20,0,0,0,0,0,0,0x5a5e80,0x9ae300,0xed6900,0x12ded80,0x1807380,0x1c0f800,0x2137e00,0x2660400,0x2a68880,0x2f90e80,0x3399300,0x38c1900,0x3cc9d80,0x41f2380,0x45fa800,0x4b19080,0x4f21500,0x543fd80,0x5848200,0x5aec100,0x5ebde00,0x5ebde00,0x5ebde00,0x5ebde00,0x5ebde00,0x5ebde00,0x5ebde00,0x5ebde00,0x5ebde00,0x5ebde00,0x5ebde00,0x5ebde00,781,1589,2581,3389,4381,5189,6181,7173,7981,8973,9781,10773,11581,12573,13381,14358,15166,16143,16951,15688,15688,15688,15688,15688,20,19,0,0,0,0,0,0,0x5a6900,0x9aed80,0xed7380,0x12df800,0x1807e00,0x1c10280,0x2138880,0x2660e80,0x2a69300,0x2f91900,0x3399d80,0x38c2380,0x3cca800,0x41f2e00,0x45fb280,0x4b19b00,0x4f21f80,0x5440800,0x5848c80,0x5197500,0x5197500,0x5197500,0x5197500,0x5197500,0x5197500,0x5197500,0x5197500,0x5197500,0x5197500,0x5197500,0x5197500,0x5197500,782,1590,2582,3390,4382,5190,6182,7174,7982,8974,9782,10774,11582,12574,13382,14359,15167,16144,16952,15944,18196,18196,18196,18196,21,20,0,0,0,0,0,0,0x5a7380,0x9af800,0xed7e00,0x12e0280,0x1808880,0x1c10d00,0x2139300,0x2661900,0x2a69d80,0x2f92380,0x339a800,0x38c2e00,0x3ccb280,0x41f3880,0x45fbd00,0x4b1a580,0x4f22a00,0x5441280,0x5849700,0x52e6a80,0x5ebe880,0x5ebe880,0x5ebe880,0x5ebe880,0x5ebe880,0x5ebe880,0x5ebe880,0x5ebe880,0x5ebe880,0x5ebe880,0x5ebe880,0x5ebe880,783,1591,2583,3391,4383,5191,6183,7175,7983,8975,9783,10775,11583,12575,13383,14360,15168,16145,16953,17471,18197,18197,18197,18197,21,20,0,0,0,0,0,0,0x5a7e00,0x9b0280,0xed8880,0x12e0d00,0x1809300,0x1c11780,0x2139d80,0x2662380,0x2a6a800,0x2f92e00,0x339b280,0x38c3880,0x3ccbd00,0x41f4300,0x45fc780,0x4b1b000,0x4f23480,0x5441d00,0x584a180,0x5aecb80,0x5ebf300,0x5ebf300,0x5ebf300,0x5ebf300,0x5ebf300,0x5ebf300,0x5ebf300,0x5ebf300,0x5ebf300,0x5ebf300,0x5ebf300,0x5ebf300,784,1592,2584,3392,4384,5376,6184,7176,7984,8976,9784,10776,11584,12576,13568,14361,15169,16146,16954,17472,18198,18198,18198,18198,21,20,0,0,0,0,0,0,0x5a8880,0x9b0d00,0xed9300,0x12e1780,0x1809d80,0x1d32380,0x213a800,0x2662e00,0x2a6b280,0x2f93880,0x339bd00,0x38c4300,0x3ccc780,0x41f4d80,0x471d380,0x4b1ba80,0x4f23f00,0x5442780,0x584ac00,0x5aed600,0x5ebfd80,0x5ebfd80,0x5ebfd80,0x5ebfd80,0x5ebfd80,0x5ebfd80,0x5ebfd80,0x5ebfd80,0x5ebfd80,0x5ebfd80,0x5ebfd80,0x5ebfd80,785,1593,2585,3393,4385,5377,6185,7177,7985,8977,9785,10777,11585,12577,13569,14362,15170,16147,16955,16200,16200,16200,16200,16200,20,19,0,0,0,0,0,0,0x5a9300,0x9b1780,0xed9d80,0x12e2200,0x180a800,0x1d32e00,0x213b280,0x2663880,0x2a6bd00,0x2f94300,0x339c780,0x38c4d80,0x3ccd200,0x41f5800,0x471de00,0x4b1c500,0x4f24980,0x5443200,0x584b680,0x5436000,0x5436000,0x5436000,0x5436000,0x5436000,0x5436000,0x5436000,0x5436000,0x5436000,0x5436000,0x5436000,0x5436000,0x5436000,786,1594,2586,3394,4386,5378,6186,7178,7986,8978,9786,10778,11586,12578,13570,14363,15171,16148,16956,17473,18199,18245,18245,18245,22,21,0,0,0,0,0,0,0x5a9d80,0x9b2200,0xeda800,0x12e2c80,0x180b280,0x1d33880,0x213bd00,0x2664300,0x2a6c780,0x2f94d80,0x339d200,0x38c5800,0x3ccdc80,0x41f6280,0x471e880,0x4b1cf80,0x4f25400,0x5443c80,0x584c100,0x5aee080,0x5ec0800,0x5edeb00,0x5edeb00,0x5edeb00,0x5edeb00,0x5edeb00,0x5edeb00,0x5edeb00,0x5edeb00,0x5edeb00,0x5edeb00,0x5edeb00,787,1595,2587,3395,4387,5379,6187,7179,7987,8979,9787,10779,11587,12579,13571,14364,15172,16149,16957,17474,18200,18200,18200,18200,21,20,0,0,0,0,0,0,0x5aa800,0x9b2c80,0xedb280,0x12e3700,0x180bd00,0x1d34300,0x213c780,0x2664d80,0x2a6d200,0x2f95800,0x339dc80,0x38c6280,0x3cce700,0x41f6d00,0x471f300,0x4b1da00,0x4f25e80,0x5444700,0x584cb80,0x5aeeb00,0x5ec1280,0x5ec1280,0x5ec1280,0x5ec1280,0x5ec1280,0x5ec1280,0x5ec1280,0x5ec1280,0x5ec1280,0x5ec1280,0x5ec1280,0x5ec1280,788,1596,2588,3396,4388,5380,6188,7180,7988,8980,9788,10780,11588,12580,13572,14365,15173,16150,16958,17475,18201,18201,18201,18201,21,20,0,0,0,0,0,0,0x5ab280,0x9b3700,0xedbd00,0x12e4180,0x180c780,0x1d34d80,0x213d200,0x2665800,0x2a6dc80,0x2f96280,0x339e700,0x38c6d00,0x3ccf180,0x41f7780,0x471fd80,0x4b1e480,0x4f26900,0x5445180,0x584d600,0x5aef580,0x5ec1d00,0x5ec1d00,0x5ec1d00,0x5ec1d00,0x5ec1d00,0x5ec1d00,0x5ec1d00,0x5ec1d00,0x5ec1d00,0x5ec1d00,0x5ec1d00,0x5ec1d00,789,1597,2589,3397,4389,5381,6189,7181,7989,8981,9789,10781,11589,12581,13573,14366,15174,16151,16959,16456,16456,16456,16456,16456,20,19,0,0,0,0,0,0,0x5abd00,0x9b4180,0xedc780,0x12e4c00,0x180d200,0x1d35800,0x213dc80,0x2666280,0x2a6e700,0x2f96d00,0x339f180,0x38c7780,0x3ccfc00,0x41f8200,0x4720800,0x4b1ef00,0x4f27380,0x5445c00,0x584e080,0x5585580,0x5585580,0x5585580,0x5585580,0x5585580,0x5585580,0x5585580,0x5585580,0x5585580,0x5585580,0x5585580,0x5585580,0x5585580,790,1598,2590,3398,4390,5382,6190,7182,7990,8982,9790,10782,11590,12582,13574,14367,15175,16152,16960,16712,18202,18202,18202,18202,21,20,0,0,0,0,0,0,0x5ac780,0x9b4c00,0xedd200,0x12e5680,0x180dc80,0x1d36280,0x213e700,0x2666d00,0x2a6f180,0x2f97780,0x339fc00,0x38c8200,0x3cd0680,0x41f8c80,0x4721280,0x4b1f980,0x4f27e00,0x5446680,0x584eb00,0x56d4b00,0x5ec2780,0x5ec2780,0x5ec2780,0x5ec2780,0x5ec2780,0x5ec2780,0x5ec2780,0x5ec2780,0x5ec2780,0x5ec2780,0x5ec2780,0x5ec2780,791,1599,2591,3399,4391,5383,6191,7183,7991,8983,9791,10783,11591,12583,13575,14368,15360,16153,16961,17476,18203,18203,18203,18203,21,20,0,0,0,0,0,0,0x5ad200,0x9b5680,0xeddc80,0x12e6100,0x180e700,0x1d36d00,0x213f180,0x2667780,0x2a6fc00,0x2f98200,0x33a0680,0x38c8c80,0x3cd1100,0x41f9700,0x4721d00,0x4b20400,0x5048a00,0x5447100,0x584f580,0x5af0000,0x5ec3200,0x5ec3200,0x5ec3200,0x5ec3200,0x5ec3200,0x5ec3200,0x5ec3200,0x5ec3200,0x5ec3200,0x5ec3200,0x5ec3200,0x5ec3200,792,1600,2592,3584,4392,5384,6192,7184,7992,8984,9792,10784,11776,12584,13576,14369,15361,16154,16962,17477,18204,18204,18204,18204,21,20,0,0,0,0,0,0,0x5adc80,0x9b6100,0xede700,0x1406d00,0x180f180,0x1d37780,0x213fc00,0x2668200,0x2a70680,0x2f98c80,0x33a1100,0x38c9700,0x3df1d00,0x41fa180,0x4722780,0x4b20e80,0x5049480,0x5447b80,0x5850000,0x5af0a80,0x5ec3c80,0x5ec3c80,0x5ec3c80,0x5ec3c80,0x5ec3c80,0x5ec3c80,0x5ec3c80,0x5ec3c80,0x5ec3c80,0x5ec3c80,0x5ec3c80,0x5ec3c80,793,1601,2593,3585,4393,5385,6193,7185,7993,8985,9793,10785,11777,12585,13577,14370,15362,16155,16963,16968,16968,16968,16968,16968,20,19,0,0,0,0,0,0,0x5ae700,0x9b6b80,0xedf180,0x1407780,0x180fc00,0x1d38200,0x2140680,0x2668c80,0x2a71100,0x2f99700,0x33a1b80,0x38ca180,0x3df2780,0x41fac00,0x4723200,0x4b21900,0x5049f00,0x5448600,0x5850a80,0x5824080,0x5824080,0x5824080,0x5824080,0x5824080,0x5824080,0x5824080,0x5824080,0x5824080,0x5824080,0x5824080,0x5824080,0x5824080,794,1602,2594,3586,4394,5386,6194,7186,7994,8986,9794,10786,11778,12586,13578,14371,15363,16156,16964,17478,18205,18246,18246,18246,22,21,0,0,0,0,0,0,0x5af180,0x9b7600,0xedfc00,0x1408200,0x1810680,0x1d38c80,0x2141100,0x2669700,0x2a71b80,0x2f9a180,0x33a2600,0x38cac00,0x3df3200,0x41fb680,0x4723c80,0x4b22380,0x504a980,0x5449080,0x5851500,0x5af1500,0x5ec4700,0x5edf580,0x5edf580,0x5edf580,0x5edf580,0x5edf580,0x5edf580,0x5edf580,0x5edf580,0x5edf580,0x5edf580,0x5edf580,795,1603,2595,3587,4395,5387,6195,7187,7995,8987,9795,10787,11779,12587,13579,14372,15364,16157,16965,17479,18206,18206,18206,18206,21,20,0,0,0,0,0,0,0x5afc00,0x9b8080,0xee0680,0x1408c80,0x1811100,0x1d39700,0x2141b80,0x266a180,0x2a72600,0x2f9ac00,0x33a3080,0x38cb680,0x3df3c80,0x41fc100,0x4724700,0x4b22e00,0x504b400,0x5449b00,0x5851f80,0x5af1f80,0x5ec5180,0x5ec5180,0x5ec5180,0x5ec5180,0x5ec5180,0x5ec5180,0x5ec5180,0x5ec5180,0x5ec5180,0x5ec5180,0x5ec5180,0x5ec5180,796,1604,2596,3588,4396,5388,6196,7188,7996,8988,9796,10788,11780,12588,13580,14373,15365,16158,16966,17664,18207,18207,18207,18207,21,20,0,0,0,0,0,0,0x5b0680,0x9b8b00,0xee1100,0x1409700,0x1811b80,0x1d3a180,0x2142600,0x266ac00,0x2a73080,0x2f9b680,0x33a3b00,0x38cc100,0x3df4700,0x41fcb80,0x4725180,0x4b23880,0x504be80,0x544a580,0x5852a00,0x5c12b80,0x5ec5c00,0x5ec5c00,0x5ec5c00,0x5ec5c00,0x5ec5c00,0x5ec5c00,0x5ec5c00,0x5ec5c00,0x5ec5c00,0x5ec5c00,0x5ec5c00,0x5ec5c00,797,1605,2597,3589,4397,5389,6197,7189,7997,8989,9797,10789,11781,12589,13581,14374,15366,16159,16967,17224,17224,17224,17224,17224,20,19,0,0,0,0,0,0,0x5b1100,0x9b9580,0xee1b80,0x140a180,0x1812600,0x1d3ac00,0x2143080,0x266b680,0x2a73b00,0x2f9c100,0x33a4580,0x38ccb80,0x3df5180,0x41fd600,0x4725c00,0x4b24300,0x504c900,0x544b000,0x5853480,0x5973600,0x5973600,0x5973600,0x5973600,0x5973600,0x5973600,0x5973600,0x5973600,0x5973600,0x5973600,0x5973600,0x5973600,0x5973600,798,1606,2598,3590,4398,5390,6198,7190,7998,8990,9798,10790,11782,12590,13582,14375,15367,16160,17152,17480,18208,18208,18208,18208,21,20,0,0,0,0,0,0,0x5b1b80,0x9ba000,0xee2600,0x140ac00,0x1813080,0x1d3b680,0x2143b00,0x266c100,0x2a74580,0x2f9cb80,0x33a5000,0x38cd600,0x3df5c00,0x41fe080,0x4726680,0x4b24d80,0x504d380,0x544ba80,0x5974080,0x5ac2b80,0x5ec6680,0x5ec6680,0x5ec6680,0x5ec6680,0x5ec6680,0x5ec6680,0x5ec6680,0x5ec6680,0x5ec6680,0x5ec6680,0x5ec6680,0x5ec6680,799,1607,2599,3591,4399,5391,6199,7191,7999,8991,9799,10791,11783,12591,13583,14376,15368,16161,17153,17665,18209,18209,18209,18209,21,20,0,0,0,0,0,0,0x5b2600,0x9baa80,0xee3080,0x140b680,0x1813b00,0x1d3c100,0x2144580,0x266cb80,0x2a75000,0x2f9d600,0x33a5a80,0x38ce080,0x3df6680,0x41feb00,0x4727100,0x4b25800,0x504de00,0x544c500,0x5974b00,0x5c13600,0x5ec7100,0x5ec7100,0x5ec7100,0x5ec7100,0x5ec7100,0x5ec7100,0x5ec7100,0x5ec7100,0x5ec7100,0x5ec7100,0x5ec7100,0x5ec7100,800,1792,2600,3592,4400,5392,6200,7192,8000,8992,9984,10792,11784,12592,13584,14377,15369,16162,17154,17666,18210,18210,18210,18210,21,20,0,0,0,0,0,0,0x5b3080,0xadb680,0xee3b00,0x140c100,0x1814580,0x1d3cb80,0x2145000,0x266d600,0x2a75a80,0x2f9e080,0x34c6680,0x38ceb00,0x3df7100,0x41ff580,0x4727b80,0x4b26280,0x504e880,0x544cf80,0x5975580,0x5c14080,0x5ec7b80,0x5ec7b80,0x5ec7b80,0x5ec7b80,0x5ec7b80,0x5ec7b80,0x5ec7b80,0x5ec7b80,0x5ec7b80,0x5ec7b80,0x5ec7b80,0x5ec7b80,801,1793,2601,3593,4401,5393,6201,7193,8001,8993,9985,10793,11785,12593,13585,14378,15370,16163,17155,17736,17736,17736,17736,17736,20,19,0,0,0,0,0,0,0x5b3b00,0xadc100,0xee4580,0x140cb80,0x1815000,0x1d3d600,0x2145a80,0x266e080,0x2a76500,0x2f9eb00,0x34c7100,0x38cf580,0x3df7b80,0x4200000,0x4728600,0x4b26d00,0x504f300,0x544da00,0x5976000,0x5c12100,0x5c12100,0x5c12100,0x5c12100,0x5c12100,0x5c12100,0x5c12100,0x5c12100,0x5c12100,0x5c12100,0x5c12100,0x5c12100,0x5c12100,802,1794,2602,3594,4402,5394,6202,7194,8002,8994,9986,10794,11786,12594,13586,14379,15371,16164,17156,17667,18211,18247,18247,18247,22,21,0,0,0,0,0,0,0x5b4580,0xadcb80,0xee5000,0x140d600,0x1815a80,0x1d3e080,0x2146500,0x266eb00,0x2a76f80,0x2f9f580,0x34c7b80,0x38d0000,0x3df8600,0x4200a80,0x4729080,0x4b27780,0x504fd80,0x544e480,0x5976a80,0x5c14b00,0x5ec8600,0x5ee0000,0x5ee0000,0x5ee0000,0x5ee0000,0x5ee0000,0x5ee0000,0x5ee0000,0x5ee0000,0x5ee0000,0x5ee0000,0x5ee0000,803,1795,2603,3595,4403,5395,6203,7195,8003,8995,9987,10795,11787,12595,13587,14380,15372,16165,17157,17668,18212,18212,18212,18212,21,20,0,0,0,0,0,0,0x5b5000,0xadd600,0xee5a80,0x140e080,0x1816500,0x1d3eb00,0x2146f80,0x266f580,0x2a77a00,0x2fa0000,0x34c8600,0x38d0a80,0x3df9080,0x4201500,0x4729b00,0x4b28200,0x5050800,0x544ef00,0x5977500,0x5c15580,0x5ec9080,0x5ec9080,0x5ec9080,0x5ec9080,0x5ec9080,0x5ec9080,0x5ec9080,0x5ec9080,0x5ec9080,0x5ec9080,0x5ec9080,0x5ec9080,804,1796,2604,3596,4404,5396,6204,7196,8004,8996,9988,10796,11788,12596,13588,14381,15373,16166,17158,17669,18213,18213,18213,18213,21,20,0,0,0,0,0,0,0x5b5a80,0xade080,0xee6500,0x140eb00,0x1816f80,0x1d3f580,0x2147a00,0x2670000,0x2a78480,0x2fa0a80,0x34c9080,0x38d1500,0x3df9b00,0x4201f80,0x472a580,0x4b28c80,0x5051280,0x544f980,0x5977f80,0x5c16000,0x5ec9b00,0x5ec9b00,0x5ec9b00,0x5ec9b00,0x5ec9b00,0x5ec9b00,0x5ec9b00,0x5ec9b00,0x5ec9b00,0x5ec9b00,0x5ec9b00,0x5ec9b00,805,1797,2605,3597,4405,5397,6205,7197,8005,8997,9989,10797,11789,12597,13589,14382,15374,16167,17159,17992,17992,17992,17992,17992,20,19,0,0,0,0,0,0,0x5b6500,0xadeb00,0xee6f80,0x140f580,0x1817a00,0x1d40000,0x2148480,0x2670a80,0x2a78f00,0x2fa1500,0x34c9b00,0x38d1f80,0x3dfa580,0x4202a00,0x472b000,0x4b29700,0x5051d00,0x5450400,0x5978a00,0x5d61680,0x5d61680,0x5d61680,0x5d61680,0x5d61680,0x5d61680,0x5d61680,0x5d61680,0x5d61680,0x5d61680,0x5d61680,0x5d61680,0x5d61680,806,1798,2606,3598,4406,5398,6206,7198,8006,8998,9990,10798,11790,12598,13590,14383,15375,16168,17160,18248,18214,18214,18214,18214,21,20,0,0,0,0,0,0,0x5b6f80,0xadf580,0xee7a00,0x1410000,0x1818480,0x1d40a80,0x2148f00,0x2671500,0x2a79980,0x2fa1f80,0x34ca580,0x38d2a00,0x3dfb000,0x4203480,0x472ba80,0x4b2a180,0x5052780,0x5450e80,0x5979480,0x5eb0c00,0x5eca580,0x5eca580,0x5eca580,0x5eca580,0x5eca580,0x5eca580,0x5eca580,0x5eca580,0x5eca580,0x5eca580,0x5eca580,0x5eca580,807,1799,2607,3599,4407,5399,6207,7199,8007,8999,9991,10799,11791,12599,13591,14384,15376,16169,17161,17670,18215,18215,18215,18215,21,20,0,0,0,0,0,0,0x5b7a00,0xae0000,0xee8480,0x1410a80,0x1818f00,0x1d41500,0x2149980,0x2671f80,0x2a7a400,0x2fa2a00,0x34cb000,0x38d3480,0x3dfba80,0x4203f00,0x472c500,0x4b2ac00,0x5053200,0x5451900,0x5979f00,0x5c16a80,0x5ecb000,0x5ecb000,0x5ecb000,0x5ecb000,0x5ecb000,0x5ecb000,0x5ecb000,0x5ecb000,0x5ecb000,0x5ecb000,0x5ecb000,0x5ecb000};

__global__ __launch_bounds__(256, 1) void swin_main(const uint16_t* x, const uint16_t* mask, char* ws, uint16_t* out,
                                                    const uint16_t* wqkv, const uint16_t* wo, const uint16_t* ls,
                                                    const uint16_t* w1, const uint16_t* w2, const uint16_t* rc,
                                                    const int* ridx) {
  __shared__ __attribute__((aligned(16))) char smem[LDS_BYTES];
  const int wave = __builtin_amdgcn_readfirstlane(threadIdx.x >> 6);
  const unsigned lds0 = (unsigned)(size_t)smem;
  asm volatile(
      ".p2align 5\n"
      "s_load_dwordx8 s[16:23], %0, 0\n"
      "s_load_dwordx4 s[4:7], %0, 32\n"
      "s_load_dwordx8 s[80:87], %0, 48\n"
      "s_load_dwordx2 s[90:91], %0, 80\n"
      "s_mov_b32 s24, %1\n"
      "s_mov_b32 s25, %2\n"
      "s_mov_b64 s[60:61], %3\n"
      "s_getpc_b64 s[12:13]\n"
      "L_ipf0_%=:\n"
      "v_mbcnt_lo_u32_b32 v168, -1, 0\n"
      "v_mbcnt_hi_u32_b32 v168, -1, v168\n"
      "s_lshr_b32 s28, s25, 3\n"
      "s_lshl_b32 s29, s28, 2\n"
      "s_add_u32 s29, s29, s24\n"
      "s_mul_i32 s29, s29, 14\n"
      "v_min_u32_e64 v169, 13, v168\n"
      "v_add_lshl_u32 v169, v169, s29, 7\n"
      "v_min_u32 v169, 221056, v169\n"
      "v_lshlrev_b32_e64 v171, 4, v168\n"
      "s_lshl_b32 s29, s24, 6\n"
      "s_add_u32 s54, s60, s29\n"
      "s_addc_u32 s55, s61, 0\n"
      "s_and_b32 s29, s29, 64\n"
      "v_xor_b32_e64 v170, s29, v171\n"
      "s_mul_i32 s30, s24, 9216\n"
      "v_add_u32_e64 v170, s30, v170\n"
      "s_add_u32 s2, s30, 36864\n"
      "v_xor_b32 v195, 32, v170\n"
      "s_mov_b64 s[34:35], 0xffff\n"
      "v_cndmask_b32 v193, v195, v170, s[34:35]\n"
      "s_lshl_b64 s[34:35], s[34:35], 48\n"
      "v_cndmask_b32 v192, v170, v195, s[34:35]\n"
      "s_lshl_b64 s[34:35], -1, 32\n"
      "v_cndmask_b32 v194, v195, v170, s[34:35]\n"
      "s_waitcnt lgkmcnt(0)\n"
      "global_load_dword v31, v169, s[4:5]\n"
      "s_add_u32 m0, s2, 0\n"
      "s_nop 0\n"
      "global_load_lds_dwordx4 v170, s[4:5]\n"
      "global_load_lds_dwordx4 v170, s[4:5] offset:1024\n"
      "global_load_lds_dwordx4 v193, s[4:5] offset:2048\n"
      "global_load_lds_dwordx4 v195, s[4:5] offset:3072\n"
      "s_add_u32 s4, s4, 4096\n"
      "s_addc_u32 s5, s5, 0\n"
      "s_add_u32 m0, s2, 4096\n"
      "s_nop 0\n"
      "global_load_lds_dwordx4 v194, s[4:5]\n"
      "global_load_lds_dwordx4 v170, s[4:5] offset:1024\n"
      "global_load_lds_dwordx4 v192, s[4:5] offset:2048\n"
      "global_load_lds_dwordx4 v195, s[4:5] offset:3072\n"
      "s_add_u32 s4, s4, 4096\n"
      "s_addc_u32 s5, s5, 0\n"
      "s_add_u32 m0, s2, 8192\n"
      "s_nop 0\n"
      "global_load_lds_dwordx4 v195, s[4:5]\n"
      "s_add_u32 s4, s4, 28672\n"
      "s_addc_u32 s5, s5, 0\n"
      "s_load_dwordx16 s[36:51], s[82:83], 0\n"
      "s_load_dwordx2 s[8:9], s[80:81], 0\n"
      "s_load_dwordx16 s[64:79], s[84:85], 0\n"
      "s_lshl_b32 s29, s24, 8\n"
      "v_lshl_add_u32 v196, v168, 2, s29\n"
      "v_min_u32 v196, 672, v196\n"
      "v_add_u32 v199, 150640, v196\n"
      "s_lshl_b32 s29, s28, 2\n"
      "s_add_u32 s29, s29, s24\n"
      "s_mov_b64 s[34:35], s[16:17]\n"
      "s_mov_b32 s30, s29\n"
      "s_sub_u32 s31, s29, 48\n"
      "s_cmp_ge_u32 s29, 48\n"
      "s_cselect_b64 s[34:35], s[22:23], s[34:35]\n"
      "s_cselect_b32 s30, s31, s30\n"
      "s_sub_u32 s31, s29, 96\n"
      "s_cmp_ge_u32 s29, 96\n"
      "s_cselect_b64 s[34:35], s[18:19], s[34:35]\n"
      "s_cselect_b32 s30, s31, s30\n"
      "s_sub_u32 s31, s29, 54\n"
      "s_cmp_ge_u32 s29, 109\n"
      "s_cselect_b64 s[34:35], s[20:21], s[34:35]\n"
      "s_cselect_b32 s30, s31, s30\n"
      "s_lshl_b32 s30, s30, 21\n"
      "s_load_dword s100, s[34:35], s30\n"
      "s_and_b32 s29, s29, 7\n"
      "s_cmp_eq_u32 s29, 0\n"
      "s_cselect_b64 s[34:35], s[6:7], s[34:35]\n"
      "s_cmp_eq_u32 s29, 1\n"
      "s_cselect_b64 s[34:35], s[90:91], s[34:35]\n"
      "s_cmp_eq_u32 s29, 2\n"
      "s_cselect_b64 s[34:35], s[86:87], s[34:35]\n"
      "s_cmp_eq_u32 s29, 3\n"
      "s_cselect_b64 s[34:35], s[60:61], s[34:35]\n"
      "s_cmp_eq_u32 s29, 4\n"
      "s_cselect_b64 s[34:35], s[12:13], s[34:35]\n"
      "s_load_dword s100, s[34:35], 0\n"
      "s_add_u32 m0, s2, 36864\n"
      "s_nop 0\n"
      "global_load_lds_dwordx4 v170, s[4:5]\n"
      "global_load_lds_dwordx4 v170, s[4:5] offset:1024\n"
      "global_load_lds_dwordx4 v193, s[4:5] offset:2048\n"
      "global_load_lds_dwordx4 v195, s[4:5] offset:3072\n"
      "s_add_u32 s4, s4, 4096\n"
      "s_addc_u32 s5, s5, 0\n"
      "s_add_u32 m0, s2, 40960\n"
      "s_nop 0\n"
      "global_load_lds_dwordx4 v194, s[4:5]\n"
      "global_load_lds_dwordx4 v170, s[4:5] offset:1024\n"
      "global_load_lds_dwordx4 v192, s[4:5] offset:2048\n"
      "global_load_lds_dwordx4 v195, s[4:5] offset:3072\n"
      "s_add_u32 s4, s4, 4096\n"
      "s_addc_u32 s5, s5, 0\n"
      "s_add_u32 m0, s2, 45056\n"
      "s_nop 0\n"
      "global_load_lds_dwordx4 v195, s[4:5]\n"
      "s_add_u32 s4, s4, 32768\n"
      "s_addc_u32 s5, s5, 0\n"
      "v_add_u32 v197, 4096, v195\n"
      "v_add_u32 v229, s2, v171\n"
      "v_lshrrev_b32 v230, 2, v168\n"
      "global_load_dwordx4 a[72:75], v170, s[4:5] offset:-4096\n"
      "v_and_b32 v198, 3, v230\n"
      "v_lshrrev_b32_e64 v228, 4, v168\n"
      "global_load_dwordx4 a[76:79], v170, s[4:5] offset:-3072\n"
      "v_lshl_add_u32 v198, v228, 3, v198\n"
      "v_mul_u32_u24 v198, 1152, v198\n"
      "global_load_dwordx4 a[80:83], v193, s[4:5] offset:-2048\n"
      "v_and_b32_e64 v228, 1, v168\n"
      "v_lshl_add_u32 v198, v228, 3, v198\n"
      "global_load_dwordx4 a[84:87], v195, s[4:5] offset:-1024\n"
      "v_add_u32 v198, 36864, v198\n"
      "v_and_b32_e64 v230, 6, v230\n"
      "global_load_dwordx4 a[88:91], v194, s[4:5]\n"
      "v_bfe_u32 v228, v168, 1, 1\n"
      "s_mul_i32 s29, s24, 6\n"
      "global_load_dwordx4 a[92:95], v170, s[4:5] offset:1024\n"
      "v_add_u32 v228, s29, v228\n"
      "v_xor_b32_e64 v148, v228, v230\n"
      "global_load_dwordx4 a[96:99], v192, s[4:5] offset:2048\n"
      "v_lshl_add_u32 v148, v148, 4, v198\n"
      "v_add_u32_e64 v228, 2, v228\n"
      "v_xor_b32_e64 v149, v228, v230\n"
      "global_load_dwordx4 a[100:103], v195, s[4:5] offset:3072\n"
      "v_lshl_add_u32 v149, v149, 4, v198\n"
      "v_add_u32_e64 v228, 2, v228\n"
      "global_load_dwordx4 a[104:107], v197, s[4:5]\n"
      "v_xor_b32_e64 v231, v228, v230\n"
      "v_lshl_add_u32 v231, v231, 4, v198\n"
      "s_movk_i32 s29, 18\n"
      "s_mov_b32 s30, 73600\n"
      "s_cmp_eq_u32 s24, 1\n"
      "v_add_u32 v170, 36864, v170\n"
      "v_add_u32 v195, 36864, v195\n"
      "v_add_u32 v193, 36864, v193\n"
      "v_add_u32 v194, 36864, v194\n"
      "v_add_u32 v192, 36864, v192\n"
      "v_add_u32 v197, 36864, v197\n"
      "global_load_dwordx4 a[108:111], v170, s[4:5] offset:-4096\n"
      "s_cselect_b64 s[12:13], s[6:7], s[12:13]\n"
      "s_cmp_eq_u32 s24, 2\n"
      "s_cselect_b32 s29, 16, s29\n"
      "s_cselect_b32 s30, L_cend_%=-L_ipf0_%=-128, s30\n"
      "s_cmp_eq_u32 s24, 3\n"
      "global_load_dwordx4 a[112:115], v170, s[4:5] offset:-3072\n"
      "s_cselect_b64 s[12:13], s[60:61], s[12:13]\n"
      "s_cselect_b32 s29, 10, s29\n"
      "s_cselect_b32 s30, 40832, s30\n"
      "global_load_dwordx4 a[116:119], v193, s[4:5] offset:-2048\n"
      "s_cmp_eq_u32 s24, 0\n"
      "s_cselect_b64 s[12:13], s[90:91], s[12:13]\n"
      "global_load_dwordx4 a[120:123], v195, s[4:5] offset:-1024\n"
      "s_cselect_b32 s29, 3, s29\n"
      "s_cselect_b32 s30, 9476, s30\n"
      "global_load_dwordx4 a[124:127], v194, s[4:5]\n"
      "s_mul_i32 s28, s28, s29\n"
      "s_sub_u32 s29, s29, 1\n"
      "global_load_dwordx4 a[128:131], v170, s[4:5] offset:1024\n"
      "v_min_u32 v169, s29, v168\n"
      "v_add_lshl_u32 v169, v169, s28, 7\n"
      "global_load_dwordx4 a[132:135], v192, s[4:5] offset:2048\n"
      "v_min_u32_e64 v169, s30, v169\n"
      "s_mul_i32 s28, s24, 10240\n"
      "global_load_dwordx4 a[136:139], v195, s[4:5] offset:3072\n"
      "s_add_u32 s28, s28, 4096\n"
      "v_add_u32 v198, s28, v171\n"
      "global_load_dwordx4 a[140:143], v197, s[4:5]\n"
      "v_add_u32 v230, 8192, v198\n"
      "global_load_dword v196, v196, s[86:87]\n"
      "s_mov_b64 s[56:57], s[90:91]\n"
      "s_load_dwordx16 s[84:99], s[84:85], 64\n"
      "s_lshl_b32 s29, s25, 8\n"
      "s_add_u32 s52, s60, s29\n"
      "s_addc_u32 s53, s61, 0\n"
      "s_lshl_b32 s29, s24, 5\n"
      "v_lshrrev_b32_e64 v151, 3, v168\n"
      "v_lshl_add_u32 v228, v151, 2, s29\n"
      "v_add_u32 v228, 41408, v228\n"
      "global_load_dword v228, v228, s[52:53]\n"
      "global_load_dword v30, v169, s[12:13]\n"
      "v_xor_b32 v151, v151, v168\n"
      "v_and_b32 v151, 7, v151\n"
      "s_waitcnt vmcnt(21)\n"
      "s_barrier\n"
      "ds_read_b64_tr_b16 a[0:1], v148\n"
      "ds_read_b64_tr_b16 a[2:3], v148 offset:4608\n"
      "ds_read_b64_tr_b16 a[4:5], v149\n"
      "ds_read_b64_tr_b16 a[6:7], v149 offset:4608\n"
      "ds_read_b64_tr_b16 a[8:9], v231\n"
      "ds_read_b64_tr_b16 a[10:11], v231 offset:4608\n"
      "ds_read_b64_tr_b16 a[12:13], v148 offset:384\n"
      "ds_read_b64_tr_b16 a[14:15], v148 offset:4992\n"
      "ds_read_b64_tr_b16 a[16:17], v149 offset:384\n"
      "v_add_u32 v170, 36864, v170\n"
      "v_add_u32 v195, 36864, v195\n"
      "v_add_u32 v193, 36864, v193\n"
      "v_add_u32 v194, 36864, v194\n"
      "v_add_u32 v192, 36864, v192\n"
      "v_add_u32 v197, 36864, v197\n"
      "global_load_dwordx4 a[144:147], v170, s[4:5] offset:-4096\n"
      "ds_read_b64_tr_b16 a[18:19], v149 offset:4992\n"
      "ds_read_b64_tr_b16 a[20:21], v231 offset:384\n"
      "ds_read_b64_tr_b16 a[22:23], v231 offset:4992\n"
      "ds_read_b64_tr_b16 a[24:25], v148 offset:768\n"
      "ds_read_b64_tr_b16 a[26:27], v148 offset:5376\n"
      "ds_read_b64_tr_b16 a[28:29], v149 offset:768\n"
      "ds_read_b64_tr_b16 a[30:31], v149 offset:5376\n"
      "ds_read_b64_tr_b16 a[32:33], v231 offset:768\n"
      "ds_read_b64_tr_b16 a[34:35], v231 offset:5376\n"
      "ds_read_b64_tr_b16 a[36:37], v148 offset:36864\n"
      "ds_read_b64_tr_b16 a[38:39], v148 offset:41472\n"
      "global_load_dwordx4 a[148:151], v170, s[4:5] offset:-3072\n"
      "ds_read_b64_tr_b16 a[40:41], v149 offset:36864\n"
      "ds_read_b64_tr_b16 a[42:43], v149 offset:41472\n"
      "ds_read_b64_tr_b16 a[44:45], v231 offset:36864\n"
      "ds_read_b64_tr_b16 a[46:47], v231 offset:41472\n"
      "global_load_dwordx4 a[152:155], v193, s[4:5] offset:-2048\n"
      "ds_read_b64_tr_b16 a[48:49], v148 offset:37248\n"
      "ds_read_b64_tr_b16 a[50:51], v148 offset:41856\n"
      "ds_read_b64_tr_b16 a[52:53], v149 offset:37248\n"
      "ds_read_b64_tr_b16 a[54:55], v149 offset:41856\n"
      "global_load_dwordx4 a[156:159], v195, s[4:5] offset:-1024\n"
      "ds_read_b64_tr_b16 a[56:57], v231 offset:37248\n"
      "ds_read_b64_tr_b16 a[58:59], v231 offset:41856\n"
      "ds_read_b64_tr_b16 a[60:61], v148 offset:37632\n"
      "ds_read_b64_tr_b16 a[62:63], v148 offset:42240\n"
      "global_load_dwordx4 a[160:163], v194, s[4:5]\n"
      "ds_read_b64_tr_b16 a[64:65], v149 offset:37632\n"
      "ds_read_b64_tr_b16 a[66:67], v149 offset:42240\n"
      "ds_read_b64_tr_b16 a[68:69], v231 offset:37632\n"
      "ds_read_b64_tr_b16 a[70:71], v231 offset:42240\n"
      "s_waitcnt lgkmcnt(0)\n"
      "s_pack_ll_b32_b16 s28, s36, s44\n"
      "s_waitcnt vmcnt(7)\n"
      "v_dot2_f32_bf16 v150, s28, v196, 0\n"
      "s_pack_hh_b32_b16 s29, s36, s44\n"
      "v_dot2_f32_bf16 v168, s29, v196, 0\n"
      "s_pack_ll_b32_b16 s30, s37, s45\n"
      "v_dot2_f32_bf16 v240, s30, v196, 0\n"
      "s_pack_hh_b32_b16 s31, s37, s45\n"
      "v_dot2_f32_bf16 v241, s31, v196, 0\n"
      "s_pack_ll_b32_b16 s28, s38, s46\n"
      "v_dot2_f32_bf16 v243, s28, v196, 0\n"
      "s_pack_hh_b32_b16 s29, s38, s46\n"
      "v_dot2_f32_bf16 v242, s29, v196, 0\n"
      "s_pack_ll_b32_b16 s30, s39, s47\n"
      "v_dot2_f32_bf16 v169, s30, v196, 0\n"
      "s_pack_hh_b32_b16 s31, s39, s47\n"
      "v_dot2_f32_bf16 v249, s31, v196, 0\n"
      "s_pack_ll_b32_b16 s28, s40, s48\n"
      "v_dot2_f32_bf16 v248, s28, v196, 0\n"
      "s_waitcnt vmcnt(6)\n"
      "v_lshl_add_u32 v228, v151, 4, v228\n"
      "s_lshl_b32 s10, s24, 10\n"
      "s_add_u32 m0, s10, 18432\n"
      "s_nop 0\n"
      "global_load_lds_dwordx4 v228, s[16:17]\n"
      "s_add_u32 m0, s10, 24448\n"
      "s_nop 0\n"
      "global_load_lds_dwordx4 v228, s[16:17] offset:128\n"
      "s_add_u32 m0, s10, 30464\n"
      "s_nop 0\n"
      "global_load_lds_dwordx4 v228, s[16:17] offset:256\n"
      "v_add_u32_e64 v151, s10, v171\n"
      "s_add_u32 m0, s10, 124928\n"
      "s_nop 0\n"
      "global_load_lds_dwordx4 v151, s[56:57]\n"
      "v_add_u32 v151, 4096, v151\n"
      "s_add_u32 m0, s10, 129024\n"
      "s_nop 0\n"
      "global_load_lds_dwordx4 v151, s[56:57]\n"
      "v_add_u32 v151, 4096, v151\n"
      "v_min_u32 v151, 9600, v151\n"
      "s_add_u32 m0, s10, 133120\n"
      "s_nop 0\n"
      "global_load_lds_dwordx4 v151, s[56:57]\n"
      "s_pack_hh_b32_b16 s29, s40, s48\n"
      "v_dot2_f32_bf16 v204, s29, v196, 0\n"
      "s_pack_ll_b32_b16 s30, s41, s49\n"
      "v_dot2_f32_bf16 v171, s30, v196, 0\n"
      "s_pack_hh_b32_b16 s31, s41, s49\n"
      "v_dot2_f32_bf16 v207, s31, v196, 0\n"
      "s_pack_ll_b32_b16 s28, s42, s50\n"
      "v_dot2_f32_bf16 v151, s28, v196, 0\n"
      "s_pack_hh_b32_b16 s29, s42, s50\n"
      "v_dot2_f32_bf16 v206, s29, v196, 0\n"
      "s_pack_ll_b32_b16 s30, s43, s51\n"
      "v_dot2_f32_bf16 v250, s30, v196, 0\n"
      "s_pack_hh_b32_b16 s31, s43, s51\n"
      "v_dot2_f32_bf16 v205, s31, v196, 0\n"
      "s_load_dwordx8 s[36:43], s[60:61], 41216\n"
      "s_load_dwordx4 s[44:47], s[60:61], 41248\n"
      "s_mov_b64 s[48:49], s[60:61]\n"
      "v_max_f32 v150, 0, v150\n"
      "v_max_f32 v168, 0, v168\n"
      "v_cvt_pk_bf16_f32 v150, v150, v168\n"
      "v_max_f32 v240, 0, v240\n"
      "v_max_f32 v241, 0, v241\n"
      "global_load_dwordx4 v[114:117], v198, s[60:61] offset:-4096\n"
      "v_cvt_pk_bf16_f32 v240, v240, v241\n"
      "v_max_f32 v243, 0, v243\n"
      "v_max_f32_e64 v242, 0, v242\n"
      "v_cvt_pk_bf16_f32 v243, v243, v242\n"
      "v_max_f32 v169, 0, v169\n"
      "global_load_dwordx4 a[164:167], v170, s[4:5] offset:1024\n"
      "v_max_f32 v249, 0, v249\n"
      "v_cvt_pk_bf16_f32 v169, v169, v249\n"
      "v_max_f32 v248, 0, v248\n"
      "v_max_f32 v204, 0, v204\n"
      "global_load_dwordx4 a[168:171], v192, s[4:5] offset:2048\n"
      "v_cvt_pk_bf16_f32 v248, v248, v204\n"
      "v_max_f32 v171, 0, v171\n"
      "v_max_f32 v207, 0, v207\n"
      "v_cvt_pk_bf16_f32 v171, v171, v207\n"
      "global_load_dwordx4 a[172:175], v195, s[4:5] offset:3072\n"
      "v_max_f32 v151, 0, v151\n"
      "v_max_f32 v206, 0, v206\n"
      "v_cvt_pk_bf16_f32 v151, v151, v206\n"
      "v_max_f32 v250, 0, v250\n"
      "global_load_dwordx4 a[176:179], v197, s[4:5]\n"
      "v_max_f32 v205, 0, v205\n"
      "v_cvt_pk_bf16_f32 v250, v250, v205\n"
      "s_waitcnt lgkmcnt(0)\n"
      "s_barrier\n"
      "s_pack_ll_b32_b16 s28, s64, s66\n"
      "v_dot2_f32_bf16 v204, s28, v150, 0\n"
      "s_pack_hh_b32_b16 s29, s64, s66\n"
      "v_dot2_f32_bf16 v205, s29, v150, 0\n"
      "s_pack_ll_b32_b16 s30, s65, s67\n"
      "v_dot2_f32_bf16 v206, s30, v150, 0\n"
      "s_pack_hh_b32_b16 s31, s65, s67\n"
      "v_dot2_f32_bf16 v207, s31, v150, 0\n"
      "s_pack_ll_b32_b16 s28, s68, s70\n"
      "v_dot2_f32_bf16 v204, s28, v240, v204\n"
      "s_pack_hh_b32_b16 s29, s68, s70\n"
      "v_dot2_f32_bf16 v205, s29, v240, v205\n"
      "s_pack_ll_b32_b16 s30, s69, s71\n"
      "v_dot2_f32_bf16 v206, s30, v240, v206\n"
      "s_pack_hh_b32_b16 s31, s69, s71\n"
      "v_dot2_f32_bf16 v207, s31, v240, v207\n"
      "v_add_u32 v170, 36864, v170\n"
      "v_add_u32 v195, 36864, v195\n"
      "v_add_u32 v193, 36864, v193\n"
      "v_add_u32 v194, 36864, v194\n"
      "v_add_u32 v192, 36864, v192\n"
      "v_add_u32 v197, 36864, v197\n"
      "global_load_dwordx4 a[180:183], v170, s[4:5] offset:-4096\n"
      "s_pack_ll_b32_b16 s28, s72, s74\n"
      "v_dot2_f32_bf16 v204, s28, v243, v204\n"
      "s_pack_hh_b32_b16 s29, s72, s74\n"
      "v_dot2_f32_bf16 v205, s29, v243, v205\n"
      "s_pack_ll_b32_b16 s30, s73, s75\n"
      "v_dot2_f32_bf16 v206, s30, v243, v206\n"
      "s_pack_hh_b32_b16 s31, s73, s75\n"
      "v_dot2_f32_bf16 v207, s31, v243, v207\n"
      "s_pack_ll_b32_b16 s28, s76, s78\n"
      "v_dot2_f32_bf16 v204, s28, v169, v204\n"
      "s_pack_hh_b32_b16 s29, s76, s78\n"
      "v_dot2_f32_bf16 v205, s29, v169, v205\n"
      "s_pack_ll_b32_b16 s30, s77, s79\n"
      "v_dot2_f32_bf16 v206, s30, v169, v206\n"
      "s_pack_hh_b32_b16 s31, s77, s79\n"
      "v_dot2_f32_bf16 v207, s31, v169, v207\n"
      "s_load_dwordx16 s[64:79], s[54:55], 40960\n"
      "s_lshl_b32 s28, s24, 4\n"
      "global_load_dwordx4 a[184:187], v170, s[4:5] offset:-3072\n"
      "s_lshr_b64 s[8:9], s[8:9], s28\n"
      "s_lshl_b32 s8, s8, 16\n"
      "v_mov_b32 v196, s8\n"
      "v_max_f32 v196, 0xc2c80000, v196\n"
      "v_min_f32 v196, 0x40930000, v196\n"
      "v_mul_f32 v196, 0x3fb8aa3b, v196\n"
      "v_exp_f32 v196, v196\n"
      "s_nop 0\n"
      "v_cvt_pk_bf16_f32 v196, v196, v196\n"
      "global_load_dwordx4 a[188:191], v193, s[4:5] offset:-2048\n"
      "v_lshlrev_b32_e64 v196, 16, v196\n"
      "s_pack_ll_b32_b16 s28, s84, s86\n"
      "v_dot2_f32_bf16 v204, s28, v248, v204\n"
      "s_pack_hh_b32_b16 s29, s84, s86\n"
      "v_dot2_f32_bf16 v205, s29, v248, v205\n"
      "s_pack_ll_b32_b16 s30, s85, s87\n"
      "v_dot2_f32_bf16 v206, s30, v248, v206\n"
      "global_load_dwordx4 a[192:195], v195, s[4:5] offset:-1024\n"
      "s_pack_hh_b32_b16 s31, s85, s87\n"
      "v_dot2_f32_bf16 v207, s31, v248, v207\n"
      "s_pack_ll_b32_b16 s28, s88, s90\n"
      "v_dot2_f32_bf16 v204, s28, v171, v204\n"
      "s_pack_hh_b32_b16 s29, s88, s90\n"
      "v_dot2_f32_bf16 v205, s29, v171, v205\n"
      "s_pack_ll_b32_b16 s30, s89, s91\n"
      "v_dot2_f32_bf16 v206, s30, v171, v206\n"
      "global_load_dwordx4 a[196:199], v194, s[4:5]\n"
      "s_pack_hh_b32_b16 s31, s89, s91\n"
      "v_dot2_f32_bf16 v207, s31, v171, v207\n"
      "s_pack_ll_b32_b16 s28, s92, s94\n"
      "v_dot2_f32_bf16 v204, s28, v151, v204\n"
      "s_pack_hh_b32_b16 s29, s92, s94\n"
      "v_dot2_f32_bf16 v205, s29, v151, v205\n"
      "s_pack_ll_b32_b16 s30, s93, s95\n"
      "v_dot2_f32_bf16 v206, s30, v151, v206\n"
      "global_load_dwordx4 a[200:203], v170, s[4:5] offset:1024\n"
      "s_pack_hh_b32_b16 s31, s93, s95\n"
      "v_dot2_f32_bf16 v207, s31, v151, v207\n"
      "s_pack_ll_b32_b16 s28, s96, s98\n"
      "v_dot2_f32_bf16 v204, s28, v250, v204\n"
      "s_pack_hh_b32_b16 s29, s96, s98\n"
      "v_dot2_f32_bf16 v205, s29, v250, v205\n"
      "s_pack_ll_b32_b16 s30, s97, s99\n"
      "global_load_dwordx4 a[204:207], v192, s[4:5] offset:2048\n"
      "v_dot2_f32_bf16 v206, s30, v250, v206\n"
      "s_pack_hh_b32_b16 s31, s97, s99\n"
      "v_dot2_f32_bf16 v207, s31, v250, v207\n"
      "s_load_dwordx16 s[80:95], s[52:53], 41280\n"
      "s_load_dwordx4 s[96:99], s[52:53], 41344\n"
      "s_load_dwordx2 s[100:101], s[52:53], 41360\n"
      "s_load_dword s26, s[52:53], 41376\n"
      "s_load_dword s3, s[52:53], 41380\n"
      "global_load_dwordx4 a[208:211], v195, s[4:5] offset:3072\n"
      "ds_write_b128 v229, a[72:75]\n"
      "ds_write_b128 v229, a[76:79] offset:1024\n"
      "ds_write_b128 v229, a[80:83] offset:2048\n"
      "ds_write_b128 v229, a[84:87] offset:3072\n"
      "ds_write_b128 v229, a[88:91] offset:4096\n"
      "ds_write_b128 v229, a[92:95] offset:5120\n"
      "ds_write_b128 v229, a[96:99] offset:6144\n"
      "global_load_dwordx4 a[212:215], v197, s[4:5]\n"
      "ds_write_b128 v229, a[100:103] offset:7168\n"
      "ds_write_b128 v229, a[104:107] offset:8192\n"
      "ds_write_b128 v229, a[108:111] offset:36864\n"
      "ds_write_b128 v229, a[112:115] offset:37888\n"
      "ds_write_b128 v229, a[116:119] offset:38912\n"
      "ds_write_b128 v229, a[120:123] offset:39936\n"
      "ds_write_b128 v229, a[124:127] offset:40960\n"
      "ds_write_b128 v229, a[128:131] offset:41984\n"
      "global_load_dwordx4 v[168:171], v198, s[60:61] offset:3072\n"
      "ds_write_b128 v229, a[132:135] offset:43008\n"
      "ds_write_b128 v229, a[136:139] offset:44032\n"
      "ds_write_b128 v229, a[140:143] offset:45056\n"
      "s_waitcnt vmcnt(15)\n"
      "s_waitcnt lgkmcnt(0)\n"
      "s_barrier\n"
      "L_sched_%=:\n"
      "ds_read_b64_tr_b16 a[76:77], v149\n"
      "s_waitcnt vmcnt(14)\n"
      "ds_read_b128 v[248:251], v114 offset:18432\n"
      "ds_read_b128 v[192:195], v115 offset:18432\n"
      "ds_read_b64_tr_b16 a[82:83], v231 offset:4608\n"
      "ds_read_b64_tr_b16 a[88:89], v149 offset:384\n"
      "v_cvt_pk_bf16_f32 v207, v206, v207\n"
      "v_cmp_nge_f32 vcc, 0x42480000, v196\n"
      "v_lshlrev_b32 v206, 16, v207\n"
      "s_waitcnt vmcnt(0)\n"
      "v_add_u32 v197, 3072, v171\n"
      "v_cvt_pk_bf16_f32 v205, v204, v205\n"
      "v_lshlrev_b32 v204, 16, v205\n"
      "s_mov_b64 s[50:51], vcc\n"
      "ds_read_b128 v[240:243], v114 offset:20480\n"
      "ds_read_b128 v[152:155], v115 offset:20480\n"
      "ds_read_b64_tr_b16 a[80:81], v231\n"
      "v_and_b32 v207, 0xffff0000, v207\n"
      "v_mul_f32 v207, 0xbfb8aa3b, v207\n"
      "v_and_b32 v205, 0xffff0000, v205\n"
      "v_mul_f32 v206, 0xbfb8aa3b, v206\n"
      "v_mul_f32 v204, 0xbfb8aa3b, v204\n"
      "v_mul_f32 v205, 0xbfb8aa3b, v205\n"
      "v_exp_f32 v207, v207\n"
      "v_exp_f32 v206, v206\n"
      "v_exp_f32 v204, v204\n"
      "v_exp_f32 v205, v205\n"
      "s_waitcnt lgkmcnt(5)\n"
      "v_mfma_f32_16x16x32_bf16 v[200:203], a[0:3], v[248:251], 0\n"
      "ds_read_b64_tr_b16 a[90:91], v149 offset:4992\n"
      "v_add_f32 v204, 1.0, v204\n"
      "v_mfma_f32_16x16x32_bf16 v[200:203], a[36:39], v[192:195], v[200:203]\n"
      "global_load_dwordx4 a[216:219], v197, s[6:7] offset:-3072\n"
      "v_mfma_f32_16x16x32_bf16 v[176:179], a[4:7], v[248:251], 0\n"
      "ds_read_b64_tr_b16 a[94:95], v231 offset:4992\n"
      "v_add_f32_e64 v205, 1.0, v205\n"
      "v_mfma_f32_16x16x32_bf16 v[176:179], a[40:43], v[192:195], v[176:179]\n"
      "ds_read_b64_tr_b16 a[100:101], v149 offset:768\n"
      "ds_read_b64_tr_b16 a[84:85], v148 offset:384\n"
      "ds_read_b64_tr_b16 a[96:97], v148 offset:768\n"
      "s_waitcnt lgkmcnt(5)\n"
      "v_mfma_f32_16x16x32_bf16 v[212:215], a[8:11], v[248:251], 0\n"
      "ds_read_b64_tr_b16 a[98:99], v148 offset:5376\n"
      "v_add_f32 v206, 1.0, v206\n"
      "v_mfma_f32_16x16x32_bf16 v[212:215], a[44:47], v[192:195], v[212:215]\n"
      "ds_read_b64_tr_b16 a[92:93], v231 offset:384\n"
      "ds_read_b64_tr_b16 a[86:87], v148 offset:4992\n"
      "v_mfma_f32_16x16x32_bf16 v[184:187], a[12:15], v[248:251], 0\n"
      "ds_read_b64_tr_b16 a[102:103], v149 offset:5376\n"
      "v_mul_f32 v196, 0x3fb8aa3b, v196\n"
      "v_mfma_f32_16x16x32_bf16 v[184:187], a[48:51], v[192:195], v[184:187]\n"
      "global_load_dwordx4 a[220:223], v197, s[6:7] offset:-2944\n"
      "v_rcp_f32_e64 v205, v205\n"
      "v_mfma_f32_16x16x32_bf16 v[244:247], a[16:19], v[248:251], 0\n"
      "ds_read_b64_tr_b16 a[78:79], v149 offset:4608\n"
      "v_add_f32_e64 v207, 1.0, v207\n"
      "v_mfma_f32_16x16x32_bf16 v[244:247], a[52:55], v[192:195], v[244:247]\n"
      "ds_read_b64_tr_b16 a[104:105], v231 offset:768\n"
      "v_rcp_f32_e64 v204, v204\n"
      "v_mfma_f32_16x16x32_bf16 v[232:235], a[20:23], v[248:251], 0\n"
      "ds_read_b64_tr_b16 a[74:75], v148 offset:4608\n"
      "ds_read_b64_tr_b16 a[72:73], v148\n"
      "v_mfma_f32_16x16x32_bf16 v[232:235], a[56:59], v[192:195], v[232:235]\n"
      "ds_read_b64_tr_b16 a[106:107], v231 offset:5376\n"
      "ds_read_b64_tr_b16 a[110:111], v148 offset:41472\n"
      "v_mfma_f32_16x16x32_bf16 v[216:219], a[24:27], v[248:251], 0\n"
      "ds_read_b64_tr_b16 a[112:113], v149 offset:36864\n"
      "v_cvt_pk_bf16_f32 v205, v204, v205\n"
      "v_mfma_f32_16x16x32_bf16 v[216:219], a[60:63], v[192:195], v[216:219]\n"
      "ds_read_b64_tr_b16 a[114:115], v149 offset:41472\n"
      "v_rcp_f32 v206, v206\n"
      "v_mfma_f32_16x16x32_bf16 v[188:191], a[28:31], v[248:251], 0\n"
      "ds_read_b64_tr_b16 a[116:117], v231 offset:36864\n"
      "v_lshlrev_b32 v204, 16, v205\n"
      "v_mfma_f32_16x16x32_bf16 v[188:191], a[64:67], v[192:195], v[188:191]\n"
      "ds_read_b64_tr_b16 a[118:119], v231 offset:41472\n"
      "v_and_b32 v205, 0xffff0000, v205\n"
      "v_mfma_f32_16x16x32_bf16 v[180:183], a[32:35], v[248:251], 0\n"
      "global_load_dwordx4 a[224:227], v197, s[6:7] offset:-2816\n"
      "v_add_f32 v204, -0.5, v204\n"
      "v_mfma_f32_16x16x32_bf16 v[180:183], a[68:71], v[192:195], v[180:183]\n"
      "ds_read_b64_tr_b16 a[108:109], v148 offset:36864\n"
      "v_rcp_f32 v207, v207\n"
      "v_mfma_f32_16x16x32_bf16 v[192:195], a[0:3], v[240:243], 0\n"
      "ds_read_b64_tr_b16 a[122:123], v148 offset:41856\n"
      "v_cvt_pk_bf16_f32 v207, v206, v207\n"
      "v_mfma_f32_16x16x32_bf16 v[192:195], a[36:39], v[152:155], v[192:195]\n"
      "ds_read_b64_tr_b16 a[120:121], v148 offset:37248\n"
      "ds_read_b64_tr_b16 a[124:125], v149 offset:37248\n"
      "v_mfma_f32_16x16x32_bf16 v[248:251], a[4:7], v[240:243], 0\n"
      "ds_read_b64_tr_b16 a[126:127], v149 offset:41856\n"
      "v_lshlrev_b32 v206, 16, v207\n"
      "v_mfma_f32_16x16x32_bf16 v[248:251], a[40:43], v[152:155], v[248:251]\n"
      "ds_read_b64_tr_b16 a[128:129], v231 offset:37248\n"
      "v_add_f32 v205, -0.5, v205\n"
      "v_mfma_f32_16x16x32_bf16 v[144:147], a[8:11], v[240:243], 0\n"
      "ds_read_b64_tr_b16 a[130:131], v231 offset:41856\n"
      "v_mul_f32 v204, 0x41b8aa3b, v204\n"
      "v_mfma_f32_16x16x32_bf16 v[144:147], a[44:47], v[152:155], v[144:147]\n"
      "global_load_dwordx4 a[228:231], v197, s[6:7]\n"
      "v_mfma_f32_16x16x32_bf16 v[140:143], a[12:15], v[240:243], 0\n"
      "ds_read_b64_tr_b16 a[134:135], v148 offset:42240\n"
      "v_add_f32_e64 v206, -0.5, v206\n"
      "v_mfma_f32_16x16x32_bf16 v[140:143], a[48:51], v[152:155], v[140:143]\n"
      "global_load_dwordx4 a[232:235], v197, s[6:7] offset:128\n"
      "v_mfma_f32_16x16x32_bf16 v[164:167], a[16:19], v[240:243], 0\n"
      "ds_read_b64_tr_b16 a[136:137], v149 offset:37632\n"
      "v_mul_f32 v206, 0x41b8aa3b, v206\n"
      "v_mfma_f32_16x16x32_bf16 v[208:211], a[20:23], v[240:243], 0\n"
      "ds_read_b64_tr_b16 a[138:139], v149 offset:42240\n"
      "v_and_b32 v207, 0xffff0000, v207\n"
      "v_mfma_f32_16x16x32_bf16 v[208:211], a[56:59], v[152:155], v[208:211]\n"
      "ds_read_b64_tr_b16 a[140:141], v231 offset:37632\n"
      "v_add_f32_e64 v207, -0.5, v207\n"
      "v_mul_f32 v205, 0x41b8aa3b, v205\n"
      "v_mul_f32 v207, 0x41b8aa3b, v207\n"
      "v_mfma_f32_16x16x32_bf16 v[224:227], a[24:27], v[240:243], 0\n"
      "ds_read_b64_tr_b16 a[132:133], v148 offset:37632\n"
      "v_mfma_f32_16x16x32_bf16 v[164:167], a[52:55], v[152:155], v[164:167]\n"
      "global_load_dwordx4 a[236:239], v197, s[6:7] offset:256\n"
      "v_add_u32 v197, 9216, v171\n"
      "v_mfma_f32_16x16x32_bf16 v[136:139], a[28:31], v[240:243], 0\n"
      "global_load_dwordx4 a[240:243], v197, s[6:7] offset:-3072\n"
      "ds_write_b32 v199, v205 offset:768\n"
      "ds_write_b32 v199, v206 offset:1536\n"
      "v_mfma_f32_16x16x32_bf16 v[136:139], a[64:67], v[152:155], v[136:139]\n"
      "ds_write_b32 v199, v207 offset:2304\n"
      "v_mfma_f32_16x16x32_bf16 v[224:227], a[60:63], v[152:155], v[224:227]\n"
      "ds_write_b32 v199, v204\n"
      "v_mfma_f32_16x16x32_bf16 v[252:255], a[32:35], v[240:243], 0\n"
      "ds_read_b64_tr_b16 a[142:143], v231 offset:42240\n"
      "v_mfma_f32_16x16x32_bf16 v[252:255], a[68:71], v[152:155], v[252:255]\n"
      "global_load_dwordx4 a[244:247], v197, s[6:7] offset:-2944\n"
      "global_load_dwordx4 a[248:251], v197, s[6:7] offset:-2816\n"
      "s_waitcnt lgkmcnt(0)\n"
      "s_barrier\n"
      "ds_read_b128 v[204:207], v114 offset:24576\n"
      "ds_read_b128 v[152:155], v115 offset:24576\n"
      "v_mov_b32 v43, 0\n"
      "v_readfirstlane_b32 s36, v196\n"
      "v_mov_b32 v65, 0\n"
      "v_mov_b32 v57, 0\n"
      "v_mov_b32 v42, 0\n"
      "v_mov_b32 v61, 0\n"
      "s_mov_b32 s11, 100466688\n"
      "v_add_u32 v196, 3136, v168\n"
      "s_sub_u32 s16, s16, 99876864\n"
      "ds_read2_b32 v[98:99], v196 offset0:32 offset1:33\n"
      "global_load_dwordx4 v[118:121], v198, s[48:49] offset:-3072\n"
      "v_mov_b32 v36, 0\n"
      "s_subb_u32 s17, s17, 0\n"
      "v_mov_b32 v199, 134528\n"
      "v_mov_b32_e64 v49, 0\n"
      "s_sub_u32 s22, s22, 99876864\n"
      "ds_read2_b32 v[86:87], v168 offset0:32 offset1:33\n"
      "v_mov_b32 v53, 0\n"
      "s_subb_u32 s23, s23, 0\n"
      "ds_read2_b32 v[96:97], v196 offset0:18 offset1:19\n"
      "ds_read2_b32 v[78:79], v168 offset0:0 offset1:1\n"
      "ds_read_b128 v[160:163], v115 offset:26624\n"
      "s_add_u32 s12, s20, 16777216\n"
      "s_waitcnt lgkmcnt(5)\n"
      "v_mfma_f32_16x16x32_bf16 v[200:203], a[72:75], v[204:207], v[200:203]\n"
      "ds_read2_b32 v[82:83], v168 offset0:16 offset1:17\n"
      "v_add_u32 v150, 6272, v168\n"
      "v_mfma_f32_16x16x32_bf16 v[200:203], a[108:111], v[152:155], v[200:203]\n"
      "ds_read_b128 v[240:243], v114 offset:26624\n"
      "ds_read2_b32 v[84:85], v168 offset0:18 offset1:19\n"
      "v_mfma_f32_16x16x32_bf16 v[176:179], a[76:79], v[204:207], v[176:179]\n"
      "ds_read2_b32 v[88:89], v168 offset0:34 offset1:35\n"
      "s_addc_u32 s13, s21, 0\n"
      "v_mfma_f32_16x16x32_bf16 v[176:179], a[112:115], v[152:155], v[176:179]\n"
      "global_load_dwordx4 v[122:125], v198, s[48:49] offset:-2048\n"
      "v_mfma_f32_16x16x32_bf16 v[212:215], a[80:83], v[204:207], v[212:215]\n"
      "ds_read2_b32 v[100:101], v196 offset0:34 offset1:35\n"
      "s_mul_i32 s2, s24, 768\n"
      "v_mfma_f32_16x16x32_bf16 v[212:215], a[116:119], v[152:155], v[212:215]\n"
      "ds_read2_b32 v[94:95], v196 offset0:16 offset1:17\n"
      "ds_read2_b32 v[90:91], v196 offset0:0 offset1:1\n"
      "v_mfma_f32_16x16x32_bf16 v[184:187], a[84:87], v[204:207], v[184:187]\n"
      "ds_read2_b32 v[80:81], v168 offset0:2 offset1:3\n"
      "v_mov_b32 v48, 0\n"
      "v_mfma_f32_16x16x32_bf16 v[184:187], a[120:123], v[152:155], v[184:187]\n"
      "ds_read2_b32 v[106:107], v150 offset0:16 offset1:17\n"
      "ds_read2_b32 v[92:93], v196 offset0:2 offset1:3\n"
      "v_mfma_f32_16x16x32_bf16 v[244:247], a[88:91], v[204:207], v[244:247]\n"
      "ds_read_b32 v73, v170 offset:64\n"
      "ds_read_b32 v70, v169 offset:192\n"
      "v_mfma_f32_16x16x32_bf16 v[244:247], a[124:127], v[152:155], v[244:247]\n"
      "ds_read2_b32 v[108:109], v150 offset0:18 offset1:19\n"
      "ds_read2_b32 v[110:111], v150 offset0:32 offset1:33\n"
      "v_mfma_f32_16x16x32_bf16 v[232:235], a[92:95], v[204:207], v[232:235]\n"
      "ds_read2_b32 v[104:105], v150 offset0:2 offset1:3\n"
      "ds_read_b32 v72, v169 offset:3328\n"
      "v_mfma_f32_16x16x32_bf16 v[232:235], a[128:131], v[152:155], v[232:235]\n"
      "s_waitcnt lgkmcnt(12)\n"
      "ds_read2_b32 v[102:103], v150 offset0:0 offset1:1\n"
      "v_mfma_f32_16x16x32_bf16 v[216:219], a[96:99], v[204:207], v[216:219]\n"
      "global_load_dwordx4 v[236:239], v230, s[48:49] offset:-3072\n"
      "v_mfma_f32_16x16x32_bf16 v[216:219], a[132:135], v[152:155], v[216:219]\n"
      "ds_write_b128 v229, a[192:195] offset:39936\n"
      "v_mfma_f32_16x16x32_bf16 v[188:191], a[100:103], v[204:207], v[188:191]\n"
      "s_add_u32 s2, s2, 150640\n"
      "ds_read_b32 v74, v169 offset:6464\n"
      "v_mfma_f32_16x16x32_bf16 v[188:191], a[136:139], v[152:155], v[188:191]\n"
      "ds_write_b128 v229, a[160:163] offset:4096\n"
      "v_mfma_f32_16x16x32_bf16 v[180:183], a[104:107], v[204:207], v[180:183]\n"
      "ds_read_b32 v77, v199\n"
      "v_mov_b32_e64 v37, 0\n"
      "v_mfma_f32_16x16x32_bf16 v[180:183], a[140:143], v[152:155], v[180:183]\n"
      "ds_read2_b32 v[112:113], v150 offset0:34 offset1:35\n"
      "ds_read_b32 v75, v170 offset:128\n"
      "v_mfma_f32_16x16x32_bf16 v[192:195], a[72:75], v[240:243], v[192:195]\n"
      "ds_write_b128 v229, a[168:171] offset:6144\n"
      "v_mfma_f32_16x16x32_bf16 v[192:195], a[108:111], v[160:163], v[192:195]\n"
      "ds_write_b128 v229, a[144:147]\n"
      "v_mfma_f32_16x16x32_bf16 v[248:251], a[76:79], v[240:243], v[248:251]\n"
      "global_load_dwordx4 v[204:207], v230, s[48:49] offset:-4096\n"
      "v_mfma_f32_16x16x32_bf16 v[248:251], a[112:115], v[160:163], v[248:251]\n"
      "ds_write_b128 v229, a[148:151] offset:1024\n"
      "v_mfma_f32_16x16x32_bf16 v[144:147], a[80:83], v[240:243], v[144:147]\n"
      "ds_read_b32 v71, v170\n"
      "v_mfma_f32_16x16x32_bf16 v[144:147], a[116:119], v[160:163], v[144:147]\n"
      "ds_write_b128 v229, a[152:155] offset:2048\n"
      "v_mfma_f32_16x16x32_bf16 v[140:143], a[84:87], v[240:243], v[140:143]\n"
      "ds_write_b128 v229, a[204:207] offset:43008\n"
      "v_mfma_f32_16x16x32_bf16 v[140:143], a[120:123], v[160:163], v[140:143]\n"
      "ds_write_b128 v229, a[156:159] offset:3072\n"
      "v_mfma_f32_16x16x32_bf16 v[208:211], a[92:95], v[240:243], v[208:211]\n"
      "ds_write_b128 v229, a[164:167] offset:5120\n"
      "v_mfma_f32_16x16x32_bf16 v[224:227], a[96:99], v[240:243], v[224:227]\n"
      "ds_write_b128 v229, a[200:203] offset:41984\n"
      "v_mfma_f32_16x16x32_bf16 v[136:139], a[100:103], v[240:243], v[136:139]\n"
      "ds_write_b128 v229, a[196:199] offset:40960\n"
      "v_mfma_f32_16x16x32_bf16 v[224:227], a[132:135], v[160:163], v[224:227]\n"
      "ds_write_b128 v229, a[212:215] offset:45056\n"
      "v_mfma_f32_16x16x32_bf16 v[252:255], a[104:107], v[240:243], v[252:255]\n"
      "ds_write_b128 v229, a[180:183] offset:36864\n"
      "v_mfma_f32_16x16x32_bf16 v[164:167], a[88:91], v[240:243], v[164:167]\n"
      "ds_write_b128 v229, a[188:191] offset:38912\n"
      "v_mfma_f32_16x16x32_bf16 v[136:139], a[136:139], v[160:163], v[136:139]\n"
      "ds_write_b128 v229, a[172:175] offset:7168\n"
      "v_mfma_f32_16x16x32_bf16 v[164:167], a[124:127], v[160:163], v[164:167]\n"
      "ds_write_b128 v229, a[176:179] offset:8192\n"
      "v_mfma_f32_16x16x32_bf16 v[252:255], a[140:143], v[160:163], v[252:255]\n"
      "ds_write_b128 v229, a[208:211] offset:44032\n"
      "v_mfma_f32_16x16x32_bf16 v[208:211], a[128:131], v[160:163], v[208:211]\n"
      "ds_write_b128 v229, a[184:187] offset:37888\n"
      "s_waitcnt lgkmcnt(0)\n"
      "s_barrier\n"
      "v_lshl_add_u32 v85, v85, 2, s2\n"
      "s_mov_b32 s60, 100270464\n"
      "v_lshl_add_u32 v87, v87, 2, s2\n"
      "s_mov_b32 s61, 196224\n"
      "v_lshl_add_u32 v78, v78, 2, s2\n"
      "s_lshr_b32 s8, s80, 8\n"
      "s_and_b32 s9, s80, 0xff\n"
      "v_lshl_add_u32 v86, v86, 2, s2\n"
      "s_waitcnt vmcnt(1)\n"
      "v_mov_b32 v153, v237\n"
      "s_mul_i32 s10, s8, 73\n"
      "ds_read_b64_tr_b16 a[148:149], v149\n"
      "s_add_u32 s10, s10, s9\n"
      "ds_read_b64_tr_b16 a[172:173], v149 offset:768\n"
      "v_lshl_add_u32 v80, v80, 2, s2\n"
      "v_lshl_add_u32 v79, v79, 2, s2\n"
      "v_mov_b32 v68, v236\n"
      "v_mov_b32 v155, v237\n"
      "v_mov_b32_e64 v152, v237\n"
      "v_mov_b32_e64 v67, v236\n"
      "v_mov_b32_e64 v66, v236\n"
      "v_mov_b32_e64 v69, v236\n"
      "v_lshl_add_u32 v89, v89, 2, s2\n"
      "s_mul_i32 s28, s10, 4802\n"
      "v_mov_b32 v154, v237\n"
      "s_add_u32 s56, s18, s28\n"
      "ds_read_b64_tr_b16 a[210:211], v149 offset:42240\n"
      "ds_read_b64_tr_b16 a[206:207], v148 offset:42240\n"
      "v_lshl_add_u32 v84, v84, 2, s2\n"
      "v_mov_b32 v237, 0\n"
      "s_addc_u32 s57, s19, 0\n"
      "ds_read_b64_tr_b16 a[146:147], v148 offset:4608\n"
      "s_and_b32 s56, s56, -16\n"
      "s_sub_u32 s58, s56, 110592\n"
      "ds_read_b64_tr_b16 a[188:189], v231 offset:36864\n"
      "ds_read_b64_tr_b16 a[144:145], v148\n"
      "s_subb_u32 s59, s57, 0\n"
      "ds_read_b128 v[172:175], v114 offset:32768\n"
      "ds_read_b128 v[156:159], v115 offset:30720\n"
      "s_sub_u32 s56, s56, 73728\n"
      "ds_read_b128 v[160:163], v114 offset:30720\n"
      "s_subb_u32 s57, s57, 0\n"
      "ds_read_b64_tr_b16 a[154:155], v231 offset:4608\n"
      "ds_read_b64_tr_b16 a[164:165], v231 offset:384\n"
      "v_lshl_add_u32 v81, v81, 2, s2\n"
      "s_mul_i32 s28, s8, 1373568\n"
      "v_lshl_add_u32 v82, v82, 2, s2\n"
      "s_mul_i32 s29, s9, 2688\n"
      "s_add_u32 s28, s28, s29\n"
      "ds_read_b64_tr_b16 a[184:185], v149 offset:36864\n"
      "s_cmp_eq_u32 s8, 72\n"
      "ds_read_b64_tr_b16 a[166:167], v231 offset:4992\n"
      "ds_read_b64_tr_b16 a[212:213], v231 offset:37632\n"
      "s_cselect_b32 s8, 0, 100270464\n"
      "v_mov_b32 v236, 0\n"
      "s_cmp_eq_u32 s9, 72\n"
      "ds_read_b64_tr_b16 a[196:197], v149 offset:37248\n"
      "s_cselect_b32 s9, 0, 196224\n"
      "ds_read_b64_tr_b16 a[150:151], v149 offset:4608\n"
      "ds_read_b64_tr_b16 a[208:209], v149 offset:37632\n"
      "ds_read_b64_tr_b16 a[194:195], v148 offset:41856\n"
      "ds_read_b64_tr_b16 a[156:157], v148 offset:384\n"
      "s_add_u32 s10, s8, s9\n"
      "ds_read_b64_tr_b16 a[198:199], v149 offset:41856\n"
      "v_lshl_add_u32 v83, v83, 2, s2\n"
      "s_add_u32 s28, s28, s10\n"
      "ds_read_b64_tr_b16 a[170:171], v148 offset:5376\n"
      "ds_read_b64_tr_b16 a[192:193], v148 offset:37248\n"
      "ds_read_b64_tr_b16 a[182:183], v148 offset:41472\n"
      "ds_read_b64_tr_b16 a[200:201], v231 offset:37248\n"
      "ds_read_b64_tr_b16 a[186:187], v149 offset:41472\n"
      "ds_read_b64_tr_b16 a[158:159], v148 offset:4992\n"
      "ds_read_b64_tr_b16 a[180:181], v148 offset:36864\n"
      "s_add_u32 s52, s16, s28\n"
      "v_lshl_add_u32 v88, v88, 2, s2\n"
      "v_lshl_add_u32 v91, v91, 2, s2\n"
      "s_addc_u32 s53, s17, 0\n"
      "s_cmp_lg_u32 s10, s11\n"
      "v_add_u32 v228, 99876864, v228\n"
      "s_cbranch_scc1 L_edge_1_%=\n"
      "L_eb_2_%=:\n"
      "s_add_u32 m0, s76, 0\n"
      "ds_read_b64_tr_b16 a[204:205], v148 offset:37632\n"
      "ds_read_b64_tr_b16 a[190:191], v231 offset:41472\n"
      "ds_read_b64_tr_b16 a[174:175], v149 offset:5376\n"
      "global_load_lds_dwordx4 v116, s[52:53]\n"
      "ds_read_b64_tr_b16 a[152:153], v231\n"
      "ds_read_b64_tr_b16 a[202:203], v231 offset:41856\n"
      "ds_read_b32 v84, v84\n"
      "ds_read_b64_tr_b16 a[178:179], v231 offset:5376\n"
      "v_lshl_add_u32 v90, v90, 2, s2\n"
      "s_add_u32 m0, s76, 6016\n"
      "ds_read_b32 v89, v89\n"
      "ds_read_b32 v87, v87\n"
      "ds_read_b64_tr_b16 a[160:161], v149 offset:384\n"
      "ds_read_b64_tr_b16 a[176:177], v231 offset:768\n"
      "ds_read_b64_tr_b16 a[168:169], v148 offset:768\n"
      "v_mov_b32 v240, 0\n"
      "v_mov_b32_e64 v241, 0\n"
      "s_waitcnt lgkmcnt(12)\n"
      "ds_read_b64_tr_b16 a[162:163], v149 offset:4992\n"
      "v_mfma_f32_16x16x32_bf16 v[200:203], a[144:147], v[160:163], v[200:203]\n"
      "ds_read_b128 v[148:151], v115 offset:32768\n"
      "ds_read_b64_tr_b16 a[214:215], v231 offset:42240\n"
      "global_load_lds_dwordx4 v116, s[52:53] offset:128\n"
      "v_lshl_add_u32 v75, v75, 2, s2\n"
      "v_mfma_f32_16x16x32_bf16 v[200:203], a[180:183], v[156:159], v[200:203]\n"
      "ds_read_b32 v80, v80\n"
      "ds_read_b32 v79, v79\n"
      "ds_read_b32 v82, v82\n"
      "ds_read_b32 v83, v83\n"
      "v_lshl_add_u32 v99, v99, 2, s2\n"
      "v_mfma_f32_16x16x32_bf16 v[176:179], a[148:151], v[160:163], v[176:179]\n"
      "s_waitcnt lgkmcnt(10)\n"
      "ds_read_b32 v85, v85\n"
      "v_mfma_f32_16x16x32_bf16 v[176:179], a[184:187], v[156:159], v[176:179]\n"
      "s_waitcnt vmcnt(2)\n"
      "ds_write_b32 v205, v228\n"
      "ds_read_b32 v81, v81\n"
      "v_mfma_f32_16x16x32_bf16 v[212:215], a[152:155], v[160:163], v[212:215]\n"
      "s_add_u32 m0, s76, 12032\n"
      "v_lshl_add_u32 v92, v92, 2, s2\n"
      "v_lshl_add_u32 v93, v93, 2, s2\n"
      "v_mfma_f32_16x16x32_bf16 v[212:215], a[188:191], v[156:159], v[212:215]\n"
      "global_load_lds_dwordx4 v116, s[52:53] offset:256\n"
      "s_waitcnt lgkmcnt(7)\n"
      "v_mfma_f32_16x16x32_bf16 v[184:187], a[156:159], v[160:163], v[184:187]\n"
      "ds_read_b32 v86, v86\n"
      "v_lshl_add_u32 v94, v94, 2, s2\n"
      "v_mfma_f32_16x16x32_bf16 v[184:187], a[192:195], v[156:159], v[184:187]\n"
      "s_add_u32 m0, s77, 0\n"
      "ds_read_b32 v88, v88\n"
      "global_load_lds_dwordx4 v117, s[52:53]\n"
      "v_lshl_add_u32 v112, v112, 2, s2\n"
      "v_lshl_add_u32 v108, v108, 2, s2\n"
      "v_lshl_add_u32 v101, v101, 2, s2\n"
      "v_lshl_add_u32 v95, v95, 2, s2\n"
      "s_add_u32 m0, s78, 0\n"
      "v_lshl_add_u32 v100, v100, 2, s2\n"
      "s_mov_b32 exec_hi, 0\n"
      "global_load_lds_dwordx4 v118, s[52:53]\n"
      "s_mov_b32 exec_hi, -1\n"
      "v_mfma_f32_16x16x32_bf16 v[244:247], a[160:163], v[160:163], v[244:247]\n"
      "ds_read_b32 v90, v90\n"
      "s_add_u32 m0, s76, 110592\n"
      "v_mfma_f32_16x16x32_bf16 v[244:247], a[196:199], v[156:159], v[244:247]\n"
      "ds_read_b32 v78, v78\n"
      "v_lshl_add_u32 v98, v98, 2, s2\n"
      "v_mfma_f32_16x16x32_bf16 v[188:191], a[172:175], v[160:163], v[188:191]\n"
      "global_load_lds_dwordx4 v119, s[56:57]\n"
      "s_add_u32 m0, s79, 0\n"
      "v_mfma_f32_16x16x32_bf16 v[232:235], a[164:167], v[160:163], v[232:235]\n"
      "ds_read_b32 v94, v94\n"
      "global_load_lds_dword v122, s[58:59]\n"
      "global_load_dwordx4 v[126:129], v198, s[48:49] offset:-1024\n"
      "v_mfma_f32_16x16x32_bf16 v[216:219], a[168:171], v[160:163], v[216:219]\n"
      "ds_read_b32 v95, v95\n"
      "v_lshl_add_u32 v73, v73, 2, s2\n"
      "v_mfma_f32_16x16x32_bf16 v[216:219], a[204:207], v[156:159], v[216:219]\n"
      "global_load_dwordx4 v[130:133], v198, s[48:49]\n"
      "ds_read_b32 v93, v93\n"
      "v_lshl_add_u32 v74, v74, 2, s2\n"
      "v_lshl_add_u32 v71, v71, 2, s2\n"
      "v_mfma_f32_16x16x32_bf16 v[232:235], a[200:203], v[156:159], v[232:235]\n"
      "global_load_dwordx4 v[228:231], v198, s[48:49] offset:1024\n"
      "global_load_dwordx4 a[252:255], v197, s[6:7]\n"
      "v_lshl_add_u32 v109, v109, 2, s2\n"
      "v_lshl_add_u32 v103, v103, 2, s2\n"
      "v_lshl_add_u32 v70, v70, 2, s2\n"
      "v_lshl_add_u32 v106, v106, 2, s2\n"
      "v_lshl_add_u32 v72, v72, 2, s2\n"
      "v_mfma_f32_16x16x32_bf16 v[180:183], a[176:179], v[160:163], v[180:183]\n"
      "ds_read_b32 v92, v92\n"
      "v_lshl_add_u32 v105, v105, 2, s2\n"
      "v_mfma_f32_16x16x32_bf16 v[180:183], a[212:215], v[156:159], v[180:183]\n"
      "ds_read_b32 v91, v91\n"
      "v_lshl_add_u32 v102, v102, 2, s2\n"
      "v_lshl_add_u32 v110, v110, 2, s2\n"
      "v_lshl_add_u32 v111, v111, 2, s2\n"
      "v_lshl_add_u32 v97, v97, 2, s2\n"
      "v_lshl_add_u32 v107, v107, 2, s2\n"
      "v_fma_f32 v205, v184, v184, s38\n"
      "v_lshl_add_u32 v96, v96, 2, s2\n"
      "v_mfma_f32_16x16x32_bf16 v[188:191], a[208:211], v[156:159], v[188:191]\n"
      "global_load_dwordx4 v[0:3], v197, s[6:7] offset:128\n"
      "v_mfma_f32_16x16x32_bf16 v[192:195], a[144:147], v[172:175], v[192:195]\n"
      "ds_read_b32 v96, v96\n"
      "v_fma_f32 v242, v200, v200, s38\n"
      "v_mfma_f32_16x16x32_bf16 v[192:195], a[180:183], v[148:151], v[192:195]\n"
      "global_load_dwordx4 v[4:7], v197, s[6:7] offset:256\n"
      "v_fmac_f32_e64 v205, v185, v185\n"
      "v_mfma_f32_16x16x32_bf16 v[248:251], a[148:151], v[172:175], v[248:251]\n"
      "ds_read_b32 v97, v97\n"
      "v_add_u32 v197, 15360, v171\n"
      "v_mfma_f32_16x16x32_bf16 v[248:251], a[184:187], v[148:151], v[248:251]\n"
      "ds_read_b32 v99, v99\n"
      "v_fmac_f32 v242, v201, v201\n"
      "v_fmac_f32 v242, v202, v202\n"
      "v_lshl_add_u32 v104, v104, 2, s2\n"
      "v_fmac_f32 v242, v203, v203\n"
      "v_fmac_f32 v242, v176, v176\n"
      "v_fmac_f32 v242, v177, v177\n"
      "v_fmac_f32 v205, v186, v186\n"
      "v_fmac_f32_e64 v242, v178, v178\n"
      "v_mfma_f32_16x16x32_bf16 v[144:147], a[152:155], v[172:175], v[144:147]\n"
      "ds_read_b32 v105, v105\n"
      "ds_read_b32 v98, v98\n"
      "ds_read_b32 v111, v111\n"
      "global_load_dwordx4 v[8:11], v197, s[6:7] offset:-3072\n"
      "v_fmac_f32 v205, v187, v187\n"
      "v_fmac_f32 v205, v244, v244\n"
      "v_fmac_f32 v205, v245, v245\n"
      "v_fmac_f32 v242, v179, v179\n"
      "v_fmac_f32 v205, v246, v246\n"
      "v_lshl_add_u32 v113, v113, 2, s2\n"
      "v_fmac_f32 v205, v247, v247\n"
      "v_fmac_f32 v205, v232, v232\n"
      "v_fmac_f32 v242, v212, v212\n"
      "v_mfma_f32_16x16x32_bf16 v[140:143], a[156:159], v[172:175], v[140:143]\n"
      "ds_read_b32 v71, v71\n"
      "v_fmac_f32 v205, v233, v233\n"
      "v_fmac_f32 v205, v234, v234\n"
      "v_fmac_f32 v242, v213, v213\n"
      "v_fmac_f32 v242, v214, v214\n"
      "v_fmac_f32 v242, v215, v215\n"
      "v_fmac_f32 v205, v235, v235\n"
      "v_mfma_f32_16x16x32_bf16 v[140:143], a[192:195], v[148:151], v[140:143]\n"
      "ds_read_b32 v104, v104\n"
      "v_permlane16_swap_b32 v242, v205\n"
      "v_add_f32 v242, v242, v205\n"
      "v_mov_b32_e64 v205, v242\n"
      "v_mfma_f32_16x16x32_bf16 v[144:147], a[188:191], v[148:151], v[144:147]\n"
      "ds_read_b32 v101, v101\n"
      "ds_read_b32 v108, v108\n"
      "ds_read_b32 v102, v102\n"
      "ds_read_b32 v106, v106\n"
      "ds_read_b32 v107, v107\n"
      "global_load_dwordx4 v[12:15], v197, s[6:7] offset:-2944\n"
      "v_permlane32_swap_b32 v242, v205\n"
      "v_add_f32 v242, v242, v205\n"
      "v_rsq_f32 v242, v242\n"
      "v_lshl_add_u32 v77, v77, 2, s2\n"
      "v_mov_b32 v205, v242\n"
      "v_mfma_f32_16x16x32_bf16 v[164:167], a[160:163], v[172:175], v[164:167]\n"
      "ds_read_b32 v109, v109\n"
      "ds_read_b32 v103, v103\n"
      "ds_read_b32 v74, v74\n"
      "v_permlane16_swap_b32 v242, v205\n"
      "v_mul_f32 v244, v205, v244\n"
      "v_mul_f32 v247, v205, v247\n"
      "v_mul_f32 v234, v205, v234\n"
      "v_mul_f32 v187, v205, v187\n"
      "v_mul_f32 v185, v205, v185\n"
      "v_mul_f32 v245, v205, v245\n"
      "v_mul_f32 v242, s36, v242\n"
      "v_mul_f32 v215, v242, v215\n"
      "v_mul_f32 v184, v205, v184\n"
      "v_mfma_f32_16x16x32_bf16 v[164:167], a[196:199], v[148:151], v[164:167]\n"
      "ds_read_b32 v110, v110\n"
      "global_load_dwordx4 v[16:19], v197, s[6:7] offset:-2816\n"
      "v_mul_f32 v233, v205, v233\n"
      "v_mul_f32 v177, v242, v177\n"
      "v_cvt_pk_bf16_f32 v184, v184, v185\n"
      "v_mul_f32 v186, v205, v186\n"
      "v_cvt_pk_bf16_f32 v185, v186, v187\n"
      "v_mul_f32 v200, v242, v200\n"
      "v_mul_f32 v212, v242, v212\n"
      "v_mul_f32 v246, v205, v246\n"
      "v_mul_f32 v202, v242, v202\n"
      "v_mul_f32 v232, v205, v232\n"
      "v_mul_f32 v176, v242, v176\n"
      "v_mul_f32 v201, v242, v201\n"
      "v_mfma_f32_16x16x32_bf16 v[208:211], a[164:167], v[172:175], v[208:211]\n"
      "ds_read_b32 v70, v70\n"
      "v_cvt_pk_bf16_f32 v187, v246, v247\n"
      "v_mfma_f32_16x16x32_bf16 v[208:211], a[200:203], v[148:151], v[208:211]\n"
      "ds_read_b32 v113, v113\n"
      "ds_read_b32 v72, v72\n"
      "ds_read_b32 v112, v112\n"
      "ds_read_b32 v77, v77\n"
      "ds_read_b32 v73, v73\n"
      "ds_read_b32 v75, v75\n"
      "global_load_dwordx4 v[20:23], v197, s[6:7]\n"
      "ds_read_b32 v100, v100\n"
      "global_load_dwordx4 v[24:27], v197, s[6:7] offset:128\n"
      "global_load_dwordx4 v[28:31], v197, s[6:7] offset:256\n"
      "s_waitcnt vmcnt(9)\n"
      "v_mov_b32 v135, v229\n"
      "v_mul_f32 v235, v205, v235\n"
      "v_mov_b32 v134, v228\n"
      "v_mfma_f32_16x16x32_bf16 v[224:227], a[168:171], v[172:175], v[224:227]\n"
      "ds_read_b32 v205, v122\n"
      "v_mul_f32 v179, v242, v179\n"
      "s_waitcnt lgkmcnt(6)\n"
      "ds_read_b128 v[168:171], v119 offset:36864\n"
      "v_mul_f32 v178, v242, v178\n"
      "s_waitcnt lgkmcnt(5)\n"
      "v_cvt_pk_bf16_f32 v186, v244, v245\n"
      "v_cvt_pk_bf16_f32 v200, v200, v201\n"
      "v_mul_f32 v213, v242, v213\n"
      "v_mul_f32 v203, v242, v203\n"
      "v_add_u32 v228, 36864, v231\n"
      "v_mfma_f32_16x16x32_bf16 v[224:227], a[204:207], v[148:151], v[224:227]\n"
      "s_mov_b32 exec_lo, 0xffff0000\n"
      "s_mov_b32 exec_hi, 0xffffffff\n"
      "v_mov_b32 v70, 0xf149f2ca\n"
      "v_mov_b32 v72, 0xf149f2ca\n"
      "v_mov_b32 v74, 0xf149f2ca\n"
      "s_mov_b64 exec, -1\n"
      "v_mul_f32 v214, v242, v214\n"
      "s_mov_b32 exec_lo, 0x0\n"
      "s_mov_b32 exec_hi, 0xfffe0000\n"
      "v_mov_b32 v77, 0xf149f2ca\n"
      "s_mov_b64 exec, -1\n"
      "v_cvt_pk_bf16_f32 v33, v214, v215\n"
      "v_cvt_pk_bf16_f32 v35, v234, v235\n"
      "v_mfma_f32_16x16x32_bf16 v[136:139], a[172:175], v[172:175], v[136:139]\n"
      "s_mov_b32 s4, 0\n"
      "v_cvt_pk_bf16_f32 v32, v212, v213\n"
      "v_mov_b32_e64 v242, -1\n"
      "v_mfma_f32_16x16x32_bf16 v[136:139], a[208:211], v[148:151], v[136:139]\n"
      "ds_write_b32 v129, v242 offset:1024\n"
      "v_cvt_pk_bf16_f32 v34, v232, v233\n"
      "v_cvt_pk_bf16_f32 v201, v202, v203\n"
      "v_add_u32 v229, 36864, v230\n"
      "v_cvt_pk_bf16_f32 v203, v178, v179\n"
      "v_cvt_pk_bf16_f32 v202, v176, v177\n"
      "v_mfma_f32_16x16x32_bf16 v[252:255], a[176:179], v[172:175], v[252:255]\n"
      "s_waitcnt lgkmcnt(1)\n"
      "s_mov_b32 s5, 0\n"
      "ds_write_b32 v129, v242 offset:3072\n"
      "v_or3_b32 v168, v168, v169, v170\n"
      "v_and_b32_e64 v168, s40, v168\n"
      "v_bitop3_b32 v205, v205, v171, s40 bitop3:0xa8\n"
      "v_or3_b32 v168, v168, v205, s4\n"
      "v_mfma_f32_16x16x32_bf16 v[252:255], a[212:215], v[148:151], v[252:255]\n"
      "ds_write_b32 v129, v242\n"
      "ds_write_b32 v129, v168 offset:2048\n"
      "s_waitcnt lgkmcnt(0)\n"
      "s_barrier\n"
      "v_fma_f32 v242, v192, v192, s38\n"
      "v_fma_f32 v205, v140, v140, s38\n"
      "v_fmac_f32 v205, v141, v141\n"
      "v_fmac_f32 v205, v142, v142\n"
      "v_fmac_f32 v242, v193, v193\n"
      "v_fmac_f32 v205, v143, v143\n"
      "v_fmac_f32 v205, v164, v164\n"
      "v_fmac_f32 v242, v194, v194\n"
      "v_fmac_f32 v242, v195, v195\n"
      "v_cvt_pk_bf16_f32 v216, v216, v217\n"
      "v_fmac_f32 v242, v248, v248\n"
      "v_fmac_f32 v242, v249, v249\n"
      "v_fmac_f32 v242, v250, v250\n"
      "v_fmac_f32 v205, v165, v165\n"
      "v_fmac_f32 v242, v251, v251\n"
      "v_cvt_pk_bf16_f32 v180, v180, v181\n"
      "v_fmac_f32 v242, v144, v144\n"
      "v_fmac_f32 v205, v166, v166\n"
      "v_fmac_f32 v242, v145, v145\n"
      "v_fmac_f32 v205, v167, v167\n"
      "v_fmac_f32 v242, v146, v146\n"
      "v_fmac_f32 v242, v147, v147\n"
      "v_fmac_f32 v205, v208, v208\n"
      "v_fmac_f32 v205, v209, v209\n"
      "v_fmac_f32 v205, v210, v210\n"
      "v_fmac_f32 v205, v211, v211\n"
      "v_cvt_pk_bf16_f32 v217, v218, v219\n"
      "v_cvt_pk_bf16_f32 v188, v188, v189\n"
      "v_permlane16_swap_b32 v242, v205\n"
      "v_add_f32 v242, v242, v205\n"
      "v_mov_b32 v205, v242\n"
      "v_cvt_pk_bf16_f32 v189, v190, v191\n"
      "v_cvt_pk_bf16_f32 v181, v182, v183\n"
      "v_permlane32_swap_b32 v242, v205\n"
      "v_add_f32 v242, v242, v205\n"
      "v_rsq_f32 v242, v242\n"
      "s_mov_b32 s27, 0\n"
      "ds_read_b128 v[196:199], v115\n"
      "v_mov_b32 v205, v242\n"
      "s_add_u32 s28, s27, 1\n"
      "s_min_u32 m0, s28, s3\n"
      "ds_read_b128 v[148:151], v114\n"
      "v_permlane16_swap_b32 v242, v205\n"
      "v_mul_f32 v165, v205, v165\n"
      "v_mul_f32 v143, v205, v143\n"
      "v_mul_f32 v140, v205, v140\n"
      "v_mul_f32 v242, s36, v242\n"
      "v_mul_f32 v164, v205, v164\n"
      "v_mul_f32 v142, v205, v142\n"
      "v_mul_f32 v141, v205, v141\n"
      "v_mul_f32 v192, v242, v192\n"
      "v_cvt_pk_bf16_f32 v140, v140, v141\n"
      "v_mul_f32 v195, v242, v195\n"
      "v_cvt_pk_bf16_f32 v141, v142, v143\n"
      "s_movrels_b32 s9, s80\n"
      "ds_write_b128 v206, v[200:203]\n"
      "ds_read_b128 v[168:171], v128 offset:2048\n"
      "ds_write_b128 v206, v[32:35] offset:16\n"
      "v_mul_f32 v193, v242, v193\n"
      "v_mul_f32 v208, v205, v208\n"
      "v_mul_f32 v194, v242, v194\n"
      "s_lshr_b32 s8, s9, 8\n"
      "v_mul_f32 v209, v205, v209\n"
      "v_mul_f32 v167, v205, v167\n"
      "s_and_b32 s9, s9, 0xff\n"
      "v_mul_f32 v210, v205, v210\n"
      "v_mul_f32_e64 v166, v205, v166\n"
      "v_cvt_pk_bf16_f32 v143, v166, v167\n"
      "s_mul_i32 s10, s8, 73\n"
      "ds_write_b128 v206, v[184:187] offset:32\n"
      "v_cvt_pk_bf16_f32 v142, v164, v165\n"
      "v_mul_f32 v248, v242, v248\n"
      "s_waitcnt lgkmcnt(4)\n"
      "v_cvt_pk_bf16_f32 v40, v208, v209\n"
      "v_cvt_pk_bf16_f32 v192, v192, v193\n"
      "v_mul_f32 v211, v205, v211\n"
      "s_add_u32 s10, s10, s9\n"
      "v_cvt_pk_bf16_f32 v41, v210, v211\n"
      "v_mfma_f32_16x16x32_bf16 v[184:187], a[0:3], v[148:151], 0\n"
      "ds_read_b128 v[232:235], v114 offset:6144\n"
      "v_mul_f32 v249, v242, v249\n"
      "v_mfma_f32_16x16x32_bf16 v[184:187], a[36:39], v[196:199], v[184:187]\n"
      "ds_write_b64 v207, v[216:217]\n"
      "s_mul_i32 s28, s10, 4802\n"
      "v_mfma_f32_16x16x32_bf16 v[200:203], a[4:7], v[148:151], 0\n"
      "s_add_u32 s56, s18, s28\n"
      "ds_read_b128 v[212:215], v115 offset:6144\n"
      "v_mfma_f32_16x16x32_bf16 v[200:203], a[40:43], v[196:199], v[200:203]\n"
      "ds_write_b64 v207, v[188:189] offset:32\n"
      "v_mfma_f32_16x16x32_bf16 v[188:191], a[8:11], v[148:151], 0\n"
      "s_addc_u32 s57, s19, 0\n"
      "s_and_b32 s56, s56, -16\n"
      "s_sub_u32 s58, s56, 110592\n"
      "v_mfma_f32_16x16x32_bf16 v[188:191], a[44:47], v[196:199], v[188:191]\n"
      "ds_write_b64 v207, v[180:181] offset:64\n"
      "v_cvt_pk_bf16_f32 v193, v194, v195\n"
      "v_mfma_f32_16x16x32_bf16 v[180:183], a[12:15], v[148:151], 0\n"
      "v_mul_f32_e64 v250, v242, v250\n"
      "v_cvt_pk_bf16_f32 v194, v248, v249\n"
      "v_mfma_f32_16x16x32_bf16 v[180:183], a[48:51], v[196:199], v[180:183]\n"
      "s_subb_u32 s59, s57, 0\n"
      "v_mul_f32 v251, v242, v251\n"
      "v_cvt_pk_bf16_f32 v195, v250, v251\n"
      "v_mfma_f32_16x16x32_bf16 v[248:251], a[16:19], v[148:151], 0\n"
      "s_sub_u32 s56, s56, 73728\n"
      "v_mul_f32_e64 v144, v242, v144\n"
      "v_mul_f32_e64 v145, v242, v145\n"
      "v_mfma_f32_16x16x32_bf16 v[248:251], a[52:55], v[196:199], v[248:251]\n"
      "ds_read_b128 v[244:247], v114 offset:12288\n"
      "s_waitcnt lgkmcnt(3)\n"
      "v_mfma_f32_16x16x32_bf16 v[176:179], a[20:23], v[148:151], 0\n"
      "s_subb_u32 s57, s57, 0\n"
      "v_mul_f32_e64 v146, v242, v146\n"
      "v_cvt_pk_bf16_f32 v38, v144, v145\n"
      "v_mfma_f32_16x16x32_bf16 v[176:179], a[56:59], v[196:199], v[176:179]\n"
      "ds_read_b128 v[164:167], v115 offset:12288\n"
      "v_mul_f32 v147, v242, v147\n"
      "v_mfma_f32_16x16x32_bf16 v[216:219], v[148:151], a[24:27], 0\n"
      "s_mul_i32 s28, s8, 1373568\n"
      "v_cvt_pk_bf16_f32 v39, v146, v147\n"
      "s_mul_i32 s29, s9, 2688\n"
      "v_mfma_f32_16x16x32_bf16 v[216:219], v[196:199], a[60:63], v[216:219]\n"
      "s_add_u32 s28, s28, s29\n"
      "v_cvt_pk_bf16_f32 v224, v224, v225\n"
      "v_cvt_pk_bf16_f32 v136, v136, v137\n"
      "v_mfma_f32_16x16x32_bf16 v[172:175], v[148:151], a[28:31], 0\n"
      "v_cvt_pk_bf16_f32 v225, v226, v227\n"
      "s_cmp_eq_u32 s8, 72\n"
      "v_mfma_f32_16x16x32_bf16 v[172:175], v[196:199], a[64:67], v[172:175]\n"
      "s_cselect_b32 s8, 0, 100270464\n"
      "v_cvt_pk_bf16_f32 v252, v252, v253\n"
      "v_cvt_pk_bf16_f32 v253, v254, v255\n"
      "v_mfma_f32_16x16x32_bf16 v[144:147], v[148:151], a[32:35], 0\n"
      "s_cmp_eq_u32 s9, 72\n"
      "s_cselect_b32 s9, 0, 196224\n"
      "s_add_u32 s10, s8, s9\n"
      "v_mfma_f32_16x16x32_bf16 v[144:147], v[196:199], a[68:71], v[144:147]\n"
      "s_add_u32 s28, s28, s10\n"
      "v_bitop3_b32 v170, v170, v171, s4 bitop3:0x7e\n"
      "s_add_u32 s52, s16, s28\n"
      "v_mfma_f32_16x16x32_bf16 v[184:187], a[72:75], v[232:235], v[184:187]\n"
      "s_addc_u32 s53, s17, 0\n"
      "s_cmp_lg_u32 s10, s11\n"
      "v_bitop3_b32 v168, v168, v169, s4 bitop3:0x7e\n"
      "v_mfma_f32_16x16x32_bf16 v[184:187], a[108:111], v[212:215], v[184:187]\n"
      "s_cbranch_scc1 L_edge_6_%=\n"
      "L_eb_7_%=:\n"
      "v_mfma_f32_16x16x32_bf16 v[200:203], a[76:79], v[232:235], v[200:203]\n"
      "s_add_u32 m0, s76, 18432\n"
      "v_or_b32 v168, v168, v170\n"
      "v_cvt_pk_bf16_f32 v137, v138, v139\n"
      "v_mfma_f32_16x16x32_bf16 v[200:203], a[112:115], v[212:215], v[200:203]\n"
      "v_cmp_ne_u32 vcc, 0, v168\n"
      "v_mfma_f32_16x16x32_bf16 v[188:191], a[80:83], v[232:235], v[188:191]\n"
      "global_load_lds_dwordx4 v116, s[52:53]\n"
      "v_mfma_f32_16x16x32_bf16 v[188:191], a[116:119], v[212:215], v[188:191]\n"
      "s_mov_b32 exec_lo, 0x003f003f\n"
      "s_mov_b32 exec_hi, 0x003f003f\n"
      "ds_write_b128 v206, v[192:195] offset:18688\n"
      "ds_write_b128 v206, v[38:41] offset:18704\n"
      "ds_write_b128 v206, v[140:143] offset:18720\n"
      "ds_write_b64 v207, v[224:225] offset:18688\n"
      "ds_write_b64 v207, v[136:137] offset:18720\n"
      "ds_write_b64 v207, v[252:253] offset:18752\n"
      "s_mov_b64 exec, -1\n"
      "v_mfma_f32_16x16x32_bf16 v[180:183], a[84:87], v[232:235], v[180:183]\n"
      "s_mov_b64 s[48:49], vcc\n"
      "s_add_u32 m0, s76, 24448\n"
      "v_mfma_f32_16x16x32_bf16 v[180:183], a[120:123], v[212:215], v[180:183]\n"
      "global_load_lds_dwordx4 v116, s[52:53] offset:128\n"
      "v_mfma_f32_16x16x32_bf16 v[248:251], a[88:91], v[232:235], v[248:251]\n"
      "s_add_u32 m0, s76, 30464\n"
      "v_mfma_f32_16x16x32_bf16 v[248:251], a[124:127], v[212:215], v[248:251]\n"
      "v_mfma_f32_16x16x32_bf16 v[176:179], a[92:95], v[232:235], v[176:179]\n"
      "ds_read_b128 v[192:195], v114 offset:2048\n"
      "v_mfma_f32_16x16x32_bf16 v[176:179], a[128:131], v[212:215], v[176:179]\n"
      "s_cbranch_vccnz L_flg_3_%=\n"
      "L_fb_4_%=:\n"
      "v_mfma_f32_16x16x32_bf16 v[168:171], v[152:155], a[216:219], 0\n"
      "v_mfma_f32_16x16x32_bf16 v[168:171], v[152:155], a[220:223], v[168:171]\n"
      "s_nop 0\n"
      "v_mfma_f32_16x16x32_bf16 v[168:171], v[152:155], a[224:227], v[168:171]\n"
      "ds_read_b128 v[196:199], v115 offset:2048\n"
      "v_mfma_f32_16x16x32_bf16 v[216:219], v[232:235], a[96:99], v[216:219]\n"
      "v_mfma_f32_16x16x32_bf16 v[216:219], v[212:215], a[132:135], v[216:219]\n"
      "global_load_lds_dwordx4 v116, s[52:53] offset:256\n"
      "v_mfma_f32_16x16x32_bf16 v[172:175], v[232:235], a[100:103], v[172:175]\n"
      "s_nop 0\n"
      "v_mfma_f32_16x16x32_bf16 v[172:175], v[212:215], a[136:139], v[172:175]\n"
      "s_waitcnt lgkmcnt(2)\n"
      "s_nop 0\n"
      "v_add_f32_dpp v168, v168, v168 quad_perm:[1,0,3,2] row_mask:0xf bank_mask:0xf\n"
      "v_mfma_f32_16x16x32_bf16 v[144:147], v[232:235], a[104:107], v[144:147]\n"
      "s_nop 0\n"
      "v_add_f32_dpp v168, v168, v168 quad_perm:[2,3,0,1] row_mask:0xf bank_mask:0xf\n"
      "v_mfma_f32_16x16x32_bf16 v[144:147], v[212:215], a[140:143], v[144:147]\n"
      "s_nop 0\n"
      "v_add_f32_dpp v168, v168, v168 row_half_mirror row_mask:0xf bank_mask:0xf\n"
      "v_mfma_f32_16x16x32_bf16 v[184:187], a[144:147], v[244:247], v[184:187]\n"
      "s_nop 0\n"
      "v_mul_f32 v171, 0xbbaaaaab, v168\n"
      "v_mfma_f32_16x16x32_bf16 v[184:187], a[180:183], v[164:167], v[184:187]\n"
      "v_mul_f32 v170, 0xbbaaaaab, v168\n"
      "v_mfma_f32_16x16x32_bf16 v[200:203], a[148:151], v[244:247], v[200:203]\n"
      "s_nop 0\n"
      "v_mul_f32 v169, 0xbbaaaaab, v168\n"
      "v_mul_f32 v168, 0xbbaaaaab, v168\n"
      "v_mfma_f32_16x16x32_bf16 v[200:203], a[184:187], v[164:167], v[200:203]\n"
      "s_nop 0\n"
      "v_mfma_f32_16x16x32_bf16 v[148:151], v[238:241], a[216:219], v[168:171]\n"
      "v_mfma_f32_16x16x32_bf16 v[232:235], v[236:239], a[216:219], v[168:171]\n"
      "v_fma_f32 v242, v184, v184, s38\n"
      "v_mfma_f32_16x16x32_bf16 v[206:209], v[238:241], a[220:223], v[168:171]\n"
      "s_add_u32 m0, s77, 18432\n"
      "s_waitcnt lgkmcnt(0)\n"
      "v_mfma_f32_16x16x32_bf16 v[212:215], v[236:239], a[220:223], v[168:171]\n"
      "s_nop 1\n"
      "v_cvt_pk_bf16_f32 v148, v148, v149\n"
      "v_mfma_f32_16x16x32_bf16 v[136:139], v[238:241], a[224:227], v[168:171]\n"
      "v_cvt_pk_bf16_f32 v149, v150, v151\n"
      "v_cvt_pk_bf16_f32 v150, v232, v233\n"
      "v_mfma_f32_16x16x32_bf16 v[224:227], v[236:239], a[224:227], v[168:171]\n"
      "v_cvt_pk_bf16_f32 v206, v206, v207\n"
      "v_cvt_pk_bf16_f32 v151, v234, v235\n"
      "v_mfma_f32_16x16x32_bf16 v[188:191], a[152:155], v[244:247], v[188:191]\n"
      "v_cvt_pk_bf16_f32 v207, v208, v209\n"
      "v_cvt_pk_bf16_f32 v208, v212, v213\n"
      "v_mfma_f32_16x16x32_bf16 v[188:191], a[188:191], v[164:167], v[188:191]\n"
      "ds_write_b128 v204, v[148:151]\n"
      "v_mfma_f32_16x16x32_bf16 v[168:171], v[152:155], a[228:231], 0\n"
      "global_load_lds_dwordx4 v117, s[52:53]\n"
      "v_mfma_f32_16x16x32_bf16 v[168:171], v[152:155], a[232:235], v[168:171]\n"
      "v_cvt_pk_bf16_f32 v209, v214, v215\n"
      "v_cvt_pk_bf16_f32 v136, v136, v137\n"
      "v_mfma_f32_16x16x32_bf16 v[180:183], a[156:159], v[244:247], v[180:183]\n"
      "v_cvt_pk_bf16_f32 v137, v138, v139\n"
      "v_cvt_pk_bf16_f32 v138, v224, v225\n"
      "v_mfma_f32_16x16x32_bf16 v[180:183], a[192:195], v[164:167], v[180:183]\n"
      "ds_write_b128 v204, v[206:209] offset:128\n"
      "v_mfma_f32_16x16x32_bf16 v[168:171], v[152:155], a[236:239], v[168:171]\n"
      "v_cvt_pk_bf16_f32 v139, v226, v227\n"
      "v_fmac_f32_e64 v242, v185, v185\n"
      "v_mfma_f32_16x16x32_bf16 v[248:251], a[160:163], v[244:247], v[248:251]\n"
      "v_fmac_f32 v242, v186, v186\n"
      "v_fmac_f32 v242, v187, v187\n"
      "v_mfma_f32_16x16x32_bf16 v[248:251], a[196:199], v[164:167], v[248:251]\n"
      "ds_write_b128 v204, v[136:139] offset:256\n"
      "v_mfma_f32_16x16x32_bf16 v[176:179], a[164:167], v[244:247], v[176:179]\n"
      "v_add_f32_dpp v168, v168, v168 quad_perm:[1,0,3,2] row_mask:0xf bank_mask:0xf\n"
      "v_fma_f32 v243, v180, v180, s38\n"
      "v_mfma_f32_16x16x32_bf16 v[176:179], a[200:203], v[164:167], v[176:179]\n"
      "v_add_f32_dpp v168, v168, v168 quad_perm:[2,3,0,1] row_mask:0xf bank_mask:0xf\n"
      "v_fmac_f32_e64 v243, v181, v181\n"
      "v_mfma_f32_16x16x32_bf16 v[216:219], v[244:247], a[168:171], v[216:219]\n"
      "v_add_f32_dpp v168, v168, v168 row_half_mirror row_mask:0xf bank_mask:0xf\n"
      "v_mul_f32 v171, 0xbbaaaaab, v168\n"
      "v_mfma_f32_16x16x32_bf16 v[216:219], v[164:167], a[204:207], v[216:219]\n"
      "v_mul_f32 v169, 0xbbaaaaab, v168\n"
      "v_mul_f32 v170, 0xbbaaaaab, v168\n"
      "v_mfma_f32_16x16x32_bf16 v[172:175], v[244:247], a[172:175], v[172:175]\n"
      "v_mul_f32 v168, 0xbbaaaaab, v168\n"
      "v_fmac_f32 v243, v182, v182\n"
      "v_mfma_f32_16x16x32_bf16 v[172:175], v[164:167], a[208:211], v[172:175]\n"
      "v_fmac_f32 v242, v200, v200\n"
      "v_fmac_f32 v243, v183, v183\n"
      "v_mfma_f32_16x16x32_bf16 v[212:215], v[238:241], a[228:231], v[168:171]\n"
      "v_fmac_f32 v243, v248, v248\n"
      "v_fmac_f32_e64 v242, v201, v201\n"
      "v_mfma_f32_16x16x32_bf16 v[206:209], v[236:239], a[228:231], v[168:171]\n"
      "ds_read_b128 v[148:151], v114 offset:8192\n"
      "v_fmac_f32 v243, v249, v249\n"
      "v_mfma_f32_16x16x32_bf16 v[232:235], v[238:241], a[232:235], v[168:171]\n"
      "v_fmac_f32 v243, v250, v250\n"
      "v_mfma_f32_16x16x32_bf16 v[252:255], v[236:239], a[232:235], v[168:171]\n"
      "s_add_u32 m0, s78, 18432\n"
      "v_cvt_pk_bf16_f32 v212, v212, v213\n"
      "v_cvt_pk_bf16_f32 v213, v214, v215\n"
      "v_mfma_f32_16x16x32_bf16 v[136:139], v[238:241], a[236:239], v[168:171]\n"
      "v_cvt_pk_bf16_f32 v215, v208, v209\n"
      "v_cvt_pk_bf16_f32 v214, v206, v207\n"
      "v_mfma_f32_16x16x32_bf16 v[206:209], v[236:239], a[236:239], v[168:171]\n"
      "ds_write_b128 v204, v[212:215] offset:3072\n"
      "v_mfma_f32_16x16x32_bf16 v[144:147], v[244:247], a[176:179], v[144:147]\n"
      "ds_read_b128 v[212:215], v115 offset:8192\n"
      "v_cvt_pk_bf16_f32 v232, v232, v233\n"
      "v_mfma_f32_16x16x32_bf16 v[144:147], v[164:167], a[212:215], v[144:147]\n"
      "v_cvt_pk_bf16_f32 v233, v234, v235\n"
      "v_cvt_pk_bf16_f32 v234, v252, v253\n"
      "v_mfma_f32_16x16x32_bf16 v[168:171], a[0:3], v[192:195], 0\n"
      "v_cvt_pk_bf16_f32 v136, v136, v137\n"
      "v_cvt_pk_bf16_f32 v235, v254, v255\n"
      "v_mfma_f32_16x16x32_bf16 v[168:171], a[36:39], v[196:199], v[168:171]\n"
      "ds_write_b128 v204, v[232:235] offset:3200\n"
      "v_mfma_f32_16x16x32_bf16 v[232:235], a[4:7], v[192:195], 0\n"
      "v_fmac_f32 v242, v202, v202\n"
      "v_cvt_pk_bf16_f32 v137, v138, v139\n"
      "v_mfma_f32_16x16x32_bf16 v[232:235], a[40:43], v[196:199], v[232:235]\n"
      "s_mov_b32 exec_hi, 0\n"
      "global_load_lds_dwordx4 v118, s[52:53]\n"
      "s_mov_b32 exec_hi, -1\n"
      "v_mfma_f32_16x16x32_bf16 v[244:247], a[8:11], v[192:195], 0\n"
      "v_cvt_pk_bf16_f32 v139, v208, v209\n"
      "v_fmac_f32 v242, v203, v203\n"
      "v_mfma_f32_16x16x32_bf16 v[244:247], a[44:47], v[196:199], v[244:247]\n"
      "v_fmac_f32 v243, v251, v251\n"
      "v_cvt_pk_bf16_f32 v138, v206, v207\n"
      "v_mfma_f32_16x16x32_bf16 v[164:167], a[12:15], v[192:195], 0\n"
      "v_fmac_f32 v242, v188, v188\n"
      "v_fmac_f32_e64 v243, v176, v176\n"
      "v_mfma_f32_16x16x32_bf16 v[164:167], a[48:51], v[196:199], v[164:167]\n"
      "ds_write_b128 v204, v[136:139] offset:3328\n"
      "v_mfma_f32_16x16x32_bf16 v[206:209], a[16:19], v[192:195], 0\n"
      "v_fmac_f32 v242, v189, v189\n"
      "v_fmac_f32 v243, v177, v177\n"
      "v_mfma_f32_16x16x32_bf16 v[206:209], a[52:55], v[196:199], v[206:209]\n"
      "v_fmac_f32 v243, v178, v178\n"
      "v_fmac_f32 v242, v190, v190\n"
      "v_mfma_f32_16x16x32_bf16 v[136:139], a[20:23], v[192:195], 0\n"
      "s_add_u32 m0, s76, 115456\n"
      "v_fmac_f32 v243, v179, v179\n"
      "v_fmac_f32 v242, v191, v191\n"
      "v_mfma_f32_16x16x32_bf16 v[136:139], a[56:59], v[196:199], v[136:139]\n"
      "s_waitcnt lgkmcnt(1)\n"
      "v_permlane16_swap_b32 v242, v243\n"
      "v_mfma_f32_16x16x32_bf16 v[252:255], v[192:195], a[24:27], 0\n"
      "v_add_f32 v242, v242, v243\n"
      "v_mov_b32 v243, v242\n"
      "v_mfma_f32_16x16x32_bf16 v[252:255], v[196:199], a[60:63], v[252:255]\n"
      "s_nop 0\n"
      "v_permlane32_swap_b32 v242, v243\n"
      "v_mfma_f32_16x16x32_bf16 v[156:159], v[192:195], a[28:31], 0\n"
      "v_add_f32 v242, v242, v243\n"
      "v_mfma_f32_16x16x32_bf16 v[156:159], v[196:199], a[64:67], v[156:159]\n"
      "v_rsq_f32 v242, v242\n"
      "v_mfma_f32_16x16x32_bf16 v[168:171], a[72:75], v[148:151], v[168:171]\n"
      "v_mov_b32_e64 v243, v242\n"
      "v_mfma_f32_16x16x32_bf16 v[168:171], a[108:111], v[212:215], v[168:171]\n"
      "s_nop 0\n"
      "v_permlane16_swap_b32 v242, v243\n"
      "v_mfma_f32_16x16x32_bf16 v[232:235], a[76:79], v[148:151], v[232:235]\n"
      "global_load_lds_dwordx4 v119, s[56:57]\n"
      "v_mfma_f32_16x16x32_bf16 v[232:235], a[112:115], v[212:215], v[232:235]\n"
      "v_mul_f32 v181, v243, v181\n"
      "v_mul_f32 v180, v243, v180\n"
      "v_mfma_f32_16x16x32_bf16 v[244:247], a[80:83], v[148:151], v[244:247]\n"
      "v_cvt_pk_bf16_f32 v180, v180, v181\n"
      "v_mul_f32 v182, v243, v182\n"
      "v_mfma_f32_16x16x32_bf16 v[244:247], a[116:119], v[212:215], v[244:247]\n"
      "s_add_u32 m0, s79, 4864\n"
      "v_mul_f32 v183, v243, v183\n"
      "v_cvt_pk_bf16_f32 v181, v182, v183\n"
      "v_mfma_f32_16x16x32_bf16 v[164:167], a[84:87], v[148:151], v[164:167]\n"
      "v_mul_f32 v249, v243, v249\n"
      "v_mul_f32 v248, v243, v248\n"
      "v_mfma_f32_16x16x32_bf16 v[164:167], a[120:123], v[212:215], v[164:167]\n"
      "v_mul_f32 v250, v243, v250\n"
      "v_mul_f32 v251, v243, v251\n"
      "v_mfma_f32_16x16x32_bf16 v[140:143], v[192:195], a[32:35], 0\n"
      "v_cvt_pk_bf16_f32 v182, v248, v249\n"
      "v_mul_f32_e64 v179, v243, v179\n"
      "v_mfma_f32_16x16x32_bf16 v[140:143], v[196:199], a[68:71], v[140:143]\n"
      "ds_read_b128 v[192:195], v115 offset:14336\n"
      "v_mul_f32 v176, v243, v176\n"
      "v_mfma_f32_16x16x32_bf16 v[206:209], a[88:91], v[148:151], v[206:209]\n"
      "ds_read_b128 v[220:223], v114 offset:14336\n"
      "v_mul_f32 v177, v243, v177\n"
      "v_mfma_f32_16x16x32_bf16 v[206:209], a[124:127], v[212:215], v[206:209]\n"
      "v_mfma_f32_16x16x32_bf16 v[196:199], v[152:155], a[240:243], 0\n"
      "v_mul_f32 v242, s36, v242\n"
      "v_mul_f32 v190, v242, v190\n"
      "v_mfma_f32_16x16x32_bf16 v[196:199], v[152:155], a[244:247], v[196:199]\n"
      "v_mul_f32 v186, v242, v186\n"
      "v_mul_f32 v187, v242, v187\n"
      "v_mfma_f32_16x16x32_bf16 v[196:199], v[152:155], a[248:251], v[196:199]\n"
      "v_cvt_pk_bf16_f32 v34, v176, v177\n"
      "v_mul_f32_e64 v188, v242, v188\n"
      "v_mfma_f32_16x16x32_bf16 v[136:139], a[92:95], v[148:151], v[136:139]\n"
      "v_mul_f32 v185, v242, v185\n"
      "v_mul_f32 v178, v243, v178\n"
      "v_mfma_f32_16x16x32_bf16 v[136:139], a[128:131], v[212:215], v[136:139]\n"
      "v_cvt_pk_bf16_f32 v183, v250, v251\n"
      "v_mul_f32_e64 v184, v242, v184\n"
      "v_mfma_f32_16x16x32_bf16 v[252:255], v[148:151], a[96:99], v[252:255]\n"
      "v_add_f32_dpp v196, v196, v196 quad_perm:[1,0,3,2] row_mask:0xf bank_mask:0xf\n"
      "v_cvt_pk_bf16_f32 v184, v184, v185\n"
      "v_mfma_f32_16x16x32_bf16 v[252:255], v[212:215], a[132:135], v[252:255]\n"
      "s_nop 0\n"
      "v_cvt_pk_bf16_f32 v35, v178, v179\n"
      "s_waitcnt lgkmcnt(0)\n"
      "v_mfma_f32_16x16x32_bf16 v[156:159], v[148:151], a[100:103], v[156:159]\n"
      "ds_read_b128 v[248:251], v114 offset:4096\n"
      "v_mul_f32 v202, v242, v202\n"
      "v_mfma_f32_16x16x32_bf16 v[156:159], v[212:215], a[136:139], v[156:159]\n"
      "v_mul_f32 v203, v242, v203\n"
      "v_cvt_pk_bf16_f32 v185, v186, v187\n"
      "v_mfma_f32_16x16x32_bf16 v[140:143], v[148:151], a[104:107], v[140:143]\n"
      "v_mfma_f32_16x16x32_bf16 v[140:143], v[212:215], a[140:143], v[140:143]\n"
      "v_cvt_pk_bf16_f32 v187, v202, v203\n"
      "v_mfma_f32_16x16x32_bf16 v[168:171], a[144:147], v[220:223], v[168:171]\n"
      "v_mul_f32_e64 v200, v242, v200\n"
      "v_add_f32_dpp v196, v196, v196 quad_perm:[2,3,0,1] row_mask:0xf bank_mask:0xf\n"
      "v_mfma_f32_16x16x32_bf16 v[168:171], a[180:183], v[192:195], v[168:171]\n"
      "v_mul_f32_e64 v191, v242, v191\n"
      "v_mfma_f32_16x16x32_bf16 v[232:235], a[148:151], v[220:223], v[232:235]\n"
      "v_add_f32_dpp v196, v196, v196 row_half_mirror row_mask:0xf bank_mask:0xf\n"
      "v_mul_f32 v198, 0xbbaaaaab, v196\n"
      "v_mfma_f32_16x16x32_bf16 v[232:235], a[184:187], v[192:195], v[232:235]\n"
      "v_mul_f32 v197, 0xbbaaaaab, v196\n"
      "s_waitcnt vmcnt(14)\n"
      "v_mfma_f32_16x16x32_bf16 v[148:151], v[152:155], a[252:255], 0\n"
      "v_cvt_pk_bf16_f32 v216, v216, v217\n"
      "s_waitcnt vmcnt(13)\n"
      "v_mfma_f32_16x16x32_bf16 v[148:151], v[152:155], v[0:3], v[148:151]\n"
      "v_mul_f32 v189, v242, v189\n"
      "v_mul_f32 v199, 0xbbaaaaab, v196\n"
      "s_waitcnt vmcnt(12)\n"
      "v_mfma_f32_16x16x32_bf16 v[148:151], v[152:155], v[4:7], v[148:151]\n"
      "v_cvt_pk_bf16_f32 v33, v190, v191\n"
      "v_mul_f32 v196, 0xbbaaaaab, v196\n"
      "v_mfma_f32_16x16x32_bf16 v[244:247], a[152:155], v[220:223], v[244:247]\n"
      "v_cvt_pk_bf16_f32 v32, v188, v189\n"
      "v_mul_f32_e64 v201, v242, v201\n"
      "v_mfma_f32_16x16x32_bf16 v[244:247], a[188:191], v[192:195], v[244:247]\n"
      "v_cvt_pk_bf16_f32 v186, v200, v201\n"
      "v_mfma_f32_16x16x32_bf16 v[188:191], v[236:239], a[240:243], v[196:199]\n"
      "global_load_lds_dword v122, s[58:59]\n"
      "v_mfma_f32_16x16x32_bf16 v[212:215], v[238:241], a[240:243], v[196:199]\n"
      "v_mfma_f32_16x16x32_bf16 v[164:167], a[156:159], v[220:223], v[164:167]\n"
      "v_mfma_f32_16x16x32_bf16 v[164:167], a[192:195], v[192:195], v[164:167]\n"
      "v_mfma_f32_16x16x32_bf16 v[206:209], a[160:163], v[220:223], v[206:209]\n"
      "ds_read_b128 v[200:203], v115 offset:4096\n"
      "v_add_f32_dpp v148, v148, v148 quad_perm:[1,0,3,2] row_mask:0xf bank_mask:0xf\n"
      "v_mfma_f32_16x16x32_bf16 v[206:209], a[196:199], v[192:195], v[206:209]\n"
      "s_nop 1\n"
      "v_cvt_pk_bf16_f32 v212, v212, v213\n"
      "v_mfma_f32_16x16x32_bf16 v[136:139], a[164:167], v[220:223], v[136:139]\n"
      "s_nop 0\n"
      "v_cvt_pk_bf16_f32 v213, v214, v215\n"
      "v_cvt_pk_bf16_f32 v215, v190, v191\n"
      "v_mfma_f32_16x16x32_bf16 v[136:139], a[200:203], v[192:195], v[136:139]\n"
      "v_add_f32_dpp v148, v148, v148 quad_perm:[2,3,0,1] row_mask:0xf bank_mask:0xf\n"
      "v_cvt_pk_bf16_f32 v214, v188, v189\n"
      "v_mfma_f32_16x16x32_bf16 v[188:191], v[238:241], a[244:247], v[196:199]\n"
      "ds_write_b128 v204, v[212:215] offset:6144\n"
      "v_mfma_f32_16x16x32_bf16 v[212:215], v[236:239], a[244:247], v[196:199]\n"
      "v_add_f32_dpp v148, v148, v148 row_half_mirror row_mask:0xf bank_mask:0xf\n"
      "v_mul_f32 v151, 0xbbaaaaab, v148\n"
      "v_mfma_f32_16x16x32_bf16 v[252:255], v[220:223], a[168:171], v[252:255]\n"
      "ds_read_b128 v[160:163], v115 offset:10240\n"
      "v_mfma_f32_16x16x32_bf16 v[252:255], v[192:195], a[204:207], v[252:255]\n"
      "s_nop 0\n"
      "v_cvt_pk_bf16_f32 v188, v188, v189\n"
      "v_cvt_pk_bf16_f32 v189, v190, v191\n"
      "v_mfma_f32_16x16x32_bf16 v[156:159], v[220:223], a[172:175], v[156:159]\n"
      "s_waitcnt lgkmcnt(2)\n"
      "v_cvt_pk_bf16_f32 v190, v212, v213\n"
      "v_cvt_pk_bf16_f32 v191, v214, v215\n"
      "v_mfma_f32_16x16x32_bf16 v[156:159], v[192:195], a[208:211], v[156:159]\n"
      "ds_write_b128 v204, v[188:191] offset:6272\n"
      "v_mfma_f32_16x16x32_bf16 v[188:191], v[238:241], a[248:251], v[196:199]\n"
      "v_mul_f32 v150, 0xbbaaaaab, v148\n"
      "v_mul_f32 v149, 0xbbaaaaab, v148\n"
      "v_mfma_f32_16x16x32_bf16 v[212:215], v[236:239], a[248:251], v[196:199]\n"
      "v_mul_f32 v148, 0xbbaaaaab, v148\n"
      "v_cvt_pk_bf16_f32 v217, v218, v219\n"
      "v_mfma_f32_16x16x32_bf16 v[140:143], v[220:223], a[176:179], v[140:143]\n"
      "v_cvt_pk_bf16_f32 v218, v252, v253\n"
      "v_cvt_pk_bf16_f32 v219, v254, v255\n"
      "v_mfma_f32_16x16x32_bf16 v[140:143], v[192:195], a[212:215], v[140:143]\n"
      "v_cvt_pk_bf16_f32 v188, v188, v189\n"
      "v_cvt_pk_bf16_f32 v189, v190, v191\n"
      "v_mfma_f32_16x16x32_bf16 v[196:199], v[238:241], a[252:255], v[148:151]\n"
      "v_cvt_pk_bf16_f32 v190, v212, v213\n"
      "v_cvt_pk_bf16_f32 v191, v214, v215\n"
      "v_mfma_f32_16x16x32_bf16 v[176:179], v[236:239], a[252:255], v[148:151]\n"
      "ds_write_b128 v204, v[188:191] offset:6400\n"
      "v_mfma_f32_16x16x32_bf16 v[212:215], v[238:241], v[0:3], v[148:151]\n"
      "v_cvt_pk_bf16_f32 v172, v172, v173\n"
      "v_cvt_pk_bf16_f32 v173, v174, v175\n"
      "v_mfma_f32_16x16x32_bf16 v[188:191], v[236:239], v[0:3], v[148:151]\n"
      "v_cvt_pk_bf16_f32 v196, v196, v197\n"
      "v_cvt_pk_bf16_f32 v197, v198, v199\n"
      "v_mfma_f32_16x16x32_bf16 v[192:195], v[238:241], v[4:7], v[148:151]\n"
      "v_cvt_pk_bf16_f32 v198, v176, v177\n"
      "v_cvt_pk_bf16_f32 v199, v178, v179\n"
      "v_mfma_f32_16x16x32_bf16 v[176:179], v[236:239], v[4:7], v[148:151]\n"
      "ds_write_b128 v204, v[196:199] offset:9216\n"
      "v_mfma_f32_16x16x32_bf16 v[252:255], a[0:3], v[248:251], 0\n"
      "ds_read_b128 v[220:223], v114 offset:10240\n"
      "v_cvt_pk_bf16_f32 v212, v212, v213\n"
      "v_mfma_f32_16x16x32_bf16 v[252:255], a[36:39], v[200:203], v[252:255]\n"
      "v_cvt_pk_bf16_f32 v213, v214, v215\n"
      "v_cvt_pk_bf16_f32 v215, v190, v191\n"
      "v_cvt_pk_bf16_f32 v214, v188, v189\n"
      "v_mfma_f32_16x16x32_bf16 v[196:199], a[4:7], v[248:251], 0\n"
      "v_cvt_pk_bf16_f32 v192, v192, v193\n"
      "v_cvt_pk_bf16_f32 v193, v194, v195\n"
      "v_mfma_f32_16x16x32_bf16 v[196:199], a[40:43], v[200:203], v[196:199]\n"
      "ds_write_b128 v204, v[212:215] offset:9344\n"
      "v_mfma_f32_16x16x32_bf16 v[148:151], a[8:11], v[248:251], 0\n"
      "v_cvt_pk_bf16_f32 v194, v176, v177\n"
      "v_cvt_pk_bf16_f32 v195, v178, v179\n"
      "v_mfma_f32_16x16x32_bf16 v[148:151], a[44:47], v[200:203], v[148:151]\n"
      "ds_write_b128 v204, v[192:195] offset:9472\n"
      "v_mfma_f32_16x16x32_bf16 v[212:215], a[12:15], v[248:251], 0\n"
      "v_cvt_pk_bf16_f32 v175, v158, v159\n"
      "v_cvt_pk_bf16_f32 v174, v156, v157\n"
      "v_mfma_f32_16x16x32_bf16 v[212:215], a[48:51], v[200:203], v[212:215]\n"
      "v_cvt_pk_bf16_f32 v144, v144, v145\n"
      "v_cvt_pk_bf16_f32 v145, v146, v147\n"
      "v_mfma_f32_16x16x32_bf16 v[192:195], a[16:19], v[248:251], 0\n"
      "s_nop 0\n"
      "v_cvt_pk_bf16_f32 v146, v140, v141\n"
      "v_cvt_pk_bf16_f32 v147, v142, v143\n"
      "v_mfma_f32_16x16x32_bf16 v[192:195], a[52:55], v[200:203], v[192:195]\n"
      "s_waitcnt lgkmcnt(2)\n"
      "v_fma_f32 v243, v164, v164, s38\n"
      "v_fma_f32 v205, v168, v168, s38\n"
      "v_mfma_f32_16x16x32_bf16 v[188:191], a[20:23], v[248:251], 0\n"
      "v_fmac_f32 v205, v169, v169\n"
      "v_fmac_f32 v243, v165, v165\n"
      "v_mfma_f32_16x16x32_bf16 v[188:191], a[56:59], v[200:203], v[188:191]\n"
      "v_fmac_f32 v205, v170, v170\n"
      "v_fmac_f32 v243, v166, v166\n"
      "v_mfma_f32_16x16x32_bf16 v[252:255], a[72:75], v[220:223], v[252:255]\n"
      "v_fmac_f32 v205, v171, v171\n"
      "v_fmac_f32 v243, v167, v167\n"
      "v_mfma_f32_16x16x32_bf16 v[252:255], a[108:111], v[160:163], v[252:255]\n"
      "v_fmac_f32 v243, v206, v206\n"
      "v_fmac_f32 v205, v232, v232\n"
      "v_mfma_f32_16x16x32_bf16 v[196:199], a[76:79], v[220:223], v[196:199]\n"
      "v_fmac_f32 v243, v207, v207\n"
      "v_fmac_f32 v205, v233, v233\n"
      "v_mfma_f32_16x16x32_bf16 v[196:199], a[112:115], v[160:163], v[196:199]\n"
      "v_fmac_f32 v205, v234, v234\n"
      "v_fmac_f32 v243, v208, v208\n"
      "v_mfma_f32_16x16x32_bf16 v[148:151], a[80:83], v[220:223], v[148:151]\n"
      "v_fmac_f32 v243, v209, v209\n"
      "v_fmac_f32 v205, v235, v235\n"
      "v_mfma_f32_16x16x32_bf16 v[148:151], a[116:119], v[160:163], v[148:151]\n"
      "v_fmac_f32 v243, v136, v136\n"
      "v_fmac_f32 v205, v244, v244\n"
      "v_mfma_f32_16x16x32_bf16 v[212:215], a[84:87], v[220:223], v[212:215]\n"
      "v_fmac_f32 v243, v137, v137\n"
      "v_fmac_f32 v205, v245, v245\n"
      "v_mfma_f32_16x16x32_bf16 v[212:215], a[120:123], v[160:163], v[212:215]\n"
      "v_fmac_f32 v243, v138, v138\n"
      "v_fmac_f32 v205, v246, v246\n"
      "v_mfma_f32_16x16x32_bf16 v[192:195], a[88:91], v[220:223], v[192:195]\n"
      "v_fmac_f32 v243, v139, v139\n"
      "v_fmac_f32 v205, v247, v247\n"
      "v_mfma_f32_16x16x32_bf16 v[192:195], a[124:127], v[160:163], v[192:195]\n"
      "s_nop 0\n"
      "v_permlane16_swap_b32 v205, v243\n"
      "v_mfma_f32_16x16x32_bf16 v[188:191], a[92:95], v[220:223], v[188:191]\n"
      "v_add_f32 v205, v205, v243\n"
      "v_mov_b32 v243, v205\n"
      "v_mfma_f32_16x16x32_bf16 v[188:191], a[128:131], v[160:163], v[188:191]\n"
      "s_nop 0\n"
      "v_permlane32_swap_b32 v205, v243\n"
      "v_mfma_f32_16x16x32_bf16 v[176:179], v[248:251], a[24:27], 0\n"
      "v_add_f32_e64 v205, v205, v243\n"
      "v_mfma_f32_16x16x32_bf16 v[176:179], v[200:203], a[60:63], v[176:179]\n"
      "v_rsq_f32 v205, v205\n"
      "v_mfma_f32_16x16x32_bf16 v[176:179], v[220:223], a[96:99], v[176:179]\n"
      "v_mov_b32 v243, v205\n"
      "v_mfma_f32_16x16x32_bf16 v[176:179], v[160:163], a[132:135], v[176:179]\n"
      "s_nop 0\n"
      "v_permlane16_swap_b32 v205, v243\n"
      "v_mul_f32 v207, v243, v207\n"
      "v_mfma_f32_16x16x32_bf16 v[156:159], v[248:251], a[28:31], 0\n"
      "v_mul_f32 v164, v243, v164\n"
      "v_mul_f32_e64 v165, v243, v165\n"
      "v_mfma_f32_16x16x32_bf16 v[156:159], v[200:203], a[64:67], v[156:159]\n"
      "v_cvt_pk_bf16_f32 v164, v164, v165\n"
      "v_mul_f32 v166, v243, v166\n"
      "v_mfma_f32_16x16x32_bf16 v[156:159], v[220:223], a[100:103], v[156:159]\n"
      "v_mul_f32 v167, v243, v167\n"
      "v_cvt_pk_bf16_f32 v165, v166, v167\n"
      "v_mfma_f32_16x16x32_bf16 v[156:159], v[160:163], a[136:139], v[156:159]\n"
      "v_mul_f32 v206, v243, v206\n"
      "v_mul_f32 v209, v243, v209\n"
      "v_mfma_f32_16x16x32_bf16 v[140:143], v[248:251], a[32:35], 0\n"
      "v_mul_f32_e64 v208, v243, v208\n"
      "v_cvt_pk_bf16_f32 v166, v206, v207\n"
      "v_mfma_f32_16x16x32_bf16 v[140:143], v[200:203], a[68:71], v[140:143]\n"
      "ds_read_b128 v[200:203], v115 offset:16384\n"
      "v_cvt_pk_bf16_f32 v167, v208, v209\n"
      "v_mfma_f32_16x16x32_bf16 v[140:143], v[220:223], a[104:107], v[140:143]\n"
      "ds_read_b128 v[206:209], v114 offset:16384\n"
      "v_mul_f32_e64 v136, v243, v136\n"
      "v_mfma_f32_16x16x32_bf16 v[140:143], v[160:163], a[140:143], v[140:143]\n"
      "v_mul_f32 v137, v243, v137\n"
      "v_cvt_pk_bf16_f32 v40, v136, v137\n"
      "s_waitcnt vmcnt(12)\n"
      "v_mfma_f32_16x16x32_bf16 v[248:251], v[152:155], v[8:11], 0\n"
      "v_mul_f32 v138, v243, v138\n"
      "v_mul_f32 v139, v243, v139\n"
      "s_waitcnt vmcnt(11)\n"
      "v_mfma_f32_16x16x32_bf16 v[248:251], v[152:155], v[12:15], v[248:251]\n"
      "v_mul_f32 v205, s36, v205\n"
      "v_cvt_pk_bf16_f32 v41, v138, v139\n"
      "s_waitcnt vmcnt(10)\n"
      "v_mfma_f32_16x16x32_bf16 v[248:251], v[152:155], v[16:19], v[248:251]\n"
      "v_mul_f32 v235, v205, v235\n"
      "v_mul_f32 v246, v205, v246\n"
      "v_mul_f32 v168, v205, v168\n"
      "v_mul_f32 v169, v205, v169\n"
      "v_mul_f32 v247, v205, v247\n"
      "v_mul_f32 v232, v205, v232\n"
      "s_waitcnt vmcnt(9)\n"
      "v_mfma_f32_16x16x32_bf16 v[160:163], v[152:155], v[20:23], 0\n"
      "v_cvt_pk_bf16_f32 v168, v168, v169\n"
      "v_mul_f32 v170, v205, v170\n"
      "s_waitcnt vmcnt(8)\n"
      "v_mfma_f32_16x16x32_bf16 v[160:163], v[152:155], v[24:27], v[160:163]\n"
      "v_mul_f32 v171, v205, v171\n"
      "v_cvt_pk_bf16_f32 v169, v170, v171\n"
      "s_waitcnt vmcnt(7)\n"
      "v_mfma_f32_16x16x32_bf16 v[160:163], v[152:155], v[28:31], v[160:163]\n"
      "s_waitcnt lgkmcnt(0)\n"
      "v_mul_f32 v244, v205, v244\n"
      "v_mul_f32_e64 v234, v205, v234\n"
      "v_mfma_f32_16x16x32_bf16 v[252:255], a[144:147], v[206:209], v[252:255]\n"
      "v_mul_f32_e64 v245, v205, v245\n"
      "v_cvt_pk_bf16_f32 v171, v234, v235\n"
      "v_mfma_f32_16x16x32_bf16 v[252:255], a[180:183], v[200:203], v[252:255]\n"
      "v_mul_f32_e64 v233, v205, v233\n"
      "v_add_f32_dpp v160, v160, v160 quad_perm:[1,0,3,2] row_mask:0xf bank_mask:0xf\n"
      "v_mfma_f32_16x16x32_bf16 v[196:199], a[148:151], v[206:209], v[196:199]\n"
      "v_add_f32_dpp v248, v248, v248 quad_perm:[1,0,3,2] row_mask:0xf bank_mask:0xf\n"
      "v_cvt_pk_bf16_f32 v39, v246, v247\n"
      "v_mfma_f32_16x16x32_bf16 v[196:199], a[184:187], v[200:203], v[196:199]\n"
      "s_nop 0\n"
      "v_cvt_pk_bf16_f32 v170, v232, v233\n"
      "v_add_f32_dpp v248, v248, v248 quad_perm:[2,3,0,1] row_mask:0xf bank_mask:0xf\n"
      "v_mfma_f32_16x16x32_bf16 v[148:151], a[152:155], v[206:209], v[148:151]\n"
      "s_nop 0\n"
      "v_add_f32_dpp v248, v248, v248 row_half_mirror row_mask:0xf bank_mask:0xf\n"
      "v_mul_f32 v251, 0xbbaaaaab, v248\n"
      "v_mfma_f32_16x16x32_bf16 v[148:151], a[188:191], v[200:203], v[148:151]\n"
      "v_mul_f32 v250, 0xbbaaaaab, v248\n"
      "v_mul_f32 v249, 0xbbaaaaab, v248\n"
      "v_mfma_f32_16x16x32_bf16 v[212:215], a[156:159], v[206:209], v[212:215]\n"
      "s_nop 0\n"
      "v_mul_f32 v248, 0xbbaaaaab, v248\n"
      "v_add_f32_dpp v160, v160, v160 quad_perm:[2,3,0,1] row_mask:0xf bank_mask:0xf\n"
      "v_mfma_f32_16x16x32_bf16 v[212:215], a[192:195], v[200:203], v[212:215]\n"
      "s_nop 0\n"
      "v_add_f32_dpp v160, v160, v160 row_half_mirror row_mask:0xf bank_mask:0xf\n"
      "v_cvt_pk_bf16_f32 v38, v244, v245\n"
      "v_mfma_f32_16x16x32_bf16 v[232:235], v[236:239], v[8:11], v[248:251]\n"
      "v_mfma_f32_16x16x32_bf16 v[244:247], v[238:241], v[8:11], v[248:251]\n"
      "ds_read_b64 v[242:243], v133 offset:16\n"
      "v_mul_f32 v163, 0xbbaaaaab, v160\n"
      "v_mfma_f32_16x16x32_bf16 v[224:227], v[236:239], v[16:19], v[248:251]\n"
      "ds_read_u16 v56, v134 offset:32\n"
      "v_mul_f32 v162, 0xbbaaaaab, v160\n"
      "v_mfma_f32_16x16x32_bf16 v[152:155], v[238:241], v[12:15], v[248:251]\n"
      "s_nop 1\n"
      "v_cvt_pk_bf16_f32 v244, v244, v245\n"
      "v_mfma_f32_16x16x32_bf16 v[192:195], a[160:163], v[206:209], v[192:195]\n"
      "s_nop 0\n"
      "v_cvt_pk_bf16_f32 v245, v246, v247\n"
      "v_mfma_f32_16x16x32_bf16 v[192:195], a[196:199], v[200:203], v[192:195]\n"
      "v_cvt_pk_bf16_f32 v247, v234, v235\n"
      "v_mfma_f32_16x16x32_bf16 v[188:191], a[164:167], v[206:209], v[188:191]\n"
      "v_cvt_pk_bf16_f32 v246, v232, v233\n"
      "v_mul_f32 v161, 0xbbaaaaab, v160\n"
      "v_mfma_f32_16x16x32_bf16 v[188:191], a[200:203], v[200:203], v[188:191]\n"
      "v_mfma_f32_16x16x32_bf16 v[232:235], v[238:241], v[16:19], v[248:251]\n"
      "ds_write_b128 v204, v[244:247] offset:12288\n"
      "v_mfma_f32_16x16x32_bf16 v[244:247], v[236:239], v[12:15], v[248:251]\n"
      "ds_read_u16 v60, v134 offset:64\n"
      "v_mfma_f32_16x16x32_bf16 v[176:179], v[206:209], a[168:171], v[176:179]\n"
      "v_mfma_f32_16x16x32_bf16 v[176:179], v[200:203], a[204:207], v[176:179]\n"
      "v_cvt_pk_bf16_f32 v152, v152, v153\n"
      "v_mfma_f32_16x16x32_bf16 v[156:159], v[206:209], a[172:175], v[156:159]\n"
      "v_mfma_f32_16x16x32_bf16 v[156:159], v[200:203], a[208:211], v[156:159]\n"
      "v_mul_f32 v160, 0xbbaaaaab, v160\n"
      "v_cvt_pk_bf16_f32 v232, v232, v233\n"
      "v_mfma_f32_16x16x32_bf16 v[140:143], v[206:209], a[176:179], v[140:143]\n"
      "v_cvt_pk_bf16_f32 v153, v154, v155\n"
      "v_cvt_pk_bf16_f32 v155, v246, v247\n"
      "v_mfma_f32_16x16x32_bf16 v[140:143], v[200:203], a[212:215], v[140:143]\n"
      "v_cvt_pk_bf16_f32 v233, v234, v235\n"
      "v_cvt_pk_bf16_f32 v154, v244, v245\n"
      "v_mfma_f32_16x16x32_bf16 v[200:203], v[238:241], v[20:23], v[160:163]\n"
      "ds_write_b128 v204, v[152:155] offset:12416\n"
      "v_mfma_f32_16x16x32_bf16 v[136:139], v[236:239], v[24:27], v[160:163]\n"
      "v_cvt_pk_bf16_f32 v234, v224, v225\n"
      "v_cvt_pk_bf16_f32 v235, v226, v227\n"
      "v_mfma_f32_16x16x32_bf16 v[244:247], v[236:239], v[20:23], v[160:163]\n"
      "ds_read_b128 v[248:251], v132 offset:32\n"
      "v_mfma_f32_16x16x32_bf16 v[206:209], v[238:241], v[28:31], v[160:163]\n"
      "ds_write_b128 v204, v[232:235] offset:12544\n"
      "v_mfma_f32_16x16x32_bf16 v[152:155], v[238:241], v[24:27], v[160:163]\n"
      "ds_read_b64 v[240:241], v132 offset:24\n"
      "v_cvt_pk_bf16_f32 v200, v200, v201\n"
      "v_mfma_f32_16x16x32_bf16 v[232:235], v[236:239], v[28:31], v[160:163]\n"
      "ds_read_b128 v[236:239], v133\n"
      "v_cvt_pk_bf16_f32 v201, v202, v203\n"
      "v_cvt_pk_bf16_f32 v202, v244, v245\n"
      "v_cvt_pk_bf16_f32 v206, v206, v207\n"
      "v_cvt_pk_bf16_f32 v207, v208, v209\n"
      "v_cvt_pk_bf16_f32 v203, v246, v247\n"
      "v_cvt_pk_bf16_f32 v152, v152, v153\n"
      "v_cvt_pk_bf16_f32 v153, v154, v155\n"
      "v_cvt_pk_bf16_f32 v208, v232, v233\n"
      "v_cvt_pk_bf16_f32 v155, v138, v139\n"
      "v_cvt_pk_bf16_f32 v209, v234, v235\n"
      "v_cvt_pk_bf16_f32 v154, v136, v137\n"
      "s_waitcnt vmcnt(0)\n"
      "ds_write_b128 v204, v[152:155] offset:15488\n"
      "ds_write_b128 v204, v[206:209] offset:15616\n"
      "ds_write_b128 v204, v[200:203] offset:15360\n"
      "ds_read_b128 v[204:207], v119 offset:41728\n"
      "ds_read_b32 v234, v122 offset:4864\n"
      "ds_read_u16 v52, v134\n"
      "s_waitcnt lgkmcnt(2)\n"
      "v_or3_b32 v204, v204, v205, v206\n"
      "v_and_b32 v204, s40, v204\n"
      "s_waitcnt lgkmcnt(1)\n"
      "v_bitop3_b32 v234, v234, v207, s40 bitop3:0xa8\n"
      "v_or3_b32 v204, v204, v234, s4\n"
      "ds_write_b32 v129, v204 offset:3072\n"
      "s_waitcnt lgkmcnt(0)\n"
      "s_barrier\n"
      "v_mfma_f32_16x16x32_bf16 v[204:207], v[180:183], v[184:187], v[78:81]\n"
      "ds_read_b64_tr_b16 a[216:217], v230\n"
      "v_fma_f32 v155, v252, v252, s38\n"
      "v_mfma_f32_16x16x32_bf16 v[204:207], v[34:37], v[32:35], v[204:207]\n"
      "ds_read_b64_tr_b16 a[220:221], v230 offset:8\n"
      "v_fma_f32 v154, v212, v212, s38\n"
      "v_mfma_f32_16x16x32_bf16 v[232:235], v[164:167], v[184:187], v[82:85]\n"
      "ds_read_b64_tr_b16 a[224:225], v231\n"
      "v_fmac_f32 v155, v253, v253\n"
      "v_mfma_f32_16x16x32_bf16 v[232:235], v[40:43], v[32:35], v[232:235]\n"
      "ds_read_b64_tr_b16 a[218:219], v230 offset:6144\n"
      "v_fmac_f32 v154, v213, v213\n"
      "v_mfma_f32_16x16x32_bf16 v[200:203], v[248:251], v[184:187], v[70:73]\n"
      "ds_read_b64_tr_b16 a[222:223], v230 offset:6152\n"
      "v_fmac_f32_e64 v155, v254, v254\n"
      "v_mfma_f32_16x16x32_bf16 v[200:203], v[236:239], v[180:183], v[200:203]\n"
      "ds_read_b64_tr_b16 a[226:227], v231 offset:6144\n"
      "v_fmac_f32 v154, v214, v214\n"
      "v_mfma_f32_16x16x32_bf16 v[200:203], v[240:243], v[32:35], v[200:203]\n"
      "ds_read_b64_tr_b16 a[228:229], v230 offset:18432\n"
      "v_fmac_f32 v155, v255, v255\n"
      "v_fmac_f32 v154, v215, v215\n"
      "v_fmac_f32 v155, v196, v196\n"
      "v_fmac_f32 v154, v192, v192\n"
      "v_fmac_f32 v155, v197, v197\n"
      "v_fmac_f32 v154, v193, v193\n"
      "v_fmac_f32 v155, v198, v198\n"
      "v_fmac_f32 v154, v194, v194\n"
      "v_fmac_f32 v155, v199, v199\n"
      "v_fmac_f32 v154, v195, v195\n"
      "v_fmac_f32 v155, v148, v148\n"
      "v_fmac_f32 v154, v188, v188\n"
      "v_fmac_f32 v155, v149, v149\n"
      "v_fmac_f32 v154, v189, v189\n"
      "v_fmac_f32 v155, v150, v150\n"
      "v_fmac_f32 v154, v190, v190\n"
      "v_fmac_f32 v155, v151, v151\n"
      "v_fmac_f32 v154, v191, v191\n"
      "s_nop 1\n"
      "v_permlane16_swap_b32 v155, v154\n"
      "v_add_f32 v155, v155, v154\n"
      "v_mov_b32 v154, v155\n"
      "s_nop 1\n"
      "v_permlane32_swap_b32 v155, v154\n"
      "v_add_f32 v155, v155, v154\n"
      "v_rsq_f32 v155, v155\n"
      "v_cvt_pk_bf16_f32 v50, v176, v177\n"
      "v_mov_b32 v154, v155\n"
      "v_cvt_pk_bf16_f32 v51, v178, v179\n"
      "s_nop 0\n"
      "v_permlane16_swap_b32 v155, v154\n"
      "v_mul_f32 v212, v154, v212\n"
      "v_mul_f32 v213, v154, v213\n"
      "v_cvt_pk_bf16_f32 v212, v212, v213\n"
      "v_mul_f32 v214, v154, v214\n"
      "v_mul_f32 v215, v154, v215\n"
      "v_cvt_pk_bf16_f32 v213, v214, v215\n"
      "v_mul_f32 v192, v154, v192\n"
      "v_mul_f32 v193, v154, v193\n"
      "v_cvt_pk_bf16_f32 v214, v192, v193\n"
      "v_mul_f32 v194, v154, v194\n"
      "v_mul_f32 v195, v154, v195\n"
      "v_cvt_pk_bf16_f32 v215, v194, v195\n"
      "v_mul_f32 v188, v154, v188\n"
      "v_mul_f32 v189, v154, v189\n"
      "v_cvt_pk_bf16_f32 v46, v188, v189\n"
      "v_mul_f32 v190, v154, v190\n"
      "v_mul_f32 v191, v154, v191\n"
      "v_cvt_pk_bf16_f32 v47, v190, v191\n"
      "v_mul_f32 v155, s36, v155\n"
      "v_mul_f32 v252, v155, v252\n"
      "v_mfma_f32_16x16x32_bf16 v[192:195], v[212:215], v[184:187], v[86:89]\n"
      "ds_read_b64_tr_b16 a[232:233], v230 offset:18440\n"
      "v_mul_f32_e64 v253, v155, v253\n"
      "v_mfma_f32_16x16x32_bf16 v[192:195], v[46:49], v[32:35], v[192:195]\n"
      "ds_read_b64_tr_b16 a[236:237], v231 offset:18432\n"
      "ds_read_b64_tr_b16 a[230:231], v230 offset:24576\n"
      "ds_read_b64_tr_b16 a[234:235], v230 offset:24584\n"
      "ds_read_b64_tr_b16 a[238:239], v231 offset:24576\n"
      "ds_read_b64_tr_b16 a[240:241], v229\n"
      "ds_read_b64_tr_b16 a[244:245], v229 offset:8\n"
      "ds_read_b64_tr_b16 a[248:249], v228\n"
      "ds_read_b64_tr_b16 a[242:243], v229 offset:6144\n"
      "ds_read_b64_tr_b16 a[246:247], v229 offset:6152\n"
      "ds_read_b64_tr_b16 a[250:251], v228 offset:6144\n"
      "ds_read_b64_tr_b16 a[252:253], v229 offset:18432\n"
      "ds_read_b64_tr_b16 v[0:1], v229 offset:18440\n"
      "ds_read_b64_tr_b16 v[4:5], v228 offset:18432\n"
      "ds_read_b64_tr_b16 a[254:255], v229 offset:24576\n"
      "ds_read_b64_tr_b16 v[2:3], v229 offset:24584\n"
      "ds_read_b64_tr_b16 v[6:7], v228 offset:24576\n"
      "ds_read_b64_tr_b16 v[8:9], v230 offset:12288\n"
      "ds_read_b64_tr_b16 v[12:13], v230 offset:12296\n"
      "ds_read_b64_tr_b16 v[16:17], v231 offset:12288\n"
      "ds_read_b64_tr_b16 v[10:11], v230 offset:30720\n"
      "ds_read_b64_tr_b16 v[14:15], v230 offset:30728\n"
      "ds_read_b64_tr_b16 v[18:19], v231 offset:30720\n"
      "ds_read_b64_tr_b16 v[20:21], v229 offset:12288\n"
      "ds_read_b64_tr_b16 v[24:25], v229 offset:12296\n"
      "ds_read_b64_tr_b16 v[28:29], v228 offset:12288\n"
      "ds_read_b64_tr_b16 v[22:23], v229 offset:30720\n"
      "ds_read_b64_tr_b16 v[26:27], v229 offset:30728\n"
      "ds_read_b64_tr_b16 v[30:31], v228 offset:30720\n"
      "v_cvt_pk_bf16_f32 v252, v252, v253\n"
      "v_mul_f32 v254, v155, v254\n"
      "v_mul_f32 v255, v155, v255\n"
      "v_cvt_pk_bf16_f32 v253, v254, v255\n"
      "v_mul_f32 v196, v155, v196\n"
      "v_mul_f32 v197, v155, v197\n"
      "v_cvt_pk_bf16_f32 v254, v196, v197\n"
      "v_mul_f32 v198, v155, v198\n"
      "v_mul_f32 v199, v155, v199\n"
      "v_cvt_pk_bf16_f32 v255, v198, v199\n"
      "v_mul_f32 v148, v155, v148\n"
      "v_mul_f32 v149, v155, v149\n"
      "v_cvt_pk_bf16_f32 v44, v148, v149\n"
      "v_mul_f32 v150, v155, v150\n"
      "v_mul_f32 v151, v155, v151\n"
      "v_cvt_pk_bf16_f32 v45, v150, v151\n"
      "v_cvt_pk_bf16_f32 v54, v156, v157\n"
      "v_cvt_pk_bf16_f32 v55, v158, v159\n"
      "v_cvt_pk_bf16_f32 v58, v140, v141\n"
      "v_cvt_pk_bf16_f32 v59, v142, v143\n"
      "s_waitcnt lgkmcnt(0)\n"
      "s_barrier\n"
      "s_or_b64 s[34:35], s[48:49], s[50:51]\n"
      "s_cbranch_scc1 L_gen_8_%=\n"
      "L_fast_11_%=:\n"
      "v_mfma_f32_16x16x32_bf16 v[196:199], v[180:183], v[168:171], v[90:93]\n"
      "v_exp_f32_e64 v204, v204\n"
      "v_mfma_f32_16x16x32_bf16 v[196:199], v[34:37], v[38:41], v[196:199]\n"
      "ds_read_b128 v[148:151], v128 offset:3072\n"
      "v_exp_f32 v233, v233\n"
      "v_mfma_f32_16x16x32_bf16 v[184:187], v[164:167], v[168:171], v[94:97]\n"
      "v_exp_f32 v205, v205\n"
      "v_mfma_f32_16x16x32_bf16 v[184:187], v[40:43], v[38:41], v[184:187]\n"
      "v_exp_f32 v235, v235\n"
      "v_exp_f32 v206, v206\n"
      "v_mfma_f32_16x16x32_bf16 v[228:231], v[248:251], v[168:171], v[72:75]\n"
      "v_exp_f32_e64 v193, v193\n"
      "v_mfma_f32_16x16x32_bf16 v[228:231], v[236:239], v[164:167], v[228:231]\n"
      "v_exp_f32 v207, v207\n"
      "v_mfma_f32_16x16x32_bf16 v[228:231], v[240:243], v[38:41], v[228:231]\n"
      "v_exp_f32 v232, v232\n"
      "v_mfma_f32_16x16x32_bf16 v[188:191], v[212:215], v[168:171], v[98:101]\n"
      "v_exp_f32 v197, v197\n"
      "v_mfma_f32_16x16x32_bf16 v[188:191], v[46:49], v[38:41], v[188:191]\n"
      "v_cvt_pk_bf16_f32 v204, v204, v205\n"
      "v_exp_f32 v196, v196\n"
      "v_mfma_f32_16x16x32_bf16 v[176:179], v[180:183], v[252:255], v[102:105]\n"
      "v_exp_f32_e64 v234, v234\n"
      "v_mfma_f32_16x16x32_bf16 v[176:179], v[34:37], v[44:47], v[176:179]\n"
      "v_cvt_pk_bf16_f32 v205, v206, v207\n"
      "v_cvt_pk_bf16_f32 v206, v232, v233\n"
      "v_cvt_pk_bf16_f32 v207, v234, v235\n"
      "v_mfma_f32_16x16x32_bf16 v[180:183], v[164:167], v[252:255], v[106:109]\n"
      "v_exp_f32 v192, v192\n"
      "v_mfma_f32_16x16x32_bf16 v[180:183], v[40:43], v[44:47], v[180:183]\n"
      "s_waitcnt lgkmcnt(0)\n"
      "v_exp_f32 v191, v191\n"
      "v_mfma_f32_16x16x32_bf16 v[232:235], v[248:251], v[252:255], v[74:77]\n"
      "v_exp_f32 v194, v194\n"
      "v_mfma_f32_16x16x32_bf16 v[232:235], v[236:239], v[212:215], v[232:235]\n"
      "v_exp_f32_e64 v195, v195\n"
      "v_mfma_f32_16x16x32_bf16 v[232:235], v[240:243], v[44:47], v[232:235]\n"
      "v_exp_f32 v200, v200\n"
      "v_exp_f32 v228, v228\n"
      "v_bitop3_b32 v148, v148, v149, s4 bitop3:0x7e\n"
      "v_mfma_f32_16x16x32_bf16 v[152:155], v[212:215], v[252:255], v[110:113]\n"
      "v_cvt_pk_bf16_f32 v62, v192, v193\n"
      "v_cvt_pk_bf16_f32 v63, v194, v195\n"
      "v_mfma_f32_16x16x32_bf16 v[152:155], v[46:49], v[44:47], v[152:155]\n"
      "v_exp_f32 v189, v189\n"
      "v_exp_f32 v190, v190\n"
      "v_exp_f32 v185, v185\n"
      "v_mfma_f32_16x16x32_bf16 v[212:215], v[236:239], v[248:251], v[76:79]\n"
      "v_bitop3_b32 v150, v150, v151, s4 bitop3:0x7e\n"
      "v_or_b32 v148, v148, v150\n"
      "v_cvt_pk_bf16_f32 v64, v200, 0\n"
      "v_mfma_f32_16x16x32_bf16 v[244:247], v[66:69], v[204:207], 0\n"
      "v_cmp_ne_u32 vcc, 0, v148\n"
      "v_cndmask_b32 v201, v201, v229, s[42:43]\n"
      "v_mfma_f32_16x16x32_bf16 v[244:247], v[66:69], v[62:65], v[244:247]\n"
      "s_mov_b64 s[48:49], vcc\n"
      "v_exp_f32 v199, v199\n"
      "v_exp_f32 v186, v186\n"
      "v_mfma_f32_16x16x32_bf16 v[248:251], v[216:219], v[204:207], 0\n"
      "s_cbranch_vccnz L_flg_12_%=\n"
      "L_fb_13_%=:\n"
      "v_mfma_f32_16x16x32_bf16 v[248:251], v[50:53], v[62:65], v[248:251]\n"
      "s_add_u32 s28, s27, 2\n"
      "v_exp_f32 v188, v188\n"
      "v_cvt_pk_bf16_f32 v196, v196, v197\n"
      "v_mfma_f32_16x16x32_bf16 v[148:151], v[172:175], v[204:207], 0\n"
      "s_min_u32 m0, s28, s3\n"
      "v_exp_f32_e64 v198, v198\n"
      "v_mfma_f32_16x16x32_bf16 v[148:151], v[54:57], v[62:65], v[148:151]\n"
      "s_movrels_b32 s9, s80\n"
      "v_exp_f32 v179, v179\n"
      "v_mfma_f32_16x16x32_bf16 v[234:237], v[144:147], v[204:207], 0\n"
      "s_lshr_b32 s8, s9, 8\n"
      "v_exp_f32 v184, v184\n"
      "v_mfma_f32_16x16x32_bf16 v[234:237], v[58:61], v[62:65], v[234:237]\n"
      "s_and_b32 s9, s9, 0xff\n"
      "v_exp_f32_e64 v187, v187\n"
      "v_cvt_pk_bf16_f32 v197, v198, v199\n"
      "v_cvt_pk_bf16_f32 v64, v228, 0\n"
      "v_cvt_pk_bf16_f32 v198, v184, v185\n"
      "v_cvt_pk_bf16_f32 v199, v186, v187\n"
      "v_rcp_f32_e64 v244, v244\n"
      "v_cvt_pk_bf16_f32 v62, v188, v189\n"
      "v_mfma_f32_16x16x16_bf16 v[212:215], v[242:243], v[240:241], v[212:215]\n"
      "s_mul_i32 s10, s8, 73\n"
      "v_cvt_pk_bf16_f32 v63, v190, v191\n"
      "v_mul_f32 v251, v244, v251\n"
      "v_mul_f32 v249, v244, v249\n"
      "v_mfma_f32_16x16x32_bf16 v[184:187], v[66:69], v[196:199], 0\n"
      "ds_read_b128 v[168:171], v114 offset:18432\n"
      "v_mul_f32_e64 v248, v244, v248\n"
      "v_mfma_f32_16x16x32_bf16 v[184:187], v[66:69], v[62:65], v[184:187]\n"
      "ds_read_b128 v[192:195], v115 offset:18432\n"
      "v_exp_f32 v152, v152\n"
      "v_mul_f32 v150, v244, v150\n"
      "v_mfma_f32_16x16x32_bf16 v[188:191], v[216:219], v[196:199], 0\n"
      "s_add_u32 s10, s10, s9\n"
      "v_mul_f32 v250, v244, v250\n"
      "v_cvt_pk_bf16_f32 v248, v248, v249\n"
      "v_mfma_f32_16x16x32_bf16 v[188:191], v[50:53], v[62:65], v[188:191]\n"
      "s_mul_i32 s28, s10, 4802\n"
      "v_rcp_f32 v184, v184\n"
      "v_mfma_f32_16x16x32_bf16 v[252:255], v[172:175], v[196:199], 0\n"
      "s_add_u32 s56, s18, s28\n"
      "v_mul_f32 v148, v244, v148\n"
      "v_mul_f32 v149, v244, v149\n"
      "v_mfma_f32_16x16x32_bf16 v[252:255], v[54:57], v[62:65], v[252:255]\n"
      "s_addc_u32 s57, s19, 0\n"
      "v_exp_f32 v178, v178\n"
      "v_exp_f32 v182, v182\n"
      "v_exp_f32 v155, v155\n"
      "v_cvt_pk_bf16_f32 v249, v250, v251\n"
      "v_exp_f32_e64 v181, v181\n"
      "v_cvt_pk_bf16_f32 v250, v148, v149\n"
      "v_mfma_f32_16x16x32_bf16 v[164:167], v[144:147], v[196:199], 0\n"
      "s_and_b32 s56, s56, -16\n"
      "v_mul_f32 v151, v244, v151\n"
      "v_cvt_pk_bf16_f32 v251, v150, v151\n"
      "v_mfma_f32_16x16x32_bf16 v[164:167], v[58:61], v[62:65], v[164:167]\n"
      "ds_write_b128 v124, v[248:251]\n"
      "v_exp_f32_e64 v154, v154\n"
      "v_cndmask_b32 v201, v201, v233, s[44:45]\n"
      "v_cndmask_b32 v201, v201, v213, s[46:47]\n"
      "s_waitcnt lgkmcnt(1)\n"
      "v_mul_f32 v235, v244, v235\n"
      "v_mfma_f32_16x16x32_bf16 v[196:199], a[0:3], v[168:171], 0\n"
      "s_sub_u32 s58, s56, 110592\n"
      "v_mul_f32 v237, v244, v237\n"
      "v_mul_f32 v236, v244, v236\n"
      "v_mfma_f32_16x16x32_bf16 v[196:199], a[36:39], v[192:195], v[196:199]\n"
      "s_subb_u32 s59, s57, 0\n"
      "v_exp_f32 v177, v177\n"
      "v_exp_f32 v232, v232\n"
      "v_mul_f32 v190, v184, v190\n"
      "v_exp_f32 v176, v176\n"
      "v_mul_f32 v234, v244, v234\n"
      "v_cvt_pk_bf16_f32 v176, v176, v177\n"
      "v_exp_f32 v180, v180\n"
      "v_exp_f32 v183, v183\n"
      "v_mfma_f32_16x16x32_bf16 v[228:231], a[4:7], v[168:171], 0\n"
      "s_sub_u32 s56, s56, 73728\n"
      "v_exp_f32 v201, v201\n"
      "v_mfma_f32_16x16x32_bf16 v[228:231], a[40:43], v[192:195], v[228:231]\n"
      "s_subb_u32 s57, s57, 0\n"
      "v_cvt_pk_bf16_f32 v177, v178, v179\n"
      "v_exp_f32_e64 v153, v153\n"
      "v_cvt_pk_bf16_f32 v178, v180, v181\n"
      "v_cvt_pk_bf16_f32 v201, v201, v201\n"
      "v_cvt_pk_bf16_f32 v62, v152, v153\n"
      "v_cvt_pk_bf16_f32 v234, v234, v235\n"
      "v_cvt_pk_bf16_f32 v179, v182, v183\n"
      "v_cvt_pk_bf16_f32 v235, v236, v237\n"
      "v_mul_f32_e64 v166, v184, v166\n"
      "v_cvt_pk_bf16_f32 v63, v154, v155\n"
      "v_cvt_pk_bf16_f32 v64, v232, 0\n"
      "v_mfma_f32_16x16x32_bf16 v[148:151], v[66:69], v[176:179], 0\n"
      "s_mul_i32 s28, s8, 1373568\n"
      "v_mul_f32 v189, v184, v189\n"
      "v_mul_f32 v188, v184, v188\n"
      "v_mfma_f32_16x16x32_bf16 v[148:151], v[66:69], v[62:65], v[148:151]\n"
      "ds_write_b16 v130, v201\n"
      "v_mfma_f32_16x16x32_bf16 v[248:251], v[216:219], v[176:179], 0\n"
      "ds_read_b128 v[152:155], v131\n"
      "v_cvt_pk_bf16_f32 v188, v188, v189\n"
      "v_mfma_f32_16x16x32_bf16 v[248:251], v[50:53], v[62:65], v[248:251]\n"
      "ds_read_b128 v[180:183], v131 offset:64\n"
      "v_mul_f32 v255, v184, v255\n"
      "v_mov_b32 v232, 0\n"
      "v_mul_f32 v165, v184, v165\n"
      "v_mfma_f32_16x16x32_bf16 v[240:243], v[172:175], v[176:179], 0\n"
      "s_mul_i32 s29, s9, 2688\n"
      "v_mul_f32 v191, v184, v191\n"
      "v_cvt_pk_bf16_f32 v189, v190, v191\n"
      "v_mfma_f32_16x16x32_bf16 v[240:243], v[54:57], v[62:65], v[240:243]\n"
      "s_add_u32 s28, s28, s29\n"
      "v_mul_f32 v252, v184, v252\n"
      "v_mul_f32 v164, v184, v164\n"
      "v_mul_f32 v253, v184, v253\n"
      "v_mfma_f32_16x16x32_bf16 v[204:207], v[144:147], v[176:179], 0\n"
      "s_cmp_eq_u32 s8, 72\n"
      "v_mul_f32_e64 v254, v184, v254\n"
      "v_cvt_pk_bf16_f32 v190, v252, v253\n"
      "v_mfma_f32_16x16x32_bf16 v[204:207], v[58:61], v[62:65], v[204:207]\n"
      "s_cselect_b32 s8, 0, 100270464\n"
      "v_mul_f32_e64 v167, v184, v167\n"
      "v_cvt_pk_bf16_f32 v164, v164, v165\n"
      "v_cvt_pk_bf16_f32 v191, v254, v255\n"
      "v_cvt_pk_bf16_f32 v165, v166, v167\n"
      "s_cmp_eq_u32 s9, 72\n"
      "ds_write_b128 v124, v[188:191] offset:1024\n"
      "ds_write2st64_b64 v125, v[234:235], v[164:165] offset0:0 offset1:2\n"
      "s_waitcnt lgkmcnt(2)\n"
      "v_mfma_f32_16x16x32_bf16 v[188:191], v[152:155], v[66:69], 0\n"
      "s_cselect_b32 s9, 0, 196224\n"
      "s_add_u32 s10, s8, s9\n"
      "s_add_u32 s28, s28, s10\n"
      "v_mfma_f32_16x16x32_bf16 v[188:191], v[180:183], v[66:69], v[188:191]\n"
      "ds_write_b32 v129, v232\n"
      "v_mfma_f32_16x16x32_bf16 v[176:179], v[152:155], v[216:219], 0\n"
      "s_add_u32 s52, s16, s28\n"
      "s_addc_u32 s53, s17, 0\n"
      "s_cmp_lg_u32 s10, s11\n"
      "v_mfma_f32_16x16x32_bf16 v[176:179], v[180:183], v[50:53], v[176:179]\n"
      "s_cbranch_scc1 L_edge_15_%=\n"
      "L_eb_16_%=:\n"
      "v_mfma_f32_16x16x32_bf16 v[164:167], v[152:155], v[172:175], 0\n"
      "s_nop 0\n"
      "v_mov_b32_dpp v188, v188 quad_perm:[0,0,0,0] row_mask:0xf bank_mask:0xf\n"
      "v_mfma_f32_16x16x32_bf16 v[164:167], v[180:183], v[54:57], v[164:167]\n"
      "v_rcp_f32_e64 v188, v188\n"
      "v_mfma_f32_16x16x32_bf16 v[236:239], v[152:155], v[144:147], 0\n"
      "v_mfma_f32_16x16x32_bf16 v[236:239], v[180:183], v[58:61], v[236:239]\n"
      "s_nop 4\n"
      "v_cndmask_b32 v176, v176, v164, s[42:43]\n"
      "s_nop 1\n"
      "v_cndmask_b32 v176, v176, v236, s[44:45]\n"
      "v_mul_f32 v176, v188, v176\n"
      "v_cvt_pk_bf16_f32 v176, v176, v176\n"
      "ds_write_b16 v135, v176\n"
      "L_join_10_%=:\n"
      "s_mov_b32 s27, 1\n"
      ".p2align 6\n"
      "L_loop_%=:\n"
      "v_mfma_f32_16x16x32_bf16 v[172:175], a[8:11], v[168:171], 0\n"
      "ds_read_b128 v[236:239], v114 offset:24576\n"
      "s_add_u32 m0, s76, 0\n"
      "v_mfma_f32_16x16x32_bf16 v[172:175], a[44:47], v[192:195], v[172:175]\n"
      "ds_read_b128 v[164:167], v115 offset:24576\n"
      "v_rcp_f32 v148, v148\n"
      "v_mfma_f32_16x16x32_bf16 v[252:255], a[12:15], v[168:171], 0\n"
      "v_mul_f32 v249, v148, v249\n"
      "v_mul_f32 v248, v148, v248\n"
      "v_mfma_f32_16x16x32_bf16 v[252:255], a[48:51], v[192:195], v[252:255]\n"
      "ds_read_b128 v[216:219], v114 offset:30720\n"
      "v_cvt_pk_bf16_f32 v248, v248, v249\n"
      "v_mfma_f32_16x16x32_bf16 v[144:147], a[16:19], v[168:171], 0\n"
      "v_mul_f32 v251, v148, v251\n"
      "v_mul_f32 v250, v148, v250\n"
      "v_mfma_f32_16x16x32_bf16 v[144:147], a[52:55], v[192:195], v[144:147]\n"
      "s_waitcnt lgkmcnt(3)\n"
      "s_barrier\n"
      "v_mfma_f32_16x16x32_bf16 v[156:159], a[20:23], v[168:171], 0\n"
      "ds_read_b128 v[160:163], v128\n"
      "v_cvt_pk_bf16_f32 v249, v250, v251\n"
      "v_mfma_f32_16x16x32_bf16 v[156:159], a[56:59], v[192:195], v[156:159]\n"
      "global_load_lds_dwordx4 v116, s[52:53]\n"
      "v_mfma_f32_16x16x32_bf16 v[140:143], v[168:171], a[24:27], 0\n"
      "v_mul_f32 v240, v148, v240\n"
      "v_mul_f32 v241, v148, v241\n"
      "v_mfma_f32_16x16x32_bf16 v[140:143], v[192:195], a[60:63], v[140:143]\n"
      "v_mul_f32_e64 v242, v148, v242\n"
      "v_cvt_pk_bf16_f32 v250, v240, v241\n"
      "v_mfma_f32_16x16x32_bf16 v[224:227], v[168:171], a[28:31], 0\n"
      "s_add_u32 m0, s76, 6016\n"
      "v_mul_f32_e64 v243, v148, v243\n"
      "v_cvt_pk_bf16_f32 v251, v242, v243\n"
      "v_mfma_f32_16x16x32_bf16 v[224:227], v[192:195], a[64:67], v[224:227]\n"
      "ds_write_b128 v124, v[248:251] offset:2048\n"
      "v_mfma_f32_16x16x32_bf16 v[136:139], v[168:171], a[32:35], 0\n"
      "ds_read_b128 v[248:251], v115 offset:30720\n"
      "v_mul_f32 v204, v148, v204\n"
      "v_mfma_f32_16x16x32_bf16 v[136:139], v[192:195], a[68:71], v[136:139]\n"
      "s_waitcnt lgkmcnt(3)\n"
      "v_mul_f32_e64 v205, v148, v205\n"
      "v_cvt_pk_bf16_f32 v204, v204, v205\n"
      "v_mfma_f32_16x16x32_bf16 v[196:199], a[72:75], v[236:239], v[196:199]\n"
      "s_waitcnt lgkmcnt(2)\n"
      "v_mul_f32 v206, v148, v206\n"
      "v_mul_f32_e64 v207, v148, v207\n"
      "v_mfma_f32_16x16x32_bf16 v[196:199], a[108:111], v[164:167], v[196:199]\n"
      "global_load_lds_dwordx4 v116, s[52:53] offset:128\n"
      "v_mfma_f32_16x16x32_bf16 v[228:231], a[76:79], v[236:239], v[228:231]\n"
      "v_cvt_pk_bf16_f32 v205, v206, v207\n"
      "v_add_f32 v160, |v160|, |v161|\n"
      "v_mfma_f32_16x16x32_bf16 v[228:231], a[112:115], v[164:167], v[228:231]\n"
      "ds_write_b64 v125, v[204:205] offset:2048\n"
      "v_mfma_f32_16x16x32_bf16 v[172:175], a[80:83], v[236:239], v[172:175]\n"
      "v_add_f32 v162, |v162|, |v163|\n"
      "v_add_f32 v160, v160, v162\n"
      "v_mfma_f32_16x16x32_bf16 v[172:175], a[116:119], v[164:167], v[172:175]\n"
      "s_add_u32 m0, s76, 12032\n"
      "v_rsq_f32 v160, v160\n"
      "v_mfma_f32_16x16x32_bf16 v[252:255], a[84:87], v[236:239], v[252:255]\n"
      "v_mul_f32 v160, s41, v160\n"
      "v_mov_b32 v150, v160\n"
      "v_mfma_f32_16x16x32_bf16 v[252:255], a[120:123], v[164:167], v[252:255]\n"
      "s_nop 0\n"
      "v_permlane32_swap_b32 v160, v150\n"
      "v_mfma_f32_16x16x32_bf16 v[144:147], a[88:91], v[236:239], v[144:147]\n"
      "ds_read_b128 v[192:195], v114 offset:20480\n"
      "v_mov_b32_e64 v148, v160\n"
      "v_mfma_f32_16x16x32_bf16 v[144:147], a[124:127], v[164:167], v[144:147]\n"
      "global_load_lds_dwordx4 v116, s[52:53] offset:256\n"
      "v_mfma_f32_16x16x32_bf16 v[156:159], a[92:95], v[236:239], v[156:159]\n"
      "s_add_u32 m0, s77, 0\n"
      "v_permlane16_swap_b32 v160, v148\n"
      "v_mfma_f32_16x16x32_bf16 v[156:159], a[128:131], v[164:167], v[156:159]\n"
      "ds_read_b128 v[240:243], v115 offset:20480\n"
      "v_mul_f32 v232, v160, v232\n"
      "v_mfma_f32_16x16x32_bf16 v[140:143], v[236:239], a[96:99], v[140:143]\n"
      "v_mul_f32 v233, v160, v233\n"
      "v_cvt_pk_bf16_f32 v232, v232, v233\n"
      "v_mfma_f32_16x16x32_bf16 v[140:143], v[164:167], a[132:135], v[140:143]\n"
      "v_mul_f32 v234, v160, v234\n"
      "v_mul_f32 v235, v160, v235\n"
      "v_mfma_f32_16x16x32_bf16 v[224:227], v[236:239], a[100:103], v[224:227]\n"
      "v_mul_f32_e64 v152, v160, v152\n"
      "v_cvt_pk_bf16_f32 v233, v234, v235\n"
      "v_mfma_f32_16x16x32_bf16 v[224:227], v[164:167], a[136:139], v[224:227]\n"
      "s_waitcnt lgkmcnt(2)\n"
      "v_mul_f32 v153, v160, v153\n"
      "v_cvt_pk_bf16_f32 v234, v152, v153\n"
      "v_mfma_f32_16x16x32_bf16 v[136:139], v[236:239], a[104:107], v[136:139]\n"
      "v_mul_f32 v154, v160, v154\n"
      "v_mul_f32 v155, v160, v155\n"
      "v_mfma_f32_16x16x32_bf16 v[136:139], v[164:167], a[140:143], v[136:139]\n"
      "v_cvt_pk_bf16_f32 v235, v154, v155\n"
      "v_mul_f32_e64 v180, v160, v180\n"
      "v_mfma_f32_16x16x32_bf16 v[196:199], a[144:147], v[216:219], v[196:199]\n"
      "global_load_lds_dwordx4 v117, s[52:53]\n"
      "v_mfma_f32_16x16x32_bf16 v[196:199], a[180:183], v[248:251], v[196:199]\n"
      "v_mul_f32_e64 v181, v160, v181\n"
      "v_cvt_pk_bf16_f32 v180, v180, v181\n"
      "v_mfma_f32_16x16x32_bf16 v[228:231], a[148:151], v[216:219], v[228:231]\n"
      "s_waitcnt lgkmcnt(0)\n"
      "v_mul_f32 v182, v160, v182\n"
      "v_mul_f32_e64 v183, v160, v183\n"
      "v_mfma_f32_16x16x32_bf16 v[228:231], a[184:187], v[248:251], v[228:231]\n"
      "ds_write_b128 v126, v[232:235] offset:18432\n"
      "v_mfma_f32_16x16x32_bf16 v[172:175], a[152:155], v[216:219], v[172:175]\n"
      "v_cvt_pk_bf16_f32 v181, v182, v183\n"
      "v_mul_f32 v176, v148, v176\n"
      "v_mfma_f32_16x16x32_bf16 v[172:175], a[188:191], v[248:251], v[172:175]\n"
      "v_mul_f32 v177, v148, v177\n"
      "v_cvt_pk_bf16_f32 v176, v176, v177\n"
      "v_mfma_f32_16x16x32_bf16 v[252:255], a[156:159], v[216:219], v[252:255]\n"
      "v_mul_f32 v178, v148, v178\n"
      "v_mul_f32 v179, v148, v179\n"
      "v_mfma_f32_16x16x32_bf16 v[252:255], a[192:195], v[248:251], v[252:255]\n"
      "s_add_u32 m0, s78, 0\n"
      "v_mul_f32 v244, v148, v244\n"
      "v_cvt_pk_bf16_f32 v177, v178, v179\n"
      "v_mfma_f32_16x16x32_bf16 v[144:147], a[160:163], v[216:219], v[144:147]\n"
      "ds_read_b128 v[232:235], v114 offset:26624\n"
      "v_mul_f32_e64 v245, v148, v245\n"
      "v_mfma_f32_16x16x32_bf16 v[144:147], a[196:199], v[248:251], v[144:147]\n"
      "ds_read_b128 v[152:155], v115 offset:26624\n"
      "v_cvt_pk_bf16_f32 v178, v244, v245\n"
      "v_mfma_f32_16x16x32_bf16 v[156:159], a[164:167], v[216:219], v[156:159]\n"
      "v_mul_f32 v246, v148, v246\n"
      "v_mul_f32 v247, v148, v247\n"
      "v_mfma_f32_16x16x32_bf16 v[156:159], a[200:203], v[248:251], v[156:159]\n"
      "s_mov_b32 exec_hi, 0\n"
      "global_load_lds_dwordx4 v118, s[52:53]\n"
      "s_mov_b32 exec_hi, -1\n"
      "v_mfma_f32_16x16x32_bf16 v[140:143], v[216:219], a[168:171], v[140:143]\n"
      "s_add_u32 m0, s76, 110592\n"
      "v_cvt_pk_bf16_f32 v179, v246, v247\n"
      "v_mul_f32_e64 v200, v148, v200\n"
      "v_mfma_f32_16x16x32_bf16 v[140:143], v[248:251], a[204:207], v[140:143]\n"
      "ds_write_b128 v126, v[176:179] offset:20480\n"
      "v_mfma_f32_16x16x32_bf16 v[224:227], v[216:219], a[172:175], v[224:227]\n"
      "v_mul_f32_e64 v201, v148, v201\n"
      "v_cvt_pk_bf16_f32 v200, v200, v201\n"
      "v_mfma_f32_16x16x32_bf16 v[224:227], v[248:251], a[208:211], v[224:227]\n"
      "v_mul_f32 v203, v148, v203\n"
      "v_mul_f32_e64 v202, v148, v202\n"
      "v_mfma_f32_16x16x32_bf16 v[136:139], v[216:219], a[176:179], v[136:139]\n"
      "s_waitcnt lgkmcnt(1)\n"
      "v_cvt_pk_bf16_f32 v201, v202, v203\n"
      "v_mul_f32_e64 v184, v150, v184\n"
      "v_mfma_f32_16x16x32_bf16 v[136:139], v[248:251], a[212:215], v[136:139]\n"
      "ds_write2st64_b64 v127, v[180:181], v[200:201] offset0:36 offset1:40\n"
      "v_mfma_f32_16x16x32_bf16 v[200:203], a[0:3], v[192:195], 0\n"
      "global_load_lds_dwordx4 v119, s[56:57]\n"
      "v_mfma_f32_16x16x32_bf16 v[200:203], a[36:39], v[240:243], v[200:203]\n"
      "s_add_u32 m0, s79, 0\n"
      "v_mul_f32 v185, v150, v185\n"
      "v_cvt_pk_bf16_f32 v184, v184, v185\n"
      "v_mfma_f32_16x16x32_bf16 v[180:183], a[4:7], v[192:195], 0\n"
      "v_mul_f32 v187, v150, v187\n"
      "v_mul_f32 v186, v150, v186\n"
      "v_mfma_f32_16x16x32_bf16 v[180:183], a[40:43], v[240:243], v[180:183]\n"
      "s_sub_u32 s28, s27, 3\n"
      "v_mul_f32 v212, v150, v212\n"
      "v_cvt_pk_bf16_f32 v185, v186, v187\n"
      "v_mfma_f32_16x16x32_bf16 v[244:247], a[8:11], v[192:195], 0\n"
      "s_cselect_b64 s[54:55], s[12:13], s[22:23]\n"
      "v_mul_f32 v213, v150, v213\n"
      "v_cvt_pk_bf16_f32 v186, v212, v213\n"
      "v_mfma_f32_16x16x32_bf16 v[244:247], a[44:47], v[240:243], v[244:247]\n"
      "v_mul_f32 v214, v150, v214\n"
      "v_mul_f32 v215, v150, v215\n"
      "v_mfma_f32_16x16x32_bf16 v[248:251], a[12:15], v[192:195], 0\n"
      "ds_read_b128 v[168:171], v114 offset:32768\n"
      "v_cvt_pk_bf16_f32 v187, v214, v215\n"
      "v_mfma_f32_16x16x32_bf16 v[248:251], a[48:51], v[240:243], v[248:251]\n"
      "ds_write_b128 v126, v[184:187] offset:22528\n"
      "v_mfma_f32_16x16x32_bf16 v[176:179], a[16:19], v[192:195], 0\n"
      "global_load_lds_dword v122, s[58:59]\n"
      "v_mfma_f32_16x16x32_bf16 v[176:179], a[52:55], v[240:243], v[176:179]\n"
      "ds_read_b128 v[212:215], v115 offset:32768\n"
      "v_mul_f32 v188, v150, v188\n"
      "v_mfma_f32_16x16x32_bf16 v[184:187], a[20:23], v[192:195], 0\n"
      "s_max_i32 m0, s28, 0\n"
      "v_mul_f32_e64 v189, v150, v189\n"
      "v_cvt_pk_bf16_f32 v188, v188, v189\n"
      "v_mfma_f32_16x16x32_bf16 v[184:187], a[56:59], v[240:243], v[184:187]\n"
      "s_movrels_b32 s9, s80\n"
      "v_mul_f32 v190, v150, v190\n"
      "v_mul_f32 v191, v150, v191\n"
      "v_mfma_f32_16x16x32_bf16 v[148:151], v[192:195], a[24:27], 0\n"
      "s_lshr_b32 s8, s9, 8\n"
      "v_fma_f32 v236, v196, v196, s38\n"
      "v_cvt_pk_bf16_f32 v189, v190, v191\n"
      "v_mfma_f32_16x16x32_bf16 v[148:151], v[240:243], a[60:63], v[148:151]\n"
      "ds_write_b64 v127, v[188:189] offset:22528\n"
      "v_mfma_f32_16x16x32_bf16 v[188:191], v[192:195], a[28:31], 0\n"
      "s_and_b32 s9, s9, 0xff\n"
      "v_fma_f32 v238, v252, v252, s38\n"
      "v_fmac_f32_e64 v236, v197, v197\n"
      "v_mfma_f32_16x16x32_bf16 v[188:191], v[240:243], a[64:67], v[188:191]\n"
      "s_mul_i32 s28, s8, 1373568\n"
      "v_fmac_f32 v238, v253, v253\n"
      "v_fmac_f32 v236, v198, v198\n"
      "v_mfma_f32_16x16x32_bf16 v[204:207], v[192:195], a[32:35], 0\n"
      "s_mul_i32 s29, s9, 2688\n"
      "v_fmac_f32 v238, v254, v254\n"
      "v_fmac_f32 v236, v199, v199\n"
      "v_mfma_f32_16x16x32_bf16 v[204:207], v[240:243], a[68:71], v[204:207]\n"
      "s_waitcnt lgkmcnt(1)\n"
      "v_fmac_f32 v238, v255, v255\n"
      "v_fmac_f32 v236, v228, v228\n"
      "v_mfma_f32_16x16x32_bf16 v[200:203], a[72:75], v[232:235], v[200:203]\n"
      "s_add_u32 s28, s28, s29\n"
      "v_fmac_f32 v236, v229, v229\n"
      "v_fmac_f32 v238, v144, v144\n"
      "v_mfma_f32_16x16x32_bf16 v[200:203], a[108:111], v[152:155], v[200:203]\n"
      "s_cmp_eq_u32 s8, 72\n"
      "v_fmac_f32 v238, v145, v145\n"
      "v_fmac_f32 v236, v230, v230\n"
      "v_mfma_f32_16x16x32_bf16 v[180:183], a[76:79], v[232:235], v[180:183]\n"
      "s_cselect_b32 s8, 0, 100270464\n"
      "v_fmac_f32 v238, v146, v146\n"
      "v_fmac_f32 v236, v231, v231\n"
      "v_mfma_f32_16x16x32_bf16 v[180:183], a[112:115], v[152:155], v[180:183]\n"
      "s_cmp_eq_u32 s9, 72\n"
      "v_fmac_f32 v236, v172, v172\n"
      "v_fmac_f32 v238, v147, v147\n"
      "v_mfma_f32_16x16x32_bf16 v[244:247], a[80:83], v[232:235], v[244:247]\n"
      "ds_read_b128 v[240:243], v114 offset:22528\n"
      "v_fmac_f32_e64 v238, v156, v156\n"
      "v_mfma_f32_16x16x32_bf16 v[244:247], a[116:119], v[152:155], v[244:247]\n"
      "s_cselect_b32 s9, 0, 196224\n"
      "v_fmac_f32 v236, v173, v173\n"
      "v_fmac_f32 v238, v157, v157\n"
      "v_mfma_f32_16x16x32_bf16 v[248:251], a[84:87], v[232:235], v[248:251]\n"
      "ds_read_b128 v[192:195], v115 offset:22528\n"
      "v_fmac_f32 v236, v174, v174\n"
      "v_mfma_f32_16x16x32_bf16 v[248:251], a[120:123], v[152:155], v[248:251]\n"
      "s_add_u32 s10, s8, s9\n"
      "v_fmac_f32 v236, v175, v175\n"
      "v_fmac_f32 v238, v158, v158\n"
      "v_mfma_f32_16x16x32_bf16 v[176:179], a[88:91], v[232:235], v[176:179]\n"
      "s_add_u32 s28, s28, s10\n"
      "v_cvt_pk_bf16_f32 v140, v140, v141\n"
      "v_fmac_f32 v238, v159, v159\n"
      "v_mfma_f32_16x16x32_bf16 v[176:179], a[124:127], v[152:155], v[176:179]\n"
      "s_add_u32 s54, s54, s28\n"
      "v_permlane16_swap_b32 v236, v238\n"
      "v_mfma_f32_16x16x32_bf16 v[184:187], a[92:95], v[232:235], v[184:187]\n"
      "s_addc_u32 s55, s55, 0\n"
      "v_add_f32 v236, v236, v238\n"
      "v_mov_b32 v238, v236\n"
      "v_mfma_f32_16x16x32_bf16 v[184:187], a[128:131], v[152:155], v[184:187]\n"
      "s_cmp_lg_u32 s10, s11\n"
      "v_permlane32_swap_b32 v236, v238\n"
      "v_add_f32 v236, v236, v238\n"
      "v_mfma_f32_16x16x32_bf16 v[148:151], v[232:235], a[96:99], v[148:151]\n"
      "s_cbranch_scc1 L_edge_17_%=\n"
      "L_eb_18_%=:\n"
      "v_mfma_f32_16x16x32_bf16 v[148:151], v[152:155], a[132:135], v[148:151]\n"
      "v_rsq_f32 v236, v236\n"
      "v_mfma_f32_16x16x32_bf16 v[188:191], v[232:235], a[100:103], v[188:191]\n"
      "s_mov_b32 s5, s4\n"
      "v_mov_b32 v238, v236\n"
      "v_cvt_pk_bf16_f32 v141, v142, v143\n"
      "v_mfma_f32_16x16x32_bf16 v[188:191], v[152:155], a[136:139], v[188:191]\n"
      "s_add_u32 s4, s27, 1\n"
      "v_permlane16_swap_b32 v236, v238\n"
      "v_mfma_f32_16x16x32_bf16 v[204:207], v[232:235], a[104:107], v[204:207]\n"
      "s_waitcnt lgkmcnt(0)\n"
      "v_mul_f32 v252, v238, v252\n"
      "v_mul_f32_e64 v253, v238, v253\n"
      "v_mfma_f32_16x16x32_bf16 v[204:207], v[152:155], a[140:143], v[204:207]\n"
      "s_lshl_b32 s4, s4, 30\n"
      "v_cvt_pk_bf16_f32 v252, v252, v253\n"
      "v_mul_f32 v254, v238, v254\n"
      "v_mfma_f32_16x16x32_bf16 v[200:203], a[144:147], v[168:171], v[200:203]\n"
      "v_mul_f32 v255, v238, v255\n"
      "v_cvt_pk_bf16_f32 v253, v254, v255\n"
      "v_mfma_f32_16x16x32_bf16 v[200:203], a[180:183], v[212:215], v[200:203]\n"
      "ds_read_b128 v[232:235], v119\n"
      "v_mul_f32 v144, v238, v144\n"
      "v_mfma_f32_16x16x32_bf16 v[180:183], a[148:151], v[168:171], v[180:183]\n"
      "v_mul_f32 v145, v238, v145\n"
      "v_cvt_pk_bf16_f32 v254, v144, v145\n"
      "v_mfma_f32_16x16x32_bf16 v[180:183], a[184:187], v[212:215], v[180:183]\n"
      "v_mul_f32 v146, v238, v146\n"
      "v_mul_f32 v147, v238, v147\n"
      "v_mfma_f32_16x16x32_bf16 v[244:247], a[152:155], v[168:171], v[244:247]\n"
      "s_and_b32 s4, s4, 0x80000000\n"
      "v_cvt_pk_bf16_f32 v255, v146, v147\n"
      "v_mul_f32 v156, v238, v156\n"
      "v_mfma_f32_16x16x32_bf16 v[244:247], a[188:191], v[212:215], v[244:247]\n"
      "v_mul_f32_e64 v157, v238, v157\n"
      "v_cvt_pk_bf16_f32 v34, v156, v157\n"
      "v_mfma_f32_16x16x32_bf16 v[248:251], a[156:159], v[168:171], v[248:251]\n"
      "ds_read_b128 v[164:167], v114 offset:28672\n"
      "v_mul_f32 v158, v238, v158\n"
      "v_mfma_f32_16x16x32_bf16 v[248:251], a[192:195], v[212:215], v[248:251]\n"
      "v_mul_f32 v159, v238, v159\n"
      "v_cvt_pk_bf16_f32 v35, v158, v159\n"
      "v_mfma_f32_16x16x32_bf16 v[176:179], a[160:163], v[168:171], v[176:179]\n"
      "ds_read_b128 v[152:155], v115 offset:28672\n"
      "v_mul_f32 v236, s36, v236\n"
      "v_mfma_f32_16x16x32_bf16 v[176:179], a[196:199], v[212:215], v[176:179]\n"
      "s_waitcnt lgkmcnt(2)\n"
      "v_mul_f32 v196, v236, v196\n"
      "v_mul_f32 v197, v236, v197\n"
      "v_mfma_f32_16x16x32_bf16 v[184:187], a[164:167], v[168:171], v[184:187]\n"
      "global_store_dwordx4 v116, v[232:235], s[54:55] sc1\n"
      "v_mfma_f32_16x16x32_bf16 v[184:187], a[200:203], v[212:215], v[184:187]\n"
      "v_mul_f32 v198, v236, v198\n"
      "v_cvt_pk_bf16_f32 v196, v196, v197\n"
      "v_mfma_f32_16x16x32_bf16 v[148:151], v[168:171], a[168:171], v[148:151]\n"
      "v_mul_f32 v199, v236, v199\n"
      "v_cvt_pk_bf16_f32 v197, v198, v199\n"
      "v_mfma_f32_16x16x32_bf16 v[148:151], v[212:215], a[204:207], v[148:151]\n"
      "v_mul_f32 v229, v236, v229\n"
      "v_mul_f32 v228, v236, v228\n"
      "v_mfma_f32_16x16x32_bf16 v[188:191], v[168:171], a[172:175], v[188:191]\n"
      "v_cvt_pk_bf16_f32 v198, v228, v229\n"
      "v_mul_f32 v230, v236, v230\n"
      "v_mfma_f32_16x16x32_bf16 v[188:191], v[212:215], a[208:211], v[188:191]\n"
      "v_mul_f32 v231, v236, v231\n"
      "v_cvt_pk_bf16_f32 v199, v230, v231\n"
      "v_mfma_f32_16x16x32_bf16 v[204:207], v[168:171], a[176:179], v[204:207]\n"
      "v_mul_f32 v172, v236, v172\n"
      "v_mul_f32 v173, v236, v173\n"
      "v_mfma_f32_16x16x32_bf16 v[204:207], v[212:215], a[212:215], v[204:207]\n"
      "v_mul_f32_e64 v174, v236, v174\n"
      "v_cvt_pk_bf16_f32 v32, v172, v173\n"
      "v_mfma_f32_16x16x32_bf16 v[168:171], a[0:3], v[240:243], 0\n"
      "v_mul_f32_e64 v175, v236, v175\n"
      "v_cvt_pk_bf16_f32 v33, v174, v175\n"
      "v_mfma_f32_16x16x32_bf16 v[168:171], a[36:39], v[192:195], v[168:171]\n"
      "s_nop 0\n"
      "v_cvt_pk_bf16_f32 v143, v150, v151\n"
      "v_cvt_pk_bf16_f32 v142, v148, v149\n"
      "v_mfma_f32_16x16x32_bf16 v[212:215], a[4:7], v[240:243], 0\n"
      "s_waitcnt lgkmcnt(0)\n"
      "v_cvt_pk_bf16_f32 v224, v224, v225\n"
      "v_cvt_pk_bf16_f32 v225, v226, v227\n"
      "v_mfma_f32_16x16x32_bf16 v[212:215], a[40:43], v[192:195], v[212:215]\n"
      "ds_read_b128 v[232:235], v119 offset:6144\n"
      "v_cvt_pk_bf16_f32 v226, v188, v189\n"
      "v_mfma_f32_16x16x32_bf16 v[148:151], a[8:11], v[240:243], 0\n"
      "v_cvt_pk_bf16_f32 v227, v190, v191\n"
      "v_cvt_pk_bf16_f32 v136, v136, v137\n"
      "v_mfma_f32_16x16x32_bf16 v[148:151], a[44:47], v[192:195], v[148:151]\n"
      "v_cvt_pk_bf16_f32 v137, v138, v139\n"
      "v_cvt_pk_bf16_f32 v138, v204, v205\n"
      "v_mfma_f32_16x16x32_bf16 v[188:191], a[12:15], v[240:243], 0\n"
      "v_fma_f32 v145, v200, v200, s38\n"
      "v_cvt_pk_bf16_f32 v139, v206, v207\n"
      "v_mfma_f32_16x16x32_bf16 v[188:191], a[48:51], v[192:195], v[188:191]\n"
      "v_fmac_f32_e64 v145, v201, v201\n"
      "v_fma_f32 v146, v248, v248, s38\n"
      "v_mfma_f32_16x16x32_bf16 v[228:231], a[16:19], v[240:243], 0\n"
      "v_fmac_f32 v145, v202, v202\n"
      "v_fmac_f32 v146, v249, v249\n"
      "v_mfma_f32_16x16x32_bf16 v[228:231], a[52:55], v[192:195], v[228:231]\n"
      "v_fmac_f32 v145, v203, v203\n"
      "v_fmac_f32 v146, v250, v250\n"
      "v_mfma_f32_16x16x32_bf16 v[204:207], a[20:23], v[240:243], 0\n"
      "v_fmac_f32 v146, v251, v251\n"
      "v_fmac_f32 v145, v180, v180\n"
      "v_mfma_f32_16x16x32_bf16 v[204:207], a[56:59], v[192:195], v[204:207]\n"
      "s_waitcnt lgkmcnt(0)\n"
      "v_fmac_f32 v146, v176, v176\n"
      "v_fmac_f32_e64 v145, v181, v181\n"
      "v_mfma_f32_16x16x32_bf16 v[172:175], v[240:243], a[24:27], 0\n"
      "global_store_dwordx4 v116, v[232:235], s[54:55] offset:128 sc1\n"
      "v_mfma_f32_16x16x32_bf16 v[172:175], v[192:195], a[60:63], v[172:175]\n"
      "ds_read_b128 v[156:159], v114 offset:34816\n"
      "v_fmac_f32 v146, v177, v177\n"
      "v_mfma_f32_16x16x32_bf16 v[236:239], v[240:243], a[28:31], 0\n"
      "v_fmac_f32 v146, v178, v178\n"
      "v_fmac_f32 v146, v179, v179\n"
      "v_mfma_f32_16x16x32_bf16 v[236:239], v[192:195], a[64:67], v[236:239]\n"
      "v_fmac_f32 v145, v182, v182\n"
      "v_fmac_f32 v145, v183, v183\n"
      "v_mfma_f32_16x16x32_bf16 v[220:223], v[240:243], a[32:35], 0\n"
      "v_fmac_f32 v146, v184, v184\n"
      "v_fmac_f32 v145, v244, v244\n"
      "v_mfma_f32_16x16x32_bf16 v[220:223], v[192:195], a[68:71], v[220:223]\n"
      "v_fmac_f32 v145, v245, v245\n"
      "v_fmac_f32_e64 v145, v246, v246\n"
      "v_mfma_f32_16x16x32_bf16 v[168:171], a[72:75], v[164:167], v[168:171]\n"
      "ds_read_b128 v[232:235], v115 offset:34816\n"
      "v_fmac_f32 v146, v185, v185\n"
      "v_mfma_f32_16x16x32_bf16 v[168:171], a[108:111], v[152:155], v[168:171]\n"
      "v_fmac_f32 v146, v186, v186\n"
      "v_mfma_f32_16x16x32_bf16 v[212:215], a[76:79], v[164:167], v[212:215]\n"
      "v_fmac_f32 v146, v187, v187\n"
      "v_fmac_f32 v145, v247, v247\n"
      "v_mfma_f32_16x16x32_bf16 v[212:215], a[112:115], v[152:155], v[212:215]\n"
      "s_nop 0\n"
      "v_permlane16_swap_b32 v145, v146\n"
      "v_mfma_f32_16x16x32_bf16 v[148:151], a[80:83], v[164:167], v[148:151]\n"
      "v_add_f32 v145, v145, v146\n"
      "v_mov_b32 v146, v145\n"
      "v_mfma_f32_16x16x32_bf16 v[148:151], a[116:119], v[152:155], v[148:151]\n"
      "s_nop 0\n"
      "v_permlane32_swap_b32 v145, v146\n"
      "v_mfma_f32_16x16x32_bf16 v[188:191], a[84:87], v[164:167], v[188:191]\n"
      "ds_read_b128 v[240:243], v132 offset:1200\n"
      "v_add_f32 v145, v145, v146\n"
      "v_mfma_f32_16x16x32_bf16 v[188:191], a[120:123], v[152:155], v[188:191]\n"
      "v_rsq_f32 v145, v145\n"
      "v_mfma_f32_16x16x32_bf16 v[228:231], a[88:91], v[164:167], v[228:231]\n"
      "v_mov_b32 v146, v145\n"
      "v_mfma_f32_16x16x32_bf16 v[228:231], a[124:127], v[152:155], v[228:231]\n"
      "s_nop 0\n"
      "v_permlane16_swap_b32 v145, v146\n"
      "v_mfma_f32_16x16x32_bf16 v[204:207], a[92:95], v[164:167], v[204:207]\n"
      "v_mul_f32 v250, v146, v250\n"
      "v_mul_f32 v248, v146, v248\n"
      "v_mfma_f32_16x16x32_bf16 v[204:207], a[128:131], v[152:155], v[204:207]\n"
      "v_mul_f32 v251, v146, v251\n"
      "v_mul_f32 v249, v146, v249\n"
      "v_mfma_f32_16x16x32_bf16 v[172:175], v[164:167], a[96:99], v[172:175]\n"
      "s_waitcnt lgkmcnt(1)\n"
      "v_mul_f32_e64 v177, v146, v177\n"
      "v_cvt_pk_bf16_f32 v248, v248, v249\n"
      "v_mfma_f32_16x16x32_bf16 v[172:175], v[152:155], a[132:135], v[172:175]\n"
      "ds_read_b64 v[192:193], v132 offset:1192\n"
      "v_cvt_pk_bf16_f32 v249, v250, v251\n"
      "v_mfma_f32_16x16x32_bf16 v[236:239], v[164:167], a[100:103], v[236:239]\n"
      "ds_read_u16 v52, v134 offset:1168\n"
      "v_mul_f32_e64 v176, v146, v176\n"
      "v_mfma_f32_16x16x32_bf16 v[236:239], v[152:155], a[136:139], v[236:239]\n"
      "ds_read_u16 v56, v134 offset:1200\n"
      "v_mul_f32_e64 v178, v146, v178\n"
      "v_mfma_f32_16x16x32_bf16 v[220:223], v[164:167], a[104:107], v[220:223]\n"
      "ds_read_b128 v[216:219], v133 offset:1168\n"
      "v_cvt_pk_bf16_f32 v250, v176, v177\n"
      "v_mfma_f32_16x16x32_bf16 v[220:223], v[152:155], a[140:143], v[220:223]\n"
      "ds_read_b64 v[194:195], v133 offset:1184\n"
      "v_mfma_f32_16x16x32_bf16 v[168:171], a[144:147], v[156:159], v[168:171]\n"
      "ds_read_u16 v60, v134 offset:1232\n"
      "v_mul_f32_e64 v179, v146, v179\n"
      "v_mfma_f32_16x16x32_bf16 v[168:171], a[180:183], v[232:235], v[168:171]\n"
      "ds_read_b128 v[164:167], v123\n"
      "v_cvt_pk_bf16_f32 v251, v178, v179\n"
      "v_mfma_f32_16x16x32_bf16 v[212:215], a[148:151], v[156:159], v[212:215]\n"
      "v_mul_f32 v185, v146, v185\n"
      "v_mul_f32 v184, v146, v184\n"
      "v_mfma_f32_16x16x32_bf16 v[212:215], a[184:187], v[232:235], v[212:215]\n"
      "v_mul_f32_e64 v186, v146, v186\n"
      "v_cvt_pk_bf16_f32 v40, v184, v185\n"
      "v_mfma_f32_16x16x32_bf16 v[148:151], a[152:155], v[156:159], v[148:151]\n"
      "ds_read_b128 v[176:179], v123 offset:3072\n"
      "v_mul_f32 v187, v146, v187\n"
      "v_mfma_f32_16x16x32_bf16 v[148:151], a[188:191], v[232:235], v[148:151]\n"
      "v_cvt_pk_bf16_f32 v41, v186, v187\n"
      "v_mul_f32 v145, s36, v145\n"
      "v_mfma_f32_16x16x32_bf16 v[188:191], a[156:159], v[156:159], v[188:191]\n"
      "v_mul_f32 v201, v145, v201\n"
      "v_mul_f32 v200, v145, v200\n"
      "v_mfma_f32_16x16x32_bf16 v[188:191], a[192:195], v[232:235], v[188:191]\n"
      "v_cvt_pk_bf16_f32 v200, v200, v201\n"
      "v_mul_f32 v202, v145, v202\n"
      "v_mfma_f32_16x16x32_bf16 v[228:231], a[160:163], v[156:159], v[228:231]\n"
      "v_mul_f32 v203, v145, v203\n"
      "v_cvt_pk_bf16_f32 v201, v202, v203\n"
      "v_mfma_f32_16x16x32_bf16 v[228:231], a[196:199], v[232:235], v[228:231]\n"
      "v_mul_f32 v181, v145, v181\n"
      "v_mul_f32 v180, v145, v180\n"
      "v_mfma_f32_16x16x32_bf16 v[204:207], a[164:167], v[156:159], v[204:207]\n"
      "ds_read_b128 v[160:163], v123 offset:9216\n"
      "v_cvt_pk_bf16_f32 v202, v180, v181\n"
      "v_mfma_f32_16x16x32_bf16 v[204:207], a[200:203], v[232:235], v[204:207]\n"
      "ds_read_b128 v[184:187], v123 offset:6144\n"
      "v_mul_f32 v244, v145, v244\n"
      "v_mfma_f32_16x16x32_bf16 v[172:175], v[156:159], a[168:171], v[172:175]\n"
      "v_mul_f32 v183, v145, v183\n"
      "v_mul_f32_e64 v182, v145, v182\n"
      "v_mfma_f32_16x16x32_bf16 v[172:175], v[232:235], a[204:207], v[172:175]\n"
      "v_mul_f32_e64 v245, v145, v245\n"
      "v_cvt_pk_bf16_f32 v203, v182, v183\n"
      "v_mfma_f32_16x16x32_bf16 v[236:239], v[156:159], a[172:175], v[236:239]\n"
      "v_mul_f32 v246, v145, v246\n"
      "v_mfma_f32_16x16x32_bf16 v[236:239], v[232:235], a[208:211], v[236:239]\n"
      "s_or_b64 s[34:35], s[48:49], s[50:51]\n"
      "v_cvt_pk_bf16_f32 v38, v244, v245\n"
      "v_mul_f32 v247, v145, v247\n"
      "v_mfma_f32_16x16x32_bf16 v[220:223], v[156:159], a[176:179], v[220:223]\n"
      "s_waitcnt lgkmcnt(2)\n"
      "v_fma_f32 v156, v188, v188, s38\n"
      "v_mfma_f32_16x16x32_bf16 v[220:223], v[232:235], a[212:215], v[220:223]\n"
      "v_fma_f32 v158, v168, v168, s38\n"
      "v_fmac_f32 v158, v169, v169\n"
      "v_mfma_f32_16x16x32_bf16 v[180:183], a[216:219], v[164:167], 0\n"
      "v_fmac_f32 v156, v189, v189\n"
      "v_cvt_pk_bf16_f32 v39, v246, v247\n"
      "v_mfma_f32_16x16x32_bf16 v[180:183], a[228:231], v[176:179], v[180:183]\n"
      "v_fmac_f32 v156, v190, v190\n"
      "v_fmac_f32 v158, v170, v170\n"
      "v_mfma_f32_16x16x32_bf16 v[232:235], a[220:223], v[164:167], 0\n"
      "v_fmac_f32 v156, v191, v191\n"
      "v_fmac_f32 v158, v171, v171\n"
      "v_mfma_f32_16x16x32_bf16 v[232:235], a[232:235], v[176:179], v[232:235]\n"
      "v_fmac_f32 v158, v212, v212\n"
      "v_fmac_f32 v156, v228, v228\n"
      "v_mfma_f32_16x16x32_bf16 v[152:155], a[224:227], v[164:167], 0\n"
      "v_fmac_f32 v156, v229, v229\n"
      "v_fmac_f32 v158, v213, v213\n"
      "v_mfma_f32_16x16x32_bf16 v[152:155], a[236:239], v[176:179], v[152:155]\n"
      "s_waitcnt lgkmcnt(0)\n"
      "v_fmac_f32 v158, v214, v214\n"
      "v_fmac_f32_e64 v156, v230, v230\n"
      "v_mfma_f32_16x16x32_bf16 v[180:183], a[240:243], v[184:187], v[180:183]\n"
      "ds_read_b128 v[244:247], v123 offset:12288\n"
      "v_fmac_f32 v158, v215, v215\n"
      "v_mfma_f32_16x16x32_bf16 v[180:183], a[252:255], v[160:163], v[180:183]\n"
      "ds_read_b128 v[176:179], v123 offset:15360\n"
      "v_fmac_f32 v156, v231, v231\n"
      "v_mfma_f32_16x16x32_bf16 v[232:235], a[244:247], v[184:187], v[232:235]\n"
      "v_fmac_f32 v156, v204, v204\n"
      "v_fmac_f32 v158, v148, v148\n"
      "v_mfma_f32_16x16x32_bf16 v[232:235], v[0:3], v[160:163], v[232:235]\n"
      "v_fmac_f32 v156, v205, v205\n"
      "v_fmac_f32 v158, v149, v149\n"
      "v_mfma_f32_16x16x32_bf16 v[152:155], a[248:251], v[184:187], v[152:155]\n"
      "v_fmac_f32 v156, v206, v206\n"
      "v_fmac_f32 v158, v150, v150\n"
      "v_mfma_f32_16x16x32_bf16 v[152:155], v[4:7], v[160:163], v[152:155]\n"
      "v_fmac_f32 v156, v207, v207\n"
      "v_fmac_f32 v158, v151, v151\n"
      "v_mfma_f32_16x16x32_bf16 v[184:187], v[252:255], v[196:199], v[78:81]\n"
      "s_nop 0\n"
      "v_permlane16_swap_b32 v158, v156\n"
      "v_mfma_f32_16x16x32_bf16 v[184:187], v[34:37], v[32:35], v[184:187]\n"
      "v_add_f32 v158, v158, v156\n"
      "v_mov_b32 v156, v158\n"
      "v_add_u32 v132, 2336, v132\n"
      "v_add_u32 v133, 2336, v133\n"
      "v_permlane32_swap_b32 v158, v156\n"
      "v_add_f32 v158, v158, v156\n"
      "v_rsq_f32 v158, v158\n"
      "s_waitcnt lgkmcnt(0)\n"
      "v_mfma_f32_16x16x32_bf16 v[180:183], v[8:11], v[244:247], v[180:183]\n"
      "v_add_u32 v134, 2336, v134\n"
      "v_mov_b32 v156, v158\n"
      "v_mfma_f32_16x16x32_bf16 v[180:183], v[20:23], v[176:179], v[180:183]\n"
      "s_nop 0\n"
      "v_permlane16_swap_b32 v158, v156\n"
      "v_mul_f32 v190, v156, v190\n"
      "v_mfma_f32_16x16x32_bf16 v[232:235], v[12:15], v[244:247], v[232:235]\n"
      "v_mul_f32 v189, v156, v189\n"
      "v_mul_f32 v188, v156, v188\n"
      "v_mfma_f32_16x16x32_bf16 v[232:235], v[24:27], v[176:179], v[232:235]\n"
      "v_mul_f32 v207, v156, v207\n"
      "v_cvt_pk_bf16_f32 v188, v188, v189\n"
      "v_mfma_f32_16x16x32_bf16 v[152:155], v[16:19], v[244:247], v[152:155]\n"
      "v_mul_f32 v191, v156, v191\n"
      "v_cvt_pk_bf16_f32 v189, v190, v191\n"
      "v_mfma_f32_16x16x32_bf16 v[152:155], v[28:31], v[176:179], v[152:155]\n"
      "v_mul_f32 v231, v156, v231\n"
      "v_mul_f32 v229, v156, v229\n"
      "v_mul_f32_e64 v228, v156, v228\n"
      "v_mfma_f32_16x16x32_bf16 v[244:247], v[248:251], v[196:199], v[82:85]\n"
      "v_cvt_pk_bf16_f32 v190, v228, v229\n"
      "v_mul_f32 v230, v156, v230\n"
      "v_mfma_f32_16x16x32_bf16 v[244:247], v[40:43], v[32:35], v[244:247]\n"
      "v_cvt_pk_bf16_f32 v191, v230, v231\n"
      "v_mul_f32 v158, s36, v158\n"
      "v_mfma_f32_16x16x32_bf16 v[228:231], v[240:243], v[196:199], v[70:73]\n"
      "v_mul_f32 v204, v156, v204\n"
      "v_mul_f32 v205, v156, v205\n"
      "v_mfma_f32_16x16x32_bf16 v[228:231], v[216:219], v[252:255], v[228:231]\n"
      "v_cvt_pk_bf16_f32 v46, v204, v205\n"
      "v_mul_f32 v206, v156, v206\n"
      "v_mfma_f32_16x16x32_bf16 v[228:231], v[192:195], v[32:35], v[228:231]\n"
      "v_mul_f32 v168, v158, v168\n"
      "v_cvt_pk_bf16_f32 v47, v206, v207\n"
      "v_mfma_f32_16x16x32_bf16 v[176:179], v[188:191], v[196:199], v[86:89]\n"
      "s_waitcnt vmcnt(2)\n"
      "v_mul_f32 v169, v158, v169\n"
      "v_cvt_pk_bf16_f32 v168, v168, v169\n"
      "v_mfma_f32_16x16x32_bf16 v[176:179], v[46:49], v[32:35], v[176:179]\n"
      "ds_read_b128 v[204:207], v119 offset:36864\n"
      "ds_read_b32 v231, v122\n"
      "v_mul_f32 v150, v158, v150\n"
      "v_cvt_pk_bf16_f32 v51, v174, v175\n"
      "v_fma_f32 v230, v180, v180, s39\n"
      "v_mul_f32 v148, v158, v148\n"
      "v_fmac_f32 v230, v181, v181\n"
      "v_mul_f32 v170, v158, v170\n"
      "v_mul_f32 v171, v158, v171\n"
      "v_cvt_pk_bf16_f32 v59, v222, v223\n"
      "v_cvt_pk_bf16_f32 v54, v236, v237\n"
      "v_mul_f32 v212, v158, v212\n"
      "v_mul_f32 v149, v158, v149\n"
      "v_fmac_f32 v230, v182, v182\n"
      "v_mul_f32 v215, v158, v215\n"
      "v_fmac_f32 v230, v183, v183\n"
      "v_fmac_f32 v230, v232, v232\n"
      "v_fmac_f32 v230, v233, v233\n"
      "v_fmac_f32 v230, v234, v234\n"
      "v_mul_f32 v214, v158, v214\n"
      "v_cvt_pk_bf16_f32 v169, v170, v171\n"
      "v_fmac_f32 v230, v235, v235\n"
      "v_mul_f32 v213, v158, v213\n"
      "v_mul_f32 v151, v158, v151\n"
      "v_cvt_pk_bf16_f32 v58, v220, v221\n"
      "v_fmac_f32 v230, v152, v152\n"
      "v_cvt_pk_bf16_f32 v171, v214, v215\n"
      "v_cvt_pk_bf16_f32 v45, v150, v151\n"
      "v_cvt_pk_bf16_f32 v170, v212, v213\n"
      "v_fmac_f32 v230, v153, v153\n"
      "v_fmac_f32 v230, v154, v154\n"
      "v_cvt_pk_bf16_f32 v55, v238, v239\n"
      "v_fmac_f32 v230, v155, v155\n"
      "s_waitcnt lgkmcnt(0)\n"
      "v_cvt_pk_bf16_f32 v44, v148, v149\n"
      "v_cvt_pk_bf16_f32 v50, v172, v173\n"
      "v_or3_b32 v204, v204, v205, v206\n"
      "v_and_b32_e64 v204, s40, v204\n"
      "v_bitop3_b32 v231, v231, v207, s40 bitop3:0xa8\n"
      "v_or3_b32 v204, v204, v231, s4\n"
      "ds_write_b32 v129, v204 offset:2048\n"
      "s_cbranch_scc1 L_gen_19_%=\n"
      "L_fast_22_%=:\n"
      "v_mfma_f32_16x16x32_bf16 v[148:151], v[252:255], v[200:203], v[90:93]\n"
      "ds_read_b128 v[204:207], v119 offset:12288\n"
      "v_exp_f32 v246, v246\n"
      "v_mfma_f32_16x16x32_bf16 v[148:151], v[34:37], v[38:41], v[148:151]\n"
      "ds_read_b128 v[156:159], v123 offset:1024\n"
      "v_exp_f32 v184, v184\n"
      "v_mfma_f32_16x16x32_bf16 v[208:211], v[248:251], v[200:203], v[94:97]\n"
      "ds_read_b128 v[212:215], v123 offset:4096\n"
      "v_exp_f32 v176, v176\n"
      "v_exp_f32 v228, v228\n"
      "v_exp_f32 v187, v187\n"
      "v_mfma_f32_16x16x32_bf16 v[208:211], v[40:43], v[38:41], v[208:211]\n"
      "ds_read_b128 v[196:199], v128 offset:2048\n"
      "v_exp_f32 v177, v177\n"
      "v_exp_f32 v185, v185\n"
      "v_cvt_pk_bf16_f32 v62, v176, v177\n"
      "v_mfma_f32_16x16x32_bf16 v[220:223], v[240:243], v[200:203], v[72:75]\n"
      "ds_read_b128 v[172:175], v123 offset:7168\n"
      "v_cvt_pk_bf16_f32 v184, v184, v185\n"
      "v_mfma_f32_16x16x32_bf16 v[220:223], v[216:219], v[248:251], v[220:223]\n"
      "v_exp_f32_e64 v186, v186\n"
      "v_mfma_f32_16x16x32_bf16 v[220:223], v[192:195], v[38:41], v[220:223]\n"
      "ds_read_b128 v[236:239], v123 offset:10240\n"
      "v_cvt_pk_bf16_f32 v64, v228, 0\n"
      "v_exp_f32_e64 v178, v178\n"
      "v_cvt_pk_bf16_f32 v185, v186, v187\n"
      "v_mfma_f32_16x16x32_bf16 v[164:167], v[188:191], v[200:203], v[98:101]\n"
      "v_exp_f32 v179, v179\n"
      "v_mfma_f32_16x16x32_bf16 v[164:167], v[46:49], v[38:41], v[164:167]\n"
      "s_waitcnt lgkmcnt(3)\n"
      "v_exp_f32_e64 v247, v247\n"
      "v_mfma_f32_16x16x32_bf16 v[200:203], a[216:219], v[156:159], 0\n"
      "v_exp_f32 v244, v244\n"
      "v_mfma_f32_16x16x32_bf16 v[200:203], a[228:231], v[212:215], v[200:203]\n"
      "global_store_dwordx4 v116, v[204:207], s[54:55] offset:256 sc1\n"
      "v_exp_f32 v245, v245\n"
      "v_cvt_pk_bf16_f32 v63, v178, v179\n"
      "v_cvt_pk_bf16_f32 v186, v244, v245\n"
      "v_cvt_pk_bf16_f32 v187, v246, v247\n"
      "v_mfma_f32_16x16x32_bf16 v[244:247], a[220:223], v[156:159], 0\n"
      "s_waitcnt lgkmcnt(1)\n"
      "v_bitop3_b32 v196, v196, v197, s4 bitop3:0x7e\n"
      "v_bitop3_b32 v198, v198, v199, s4 bitop3:0x7e\n"
      "v_mfma_f32_16x16x32_bf16 v[244:247], a[232:235], v[212:215], v[244:247]\n"
      "s_waitcnt lgkmcnt(0)\n"
      "v_or_b32_e64 v196, v196, v198\n"
      "v_cmp_ne_u32 vcc, 0, v196\n"
      "v_mfma_f32_16x16x32_bf16 v[176:179], a[224:227], v[156:159], 0\n"
      "s_mov_b64 s[48:49], vcc\n"
      "ds_read_b128 v[204:207], v123 offset:13312\n"
      "v_mfma_f32_16x16x32_bf16 v[176:179], a[236:239], v[212:215], v[176:179]\n"
      "ds_read_b128 v[212:215], v123 offset:16384\n"
      "v_exp_f32 v149, v149\n"
      "v_mfma_f32_16x16x32_bf16 v[200:203], a[240:243], v[172:175], v[200:203]\n"
      "s_cbranch_vccnz L_flg_23_%=\n"
      "L_fb_24_%=:\n"
      "v_exp_f32 v208, v208\n"
      "v_mfma_f32_16x16x32_bf16 v[200:203], a[252:255], v[236:239], v[200:203]\n"
      "ds_read_b128 v[156:159], v120\n"
      "v_exp_f32 v148, v148\n"
      "v_mfma_f32_16x16x32_bf16 v[244:247], a[244:247], v[172:175], v[244:247]\n"
      "v_exp_f32 v150, v150\n"
      "v_mfma_f32_16x16x32_bf16 v[244:247], v[0:3], v[236:239], v[244:247]\n"
      "v_exp_f32 v151, v151\n"
      "v_exp_f32_e64 v209, v209\n"
      "v_cvt_pk_bf16_f32 v148, v148, v149\n"
      "v_mfma_f32_16x16x32_bf16 v[176:179], a[248:251], v[172:175], v[176:179]\n"
      "ds_read_b128 v[172:175], v123 offset:2048\n"
      "v_cvt_pk_bf16_f32 v149, v150, v151\n"
      "v_mfma_f32_16x16x32_bf16 v[176:179], v[4:7], v[236:239], v[176:179]\n"
      "s_waitcnt lgkmcnt(2)\n"
      "v_cvt_pk_bf16_f32 v150, v208, v209\n"
      "v_mfma_f32_16x16x32_bf16 v[200:203], v[8:11], v[204:207], v[200:203]\n"
      "v_exp_f32 v210, v210\n"
      "v_mfma_f32_16x16x32_bf16 v[200:203], v[20:23], v[212:215], v[200:203]\n"
      "ds_read_b128 v[196:199], v123 offset:5120\n"
      "v_exp_f32 v211, v211\n"
      "v_mfma_f32_16x16x32_bf16 v[244:247], v[12:15], v[204:207], v[244:247]\n"
      "s_waitcnt lgkmcnt(2)\n"
      "v_exp_f32_e64 v164, v164\n"
      "v_mfma_f32_16x16x32_bf16 v[244:247], v[24:27], v[212:215], v[244:247]\n"
      "global_store_dwordx4 v117, v[156:159], s[54:55] sc1\n"
      "v_mfma_f32_16x16x32_bf16 v[176:179], v[16:19], v[204:207], v[176:179]\n"
      "v_fma_f32 v228, v200, v200, s39\n"
      "v_cvt_pk_bf16_f32 v151, v210, v211\n"
      "v_mfma_f32_16x16x32_bf16 v[176:179], v[28:31], v[212:215], v[176:179]\n"
      "v_exp_f32 v165, v165\n"
      "v_mfma_f32_16x16x32_bf16 v[212:215], v[66:69], v[184:187], 0\n"
      "v_exp_f32 v166, v166\n"
      "v_mfma_f32_16x16x32_bf16 v[212:215], v[66:69], v[62:65], v[212:215]\n"
      "v_exp_f32 v167, v167\n"
      "v_mfma_f32_16x16x32_bf16 v[208:211], v[140:143], v[184:187], 0\n"
      "v_exp_f32 v220, v220\n"
      "v_mfma_f32_16x16x32_bf16 v[208:211], v[50:53], v[62:65], v[208:211]\n"
      "v_fmac_f32 v228, v201, v201\n"
      "v_fmac_f32 v228, v202, v202\n"
      "v_mfma_f32_16x16x32_bf16 v[204:207], v[224:227], v[184:187], 0\n"
      "v_fmac_f32 v228, v203, v203\n"
      "v_fmac_f32 v228, v244, v244\n"
      "v_mfma_f32_16x16x32_bf16 v[204:207], v[54:57], v[62:65], v[204:207]\n"
      "v_fmac_f32 v228, v245, v245\n"
      "v_fmac_f32 v228, v246, v246\n"
      "v_mfma_f32_16x16x32_bf16 v[236:239], v[136:139], v[184:187], 0\n"
      "v_fmac_f32 v228, v247, v247\n"
      "v_fmac_f32 v228, v176, v176\n"
      "v_mfma_f32_16x16x32_bf16 v[236:239], v[58:61], v[62:65], v[236:239]\n"
      "v_fmac_f32 v228, v177, v177\n"
      "v_fmac_f32 v228, v178, v178\n"
      "v_mfma_f32_16x16x32_bf16 v[156:159], v[252:255], v[168:171], v[102:105]\n"
      "v_cvt_pk_bf16_f32 v62, v164, v165\n"
      "v_fmac_f32_e64 v228, v179, v179\n"
      "v_mfma_f32_16x16x32_bf16 v[156:159], v[34:37], v[44:47], v[156:159]\n"
      "ds_read_b128 v[184:187], v121\n"
      "v_cvt_pk_bf16_f32 v63, v166, v167\n"
      "v_mfma_f32_16x16x32_bf16 v[164:167], v[248:251], v[168:171], v[106:109]\n"
      "v_cvt_pk_bf16_f32 v64, v220, 0\n"
      "v_cndmask_b32 v229, v229, v221, s[42:43]\n"
      "v_mfma_f32_16x16x32_bf16 v[164:167], v[40:43], v[44:47], v[164:167]\n"
      "v_rcp_f32_e64 v212, v212\n"
      "v_mfma_f32_16x16x32_bf16 v[248:251], v[240:243], v[168:171], v[74:77]\n"
      "v_mul_f32 v208, v212, v208\n"
      "v_mul_f32 v209, v212, v209\n"
      "v_mfma_f32_16x16x32_bf16 v[248:251], v[216:219], v[188:191], v[248:251]\n"
      "v_mul_f32_e64 v210, v212, v210\n"
      "v_cvt_pk_bf16_f32 v208, v208, v209\n"
      "v_mfma_f32_16x16x32_bf16 v[248:251], v[192:195], v[44:47], v[248:251]\n"
      "v_mul_f32_e64 v211, v212, v211\n"
      "v_cvt_pk_bf16_f32 v209, v210, v211\n"
      "v_mfma_f32_16x16x32_bf16 v[220:223], v[188:191], v[168:171], v[110:113]\n"
      "ds_read_b128 v[168:171], v123 offset:8192\n"
      "v_mul_f32 v204, v212, v204\n"
      "v_mfma_f32_16x16x32_bf16 v[220:223], v[46:49], v[44:47], v[220:223]\n"
      "v_mul_f32 v205, v212, v205\n"
      "v_cvt_pk_bf16_f32 v210, v204, v205\n"
      "v_mfma_f32_16x16x32_bf16 v[188:191], v[216:219], v[240:243], v[76:79]\n"
      "ds_read_b128 v[216:219], v123 offset:11264\n"
      "v_mul_f32_e64 v206, v212, v206\n"
      "v_mfma_f32_16x16x32_bf16 v[250:253], v[66:69], v[148:151], 0\n"
      "ds_read_b128 v[240:243], v123 offset:14336\n"
      "v_mul_f32 v207, v212, v207\n"
      "v_mfma_f32_16x16x32_bf16 v[250:253], v[66:69], v[62:65], v[250:253]\n"
      "s_waitcnt lgkmcnt(3)\n"
      "v_cvt_pk_bf16_f32 v211, v206, v207\n"
      "v_mul_f32_e64 v236, v212, v236\n"
      "v_mfma_f32_16x16x16_bf16 v[188:191], v[194:195], v[192:193], v[188:191]\n"
      "ds_write_b128 v124, v[208:211] offset:18432\n"
      "v_mfma_f32_16x16x32_bf16 v[204:207], v[140:143], v[148:151], 0\n"
      "v_mul_f32 v237, v212, v237\n"
      "v_cvt_pk_bf16_f32 v236, v236, v237\n"
      "v_mfma_f32_16x16x32_bf16 v[204:207], v[50:53], v[62:65], v[204:207]\n"
      "s_mov_b32 exec_hi, 0\n"
      "global_store_dwordx4 v118, v[184:187], s[54:55] sc1\n"
      "s_mov_b32 exec_hi, -1\n"
      "v_mfma_f32_16x16x32_bf16 v[192:195], v[224:227], v[148:151], 0\n"
      "s_add_u32 s28, s27, 2\n"
      "v_mul_f32_e64 v238, v212, v238\n"
      "v_mul_f32 v239, v212, v239\n"
      "v_mfma_f32_16x16x32_bf16 v[192:195], v[54:57], v[62:65], v[192:195]\n"
      "s_waitcnt lgkmcnt(2)\n"
      "v_cvt_pk_bf16_f32 v237, v238, v239\n"
      "v_cndmask_b32 v229, v229, v249, s[44:45]\n"
      "v_mfma_f32_16x16x32_bf16 v[208:211], v[136:139], v[148:151], 0\n"
      "s_min_u32 m0, s28, s3\n"
      "v_cndmask_b32 v229, v229, v189, s[46:47]\n"
      "s_movrels_b32 s9, s80\n"
      "v_mfma_f32_16x16x32_bf16 v[208:211], v[58:61], v[62:65], v[208:211]\n"
      "s_lshr_b32 s8, s9, 8\n"
      "v_exp_f32 v229, v229\n"
      "v_mfma_f32_16x16x32_bf16 v[212:215], a[216:219], v[172:175], 0\n"
      "s_and_b32 s9, s9, 0xff\n"
      "v_cvt_pk_bf16_f32 v229, v229, v229\n"
      "s_mul_i32 s10, s8, 73\n"
      "v_mfma_f32_16x16x32_bf16 v[212:215], a[228:231], v[196:199], v[212:215]\n"
      "ds_write_b16 v130, v229\n"
      "v_mfma_f32_16x16x32_bf16 v[184:187], a[220:223], v[172:175], 0\n"
      "s_add_u32 s10, s10, s9\n"
      "ds_read_b128 v[160:163], v131\n"
      "v_mfma_f32_16x16x32_bf16 v[184:187], a[232:235], v[196:199], v[184:187]\n"
      "ds_read_b128 v[252:255], v131 offset:64\n"
      "v_exp_f32 v158, v158\n"
      "v_exp_f32 v159, v159\n"
      "v_exp_f32 v156, v156\n"
      "v_mfma_f32_16x16x32_bf16 v[212:215], a[240:243], v[168:171], v[212:215]\n"
      "s_mul_i32 s28, s10, 4802\n"
      "v_exp_f32 v157, v157\n"
      "v_mfma_f32_16x16x32_bf16 v[212:215], a[252:255], v[216:219], v[212:215]\n"
      "s_add_u32 s56, s18, s28\n"
      "v_exp_f32 v165, v165\n"
      "v_mfma_f32_16x16x32_bf16 v[184:187], a[244:247], v[168:171], v[184:187]\n"
      "s_addc_u32 s57, s19, 0\n"
      "v_cvt_pk_bf16_f32 v156, v156, v157\n"
      "s_and_b32 s56, s56, -16\n"
      "v_mfma_f32_16x16x32_bf16 v[184:187], v[0:3], v[216:219], v[184:187]\n"
      "s_sub_u32 s58, s56, 110592\n"
      "v_exp_f32 v166, v166\n"
      "v_exp_f32_e64 v164, v164\n"
      "v_cvt_pk_bf16_f32 v157, v158, v159\n"
      "v_cvt_pk_bf16_f32 v158, v164, v165\n"
      "v_mfma_f32_16x16x32_bf16 v[188:191], a[224:227], v[172:175], 0\n"
      "ds_read_b128 v[148:151], v123 offset:17408\n"
      "s_subb_u32 s59, s57, 0\n"
      "v_mfma_f32_16x16x32_bf16 v[188:191], a[236:239], v[196:199], v[188:191]\n"
      "s_waitcnt lgkmcnt(1)\n"
      "v_exp_f32_e64 v167, v167\n"
      "v_mfma_f32_16x16x32_bf16 v[188:191], a[248:251], v[168:171], v[188:191]\n"
      "ds_read_b128 v[168:171], v114\n"
      "v_cvt_pk_bf16_f32 v159, v166, v167\n"
      "v_mfma_f32_16x16x32_bf16 v[188:191], v[4:7], v[216:219], v[188:191]\n"
      "ds_read_b128 v[172:175], v115\n"
      "v_exp_f32 v220, v220\n"
      "v_mfma_f32_16x16x32_bf16 v[196:199], v[160:163], v[66:69], 0\n"
      "s_sub_u32 s56, s56, 73728\n"
      "v_exp_f32 v221, v221\n"
      "v_mfma_f32_16x16x32_bf16 v[196:199], v[252:255], v[66:69], v[196:199]\n"
      "s_subb_u32 s57, s57, 0\n"
      "v_exp_f32 v222, v222\n"
      "v_mfma_f32_16x16x32_bf16 v[164:167], v[160:163], v[140:143], 0\n"
      "s_mul_i32 s28, s8, 1373568\n"
      "v_exp_f32_e64 v223, v223\n"
      "v_mfma_f32_16x16x32_bf16 v[164:167], v[252:255], v[50:53], v[164:167]\n"
      "s_mul_i32 s29, s9, 2688\n"
      "v_exp_f32 v248, v248\n"
      "v_mfma_f32_16x16x32_bf16 v[216:219], v[160:163], v[224:227], 0\n"
      "s_waitcnt lgkmcnt(2)\n"
      "v_cvt_pk_bf16_f32 v63, v222, v223\n"
      "v_cvt_pk_bf16_f32 v62, v220, v221\n"
      "v_mfma_f32_16x16x32_bf16 v[216:219], v[252:255], v[54:57], v[216:219]\n"
      "s_add_u32 s28, s28, s29\n"
      "v_rcp_f32 v250, v250\n"
      "v_mfma_f32_16x16x32_bf16 v[212:215], v[8:11], v[240:243], v[212:215]\n"
      "s_cmp_eq_u32 s8, 72\n"
      "v_mul_f32 v204, v250, v204\n"
      "v_mul_f32 v205, v250, v205\n"
      "v_mfma_f32_16x16x32_bf16 v[212:215], v[20:23], v[148:151], v[212:215]\n"
      "s_waitcnt lgkmcnt(0)\n"
      "v_mul_f32 v206, v250, v206\n"
      "v_cvt_pk_bf16_f32 v64, v248, 0\n"
      "v_mul_f32_e64 v195, v250, v195\n"
      "v_cvt_pk_bf16_f32 v204, v204, v205\n"
      "v_mfma_f32_16x16x32_bf16 v[184:187], v[12:15], v[240:243], v[184:187]\n"
      "s_cselect_b32 s8, 0, 100270464\n"
      "v_mul_f32_e64 v207, v250, v207\n"
      "v_cvt_pk_bf16_f32 v205, v206, v207\n"
      "v_mfma_f32_16x16x32_bf16 v[184:187], v[24:27], v[148:151], v[184:187]\n"
      "s_cmp_eq_u32 s9, 72\n"
      "v_mul_f32 v192, v250, v192\n"
      "v_mul_f32 v193, v250, v193\n"
      "v_mfma_f32_16x16x32_bf16 v[188:191], v[16:19], v[240:243], v[188:191]\n"
      "s_cselect_b32 s9, 0, 196224\n"
      "v_mul_f32 v194, v250, v194\n"
      "v_cvt_pk_bf16_f32 v206, v192, v193\n"
      "v_mfma_f32_16x16x32_bf16 v[188:191], v[28:31], v[148:151], v[188:191]\n"
      "s_add_u32 s10, s8, s9\n"
      "v_cvt_pk_bf16_f32 v207, v194, v195\n"
      "v_cndmask_b32 v164, v164, v216, s[42:43]\n"
      "v_mfma_f32_16x16x32_bf16 v[192:195], v[66:69], v[156:159], 0\n"
      "s_add_u32 s28, s28, s10\n"
      "v_mul_f32 v208, v250, v208\n"
      "v_mul_f32 v209, v250, v209\n"
      "v_mfma_f32_16x16x32_bf16 v[192:195], v[66:69], v[62:65], v[192:195]\n"
      "ds_write_b128 v124, v[204:207] offset:19456\n"
      "v_mfma_f32_16x16x32_bf16 v[240:243], v[140:143], v[156:159], 0\n"
      "s_add_u32 s52, s16, s28\n"
      "v_mul_f32 v210, v250, v210\n"
      "v_cvt_pk_bf16_f32 v208, v208, v209\n"
      "v_mfma_f32_16x16x32_bf16 v[240:243], v[50:53], v[62:65], v[240:243]\n"
      "s_addc_u32 s53, s17, 0\n"
      "v_mul_f32_e64 v211, v250, v211\n"
      "v_cvt_pk_bf16_f32 v209, v210, v211\n"
      "v_mfma_f32_16x16x32_bf16 v[248:251], v[224:227], v[156:159], 0\n"
      "s_add_u32 s27, s27, 1\n"
      "v_fma_f32 v229, v212, v212, s39\n"
      "v_fmac_f32 v229, v213, v213\n"
      "v_mfma_f32_16x16x32_bf16 v[248:251], v[54:57], v[62:65], v[248:251]\n"
      "ds_write2st64_b64 v125, v[236:237], v[208:209] offset0:36 offset1:38\n"
      "v_mfma_f32_16x16x32_bf16 v[148:151], v[136:139], v[156:159], 0\n"
      "v_fmac_f32 v229, v214, v214\n"
      "v_fmac_f32 v229, v215, v215\n"
      "v_mfma_f32_16x16x32_bf16 v[148:151], v[58:61], v[62:65], v[148:151]\n"
      "s_cmp_ge_u32 s27, s26\n"
      "v_mov_b32_dpp v196, v196 quad_perm:[0,0,0,0] row_mask:0xf bank_mask:0xf\n"
      "v_fmac_f32 v229, v184, v184\n"
      "v_fmac_f32_e64 v229, v185, v185\n"
      "v_mfma_f32_16x16x32_bf16 v[224:227], v[160:163], v[136:139], 0\n"
      "v_fmac_f32 v229, v186, v186\n"
      "v_fmac_f32 v229, v187, v187\n"
      "v_mfma_f32_16x16x32_bf16 v[224:227], v[252:255], v[58:61], v[224:227]\n"
      "v_fmac_f32 v229, v188, v188\n"
      "v_fmac_f32 v229, v189, v189\n"
      "v_mfma_f32_16x16x32_bf16 v[204:207], a[0:3], v[168:171], 0\n"
      "s_cselect_b32 s28, 1, 0\n"
      "v_fmac_f32 v229, v190, v190\n"
      "v_fmac_f32 v229, v191, v191\n"
      "v_mfma_f32_16x16x32_bf16 v[204:207], a[36:39], v[172:175], v[204:207]\n"
      "s_xor_b32 s29, s10, s11\n"
      "v_permlane16_swap_b32 v230, v228\n"
      "v_mfma_f32_16x16x32_bf16 v[160:163], a[4:7], v[168:171], 0\n"
      "v_add_f32 v230, v230, v228\n"
      "v_mov_b32 v231, v229\n"
      "v_mfma_f32_16x16x32_bf16 v[160:163], a[40:43], v[172:175], v[160:163]\n"
      "s_or_b32 s28, s28, s29\n"
      "v_permlane16_swap_b32 v229, v231\n"
      "v_add_f32 v229, v229, v231\n"
      "v_cndmask_b32 v164, v164, v224, s[44:45]\n"
      "v_rcp_f32 v196, v196\n"
      "v_permlane32_swap_b32 v230, v229\n"
      "v_mul_f32_e64 v164, v196, v164\n"
      "v_add_f32 v230, v230, v229\n"
      "v_cvt_pk_bf16_f32 v164, v164, v164\n"
      "ds_write_b32 v129, v230 offset:1024\n"
      "ds_write_b16 v135, v164 offset:16\n"
      "L_join_21_%=:\n"
      "s_cbranch_scc1 L_tail1_%=\n"
      "L_mid_%=:\n"
      "v_mfma_f32_16x16x32_bf16 v[196:199], a[8:11], v[168:171], 0\n"
      "s_add_u32 m0, s76, 18432\n"
      "ds_read_b128 v[228:231], v114 offset:6144\n"
      "v_mfma_f32_16x16x32_bf16 v[196:199], a[44:47], v[172:175], v[196:199]\n"
      "ds_read_b128 v[208:211], v115 offset:6144\n"
      "v_rcp_f32 v192, v192\n"
      "v_mfma_f32_16x16x32_bf16 v[164:167], a[12:15], v[168:171], 0\n"
      "v_mul_f32 v241, v192, v241\n"
      "v_mul_f32_e64 v240, v192, v240\n"
      "v_mfma_f32_16x16x32_bf16 v[164:167], a[48:51], v[172:175], v[164:167]\n"
      "ds_read_b128 v[224:227], v114 offset:12288\n"
      "v_cvt_pk_bf16_f32 v240, v240, v241\n"
      "v_mfma_f32_16x16x32_bf16 v[156:159], a[16:19], v[168:171], 0\n"
      "v_mul_f32 v242, v192, v242\n"
      "v_mul_f32 v243, v192, v243\n"
      "v_mfma_f32_16x16x32_bf16 v[156:159], a[52:55], v[172:175], v[156:159]\n"
      "s_waitcnt lgkmcnt(3)\n"
      "s_barrier\n"
      "v_mfma_f32_16x16x32_bf16 v[136:139], a[20:23], v[168:171], 0\n"
      "ds_read_b128 v[140:143], v128 offset:1024\n"
      "v_cvt_pk_bf16_f32 v241, v242, v243\n"
      "v_mfma_f32_16x16x32_bf16 v[136:139], a[56:59], v[172:175], v[136:139]\n"
      "global_load_lds_dwordx4 v116, s[52:53]\n"
      "v_mul_f32 v248, v192, v248\n"
      "v_mfma_f32_16x16x32_bf16 v[220:223], v[168:171], a[24:27], 0\n"
      "s_add_u32 m0, s76, 24448\n"
      "v_mul_f32 v250, v192, v250\n"
      "v_mul_f32_e64 v251, v192, v251\n"
      "v_mfma_f32_16x16x32_bf16 v[220:223], v[172:175], a[60:63], v[220:223]\n"
      "v_mul_f32_e64 v249, v192, v249\n"
      "v_cvt_pk_bf16_f32 v242, v248, v249\n"
      "v_mfma_f32_16x16x32_bf16 v[236:239], v[168:171], a[28:31], 0\n"
      "v_cvt_pk_bf16_f32 v243, v250, v251\n"
      "v_mul_f32_e64 v148, v192, v148\n"
      "v_mfma_f32_16x16x32_bf16 v[236:239], v[172:175], a[64:67], v[236:239]\n"
      "ds_write_b128 v124, v[240:243] offset:20480\n"
      "v_mfma_f32_16x16x32_bf16 v[252:255], v[168:171], a[32:35], 0\n"
      "s_waitcnt lgkmcnt(2)\n"
      "ds_read_b128 v[168:171], v115 offset:12288\n"
      "v_mfma_f32_16x16x32_bf16 v[252:255], v[172:175], a[68:71], v[252:255]\n"
      "v_mul_f32 v149, v192, v149\n"
      "v_cvt_pk_bf16_f32 v148, v148, v149\n"
      "v_mfma_f32_16x16x32_bf16 v[204:207], a[72:75], v[228:231], v[204:207]\n"
      "s_waitcnt lgkmcnt(2)\n"
      "v_mul_f32 v150, v192, v150\n"
      "v_mul_f32_e64 v151, v192, v151\n"
      "v_mfma_f32_16x16x32_bf16 v[204:207], a[108:111], v[208:211], v[204:207]\n"
      "global_load_lds_dwordx4 v116, s[52:53] offset:128\n"
      "v_mfma_f32_16x16x32_bf16 v[160:163], a[76:79], v[228:231], v[160:163]\n"
      "v_cvt_pk_bf16_f32 v149, v150, v151\n"
      "v_add_f32 v140, |v140|, |v141|\n"
      "v_mfma_f32_16x16x32_bf16 v[160:163], a[112:115], v[208:211], v[160:163]\n"
      "ds_write_b64 v125, v[148:149] offset:20480\n"
      "v_mfma_f32_16x16x32_bf16 v[196:199], a[80:83], v[228:231], v[196:199]\n"
      "v_add_f32 v142, |v142|, |v143|\n"
      "v_add_f32 v140, v140, v142\n"
      "v_mfma_f32_16x16x32_bf16 v[196:199], a[116:119], v[208:211], v[196:199]\n"
      "s_add_u32 m0, s76, 30464\n"
      "v_rsq_f32 v140, v140\n"
      "v_mfma_f32_16x16x32_bf16 v[164:167], a[84:87], v[228:231], v[164:167]\n"
      "v_mul_f32 v140, s41, v140\n"
      "v_mov_b32 v249, v140\n"
      "v_mfma_f32_16x16x32_bf16 v[164:167], a[120:123], v[208:211], v[164:167]\n"
      "s_nop 0\n"
      "v_permlane32_swap_b32 v140, v249\n"
      "v_mfma_f32_16x16x32_bf16 v[156:159], a[88:91], v[228:231], v[156:159]\n"
      "ds_read_b128 v[192:195], v114 offset:2048\n"
      "v_mov_b32_e64 v251, v140\n"
      "v_mfma_f32_16x16x32_bf16 v[156:159], a[124:127], v[208:211], v[156:159]\n"
      "global_load_lds_dwordx4 v116, s[52:53] offset:256\n"
      "v_mfma_f32_16x16x32_bf16 v[136:139], a[92:95], v[228:231], v[136:139]\n"
      "v_permlane16_swap_b32_e64 v140, v251\n"
      "v_mfma_f32_16x16x32_bf16 v[136:139], a[128:131], v[208:211], v[136:139]\n"
      "ds_read_b128 v[148:151], v115 offset:2048\n"
      "v_mul_f32 v180, v140, v180\n"
      "v_mfma_f32_16x16x32_bf16 v[220:223], v[228:231], a[96:99], v[220:223]\n"
      "s_waitcnt lgkmcnt(2)\n"
      "v_mul_f32_e64 v181, v140, v181\n"
      "v_cvt_pk_bf16_f32 v180, v180, v181\n"
      "v_mfma_f32_16x16x32_bf16 v[220:223], v[208:211], a[132:135], v[220:223]\n"
      "v_mul_f32 v182, v140, v182\n"
      "v_mul_f32 v183, v140, v183\n"
      "v_mfma_f32_16x16x32_bf16 v[236:239], v[228:231], a[100:103], v[236:239]\n"
      "v_cvt_pk_bf16_f32 v181, v182, v183\n"
      "v_mul_f32 v232, v140, v232\n"
      "v_mfma_f32_16x16x32_bf16 v[236:239], v[208:211], a[136:139], v[236:239]\n"
      "s_add_u32 m0, s77, 18432\n"
      "v_mul_f32 v233, v140, v233\n"
      "v_cvt_pk_bf16_f32 v182, v232, v233\n"
      "v_mfma_f32_16x16x32_bf16 v[252:255], v[228:231], a[104:107], v[252:255]\n"
      "v_mul_f32 v235, v140, v235\n"
      "v_mul_f32 v234, v140, v234\n"
      "v_mfma_f32_16x16x32_bf16 v[252:255], v[208:211], a[140:143], v[252:255]\n"
      "v_mul_f32_e64 v152, v140, v152\n"
      "v_cvt_pk_bf16_f32 v183, v234, v235\n"
      "v_mfma_f32_16x16x32_bf16 v[204:207], a[144:147], v[224:227], v[204:207]\n"
      "global_load_lds_dwordx4 v117, s[52:53]\n"
      "v_mfma_f32_16x16x32_bf16 v[204:207], a[180:183], v[168:171], v[204:207]\n"
      "v_mul_f32 v153, v140, v153\n"
      "v_cvt_pk_bf16_f32 v152, v152, v153\n"
      "v_mfma_f32_16x16x32_bf16 v[160:163], a[148:151], v[224:227], v[160:163]\n"
      "v_mul_f32 v155, v140, v155\n"
      "v_mul_f32_e64 v154, v140, v154\n"
      "v_mfma_f32_16x16x32_bf16 v[160:163], a[184:187], v[168:171], v[160:163]\n"
      "ds_write_b128 v126, v[180:183]\n"
      "v_mfma_f32_16x16x32_bf16 v[196:199], a[152:155], v[224:227], v[196:199]\n"
      "v_cvt_pk_bf16_f32 v153, v154, v155\n"
      "v_mul_f32 v200, v251, v200\n"
      "v_mfma_f32_16x16x32_bf16 v[196:199], a[188:191], v[168:171], v[196:199]\n"
      "v_mul_f32 v201, v251, v201\n"
      "v_cvt_pk_bf16_f32 v200, v200, v201\n"
      "v_mfma_f32_16x16x32_bf16 v[164:167], a[156:159], v[224:227], v[164:167]\n"
      "s_waitcnt lgkmcnt(1)\n"
      "v_mul_f32 v203, v251, v203\n"
      "v_mul_f32_e64 v202, v251, v202\n"
      "v_mfma_f32_16x16x32_bf16 v[164:167], a[192:195], v[168:171], v[164:167]\n"
      "s_add_u32 m0, s78, 18432\n"
      "v_mul_f32_e64 v244, v251, v244\n"
      "v_cvt_pk_bf16_f32 v201, v202, v203\n"
      "v_mfma_f32_16x16x32_bf16 v[156:159], a[160:163], v[224:227], v[156:159]\n"
      "ds_read_b128 v[232:235], v114 offset:8192\n"
      "v_mul_f32_e64 v245, v251, v245\n"
      "v_mfma_f32_16x16x32_bf16 v[156:159], a[196:199], v[168:171], v[156:159]\n"
      "ds_read_b128 v[180:183], v115 offset:8192\n"
      "v_cvt_pk_bf16_f32 v202, v244, v245\n"
      "v_mfma_f32_16x16x32_bf16 v[136:139], a[164:167], v[224:227], v[136:139]\n"
      "v_mul_f32 v246, v251, v246\n"
      "v_mul_f32 v247, v251, v247\n"
      "v_mfma_f32_16x16x32_bf16 v[136:139], a[200:203], v[168:171], v[136:139]\n"
      "s_mov_b32 exec_hi, 0\n"
      "global_load_lds_dwordx4 v118, s[52:53]\n"
      "s_mov_b32 exec_hi, -1\n"
      "v_mfma_f32_16x16x32_bf16 v[220:223], v[224:227], a[168:171], v[220:223]\n"
      "v_mul_f32_e64 v176, v251, v176\n"
      "v_cvt_pk_bf16_f32 v203, v246, v247\n"
      "v_mfma_f32_16x16x32_bf16 v[220:223], v[168:171], a[204:207], v[220:223]\n"
      "ds_write_b128 v126, v[200:203] offset:2048\n"
      "v_mfma_f32_16x16x32_bf16 v[236:239], v[224:227], a[172:175], v[236:239]\n"
      "v_mul_f32 v177, v251, v177\n"
      "v_cvt_pk_bf16_f32 v176, v176, v177\n"
      "v_mfma_f32_16x16x32_bf16 v[236:239], v[168:171], a[208:211], v[236:239]\n"
      "s_add_u32 m0, s76, 115456\n"
      "v_mul_f32 v179, v251, v179\n"
      "v_mul_f32 v178, v251, v178\n"
      "v_mfma_f32_16x16x32_bf16 v[252:255], v[224:227], a[176:179], v[252:255]\n"
      "s_waitcnt lgkmcnt(1)\n"
      "v_mul_f32_e64 v212, v249, v212\n"
      "v_cvt_pk_bf16_f32 v177, v178, v179\n"
      "v_mfma_f32_16x16x32_bf16 v[252:255], v[168:171], a[212:215], v[252:255]\n"
      "ds_write2st64_b64 v127, v[152:153], v[176:177] offset0:0 offset1:4\n"
      "v_mfma_f32_16x16x32_bf16 v[176:179], a[0:3], v[192:195], 0\n"
      "global_load_lds_dwordx4 v119, s[56:57]\n"
      "v_mfma_f32_16x16x32_bf16 v[176:179], a[36:39], v[148:151], v[176:179]\n"
      "s_add_u32 m0, s79, 4864\n"
      "v_mul_f32_e64 v213, v249, v213\n"
      "v_cvt_pk_bf16_f32 v212, v212, v213\n"
      "v_mfma_f32_16x16x32_bf16 v[152:155], a[4:7], v[192:195], 0\n"
      "v_mul_f32 v215, v249, v215\n"
      "v_mul_f32 v214, v249, v214\n"
      "v_mfma_f32_16x16x32_bf16 v[152:155], a[40:43], v[148:151], v[152:155]\n"
      "s_sub_u32 s28, s27, 3\n"
      "v_mul_f32 v184, v249, v184\n"
      "v_cvt_pk_bf16_f32 v213, v214, v215\n"
      "v_mfma_f32_16x16x32_bf16 v[200:203], a[8:11], v[192:195], 0\n"
      "s_cselect_b64 s[54:55], s[12:13], s[22:23]\n"
      "v_mul_f32 v185, v249, v185\n"
      "v_cvt_pk_bf16_f32 v214, v184, v185\n"
      "v_mfma_f32_16x16x32_bf16 v[200:203], a[44:47], v[148:151], v[200:203]\n"
      "v_mul_f32 v187, v249, v187\n"
      "v_mul_f32 v186, v249, v186\n"
      "v_mfma_f32_16x16x32_bf16 v[168:171], a[12:15], v[192:195], 0\n"
      "ds_read_b128 v[244:247], v114 offset:14336\n"
      "v_cvt_pk_bf16_f32 v215, v186, v187\n"
      "v_mfma_f32_16x16x32_bf16 v[168:171], a[48:51], v[148:151], v[168:171]\n"
      "ds_write_b128 v126, v[212:215] offset:4096\n"
      "v_mfma_f32_16x16x32_bf16 v[240:243], a[16:19], v[192:195], 0\n"
      "global_load_lds_dword v122, s[58:59]\n"
      "v_mfma_f32_16x16x32_bf16 v[240:243], a[52:55], v[148:151], v[240:243]\n"
      "ds_read_b128 v[184:187], v115 offset:14336\n"
      "v_mul_f32_e64 v188, v249, v188\n"
      "v_mfma_f32_16x16x32_bf16 v[212:215], a[20:23], v[192:195], 0\n"
      "s_max_i32 m0, s28, 0\n"
      "v_mul_f32 v189, v249, v189\n"
      "v_cvt_pk_bf16_f32 v188, v188, v189\n"
      "v_mfma_f32_16x16x32_bf16 v[212:215], a[56:59], v[148:151], v[212:215]\n"
      "s_movrels_b32 s9, s80\n"
      "v_mul_f32 v190, v249, v190\n"
      "v_mul_f32 v191, v249, v191\n"
      "v_mfma_f32_16x16x32_bf16 v[248:251], v[192:195], a[24:27], 0\n"
      "s_lshr_b32 s8, s9, 8\n"
      "v_cvt_pk_bf16_f32 v189, v190, v191\n"
      "v_fma_f32 v209, v204, v204, s38\n"
      "v_mfma_f32_16x16x32_bf16 v[248:251], v[148:151], a[60:63], v[248:251]\n"
      "ds_write_b64 v127, v[188:189] offset:4096\n"
      "v_mfma_f32_16x16x32_bf16 v[188:191], v[192:195], a[28:31], 0\n"
      "s_and_b32 s9, s9, 0xff\n"
      "v_fmac_f32_e64 v209, v205, v205\n"
      "v_fma_f32 v211, v164, v164, s38\n"
      "v_mfma_f32_16x16x32_bf16 v[188:191], v[148:151], a[64:67], v[188:191]\n"
      "s_mul_i32 s28, s8, 1373568\n"
      "v_fmac_f32 v211, v165, v165\n"
      "v_fmac_f32 v209, v206, v206\n"
      "v_mfma_f32_16x16x32_bf16 v[228:231], v[192:195], a[32:35], 0\n"
      "s_mul_i32 s29, s9, 2688\n"
      "v_fmac_f32 v209, v207, v207\n"
      "v_fmac_f32 v211, v166, v166\n"
      "v_mfma_f32_16x16x32_bf16 v[228:231], v[148:151], a[68:71], v[228:231]\n"
      "s_waitcnt lgkmcnt(1)\n"
      "v_fmac_f32 v209, v160, v160\n"
      "v_fmac_f32_e64 v211, v167, v167\n"
      "v_mfma_f32_16x16x32_bf16 v[176:179], a[72:75], v[232:235], v[176:179]\n"
      "s_add_u32 s28, s28, s29\n"
      "v_fmac_f32 v211, v156, v156\n"
      "v_fmac_f32_e64 v209, v161, v161\n"
      "v_mfma_f32_16x16x32_bf16 v[176:179], a[108:111], v[180:183], v[176:179]\n"
      "s_cmp_eq_u32 s8, 72\n"
      "v_fmac_f32 v211, v157, v157\n"
      "v_fmac_f32 v209, v162, v162\n"
      "v_mfma_f32_16x16x32_bf16 v[152:155], a[76:79], v[232:235], v[152:155]\n"
      "s_cselect_b32 s8, 0, 100270464\n"
      "v_fmac_f32 v211, v158, v158\n"
      "v_fmac_f32 v209, v163, v163\n"
      "v_mfma_f32_16x16x32_bf16 v[152:155], a[112:115], v[180:183], v[152:155]\n"
      "s_cmp_eq_u32 s9, 72\n"
      "v_fmac_f32 v209, v196, v196\n"
      "v_fmac_f32 v211, v159, v159\n"
      "v_mfma_f32_16x16x32_bf16 v[200:203], a[80:83], v[232:235], v[200:203]\n"
      "ds_read_b128 v[192:195], v114 offset:4096\n"
      "v_fmac_f32_e64 v211, v136, v136\n"
      "v_mfma_f32_16x16x32_bf16 v[200:203], a[116:119], v[180:183], v[200:203]\n"
      "s_cselect_b32 s9, 0, 196224\n"
      "v_fmac_f32 v209, v197, v197\n"
      "v_fmac_f32 v211, v137, v137\n"
      "v_mfma_f32_16x16x32_bf16 v[168:171], a[84:87], v[232:235], v[168:171]\n"
      "ds_read_b128 v[148:151], v115 offset:4096\n"
      "v_fmac_f32 v209, v198, v198\n"
      "v_mfma_f32_16x16x32_bf16 v[168:171], a[120:123], v[180:183], v[168:171]\n"
      "s_add_u32 s10, s8, s9\n"
      "v_fmac_f32 v211, v138, v138\n"
      "v_fmac_f32 v209, v199, v199\n"
      "v_mfma_f32_16x16x32_bf16 v[240:243], a[88:91], v[232:235], v[240:243]\n"
      "s_add_u32 s28, s28, s10\n"
      "v_cvt_pk_bf16_f32 v220, v220, v221\n"
      "v_fmac_f32 v211, v139, v139\n"
      "v_mfma_f32_16x16x32_bf16 v[240:243], a[124:127], v[180:183], v[240:243]\n"
      "s_add_u32 s54, s54, s28\n"
      "v_permlane16_swap_b32 v209, v211\n"
      "v_mfma_f32_16x16x32_bf16 v[212:215], a[92:95], v[232:235], v[212:215]\n"
      "s_addc_u32 s55, s55, 0\n"
      "v_add_f32 v209, v209, v211\n"
      "v_mov_b32 v211, v209\n"
      "v_mfma_f32_16x16x32_bf16 v[212:215], a[128:131], v[180:183], v[212:215]\n"
      "s_cmp_lg_u32 s10, s11\n"
      "v_permlane32_swap_b32 v209, v211\n"
      "v_mfma_f32_16x16x32_bf16 v[248:251], v[232:235], a[96:99], v[248:251]\n"
      "s_cbranch_scc1 L_edge_26_%=\n"
      "L_eb_27_%=:\n"
      "v_add_f32 v209, v209, v211\n"
      "v_mfma_f32_16x16x32_bf16 v[248:251], v[180:183], a[132:135], v[248:251]\n"
      "v_rsq_f32 v209, v209\n"
      "v_mfma_f32_16x16x32_bf16 v[188:191], v[232:235], a[100:103], v[188:191]\n"
      "s_mov_b32 s5, s4\n"
      "v_cvt_pk_bf16_f32 v221, v222, v223\n"
      "v_mov_b32 v211, v209\n"
      "v_mfma_f32_16x16x32_bf16 v[188:191], v[180:183], a[136:139], v[188:191]\n"
      "s_add_u32 s4, s27, 1\n"
      "v_permlane16_swap_b32 v209, v211\n"
      "v_mfma_f32_16x16x32_bf16 v[228:231], v[232:235], a[104:107], v[228:231]\n"
      "s_waitcnt lgkmcnt(0)\n"
      "v_mul_f32 v164, v211, v164\n"
      "v_mul_f32_e64 v165, v211, v165\n"
      "v_mfma_f32_16x16x32_bf16 v[228:231], v[180:183], a[140:143], v[228:231]\n"
      "s_lshl_b32 s4, s4, 30\n"
      "v_mul_f32 v166, v211, v166\n"
      "v_cvt_pk_bf16_f32 v164, v164, v165\n"
      "v_mfma_f32_16x16x32_bf16 v[176:179], a[144:147], v[244:247], v[176:179]\n"
      "v_mul_f32 v167, v211, v167\n"
      "v_cvt_pk_bf16_f32 v165, v166, v167\n"
      "v_mfma_f32_16x16x32_bf16 v[176:179], a[180:183], v[184:187], v[176:179]\n"
      "ds_read_b128 v[232:235], v119 offset:18432\n"
      "v_mul_f32 v156, v211, v156\n"
      "v_mfma_f32_16x16x32_bf16 v[152:155], a[148:151], v[244:247], v[152:155]\n"
      "v_mul_f32 v157, v211, v157\n"
      "v_cvt_pk_bf16_f32 v166, v156, v157\n"
      "v_mfma_f32_16x16x32_bf16 v[152:155], a[184:187], v[184:187], v[152:155]\n"
      "v_mul_f32 v158, v211, v158\n"
      "v_mul_f32 v159, v211, v159\n"
      "v_mfma_f32_16x16x32_bf16 v[200:203], a[152:155], v[244:247], v[200:203]\n"
      "v_cvt_pk_bf16_f32 v167, v158, v159\n"
      "v_mul_f32 v136, v211, v136\n"
      "v_mfma_f32_16x16x32_bf16 v[200:203], a[188:191], v[184:187], v[200:203]\n"
      "s_and_b32 s4, s4, 0x80000000\n"
      "v_mul_f32 v137, v211, v137\n"
      "v_cvt_pk_bf16_f32 v34, v136, v137\n"
      "v_mfma_f32_16x16x32_bf16 v[168:171], a[156:159], v[244:247], v[168:171]\n"
      "v_mul_f32 v139, v211, v139\n"
      "v_mul_f32_e64 v138, v211, v138\n"
      "v_mfma_f32_16x16x32_bf16 v[168:171], a[192:195], v[184:187], v[168:171]\n"
      "ds_read_b128 v[180:183], v114 offset:10240\n"
      "v_cvt_pk_bf16_f32 v35, v138, v139\n"
      "v_mfma_f32_16x16x32_bf16 v[240:243], a[160:163], v[244:247], v[240:243]\n"
      "ds_read_b128 v[136:139], v115 offset:10240\n"
      "v_mul_f32 v209, s36, v209\n"
      "v_mfma_f32_16x16x32_bf16 v[240:243], a[196:199], v[184:187], v[240:243]\n"
      "s_waitcnt lgkmcnt(2)\n"
      "v_mul_f32 v205, v209, v205\n"
      "v_mul_f32 v204, v209, v204\n"
      "v_mfma_f32_16x16x32_bf16 v[212:215], a[164:167], v[244:247], v[212:215]\n"
      "global_store_dwordx4 v116, v[232:235], s[54:55] sc1\n"
      "v_mfma_f32_16x16x32_bf16 v[212:215], a[200:203], v[184:187], v[212:215]\n"
      "v_cvt_pk_bf16_f32 v204, v204, v205\n"
      "v_mul_f32 v206, v209, v206\n"
      "v_mfma_f32_16x16x32_bf16 v[248:251], v[244:247], a[168:171], v[248:251]\n"
      "v_mul_f32 v207, v209, v207\n"
      "v_cvt_pk_bf16_f32 v205, v206, v207\n"
      "v_mfma_f32_16x16x32_bf16 v[248:251], v[184:187], a[204:207], v[248:251]\n"
      "v_mul_f32 v160, v209, v160\n"
      "v_mul_f32 v161, v209, v161\n"
      "v_mfma_f32_16x16x32_bf16 v[188:191], v[244:247], a[172:175], v[188:191]\n"
      "v_mul_f32_e64 v162, v209, v162\n"
      "v_cvt_pk_bf16_f32 v206, v160, v161\n"
      "v_mfma_f32_16x16x32_bf16 v[188:191], v[184:187], a[208:211], v[188:191]\n"
      "v_mul_f32_e64 v163, v209, v163\n"
      "v_cvt_pk_bf16_f32 v207, v162, v163\n"
      "v_mfma_f32_16x16x32_bf16 v[228:231], v[244:247], a[176:179], v[228:231]\n"
      "v_mul_f32 v196, v209, v196\n"
      "v_mul_f32_e64 v197, v209, v197\n"
      "v_mfma_f32_16x16x32_bf16 v[228:231], v[184:187], a[212:215], v[228:231]\n"
      "s_waitcnt lgkmcnt(0)\n"
      "v_cvt_pk_bf16_f32 v32, v196, v197\n"
      "v_mul_f32 v198, v209, v198\n"
      "v_mfma_f32_16x16x32_bf16 v[184:187], a[0:3], v[192:195], 0\n"
      "v_mul_f32 v199, v209, v199\n"
      "v_cvt_pk_bf16_f32 v33, v198, v199\n"
      "v_mfma_f32_16x16x32_bf16 v[184:187], a[36:39], v[148:151], v[184:187]\n"
      "v_cvt_pk_bf16_f32 v222, v248, v249\n"
      "v_cvt_pk_bf16_f32 v223, v250, v251\n"
      "v_mfma_f32_16x16x32_bf16 v[244:247], a[4:7], v[192:195], 0\n"
      "v_cvt_pk_bf16_f32 v236, v236, v237\n"
      "v_cvt_pk_bf16_f32 v237, v238, v239\n"
      "v_mfma_f32_16x16x32_bf16 v[244:247], a[40:43], v[148:151], v[244:247]\n"
      "v_cvt_pk_bf16_f32 v238, v188, v189\n"
      "v_cvt_pk_bf16_f32 v239, v190, v191\n"
      "v_mfma_f32_16x16x32_bf16 v[196:199], a[8:11], v[192:195], 0\n"
      "ds_read_b128 v[232:235], v119 offset:24576\n"
      "v_cvt_pk_bf16_f32 v252, v252, v253\n"
      "v_mfma_f32_16x16x32_bf16 v[196:199], a[44:47], v[148:151], v[196:199]\n"
      "v_cvt_pk_bf16_f32 v253, v254, v255\n"
      "v_cvt_pk_bf16_f32 v254, v228, v229\n"
      "v_mfma_f32_16x16x32_bf16 v[248:251], a[12:15], v[192:195], 0\n"
      "v_fma_f32 v209, v176, v176, s38\n"
      "v_cvt_pk_bf16_f32 v255, v230, v231\n"
      "v_mfma_f32_16x16x32_bf16 v[248:251], a[48:51], v[148:151], v[248:251]\n"
      "v_fma_f32 v211, v168, v168, s38\n"
      "v_fmac_f32 v209, v177, v177\n"
      "v_mfma_f32_16x16x32_bf16 v[188:191], a[16:19], v[192:195], 0\n"
      "v_fmac_f32 v209, v178, v178\n"
      "v_fmac_f32 v211, v169, v169\n"
      "v_mfma_f32_16x16x32_bf16 v[188:191], a[52:55], v[148:151], v[188:191]\n"
      "v_fmac_f32 v209, v179, v179\n"
      "v_fmac_f32 v211, v170, v170\n"
      "v_mfma_f32_16x16x32_bf16 v[228:231], a[20:23], v[192:195], 0\n"
      "v_fmac_f32 v211, v171, v171\n"
      "v_fmac_f32 v209, v152, v152\n"
      "v_mfma_f32_16x16x32_bf16 v[228:231], a[56:59], v[148:151], v[228:231]\n"
      "v_fmac_f32 v209, v153, v153\n"
      "v_fmac_f32 v211, v240, v240\n"
      "v_mfma_f32_16x16x32_bf16 v[160:163], v[192:195], a[24:27], 0\n"
      "s_waitcnt lgkmcnt(0)\n"
      "v_fmac_f32_e64 v211, v241, v241\n"
      "v_mfma_f32_16x16x32_bf16 v[160:163], v[148:151], a[60:63], v[160:163]\n"
      "global_store_dwordx4 v116, v[232:235], s[54:55] offset:128 sc1\n"
      "v_mfma_f32_16x16x32_bf16 v[156:159], v[192:195], a[28:31], 0\n"
      "ds_read_b128 v[224:227], v114 offset:16384\n"
      "v_fmac_f32_e64 v209, v154, v154\n"
      "v_mfma_f32_16x16x32_bf16 v[156:159], v[148:151], a[64:67], v[156:159]\n"
      "v_fmac_f32 v211, v242, v242\n"
      "v_fmac_f32 v209, v155, v155\n"
      "v_mfma_f32_16x16x32_bf16 v[172:175], v[192:195], a[32:35], 0\n"
      "ds_read_b128 v[232:235], v115 offset:16384\n"
      "v_fmac_f32 v211, v243, v243\n"
      "v_mfma_f32_16x16x32_bf16 v[172:175], v[148:151], a[68:71], v[172:175]\n"
      "v_fmac_f32 v209, v200, v200\n"
      "v_fmac_f32 v211, v212, v212\n"
      "v_mfma_f32_16x16x32_bf16 v[184:187], a[72:75], v[180:183], v[184:187]\n"
      "v_fmac_f32 v211, v213, v213\n"
      "v_fmac_f32 v209, v201, v201\n"
      "v_mfma_f32_16x16x32_bf16 v[184:187], a[108:111], v[136:139], v[184:187]\n"
      "v_fmac_f32 v211, v214, v214\n"
      "v_fmac_f32 v209, v202, v202\n"
      "v_mfma_f32_16x16x32_bf16 v[244:247], a[76:79], v[180:183], v[244:247]\n"
      "v_fmac_f32 v209, v203, v203\n"
      "v_fmac_f32 v211, v215, v215\n"
      "v_mfma_f32_16x16x32_bf16 v[244:247], a[112:115], v[136:139], v[244:247]\n"
      "s_nop 0\n"
      "v_permlane16_swap_b32 v209, v211\n"
      "v_mfma_f32_16x16x32_bf16 v[196:199], a[80:83], v[180:183], v[196:199]\n"
      "v_add_f32 v209, v209, v211\n"
      "v_mov_b32 v211, v209\n"
      "v_mfma_f32_16x16x32_bf16 v[196:199], a[116:119], v[136:139], v[196:199]\n"
      "s_nop 0\n"
      "v_permlane32_swap_b32 v209, v211\n"
      "v_mfma_f32_16x16x32_bf16 v[248:251], a[84:87], v[180:183], v[248:251]\n"
      "v_add_f32 v209, v209, v211\n"
      "v_mfma_f32_16x16x32_bf16 v[248:251], a[120:123], v[136:139], v[248:251]\n"
      "v_rsq_f32 v209, v209\n"
      "v_mfma_f32_16x16x32_bf16 v[188:191], a[88:91], v[180:183], v[188:191]\n"
      "v_mov_b32 v211, v209\n"
      "v_add_u32_e64 v135, 32, v135\n"
      "v_mfma_f32_16x16x32_bf16 v[188:191], a[124:127], v[136:139], v[188:191]\n"
      "v_permlane16_swap_b32 v209, v211\n"
      "v_mfma_f32_16x16x32_bf16 v[228:231], a[92:95], v[180:183], v[228:231]\n"
      "v_mul_f32 v168, v211, v168\n"
      "v_mul_f32_e64 v169, v211, v169\n"
      "v_mfma_f32_16x16x32_bf16 v[228:231], a[128:131], v[136:139], v[228:231]\n"
      "ds_read_b128 v[192:195], v132 offset:32\n"
      "v_cvt_pk_bf16_f32 v168, v168, v169\n"
      "v_mfma_f32_16x16x32_bf16 v[160:163], v[180:183], a[96:99], v[160:163]\n"
      "s_waitcnt lgkmcnt(1)\n"
      "v_mul_f32 v171, v211, v171\n"
      "v_mul_f32_e64 v170, v211, v170\n"
      "v_mfma_f32_16x16x32_bf16 v[160:163], v[136:139], a[132:135], v[160:163]\n"
      "ds_read_b64 v[148:149], v132 offset:24\n"
      "v_cvt_pk_bf16_f32 v169, v170, v171\n"
      "v_mfma_f32_16x16x32_bf16 v[156:159], v[180:183], a[100:103], v[156:159]\n"
      "ds_read_u16 v52, v134\n"
      "v_mul_f32_e64 v240, v211, v240\n"
      "v_mfma_f32_16x16x32_bf16 v[156:159], v[136:139], a[136:139], v[156:159]\n"
      "ds_read_u16 v56, v134 offset:32\n"
      "v_mul_f32_e64 v241, v211, v241\n"
      "v_mfma_f32_16x16x32_bf16 v[172:175], v[180:183], a[104:107], v[172:175]\n"
      "ds_read_b128 v[140:143], v133\n"
      "v_cvt_pk_bf16_f32 v170, v240, v241\n"
      "v_mfma_f32_16x16x32_bf16 v[172:175], v[136:139], a[140:143], v[172:175]\n"
      "ds_read_b64 v[150:151], v133 offset:16\n"
      "v_mul_f32 v242, v211, v242\n"
      "v_mfma_f32_16x16x32_bf16 v[184:187], a[144:147], v[224:227], v[184:187]\n"
      "ds_read_u16 v60, v134 offset:64\n"
      "v_mul_f32 v243, v211, v243\n"
      "v_mfma_f32_16x16x32_bf16 v[184:187], a[180:183], v[232:235], v[184:187]\n"
      "ds_read_b128 v[136:139], v123 offset:18432\n"
      "v_cvt_pk_bf16_f32 v171, v242, v243\n"
      "v_mfma_f32_16x16x32_bf16 v[244:247], a[148:151], v[224:227], v[244:247]\n"
      "v_mul_f32 v213, v211, v213\n"
      "v_mul_f32 v212, v211, v212\n"
      "v_mfma_f32_16x16x32_bf16 v[244:247], a[184:187], v[232:235], v[244:247]\n"
      "v_mul_f32_e64 v214, v211, v214\n"
      "v_cvt_pk_bf16_f32 v40, v212, v213\n"
      "v_mfma_f32_16x16x32_bf16 v[196:199], a[152:155], v[224:227], v[196:199]\n"
      "ds_read_b128 v[240:243], v123 offset:21504\n"
      "v_mul_f32 v215, v211, v215\n"
      "v_mfma_f32_16x16x32_bf16 v[196:199], a[188:191], v[232:235], v[196:199]\n"
      "v_mul_f32 v209, s36, v209\n"
      "v_cvt_pk_bf16_f32 v41, v214, v215\n"
      "v_mfma_f32_16x16x32_bf16 v[248:251], a[156:159], v[224:227], v[248:251]\n"
      "v_mul_f32 v176, v209, v176\n"
      "v_mul_f32 v177, v209, v177\n"
      "v_mfma_f32_16x16x32_bf16 v[248:251], a[192:195], v[232:235], v[248:251]\n"
      "v_cvt_pk_bf16_f32 v176, v176, v177\n"
      "v_mul_f32 v178, v209, v178\n"
      "v_mfma_f32_16x16x32_bf16 v[188:191], a[160:163], v[224:227], v[188:191]\n"
      "v_mul_f32 v179, v209, v179\n"
      "v_cvt_pk_bf16_f32 v177, v178, v179\n"
      "v_mfma_f32_16x16x32_bf16 v[188:191], a[196:199], v[232:235], v[188:191]\n"
      "v_mul_f32 v153, v209, v153\n"
      "v_mul_f32 v152, v209, v152\n"
      "v_mfma_f32_16x16x32_bf16 v[228:231], a[164:167], v[224:227], v[228:231]\n"
      "s_or_b64 s[34:35], s[48:49], s[50:51]\n"
      "v_mul_f32 v202, v209, v202\n"
      "v_mfma_f32_16x16x32_bf16 v[228:231], a[200:203], v[232:235], v[228:231]\n"
      "ds_read_b128 v[212:215], v123 offset:24576\n"
      "v_mul_f32 v154, v209, v154\n"
      "v_mfma_f32_16x16x32_bf16 v[160:163], v[224:227], a[168:171], v[160:163]\n"
      "v_mul_f32 v155, v209, v155\n"
      "v_mul_f32_e64 v201, v209, v201\n"
      "v_mfma_f32_16x16x32_bf16 v[160:163], v[232:235], a[204:207], v[160:163]\n"
      "v_cvt_pk_bf16_f32 v179, v154, v155\n"
      "v_mul_f32 v200, v209, v200\n"
      "v_mfma_f32_16x16x32_bf16 v[156:159], v[224:227], a[172:175], v[156:159]\n"
      "s_waitcnt lgkmcnt(1)\n"
      "v_cvt_pk_bf16_f32 v178, v152, v153\n"
      "v_cvt_pk_bf16_f32 v38, v200, v201\n"
      "v_mfma_f32_16x16x32_bf16 v[156:159], v[232:235], a[208:211], v[156:159]\n"
      "ds_read_b128 v[144:147], v123 offset:27648\n"
      "v_mul_f32_e64 v203, v209, v203\n"
      "v_mfma_f32_16x16x32_bf16 v[172:175], v[224:227], a[176:179], v[172:175]\n"
      "v_fma_f32 v226, v248, v248, s38\n"
      "v_cvt_pk_bf16_f32 v39, v202, v203\n"
      "v_mfma_f32_16x16x32_bf16 v[172:175], v[232:235], a[212:215], v[172:175]\n"
      "v_fmac_f32 v226, v249, v249\n"
      "v_fmac_f32 v226, v250, v250\n"
      "v_mfma_f32_16x16x32_bf16 v[232:235], a[216:219], v[136:139], 0\n"
      "v_fma_f32 v225, v184, v184, s38\n"
      "v_fmac_f32 v225, v185, v185\n"
      "v_mfma_f32_16x16x32_bf16 v[232:235], a[228:231], v[240:243], v[232:235]\n"
      "v_fmac_f32 v225, v186, v186\n"
      "v_cvt_pk_bf16_f32 v50, v160, v161\n"
      "v_mfma_f32_16x16x32_bf16 v[152:155], a[220:223], v[136:139], 0\n"
      "v_fmac_f32 v226, v251, v251\n"
      "v_fmac_f32 v225, v187, v187\n"
      "v_mfma_f32_16x16x32_bf16 v[152:155], a[232:235], v[240:243], v[152:155]\n"
      "v_cvt_pk_bf16_f32 v55, v158, v159\n"
      "v_fmac_f32 v225, v244, v244\n"
      "v_fmac_f32 v226, v188, v188\n"
      "v_mfma_f32_16x16x32_bf16 v[180:183], a[224:227], v[136:139], 0\n"
      "v_fmac_f32 v225, v245, v245\n"
      "v_fmac_f32 v226, v189, v189\n"
      "v_mfma_f32_16x16x32_bf16 v[180:183], a[236:239], v[240:243], v[180:183]\n"
      "s_waitcnt lgkmcnt(0)\n"
      "v_fmac_f32 v225, v246, v246\n"
      "v_fmac_f32 v226, v190, v190\n"
      "v_mfma_f32_16x16x32_bf16 v[232:235], a[240:243], v[212:215], v[232:235]\n"
      "ds_read_b128 v[240:243], v123 offset:30720\n"
      "v_fmac_f32 v225, v247, v247\n"
      "v_mfma_f32_16x16x32_bf16 v[232:235], a[252:255], v[144:147], v[232:235]\n"
      "ds_read_b128 v[200:203], v123 offset:33792\n"
      "v_fmac_f32 v226, v191, v191\n"
      "v_mfma_f32_16x16x32_bf16 v[152:155], a[244:247], v[212:215], v[152:155]\n"
      "v_fmac_f32 v225, v196, v196\n"
      "v_fmac_f32_e64 v226, v228, v228\n"
      "v_mfma_f32_16x16x32_bf16 v[152:155], v[0:3], v[144:147], v[152:155]\n"
      "v_fmac_f32 v225, v197, v197\n"
      "v_fmac_f32 v226, v229, v229\n"
      "v_mfma_f32_16x16x32_bf16 v[180:183], a[248:251], v[212:215], v[180:183]\n"
      "v_fmac_f32 v225, v198, v198\n"
      "v_fmac_f32 v226, v230, v230\n"
      "v_mfma_f32_16x16x32_bf16 v[180:183], v[4:7], v[144:147], v[180:183]\n"
      "v_fmac_f32 v225, v199, v199\n"
      "v_fmac_f32 v226, v231, v231\n"
      "v_mfma_f32_16x16x32_bf16 v[212:215], v[164:167], v[204:207], v[78:81]\n"
      "s_nop 0\n"
      "v_permlane16_swap_b32 v225, v226\n"
      "v_mfma_f32_16x16x32_bf16 v[212:215], v[34:37], v[32:35], v[212:215]\n"
      "v_add_f32 v225, v225, v226\n"
      "v_mov_b32 v226, v225\n"
      "v_mfma_f32_16x16x32_bf16 v[144:147], v[168:171], v[204:207], v[82:85]\n"
      "s_nop 0\n"
      "v_permlane32_swap_b32 v225, v226\n"
      "v_add_f32 v225, v225, v226\n"
      "v_mfma_f32_16x16x32_bf16 v[144:147], v[40:43], v[32:35], v[144:147]\n"
      "s_waitcnt lgkmcnt(0)\n"
      "v_rsq_f32 v225, v225\n"
      "v_mfma_f32_16x16x32_bf16 v[232:235], v[8:11], v[240:243], v[232:235]\n"
      "v_mov_b32 v226, v225\n"
      "v_cvt_pk_bf16_f32 v51, v162, v163\n"
      "v_mfma_f32_16x16x32_bf16 v[232:235], v[20:23], v[200:203], v[232:235]\n"
      "v_permlane16_swap_b32 v225, v226\n"
      "v_mul_f32 v188, v226, v188\n"
      "v_mul_f32 v230, v226, v230\n"
      "v_mul_f32 v250, v226, v250\n"
      "v_mul_f32 v189, v226, v189\n"
      "v_mul_f32 v225, s36, v225\n"
      "v_mfma_f32_16x16x32_bf16 v[152:155], v[12:15], v[240:243], v[152:155]\n"
      "v_mul_f32 v248, v226, v248\n"
      "v_mul_f32 v249, v226, v249\n"
      "v_mfma_f32_16x16x32_bf16 v[152:155], v[24:27], v[200:203], v[152:155]\n"
      "v_cvt_pk_bf16_f32 v248, v248, v249\n"
      "v_mul_f32 v228, v226, v228\n"
      "v_mfma_f32_16x16x32_bf16 v[180:183], v[16:19], v[240:243], v[180:183]\n"
      "v_mul_f32 v251, v226, v251\n"
      "v_cvt_pk_bf16_f32 v249, v250, v251\n"
      "v_mfma_f32_16x16x32_bf16 v[180:183], v[28:31], v[200:203], v[180:183]\n"
      "v_mul_f32_e64 v231, v226, v231\n"
      "v_cvt_pk_bf16_f32 v47, v230, v231\n"
      "v_mfma_f32_16x16x32_bf16 v[240:243], v[192:195], v[204:207], v[70:73]\n"
      "v_cvt_pk_bf16_f32 v250, v188, v189\n"
      "v_mul_f32 v190, v226, v190\n"
      "v_mfma_f32_16x16x32_bf16 v[240:243], v[140:143], v[164:167], v[240:243]\n"
      "v_mul_f32 v191, v226, v191\n"
      "v_cvt_pk_bf16_f32 v251, v190, v191\n"
      "v_mfma_f32_16x16x32_bf16 v[240:243], v[148:151], v[32:35], v[240:243]\n"
      "v_mul_f32 v184, v225, v184\n"
      "v_mul_f32 v229, v226, v229\n"
      "v_cvt_pk_bf16_f32 v46, v228, v229\n"
      "v_mfma_f32_16x16x32_bf16 v[200:203], v[248:251], v[204:207], v[86:89]\n"
      "s_waitcnt vmcnt(2)\n"
      "v_mul_f32 v185, v225, v185\n"
      "v_cvt_pk_bf16_f32 v184, v184, v185\n"
      "v_mfma_f32_16x16x32_bf16 v[200:203], v[46:49], v[32:35], v[200:203]\n"
      "ds_read_b32 v243, v122 offset:4864\n"
      "ds_read_b128 v[188:191], v119 offset:41728\n"
      "v_fma_f32 v159, v232, v232, s39\n"
      "v_mul_f32 v187, v225, v187\n"
      "v_mul_f32 v199, v225, v199\n"
      "v_fmac_f32_e64 v159, v233, v233\n"
      "v_cvt_pk_bf16_f32 v59, v174, v175\n"
      "v_mul_f32 v246, v225, v246\n"
      "v_fmac_f32 v159, v234, v234\n"
      "v_mul_f32 v247, v225, v247\n"
      "v_mul_f32 v186, v225, v186\n"
      "v_mul_f32 v244, v225, v244\n"
      "v_fmac_f32 v159, v235, v235\n"
      "v_fmac_f32 v159, v152, v152\n"
      "v_fmac_f32 v159, v153, v153\n"
      "v_mul_f32 v197, v225, v197\n"
      "v_fmac_f32 v159, v154, v154\n"
      "v_fmac_f32 v159, v155, v155\n"
      "v_mul_f32 v245, v225, v245\n"
      "v_mul_f32 v196, v225, v196\n"
      "v_fmac_f32 v159, v180, v180\n"
      "v_cvt_pk_bf16_f32 v44, v196, v197\n"
      "v_mul_f32_e64 v198, v225, v198\n"
      "v_cvt_pk_bf16_f32 v45, v198, v199\n"
      "v_cvt_pk_bf16_f32 v58, v172, v173\n"
      "v_fmac_f32_e64 v159, v181, v181\n"
      "v_cvt_pk_bf16_f32 v185, v186, v187\n"
      "v_cvt_pk_bf16_f32 v187, v246, v247\n"
      "v_cvt_pk_bf16_f32 v54, v156, v157\n"
      "v_fmac_f32 v159, v182, v182\n"
      "v_fmac_f32_e64 v159, v183, v183\n"
      "v_cvt_pk_bf16_f32 v186, v244, v245\n"
      "s_waitcnt lgkmcnt(0)\n"
      "v_or3_b32 v188, v188, v189, v190\n"
      "v_and_b32 v188, s40, v188\n"
      "v_bitop3_b32 v243, v243, v191, s40 bitop3:0xa8\n"
      "v_or3_b32 v188, v188, v243, s4\n"
      "ds_write_b32 v129, v188 offset:3072\n"
      "s_cbranch_scc1 L_gen_28_%=\n"
      "L_fast_31_%=:\n"
      "v_mfma_f32_16x16x32_bf16 v[196:199], v[164:167], v[176:179], v[90:93]\n"
      "ds_read_b128 v[244:247], v119 offset:30720\n"
      "v_exp_f32 v200, v200\n"
      "v_exp_f32 v146, v146\n"
      "v_mfma_f32_16x16x32_bf16 v[196:199], v[34:37], v[38:41], v[196:199]\n"
      "ds_read_b128 v[204:207], v123 offset:19456\n"
      "v_exp_f32 v213, v213\n"
      "v_mfma_f32_16x16x32_bf16 v[188:191], v[168:171], v[176:179], v[94:97]\n"
      "ds_read_b128 v[160:163], v123 offset:22528\n"
      "v_exp_f32 v202, v202\n"
      "v_mfma_f32_16x16x32_bf16 v[188:191], v[40:43], v[38:41], v[188:191]\n"
      "ds_read_b128 v[172:175], v128 offset:3072\n"
      "v_exp_f32 v203, v203\n"
      "v_exp_f32 v215, v215\n"
      "v_exp_f32_e64 v212, v212\n"
      "v_mfma_f32_16x16x32_bf16 v[228:231], v[192:195], v[176:179], v[72:75]\n"
      "ds_read_b128 v[136:139], v123 offset:25600\n"
      "v_cvt_pk_bf16_f32 v212, v212, v213\n"
      "v_mfma_f32_16x16x32_bf16 v[228:231], v[140:143], v[168:171], v[228:231]\n"
      "v_exp_f32 v214, v214\n"
      "v_mfma_f32_16x16x32_bf16 v[228:231], v[148:151], v[38:41], v[228:231]\n"
      "ds_read_b128 v[208:211], v123 offset:28672\n"
      "v_exp_f32 v147, v147\n"
      "v_exp_f32 v145, v145\n"
      "v_exp_f32 v240, v240\n"
      "v_mfma_f32_16x16x32_bf16 v[224:227], v[248:251], v[176:179], v[98:101]\n"
      "s_waitcnt lgkmcnt(4)\n"
      "v_exp_f32 v201, v201\n"
      "v_mfma_f32_16x16x32_bf16 v[224:227], v[46:49], v[38:41], v[224:227]\n"
      "s_waitcnt lgkmcnt(3)\n"
      "v_exp_f32 v144, v144\n"
      "v_mfma_f32_16x16x32_bf16 v[176:179], a[216:219], v[204:207], 0\n"
      "global_store_dwordx4 v116, v[244:247], s[54:55] offset:256 sc1\n"
      "v_exp_f32 v196, v196\n"
      "v_mfma_f32_16x16x32_bf16 v[176:179], a[228:231], v[160:163], v[176:179]\n"
      "s_waitcnt lgkmcnt(2)\n"
      "v_cvt_pk_bf16_f32 v62, v200, v201\n"
      "v_cvt_pk_bf16_f32 v63, v202, v203\n"
      "v_cvt_pk_bf16_f32 v213, v214, v215\n"
      "v_mfma_f32_16x16x32_bf16 v[244:247], a[220:223], v[204:207], 0\n"
      "v_bitop3_b32 v174, v174, v175, s4 bitop3:0x7e\n"
      "v_bitop3_b32 v172, v172, v173, s4 bitop3:0x7e\n"
      "v_mfma_f32_16x16x32_bf16 v[244:247], a[232:235], v[160:163], v[244:247]\n"
      "s_waitcnt lgkmcnt(0)\n"
      "v_or_b32 v172, v172, v174\n"
      "v_cmp_ne_u32 vcc, 0, v172\n"
      "v_mfma_f32_16x16x32_bf16 v[200:203], a[224:227], v[204:207], 0\n"
      "ds_read_b128 v[204:207], v123 offset:31744\n"
      "s_mov_b64 s[48:49], vcc\n"
      "v_mfma_f32_16x16x32_bf16 v[200:203], a[236:239], v[160:163], v[200:203]\n"
      "ds_read_b128 v[160:163], v123 offset:34816\n"
      "v_exp_f32 v197, v197\n"
      "v_exp_f32 v199, v199\n"
      "v_cvt_pk_bf16_f32 v214, v144, v145\n"
      "v_cvt_pk_bf16_f32 v215, v146, v147\n"
      "v_mfma_f32_16x16x32_bf16 v[176:179], a[240:243], v[136:139], v[176:179]\n"
      "v_exp_f32 v189, v189\n"
      "v_mfma_f32_16x16x32_bf16 v[176:179], a[252:255], v[208:211], v[176:179]\n"
      "ds_read_b128 v[144:147], v120 offset:18432\n"
      "v_cvt_pk_bf16_f32 v196, v196, v197\n"
      "v_exp_f32 v191, v191\n"
      "v_exp_f32 v188, v188\n"
      "v_mfma_f32_16x16x32_bf16 v[244:247], a[244:247], v[136:139], v[244:247]\n"
      "v_exp_f32 v198, v198\n"
      "v_mfma_f32_16x16x32_bf16 v[244:247], v[0:3], v[208:211], v[244:247]\n"
      "s_cbranch_vccnz L_flg_32_%=\n"
      "L_fb_33_%=:\n"
      "v_mfma_f32_16x16x32_bf16 v[200:203], a[248:251], v[136:139], v[200:203]\n"
      "ds_read_b128 v[136:139], v123 offset:20480\n"
      "v_mfma_f32_16x16x32_bf16 v[200:203], v[4:7], v[208:211], v[200:203]\n"
      "s_waitcnt lgkmcnt(2)\n"
      "v_cvt_pk_bf16_f32 v197, v198, v199\n"
      "v_cvt_pk_bf16_f32 v64, v240, 0\n"
      "v_mfma_f32_16x16x32_bf16 v[176:179], v[8:11], v[204:207], v[176:179]\n"
      "v_exp_f32_e64 v190, v190\n"
      "v_mfma_f32_16x16x32_bf16 v[176:179], v[20:23], v[160:163], v[176:179]\n"
      "ds_read_b128 v[208:211], v123 offset:23552\n"
      "v_cvt_pk_bf16_f32 v198, v188, v189\n"
      "v_mfma_f32_16x16x32_bf16 v[244:247], v[12:15], v[204:207], v[244:247]\n"
      "s_waitcnt lgkmcnt(2)\n"
      "v_exp_f32 v224, v224\n"
      "v_mfma_f32_16x16x32_bf16 v[244:247], v[24:27], v[160:163], v[244:247]\n"
      "global_store_dwordx4 v117, v[144:147], s[54:55] sc1\n"
      "v_mfma_f32_16x16x32_bf16 v[200:203], v[16:19], v[204:207], v[200:203]\n"
      "v_cvt_pk_bf16_f32 v199, v190, v191\n"
      "v_fma_f32 v156, v176, v176, s39\n"
      "v_mfma_f32_16x16x32_bf16 v[200:203], v[28:31], v[160:163], v[200:203]\n"
      "v_exp_f32_e64 v225, v225\n"
      "v_mfma_f32_16x16x32_bf16 v[188:191], v[66:69], v[212:215], 0\n"
      "v_exp_f32 v226, v226\n"
      "v_mfma_f32_16x16x32_bf16 v[188:191], v[66:69], v[62:65], v[188:191]\n"
      "v_exp_f32 v227, v227\n"
      "v_mfma_f32_16x16x32_bf16 v[204:207], v[220:223], v[212:215], 0\n"
      "v_exp_f32 v228, v228\n"
      "v_mfma_f32_16x16x32_bf16 v[204:207], v[50:53], v[62:65], v[204:207]\n"
      "v_fmac_f32 v156, v177, v177\n"
      "v_fmac_f32 v156, v178, v178\n"
      "v_mfma_f32_16x16x32_bf16 v[160:163], v[236:239], v[212:215], 0\n"
      "v_fmac_f32 v156, v179, v179\n"
      "v_fmac_f32 v156, v244, v244\n"
      "v_mfma_f32_16x16x32_bf16 v[160:163], v[54:57], v[62:65], v[160:163]\n"
      "v_fmac_f32 v156, v245, v245\n"
      "v_fmac_f32 v156, v246, v246\n"
      "v_mfma_f32_16x16x32_bf16 v[144:147], v[252:255], v[212:215], 0\n"
      "v_fmac_f32 v156, v247, v247\n"
      "v_fmac_f32 v156, v200, v200\n"
      "v_mfma_f32_16x16x32_bf16 v[144:147], v[58:61], v[62:65], v[144:147]\n"
      "v_fmac_f32 v156, v201, v201\n"
      "v_fmac_f32 v156, v202, v202\n"
      "v_mfma_f32_16x16x32_bf16 v[216:219], v[164:167], v[184:187], v[102:105]\n"
      "v_fmac_f32 v156, v203, v203\n"
      "v_cvt_pk_bf16_f32 v62, v224, v225\n"
      "v_mfma_f32_16x16x32_bf16 v[216:219], v[34:37], v[44:47], v[216:219]\n"
      "ds_read_b128 v[212:215], v121 offset:18432\n"
      "v_cvt_pk_bf16_f32 v63, v226, v227\n"
      "v_mfma_f32_16x16x32_bf16 v[164:167], v[168:171], v[184:187], v[106:109]\n"
      "v_cndmask_b32 v241, v241, v229, s[42:43]\n"
      "v_cvt_pk_bf16_f32 v64, v228, 0\n"
      "v_mfma_f32_16x16x32_bf16 v[164:167], v[40:43], v[44:47], v[164:167]\n"
      "v_rcp_f32 v188, v188\n"
      "v_mfma_f32_16x16x32_bf16 v[228:231], v[192:195], v[184:187], v[74:77]\n"
      "s_waitcnt lgkmcnt(1)\n"
      "v_mul_f32 v205, v188, v205\n"
      "v_mul_f32 v204, v188, v204\n"
      "v_mfma_f32_16x16x32_bf16 v[228:231], v[140:143], v[248:251], v[228:231]\n"
      "v_cvt_pk_bf16_f32 v204, v204, v205\n"
      "v_mul_f32 v206, v188, v206\n"
      "v_mfma_f32_16x16x32_bf16 v[228:231], v[148:151], v[44:47], v[228:231]\n"
      "v_mul_f32 v207, v188, v207\n"
      "v_cvt_pk_bf16_f32 v205, v206, v207\n"
      "v_mfma_f32_16x16x32_bf16 v[224:227], v[248:251], v[184:187], v[110:113]\n"
      "ds_read_b128 v[168:171], v123 offset:26624\n"
      "v_mul_f32 v160, v188, v160\n"
      "v_mfma_f32_16x16x32_bf16 v[224:227], v[46:49], v[44:47], v[224:227]\n"
      "v_mul_f32 v161, v188, v161\n"
      "v_cvt_pk_bf16_f32 v206, v160, v161\n"
      "v_mfma_f32_16x16x32_bf16 v[184:187], v[140:143], v[192:195], v[76:79]\n"
      "ds_read_b128 v[192:195], v123 offset:29696\n"
      "v_mul_f32_e64 v162, v188, v162\n"
      "v_mfma_f32_16x16x32_bf16 v[140:143], v[66:69], v[196:199], 0\n"
      "ds_read_b128 v[248:251], v123 offset:32768\n"
      "v_mul_f32 v163, v188, v163\n"
      "v_mfma_f32_16x16x32_bf16 v[140:143], v[66:69], v[62:65], v[140:143]\n"
      "s_waitcnt lgkmcnt(3)\n"
      "v_mul_f32_e64 v144, v188, v144\n"
      "v_cvt_pk_bf16_f32 v207, v162, v163\n"
      "v_mfma_f32_16x16x16_bf16 v[184:187], v[150:151], v[148:149], v[184:187]\n"
      "ds_write_b128 v124, v[204:207]\n"
      "v_mfma_f32_16x16x32_bf16 v[204:207], v[220:223], v[196:199], 0\n"
      "v_mul_f32 v145, v188, v145\n"
      "v_cvt_pk_bf16_f32 v144, v144, v145\n"
      "v_mfma_f32_16x16x32_bf16 v[204:207], v[50:53], v[62:65], v[204:207]\n"
      "s_mov_b32 exec_hi, 0\n"
      "global_store_dwordx4 v118, v[212:215], s[54:55] sc1\n"
      "s_mov_b32 exec_hi, -1\n"
      "v_mfma_f32_16x16x32_bf16 v[148:151], v[236:239], v[196:199], 0\n"
      "s_add_u32 s28, s27, 2\n"
      "v_mul_f32_e64 v146, v188, v146\n"
      "v_mul_f32 v147, v188, v147\n"
      "v_mfma_f32_16x16x32_bf16 v[148:151], v[54:57], v[62:65], v[148:151]\n"
      "s_waitcnt lgkmcnt(2)\n"
      "v_cndmask_b32 v241, v241, v229, s[44:45]\n"
      "v_cvt_pk_bf16_f32 v145, v146, v147\n"
      "v_mfma_f32_16x16x32_bf16 v[160:163], v[252:255], v[196:199], 0\n"
      "s_min_u32 m0, s28, s3\n"
      "v_cndmask_b32 v241, v241, v185, s[46:47]\n"
      "s_movrels_b32 s9, s80\n"
      "v_mfma_f32_16x16x32_bf16 v[160:163], v[58:61], v[62:65], v[160:163]\n"
      "s_lshr_b32 s8, s9, 8\n"
      "v_exp_f32 v241, v241\n"
      "v_mfma_f32_16x16x32_bf16 v[184:187], a[216:219], v[136:139], 0\n"
      "s_and_b32 s9, s9, 0xff\n"
      "v_cvt_pk_bf16_f32 v241, v241, v241\n"
      "s_mul_i32 s10, s8, 73\n"
      "v_mfma_f32_16x16x32_bf16 v[184:187], a[228:231], v[208:211], v[184:187]\n"
      "ds_write_b16 v130, v241\n"
      "v_mfma_f32_16x16x32_bf16 v[212:215], a[220:223], v[136:139], 0\n"
      "s_add_u32 s10, s10, s9\n"
      "ds_read_b128 v[196:199], v131\n"
      "v_mfma_f32_16x16x32_bf16 v[212:215], a[232:235], v[208:211], v[212:215]\n"
      "ds_read_b128 v[172:175], v131 offset:64\n"
      "v_exp_f32 v165, v165\n"
      "v_exp_f32 v216, v216\n"
      "v_exp_f32 v166, v166\n"
      "v_exp_f32 v218, v218\n"
      "v_mfma_f32_16x16x32_bf16 v[184:187], a[240:243], v[168:171], v[184:187]\n"
      "s_mul_i32 s28, s10, 4802\n"
      "v_exp_f32 v217, v217\n"
      "v_mfma_f32_16x16x32_bf16 v[184:187], a[252:255], v[192:195], v[184:187]\n"
      "s_add_u32 s56, s18, s28\n"
      "v_exp_f32 v219, v219\n"
      "v_mfma_f32_16x16x32_bf16 v[212:215], a[244:247], v[168:171], v[212:215]\n"
      "s_addc_u32 s57, s19, 0\n"
      "s_and_b32 s56, s56, -16\n"
      "v_cvt_pk_bf16_f32 v216, v216, v217\n"
      "v_mfma_f32_16x16x32_bf16 v[212:215], v[0:3], v[192:195], v[212:215]\n"
      "s_sub_u32 s58, s56, 110592\n"
      "v_exp_f32 v164, v164\n"
      "v_cvt_pk_bf16_f32 v217, v218, v219\n"
      "v_cvt_pk_bf16_f32 v218, v164, v165\n"
      "v_mfma_f32_16x16x32_bf16 v[188:191], a[224:227], v[136:139], 0\n"
      "ds_read_b128 v[240:243], v123 offset:35840\n"
      "s_subb_u32 s59, s57, 0\n"
      "v_mfma_f32_16x16x32_bf16 v[188:191], a[236:239], v[208:211], v[188:191]\n"
      "s_waitcnt lgkmcnt(1)\n"
      "v_exp_f32 v167, v167\n"
      "v_mfma_f32_16x16x32_bf16 v[188:191], a[248:251], v[168:171], v[188:191]\n"
      "ds_read_b128 v[168:171], v114 offset:18432\n"
      "v_cvt_pk_bf16_f32 v219, v166, v167\n"
      "v_mfma_f32_16x16x32_bf16 v[188:191], v[4:7], v[192:195], v[188:191]\n"
      "ds_read_b128 v[192:195], v115 offset:18432\n"
      "v_exp_f32_e64 v224, v224\n"
      "v_mfma_f32_16x16x32_bf16 v[136:139], v[196:199], v[66:69], 0\n"
      "s_sub_u32 s56, s56, 73728\n"
      "v_exp_f32 v225, v225\n"
      "v_mfma_f32_16x16x32_bf16 v[136:139], v[172:175], v[66:69], v[136:139]\n"
      "s_subb_u32 s57, s57, 0\n"
      "v_exp_f32 v226, v226\n"
      "v_mfma_f32_16x16x32_bf16 v[208:211], v[196:199], v[220:223], 0\n"
      "s_mul_i32 s28, s8, 1373568\n"
      "v_exp_f32 v227, v227\n"
      "v_mfma_f32_16x16x32_bf16 v[208:211], v[172:175], v[50:53], v[208:211]\n"
      "s_mul_i32 s29, s9, 2688\n"
      "v_exp_f32 v228, v228\n"
      "v_mfma_f32_16x16x32_bf16 v[164:167], v[196:199], v[236:239], 0\n"
      "s_waitcnt lgkmcnt(2)\n"
      "v_cvt_pk_bf16_f32 v63, v226, v227\n"
      "v_cvt_pk_bf16_f32 v62, v224, v225\n"
      "v_mfma_f32_16x16x32_bf16 v[164:167], v[172:175], v[54:57], v[164:167]\n"
      "s_add_u32 s28, s28, s29\n"
      "v_rcp_f32 v140, v140\n"
      "v_mfma_f32_16x16x32_bf16 v[184:187], v[8:11], v[248:251], v[184:187]\n"
      "s_cmp_eq_u32 s8, 72\n"
      "v_mul_f32 v204, v140, v204\n"
      "v_mul_f32 v205, v140, v205\n"
      "v_mfma_f32_16x16x32_bf16 v[184:187], v[20:23], v[240:243], v[184:187]\n"
      "s_waitcnt lgkmcnt(0)\n"
      "v_mul_f32 v149, v140, v149\n"
      "v_mul_f32 v206, v140, v206\n"
      "v_cvt_pk_bf16_f32 v204, v204, v205\n"
      "v_mfma_f32_16x16x32_bf16 v[212:215], v[12:15], v[248:251], v[212:215]\n"
      "s_cselect_b32 s8, 0, 100270464\n"
      "v_mul_f32 v207, v140, v207\n"
      "v_cvt_pk_bf16_f32 v205, v206, v207\n"
      "v_mfma_f32_16x16x32_bf16 v[212:215], v[24:27], v[240:243], v[212:215]\n"
      "s_cmp_eq_u32 s9, 72\n"
      "v_cvt_pk_bf16_f32 v64, v228, 0\n"
      "v_mul_f32 v148, v140, v148\n"
      "v_mfma_f32_16x16x32_bf16 v[188:191], v[16:19], v[248:251], v[188:191]\n"
      "s_cselect_b32 s9, 0, 196224\n"
      "v_mul_f32 v150, v140, v150\n"
      "v_cvt_pk_bf16_f32 v206, v148, v149\n"
      "v_mfma_f32_16x16x32_bf16 v[188:191], v[28:31], v[240:243], v[188:191]\n"
      "s_add_u32 s10, s8, s9\n"
      "v_mul_f32 v151, v140, v151\n"
      "v_cvt_pk_bf16_f32 v207, v150, v151\n"
      "v_cndmask_b32 v208, v208, v164, s[42:43]\n"
      "v_mfma_f32_16x16x32_bf16 v[148:151], v[66:69], v[216:219], 0\n"
      "s_add_u32 s28, s28, s10\n"
      "v_mul_f32 v161, v140, v161\n"
      "v_mul_f32_e64 v160, v140, v160\n"
      "v_mfma_f32_16x16x32_bf16 v[148:151], v[66:69], v[62:65], v[148:151]\n"
      "ds_write_b128 v124, v[204:207] offset:1024\n"
      "v_mfma_f32_16x16x32_bf16 v[248:251], v[220:223], v[216:219], 0\n"
      "s_add_u32 s52, s16, s28\n"
      "v_mul_f32 v162, v140, v162\n"
      "v_cvt_pk_bf16_f32 v160, v160, v161\n"
      "v_mfma_f32_16x16x32_bf16 v[248:251], v[50:53], v[62:65], v[248:251]\n"
      "s_addc_u32 s53, s17, 0\n"
      "v_mul_f32 v163, v140, v163\n"
      "v_cvt_pk_bf16_f32 v161, v162, v163\n"
      "v_mfma_f32_16x16x32_bf16 v[240:243], v[236:239], v[216:219], 0\n"
      "s_add_u32 s27, s27, 1\n"
      "v_fma_f32 v147, v184, v184, s39\n"
      "v_fmac_f32 v147, v185, v185\n"
      "v_mfma_f32_16x16x32_bf16 v[240:243], v[54:57], v[62:65], v[240:243]\n"
      "ds_write2st64_b64 v125, v[144:145], v[160:161] offset0:0 offset1:2\n"
      "v_mov_b32_dpp v136, v136 quad_perm:[0,0,0,0] row_mask:0xf bank_mask:0xf\n"
      "v_mfma_f32_16x16x32_bf16 v[204:207], v[252:255], v[216:219], 0\n"
      "s_cmp_ge_u32 s27, s26\n"
      "v_fmac_f32 v147, v186, v186\n"
      "v_fmac_f32_e64 v147, v187, v187\n"
      "v_mfma_f32_16x16x32_bf16 v[204:207], v[58:61], v[62:65], v[204:207]\n"
      "s_cselect_b32 s28, 1, 0\n"
      "v_rcp_f32 v136, v136\n"
      "v_fmac_f32 v147, v212, v212\n"
      "v_fmac_f32 v147, v213, v213\n"
      "v_mfma_f32_16x16x32_bf16 v[236:239], v[196:199], v[252:255], 0\n"
      "s_xor_b32 s29, s10, s11\n"
      "v_fmac_f32 v147, v214, v214\n"
      "v_fmac_f32 v147, v215, v215\n"
      "v_mfma_f32_16x16x32_bf16 v[236:239], v[172:175], v[58:61], v[236:239]\n"
      "v_fmac_f32 v147, v188, v188\n"
      "v_fmac_f32_e64 v147, v189, v189\n"
      "v_mfma_f32_16x16x32_bf16 v[196:199], a[0:3], v[168:171], 0\n"
      "v_fmac_f32 v147, v190, v190\n"
      "v_fmac_f32 v147, v191, v191\n"
      "v_mfma_f32_16x16x32_bf16 v[196:199], a[36:39], v[192:195], v[196:199]\n"
      "v_permlane16_swap_b32 v159, v156\n"
      "v_mov_b32 v138, v147\n"
      "v_mfma_f32_16x16x32_bf16 v[228:231], a[4:7], v[168:171], 0\n"
      "s_or_b32 s28, s28, s29\n"
      "v_permlane16_swap_b32 v147, v138\n"
      "v_add_f32 v159, v159, v156\n"
      "v_add_f32 v147, v147, v138\n"
      "v_cndmask_b32 v208, v208, v236, s[44:45]\n"
      "v_mul_f32 v208, v136, v208\n"
      "v_permlane32_swap_b32 v159, v147\n"
      "v_cvt_pk_bf16_f32 v208, v208, v208\n"
      "v_add_f32_e64 v159, v159, v147\n"
      "v_mfma_f32_16x16x32_bf16 v[228:231], a[40:43], v[192:195], v[228:231]\n"
      "ds_write_b32 v129, v159\n"
      "ds_write_b16 v135, v208\n"
      "L_join_30_%=:\n"
      "s_cbranch_scc0 L_loop_%=\n"
      "L_tail0_%=:\n"
      "s_cmp_eq_u32 s10, s11\n"
      "s_cbranch_scc1 L_sk_35_%=\n"
      "s_mov_b32 s11, s10\n"
      "s_sub_u32 s28, s60, s8\n"
      "s_sub_u32 s29, s61, s9\n"
      "s_mov_b32 s60, s8\n"
      "s_mov_b32 s61, s9\n"
      "s_mov_b64 exec, s[64:65]\n"
      "v_add_u32 v116, s28, v116\n"
      "s_mov_b64 exec, s[70:71]\n"
      "v_add_u32 v116, s29, v116\n"
      "s_mov_b64 exec, s[66:67]\n"
      "v_add_u32 v117, s28, v117\n"
      "s_mov_b64 exec, s[72:73]\n"
      "v_add_u32 v117, s29, v117\n"
      "s_mov_b64 exec, s[68:69]\n"
      "v_add_u32_e64 v118, s28, v118\n"
      "s_mov_b64 exec, s[74:75]\n"
      "v_add_u32 v118, s29, v118\n"
      "s_mov_b64 exec, -1\n"
      "s_nop 4\n"
      "L_sk_35_%=:\n"
      "s_cmp_lt_u32 s27, s26\n"
      "s_cbranch_scc1 L_loop_%=\n"
      "s_nop 7\n"
      "v_mbcnt_lo_u32_b32 v208, -1, 0\n"
      "v_mbcnt_hi_u32_b32 v208, -1, v208\n"
      "v_rcp_f32 v148, v148\n"
      "v_and_b32 v149, 15, v208\n"
      "v_pk_mul_f32 v[240:241], v[240:241], v[148:149] op_sel_hi:[1,0]\n"
      "v_pk_mul_f32 v[204:205], v[204:205], v[148:149] op_sel_hi:[1,0]\n"
      "v_cvt_pk_bf16_f32 v204, v204, v205\n"
      "v_pk_mul_f32 v[206:207], v[206:207], v[148:149] op_sel_hi:[1,0]\n"
      "v_cvt_pk_bf16_f32 v205, v206, v207\n"
      "v_pk_mul_f32 v[242:243], v[242:243], v[148:149] op_sel_hi:[1,0]\n"
      "v_lshrrev_b32_e64 v196, 4, v208\n"
      "v_pk_mul_f32 v[248:249], v[248:249], v[148:149] op_sel_hi:[1,0]\n"
      "v_mul_u32_u24 v196, 352, v196\n"
      "v_cvt_pk_bf16_f32 v248, v248, v249\n"
      "v_pk_mul_f32 v[250:251], v[250:251], v[148:149] op_sel_hi:[1,0]\n"
      "v_cvt_pk_bf16_f32 v249, v250, v251\n"
      "v_lshl_add_u32 v196, v149, 4, v196\n"
      "v_cvt_pk_bf16_f32 v251, v242, v243\n"
      "v_cvt_pk_bf16_f32 v250, v240, v241\n"
      "v_add_u32 v196, 150640, v196\n"
      "ds_write_b64 v125, v[204:205] offset:2048\n"
      "ds_write_b128 v124, v[248:251] offset:2048\n"
      "s_waitcnt lgkmcnt(0)\n"
      "s_barrier\n"
      "ds_read_b128 v[240:243], v123 offset:3072\n"
      "ds_read_b128 v[148:151], v123\n"
      "ds_read_b128 v[248:251], v128\n"
      "ds_read_b128 v[228:231], v120\n"
      "ds_read_b128 v[192:195], v119\n"
      "s_sub_u32 s28, s27, 3\n"
      "s_cselect_b64 s[54:55], s[12:13], s[22:23]\n"
      "ds_read_b128 v[172:175], v123 offset:6144\n"
      "ds_read_b128 v[236:239], v119 offset:12288\n"
      "s_max_i32 m0, s28, 0\n"
      "ds_read_b128 v[220:223], v123 offset:9216\n"
      "s_movrels_b32 s9, s80\n"
      "s_lshr_b32 s8, s9, 8\n"
      "s_and_b32 s9, s9, 0xff\n"
      "s_mul_i32 s28, s8, 1373568\n"
      "s_mul_i32 s29, s9, 2688\n"
      "ds_read_b128 v[168:171], v119 offset:6144\n"
      "s_add_u32 s28, s28, s29\n"
      "s_cmp_eq_u32 s8, 72\n"
      "ds_read_b128 v[160:163], v121\n"
      "s_cselect_b32 s8, 0, 100270464\n"
      "s_cmp_eq_u32 s9, 72\n"
      "s_cselect_b32 s9, 0, 196224\n"
      "s_add_u32 s10, s8, s9\n"
      "s_add_u32 s28, s28, s10\n"
      "s_waitcnt lgkmcnt(8)\n"
      "v_mfma_f32_16x16x32_bf16 v[204:207], a[216:219], v[148:151], 0\n"
      "s_add_u32 s54, s54, s28\n"
      "s_waitcnt lgkmcnt(7)\n"
      "v_add_f32 v250, |v250|, |v251|\n"
      "v_mfma_f32_16x16x32_bf16 v[204:207], a[228:231], v[240:243], v[204:207]\n"
      "ds_read_b128 v[156:159], v123 offset:12288\n"
      "v_add_f32 v248, |v248|, |v249|\n"
      "v_mfma_f32_16x16x32_bf16 v[164:167], a[220:223], v[148:151], 0\n"
      "ds_read_b128 v[144:147], v123 offset:15360\n"
      "v_add_f32 v248, v248, v250\n"
      "v_mfma_f32_16x16x32_bf16 v[164:167], a[232:235], v[240:243], v[164:167]\n"
      "ds_read_b128 v[250:253], v123 offset:1024\n"
      "v_rsq_f32 v248, v248\n"
      "v_mfma_f32_16x16x32_bf16 v[224:227], a[224:227], v[148:151], 0\n"
      "s_waitcnt lgkmcnt(5)\n"
      "v_mul_f32 v248, s41, v248\n"
      "v_mov_b32 v249, v248\n"
      "v_mfma_f32_16x16x32_bf16 v[224:227], a[236:239], v[240:243], v[224:227]\n"
      "s_addc_u32 s55, s55, 0\n"
      "v_permlane32_swap_b32 v248, v249\n"
      "v_mov_b32_e64 v254, v248\n"
      "v_mfma_f32_16x16x32_bf16 v[164:167], a[244:247], v[172:175], v[164:167]\n"
      "ds_read_b128 v[148:151], v123 offset:4096\n"
      "s_cmp_lg_u32 s10, s11\n"
      "v_permlane16_swap_b32_e64 v248, v254\n"
      "v_mfma_f32_16x16x32_bf16 v[204:207], a[240:243], v[172:175], v[204:207]\n"
      "s_cbranch_scc1 L_edge_36_%=\n"
      "L_eb_37_%=:\n"
      "v_mul_f32_e64 v233, v248, v233\n"
      "v_mfma_f32_16x16x32_bf16 v[204:207], a[252:255], v[220:223], v[204:207]\n"
      "global_store_dwordx4 v116, v[192:195], s[54:55] sc1\n"
      "v_mfma_f32_16x16x32_bf16 v[164:167], v[0:3], v[220:223], v[164:167]\n"
      "ds_read_b128 v[140:143], v123 offset:10240\n"
      "v_mul_f32_e64 v232, v248, v232\n"
      "v_mfma_f32_16x16x32_bf16 v[224:227], a[248:251], v[172:175], v[224:227]\n"
      "ds_read_b128 v[192:195], v123 offset:13312\n"
      "v_mul_f32 v155, v248, v155\n"
      "v_mfma_f32_16x16x32_bf16 v[224:227], v[4:7], v[220:223], v[224:227]\n"
      "ds_read_b128 v[220:223], v123 offset:16384\n"
      "v_cvt_pk_bf16_f32 v232, v232, v233\n"
      "s_waitcnt lgkmcnt(4)\n"
      "v_mfma_f32_16x16x32_bf16 v[204:207], v[8:11], v[156:159], v[204:207]\n"
      "ds_read_b128 v[240:243], v123 offset:7168\n"
      "v_mul_f32 v234, v248, v234\n"
      "v_mfma_f32_16x16x32_bf16 v[204:207], v[20:23], v[144:147], v[204:207]\n"
      "v_mul_f32 v235, v248, v235\n"
      "v_cvt_pk_bf16_f32 v233, v234, v235\n"
      "v_mfma_f32_16x16x32_bf16 v[164:167], v[12:15], v[156:159], v[164:167]\n"
      "s_waitcnt lgkmcnt(4)\n"
      "v_mul_f32 v154, v248, v154\n"
      "v_mul_f32_e64 v153, v248, v153\n"
      "v_mfma_f32_16x16x32_bf16 v[164:167], v[24:27], v[144:147], v[164:167]\n"
      "global_store_dwordx4 v116, v[168:171], s[54:55] offset:128 sc1\n"
      "v_mfma_f32_16x16x32_bf16 v[224:227], v[16:19], v[156:159], v[224:227]\n"
      "v_mul_f32 v180, v248, v180\n"
      "v_mul_f32 v152, v248, v152\n"
      "v_mfma_f32_16x16x32_bf16 v[224:227], v[28:31], v[144:147], v[224:227]\n"
      "v_cvt_pk_bf16_f32 v234, v152, v153\n"
      "v_cvt_pk_bf16_f32 v235, v154, v155\n"
      "v_mfma_f32_16x16x32_bf16 v[156:159], a[216:219], v[250:253], 0\n"
      "ds_read_b128 v[152:155], v123 offset:2048\n"
      "v_mul_f32_e64 v181, v248, v181\n"
      "v_mfma_f32_16x16x32_bf16 v[156:159], a[228:231], v[148:151], v[156:159]\n"
      "ds_write_b128 v126, v[232:235] offset:18432\n"
      "v_mfma_f32_16x16x32_bf16 v[168:171], a[220:223], v[250:253], 0\n"
      "ds_read_b128 v[232:235], v123 offset:5120\n"
      "v_cvt_pk_bf16_f32 v180, v180, v181\n"
      "v_mfma_f32_16x16x32_bf16 v[168:171], a[232:235], v[148:151], v[168:171]\n"
      "s_waitcnt lgkmcnt(3)\n"
      "v_mul_f32 v183, v248, v183\n"
      "v_mul_f32 v182, v248, v182\n"
      "v_mfma_f32_16x16x32_bf16 v[136:139], a[224:227], v[250:253], 0\n"
      "s_lshl_b32 s29, s24, 8\n"
      "v_mul_f32_e64 v176, v254, v176\n"
      "v_cvt_pk_bf16_f32 v181, v182, v183\n"
      "v_mfma_f32_16x16x32_bf16 v[136:139], a[236:239], v[148:151], v[136:139]\n"
      "ds_write_b64 v127, v[180:181] offset:18432\n"
      "v_mfma_f32_16x16x32_bf16 v[156:159], a[240:243], v[240:243], v[156:159]\n"
      "ds_read_b128 v[148:151], v123 offset:8192\n"
      "v_mul_f32_e64 v177, v254, v177\n"
      "v_mfma_f32_16x16x32_bf16 v[156:159], a[252:255], v[140:143], v[156:159]\n"
      "ds_read_b128 v[180:183], v123 offset:11264\n"
      "v_cvt_pk_bf16_f32 v176, v176, v177\n"
      "v_mfma_f32_16x16x32_bf16 v[168:171], a[244:247], v[240:243], v[168:171]\n"
      "v_mul_f32 v179, v254, v179\n"
      "v_mul_f32 v178, v254, v178\n"
      "v_mfma_f32_16x16x32_bf16 v[168:171], v[0:3], v[140:143], v[168:171]\n"
      "v_mul_f32 v244, v254, v244\n"
      "v_cvt_pk_bf16_f32 v177, v178, v179\n"
      "v_mfma_f32_16x16x32_bf16 v[136:139], a[248:251], v[240:243], v[136:139]\n"
      "v_mul_f32 v245, v254, v245\n"
      "v_cvt_pk_bf16_f32 v178, v244, v245\n"
      "v_mfma_f32_16x16x32_bf16 v[136:139], v[4:7], v[140:143], v[136:139]\n"
      "v_mul_f32 v246, v254, v246\n"
      "v_mul_f32 v247, v254, v247\n"
      "v_mfma_f32_16x16x32_bf16 v[156:159], v[8:11], v[192:195], v[156:159]\n"
      "s_add_u32 s29, s29, 159088\n"
      "v_cvt_pk_bf16_f32 v179, v246, v247\n"
      "v_mul_f32_e64 v200, v254, v200\n"
      "v_mfma_f32_16x16x32_bf16 v[156:159], v[20:23], v[220:223], v[156:159]\n"
      "ds_write_b128 v126, v[176:179] offset:20480\n"
      "v_mfma_f32_16x16x32_bf16 v[168:171], v[12:15], v[192:195], v[168:171]\n"
      "ds_read_b128 v[176:179], v123 offset:14336\n"
      "v_mul_f32_e64 v201, v254, v201\n"
      "v_mfma_f32_16x16x32_bf16 v[168:171], v[24:27], v[220:223], v[168:171]\n"
      "ds_read_b128 v[244:247], v123 offset:17408\n"
      "v_cvt_pk_bf16_f32 v200, v200, v201\n"
      "v_mfma_f32_16x16x32_bf16 v[136:139], v[16:19], v[192:195], v[136:139]\n"
      "s_waitcnt lgkmcnt(3)\n"
      "v_mul_f32 v202, v254, v202\n"
      "v_mul_f32_e64 v203, v254, v203\n"
      "v_mfma_f32_16x16x32_bf16 v[136:139], v[28:31], v[220:223], v[136:139]\n"
      "v_fma_f32 v248, v204, v204, s39\n"
      "v_cvt_pk_bf16_f32 v201, v202, v203\n"
      "v_mfma_f32_16x16x32_bf16 v[240:243], a[216:219], v[152:155], 0\n"
      "global_store_dwordx4 v116, v[236:239], s[54:55] offset:256 sc1\n"
      "v_mfma_f32_16x16x32_bf16 v[240:243], a[228:231], v[232:235], v[240:243]\n"
      "ds_write_b64 v127, v[200:201] offset:20480\n"
      "v_mfma_f32_16x16x32_bf16 v[192:195], a[220:223], v[152:155], 0\n"
      "ds_read_b128 v[250:253], v196\n"
      "v_fmac_f32_e64 v248, v205, v205\n"
      "v_mfma_f32_16x16x32_bf16 v[192:195], a[232:235], v[232:235], v[192:195]\n"
      "ds_read_b128 v[200:203], v196 offset:1408\n"
      "v_fmac_f32 v248, v206, v206\n"
      "v_mfma_f32_16x16x32_bf16 v[236:239], a[224:227], v[152:155], 0\n"
      "v_fmac_f32 v248, v207, v207\n"
      "v_fmac_f32_e64 v248, v164, v164\n"
      "v_mfma_f32_16x16x32_bf16 v[236:239], a[236:239], v[232:235], v[236:239]\n"
      "v_fmac_f32 v248, v165, v165\n"
      "v_fmac_f32 v248, v166, v166\n"
      "v_mfma_f32_16x16x32_bf16 v[240:243], a[240:243], v[148:151], v[240:243]\n"
      "s_waitcnt lgkmcnt(3)\n"
      "v_fmac_f32 v248, v167, v167\n"
      "v_fmac_f32 v248, v224, v224\n"
      "v_mfma_f32_16x16x32_bf16 v[240:243], a[252:255], v[180:183], v[240:243]\n"
      "v_fmac_f32 v248, v225, v225\n"
      "v_fmac_f32_e64 v248, v226, v226\n"
      "v_mfma_f32_16x16x32_bf16 v[192:195], a[244:247], v[148:151], v[192:195]\n"
      "global_store_dwordx4 v117, v[228:231], s[54:55] sc1\n"
      "v_mfma_f32_16x16x32_bf16 v[192:195], v[0:3], v[180:183], v[192:195]\n"
      "v_mul_f32 v184, v249, v184\n"
      "v_fmac_f32 v248, v227, v227\n"
      "v_mfma_f32_16x16x32_bf16 v[236:239], a[248:251], v[148:151], v[236:239]\n"
      "ds_read_b128 v[152:155], v196 offset:2816\n"
      "v_mul_f32 v185, v249, v185\n"
      "v_mfma_f32_16x16x32_bf16 v[236:239], v[4:7], v[180:183], v[236:239]\n"
      "v_mul_f32 v186, v249, v186\n"
      "v_cvt_pk_bf16_f32 v184, v184, v185\n"
      "v_mfma_f32_16x16x32_bf16 v[240:243], v[8:11], v[176:179], v[240:243]\n"
      "ds_read_b128 v[148:151], v196 offset:4224\n"
      "v_mul_f32 v187, v249, v187\n"
      "v_mfma_f32_16x16x32_bf16 v[240:243], v[20:23], v[244:247], v[240:243]\n"
      "s_waitcnt lgkmcnt(2)\n"
      "v_cvt_pk_bf16_f32 v185, v186, v187\n"
      "v_mul_f32 v212, v249, v212\n"
      "v_mfma_f32_16x16x32_bf16 v[192:195], v[12:15], v[176:179], v[192:195]\n"
      "v_mul_f32 v213, v249, v213\n"
      "v_cvt_pk_bf16_f32 v186, v212, v213\n"
      "v_mfma_f32_16x16x32_bf16 v[192:195], v[24:27], v[244:247], v[192:195]\n"
      "ds_read_b128 v[180:183], v196 offset:5632\n"
      "v_mul_f32 v214, v249, v214\n"
      "v_mfma_f32_16x16x32_bf16 v[236:239], v[16:19], v[176:179], v[236:239]\n"
      "v_mul_f32 v215, v249, v215\n"
      "v_cvt_pk_bf16_f32 v187, v214, v215\n"
      "v_mfma_f32_16x16x32_bf16 v[236:239], v[28:31], v[244:247], v[236:239]\n"
      "ds_write_b128 v126, v[184:187] offset:22528\n"
      "v_mfma_f32_16x16x32_bf16 v[184:187], a[216:219], v[250:253], 0\n"
      "ds_read_b128 v[212:215], v196 offset:7040\n"
      "v_mul_f32 v190, v249, v190\n"
      "v_mfma_f32_16x16x32_bf16 v[184:187], a[228:231], v[200:203], v[184:187]\n"
      "v_mul_f32 v189, v249, v189\n"
      "v_mfma_f32_16x16x32_bf16 v[244:247], a[220:223], v[250:253], 0\n"
      "s_waitcnt lgkmcnt(3)\n"
      "v_mul_f32 v188, v249, v188\n"
      "v_cvt_pk_bf16_f32 v188, v188, v189\n"
      "v_mfma_f32_16x16x32_bf16 v[244:247], a[232:235], v[200:203], v[244:247]\n"
      "ds_read_b128 v[176:179], v196 offset:256\n"
      "v_mul_f32_e64 v191, v249, v191\n"
      "v_mfma_f32_16x16x32_bf16 v[232:235], a[224:227], v[250:253], 0\n"
      "v_cvt_pk_bf16_f32 v189, v190, v191\n"
      "v_fma_f32 v249, v156, v156, s39\n"
      "v_mfma_f32_16x16x32_bf16 v[232:235], a[236:239], v[200:203], v[232:235]\n"
      "ds_write_b64 v127, v[188:189] offset:22528\n"
      "v_mfma_f32_16x16x32_bf16 v[184:187], a[240:243], v[152:155], v[184:187]\n"
      "ds_read_b128 v[188:191], v196 offset:1664\n"
      "v_fmac_f32 v249, v157, v157\n"
      "v_mfma_f32_16x16x32_bf16 v[184:187], a[252:255], v[148:151], v[184:187]\n"
      "v_fmac_f32 v249, v158, v158\n"
      "v_fmac_f32 v249, v159, v159\n"
      "v_mfma_f32_16x16x32_bf16 v[244:247], a[244:247], v[152:155], v[244:247]\n"
      "s_waitcnt lgkmcnt(3)\n"
      "v_fmac_f32 v249, v168, v168\n"
      "v_fmac_f32 v249, v169, v169\n"
      "v_mfma_f32_16x16x32_bf16 v[244:247], v[0:3], v[148:151], v[244:247]\n"
      "ds_read_b128 v[200:203], v196 offset:3072\n"
      "v_fmac_f32_e64 v249, v170, v170\n"
      "v_mfma_f32_16x16x32_bf16 v[232:235], a[248:251], v[152:155], v[232:235]\n"
      "ds_read_b128 v[152:155], v196 offset:4480\n"
      "v_fmac_f32 v249, v171, v171\n"
      "v_mfma_f32_16x16x32_bf16 v[232:235], v[4:7], v[148:151], v[232:235]\n"
      "v_fmac_f32 v249, v136, v136\n"
      "v_lshl_add_u32 v199, v208, 2, s29\n"
      "v_mfma_f32_16x16x32_bf16 v[184:187], v[8:11], v[180:183], v[184:187]\n"
      "v_fmac_f32 v249, v137, v137\n"
      "v_fmac_f32 v249, v138, v138\n"
      "v_mfma_f32_16x16x32_bf16 v[184:187], v[20:23], v[212:215], v[184:187]\n"
      "s_lshl_b32 s29, s24, 3\n"
      "v_fmac_f32 v249, v139, v139\n"
      "v_mfma_f32_16x16x32_bf16 v[244:247], v[12:15], v[180:183], v[244:247]\n"
      "ds_read_b128 v[148:151], v196 offset:5888\n"
      "v_mfma_f32_16x16x32_bf16 v[244:247], v[24:27], v[212:215], v[244:247]\n"
      "ds_read_b32 v199, v199\n"
      "v_fma_f32 v197, v240, v240, s39\n"
      "v_mfma_f32_16x16x32_bf16 v[232:235], v[16:19], v[180:183], v[232:235]\n"
      "s_waitcnt lgkmcnt(4)\n"
      "v_fmac_f32 v197, v241, v241\n"
      "v_lshrrev_b32_e64 v251, 3, v208\n"
      "v_mfma_f32_16x16x32_bf16 v[232:235], v[28:31], v[212:215], v[232:235]\n"
      "v_fmac_f32_e64 v197, v242, v242\n"
      "v_add_u32_e64 v251, s29, v251\n"
      "v_mfma_f32_16x16x32_bf16 v[220:223], a[216:219], v[176:179], 0\n"
      "ds_read_b128 v[180:183], v196 offset:7296\n"
      "v_cmp_gt_u32 s[48:49], s26, v251\n"
      "v_mfma_f32_16x16x32_bf16 v[220:223], a[228:231], v[188:191], v[220:223]\n"
      "s_mov_b32 exec_hi, 0\n"
      "global_store_dwordx4 v118, v[160:163], s[54:55] sc1\n"
      "s_mov_b32 exec_hi, -1\n"
      "v_mfma_f32_16x16x32_bf16 v[250:253], a[220:223], v[176:179], 0\n"
      "s_waitcnt lgkmcnt(3)\n"
      "v_fmac_f32 v197, v243, v243\n"
      "v_fmac_f32 v197, v192, v192\n"
      "v_mfma_f32_16x16x32_bf16 v[250:253], a[232:235], v[188:191], v[250:253]\n"
      "v_fmac_f32 v197, v193, v193\n"
      "v_fmac_f32 v197, v194, v194\n"
      "v_mfma_f32_16x16x32_bf16 v[212:215], a[224:227], v[176:179], 0\n"
      "v_fmac_f32 v197, v195, v195\n"
      "v_fmac_f32 v197, v236, v236\n"
      "v_mfma_f32_16x16x32_bf16 v[212:215], a[236:239], v[188:191], v[212:215]\n"
      "v_fmac_f32 v197, v237, v237\n"
      "v_fmac_f32 v197, v238, v238\n"
      "v_mfma_f32_16x16x32_bf16 v[220:223], a[240:243], v[200:203], v[220:223]\n"
      "v_fmac_f32 v197, v239, v239\n"
      "v_mov_b32 v177, v197\n"
      "v_mfma_f32_16x16x32_bf16 v[220:223], a[252:255], v[152:155], v[220:223]\n"
      "v_permlane16_swap_b32 v248, v249\n"
      "v_mfma_f32_16x16x32_bf16 v[250:253], a[244:247], v[200:203], v[250:253]\n"
      "v_fma_f32 v176, v184, v184, s39\n"
      "v_add_f32 v248, v248, v249\n"
      "v_mfma_f32_16x16x32_bf16 v[250:253], v[0:3], v[152:155], v[250:253]\n"
      "v_permlane16_swap_b32 v197, v177\n"
      "v_mfma_f32_16x16x32_bf16 v[212:215], a[248:251], v[200:203], v[212:215]\n"
      "v_add_f32 v197, v197, v177\n"
      "v_fmac_f32 v176, v185, v185\n"
      "v_mfma_f32_16x16x32_bf16 v[212:215], v[4:7], v[152:155], v[212:215]\n"
      "s_waitcnt lgkmcnt(0)\n"
      "v_permlane32_swap_b32 v248, v197\n"
      "v_mfma_f32_16x16x32_bf16 v[220:223], v[8:11], v[148:151], v[220:223]\n"
      "v_add_f32_e64 v248, v248, v197\n"
      "v_mfma_f32_16x16x32_bf16 v[220:223], v[20:23], v[180:183], v[220:223]\n"
      "ds_write_b32 v129, v248 offset:1024\n"
      "v_mfma_f32_16x16x32_bf16 v[250:253], v[12:15], v[148:151], v[250:253]\n"
      "v_fmac_f32 v176, v186, v186\n"
      "v_fmac_f32 v176, v187, v187\n"
      "v_mfma_f32_16x16x32_bf16 v[250:253], v[24:27], v[180:183], v[250:253]\n"
      "v_fmac_f32 v176, v244, v244\n"
      "v_fmac_f32 v176, v245, v245\n"
      "v_mfma_f32_16x16x32_bf16 v[212:215], v[16:19], v[148:151], v[212:215]\n"
      "v_fmac_f32 v176, v246, v246\n"
      "v_fmac_f32 v176, v247, v247\n"
      "v_mfma_f32_16x16x32_bf16 v[212:215], v[28:31], v[180:183], v[212:215]\n"
      "v_fmac_f32 v176, v232, v232\n"
      "v_fmac_f32 v176, v233, v233\n"
      "v_fmac_f32 v176, v234, v234\n"
      "v_fmac_f32 v176, v235, v235\n"
      "v_pk_mul_f32 v[248:249], v[220:221], v[220:221]\n"
      "v_pk_fma_f32 v[248:249], v[222:223], v[222:223], v[248:249]\n"
      "v_pk_fma_f32 v[248:249], v[250:251], v[250:251], v[248:249]\n"
      "v_pk_fma_f32 v[248:249], v[252:253], v[252:253], v[248:249]\n"
      "v_pk_fma_f32 v[248:249], v[212:213], v[212:213], v[248:249]\n"
      "v_pk_fma_f32 v[248:249], v[214:215], v[214:215], v[248:249]\n"
      "v_add_f32 v248, s39, v248\n"
      "v_add_f32 v149, v248, v249\n"
      "v_mov_b32 v148, v149\n"
      "v_mov_b32 v151, v148\n"
      "v_permlane16_swap_b32 v176, v149\n"
      "v_add_f32 v176, v176, v149\n"
      "v_permlane16_swap_b32 v148, v151\n"
      "v_add_f32 v148, v148, v151\n"
      "s_nop 1\n"
      "v_permlane32_swap_b32 v176, v148\n"
      "v_add_f32_e64 v176, v176, v148\n"
      "ds_write_b32 v129, v176 offset:2048\n"
      "s_waitcnt lgkmcnt(0)\n"
      "s_barrier\n"
      "s_add_u32 s27, s27, 1\n"
      "ds_read_b128 v[148:151], v128 offset:1024\n"
      "ds_read_b128 v[180:183], v128 offset:2048\n"
      "s_sub_u32 s28, s27, 3\n"
      "ds_read_b128 v[152:155], v119 offset:18432\n"
      "ds_read_b128 v[188:191], v120 offset:18432\n"
      "s_cselect_b64 s[54:55], s[12:13], s[22:23]\n"
      "s_max_i32 m0, s28, 0\n"
      "ds_read_b128 v[200:203], v121 offset:18432\n"
      "ds_read_b128 v[228:231], v119 offset:24576\n"
      "ds_read_b128 v[176:179], v119 offset:30720\n"
      "s_movrels_b32 s9, s80\n"
      "s_lshr_b32 s8, s9, 8\n"
      "s_and_b32 s9, s9, 0xff\n"
      "s_mul_i32 s28, s8, 1373568\n"
      "s_mul_i32 s29, s9, 2688\n"
      "s_add_u32 s28, s28, s29\n"
      "s_cmp_eq_u32 s8, 72\n"
      "s_cselect_b32 s8, 0, 100270464\n"
      "s_cmp_eq_u32 s9, 72\n"
      "s_cselect_b32 s9, 0, 196224\n"
      "s_add_u32 s10, s8, s9\n"
      "s_add_u32 s28, s28, s10\n"
      "s_add_u32 s54, s54, s28\n"
      "s_addc_u32 s55, s55, 0\n"
      "s_cmp_lg_u32 s10, s11\n"
      "s_cbranch_scc1 L_edge_38_%=\n"
      "L_eb_39_%=:\n"
      "s_waitcnt lgkmcnt(5)\n"
      "v_add_f32 v150, |v150|, |v151|\n"
      "v_add_f32 v148, |v148|, |v149|\n"
      "v_add_f32 v148, v148, v150\n"
      "v_rsq_f32 v148, v148\n"
      "v_add_f32 v180, |v180|, |v181|\n"
      "v_mul_f32 v148, s41, v148\n"
      "v_mov_b32 v249, v148\n"
      "v_add_f32 v182, |v182|, |v183|\n"
      "v_add_f32 v180, v180, v182\n"
      "v_rsq_f32 v180, v180\n"
      "v_permlane32_swap_b32 v148, v249\n"
      "v_pk_mul_f32 v[238:239], v[238:239], v[248:249] op_sel:[0,1]\n"
      "v_mov_b32 v181, v148\n"
      "v_mul_f32_e64 v180, s41, v180\n"
      "v_subrev_u32 v182, 73728, v127\n"
      "v_permlane16_swap_b32 v148, v181\n"
      "v_pk_mul_f32 v[236:237], v[236:237], v[248:249] op_sel:[0,1]\n"
      "v_pk_mul_f32 v[138:139], v[138:139], v[180:181] op_sel:[0,1]\n"
      "v_mov_b32 v248, v180\n"
      "v_pk_mul_f32 v[226:227], v[226:227], v[148:149] op_sel_hi:[1,0]\n"
      "v_pk_mul_f32 v[224:225], v[224:225], v[148:149] op_sel_hi:[1,0]\n"
      "v_cvt_pk_bf16_f32 v236, v236, v237\n"
      "v_pk_mul_f32 v[156:157], v[156:157], v[180:181] op_sel:[0,1]\n"
      "v_pk_mul_f32 v[242:243], v[242:243], v[248:249] op_sel:[0,1]\n"
      "v_pk_mul_f32 v[204:205], v[204:205], v[148:149] op_sel_hi:[1,0]\n"
      "v_pk_mul_f32 v[158:159], v[158:159], v[180:181] op_sel:[0,1]\n"
      "v_permlane32_swap_b32 v180, v248\n"
      "v_mov_b32 v248, v180\n"
      "s_waitcnt lgkmcnt(0)\n"
      "v_cvt_pk_bf16_f32 v156, v156, v157\n"
      "v_pk_mul_f32 v[240:241], v[240:241], v[248:249] op_sel:[0,1]\n"
      "v_permlane16_swap_b32 v180, v248\n"
      "v_pk_mul_f32 v[136:137], v[136:137], v[180:181] op_sel:[0,1]\n"
      "v_pk_mul_f32 v[186:187], v[186:187], v[180:181] op_sel_hi:[1,0]\n"
      "v_cvt_pk_bf16_f32 v237, v238, v239\n"
      "v_pk_mul_f32 v[214:215], v[214:215], v[248:249] op_sel_hi:[1,0]\n"
      "v_pk_mul_f32 v[212:213], v[212:213], v[248:249] op_sel_hi:[1,0]\n"
      "v_pk_mul_f32 v[164:165], v[164:165], v[148:149] op_sel_hi:[1,0]\n"
      "v_cvt_pk_bf16_f32 v240, v240, v241\n"
      "v_cvt_pk_bf16_f32 v136, v136, v137\n"
      "v_cvt_pk_bf16_f32 v224, v224, v225\n"
      "v_pk_mul_f32 v[170:171], v[170:171], v[180:181] op_sel:[0,1]\n"
      "v_pk_mul_f32 v[232:233], v[232:233], v[180:181] op_sel_hi:[1,0]\n"
      "v_cvt_pk_bf16_f32 v212, v212, v213\n"
      "v_cvt_pk_bf16_f32 v241, v242, v243\n"
      "v_pk_mul_f32 v[194:195], v[194:195], v[248:249] op_sel:[0,1]\n"
      "v_pk_mul_f32 v[234:235], v[234:235], v[180:181] op_sel_hi:[1,0]\n"
      "v_cvt_pk_bf16_f32 v225, v226, v227\n"
      "v_cvt_pk_bf16_f32 v157, v158, v159\n"
      "v_pk_mul_f32 v[252:253], v[252:253], v[248:249] op_sel_hi:[1,0]\n"
      "v_cvt_pk_bf16_f32 v137, v138, v139\n"
      "v_pk_mul_f32 v[244:245], v[244:245], v[180:181] op_sel_hi:[1,0]\n"
      "v_pk_mul_f32 v[166:167], v[166:167], v[148:149] op_sel_hi:[1,0]\n"
      "global_store_dwordx4 v116, v[152:155], s[54:55] sc1\n"
      "v_pk_mul_f32 v[184:185], v[184:185], v[180:181] op_sel_hi:[1,0]\n"
      "v_pk_mul_f32 v[192:193], v[192:193], v[248:249] op_sel:[0,1]\n"
      "v_cvt_pk_bf16_f32 v184, v184, v185\n"
      "v_cvt_pk_bf16_f32 v185, v186, v187\n"
      "v_pk_mul_f32 v[168:169], v[168:169], v[180:181] op_sel:[0,1]\n"
      "v_pk_mul_f32 v[222:223], v[222:223], v[248:249] op_sel_hi:[1,0]\n"
      "v_cvt_pk_bf16_f32 v158, v168, v169\n"
      "v_cvt_pk_bf16_f32 v232, v232, v233\n"
      "v_pk_mul_f32 v[220:221], v[220:221], v[248:249] op_sel_hi:[1,0]\n"
      "v_cvt_pk_bf16_f32 v233, v234, v235\n"
      "v_pk_mul_f32 v[206:207], v[206:207], v[148:149] op_sel_hi:[1,0]\n"
      "v_pk_mul_f32 v[246:247], v[246:247], v[180:181] op_sel_hi:[1,0]\n"
      "v_subrev_u32 v249, 73728, v126\n"
      "v_cvt_pk_bf16_f32 v159, v170, v171\n"
      "v_cvt_pk_bf16_f32 v204, v204, v205\n"
      "v_cvt_pk_bf16_f32 v205, v206, v207\n"
      "v_cvt_pk_bf16_f32 v206, v164, v165\n"
      "v_cvt_pk_bf16_f32 v243, v194, v195\n"
      "v_cvt_pk_bf16_f32 v220, v220, v221\n"
      "v_cvt_pk_bf16_f32 v213, v214, v215\n"
      "v_cvt_pk_bf16_f32 v207, v166, v167\n"
      "v_cvt_pk_bf16_f32 v221, v222, v223\n"
      "v_cvt_pk_bf16_f32 v186, v244, v245\n"
      "v_cvt_pk_bf16_f32 v242, v192, v193\n"
      "v_pk_mul_f32 v[250:251], v[250:251], v[248:249] op_sel_hi:[1,0]\n"
      "v_cvt_pk_bf16_f32 v187, v246, v247\n"
      "ds_write_b64 v127, v[136:137] offset:2048\n"
      "global_store_dwordx4 v116, v[228:231], s[54:55] offset:128 sc1\n"
      "global_store_dwordx4 v116, v[176:179], s[54:55] offset:256 sc1\n"
      "ds_write_b128 v249, v[184:187]\n"
      "ds_write_b64 v127, v[236:237] offset:4096\n"
      "ds_write_b128 v126, v[204:207]\n"
      "ds_write_b64 v127, v[224:225]\n"
      "global_store_dwordx4 v117, v[188:191], s[54:55] sc1\n"
      "ds_write_b64 v182, v[232:233]\n"
      "v_cvt_pk_bf16_f32 v222, v250, v251\n"
      "v_cvt_pk_bf16_f32 v223, v252, v253\n"
      "ds_write_b128 v126, v[156:159] offset:2048\n"
      "ds_write_b128 v249, v[220:223] offset:2048\n"
      "ds_write_b128 v126, v[240:243] offset:4096\n"
      "s_mov_b32 exec_hi, 0\n"
      "global_store_dwordx4 v118, v[200:203], s[54:55] sc1\n"
      "s_mov_b32 exec_hi, -1\n"
      "ds_write_b64 v182, v[212:213] offset:2048\n"
      "s_waitcnt lgkmcnt(0)\n"
      "s_barrier\n"
      "ds_read_b128 v[184:187], v119 offset:6144\n"
      "ds_read_b128 v[232:235], v119\n"
      "s_add_u32 s27, s27, 1\n"
      "v_subrev_u32 v182, 73728, v119\n"
      "s_sub_u32 s28, s27, 3\n"
      "ds_read_b128 v[176:179], v182\n"
      "ds_read_b128 v[244:247], v121\n"
      "ds_read_b128 v[212:215], v182 offset:6144\n"
      "s_cselect_b64 s[54:55], s[12:13], s[22:23]\n"
      "ds_read_b128 v[188:191], v182 offset:12288\n"
      "ds_read_b128 v[200:203], v120\n"
      "s_max_i32 m0, s28, 0\n"
      "ds_read_b128 v[248:251], v119 offset:12288\n"
      "s_movrels_b32 s9, s80\n"
      "s_lshr_b32 s8, s9, 8\n"
      "s_and_b32 s9, s9, 0xff\n"
      "s_mul_i32 s28, s8, 1373568\n"
      "s_mul_i32 s29, s9, 2688\n"
      "s_add_u32 s28, s28, s29\n"
      "s_cmp_eq_u32 s8, 72\n"
      "s_cselect_b32 s8, 0, 100270464\n"
      "s_cmp_eq_u32 s9, 72\n"
      "s_cselect_b32 s9, 0, 196224\n"
      "s_add_u32 s10, s8, s9\n"
      "s_add_u32 s28, s28, s10\n"
      "s_add_u32 s54, s54, s28\n"
      "s_addc_u32 s55, s55, 0\n"
      "s_cmp_lg_u32 s10, s11\n"
      "s_waitcnt lgkmcnt(6)\n"
      "s_cbranch_scc1 L_edge_40_%=\n"
      "L_eb_41_%=:\n"
      "global_store_dwordx4 v116, v[232:235], s[54:55] sc1\n"
      "global_store_dwordx4 v116, v[184:187], s[54:55] offset:128 sc1\n"
      "s_waitcnt lgkmcnt(2)\n"
      "s_waitcnt lgkmcnt(1)\n"
      "s_waitcnt lgkmcnt(0)\n"
      "global_store_dwordx4 v116, v[248:251], s[54:55] offset:256 sc1\n"
      "global_store_dwordx4 v117, v[200:203], s[54:55] sc1\n"
      "s_mov_b32 exec_hi, 0\n"
      "global_store_dwordx4 v118, v[244:247], s[54:55] sc1\n"
      "s_mov_b32 exec_hi, -1\n"
      "s_mov_b64 exec, s[48:49]\n"
      "s_cbranch_execz L_np_42_%=\n"
      "global_store_dwordx4 v199, v[176:179], s[22:23] sc1\n"
      "global_store_dwordx4 v199, v[212:215], s[22:23] offset:128 sc1\n"
      "global_store_dwordx4 v199, v[188:191], s[22:23] offset:256 sc1\n"
      "L_np_42_%=:\n"
      "s_mov_b64 exec, -1\n"
      "s_nop 0\n"
      "s_endpgm\n"
      "L_tail1_%=:\n"
      "s_cmp_eq_u32 s10, s11\n"
      "s_cbranch_scc1 L_sk_43_%=\n"
      "s_mov_b32 s11, s10\n"
      "s_sub_u32 s28, s60, s8\n"
      "s_sub_u32 s29, s61, s9\n"
      "s_mov_b32 s60, s8\n"
      "s_mov_b32 s61, s9\n"
      "s_mov_b64 exec, s[64:65]\n"
      "v_add_u32 v116, s28, v116\n"
      "s_mov_b64 exec, s[70:71]\n"
      "v_add_u32 v116, s29, v116\n"
      "s_mov_b64 exec, s[66:67]\n"
      "v_add_u32 v117, s28, v117\n"
      "s_mov_b64 exec, s[72:73]\n"
      "v_add_u32 v117, s29, v117\n"
      "s_mov_b64 exec, s[68:69]\n"
      "v_add_u32 v118, s28, v118\n"
      "s_mov_b64 exec, s[74:75]\n"
      "v_add_u32 v118, s29, v118\n"
      "s_mov_b64 exec, -1\n"
      "s_nop 4\n"
      "L_sk_43_%=:\n"
      "s_cmp_lt_u32 s27, s26\n"
      "s_cbranch_scc1 L_mid_%=\n"
      "s_nop 7\n"
      "v_mbcnt_lo_u32_b32 v193, -1, 0\n"
      "v_mbcnt_hi_u32_b32 v193, -1, v193\n"
      "v_rcp_f32 v192, v192\n"
      "v_and_b32 v221, 15, v193\n"
      "v_pk_mul_f32 v[248:249], v[248:249], v[192:193] op_sel_hi:[1,0]\n"
      "v_pk_mul_f32 v[148:149], v[148:149], v[192:193] op_sel_hi:[1,0]\n"
      "v_cvt_pk_bf16_f32 v148, v148, v149\n"
      "v_pk_mul_f32 v[150:151], v[150:151], v[192:193] op_sel_hi:[1,0]\n"
      "v_cvt_pk_bf16_f32 v149, v150, v151\n"
      "v_pk_mul_f32 v[250:251], v[250:251], v[192:193] op_sel_hi:[1,0]\n"
      "v_lshrrev_b32_e64 v194, 4, v193\n"
      "v_pk_mul_f32 v[240:241], v[240:241], v[192:193] op_sel_hi:[1,0]\n"
      "v_mul_u32_u24 v194, 352, v194\n"
      "v_cvt_pk_bf16_f32 v240, v240, v241\n"
      "v_pk_mul_f32 v[242:243], v[242:243], v[192:193] op_sel_hi:[1,0]\n"
      "v_cvt_pk_bf16_f32 v241, v242, v243\n"
      "v_lshl_add_u32 v194, v221, 4, v194\n"
      "v_cvt_pk_bf16_f32 v243, v250, v251\n"
      "v_cvt_pk_bf16_f32 v242, v248, v249\n"
      "v_add_u32 v194, 150640, v194\n"
      "ds_write_b64 v125, v[148:149] offset:20480\n"
      "ds_write_b128 v124, v[240:243] offset:20480\n"
      "s_waitcnt lgkmcnt(0)\n"
      "s_barrier\n"
      "ds_read_b128 v[228:231], v123 offset:21504\n"
      "ds_read_b128 v[220:223], v123 offset:18432\n"
      "s_sub_u32 s28, s27, 3\n"
      "s_cselect_b64 s[54:55], s[12:13], s[22:23]\n"
      "ds_read_b128 v[148:151], v128 offset:1024\n"
      "ds_read_b128 v[248:251], v119 offset:30720\n"
      "s_max_i32 m0, s28, 0\n"
      "ds_read_b128 v[156:159], v119 offset:18432\n"
      "ds_read_b128 v[240:243], v119 offset:24576\n"
      "ds_read_b128 v[140:143], v123 offset:24576\n"
      "ds_read_b128 v[168:171], v123 offset:27648\n"
      "s_movrels_b32 s9, s80\n"
      "s_lshr_b32 s8, s9, 8\n"
      "s_and_b32 s9, s9, 0xff\n"
      "s_mul_i32 s28, s8, 1373568\n"
      "s_mul_i32 s29, s9, 2688\n"
      "s_add_u32 s28, s28, s29\n"
      "s_cmp_eq_u32 s8, 72\n"
      "ds_read_b128 v[204:207], v120 offset:18432\n"
      "s_cselect_b32 s8, 0, 100270464\n"
      "s_cmp_eq_u32 s9, 72\n"
      "s_cselect_b32 s9, 0, 196224\n"
      "s_add_u32 s10, s8, s9\n"
      "ds_read_b128 v[224:227], v121 offset:18432\n"
      "s_add_u32 s28, s28, s10\n"
      "s_waitcnt lgkmcnt(8)\n"
      "v_mfma_f32_16x16x32_bf16 v[252:255], a[216:219], v[220:223], 0\n"
      "ds_read_b128 v[144:147], v123 offset:30720\n"
      "s_waitcnt lgkmcnt(8)\n"
      "v_mfma_f32_16x16x32_bf16 v[252:255], a[228:231], v[228:231], v[252:255]\n"
      "s_add_u32 s54, s54, s28\n"
      "v_add_f32 v150, |v150|, |v151|\n"
      "v_add_f32 v148, |v148|, |v149|\n"
      "v_mfma_f32_16x16x32_bf16 v[136:139], a[220:223], v[220:223], 0\n"
      "ds_read_b128 v[236:239], v123 offset:33792\n"
      "v_add_f32 v148, v148, v150\n"
      "v_mfma_f32_16x16x32_bf16 v[136:139], a[232:235], v[228:231], v[136:139]\n"
      "ds_read_b128 v[160:163], v123 offset:19456\n"
      "v_rsq_f32 v148, v148\n"
      "s_waitcnt lgkmcnt(5)\n"
      "v_mul_f32 v148, s41, v148\n"
      "v_mfma_f32_16x16x32_bf16 v[208:211], a[224:227], v[220:223], 0\n"
      "ds_read_b128 v[220:223], v123 offset:22528\n"
      "v_mov_b32 v195, v148\n"
      "v_mfma_f32_16x16x32_bf16 v[208:211], a[236:239], v[228:231], v[208:211]\n"
      "s_addc_u32 s55, s55, 0\n"
      "v_permlane32_swap_b32 v148, v195\n"
      "s_cmp_lg_u32 s10, s11\n"
      "v_mov_b32_e64 v192, v148\n"
      "v_mfma_f32_16x16x32_bf16 v[252:255], a[240:243], v[140:143], v[252:255]\n"
      "s_cbranch_scc1 L_edge_44_%=\n"
      "L_eb_45_%=:\n"
      "v_mfma_f32_16x16x32_bf16 v[252:255], a[252:255], v[168:171], v[252:255]\n"
      "global_store_dwordx4 v116, v[156:159], s[54:55] sc1\n"
      "v_permlane16_swap_b32 v148, v192\n"
      "v_mfma_f32_16x16x32_bf16 v[136:139], a[244:247], v[140:143], v[136:139]\n"
      "v_mul_f32 v183, v148, v183\n"
      "v_mul_f32 v180, v148, v180\n"
      "v_mfma_f32_16x16x32_bf16 v[136:139], v[0:3], v[168:171], v[136:139]\n"
      "ds_read_b128 v[196:199], v123 offset:28672\n"
      "v_mul_f32 v181, v148, v181\n"
      "v_mfma_f32_16x16x32_bf16 v[208:211], a[248:251], v[140:143], v[208:211]\n"
      "ds_read_b128 v[140:143], v123 offset:31744\n"
      "v_mul_f32 v235, v148, v235\n"
      "v_mfma_f32_16x16x32_bf16 v[208:211], v[4:7], v[168:171], v[208:211]\n"
      "s_waitcnt lgkmcnt(3)\n"
      "ds_read_b128 v[168:171], v123 offset:34816\n"
      "v_mfma_f32_16x16x32_bf16 v[252:255], v[8:11], v[144:147], v[252:255]\n"
      "ds_read_b128 v[228:231], v123 offset:25600\n"
      "v_mul_f32_e64 v182, v148, v182\n"
      "v_mfma_f32_16x16x32_bf16 v[252:255], v[20:23], v[236:239], v[252:255]\n"
      "v_cvt_pk_bf16_f32 v180, v180, v181\n"
      "v_cvt_pk_bf16_f32 v181, v182, v183\n"
      "v_mfma_f32_16x16x32_bf16 v[136:139], v[12:15], v[144:147], v[136:139]\n"
      "v_mul_f32 v234, v148, v234\n"
      "v_mul_f32 v233, v148, v233\n"
      "v_mfma_f32_16x16x32_bf16 v[136:139], v[24:27], v[236:239], v[136:139]\n"
      "global_store_dwordx4 v116, v[240:243], s[54:55] offset:128 sc1\n"
      "v_mfma_f32_16x16x32_bf16 v[208:211], v[16:19], v[144:147], v[208:211]\n"
      "v_mul_f32_e64 v152, v148, v152\n"
      "v_mul_f32 v232, v148, v232\n"
      "v_mfma_f32_16x16x32_bf16 v[208:211], v[28:31], v[236:239], v[208:211]\n"
      "s_waitcnt lgkmcnt(4)\n"
      "v_cvt_pk_bf16_f32 v182, v232, v233\n"
      "v_cvt_pk_bf16_f32 v183, v234, v235\n"
      "v_mfma_f32_16x16x32_bf16 v[240:243], a[216:219], v[160:163], 0\n"
      "ds_read_b128 v[144:147], v123 offset:20480\n"
      "v_mul_f32_e64 v153, v148, v153\n"
      "v_mfma_f32_16x16x32_bf16 v[240:243], a[228:231], v[220:223], v[240:243]\n"
      "ds_write_b128 v126, v[180:183]\n"
      "v_mfma_f32_16x16x32_bf16 v[180:183], a[220:223], v[160:163], 0\n"
      "ds_read_b128 v[232:235], v123 offset:23552\n"
      "v_cvt_pk_bf16_f32 v152, v152, v153\n"
      "v_mfma_f32_16x16x32_bf16 v[180:183], a[232:235], v[220:223], v[180:183]\n"
      "s_waitcnt lgkmcnt(3)\n"
      "v_mul_f32 v155, v148, v155\n"
      "v_mul_f32 v154, v148, v154\n"
      "v_mfma_f32_16x16x32_bf16 v[156:159], a[224:227], v[160:163], 0\n"
      "s_lshl_b32 s29, s24, 8\n"
      "v_cvt_pk_bf16_f32 v153, v154, v155\n"
      "v_mul_f32_e64 v200, v192, v200\n"
      "v_mfma_f32_16x16x32_bf16 v[156:159], a[236:239], v[220:223], v[156:159]\n"
      "ds_write_b64 v127, v[152:153]\n"
      "v_mfma_f32_16x16x32_bf16 v[240:243], a[240:243], v[228:231], v[240:243]\n"
      "ds_read_b128 v[148:151], v123 offset:26624\n"
      "v_mul_f32_e64 v201, v192, v201\n"
      "v_mfma_f32_16x16x32_bf16 v[240:243], a[252:255], v[196:199], v[240:243]\n"
      "ds_read_b128 v[236:239], v123 offset:29696\n"
      "v_cvt_pk_bf16_f32 v200, v200, v201\n"
      "v_mfma_f32_16x16x32_bf16 v[180:183], a[244:247], v[228:231], v[180:183]\n"
      "s_add_u32 s29, s29, 159088\n"
      "v_mul_f32 v203, v192, v203\n"
      "v_mul_f32 v202, v192, v202\n"
      "v_mfma_f32_16x16x32_bf16 v[180:183], v[0:3], v[196:199], v[180:183]\n"
      "v_mul_f32_e64 v244, v192, v244\n"
      "v_cvt_pk_bf16_f32 v201, v202, v203\n"
      "v_mfma_f32_16x16x32_bf16 v[156:159], a[248:251], v[228:231], v[156:159]\n"
      "v_mul_f32 v245, v192, v245\n"
      "v_cvt_pk_bf16_f32 v202, v244, v245\n"
      "v_mfma_f32_16x16x32_bf16 v[156:159], v[4:7], v[196:199], v[156:159]\n"
      "v_mul_f32 v247, v192, v247\n"
      "v_mul_f32 v246, v192, v246\n"
      "v_mfma_f32_16x16x32_bf16 v[240:243], v[8:11], v[140:143], v[240:243]\n"
      "v_mul_f32 v176, v192, v176\n"
      "v_cvt_pk_bf16_f32 v203, v246, v247\n"
      "v_mfma_f32_16x16x32_bf16 v[240:243], v[20:23], v[168:171], v[240:243]\n"
      "ds_write_b128 v126, v[200:203] offset:2048\n"
      "v_mfma_f32_16x16x32_bf16 v[180:183], v[12:15], v[140:143], v[180:183]\n"
      "ds_read_b128 v[200:203], v123 offset:32768\n"
      "v_mul_f32_e64 v177, v192, v177\n"
      "v_mfma_f32_16x16x32_bf16 v[180:183], v[24:27], v[168:171], v[180:183]\n"
      "ds_read_b128 v[160:163], v123 offset:35840\n"
      "v_cvt_pk_bf16_f32 v176, v176, v177\n"
      "v_mfma_f32_16x16x32_bf16 v[156:159], v[16:19], v[140:143], v[156:159]\n"
      "s_waitcnt lgkmcnt(3)\n"
      "v_mul_f32 v179, v192, v179\n"
      "v_mul_f32_e64 v178, v192, v178\n"
      "v_mfma_f32_16x16x32_bf16 v[156:159], v[28:31], v[168:171], v[156:159]\n"
      "v_cvt_pk_bf16_f32 v177, v178, v179\n"
      "v_fma_f32 v192, v252, v252, s39\n"
      "v_mfma_f32_16x16x32_bf16 v[220:223], a[216:219], v[144:147], 0\n"
      "global_store_dwordx4 v116, v[248:251], s[54:55] offset:256 sc1\n"
      "v_mfma_f32_16x16x32_bf16 v[220:223], a[228:231], v[232:235], v[220:223]\n"
      "ds_write_b64 v127, v[176:177] offset:2048\n"
      "v_mfma_f32_16x16x32_bf16 v[244:247], a[220:223], v[144:147], 0\n"
      "ds_read_b128 v[248:251], v194\n"
      "v_fmac_f32_e64 v192, v253, v253\n"
      "v_mfma_f32_16x16x32_bf16 v[244:247], a[232:235], v[232:235], v[244:247]\n"
      "ds_read_b128 v[152:155], v194 offset:1408\n"
      "v_fmac_f32 v192, v254, v254\n"
      "v_mfma_f32_16x16x32_bf16 v[168:171], a[224:227], v[144:147], 0\n"
      "v_fmac_f32 v192, v255, v255\n"
      "v_fmac_f32 v192, v136, v136\n"
      "v_mfma_f32_16x16x32_bf16 v[168:171], a[236:239], v[232:235], v[168:171]\n"
      "v_fmac_f32 v192, v137, v137\n"
      "v_fmac_f32 v192, v138, v138\n"
      "v_mfma_f32_16x16x32_bf16 v[220:223], a[240:243], v[148:151], v[220:223]\n"
      "v_fmac_f32 v192, v139, v139\n"
      "v_fmac_f32 v192, v208, v208\n"
      "v_mfma_f32_16x16x32_bf16 v[220:223], a[252:255], v[236:239], v[220:223]\n"
      "v_fmac_f32 v192, v209, v209\n"
      "v_fmac_f32_e64 v192, v210, v210\n"
      "v_mfma_f32_16x16x32_bf16 v[244:247], a[244:247], v[148:151], v[244:247]\n"
      "global_store_dwordx4 v117, v[204:207], s[54:55] sc1\n"
      "v_mfma_f32_16x16x32_bf16 v[244:247], v[0:3], v[236:239], v[244:247]\n"
      "s_waitcnt lgkmcnt(3)\n"
      "v_fmac_f32 v192, v211, v211\n"
      "v_mul_f32_e64 v212, v195, v212\n"
      "v_mfma_f32_16x16x32_bf16 v[168:171], a[248:251], v[148:151], v[168:171]\n"
      "ds_read_b128 v[232:235], v194 offset:2816\n"
      "v_mul_f32_e64 v213, v195, v213\n"
      "v_mfma_f32_16x16x32_bf16 v[168:171], v[4:7], v[236:239], v[168:171]\n"
      "v_cvt_pk_bf16_f32 v212, v212, v213\n"
      "v_mul_f32 v214, v195, v214\n"
      "v_mfma_f32_16x16x32_bf16 v[220:223], v[8:11], v[200:203], v[220:223]\n"
      "ds_read_b128 v[148:151], v194 offset:4224\n"
      "v_mul_f32 v215, v195, v215\n"
      "v_mfma_f32_16x16x32_bf16 v[220:223], v[20:23], v[160:163], v[220:223]\n"
      "v_mul_f32 v184, v195, v184\n"
      "v_cvt_pk_bf16_f32 v213, v214, v215\n"
      "v_mfma_f32_16x16x32_bf16 v[244:247], v[12:15], v[200:203], v[244:247]\n"
      "s_waitcnt lgkmcnt(2)\n"
      "v_mul_f32 v185, v195, v185\n"
      "v_cvt_pk_bf16_f32 v214, v184, v185\n"
      "v_mfma_f32_16x16x32_bf16 v[244:247], v[24:27], v[160:163], v[244:247]\n"
      "ds_read_b128 v[144:147], v194 offset:5632\n"
      "v_mul_f32 v186, v195, v186\n"
      "v_mfma_f32_16x16x32_bf16 v[168:171], v[16:19], v[200:203], v[168:171]\n"
      "v_mul_f32_e64 v187, v195, v187\n"
      "v_cvt_pk_bf16_f32 v215, v186, v187\n"
      "v_mfma_f32_16x16x32_bf16 v[168:171], v[28:31], v[160:163], v[168:171]\n"
      "ds_write_b128 v126, v[212:215] offset:4096\n"
      "v_mfma_f32_16x16x32_bf16 v[160:163], a[216:219], v[248:251], 0\n"
      "ds_read_b128 v[236:239], v194 offset:7040\n"
      "v_mul_f32_e64 v188, v195, v188\n"
      "v_mfma_f32_16x16x32_bf16 v[160:163], a[228:231], v[152:155], v[160:163]\n"
      "v_mul_f32 v190, v195, v190\n"
      "v_mfma_f32_16x16x32_bf16 v[184:187], a[220:223], v[248:251], 0\n"
      "v_mul_f32 v189, v195, v189\n"
      "v_cvt_pk_bf16_f32 v188, v188, v189\n"
      "v_mfma_f32_16x16x32_bf16 v[184:187], a[232:235], v[152:155], v[184:187]\n"
      "ds_read_b128 v[196:199], v194 offset:256\n"
      "v_mul_f32 v191, v195, v191\n"
      "v_mfma_f32_16x16x32_bf16 v[176:179], a[224:227], v[248:251], 0\n"
      "s_waitcnt lgkmcnt(4)\n"
      "v_cvt_pk_bf16_f32 v189, v190, v191\n"
      "v_fma_f32 v195, v240, v240, s39\n"
      "v_mfma_f32_16x16x32_bf16 v[176:179], a[236:239], v[152:155], v[176:179]\n"
      "ds_write_b64 v127, v[188:189] offset:4096\n"
      "v_mfma_f32_16x16x32_bf16 v[160:163], a[240:243], v[232:235], v[160:163]\n"
      "ds_read_b128 v[152:155], v194 offset:1664\n"
      "v_fmac_f32 v195, v241, v241\n"
      "v_mfma_f32_16x16x32_bf16 v[160:163], a[252:255], v[148:151], v[160:163]\n"
      "v_fmac_f32 v195, v242, v242\n"
      "v_fmac_f32 v195, v243, v243\n"
      "v_mfma_f32_16x16x32_bf16 v[184:187], a[244:247], v[232:235], v[184:187]\n"
      "s_waitcnt lgkmcnt(3)\n"
      "v_fmac_f32 v195, v180, v180\n"
      "v_fmac_f32 v195, v181, v181\n"
      "v_mfma_f32_16x16x32_bf16 v[184:187], v[0:3], v[148:151], v[184:187]\n"
      "ds_read_b128 v[248:251], v194 offset:3072\n"
      "v_fmac_f32 v195, v182, v182\n"
      "v_mfma_f32_16x16x32_bf16 v[176:179], a[248:251], v[232:235], v[176:179]\n"
      "ds_read_b128 v[228:231], v194 offset:4480\n"
      "v_fmac_f32 v195, v183, v183\n"
      "v_mfma_f32_16x16x32_bf16 v[176:179], v[4:7], v[148:151], v[176:179]\n"
      "v_fmac_f32 v195, v156, v156\n"
      "v_fmac_f32 v195, v157, v157\n"
      "v_mfma_f32_16x16x32_bf16 v[160:163], v[8:11], v[144:147], v[160:163]\n"
      "v_fmac_f32 v195, v158, v158\n"
      "v_fmac_f32 v195, v159, v159\n"
      "v_mfma_f32_16x16x32_bf16 v[160:163], v[20:23], v[236:239], v[160:163]\n"
      "ds_read_b128 v[232:235], v194 offset:5888\n"
      "v_lshl_add_u32 v141, v193, 2, s29\n"
      "v_mfma_f32_16x16x32_bf16 v[184:187], v[12:15], v[144:147], v[184:187]\n"
      "s_waitcnt lgkmcnt(3)\n"
      "v_lshrrev_b32 v150, 3, v193\n"
      "v_mfma_f32_16x16x32_bf16 v[184:187], v[24:27], v[236:239], v[184:187]\n"
      "ds_read_b32 v141, v141\n"
      "v_fma_f32 v193, v220, v220, s39\n"
      "v_mfma_f32_16x16x32_bf16 v[176:179], v[16:19], v[144:147], v[176:179]\n"
      "ds_read_b128 v[212:215], v194 offset:7296\n"
      "v_fmac_f32 v193, v221, v221\n"
      "v_mfma_f32_16x16x32_bf16 v[176:179], v[28:31], v[236:239], v[176:179]\n"
      "s_lshl_b32 s29, s24, 3\n"
      "v_add_u32 v150, s29, v150\n"
      "v_cmp_gt_u32 s[48:49], s26, v150\n"
      "v_mfma_f32_16x16x32_bf16 v[148:151], a[216:219], v[196:199], 0\n"
      "v_fmac_f32 v193, v222, v222\n"
      "v_mfma_f32_16x16x32_bf16 v[148:151], a[228:231], v[152:155], v[148:151]\n"
      "s_mov_b32 exec_hi, 0\n"
      "global_store_dwordx4 v118, v[224:227], s[54:55] sc1\n"
      "s_mov_b32 exec_hi, -1\n"
      "v_mfma_f32_16x16x32_bf16 v[188:191], a[220:223], v[196:199], 0\n"
      "v_fmac_f32 v193, v223, v223\n"
      "v_fmac_f32 v193, v244, v244\n"
      "v_mfma_f32_16x16x32_bf16 v[188:191], a[232:235], v[152:155], v[188:191]\n"
      "v_fmac_f32 v193, v245, v245\n"
      "v_fmac_f32 v193, v246, v246\n"
      "v_mfma_f32_16x16x32_bf16 v[200:203], a[224:227], v[196:199], 0\n"
      "v_fmac_f32 v193, v247, v247\n"
      "v_fmac_f32 v193, v168, v168\n"
      "v_mfma_f32_16x16x32_bf16 v[200:203], a[236:239], v[152:155], v[200:203]\n"
      "s_waitcnt lgkmcnt(1)\n"
      "v_fmac_f32 v193, v169, v169\n"
      "v_fmac_f32 v193, v170, v170\n"
      "v_mfma_f32_16x16x32_bf16 v[148:151], a[240:243], v[248:251], v[148:151]\n"
      "v_fmac_f32 v193, v171, v171\n"
      "v_mov_b32 v143, v193\n"
      "v_mfma_f32_16x16x32_bf16 v[148:151], a[252:255], v[228:231], v[148:151]\n"
      "v_permlane16_swap_b32 v192, v195\n"
      "v_mfma_f32_16x16x32_bf16 v[188:191], a[244:247], v[248:251], v[188:191]\n"
      "v_fma_f32 v194, v160, v160, s39\n"
      "v_add_f32 v192, v192, v195\n"
      "v_mfma_f32_16x16x32_bf16 v[188:191], v[0:3], v[228:231], v[188:191]\n"
      "v_permlane16_swap_b32 v193, v143\n"
      "v_mfma_f32_16x16x32_bf16 v[200:203], a[248:251], v[248:251], v[200:203]\n"
      "s_waitcnt lgkmcnt(0)\n"
      "v_fmac_f32 v194, v161, v161\n"
      "v_add_f32 v193, v193, v143\n"
      "v_mfma_f32_16x16x32_bf16 v[200:203], v[4:7], v[228:231], v[200:203]\n"
      "s_nop 0\n"
      "v_permlane32_swap_b32 v192, v193\n"
      "v_mfma_f32_16x16x32_bf16 v[148:151], v[8:11], v[232:235], v[148:151]\n"
      "v_fmac_f32 v194, v162, v162\n"
      "v_add_f32 v192, v192, v193\n"
      "v_mfma_f32_16x16x32_bf16 v[148:151], v[20:23], v[212:215], v[148:151]\n"
      "ds_write_b32 v129, v192\n"
      "v_mfma_f32_16x16x32_bf16 v[188:191], v[12:15], v[232:235], v[188:191]\n"
      "v_fmac_f32 v194, v163, v163\n"
      "v_fmac_f32 v194, v184, v184\n"
      "v_mfma_f32_16x16x32_bf16 v[188:191], v[24:27], v[212:215], v[188:191]\n"
      "v_fmac_f32 v194, v185, v185\n"
      "v_fmac_f32 v194, v186, v186\n"
      "v_mfma_f32_16x16x32_bf16 v[200:203], v[16:19], v[232:235], v[200:203]\n"
      "v_fmac_f32 v194, v187, v187\n"
      "v_mfma_f32_16x16x32_bf16 v[200:203], v[28:31], v[212:215], v[200:203]\n"
      "v_fmac_f32 v194, v176, v176\n"
      "v_fmac_f32 v194, v177, v177\n"
      "v_fmac_f32 v194, v178, v178\n"
      "v_fmac_f32 v194, v179, v179\n"
      "v_pk_mul_f32 v[142:143], v[148:149], v[148:149]\n"
      "v_pk_fma_f32 v[142:143], v[150:151], v[150:151], v[142:143]\n"
      "v_pk_fma_f32 v[142:143], v[188:189], v[188:189], v[142:143]\n"
      "v_pk_fma_f32 v[142:143], v[190:191], v[190:191], v[142:143]\n"
      "v_pk_fma_f32 v[142:143], v[200:201], v[200:201], v[142:143]\n"
      "v_pk_fma_f32 v[142:143], v[202:203], v[202:203], v[142:143]\n"
      "v_add_f32 v142, s39, v142\n"
      "v_add_f32 v193, v142, v143\n"
      "v_mov_b32 v192, v193\n"
      "v_mov_b32 v195, v192\n"
      "v_permlane16_swap_b32 v194, v193\n"
      "v_add_f32 v194, v194, v193\n"
      "v_permlane16_swap_b32 v192, v195\n"
      "v_add_f32 v192, v192, v195\n"
      "s_nop 1\n"
      "v_permlane32_swap_b32 v194, v192\n"
      "v_add_f32 v194, v194, v192\n"
      "ds_write_b32 v129, v194 offset:2048\n"
      "s_waitcnt lgkmcnt(0)\n"
      "s_barrier\n"
      "s_add_u32 s27, s27, 1\n"
      "ds_read_b128 v[232:235], v128 offset:2048\n"
      "ds_read_b128 v[192:195], v128\n"
      "s_sub_u32 s28, s27, 3\n"
      "s_cselect_b64 s[54:55], s[12:13], s[22:23]\n"
      "s_max_i32 m0, s28, 0\n"
      "ds_read_b128 v[248:251], v119 offset:12288\n"
      "s_movrels_b32 s9, s80\n"
      "s_lshr_b32 s8, s9, 8\n"
      "ds_read_b128 v[144:147], v119\n"
      "s_and_b32 s9, s9, 0xff\n"
      "s_mul_i32 s28, s8, 1373568\n"
      "s_mul_i32 s29, s9, 2688\n"
      "s_add_u32 s28, s28, s29\n"
      "s_cmp_eq_u32 s8, 72\n"
      "ds_read_b128 v[228:231], v120\n"
      "s_cselect_b32 s8, 0, 100270464\n"
      "ds_read_b128 v[212:215], v119 offset:6144\n"
      "s_cmp_eq_u32 s9, 72\n"
      "s_cselect_b32 s9, 0, 196224\n"
      "ds_read_b128 v[152:155], v121\n"
      "s_add_u32 s10, s8, s9\n"
      "s_add_u32 s28, s28, s10\n"
      "s_add_u32 s54, s54, s28\n"
      "s_addc_u32 s55, s55, 0\n"
      "s_cmp_lg_u32 s10, s11\n"
      "s_cbranch_scc1 L_edge_46_%=\n"
      "L_eb_47_%=:\n"
      "s_waitcnt lgkmcnt(5)\n"
      "v_add_f32 v194, |v194|, |v195|\n"
      "v_add_f32 v192, |v192|, |v193|\n"
      "v_add_f32 v192, v192, v194\n"
      "v_add_f32 v232, |v232|, |v233|\n"
      "v_rsq_f32 v192, v192\n"
      "v_subrev_u32 v233, 73728, v127\n"
      "v_mul_f32 v192, s41, v192\n"
      "v_mov_b32_e64 v195, v192\n"
      "v_add_f32 v234, |v234|, |v235|\n"
      "v_add_f32 v232, v232, v234\n"
      "v_permlane32_swap_b32 v192, v195\n"
      "v_mov_b32 v235, v192\n"
      "v_rsq_f32 v232, v232\n"
      "v_pk_mul_f32 v[220:221], v[220:221], v[194:195] op_sel:[0,1]\n"
      "v_pk_mul_f32 v[168:169], v[168:169], v[194:195] op_sel:[0,1]\n"
      "v_permlane16_swap_b32_e64 v192, v235\n"
      "v_pk_mul_f32 v[240:241], v[240:241], v[234:235] op_sel:[0,1]\n"
      "v_pk_mul_f32 v[252:253], v[252:253], v[192:193] op_sel_hi:[1,0]\n"
      "v_pk_mul_f32 v[136:137], v[136:137], v[192:193] op_sel_hi:[1,0]\n"
      "v_cvt_pk_bf16_f32 v252, v252, v253\n"
      "v_mul_f32 v232, s41, v232\n"
      "v_pk_mul_f32 v[158:159], v[158:159], v[234:235] op_sel:[0,1]\n"
      "v_mov_b32 v234, v232\n"
      "s_waitcnt lgkmcnt(0)\n"
      "global_store_dwordx4 v116, v[144:147], s[54:55] sc1\n"
      "v_pk_mul_f32 v[254:255], v[254:255], v[192:193] op_sel_hi:[1,0]\n"
      "v_pk_mul_f32 v[156:157], v[156:157], v[234:235] op_sel:[0,1]\n"
      "v_permlane32_swap_b32 v232, v234\n"
      "v_pk_mul_f32 v[170:171], v[170:171], v[194:195] op_sel:[0,1]\n"
      "v_mov_b32_e64 v234, v232\n"
      "v_pk_mul_f32 v[208:209], v[208:209], v[192:193] op_sel_hi:[1,0]\n"
      "v_cvt_pk_bf16_f32 v253, v254, v255\n"
      "v_pk_mul_f32 v[222:223], v[222:223], v[194:195] op_sel:[0,1]\n"
      "v_permlane16_swap_b32_e64 v232, v234\n"
      "v_pk_mul_f32 v[138:139], v[138:139], v[192:193] op_sel_hi:[1,0]\n"
      "v_cvt_pk_bf16_f32 v220, v220, v221\n"
      "v_pk_mul_f32 v[186:187], v[186:187], v[232:233] op_sel_hi:[1,0]\n"
      "v_pk_mul_f32 v[162:163], v[162:163], v[232:233] op_sel_hi:[1,0]\n"
      "v_cvt_pk_bf16_f32 v240, v240, v241\n"
      "v_pk_mul_f32 v[178:179], v[178:179], v[232:233] op_sel_hi:[1,0]\n"
      "v_pk_mul_f32 v[190:191], v[190:191], v[234:235] op_sel_hi:[1,0]\n"
      "v_pk_mul_f32 v[148:149], v[148:149], v[234:235] op_sel_hi:[1,0]\n"
      "v_pk_mul_f32 v[184:185], v[184:185], v[232:233] op_sel_hi:[1,0]\n"
      "v_pk_mul_f32 v[160:161], v[160:161], v[232:233] op_sel_hi:[1,0]\n"
      "v_cvt_pk_bf16_f32 v254, v136, v137\n"
      "v_cvt_pk_bf16_f32 v148, v148, v149\n"
      "v_pk_mul_f32 v[150:151], v[150:151], v[234:235] op_sel_hi:[1,0]\n"
      "v_cvt_pk_bf16_f32 v149, v150, v151\n"
      "v_pk_mul_f32 v[180:181], v[180:181], v[234:235] op_sel:[0,1]\n"
      "v_pk_mul_f32 v[176:177], v[176:177], v[232:233] op_sel_hi:[1,0]\n"
      "v_cvt_pk_bf16_f32 v160, v160, v161\n"
      "v_pk_mul_f32 v[188:189], v[188:189], v[234:235] op_sel_hi:[1,0]\n"
      "v_pk_mul_f32 v[246:247], v[246:247], v[194:195] op_sel:[0,1]\n"
      "v_pk_mul_f32 v[182:183], v[182:183], v[234:235] op_sel:[0,1]\n"
      "v_cvt_pk_bf16_f32 v151, v190, v191\n"
      "v_pk_mul_f32 v[242:243], v[242:243], v[234:235] op_sel:[0,1]\n"
      "v_pk_mul_f32 v[200:201], v[200:201], v[234:235] op_sel_hi:[1,0]\n"
      "v_cvt_pk_bf16_f32 v156, v156, v157\n"
      "v_pk_mul_f32 v[210:211], v[210:211], v[192:193] op_sel_hi:[1,0]\n"
      "v_cvt_pk_bf16_f32 v161, v162, v163\n"
      "v_pk_mul_f32 v[202:203], v[202:203], v[234:235] op_sel_hi:[1,0]\n"
      "v_cvt_pk_bf16_f32 v255, v138, v139\n"
      "v_cvt_pk_bf16_f32 v162, v184, v185\n"
      "v_cvt_pk_bf16_f32 v157, v158, v159\n"
      "v_subrev_u32 v235, 73728, v126\n"
      "v_cvt_pk_bf16_f32 v208, v208, v209\n"
      "v_cvt_pk_bf16_f32 v168, v168, v169\n"
      "v_cvt_pk_bf16_f32 v163, v186, v187\n"
      "v_cvt_pk_bf16_f32 v200, v200, v201\n"
      "v_cvt_pk_bf16_f32 v169, v170, v171\n"
      "v_cvt_pk_bf16_f32 v209, v210, v211\n"
      "ds_write_b64 v127, v[156:157] offset:20480\n"
      "global_store_dwordx4 v116, v[212:215], s[54:55] offset:128 sc1\n"
      "ds_write_b64 v127, v[208:209] offset:18432\n"
      "v_cvt_pk_bf16_f32 v221, v222, v223\n"
      "v_pk_mul_f32 v[244:245], v[244:245], v[194:195] op_sel:[0,1]\n"
      "v_cvt_pk_bf16_f32 v222, v244, v245\n"
      "v_cvt_pk_bf16_f32 v241, v242, v243\n"
      "v_cvt_pk_bf16_f32 v201, v202, v203\n"
      "v_cvt_pk_bf16_f32 v242, v180, v181\n"
      "v_cvt_pk_bf16_f32 v223, v246, v247\n"
      "v_cvt_pk_bf16_f32 v176, v176, v177\n"
      "v_cvt_pk_bf16_f32 v243, v182, v183\n"
      "v_cvt_pk_bf16_f32 v177, v178, v179\n"
      "v_cvt_pk_bf16_f32 v150, v188, v189\n"
      "ds_write_b128 v235, v[148:151] offset:20480\n"
      "ds_write_b64 v233, v[176:177] offset:18432\n"
      "global_store_dwordx4 v116, v[248:251], s[54:55] offset:256 sc1\n"
      "ds_write_b128 v126, v[240:243] offset:20480\n"
      "ds_write_b64 v127, v[168:169] offset:22528\n"
      "ds_write_b64 v233, v[200:201] offset:20480\n"
      "ds_write_b128 v126, v[220:223] offset:22528\n"
      "ds_write_b128 v126, v[252:255] offset:18432\n"
      "ds_write_b128 v235, v[160:163] offset:18432\n"
      "global_store_dwordx4 v117, v[228:231], s[54:55] sc1\n"
      "s_mov_b32 exec_hi, 0\n"
      "global_store_dwordx4 v118, v[152:155], s[54:55] sc1\n"
      "s_mov_b32 exec_hi, -1\n"
      "s_waitcnt lgkmcnt(0)\n"
      "s_barrier\n"
      "ds_read_b128 v[200:203], v119 offset:24576\n"
      "ds_read_b128 v[228:231], v119 offset:18432\n"
      "s_add_u32 s27, s27, 1\n"
      "v_subrev_u32 v143, 73728, v119\n"
      "s_sub_u32 s28, s27, 3\n"
      "ds_read_b128 v[208:211], v143 offset:18432\n"
      "ds_read_b128 v[160:163], v121 offset:18432\n"
      "ds_read_b128 v[248:251], v143 offset:24576\n"
      "s_cselect_b64 s[54:55], s[12:13], s[22:23]\n"
      "ds_read_b128 v[152:155], v143 offset:30720\n"
      "ds_read_b128 v[168:171], v120 offset:18432\n"
      "s_max_i32 m0, s28, 0\n"
      "ds_read_b128 v[212:215], v119 offset:30720\n"
      "s_movrels_b32 s9, s80\n"
      "s_lshr_b32 s8, s9, 8\n"
      "s_and_b32 s9, s9, 0xff\n"
      "s_mul_i32 s28, s8, 1373568\n"
      "s_mul_i32 s29, s9, 2688\n"
      "s_add_u32 s28, s28, s29\n"
      "s_cmp_eq_u32 s8, 72\n"
      "s_cselect_b32 s8, 0, 100270464\n"
      "s_cmp_eq_u32 s9, 72\n"
      "s_cselect_b32 s9, 0, 196224\n"
      "s_add_u32 s10, s8, s9\n"
      "s_add_u32 s28, s28, s10\n"
      "s_add_u32 s54, s54, s28\n"
      "s_addc_u32 s55, s55, 0\n"
      "s_cmp_lg_u32 s10, s11\n"
      "s_waitcnt lgkmcnt(6)\n"
      "s_cbranch_scc1 L_edge_48_%=\n"
      "L_eb_49_%=:\n"
      "global_store_dwordx4 v116, v[228:231], s[54:55] sc1\n"
      "global_store_dwordx4 v116, v[200:203], s[54:55] offset:128 sc1\n"
      "s_waitcnt lgkmcnt(2)\n"
      "s_waitcnt lgkmcnt(1)\n"
      "s_waitcnt lgkmcnt(0)\n"
      "global_store_dwordx4 v116, v[212:215], s[54:55] offset:256 sc1\n"
      "global_store_dwordx4 v117, v[168:171], s[54:55] sc1\n"
      "s_mov_b32 exec_hi, 0\n"
      "global_store_dwordx4 v118, v[160:163], s[54:55] sc1\n"
      "s_mov_b32 exec_hi, -1\n"
      "s_mov_b64 exec, s[48:49]\n"
      "s_cbranch_execz L_np_50_%=\n"
      "global_store_dwordx4 v141, v[208:211], s[22:23] sc1\n"
      "global_store_dwordx4 v141, v[248:251], s[22:23] offset:128 sc1\n"
      "global_store_dwordx4 v141, v[152:155], s[22:23] offset:256 sc1\n"
      "L_np_50_%=:\n"
      "s_mov_b64 exec, -1\n"
      "s_nop 0\n"
      "s_endpgm\n"
      "L_gen_8_%=:\n"
      "s_waitcnt lgkmcnt(0)\n"
      "s_nop 7\n"
      "s_nop 7\n"
      "s_cmp_eq_u64 s[48:49], 0\n"
      "s_cbranch_scc1 L_go_51_%=\n"
      "s_mov_b32 m0, s27\n"
      "s_nop 0\n"
      "s_movrels_b32 s9, s80\n"
      "s_lshr_b32 s8, s9, 8\n"
      "s_and_b32 s9, s9, 0xff\n"
      "s_mul_i32 s10, s8, 73\n"
      "s_add_u32 s10, s10, s9\n"
      "s_mul_i32 s30, s10, 4802\n"
      "s_add_u32 s30, s30, s18\n"
      "s_and_b32 s29, s30, 15\n"
      "s_add_u32 s30, s29, 110592\n"
      "s_lshl_b64 s[34:35], s[48:49], 4\n"
      "s_lshr_b64 s[34:35], s[34:35], 5\n"
      "s_cbranch_scc1 L_gen_9_%=\n"
      "v_mbcnt_lo_u32_b32 v148, -1, 0\n"
      "v_mbcnt_hi_u32_b32 v148, -1, v148\n"
      "v_lshlrev_b32 v149, 1, v148\n"
      "v_add_u32 v150, 4802, v149\n"
      "v_cmp_gt_u32 vcc, s29, v149\n"
      "s_nop 1\n"
      "v_cndmask_b32 v149, v150, v149, vcc\n"
      "v_add_u32 v149, 110592, v149\n"
      "v_mov_b32 v150, 0\n"
      "s_mov_b64 exec, 0x7f\n"
      "ds_write_b16 v149, v150\n"
      "s_mov_b64 exec, -1\n"
      "v_lshlrev_b32_e64 v149, 4, v148\n"
      "v_add_u32 v149, 110592, v149\n"
      "v_min_u32 v148, 111296, v149\n"
      "ds_read_b128 v[196:199], v149\n"
      "ds_read_b128 v[228:231], v149 offset:1024\n"
      "ds_read_b128 v[150:153], v149 offset:2048\n"
      "ds_read_b128 v[136:139], v149 offset:3072\n"
      "ds_read_b128 v[140:143], v148 offset:4096\n"
      "s_waitcnt lgkmcnt(4)\n"
      "v_or3_b32 v196, v196, v197, v198\n"
      "s_waitcnt lgkmcnt(3)\n"
      "v_or3_b32 v196, v196, v199, v228\n"
      "v_or3_b32 v196, v196, v229, v230\n"
      "s_waitcnt lgkmcnt(2)\n"
      "v_or3_b32 v196, v196, v231, v150\n"
      "v_or3_b32 v196, v196, v151, v152\n"
      "s_waitcnt lgkmcnt(1)\n"
      "v_or3_b32 v196, v196, v153, v136\n"
      "v_or3_b32 v196, v196, v137, v138\n"
      "s_waitcnt lgkmcnt(0)\n"
      "v_or3_b32 v196, v196, v139, v140\n"
      "v_or3_b32 v196, v196, v141, v142\n"
      "v_bitop3_b32 v196, v196, v143, s40 bitop3:0xa8\n"
      "v_cmp_ne_u32 s[48:49], 0, v196\n"
      "s_cmp_lg_u64 s[48:49], 0\n"
      "s_cbranch_scc1 L_gen_9_%=\n"
      "s_cmp_eq_u64 s[50:51], 0\n"
      "s_cbranch_scc1 L_fast_11_%=\n"
      "L_go_51_%=:\n"
      "v_mfma_f32_16x16x32_bf16 v[148:151], v[180:183], v[168:171], v[90:93]\n"
      "ds_read_b128 v[196:199], v128 offset:3072\n"
      "v_max3_f32 v231, v204, v205, v206\n"
      "v_mfma_f32_16x16x32_bf16 v[148:151], v[34:37], v[38:41], v[148:151]\n"
      "v_max3_f32 v229, v207, v232, v233\n"
      "v_max3_f32 v231, v231, v234, v235\n"
      "v_mfma_f32_16x16x32_bf16 v[140:143], v[164:167], v[168:171], v[94:97]\n"
      "v_max3_f32 v229, v229, v192, v193\n"
      "v_max3_f32 v231, v231, v194, v195\n"
      "v_mfma_f32_16x16x32_bf16 v[140:143], v[40:43], v[38:41], v[140:143]\n"
      "v_max3_f32 v231, v231, v229, v200\n"
      "v_mov_b32 v229, v231\n"
      "v_mfma_f32_16x16x32_bf16 v[136:139], v[248:251], v[168:171], v[72:75]\n"
      "s_nop 0\n"
      "v_permlane16_swap_b32_e64 v231, v229\n"
      "v_mfma_f32_16x16x32_bf16 v[136:139], v[236:239], v[164:167], v[136:139]\n"
      "v_max_f32 v231, v231, v229\n"
      "v_mov_b32 v229, v231\n"
      "v_mfma_f32_16x16x32_bf16 v[136:139], v[240:243], v[38:41], v[136:139]\n"
      "s_nop 0\n"
      "v_permlane32_swap_b32 v231, v229\n"
      "v_mfma_f32_16x16x32_bf16 v[152:155], v[212:215], v[168:171], v[98:101]\n"
      "v_max_f32 v231, v231, v229\n"
      "v_sub_f32 v204, v204, v231\n"
      "v_mfma_f32_16x16x32_bf16 v[152:155], v[46:49], v[38:41], v[152:155]\n"
      "v_sub_f32 v205, v205, v231\n"
      "v_sub_f32 v206, v206, v231\n"
      "v_mfma_f32_16x16x32_bf16 v[168:171], v[180:183], v[252:255], v[102:105]\n"
      "v_sub_f32 v207, v207, v231\n"
      "v_sub_f32 v232, v232, v231\n"
      "v_mfma_f32_16x16x32_bf16 v[168:171], v[34:37], v[44:47], v[168:171]\n"
      "v_sub_f32 v233, v233, v231\n"
      "v_sub_f32 v234, v234, v231\n"
      "v_mfma_f32_16x16x32_bf16 v[226:229], v[164:167], v[252:255], v[106:109]\n"
      "s_waitcnt lgkmcnt(0)\n"
      "v_sub_f32 v235, v235, v231\n"
      "v_sub_f32 v192, v192, v231\n"
      "v_mfma_f32_16x16x32_bf16 v[226:229], v[40:43], v[44:47], v[226:229]\n"
      "v_sub_f32 v193, v193, v231\n"
      "v_sub_f32 v194, v194, v231\n"
      "v_mfma_f32_16x16x32_bf16 v[164:167], v[248:251], v[252:255], v[74:77]\n"
      "v_sub_f32 v195, v195, v231\n"
      "v_sub_f32 v200, v200, v231\n"
      "v_mfma_f32_16x16x32_bf16 v[164:167], v[236:239], v[212:215], v[164:167]\n"
      "v_exp_f32 v204, v204\n"
      "v_mfma_f32_16x16x32_bf16 v[164:167], v[240:243], v[44:47], v[164:167]\n"
      "v_exp_f32 v205, v205\n"
      "v_mfma_f32_16x16x32_bf16 v[180:183], v[212:215], v[252:255], v[110:113]\n"
      "v_exp_f32 v206, v206\n"
      "v_mfma_f32_16x16x32_bf16 v[180:183], v[46:49], v[44:47], v[180:183]\n"
      "v_cvt_pk_bf16_f32 v204, v204, v205\n"
      "v_exp_f32 v207, v207\n"
      "v_mfma_f32_16x16x32_bf16 v[212:215], v[236:239], v[248:251], v[76:79]\n"
      "v_exp_f32 v232, v232\n"
      "v_cvt_pk_bf16_f32 v205, v206, v207\n"
      "v_exp_f32 v233, v233\n"
      "v_exp_f32 v234, v234\n"
      "v_cvt_pk_bf16_f32 v206, v232, v233\n"
      "v_exp_f32 v235, v235\n"
      "v_mfma_f32_16x16x16_bf16 v[212:215], v[242:243], v[240:241], v[212:215]\n"
      "v_exp_f32 v192, v192\n"
      "v_cvt_pk_bf16_f32 v207, v234, v235\n"
      "v_exp_f32 v193, v193\n"
      "v_exp_f32 v194, v194\n"
      "v_exp_f32_e64 v195, v195\n"
      "v_exp_f32_e64 v200, v200\n"
      "v_cvt_pk_bf16_f32 v62, v192, v193\n"
      "v_cvt_pk_bf16_f32 v63, v194, v195\n"
      "v_cvt_pk_bf16_f32 v64, v200, 0\n"
      "v_bitop3_b32 v196, v196, v197, s4 bitop3:0x7e\n"
      "v_bitop3_b32 v198, v198, v199, s4 bitop3:0x7e\n"
      "v_mfma_f32_16x16x32_bf16 v[248:251], v[66:69], v[204:207], 0\n"
      "v_or_b32 v196, v196, v198\n"
      "v_cmp_ne_u32 vcc, 0, v196\n"
      "v_mfma_f32_16x16x32_bf16 v[248:251], v[66:69], v[62:65], v[248:251]\n"
      "s_mov_b64 s[48:49], vcc\n"
      "v_max3_f32 v231, v148, v149, v150\n"
      "v_max3_f32 v230, v151, v140, v141\n"
      "v_mfma_f32_16x16x32_bf16 v[240:243], v[216:219], v[204:207], 0\n"
      "s_cbranch_vccnz L_flg_52_%=\n"
      "L_fb_53_%=:\n"
      "v_mfma_f32_16x16x32_bf16 v[240:243], v[50:53], v[62:65], v[240:243]\n"
      "v_max3_f32 v231, v231, v142, v143\n"
      "v_max3_f32 v230, v230, v152, v153\n"
      "v_mfma_f32_16x16x32_bf16 v[192:195], v[172:175], v[204:207], 0\n"
      "v_max3_f32 v231, v231, v154, v155\n"
      "v_max3_f32 v231, v231, v230, v136\n"
      "v_mfma_f32_16x16x32_bf16 v[192:195], v[54:57], v[62:65], v[192:195]\n"
      "v_mov_b32_e64 v230, v231\n"
      "v_mfma_f32_16x16x32_bf16 v[196:199], v[144:147], v[204:207], 0\n"
      "s_nop 0\n"
      "v_permlane16_swap_b32 v231, v230\n"
      "v_mfma_f32_16x16x32_bf16 v[196:199], v[58:61], v[62:65], v[196:199]\n"
      "v_max_f32 v231, v231, v230\n"
      "v_mov_b32 v230, v231\n"
      "v_rcp_f32 v248, v248\n"
      "s_nop 0\n"
      "v_permlane32_swap_b32 v231, v230\n"
      "v_max_f32 v231, v231, v230\n"
      "v_sub_f32 v148, v148, v231\n"
      "v_sub_f32 v149, v149, v231\n"
      "v_sub_f32 v150, v150, v231\n"
      "v_sub_f32 v151, v151, v231\n"
      "v_sub_f32 v140, v140, v231\n"
      "v_sub_f32 v141, v141, v231\n"
      "v_sub_f32 v142, v142, v231\n"
      "v_sub_f32 v143, v143, v231\n"
      "v_sub_f32 v152, v152, v231\n"
      "v_sub_f32 v153, v153, v231\n"
      "v_sub_f32 v154, v154, v231\n"
      "v_sub_f32 v155, v155, v231\n"
      "v_sub_f32 v136, v136, v231\n"
      "v_exp_f32 v148, v148\n"
      "v_exp_f32 v149, v149\n"
      "v_exp_f32 v150, v150\n"
      "v_cvt_pk_bf16_f32 v148, v148, v149\n"
      "v_exp_f32 v151, v151\n"
      "v_exp_f32 v140, v140\n"
      "v_cvt_pk_bf16_f32 v149, v150, v151\n"
      "v_exp_f32 v141, v141\n"
      "v_exp_f32 v142, v142\n"
      "v_cvt_pk_bf16_f32 v150, v140, v141\n"
      "v_exp_f32 v143, v143\n"
      "v_exp_f32 v152, v152\n"
      "v_cvt_pk_bf16_f32 v151, v142, v143\n"
      "v_exp_f32 v153, v153\n"
      "v_exp_f32 v154, v154\n"
      "v_exp_f32 v155, v155\n"
      "v_exp_f32 v136, v136\n"
      "v_cvt_pk_bf16_f32 v62, v152, v153\n"
      "v_cvt_pk_bf16_f32 v63, v154, v155\n"
      "v_cvt_pk_bf16_f32 v64, v136, 0\n"
      "v_mul_f32 v240, v248, v240\n"
      "v_mul_f32 v241, v248, v241\n"
      "v_mfma_f32_16x16x32_bf16 v[230:233], v[66:69], v[148:151], 0\n"
      "s_add_u32 s28, s27, 2\n"
      "v_cvt_pk_bf16_f32 v240, v240, v241\n"
      "v_mul_f32 v242, v248, v242\n"
      "v_mfma_f32_16x16x32_bf16 v[230:233], v[66:69], v[62:65], v[230:233]\n"
      "s_min_u32 m0, s28, s3\n"
      "v_mul_f32 v243, v248, v243\n"
      "v_cvt_pk_bf16_f32 v241, v242, v243\n"
      "v_mfma_f32_16x16x32_bf16 v[152:155], v[216:219], v[148:151], 0\n"
      "s_movrels_b32 s9, s80\n"
      "v_mul_f32 v192, v248, v192\n"
      "v_mul_f32_e64 v193, v248, v193\n"
      "v_mfma_f32_16x16x32_bf16 v[152:155], v[50:53], v[62:65], v[152:155]\n"
      "s_lshr_b32 s8, s9, 8\n"
      "v_cvt_pk_bf16_f32 v242, v192, v193\n"
      "v_mul_f32 v194, v248, v194\n"
      "v_mfma_f32_16x16x32_bf16 v[190:193], v[172:175], v[148:151], 0\n"
      "s_and_b32 s9, s9, 0xff\n"
      "v_mul_f32_e64 v195, v248, v195\n"
      "v_cvt_pk_bf16_f32 v243, v194, v195\n"
      "v_mfma_f32_16x16x32_bf16 v[190:193], v[54:57], v[62:65], v[190:193]\n"
      "ds_write_b128 v124, v[240:243]\n"
      "v_mfma_f32_16x16x32_bf16 v[140:143], v[144:147], v[148:151], 0\n"
      "s_mul_i32 s10, s8, 73\n"
      "v_mul_f32 v196, v248, v196\n"
      "v_mul_f32 v197, v248, v197\n"
      "v_mfma_f32_16x16x32_bf16 v[140:143], v[58:61], v[62:65], v[140:143]\n"
      "s_add_u32 s10, s10, s9\n"
      "v_cvt_pk_bf16_f32 v196, v196, v197\n"
      "v_mul_f32 v198, v248, v198\n"
      "v_mul_f32_e64 v199, v248, v199\n"
      "v_cvt_pk_bf16_f32 v197, v198, v199\n"
      "v_max3_f32 v231, v168, v169, v170\n"
      "v_max3_f32 v194, v171, v226, v227\n"
      "v_max3_f32 v231, v231, v228, v229\n"
      "v_max3_f32 v194, v194, v180, v181\n"
      "v_max3_f32 v231, v231, v182, v183\n"
      "v_max3_f32 v231, v231, v194, v164\n"
      "v_mov_b32_e64 v194, v231\n"
      "s_mul_i32 s28, s10, 4802\n"
      "s_nop 0\n"
      "v_permlane16_swap_b32 v231, v194\n"
      "v_max_f32 v231, v231, v194\n"
      "v_mov_b32 v194, v231\n"
      "v_cndmask_b32 v201, v201, v137, s[42:43]\n"
      "v_cndmask_b32 v201, v201, v165, s[44:45]\n"
      "v_permlane32_swap_b32 v231, v194\n"
      "v_max_f32 v231, v231, v194\n"
      "v_sub_f32 v168, v168, v231\n"
      "v_sub_f32 v169, v169, v231\n"
      "v_sub_f32 v170, v170, v231\n"
      "v_sub_f32 v171, v171, v231\n"
      "v_sub_f32 v226, v226, v231\n"
      "v_sub_f32 v227, v227, v231\n"
      "v_sub_f32 v228, v228, v231\n"
      "v_sub_f32 v229, v229, v231\n"
      "v_sub_f32 v180, v180, v231\n"
      "v_sub_f32 v181, v181, v231\n"
      "v_sub_f32 v182, v182, v231\n"
      "v_sub_f32 v183, v183, v231\n"
      "v_sub_f32 v164, v164, v231\n"
      "v_exp_f32 v168, v168\n"
      "v_exp_f32 v169, v169\n"
      "v_exp_f32 v170, v170\n"
      "v_cvt_pk_bf16_f32 v168, v168, v169\n"
      "v_exp_f32 v171, v171\n"
      "v_exp_f32 v226, v226\n"
      "v_cvt_pk_bf16_f32 v169, v170, v171\n"
      "v_exp_f32 v227, v227\n"
      "v_exp_f32 v228, v228\n"
      "v_cvt_pk_bf16_f32 v170, v226, v227\n"
      "v_exp_f32 v229, v229\n"
      "v_exp_f32 v180, v180\n"
      "v_cvt_pk_bf16_f32 v171, v228, v229\n"
      "v_exp_f32 v181, v181\n"
      "v_exp_f32 v182, v182\n"
      "v_exp_f32 v183, v183\n"
      "v_exp_f32 v164, v164\n"
      "v_cvt_pk_bf16_f32 v62, v180, v181\n"
      "v_cvt_pk_bf16_f32 v63, v182, v183\n"
      "v_cvt_pk_bf16_f32 v64, v164, 0\n"
      "v_cndmask_b32 v201, v201, v213, s[46:47]\n"
      "v_rcp_f32_e64 v230, v230\n"
      "v_mfma_f32_16x16x32_bf16 v[148:151], v[66:69], v[168:171], 0\n"
      "s_add_u32 s56, s18, s28\n"
      "v_max_f32_dpp v194, v201, v201 row_ror:8 row_mask:0xf bank_mask:0xf\n"
      "v_mul_f32 v152, v230, v152\n"
      "v_mfma_f32_16x16x32_bf16 v[148:151], v[66:69], v[62:65], v[148:151]\n"
      "s_addc_u32 s57, s19, 0\n"
      "v_max_f32_dpp v194, v194, v194 row_ror:4 row_mask:0xf bank_mask:0xf\n"
      "v_mul_f32_e64 v153, v230, v153\n"
      "v_mfma_f32_16x16x32_bf16 v[248:251], v[216:219], v[168:171], 0\n"
      "s_and_b32 s56, s56, -16\n"
      "v_max_f32_dpp v194, v194, v194 row_ror:2 row_mask:0xf bank_mask:0xf\n"
      "v_cvt_pk_bf16_f32 v152, v152, v153\n"
      "v_mfma_f32_16x16x32_bf16 v[248:251], v[50:53], v[62:65], v[248:251]\n"
      "s_sub_u32 s58, s56, 110592\n"
      "v_max_f32_dpp v194, v194, v194 row_ror:1 row_mask:0xf bank_mask:0xf\n"
      "v_mul_f32 v154, v230, v154\n"
      "v_mfma_f32_16x16x32_bf16 v[240:243], v[172:175], v[168:171], 0\n"
      "s_subb_u32 s59, s57, 0\n"
      "v_max_f32_dpp v194, v194, v194 row_bcast:15 row_mask:0xa bank_mask:0xf\n"
      "v_mul_f32_e64 v155, v230, v155\n"
      "v_mfma_f32_16x16x32_bf16 v[240:243], v[54:57], v[62:65], v[240:243]\n"
      "s_sub_u32 s56, s56, 73728\n"
      "v_max_f32_dpp v194, v194, v194 row_bcast:31 row_mask:0xc bank_mask:0xf\n"
      "v_cvt_pk_bf16_f32 v153, v154, v155\n"
      "v_mfma_f32_16x16x32_bf16 v[204:207], v[144:147], v[168:171], 0\n"
      "s_subb_u32 s57, s57, 0\n"
      "v_readlane_b32 s31, v194, 63\n"
      "v_mul_f32 v190, v230, v190\n"
      "v_mfma_f32_16x16x32_bf16 v[204:207], v[58:61], v[62:65], v[204:207]\n"
      "s_mul_i32 s28, s8, 1373568\n"
      "v_subrev_f32 v201, s31, v201\n"
      "v_exp_f32 v201, v201\n"
      "v_mul_f32_e64 v191, v230, v191\n"
      "v_cvt_pk_bf16_f32 v201, v201, v201\n"
      "v_cvt_pk_bf16_f32 v154, v190, v191\n"
      "v_mul_f32 v192, v230, v192\n"
      "v_mul_f32 v193, v230, v193\n"
      "v_cvt_pk_bf16_f32 v155, v192, v193\n"
      "v_mul_f32 v140, v230, v140\n"
      "v_mul_f32 v141, v230, v141\n"
      "v_cvt_pk_bf16_f32 v140, v140, v141\n"
      "v_mul_f32 v142, v230, v142\n"
      "v_mul_f32 v143, v230, v143\n"
      "v_cvt_pk_bf16_f32 v141, v142, v143\n"
      "s_mul_i32 s29, s9, 2688\n"
      "ds_write_b16 v130, v201\n"
      "v_mov_b32 v198, 0\n"
      "s_add_u32 s28, s28, s29\n"
      "ds_read_b128 v[228:231], v131\n"
      "ds_read_b128 v[180:183], v131 offset:64\n"
      "ds_write_b128 v124, v[152:155] offset:1024\n"
      "ds_write2st64_b64 v125, v[196:197], v[140:141] offset0:0 offset1:2\n"
      "ds_write_b32 v129, v198\n"
      "ds_read_b128 v[168:171], v114 offset:18432\n"
      "ds_read_b128 v[192:195], v115 offset:18432\n"
      "s_cmp_eq_u32 s8, 72\n"
      "s_cselect_b32 s8, 0, 100270464\n"
      "s_cmp_eq_u32 s9, 72\n"
      "s_cselect_b32 s9, 0, 196224\n"
      "s_add_u32 s10, s8, s9\n"
      "s_add_u32 s28, s28, s10\n"
      "s_add_u32 s52, s16, s28\n"
      "s_addc_u32 s53, s17, 0\n"
      "s_cmp_lg_u32 s10, s11\n"
      "s_cbranch_scc1 L_edge_55_%=\n"
      "L_eb_56_%=:\n"
      "s_waitcnt lgkmcnt(5)\n"
      "v_mfma_f32_16x16x32_bf16 v[152:155], v[228:231], v[66:69], 0\n"
      "v_mfma_f32_16x16x32_bf16 v[152:155], v[180:183], v[66:69], v[152:155]\n"
      "v_mfma_f32_16x16x32_bf16 v[140:143], v[228:231], v[216:219], 0\n"
      "v_mfma_f32_16x16x32_bf16 v[140:143], v[180:183], v[50:53], v[140:143]\n"
      "v_mfma_f32_16x16x32_bf16 v[216:219], v[228:231], v[172:175], 0\n"
      "s_waitcnt lgkmcnt(0)\n"
      "v_mfma_f32_16x16x32_bf16 v[216:219], v[180:183], v[54:57], v[216:219]\n"
      "v_mfma_f32_16x16x32_bf16 v[172:175], v[228:231], v[144:147], 0\n"
      "s_nop 1\n"
      "v_mov_b32_dpp v152, v152 quad_perm:[0,0,0,0] row_mask:0xf bank_mask:0xf\n"
      "v_mfma_f32_16x16x32_bf16 v[172:175], v[180:183], v[58:61], v[172:175]\n"
      "v_rcp_f32 v152, v152\n"
      "v_mfma_f32_16x16x32_bf16 v[196:199], a[0:3], v[168:171], 0\n"
      "s_nop 0\n"
      "v_cndmask_b32 v140, v140, v216, s[42:43]\n"
      "v_mfma_f32_16x16x32_bf16 v[196:199], a[36:39], v[192:195], v[196:199]\n"
      "s_nop 0\n"
      "v_mfma_f32_16x16x32_bf16 v[228:231], a[4:7], v[168:171], 0\n"
      "s_nop 1\n"
      "v_cndmask_b32 v140, v140, v172, s[44:45]\n"
      "v_mfma_f32_16x16x32_bf16 v[228:231], a[40:43], v[192:195], v[228:231]\n"
      "v_mul_f32 v140, v152, v140\n"
      "v_cvt_pk_bf16_f32 v140, v140, v140\n"
      "ds_write_b16 v135, v140\n"
      "s_branch L_join_10_%=\n"
      "L_flg_52_%=:\n"
      "v_cmp_gt_i32 vcc, 0, v196\n"
      "s_cbranch_vccz L_fb_53_%=\n"
      "s_sleep 1\n"
      "ds_read_b128 v[196:199], v128 offset:3072\n"
      "s_waitcnt lgkmcnt(0)\n"
      "v_bitop3_b32 v196, v196, v197, s4 bitop3:0x7e\n"
      "v_bitop3_b32 v198, v198, v199, s4 bitop3:0x7e\n"
      "v_or_b32 v196, v196, v198\n"
      "v_cmp_ne_u32 s[48:49], 0, v196\n"
      "s_branch L_flg_52_%=\n"
      "L_edge_55_%=:\n"
      "s_mov_b32 s11, s10\n"
      "s_sub_u32 s28, s60, s8\n"
      "s_sub_u32 s29, s61, s9\n"
      "s_mov_b32 s60, s8\n"
      "s_mov_b32 s61, s9\n"
      "s_mov_b64 exec, s[64:65]\n"
      "v_add_u32 v116, s28, v116\n"
      "s_mov_b64 exec, s[70:71]\n"
      "v_add_u32 v116, s29, v116\n"
      "s_mov_b64 exec, s[66:67]\n"
      "v_add_u32 v117, s28, v117\n"
      "s_mov_b64 exec, s[72:73]\n"
      "v_add_u32 v117, s29, v117\n"
      "s_mov_b64 exec, s[68:69]\n"
      "v_add_u32 v118, s28, v118\n"
      "s_mov_b64 exec, s[74:75]\n"
      "v_add_u32_e64 v118, s29, v118\n"
      "s_mov_b64 exec, -1\n"
      "s_nop 4\n"
      "s_branch L_eb_56_%=\n"
      "L_gen_9_%=:\n"
      "s_waitcnt lgkmcnt(0)\n"
      "s_nop 7\n"
      "s_nop 7\n"
      "v_mfma_f32_16x16x32_bf16 v[148:151], v[180:183], v[168:171], v[90:93]\n"
      "s_movk_i32 s31, 0x3fb9\n"
      "v_mbcnt_lo_u32_b32 v196, -1, 0\n"
      "v_mbcnt_hi_u32_b32 v196, -1, v196\n"
      "v_mfma_f32_16x16x32_bf16 v[148:151], v[34:37], v[38:41], v[148:151]\n"
      "v_and_b32 v197, 15, v196\n"
      "v_lshrrev_b32 v198, 4, v196\n"
      "v_mfma_f32_16x16x32_bf16 v[228:231], v[164:167], v[168:171], v[94:97]\n"
      "v_mul_u32_u24 v199, 0x62, v197\n"
      "v_lshl_add_u32 v199, v198, 3, v199\n"
      "v_mfma_f32_16x16x32_bf16 v[228:231], v[40:43], v[38:41], v[228:231]\n"
      "v_add_u32 v199, s30, v199\n"
      "v_and_b32 v196, -4, v199\n"
      "v_and_b32 v199, 2, v199\n"
      "v_lshlrev_b32 v198, 2, v198\n"
      "v_sub_u32 v197, v197, v198\n"
      "v_and_b32 v198, 1, v197\n"
      "v_lshlrev_b32_e64 v198, 4, v198\n"
      "v_lshlrev_b32 v198, v198, s31\n"
      "v_lshrrev_b32_e64 v197, 1, v197\n"
      "v_cmp_eq_u32 vcc, 0, v197\n"
      "s_nop 1\n"
      "v_cndmask_b32 v202, 0, v198, vcc\n"
      "v_cmp_eq_u32 vcc, 1, v197\n"
      "s_nop 1\n"
      "v_cndmask_b32 v203, 0, v198, vcc\n"
      "v_mfma_f32_16x16x32_bf16 v[136:139], v[248:251], v[168:171], v[72:75]\n"
      "ds_read2_b32 v[140:141], v196 offset0:0 offset1:1\n"
      "v_mfma_f32_16x16x32_bf16 v[136:139], v[236:239], v[164:167], v[136:139]\n"
      "ds_read2_b32 v[142:143], v196 offset0:8 offset1:9\n"
      "v_mfma_f32_16x16x32_bf16 v[136:139], v[240:243], v[38:41], v[136:139]\n"
      "ds_read2_b32 v[152:153], v196 offset0:16 offset1:17\n"
      "ds_read2_b32 v[154:155], v196 offset0:2 offset1:10\n"
      "ds_read2_b32 v[156:157], v196 offset0:18 offset1:24\n"
      "v_add_u32 v196, 1568, v196\n"
      "ds_read2_b32 v[158:159], v196 offset0:0 offset1:1\n"
      "ds_read2_b32 v[160:161], v196 offset0:8 offset1:9\n"
      "ds_read2_b32 v[162:163], v196 offset0:16 offset1:17\n"
      "ds_read2_b32 v[176:177], v196 offset0:2 offset1:10\n"
      "ds_read2_b32 v[178:179], v196 offset0:18 offset1:24\n"
      "v_add_u32 v196, 1568, v196\n"
      "ds_read2_b32 v[138:139], v196 offset0:0 offset1:1\n"
      "ds_read2_b32 v[184:185], v196 offset0:8 offset1:9\n"
      "ds_read2_b32 v[186:187], v196 offset0:16 offset1:17\n"
      "ds_read2_b32 v[188:189], v196 offset0:2 offset1:10\n"
      "ds_read2_b32 v[190:191], v196 offset0:18 offset1:24\n"
      "s_waitcnt lgkmcnt(13)\n"
      "v_alignbyte_b32 v140, v141, v140, v199\n"
      "v_alignbyte_b32 v142, v143, v142, v199\n"
      "s_waitcnt lgkmcnt(12)\n"
      "v_alignbyte_b32 v152, v153, v152, v199\n"
      "s_waitcnt lgkmcnt(11)\n"
      "v_alignbyte_b32 v141, v154, v141, v199\n"
      "v_alignbyte_b32 v143, v155, v143, v199\n"
      "v_mfma_f32_16x16x32_bf16 v[208:211], v[212:215], v[168:171], v[98:101]\n"
      "s_waitcnt lgkmcnt(10)\n"
      "v_alignbyte_b32 v153, v156, v153, v199\n"
      "v_alignbyte_b32 v157, v157, v157, v199\n"
      "v_mfma_f32_16x16x32_bf16 v[208:211], v[46:49], v[38:41], v[208:211]\n"
      "s_waitcnt lgkmcnt(9)\n"
      "v_lshlrev_b32 v157, 16, v157\n"
      "v_fmac_f32_e64 v200, s37, v157\n"
      "v_mfma_f32_16x16x16_bf16 v[204:207], v[202:203], v[140:141], v[204:207]\n"
      "ds_read_b128 v[168:171], v128 offset:3072\n"
      "v_alignbyte_b32 v158, v159, v158, v199\n"
      "v_mfma_f32_16x16x16_bf16 v[232:235], v[202:203], v[142:143], v[232:235]\n"
      "s_waitcnt lgkmcnt(6)\n"
      "v_alignbyte_b32 v159, v176, v159, v199\n"
      "v_mfma_f32_16x16x16_bf16 v[192:195], v[202:203], v[152:153], v[192:195]\n"
      "s_nop 0\n"
      "v_alignbyte_b32 v160, v161, v160, v199\n"
      "v_alignbyte_b32 v161, v177, v161, v199\n"
      "v_mfma_f32_16x16x32_bf16 v[140:143], v[180:183], v[252:255], v[102:105]\n"
      "v_alignbyte_b32 v162, v163, v162, v199\n"
      "v_max3_f32 v198, v204, v205, v206\n"
      "v_mfma_f32_16x16x32_bf16 v[140:143], v[34:37], v[44:47], v[140:143]\n"
      "v_alignbyte_b32 v163, v178, v163, v199\n"
      "v_alignbyte_b32 v179, v179, v179, v199\n"
      "v_mfma_f32_16x16x32_bf16 v[180:183], v[164:167], v[252:255], v[106:109]\n"
      "s_waitcnt lgkmcnt(1)\n"
      "v_lshlrev_b32 v179, 16, v179\n"
      "v_alignbyte_b32 v138, v139, v138, v199\n"
      "v_mfma_f32_16x16x32_bf16 v[180:183], v[40:43], v[44:47], v[180:183]\n"
      "v_alignbyte_b32 v139, v188, v139, v199\n"
      "v_max3_f32 v197, v207, v232, v233\n"
      "v_mfma_f32_16x16x32_bf16 v[164:167], v[248:251], v[252:255], v[74:77]\n"
      "v_alignbyte_b32 v184, v185, v184, v199\n"
      "v_alignbyte_b32 v185, v189, v185, v199\n"
      "v_mfma_f32_16x16x32_bf16 v[164:167], v[236:239], v[212:215], v[164:167]\n"
      "v_alignbyte_b32 v186, v187, v186, v199\n"
      "v_max3_f32 v198, v198, v234, v235\n"
      "v_mfma_f32_16x16x32_bf16 v[164:167], v[240:243], v[44:47], v[164:167]\n"
      "v_alignbyte_b32 v187, v190, v187, v199\n"
      "v_alignbyte_b32 v191, v191, v191, v199\n"
      "v_mfma_f32_16x16x32_bf16 v[152:155], v[212:215], v[252:255], v[110:113]\n"
      "v_lshlrev_b32_e64 v191, 16, v191\n"
      "v_max3_f32 v197, v197, v192, v193\n"
      "v_mfma_f32_16x16x32_bf16 v[152:155], v[46:49], v[44:47], v[152:155]\n"
      "v_max3_f32 v198, v198, v194, v195\n"
      "v_max3_f32 v198, v198, v197, v200\n"
      "v_mfma_f32_16x16x32_bf16 v[212:215], v[236:239], v[248:251], v[76:79]\n"
      "v_mov_b32 v197, v198\n"
      "s_nop 1\n"
      "v_permlane16_swap_b32 v198, v197\n"
      "v_max_f32 v198, v198, v197\n"
      "v_mfma_f32_16x16x16_bf16 v[148:151], v[202:203], v[158:159], v[148:151]\n"
      "v_mov_b32 v197, v198\n"
      "v_mfma_f32_16x16x16_bf16 v[212:215], v[242:243], v[240:241], v[212:215]\n"
      "s_nop 0\n"
      "v_permlane32_swap_b32_e64 v198, v197\n"
      "v_mfma_f32_16x16x16_bf16 v[228:231], v[202:203], v[160:161], v[228:231]\n"
      "v_max_f32 v198, v198, v197\n"
      "v_sub_f32 v204, v204, v198\n"
      "v_mfma_f32_16x16x16_bf16 v[208:211], v[202:203], v[162:163], v[208:211]\n"
      "v_sub_f32 v205, v205, v198\n"
      "v_sub_f32 v206, v206, v198\n"
      "v_sub_f32 v207, v207, v198\n"
      "v_sub_f32 v232, v232, v198\n"
      "v_sub_f32 v233, v233, v198\n"
      "v_sub_f32 v234, v234, v198\n"
      "v_sub_f32 v235, v235, v198\n"
      "v_sub_f32 v192, v192, v198\n"
      "v_sub_f32 v193, v193, v198\n"
      "v_sub_f32 v194, v194, v198\n"
      "v_sub_f32 v195, v195, v198\n"
      "v_sub_f32 v200, v200, v198\n"
      "v_exp_f32 v204, v204\n"
      "v_exp_f32 v205, v205\n"
      "v_exp_f32 v206, v206\n"
      "v_cvt_pk_bf16_f32 v204, v204, v205\n"
      "v_exp_f32 v207, v207\n"
      "v_exp_f32 v232, v232\n"
      "v_cvt_pk_bf16_f32 v205, v206, v207\n"
      "v_exp_f32 v233, v233\n"
      "v_exp_f32 v234, v234\n"
      "v_cvt_pk_bf16_f32 v206, v232, v233\n"
      "v_exp_f32 v235, v235\n"
      "v_exp_f32 v192, v192\n"
      "v_cvt_pk_bf16_f32 v207, v234, v235\n"
      "v_mfma_f32_16x16x16_bf16 v[140:143], v[202:203], v[138:139], v[140:143]\n"
      "s_waitcnt lgkmcnt(0)\n"
      "v_exp_f32_e64 v193, v193\n"
      "v_mfma_f32_16x16x16_bf16 v[180:183], v[202:203], v[184:185], v[180:183]\n"
      "v_exp_f32 v194, v194\n"
      "v_mfma_f32_16x16x16_bf16 v[152:155], v[202:203], v[186:187], v[152:155]\n"
      "v_exp_f32 v195, v195\n"
      "v_exp_f32 v200, v200\n"
      "v_fmac_f32 v136, s37, v179\n"
      "v_cvt_pk_bf16_f32 v62, v192, v193\n"
      "v_cvt_pk_bf16_f32 v63, v194, v195\n"
      "v_cvt_pk_bf16_f32 v64, v200, 0\n"
      "v_bitop3_b32 v168, v168, v169, s4 bitop3:0x7e\n"
      "v_bitop3_b32 v170, v170, v171, s4 bitop3:0x7e\n"
      "v_mfma_f32_16x16x32_bf16 v[248:251], v[66:69], v[204:207], 0\n"
      "v_or_b32 v168, v168, v170\n"
      "v_cmp_ne_u32 vcc, 0, v168\n"
      "v_mfma_f32_16x16x32_bf16 v[248:251], v[66:69], v[62:65], v[248:251]\n"
      "s_mov_b64 s[48:49], vcc\n"
      "v_max3_f32 v198, v148, v149, v150\n"
      "v_max3_f32 v197, v151, v228, v229\n"
      "v_mfma_f32_16x16x32_bf16 v[240:243], v[216:219], v[204:207], 0\n"
      "s_cbranch_vccnz L_flg_63_%=\n"
      "L_fb_64_%=:\n"
      "v_mfma_f32_16x16x32_bf16 v[240:243], v[50:53], v[62:65], v[240:243]\n"
      "v_max3_f32 v198, v198, v230, v231\n"
      "v_max3_f32 v197, v197, v208, v209\n"
      "v_mfma_f32_16x16x32_bf16 v[168:171], v[172:175], v[204:207], 0\n"
      "v_max3_f32 v198, v198, v210, v211\n"
      "v_max3_f32 v198, v198, v197, v136\n"
      "v_mfma_f32_16x16x32_bf16 v[168:171], v[54:57], v[62:65], v[168:171]\n"
      "v_mov_b32 v197, v198\n"
      "v_fmac_f32 v164, s37, v191\n"
      "v_mfma_f32_16x16x32_bf16 v[192:195], v[144:147], v[204:207], 0\n"
      "v_permlane16_swap_b32 v198, v197\n"
      "v_mfma_f32_16x16x32_bf16 v[192:195], v[58:61], v[62:65], v[192:195]\n"
      "v_max_f32 v198, v198, v197\n"
      "v_mov_b32 v197, v198\n"
      "s_nop 1\n"
      "v_permlane32_swap_b32 v198, v197\n"
      "v_max_f32 v198, v198, v197\n"
      "v_sub_f32 v148, v148, v198\n"
      "v_sub_f32 v149, v149, v198\n"
      "v_sub_f32 v150, v150, v198\n"
      "v_sub_f32 v151, v151, v198\n"
      "v_sub_f32 v228, v228, v198\n"
      "v_sub_f32 v229, v229, v198\n"
      "v_sub_f32 v230, v230, v198\n"
      "v_sub_f32 v231, v231, v198\n"
      "v_sub_f32 v208, v208, v198\n"
      "v_sub_f32 v209, v209, v198\n"
      "v_sub_f32 v210, v210, v198\n"
      "v_sub_f32 v211, v211, v198\n"
      "v_sub_f32 v136, v136, v198\n"
      "v_exp_f32 v148, v148\n"
      "v_exp_f32 v149, v149\n"
      "v_exp_f32 v150, v150\n"
      "v_cvt_pk_bf16_f32 v148, v148, v149\n"
      "v_exp_f32 v151, v151\n"
      "v_exp_f32 v228, v228\n"
      "v_cvt_pk_bf16_f32 v149, v150, v151\n"
      "v_exp_f32 v229, v229\n"
      "v_exp_f32 v230, v230\n"
      "v_cvt_pk_bf16_f32 v150, v228, v229\n"
      "v_exp_f32 v231, v231\n"
      "v_exp_f32 v208, v208\n"
      "v_cvt_pk_bf16_f32 v151, v230, v231\n"
      "v_exp_f32 v209, v209\n"
      "v_exp_f32 v210, v210\n"
      "v_exp_f32 v211, v211\n"
      "v_exp_f32 v136, v136\n"
      "v_cvt_pk_bf16_f32 v62, v208, v209\n"
      "v_cvt_pk_bf16_f32 v63, v210, v211\n"
      "v_cvt_pk_bf16_f32 v64, v136, 0\n"
      "v_rcp_f32 v248, v248\n"
      "v_max3_f32 v249, v140, v141, v142\n"
      "v_mfma_f32_16x16x32_bf16 v[228:231], v[66:69], v[148:151], 0\n"
      "v_mul_f32 v240, v248, v240\n"
      "v_mul_f32 v241, v248, v241\n"
      "v_mfma_f32_16x16x32_bf16 v[228:231], v[66:69], v[62:65], v[228:231]\n"
      "s_add_u32 s28, s27, 2\n"
      "v_cvt_pk_bf16_f32 v240, v240, v241\n"
      "v_mul_f32_e64 v242, v248, v242\n"
      "v_mfma_f32_16x16x32_bf16 v[208:211], v[216:219], v[148:151], 0\n"
      "s_min_u32 m0, s28, s3\n"
      "v_mul_f32 v243, v248, v243\n"
      "v_cvt_pk_bf16_f32 v241, v242, v243\n"
      "v_mfma_f32_16x16x32_bf16 v[208:211], v[50:53], v[62:65], v[208:211]\n"
      "s_movrels_b32 s9, s80\n"
      "v_mul_f32 v168, v248, v168\n"
      "v_mul_f32 v169, v248, v169\n"
      "v_mfma_f32_16x16x32_bf16 v[230:233], v[172:175], v[148:151], 0\n"
      "s_lshr_b32 s8, s9, 8\n"
      "v_cvt_pk_bf16_f32 v242, v168, v169\n"
      "v_mul_f32 v170, v248, v170\n"
      "v_mfma_f32_16x16x32_bf16 v[230:233], v[54:57], v[62:65], v[230:233]\n"
      "s_and_b32 s9, s9, 0xff\n"
      "v_mul_f32 v171, v248, v171\n"
      "v_cvt_pk_bf16_f32 v243, v170, v171\n"
      "v_mfma_f32_16x16x32_bf16 v[168:171], v[144:147], v[148:151], 0\n"
      "s_mul_i32 s10, s8, 73\n"
      "v_mul_f32 v192, v248, v192\n"
      "v_mul_f32 v193, v248, v193\n"
      "v_mfma_f32_16x16x32_bf16 v[168:171], v[58:61], v[62:65], v[168:171]\n"
      "ds_write_b128 v124, v[240:243]\n"
      "v_cvt_pk_bf16_f32 v192, v192, v193\n"
      "v_mul_f32 v194, v248, v194\n"
      "v_mul_f32 v195, v248, v195\n"
      "v_cvt_pk_bf16_f32 v193, v194, v195\n"
      "v_max3_f32 v229, v143, v180, v181\n"
      "v_max3_f32 v249, v249, v182, v183\n"
      "v_max3_f32 v229, v229, v152, v153\n"
      "v_max3_f32 v249, v249, v154, v155\n"
      "v_max3_f32 v249, v249, v229, v164\n"
      "v_mov_b32 v229, v249\n"
      "s_add_u32 s10, s10, s9\n"
      "s_nop 0\n"
      "v_permlane16_swap_b32 v249, v229\n"
      "v_max_f32 v249, v249, v229\n"
      "v_mov_b32 v229, v249\n"
      "v_cndmask_b32 v201, v201, v137, s[42:43]\n"
      "v_cndmask_b32 v201, v201, v165, s[44:45]\n"
      "v_permlane32_swap_b32 v249, v229\n"
      "v_max_f32 v249, v249, v229\n"
      "v_sub_f32 v140, v140, v249\n"
      "v_sub_f32 v141, v141, v249\n"
      "v_sub_f32 v142, v142, v249\n"
      "v_sub_f32 v143, v143, v249\n"
      "v_sub_f32 v180, v180, v249\n"
      "v_sub_f32 v181, v181, v249\n"
      "v_sub_f32 v182, v182, v249\n"
      "v_sub_f32 v183, v183, v249\n"
      "v_sub_f32 v152, v152, v249\n"
      "v_sub_f32 v153, v153, v249\n"
      "v_sub_f32 v154, v154, v249\n"
      "v_sub_f32 v155, v155, v249\n"
      "v_sub_f32 v164, v164, v249\n"
      "v_exp_f32 v140, v140\n"
      "v_exp_f32 v141, v141\n"
      "v_exp_f32 v142, v142\n"
      "v_cvt_pk_bf16_f32 v140, v140, v141\n"
      "v_exp_f32 v143, v143\n"
      "v_exp_f32 v180, v180\n"
      "v_cvt_pk_bf16_f32 v141, v142, v143\n"
      "v_exp_f32 v181, v181\n"
      "v_exp_f32 v182, v182\n"
      "v_cvt_pk_bf16_f32 v142, v180, v181\n"
      "v_exp_f32 v183, v183\n"
      "v_exp_f32 v152, v152\n"
      "v_cvt_pk_bf16_f32 v143, v182, v183\n"
      "v_exp_f32 v153, v153\n"
      "v_exp_f32 v154, v154\n"
      "v_exp_f32 v155, v155\n"
      "v_exp_f32 v164, v164\n"
      "v_cvt_pk_bf16_f32 v62, v152, v153\n"
      "v_cvt_pk_bf16_f32 v63, v154, v155\n"
      "v_cvt_pk_bf16_f32 v64, v164, 0\n"
      "v_cndmask_b32 v201, v201, v213, s[46:47]\n"
      "v_mbcnt_lo_u32_b32 v196, -1, 0\n"
      "v_mfma_f32_16x16x32_bf16 v[148:151], v[66:69], v[140:143], 0\n"
      "s_mul_i32 s28, s10, 4802\n"
      "v_mbcnt_hi_u32_b32 v196, -1, v196\n"
      "v_min_u32 v196, 48, v196\n"
      "v_mfma_f32_16x16x32_bf16 v[148:151], v[66:69], v[62:65], v[148:151]\n"
      "s_add_u32 s56, s18, s28\n"
      "v_lshlrev_b32 v196, 1, v196\n"
      "v_add_u32 v196, s30, v196\n"
      "v_mfma_f32_16x16x32_bf16 v[248:251], v[216:219], v[140:143], 0\n"
      "ds_read_u16 v229, v196 offset:4704\n"
      "s_addc_u32 s57, s19, 0\n"
      "v_mfma_f32_16x16x32_bf16 v[248:251], v[50:53], v[62:65], v[248:251]\n"
      "s_and_b32 s56, s56, -16\n"
      "v_rcp_f32_e64 v228, v228\n"
      "v_mfma_f32_16x16x32_bf16 v[240:243], v[172:175], v[140:143], 0\n"
      "s_sub_u32 s58, s56, 110592\n"
      "v_mul_f32 v208, v228, v208\n"
      "v_mul_f32 v209, v228, v209\n"
      "v_mfma_f32_16x16x32_bf16 v[240:243], v[54:57], v[62:65], v[240:243]\n"
      "s_subb_u32 s59, s57, 0\n"
      "v_cvt_pk_bf16_f32 v208, v208, v209\n"
      "v_mul_f32 v210, v228, v210\n"
      "v_mfma_f32_16x16x32_bf16 v[204:207], v[144:147], v[140:143], 0\n"
      "s_sub_u32 s56, s56, 73728\n"
      "v_mul_f32 v211, v228, v211\n"
      "v_cvt_pk_bf16_f32 v209, v210, v211\n"
      "v_mfma_f32_16x16x32_bf16 v[204:207], v[58:61], v[62:65], v[204:207]\n"
      "s_subb_u32 s57, s57, 0\n"
      "v_mul_f32 v230, v228, v230\n"
      "v_mul_f32 v231, v228, v231\n"
      "v_cvt_pk_bf16_f32 v210, v230, v231\n"
      "v_mul_f32 v232, v228, v232\n"
      "v_mul_f32 v233, v228, v233\n"
      "v_cvt_pk_bf16_f32 v211, v232, v233\n"
      "v_mul_f32 v168, v228, v168\n"
      "v_mul_f32 v169, v228, v169\n"
      "v_cvt_pk_bf16_f32 v168, v168, v169\n"
      "v_mul_f32 v170, v228, v170\n"
      "s_mul_i32 s28, s8, 1373568\n"
      "ds_write_b128 v124, v[208:211] offset:1024\n"
      "s_waitcnt lgkmcnt(1)\n"
      "v_lshlrev_b32_e64 v229, 16, v229\n"
      "v_fmac_f32_e64 v201, s37, v229\n"
      "v_mul_f32_e64 v171, v228, v171\n"
      "v_cvt_pk_bf16_f32 v169, v170, v171\n"
      "v_max_f32_dpp v170, v201, v201 row_ror:8 row_mask:0xf bank_mask:0xf\n"
      "s_mul_i32 s29, s9, 2688\n"
      "s_nop 0\n"
      "v_max_f32_dpp v170, v170, v170 row_ror:4 row_mask:0xf bank_mask:0xf\n"
      "s_add_u32 s28, s28, s29\n"
      "s_nop 0\n"
      "v_max_f32_dpp v170, v170, v170 row_ror:2 row_mask:0xf bank_mask:0xf\n"
      "s_cmp_eq_u32 s8, 72\n"
      "s_nop 0\n"
      "v_max_f32_dpp v170, v170, v170 row_ror:1 row_mask:0xf bank_mask:0xf\n"
      "s_cselect_b32 s8, 0, 100270464\n"
      "s_nop 0\n"
      "v_max_f32_dpp v170, v170, v170 row_bcast:15 row_mask:0xa bank_mask:0xf\n"
      "s_cmp_eq_u32 s9, 72\n"
      "s_nop 0\n"
      "v_max_f32_dpp v170, v170, v170 row_bcast:31 row_mask:0xc bank_mask:0xf\n"
      "s_nop 0\n"
      "v_readlane_b32 s31, v170, 63\n"
      "s_cselect_b32 s9, 0, 196224\n"
      "s_nop 0\n"
      "v_subrev_f32 v201, s31, v201\n"
      "v_exp_f32 v201, v201\n"
      "s_nop 0\n"
      "v_cvt_pk_bf16_f32 v201, v201, v201\n"
      "s_add_u32 s10, s8, s9\n"
      "ds_write_b16 v130, v201\n"
      "v_mov_b32 v170, 0\n"
      "s_add_u32 s28, s28, s10\n"
      "ds_read_b128 v[228:231], v131\n"
      "ds_read_b128 v[196:199], v131 offset:64\n"
      "ds_write2st64_b64 v125, v[192:193], v[168:169] offset0:0 offset1:2\n"
      "ds_write_b32 v129, v170\n"
      "ds_read_b128 v[168:171], v114 offset:18432\n"
      "ds_read_b128 v[192:195], v115 offset:18432\n"
      "s_add_u32 s52, s16, s28\n"
      "s_addc_u32 s53, s17, 0\n"
      "s_cmp_lg_u32 s10, s11\n"
      "s_cbranch_scc1 L_edge_66_%=\n"
      "L_eb_67_%=:\n"
      "s_waitcnt lgkmcnt(4)\n"
      "v_mfma_f32_16x16x32_bf16 v[208:211], v[228:231], v[66:69], 0\n"
      "v_mfma_f32_16x16x32_bf16 v[208:211], v[196:199], v[66:69], v[208:211]\n"
      "s_nop 0\n"
      "v_mfma_f32_16x16x32_bf16 v[140:143], v[228:231], v[216:219], 0\n"
      "v_mfma_f32_16x16x32_bf16 v[140:143], v[196:199], v[50:53], v[140:143]\n"
      "s_waitcnt lgkmcnt(0)\n"
      "v_mfma_f32_16x16x32_bf16 v[216:219], v[228:231], v[172:175], 0\n"
      "s_nop 0\n"
      "v_mfma_f32_16x16x32_bf16 v[216:219], v[196:199], v[54:57], v[216:219]\n"
      "s_nop 0\n"
      "v_mfma_f32_16x16x32_bf16 v[172:175], v[228:231], v[144:147], 0\n"
      "s_nop 1\n"
      "v_mov_b32_dpp v208, v208 quad_perm:[0,0,0,0] row_mask:0xf bank_mask:0xf\n"
      "v_mfma_f32_16x16x32_bf16 v[172:175], v[196:199], v[58:61], v[172:175]\n"
      "v_rcp_f32 v208, v208\n"
      "v_mfma_f32_16x16x32_bf16 v[196:199], a[0:3], v[168:171], 0\n"
      "s_nop 0\n"
      "v_cndmask_b32 v140, v140, v216, s[42:43]\n"
      "v_mfma_f32_16x16x32_bf16 v[196:199], a[36:39], v[192:195], v[196:199]\n"
      "s_nop 0\n"
      "v_mfma_f32_16x16x32_bf16 v[228:231], a[4:7], v[168:171], 0\n"
      "s_nop 1\n"
      "v_cndmask_b32 v140, v140, v172, s[44:45]\n"
      "v_mfma_f32_16x16x32_bf16 v[228:231], a[40:43], v[192:195], v[228:231]\n"
      "v_mul_f32 v140, v208, v140\n"
      "v_cvt_pk_bf16_f32 v140, v140, v140\n"
      "ds_write_b16 v135, v140\n"
      "s_branch L_join_10_%=\n"
      "L_flg_63_%=:\n"
      "v_cmp_gt_i32 vcc, 0, v168\n"
      "s_cbranch_vccz L_fb_64_%=\n"
      "s_sleep 1\n"
      "ds_read_b128 v[168:171], v128 offset:3072\n"
      "s_waitcnt lgkmcnt(0)\n"
      "v_bitop3_b32 v168, v168, v169, s4 bitop3:0x7e\n"
      "v_bitop3_b32 v170, v170, v171, s4 bitop3:0x7e\n"
      "v_or_b32 v168, v168, v170\n"
      "v_cmp_ne_u32 s[48:49], 0, v168\n"
      "s_branch L_flg_63_%=\n"
      "L_edge_66_%=:\n"
      "s_mov_b32 s11, s10\n"
      "s_sub_u32 s28, s60, s8\n"
      "s_sub_u32 s29, s61, s9\n"
      "s_mov_b32 s60, s8\n"
      "s_mov_b32 s61, s9\n"
      "s_mov_b64 exec, s[64:65]\n"
      "v_add_u32 v116, s28, v116\n"
      "s_mov_b64 exec, s[70:71]\n"
      "v_add_u32 v116, s29, v116\n"
      "s_mov_b64 exec, s[66:67]\n"
      "v_add_u32 v117, s28, v117\n"
      "s_mov_b64 exec, s[72:73]\n"
      "v_add_u32 v117, s29, v117\n"
      "s_mov_b64 exec, s[68:69]\n"
      "v_add_u32 v118, s28, v118\n"
      "s_mov_b64 exec, s[74:75]\n"
      "v_add_u32 v118, s29, v118\n"
      "s_mov_b64 exec, -1\n"
      "s_nop 4\n"
      "s_branch L_eb_67_%=\n"
      "L_gen_19_%=:\n"
      "s_waitcnt lgkmcnt(0)\n"
      "s_nop 7\n"
      "s_nop 7\n"
      "s_cmp_eq_u64 s[48:49], 0\n"
      "s_cbranch_scc1 L_go_77_%=\n"
      "s_mov_b32 m0, s27\n"
      "s_nop 0\n"
      "s_movrels_b32 s9, s80\n"
      "s_lshr_b32 s8, s9, 8\n"
      "s_and_b32 s9, s9, 0xff\n"
      "s_mul_i32 s10, s8, 73\n"
      "s_add_u32 s10, s10, s9\n"
      "s_mul_i32 s30, s10, 4802\n"
      "s_add_u32 s30, s30, s18\n"
      "s_and_b32 s29, s30, 15\n"
      "s_add_u32 s30, s29, 115456\n"
      "s_lshl_b64 s[34:35], s[48:49], 4\n"
      "s_lshr_b64 s[34:35], s[34:35], 5\n"
      "s_cbranch_scc1 L_gen_20_%=\n"
      "v_mbcnt_lo_u32_b32 v148, -1, 0\n"
      "v_mbcnt_hi_u32_b32 v148, -1, v148\n"
      "v_lshlrev_b32 v149, 1, v148\n"
      "v_add_u32 v150, 4802, v149\n"
      "v_cmp_gt_u32 vcc, s29, v149\n"
      "s_nop 1\n"
      "v_cndmask_b32 v149, v150, v149, vcc\n"
      "v_add_u32 v149, 115456, v149\n"
      "v_mov_b32 v150, 0\n"
      "s_mov_b64 exec, 0x7f\n"
      "ds_write_b16 v149, v150\n"
      "s_mov_b64 exec, -1\n"
      "v_lshlrev_b32 v149, 4, v148\n"
      "v_add_u32 v149, 115456, v149\n"
      "v_min_u32 v148, 116160, v149\n"
      "ds_read_b128 v[212:215], v149\n"
      "ds_read_b128 v[172:175], v149 offset:1024\n"
      "ds_read_b128 v[204:207], v149 offset:2048\n"
      "ds_read_b128 v[160:163], v149 offset:3072\n"
      "ds_read_b128 v[144:147], v148 offset:4096\n"
      "s_waitcnt lgkmcnt(4)\n"
      "v_or3_b32 v212, v212, v213, v214\n"
      "s_waitcnt lgkmcnt(3)\n"
      "v_or3_b32 v212, v212, v215, v172\n"
      "v_or3_b32 v212, v212, v173, v174\n"
      "s_waitcnt lgkmcnt(2)\n"
      "v_or3_b32 v212, v212, v175, v204\n"
      "v_or3_b32 v212, v212, v205, v206\n"
      "s_waitcnt lgkmcnt(1)\n"
      "v_or3_b32 v212, v212, v207, v160\n"
      "v_or3_b32 v212, v212, v161, v162\n"
      "s_waitcnt lgkmcnt(0)\n"
      "v_or3_b32 v212, v212, v163, v144\n"
      "v_or3_b32 v212, v212, v145, v146\n"
      "v_bitop3_b32 v212, v212, v147, s40 bitop3:0xa8\n"
      "v_cmp_ne_u32 s[48:49], 0, v212\n"
      "s_cmp_lg_u64 s[48:49], 0\n"
      "s_cbranch_scc1 L_gen_20_%=\n"
      "s_cmp_eq_u64 s[50:51], 0\n"
      "s_cbranch_scc1 L_fast_22_%=\n"
      "L_go_77_%=:\n"
      "v_mfma_f32_16x16x32_bf16 v[148:151], v[252:255], v[200:203], v[90:93]\n"
      "ds_read_b128 v[212:215], v128 offset:2048\n"
      "v_max3_f32 v175, v184, v185, v186\n"
      "v_mfma_f32_16x16x32_bf16 v[148:151], v[34:37], v[38:41], v[148:151]\n"
      "v_max3_f32 v173, v187, v244, v245\n"
      "v_max3_f32 v175, v175, v246, v247\n"
      "v_mfma_f32_16x16x32_bf16 v[204:207], v[248:251], v[200:203], v[94:97]\n"
      "s_nop 0\n"
      "v_max3_f32 v173, v173, v176, v177\n"
      "v_max3_f32 v175, v175, v178, v179\n"
      "v_mfma_f32_16x16x32_bf16 v[204:207], v[40:43], v[38:41], v[204:207]\n"
      "v_max3_f32 v175, v175, v173, v228\n"
      "v_mov_b32 v173, v175\n"
      "v_mfma_f32_16x16x32_bf16 v[160:163], v[240:243], v[200:203], v[72:75]\n"
      "s_nop 0\n"
      "v_permlane16_swap_b32 v175, v173\n"
      "v_mfma_f32_16x16x32_bf16 v[160:163], v[216:219], v[248:251], v[160:163]\n"
      "v_max_f32 v175, v175, v173\n"
      "v_mov_b32 v173, v175\n"
      "v_mfma_f32_16x16x32_bf16 v[160:163], v[192:195], v[38:41], v[160:163]\n"
      "s_nop 0\n"
      "v_permlane32_swap_b32 v175, v173\n"
      "v_mfma_f32_16x16x32_bf16 v[144:147], v[188:191], v[200:203], v[98:101]\n"
      "v_max_f32 v175, v175, v173\n"
      "v_sub_f32 v184, v184, v175\n"
      "v_mfma_f32_16x16x32_bf16 v[144:147], v[46:49], v[38:41], v[144:147]\n"
      "s_waitcnt lgkmcnt(0)\n"
      "v_sub_f32 v185, v185, v175\n"
      "v_sub_f32 v186, v186, v175\n"
      "v_sub_f32 v187, v187, v175\n"
      "v_sub_f32 v244, v244, v175\n"
      "v_sub_f32 v245, v245, v175\n"
      "v_sub_f32 v246, v246, v175\n"
      "v_sub_f32 v247, v247, v175\n"
      "v_sub_f32 v176, v176, v175\n"
      "v_sub_f32 v177, v177, v175\n"
      "v_sub_f32 v178, v178, v175\n"
      "v_sub_f32 v179, v179, v175\n"
      "v_sub_f32 v228, v228, v175\n"
      "v_exp_f32 v184, v184\n"
      "v_exp_f32 v185, v185\n"
      "v_exp_f32 v186, v186\n"
      "v_cvt_pk_bf16_f32 v184, v184, v185\n"
      "v_exp_f32 v187, v187\n"
      "v_exp_f32_e64 v244, v244\n"
      "v_cvt_pk_bf16_f32 v185, v186, v187\n"
      "v_exp_f32 v245, v245\n"
      "v_exp_f32 v246, v246\n"
      "v_cvt_pk_bf16_f32 v186, v244, v245\n"
      "v_exp_f32 v247, v247\n"
      "v_exp_f32 v176, v176\n"
      "v_cvt_pk_bf16_f32 v187, v246, v247\n"
      "v_exp_f32 v177, v177\n"
      "v_exp_f32 v178, v178\n"
      "v_exp_f32 v179, v179\n"
      "v_mfma_f32_16x16x32_bf16 v[244:247], v[252:255], v[168:171], v[102:105]\n"
      "v_exp_f32 v228, v228\n"
      "v_mfma_f32_16x16x32_bf16 v[244:247], v[34:37], v[44:47], v[244:247]\n"
      "v_cvt_pk_bf16_f32 v62, v176, v177\n"
      "v_cvt_pk_bf16_f32 v63, v178, v179\n"
      "v_cvt_pk_bf16_f32 v64, v228, 0\n"
      "v_bitop3_b32 v212, v212, v213, s4 bitop3:0x7e\n"
      "v_bitop3_b32 v214, v214, v215, s4 bitop3:0x7e\n"
      "v_mfma_f32_16x16x32_bf16 v[200:203], v[66:69], v[184:187], 0\n"
      "ds_read_b128 v[176:179], v119 offset:12288\n"
      "v_or_b32 v212, v212, v214\n"
      "v_mfma_f32_16x16x32_bf16 v[200:203], v[66:69], v[62:65], v[200:203]\n"
      "v_cmp_ne_u32 vcc, 0, v212\n"
      "v_max3_f32 v162, v148, v149, v150\n"
      "v_mfma_f32_16x16x32_bf16 v[172:175], v[140:143], v[184:187], 0\n"
      "s_mov_b64 s[48:49], vcc\n"
      "v_max3_f32 v163, v151, v204, v205\n"
      "v_max3_f32 v162, v162, v206, v207\n"
      "v_mfma_f32_16x16x32_bf16 v[172:175], v[50:53], v[62:65], v[172:175]\n"
      "s_cbranch_vccnz L_flg_78_%=\n"
      "L_fb_79_%=:\n"
      "v_max3_f32 v163, v163, v144, v145\n"
      "v_mfma_f32_16x16x32_bf16 v[212:215], v[224:227], v[184:187], 0\n"
      "v_max3_f32 v162, v162, v146, v147\n"
      "v_max3_f32 v162, v162, v163, v160\n"
      "v_mfma_f32_16x16x32_bf16 v[212:215], v[54:57], v[62:65], v[212:215]\n"
      "v_mov_b32 v163, v162\n"
      "v_mfma_f32_16x16x32_bf16 v[252:255], v[136:139], v[184:187], 0\n"
      "s_nop 0\n"
      "v_permlane16_swap_b32_e64 v162, v163\n"
      "v_mfma_f32_16x16x32_bf16 v[252:255], v[58:61], v[62:65], v[252:255]\n"
      "v_max_f32 v162, v162, v163\n"
      "v_mov_b32 v163, v162\n"
      "s_nop 1\n"
      "v_permlane32_swap_b32 v162, v163\n"
      "v_max_f32 v162, v162, v163\n"
      "v_sub_f32 v148, v148, v162\n"
      "v_sub_f32 v149, v149, v162\n"
      "v_sub_f32 v150, v150, v162\n"
      "v_sub_f32 v151, v151, v162\n"
      "v_sub_f32 v204, v204, v162\n"
      "v_sub_f32 v205, v205, v162\n"
      "v_sub_f32 v206, v206, v162\n"
      "v_sub_f32 v207, v207, v162\n"
      "v_sub_f32 v144, v144, v162\n"
      "v_sub_f32 v145, v145, v162\n"
      "v_sub_f32 v146, v146, v162\n"
      "v_sub_f32 v147, v147, v162\n"
      "v_sub_f32 v160, v160, v162\n"
      "v_exp_f32 v148, v148\n"
      "v_exp_f32 v149, v149\n"
      "v_mfma_f32_16x16x32_bf16 v[184:187], v[248:251], v[168:171], v[106:109]\n"
      "s_waitcnt lgkmcnt(0)\n"
      "v_exp_f32 v150, v150\n"
      "v_mfma_f32_16x16x32_bf16 v[184:187], v[40:43], v[44:47], v[184:187]\n"
      "global_store_dwordx4 v116, v[176:179], s[54:55] offset:256 sc1\n"
      "v_cvt_pk_bf16_f32 v148, v148, v149\n"
      "v_exp_f32 v151, v151\n"
      "v_mfma_f32_16x16x32_bf16 v[162:165], v[240:243], v[168:171], v[74:77]\n"
      "v_exp_f32 v204, v204\n"
      "v_mfma_f32_16x16x32_bf16 v[162:165], v[216:219], v[188:191], v[162:165]\n"
      "v_cvt_pk_bf16_f32 v149, v150, v151\n"
      "v_exp_f32 v205, v205\n"
      "v_mfma_f32_16x16x32_bf16 v[162:165], v[192:195], v[44:47], v[162:165]\n"
      "v_exp_f32 v206, v206\n"
      "v_cvt_pk_bf16_f32 v150, v204, v205\n"
      "v_exp_f32_e64 v207, v207\n"
      "v_mfma_f32_16x16x32_bf16 v[248:251], v[188:191], v[168:171], v[110:113]\n"
      "v_exp_f32 v144, v144\n"
      "v_mfma_f32_16x16x32_bf16 v[248:251], v[46:49], v[44:47], v[248:251]\n"
      "v_cvt_pk_bf16_f32 v151, v206, v207\n"
      "v_exp_f32 v145, v145\n"
      "v_mfma_f32_16x16x32_bf16 v[188:191], v[216:219], v[240:243], v[76:79]\n"
      "v_exp_f32 v146, v146\n"
      "v_exp_f32 v147, v147\n"
      "v_exp_f32_e64 v160, v160\n"
      "v_cvt_pk_bf16_f32 v62, v144, v145\n"
      "v_cvt_pk_bf16_f32 v63, v146, v147\n"
      "v_cvt_pk_bf16_f32 v64, v160, 0\n"
      "v_mfma_f32_16x16x16_bf16 v[188:191], v[194:195], v[192:193], v[188:191]\n"
      "v_rcp_f32_e64 v200, v200\n"
      "v_mfma_f32_16x16x32_bf16 v[176:179], v[66:69], v[148:151], 0\n"
      "v_mul_f32 v172, v200, v172\n"
      "v_mul_f32 v173, v200, v173\n"
      "v_mfma_f32_16x16x32_bf16 v[176:179], v[66:69], v[62:65], v[176:179]\n"
      "v_cvt_pk_bf16_f32 v172, v172, v173\n"
      "v_mul_f32 v174, v200, v174\n"
      "v_mfma_f32_16x16x32_bf16 v[168:171], v[140:143], v[148:151], 0\n"
      "v_mul_f32 v175, v200, v175\n"
      "v_cvt_pk_bf16_f32 v173, v174, v175\n"
      "v_mfma_f32_16x16x32_bf16 v[168:171], v[50:53], v[62:65], v[168:171]\n"
      "v_mul_f32 v212, v200, v212\n"
      "v_mul_f32 v213, v200, v213\n"
      "v_mfma_f32_16x16x32_bf16 v[204:207], v[224:227], v[148:151], 0\n"
      "v_cvt_pk_bf16_f32 v174, v212, v213\n"
      "v_mul_f32_e64 v214, v200, v214\n"
      "v_mfma_f32_16x16x32_bf16 v[204:207], v[54:57], v[62:65], v[204:207]\n"
      "v_mul_f32_e64 v215, v200, v215\n"
      "v_cvt_pk_bf16_f32 v175, v214, v215\n"
      "v_mfma_f32_16x16x32_bf16 v[212:215], v[136:139], v[148:151], 0\n"
      "ds_read_b128 v[148:151], v120\n"
      "v_mul_f32 v252, v200, v252\n"
      "v_mfma_f32_16x16x32_bf16 v[212:215], v[58:61], v[62:65], v[212:215]\n"
      "ds_write_b128 v124, v[172:175] offset:18432\n"
      "v_mul_f32 v253, v200, v253\n"
      "v_cvt_pk_bf16_f32 v252, v252, v253\n"
      "v_mul_f32 v254, v200, v254\n"
      "v_mul_f32 v255, v200, v255\n"
      "v_cvt_pk_bf16_f32 v253, v254, v255\n"
      "v_max3_f32 v160, v244, v245, v246\n"
      "v_max3_f32 v177, v247, v184, v185\n"
      "v_max3_f32 v160, v160, v186, v187\n"
      "v_max3_f32 v177, v177, v248, v249\n"
      "v_max3_f32 v160, v160, v250, v251\n"
      "v_max3_f32 v160, v160, v177, v162\n"
      "v_mov_b32 v177, v160\n"
      "s_nop 1\n"
      "v_permlane16_swap_b32 v160, v177\n"
      "v_max_f32 v160, v160, v177\n"
      "v_mov_b32 v177, v160\n"
      "s_nop 1\n"
      "v_permlane32_swap_b32 v160, v177\n"
      "v_max_f32 v160, v160, v177\n"
      "v_sub_f32 v244, v244, v160\n"
      "v_sub_f32 v245, v245, v160\n"
      "v_sub_f32 v246, v246, v160\n"
      "v_sub_f32 v247, v247, v160\n"
      "v_sub_f32 v184, v184, v160\n"
      "v_sub_f32 v185, v185, v160\n"
      "v_sub_f32 v186, v186, v160\n"
      "v_sub_f32 v187, v187, v160\n"
      "v_sub_f32 v248, v248, v160\n"
      "v_sub_f32 v249, v249, v160\n"
      "v_sub_f32 v250, v250, v160\n"
      "v_sub_f32 v251, v251, v160\n"
      "v_sub_f32 v162, v162, v160\n"
      "v_exp_f32 v244, v244\n"
      "v_exp_f32 v245, v245\n"
      "v_exp_f32 v246, v246\n"
      "v_cvt_pk_bf16_f32 v244, v244, v245\n"
      "v_exp_f32 v247, v247\n"
      "v_exp_f32 v184, v184\n"
      "v_cvt_pk_bf16_f32 v245, v246, v247\n"
      "v_exp_f32 v185, v185\n"
      "v_exp_f32 v186, v186\n"
      "v_cvt_pk_bf16_f32 v246, v184, v185\n"
      "v_exp_f32 v187, v187\n"
      "v_exp_f32 v248, v248\n"
      "v_cvt_pk_bf16_f32 v247, v186, v187\n"
      "v_exp_f32 v249, v249\n"
      "v_exp_f32 v250, v250\n"
      "v_exp_f32 v251, v251\n"
      "v_exp_f32 v162, v162\n"
      "v_cvt_pk_bf16_f32 v62, v248, v249\n"
      "v_cvt_pk_bf16_f32 v63, v250, v251\n"
      "v_cvt_pk_bf16_f32 v64, v162, 0\n"
      "v_rcp_f32 v176, v176\n"
      "v_mfma_f32_16x16x32_bf16 v[192:195], v[66:69], v[244:247], 0\n"
      "ds_read_b128 v[184:187], v123 offset:1024\n"
      "v_mul_f32 v168, v176, v168\n"
      "v_mfma_f32_16x16x32_bf16 v[192:195], v[66:69], v[62:65], v[192:195]\n"
      "ds_read_b128 v[172:175], v123 offset:4096\n"
      "v_mul_f32 v169, v176, v169\n"
      "v_mfma_f32_16x16x32_bf16 v[240:243], v[140:143], v[244:247], 0\n"
      "s_waitcnt lgkmcnt(2)\n"
      "v_cvt_pk_bf16_f32 v168, v168, v169\n"
      "v_mul_f32_e64 v170, v176, v170\n"
      "v_mfma_f32_16x16x32_bf16 v[240:243], v[50:53], v[62:65], v[240:243]\n"
      "global_store_dwordx4 v117, v[148:151], s[54:55] sc1\n"
      "v_mfma_f32_16x16x32_bf16 v[248:251], v[224:227], v[244:247], 0\n"
      "ds_read_b128 v[144:147], v123 offset:7168\n"
      "v_mul_f32_e64 v171, v176, v171\n"
      "v_mfma_f32_16x16x32_bf16 v[248:251], v[54:57], v[62:65], v[248:251]\n"
      "ds_read_b128 v[216:219], v121\n"
      "v_cvt_pk_bf16_f32 v169, v170, v171\n"
      "v_mfma_f32_16x16x32_bf16 v[148:151], v[136:139], v[244:247], 0\n"
      "ds_read_b128 v[164:167], v123 offset:10240\n"
      "v_mul_f32 v204, v176, v204\n"
      "v_mfma_f32_16x16x32_bf16 v[148:151], v[58:61], v[62:65], v[148:151]\n"
      "v_mul_f32 v205, v176, v205\n"
      "v_cvt_pk_bf16_f32 v170, v204, v205\n"
      "v_mul_f32 v206, v176, v206\n"
      "v_mul_f32 v207, v176, v207\n"
      "v_cvt_pk_bf16_f32 v171, v206, v207\n"
      "v_mul_f32 v212, v176, v212\n"
      "v_mul_f32 v213, v176, v213\n"
      "v_cvt_pk_bf16_f32 v212, v212, v213\n"
      "s_waitcnt lgkmcnt(3)\n"
      "v_mfma_f32_16x16x32_bf16 v[200:203], a[216:219], v[184:187], 0\n"
      "ds_read_b128 v[204:207], v123 offset:13312\n"
      "v_mul_f32 v214, v176, v214\n"
      "v_mfma_f32_16x16x32_bf16 v[200:203], a[228:231], v[172:175], v[200:203]\n"
      "ds_write_b128 v124, v[168:171] offset:19456\n"
      "v_mfma_f32_16x16x32_bf16 v[244:247], a[220:223], v[184:187], 0\n"
      "ds_read_b128 v[168:171], v123 offset:16384\n"
      "v_mul_f32_e64 v215, v176, v215\n"
      "v_mfma_f32_16x16x32_bf16 v[244:247], a[232:235], v[172:175], v[244:247]\n"
      "s_waitcnt lgkmcnt(4)\n"
      "v_cvt_pk_bf16_f32 v213, v214, v215\n"
      "v_cndmask_b32 v229, v229, v161, s[42:43]\n"
      "s_waitcnt lgkmcnt(3)\n"
      "v_mfma_f32_16x16x32_bf16 v[176:179], a[224:227], v[184:187], 0\n"
      "ds_read_b128 v[158:161], v123 offset:2048\n"
      "v_cndmask_b32 v229, v229, v163, s[44:45]\n"
      "v_mfma_f32_16x16x32_bf16 v[176:179], a[236:239], v[172:175], v[176:179]\n"
      "ds_write2st64_b64 v125, v[252:253], v[212:213] offset0:36 offset1:38\n"
      "v_mfma_f32_16x16x32_bf16 v[200:203], a[240:243], v[144:147], v[200:203]\n"
      "ds_read_b128 v[172:175], v123 offset:5120\n"
      "v_cndmask_b32 v229, v229, v189, s[46:47]\n"
      "v_mfma_f32_16x16x32_bf16 v[200:203], a[252:255], v[164:167], v[200:203]\n"
      "ds_read_b128 v[252:255], v123 offset:8192\n"
      "v_max_f32_dpp v163, v229, v229 row_ror:8 row_mask:0xf bank_mask:0xf\n"
      "v_mfma_f32_16x16x32_bf16 v[244:247], a[244:247], v[144:147], v[244:247]\n"
      "ds_read_b128 v[196:199], v123 offset:11264\n"
      "v_max_f32_dpp v163, v163, v163 row_ror:4 row_mask:0xf bank_mask:0xf\n"
      "v_mfma_f32_16x16x32_bf16 v[244:247], v[0:3], v[164:167], v[244:247]\n"
      "s_waitcnt lgkmcnt(5)\n"
      "v_max_f32_dpp v163, v163, v163 row_ror:2 row_mask:0xf bank_mask:0xf\n"
      "v_mfma_f32_16x16x32_bf16 v[176:179], a[248:251], v[144:147], v[176:179]\n"
      "ds_read_b128 v[144:147], v123 offset:14336\n"
      "v_max_f32_dpp v163, v163, v163 row_ror:1 row_mask:0xf bank_mask:0xf\n"
      "v_mfma_f32_16x16x32_bf16 v[176:179], v[4:7], v[164:167], v[176:179]\n"
      "ds_read_b128 v[164:167], v123 offset:17408\n"
      "v_max_f32_dpp v163, v163, v163 row_bcast:15 row_mask:0xa bank_mask:0xf\n"
      "v_mfma_f32_16x16x32_bf16 v[200:203], v[8:11], v[204:207], v[200:203]\n"
      "s_nop 0\n"
      "v_max_f32_dpp v163, v163, v163 row_bcast:31 row_mask:0xc bank_mask:0xf\n"
      "v_mfma_f32_16x16x32_bf16 v[200:203], v[20:23], v[168:171], v[200:203]\n"
      "s_mov_b32 exec_hi, 0\n"
      "global_store_dwordx4 v118, v[216:219], s[54:55] sc1\n"
      "s_mov_b32 exec_hi, -1\n"
      "s_waitcnt lgkmcnt(4)\n"
      "v_mfma_f32_16x16x32_bf16 v[244:247], v[12:15], v[204:207], v[244:247]\n"
      "s_add_u32 s28, s27, 2\n"
      "v_readlane_b32 s31, v163, 63\n"
      "s_min_u32 m0, s28, s3\n"
      "v_mfma_f32_16x16x32_bf16 v[244:247], v[24:27], v[168:171], v[244:247]\n"
      "s_movrels_b32 s9, s80\n"
      "v_subrev_f32 v229, s31, v229\n"
      "v_exp_f32_e64 v229, v229\n"
      "v_mfma_f32_16x16x32_bf16 v[176:179], v[16:19], v[204:207], v[176:179]\n"
      "s_waitcnt lgkmcnt(2)\n"
      "v_cvt_pk_bf16_f32 v229, v229, v229\n"
      "v_fma_f32 v163, v200, v200, s39\n"
      "v_mfma_f32_16x16x32_bf16 v[176:179], v[28:31], v[168:171], v[176:179]\n"
      "ds_write_b16 v130, v229\n"
      "v_mfma_f32_16x16x32_bf16 v[212:215], a[216:219], v[158:161], 0\n"
      "ds_read_b128 v[204:207], v131\n"
      "v_fmac_f32 v163, v201, v201\n"
      "v_mfma_f32_16x16x32_bf16 v[212:215], a[228:231], v[172:175], v[212:215]\n"
      "ds_read_b128 v[216:219], v131 offset:64\n"
      "v_fmac_f32 v163, v202, v202\n"
      "v_mfma_f32_16x16x32_bf16 v[184:187], a[220:223], v[158:161], 0\n"
      "s_waitcnt lgkmcnt(3)\n"
      "v_fmac_f32 v163, v203, v203\n"
      "v_fmac_f32 v163, v244, v244\n"
      "v_mfma_f32_16x16x32_bf16 v[184:187], a[232:235], v[172:175], v[184:187]\n"
      "s_lshr_b32 s8, s9, 8\n"
      "v_fmac_f32 v163, v245, v245\n"
      "v_fmac_f32 v163, v246, v246\n"
      "v_mfma_f32_16x16x32_bf16 v[188:191], a[224:227], v[158:161], 0\n"
      "s_and_b32 s9, s9, 0xff\n"
      "v_fmac_f32 v163, v247, v247\n"
      "v_fmac_f32 v163, v176, v176\n"
      "v_mfma_f32_16x16x32_bf16 v[188:191], a[236:239], v[172:175], v[188:191]\n"
      "s_mul_i32 s10, s8, 73\n"
      "v_fmac_f32 v163, v177, v177\n"
      "v_fmac_f32 v163, v178, v178\n"
      "v_mfma_f32_16x16x32_bf16 v[212:215], a[240:243], v[252:255], v[212:215]\n"
      "s_add_u32 s10, s10, s9\n"
      "v_fmac_f32 v163, v179, v179\n"
      "s_mul_i32 s28, s10, 4802\n"
      "v_mfma_f32_16x16x32_bf16 v[212:215], a[252:255], v[196:199], v[212:215]\n"
      "s_add_u32 s56, s18, s28\n"
      "s_addc_u32 s57, s19, 0\n"
      "v_mfma_f32_16x16x32_bf16 v[184:187], a[244:247], v[252:255], v[184:187]\n"
      "s_and_b32 s56, s56, -16\n"
      "v_permlane16_swap_b32_e64 v230, v163\n"
      "v_mfma_f32_16x16x32_bf16 v[184:187], v[0:3], v[196:199], v[184:187]\n"
      "s_waitcnt lgkmcnt(0)\n"
      "v_add_f32 v230, v230, v163\n"
      "v_mfma_f32_16x16x32_bf16 v[188:191], a[248:251], v[252:255], v[188:191]\n"
      "s_sub_u32 s58, s56, 110592\n"
      "s_subb_u32 s59, s57, 0\n"
      "s_sub_u32 s56, s56, 73728\n"
      "v_mfma_f32_16x16x32_bf16 v[188:191], v[4:7], v[196:199], v[188:191]\n"
      "s_subb_u32 s57, s57, 0\n"
      "s_mul_i32 s28, s8, 1373568\n"
      "v_mfma_f32_16x16x32_bf16 v[212:215], v[8:11], v[144:147], v[212:215]\n"
      "s_mul_i32 s29, s9, 2688\n"
      "s_add_u32 s28, s28, s29\n"
      "s_cmp_eq_u32 s8, 72\n"
      "v_mfma_f32_16x16x32_bf16 v[212:215], v[20:23], v[164:167], v[212:215]\n"
      "s_cselect_b32 s8, 0, 100270464\n"
      "s_cmp_eq_u32 s9, 72\n"
      "v_mfma_f32_16x16x32_bf16 v[184:187], v[12:15], v[144:147], v[184:187]\n"
      "s_cselect_b32 s9, 0, 196224\n"
      "s_add_u32 s10, s8, s9\n"
      "s_add_u32 s28, s28, s10\n"
      "v_mfma_f32_16x16x32_bf16 v[184:187], v[24:27], v[164:167], v[184:187]\n"
      "ds_read_b128 v[168:171], v114\n"
      "v_fma_f32 v163, v212, v212, s39\n"
      "v_mfma_f32_16x16x32_bf16 v[188:191], v[16:19], v[144:147], v[188:191]\n"
      "ds_read_b128 v[172:175], v115\n"
      "v_fmac_f32 v163, v213, v213\n"
      "v_mfma_f32_16x16x32_bf16 v[188:191], v[28:31], v[164:167], v[188:191]\n"
      "s_add_u32 s52, s16, s28\n"
      "v_fmac_f32 v163, v214, v214\n"
      "v_fmac_f32 v163, v215, v215\n"
      "v_mfma_f32_16x16x32_bf16 v[158:161], v[204:207], v[66:69], 0\n"
      "s_addc_u32 s53, s17, 0\n"
      "v_fmac_f32 v163, v184, v184\n"
      "v_fmac_f32 v163, v185, v185\n"
      "v_mfma_f32_16x16x32_bf16 v[158:161], v[216:219], v[66:69], v[158:161]\n"
      "s_add_u32 s27, s27, 1\n"
      "v_fmac_f32 v163, v186, v186\n"
      "v_fmac_f32 v163, v187, v187\n"
      "v_mfma_f32_16x16x32_bf16 v[164:167], v[204:207], v[140:143], 0\n"
      "s_cmp_ge_u32 s27, s26\n"
      "v_fmac_f32 v163, v188, v188\n"
      "v_fmac_f32_e64 v163, v189, v189\n"
      "v_mfma_f32_16x16x32_bf16 v[164:167], v[216:219], v[50:53], v[164:167]\n"
      "s_cselect_b32 s28, 1, 0\n"
      "v_fmac_f32 v163, v190, v190\n"
      "v_fmac_f32 v163, v191, v191\n"
      "v_mfma_f32_16x16x32_bf16 v[140:143], v[204:207], v[224:227], 0\n"
      "s_xor_b32 s29, s10, s11\n"
      "v_mov_b32 v160, v163\n"
      "v_mov_b32_dpp v158, v158 quad_perm:[0,0,0,0] row_mask:0xf bank_mask:0xf\n"
      "v_mfma_f32_16x16x32_bf16 v[140:143], v[216:219], v[54:57], v[140:143]\n"
      "s_or_b32 s28, s28, s29\n"
      "v_permlane16_swap_b32_e64 v163, v160\n"
      "v_mfma_f32_16x16x32_bf16 v[224:227], v[204:207], v[136:139], 0\n"
      "s_waitcnt lgkmcnt(0)\n"
      "v_add_f32 v163, v163, v160\n"
      "v_mfma_f32_16x16x32_bf16 v[224:227], v[216:219], v[58:61], v[224:227]\n"
      "s_nop 0\n"
      "v_permlane32_swap_b32 v230, v163\n"
      "v_mfma_f32_16x16x32_bf16 v[204:207], a[0:3], v[168:171], 0\n"
      "v_add_f32_e64 v230, v230, v163\n"
      "v_cndmask_b32 v164, v164, v140, s[42:43]\n"
      "v_mfma_f32_16x16x32_bf16 v[204:207], a[36:39], v[172:175], v[204:207]\n"
      "ds_write_b32 v129, v230 offset:1024\n"
      "v_mfma_f32_16x16x32_bf16 v[160:163], a[4:7], v[168:171], 0\n"
      "v_cndmask_b32 v164, v164, v224, s[44:45]\n"
      "v_mfma_f32_16x16x32_bf16 v[160:163], a[40:43], v[172:175], v[160:163]\n"
      "v_rcp_f32 v158, v158\n"
      "s_nop 0\n"
      "v_mul_f32 v164, v158, v164\n"
      "v_cvt_pk_bf16_f32 v164, v164, v164\n"
      "ds_write_b16 v135, v164 offset:16\n"
      "s_branch L_join_21_%=\n"
      "L_flg_78_%=:\n"
      "v_cmp_gt_i32 vcc, 0, v212\n"
      "s_cbranch_vccz L_fb_79_%=\n"
      "s_sleep 1\n"
      "ds_read_b128 v[212:215], v128 offset:2048\n"
      "s_waitcnt lgkmcnt(0)\n"
      "v_bitop3_b32 v212, v212, v213, s4 bitop3:0x7e\n"
      "v_bitop3_b32 v214, v214, v215, s4 bitop3:0x7e\n"
      "v_or_b32 v212, v212, v214\n"
      "v_cmp_ne_u32 s[48:49], 0, v212\n"
      "s_branch L_flg_78_%=\n"
      "L_gen_20_%=:\n"
      "s_waitcnt lgkmcnt(0)\n"
      "s_nop 7\n"
      "s_nop 7\n"
      "v_mbcnt_lo_u32_b32 v231, -1, 0\n"
      "v_mbcnt_hi_u32_b32 v231, -1, v231\n"
      "v_and_b32 v220, 15, v231\n"
      "v_lshrrev_b32_e64 v221, 4, v231\n"
      "v_mul_u32_u24 v156, 0x62, v220\n"
      "v_lshl_add_u32 v156, v221, 3, v156\n"
      "v_add_u32 v156, s30, v156\n"
      "v_and_b32 v231, -4, v156\n"
      "v_and_b32 v156, 2, v156\n"
      "v_lshlrev_b32 v221, 2, v221\n"
      "v_sub_u32 v220, v220, v221\n"
      "v_and_b32 v221, 1, v220\n"
      "v_lshlrev_b32 v221, 4, v221\n"
      "v_lshrrev_b32 v220, 1, v220\n"
      "v_cmp_eq_u32 vcc, 0, v220\n"
      "s_movk_i32 s31, 0x3fb9\n"
      "v_lshlrev_b32 v221, v221, s31\n"
      "ds_read2_b32 v[214:215], v231 offset0:0 offset1:1\n"
      "ds_read2_b32 v[172:173], v231 offset0:8 offset1:9\n"
      "ds_read2_b32 v[210:211], v231 offset0:16 offset1:17\n"
      "ds_read2_b32 v[160:161], v231 offset0:2 offset1:10\n"
      "ds_read2_b32 v[150:151], v231 offset0:18 offset1:24\n"
      "v_add_u32 v231, 1568, v231\n"
      "ds_read2_b32 v[148:149], v231 offset0:0 offset1:1\n"
      "ds_read2_b32 v[212:213], v231 offset0:8 offset1:9\n"
      "ds_read2_b32 v[204:205], v231 offset0:16 offset1:17\n"
      "ds_read2_b32 v[238:239], v231 offset0:2 offset1:10\n"
      "ds_read2_b32 v[206:207], v231 offset0:18 offset1:24\n"
      "v_add_u32 v231, 1568, v231\n"
      "ds_read2_b32 v[174:175], v231 offset0:0 offset1:1\n"
      "ds_read2_b32 v[162:163], v231 offset0:8 offset1:9\n"
      "ds_read2_b32 v[236:237], v231 offset0:16 offset1:17\n"
      "ds_read2_b32 v[208:209], v231 offset0:2 offset1:10\n"
      "ds_read2_b32 v[196:197], v231 offset0:18 offset1:24\n"
      "v_cndmask_b32 v198, 0, v221, vcc\n"
      "v_cmp_eq_u32 vcc, 1, v220\n"
      "s_waitcnt lgkmcnt(13)\n"
      "v_alignbyte_b32 v214, v215, v214, v156\n"
      "v_cndmask_b32 v199, 0, v221, vcc\n"
      "v_mfma_f32_16x16x32_bf16 v[220:223], v[252:255], v[200:203], v[90:93]\n"
      "s_waitcnt lgkmcnt(11)\n"
      "v_alignbyte_b32 v215, v160, v215, v156\n"
      "v_mfma_f32_16x16x32_bf16 v[220:223], v[34:37], v[38:41], v[220:223]\n"
      "v_alignbyte_b32 v172, v173, v172, v156\n"
      "v_alignbyte_b32 v173, v161, v173, v156\n"
      "v_mfma_f32_16x16x32_bf16 v[144:147], v[248:251], v[200:203], v[94:97]\n"
      "s_waitcnt lgkmcnt(8)\n"
      "v_alignbyte_b32 v210, v211, v210, v156\n"
      "v_alignbyte_b32 v211, v150, v211, v156\n"
      "v_mfma_f32_16x16x32_bf16 v[144:147], v[40:43], v[38:41], v[144:147]\n"
      "v_alignbyte_b32 v151, v151, v151, v156\n"
      "v_lshlrev_b32_e64 v151, 16, v151\n"
      "v_mfma_f32_16x16x32_bf16 v[158:161], v[240:243], v[200:203], v[72:75]\n"
      "s_waitcnt lgkmcnt(5)\n"
      "v_fmac_f32 v228, s37, v151\n"
      "v_alignbyte_b32 v148, v149, v148, v156\n"
      "v_mfma_f32_16x16x32_bf16 v[158:161], v[216:219], v[248:251], v[158:161]\n"
      "v_alignbyte_b32 v149, v238, v149, v156\n"
      "v_alignbyte_b32 v212, v213, v212, v156\n"
      "v_mfma_f32_16x16x32_bf16 v[158:161], v[192:195], v[38:41], v[158:161]\n"
      "v_alignbyte_b32 v213, v239, v213, v156\n"
      "v_alignbyte_b32 v204, v205, v204, v156\n"
      "v_mfma_f32_16x16x32_bf16 v[164:167], v[188:191], v[200:203], v[98:101]\n"
      "v_alignbyte_b32 v205, v206, v205, v156\n"
      "v_alignbyte_b32 v207, v207, v207, v156\n"
      "v_mfma_f32_16x16x32_bf16 v[164:167], v[46:49], v[38:41], v[164:167]\n"
      "v_lshlrev_b32 v207, 16, v207\n"
      "v_mfma_f32_16x16x16_bf16 v[184:187], v[198:199], v[214:215], v[184:187]\n"
      "v_mfma_f32_16x16x16_bf16 v[244:247], v[198:199], v[172:173], v[244:247]\n"
      "v_mfma_f32_16x16x16_bf16 v[176:179], v[198:199], v[210:211], v[176:179]\n"
      "ds_read_b128 v[200:203], v128 offset:2048\n"
      "s_nop 4\n"
      "v_max3_f32 v151, v184, v185, v186\n"
      "v_max3_f32 v173, v187, v244, v245\n"
      "v_mfma_f32_16x16x16_bf16 v[220:223], v[198:199], v[148:149], v[220:223]\n"
      "v_max3_f32 v151, v151, v246, v247\n"
      "v_max3_f32 v173, v173, v176, v177\n"
      "v_mfma_f32_16x16x16_bf16 v[144:147], v[198:199], v[212:213], v[144:147]\n"
      "v_max3_f32 v151, v151, v178, v179\n"
      "v_max3_f32 v151, v151, v173, v228\n"
      "v_mfma_f32_16x16x16_bf16 v[164:167], v[198:199], v[204:205], v[164:167]\n"
      "v_mov_b32 v173, v151\n"
      "v_fmac_f32 v158, s37, v207\n"
      "s_nop 0\n"
      "v_permlane16_swap_b32 v151, v173\n"
      "v_max_f32 v151, v151, v173\n"
      "v_mov_b32 v172, v151\n"
      "s_waitcnt lgkmcnt(1)\n"
      "v_alignbyte_b32 v174, v175, v174, v156\n"
      "v_permlane32_swap_b32 v151, v172\n"
      "v_max_f32 v151, v151, v172\n"
      "v_sub_f32 v184, v184, v151\n"
      "v_sub_f32 v185, v185, v151\n"
      "v_sub_f32 v186, v186, v151\n"
      "v_sub_f32 v187, v187, v151\n"
      "v_sub_f32 v244, v244, v151\n"
      "v_sub_f32 v245, v245, v151\n"
      "v_sub_f32 v246, v246, v151\n"
      "v_sub_f32 v247, v247, v151\n"
      "v_sub_f32 v176, v176, v151\n"
      "v_sub_f32 v177, v177, v151\n"
      "v_sub_f32 v178, v178, v151\n"
      "v_sub_f32 v179, v179, v151\n"
      "v_sub_f32 v228, v228, v151\n"
      "v_exp_f32 v184, v184\n"
      "v_exp_f32 v185, v185\n"
      "v_exp_f32 v186, v186\n"
      "v_cvt_pk_bf16_f32 v184, v184, v185\n"
      "v_exp_f32 v187, v187\n"
      "v_exp_f32 v244, v244\n"
      "v_cvt_pk_bf16_f32 v185, v186, v187\n"
      "v_exp_f32 v245, v245\n"
      "v_exp_f32 v246, v246\n"
      "v_cvt_pk_bf16_f32 v186, v244, v245\n"
      "v_exp_f32 v247, v247\n"
      "v_exp_f32 v176, v176\n"
      "v_cvt_pk_bf16_f32 v187, v246, v247\n"
      "v_exp_f32 v177, v177\n"
      "v_exp_f32 v178, v178\n"
      "v_exp_f32 v179, v179\n"
      "v_exp_f32_e64 v228, v228\n"
      "v_cvt_pk_bf16_f32 v62, v176, v177\n"
      "v_cvt_pk_bf16_f32 v63, v178, v179\n"
      "v_cvt_pk_bf16_f32 v64, v228, 0\n"
      "v_max3_f32 v172, v220, v221, v222\n"
      "v_max3_f32 v173, v223, v144, v145\n"
      "v_max3_f32 v172, v172, v146, v147\n"
      "v_max3_f32 v173, v173, v164, v165\n"
      "v_max3_f32 v172, v172, v166, v167\n"
      "v_max3_f32 v172, v172, v173, v158\n"
      "v_mov_b32_e64 v173, v172\n"
      "v_alignbyte_b32 v175, v208, v175, v156\n"
      "s_nop 0\n"
      "v_permlane16_swap_b32 v172, v173\n"
      "v_max_f32 v172, v172, v173\n"
      "v_mov_b32 v173, v172\n"
      "v_alignbyte_b32 v162, v163, v162, v156\n"
      "v_alignbyte_b32 v163, v209, v163, v156\n"
      "v_permlane32_swap_b32 v172, v173\n"
      "v_max_f32 v172, v172, v173\n"
      "v_sub_f32 v220, v220, v172\n"
      "v_sub_f32 v221, v221, v172\n"
      "v_sub_f32 v222, v222, v172\n"
      "v_sub_f32 v223, v223, v172\n"
      "v_sub_f32 v144, v144, v172\n"
      "v_sub_f32 v145, v145, v172\n"
      "v_sub_f32 v146, v146, v172\n"
      "v_sub_f32 v147, v147, v172\n"
      "v_alignbyte_b32 v236, v237, v236, v156\n"
      "v_sub_f32 v164, v164, v172\n"
      "v_sub_f32 v165, v165, v172\n"
      "v_alignbyte_b32 v237, v196, v237, v156\n"
      "v_mfma_f32_16x16x32_bf16 v[244:247], v[66:69], v[184:187], 0\n"
      "s_waitcnt lgkmcnt(0)\n"
      "v_alignbyte_b32 v197, v197, v197, v156\n"
      "v_lshlrev_b32 v197, 16, v197\n"
      "v_mfma_f32_16x16x32_bf16 v[244:247], v[66:69], v[62:65], v[244:247]\n"
      "v_bitop3_b32 v200, v200, v201, s4 bitop3:0x7e\n"
      "v_bitop3_b32 v202, v202, v203, s4 bitop3:0x7e\n"
      "v_mfma_f32_16x16x32_bf16 v[176:179], v[140:143], v[184:187], 0\n"
      "v_or_b32 v200, v200, v202\n"
      "v_cmp_ne_u32 vcc, 0, v200\n"
      "v_mfma_f32_16x16x32_bf16 v[176:179], v[50:53], v[62:65], v[176:179]\n"
      "s_mov_b64 s[48:49], vcc\n"
      "v_sub_f32 v166, v166, v172\n"
      "v_sub_f32 v167, v167, v172\n"
      "v_sub_f32 v158, v158, v172\n"
      "v_exp_f32 v220, v220\n"
      "v_exp_f32 v221, v221\n"
      "v_exp_f32 v222, v222\n"
      "s_cbranch_vccnz L_flg_84_%=\n"
      "L_fb_85_%=:\n"
      "v_mfma_f32_16x16x32_bf16 v[200:203], v[224:227], v[184:187], 0\n"
      "v_cvt_pk_bf16_f32 v220, v220, v221\n"
      "v_mfma_f32_16x16x32_bf16 v[200:203], v[54:57], v[62:65], v[200:203]\n"
      "ds_read_b128 v[148:151], v119 offset:12288\n"
      "v_exp_f32 v223, v223\n"
      "v_mfma_f32_16x16x32_bf16 v[212:215], v[136:139], v[184:187], 0\n"
      "v_exp_f32 v144, v144\n"
      "v_mfma_f32_16x16x32_bf16 v[212:215], v[58:61], v[62:65], v[212:215]\n"
      "v_cvt_pk_bf16_f32 v221, v222, v223\n"
      "v_exp_f32 v145, v145\n"
      "v_mfma_f32_16x16x32_bf16 v[184:187], v[252:255], v[168:171], v[102:105]\n"
      "v_exp_f32 v146, v146\n"
      "v_mfma_f32_16x16x32_bf16 v[184:187], v[34:37], v[44:47], v[184:187]\n"
      "v_cvt_pk_bf16_f32 v222, v144, v145\n"
      "v_exp_f32 v147, v147\n"
      "v_mfma_f32_16x16x32_bf16 v[204:207], v[248:251], v[168:171], v[106:109]\n"
      "v_exp_f32 v164, v164\n"
      "v_mfma_f32_16x16x32_bf16 v[204:207], v[40:43], v[44:47], v[204:207]\n"
      "v_cvt_pk_bf16_f32 v223, v146, v147\n"
      "v_exp_f32 v165, v165\n"
      "v_mfma_f32_16x16x16_bf16 v[184:187], v[198:199], v[174:175], v[184:187]\n"
      "s_waitcnt lgkmcnt(0)\n"
      "v_exp_f32 v166, v166\n"
      "v_exp_f32 v167, v167\n"
      "v_mfma_f32_16x16x16_bf16 v[204:207], v[198:199], v[162:163], v[204:207]\n"
      "global_store_dwordx4 v116, v[148:151], s[54:55] offset:256 sc1\n"
      "v_mfma_f32_16x16x32_bf16 v[172:175], v[240:243], v[168:171], v[74:77]\n"
      "v_exp_f32_e64 v158, v158\n"
      "v_mfma_f32_16x16x32_bf16 v[172:175], v[216:219], v[188:191], v[172:175]\n"
      "v_cvt_pk_bf16_f32 v62, v164, v165\n"
      "v_cvt_pk_bf16_f32 v63, v166, v167\n"
      "v_mfma_f32_16x16x32_bf16 v[172:175], v[192:195], v[44:47], v[172:175]\n"
      "v_cvt_pk_bf16_f32 v64, v158, 0\n"
      "v_rcp_f32 v244, v244\n"
      "v_mfma_f32_16x16x32_bf16 v[148:151], v[188:191], v[168:171], v[110:113]\n"
      "v_mul_f32 v176, v244, v176\n"
      "v_mul_f32 v177, v244, v177\n"
      "v_mfma_f32_16x16x32_bf16 v[148:151], v[46:49], v[44:47], v[148:151]\n"
      "v_cvt_pk_bf16_f32 v176, v176, v177\n"
      "v_mul_f32 v178, v244, v178\n"
      "v_mfma_f32_16x16x32_bf16 v[188:191], v[216:219], v[240:243], v[76:79]\n"
      "v_fmac_f32 v172, s37, v197\n"
      "v_mul_f32 v179, v244, v179\n"
      "v_cvt_pk_bf16_f32 v177, v178, v179\n"
      "v_mfma_f32_16x16x16_bf16 v[148:151], v[198:199], v[236:237], v[148:151]\n"
      "v_mul_f32 v200, v244, v200\n"
      "v_mul_f32 v201, v244, v201\n"
      "v_mfma_f32_16x16x16_bf16 v[188:191], v[194:195], v[192:193], v[188:191]\n"
      "v_cvt_pk_bf16_f32 v178, v200, v201\n"
      "v_mul_f32 v202, v244, v202\n"
      "v_mfma_f32_16x16x32_bf16 v[168:171], v[66:69], v[220:223], 0\n"
      "v_mul_f32 v203, v244, v203\n"
      "v_cvt_pk_bf16_f32 v179, v202, v203\n"
      "v_mfma_f32_16x16x32_bf16 v[168:171], v[66:69], v[62:65], v[168:171]\n"
      "ds_write_b128 v124, v[176:179] offset:18432\n"
      "v_mfma_f32_16x16x32_bf16 v[176:179], v[140:143], v[220:223], 0\n"
      "v_mul_f32 v212, v244, v212\n"
      "v_mul_f32 v213, v244, v213\n"
      "v_mfma_f32_16x16x32_bf16 v[176:179], v[50:53], v[62:65], v[176:179]\n"
      "v_cvt_pk_bf16_f32 v212, v212, v213\n"
      "v_mul_f32_e64 v214, v244, v214\n"
      "v_mfma_f32_16x16x32_bf16 v[200:203], v[224:227], v[220:223], 0\n"
      "v_mul_f32_e64 v215, v244, v215\n"
      "v_cvt_pk_bf16_f32 v213, v214, v215\n"
      "v_mfma_f32_16x16x32_bf16 v[200:203], v[54:57], v[62:65], v[200:203]\n"
      "ds_read_b128 v[244:247], v120\n"
      "v_max3_f32 v214, v184, v185, v186\n"
      "v_mfma_f32_16x16x32_bf16 v[160:163], v[136:139], v[220:223], 0\n"
      "v_max3_f32 v215, v187, v204, v205\n"
      "v_max3_f32 v214, v214, v206, v207\n"
      "v_mfma_f32_16x16x32_bf16 v[160:163], v[58:61], v[62:65], v[160:163]\n"
      "v_max3_f32 v215, v215, v148, v149\n"
      "v_max3_f32 v214, v214, v150, v151\n"
      "v_max3_f32 v214, v214, v215, v172\n"
      "v_mov_b32 v215, v214\n"
      "s_nop 1\n"
      "v_permlane16_swap_b32 v214, v215\n"
      "v_max_f32 v214, v214, v215\n"
      "v_mov_b32 v215, v214\n"
      "v_rcp_f32 v168, v168\n"
      "s_nop 0\n"
      "v_permlane32_swap_b32 v214, v215\n"
      "v_max_f32 v214, v214, v215\n"
      "v_sub_f32 v184, v184, v214\n"
      "v_sub_f32 v185, v185, v214\n"
      "v_sub_f32 v186, v186, v214\n"
      "v_sub_f32 v187, v187, v214\n"
      "v_sub_f32 v204, v204, v214\n"
      "v_sub_f32 v205, v205, v214\n"
      "v_sub_f32 v206, v206, v214\n"
      "v_sub_f32 v207, v207, v214\n"
      "v_sub_f32 v148, v148, v214\n"
      "v_sub_f32 v149, v149, v214\n"
      "v_sub_f32 v150, v150, v214\n"
      "v_sub_f32 v151, v151, v214\n"
      "v_sub_f32 v172, v172, v214\n"
      "v_exp_f32 v184, v184\n"
      "v_exp_f32 v185, v185\n"
      "v_exp_f32 v186, v186\n"
      "v_cvt_pk_bf16_f32 v184, v184, v185\n"
      "v_exp_f32 v187, v187\n"
      "v_exp_f32_e64 v204, v204\n"
      "v_cvt_pk_bf16_f32 v185, v186, v187\n"
      "v_exp_f32 v205, v205\n"
      "v_exp_f32 v206, v206\n"
      "v_cvt_pk_bf16_f32 v186, v204, v205\n"
      "v_exp_f32 v207, v207\n"
      "v_exp_f32 v148, v148\n"
      "v_cvt_pk_bf16_f32 v187, v206, v207\n"
      "v_exp_f32 v149, v149\n"
      "v_exp_f32 v150, v150\n"
      "v_exp_f32 v151, v151\n"
      "v_exp_f32 v172, v172\n"
      "v_cvt_pk_bf16_f32 v62, v148, v149\n"
      "v_cvt_pk_bf16_f32 v63, v150, v151\n"
      "v_cvt_pk_bf16_f32 v64, v172, 0\n"
      "v_mul_f32 v176, v168, v176\n"
      "v_mul_f32 v177, v168, v177\n"
      "v_mfma_f32_16x16x32_bf16 v[192:195], v[66:69], v[184:187], 0\n"
      "ds_read_b128 v[204:207], v123 offset:1024\n"
      "v_cvt_pk_bf16_f32 v176, v176, v177\n"
      "v_mfma_f32_16x16x32_bf16 v[192:195], v[66:69], v[62:65], v[192:195]\n"
      "ds_read_b128 v[196:199], v123 offset:4096\n"
      "v_mul_f32 v178, v168, v178\n"
      "v_mfma_f32_16x16x32_bf16 v[240:243], v[140:143], v[184:187], 0\n"
      "s_waitcnt lgkmcnt(2)\n"
      "v_mul_f32_e64 v179, v168, v179\n"
      "v_cvt_pk_bf16_f32 v177, v178, v179\n"
      "v_mfma_f32_16x16x32_bf16 v[240:243], v[50:53], v[62:65], v[240:243]\n"
      "global_store_dwordx4 v117, v[244:247], s[54:55] sc1\n"
      "v_mfma_f32_16x16x32_bf16 v[248:251], v[224:227], v[184:187], 0\n"
      "ds_read_b128 v[214:217], v121\n"
      "v_mul_f32 v200, v168, v200\n"
      "v_mfma_f32_16x16x32_bf16 v[248:251], v[54:57], v[62:65], v[248:251]\n"
      "ds_read_b128 v[252:255], v123 offset:7168\n"
      "v_mul_f32 v201, v168, v201\n"
      "v_cvt_pk_bf16_f32 v178, v200, v201\n"
      "v_mfma_f32_16x16x32_bf16 v[148:151], v[136:139], v[184:187], 0\n"
      "ds_read_b128 v[184:187], v123 offset:10240\n"
      "v_mul_f32 v202, v168, v202\n"
      "v_mfma_f32_16x16x32_bf16 v[148:151], v[58:61], v[62:65], v[148:151]\n"
      "v_mul_f32 v203, v168, v203\n"
      "v_cvt_pk_bf16_f32 v179, v202, v203\n"
      "v_mul_f32 v160, v168, v160\n"
      "v_mul_f32 v161, v168, v161\n"
      "v_cvt_pk_bf16_f32 v160, v160, v161\n"
      "v_mul_f32 v162, v168, v162\n"
      "v_mul_f32 v163, v168, v163\n"
      "s_waitcnt lgkmcnt(3)\n"
      "v_mfma_f32_16x16x32_bf16 v[200:203], a[216:219], v[204:207], 0\n"
      "ds_read_b128 v[168:171], v123 offset:13312\n"
      "v_cvt_pk_bf16_f32 v161, v162, v163\n"
      "v_mfma_f32_16x16x32_bf16 v[200:203], a[228:231], v[196:199], v[200:203]\n"
      "ds_write_b128 v124, v[176:179] offset:19456\n"
      "v_mfma_f32_16x16x32_bf16 v[244:247], a[220:223], v[204:207], 0\n"
      "ds_read_b128 v[162:165], v123 offset:16384\n"
      "v_cndmask_b32 v229, v229, v159, s[42:43]\n"
      "v_mfma_f32_16x16x32_bf16 v[244:247], a[232:235], v[196:199], v[244:247]\n"
      "ds_write2st64_b64 v125, v[212:213], v[160:161] offset0:36 offset1:38\n"
      "s_waitcnt lgkmcnt(4)\n"
      "v_mfma_f32_16x16x32_bf16 v[176:179], a[224:227], v[204:207], 0\n"
      "ds_read_b128 v[204:207], v123 offset:2048\n"
      "v_cndmask_b32 v229, v229, v173, s[44:45]\n"
      "v_mfma_f32_16x16x32_bf16 v[176:179], a[236:239], v[196:199], v[176:179]\n"
      "ds_read_b128 v[172:175], v123 offset:5120\n"
      "v_cndmask_b32 v229, v229, v189, s[46:47]\n"
      "v_mfma_f32_16x16x32_bf16 v[200:203], a[240:243], v[252:255], v[200:203]\n"
      "v_mbcnt_lo_u32_b32 v231, -1, 0\n"
      "v_mbcnt_hi_u32_b32 v231, -1, v231\n"
      "v_mfma_f32_16x16x32_bf16 v[200:203], a[252:255], v[184:187], v[200:203]\n"
      "s_mov_b32 exec_hi, 0\n"
      "global_store_dwordx4 v118, v[214:217], s[54:55] sc1\n"
      "s_mov_b32 exec_hi, -1\n"
      "v_mfma_f32_16x16x32_bf16 v[244:247], a[244:247], v[252:255], v[244:247]\n"
      "s_add_u32 s28, s27, 2\n"
      "v_min_u32 v231, 48, v231\n"
      "v_lshlrev_b32 v231, 1, v231\n"
      "v_mfma_f32_16x16x32_bf16 v[244:247], v[0:3], v[184:187], v[244:247]\n"
      "s_waitcnt lgkmcnt(3)\n"
      "v_add_u32 v231, s30, v231\n"
      "v_mfma_f32_16x16x32_bf16 v[176:179], a[248:251], v[252:255], v[176:179]\n"
      "ds_read_u16 v191, v231 offset:4704\n"
      "s_min_u32 m0, s28, s3\n"
      "v_mfma_f32_16x16x32_bf16 v[176:179], v[4:7], v[184:187], v[176:179]\n"
      "ds_read_b128 v[252:255], v123 offset:8192\n"
      "v_mfma_f32_16x16x32_bf16 v[200:203], v[8:11], v[168:171], v[200:203]\n"
      "ds_read_b128 v[158:161], v123 offset:11264\n"
      "s_movrels_b32 s9, s80\n"
      "v_mfma_f32_16x16x32_bf16 v[200:203], v[20:23], v[162:165], v[200:203]\n"
      "s_waitcnt lgkmcnt(3)\n"
      "s_lshr_b32 s8, s9, 8\n"
      "v_mfma_f32_16x16x32_bf16 v[244:247], v[12:15], v[168:171], v[244:247]\n"
      "s_and_b32 s9, s9, 0xff\n"
      "s_mul_i32 s10, s8, 73\n"
      "s_add_u32 s10, s10, s9\n"
      "v_mfma_f32_16x16x32_bf16 v[244:247], v[24:27], v[162:165], v[244:247]\n"
      "s_mul_i32 s28, s10, 4802\n"
      "s_add_u32 s56, s18, s28\n"
      "v_mfma_f32_16x16x32_bf16 v[176:179], v[16:19], v[168:171], v[176:179]\n"
      "s_addc_u32 s57, s19, 0\n"
      "s_and_b32 s56, s56, -16\n"
      "s_sub_u32 s58, s56, 110592\n"
      "v_mfma_f32_16x16x32_bf16 v[176:179], v[28:31], v[162:165], v[176:179]\n"
      "s_subb_u32 s59, s57, 0\n"
      "s_sub_u32 s56, s56, 73728\n"
      "v_mfma_f32_16x16x32_bf16 v[212:215], a[216:219], v[204:207], 0\n"
      "s_waitcnt lgkmcnt(2)\n"
      "v_lshlrev_b32 v191, 16, v191\n"
      "v_mfma_f32_16x16x32_bf16 v[212:215], a[228:231], v[172:175], v[212:215]\n"
      "ds_read_b128 v[168:171], v123 offset:14336\n"
      "v_fmac_f32_e64 v229, s37, v191\n"
      "v_mfma_f32_16x16x32_bf16 v[184:187], a[220:223], v[204:207], 0\n"
      "s_waitcnt lgkmcnt(1)\n"
      "v_max_f32_dpp v147, v229, v229 row_ror:8 row_mask:0xf bank_mask:0xf\n"
      "v_mfma_f32_16x16x32_bf16 v[184:187], a[232:235], v[172:175], v[184:187]\n"
      "ds_read_b128 v[162:165], v123 offset:17408\n"
      "v_max_f32_dpp v147, v147, v147 row_ror:4 row_mask:0xf bank_mask:0xf\n"
      "v_mfma_f32_16x16x32_bf16 v[188:191], a[224:227], v[204:207], 0\n"
      "s_subb_u32 s57, s57, 0\n"
      "v_max_f32_dpp v147, v147, v147 row_ror:2 row_mask:0xf bank_mask:0xf\n"
      "v_fma_f32 v219, v200, v200, s39\n"
      "v_mfma_f32_16x16x32_bf16 v[188:191], a[236:239], v[172:175], v[188:191]\n"
      "s_mul_i32 s28, s8, 1373568\n"
      "v_max_f32_dpp v147, v147, v147 row_ror:1 row_mask:0xf bank_mask:0xf\n"
      "v_fmac_f32_e64 v219, v201, v201\n"
      "v_mfma_f32_16x16x32_bf16 v[212:215], a[240:243], v[252:255], v[212:215]\n"
      "s_mul_i32 s29, s9, 2688\n"
      "v_max_f32_dpp v147, v147, v147 row_bcast:15 row_mask:0xa bank_mask:0xf\n"
      "v_fmac_f32 v219, v202, v202\n"
      "v_mfma_f32_16x16x32_bf16 v[212:215], a[252:255], v[158:161], v[212:215]\n"
      "s_add_u32 s28, s28, s29\n"
      "v_max_f32_dpp v147, v147, v147 row_bcast:31 row_mask:0xc bank_mask:0xf\n"
      "v_fmac_f32 v219, v203, v203\n"
      "v_mfma_f32_16x16x32_bf16 v[184:187], a[244:247], v[252:255], v[184:187]\n"
      "s_cmp_eq_u32 s8, 72\n"
      "v_readlane_b32 s31, v147, 63\n"
      "v_fmac_f32 v219, v244, v244\n"
      "v_mfma_f32_16x16x32_bf16 v[184:187], v[0:3], v[158:161], v[184:187]\n"
      "s_cselect_b32 s8, 0, 100270464\n"
      "v_subrev_f32 v229, s31, v229\n"
      "v_exp_f32 v229, v229\n"
      "v_mfma_f32_16x16x32_bf16 v[188:191], a[248:251], v[252:255], v[188:191]\n"
      "s_cmp_eq_u32 s9, 72\n"
      "v_cvt_pk_bf16_f32 v229, v229, v229\n"
      "v_fmac_f32 v219, v245, v245\n"
      "v_mfma_f32_16x16x32_bf16 v[188:191], v[4:7], v[158:161], v[188:191]\n"
      "s_waitcnt lgkmcnt(0)\n"
      "v_fmac_f32 v219, v246, v246\n"
      "v_fmac_f32 v219, v247, v247\n"
      "v_mfma_f32_16x16x32_bf16 v[212:215], v[8:11], v[168:171], v[212:215]\n"
      "s_cselect_b32 s9, 0, 196224\n"
      "v_fmac_f32 v219, v176, v176\n"
      "v_fmac_f32 v219, v177, v177\n"
      "v_mfma_f32_16x16x32_bf16 v[212:215], v[20:23], v[162:165], v[212:215]\n"
      "ds_write_b16 v130, v229\n"
      "v_mfma_f32_16x16x32_bf16 v[184:187], v[12:15], v[168:171], v[184:187]\n"
      "ds_read_b128 v[204:207], v131\n"
      "v_fmac_f32 v219, v178, v178\n"
      "v_mfma_f32_16x16x32_bf16 v[184:187], v[24:27], v[162:165], v[184:187]\n"
      "ds_read_b128 v[158:161], v131 offset:64\n"
      "v_fmac_f32 v219, v179, v179\n"
      "v_mfma_f32_16x16x32_bf16 v[188:191], v[16:19], v[168:171], v[188:191]\n"
      "ds_read_b128 v[168:171], v114\n"
      "v_fma_f32 v217, v212, v212, s39\n"
      "v_mfma_f32_16x16x32_bf16 v[188:191], v[28:31], v[162:165], v[188:191]\n"
      "ds_read_b128 v[172:175], v115\n"
      "v_fmac_f32 v217, v213, v213\n"
      "v_fmac_f32 v217, v214, v214\n"
      "v_fmac_f32 v217, v215, v215\n"
      "v_fmac_f32 v217, v184, v184\n"
      "v_fmac_f32 v217, v185, v185\n"
      "v_fmac_f32 v217, v186, v186\n"
      "v_fmac_f32 v217, v187, v187\n"
      "v_fmac_f32 v217, v188, v188\n"
      "v_fmac_f32 v217, v189, v189\n"
      "v_fmac_f32 v217, v190, v190\n"
      "v_fmac_f32 v217, v191, v191\n"
      "v_permlane16_swap_b32 v230, v219\n"
      "v_mov_b32 v163, v217\n"
      "v_add_f32_e64 v230, v230, v219\n"
      "s_nop 0\n"
      "v_permlane16_swap_b32 v217, v163\n"
      "v_add_f32_e64 v217, v217, v163\n"
      "s_add_u32 s10, s8, s9\n"
      "s_nop 0\n"
      "v_permlane32_swap_b32_e64 v230, v217\n"
      "s_waitcnt lgkmcnt(2)\n"
      "v_mfma_f32_16x16x32_bf16 v[144:147], v[204:207], v[66:69], 0\n"
      "s_add_u32 s28, s28, s10\n"
      "v_add_f32_e64 v230, v230, v217\n"
      "s_add_u32 s52, s16, s28\n"
      "v_mfma_f32_16x16x32_bf16 v[144:147], v[158:161], v[66:69], v[144:147]\n"
      "ds_write_b32 v129, v230 offset:1024\n"
      "s_waitcnt lgkmcnt(1)\n"
      "v_mfma_f32_16x16x32_bf16 v[216:219], v[204:207], v[140:143], 0\n"
      "s_addc_u32 s53, s17, 0\n"
      "s_add_u32 s27, s27, 1\n"
      "s_cmp_ge_u32 s27, s26\n"
      "v_mfma_f32_16x16x32_bf16 v[216:219], v[158:161], v[50:53], v[216:219]\n"
      "s_cselect_b32 s28, 1, 0\n"
      "v_mov_b32_dpp v144, v144 quad_perm:[0,0,0,0] row_mask:0xf bank_mask:0xf\n"
      "v_rcp_f32 v144, v144\n"
      "v_mfma_f32_16x16x32_bf16 v[140:143], v[204:207], v[224:227], 0\n"
      "s_xor_b32 s29, s10, s11\n"
      "s_or_b32 s28, s28, s29\n"
      "v_mfma_f32_16x16x32_bf16 v[140:143], v[158:161], v[54:57], v[140:143]\n"
      "v_mfma_f32_16x16x32_bf16 v[208:211], v[204:207], v[136:139], 0\n"
      "v_mfma_f32_16x16x32_bf16 v[208:211], v[158:161], v[58:61], v[208:211]\n"
      "v_mfma_f32_16x16x32_bf16 v[204:207], a[0:3], v[168:171], 0\n"
      "v_mfma_f32_16x16x32_bf16 v[204:207], a[36:39], v[172:175], v[204:207]\n"
      "v_mfma_f32_16x16x32_bf16 v[160:163], a[4:7], v[168:171], 0\n"
      "v_mfma_f32_16x16x32_bf16 v[160:163], a[40:43], v[172:175], v[160:163]\n"
      "s_nop 1\n"
      "v_cndmask_b32 v216, v216, v140, s[42:43]\n"
      "s_nop 0\n"
      "v_cndmask_b32 v216, v216, v208, s[44:45]\n"
      "v_mul_f32_e64 v216, v144, v216\n"
      "v_cvt_pk_bf16_f32 v216, v216, v216\n"
      "ds_write_b16 v135, v216 offset:16\n"
      "s_branch L_join_21_%=\n"
      "L_flg_84_%=:\n"
      "v_cmp_gt_i32 vcc, 0, v200\n"
      "s_cbranch_vccz L_fb_85_%=\n"
      "s_sleep 1\n"
      "ds_read_b128 v[200:203], v128 offset:2048\n"
      "s_waitcnt lgkmcnt(0)\n"
      "v_bitop3_b32 v200, v200, v201, s4 bitop3:0x7e\n"
      "v_bitop3_b32 v202, v202, v203, s4 bitop3:0x7e\n"
      "v_or_b32 v200, v200, v202\n"
      "v_cmp_ne_u32 s[48:49], 0, v200\n"
      "s_branch L_flg_84_%=\n"
      "L_gen_28_%=:\n"
      "s_waitcnt lgkmcnt(0)\n"
      "s_nop 7\n"
      "s_nop 7\n"
      "s_cmp_eq_u64 s[48:49], 0\n"
      "s_cbranch_scc1 L_go_91_%=\n"
      "s_mov_b32 m0, s27\n"
      "s_nop 0\n"
      "s_movrels_b32 s9, s80\n"
      "s_lshr_b32 s8, s9, 8\n"
      "s_and_b32 s9, s9, 0xff\n"
      "s_mul_i32 s10, s8, 73\n"
      "s_add_u32 s10, s10, s9\n"
      "s_mul_i32 s30, s10, 4802\n"
      "s_add_u32 s30, s30, s18\n"
      "s_and_b32 s29, s30, 15\n"
      "s_add_u32 s30, s29, 110592\n"
      "s_lshl_b64 s[34:35], s[48:49], 4\n"
      "s_lshr_b64 s[34:35], s[34:35], 5\n"
      "s_cbranch_scc1 L_gen_29_%=\n"
      "v_mbcnt_lo_u32_b32 v242, -1, 0\n"
      "v_mbcnt_hi_u32_b32 v242, -1, v242\n"
      "v_lshlrev_b32_e64 v243, 1, v242\n"
      "v_add_u32 v204, 4802, v243\n"
      "v_cmp_gt_u32 vcc, s29, v243\n"
      "s_nop 1\n"
      "v_cndmask_b32 v243, v204, v243, vcc\n"
      "v_add_u32 v243, 110592, v243\n"
      "v_mov_b32 v204, 0\n"
      "s_mov_b64 exec, 0x7f\n"
      "ds_write_b16 v243, v204\n"
      "s_mov_b64 exec, -1\n"
      "v_lshlrev_b32_e64 v243, 4, v242\n"
      "v_add_u32 v243, 110592, v243\n"
      "v_min_u32 v242, 111296, v243\n"
      "ds_read_b128 v[204:207], v243\n"
      "ds_read_b128 v[244:247], v243 offset:1024\n"
      "ds_read_b128 v[188:191], v243 offset:2048\n"
      "ds_read_b128 v[196:199], v243 offset:3072\n"
      "ds_read_b128 v[228:231], v242 offset:4096\n"
      "s_waitcnt lgkmcnt(4)\n"
      "v_or3_b32 v204, v204, v205, v206\n"
      "s_waitcnt lgkmcnt(3)\n"
      "v_or3_b32 v204, v204, v207, v244\n"
      "v_or3_b32 v204, v204, v245, v246\n"
      "s_waitcnt lgkmcnt(2)\n"
      "v_or3_b32 v204, v204, v247, v188\n"
      "v_or3_b32 v204, v204, v189, v190\n"
      "s_waitcnt lgkmcnt(1)\n"
      "v_or3_b32 v204, v204, v191, v196\n"
      "v_or3_b32 v204, v204, v197, v198\n"
      "s_waitcnt lgkmcnt(0)\n"
      "v_or3_b32 v204, v204, v199, v228\n"
      "v_or3_b32 v204, v204, v229, v230\n"
      "v_bitop3_b32 v204, v204, v231, s40 bitop3:0xa8\n"
      "v_cmp_ne_u32 s[48:49], 0, v204\n"
      "s_cmp_lg_u64 s[48:49], 0\n"
      "s_cbranch_scc1 L_gen_29_%=\n"
      "s_cmp_eq_u64 s[50:51], 0\n"
      "s_cbranch_scc1 L_fast_31_%=\n"
      "L_go_91_%=:\n"
      "v_mfma_f32_16x16x32_bf16 v[204:207], v[164:167], v[176:179], v[90:93]\n"
      "ds_read_b128 v[244:247], v128 offset:3072\n"
      "v_max3_f32 v242, v212, v213, v214\n"
      "v_mfma_f32_16x16x32_bf16 v[204:207], v[34:37], v[38:41], v[204:207]\n"
      "v_max3_f32 v243, v215, v144, v145\n"
      "v_max3_f32 v242, v242, v146, v147\n"
      "v_mfma_f32_16x16x32_bf16 v[188:191], v[168:171], v[176:179], v[94:97]\n"
      "v_max3_f32 v243, v243, v200, v201\n"
      "v_max3_f32 v242, v242, v202, v203\n"
      "v_mfma_f32_16x16x32_bf16 v[188:191], v[40:43], v[38:41], v[188:191]\n"
      "v_max3_f32 v242, v242, v243, v240\n"
      "v_mov_b32 v243, v242\n"
      "v_mfma_f32_16x16x32_bf16 v[196:199], v[192:195], v[176:179], v[72:75]\n"
      "s_nop 0\n"
      "v_permlane16_swap_b32_e64 v242, v243\n"
      "v_mfma_f32_16x16x32_bf16 v[196:199], v[140:143], v[168:171], v[196:199]\n"
      "v_max_f32 v242, v242, v243\n"
      "v_mov_b32 v243, v242\n"
      "v_mfma_f32_16x16x32_bf16 v[196:199], v[148:151], v[38:41], v[196:199]\n"
      "s_nop 0\n"
      "v_permlane32_swap_b32 v242, v243\n"
      "v_mfma_f32_16x16x32_bf16 v[228:231], v[248:251], v[176:179], v[98:101]\n"
      "v_max_f32 v242, v242, v243\n"
      "v_sub_f32 v212, v212, v242\n"
      "v_mfma_f32_16x16x32_bf16 v[228:231], v[46:49], v[38:41], v[228:231]\n"
      "s_waitcnt lgkmcnt(0)\n"
      "v_sub_f32 v213, v213, v242\n"
      "v_sub_f32 v214, v214, v242\n"
      "v_sub_f32 v215, v215, v242\n"
      "v_sub_f32 v144, v144, v242\n"
      "v_sub_f32 v145, v145, v242\n"
      "v_sub_f32 v146, v146, v242\n"
      "v_sub_f32 v147, v147, v242\n"
      "v_sub_f32 v200, v200, v242\n"
      "v_sub_f32 v201, v201, v242\n"
      "v_sub_f32 v202, v202, v242\n"
      "v_sub_f32 v203, v203, v242\n"
      "v_sub_f32 v240, v240, v242\n"
      "v_exp_f32 v212, v212\n"
      "v_exp_f32 v213, v213\n"
      "v_exp_f32 v214, v214\n"
      "v_cvt_pk_bf16_f32 v212, v212, v213\n"
      "v_exp_f32 v215, v215\n"
      "v_exp_f32 v144, v144\n"
      "v_cvt_pk_bf16_f32 v213, v214, v215\n"
      "v_exp_f32 v145, v145\n"
      "v_exp_f32 v146, v146\n"
      "v_cvt_pk_bf16_f32 v214, v144, v145\n"
      "v_exp_f32_e64 v147, v147\n"
      "v_exp_f32_e64 v200, v200\n"
      "v_cvt_pk_bf16_f32 v215, v146, v147\n"
      "v_exp_f32_e64 v201, v201\n"
      "v_exp_f32_e64 v202, v202\n"
      "v_exp_f32_e64 v203, v203\n"
      "v_mfma_f32_16x16x32_bf16 v[144:147], v[164:167], v[184:187], v[102:105]\n"
      "v_exp_f32_e64 v240, v240\n"
      "v_mfma_f32_16x16x32_bf16 v[144:147], v[34:37], v[44:47], v[144:147]\n"
      "v_cvt_pk_bf16_f32 v62, v200, v201\n"
      "v_cvt_pk_bf16_f32 v63, v202, v203\n"
      "v_cvt_pk_bf16_f32 v64, v240, 0\n"
      "v_bitop3_b32 v244, v244, v245, s4 bitop3:0x7e\n"
      "v_bitop3_b32 v246, v246, v247, s4 bitop3:0x7e\n"
      "v_mfma_f32_16x16x32_bf16 v[200:203], v[66:69], v[212:215], 0\n"
      "ds_read_b128 v[176:179], v119 offset:30720\n"
      "v_or_b32 v244, v244, v246\n"
      "v_mfma_f32_16x16x32_bf16 v[200:203], v[66:69], v[62:65], v[200:203]\n"
      "v_cmp_ne_u32 vcc, 0, v244\n"
      "v_max3_f32 v198, v204, v205, v206\n"
      "v_mfma_f32_16x16x32_bf16 v[164:167], v[220:223], v[212:215], 0\n"
      "s_mov_b64 s[48:49], vcc\n"
      "v_max3_f32 v199, v207, v188, v189\n"
      "v_max3_f32 v198, v198, v190, v191\n"
      "v_mfma_f32_16x16x32_bf16 v[164:167], v[50:53], v[62:65], v[164:167]\n"
      "s_cbranch_vccnz L_flg_92_%=\n"
      "L_fb_93_%=:\n"
      "v_max3_f32 v199, v199, v228, v229\n"
      "v_mfma_f32_16x16x32_bf16 v[244:247], v[236:239], v[212:215], 0\n"
      "v_max3_f32 v198, v198, v230, v231\n"
      "v_max3_f32 v198, v198, v199, v196\n"
      "v_mfma_f32_16x16x32_bf16 v[244:247], v[54:57], v[62:65], v[244:247]\n"
      "v_mov_b32 v199, v198\n"
      "v_mfma_f32_16x16x32_bf16 v[136:139], v[252:255], v[212:215], 0\n"
      "s_nop 0\n"
      "v_permlane16_swap_b32_e64 v198, v199\n"
      "v_mfma_f32_16x16x32_bf16 v[136:139], v[58:61], v[62:65], v[136:139]\n"
      "v_max_f32 v198, v198, v199\n"
      "v_mov_b32 v199, v198\n"
      "v_mfma_f32_16x16x32_bf16 v[212:215], v[168:171], v[184:187], v[106:109]\n"
      "s_nop 0\n"
      "v_permlane32_swap_b32 v198, v199\n"
      "v_mfma_f32_16x16x32_bf16 v[212:215], v[40:43], v[44:47], v[212:215]\n"
      "v_max_f32 v198, v198, v199\n"
      "v_sub_f32 v204, v204, v198\n"
      "v_sub_f32 v205, v205, v198\n"
      "v_sub_f32 v206, v206, v198\n"
      "v_sub_f32 v207, v207, v198\n"
      "v_sub_f32 v188, v188, v198\n"
      "v_sub_f32 v189, v189, v198\n"
      "v_sub_f32 v190, v190, v198\n"
      "v_sub_f32 v191, v191, v198\n"
      "v_sub_f32 v228, v228, v198\n"
      "v_sub_f32 v229, v229, v198\n"
      "v_sub_f32 v230, v230, v198\n"
      "v_sub_f32 v231, v231, v198\n"
      "v_sub_f32 v196, v196, v198\n"
      "v_exp_f32 v204, v204\n"
      "v_exp_f32 v205, v205\n"
      "v_exp_f32 v206, v206\n"
      "v_cvt_pk_bf16_f32 v204, v204, v205\n"
      "v_exp_f32 v207, v207\n"
      "v_mfma_f32_16x16x32_bf16 v[168:171], v[192:195], v[184:187], v[74:77]\n"
      "s_waitcnt lgkmcnt(0)\n"
      "v_exp_f32 v188, v188\n"
      "v_mfma_f32_16x16x32_bf16 v[168:171], v[140:143], v[248:251], v[168:171]\n"
      "global_store_dwordx4 v116, v[176:179], s[54:55] offset:256 sc1\n"
      "v_mfma_f32_16x16x32_bf16 v[168:171], v[148:151], v[44:47], v[168:171]\n"
      "v_cvt_pk_bf16_f32 v205, v206, v207\n"
      "v_exp_f32 v189, v189\n"
      "v_mfma_f32_16x16x32_bf16 v[176:179], v[248:251], v[184:187], v[110:113]\n"
      "v_exp_f32 v190, v190\n"
      "v_mfma_f32_16x16x32_bf16 v[176:179], v[46:49], v[44:47], v[176:179]\n"
      "v_cvt_pk_bf16_f32 v206, v188, v189\n"
      "v_exp_f32 v191, v191\n"
      "v_exp_f32 v228, v228\n"
      "v_cvt_pk_bf16_f32 v207, v190, v191\n"
      "v_mfma_f32_16x16x32_bf16 v[184:187], v[140:143], v[192:195], v[76:79]\n"
      "v_exp_f32 v229, v229\n"
      "v_exp_f32 v230, v230\n"
      "v_exp_f32 v231, v231\n"
      "v_exp_f32 v196, v196\n"
      "v_cvt_pk_bf16_f32 v62, v228, v229\n"
      "v_cvt_pk_bf16_f32 v63, v230, v231\n"
      "v_mfma_f32_16x16x16_bf16 v[184:187], v[150:151], v[148:149], v[184:187]\n"
      "ds_read_b128 v[248:251], v120 offset:18432\n"
      "v_cvt_pk_bf16_f32 v64, v196, 0\n"
      "v_rcp_f32_e64 v200, v200\n"
      "v_max3_f32 v170, v144, v145, v146\n"
      "v_mfma_f32_16x16x32_bf16 v[188:191], v[66:69], v[204:207], 0\n"
      "v_mul_f32 v164, v200, v164\n"
      "v_mul_f32 v165, v200, v165\n"
      "v_mfma_f32_16x16x32_bf16 v[188:191], v[66:69], v[62:65], v[188:191]\n"
      "v_cvt_pk_bf16_f32 v164, v164, v165\n"
      "v_mul_f32 v166, v200, v166\n"
      "v_mfma_f32_16x16x32_bf16 v[192:195], v[220:223], v[204:207], 0\n"
      "v_mul_f32 v167, v200, v167\n"
      "v_cvt_pk_bf16_f32 v165, v166, v167\n"
      "v_mfma_f32_16x16x32_bf16 v[192:195], v[50:53], v[62:65], v[192:195]\n"
      "v_mul_f32 v244, v200, v244\n"
      "v_mul_f32 v245, v200, v245\n"
      "v_cvt_pk_bf16_f32 v166, v244, v245\n"
      "v_mfma_f32_16x16x32_bf16 v[228:231], v[236:239], v[204:207], 0\n"
      "v_mul_f32 v246, v200, v246\n"
      "v_mul_f32 v247, v200, v247\n"
      "v_mfma_f32_16x16x32_bf16 v[228:231], v[54:57], v[62:65], v[228:231]\n"
      "v_cvt_pk_bf16_f32 v167, v246, v247\n"
      "v_mul_f32 v136, v200, v136\n"
      "v_mul_f32 v137, v200, v137\n"
      "v_cvt_pk_bf16_f32 v136, v136, v137\n"
      "v_mul_f32 v138, v200, v138\n"
      "v_mul_f32 v139, v200, v139\n"
      "v_cvt_pk_bf16_f32 v137, v138, v139\n"
      "v_mfma_f32_16x16x32_bf16 v[200:203], v[252:255], v[204:207], 0\n"
      "v_max3_f32 v171, v147, v212, v213\n"
      "v_max3_f32 v170, v170, v214, v215\n"
      "v_mfma_f32_16x16x32_bf16 v[200:203], v[58:61], v[62:65], v[200:203]\n"
      "ds_write_b128 v124, v[164:167]\n"
      "v_max3_f32 v171, v171, v176, v177\n"
      "v_max3_f32 v170, v170, v178, v179\n"
      "v_max3_f32 v170, v170, v171, v168\n"
      "v_mov_b32 v171, v170\n"
      "s_waitcnt lgkmcnt(1)\n"
      "s_nop 0\n"
      "v_permlane16_swap_b32 v170, v171\n"
      "v_max_f32 v170, v170, v171\n"
      "v_mov_b32 v171, v170\n"
      "s_nop 1\n"
      "v_permlane32_swap_b32 v170, v171\n"
      "v_max_f32 v170, v170, v171\n"
      "v_sub_f32 v144, v144, v170\n"
      "v_sub_f32 v145, v145, v170\n"
      "v_sub_f32 v146, v146, v170\n"
      "v_sub_f32 v147, v147, v170\n"
      "v_sub_f32 v212, v212, v170\n"
      "v_sub_f32 v213, v213, v170\n"
      "v_sub_f32 v214, v214, v170\n"
      "v_sub_f32 v215, v215, v170\n"
      "v_sub_f32 v176, v176, v170\n"
      "v_sub_f32 v177, v177, v170\n"
      "v_sub_f32 v178, v178, v170\n"
      "v_sub_f32 v179, v179, v170\n"
      "v_sub_f32 v168, v168, v170\n"
      "v_exp_f32 v144, v144\n"
      "v_exp_f32 v145, v145\n"
      "v_exp_f32_e64 v146, v146\n"
      "v_cvt_pk_bf16_f32 v144, v144, v145\n"
      "v_exp_f32 v147, v147\n"
      "v_exp_f32 v212, v212\n"
      "v_cvt_pk_bf16_f32 v145, v146, v147\n"
      "v_exp_f32 v213, v213\n"
      "v_exp_f32 v214, v214\n"
      "v_cvt_pk_bf16_f32 v146, v212, v213\n"
      "v_exp_f32 v215, v215\n"
      "v_exp_f32 v176, v176\n"
      "v_cvt_pk_bf16_f32 v147, v214, v215\n"
      "v_exp_f32 v177, v177\n"
      "v_exp_f32 v178, v178\n"
      "v_exp_f32 v179, v179\n"
      "v_exp_f32 v168, v168\n"
      "v_cvt_pk_bf16_f32 v62, v176, v177\n"
      "v_cvt_pk_bf16_f32 v63, v178, v179\n"
      "v_cvt_pk_bf16_f32 v64, v168, 0\n"
      "v_rcp_f32_e64 v188, v188\n"
      "v_mfma_f32_16x16x32_bf16 v[148:151], v[66:69], v[144:147], 0\n"
      "global_store_dwordx4 v117, v[248:251], s[54:55] sc1\n"
      "v_mfma_f32_16x16x32_bf16 v[148:151], v[66:69], v[62:65], v[148:151]\n"
      "ds_read_b128 v[212:215], v123 offset:19456\n"
      "v_mul_f32 v192, v188, v192\n"
      "v_mfma_f32_16x16x32_bf16 v[248:251], v[220:223], v[144:147], 0\n"
      "ds_read_b128 v[170:173], v123 offset:22528\n"
      "v_mul_f32 v193, v188, v193\n"
      "v_mfma_f32_16x16x32_bf16 v[248:251], v[50:53], v[62:65], v[248:251]\n"
      "ds_read_b128 v[164:167], v123 offset:25600\n"
      "v_cvt_pk_bf16_f32 v192, v192, v193\n"
      "v_mfma_f32_16x16x32_bf16 v[204:207], v[252:255], v[144:147], 0\n"
      "ds_read_b128 v[140:143], v121 offset:18432\n"
      "v_mul_f32 v194, v188, v194\n"
      "v_mfma_f32_16x16x32_bf16 v[204:207], v[58:61], v[62:65], v[204:207]\n"
      "v_mul_f32 v195, v188, v195\n"
      "v_cvt_pk_bf16_f32 v193, v194, v195\n"
      "v_mul_f32 v228, v188, v228\n"
      "v_mul_f32 v229, v188, v229\n"
      "v_cvt_pk_bf16_f32 v194, v228, v229\n"
      "v_mul_f32 v230, v188, v230\n"
      "v_mul_f32 v231, v188, v231\n"
      "v_cvt_pk_bf16_f32 v195, v230, v231\n"
      "v_mul_f32_e64 v200, v188, v200\n"
      "v_mul_f32_e64 v201, v188, v201\n"
      "v_cvt_pk_bf16_f32 v200, v200, v201\n"
      "v_mul_f32_e64 v202, v188, v202\n"
      "v_mul_f32_e64 v203, v188, v203\n"
      "v_cvt_pk_bf16_f32 v201, v202, v203\n"
      "v_cndmask_b32 v241, v241, v197, s[42:43]\n"
      "v_cndmask_b32 v241, v241, v169, s[44:45]\n"
      "v_cndmask_b32 v241, v241, v185, s[46:47]\n"
      "s_nop 1\n"
      "v_max_f32_dpp v169, v241, v241 row_ror:8 row_mask:0xf bank_mask:0xf\n"
      "s_waitcnt lgkmcnt(2)\n"
      "v_mfma_f32_16x16x32_bf16 v[176:179], a[216:219], v[212:215], 0\n"
      "ds_read_b128 v[184:187], v123 offset:28672\n"
      "v_max_f32_dpp v169, v169, v169 row_ror:4 row_mask:0xf bank_mask:0xf\n"
      "v_mfma_f32_16x16x32_bf16 v[176:179], a[228:231], v[170:173], v[176:179]\n"
      "ds_write_b128 v124, v[192:195] offset:1024\n"
      "v_mfma_f32_16x16x32_bf16 v[244:247], a[220:223], v[212:215], 0\n"
      "ds_read_b128 v[188:191], v123 offset:31744\n"
      "v_max_f32_dpp v169, v169, v169 row_ror:2 row_mask:0xf bank_mask:0xf\n"
      "v_mfma_f32_16x16x32_bf16 v[244:247], a[232:235], v[170:173], v[244:247]\n"
      "ds_write2st64_b64 v125, v[136:137], v[200:201] offset0:0 offset1:2\n"
      "v_mfma_f32_16x16x32_bf16 v[200:203], a[224:227], v[212:215], 0\n"
      "ds_read_b128 v[212:215], v123 offset:34816\n"
      "v_max_f32_dpp v169, v169, v169 row_ror:1 row_mask:0xf bank_mask:0xf\n"
      "v_mfma_f32_16x16x32_bf16 v[200:203], a[236:239], v[170:173], v[200:203]\n"
      "ds_read_b128 v[192:195], v123 offset:20480\n"
      "v_max_f32_dpp v169, v169, v169 row_bcast:15 row_mask:0xa bank_mask:0xf\n"
      "s_waitcnt lgkmcnt(6)\n"
      "s_nop 0\n"
      "v_max_f32_dpp v169, v169, v169 row_bcast:31 row_mask:0xc bank_mask:0xf\n"
      "s_nop 0\n"
      "v_readlane_b32 s31, v169, 63\n"
      "s_waitcnt lgkmcnt(5)\n"
      "v_mfma_f32_16x16x32_bf16 v[176:179], a[240:243], v[164:167], v[176:179]\n"
      "ds_read_b128 v[168:171], v123 offset:23552\n"
      "v_subrev_f32 v241, s31, v241\n"
      "v_mfma_f32_16x16x32_bf16 v[176:179], a[252:255], v[184:187], v[176:179]\n"
      "ds_read_b128 v[228:231], v123 offset:26624\n"
      "v_exp_f32 v241, v241\n"
      "v_mfma_f32_16x16x32_bf16 v[244:247], a[244:247], v[164:167], v[244:247]\n"
      "v_cvt_pk_bf16_f32 v241, v241, v241\n"
      "v_mfma_f32_16x16x32_bf16 v[244:247], v[0:3], v[184:187], v[244:247]\n"
      "ds_write_b16 v130, v241\n"
      "v_mfma_f32_16x16x32_bf16 v[200:203], a[248:251], v[164:167], v[200:203]\n"
      "ds_read_b128 v[196:199], v131\n"
      "s_waitcnt lgkmcnt(5)\n"
      "v_mfma_f32_16x16x32_bf16 v[200:203], v[4:7], v[184:187], v[200:203]\n"
      "ds_read_b128 v[164:167], v131 offset:64\n"
      "v_mfma_f32_16x16x32_bf16 v[176:179], v[8:11], v[188:191], v[176:179]\n"
      "v_mfma_f32_16x16x32_bf16 v[176:179], v[20:23], v[212:215], v[176:179]\n"
      "s_mov_b32 exec_hi, 0\n"
      "global_store_dwordx4 v118, v[140:143], s[54:55] sc1\n"
      "s_mov_b32 exec_hi, -1\n"
      "s_waitcnt lgkmcnt(4)\n"
      "v_mfma_f32_16x16x32_bf16 v[244:247], v[12:15], v[188:191], v[244:247]\n"
      "s_add_u32 s28, s27, 2\n"
      "s_min_u32 m0, s28, s3\n"
      "v_mfma_f32_16x16x32_bf16 v[244:247], v[24:27], v[212:215], v[244:247]\n"
      "s_movrels_b32 s9, s80\n"
      "v_fma_f32 v156, v176, v176, s39\n"
      "v_fmac_f32 v156, v177, v177\n"
      "v_mfma_f32_16x16x32_bf16 v[240:243], v[236:239], v[144:147], 0\n"
      "ds_read_b128 v[144:147], v123 offset:29696\n"
      "v_fmac_f32 v156, v178, v178\n"
      "v_mfma_f32_16x16x32_bf16 v[240:243], v[54:57], v[62:65], v[240:243]\n"
      "s_lshr_b32 s8, s9, 8\n"
      "v_fmac_f32 v156, v179, v179\n"
      "v_fmac_f32 v156, v244, v244\n"
      "v_mfma_f32_16x16x32_bf16 v[200:203], v[16:19], v[188:191], v[200:203]\n"
      "ds_read_b128 v[140:143], v123 offset:32768\n"
      "s_waitcnt lgkmcnt(2)\n"
      "v_mfma_f32_16x16x32_bf16 v[200:203], v[28:31], v[212:215], v[200:203]\n"
      "s_and_b32 s9, s9, 0xff\n"
      "v_fmac_f32 v156, v245, v245\n"
      "v_fmac_f32 v156, v246, v246\n"
      "v_mfma_f32_16x16x32_bf16 v[184:187], a[216:219], v[192:195], 0\n"
      "s_mul_i32 s10, s8, 73\n"
      "v_fmac_f32_e64 v156, v247, v247\n"
      "s_add_u32 s10, s10, s9\n"
      "v_mfma_f32_16x16x32_bf16 v[184:187], a[228:231], v[168:171], v[184:187]\n"
      "s_mul_i32 s28, s10, 4802\n"
      "v_fmac_f32 v156, v200, v200\n"
      "v_fmac_f32 v156, v201, v201\n"
      "v_mfma_f32_16x16x32_bf16 v[212:215], a[220:223], v[192:195], 0\n"
      "s_add_u32 s56, s18, s28\n"
      "v_fmac_f32_e64 v156, v202, v202\n"
      "v_fmac_f32_e64 v156, v203, v203\n"
      "v_mfma_f32_16x16x32_bf16 v[212:215], a[232:235], v[168:171], v[212:215]\n"
      "s_addc_u32 s57, s19, 0\n"
      "s_and_b32 s56, s56, -16\n"
      "v_mfma_f32_16x16x32_bf16 v[188:191], a[224:227], v[192:195], 0\n"
      "s_waitcnt lgkmcnt(1)\n"
      "s_sub_u32 s58, s56, 110592\n"
      "v_mfma_f32_16x16x32_bf16 v[188:191], a[236:239], v[168:171], v[188:191]\n"
      "ds_read_b128 v[136:139], v123 offset:35840\n"
      "v_mfma_f32_16x16x32_bf16 v[184:187], a[240:243], v[228:231], v[184:187]\n"
      "s_subb_u32 s59, s57, 0\n"
      "s_sub_u32 s56, s56, 73728\n"
      "s_subb_u32 s57, s57, 0\n"
      "v_mfma_f32_16x16x32_bf16 v[184:187], a[252:255], v[144:147], v[184:187]\n"
      "s_mul_i32 s28, s8, 1373568\n"
      "s_mul_i32 s29, s9, 2688\n"
      "v_mfma_f32_16x16x32_bf16 v[212:215], a[244:247], v[228:231], v[212:215]\n"
      "s_add_u32 s28, s28, s29\n"
      "v_permlane16_swap_b32 v159, v156\n"
      "v_mfma_f32_16x16x32_bf16 v[212:215], v[0:3], v[144:147], v[212:215]\n"
      "s_cmp_eq_u32 s8, 72\n"
      "v_add_f32_e64 v159, v159, v156\n"
      "v_mfma_f32_16x16x32_bf16 v[188:191], a[248:251], v[228:231], v[188:191]\n"
      "s_cselect_b32 s8, 0, 100270464\n"
      "s_cmp_eq_u32 s9, 72\n"
      "s_cselect_b32 s9, 0, 196224\n"
      "v_mfma_f32_16x16x32_bf16 v[188:191], v[4:7], v[144:147], v[188:191]\n"
      "s_add_u32 s10, s8, s9\n"
      "s_add_u32 s28, s28, s10\n"
      "v_mfma_f32_16x16x32_bf16 v[226:229], v[196:199], v[66:69], 0\n"
      "s_add_u32 s52, s16, s28\n"
      "s_addc_u32 s53, s17, 0\n"
      "s_add_u32 s27, s27, 1\n"
      "v_mfma_f32_16x16x32_bf16 v[226:229], v[164:167], v[66:69], v[226:229]\n"
      "s_waitcnt lgkmcnt(0)\n"
      "s_cmp_ge_u32 s27, s26\n"
      "v_mfma_f32_16x16x32_bf16 v[184:187], v[8:11], v[140:143], v[184:187]\n"
      "s_cselect_b32 s28, 1, 0\n"
      "s_xor_b32 s29, s10, s11\n"
      "s_or_b32 s28, s28, s29\n"
      "v_mfma_f32_16x16x32_bf16 v[184:187], v[20:23], v[136:139], v[184:187]\n"
      "s_nop 0\n"
      "v_mov_b32_dpp v226, v226 quad_perm:[0,0,0,0] row_mask:0xf bank_mask:0xf\n"
      "v_rcp_f32 v226, v226\n"
      "v_mfma_f32_16x16x32_bf16 v[212:215], v[12:15], v[140:143], v[212:215]\n"
      "v_mfma_f32_16x16x32_bf16 v[212:215], v[24:27], v[136:139], v[212:215]\n"
      "ds_read_b128 v[168:171], v114 offset:18432\n"
      "v_mfma_f32_16x16x32_bf16 v[188:191], v[16:19], v[140:143], v[188:191]\n"
      "ds_read_b128 v[192:195], v115 offset:18432\n"
      "v_fma_f32 v228, v184, v184, s39\n"
      "v_mfma_f32_16x16x32_bf16 v[188:191], v[28:31], v[136:139], v[188:191]\n"
      "v_fmac_f32 v228, v185, v185\n"
      "v_fmac_f32 v228, v186, v186\n"
      "v_mfma_f32_16x16x32_bf16 v[136:139], v[196:199], v[220:223], 0\n"
      "v_fmac_f32 v228, v187, v187\n"
      "v_fmac_f32 v228, v212, v212\n"
      "v_mfma_f32_16x16x32_bf16 v[136:139], v[164:167], v[50:53], v[136:139]\n"
      "v_fmac_f32 v228, v213, v213\n"
      "v_fmac_f32 v228, v214, v214\n"
      "v_mfma_f32_16x16x32_bf16 v[220:223], v[196:199], v[236:239], 0\n"
      "v_fmac_f32 v228, v215, v215\n"
      "v_fmac_f32 v228, v188, v188\n"
      "v_mfma_f32_16x16x32_bf16 v[220:223], v[164:167], v[54:57], v[220:223]\n"
      "v_fmac_f32 v228, v189, v189\n"
      "v_fmac_f32 v228, v190, v190\n"
      "v_mfma_f32_16x16x32_bf16 v[236:239], v[196:199], v[252:255], 0\n"
      "v_fmac_f32 v228, v191, v191\n"
      "v_mov_b32 v229, v228\n"
      "v_mfma_f32_16x16x32_bf16 v[236:239], v[164:167], v[58:61], v[236:239]\n"
      "s_nop 0\n"
      "v_permlane16_swap_b32 v228, v229\n"
      "v_add_f32 v228, v228, v229\n"
      "v_cndmask_b32 v136, v136, v220, s[42:43]\n"
      "s_waitcnt lgkmcnt(0)\n"
      "v_mfma_f32_16x16x32_bf16 v[196:199], a[0:3], v[168:171], 0\n"
      "v_permlane32_swap_b32_e64 v159, v228\n"
      "v_mfma_f32_16x16x32_bf16 v[196:199], a[36:39], v[192:195], v[196:199]\n"
      "v_add_f32 v159, v159, v228\n"
      "v_cndmask_b32 v136, v136, v236, s[44:45]\n"
      "v_mfma_f32_16x16x32_bf16 v[228:231], a[4:7], v[168:171], 0\n"
      "v_mul_f32 v136, v226, v136\n"
      "v_cvt_pk_bf16_f32 v136, v136, v136\n"
      "v_mfma_f32_16x16x32_bf16 v[228:231], a[40:43], v[192:195], v[228:231]\n"
      "ds_write_b32 v129, v159\n"
      "ds_write_b16 v135, v136\n"
      "s_branch L_join_30_%=\n"
      "L_flg_92_%=:\n"
      "v_cmp_gt_i32 vcc, 0, v244\n"
      "s_cbranch_vccz L_fb_93_%=\n"
      "s_sleep 1\n"
      "ds_read_b128 v[244:247], v128 offset:3072\n"
      "s_waitcnt lgkmcnt(0)\n"
      "v_bitop3_b32 v244, v244, v245, s4 bitop3:0x7e\n"
      "v_bitop3_b32 v246, v246, v247, s4 bitop3:0x7e\n"
      "v_or_b32 v244, v244, v246\n"
      "v_cmp_ne_u32 s[48:49], 0, v244\n"
      "s_branch L_flg_92_%=\n"
      "L_gen_29_%=:\n"
      "s_waitcnt lgkmcnt(0)\n"
      "s_nop 7\n"
      "s_nop 7\n"
      "v_mbcnt_lo_u32_b32 v242, -1, 0\n"
      "v_mbcnt_hi_u32_b32 v242, -1, v242\n"
      "v_and_b32 v243, 15, v242\n"
      "v_lshrrev_b32 v204, 4, v242\n"
      "v_mul_u32_u24 v205, 0x62, v243\n"
      "v_lshl_add_u32 v205, v204, 3, v205\n"
      "v_add_u32 v205, s30, v205\n"
      "v_and_b32 v242, -4, v205\n"
      "v_and_b32 v205, 2, v205\n"
      "v_lshlrev_b32 v204, 2, v204\n"
      "v_sub_u32 v243, v243, v204\n"
      "v_and_b32 v204, 1, v243\n"
      "v_lshlrev_b32 v204, 4, v204\n"
      "v_lshrrev_b32 v243, 1, v243\n"
      "v_cmp_eq_u32 vcc, 0, v243\n"
      "s_movk_i32 s31, 0x3fb9\n"
      "v_lshlrev_b32 v204, v204, s31\n"
      "ds_read2_b32 v[206:207], v242 offset0:0 offset1:1\n"
      "ds_read2_b32 v[244:245], v242 offset0:8 offset1:9\n"
      "ds_read2_b32 v[246:247], v242 offset0:16 offset1:17\n"
      "ds_read2_b32 v[188:189], v242 offset0:2 offset1:10\n"
      "ds_read2_b32 v[190:191], v242 offset0:18 offset1:24\n"
      "v_add_u32 v242, 1568, v242\n"
      "ds_read2_b32 v[196:197], v242 offset0:0 offset1:1\n"
      "ds_read2_b32 v[198:199], v242 offset0:8 offset1:9\n"
      "ds_read2_b32 v[228:229], v242 offset0:16 offset1:17\n"
      "ds_read2_b32 v[230:231], v242 offset0:2 offset1:10\n"
      "ds_read2_b32 v[156:157], v242 offset0:18 offset1:24\n"
      "v_add_u32 v242, 1568, v242\n"
      "ds_read2_b32 v[136:137], v242 offset0:0 offset1:1\n"
      "ds_read2_b32 v[138:139], v242 offset0:8 offset1:9\n"
      "ds_read2_b32 v[160:161], v242 offset0:16 offset1:17\n"
      "ds_read2_b32 v[162:163], v242 offset0:2 offset1:10\n"
      "ds_read2_b32 v[172:173], v242 offset0:18 offset1:24\n"
      "v_cndmask_b32 v174, 0, v204, vcc\n"
      "v_cmp_eq_u32 vcc, 1, v243\n"
      "s_waitcnt lgkmcnt(13)\n"
      "v_alignbyte_b32 v206, v207, v206, v205\n"
      "v_cndmask_b32 v175, 0, v204, vcc\n"
      "v_mfma_f32_16x16x32_bf16 v[208:211], v[164:167], v[176:179], v[90:93]\n"
      "s_waitcnt lgkmcnt(11)\n"
      "v_alignbyte_b32 v207, v188, v207, v205\n"
      "v_mfma_f32_16x16x32_bf16 v[208:211], v[34:37], v[38:41], v[208:211]\n"
      "v_alignbyte_b32 v244, v245, v244, v205\n"
      "v_alignbyte_b32 v245, v189, v245, v205\n"
      "v_mfma_f32_16x16x32_bf16 v[216:219], v[168:171], v[176:179], v[94:97]\n"
      "s_waitcnt lgkmcnt(8)\n"
      "v_alignbyte_b32 v246, v247, v246, v205\n"
      "v_alignbyte_b32 v247, v190, v247, v205\n"
      "v_mfma_f32_16x16x32_bf16 v[216:219], v[40:43], v[38:41], v[216:219]\n"
      "v_alignbyte_b32 v191, v191, v191, v205\n"
      "v_lshlrev_b32_e64 v191, 16, v191\n"
      "v_mfma_f32_16x16x32_bf16 v[224:227], v[192:195], v[176:179], v[72:75]\n"
      "s_waitcnt lgkmcnt(5)\n"
      "v_fmac_f32 v240, s37, v191\n"
      "v_alignbyte_b32 v196, v197, v196, v205\n"
      "v_mfma_f32_16x16x32_bf16 v[224:227], v[140:143], v[168:171], v[224:227]\n"
      "v_alignbyte_b32 v197, v230, v197, v205\n"
      "v_alignbyte_b32 v198, v199, v198, v205\n"
      "v_mfma_f32_16x16x32_bf16 v[224:227], v[148:151], v[38:41], v[224:227]\n"
      "v_alignbyte_b32 v199, v231, v199, v205\n"
      "v_alignbyte_b32 v228, v229, v228, v205\n"
      "v_mfma_f32_16x16x32_bf16 v[188:191], v[248:251], v[176:179], v[98:101]\n"
      "v_alignbyte_b32 v229, v156, v229, v205\n"
      "v_alignbyte_b32 v157, v157, v157, v205\n"
      "v_mfma_f32_16x16x32_bf16 v[188:191], v[46:49], v[38:41], v[188:191]\n"
      "v_lshlrev_b32_e64 v157, 16, v157\n"
      "v_mfma_f32_16x16x16_bf16 v[212:215], v[174:175], v[206:207], v[212:215]\n"
      "v_mfma_f32_16x16x16_bf16 v[144:147], v[174:175], v[244:245], v[144:147]\n"
      "s_nop 0\n"
      "v_mfma_f32_16x16x16_bf16 v[200:203], v[174:175], v[246:247], v[200:203]\n"
      "ds_read_b128 v[176:179], v128 offset:3072\n"
      "s_nop 4\n"
      "v_max3_f32 v243, v212, v213, v214\n"
      "v_max3_f32 v206, v215, v144, v145\n"
      "v_mfma_f32_16x16x16_bf16 v[208:211], v[174:175], v[196:197], v[208:211]\n"
      "v_max3_f32 v243, v243, v146, v147\n"
      "v_max3_f32 v206, v206, v200, v201\n"
      "v_mfma_f32_16x16x16_bf16 v[216:219], v[174:175], v[198:199], v[216:219]\n"
      "v_max3_f32 v243, v243, v202, v203\n"
      "v_max3_f32 v243, v243, v206, v240\n"
      "v_mfma_f32_16x16x16_bf16 v[188:191], v[174:175], v[228:229], v[188:191]\n"
      "v_mov_b32 v206, v243\n"
      "v_fmac_f32 v224, s37, v157\n"
      "s_nop 0\n"
      "v_permlane16_swap_b32 v243, v206\n"
      "v_max_f32 v243, v243, v206\n"
      "v_mov_b32_e64 v206, v243\n"
      "s_waitcnt lgkmcnt(1)\n"
      "v_alignbyte_b32 v136, v137, v136, v205\n"
      "v_permlane32_swap_b32 v243, v206\n"
      "v_max_f32 v243, v243, v206\n"
      "v_sub_f32 v212, v212, v243\n"
      "v_sub_f32 v213, v213, v243\n"
      "v_sub_f32 v214, v214, v243\n"
      "v_sub_f32 v215, v215, v243\n"
      "v_sub_f32 v144, v144, v243\n"
      "v_sub_f32 v145, v145, v243\n"
      "v_sub_f32 v146, v146, v243\n"
      "v_sub_f32 v147, v147, v243\n"
      "v_sub_f32 v200, v200, v243\n"
      "v_sub_f32 v201, v201, v243\n"
      "v_sub_f32 v202, v202, v243\n"
      "v_sub_f32 v203, v203, v243\n"
      "v_sub_f32 v240, v240, v243\n"
      "v_exp_f32 v212, v212\n"
      "v_exp_f32 v213, v213\n"
      "v_exp_f32 v214, v214\n"
      "v_cvt_pk_bf16_f32 v212, v212, v213\n"
      "v_exp_f32 v215, v215\n"
      "v_exp_f32 v144, v144\n"
      "v_cvt_pk_bf16_f32 v213, v214, v215\n"
      "v_exp_f32 v145, v145\n"
      "v_exp_f32 v146, v146\n"
      "v_cvt_pk_bf16_f32 v214, v144, v145\n"
      "v_exp_f32 v147, v147\n"
      "v_exp_f32 v200, v200\n"
      "v_cvt_pk_bf16_f32 v215, v146, v147\n"
      "v_exp_f32 v201, v201\n"
      "v_exp_f32 v202, v202\n"
      "v_exp_f32 v203, v203\n"
      "v_exp_f32 v240, v240\n"
      "v_cvt_pk_bf16_f32 v62, v200, v201\n"
      "v_cvt_pk_bf16_f32 v63, v202, v203\n"
      "v_cvt_pk_bf16_f32 v64, v240, 0\n"
      "v_max3_f32 v240, v208, v209, v210\n"
      "v_max3_f32 v243, v211, v216, v217\n"
      "v_max3_f32 v240, v240, v218, v219\n"
      "v_max3_f32 v243, v243, v188, v189\n"
      "v_max3_f32 v240, v240, v190, v191\n"
      "v_max3_f32 v240, v240, v243, v224\n"
      "v_mfma_f32_16x16x32_bf16 v[200:203], v[66:69], v[212:215], 0\n"
      "v_alignbyte_b32 v137, v162, v137, v205\n"
      "v_mov_b32_e64 v243, v240\n"
      "v_mfma_f32_16x16x32_bf16 v[200:203], v[66:69], v[62:65], v[200:203]\n"
      "v_alignbyte_b32 v138, v139, v138, v205\n"
      "v_alignbyte_b32 v139, v163, v139, v205\n"
      "v_mfma_f32_16x16x32_bf16 v[244:247], v[220:223], v[212:215], 0\n"
      "v_alignbyte_b32 v160, v161, v160, v205\n"
      "v_mfma_f32_16x16x32_bf16 v[244:247], v[50:53], v[62:65], v[244:247]\n"
      "s_waitcnt lgkmcnt(0)\n"
      "v_alignbyte_b32 v161, v172, v161, v205\n"
      "v_alignbyte_b32 v173, v173, v173, v205\n"
      "v_lshlrev_b32 v173, 16, v173\n"
      "v_bitop3_b32 v176, v176, v177, s4 bitop3:0x7e\n"
      "v_bitop3_b32 v178, v178, v179, s4 bitop3:0x7e\n"
      "v_or_b32 v176, v176, v178\n"
      "v_cmp_ne_u32 vcc, 0, v176\n"
      "v_permlane16_swap_b32 v240, v243\n"
      "v_max_f32 v240, v240, v243\n"
      "v_mov_b32 v243, v240\n"
      "s_mov_b64 s[48:49], vcc\n"
      "s_nop 0\n"
      "v_permlane32_swap_b32 v240, v243\n"
      "v_max_f32 v240, v240, v243\n"
      "v_sub_f32 v208, v208, v240\n"
      "v_sub_f32 v209, v209, v240\n"
      "v_sub_f32 v210, v210, v240\n"
      "v_sub_f32 v211, v211, v240\n"
      "v_sub_f32 v216, v216, v240\n"
      "v_sub_f32 v217, v217, v240\n"
      "v_sub_f32 v218, v218, v240\n"
      "v_sub_f32 v219, v219, v240\n"
      "v_sub_f32 v188, v188, v240\n"
      "v_sub_f32 v189, v189, v240\n"
      "v_sub_f32 v190, v190, v240\n"
      "v_sub_f32 v191, v191, v240\n"
      "v_sub_f32_e64 v224, v224, v240\n"
      "v_mfma_f32_16x16x32_bf16 v[204:207], v[236:239], v[212:215], 0\n"
      "s_cbranch_vccnz L_flg_98_%=\n"
      "L_fb_99_%=:\n"
      "v_mfma_f32_16x16x32_bf16 v[204:207], v[54:57], v[62:65], v[204:207]\n"
      "ds_read_b128 v[176:179], v119 offset:30720\n"
      "v_exp_f32 v208, v208\n"
      "v_mfma_f32_16x16x32_bf16 v[196:199], v[252:255], v[212:215], 0\n"
      "v_exp_f32 v209, v209\n"
      "v_mfma_f32_16x16x32_bf16 v[196:199], v[58:61], v[62:65], v[196:199]\n"
      "v_exp_f32 v210, v210\n"
      "v_mfma_f32_16x16x32_bf16 v[212:215], v[164:167], v[184:187], v[102:105]\n"
      "v_cvt_pk_bf16_f32 v208, v208, v209\n"
      "v_mfma_f32_16x16x32_bf16 v[212:215], v[34:37], v[44:47], v[212:215]\n"
      "v_exp_f32 v211, v211\n"
      "v_mfma_f32_16x16x32_bf16 v[228:231], v[168:171], v[184:187], v[106:109]\n"
      "v_exp_f32 v216, v216\n"
      "v_mfma_f32_16x16x32_bf16 v[228:231], v[40:43], v[44:47], v[228:231]\n"
      "v_cvt_pk_bf16_f32 v209, v210, v211\n"
      "v_exp_f32 v217, v217\n"
      "v_mfma_f32_16x16x16_bf16 v[212:215], v[174:175], v[136:137], v[212:215]\n"
      "v_exp_f32 v218, v218\n"
      "v_cvt_pk_bf16_f32 v210, v216, v217\n"
      "v_exp_f32 v219, v219\n"
      "v_mfma_f32_16x16x16_bf16 v[228:231], v[174:175], v[138:139], v[228:231]\n"
      "s_waitcnt lgkmcnt(0)\n"
      "v_exp_f32 v188, v188\n"
      "v_cvt_pk_bf16_f32 v211, v218, v219\n"
      "v_exp_f32 v189, v189\n"
      "v_exp_f32 v190, v190\n"
      "v_exp_f32 v191, v191\n"
      "v_exp_f32 v224, v224\n"
      "v_cvt_pk_bf16_f32 v62, v188, v189\n"
      "v_cvt_pk_bf16_f32 v63, v190, v191\n"
      "v_mfma_f32_16x16x32_bf16 v[188:191], v[192:195], v[184:187], v[74:77]\n"
      "global_store_dwordx4 v116, v[176:179], s[54:55] offset:256 sc1\n"
      "v_mfma_f32_16x16x32_bf16 v[188:191], v[140:143], v[248:251], v[188:191]\n"
      "v_cvt_pk_bf16_f32 v64, v224, 0\n"
      "v_rcp_f32 v200, v200\n"
      "v_mfma_f32_16x16x32_bf16 v[188:191], v[148:151], v[44:47], v[188:191]\n"
      "v_mul_f32 v244, v200, v244\n"
      "v_mul_f32 v245, v200, v245\n"
      "v_mfma_f32_16x16x32_bf16 v[176:179], v[248:251], v[184:187], v[110:113]\n"
      "v_cvt_pk_bf16_f32 v244, v244, v245\n"
      "v_mul_f32 v246, v200, v246\n"
      "v_mfma_f32_16x16x32_bf16 v[176:179], v[46:49], v[44:47], v[176:179]\n"
      "v_mul_f32 v247, v200, v247\n"
      "v_cvt_pk_bf16_f32 v245, v246, v247\n"
      "v_mfma_f32_16x16x32_bf16 v[184:187], v[140:143], v[192:195], v[76:79]\n"
      "v_fmac_f32 v188, s37, v173\n"
      "v_mul_f32_e64 v204, v200, v204\n"
      "v_mfma_f32_16x16x32_bf16 v[192:195], v[66:69], v[208:211], 0\n"
      "v_mul_f32_e64 v205, v200, v205\n"
      "v_cvt_pk_bf16_f32 v246, v204, v205\n"
      "v_mfma_f32_16x16x32_bf16 v[192:195], v[66:69], v[62:65], v[192:195]\n"
      "ds_read_b128 v[248:251], v120 offset:18432\n"
      "v_mul_f32 v206, v200, v206\n"
      "v_mfma_f32_16x16x16_bf16 v[184:187], v[150:151], v[148:149], v[184:187]\n"
      "v_mul_f32 v207, v200, v207\n"
      "v_cvt_pk_bf16_f32 v247, v206, v207\n"
      "v_mfma_f32_16x16x16_bf16 v[176:179], v[174:175], v[160:161], v[176:179]\n"
      "ds_write_b128 v124, v[244:247]\n"
      "v_mfma_f32_16x16x32_bf16 v[244:247], v[220:223], v[208:211], 0\n"
      "v_mul_f32 v196, v200, v196\n"
      "v_mul_f32 v197, v200, v197\n"
      "v_mfma_f32_16x16x32_bf16 v[244:247], v[50:53], v[62:65], v[244:247]\n"
      "v_cvt_pk_bf16_f32 v196, v196, v197\n"
      "v_mul_f32 v198, v200, v198\n"
      "v_mfma_f32_16x16x32_bf16 v[168:171], v[236:239], v[208:211], 0\n"
      "v_mul_f32 v199, v200, v199\n"
      "v_cvt_pk_bf16_f32 v197, v198, v199\n"
      "v_mfma_f32_16x16x32_bf16 v[168:171], v[54:57], v[62:65], v[168:171]\n"
      "v_max3_f32 v190, v212, v213, v214\n"
      "v_max3_f32 v191, v215, v228, v229\n"
      "v_mfma_f32_16x16x32_bf16 v[200:203], v[252:255], v[208:211], 0\n"
      "v_max3_f32 v190, v190, v230, v231\n"
      "v_max3_f32 v191, v191, v176, v177\n"
      "v_mfma_f32_16x16x32_bf16 v[200:203], v[58:61], v[62:65], v[200:203]\n"
      "s_waitcnt lgkmcnt(1)\n"
      "v_max3_f32 v190, v190, v178, v179\n"
      "v_max3_f32 v190, v190, v191, v188\n"
      "v_mov_b32 v191, v190\n"
      "s_nop 1\n"
      "v_permlane16_swap_b32 v190, v191\n"
      "v_max_f32 v190, v190, v191\n"
      "v_mov_b32 v191, v190\n"
      "v_rcp_f32 v192, v192\n"
      "s_nop 0\n"
      "v_permlane32_swap_b32 v190, v191\n"
      "v_max_f32 v190, v190, v191\n"
      "v_sub_f32 v212, v212, v190\n"
      "v_sub_f32 v213, v213, v190\n"
      "v_sub_f32 v214, v214, v190\n"
      "v_sub_f32 v215, v215, v190\n"
      "v_sub_f32 v228, v228, v190\n"
      "v_sub_f32 v229, v229, v190\n"
      "v_sub_f32 v230, v230, v190\n"
      "v_sub_f32 v231, v231, v190\n"
      "v_sub_f32 v176, v176, v190\n"
      "v_sub_f32 v177, v177, v190\n"
      "v_sub_f32 v178, v178, v190\n"
      "v_sub_f32 v179, v179, v190\n"
      "v_sub_f32 v188, v188, v190\n"
      "v_exp_f32 v212, v212\n"
      "v_exp_f32 v213, v213\n"
      "v_exp_f32 v214, v214\n"
      "v_cvt_pk_bf16_f32 v212, v212, v213\n"
      "v_exp_f32 v215, v215\n"
      "v_exp_f32 v228, v228\n"
      "v_cvt_pk_bf16_f32 v213, v214, v215\n"
      "v_exp_f32 v229, v229\n"
      "v_exp_f32 v230, v230\n"
      "v_cvt_pk_bf16_f32 v214, v228, v229\n"
      "v_exp_f32 v231, v231\n"
      "v_exp_f32 v176, v176\n"
      "v_cvt_pk_bf16_f32 v215, v230, v231\n"
      "v_exp_f32 v177, v177\n"
      "v_exp_f32 v178, v178\n"
      "v_exp_f32 v179, v179\n"
      "v_exp_f32 v188, v188\n"
      "v_cvt_pk_bf16_f32 v62, v176, v177\n"
      "v_cvt_pk_bf16_f32 v63, v178, v179\n"
      "v_cvt_pk_bf16_f32 v64, v188, 0\n"
      "v_mul_f32 v244, v192, v244\n"
      "v_mul_f32 v245, v192, v245\n"
      "v_mfma_f32_16x16x32_bf16 v[148:151], v[66:69], v[212:215], 0\n"
      "global_store_dwordx4 v117, v[248:251], s[54:55] sc1\n"
      "v_mfma_f32_16x16x32_bf16 v[148:151], v[66:69], v[62:65], v[148:151]\n"
      "ds_read_b128 v[228:231], v123 offset:19456\n"
      "v_cvt_pk_bf16_f32 v244, v244, v245\n"
      "v_mfma_f32_16x16x32_bf16 v[248:251], v[220:223], v[212:215], 0\n"
      "ds_read_b128 v[208:211], v123 offset:22528\n"
      "v_mul_f32_e64 v246, v192, v246\n"
      "v_mfma_f32_16x16x32_bf16 v[248:251], v[50:53], v[62:65], v[248:251]\n"
      "ds_read_b128 v[140:143], v121 offset:18432\n"
      "v_mul_f32_e64 v247, v192, v247\n"
      "v_mfma_f32_16x16x32_bf16 v[204:207], v[252:255], v[212:215], 0\n"
      "ds_read_b128 v[172:175], v123 offset:25600\n"
      "v_cvt_pk_bf16_f32 v245, v246, v247\n"
      "v_mfma_f32_16x16x32_bf16 v[204:207], v[58:61], v[62:65], v[204:207]\n"
      "v_mul_f32 v168, v192, v168\n"
      "v_mul_f32 v169, v192, v169\n"
      "v_cvt_pk_bf16_f32 v246, v168, v169\n"
      "v_mul_f32 v170, v192, v170\n"
      "v_mul_f32 v171, v192, v171\n"
      "v_cvt_pk_bf16_f32 v247, v170, v171\n"
      "v_mul_f32 v200, v192, v200\n"
      "v_mul_f32 v201, v192, v201\n"
      "v_cvt_pk_bf16_f32 v200, v200, v201\n"
      "v_mul_f32_e64 v202, v192, v202\n"
      "v_mul_f32_e64 v203, v192, v203\n"
      "v_cvt_pk_bf16_f32 v201, v202, v203\n"
      "v_cndmask_b32 v241, v241, v225, s[42:43]\n"
      "v_cndmask_b32 v241, v241, v189, s[44:45]\n"
      "v_cndmask_b32 v241, v241, v185, s[46:47]\n"
      "v_mbcnt_lo_u32_b32 v242, -1, 0\n"
      "v_mbcnt_hi_u32_b32 v242, -1, v242\n"
      "v_min_u32_e64 v242, 48, v242\n"
      "v_lshlrev_b32_e64 v242, 1, v242\n"
      "v_add_u32_e64 v242, s30, v242\n"
      "s_waitcnt lgkmcnt(2)\n"
      "v_mfma_f32_16x16x32_bf16 v[176:179], a[216:219], v[228:231], 0\n"
      "ds_read_b128 v[184:187], v123 offset:28672\n"
      "s_waitcnt lgkmcnt(2)\n"
      "v_mfma_f32_16x16x32_bf16 v[176:179], a[228:231], v[208:211], v[176:179]\n"
      "ds_write_b128 v124, v[244:247] offset:1024\n"
      "v_mfma_f32_16x16x32_bf16 v[244:247], a[220:223], v[228:231], 0\n"
      "ds_read_u16 v240, v242 offset:4704\n"
      "v_mfma_f32_16x16x32_bf16 v[244:247], a[232:235], v[208:211], v[244:247]\n"
      "ds_write2st64_b64 v125, v[196:197], v[200:201] offset0:0 offset1:2\n"
      "v_mfma_f32_16x16x32_bf16 v[200:203], a[224:227], v[228:231], 0\n"
      "ds_read_b128 v[188:191], v123 offset:31744\n"
      "v_mfma_f32_16x16x32_bf16 v[200:203], a[236:239], v[208:211], v[200:203]\n"
      "ds_read_b128 v[168:171], v123 offset:34816\n"
      "ds_read_b128 v[192:195], v123 offset:20480\n"
      "ds_read_b128 v[228:231], v123 offset:23552\n"
      "ds_read_b128 v[196:199], v123 offset:26624\n"
      "s_waitcnt lgkmcnt(8)\n"
      "v_mfma_f32_16x16x32_bf16 v[176:179], a[240:243], v[172:175], v[176:179]\n"
      "v_mfma_f32_16x16x32_bf16 v[176:179], a[252:255], v[184:187], v[176:179]\n"
      "s_mov_b32 exec_hi, 0\n"
      "global_store_dwordx4 v118, v[140:143], s[54:55] sc1\n"
      "s_mov_b32 exec_hi, -1\n"
      "v_mfma_f32_16x16x32_bf16 v[244:247], a[244:247], v[172:175], v[244:247]\n"
      "ds_read_b128 v[208:211], v123 offset:29696\n"
      "s_waitcnt lgkmcnt(7)\n"
      "v_mfma_f32_16x16x32_bf16 v[244:247], v[0:3], v[184:187], v[244:247]\n"
      "s_add_u32 s28, s27, 2\n"
      "v_lshlrev_b32 v240, 16, v240\n"
      "v_fmac_f32 v241, s37, v240\n"
      "v_mfma_f32_16x16x32_bf16 v[200:203], a[248:251], v[172:175], v[200:203]\n"
      "s_min_u32 m0, s28, s3\n"
      "v_max_f32_dpp v240, v241, v241 row_ror:8 row_mask:0xf bank_mask:0xf\n"
      "s_movrels_b32 s9, s80\n"
      "v_mfma_f32_16x16x32_bf16 v[200:203], v[4:7], v[184:187], v[200:203]\n"
      "s_waitcnt lgkmcnt(4)\n"
      "v_max_f32_dpp v240, v240, v240 row_ror:4 row_mask:0xf bank_mask:0xf\n"
      "v_mfma_f32_16x16x32_bf16 v[176:179], v[8:11], v[188:191], v[176:179]\n"
      "s_waitcnt lgkmcnt(2)\n"
      "v_max_f32_dpp v240, v240, v240 row_ror:2 row_mask:0xf bank_mask:0xf\n"
      "v_mfma_f32_16x16x32_bf16 v[176:179], v[20:23], v[168:171], v[176:179]\n"
      "ds_read_b128 v[172:175], v123 offset:32768\n"
      "v_max_f32_dpp v240, v240, v240 row_ror:1 row_mask:0xf bank_mask:0xf\n"
      "v_mfma_f32_16x16x32_bf16 v[244:247], v[12:15], v[188:191], v[244:247]\n"
      "s_lshr_b32 s8, s9, 8\n"
      "v_max_f32_dpp v240, v240, v240 row_bcast:15 row_mask:0xa bank_mask:0xf\n"
      "s_and_b32 s9, s9, 0xff\n"
      "v_mfma_f32_16x16x32_bf16 v[244:247], v[24:27], v[168:171], v[244:247]\n"
      "s_mul_i32 s10, s8, 73\n"
      "v_max_f32_dpp v240, v240, v240 row_bcast:31 row_mask:0xc bank_mask:0xf\n"
      "v_fma_f32 v157, v176, v176, s39\n"
      "v_mfma_f32_16x16x32_bf16 v[200:203], v[16:19], v[188:191], v[200:203]\n"
      "s_waitcnt lgkmcnt(1)\n"
      "v_readlane_b32 s31, v240, 63\n"
      "v_mfma_f32_16x16x32_bf16 v[200:203], v[28:31], v[168:171], v[200:203]\n"
      "s_add_u32 s10, s10, s9\n"
      "v_subrev_f32 v241, s31, v241\n"
      "v_exp_f32 v241, v241\n"
      "v_mfma_f32_16x16x32_bf16 v[184:187], a[216:219], v[192:195], 0\n"
      "s_mul_i32 s28, s10, 4802\n"
      "v_cvt_pk_bf16_f32 v241, v241, v241\n"
      "v_fmac_f32_e64 v157, v177, v177\n"
      "v_mfma_f32_16x16x32_bf16 v[184:187], a[228:231], v[228:231], v[184:187]\n"
      "ds_write_b16 v130, v241\n"
      "v_mfma_f32_16x16x32_bf16 v[240:243], v[236:239], v[212:215], 0\n"
      "ds_read_b128 v[140:143], v131\n"
      "v_fmac_f32 v157, v178, v178\n"
      "v_mfma_f32_16x16x32_bf16 v[240:243], v[54:57], v[62:65], v[240:243]\n"
      "ds_read_b128 v[216:219], v131 offset:64\n"
      "v_fmac_f32 v157, v179, v179\n"
      "v_mfma_f32_16x16x32_bf16 v[212:215], a[220:223], v[192:195], 0\n"
      "s_add_u32 s56, s18, s28\n"
      "v_fmac_f32 v157, v244, v244\n"
      "v_fmac_f32 v157, v245, v245\n"
      "v_mfma_f32_16x16x32_bf16 v[212:215], a[232:235], v[228:231], v[212:215]\n"
      "s_addc_u32 s57, s19, 0\n"
      "v_fmac_f32 v157, v246, v246\n"
      "v_fmac_f32 v157, v247, v247\n"
      "v_mfma_f32_16x16x32_bf16 v[188:191], a[224:227], v[192:195], 0\n"
      "ds_read_b128 v[136:139], v123 offset:35840\n"
      "v_fmac_f32_e64 v157, v200, v200\n"
      "v_mfma_f32_16x16x32_bf16 v[188:191], a[236:239], v[228:231], v[188:191]\n"
      "s_and_b32 s56, s56, -16\n"
      "v_fmac_f32 v157, v201, v201\n"
      "v_fmac_f32_e64 v157, v202, v202\n"
      "v_mfma_f32_16x16x32_bf16 v[184:187], a[240:243], v[196:199], v[184:187]\n"
      "s_sub_u32 s58, s56, 110592\n"
      "v_fmac_f32 v157, v203, v203\n"
      "s_subb_u32 s59, s57, 0\n"
      "v_mfma_f32_16x16x32_bf16 v[184:187], a[252:255], v[208:211], v[184:187]\n"
      "s_sub_u32 s56, s56, 73728\n"
      "s_subb_u32 s57, s57, 0\n"
      "v_mfma_f32_16x16x32_bf16 v[212:215], a[244:247], v[196:199], v[212:215]\n"
      "s_mul_i32 s28, s8, 1373568\n"
      "v_permlane16_swap_b32 v159, v157\n"
      "v_mfma_f32_16x16x32_bf16 v[212:215], v[0:3], v[208:211], v[212:215]\n"
      "s_waitcnt lgkmcnt(1)\n"
      "v_add_f32_e64 v159, v159, v157\n"
      "v_mfma_f32_16x16x32_bf16 v[188:191], a[248:251], v[196:199], v[188:191]\n"
      "s_mul_i32 s29, s9, 2688\n"
      "s_add_u32 s28, s28, s29\n"
      "s_cmp_eq_u32 s8, 72\n"
      "v_mfma_f32_16x16x32_bf16 v[188:191], v[4:7], v[208:211], v[188:191]\n"
      "s_cselect_b32 s8, 0, 100270464\n"
      "s_cmp_eq_u32 s9, 72\n"
      "v_mfma_f32_16x16x32_bf16 v[166:169], v[140:143], v[66:69], 0\n"
      "s_waitcnt lgkmcnt(0)\n"
      "s_cselect_b32 s9, 0, 196224\n"
      "v_mfma_f32_16x16x32_bf16 v[166:169], v[216:219], v[66:69], v[166:169]\n"
      "s_add_u32 s10, s8, s9\n"
      "s_add_u32 s28, s28, s10\n"
      "v_mfma_f32_16x16x32_bf16 v[184:187], v[8:11], v[172:175], v[184:187]\n"
      "s_add_u32 s52, s16, s28\n"
      "s_addc_u32 s53, s17, 0\n"
      "s_add_u32 s27, s27, 1\n"
      "v_mfma_f32_16x16x32_bf16 v[184:187], v[20:23], v[136:139], v[184:187]\n"
      "s_cmp_ge_u32 s27, s26\n"
      "v_mov_b32_dpp v166, v166 quad_perm:[0,0,0,0] row_mask:0xf bank_mask:0xf\n"
      "v_rcp_f32_e64 v166, v166\n"
      "v_mfma_f32_16x16x32_bf16 v[212:215], v[12:15], v[172:175], v[212:215]\n"
      "s_cselect_b32 s28, 1, 0\n"
      "s_xor_b32 s29, s10, s11\n"
      "s_or_b32 s28, s28, s29\n"
      "v_mfma_f32_16x16x32_bf16 v[212:215], v[24:27], v[136:139], v[212:215]\n"
      "ds_read_b128 v[168:171], v114 offset:18432\n"
      "v_fma_f32 v228, v184, v184, s39\n"
      "v_mfma_f32_16x16x32_bf16 v[188:191], v[16:19], v[172:175], v[188:191]\n"
      "ds_read_b128 v[192:195], v115 offset:18432\n"
      "v_fmac_f32 v228, v185, v185\n"
      "v_mfma_f32_16x16x32_bf16 v[188:191], v[28:31], v[136:139], v[188:191]\n"
      "v_fmac_f32 v228, v186, v186\n"
      "v_fmac_f32 v228, v187, v187\n"
      "v_mfma_f32_16x16x32_bf16 v[136:139], v[140:143], v[220:223], 0\n"
      "v_fmac_f32 v228, v212, v212\n"
      "v_fmac_f32 v228, v213, v213\n"
      "v_mfma_f32_16x16x32_bf16 v[136:139], v[216:219], v[50:53], v[136:139]\n"
      "v_fmac_f32 v228, v214, v214\n"
      "v_fmac_f32 v228, v215, v215\n"
      "v_mfma_f32_16x16x32_bf16 v[196:199], v[140:143], v[236:239], 0\n"
      "v_fmac_f32 v228, v188, v188\n"
      "v_fmac_f32 v228, v189, v189\n"
      "v_mfma_f32_16x16x32_bf16 v[196:199], v[216:219], v[54:57], v[196:199]\n"
      "v_fmac_f32 v228, v190, v190\n"
      "v_fmac_f32 v228, v191, v191\n"
      "v_mfma_f32_16x16x32_bf16 v[236:239], v[140:143], v[252:255], 0\n"
      "v_mov_b32 v229, v228\n"
      "v_mfma_f32_16x16x32_bf16 v[236:239], v[216:219], v[58:61], v[236:239]\n"
      "s_nop 0\n"
      "v_permlane16_swap_b32 v228, v229\n"
      "v_add_f32 v228, v228, v229\n"
      "v_cndmask_b32 v136, v136, v196, s[42:43]\n"
      "s_waitcnt lgkmcnt(0)\n"
      "v_mfma_f32_16x16x32_bf16 v[196:199], a[0:3], v[168:171], 0\n"
      "v_permlane32_swap_b32_e64 v159, v228\n"
      "v_mfma_f32_16x16x32_bf16 v[196:199], a[36:39], v[192:195], v[196:199]\n"
      "v_add_f32_e64 v159, v159, v228\n"
      "v_cndmask_b32 v136, v136, v236, s[44:45]\n"
      "v_mfma_f32_16x16x32_bf16 v[228:231], a[4:7], v[168:171], 0\n"
      "v_mul_f32_e64 v136, v166, v136\n"
      "v_cvt_pk_bf16_f32 v136, v136, v136\n"
      "v_mfma_f32_16x16x32_bf16 v[228:231], a[40:43], v[192:195], v[228:231]\n"
      "ds_write_b32 v129, v159\n"
      "ds_write_b16 v135, v136\n"
      "s_branch L_join_30_%=\n"
      "L_flg_98_%=:\n"
      "v_cmp_gt_i32 vcc, 0, v176\n"
      "s_cbranch_vccz L_fb_99_%=\n"
      "s_sleep 1\n"
      "ds_read_b128 v[176:179], v128 offset:3072\n"
      "s_waitcnt lgkmcnt(0)\n"
      "v_bitop3_b32 v176, v176, v177, s4 bitop3:0x7e\n"
      "v_bitop3_b32 v178, v178, v179, s4 bitop3:0x7e\n"
      "v_or_b32 v176, v176, v178\n"
      "v_cmp_ne_u32 s[48:49], 0, v176\n"
      "s_branch L_flg_98_%=\n"
      "L_edge_1_%=:\n"
      "s_mov_b32 s11, s10\n"
      "s_sub_u32 s28, s60, s8\n"
      "s_sub_u32 s29, s61, s9\n"
      "s_mov_b32 s60, s8\n"
      "s_mov_b32 s61, s9\n"
      "s_mov_b64 exec, s[64:65]\n"
      "v_add_u32 v116, s28, v116\n"
      "s_mov_b64 exec, s[70:71]\n"
      "v_add_u32 v116, s29, v116\n"
      "s_mov_b64 exec, s[66:67]\n"
      "v_add_u32 v117, s28, v117\n"
      "s_mov_b64 exec, s[72:73]\n"
      "v_add_u32 v117, s29, v117\n"
      "s_mov_b64 exec, s[68:69]\n"
      "v_add_u32 v118, s28, v118\n"
      "s_mov_b64 exec, s[74:75]\n"
      "v_add_u32 v118, s29, v118\n"
      "s_mov_b64 exec, -1\n"
      "s_nop 4\n"
      "s_branch L_eb_2_%=\n"
      "L_flg_3_%=:\n"
      "v_cmp_gt_i32 vcc, 0, v168\n"
      "s_cbranch_vccz L_fb_4_%=\n"
      "s_sleep 1\n"
      "ds_read_b128 v[168:171], v128 offset:2048\n"
      "s_waitcnt lgkmcnt(0)\n"
      "v_bitop3_b32 v168, v168, v169, s4 bitop3:0x7e\n"
      "v_bitop3_b32 v170, v170, v171, s4 bitop3:0x7e\n"
      "v_or_b32 v168, v168, v170\n"
      "v_cmp_ne_u32 s[48:49], 0, v168\n"
      "s_branch L_flg_3_%=\n"
      "L_edge_6_%=:\n"
      "s_mov_b32 s11, s10\n"
      "s_sub_u32 s28, s60, s8\n"
      "s_sub_u32 s29, s61, s9\n"
      "s_mov_b32 s60, s8\n"
      "s_mov_b32 s61, s9\n"
      "s_mov_b64 exec, s[64:65]\n"
      "v_add_u32 v116, s28, v116\n"
      "s_mov_b64 exec, s[70:71]\n"
      "v_add_u32 v116, s29, v116\n"
      "s_mov_b64 exec, s[66:67]\n"
      "v_add_u32 v117, s28, v117\n"
      "s_mov_b64 exec, s[72:73]\n"
      "v_add_u32 v117, s29, v117\n"
      "s_mov_b64 exec, s[68:69]\n"
      "v_add_u32 v118, s28, v118\n"
      "s_mov_b64 exec, s[74:75]\n"
      "v_add_u32_e64 v118, s29, v118\n"
      "s_mov_b64 exec, -1\n"
      "s_nop 4\n"
      "s_branch L_eb_7_%=\n"
      "L_flg_12_%=:\n"
      "v_cmp_gt_i32 vcc, 0, v148\n"
      "s_cbranch_vccz L_fb_13_%=\n"
      "s_sleep 1\n"
      "ds_read_b128 v[148:151], v128 offset:3072\n"
      "s_waitcnt lgkmcnt(0)\n"
      "v_bitop3_b32 v148, v148, v149, s4 bitop3:0x7e\n"
      "v_bitop3_b32 v150, v150, v151, s4 bitop3:0x7e\n"
      "v_or_b32 v148, v148, v150\n"
      "v_cmp_ne_u32 s[48:49], 0, v148\n"
      "s_branch L_flg_12_%=\n"
      "L_edge_15_%=:\n"
      "s_mov_b32 s11, s10\n"
      "s_sub_u32 s28, s60, s8\n"
      "s_sub_u32 s29, s61, s9\n"
      "s_mov_b32 s60, s8\n"
      "s_mov_b32 s61, s9\n"
      "s_mov_b64 exec, s[64:65]\n"
      "v_add_u32 v116, s28, v116\n"
      "s_mov_b64 exec, s[70:71]\n"
      "v_add_u32 v116, s29, v116\n"
      "s_mov_b64 exec, s[66:67]\n"
      "v_add_u32 v117, s28, v117\n"
      "s_mov_b64 exec, s[72:73]\n"
      "v_add_u32 v117, s29, v117\n"
      "s_mov_b64 exec, s[68:69]\n"
      "v_add_u32 v118, s28, v118\n"
      "s_mov_b64 exec, s[74:75]\n"
      "v_add_u32 v118, s29, v118\n"
      "s_mov_b64 exec, -1\n"
      "s_nop 4\n"
      "s_branch L_eb_16_%=\n"
      "L_edge_17_%=:\n"
      "s_mov_b32 s11, s10\n"
      "s_sub_u32 s28, s60, s8\n"
      "s_sub_u32 s29, s61, s9\n"
      "s_mov_b32 s60, s8\n"
      "s_mov_b32 s61, s9\n"
      "s_mov_b64 exec, s[64:65]\n"
      "v_add_u32 v116, s28, v116\n"
      "s_mov_b64 exec, s[70:71]\n"
      "v_add_u32 v116, s29, v116\n"
      "s_mov_b64 exec, s[66:67]\n"
      "v_add_u32 v117, s28, v117\n"
      "s_mov_b64 exec, s[72:73]\n"
      "v_add_u32 v117, s29, v117\n"
      "s_mov_b64 exec, s[68:69]\n"
      "v_add_u32 v118, s28, v118\n"
      "s_mov_b64 exec, s[74:75]\n"
      "v_add_u32 v118, s29, v118\n"
      "s_mov_b64 exec, -1\n"
      "s_nop 4\n"
      "s_branch L_eb_18_%=\n"
      "L_flg_23_%=:\n"
      "v_cmp_gt_i32 vcc, 0, v196\n"
      "s_cbranch_vccz L_fb_24_%=\n"
      "s_sleep 1\n"
      "ds_read_b128 v[196:199], v128 offset:2048\n"
      "s_waitcnt lgkmcnt(0)\n"
      "v_bitop3_b32 v196, v196, v197, s4 bitop3:0x7e\n"
      "v_bitop3_b32 v198, v198, v199, s4 bitop3:0x7e\n"
      "v_or_b32 v196, v196, v198\n"
      "v_cmp_ne_u32 s[48:49], 0, v196\n"
      "s_branch L_flg_23_%=\n"
      "L_edge_26_%=:\n"
      "s_mov_b32 s11, s10\n"
      "s_sub_u32 s28, s60, s8\n"
      "s_sub_u32 s29, s61, s9\n"
      "s_mov_b32 s60, s8\n"
      "s_mov_b32 s61, s9\n"
      "s_mov_b64 exec, s[64:65]\n"
      "v_add_u32 v116, s28, v116\n"
      "s_mov_b64 exec, s[70:71]\n"
      "v_add_u32 v116, s29, v116\n"
      "s_mov_b64 exec, s[66:67]\n"
      "v_add_u32 v117, s28, v117\n"
      "s_mov_b64 exec, s[72:73]\n"
      "v_add_u32 v117, s29, v117\n"
      "s_mov_b64 exec, s[68:69]\n"
      "v_add_u32 v118, s28, v118\n"
      "s_mov_b64 exec, s[74:75]\n"
      "v_add_u32 v118, s29, v118\n"
      "s_mov_b64 exec, -1\n"
      "s_nop 4\n"
      "s_branch L_eb_27_%=\n"
      "L_flg_32_%=:\n"
      "v_cmp_gt_i32 vcc, 0, v172\n"
      "s_cbranch_vccz L_fb_33_%=\n"
      "s_sleep 1\n"
      "ds_read_b128 v[172:175], v128 offset:3072\n"
      "s_waitcnt lgkmcnt(0)\n"
      "v_bitop3_b32 v172, v172, v173, s4 bitop3:0x7e\n"
      "v_bitop3_b32 v174, v174, v175, s4 bitop3:0x7e\n"
      "v_or_b32 v172, v172, v174\n"
      "v_cmp_ne_u32 s[48:49], 0, v172\n"
      "s_branch L_flg_32_%=\n"
      "L_edge_36_%=:\n"
      "s_mov_b32 s11, s10\n"
      "s_sub_u32 s28, s60, s8\n"
      "s_sub_u32 s29, s61, s9\n"
      "s_mov_b32 s60, s8\n"
      "s_mov_b32 s61, s9\n"
      "s_mov_b64 exec, s[64:65]\n"
      "v_add_u32 v116, s28, v116\n"
      "s_mov_b64 exec, s[70:71]\n"
      "v_add_u32 v116, s29, v116\n"
      "s_mov_b64 exec, s[66:67]\n"
      "v_add_u32 v117, s28, v117\n"
      "s_mov_b64 exec, s[72:73]\n"
      "v_add_u32 v117, s29, v117\n"
      "s_mov_b64 exec, s[68:69]\n"
      "v_add_u32 v118, s28, v118\n"
      "s_mov_b64 exec, s[74:75]\n"
      "v_add_u32 v118, s29, v118\n"
      "s_mov_b64 exec, -1\n"
      "s_nop 4\n"
      "s_branch L_eb_37_%=\n"
      "L_edge_38_%=:\n"
      "s_mov_b32 s11, s10\n"
      "s_sub_u32 s28, s60, s8\n"
      "s_sub_u32 s29, s61, s9\n"
      "s_mov_b32 s60, s8\n"
      "s_mov_b32 s61, s9\n"
      "s_mov_b64 exec, s[64:65]\n"
      "v_add_u32 v116, s28, v116\n"
      "s_mov_b64 exec, s[70:71]\n"
      "v_add_u32 v116, s29, v116\n"
      "s_mov_b64 exec, s[66:67]\n"
      "v_add_u32 v117, s28, v117\n"
      "s_mov_b64 exec, s[72:73]\n"
      "v_add_u32 v117, s29, v117\n"
      "s_mov_b64 exec, s[68:69]\n"
      "v_add_u32 v118, s28, v118\n"
      "s_mov_b64 exec, s[74:75]\n"
      "v_add_u32 v118, s29, v118\n"
      "s_mov_b64 exec, -1\n"
      "s_nop 4\n"
      "s_branch L_eb_39_%=\n"
      "L_edge_40_%=:\n"
      "s_mov_b32 s11, s10\n"
      "s_sub_u32 s28, s60, s8\n"
      "s_sub_u32 s29, s61, s9\n"
      "s_mov_b32 s60, s8\n"
      "s_mov_b32 s61, s9\n"
      "s_mov_b64 exec, s[64:65]\n"
      "v_add_u32 v116, s28, v116\n"
      "s_mov_b64 exec, s[70:71]\n"
      "v_add_u32 v116, s29, v116\n"
      "s_mov_b64 exec, s[66:67]\n"
      "v_add_u32 v117, s28, v117\n"
      "s_mov_b64 exec, s[72:73]\n"
      "v_add_u32 v117, s29, v117\n"
      "s_mov_b64 exec, s[68:69]\n"
      "v_add_u32 v118, s28, v118\n"
      "s_mov_b64 exec, s[74:75]\n"
      "v_add_u32 v118, s29, v118\n"
      "s_mov_b64 exec, -1\n"
      "s_nop 4\n"
      "s_branch L_eb_41_%=\n"
      "L_edge_44_%=:\n"
      "s_mov_b32 s11, s10\n"
      "s_sub_u32 s28, s60, s8\n"
      "s_sub_u32 s29, s61, s9\n"
      "s_mov_b32 s60, s8\n"
      "s_mov_b32 s61, s9\n"
      "s_mov_b64 exec, s[64:65]\n"
      "v_add_u32 v116, s28, v116\n"
      "s_mov_b64 exec, s[70:71]\n"
      "v_add_u32 v116, s29, v116\n"
      "s_mov_b64 exec, s[66:67]\n"
      "v_add_u32 v117, s28, v117\n"
      "s_mov_b64 exec, s[72:73]\n"
      "v_add_u32 v117, s29, v117\n"
      "s_mov_b64 exec, s[68:69]\n"
      "v_add_u32 v118, s28, v118\n"
      "s_mov_b64 exec, s[74:75]\n"
      "v_add_u32 v118, s29, v118\n"
      "s_mov_b64 exec, -1\n"
      "s_nop 4\n"
      "s_branch L_eb_45_%=\n"
      "L_edge_46_%=:\n"
      "s_mov_b32 s11, s10\n"
      "s_sub_u32 s28, s60, s8\n"
      "s_sub_u32 s29, s61, s9\n"
      "s_mov_b32 s60, s8\n"
      "s_mov_b32 s61, s9\n"
      "s_mov_b64 exec, s[64:65]\n"
      "v_add_u32 v116, s28, v116\n"
      "s_mov_b64 exec, s[70:71]\n"
      "v_add_u32 v116, s29, v116\n"
      "s_mov_b64 exec, s[66:67]\n"
      "v_add_u32 v117, s28, v117\n"
      "s_mov_b64 exec, s[72:73]\n"
      "v_add_u32 v117, s29, v117\n"
      "s_mov_b64 exec, s[68:69]\n"
      "v_add_u32 v118, s28, v118\n"
      "s_mov_b64 exec, s[74:75]\n"
      "v_add_u32 v118, s29, v118\n"
      "s_mov_b64 exec, -1\n"
      "s_nop 4\n"
      "s_branch L_eb_47_%=\n"
      "L_edge_48_%=:\n"
      "s_mov_b32 s11, s10\n"
      "s_sub_u32 s28, s60, s8\n"
      "s_sub_u32 s29, s61, s9\n"
      "s_mov_b32 s60, s8\n"
      "s_mov_b32 s61, s9\n"
      "s_mov_b64 exec, s[64:65]\n"
      "v_add_u32 v116, s28, v116\n"
      "s_mov_b64 exec, s[70:71]\n"
      "v_add_u32 v116, s29, v116\n"
      "s_mov_b64 exec, s[66:67]\n"
      "v_add_u32 v117, s28, v117\n"
      "s_mov_b64 exec, s[72:73]\n"
      "v_add_u32 v117, s29, v117\n"
      "s_mov_b64 exec, s[68:69]\n"
      "v_add_u32 v118, s28, v118\n"
      "s_mov_b64 exec, s[74:75]\n"
      "v_add_u32 v118, s29, v118\n"
      "s_mov_b64 exec, -1\n"
      "s_nop 4\n"
      "s_branch L_eb_49_%=\n"
      "L_cend_%=:\n"
      :
      : "s"(__builtin_amdgcn_kernarg_segment_ptr()), "s"(wave), "s"(blockIdx.x), "s"(CT_TAB), "s"(lds0)
      : "v0","v1","v2","v3","v4","v5","v6","v7","v8","v9","v10","v11","v12","v13","v14","v15","v16","v17","v18","v19","v20","v21","v22","v23","v24","v25","v26","v27","v28","v29","v30","v31","v32","v33","v34","v35","v36","v37","v38","v39","v40","v41","v42","v43","v44","v45","v46","v47","v48","v49","v50","v51","v52","v53","v54","v55","v56","v57","v58","v59","v60","v61","v62","v63","v64","v65","v66","v67","v68","v69","v70","v71","v72","v73","v74","v75","v76","v77","v78","v79","v80","v81","v82","v83","v84","v85","v86","v87","v88","v89","v90","v91","v92","v93","v94","v95","v96","v97","v98","v99","v100","v101","v102","v103","v104","v105","v106","v107","v108","v109","v110","v111","v112","v113","v114","v115","v116","v117","v118","v119","v120","v121","v122","v123","v124","v125","v126","v127","v128","v129","v130","v131","v132","v133","v134","v135","v136","v137","v138","v139","v140","v141","v142","v143","v144","v145","v146","v147","v148","v149","v150","v151","v152","v153","v154","v155","v156","v157","v158","v159","v160","v161","v162","v163","v164","v165","v166","v167","v168","v169","v170","v171","v172","v173","v174","v175","v176","v177","v178","v179","v180","v181","v182","v183","v184","v185","v186","v187","v188","v189","v190","v191","v192","v193","v194","v195","v196","v197","v198","v199","v200","v201","v202","v203","v204","v205","v206","v207","v208","v209","v210","v211","v212","v213","v214","v215","v216","v217","v218","v219","v220","v221","v222","v223","v224","v225","v226","v227","v228","v229","v230","v231","v232","v233","v234","v235","v236","v237","v238","v239","v240","v241","v242","v243","v244","v245","v246","v247","v248","v249","v250","v251","v252","v253","v254","v255","a0","a1","a2","a3","a4","a5","a6","a7","a8","a9","a10","a11","a12","a13","a14","a15","a16","a17","a18","a19","a20","a21","a22","a23","a24","a25","a26","a27","a28","a29","a30","a31","a32","a33","a34","a35","a36","a37","a38","a39","a40","a41","a42","a43","a44","a45","a46","a47","a48","a49","a50","a51","a52","a53","a54","a55","a56","a57","a58","a59","a60","a61","a62","a63","a64","a65","a66","a67","a68","a69","a70","a71","a72","a73","a74","a75","a76","a77","a78","a79","a80","a81","a82","a83","a84","a85","a86","a87","a88","a89","a90","a91","a92","a93","a94","a95","a96","a97","a98","a99","a100","a101","a102","a103","a104","a105","a106","a107","a108","a109","a110","a111","a112","a113","a114","a115","a116","a117","a118","a119","a120","a121","a122","a123","a124","a125","a126","a127","a128","a129","a130","a131","a132","a133","a134","a135","a136","a137","a138","a139","a140","a141","a142","a143","a144","a145","a146","a147","a148","a149","a150","a151","a152","a153","a154","a155","a156","a157","a158","a159","a160","a161","a162","a163","a164","a165","a166","a167","a168","a169","a170","a171","a172","a173","a174","a175","a176","a177","a178","a179","a180","a181","a182","a183","a184","a185","a186","a187","a188","a189","a190","a191","a192","a193","a194","a195","a196","a197","a198","a199","a200","a201","a202","a203","a204","a205","a206","a207","a208","a209","a210","a211","a212","a213","a214","a215","a216","a217","a218","a219","a220","a221","a222","a223","a224","a225","a226","a227","a228","a229","a230","a231","a232","a233","a234","a235","a236","a237","a238","a239","a240","a241","a242","a243","a244","a245","a246","a247","a248","a249","a250","a251","a252","a253","a254","a255","s2","s3","s4","s5","s6","s7","s8","s9","s10","s11","s12","s13","s16","s17","s18","s19","s20","s21","s22","s23","s24","s25","s26","s27","s28","s29","s30","s31","s34","s35","s36","s37","s38","s39","s40","s41","s42","s43","s44","s45","s46","s47","s48","s49","s50","s51","s52","s53","s54","s55","s60","s61","s62","s63","s64","s65","s66","s67","s68","s69","s70","s71","s72","s73","s74","s75","s76","s77","s78","s79","s80","s81","s82","s83","s84","s85","s86","s87","s88","s89","s90","s91","s92","s93","s94","s95","s96","s97","s98","s99","s100","s101", "vcc", "scc", "memory");
}

}

extern "C" void kernel_launch(void* const* d_in, const int* in_sizes, int n_in, void* d_out, int out_size, void* d_ws,
                              size_t ws_size, hipStream_t stream) {
  const uint16_t* x = (const uint16_t*)d_in[0];
  const uint16_t* wqkv = (const uint16_t*)d_in[1];
  const uint16_t* wo = (const uint16_t*)d_in[2];
  const uint16_t* ls = (const uint16_t*)d_in[3];
  const uint16_t* w1 = (const uint16_t*)d_in[4];
  const uint16_t* w2 = (const uint16_t*)d_in[5];
  const uint16_t* rc = (const uint16_t*)d_in[6];
  const int* ridx = (const int*)d_in[7];
  const uint16_t* mask = (const uint16_t*)d_in[8];
  swin::swin_main<<<256, 256, 0, stream>>>(x, mask, (char*)d_ws, (uint16_t*)d_out, wqkv, wo, ls, w1, w2, rc, ridx);
}
```

```cpp
#include <hip/hip_runtime.h>
#include <hip/hip_bf16.h>
#include <stdint.h>

namespace swin {

constexpr unsigned LDS_BYTES = 163840u;

__device__ __attribute__((aligned(256))) const unsigned CT_TAB[26704] = {0,64,0,0xc0000,144,208,16,0xc0010,288,352,32,0xc0020,432,496,48,0xc0030,576,512,64,0xc0040,720,656,80,0xc0050,864,800,96,0xc0060,1008,944,112,0xc0070,1024,1088,400,0xc0190,1168,1232,384,0xc0180,1312,1376,432,0xc01b0,1456,1520,416,0xc01a0,1600,1536,464,0xc01d0,1744,1680,448,0xc01c0,1888,1824,496,0xc01f0,2032,1968,480,0xc01e0,16,80,800,0xc0320,128,192,816,0xc0330,304,368,768,0xc0300,416,480,784,0xc0310,592,528,864,0xc0360,704,640,880,0xc0370,880,816,832,0xc0340,992,928,848,0xc0350,1040,1104,1200,0xef8b0,1152,1216,1184,0xef8a0,1328,1392,1168,0xef890,1440,1504,1152,0xef880,1616,1552,1264,0xef8f0,1728,1664,1248,0xef8e0,1904,1840,1232,0xef8d0,2016,1952,1216,0xef8c0,32,96,1600,0xefa40,176,240,1616,0xefa50,256,320,1632,0xefa60,400,464,1648,0xefa70,608,544,1536,0xefa00,752,688,1552,0xefa10,832,768,1568,0xefa20,976,912,1584,0xefa30,1056,1120,2000,0xefbd0,1200,1264,1984,0xefbc0,1280,1344,2032,0xefbf0,1424,1488,2016,0xefbe0,1632,1568,1936,0xefb90,1776,1712,1920,0xefb80,1856,1792,1968,0xefbb0,2000,1936,1952,0xefba0,48,112,2400,0xefd60,160,224,2416,0xefd70,272,336,2368,0xefd40,384,448,2384,0xefd50,624,560,2336,0xefd20,736,672,2352,0xefd30,848,784,2304,0xefd00,960,896,2320,0xefd10,1072,1136,0x2fef0,0xefef0,1184,1248,0x2fee0,0xefee0,1296,1360,0x2fed0,0xefed0,1408,1472,0x2fec0,0xefec0,1648,1584,0x2feb0,0xefeb0,1760,1696,0x2fea0,0xefea0,1872,1808,0x2fe90,0xefe90,1984,1920,0x2fe80,0xefe80,0xc0100,73728,77824,90112,0xc0110,73744,77840,90128,0xc0120,73760,77856,90144,0xc0130,73776,77872,90160,0xc0140,73792,77888,90176,0xc0150,73808,77904,90192,0xc0160,73824,77920,90208,0xc0170,73840,77936,90224,0xc0290,73856,77952,90240,0xc0280,73872,77968,90256,0xc02b0,73888,77984,90272,0xc02a0,73904,78000,90288,0xc02d0,73920,78016,90304,0xc02c0,73936,78032,90320,0xc02f0,73952,78048,90336,0xc02e0,73968,78064,90352,0xc0420,73984,78080,90368,0xc0430,74000,78096,90384,0xc0400,74016,78112,90400,0xc0410,74032,78128,90416,0xc0460,74048,78144,90432,0xc0470,74064,78160,90448,0xc0440,74080,78176,90464,0xc0450,74096,78192,90480,0xef9b0,74112,78208,90496,0xef9a0,74128,78224,90512,0xef990,74144,78240,90528,0xef980,74160,78256,90544,0xef9f0,74176,78272,90560,0xef9e0,74192,78288,90576,0xef9d0,74208,78304,90592,0xef9c0,74224,78320,90608,0xc0100,74240,78336,90624,0xc0110,74256,78352,90640,0xc0120,74272,78368,90656,0xc0130,74288,78384,90672,0xc0140,74304,78400,90688,0xc0150,74320,78416,90704,0xc0160,74336,78432,90720,0xc0170,74352,78448,90736,0xc0290,74368,78464,90752,0xc0280,74384,78480,90768,0xc02b0,74400,78496,90784,0xc02a0,74416,78512,90800,0xc02d0,74432,78528,90816,0xc02c0,74448,78544,90832,0xc02f0,74464,78560,90848,0xc02e0,74480,78576,90864,0xc0420,74496,78592,90880,0xc0430,74512,78608,90896,0xc0400,74528,78624,90912,0xc0410,74544,78640,90928,0xc0460,74560,78656,90944,0xc0470,74576,78672,90960,0xc0440,74592,78688,90976,0xc0450,74608,78704,90992,0xef9b0,74624,78720,91008,0xef9a0,74640,78736,91024,0xef990,74656,78752,91040,0xef980,74672,78768,91056,0xef9f0,74688,78784,91072,0xef9e0,74704,78800,91088,0xef9d0,74720,78816,91104,0xef9c0,74736,78832,91120,0x1c000,36864,36864,49152,0x1c004,36880,36880,49168,0x1c008,36896,36896,49184,0x1c00c,36912,36912,49200,0x1c010,36928,36928,49216,0x1c014,36944,36944,49232,0x1c018,36960,36960,49248,0x1c01c,36976,36976,49264,0x1c020,36992,36992,49280,0x1c024,37008,37008,49296,0x1c028,37024,37024,49312,0x1c02c,37040,37040,49328,0x1c030,37056,37056,49344,0x1c034,37072,37072,49360,0x1c038,37088,37088,49376,0x1c03c,37104,37104,49392,0x1c040,37120,37120,49408,0x1c044,37136,37136,49424,0x1c048,37152,37152,49440,0x1c04c,37168,37168,49456,0x1c050,37184,37184,49472,0x1c054,37200,37200,49488,0x1c058,37216,37216,49504,0x1c05c,37232,37232,49520,0x1c060,37248,37248,49536,0x1c064,37264,37264,49552,0x1c068,37280,37280,49568,0x1c06c,37296,37296,49584,0x1c070,37312,37312,49600,0x1c074,37328,37328,49616,0x1c078,37344,37344,49632,0x1c07c,37360,37360,49648,0x1c080,37376,37376,49664,0x1c084,37392,37392,49680,0x1c088,37408,37408,49696,0x1c08c,37424,37424,49712,0x1c090,37440,37440,49728,0x1c094,37456,37456,49744,0x1c098,37472,37472,49760,0x1c09c,37488,37488,49776,0x1c0a0,37504,37504,49792,0x1c0a4,37520,37520,49808,0x1c0a8,37536,37536,49824,0x1c0ac,37552,37552,49840,0x1c0b0,37568,37568,49856,0x1c0b4,37584,37584,49872,0x1c0b8,37600,37600,49888,0x1c0bc,37616,37616,49904,0x1c0c0,37632,37632,49920,0x1c0c4,37648,37648,49936,0x1c0c8,37664,37664,49952,0x1c0cc,37680,37680,49968,0x1c0d0,37696,37696,49984,0x1c0d4,37712,37712,50000,0x1c0d8,37728,37728,50016,0x1c0dc,37744,37744,50032,0x1c0e0,37760,37760,50048,0x1c0e4,37776,37776,50064,0x1c0e8,37792,37792,50080,0x1c0ec,37808,37808,50096,0x1c0f0,37824,37824,50112,0x1c0f4,37840,37840,50128,0x1c0f8,37856,37856,50144,0x1c0fc,37872,37872,50160,73728,73792,0x1d600,0x1d600,73872,73936,0x1d610,0x1d610,74016,74080,0x1d620,0x1d620,74160,74224,0x1d630,0x1d630,74304,74240,0x1d640,0x1d640,74448,74384,0x1d650,0x1d650,74592,74528,0x1d660,0x1d660,74736,74672,0x1d670,0x1d670,74752,74816,0x1d680,0x1d680,74896,74960,0x1d690,0x1d690,75040,75104,0x1d6a0,0x1d6a0,75184,75248,0x1d6b0,0x1d6b0,75328,75264,0x1d6c0,0x1d6c0,75472,75408,0x1d6d0,0x1d6d0,75616,75552,0x1d6e0,0x1d6e0,75760,75696,0x1d6f0,0x1d6f0,73744,73800,0x1d700,0x1d700,73856,73944,0x1d710,0x1d710,74032,74088,0x1d720,0x1d720,74144,74232,0x1d730,0x1d730,74320,74248,0x1d740,0x1d740,74432,74392,0x1d750,0x1d750,74608,74536,0x1d760,0x1d760,74720,74680,0x1d770,0x1d770,74768,74824,0x1d780,0x1d780,74880,74968,0x1d790,0x1d790,75056,75112,0x1d7a0,0x1d7a0,75168,75256,0x1d7b0,0x1d7b0,75344,75272,0x1d7c0,0x1d7c0,75456,75416,0x1d7d0,0x1d7d0,75632,75560,0x1d7e0,0x1d7e0,75744,75704,0x1d7f0,0x1d7f0,73760,73808,0x1d800,0x1d800,73904,73920,0x1d810,0x1d810,73984,74096,0x1d820,0x1d820,74128,74208,0x1d830,0x1d830,74336,74256,0x1d840,0x1d840,74480,74368,0x1d850,0x1d850,74560,74544,0x1d860,0x1d860,74704,74656,0x1d870,0x1d870,74784,74832,0x1d880,0x1d880,74928,74944,0x1d890,0x1d890,75008,75120,0x1d8a0,0x1d8a0,75152,75232,0x1d8b0,0x1d8b0,75360,75280,0x1d8c0,0x1d8c0,75504,75392,0x1d8d0,0x1d8d0,75584,75568,0x1d8e0,0x1d8e0,75728,75680,0x1d8f0,0x1d8f0,73776,73816,0x1d900,0x1d900,73888,73928,0x1d910,0x1d910,74000,74104,0x1d920,0x1d920,74112,74216,0x1d930,0x1d930,74352,74264,0x1d940,0x1d940,74464,74376,0x1d950,0x1d950,74576,74552,0x1d960,0x1d960,74688,74664,0x1d970,0x1d970,74800,74840,0x1d980,0x1d980,74912,74952,0x1d990,0x1d990,75024,75128,0x1d9a0,0x1d9a0,75136,75240,0x1d9b0,0x1d9b0,75376,75288,0x1d9c0,0x1d9c0,75488,75400,0x1d9d0,0x1d9d0,75600,75576,0x1d9e0,0x1d9e0,75712,75688,0x1d9f0,0x1d9f0,0x1e600,0x1e600,0x1e800,0x100000,0x1e602,0x100000,0x100000,0x1e800,0x1e604,0x100000,0x100000,0x100000,0x1e606,0x100000,0x100000,0x100000,0x1e610,0x1e600,0x100000,0x100000,0x1e612,0x100000,0x100000,0x1e800,0x1e614,0x100000,0x100000,0x100000,0x1e616,0x100000,0x100000,0x100000,0x1e620,0x1e600,0x100000,0x100000,0x1e622,0x100000,0x100000,0x1e800,0x1e624,0x100000,0x100000,0x100000,0x1e626,0x100000,0x100000,0x100000,0x1e630,0x1e600,0x100000,0x100000,0x1e632,0x100000,0x100000,0x1e800,0x1e634,0x100000,0x100000,0x100000,0x1e636,0x100000,0x100000,0x100000,0x1e608,0x1e610,0x1e830,0x100000,0x1e60a,0x100000,0x100000,0x1e830,0x1e60c,0x100000,0x100000,0x100000,0x1e60e,0x100000,0x100000,0x100000,0x1e618,0x1e610,0x100000,0x100000,0x1e61a,0x100000,0x100000,0x1e830,0x1e61c,0x100000,0x100000,0x100000,0x1e61e,0x100000,0x100000,0x100000,0x1e628,0x1e610,0x100000,0x100000,0x1e62a,0x100000,0x100000,0x1e830,0x1e62c,0x100000,0x100000,0x100000,0x1e62e,0x100000,0x100000,0x100000,0x1e638,0x1e610,0x100000,0x100000,0x1e63a,0x100000,0x100000,0x1e830,0x1e63c,0x100000,0x100000,0x100000,0x1e63e,0x100000,0x100000,0x100000,0x1e640,0x1e620,0x1e860,0x100000,0x1e642,0x100000,0x100000,0x1e860,0x1e644,0x100000,0x100000,0x100000,0x1e646,0x100000,0x100000,0x100000,0x1e650,0x1e620,0x100000,0x100000,0x1e652,0x100000,0x100000,0x1e860,0x1e654,0x100000,0x100000,0x100000,0x1e656,0x100000,0x100000,0x100000,0x1e660,0x1e620,0x100000,0x100000,0x1e662,0x100000,0x100000,0x1e860,0x1e664,0x100000,0x100000,0x100000,0x1e666,0x100000,0x100000,0x100000,0x1e670,0x1e620,0x100000,0x100000,0x1e672,0x100000,0x100000,0x1e860,0x1e674,0x100000,0x100000,0x100000,0x1e676,0x100000,0x100000,0x100000,0x1e648,0x1e630,0x1e890,0x100000,0x1e64a,0x100000,0x100000,0x1e890,0x1e64c,0x100000,0x100000,0x100000,0x1e64e,0x100000,0x100000,0x100000,0x1e658,0x1e630,0x100000,0x100000,0x1e65a,0x100000,0x100000,0x1e890,0x1e65c,0x100000,0x100000,0x100000,0x1e65e,0x100000,0x100000,0x100000,0x1e668,0x1e630,0x100000,0x100000,0x1e66a,0x100000,0x100000,0x1e890,0x1e66c,0x100000,0x100000,0x100000,0x1e66e,0x100000,0x100000,0x100000,0x1e678,0x1e630,0x100000,0x100000,0x1e67a,0x100000,0x100000,0x1e890,0x1e67c,0x100000,0x100000,0x100000,0x1e67e,0x100000,0x100000,0x100000,0x1eb00,0x24c70,36864,36928,0x1eb02,0x24c72,36880,36936,0x1eb04,0x24c74,36896,36944,0x1eb06,0x24c76,36912,36952,0x1eb08,0x24dd0,37248,37312,0x1eb0a,0x24dd2,37264,37320,0x1eb0c,0x24dd4,37280,37328,0x1eb0e,0x24dd6,37296,37336,0x1eb10,0x24f30,37664,37728,0x1eb12,0x24f32,37680,37736,0x1eb14,0x24f34,37632,37744,0x1eb16,0x24f36,37648,37752,0x1eb18,0x25090,38048,38112,0x1eb1a,0x25092,38064,38120,0x1eb1c,0x25094,38016,38128,0x1eb1e,0x25096,38032,38136,0x1eb00,0x24c78,38464,38400,0x1eb02,0x24c7a,38480,38408,0x1eb04,0x24c7c,38496,38416,0x1eb06,0x24c7e,38512,38424,0x1eb08,0x24dd8,38848,38784,0x1eb0a,0x24dda,38864,38792,0x1eb0c,0x24ddc,38880,38800,0x1eb0e,0x24dde,38896,38808,0x1eb10,0x24f38,39264,39200,0x1eb12,0x24f3a,39280,39208,0x1eb14,0x24f3c,39232,39216,0x1eb16,0x24f3e,39248,39224,0x1eb18,0x25098,39648,39584,0x1eb1a,0x2509a,39664,39592,0x1eb1c,0x2509c,39616,39600,0x1eb1e,0x2509e,39632,39608,0x1eb00,0x26270,39936,40000,0x1eb02,0x26272,39952,40008,0x1eb04,0x26274,39968,40016,0x1eb06,0x26276,39984,40024,0x1eb08,0x263d0,40320,40384,0x1eb0a,0x263d2,40336,40392,0x1eb0c,0x263d4,40352,40400,0x1eb0e,0x263d6,40368,40408,0x1eb10,0x26530,40736,40800,0x1eb12,0x26532,40752,40808,0x1eb14,0x26534,40704,40816,0x1eb16,0x26536,40720,40824,0x1eb18,0x26690,41120,41184,0x1eb1a,0x26692,41136,41192,0x1eb1c,0x26694,41088,41200,0x1eb1e,0x26696,41104,41208,0x1eb00,0x100000,41536,41472,0x1eb02,0x100000,41552,41480,0x1eb04,0x100000,41568,41488,0x1eb06,0x100000,41584,41496,0x1eb08,0x100000,41920,41856,0x1eb0a,0x100000,41936,41864,0x1eb0c,0x100000,41952,41872,0x1eb0e,0x100000,41968,41880,0x1eb10,0x100000,42336,42272,0x1eb12,0x100000,42352,42280,0x1eb14,0x100000,42304,42288,0x1eb16,0x100000,42320,42296,0x1eb18,0x100000,42720,42656,0x1eb1a,0x100000,42736,42664,0x1eb1c,0x100000,42688,42672,0x1eb1e,0x100000,42704,42680,36864,36896,36928,36864,36872,36904,36936,36880,36880,36912,36944,36896,36888,36920,36952,36912,38016,38048,38080,36928,38024,38056,38088,36944,38032,38064,38096,36960,38040,38072,38104,36976,39200,39168,39264,36992,39208,39176,39272,37008,39216,39184,39280,37024,39224,39192,39288,37040,40352,40320,40416,37056,40360,40328,40424,37072,40368,40336,40432,37088,40376,40344,40440,37104,46144,46176,46080,37120,46152,46184,46088,37136,46160,46192,46096,37152,46168,46200,46104,37168,47296,47328,47232,37184,47304,47336,47240,37200,47312,47344,47248,37216,47320,47352,47256,37232,48480,48448,48416,37248,48488,48456,48424,37264,48496,48464,48432,37280,48504,48472,48440,37296,49632,49600,49568,37312,49640,49608,49576,37328,49648,49616,49584,37344,49656,49624,49592,37360,55296,55328,55360,37376,55304,55336,55368,37392,55312,55344,55376,37408,55320,55352,55384,37424,56448,56480,56512,37440,56456,56488,56520,37456,56464,56496,56528,37472,56472,56504,56536,37488,57632,57600,57696,37504,57640,57608,57704,37520,57648,57616,57712,37536,57656,57624,57720,37552,58784,58752,58848,37568,58792,58760,58856,37584,58800,58768,58864,37600,58808,58776,58872,37616,64576,64608,64512,37632,64584,64616,64520,37648,64592,64624,64528,37664,64600,64632,64536,37680,65728,65760,65664,37696,65736,65768,65672,37712,65744,65776,65680,37728,65752,65784,65688,37744,66912,66880,66848,37760,66920,66888,66856,37776,66928,66896,66864,37792,66936,66904,66872,37808,68064,68032,68000,37824,68072,68040,68008,37840,68080,68048,68016,37856,68088,68056,68024,37872,0x1e800,0x1e800,0x20cc0,0,0x1e8c4,0x1e8c4,0x20cc4,16,0x1e988,0x1e988,0x20cc8,32,0x1ea4c,0x1ea4c,0x20ccc,48,0x1eb10,0x1eb10,0x20cd0,64,0x1ebd4,0x1ebd4,0x20cd4,80,0x1ec98,0x1ec98,0x20cd8,96,0x1ed5c,0x1ed5c,0x20cdc,112,0x1ee20,0x1ee20,0x20ce0,384,0x1eee4,0x1eee4,0x20ce4,400,0x1efa8,0x1efa8,0x20ce8,416,0x1f06c,0x1f06c,0x20cec,432,0x1f130,0x1f130,0x20cf0,448,0x1f1f4,0x1f1f4,0x20cf4,464,0x1f2b8,0x1f2b8,0x20cf8,480,0x1f37c,0x1f37c,0x20cfc,496,0x1e810,0x1e800,0x20cc0,768,0x1e8d4,0x1e8c4,0x20cc4,784,0x1e998,0x1e988,0x20cc8,800,0x1ea5c,0x1ea4c,0x20ccc,816,0x1eb20,0x1eb10,0x20cd0,832,0x1ebe4,0x1ebd4,0x20cd4,848,0x1eca8,0x1ec98,0x20cd8,864,0x1ed6c,0x1ed5c,0x20cdc,880,0x1ee30,0x1ee20,0x20ce0,1152,0x1eef4,0x1eee4,0x20ce4,1168,0x1efb8,0x1efa8,0x20ce8,1184,0x1f07c,0x1f06c,0x20cec,1200,0x1f140,0x1f130,0x20cf0,1216,0x1f204,0x1f1f4,0x20cf4,1232,0x1f2c8,0x1f2b8,0x20cf8,1248,0x1f38c,0x1f37c,0x20cfc,1264,0x1e820,0x1e800,0x20cc0,1536,0x1e8e4,0x1e8c4,0x20cc4,1552,0x1e9a8,0x1e988,0x20cc8,1568,0x1ea6c,0x1ea4c,0x20ccc,1584,0x1eb30,0x1eb10,0x20cd0,1600,0x1ebf4,0x1ebd4,0x20cd4,1616,0x1ecb8,0x1ec98,0x20cd8,1632,0x1ed7c,0x1ed5c,0x20cdc,1648,0x1ee40,0x1ee20,0x20ce0,1920,0x1ef04,0x1eee4,0x20ce4,1936,0x1efc8,0x1efa8,0x20ce8,1952,0x1f08c,0x1f06c,0x20cec,1968,0x1f150,0x1f130,0x20cf0,1984,0x1f214,0x1f1f4,0x20cf4,2000,0x1f2d8,0x1f2b8,0x20cf8,2016,0x1f39c,0x1f37c,0x20cfc,2032,0x1e830,0x1e800,0x20cc0,2304,0x1e8f4,0x1e8c4,0x20cc4,2320,0x1e9b8,0x1e988,0x20cc8,2336,0x1ea7c,0x1ea4c,0x20ccc,2352,0x1eb40,0x1eb10,0x20cd0,2368,0x1ec04,0x1ebd4,0x20cd4,2384,0x1ecc8,0x1ec98,0x20cd8,2400,0x1ed8c,0x1ed5c,0x20cdc,2416,0x1ee50,0x1ee20,0x20ce0,2688,0x1ef14,0x1eee4,0x20ce4,2704,0x1efd8,0x1efa8,0x20ce8,2720,0x1f09c,0x1f06c,0x20cec,2736,0x1f160,0x1f130,0x20cf0,2752,0x1f224,0x1f1f4,0x20cf4,2768,0x1f2e8,0x1f2b8,0x20cf8,2784,0x1f3ac,0x1f37c,0x20cfc,2800,36864,0x26d70,0x1e800,0x1eb00,36880,0x26d74,0x1ec90,0x1ef90,36896,0x26d78,0x1f120,0x1f420,36912,0x26d7c,0x1f5b0,0x1f8b0,36928,0x26d80,0x1fa40,0x1fd40,36944,0x26d84,0x1fed0,0x201d0,36960,0x26d88,0x20360,0x20660,36976,0x26d8c,0x207f0,0x20af0,37248,0x26d90,0x20c80,0x20f80,37264,0x26d94,0x21110,0x21410,37280,0x26d98,0x215a0,0x218a0,37296,0x26d9c,0x21a30,0x21d30,37312,0x26da0,0x21ec0,0x221c0,37328,0x26da4,0x22350,0x22650,37344,0x26da8,0x227e0,0x22ae0,37360,0x26dac,0x22c70,0x22f70,37664,0x26db0,0x1e830,0x1eb08,37680,0x26db4,0x1ecc0,0x1ef98,37632,0x26db8,0x1f150,0x1f428,37648,0x26dbc,0x1f5e0,0x1f8b8,37728,0x26dc0,0x1fa70,0x1fd48,37744,0x26dc4,0x1ff00,0x201d8,37696,0x26dc8,0x20390,0x20668,37712,0x26dcc,0x20820,0x20af8,38048,0x26dd0,0x20cb0,0x20f88,38064,0x26dd4,0x21140,0x21418,38016,0x26dd8,0x215d0,0x218a8,38032,0x26ddc,0x21a60,0x21d38,38112,0x26de0,0x21ef0,0x221c8,38128,0x26de4,0x22380,0x22658,38080,0x26de8,0x22810,0x22ae8,38096,0x26dec,0x22ca0,0x22f78,38464,0x26df0,0x1e860,0x1eb10,38480,0x26df4,0x1ecf0,0x1efa0,38496,0x26df8,0x1f180,0x1f430,38512,0x26dfc,0x1f610,0x1f8c0,38400,0x26e00,0x1faa0,0x1fd50,38416,0x26e04,0x1ff30,0x201e0,38432,0x26e08,0x203c0,0x20670,38448,0x26e0c,0x20850,0x20b00,38848,0x26e10,0x20ce0,0x20f90,38864,0x26e14,0x21170,0x21420,38880,0x26e18,0x21600,0x218b0,38896,0x26e1c,0x21a90,0x21d40,38784,0x26e20,0x21f20,0x221d0,38800,0x26e24,0x223b0,0x22660,38816,0x26e28,0x22840,0x22af0,38832,0x26e2c,0x22cd0,0x22f80,39264,0x26e30,0x1e890,0x1eb18,39280,0x26e34,0x1ed20,0x1efa8,39232,0x26e38,0x1f1b0,0x1f438,39248,0x26e3c,0x1f640,0x1f8c8,39200,0x26e40,0x1fad0,0x1fd58,39216,0x26e44,0x1ff60,0x201e8,39168,0x26e48,0x203f0,0x20678,39184,0x26e4c,0x20880,0x20b08,39648,0x26e50,0x20d10,0x20f98,39664,0x26e54,0x211a0,0x21428,39616,0x26e58,0x21630,0x218b8,39632,0x26e5c,0x21ac0,0x21d48,39584,0x26e60,0x21f50,0x221d8,39600,0x26e64,0x223e0,0x22668,39552,0x26e68,0x22870,0x22af8,39568,0x26e6c,0x22d00,0x22f88,0x3f803f80,0x3f803f80,16256,0,0,0x3f803f80,0x3f800000,0,0,0x3f803f80,0,16256,0,0x3f803f80,0,0x3f800000,0x3f803f80,0,0,0,0,0,0,0,0,0,0,0,0,0,0,0,0x3f803f80,0,0,0,0,0,0,0,0,0,0,0,0,0,0,0,0x3f803f80,0,0,0,0,0,0,0,0,0,0,0,0,0,0,0,0x3f803f80,0,0,0,0,0,0,0,0,0,0,0,0,0,0,0,0x3f803f80,0x3f803f80,16256,0,0,0x3f803f80,0x3f800000,0,0,0x3f803f80,0,16256,0,0x3f803f80,0,0x3f800000,0x3f803f80,0,0,0,0,0,0,0,0,0,0,0,0,0,0,0,0x3f803f80,0,0,0,0,0,0,0,0,0,0,0,0,0,0,0,0x3f803f80,0,0,0,0,0,0,0,0,0,0,0,0,0,0,0,0x3f803f80,0,0,0,0,0,0,0,0,0,0,0,0,0,0,0,0x3f803f80,0x3f803f80,16256,0,0,0x3f803f80,0x3f800000,0,0,0x3f803f80,0,16256,0,0x3f803f80,0,0x3f800000,0x3f803f80,0,0,0,0,0,0,0,0,0,0,0,0,0,0,0,0x3f803f80,0,0,0,0,0,0,0,0,0,0,0,0,0,0,0,0x3f803f80,0,0,0,0,0,0,0,0,0,0,0,0,0,0,0,0x3f803f80,0,0,0,0,0,0,0,0,0,0,0,0,0,0,0,0x3f803f80,0x3f803f80,16256,0,0,0x3f803f80,0x3f800000,0,0,0x3f803f80,0,16256,0,0x3f803f80,0,0x3f800000,0,64,0x30000,0xf0000,144,208,0x30010,0xf0010,288,352,0x30020,0xf0020,432,496,0x30030,0xf0030,576,512,0x30040,0xf0040,720,656,0x30050,0xf0050,864,800,0x30060,0xf0060,1008,944,0x30070,0xf0070,1024,1088,0x30190,0xf0190,1168,1232,0x30180,0xf0180,1312,1376,0x301b0,0xf01b0,1456,1520,0x301a0,0xf01a0,1600,1536,0x301d0,0xf01d0,1744,1680,0x301c0,0xf01c0,1888,1824,0x301f0,0xf01f0,2032,1968,0x301e0,0xf01e0,16,80,0x30320,0x11f720,128,192,0x30330,0x11f730,304,368,0x30300,0x11f700,416,480,0x30310,0x11f710,592,528,0x30360,0x11f760,704,640,0x30370,0x11f770,880,816,0x30340,0x11f740,992,928,0x30350,0x11f750,1040,1104,0x304b0,0x11f8b0,1152,1216,0x304a0,0x11f8a0,1328,1392,0x30490,0x11f890,1440,1504,0x30480,0x11f880,1616,1552,0x304f0,0x11f8f0,1728,1664,0x304e0,0x11f8e0,1904,1840,0x304d0,0x11f8d0,2016,1952,0x304c0,0x11f8c0,32,96,0x30640,0x11fa40,176,240,0x30650,0x11fa50,256,320,0x30660,0x11fa60,400,464,0x30670,0x11fa70,608,544,0x30600,0x11fa00,752,688,0x30610,0x11fa10,832,768,0x30620,0x11fa20,976,912,0x30630,0x11fa30,1056,1120,0x307d0,0x11fbd0,1200,1264,0x307c0,0x11fbc0,1280,1344,0x307f0,0x11fbf0,1424,1488,0x307e0,0x11fbe0,1632,1568,0x30790,0x11fb90,1776,1712,0x30780,0x11fb80,1856,1792,0x307b0,0x11fbb0,2000,1936,0x307a0,0x11fba0,48,112,0x5fd60,0x11fd60,160,224,0x5fd70,0x11fd70,272,336,0x5fd40,0x11fd40,384,448,0x5fd50,0x11fd50,624,560,0x5fd20,0x11fd20,736,672,0x5fd30,0x11fd30,848,784,0x5fd00,0x11fd00,960,896,0x5fd10,0x11fd10,1072,1136,0x5fef0,0x11fef0,1184,1248,0x5fee0,0x11fee0,1296,1360,0x5fed0,0x11fed0,1408,1472,0x5fec0,0x11fec0,1648,1584,0x5feb0,0x11feb0,1760,1696,0x5fea0,0x11fea0,1872,1808,0x5fe90,0x11fe90,1984,1920,0x5fe80,0x11fe80,0xefb40,74752,78848,90624,0xefb50,74768,78864,90640,0xefb60,74784,78880,90656,0xefb70,74800,78896,90672,0xefb00,74816,78912,90688,0xefb10,74832,78928,90704,0xefb20,74848,78944,90720,0xefb30,74864,78960,90736,0xefcd0,74880,78976,90752,0xefcc0,74896,78992,90768,0xefcf0,74912,79008,90784,0xefce0,74928,79024,90800,0xefc90,74944,79040,90816,0xefc80,74960,79056,90832,0xefcb0,74976,79072,90848,0xefca0,74992,79088,90864,0xefe60,75008,79104,90880,0xefe70,75024,79120,90896,0xefe40,75040,79136,90912,0xefe50,75056,79152,90928,0xefe20,75072,79168,90944,0xefe30,75088,79184,90960,0xefe00,75104,79200,90976,0xefe10,75120,79216,90992,0xefff0,75136,79232,91008,0xeffe0,75152,79248,91024,0xeffd0,75168,79264,91040,0xeffc0,75184,79280,91056,0xeffb0,75200,79296,91072,0xeffa0,75216,79312,91088,0xeff90,75232,79328,91104,0xeff80,75248,79344,91120,0xefb40,75264,79360,91136,0xefb50,75280,79376,91152,0xefb60,75296,79392,91168,0xefb70,75312,79408,91184,0xefb00,75328,79424,91200,0xefb10,75344,79440,91216,0xefb20,75360,79456,91232,0xefb30,75376,79472,91248,0xefcd0,75392,79488,91264,0xefcc0,75408,79504,91280,0xefcf0,75424,79520,91296,0xefce0,75440,79536,91312,0xefc90,75456,79552,91328,0xefc80,75472,79568,91344,0xefcb0,75488,79584,91360,0xefca0,75504,79600,91376,0xefe60,75520,79616,91392,0xefe70,75536,79632,91408,0xefe40,75552,79648,91424,0xefe50,75568,79664,91440,0xefe20,75584,79680,91456,0xefe30,75600,79696,91472,0xefe00,75616,79712,91488,0xefe10,75632,79728,91504,0xefff0,75648,79744,91520,0xeffe0,75664,79760,91536,0xeffd0,75680,79776,91552,0xeffc0,75696,79792,91568,0xeffb0,75712,79808,91584,0xeffa0,75728,79824,91600,0xeff90,75744,79840,91616,0xeff80,75760,79856,91632,0x1c0a0,36864,39936,49160,0x1c0a4,36880,39952,49176,0x1c0a8,36896,39968,49192,0x1c0ac,36912,39984,49208,0x1c0b0,36928,40000,49224,0x1c0b4,36944,40016,49240,0x1c0b8,36960,40032,49256,0x1c0bc,36976,40048,49272,0x1c0c0,36992,40064,49288,0x1c0c4,37008,40080,49304,0x1c0c8,37024,40096,49320,0x1c0cc,37040,40112,49336,0x1c0d0,37056,40128,49352,0x1c0d4,37072,40144,49368,0x1c0d8,37088,40160,49384,0x1c0dc,37104,40176,49400,0x1c0e0,37120,40192,49416,0x1c0e4,37136,40208,49432,0x1c0e8,37152,40224,49448,0x1c0ec,37168,40240,49464,0x1c0f0,37184,40256,49480,0x1c0f4,37200,40272,49496,0x1c0f8,37216,40288,49512,0x1c0fc,37232,40304,49528,0x1c100,37248,40320,49544,0x1c104,37264,40336,49560,0x1c108,37280,40352,49576,0x1c10c,37296,40368,49592,0x1c110,37312,40384,49608,0x1c114,37328,40400,49624,0x1c118,37344,40416,49640,0x1c11c,37360,40432,49656,0x1c120,37376,40448,49672,0x1c124,37392,40464,49688,0x1c128,37408,40480,49704,0x1c12c,37424,40496,49720,0x1c130,37440,40512,49736,0x1c134,37456,40528,49752,0x1c138,37472,40544,49768,0x1c13c,37488,40560,49784,0x1c140,37504,40576,49800,0x1c144,37520,40592,49816,0x1c148,37536,40608,49832,0x1c14c,37552,40624,49848,0x1c150,37568,40640,49864,0x1c154,37584,40656,49880,0x1c158,37600,40672,49896,0x1c15c,37616,40688,49912,0x1c160,37632,40704,49928,0x1c164,37648,40720,49944,0x1c168,37664,40736,49960,0x1c16c,37680,40752,49976,0x1c170,37696,40768,49992,0x1c174,37712,40784,50008,0x1c178,37728,40800,50024,0x1c17c,37744,40816,50040,0x1c180,37760,40832,50056,0x1c184,37776,40848,50072,0x1c188,37792,40864,50088,0x1c18c,37808,40880,50104,0x1c190,37824,40896,50120,0x1c194,37840,40912,50136,0x1c198,37856,40928,50152,0x1c19c,37872,40944,50168,73824,79904,0x1d600,0x1d604,73968,80048,0x1d610,0x1d614,74048,80128,0x1d620,0x1d624,74192,80272,0x1d630,0x1d634,74272,80480,0x1d640,0x1d644,74416,80624,0x1d650,0x1d654,74496,80704,0x1d660,0x1d664,74640,80848,0x1d670,0x1d674,74848,80928,0x1d680,0x1d684,74992,81072,0x1d690,0x1d694,75072,81152,0x1d6a0,0x1d6a4,75216,81296,0x1d6b0,0x1d6b4,75296,81504,0x1d6c0,0x1d6c4,75440,81648,0x1d6d0,0x1d6d4,75520,81728,0x1d6e0,0x1d6e4,75664,81872,0x1d6f0,0x1d6f4,73840,79912,0x1d700,0x1d704,73952,80056,0x1d710,0x1d714,74064,80136,0x1d720,0x1d724,74176,80280,0x1d730,0x1d734,74288,80488,0x1d740,0x1d744,74400,80632,0x1d750,0x1d754,74512,80712,0x1d760,0x1d764,74624,80856,0x1d770,0x1d774,74864,80936,0x1d780,0x1d784,74976,81080,0x1d790,0x1d794,75088,81160,0x1d7a0,0x1d7a4,75200,81304,0x1d7b0,0x1d7b4,75312,81512,0x1d7c0,0x1d7c4,75424,81656,0x1d7d0,0x1d7d4,75536,81736,0x1d7e0,0x1d7e4,75648,81880,0x1d7f0,0x1d7f4,79872,79920,0x1d800,0x1d804,80016,80032,0x1d810,0x1d814,80160,80144,0x1d820,0x1d824,80304,80256,0x1d830,0x1d834,80448,80496,0x1d840,0x1d844,80592,80608,0x1d850,0x1d854,80736,80720,0x1d860,0x1d864,80880,80832,0x1d870,0x1d874,80896,80944,0x1d880,0x1d884,81040,81056,0x1d890,0x1d894,81184,81168,0x1d8a0,0x1d8a4,81328,81280,0x1d8b0,0x1d8b4,81472,81520,0x1d8c0,0x1d8c4,81616,81632,0x1d8d0,0x1d8d4,81760,81744,0x1d8e0,0x1d8e4,81904,81856,0x1d8f0,0x1d8f4,79888,79928,0x1d900,0x1d904,80000,80040,0x1d910,0x1d914,80176,80152,0x1d920,0x1d924,80288,80264,0x1d930,0x1d934,80464,80504,0x1d940,0x1d944,80576,80616,0x1d950,0x1d954,80752,80728,0x1d960,0x1d964,80864,80840,0x1d970,0x1d974,80912,80952,0x1d980,0x1d984,81024,81064,0x1d990,0x1d994,81200,81176,0x1d9a0,0x1d9a4,81312,81288,0x1d9b0,0x1d9b4,81488,81528,0x1d9c0,0x1d9c4,81600,81640,0x1d9d0,0x1d9d4,81776,81752,0x1d9e0,0x1d9e4,81888,81864,0x1d9f0,0x1d9f4,0x1e680,0x1e680,0x1e8c0,0x100000,0x1e682,0x100000,0x100000,0x1e8c0,0x1e684,0x100000,0x100000,0x100000,0x1e686,0x100000,0x100000,0x100000,0x1e690,0x1e680,0x100000,0x100000,0x1e692,0x100000,0x100000,0x1e8c0,0x1e694,0x100000,0x100000,0x100000,0x1e696,0x100000,0x100000,0x100000,0x1e6a0,0x1e680,0x100000,0x100000,0x1e6a2,0x100000,0x100000,0x1e8c0,0x1e6a4,0x100000,0x100000,0x100000,0x1e6a6,0x100000,0x100000,0x100000,0x1e6b0,0x1e680,0x100000,0x100000,0x1e6b2,0x100000,0x100000,0x1e8c0,0x1e6b4,0x100000,0x100000,0x100000,0x1e6b6,0x100000,0x100000,0x100000,0x1e688,0x1e690,0x1e8f0,0x100000,0x1e68a,0x100000,0x100000,0x1e8f0,0x1e68c,0x100000,0x100000,0x100000,0x1e68e,0x100000,0x100000,0x100000,0x1e698,0x1e690,0x100000,0x100000,0x1e69a,0x100000,0x100000,0x1e8f0,0x1e69c,0x100000,0x100000,0x100000,0x1e69e,0x100000,0x100000,0x100000,0x1e6a8,0x1e690,0x100000,0x100000,0x1e6aa,0x100000,0x100000,0x1e8f0,0x1e6ac,0x100000,0x100000,0x100000,0x1e6ae,0x100000,0x100000,0x100000,0x1e6b8,0x1e690,0x100000,0x100000,0x1e6ba,0x100000,0x100000,0x1e8f0,0x1e6bc,0x100000,0x100000,0x100000,0x1e6be,0x100000,0x100000,0x100000,0x1e6c0,0x1e6a0,0x1e920,0x100000,0x1e6c2,0x100000,0x100000,0x1e920,0x1e6c4,0x100000,0x100000,0x100000,0x1e6c6,0x100000,0x100000,0x100000,0x1e6d0,0x1e6a0,0x100000,0x100000,0x1e6d2,0x100000,0x100000,0x1e920,0x1e6d4,0x100000,0x100000,0x100000,0x1e6d6,0x100000,0x100000,0x100000,0x1e6e0,0x1e6a0,0x100000,0x100000,0x1e6e2,0x100000,0x100000,0x1e920,0x1e6e4,0x100000,0x100000,0x100000,0x1e6e6,0x100000,0x100000,0x100000,0x1e6f0,0x1e6a0,0x100000,0x100000,0x1e6f2,0x100000,0x100000,0x1e920,0x1e6f4,0x100000,0x100000,0x100000,0x1e6f6,0x100000,0x100000,0x100000,0x1e6c8,0x1e6b0,0x1e950,0x100000,0x1e6ca,0x100000,0x100000,0x1e950,0x1e6cc,0x100000,0x100000,0x100000,0x1e6ce,0x100000,0x100000,0x100000,0x1e6d8,0x1e6b0,0x100000,0x100000,0x1e6da,0x100000,0x100000,0x1e950,0x1e6dc,0x100000,0x100000,0x100000,0x1e6de,0x100000,0x100000,0x100000,0x1e6e8,0x1e6b0,0x100000,0x100000,0x1e6ea,0x100000,0x100000,0x1e950,0x1e6ec,0x100000,0x100000,0x100000,0x1e6ee,0x100000,0x100000,0x100000,0x1e6f8,0x1e6b0,0x100000,0x100000,0x1e6fa,0x100000,0x100000,0x1e950,0x1e6fc,0x100000,0x100000,0x100000,0x1e6fe,0x100000,0x100000,0x100000,0x1eb60,0x251f0,36960,37024,0x1eb62,0x251f2,36976,37032,0x1eb64,0x251f4,36992,37040,0x1eb66,0x251f6,37008,37048,0x1eb68,0x25350,37344,37408,0x1eb6a,0x25352,37360,37416,0x1eb6c,0x25354,37376,37424,0x1eb6e,0x25356,37392,37432,0x1eb70,0x254b0,37696,37760,0x1eb72,0x254b2,37712,37768,0x1eb74,0x254b4,37792,37776,0x1eb76,0x254b6,37808,37784,0x1eb78,0x25610,38080,38144,0x1eb7a,0x25612,38096,38152,0x1eb7c,0x25614,38176,38160,0x1eb7e,0x25616,38192,38168,0x1eb60,0x251f8,38432,38624,0x1eb62,0x251fa,38448,38632,0x1eb64,0x251fc,38592,38640,0x1eb66,0x251fe,38608,38648,0x1eb68,0x25358,38816,39008,0x1eb6a,0x2535a,38832,39016,0x1eb6c,0x2535c,38976,39024,0x1eb6e,0x2535e,38992,39032,0x1eb70,0x254b8,39168,39360,0x1eb72,0x254ba,39184,39368,0x1eb74,0x254bc,39392,39376,0x1eb76,0x254be,39408,39384,0x1eb78,0x25618,39552,39744,0x1eb7a,0x2561a,39568,39752,0x1eb7c,0x2561c,39776,39760,0x1eb7e,0x2561e,39792,39768,0x1eb60,0x26278,40032,40096,0x1eb62,0x2627a,40048,40104,0x1eb64,0x2627c,40064,40112,0x1eb66,0x2627e,40080,40120,0x1eb68,0x263d8,40416,40480,0x1eb6a,0x263da,40432,40488,0x1eb6c,0x263dc,40448,40496,0x1eb6e,0x263de,40464,40504,0x1eb70,0x26538,40768,40832,0x1eb72,0x2653a,40784,40840,0x1eb74,0x2653c,40864,40848,0x1eb76,0x2653e,40880,40856,0x1eb78,0x26698,41152,41216,0x1eb7a,0x2669a,41168,41224,0x1eb7c,0x2669c,41248,41232,0x1eb7e,0x2669e,41264,41240,0x1eb60,0x100000,41504,41696,0x1eb62,0x100000,41520,41704,0x1eb64,0x100000,41664,41712,0x1eb66,0x100000,41680,41720,0x1eb68,0x100000,41888,42080,0x1eb6a,0x100000,41904,42088,0x1eb6c,0x100000,42048,42096,0x1eb6e,0x100000,42064,42104,0x1eb70,0x100000,42240,42432,0x1eb72,0x100000,42256,42440,0x1eb74,0x100000,42464,42448,0x1eb76,0x100000,42480,42456,0x1eb78,0x100000,42624,42816,0x1eb7a,0x100000,42640,42824,0x1eb7c,0x100000,42848,42832,0x1eb7e,0x100000,42864,42840,36960,36992,37024,46080,36968,37000,37032,46096,36976,37008,37040,46112,36984,37016,37048,46128,38112,38144,38176,46144,38120,38152,38184,46160,38128,38160,38192,46176,38136,38168,38200,46192,39232,39328,39296,46208,39240,39336,39304,46224,39248,39344,39312,46240,39256,39352,39320,46256,40384,40480,40448,46272,40392,40488,40456,46288,40400,40496,40464,46304,40408,40504,40472,46320,46112,46272,46304,46336,46120,46280,46312,46352,46128,46288,46320,46368,46136,46296,46328,46384,47264,47424,47456,46400,47272,47432,47464,46416,47280,47440,47472,46432,47288,47448,47480,46448,48384,48608,48576,46464,48392,48616,48584,46480,48400,48624,48592,46496,48408,48632,48600,46512,49536,49760,49728,46528,49544,49768,49736,46544,49552,49776,49744,46560,49560,49784,49752,46576,55392,55424,55456,46592,55400,55432,55464,46608,55408,55440,55472,46624,55416,55448,55480,46640,56544,56576,56608,46656,56552,56584,56616,46672,56560,56592,56624,46688,56568,56600,56632,46704,57664,57760,57728,46720,57672,57768,57736,46736,57680,57776,57744,46752,57688,57784,57752,46768,58816,58912,58880,46784,58824,58920,58888,46800,58832,58928,58896,46816,58840,58936,58904,46832,64544,64704,64736,46848,64552,64712,64744,46864,64560,64720,64752,46880,64568,64728,64760,46896,65696,65856,65888,46912,65704,65864,65896,46928,65712,65872,65904,46944,65720,65880,65912,46960,66816,67040,67008,46976,66824,67048,67016,46992,66832,67056,67024,47008,66840,67064,67032,47024,67968,68192,68160,47040,67976,68200,68168,47056,67984,68208,68176,47072,67992,68216,68184,47088,0x1e800,0x1e800,0x20cc0,18432,0x1e8c4,0x1e8c4,0x20cc4,18448,0x1e988,0x1e988,0x20cc8,18464,0x1ea4c,0x1ea4c,0x20ccc,18480,0x1eb10,0x1eb10,0x20cd0,18496,0x1ebd4,0x1ebd4,0x20cd4,18512,0x1ec98,0x1ec98,0x20cd8,18528,0x1ed5c,0x1ed5c,0x20cdc,18544,0x1ee20,0x1ee20,0x20ce0,18816,0x1eee4,0x1eee4,0x20ce4,18832,0x1efa8,0x1efa8,0x20ce8,18848,0x1f06c,0x1f06c,0x20cec,18864,0x1f130,0x1f130,0x20cf0,18880,0x1f1f4,0x1f1f4,0x20cf4,18896,0x1f2b8,0x1f2b8,0x20cf8,18912,0x1f37c,0x1f37c,0x20cfc,18928,0x1e810,0x1e800,0x20cc0,19200,0x1e8d4,0x1e8c4,0x20cc4,19216,0x1e998,0x1e988,0x20cc8,19232,0x1ea5c,0x1ea4c,0x20ccc,19248,0x1eb20,0x1eb10,0x20cd0,19264,0x1ebe4,0x1ebd4,0x20cd4,19280,0x1eca8,0x1ec98,0x20cd8,19296,0x1ed6c,0x1ed5c,0x20cdc,19312,0x1ee30,0x1ee20,0x20ce0,19584,0x1eef4,0x1eee4,0x20ce4,19600,0x1efb8,0x1efa8,0x20ce8,19616,0x1f07c,0x1f06c,0x20cec,19632,0x1f140,0x1f130,0x20cf0,19648,0x1f204,0x1f1f4,0x20cf4,19664,0x1f2c8,0x1f2b8,0x20cf8,19680,0x1f38c,0x1f37c,0x20cfc,19696,0x1e820,0x1e800,0x20cc0,19968,0x1e8e4,0x1e8c4,0x20cc4,19984,0x1e9a8,0x1e988,0x20cc8,20000,0x1ea6c,0x1ea4c,0x20ccc,20016,0x1eb30,0x1eb10,0x20cd0,20032,0x1ebf4,0x1ebd4,0x20cd4,20048,0x1ecb8,0x1ec98,0x20cd8,20064,0x1ed7c,0x1ed5c,0x20cdc,20080,0x1ee40,0x1ee20,0x20ce0,20352,0x1ef04,0x1eee4,0x20ce4,20368,0x1efc8,0x1efa8,0x20ce8,20384,0x1f08c,0x1f06c,0x20cec,20400,0x1f150,0x1f130,0x20cf0,20416,0x1f214,0x1f1f4,0x20cf4,20432,0x1f2d8,0x1f2b8,0x20cf8,20448,0x1f39c,0x1f37c,0x20cfc,20464,0x1e830,0x1e800,0x20cc0,20736,0x1e8f4,0x1e8c4,0x20cc4,20752,0x1e9b8,0x1e988,0x20cc8,20768,0x1ea7c,0x1ea4c,0x20ccc,20784,0x1eb40,0x1eb10,0x20cd0,20800,0x1ec04,0x1ebd4,0x20cd4,20816,0x1ecc8,0x1ec98,0x20cd8,20832,0x1ed8c,0x1ed5c,0x20cdc,20848,0x1ee50,0x1ee20,0x20ce0,21120,0x1ef14,0x1eee4,0x20ce4,21136,0x1efd8,0x1efa8,0x20ce8,21152,0x1f09c,0x1f06c,0x20cec,21168,0x1f160,0x1f130,0x20cf0,21184,0x1f224,0x1f1f4,0x20cf4,21200,0x1f2e8,0x1f2b8,0x20cf8,21216,0x1f3ac,0x1f37c,0x20cfc,21232,55296,0x26e70,0x1e8c0,0x1eb60,55312,0x26e74,0x1ed50,0x1eff0,55328,0x26e78,0x1f1e0,0x1f480,55344,0x26e7c,0x1f670,0x1f910,55360,0x26e80,0x1fb00,0x1fda0,55376,0x26e84,0x1ff90,0x20230,55392,0x26e88,0x20420,0x206c0,55408,0x26e8c,0x208b0,0x20b50,55680,0x26e90,0x20d40,0x20fe0,55696,0x26e94,0x211d0,0x21470,55712,0x26e98,0x21660,0x21900,55728,0x26e9c,0x21af0,0x21d90,55744,0x26ea0,0x21f80,0x22220,55760,0x26ea4,0x22410,0x226b0,55776,0x26ea8,0x228a0,0x22b40,55792,0x26eac,0x22d30,0x22fd0,56096,0x26eb0,0x1e8f0,0x1eb68,56112,0x26eb4,0x1ed80,0x1eff8,56064,0x26eb8,0x1f210,0x1f488,56080,0x26ebc,0x1f6a0,0x1f918,56160,0x26ec0,0x1fb30,0x1fda8,56176,0x26ec4,0x1ffc0,0x20238,56128,0x26ec8,0x20450,0x206c8,56144,0x26ecc,0x208e0,0x20b58,56480,0x26ed0,0x20d70,0x20fe8,56496,0x26ed4,0x21200,0x21478,56448,0x26ed8,0x21690,0x21908,56464,0x26edc,0x21b20,0x21d98,56544,0x26ee0,0x21fb0,0x22228,56560,0x26ee4,0x22440,0x226b8,56512,0x26ee8,0x228d0,0x22b48,56528,0x26eec,0x22d60,0x22fd8,56896,0x26ef0,0x1e920,0x1eb70,56912,0x26ef4,0x1edb0,0x1f000,56928,0x26ef8,0x1f240,0x1f490,56944,0x26efc,0x1f6d0,0x1f920,56832,0x26f00,0x1fb60,0x1fdb0,56848,0x26f04,0x1fff0,0x20240,56864,0x26f08,0x20480,0x206d0,56880,0x26f0c,0x20910,0x20b60,57280,0x26f10,0x20da0,0x20ff0,57296,0x26f14,0x21230,0x21480,57312,0x26f18,0x216c0,0x21910,57328,0x26f1c,0x21b50,0x21da0,57216,0x26f20,0x21fe0,0x22230,57232,0x26f24,0x22470,0x226c0,57248,0x26f28,0x22900,0x22b50,57264,0x26f2c,0x22d90,0x22fe0,57696,0x26f30,0x1e950,0x1eb78,57712,0x26f34,0x1ede0,0x1f008,57664,0x26f38,0x1f270,0x1f498,57680,0x26f3c,0x1f700,0x1f928,57632,0x26f40,0x1fb90,0x1fdb8,57648,0x26f44,0x20020,0x20248,57600,0x26f48,0x204b0,0x206d8,57616,0x26f4c,0x20940,0x20b68,58080,0x26f50,0x20dd0,0x20ff8,58096,0x26f54,0x21260,0x21488,58048,0x26f58,0x216f0,0x21918,58064,0x26f5c,0x21b80,0x21da8,58016,0x26f60,0x22010,0x22238,58032,0x26f64,0x224a0,0x226c8,57984,0x26f68,0x22930,0x22b58,58000,0x26f6c,0x22dc0,0x22fe8,0x3f803f80,0x3f803f80,16256,0,0,0x3f803f80,0x3f800000,0,0,0x3f803f80,0,16256,0,0x3f803f80,0,0x3f800000,0x3f803f80,0,0,0,0,0,0,0,0,0,0,0,0,0,0,0,0x3f803f80,0,0,0,0,0,0,0,0,0,0,0,0,0,0,0,0x3f803f80,0,0,0,0,0,0,0,0,0,0,0,0,0,0,0,0x3f803f80,0,0,0,0,0,0,0,0,0,0,0,0,0,0,0,0x3f803f80,0x3f803f80,16256,0,0,0x3f803f80,0x3f800000,0,0,0x3f803f80,0,16256,0,0x3f803f80,0,0x3f800000,0x3f803f80,0,0,0,0,0,0,0,0,0,0,0,0,0,0,0,0x3f803f80,0,0,0,0,0,0,0,0,0,0,0,0,0,0,0,0x3f803f80,0,0,0,0,0,0,0,0,0,0,0,0,0,0,0,0x3f803f80,0,0,0,0,0,0,0,0,0,0,0,0,0,0,0,0x3f803f80,0x3f803f80,16256,0,0,0x3f803f80,0x3f800000,0,0,0x3f803f80,0,16256,0,0x3f803f80,0,0x3f800000,0x3f803f80,0,0,0,0,0,0,0,0,0,0,0,0,0,0,0,0x3f803f80,0,0,0,0,0,0,0,0,0,0,0,0,0,0,0,0x3f803f80,0,0,0,0,0,0,0,0,0,0,0,0,0,0,0,0x3f803f80,0,0,0,0,0,0,0,0,0,0,0,0,0,0,0,0x3f803f80,0x3f803f80,16256,0,0,0x3f803f80,0x3f800000,0,0,0x3f803f80,0,16256,0,0x3f803f80,0,0x3f800000,0,64,0x60000,0xc0080,144,208,0x60010,0xc0090,288,352,0x60020,0xc00a0,432,496,0x60030,0xc00b0,576,512,0x60040,0xc00c0,720,656,0x60050,0xc00d0,864,800,0x60060,0xc00e0,1008,944,0x60070,0xc00f0,1024,1088,0x60190,0xc0210,1168,1232,0x60180,0xc0200,1312,1376,0x601b0,0xc0230,1456,1520,0x601a0,0xc0220,1600,1536,0x601d0,0xc0250,1744,1680,0x601c0,0xc0240,1888,1824,0x601f0,0xc0270,2032,1968,0x601e0,0xc0260,16,80,0x60320,0xc03a0,128,192,0x60330,0xc03b0,304,368,0x60300,0xc0380,416,480,0x60310,0xc0390,592,528,0x60360,0xc03e0,704,640,0x60370,0xc03f0,880,816,0x60340,0xc03c0,992,928,0x60350,0xc03d0,1040,1104,0x604b0,0xef930,1152,1216,0x604a0,0xef920,1328,1392,0x60490,0xef910,1440,1504,0x60480,0xef900,1616,1552,0x604f0,0xef970,1728,1664,0x604e0,0xef960,1904,1840,0x604d0,0xef950,2016,1952,0x604c0,0xef940,32,96,0x60640,0xefac0,176,240,0x60650,0xefad0,256,320,0x60660,0xefae0,400,464,0x60670,0xefaf0,608,544,0x60600,0xefa80,752,688,0x60610,0xefa90,832,768,0x60620,0xefaa0,976,912,0x60630,0xefab0,1056,1120,0x8fbd0,0xefc50,1200,1264,0x8fbc0,0xefc40,1280,1344,0x8fbf0,0xefc70,1424,1488,0x8fbe0,0xefc60,1632,1568,0x8fb90,0xefc10,1776,1712,0x8fb80,0xefc00,1856,1792,0x8fbb0,0xefc30,2000,1936,0x8fba0,0xefc20,48,112,0x8fd60,0xefde0,160,224,0x8fd70,0xefdf0,272,336,0x8fd40,0xefdc0,384,448,0x8fd50,0xefdd0,624,560,0x8fd20,0xefda0,736,672,0x8fd30,0xefdb0,848,784,0x8fd00,0xefd80,960,896,0x8fd10,0xefd90,1072,1136,0x8fef0,0xeff70,1184,1248,0x8fee0,0xeff60,1296,1360,0x8fed0,0xeff50,1408,1472,0x8fec0,0xeff40,1648,1584,0x8feb0,0xeff30,1760,1696,0x8fea0,0xeff20,1872,1808,0x8fe90,0xeff10,1984,1920,0x8fe80,0xeff00,0xf0100,75776,83968,91136,0xf0110,75792,83984,91152,0xf0120,75808,84000,91168,0xf0130,75824,84016,91184,0xf0140,75840,84032,91200,0xf0150,75856,84048,91216,0xf0160,75872,84064,91232,0xf0170,75888,84080,91248,0xf0290,75904,84096,91264,0xf0280,75920,84112,91280,0xf02b0,75936,84128,91296,0xf02a0,75952,84144,91312,0xf02d0,75968,84160,91328,0xf02c0,75984,84176,91344,0xf02f0,76000,84192,91360,0xf02e0,76016,84208,91376,0x11f820,76032,84224,91392,0x11f830,76048,84240,91408,0x11f800,76064,84256,91424,0x11f810,76080,84272,91440,0x11f860,76096,84288,91456,0x11f870,76112,84304,91472,0x11f840,76128,84320,91488,0x11f850,76144,84336,91504,0x11f9b0,76160,84352,91520,0x11f9a0,76176,84368,91536,0x11f990,76192,84384,91552,0x11f980,76208,84400,91568,0x11f9f0,76224,84416,91584,0x11f9e0,76240,84432,91600,0x11f9d0,76256,84448,91616,0x11f9c0,76272,84464,91632,0xf0100,76288,84480,91648,0xf0110,76304,84496,91664,0xf0120,76320,84512,91680,0xf0130,76336,84528,91696,0xf0140,76352,84544,91712,0xf0150,76368,84560,91728,0xf0160,76384,84576,91744,0xf0170,76400,84592,91760,0xf0290,76416,84608,91776,0xf0280,76432,84624,91792,0xf02b0,76448,84640,91808,0xf02a0,76464,84656,91824,0xf02d0,76480,84672,91840,0xf02c0,76496,84688,91856,0xf02f0,76512,84704,91872,0xf02e0,76528,84720,91888,0x11f820,76544,84736,91904,0x11f830,76560,84752,91920,0x11f800,76576,84768,91936,0x11f810,76592,84784,91952,0x11f860,76608,84800,91968,0x11f870,76624,84816,91984,0x11f840,76640,84832,92000,0x11f850,76656,84848,92016,0x11f9b0,76672,84864,92032,0x11f9a0,76688,84880,92048,0x11f990,76704,84896,92064,0x11f980,76720,84912,92080,0x11f9f0,76736,84928,92096,0x11f9e0,76752,84944,92112,0x11f9d0,76768,84960,92128,0x11f9c0,76784,84976,92144,0x1c140,36864,43008,52224,0x1c144,36880,43024,52240,0x1c148,36896,43040,52256,0x1c14c,36912,43056,52272,0x1c150,36928,43072,52288,0x1c154,36944,43088,52304,0x1c158,36960,43104,52320,0x1c15c,36976,43120,52336,0x1c160,36992,43136,52352,0x1c164,37008,43152,52368,0x1c168,37024,43168,52384,0x1c16c,37040,43184,52400,0x1c170,37056,43200,52416,0x1c174,37072,43216,52432,0x1c178,37088,43232,52448,0x1c17c,37104,43248,52464,0x1c180,37120,43264,52480,0x1c184,37136,43280,52496,0x1c188,37152,43296,52512,0x1c18c,37168,43312,52528,0x1c190,37184,43328,52544,0x1c194,37200,43344,52560,0x1c198,37216,43360,52576,0x1c19c,37232,43376,52592,0x1c1a0,37248,43392,52608,0x1c1a4,37264,43408,52624,0x1c1a8,37280,43424,52640,0x1c1ac,37296,43440,52656,0x1c1b0,37312,43456,52672,0x1c1b4,37328,43472,52688,0x1c1b8,37344,43488,52704,0x1c1bc,37360,43504,52720,0x1c1c0,37376,43520,52736,0x1c1c4,37392,43536,52752,0x1c1c8,37408,43552,52768,0x1c1cc,37424,43568,52784,0x1c1d0,37440,43584,52800,0x1c1d4,37456,43600,52816,0x1c1d8,37472,43616,52832,0x1c1dc,37488,43632,52848,0x1c1e0,37504,43648,52864,0x1c1e4,37520,43664,52880,0x1c1e8,37536,43680,52896,0x1c1ec,37552,43696,52912,0x1c1f0,37568,43712,52928,0x1c1f4,37584,43728,52944,0x1c1f8,37600,43744,52960,0x1c1fc,37616,43760,52976,0x1c200,37632,43776,52992,0x1c204,37648,43792,53008,0x1c208,37664,43808,53024,0x1c20c,37680,43824,53040,0x1c210,37696,43840,53056,0x1c214,37712,43856,53072,0x1c218,37728,43872,53088,0x1c21c,37744,43888,53104,0x1c220,37760,43904,53120,0x1c224,37776,43920,53136,0x1c228,37792,43936,53152,0x1c22c,37808,43952,53168,0x1c230,37824,43968,53184,0x1c234,37840,43984,53200,0x1c238,37856,44000,53216,0x1c23c,37872,44016,53232,79936,86016,0x1d600,0x1d608,80080,86160,0x1d610,0x1d618,80224,86304,0x1d620,0x1d628,80368,86448,0x1d630,0x1d638,80384,86592,0x1d640,0x1d648,80528,86736,0x1d650,0x1d658,80672,86880,0x1d660,0x1d668,80816,87024,0x1d670,0x1d678,80960,87040,0x1d680,0x1d688,81104,87184,0x1d690,0x1d698,81248,87328,0x1d6a0,0x1d6a8,81392,87472,0x1d6b0,0x1d6b8,81408,87616,0x1d6c0,0x1d6c8,81552,87760,0x1d6d0,0x1d6d8,81696,87904,0x1d6e0,0x1d6e8,81840,88048,0x1d6f0,0x1d6f8,79952,86024,0x1d700,0x1d708,80064,86168,0x1d710,0x1d718,80240,86312,0x1d720,0x1d728,80352,86456,0x1d730,0x1d738,80400,86600,0x1d740,0x1d748,80512,86744,0x1d750,0x1d758,80688,86888,0x1d760,0x1d768,80800,87032,0x1d770,0x1d778,80976,87048,0x1d780,0x1d788,81088,87192,0x1d790,0x1d798,81264,87336,0x1d7a0,0x1d7a8,81376,87480,0x1d7b0,0x1d7b8,81424,87624,0x1d7c0,0x1d7c8,81536,87768,0x1d7d0,0x1d7d8,81712,87912,0x1d7e0,0x1d7e8,81824,88056,0x1d7f0,0x1d7f8,79968,86032,0x1d800,0x1d808,80112,86144,0x1d810,0x1d818,80192,86320,0x1d820,0x1d828,80336,86432,0x1d830,0x1d838,80416,86608,0x1d840,0x1d848,80560,86720,0x1d850,0x1d858,80640,86896,0x1d860,0x1d868,80784,87008,0x1d870,0x1d878,80992,87056,0x1d880,0x1d888,81136,87168,0x1d890,0x1d898,81216,87344,0x1d8a0,0x1d8a8,81360,87456,0x1d8b0,0x1d8b8,81440,87632,0x1d8c0,0x1d8c8,81584,87744,0x1d8d0,0x1d8d8,81664,87920,0x1d8e0,0x1d8e8,81808,88032,0x1d8f0,0x1d8f8,79984,86040,0x1d900,0x1d908,80096,86152,0x1d910,0x1d918,80208,86328,0x1d920,0x1d928,80320,86440,0x1d930,0x1d938,80432,86616,0x1d940,0x1d948,80544,86728,0x1d950,0x1d958,80656,86904,0x1d960,0x1d968,80768,87016,0x1d970,0x1d978,81008,87064,0x1d980,0x1d988,81120,87176,0x1d990,0x1d998,81232,87352,0x1d9a0,0x1d9a8,81344,87464,0x1d9b0,0x1d9b8,81456,87640,0x1d9c0,0x1d9c8,81568,87752,0x1d9d0,0x1d9d8,81680,87928,0x1d9e0,0x1d9e8,81792,88040,0x1d9f0,0x1d9f8,0x1e700,0x1e700,0x1e980,0x100000,0x1e702,0x100000,0x100000,0x1e980,0x1e704,0x100000,0x100000,0x100000,0x1e706,0x100000,0x100000,0x100000,0x1e710,0x1e700,0x100000,0x100000,0x1e712,0x100000,0x100000,0x1e980,0x1e714,0x100000,0x100000,0x100000,0x1e716,0x100000,0x100000,0x100000,0x1e720,0x1e700,0x100000,0x100000,0x1e722,0x100000,0x100000,0x1e980,0x1e724,0x100000,0x100000,0x100000,0x1e726,0x100000,0x100000,0x100000,0x1e730,0x1e700,0x100000,0x100000,0x1e732,0x100000,0x100000,0x1e980,0x1e734,0x100000,0x100000,0x100000,0x1e736,0x100000,0x100000,0x100000,0x1e708,0x1e710,0x1e9b0,0x100000,0x1e70a,0x100000,0x100000,0x1e9b0,0x1e70c,0x100000,0x100000,0x100000,0x1e70e,0x100000,0x100000,0x100000,0x1e718,0x1e710,0x100000,0x100000,0x1e71a,0x100000,0x100000,0x1e9b0,0x1e71c,0x100000,0x100000,0x100000,0x1e71e,0x100000,0x100000,0x100000,0x1e728,0x1e710,0x100000,0x100000,0x1e72a,0x100000,0x100000,0x1e9b0,0x1e72c,0x100000,0x100000,0x100000,0x1e72e,0x100000,0x100000,0x100000,0x1e738,0x1e710,0x100000,0x100000,0x1e73a,0x100000,0x100000,0x1e9b0,0x1e73c,0x100000,0x100000,0x100000,0x1e73e,0x100000,0x100000,0x100000,0x1e740,0x1e720,0x1e9e0,0x100000,0x1e742,0x100000,0x100000,0x1e9e0,0x1e744,0x100000,0x100000,0x100000,0x1e746,0x100000,0x100000,0x100000,0x1e750,0x1e720,0x100000,0x100000,0x1e752,0x100000,0x100000,0x1e9e0,0x1e754,0x100000,0x100000,0x100000,0x1e756,0x100000,0x100000,0x100000,0x1e760,0x1e720,0x100000,0x100000,0x1e762,0x100000,0x100000,0x1e9e0,0x1e764,0x100000,0x100000,0x100000,0x1e766,0x100000,0x100000,0x100000,0x1e770,0x1e720,0x100000,0x100000,0x1e772,0x100000,0x100000,0x1e9e0,0x1e774,0x100000,0x100000,0x100000,0x1e776,0x100000,0x100000,0x100000,0x1e748,0x1e730,0x1ea10,0x100000,0x1e74a,0x100000,0x100000,0x1ea10,0x1e74c,0x100000,0x100000,0x100000,0x1e74e,0x100000,0x100000,0x100000,0x1e758,0x1e730,0x100000,0x100000,0x1e75a,0x100000,0x100000,0x1ea10,0x1e75c,0x100000,0x100000,0x100000,0x1e75e,0x100000,0x100000,0x100000,0x1e768,0x1e730,0x100000,0x100000,0x1e76a,0x100000,0x100000,0x1ea10,0x1e76c,0x100000,0x100000,0x100000,0x1e76e,0x100000,0x100000,0x100000,0x1e778,0x1e730,0x100000,0x100000,0x1e77a,0x100000,0x100000,0x1ea10,0x1e77c,0x100000,0x100000,0x100000,0x1e77e,0x100000,0x100000,0x100000,0x1ebc0,0x25770,37056,37120,0x1ebc2,0x25772,37072,37128,0x1ebc4,0x25774,37088,37136,0x1ebc6,0x25776,37104,37144,0x1ebc8,0x258d0,37440,37504,0x1ebca,0x258d2,37456,37512,0x1ebcc,0x258d4,37472,37520,0x1ebce,0x258d6,37488,37528,0x1ebd0,0x25a30,37856,37920,0x1ebd2,0x25a32,37872,37928,0x1ebd4,0x25a34,37824,37936,0x1ebd6,0x25a36,37840,37944,0x1ebd8,0x25b90,38240,38304,0x1ebda,0x25b92,38256,38312,0x1ebdc,0x25b94,38208,38320,0x1ebde,0x25b96,38224,38328,0x1ebc0,0x25778,38528,38720,0x1ebc2,0x2577a,38544,38728,0x1ebc4,0x2577c,38560,38736,0x1ebc6,0x2577e,38576,38744,0x1ebc8,0x258d8,38912,39104,0x1ebca,0x258da,38928,39112,0x1ebcc,0x258dc,38944,39120,0x1ebce,0x258de,38960,39128,0x1ebd0,0x25a38,39328,39520,0x1ebd2,0x25a3a,39344,39528,0x1ebd4,0x25a3c,39296,39536,0x1ebd6,0x25a3e,39312,39544,0x1ebd8,0x25b98,39712,39904,0x1ebda,0x25b9a,39728,39912,0x1ebdc,0x25b9c,39680,39920,0x1ebde,0x25b9e,39696,39928,0x1ebc0,0x267f0,40128,40192,0x1ebc2,0x267f2,40144,40200,0x1ebc4,0x267f4,40160,40208,0x1ebc6,0x267f6,40176,40216,0x1ebc8,0x26950,40512,40576,0x1ebca,0x26952,40528,40584,0x1ebcc,0x26954,40544,40592,0x1ebce,0x26956,40560,40600,0x1ebd0,0x26ab0,40928,40992,0x1ebd2,0x26ab2,40944,41000,0x1ebd4,0x26ab4,40896,41008,0x1ebd6,0x26ab6,40912,41016,0x1ebd8,0x26c10,41312,41376,0x1ebda,0x26c12,41328,41384,0x1ebdc,0x26c14,41280,41392,0x1ebde,0x26c16,41296,41400,0x1ebc0,0x100000,41600,41792,0x1ebc2,0x100000,41616,41800,0x1ebc4,0x100000,41632,41808,0x1ebc6,0x100000,41648,41816,0x1ebc8,0x100000,41984,42176,0x1ebca,0x100000,42000,42184,0x1ebcc,0x100000,42016,42192,0x1ebce,0x100000,42032,42200,0x1ebd0,0x100000,42400,42592,0x1ebd2,0x100000,42416,42600,0x1ebd4,0x100000,42368,42608,0x1ebd6,0x100000,42384,42616,0x1ebd8,0x100000,42784,42976,0x1ebda,0x100000,42800,42984,0x1ebdc,0x100000,42752,42992,0x1ebde,0x100000,42768,43000,37056,37088,37120,55296,37064,37096,37128,55312,37072,37104,37136,55328,37080,37112,37144,55344,38208,38240,38272,55360,38216,38248,38280,55376,38224,38256,38288,55392,38232,38264,38296,55408,39392,39360,39456,55424,39400,39368,39464,55440,39408,39376,39472,55456,39416,39384,39480,55472,40544,40512,40608,55488,40552,40520,40616,55504,40560,40528,40624,55520,40568,40536,40632,55536,46208,46240,46400,55552,46216,46248,46408,55568,46224,46256,46416,55584,46232,46264,46424,55600,47360,47392,47552,55616,47368,47400,47560,55632,47376,47408,47568,55648,47384,47416,47576,55664,48544,48512,48736,55680,48552,48520,48744,55696,48560,48528,48752,55712,48568,48536,48760,55728,49696,49664,49888,55744,49704,49672,49896,55760,49712,49680,49904,55776,49720,49688,49912,55792,55488,55520,55552,55808,55496,55528,55560,55824,55504,55536,55568,55840,55512,55544,55576,55856,56640,56672,56704,55872,56648,56680,56712,55888,56656,56688,56720,55904,56664,56696,56728,55920,57824,57792,57888,55936,57832,57800,57896,55952,57840,57808,57904,55968,57848,57816,57912,55984,58976,58944,59040,56000,58984,58952,59048,56016,58992,58960,59056,56032,59000,58968,59064,56048,64640,64672,64832,56064,64648,64680,64840,56080,64656,64688,64848,56096,64664,64696,64856,56112,65792,65824,65984,56128,65800,65832,65992,56144,65808,65840,66000,56160,65816,65848,66008,56176,66976,66944,67168,56192,66984,66952,67176,56208,66992,66960,67184,56224,67000,66968,67192,56240,68128,68096,68320,56256,68136,68104,68328,56272,68144,68112,68336,56288,68152,68120,68344,56304,0x1e800,0x1e800,0x20cc0,36864,0x1e8c4,0x1e8c4,0x20cc4,36880,0x1e988,0x1e988,0x20cc8,36896,0x1ea4c,0x1ea4c,0x20ccc,36912,0x1eb10,0x1eb10,0x20cd0,36928,0x1ebd4,0x1ebd4,0x20cd4,36944,0x1ec98,0x1ec98,0x20cd8,36960,0x1ed5c,0x1ed5c,0x20cdc,36976,0x1ee20,0x1ee20,0x20ce0,37248,0x1eee4,0x1eee4,0x20ce4,37264,0x1efa8,0x1efa8,0x20ce8,37280,0x1f06c,0x1f06c,0x20cec,37296,0x1f130,0x1f130,0x20cf0,37312,0x1f1f4,0x1f1f4,0x20cf4,37328,0x1f2b8,0x1f2b8,0x20cf8,37344,0x1f37c,0x1f37c,0x20cfc,37360,0x1e810,0x1e800,0x20cc0,37632,0x1e8d4,0x1e8c4,0x20cc4,37648,0x1e998,0x1e988,0x20cc8,37664,0x1ea5c,0x1ea4c,0x20ccc,37680,0x1eb20,0x1eb10,0x20cd0,37696,0x1ebe4,0x1ebd4,0x20cd4,37712,0x1eca8,0x1ec98,0x20cd8,37728,0x1ed6c,0x1ed5c,0x20cdc,37744,0x1ee30,0x1ee20,0x20ce0,38016,0x1eef4,0x1eee4,0x20ce4,38032,0x1efb8,0x1efa8,0x20ce8,38048,0x1f07c,0x1f06c,0x20cec,38064,0x1f140,0x1f130,0x20cf0,38080,0x1f204,0x1f1f4,0x20cf4,38096,0x1f2c8,0x1f2b8,0x20cf8,38112,0x1f38c,0x1f37c,0x20cfc,38128,0x1e820,0x1e800,0x20cc0,38400,0x1e8e4,0x1e8c4,0x20cc4,38416,0x1e9a8,0x1e988,0x20cc8,38432,0x1ea6c,0x1ea4c,0x20ccc,38448,0x1eb30,0x1eb10,0x20cd0,38464,0x1ebf4,0x1ebd4,0x20cd4,38480,0x1ecb8,0x1ec98,0x20cd8,38496,0x1ed7c,0x1ed5c,0x20cdc,38512,0x1ee40,0x1ee20,0x20ce0,38784,0x1ef04,0x1eee4,0x20ce4,38800,0x1efc8,0x1efa8,0x20ce8,38816,0x1f08c,0x1f06c,0x20cec,38832,0x1f150,0x1f130,0x20cf0,38848,0x1f214,0x1f1f4,0x20cf4,38864,0x1f2d8,0x1f2b8,0x20cf8,38880,0x1f39c,0x1f37c,0x20cfc,38896,0x1e830,0x1e800,0x20cc0,39168,0x1e8f4,0x1e8c4,0x20cc4,39184,0x1e9b8,0x1e988,0x20cc8,39200,0x1ea7c,0x1ea4c,0x20ccc,39216,0x1eb40,0x1eb10,0x20cd0,39232,0x1ec04,0x1ebd4,0x20cd4,39248,0x1ecc8,0x1ec98,0x20cd8,39264,0x1ed8c,0x1ed5c,0x20cdc,39280,0x1ee50,0x1ee20,0x20ce0,39552,0x1ef14,0x1eee4,0x20ce4,39568,0x1efd8,0x1efa8,0x20ce8,39584,0x1f09c,0x1f06c,0x20cec,39600,0x1f160,0x1f130,0x20cf0,39616,0x1f224,0x1f1f4,0x20cf4,39632,0x1f2e8,0x1f2b8,0x20cf8,39648,0x1f3ac,0x1f37c,0x20cfc,39664,73728,0x26f70,0x1e980,0x1ebc0,73744,0x26f74,0x1ee10,0x1f050,73760,0x26f78,0x1f2a0,0x1f4e0,73776,0x26f7c,0x1f730,0x1f970,73792,0x26f80,0x1fbc0,0x1fe00,73808,0x26f84,0x20050,0x20290,73824,0x26f88,0x204e0,0x20720,73840,0x26f8c,0x20970,0x20bb0,74112,0x26f90,0x20e00,0x21040,74128,0x26f94,0x21290,0x214d0,74144,0x26f98,0x21720,0x21960,74160,0x26f9c,0x21bb0,0x21df0,74176,0x26fa0,0x22040,0x22280,74192,0x26fa4,0x224d0,0x22710,74208,0x26fa8,0x22960,0x22ba0,74224,0x26fac,0x22df0,0x23030,74528,0x26fb0,0x1e9b0,0x1ebc8,74544,0x26fb4,0x1ee40,0x1f058,74496,0x26fb8,0x1f2d0,0x1f4e8,74512,0x26fbc,0x1f760,0x1f978,74592,0x26fc0,0x1fbf0,0x1fe08,74608,0x26fc4,0x20080,0x20298,74560,0x26fc8,0x20510,0x20728,74576,0x26fcc,0x209a0,0x20bb8,74912,0x26fd0,0x20e30,0x21048,74928,0x26fd4,0x212c0,0x214d8,74880,0x26fd8,0x21750,0x21968,74896,0x26fdc,0x21be0,0x21df8,74976,0x26fe0,0x22070,0x22288,74992,0x26fe4,0x22500,0x22718,74944,0x26fe8,0x22990,0x22ba8,74960,0x26fec,0x22e20,0x23038,75328,0x26ff0,0x1e9e0,0x1ebd0,75344,0x26ff4,0x1ee70,0x1f060,75360,0x26ff8,0x1f300,0x1f4f0,75376,0x26ffc,0x1f790,0x1f980,75264,0x27000,0x1fc20,0x1fe10,75280,0x27004,0x200b0,0x202a0,75296,0x27008,0x20540,0x20730,75312,0x2700c,0x209d0,0x20bc0,75712,0x27010,0x20e60,0x21050,75728,0x27014,0x212f0,0x214e0,75744,0x27018,0x21780,0x21970,75760,0x2701c,0x21c10,0x21e00,75648,0x27020,0x220a0,0x22290,75664,0x27024,0x22530,0x22720,75680,0x27028,0x229c0,0x22bb0,75696,0x2702c,0x22e50,0x23040,76128,0x27030,0x1ea10,0x1ebd8,76144,0x27034,0x1eea0,0x1f068,76096,0x27038,0x1f330,0x1f4f8,76112,0x2703c,0x1f7c0,0x1f988,76064,0x27040,0x1fc50,0x1fe18,76080,0x27044,0x200e0,0x202a8,76032,0x27048,0x20570,0x20738,76048,0x2704c,0x20a00,0x20bc8,76512,0x27050,0x20e90,0x21058,76528,0x27054,0x21320,0x214e8,76480,0x27058,0x217b0,0x21978,76496,0x2705c,0x21c40,0x21e08,76448,0x27060,0x220d0,0x22298,76464,0x27064,0x22560,0x22728,76416,0x27068,0x229f0,0x22bb8,76432,0x2706c,0x22e80,0x23048,0x3f803f80,0x3f803f80,16256,0,0,0x3f803f80,0x3f800000,0,0,0x3f803f80,0,16256,0,0x3f803f80,0,0x3f800000,0x3f803f80,0,0,0,0,0,0,0,0,0,0,0,0,0,0,0,0x3f803f80,0,0,0,0,0,0,0,0,0,0,0,0,0,0,0,0x3f803f80,0,0,0,0,0,0,0,0,0,0,0,0,0,0,0,0x3f803f80,0,0,0,0,0,0,0,0,0,0,0,0,0,0,0,0x3f803f80,0x3f803f80,16256,0,0,0x3f803f80,0x3f800000,0,0,0x3f803f80,0,16256,0,0x3f803f80,0,0x3f800000,0x3f803f80,0,0,0,0,0,0,0,0,0,0,0,0,0,0,0,0x3f803f80,0,0,0,0,0,0,0,0,0,0,0,0,0,0,0,0x3f803f80,0,0,0,0,0,0,0,0,0,0,0,0,0,0,0,0x3f803f80,0,0,0,0,0,0,0,0,0,0,0,0,0,0,0,0x3f803f80,0x3f803f80,16256,0,0,0x3f803f80,0x3f800000,0,0,0x3f803f80,0,16256,0,0x3f803f80,0,0x3f800000,0x3f803f80,0,0,0,0,0,0,0,0,0,0,0,0,0,0,0,0x3f803f80,0,0,0,0,0,0,0,0,0,0,0,0,0,0,0,0x3f803f80,0,0,0,0,0,0,0,0,0,0,0,0,0,0,0,0x3f803f80,0,0,0,0,0,0,0,0,0,0,0,0,0,0,0,0x3f803f80,0x3f803f80,16256,0,0,0x3f803f80,0x3f800000,0,0,0x3f803f80,0,16256,0,0x3f803f80,0,0x3f800000,0,64,0x90000,0xf0080,144,208,0x90010,0xf0090,288,352,0x90020,0xf00a0,432,496,0x90030,0xf00b0,576,512,0x90040,0xf00c0,720,656,0x90050,0xf00d0,864,800,0x90060,0xf00e0,1008,944,0x90070,0xf00f0,1024,1088,0x90190,0xf0210,1168,1232,0x90180,0xf0200,1312,1376,0x901b0,0xf0230,1456,1520,0x901a0,0xf0220,1600,1536,0x901d0,0xf0250,1744,1680,0x901c0,0xf0240,1888,1824,0x901f0,0xf0270,2032,1968,0x901e0,0xf0260,16,80,0x90320,0x11f7a0,128,192,0x90330,0x11f7b0,304,368,0x90300,0x11f780,416,480,0x90310,0x11f790,592,528,0x90360,0x11f7e0,704,640,0x90370,0x11f7f0,880,816,0x90340,0x11f7c0,992,928,0x90350,0x11f7d0,1040,1104,0x904b0,0x11f930,1152,1216,0x904a0,0x11f920,1328,1392,0x90490,0x11f910,1440,1504,0x90480,0x11f900,1616,1552,0x904f0,0x11f970,1728,1664,0x904e0,0x11f960,1904,1840,0x904d0,0x11f950,2016,1952,0x904c0,0x11f940,32,96,0xbfa40,0x11fac0,176,240,0xbfa50,0x11fad0,256,320,0xbfa60,0x11fae0,400,464,0xbfa70,0x11faf0,608,544,0xbfa00,0x11fa80,752,688,0xbfa10,0x11fa90,832,768,0xbfa20,0x11faa0,976,912,0xbfa30,0x11fab0,1056,1120,0xbfbd0,0x11fc50,1200,1264,0xbfbc0,0x11fc40,1280,1344,0xbfbf0,0x11fc70,1424,1488,0xbfbe0,0x11fc60,1632,1568,0xbfb90,0x11fc10,1776,1712,0xbfb80,0x11fc00,1856,1792,0xbfbb0,0x11fc30,2000,1936,0xbfba0,0x11fc20,48,112,0xbfd60,0x11fde0,160,224,0xbfd70,0x11fdf0,272,336,0xbfd40,0x11fdc0,384,448,0xbfd50,0x11fdd0,624,560,0xbfd20,0x11fda0,736,672,0xbfd30,0x11fdb0,848,784,0xbfd00,0x11fd80,960,896,0xbfd10,0x11fd90,1072,1136,0xbfef0,0x11ff70,1184,1248,0xbfee0,0x11ff60,1296,1360,0xbfed0,0x11ff50,1408,1472,0xbfec0,0x11ff40,1648,1584,0xbfeb0,0x11ff30,1760,1696,0xbfea0,0x11ff20,1872,1808,0xbfe90,0x11ff10,1984,1920,0xbfe80,0x11ff00,0x11fb40,76800,84992,91648,0x11fb50,76816,85008,91664,0x11fb60,76832,85024,91680,0x11fb70,76848,85040,91696,0x11fb00,76864,85056,91712,0x11fb10,76880,85072,91728,0x11fb20,76896,85088,91744,0x11fb30,76912,85104,91760,0x11fcd0,76928,85120,91776,0x11fcc0,76944,85136,91792,0x11fcf0,76960,85152,91808,0x11fce0,76976,85168,91824,0x11fc90,76992,85184,91840,0x11fc80,77008,85200,91856,0x11fcb0,77024,85216,91872,0x11fca0,77040,85232,91888,0x11fe60,77056,85248,91904,0x11fe70,77072,85264,91920,0x11fe40,77088,85280,91936,0x11fe50,77104,85296,91952,0x11fe20,77120,85312,91968,0x11fe30,77136,85328,91984,0x11fe00,77152,85344,92000,0x11fe10,77168,85360,92016,0x11fff0,77184,85376,92032,0x11ffe0,77200,85392,92048,0x11ffd0,77216,85408,92064,0x11ffc0,77232,85424,92080,0x11ffb0,77248,85440,92096,0x11ffa0,77264,85456,92112,0x11ff90,77280,85472,92128,0x11ff80,77296,85488,92144,0x11fb40,77312,85504,92160,0x11fb50,77328,85520,92176,0x11fb60,77344,85536,92192,0x11fb70,77360,85552,92208,0x11fb00,77376,85568,92224,0x11fb10,77392,85584,92240,0x11fb20,77408,85600,92256,0x11fb30,77424,85616,92272,0x11fcd0,77440,85632,92288,0x11fcc0,77456,85648,92304,0x11fcf0,77472,85664,92320,0x11fce0,77488,85680,92336,0x11fc90,77504,85696,92352,0x11fc80,77520,85712,92368,0x11fcb0,77536,85728,92384,0x11fca0,77552,85744,92400,0x11fe60,77568,85760,92416,0x11fe70,77584,85776,92432,0x11fe40,77600,85792,92448,0x11fe50,77616,85808,92464,0x11fe20,77632,85824,92480,0x11fe30,77648,85840,92496,0x11fe00,77664,85856,92512,0x11fe10,77680,85872,92528,0x11fff0,77696,85888,92544,0x11ffe0,77712,85904,92560,0x11ffd0,77728,85920,92576,0x11ffc0,77744,85936,92592,0x11ffb0,77760,85952,92608,0x11ffa0,77776,85968,92624,0x11ff90,77792,85984,92640,0x11ff80,77808,86000,92656,0x1c1d0,36864,46080,52232,0x1c1d4,36880,46096,52248,0x1c1d8,36896,46112,52264,0x1c1dc,36912,46128,52280,0x1c1e0,36928,46144,52296,0x1c1e4,36944,46160,52312,0x1c1e8,36960,46176,52328,0x1c1ec,36976,46192,52344,0x1c1f0,36992,46208,52360,0x1c1f4,37008,46224,52376,0x1c1f8,37024,46240,52392,0x1c1fc,37040,46256,52408,0x1c200,37056,46272,52424,0x1c204,37072,46288,52440,0x1c208,37088,46304,52456,0x1c20c,37104,46320,52472,0x1c210,37120,46336,52488,0x1c214,37136,46352,52504,0x1c218,37152,46368,52520,0x1c21c,37168,46384,52536,0x1c220,37184,46400,52552,0x1c224,37200,46416,52568,0x1c228,37216,46432,52584,0x1c22c,37232,46448,52600,0x1c230,37248,46464,52616,0x1c234,37264,46480,52632,0x1c238,37280,46496,52648,0x1c23c,37296,46512,52664,0x1c240,37312,46528,52680,0x1c244,37328,46544,52696,0x1c248,37344,46560,52712,0x1c24c,37360,46576,52728,0x1c250,37376,46592,52744,0x1c254,37392,46608,52760,0x1c258,37408,46624,52776,0x1c25c,37424,46640,52792,0x1c260,37440,46656,52808,0x1c264,37456,46672,52824,0x1c268,37472,46688,52840,0x1c26c,37488,46704,52856,0x1c270,37504,46720,52872,0x1c274,37520,46736,52888,0x1c278,37536,46752,52904,0x1c27c,37552,46768,52920,0x1c280,37568,46784,52936,0x1c284,37584,46800,52952,0x1c288,37600,46816,52968,0x1c28c,37616,46832,52984,0x1c290,37632,46848,53000,0x1c294,37648,46864,53016,0x1c298,37664,46880,53032,0x1c29c,37680,46896,53048,0x1c2a0,37696,46912,53064,0x1c2a4,37712,46928,53080,0x1c2a8,37728,46944,53096,0x1c2ac,37744,46960,53112,0x1c2b0,37760,46976,53128,0x1c2b4,37776,46992,53144,0x1c2b8,37792,47008,53160,0x1c2bc,37808,47024,53176,0x1c2c0,37824,47040,53192,0x1c2c4,37840,47056,53208,0x1c2c8,37856,47072,53224,0x1c2cc,37872,47088,53240,86048,86112,0x1d600,0x1d60c,86192,86256,0x1d610,0x1d61c,86272,86336,0x1d620,0x1d62c,86416,86480,0x1d630,0x1d63c,86624,86560,0x1d640,0x1d64c,86768,86704,0x1d650,0x1d65c,86848,86784,0x1d660,0x1d66c,86992,86928,0x1d670,0x1d67c,87072,87136,0x1d680,0x1d68c,87216,87280,0x1d690,0x1d69c,87296,87360,0x1d6a0,0x1d6ac,87440,87504,0x1d6b0,0x1d6bc,87648,87584,0x1d6c0,0x1d6cc,87792,87728,0x1d6d0,0x1d6dc,87872,87808,0x1d6e0,0x1d6ec,88016,87952,0x1d6f0,0x1d6fc,86064,86120,0x1d700,0x1d70c,86176,86264,0x1d710,0x1d71c,86288,86344,0x1d720,0x1d72c,86400,86488,0x1d730,0x1d73c,86640,86568,0x1d740,0x1d74c,86752,86712,0x1d750,0x1d75c,86864,86792,0x1d760,0x1d76c,86976,86936,0x1d770,0x1d77c,87088,87144,0x1d780,0x1d78c,87200,87288,0x1d790,0x1d79c,87312,87368,0x1d7a0,0x1d7ac,87424,87512,0x1d7b0,0x1d7bc,87664,87592,0x1d7c0,0x1d7cc,87776,87736,0x1d7d0,0x1d7dc,87888,87816,0x1d7e0,0x1d7ec,88000,87960,0x1d7f0,0x1d7fc,86080,86128,0x1d800,0x1d80c,86224,86240,0x1d810,0x1d81c,86368,86352,0x1d820,0x1d82c,86512,86464,0x1d830,0x1d83c,86528,86576,0x1d840,0x1d84c,86672,86688,0x1d850,0x1d85c,86816,86800,0x1d860,0x1d86c,86960,86912,0x1d870,0x1d87c,87104,87152,0x1d880,0x1d88c,87248,87264,0x1d890,0x1d89c,87392,87376,0x1d8a0,0x1d8ac,87536,87488,0x1d8b0,0x1d8bc,87552,87600,0x1d8c0,0x1d8cc,87696,87712,0x1d8d0,0x1d8dc,87840,87824,0x1d8e0,0x1d8ec,87984,87936,0x1d8f0,0x1d8fc,86096,86136,0x1d900,0x1d90c,86208,86248,0x1d910,0x1d91c,86384,86360,0x1d920,0x1d92c,86496,86472,0x1d930,0x1d93c,86544,86584,0x1d940,0x1d94c,86656,86696,0x1d950,0x1d95c,86832,86808,0x1d960,0x1d96c,86944,86920,0x1d970,0x1d97c,87120,87160,0x1d980,0x1d98c,87232,87272,0x1d990,0x1d99c,87408,87384,0x1d9a0,0x1d9ac,87520,87496,0x1d9b0,0x1d9bc,87568,87608,0x1d9c0,0x1d9cc,87680,87720,0x1d9d0,0x1d9dc,87856,87832,0x1d9e0,0x1d9ec,87968,87944,0x1d9f0,0x1d9fc,0x1e780,0x1e780,0x1ea40,0x100000,0x1e782,0x100000,0x100000,0x1ea40,0x1e784,0x100000,0x100000,0x100000,0x1e786,0x100000,0x100000,0x100000,0x1e790,0x1e780,0x100000,0x100000,0x1e792,0x100000,0x100000,0x1ea40,0x1e794,0x100000,0x100000,0x100000,0x1e796,0x100000,0x100000,0x100000,0x1e7a0,0x1e780,0x100000,0x100000,0x1e7a2,0x100000,0x100000,0x1ea40,0x1e7a4,0x100000,0x100000,0x100000,0x1e7a6,0x100000,0x100000,0x100000,0x1e7b0,0x1e780,0x100000,0x100000,0x1e7b2,0x100000,0x100000,0x1ea40,0x1e7b4,0x100000,0x100000,0x100000,0x1e7b6,0x100000,0x100000,0x100000,0x1e788,0x1e790,0x1ea70,0x100000,0x1e78a,0x100000,0x100000,0x1ea70,0x1e78c,0x100000,0x100000,0x100000,0x1e78e,0x100000,0x100000,0x100000,0x1e798,0x1e790,0x100000,0x100000,0x1e79a,0x100000,0x100000,0x1ea70,0x1e79c,0x100000,0x100000,0x100000,0x1e79e,0x100000,0x100000,0x100000,0x1e7a8,0x1e790,0x100000,0x100000,0x1e7aa,0x100000,0x100000,0x1ea70,0x1e7ac,0x100000,0x100000,0x100000,0x1e7ae,0x100000,0x100000,0x100000,0x1e7b8,0x1e790,0x100000,0x100000,0x1e7ba,0x100000,0x100000,0x1ea70,0x1e7bc,0x100000,0x100000,0x100000,0x1e7be,0x100000,0x100000,0x100000,0x1e7c0,0x1e7a0,0x1eaa0,0x100000,0x1e7c2,0x100000,0x100000,0x1eaa0,0x1e7c4,0x100000,0x100000,0x100000,0x1e7c6,0x100000,0x100000,0x100000,0x1e7d0,0x1e7a0,0x100000,0x100000,0x1e7d2,0x100000,0x100000,0x1eaa0,0x1e7d4,0x100000,0x100000,0x100000,0x1e7d6,0x100000,0x100000,0x100000,0x1e7e0,0x1e7a0,0x100000,0x100000,0x1e7e2,0x100000,0x100000,0x1eaa0,0x1e7e4,0x100000,0x100000,0x100000,0x1e7e6,0x100000,0x100000,0x100000,0x1e7f0,0x1e7a0,0x100000,0x100000,0x1e7f2,0x100000,0x100000,0x1eaa0,0x1e7f4,0x100000,0x100000,0x100000,0x1e7f6,0x100000,0x100000,0x100000,0x1e7c8,0x1e7b0,0x1ead0,0x100000,0x1e7ca,0x100000,0x100000,0x1ead0,0x1e7cc,0x100000,0x100000,0x100000,0x1e7ce,0x100000,0x100000,0x100000,0x1e7d8,0x1e7b0,0x100000,0x100000,0x1e7da,0x100000,0x100000,0x1ead0,0x1e7dc,0x100000,0x100000,0x100000,0x1e7de,0x100000,0x100000,0x100000,0x1e7e8,0x1e7b0,0x100000,0x100000,0x1e7ea,0x100000,0x100000,0x1ead0,0x1e7ec,0x100000,0x100000,0x100000,0x1e7ee,0x100000,0x100000,0x100000,0x1e7f8,0x1e7b0,0x100000,0x100000,0x1e7fa,0x100000,0x100000,0x1ead0,0x1e7fc,0x100000,0x100000,0x100000,0x1e7fe,0x100000,0x100000,0x100000,0x1ec20,0x25cf0,37152,37216,0x1ec22,0x25cf2,37168,37224,0x1ec24,0x25cf4,37184,37232,0x1ec26,0x25cf6,37200,37240,0x1ec28,0x25e50,37536,37600,0x1ec2a,0x25e52,37552,37608,0x1ec2c,0x25e54,37568,37616,0x1ec2e,0x25e56,37584,37624,0x1ec30,0x25fb0,37888,37952,0x1ec32,0x25fb2,37904,37960,0x1ec34,0x25fb4,37984,37968,0x1ec36,0x25fb6,38000,37976,0x1ec38,0x26110,38272,38336,0x1ec3a,0x26112,38288,38344,0x1ec3c,0x26114,38368,38352,0x1ec3e,0x26116,38384,38360,0x1ec20,0x25cf8,38752,38688,0x1ec22,0x25cfa,38768,38696,0x1ec24,0x25cfc,38656,38704,0x1ec26,0x25cfe,38672,38712,0x1ec28,0x25e58,39136,39072,0x1ec2a,0x25e5a,39152,39080,0x1ec2c,0x25e5c,39040,39088,0x1ec2e,0x25e5e,39056,39096,0x1ec30,0x25fb8,39488,39424,0x1ec32,0x25fba,39504,39432,0x1ec34,0x25fbc,39456,39440,0x1ec36,0x25fbe,39472,39448,0x1ec38,0x26118,39872,39808,0x1ec3a,0x2611a,39888,39816,0x1ec3c,0x2611c,39840,39824,0x1ec3e,0x2611e,39856,39832,0x1ec20,0x267f8,40224,40288,0x1ec22,0x267fa,40240,40296,0x1ec24,0x267fc,40256,40304,0x1ec26,0x267fe,40272,40312,0x1ec28,0x26958,40608,40672,0x1ec2a,0x2695a,40624,40680,0x1ec2c,0x2695c,40640,40688,0x1ec2e,0x2695e,40656,40696,0x1ec30,0x26ab8,40960,41024,0x1ec32,0x26aba,40976,41032,0x1ec34,0x26abc,41056,41040,0x1ec36,0x26abe,41072,41048,0x1ec38,0x26c18,41344,41408,0x1ec3a,0x26c1a,41360,41416,0x1ec3c,0x26c1c,41440,41424,0x1ec3e,0x26c1e,41456,41432,0x1ec20,0x100000,41824,41760,0x1ec22,0x100000,41840,41768,0x1ec24,0x100000,41728,41776,0x1ec26,0x100000,41744,41784,0x1ec28,0x100000,42208,42144,0x1ec2a,0x100000,42224,42152,0x1ec2c,0x100000,42112,42160,0x1ec2e,0x100000,42128,42168,0x1ec30,0x100000,42560,42496,0x1ec32,0x100000,42576,42504,0x1ec34,0x100000,42528,42512,0x1ec36,0x100000,42544,42520,0x1ec38,0x100000,42944,42880,0x1ec3a,0x100000,42960,42888,0x1ec3c,0x100000,42912,42896,0x1ec3e,0x100000,42928,42904,37152,37184,37216,64512,37160,37192,37224,64528,37168,37200,37232,64544,37176,37208,37240,64560,38304,38336,38368,64576,38312,38344,38376,64592,38320,38352,38384,64608,38328,38360,38392,64624,39424,39520,39488,64640,39432,39528,39496,64656,39440,39536,39504,64672,39448,39544,39512,64688,40576,40672,40640,64704,40584,40680,40648,64720,40592,40688,40656,64736,40600,40696,40664,64752,46432,46336,46368,64768,46440,46344,46376,64784,46448,46352,46384,64800,46456,46360,46392,64816,47584,47488,47520,64832,47592,47496,47528,64848,47600,47504,47536,64864,47608,47512,47544,64880,48704,48672,48640,64896,48712,48680,48648,64912,48720,48688,48656,64928,48728,48696,48664,64944,49856,49824,49792,64960,49864,49832,49800,64976,49872,49840,49808,64992,49880,49848,49816,65008,55584,55616,55648,65024,55592,55624,55656,65040,55600,55632,55664,65056,55608,55640,55672,65072,56736,56768,56800,65088,56744,56776,56808,65104,56752,56784,56816,65120,56760,56792,56824,65136,57856,57952,57920,65152,57864,57960,57928,65168,57872,57968,57936,65184,57880,57976,57944,65200,59008,59104,59072,65216,59016,59112,59080,65232,59024,59120,59088,65248,59032,59128,59096,65264,64864,64768,64800,65280,64872,64776,64808,65296,64880,64784,64816,65312,64888,64792,64824,65328,66016,65920,65952,65344,66024,65928,65960,65360,66032,65936,65968,65376,66040,65944,65976,65392,67136,67104,67072,65408,67144,67112,67080,65424,67152,67120,67088,65440,67160,67128,67096,65456,68288,68256,68224,65472,68296,68264,68232,65488,68304,68272,68240,65504,68312,68280,68248,65520,0x1e800,0x1e800,0x20cc0,55296,0x1e8c4,0x1e8c4,0x20cc4,55312,0x1e988,0x1e988,0x20cc8,55328,0x1ea4c,0x1ea4c,0x20ccc,55344,0x1eb10,0x1eb10,0x20cd0,55360,0x1ebd4,0x1ebd4,0x20cd4,55376,0x1ec98,0x1ec98,0x20cd8,55392,0x1ed5c,0x1ed5c,0x20cdc,55408,0x1ee20,0x1ee20,0x20ce0,55680,0x1eee4,0x1eee4,0x20ce4,55696,0x1efa8,0x1efa8,0x20ce8,55712,0x1f06c,0x1f06c,0x20cec,55728,0x1f130,0x1f130,0x20cf0,55744,0x1f1f4,0x1f1f4,0x20cf4,55760,0x1f2b8,0x1f2b8,0x20cf8,55776,0x1f37c,0x1f37c,0x20cfc,55792,0x1e810,0x1e800,0x20cc0,56064,0x1e8d4,0x1e8c4,0x20cc4,56080,0x1e998,0x1e988,0x20cc8,56096,0x1ea5c,0x1ea4c,0x20ccc,56112,0x1eb20,0x1eb10,0x20cd0,56128,0x1ebe4,0x1ebd4,0x20cd4,56144,0x1eca8,0x1ec98,0x20cd8,56160,0x1ed6c,0x1ed5c,0x20cdc,56176,0x1ee30,0x1ee20,0x20ce0,56448,0x1eef4,0x1eee4,0x20ce4,56464,0x1efb8,0x1efa8,0x20ce8,56480,0x1f07c,0x1f06c,0x20cec,56496,0x1f140,0x1f130,0x20cf0,56512,0x1f204,0x1f1f4,0x20cf4,56528,0x1f2c8,0x1f2b8,0x20cf8,56544,0x1f38c,0x1f37c,0x20cfc,56560,0x1e820,0x1e800,0x20cc0,56832,0x1e8e4,0x1e8c4,0x20cc4,56848,0x1e9a8,0x1e988,0x20cc8,56864,0x1ea6c,0x1ea4c,0x20ccc,56880,0x1eb30,0x1eb10,0x20cd0,56896,0x1ebf4,0x1ebd4,0x20cd4,56912,0x1ecb8,0x1ec98,0x20cd8,56928,0x1ed7c,0x1ed5c,0x20cdc,56944,0x1ee40,0x1ee20,0x20ce0,57216,0x1ef04,0x1eee4,0x20ce4,57232,0x1efc8,0x1efa8,0x20ce8,57248,0x1f08c,0x1f06c,0x20cec,57264,0x1f150,0x1f130,0x20cf0,57280,0x1f214,0x1f1f4,0x20cf4,57296,0x1f2d8,0x1f2b8,0x20cf8,57312,0x1f39c,0x1f37c,0x20cfc,57328,0x1e830,0x1e800,0x20cc0,57600,0x1e8f4,0x1e8c4,0x20cc4,57616,0x1e9b8,0x1e988,0x20cc8,57632,0x1ea7c,0x1ea4c,0x20ccc,57648,0x1eb40,0x1eb10,0x20cd0,57664,0x1ec04,0x1ebd4,0x20cd4,57680,0x1ecc8,0x1ec98,0x20cd8,57696,0x1ed8c,0x1ed5c,0x20cdc,57712,0x1ee50,0x1ee20,0x20ce0,57984,0x1ef14,0x1eee4,0x20ce4,58000,0x1efd8,0x1efa8,0x20ce8,58016,0x1f09c,0x1f06c,0x20cec,58032,0x1f160,0x1f130,0x20cf0,58048,0x1f224,0x1f1f4,0x20cf4,58064,0x1f2e8,0x1f2b8,0x20cf8,58080,0x1f3ac,0x1f37c,0x20cfc,58096,92160,0x27070,0x1ea40,0x1ec20,92176,0x27074,0x1eed0,0x1f0b0,92192,0x27078,0x1f360,0x1f540,92208,0x2707c,0x1f7f0,0x1f9d0,92224,0x27080,0x1fc80,0x1fe60,92240,0x27084,0x20110,0x202f0,92256,0x27088,0x205a0,0x20780,92272,0x2708c,0x20a30,0x20c10,92544,0x27090,0x20ec0,0x210a0,92560,0x27094,0x21350,0x21530,92576,0x27098,0x217e0,0x219c0,92592,0x2709c,0x21c70,0x21e50,92608,0x270a0,0x22100,0x222e0,92624,0x270a4,0x22590,0x22770,92640,0x270a8,0x22a20,0x22c00,92656,0x270ac,0x22eb0,0x23090,92960,0x270b0,0x1ea70,0x1ec28,92976,0x270b4,0x1ef00,0x1f0b8,92928,0x270b8,0x1f390,0x1f548,92944,0x270bc,0x1f820,0x1f9d8,93024,0x270c0,0x1fcb0,0x1fe68,93040,0x270c4,0x20140,0x202f8,92992,0x270c8,0x205d0,0x20788,93008,0x270cc,0x20a60,0x20c18,93344,0x270d0,0x20ef0,0x210a8,93360,0x270d4,0x21380,0x21538,93312,0x270d8,0x21810,0x219c8,93328,0x270dc,0x21ca0,0x21e58,93408,0x270e0,0x22130,0x222e8,93424,0x270e4,0x225c0,0x22778,93376,0x270e8,0x22a50,0x22c08,93392,0x270ec,0x22ee0,0x23098,93760,0x270f0,0x1eaa0,0x1ec30,93776,0x270f4,0x1ef30,0x1f0c0,93792,0x270f8,0x1f3c0,0x1f550,93808,0x270fc,0x1f850,0x1f9e0,93696,0x27100,0x1fce0,0x1fe70,93712,0x27104,0x20170,0x20300,93728,0x27108,0x20600,0x20790,93744,0x2710c,0x20a90,0x20c20,94144,0x27110,0x20f20,0x210b0,94160,0x27114,0x213b0,0x21540,94176,0x27118,0x21840,0x219d0,94192,0x2711c,0x21cd0,0x21e60,94080,0x27120,0x22160,0x222f0,94096,0x27124,0x225f0,0x22780,94112,0x27128,0x22a80,0x22c10,94128,0x2712c,0x22f10,0x230a0,94560,0x27130,0x1ead0,0x1ec38,94576,0x27134,0x1ef60,0x1f0c8,94528,0x27138,0x1f3f0,0x1f558,94544,0x2713c,0x1f880,0x1f9e8,94496,0x27140,0x1fd10,0x1fe78,94512,0x27144,0x201a0,0x20308,94464,0x27148,0x20630,0x20798,94480,0x2714c,0x20ac0,0x20c28,94944,0x27150,0x20f50,0x210b8,94960,0x27154,0x213e0,0x21548,94912,0x27158,0x21870,0x219d8,94928,0x2715c,0x21d00,0x21e68,94880,0x27160,0x22190,0x222f8,94896,0x27164,0x22620,0x22788,94848,0x27168,0x22ab0,0x22c18,94864,0x2716c,0x22f40,0x230a8,0x3f803f80,0x3f803f80,16256,0,0,0x3f803f80,0x3f800000,0,0,0x3f803f80,0,16256,0,0x3f803f80,0,0x3f800000,0x3f803f80,0,0,0,0,0,0,0,0,0,0,0,0,0,0,0,0x3f803f80,0,0,0,0,0,0,0,0,0,0,0,0,0,0,0,0x3f803f80,0,0,0,0,0,0,0,0,0,0,0,0,0,0,0,0x3f803f80,0,0,0,0,0,0,0,0,0,0,0,0,0,0,0,0x3f803f80,0x3f803f80,16256,0,0,0x3f803f80,0x3f800000,0,0,0x3f803f80,0,16256,0,0x3f803f80,0,0x3f800000,0x3f803f80,0,0,0,0,0,0,0,0,0,0,0,0,0,0,0,0x3f803f80,0,0,0,0,0,0,0,0,0,0,0,0,0,0,0,0x3f803f80,0,0,0,0,0,0,0,0,0,0,0,0,0,0,0,0x3f803f80,0,0,0,0,0,0,0,0,0,0,0,0,0,0,0,0x3f803f80,0x3f803f80,16256,0,0,0x3f803f80,0x3f800000,0,0,0x3f803f80,0,16256,0,0x3f803f80,0,0x3f800000,0x3f803f80,0,0,0,0,0,0,0,0,0,0,0,0,0,0,0,0x3f803f80,0,0,0,0,0,0,0,0,0,0,0,0,0,0,0,0x3f803f80,0,0,0,0,0,0,0,0,0,0,0,0,0,0,0,0x3f803f80,0,0,0,0,0,0,0,0,0,0,0,0,0,0,0,0x3f803f80,0x3f803f80,16256,0,0,0x3f803f80,0x3f800000,0,0,0x3f803f80,0,16256,0,0x3f803f80,0,0x3f800000,0xffffffff,0xffffffff,0,0,0,0,0xffffffff,0xff000000,0xff000000,0xffffff,0xff000000,0xff000000,0,4096,16384,0x1c000,0xffffffff,0xffffffff,0,0,0,0,0xffffff,0xffff0000,0xffff0000,65535,0xffffff,0xffffff,1024,5120,16896,0x1c0a0,0xffffffff,0xffffffff,0,0,0,0,65535,0xffffff00,0xff000000,0xffffff,0xffff0000,0xffff0000,2048,10240,17408,0x1c140,0xffffffff,0,0,0,0,0,255,0xffffffff,0xffff0000,65535,65535,65535,3072,11264,17920,0x1c1d0,0,0x3fb8aa3b,0x2a8cbccc,0x38fba882,0x7fff7fff,0x415db3d7,0xffff0000,0,0,65535,0,0xffff0000,0,0,0,0,0,808,1800,2608,3600,4408,5400,6208,7200,8192,9000,9992,10800,11792,12600,18432,14385,18447,16170,18462,18462,18462,18462,18462,20,19,0,0,0,0,0,0,0x1b0000,0x5b8480,0xae0a80,0xee8f00,0x1411500,0x1819980,0x1d41f80,0x214a400,0x2672a00,0x2b9b000,0x2fa3480,0x34cba80,0x38d3f00,0x3dfc500,0x4204980,0x60a80,0x4b2b680,0x6a800,0x5452380,0x74580,0x74580,0x74580,0x74580,0x74580,0x74580,0x74580,0x74580,0x74580,0x74580,0x74580,0x74580,0x74580,1,809,1801,2609,3601,4409,5401,6209,7201,8193,9001,9993,10801,11793,12601,13592,14386,15377,16171,18463,18463,18463,18463,18463,20,19,0,0,0,0,0,0,0x1b0a80,0x5b8f00,0xae1500,0xee9980,0x1411f80,0x181a400,0x1d42a00,0x214ae80,0x2673480,0x2b9ba80,0x2fa3f00,0x34cc500,0x38d4980,0x3dfcf80,0x4205400,0x472cf80,0x4b2c100,0x5053c80,0x5452e00,0x75000,0x75000,0x75000,0x75000,0x75000,0x75000,0x75000,0x75000,0x75000,0x75000,0x75000,0x75000,0x75000,2,810,1802,2610,3602,4410,5402,6210,7202,8194,9002,9994,10802,11794,12602,13593,14387,15378,16172,17162,17671,18216,18216,18216,22,21,0,0,0,0,0,0,0x1b1500,0x5b9980,0xae1f80,0xeea400,0x1412a00,0x181ae80,0x1d43480,0x214b900,0x2673f00,0x2b9c500,0x2fa4980,0x34ccf80,0x38d5400,0x3dfda00,0x4205e80,0x472da00,0x4b2cb80,0x5054700,0x5453880,0x597a980,0x5c17500,0x5ecba80,0x5ecba80,0x5ecba80,0x5ecba80,0x5ecba80,0x5ecba80,0x5ecba80,0x5ecba80,0x5ecba80,0x5ecba80,0x5ecba80,3,811,1803,2611,3603,4411,5403,6211,7203,8195,9003,9995,10803,11795,12603,13594,14388,15379,16173,17163,17672,17672,17672,17672,21,20,0,0,0,0,0,0,0x1b1f80,0x5ba400,0xae2a00,0xeeae80,0x1413480,0x181b900,0x1d43f00,0x214c380,0x2674980,0x2b9cf80,0x2fa5400,0x34cda00,0x38d5e80,0x3dfe480,0x4206900,0x472e480,0x4b2d600,0x5055180,0x5454300,0x597b400,0x5c17f80,0x5c17f80,0x5c17f80,0x5c17f80,0x5c17f80,0x5c17f80,0x5c17f80,0x5c17f80,0x5c17f80,0x5c17f80,0x5c17f80,0x5c17f80,4,812,1804,2612,3604,4412,5404,6212,7204,8196,9004,9996,10804,11796,12604,13595,14389,15380,16174,18464,17673,17673,17673,17673,21,20,0,0,0,0,0,0,0x1b2a00,0x5bae80,0xae3480,0xeeb900,0x1413f00,0x181c380,0x1d44980,0x214ce00,0x2675400,0x2b9da00,0x2fa5e80,0x34ce480,0x38d6900,0x3dfef00,0x4207380,0x472ef00,0x4b2e080,0x5055c00,0x5454d80,0x75a80,0x5c18a00,0x5c18a00,0x5c18a00,0x5c18a00,0x5c18a00,0x5c18a00,0x5c18a00,0x5c18a00,0x5c18a00,0x5c18a00,0x5c18a00,0x5c18a00,5,813,1805,2613,3605,4413,5405,6213,7205,8197,9005,9997,10805,11797,12605,13596,14390,15381,16175,18465,18465,18465,18465,18465,20,19,0,0,0,0,0,0,0x1b3480,0x5bb900,0xae3f00,0xeec380,0x1414980,0x181ce00,0x1d45400,0x214d880,0x2675e80,0x2b9e480,0x2fa6900,0x34cef00,0x38d7380,0x3dff980,0x4207e00,0x472f980,0x4b2eb00,0x5056680,0x5455800,0x76500,0x76500,0x76500,0x76500,0x76500,0x76500,0x76500,0x76500,0x76500,0x76500,0x76500,0x76500,0x76500,6,814,1806,2614,3606,4414,5406,6214,7206,8198,9006,9998,10806,11798,12606,13597,14391,15382,16176,18466,17674,17674,17674,17674,21,20,0,0,0,0,0,0,0x1b3f00,0x5bc380,0xae4980,0xeece00,0x1415400,0x181d880,0x1d45e80,0x214e300,0x2676900,0x2b9ef00,0x2fa7380,0x34cf980,0x38d7e00,0x3e00400,0x4208880,0x4730400,0x4b2f580,0x5057100,0x5456280,0x76f80,0x5c19480,0x5c19480,0x5c19480,0x5c19480,0x5c19480,0x5c19480,0x5c19480,0x5c19480,0x5c19480,0x5c19480,0x5c19480,0x5c19480,7,815,1807,2615,3607,4415,5407,6215,7207,8199,9007,9999,10807,11799,12607,13598,14392,15383,16177,17164,17675,17675,17675,17675,21,20,0,0,0,0,0,0,0x1b4980,0x5bce00,0xae5400,0xeed880,0x1415e80,0x181e300,0x1d46900,0x214ed80,0x2677380,0x2b9f980,0x2fa7e00,0x34d0400,0x38d8880,0x3e00e80,0x4209300,0x4730e80,0x4b30000,0x5057b80,0x5456d00,0x597be80,0x5c19f00,0x5c19f00,0x5c19f00,0x5c19f00,0x5c19f00,0x5c19f00,0x5c19f00,0x5c19f00,0x5c19f00,0x5c19f00,0x5c19f00,0x5c19f00,8,816,1808,2616,3608,4416,5408,6400,7208,8200,9008,10000,10808,11800,12608,18433,14393,18448,16178,18467,18467,18467,18467,18467,20,19,0,0,0,0,0,0,0x1b5400,0x5bd880,0xae5e80,0xeee300,0x1416900,0x181ed80,0x1d47380,0x226f980,0x2677e00,0x2ba0400,0x2fa8880,0x34d0e80,0x38d9300,0x3e01900,0x4209d80,0x61500,0x4b30a80,0x6b280,0x5457780,0x77a00,0x77a00,0x77a00,0x77a00,0x77a00,0x77a00,0x77a00,0x77a00,0x77a00,0x77a00,0x77a00,0x77a00,0x77a00,9,817,1809,2617,3609,4417,5409,6401,7209,8201,9009,10001,10809,11801,12609,13599,14394,15384,16179,18468,18468,18468,18468,18468,20,19,0,0,0,0,0,0,0x1b5e80,0x5be300,0xae6900,0xeeed80,0x1417380,0x181f800,0x1d47e00,0x2270400,0x2678880,0x2ba0e80,0x2fa9300,0x34d1900,0x38d9d80,0x3e02380,0x420a800,0x4731900,0x4b31500,0x5058600,0x5458200,0x78480,0x78480,0x78480,0x78480,0x78480,0x78480,0x78480,0x78480,0x78480,0x78480,0x78480,0x78480,0x78480,10,818,1810,2618,3610,4418,5410,6402,7210,8202,9010,10002,10810,11802,12610,13600,14395,15385,16180,17165,17676,18217,18217,18217,22,21,0,0,0,0,0,0,0x1b6900,0x5bed80,0xae7380,0xeef800,0x1417e00,0x1820280,0x1d48880,0x2270e80,0x2679300,0x2ba1900,0x2fa9d80,0x34d2380,0x38da800,0x3e02e00,0x420b280,0x4732380,0x4b31f80,0x5059080,0x5458c80,0x597c900,0x5c1a980,0x5ecc500,0x5ecc500,0x5ecc500,0x5ecc500,0x5ecc500,0x5ecc500,0x5ecc500,0x5ecc500,0x5ecc500,0x5ecc500,0x5ecc500,11,819,1811,2619,3611,4419,5411,6403,7211,8203,9011,10003,10811,11803,12611,13601,14396,15386,16181,17166,17677,17677,17677,17677,21,20,0,0,0,0,0,0,0x1b7380,0x5bf800,0xae7e00,0xef0280,0x1418880,0x1820d00,0x1d49300,0x2271900,0x2679d80,0x2ba2380,0x2faa800,0x34d2e00,0x38db280,0x3e03880,0x420bd00,0x4732e00,0x4b32a00,0x5059b00,0x5459700,0x597d380,0x5c1b400,0x5c1b400,0x5c1b400,0x5c1b400,0x5c1b400,0x5c1b400,0x5c1b400,0x5c1b400,0x5c1b400,0x5c1b400,0x5c1b400,0x5c1b400,12,820,1812,2620,3612,4420,5412,6404,7212,8204,9012,10004,10812,11804,12612,13602,14397,15387,16182,18469,17678,17678,17678,17678,21,20,0,0,0,0,0,0,0x1b7e00,0x5c0280,0xae8880,0xef0d00,0x1419300,0x1821780,0x1d49d80,0x2272380,0x267a800,0x2ba2e00,0x2fab280,0x34d3880,0x38dbd00,0x3e04300,0x420c780,0x4733880,0x4b33480,0x505a580,0x545a180,0x78f00,0x5c1be80,0x5c1be80,0x5c1be80,0x5c1be80,0x5c1be80,0x5c1be80,0x5c1be80,0x5c1be80,0x5c1be80,0x5c1be80,0x5c1be80,0x5c1be80,13,821,1813,2621,3613,4421,5413,6405,7213,8205,9013,10005,10813,11805,12613,13603,14398,15388,16183,18470,18470,18470,18470,18470,20,19,0,0,0,0,0,0,0x1b8880,0x5c0d00,0xae9300,0xef1780,0x1419d80,0x1822200,0x1d4a800,0x2272e00,0x267b280,0x2ba3880,0x2fabd00,0x34d4300,0x38dc780,0x3e04d80,0x420d200,0x4734300,0x4b33f00,0x505b000,0x545ac00,0x79980,0x79980,0x79980,0x79980,0x79980,0x79980,0x79980,0x79980,0x79980,0x79980,0x79980,0x79980,0x79980,14,822,1814,2622,3614,4422,5414,6406,7214,8206,9014,10006,10814,11806,12614,13604,14399,15389,16184,18471,17679,17679,17679,17679,21,20,0,0,0,0,0,0,0x1b9300,0x5c1780,0xae9d80,0xef2200,0x141a800,0x1822c80,0x1d4b280,0x2273880,0x267bd00,0x2ba4300,0x2fac780,0x34d4d80,0x38dd200,0x3e05800,0x420dc80,0x4734d80,0x4b34980,0x505ba80,0x545b680,0x7a400,0x5c1c900,0x5c1c900,0x5c1c900,0x5c1c900,0x5c1c900,0x5c1c900,0x5c1c900,0x5c1c900,0x5c1c900,0x5c1c900,0x5c1c900,0x5c1c900,15,823,1815,2623,3615,4423,5415,6407,7215,8207,9015,10007,10815,11807,12615,13605,14400,15390,16185,17167,17680,17680,17680,17680,21,20,0,0,0,0,0,0,0x1b9d80,0x5c2200,0xaea800,0xef2c80,0x141b280,0x1823700,0x1d4bd00,0x2274300,0x267c780,0x2ba4d80,0x2fad200,0x34d5800,0x38ddc80,0x3e06280,0x420e700,0x4735800,0x4b35400,0x505c500,0x545c100,0x597de00,0x5c1d380,0x5c1d380,0x5c1d380,0x5c1d380,0x5c1d380,0x5c1d380,0x5c1d380,0x5c1d380,0x5c1d380,0x5c1d380,0x5c1d380,0x5c1d380,16,824,1816,2624,3616,4608,5416,6408,7216,8208,9016,10008,10816,11808,12800,18434,14401,18449,16186,18472,18472,18472,18472,18472,20,19,0,0,0,0,0,0,0x1ba800,0x5c2c80,0xaeb280,0xef3700,0x141bd00,0x1944300,0x1d4c780,0x2274d80,0x267d200,0x2ba5800,0x2fadc80,0x34d6280,0x38de700,0x3e06d00,0x432f300,0x61f80,0x4b35e80,0x6bd00,0x545cb80,0x7ae80,0x7ae80,0x7ae80,0x7ae80,0x7ae80,0x7ae80,0x7ae80,0x7ae80,0x7ae80,0x7ae80,0x7ae80,0x7ae80,0x7ae80,17,825,1817,2625,3617,4609,5417,6409,7217,8209,9017,10009,10817,11809,12801,13606,14402,15391,16187,18473,18473,18473,18473,18473,20,19,0,0,0,0,0,0,0x1bb280,0x5c3700,0xaebd00,0xef4180,0x141c780,0x1944d80,0x1d4d200,0x2275800,0x267dc80,0x2ba6280,0x2fae700,0x34d6d00,0x38df180,0x3e07780,0x432fd80,0x4736280,0x4b36900,0x505cf80,0x545d600,0x7b900,0x7b900,0x7b900,0x7b900,0x7b900,0x7b900,0x7b900,0x7b900,0x7b900,0x7b900,0x7b900,0x7b900,0x7b900,18,826,1818,2626,3618,4610,5418,6410,7218,8210,9018,10010,10818,11810,12802,13607,14403,15392,16188,17168,17681,18218,18218,18218,22,21,0,0,0,0,0,0,0x1bbd00,0x5c4180,0xaec780,0xef4c00,0x141d200,0x1945800,0x1d4dc80,0x2276280,0x267e700,0x2ba6d00,0x2faf180,0x34d7780,0x38dfc00,0x3e08200,0x4330800,0x4736d00,0x4b37380,0x505da00,0x545e080,0x597e880,0x5c1de00,0x5eccf80,0x5eccf80,0x5eccf80,0x5eccf80,0x5eccf80,0x5eccf80,0x5eccf80,0x5eccf80,0x5eccf80,0x5eccf80,0x5eccf80,19,827,1819,2627,3619,4611,5419,6411,7219,8211,9019,10011,10819,11811,12803,13608,14404,15393,16189,17169,17682,17682,17682,17682,21,20,0,0,0,0,0,0,0x1bc780,0x5c4c00,0xaed200,0xef5680,0x141dc80,0x1946280,0x1d4e700,0x2276d00,0x267f180,0x2ba7780,0x2fafc00,0x34d8200,0x38e0680,0x3e08c80,0x4331280,0x4737780,0x4b37e00,0x505e480,0x545eb00,0x597f300,0x5c1e880,0x5c1e880,0x5c1e880,0x5c1e880,0x5c1e880,0x5c1e880,0x5c1e880,0x5c1e880,0x5c1e880,0x5c1e880,0x5c1e880,0x5c1e880,20,828,1820,2628,3620,4612,5420,6412,7220,8212,9020,10012,10820,11812,12804,13609,14405,15394,16190,18474,17683,17683,17683,17683,21,20,0,0,0,0,0,0,0x1bd200,0x5c5680,0xaedc80,0xef6100,0x141e700,0x1946d00,0x1d4f180,0x2277780,0x267fc00,0x2ba8200,0x2fb0680,0x34d8c80,0x38e1100,0x3e09700,0x4331d00,0x4738200,0x4b38880,0x505ef00,0x545f580,0x7c380,0x5c1f300,0x5c1f300,0x5c1f300,0x5c1f300,0x5c1f300,0x5c1f300,0x5c1f300,0x5c1f300,0x5c1f300,0x5c1f300,0x5c1f300,0x5c1f300,21,829,1821,2629,3621,4613,5421,6413,7221,8213,9021,10013,10821,11813,12805,13610,14406,15395,16191,18475,18475,18475,18475,18475,20,19,0,0,0,0,0,0,0x1bdc80,0x5c6100,0xaee700,0xef6b80,0x141f180,0x1947780,0x1d4fc00,0x2278200,0x2680680,0x2ba8c80,0x2fb1100,0x34d9700,0x38e1b80,0x3e0a180,0x4332780,0x4738c80,0x4b39300,0x505f980,0x5460000,0x7ce00,0x7ce00,0x7ce00,0x7ce00,0x7ce00,0x7ce00,0x7ce00,0x7ce00,0x7ce00,0x7ce00,0x7ce00,0x7ce00,0x7ce00,22,830,1822,2630,3622,4614,5422,6414,7222,8214,9022,10014,10822,11814,12806,13611,14407,15396,16192,18476,17684,17684,17684,17684,21,20,0,0,0,0,0,0,0x1be700,0x5c6b80,0xaef180,0xef7600,0x141fc00,0x1948200,0x1d50680,0x2278c80,0x2681100,0x2ba9700,0x2fb1b80,0x34da180,0x38e2600,0x3e0ac00,0x4333200,0x4739700,0x4b39d80,0x5060400,0x5460a80,0x7d880,0x5c1fd80,0x5c1fd80,0x5c1fd80,0x5c1fd80,0x5c1fd80,0x5c1fd80,0x5c1fd80,0x5c1fd80,0x5c1fd80,0x5c1fd80,0x5c1fd80,0x5c1fd80,23,831,1823,2631,3623,4615,5423,6415,7223,8215,9023,10015,10823,11815,12807,13612,14592,15397,16193,17170,17685,17685,17685,17685,21,20,0,0,0,0,0,0,0x1bf180,0x5c7600,0xaefc00,0xef8080,0x1420680,0x1948c80,0x1d51100,0x2279700,0x2681b80,0x2baa180,0x2fb2600,0x34dac00,0x38e3080,0x3e0b680,0x4333c80,0x473a180,0x4c5a980,0x5060e80,0x5461500,0x597fd80,0x5c20800,0x5c20800,0x5c20800,0x5c20800,0x5c20800,0x5c20800,0x5c20800,0x5c20800,0x5c20800,0x5c20800,0x5c20800,0x5c20800,24,832,1824,2816,3624,4616,5424,6416,7224,8216,9024,10016,11008,11816,12808,18435,14593,18450,16194,18477,18477,18477,18477,18477,20,19,0,0,0,0,0,0,0x1bfc00,0x5c8080,0xaf0680,0x1018c80,0x1421100,0x1949700,0x1d51b80,0x227a180,0x2682600,0x2baac00,0x2fb3080,0x34db680,0x3a03c80,0x3e0c100,0x4334700,0x62a00,0x4c5b400,0x6c780,0x5461f80,0x7e300,0x7e300,0x7e300,0x7e300,0x7e300,0x7e300,0x7e300,0x7e300,0x7e300,0x7e300,0x7e300,0x7e300,0x7e300,25,833,1825,2817,3625,4617,5425,6417,7225,8217,9025,10017,11009,11817,12809,13613,14594,15398,16195,18478,18478,18478,18478,18478,20,19,0,0,0,0,0,0,0x1c0680,0x5c8b00,0xaf1100,0x1019700,0x1421b80,0x194a180,0x1d52600,0x227ac00,0x2683080,0x2bab680,0x2fb3b00,0x34dc100,0x3a04700,0x3e0cb80,0x4335180,0x473ac00,0x4c5be80,0x5061900,0x5462a00,0x7ed80,0x7ed80,0x7ed80,0x7ed80,0x7ed80,0x7ed80,0x7ed80,0x7ed80,0x7ed80,0x7ed80,0x7ed80,0x7ed80,0x7ed80,26,834,1826,2818,3626,4618,5426,6418,7226,8218,9026,10018,11010,11818,12810,13614,14595,15399,16196,17171,17686,18219,18219,18219,22,21,0,0,0,0,0,0,0x1c1100,0x5c9580,0xaf1b80,0x101a180,0x1422600,0x194ac00,0x1d53080,0x227b680,0x2683b00,0x2bac100,0x2fb4580,0x34dcb80,0x3a05180,0x3e0d600,0x4335c00,0x473b680,0x4c5c900,0x5062380,0x5463480,0x5980800,0x5c21280,0x5ecda00,0x5ecda00,0x5ecda00,0x5ecda00,0x5ecda00,0x5ecda00,0x5ecda00,0x5ecda00,0x5ecda00,0x5ecda00,0x5ecda00,27,835,1827,2819,3627,4619,5427,6419,7227,8219,9027,10019,11011,11819,12811,13615,14596,15400,16197,17172,17687,17687,17687,17687,21,20,0,0,0,0,0,0,0x1c1b80,0x5ca000,0xaf2600,0x101ac00,0x1423080,0x194b680,0x1d53b00,0x227c100,0x2684580,0x2bacb80,0x2fb5000,0x34dd600,0x3a05c00,0x3e0e080,0x4336680,0x473c100,0x4c5d380,0x5062e00,0x5463f00,0x5981280,0x5c21d00,0x5c21d00,0x5c21d00,0x5c21d00,0x5c21d00,0x5c21d00,0x5c21d00,0x5c21d00,0x5c21d00,0x5c21d00,0x5c21d00,0x5c21d00,28,836,1828,2820,3628,4620,5428,6420,7228,8220,9028,10020,11012,11820,12812,13616,14597,15401,16198,18479,17688,17688,17688,17688,21,20,0,0,0,0,0,0,0x1c2600,0x5caa80,0xaf3080,0x101b680,0x1423b00,0x194c100,0x1d54580,0x227cb80,0x2685000,0x2bad600,0x2fb5a80,0x34de080,0x3a06680,0x3e0eb00,0x4337100,0x473cb80,0x4c5de00,0x5063880,0x5464980,0x7f800,0x5c22780,0x5c22780,0x5c22780,0x5c22780,0x5c22780,0x5c22780,0x5c22780,0x5c22780,0x5c22780,0x5c22780,0x5c22780,0x5c22780,29,837,1829,2821,3629,4621,5429,6421,7229,8221,9029,10021,11013,11821,12813,13617,14598,15402,16199,18480,18480,18480,18480,18480,20,19,0,0,0,0,0,0,0x1c3080,0x5cb500,0xaf3b00,0x101c100,0x1424580,0x194cb80,0x1d55000,0x227d600,0x2685a80,0x2bae080,0x2fb6500,0x34deb00,0x3a07100,0x3e0f580,0x4337b80,0x473d600,0x4c5e880,0x5064300,0x5465400,0x80280,0x80280,0x80280,0x80280,0x80280,0x80280,0x80280,0x80280,0x80280,0x80280,0x80280,0x80280,0x80280,30,838,1830,2822,3630,4622,5430,6422,7230,8222,9030,10022,11014,11822,12814,13618,14599,15403,16384,18481,17689,17689,17689,17689,21,20,0,0,0,0,0,0,0x1c3b00,0x5cbf80,0xaf4580,0x101cb80,0x1425000,0x194d600,0x1d55a80,0x227e080,0x2686500,0x2baeb00,0x2fb6f80,0x34df580,0x3a07b80,0x3e10000,0x4338600,0x473e080,0x4c5f300,0x5064d80,0x5586000,0x80d00,0x5c23200,0x5c23200,0x5c23200,0x5c23200,0x5c23200,0x5c23200,0x5c23200,0x5c23200,0x5c23200,0x5c23200,0x5c23200,0x5c23200,31,839,1831,2823,3631,4623,5431,6423,7231,8223,9031,10023,11015,11823,12815,13619,14600,15404,16385,17173,17690,17690,17690,17690,21,20,0,0,0,0,0,0,0x1c4580,0x5cca00,0xaf5000,0x101d600,0x1425a80,0x194e080,0x1d56500,0x227eb00,0x2686f80,0x2baf580,0x2fb7a00,0x34e0000,0x3a08600,0x3e10a80,0x4339080,0x473eb00,0x4c5fd80,0x5065800,0x5586a80,0x5981d00,0x5c23c80,0x5c23c80,0x5c23c80,0x5c23c80,0x5c23c80,0x5c23c80,0x5c23c80,0x5c23c80,0x5c23c80,0x5c23c80,0x5c23c80,0x5c23c80,32,1024,1832,2824,3632,4624,5432,6424,7232,8224,9216,10024,11016,11824,12816,18436,14601,18451,16386,18482,18482,18482,18482,18482,20,19,0,0,0,0,0,0,0x1c5000,0x6ed600,0xaf5a80,0x101e080,0x1426500,0x194eb00,0x1d56f80,0x227f580,0x2687a00,0x2bb0000,0x30d8600,0x34e0a80,0x3a09080,0x3e11500,0x4339b00,0x63480,0x4c60800,0x6d200,0x5587500,0x81780,0x81780,0x81780,0x81780,0x81780,0x81780,0x81780,0x81780,0x81780,0x81780,0x81780,0x81780,0x81780,33,1025,1833,2825,3633,4625,5433,6425,7233,8225,9217,10025,11017,11825,12817,13620,14602,15405,16387,18483,18483,18483,18483,18483,20,19,0,0,0,0,0,0,0x1c5a80,0x6ee080,0xaf6500,0x101eb00,0x1426f80,0x194f580,0x1d57a00,0x2280000,0x2688480,0x2bb0a80,0x30d9080,0x34e1500,0x3a09b00,0x3e11f80,0x433a580,0x473f580,0x4c61280,0x5066280,0x5587f80,0x82200,0x82200,0x82200,0x82200,0x82200,0x82200,0x82200,0x82200,0x82200,0x82200,0x82200,0x82200,0x82200,34,1026,1834,2826,3634,4626,5434,6426,7234,8226,9218,10026,11018,11826,12818,13621,14603,15406,16388,17174,17691,18220,18220,18220,22,21,0,0,0,0,0,0,0x1c6500,0x6eeb00,0xaf6f80,0x101f580,0x1427a00,0x1950000,0x1d58480,0x2280a80,0x2688f00,0x2bb1500,0x30d9b00,0x34e1f80,0x3a0a580,0x3e12a00,0x433b000,0x4740000,0x4c61d00,0x5066d00,0x5588a00,0x5982780,0x5c24700,0x5ece480,0x5ece480,0x5ece480,0x5ece480,0x5ece480,0x5ece480,0x5ece480,0x5ece480,0x5ece480,0x5ece480,0x5ece480,35,1027,1835,2827,3635,4627,5435,6427,7235,8227,9219,10027,11019,11827,12819,13622,14604,15407,16389,17175,17692,17692,17692,17692,21,20,0,0,0,0,0,0,0x1c6f80,0x6ef580,0xaf7a00,0x1020000,0x1428480,0x1950a80,0x1d58f00,0x2281500,0x2689980,0x2bb1f80,0x30da580,0x34e2a00,0x3a0b000,0x3e13480,0x433ba80,0x4740a80,0x4c62780,0x5067780,0x5589480,0x5983200,0x5c25180,0x5c25180,0x5c25180,0x5c25180,0x5c25180,0x5c25180,0x5c25180,0x5c25180,0x5c25180,0x5c25180,0x5c25180,0x5c25180,36,1028,1836,2828,3636,4628,5436,6428,7236,8228,9220,10028,11020,11828,12820,13623,14605,15408,16390,17176,17693,17693,17693,17693,21,20,0,0,0,0,0,0,0x1c7a00,0x6f0000,0xaf8480,0x1020a80,0x1428f00,0x1951500,0x1d59980,0x2281f80,0x268a400,0x2bb2a00,0x30db000,0x34e3480,0x3a0ba80,0x3e13f00,0x433c500,0x4741500,0x4c63200,0x5068200,0x5589f00,0x5983c80,0x5c25c00,0x5c25c00,0x5c25c00,0x5c25c00,0x5c25c00,0x5c25c00,0x5c25c00,0x5c25c00,0x5c25c00,0x5c25c00,0x5c25c00,0x5c25c00,37,1029,1837,2829,3637,4629,5437,6429,7237,8229,9221,10029,11021,11829,12821,13624,14606,15409,16391,18484,18484,18484,18484,18484,20,19,0,0,0,0,0,0,0x1c8480,0x6f0a80,0xaf8f00,0x1021500,0x1429980,0x1951f80,0x1d5a400,0x2282a00,0x268ae80,0x2bb3480,0x30dba80,0x34e3f00,0x3a0c500,0x3e14980,0x433cf80,0x4741f80,0x4c63c80,0x5068c80,0x558a980,0x82c80,0x82c80,0x82c80,0x82c80,0x82c80,0x82c80,0x82c80,0x82c80,0x82c80,0x82c80,0x82c80,0x82c80,0x82c80,38,1030,1838,2830,3638,4630,5438,6430,7238,8230,9222,10030,11022,11830,12822,13625,14607,15410,16392,18485,17694,17694,17694,17694,21,20,0,0,0,0,0,0,0x1c8f00,0x6f1500,0xaf9980,0x1021f80,0x142a400,0x1952a00,0x1d5ae80,0x2283480,0x268b900,0x2bb3f00,0x30dc500,0x34e4980,0x3a0cf80,0x3e15400,0x433da00,0x4742a00,0x4c64700,0x5069700,0x558b400,0x83700,0x5c26680,0x5c26680,0x5c26680,0x5c26680,0x5c26680,0x5c26680,0x5c26680,0x5c26680,0x5c26680,0x5c26680,0x5c26680,0x5c26680,39,1031,1839,2831,3639,4631,5439,6431,7239,8231,9223,10031,11023,11831,12823,13626,14608,15411,16393,17177,17695,17695,17695,17695,21,20,0,0,0,0,0,0,0x1c9980,0x6f1f80,0xafa400,0x1022a00,0x142ae80,0x1953480,0x1d5b900,0x2283f00,0x268c380,0x2bb4980,0x30dcf80,0x34e5400,0x3a0da00,0x3e15e80,0x433e480,0x4743480,0x4c65180,0x506a180,0x558be80,0x5984700,0x5c27100,0x5c27100,0x5c27100,0x5c27100,0x5c27100,0x5c27100,0x5c27100,0x5c27100,0x5c27100,0x5c27100,0x5c27100,0x5c27100,40,1032,1840,2832,3640,4632,5440,6432,7424,8232,9224,10032,11024,11832,12824,18437,14609,18452,16394,18486,18486,18486,18486,18486,20,19,0,0,0,0,0,0,0x1ca400,0x6f2a00,0xafae80,0x1023480,0x142b900,0x1953f00,0x1d5c380,0x2284980,0x27acf80,0x2bb5400,0x30dda00,0x34e5e80,0x3a0e480,0x3e16900,0x433ef00,0x63f00,0x4c65c00,0x6dc80,0x558c900,0x84180,0x84180,0x84180,0x84180,0x84180,0x84180,0x84180,0x84180,0x84180,0x84180,0x84180,0x84180,0x84180,41,1033,1841,2833,3641,4633,5441,6433,7425,8233,9225,10033,11025,11833,12825,13627,14610,15412,16395,18487,18487,18487,18487,18487,20,19,0,0,0,0,0,0,0x1cae80,0x6f3480,0xafb900,0x1023f00,0x142c380,0x1954980,0x1d5ce00,0x2285400,0x27ada00,0x2bb5e80,0x30de480,0x34e6900,0x3a0ef00,0x3e17380,0x433f980,0x4743f00,0x4c66680,0x506ac00,0x558d380,0x84c00,0x84c00,0x84c00,0x84c00,0x84c00,0x84c00,0x84c00,0x84c00,0x84c00,0x84c00,0x84c00,0x84c00,0x84c00,42,1034,1842,2834,3642,4634,5442,6434,7426,8234,9226,10034,11026,11834,12826,13628,14611,15413,16396,17178,17696,18221,18221,18221,22,21,0,0,0,0,0,0,0x1cb900,0x6f3f00,0xafc380,0x1024980,0x142ce00,0x1955400,0x1d5d880,0x2285e80,0x27ae480,0x2bb6900,0x30def00,0x34e7380,0x3a0f980,0x3e17e00,0x4340400,0x4744980,0x4c67100,0x506b680,0x558de00,0x5985180,0x5c27b80,0x5ecef00,0x5ecef00,0x5ecef00,0x5ecef00,0x5ecef00,0x5ecef00,0x5ecef00,0x5ecef00,0x5ecef00,0x5ecef00,0x5ecef00,43,1035,1843,2835,3643,4635,5443,6435,7427,8235,9227,10035,11027,11835,12827,13629,14612,15414,16397,17179,17697,17697,17697,17697,21,20,0,0,0,0,0,0,0x1cc380,0x6f4980,0xafce00,0x1025400,0x142d880,0x1955e80,0x1d5e300,0x2286900,0x27aef00,0x2bb7380,0x30df980,0x34e7e00,0x3a10400,0x3e18880,0x4340e80,0x4745400,0x4c67b80,0x506c100,0x558e880,0x5985c00,0x5c28600,0x5c28600,0x5c28600,0x5c28600,0x5c28600,0x5c28600,0x5c28600,0x5c28600,0x5c28600,0x5c28600,0x5c28600,0x5c28600,44,1036,1844,2836,3644,4636,5444,6436,7428,8236,9228,10036,11028,11836,12828,13630,14613,15415,16398,17180,17698,17698,17698,17698,21,20,0,0,0,0,0,0,0x1cce00,0x6f5400,0xafd880,0x1025e80,0x142e300,0x1956900,0x1d5ed80,0x2287380,0x27af980,0x2bb7e00,0x30e0400,0x34e8880,0x3a10e80,0x3e19300,0x4341900,0x4745e80,0x4c68600,0x506cb80,0x558f300,0x5986680,0x5c29080,0x5c29080,0x5c29080,0x5c29080,0x5c29080,0x5c29080,0x5c29080,0x5c29080,0x5c29080,0x5c29080,0x5c29080,0x5c29080,45,1037,1845,2837,3645,4637,5445,6437,7429,8237,9229,10037,11029,11837,12829,13631,14614,15416,16399,18488,18488,18488,18488,18488,20,19,0,0,0,0,0,0,0x1cd880,0x6f5e80,0xafe300,0x1026900,0x142ed80,0x1957380,0x1d5f800,0x2287e00,0x27b0400,0x2bb8880,0x30e0e80,0x34e9300,0x3a11900,0x3e19d80,0x4342380,0x4746900,0x4c69080,0x506d600,0x558fd80,0x85680,0x85680,0x85680,0x85680,0x85680,0x85680,0x85680,0x85680,0x85680,0x85680,0x85680,0x85680,0x85680,46,1038,1846,2838,3646,4638,5446,6438,7430,8238,9230,10038,11030,11838,12830,13632,14615,15417,16400,18489,17699,17699,17699,17699,21,20,0,0,0,0,0,0,0x1ce300,0x6f6900,0xafed80,0x1027380,0x142f800,0x1957e00,0x1d60280,0x2288880,0x27b0e80,0x2bb9300,0x30e1900,0x34e9d80,0x3a12380,0x3e1a800,0x4342e00,0x4747380,0x4c69b00,0x506e080,0x5590800,0x86100,0x5c29b00,0x5c29b00,0x5c29b00,0x5c29b00,0x5c29b00,0x5c29b00,0x5c29b00,0x5c29b00,0x5c29b00,0x5c29b00,0x5c29b00,0x5c29b00,47,1039,1847,2839,3647,4639,5447,6439,7431,8239,9231,10039,11031,11839,12831,13633,14616,15418,16401,17181,17700,17700,17700,17700,21,20,0,0,0,0,0,0,0x1ced80,0x6f7380,0xaff800,0x1027e00,0x1430280,0x1958880,0x1d60d00,0x2289300,0x27b1900,0x2bb9d80,0x30e2380,0x34ea800,0x3a12e00,0x3e1b280,0x4343880,0x4747e00,0x4c6a580,0x506eb00,0x5591280,0x5987100,0x5c2a580,0x5c2a580,0x5c2a580,0x5c2a580,0x5c2a580,0x5c2a580,0x5c2a580,0x5c2a580,0x5c2a580,0x5c2a580,0x5c2a580,0x5c2a580,48,1040,1848,2840,3648,4640,5632,6440,7432,8240,9232,10040,11032,11840,12832,18438,14617,18453,16402,18490,18490,18490,18490,18490,20,19,0,0,0,0,0,0,0x1cf800,0x6f7e00,0xb00280,0x1028880,0x1430d00,0x1959300,0x1e81900,0x2289d80,0x27b2380,0x2bba800,0x30e2e00,0x34eb280,0x3a13880,0x3e1bd00,0x4344300,0x64980,0x4c6b000,0x6e700,0x5591d00,0x86b80,0x86b80,0x86b80,0x86b80,0x86b80,0x86b80,0x86b80,0x86b80,0x86b80,0x86b80,0x86b80,0x86b80,0x86b80,49,1041,1849,2841,3649,4641,5633,6441,7433,8241,9233,10041,11033,11841,12833,13634,14618,15419,16403,18491,18491,18491,18491,18491,20,19,0,0,0,0,0,0,0x1d0280,0x6f8880,0xb00d00,0x1029300,0x1431780,0x1959d80,0x1e82380,0x228a800,0x27b2e00,0x2bbb280,0x30e3880,0x34ebd00,0x3a14300,0x3e1c780,0x4344d80,0x4748880,0x4c6ba80,0x506f580,0x5592780,0x87600,0x87600,0x87600,0x87600,0x87600,0x87600,0x87600,0x87600,0x87600,0x87600,0x87600,0x87600,0x87600,50,1042,1850,2842,3650,4642,5634,6442,7434,8242,9234,10042,11034,11842,12834,13635,14619,15420,16404,17182,17701,18222,18222,18222,22,21,0,0,0,0,0,0,0x1d0d00,0x6f9300,0xb01780,0x1029d80,0x1432200,0x195a800,0x1e82e00,0x228b280,0x27b3880,0x2bbbd00,0x30e4300,0x34ec780,0x3a14d80,0x3e1d200,0x4345800,0x4749300,0x4c6c500,0x5070000,0x5593200,0x5987b80,0x5c2b000,0x5ecf980,0x5ecf980,0x5ecf980,0x5ecf980,0x5ecf980,0x5ecf980,0x5ecf980,0x5ecf980,0x5ecf980,0x5ecf980,0x5ecf980,51,1043,1851,2843,3651,4643,5635,6443,7435,8243,9235,10043,11035,11843,12835,13636,14620,15421,16405,17183,17702,17702,17702,17702,21,20,0,0,0,0,0,0,0x1d1780,0x6f9d80,0xb02200,0x102a800,0x1432c80,0x195b280,0x1e83880,0x228bd00,0x27b4300,0x2bbc780,0x30e4d80,0x34ed200,0x3a15800,0x3e1dc80,0x4346280,0x4749d80,0x4c6cf80,0x5070a80,0x5593c80,0x5988600,0x5c2ba80,0x5c2ba80,0x5c2ba80,0x5c2ba80,0x5c2ba80,0x5c2ba80,0x5c2ba80,0x5c2ba80,0x5c2ba80,0x5c2ba80,0x5c2ba80,0x5c2ba80,52,1044,1852,2844,3652,4644,5636,6444,7436,8244,9236,10044,11036,11844,12836,13637,14621,15422,16406,17184,17703,17703,17703,17703,21,20,0,0,0,0,0,0,0x1d2200,0x6fa800,0xb02c80,0x102b280,0x1433700,0x195bd00,0x1e84300,0x228c780,0x27b4d80,0x2bbd200,0x30e5800,0x34edc80,0x3a16280,0x3e1e700,0x4346d00,0x474a800,0x4c6da00,0x5071500,0x5594700,0x5989080,0x5c2c500,0x5c2c500,0x5c2c500,0x5c2c500,0x5c2c500,0x5c2c500,0x5c2c500,0x5c2c500,0x5c2c500,0x5c2c500,0x5c2c500,0x5c2c500,53,1045,1853,2845,3653,4645,5637,6445,7437,8245,9237,10045,11037,11845,12837,13638,14622,15423,16407,18492,18492,18492,18492,18492,20,19,0,0,0,0,0,0,0x1d2c80,0x6fb280,0xb03700,0x102bd00,0x1434180,0x195c780,0x1e84d80,0x228d200,0x27b5800,0x2bbdc80,0x30e6280,0x34ee700,0x3a16d00,0x3e1f180,0x4347780,0x474b280,0x4c6e480,0x5071f80,0x5595180,0x88080,0x88080,0x88080,0x88080,0x88080,0x88080,0x88080,0x88080,0x88080,0x88080,0x88080,0x88080,0x88080,54,1046,1854,2846,3654,4646,5638,6446,7438,8246,9238,10046,11038,11846,12838,13639,14623,15424,16408,18493,17704,17704,17704,17704,21,20,0,0,0,0,0,0,0x1d3700,0x6fbd00,0xb04180,0x102c780,0x1434c00,0x195d200,0x1e85800,0x228dc80,0x27b6280,0x2bbe700,0x30e6d00,0x34ef180,0x3a17780,0x3e1fc00,0x4348200,0x474bd00,0x4c6ef00,0x5072a00,0x5595c00,0x88b00,0x5c2cf80,0x5c2cf80,0x5c2cf80,0x5c2cf80,0x5c2cf80,0x5c2cf80,0x5c2cf80,0x5c2cf80,0x5c2cf80,0x5c2cf80,0x5c2cf80,0x5c2cf80,55,1047,1855,2847,3655,4647,5639,6447,7439,8247,9239,10047,11039,11847,12839,13824,14624,15425,16409,17185,17705,17705,17705,17705,21,20,0,0,0,0,0,0,0x1d4180,0x6fc780,0xb04c00,0x102d200,0x1435680,0x195dc80,0x1e86280,0x228e700,0x27b6d00,0x2bbf180,0x30e7780,0x34efc00,0x3a18200,0x3e20680,0x4348c80,0x486c900,0x4c6f980,0x5073480,0x5596680,0x5989b00,0x5c2da00,0x5c2da00,0x5c2da00,0x5c2da00,0x5c2da00,0x5c2da00,0x5c2da00,0x5c2da00,0x5c2da00,0x5c2da00,0x5c2da00,0x5c2da00,56,1048,1856,2848,3840,4648,5640,6448,7440,8248,9240,10048,11040,12032,12840,18439,14625,18454,16410,18494,18494,18494,18494,18494,20,19,0,0,0,0,0,0,0x1d4c00,0x6fd200,0xb05680,0x102dc80,0x1556280,0x195e700,0x1e86d00,0x228f180,0x27b7780,0x2bbfc00,0x30e8200,0x34f0680,0x3a18c80,0x3f41280,0x4349700,0x65400,0x4c70400,0x6f180,0x5597100,0x89580,0x89580,0x89580,0x89580,0x89580,0x89580,0x89580,0x89580,0x89580,0x89580,0x89580,0x89580,0x89580,57,1049,1857,2849,3841,4649,5641,6449,7441,8249,9241,10049,11041,12033,12841,13825,14626,15426,16411,18495,18495,18495,18495,18495,20,19,0,0,0,0,0,0,0x1d5680,0x6fdc80,0xb06100,0x102e700,0x1556d00,0x195f180,0x1e87780,0x228fc00,0x27b8200,0x2bc0680,0x30e8c80,0x34f1100,0x3a19700,0x3f41d00,0x434a180,0x486d380,0x4c70e80,0x5073f00,0x5597b80,0x8a000,0x8a000,0x8a000,0x8a000,0x8a000,0x8a000,0x8a000,0x8a000,0x8a000,0x8a000,0x8a000,0x8a000,0x8a000,58,1050,1858,2850,3842,4650,5642,6450,7442,8250,9242,10050,11042,12034,12842,13826,14627,15427,16412,17186,17706,18223,18223,18223,22,21,0,0,0,0,0,0,0x1d6100,0x6fe700,0xb06b80,0x102f180,0x1557780,0x195fc00,0x1e88200,0x2290680,0x27b8c80,0x2bc1100,0x30e9700,0x34f1b80,0x3a1a180,0x3f42780,0x434ac00,0x486de00,0x4c71900,0x5074980,0x5598600,0x598a580,0x5c2e480,0x5ed0400,0x5ed0400,0x5ed0400,0x5ed0400,0x5ed0400,0x5ed0400,0x5ed0400,0x5ed0400,0x5ed0400,0x5ed0400,0x5ed0400,59,1051,1859,2851,3843,4651,5643,6451,7443,8251,9243,10051,11043,12035,12843,13827,14628,15428,16413,17187,17707,17707,17707,17707,21,20,0,0,0,0,0,0,0x1d6b80,0x6ff180,0xb07600,0x102fc00,0x1558200,0x1960680,0x1e88c80,0x2291100,0x27b9700,0x2bc1b80,0x30ea180,0x34f2600,0x3a1ac00,0x3f43200,0x434b680,0x486e880,0x4c72380,0x5075400,0x5599080,0x598b000,0x5c2ef00,0x5c2ef00,0x5c2ef00,0x5c2ef00,0x5c2ef00,0x5c2ef00,0x5c2ef00,0x5c2ef00,0x5c2ef00,0x5c2ef00,0x5c2ef00,0x5c2ef00,60,1052,1860,2852,3844,4652,5644,6452,7444,8252,9244,10052,11044,12036,12844,13828,14629,15429,16414,17188,17708,17708,17708,17708,21,20,0,0,0,0,0,0,0x1d7600,0x6ffc00,0xb08080,0x1030680,0x1558c80,0x1961100,0x1e89700,0x2291b80,0x27ba180,0x2bc2600,0x30eac00,0x34f3080,0x3a1b680,0x3f43c80,0x434c100,0x486f300,0x4c72e00,0x5075e80,0x5599b00,0x598ba80,0x5c2f980,0x5c2f980,0x5c2f980,0x5c2f980,0x5c2f980,0x5c2f980,0x5c2f980,0x5c2f980,0x5c2f980,0x5c2f980,0x5c2f980,0x5c2f980,61,1053,1861,2853,3845,4653,5645,6453,7445,8253,9245,10053,11045,12037,12845,13829,14630,15430,16415,18496,18496,18496,18496,18496,20,19,0,0,0,0,0,0,0x1d8080,0x700680,0xb08b00,0x1031100,0x1559700,0x1961b80,0x1e8a180,0x2292600,0x27bac00,0x2bc3080,0x30eb680,0x34f3b00,0x3a1c100,0x3f44700,0x434cb80,0x486fd80,0x4c73880,0x5076900,0x559a580,0x8aa80,0x8aa80,0x8aa80,0x8aa80,0x8aa80,0x8aa80,0x8aa80,0x8aa80,0x8aa80,0x8aa80,0x8aa80,0x8aa80,0x8aa80,62,1054,1862,2854,3846,4654,5646,6454,7446,8254,9246,10054,11046,12038,12846,13830,14631,15431,16416,18497,17709,17709,17709,17709,21,20,0,0,0,0,0,0,0x1d8b00,0x701100,0xb09580,0x1031b80,0x155a180,0x1962600,0x1e8ac00,0x2293080,0x27bb680,0x2bc3b00,0x30ec100,0x34f4580,0x3a1cb80,0x3f45180,0x434d600,0x4870800,0x4c74300,0x5077380,0x559b000,0x8b500,0x5c30400,0x5c30400,0x5c30400,0x5c30400,0x5c30400,0x5c30400,0x5c30400,0x5c30400,0x5c30400,0x5c30400,0x5c30400,0x5c30400,63,1055,1863,2855,3847,4655,5647,6455,7447,8255,9247,10055,11047,12039,12847,13831,14632,15616,16417,17189,17710,17710,17710,17710,21,20,0,0,0,0,0,0,0x1d9580,0x701b80,0xb0a000,0x1032600,0x155ac00,0x1963080,0x1e8b680,0x2293b00,0x27bc100,0x2bc4580,0x30ecb80,0x34f5000,0x3a1d600,0x3f45c00,0x434e080,0x4871280,0x4c74d80,0x5197f80,0x559ba80,0x598c500,0x5c30e80,0x5c30e80,0x5c30e80,0x5c30e80,0x5c30e80,0x5c30e80,0x5c30e80,0x5c30e80,0x5c30e80,0x5c30e80,0x5c30e80,0x5c30e80,64,1056,2048,2856,3848,4656,5648,6456,7448,8256,9248,10240,11048,12040,12848,18440,14633,18455,16418,18498,18498,18498,18498,18498,20,19,0,0,0,0,0,0,0x1da000,0x702600,0xc2ac00,0x1033080,0x155b680,0x1963b00,0x1e8c100,0x2294580,0x27bcb80,0x2bc5000,0x30ed600,0x3615c00,0x3a1e080,0x3f46680,0x434eb00,0x65e80,0x4c75800,0x6fc00,0x559c500,0x8bf80,0x8bf80,0x8bf80,0x8bf80,0x8bf80,0x8bf80,0x8bf80,0x8bf80,0x8bf80,0x8bf80,0x8bf80,0x8bf80,0x8bf80,65,1057,2049,2857,3849,4657,5649,6457,7449,8257,9249,10241,11049,12041,12849,13832,14634,15617,16419,18499,18499,18499,18499,18499,20,19,0,0,0,0,0,0,0x1daa80,0x703080,0xc2b680,0x1033b00,0x155c100,0x1964580,0x1e8cb80,0x2295000,0x27bd600,0x2bc5a80,0x30ee080,0x3616680,0x3a1eb00,0x3f47100,0x434f580,0x4871d00,0x4c76280,0x5198a00,0x559cf80,0x8ca00,0x8ca00,0x8ca00,0x8ca00,0x8ca00,0x8ca00,0x8ca00,0x8ca00,0x8ca00,0x8ca00,0x8ca00,0x8ca00,0x8ca00,66,1058,2050,2858,3850,4658,5650,6458,7450,8258,9250,10242,11050,12042,12850,13833,14635,15618,16420,17190,17711,18224,18224,18224,22,21,0,0,0,0,0,0,0x1db500,0x703b00,0xc2c100,0x1034580,0x155cb80,0x1965000,0x1e8d600,0x2295a80,0x27be080,0x2bc6500,0x30eeb00,0x3617100,0x3a1f580,0x3f47b80,0x4350000,0x4872780,0x4c76d00,0x5199480,0x559da00,0x598cf80,0x5c31900,0x5ed0e80,0x5ed0e80,0x5ed0e80,0x5ed0e80,0x5ed0e80,0x5ed0e80,0x5ed0e80,0x5ed0e80,0x5ed0e80,0x5ed0e80,0x5ed0e80,67,1059,2051,2859,3851,4659,5651,6459,7451,8259,9251,10243,11051,12043,12851,13834,14636,15619,16421,17191,17712,17712,17712,17712,21,20,0,0,0,0,0,0,0x1dbf80,0x704580,0xc2cb80,0x1035000,0x155d600,0x1965a80,0x1e8e080,0x2296500,0x27beb00,0x2bc6f80,0x30ef580,0x3617b80,0x3a20000,0x3f48600,0x4350a80,0x4873200,0x4c77780,0x5199f00,0x559e480,0x598da00,0x5c32380,0x5c32380,0x5c32380,0x5c32380,0x5c32380,0x5c32380,0x5c32380,0x5c32380,0x5c32380,0x5c32380,0x5c32380,0x5c32380,68,1060,2052,2860,3852,4660,5652,6460,7452,8260,9252,10244,11052,12044,12852,13835,14637,15620,16422,17192,17713,17713,17713,17713,21,20,0,0,0,0,0,0,0x1dca00,0x705000,0xc2d600,0x1035a80,0x155e080,0x1966500,0x1e8eb00,0x2296f80,0x27bf580,0x2bc7a00,0x30f0000,0x3618600,0x3a20a80,0x3f49080,0x4351500,0x4873c80,0x4c78200,0x519a980,0x559ef00,0x598e480,0x5c32e00,0x5c32e00,0x5c32e00,0x5c32e00,0x5c32e00,0x5c32e00,0x5c32e00,0x5c32e00,0x5c32e00,0x5c32e00,0x5c32e00,0x5c32e00,69,1061,2053,2861,3853,4661,5653,6461,7453,8261,9253,10245,11053,12045,12853,13836,14638,15621,16423,18500,18500,18500,18500,18500,20,19,0,0,0,0,0,0,0x1dd480,0x705a80,0xc2e080,0x1036500,0x155eb00,0x1966f80,0x1e8f580,0x2297a00,0x27c0000,0x2bc8480,0x30f0a80,0x3619080,0x3a21500,0x3f49b00,0x4351f80,0x4874700,0x4c78c80,0x519b400,0x559f980,0x8d480,0x8d480,0x8d480,0x8d480,0x8d480,0x8d480,0x8d480,0x8d480,0x8d480,0x8d480,0x8d480,0x8d480,0x8d480,70,1062,2054,2862,3854,4662,5654,6462,7454,8262,9254,10246,11054,12046,12854,13837,14639,15622,16424,18501,17714,17714,17714,17714,21,20,0,0,0,0,0,0,0x1ddf00,0x706500,0xc2eb00,0x1036f80,0x155f580,0x1967a00,0x1e90000,0x2298480,0x27c0a80,0x2bc8f00,0x30f1500,0x3619b00,0x3a21f80,0x3f4a580,0x4352a00,0x4875180,0x4c79700,0x519be80,0x55a0400,0x8df00,0x5c33880,0x5c33880,0x5c33880,0x5c33880,0x5c33880,0x5c33880,0x5c33880,0x5c33880,0x5c33880,0x5c33880,0x5c33880,0x5c33880,71,1063,2055,2863,3855,4663,5655,6463,7455,8263,9255,10247,11055,12047,12855,13838,14640,15623,16425,17193,17715,17715,17715,17715,21,20,0,0,0,0,0,0,0x1de980,0x706f80,0xc2f580,0x1037a00,0x1560000,0x1968480,0x1e90a80,0x2298f00,0x27c1500,0x2bc9980,0x30f1f80,0x361a580,0x3a22a00,0x3f4b000,0x4353480,0x4875c00,0x4c7a180,0x519c900,0x55a0e80,0x598ef00,0x5c34300,0x5c34300,0x5c34300,0x5c34300,0x5c34300,0x5c34300,0x5c34300,0x5c34300,0x5c34300,0x5c34300,0x5c34300,0x5c34300,256,1064,2056,2864,3856,4664,5656,6464,7456,8448,9256,10248,11056,12048,12856,18441,14641,18456,16426,18502,18502,18502,18502,18502,20,19,0,0,0,0,0,0,0x2ff580,0x707a00,0xc30000,0x1038480,0x1560a80,0x1968f00,0x1e91500,0x2299980,0x27c1f80,0x2cea580,0x30f2a00,0x361b000,0x3a23480,0x3f4ba80,0x4353f00,0x66900,0x4c7ac00,0x70680,0x55a1900,0x8e980,0x8e980,0x8e980,0x8e980,0x8e980,0x8e980,0x8e980,0x8e980,0x8e980,0x8e980,0x8e980,0x8e980,0x8e980,257,1065,2057,2865,3857,4665,5657,6465,7457,8449,9257,10249,11057,12049,12857,13839,14642,15624,16427,18503,18503,18503,18503,18503,20,19,0,0,0,0,0,0,0x300000,0x708480,0xc30a80,0x1038f00,0x1561500,0x1969980,0x1e91f80,0x229a400,0x27c2a00,0x2ceb000,0x30f3480,0x361ba80,0x3a23f00,0x3f4c500,0x4354980,0x4876680,0x4c7b680,0x519d380,0x55a2380,0x8f400,0x8f400,0x8f400,0x8f400,0x8f400,0x8f400,0x8f400,0x8f400,0x8f400,0x8f400,0x8f400,0x8f400,0x8f400,258,1066,2058,2866,3858,4666,5658,6466,7458,8450,9258,10250,11058,12050,12858,13840,14643,15625,16428,17194,17716,18225,18225,18225,22,21,0,0,0,0,0,0,0x300a80,0x708f00,0xc31500,0x1039980,0x1561f80,0x196a400,0x1e92a00,0x229ae80,0x27c3480,0x2ceba80,0x30f3f00,0x361c500,0x3a24980,0x3f4cf80,0x4355400,0x4877100,0x4c7c100,0x519de00,0x55a2e00,0x598f980,0x5c34d80,0x5ed1900,0x5ed1900,0x5ed1900,0x5ed1900,0x5ed1900,0x5ed1900,0x5ed1900,0x5ed1900,0x5ed1900,0x5ed1900,0x5ed1900,259,1067,2059,2867,3859,4667,5659,6467,7459,8451,9259,10251,11059,12051,12859,13841,14644,15626,16429,17195,17717,17717,17717,17717,21,20,0,0,0,0,0,0,0x301500,0x709980,0xc31f80,0x103a400,0x1562a00,0x196ae80,0x1e93480,0x229b900,0x27c3f00,0x2cec500,0x30f4980,0x361cf80,0x3a25400,0x3f4da00,0x4355e80,0x4877b80,0x4c7cb80,0x519e880,0x55a3880,0x5990400,0x5c35800,0x5c35800,0x5c35800,0x5c35800,0x5c35800,0x5c35800,0x5c35800,0x5c35800,0x5c35800,0x5c35800,0x5c35800,0x5c35800,260,1068,2060,2868,3860,4668,5660,6468,7460,8452,9260,10252,11060,12052,12860,13842,14645,15627,16430,17196,17718,17718,17718,17718,21,20,0,0,0,0,0,0,0x301f80,0x70a400,0xc32a00,0x103ae80,0x1563480,0x196b900,0x1e93f00,0x229c380,0x27c4980,0x2cecf80,0x30f5400,0x361da00,0x3a25e80,0x3f4e480,0x4356900,0x4878600,0x4c7d600,0x519f300,0x55a4300,0x5990e80,0x5c36280,0x5c36280,0x5c36280,0x5c36280,0x5c36280,0x5c36280,0x5c36280,0x5c36280,0x5c36280,0x5c36280,0x5c36280,0x5c36280,261,1069,2061,2869,3861,4669,5661,6469,7461,8453,9261,10253,11061,12053,12861,13843,14646,15628,16431,18504,18504,18504,18504,18504,20,19,0,0,0,0,0,0,0x302a00,0x70ae80,0xc33480,0x103b900,0x1563f00,0x196c380,0x1e94980,0x229ce00,0x27c5400,0x2ceda00,0x30f5e80,0x361e480,0x3a26900,0x3f4ef00,0x4357380,0x4879080,0x4c7e080,0x519fd80,0x55a4d80,0x60000,0x60000,0x60000,0x60000,0x60000,0x60000,0x60000,0x60000,0x60000,0x60000,0x60000,0x60000,0x60000,262,1070,2062,2870,3862,4670,5662,6470,7462,8454,9262,10254,11062,12054,12862,13844,14647,15629,16432,72,17719,17719,17719,17719,21,20,0,0,0,0,0,0,0x303480,0x70b900,0xc33f00,0x103c380,0x1564980,0x196ce00,0x1e95400,0x229d880,0x27c5e80,0x2cee480,0x30f6900,0x361ef00,0x3a27380,0x3f4f980,0x4357e00,0x4879b00,0x4c7eb00,0x51a0800,0x55a5800,0x1af580,0x5c36d00,0x5c36d00,0x5c36d00,0x5c36d00,0x5c36d00,0x5c36d00,0x5c36d00,0x5c36d00,0x5c36d00,0x5c36d00,0x5c36d00,0x5c36d00,263,1071,2063,2871,3863,4671,5663,6471,7463,8455,9263,10255,11063,12055,12863,13845,14648,15630,16433,17197,17720,17720,17720,17720,21,20,0,0,0,0,0,0,0x303f00,0x70c380,0xc34980,0x103ce00,0x1565400,0x196d880,0x1e95e80,0x229e300,0x27c6900,0x2ceef00,0x30f7380,0x361f980,0x3a27e00,0x3f50400,0x4358880,0x487a580,0x4c7f580,0x51a1280,0x55a6280,0x5991900,0x5c37780,0x5c37780,0x5c37780,0x5c37780,0x5c37780,0x5c37780,0x5c37780,0x5c37780,0x5c37780,0x5c37780,0x5c37780,0x5c37780,264,1072,2064,2872,3864,4672,5664,6656,7464,8456,9264,10256,11064,12056,12864,18442,14649,18457,16434,328,328,328,328,328,20,19,0,0,0,0,0,0,0x304980,0x70ce00,0xc35400,0x103d880,0x1565e80,0x196e300,0x1e96900,0x23bef00,0x27c7380,0x2cef980,0x30f7e00,0x3620400,0x3a28880,0x3f50e80,0x4359300,0x67380,0x4c80000,0x71100,0x55a6d00,0x2feb00,0x2feb00,0x2feb00,0x2feb00,0x2feb00,0x2feb00,0x2feb00,0x2feb00,0x2feb00,0x2feb00,0x2feb00,0x2feb00,0x2feb00,265,1073,2065,2873,3865,4673,5665,6657,7465,8457,9265,10257,11065,12057,12865,13846,14650,15631,16435,584,584,584,584,584,20,19,0,0,0,0,0,0,0x305400,0x70d880,0xc35e80,0x103e300,0x1566900,0x196ed80,0x1e97380,0x23bf980,0x27c7e00,0x2cf0400,0x30f8880,0x3620e80,0x3a29300,0x3f51900,0x4359d80,0x487b000,0x4c80a80,0x51a1d00,0x55a7780,0x44e080,0x44e080,0x44e080,0x44e080,0x44e080,0x44e080,0x44e080,0x44e080,0x44e080,0x44e080,0x44e080,0x44e080,0x44e080,266,1074,2066,2874,3866,4674,5666,6658,7466,8458,9266,10258,11066,12058,12866,13847,14651,15632,16436,17198,17721,18226,18226,18226,22,21,0,0,0,0,0,0,0x305e80,0x70e300,0xc36900,0x103ed80,0x1567380,0x196f800,0x1e97e00,0x23c0400,0x27c8880,0x2cf0e80,0x30f9300,0x3621900,0x3a29d80,0x3f52380,0x435a800,0x487ba80,0x4c81500,0x51a2780,0x55a8200,0x5992380,0x5c38200,0x5ed2380,0x5ed2380,0x5ed2380,0x5ed2380,0x5ed2380,0x5ed2380,0x5ed2380,0x5ed2380,0x5ed2380,0x5ed2380,0x5ed2380,267,1075,2067,2875,3867,4675,5667,6659,7467,8459,9267,10259,11067,12059,12867,13848,14652,15633,16437,17199,17722,17722,17722,17722,21,20,0,0,0,0,0,0,0x306900,0x70ed80,0xc37380,0x103f800,0x1567e00,0x1970280,0x1e98880,0x23c0e80,0x27c9300,0x2cf1900,0x30f9d80,0x3622380,0x3a2a800,0x3f52e00,0x435b280,0x487c500,0x4c81f80,0x51a3200,0x55a8c80,0x5992e00,0x5c38c80,0x5c38c80,0x5c38c80,0x5c38c80,0x5c38c80,0x5c38c80,0x5c38c80,0x5c38c80,0x5c38c80,0x5c38c80,0x5c38c80,0x5c38c80,268,1076,2068,2876,3868,4676,5668,6660,7468,8460,9268,10260,11068,12060,12868,13849,14653,15634,16438,17200,17723,17723,17723,17723,21,20,0,0,0,0,0,0,0x307380,0x70f800,0xc37e00,0x1040280,0x1568880,0x1970d00,0x1e99300,0x23c1900,0x27c9d80,0x2cf2380,0x30fa800,0x3622e00,0x3a2b280,0x3f53880,0x435bd00,0x487cf80,0x4c82a00,0x51a3c80,0x55a9700,0x5993880,0x5c39700,0x5c39700,0x5c39700,0x5c39700,0x5c39700,0x5c39700,0x5c39700,0x5c39700,0x5c39700,0x5c39700,0x5c39700,0x5c39700,269,1077,2069,2877,3869,4677,5669,6661,7469,8461,9269,10261,11069,12061,12869,13850,14654,15635,16439,840,840,840,840,840,20,19,0,0,0,0,0,0,0x307e00,0x710280,0xc38880,0x1040d00,0x1569300,0x1971780,0x1e99d80,0x23c2380,0x27ca800,0x2cf2e00,0x30fb280,0x3623880,0x3a2bd00,0x3f54300,0x435c780,0x487da00,0x4c83480,0x51a4700,0x55aa180,0x59d600,0x59d600,0x59d600,0x59d600,0x59d600,0x59d600,0x59d600,0x59d600,0x59d600,0x59d600,0x59d600,0x59d600,0x59d600,270,1078,2070,2878,3870,4678,5670,6662,7470,8462,9270,10262,11070,12062,12870,13851,14655,15636,16440,1096,17724,17724,17724,17724,21,20,0,0,0,0,0,0,0x308880,0x710d00,0xc39300,0x1041780,0x1569d80,0x1972200,0x1e9a800,0x23c2e00,0x27cb280,0x2cf3880,0x30fbd00,0x3624300,0x3a2c780,0x3f54d80,0x435d200,0x487e480,0x4c83f00,0x51a5180,0x55aac00,0x6ecb80,0x5c3a180,0x5c3a180,0x5c3a180,0x5c3a180,0x5c3a180,0x5c3a180,0x5c3a180,0x5c3a180,0x5c3a180,0x5c3a180,0x5c3a180,0x5c3a180,271,1079,2071,2879,3871,4679,5671,6663,7471,8463,9271,10263,11071,12063,12871,13852,14656,15637,16441,17201,17725,17725,17725,17725,21,20,0,0,0,0,0,0,0x309300,0x711780,0xc39d80,0x1042200,0x156a800,0x1972c80,0x1e9b280,0x23c3880,0x27cbd00,0x2cf4300,0x30fc780,0x3624d80,0x3a2d200,0x3f55800,0x435dc80,0x487ef00,0x4c84980,0x51a5c00,0x55ab680,0x5994300,0x5c3ac00,0x5c3ac00,0x5c3ac00,0x5c3ac00,0x5c3ac00,0x5c3ac00,0x5c3ac00,0x5c3ac00,0x5c3ac00,0x5c3ac00,0x5c3ac00,0x5c3ac00,272,1080,2072,2880,3872,4864,5672,6664,7472,8464,9272,10264,11072,12064,13056,18443,14657,18458,16442,1352,1352,1352,1352,1352,20,19,0,0,0,0,0,0,0x309d80,0x712200,0xc3a800,0x1042c80,0x156b280,0x1a93880,0x1e9bd00,0x23c4300,0x27cc780,0x2cf4d80,0x30fd200,0x3625800,0x3a2dc80,0x3f56280,0x447e880,0x67e00,0x4c85400,0x71b80,0x55ac100,0x83c100,0x83c100,0x83c100,0x83c100,0x83c100,0x83c100,0x83c100,0x83c100,0x83c100,0x83c100,0x83c100,0x83c100,0x83c100,273,1081,2073,2881,3873,4865,5673,6665,7473,8465,9273,10265,11073,12065,13057,13853,14658,15638,16443,1608,1608,1608,1608,1608,20,19,0,0,0,0,0,0,0x30a800,0x712c80,0xc3b280,0x1043700,0x156bd00,0x1a94300,0x1e9c780,0x23c4d80,0x27cd200,0x2cf5800,0x30fdc80,0x3626280,0x3a2e700,0x3f56d00,0x447f300,0x487f980,0x4c85e80,0x51a6680,0x55acb80,0x98b680,0x98b680,0x98b680,0x98b680,0x98b680,0x98b680,0x98b680,0x98b680,0x98b680,0x98b680,0x98b680,0x98b680,0x98b680,274,1082,2074,2882,3874,4866,5674,6666,7474,8466,9274,10266,11074,12066,13058,13854,14659,15639,16444,17202,17726,18227,18227,18227,22,21,0,0,0,0,0,0,0x30b280,0x713700,0xc3bd00,0x1044180,0x156c780,0x1a94d80,0x1e9d200,0x23c5800,0x27cdc80,0x2cf6280,0x30fe700,0x3626d00,0x3a2f180,0x3f57780,0x447fd80,0x4880400,0x4c86900,0x51a7100,0x55ad600,0x5994d80,0x5c3b680,0x5ed2e00,0x5ed2e00,0x5ed2e00,0x5ed2e00,0x5ed2e00,0x5ed2e00,0x5ed2e00,0x5ed2e00,0x5ed2e00,0x5ed2e00,0x5ed2e00,275,1083,2075,2883,3875,4867,5675,6667,7475,8467,9275,10267,11075,12067,13059,13855,14660,15640,16445,17203,17727,17727,17727,17727,21,20,0,0,0,0,0,0,0x30bd00,0x714180,0xc3c780,0x1044c00,0x156d200,0x1a95800,0x1e9dc80,0x23c6280,0x27ce700,0x2cf6d00,0x30ff180,0x3627780,0x3a2fc00,0x3f58200,0x4480800,0x4880e80,0x4c87380,0x51a7b80,0x55ae080,0x5995800,0x5c3c100,0x5c3c100,0x5c3c100,0x5c3c100,0x5c3c100,0x5c3c100,0x5c3c100,0x5c3c100,0x5c3c100,0x5c3c100,0x5c3c100,0x5c3c100,276,1084,2076,2884,3876,4868,5676,6668,7476,8468,9276,10268,11076,12068,13060,13856,14661,15641,16446,17204,17728,17728,17728,17728,21,20,0,0,0,0,0,0,0x30c780,0x714c00,0xc3d200,0x1045680,0x156dc80,0x1a96280,0x1e9e700,0x23c6d00,0x27cf180,0x2cf7780,0x30ffc00,0x3628200,0x3a30680,0x3f58c80,0x4481280,0x4881900,0x4c87e00,0x51a8600,0x55aeb00,0x5996280,0x5c3cb80,0x5c3cb80,0x5c3cb80,0x5c3cb80,0x5c3cb80,0x5c3cb80,0x5c3cb80,0x5c3cb80,0x5c3cb80,0x5c3cb80,0x5c3cb80,0x5c3cb80,277,1085,2077,2885,3877,4869,5677,6669,7477,8469,9277,10269,11077,12069,13061,13857,14662,15642,16447,1864,1864,1864,1864,1864,20,19,0,0,0,0,0,0,0x30d200,0x715680,0xc3dc80,0x1046100,0x156e700,0x1a96d00,0x1e9f180,0x23c7780,0x27cfc00,0x2cf8200,0x3100680,0x3628c80,0x3a31100,0x3f59700,0x4481d00,0x4882380,0x4c88880,0x51a9080,0x55af580,0xadac00,0xadac00,0xadac00,0xadac00,0xadac00,0xadac00,0xadac00,0xadac00,0xadac00,0xadac00,0xadac00,0xadac00,0xadac00,278,1086,2078,2886,3878,4870,5678,6670,7478,8470,9278,10270,11078,12070,13062,13858,14663,15643,16448,2120,17729,17729,17729,17729,21,20,0,0,0,0,0,0,0x30dc80,0x716100,0xc3e700,0x1046b80,0x156f180,0x1a97780,0x1e9fc00,0x23c8200,0x27d0680,0x2cf8c80,0x3101100,0x3629700,0x3a31b80,0x3f5a180,0x4482780,0x4882e00,0x4c89300,0x51a9b00,0x55b0000,0xc2a180,0x5c3d600,0x5c3d600,0x5c3d600,0x5c3d600,0x5c3d600,0x5c3d600,0x5c3d600,0x5c3d600,0x5c3d600,0x5c3d600,0x5c3d600,0x5c3d600,279,1087,2079,2887,3879,4871,5679,6671,7479,8471,9279,10271,11079,12071,13063,13859,14848,15644,16449,17205,17730,17730,17730,17730,21,20,0,0,0,0,0,0,0x30e700,0x716b80,0xc3f180,0x1047600,0x156fc00,0x1a98200,0x1ea0680,0x23c8c80,0x27d1100,0x2cf9700,0x3101b80,0x362a180,0x3a32600,0x3f5ac00,0x4483200,0x4883880,0x4da9f00,0x51aa580,0x55b0a80,0x5996d00,0x5c3e080,0x5c3e080,0x5c3e080,0x5c3e080,0x5c3e080,0x5c3e080,0x5c3e080,0x5c3e080,0x5c3e080,0x5c3e080,0x5c3e080,0x5c3e080,280,1088,2080,3072,3880,4872,5680,6672,7480,8472,9280,10272,11264,12072,13064,18444,14849,18459,16450,2376,2376,2376,2376,2376,20,19,0,0,0,0,0,0,0x30f180,0x717600,0xc3fc00,0x1168200,0x1570680,0x1a98c80,0x1ea1100,0x23c9700,0x27d1b80,0x2cfa180,0x3102600,0x362ac00,0x3b53200,0x3f5b680,0x4483c80,0x68880,0x4daa980,0x72600,0x55b1500,0xd79700,0xd79700,0xd79700,0xd79700,0xd79700,0xd79700,0xd79700,0xd79700,0xd79700,0xd79700,0xd79700,0xd79700,0xd79700,281,1089,2081,3073,3881,4873,5681,6673,7481,8473,9281,10273,11265,12073,13065,13860,14850,15645,16451,2632,2632,2632,2632,2632,20,19,0,0,0,0,0,0,0x30fc00,0x718080,0xc40680,0x1168c80,0x1571100,0x1a99700,0x1ea1b80,0x23ca180,0x27d2600,0x2cfac00,0x3103080,0x362b680,0x3b53c80,0x3f5c100,0x4484700,0x4884300,0x4dab400,0x51ab000,0x55b1f80,0xec8c80,0xec8c80,0xec8c80,0xec8c80,0xec8c80,0xec8c80,0xec8c80,0xec8c80,0xec8c80,0xec8c80,0xec8c80,0xec8c80,0xec8c80,282,1090,2082,3074,3882,4874,5682,6674,7482,8474,9282,10274,11266,12074,13066,13861,14851,15646,16452,17206,17731,18228,18228,18228,22,21,0,0,0,0,0,0,0x310680,0x718b00,0xc41100,0x1169700,0x1571b80,0x1a9a180,0x1ea2600,0x23cac00,0x27d3080,0x2cfb680,0x3103b00,0x362c100,0x3b54700,0x3f5cb80,0x4485180,0x4884d80,0x4dabe80,0x51aba80,0x55b2a00,0x5997780,0x5c3eb00,0x5ed3880,0x5ed3880,0x5ed3880,0x5ed3880,0x5ed3880,0x5ed3880,0x5ed3880,0x5ed3880,0x5ed3880,0x5ed3880,0x5ed3880,283,1091,2083,3075,3883,4875,5683,6675,7483,8475,9283,10275,11267,12075,13067,13862,14852,15647,16453,17207,17732,17732,17732,17732,21,20,0,0,0,0,0,0,0x311100,0x719580,0xc41b80,0x116a180,0x1572600,0x1a9ac00,0x1ea3080,0x23cb680,0x27d3b00,0x2cfc100,0x3104580,0x362cb80,0x3b55180,0x3f5d600,0x4485c00,0x4885800,0x4dac900,0x51ac500,0x55b3480,0x5998200,0x5c3f580,0x5c3f580,0x5c3f580,0x5c3f580,0x5c3f580,0x5c3f580,0x5c3f580,0x5c3f580,0x5c3f580,0x5c3f580,0x5c3f580,0x5c3f580,284,1092,2084,3076,3884,4876,5684,6676,7484,8476,9284,10276,11268,12076,13068,13863,14853,15648,16454,17208,17733,17733,17733,17733,21,20,0,0,0,0,0,0,0x311b80,0x71a000,0xc42600,0x116ac00,0x1573080,0x1a9b680,0x1ea3b00,0x23cc100,0x27d4580,0x2cfcb80,0x3105000,0x362d600,0x3b55c00,0x3f5e080,0x4486680,0x4886280,0x4dad380,0x51acf80,0x55b3f00,0x5998c80,0x5c40000,0x5c40000,0x5c40000,0x5c40000,0x5c40000,0x5c40000,0x5c40000,0x5c40000,0x5c40000,0x5c40000,0x5c40000,0x5c40000,285,1093,2085,3077,3885,4877,5685,6677,7485,8477,9285,10277,11269,12077,13069,13864,14854,15649,16455,2888,2888,2888,2888,2888,20,19,0,0,0,0,0,0,0x312600,0x71aa80,0xc43080,0x116b680,0x1573b00,0x1a9c100,0x1ea4580,0x23ccb80,0x27d5000,0x2cfd600,0x3105a80,0x362e080,0x3b56680,0x3f5eb00,0x4487100,0x4886d00,0x4dade00,0x51ada00,0x55b4980,0x1018200,0x1018200,0x1018200,0x1018200,0x1018200,0x1018200,0x1018200,0x1018200,0x1018200,0x1018200,0x1018200,0x1018200,0x1018200,286,1094,2086,3078,3886,4878,5686,6678,7486,8478,9286,10278,11270,12078,13070,13865,14855,15650,16640,3144,17734,17734,17734,17734,21,20,0,0,0,0,0,0,0x313080,0x71b500,0xc43b00,0x116c100,0x1574580,0x1a9cb80,0x1ea5000,0x23cd600,0x27d5a80,0x2cfe080,0x3106500,0x362eb00,0x3b57100,0x3f5f580,0x4487b80,0x4887780,0x4dae880,0x51ae480,0x56d5580,0x1167780,0x5c40a80,0x5c40a80,0x5c40a80,0x5c40a80,0x5c40a80,0x5c40a80,0x5c40a80,0x5c40a80,0x5c40a80,0x5c40a80,0x5c40a80,0x5c40a80,287,1095,2087,3079,3887,4879,5687,6679,7487,8479,9287,10279,11271,12079,13071,13866,14856,15651,16641,17209,17735,17735,17735,17735,21,20,0,0,0,0,0,0,0x313b00,0x71bf80,0xc44580,0x116cb80,0x1575000,0x1a9d600,0x1ea5a80,0x23ce080,0x27d6500,0x2cfeb00,0x3106f80,0x362f580,0x3b57b80,0x3f60000,0x4488600,0x4888200,0x4daf300,0x51aef00,0x56d6000,0x5999700,0x5c41500,0x5c41500,0x5c41500,0x5c41500,0x5c41500,0x5c41500,0x5c41500,0x5c41500,0x5c41500,0x5c41500,0x5c41500,0x5c41500,288,1280,2088,3080,3888,4880,5688,6680,7488,8480,9472,10280,11272,12080,13072,18445,14857,18460,16642,3400,3400,3400,3400,3400,20,19,0,0,0,0,0,0,0x314580,0x83cb80,0xc45000,0x116d600,0x1575a80,0x1a9e080,0x1ea6500,0x23ceb00,0x27d6f80,0x2cff580,0x3227b80,0x3630000,0x3b58600,0x3f60a80,0x4489080,0x69300,0x4dafd80,0x73080,0x56d6a80,0x12b6d00,0x12b6d00,0x12b6d00,0x12b6d00,0x12b6d00,0x12b6d00,0x12b6d00,0x12b6d00,0x12b6d00,0x12b6d00,0x12b6d00,0x12b6d00,0x12b6d00,289,1281,2089,3081,3889,4881,5689,6681,7489,8481,9473,10281,11273,12081,13073,13867,14858,15652,16643,3656,3656,3656,3656,3656,20,19,0,0,0,0,0,0,0x315000,0x83d600,0xc45a80,0x116e080,0x1576500,0x1a9eb00,0x1ea6f80,0x23cf580,0x27d7a00,0x2d00000,0x3228600,0x3630a80,0x3b59080,0x3f61500,0x4489b00,0x4888c80,0x4db0800,0x51af980,0x56d7500,0x1406280,0x1406280,0x1406280,0x1406280,0x1406280,0x1406280,0x1406280,0x1406280,0x1406280,0x1406280,0x1406280,0x1406280,0x1406280,290,1282,2090,3082,3890,4882,5690,6682,7490,8482,9474,10282,11274,12082,13074,13868,14859,15653,16644,17210,17920,18229,18229,18229,22,21,0,0,0,0,0,0,0x315a80,0x83e080,0xc46500,0x116eb00,0x1576f80,0x1a9f580,0x1ea7a00,0x23d0000,0x27d8480,0x2d00a80,0x3229080,0x3631500,0x3b59b00,0x3f61f80,0x448a580,0x4889700,0x4db1280,0x51b0400,0x56d7f80,0x599a180,0x5d62100,0x5ed4300,0x5ed4300,0x5ed4300,0x5ed4300,0x5ed4300,0x5ed4300,0x5ed4300,0x5ed4300,0x5ed4300,0x5ed4300,0x5ed4300,291,1283,2091,3083,3891,4883,5691,6683,7491,8483,9475,10283,11275,12083,13075,13869,14860,15654,16645,17211,17921,17921,17921,17921,21,20,0,0,0,0,0,0,0x316500,0x83eb00,0xc46f80,0x116f580,0x1577a00,0x1aa0000,0x1ea8480,0x23d0a80,0x27d8f00,0x2d01500,0x3229b00,0x3631f80,0x3b5a580,0x3f62a00,0x448b000,0x488a180,0x4db1d00,0x51b0e80,0x56d8a00,0x599ac00,0x5d62b80,0x5d62b80,0x5d62b80,0x5d62b80,0x5d62b80,0x5d62b80,0x5d62b80,0x5d62b80,0x5d62b80,0x5d62b80,0x5d62b80,0x5d62b80,292,1284,2092,3084,3892,4884,5692,6684,7492,8484,9476,10284,11276,12084,13076,13870,14861,15655,16646,17212,17922,17922,17922,17922,21,20,0,0,0,0,0,0,0x316f80,0x83f580,0xc47a00,0x1170000,0x1578480,0x1aa0a80,0x1ea8f00,0x23d1500,0x27d9980,0x2d01f80,0x322a580,0x3632a00,0x3b5b000,0x3f63480,0x448ba80,0x488ac00,0x4db2780,0x51b1900,0x56d9480,0x599b680,0x5d63600,0x5d63600,0x5d63600,0x5d63600,0x5d63600,0x5d63600,0x5d63600,0x5d63600,0x5d63600,0x5d63600,0x5d63600,0x5d63600,293,1285,2093,3085,3893,4885,5693,6685,7493,8485,9477,10285,11277,12085,13077,13871,14862,15656,16647,3912,3912,3912,3912,3912,20,19,0,0,0,0,0,0,0x317a00,0x840000,0xc48480,0x1170a80,0x1578f00,0x1aa1500,0x1ea9980,0x23d1f80,0x27da400,0x2d02a00,0x322b000,0x3633480,0x3b5ba80,0x3f63f00,0x448c500,0x488b680,0x4db3200,0x51b2380,0x56d9f00,0x1555800,0x1555800,0x1555800,0x1555800,0x1555800,0x1555800,0x1555800,0x1555800,0x1555800,0x1555800,0x1555800,0x1555800,0x1555800,294,1286,2094,3086,3894,4886,5694,6686,7494,8486,9478,10286,11278,12086,13078,13872,14863,15657,16648,4168,17923,17923,17923,17923,21,20,0,0,0,0,0,0,0x318480,0x840a80,0xc48f00,0x1171500,0x1579980,0x1aa1f80,0x1eaa400,0x23d2a00,0x27dae80,0x2d03480,0x322ba80,0x3633f00,0x3b5c500,0x3f64980,0x448cf80,0x488c100,0x4db3c80,0x51b2e00,0x56da980,0x16a4d80,0x5d64080,0x5d64080,0x5d64080,0x5d64080,0x5d64080,0x5d64080,0x5d64080,0x5d64080,0x5d64080,0x5d64080,0x5d64080,0x5d64080,295,1287,2095,3087,3895,4887,5695,6687,7495,8487,9479,10287,11279,12087,13079,13873,14864,15658,16649,17213,17924,17924,17924,17924,21,20,0,0,0,0,0,0,0x318f00,0x841500,0xc49980,0x1171f80,0x157a400,0x1aa2a00,0x1eaae80,0x23d3480,0x27db900,0x2d03f00,0x322c500,0x3634980,0x3b5cf80,0x3f65400,0x448da00,0x488cb80,0x4db4700,0x51b3880,0x56db400,0x599c100,0x5d64b00,0x5d64b00,0x5d64b00,0x5d64b00,0x5d64b00,0x5d64b00,0x5d64b00,0x5d64b00,0x5d64b00,0x5d64b00,0x5d64b00,0x5d64b00,296,1288,2096,3088,3896,4888,5696,6688,7680,8488,9480,10288,11280,12088,13080,18446,14865,18461,16650,4424,4424,4424,4424,4424,20,19,0,0,0,0,0,0,0x319980,0x841f80,0xc4a400,0x1172a00,0x157ae80,0x1aa3480,0x1eab900,0x23d3f00,0x28fc500,0x2d04980,0x322cf80,0x3635400,0x3b5da00,0x3f65e80,0x448e480,0x69d80,0x4db5180,0x73b00,0x56dbe80,0x17f4300,0x17f4300,0x17f4300,0x17f4300,0x17f4300,0x17f4300,0x17f4300,0x17f4300,0x17f4300,0x17f4300,0x17f4300,0x17f4300,0x17f4300,297,1289,2097,3089,3897,4889,5697,6689,7681,8489,9481,10289,11281,12089,13081,13874,14866,15659,16651,4680,4680,4680,4680,4680,20,19,0,0,0,0,0,0,0x31a400,0x842a00,0xc4ae80,0x1173480,0x157b900,0x1aa3f00,0x1eac380,0x23d4980,0x28fcf80,0x2d05400,0x322da00,0x3635e80,0x3b5e480,0x3f66900,0x448ef00,0x488d600,0x4db5c00,0x51b4300,0x56dc900,0x1943880,0x1943880,0x1943880,0x1943880,0x1943880,0x1943880,0x1943880,0x1943880,0x1943880,0x1943880,0x1943880,0x1943880,0x1943880,298,1290,2098,3090,3898,4890,5698,6690,7682,8490,9482,10290,11282,12090,13082,13875,14867,15660,16652,17214,17925,18230,18230,18230,22,21,0,0,0,0,0,0,0x31ae80,0x843480,0xc4b900,0x1173f00,0x157c380,0x1aa4980,0x1eace00,0x23d5400,0x28fda00,0x2d05e80,0x322e480,0x3636900,0x3b5ef00,0x3f67380,0x448f980,0x488e080,0x4db6680,0x51b4d80,0x56dd380,0x599cb80,0x5d65580,0x5ed4d80,0x5ed4d80,0x5ed4d80,0x5ed4d80,0x5ed4d80,0x5ed4d80,0x5ed4d80,0x5ed4d80,0x5ed4d80,0x5ed4d80,0x5ed4d80,299,1291,2099,3091,3899,4891,5699,6691,7683,8491,9483,10291,11283,12091,13083,13876,14868,15661,16653,17215,17926,17926,17926,17926,21,20,0,0,0,0,0,0,0x31b900,0x843f00,0xc4c380,0x1174980,0x157ce00,0x1aa5400,0x1ead880,0x23d5e80,0x28fe480,0x2d06900,0x322ef00,0x3637380,0x3b5f980,0x3f67e00,0x4490400,0x488eb00,0x4db7100,0x51b5800,0x56dde00,0x599d600,0x5d66000,0x5d66000,0x5d66000,0x5d66000,0x5d66000,0x5d66000,0x5d66000,0x5d66000,0x5d66000,0x5d66000,0x5d66000,0x5d66000,300,1292,2100,3092,3900,4892,5700,6692,7684,8492,9484,10292,11284,12092,13084,13877,14869,15662,16654,17216,17927,17927,17927,17927,21,20,0,0,0,0,0,0,0x31c380,0x844980,0xc4ce00,0x1175400,0x157d880,0x1aa5e80,0x1eae300,0x23d6900,0x28fef00,0x2d07380,0x322f980,0x3637e00,0x3b60400,0x3f68880,0x4490e80,0x488f580,0x4db7b80,0x51b6280,0x56de880,0x599e080,0x5d66a80,0x5d66a80,0x5d66a80,0x5d66a80,0x5d66a80,0x5d66a80,0x5d66a80,0x5d66a80,0x5d66a80,0x5d66a80,0x5d66a80,0x5d66a80,301,1293,2101,3093,3901,4893,5701,6693,7685,8493,9485,10293,11285,12093,13085,13878,14870,15663,16655,4936,4936,4936,4936,4936,20,19,0,0,0,0,0,0,0x31ce00,0x845400,0xc4d880,0x1175e80,0x157e300,0x1aa6900,0x1eaed80,0x23d7380,0x28ff980,0x2d07e00,0x3230400,0x3638880,0x3b60e80,0x3f69300,0x4491900,0x4890000,0x4db8600,0x51b6d00,0x56df300,0x1a92e00,0x1a92e00,0x1a92e00,0x1a92e00,0x1a92e00,0x1a92e00,0x1a92e00,0x1a92e00,0x1a92e00,0x1a92e00,0x1a92e00,0x1a92e00,0x1a92e00,302,1294,2102,3094,3902,4894,5702,6694,7686,8494,9486,10294,11286,12094,13086,13879,14871,15664,16656,5192,17928,17928,17928,17928,21,20,0,0,0,0,0,0,0x31d880,0x845e80,0xc4e300,0x1176900,0x157ed80,0x1aa7380,0x1eaf800,0x23d7e00,0x2900400,0x2d08880,0x3230e80,0x3639300,0x3b61900,0x3f69d80,0x4492380,0x4890a80,0x4db9080,0x51b7780,0x56dfd80,0x1be2380,0x5d67500,0x5d67500,0x5d67500,0x5d67500,0x5d67500,0x5d67500,0x5d67500,0x5d67500,0x5d67500,0x5d67500,0x5d67500,0x5d67500,303,1295,2103,3095,3903,4895,5703,6695,7687,8495,9487,10295,11287,12095,13087,13880,14872,15665,16657,17217,17929,17929,17929,17929,21,20,0,0,0,0,0,0,0x31e300,0x846900,0xc4ed80,0x1177380,0x157f800,0x1aa7e00,0x1eb0280,0x23d8880,0x2900e80,0x2d09300,0x3231900,0x3639d80,0x3b62380,0x3f6a800,0x4492e00,0x4891500,0x4db9b00,0x51b8200,0x56e0800,0x599eb00,0x5d67f80,0x5d67f80,0x5d67f80,0x5d67f80,0x5d67f80,0x5d67f80,0x5d67f80,0x5d67f80,0x5d67f80,0x5d67f80,0x5d67f80,0x5d67f80,304,1296,2104,3096,3904,4896,5888,6696,7688,8496,9488,10296,11288,12096,13088,13881,14873,15666,16658,17218,17930,17930,17930,17930,21,20,0,0,0,0,0,0,0x31ed80,0x847380,0xc4f800,0x1177e00,0x1580280,0x1aa8880,0x1fd0e80,0x23d9300,0x2901900,0x2d09d80,0x3232380,0x363a800,0x3b62e00,0x3f6b280,0x4493880,0x4891f80,0x4dba580,0x51b8c80,0x56e1280,0x599f580,0x5d68a00,0x5d68a00,0x5d68a00,0x5d68a00,0x5d68a00,0x5d68a00,0x5d68a00,0x5d68a00,0x5d68a00,0x5d68a00,0x5d68a00,0x5d68a00,305,1297,2105,3097,3905,4897,5889,6697,7689,8497,9489,10297,11289,12097,13089,13882,14874,15667,16659,5448,5448,5448,5448,5448,20,19,0,0,0,0,0,0,0x31f800,0x847e00,0xc50280,0x1178880,0x1580d00,0x1aa9300,0x1fd1900,0x23d9d80,0x2902380,0x2d0a800,0x3232e00,0x363b280,0x3b63880,0x3f6bd00,0x4494300,0x4892a00,0x4dbb000,0x51b9700,0x56e1d00,0x1d31900,0x1d31900,0x1d31900,0x1d31900,0x1d31900,0x1d31900,0x1d31900,0x1d31900,0x1d31900,0x1d31900,0x1d31900,0x1d31900,0x1d31900,306,1298,2106,3098,3906,4898,5890,6698,7690,8498,9490,10298,11290,12098,13090,13883,14875,15668,16660,17219,17931,18231,18231,18231,22,21,0,0,0,0,0,0,0x320280,0x848880,0xc50d00,0x1179300,0x1581780,0x1aa9d80,0x1fd2380,0x23da800,0x2902e00,0x2d0b280,0x3233880,0x363bd00,0x3b64300,0x3f6c780,0x4494d80,0x4893480,0x4dbba80,0x51ba180,0x56e2780,0x59a0000,0x5d69480,0x5ed5800,0x5ed5800,0x5ed5800,0x5ed5800,0x5ed5800,0x5ed5800,0x5ed5800,0x5ed5800,0x5ed5800,0x5ed5800,0x5ed5800,307,1299,2107,3099,3907,4899,5891,6699,7691,8499,9491,10299,11291,12099,13091,13884,14876,15669,16661,17220,17932,17932,17932,17932,21,20,0,0,0,0,0,0,0x320d00,0x849300,0xc51780,0x1179d80,0x1582200,0x1aaa800,0x1fd2e00,0x23db280,0x2903880,0x2d0bd00,0x3234300,0x363c780,0x3b64d80,0x3f6d200,0x4495800,0x4893f00,0x4dbc500,0x51bac00,0x56e3200,0x59a0a80,0x5d69f00,0x5d69f00,0x5d69f00,0x5d69f00,0x5d69f00,0x5d69f00,0x5d69f00,0x5d69f00,0x5d69f00,0x5d69f00,0x5d69f00,0x5d69f00,308,1300,2108,3100,3908,4900,5892,6700,7692,8500,9492,10300,11292,12100,13092,13885,14877,15670,16662,17221,17933,17933,17933,17933,21,20,0,0,0,0,0,0,0x321780,0x849d80,0xc52200,0x117a800,0x1582c80,0x1aab280,0x1fd3880,0x23dbd00,0x2904300,0x2d0c780,0x3234d80,0x363d200,0x3b65800,0x3f6dc80,0x4496280,0x4894980,0x4dbcf80,0x51bb680,0x56e3c80,0x59a1500,0x5d6a980,0x5d6a980,0x5d6a980,0x5d6a980,0x5d6a980,0x5d6a980,0x5d6a980,0x5d6a980,0x5d6a980,0x5d6a980,0x5d6a980,0x5d6a980,309,1301,2109,3101,3909,4901,5893,6701,7693,8501,9493,10301,11293,12101,13093,13886,14878,15671,16663,5704,5704,5704,5704,5704,20,19,0,0,0,0,0,0,0x322200,0x84a800,0xc52c80,0x117b280,0x1583700,0x1aabd00,0x1fd4300,0x23dc780,0x2904d80,0x2d0d200,0x3235800,0x363dc80,0x3b66280,0x3f6e700,0x4496d00,0x4895400,0x4dbda00,0x51bc100,0x56e4700,0x1e80e80,0x1e80e80,0x1e80e80,0x1e80e80,0x1e80e80,0x1e80e80,0x1e80e80,0x1e80e80,0x1e80e80,0x1e80e80,0x1e80e80,0x1e80e80,0x1e80e80,310,1302,2110,3102,3910,4902,5894,6702,7694,8502,9494,10302,11294,12102,13094,13887,14879,15672,16664,5960,17934,17934,17934,17934,21,20,0,0,0,0,0,0,0x322c80,0x84b280,0xc53700,0x117bd00,0x1584180,0x1aac780,0x1fd4d80,0x23dd200,0x2905800,0x2d0dc80,0x3236280,0x363e700,0x3b66d00,0x3f6f180,0x4497780,0x4895e80,0x4dbe480,0x51bcb80,0x56e5180,0x1fd0400,0x5d6b400,0x5d6b400,0x5d6b400,0x5d6b400,0x5d6b400,0x5d6b400,0x5d6b400,0x5d6b400,0x5d6b400,0x5d6b400,0x5d6b400,0x5d6b400,311,1303,2111,3103,3911,4903,5895,6703,7695,8503,9495,10303,11295,12103,13095,13888,14880,15673,16665,17222,17935,17935,17935,17935,21,20,0,0,0,0,0,0,0x323700,0x84bd00,0xc54180,0x117c780,0x1584c00,0x1aad200,0x1fd5800,0x23ddc80,0x2906280,0x2d0e700,0x3236d00,0x363f180,0x3b67780,0x3f6fc00,0x4498200,0x4896900,0x4dbef00,0x51bd600,0x56e5c00,0x59a1f80,0x5d6be80,0x5d6be80,0x5d6be80,0x5d6be80,0x5d6be80,0x5d6be80,0x5d6be80,0x5d6be80,0x5d6be80,0x5d6be80,0x5d6be80,0x5d6be80,312,1304,2112,3104,4096,4904,5896,6704,7696,8504,9496,10304,11296,12288,13096,13889,14881,15674,16666,17223,17936,17936,17936,17936,21,20,0,0,0,0,0,0,0x324180,0x84c780,0xc54c00,0x117d200,0x16a5800,0x1aadc80,0x1fd6280,0x23de700,0x2906d00,0x2d0f180,0x3237780,0x363fc00,0x3b68200,0x4090800,0x4498c80,0x4897380,0x4dbf980,0x51be080,0x56e6680,0x59a2a00,0x5d6c900,0x5d6c900,0x5d6c900,0x5d6c900,0x5d6c900,0x5d6c900,0x5d6c900,0x5d6c900,0x5d6c900,0x5d6c900,0x5d6c900,0x5d6c900,313,1305,2113,3105,4097,4905,5897,6705,7697,8505,9497,10305,11297,12289,13097,13890,14882,15675,16667,6216,6216,6216,6216,6216,20,19,0,0,0,0,0,0,0x324c00,0x84d200,0xc55680,0x117dc80,0x16a6280,0x1aae700,0x1fd6d00,0x23df180,0x2907780,0x2d0fc00,0x3238200,0x3640680,0x3b68c80,0x4091280,0x4499700,0x4897e00,0x4dc0400,0x51beb00,0x56e7100,0x211f980,0x211f980,0x211f980,0x211f980,0x211f980,0x211f980,0x211f980,0x211f980,0x211f980,0x211f980,0x211f980,0x211f980,0x211f980,314,1306,2114,3106,4098,4906,5898,6706,7698,8506,9498,10306,11298,12290,13098,13891,14883,15676,16668,17408,17937,18232,18232,18232,22,21,0,0,0,0,0,0,0x325680,0x84dc80,0xc56100,0x117e700,0x16a6d00,0x1aaf180,0x1fd7780,0x23dfc00,0x2908200,0x2d10680,0x3238c80,0x3641100,0x3b69700,0x4091d00,0x449a180,0x4898880,0x4dc0e80,0x51bf580,0x56e7b80,0x5ac3600,0x5d6d380,0x5ed6280,0x5ed6280,0x5ed6280,0x5ed6280,0x5ed6280,0x5ed6280,0x5ed6280,0x5ed6280,0x5ed6280,0x5ed6280,0x5ed6280,315,1307,2115,3107,4099,4907,5899,6707,7699,8507,9499,10307,11299,12291,13099,13892,14884,15677,16669,17409,17938,17938,17938,17938,21,20,0,0,0,0,0,0,0x326100,0x84e700,0xc56b80,0x117f180,0x16a7780,0x1aafc00,0x1fd8200,0x23e0680,0x2908c80,0x2d11100,0x3239700,0x3641b80,0x3b6a180,0x4092780,0x449ac00,0x4899300,0x4dc1900,0x51c0000,0x56e8600,0x5ac4080,0x5d6de00,0x5d6de00,0x5d6de00,0x5d6de00,0x5d6de00,0x5d6de00,0x5d6de00,0x5d6de00,0x5d6de00,0x5d6de00,0x5d6de00,0x5d6de00,316,1308,2116,3108,4100,4908,5900,6708,7700,8508,9500,10308,11300,12292,13100,13893,14885,15678,16670,17410,17939,17939,17939,17939,21,20,0,0,0,0,0,0,0x326b80,0x84f180,0xc57600,0x117fc00,0x16a8200,0x1ab0680,0x1fd8c80,0x23e1100,0x2909700,0x2d11b80,0x323a180,0x3642600,0x3b6ac00,0x4093200,0x449b680,0x4899d80,0x4dc2380,0x51c0a80,0x56e9080,0x5ac4b00,0x5d6e880,0x5d6e880,0x5d6e880,0x5d6e880,0x5d6e880,0x5d6e880,0x5d6e880,0x5d6e880,0x5d6e880,0x5d6e880,0x5d6e880,0x5d6e880,317,1309,2117,3109,4101,4909,5901,6709,7701,8509,9501,10309,11301,12293,13101,13894,14886,15679,16671,6472,6472,6472,6472,6472,20,19,0,0,0,0,0,0,0x327600,0x84fc00,0xc58080,0x1180680,0x16a8c80,0x1ab1100,0x1fd9700,0x23e1b80,0x290a180,0x2d12600,0x323ac00,0x3643080,0x3b6b680,0x4093c80,0x449c100,0x489a800,0x4dc2e00,0x51c1500,0x56e9b00,0x226ef00,0x226ef00,0x226ef00,0x226ef00,0x226ef00,0x226ef00,0x226ef00,0x226ef00,0x226ef00,0x226ef00,0x226ef00,0x226ef00,0x226ef00,318,1310,2118,3110,4102,4910,5902,6710,7702,8510,9502,10310,11302,12294,13102,13895,14887,15680,16672,6728,17940,17940,17940,17940,21,20,0,0,0,0,0,0,0x328080,0x850680,0xc58b00,0x1181100,0x16a9700,0x1ab1b80,0x1fda180,0x23e2600,0x290ac00,0x2d13080,0x323b680,0x3643b00,0x3b6c100,0x4094700,0x449cb80,0x489b280,0x4dc3880,0x51c1f80,0x56ea580,0x23be480,0x5d6f300,0x5d6f300,0x5d6f300,0x5d6f300,0x5d6f300,0x5d6f300,0x5d6f300,0x5d6f300,0x5d6f300,0x5d6f300,0x5d6f300,0x5d6f300,319,1311,2119,3111,4103,4911,5903,6711,7703,8511,9503,10311,11303,12295,13103,14080,14888,15681,16673,17411,17941,17941,17941,17941,21,20,0,0,0,0,0,0,0x328b00,0x851100,0xc59580,0x1181b80,0x16aa180,0x1ab2600,0x1fdac00,0x23e3080,0x290b680,0x2d13b00,0x323c100,0x3644580,0x3b6cb80,0x4095180,0x449d600,0x49bbe80,0x4dc4300,0x51c2a00,0x56eb000,0x5ac5580,0x5d6fd80,0x5d6fd80,0x5d6fd80,0x5d6fd80,0x5d6fd80,0x5d6fd80,0x5d6fd80,0x5d6fd80,0x5d6fd80,0x5d6fd80,0x5d6fd80,0x5d6fd80,320,1312,2304,3112,4104,4912,5904,6712,7704,8512,9504,10496,11304,12296,13104,14081,14889,15682,16674,17412,17942,17942,17942,17942,21,20,0,0,0,0,0,0,0x329580,0x851b80,0xd7a180,0x1182600,0x16aac00,0x1ab3080,0x1fdb680,0x23e3b00,0x290c100,0x2d14580,0x323cb80,0x3765180,0x3b6d600,0x4095c00,0x449e080,0x49bc900,0x4dc4d80,0x51c3480,0x56eba80,0x5ac6000,0x5d70800,0x5d70800,0x5d70800,0x5d70800,0x5d70800,0x5d70800,0x5d70800,0x5d70800,0x5d70800,0x5d70800,0x5d70800,0x5d70800,321,1313,2305,3113,4105,4913,5905,6713,7705,8513,9505,10497,11305,12297,13105,14082,14890,15683,16675,6984,6984,6984,6984,6984,20,19,0,0,0,0,0,0,0x32a000,0x852600,0xd7ac00,0x1183080,0x16ab680,0x1ab3b00,0x1fdc100,0x23e4580,0x290cb80,0x2d15000,0x323d600,0x3765c00,0x3b6e080,0x4096680,0x449eb00,0x49bd380,0x4dc5800,0x51c3f00,0x56ec500,0x250da00,0x250da00,0x250da00,0x250da00,0x250da00,0x250da00,0x250da00,0x250da00,0x250da00,0x250da00,0x250da00,0x250da00,0x250da00,322,1314,2306,3114,4106,4914,5906,6714,7706,8514,9506,10498,11306,12298,13106,14083,14891,15684,16676,17413,17943,18233,18233,18233,22,21,0,0,0,0,0,0,0x32aa80,0x853080,0xd7b680,0x1183b00,0x16ac100,0x1ab4580,0x1fdcb80,0x23e5000,0x290d600,0x2d15a80,0x323e080,0x3766680,0x3b6eb00,0x4097100,0x449f580,0x49bde00,0x4dc6280,0x51c4980,0x56ecf80,0x5ac6a80,0x5d71280,0x5ed6d00,0x5ed6d00,0x5ed6d00,0x5ed6d00,0x5ed6d00,0x5ed6d00,0x5ed6d00,0x5ed6d00,0x5ed6d00,0x5ed6d00,0x5ed6d00,323,1315,2307,3115,4107,4915,5907,6715,7707,8515,9507,10499,11307,12299,13107,14084,14892,15685,16677,17414,17944,17944,17944,17944,21,20,0,0,0,0,0,0,0x32b500,0x853b00,0xd7c100,0x1184580,0x16acb80,0x1ab5000,0x1fdd600,0x23e5a80,0x290e080,0x2d16500,0x323eb00,0x3767100,0x3b6f580,0x4097b80,0x44a0000,0x49be880,0x4dc6d00,0x51c5400,0x56eda00,0x5ac7500,0x5d71d00,0x5d71d00,0x5d71d00,0x5d71d00,0x5d71d00,0x5d71d00,0x5d71d00,0x5d71d00,0x5d71d00,0x5d71d00,0x5d71d00,0x5d71d00,324,1316,2308,3116,4108,4916,5908,6716,7708,8516,9508,10500,11308,12300,13108,14085,14893,15686,16678,17415,17945,17945,17945,17945,21,20,0,0,0,0,0,0,0x32bf80,0x854580,0xd7cb80,0x1185000,0x16ad600,0x1ab5a80,0x1fde080,0x23e6500,0x290eb00,0x2d16f80,0x323f580,0x3767b80,0x3b70000,0x4098600,0x44a0a80,0x49bf300,0x4dc7780,0x51c5e80,0x56ee480,0x5ac7f80,0x5d72780,0x5d72780,0x5d72780,0x5d72780,0x5d72780,0x5d72780,0x5d72780,0x5d72780,0x5d72780,0x5d72780,0x5d72780,0x5d72780,325,1317,2309,3117,4109,4917,5909,6717,7709,8517,9509,10501,11309,12301,13109,14086,14894,15687,16679,7240,7240,7240,7240,7240,20,19,0,0,0,0,0,0,0x32ca00,0x855000,0xd7d600,0x1185a80,0x16ae080,0x1ab6500,0x1fdeb00,0x23e6f80,0x290f580,0x2d17a00,0x3240000,0x3768600,0x3b70a80,0x4099080,0x44a1500,0x49bfd80,0x4dc8200,0x51c6900,0x56eef00,0x265cf80,0x265cf80,0x265cf80,0x265cf80,0x265cf80,0x265cf80,0x265cf80,0x265cf80,0x265cf80,0x265cf80,0x265cf80,0x265cf80,0x265cf80,326,1318,2310,3118,4110,4918,5910,6718,7710,8518,9510,10502,11310,12302,13110,14087,14895,15872,16680,7496,17946,17946,17946,17946,21,20,0,0,0,0,0,0,0x32d480,0x855a80,0xd7e080,0x1186500,0x16aeb00,0x1ab6f80,0x1fdf580,0x23e7a00,0x2910000,0x2d18480,0x3240a80,0x3769080,0x3b71500,0x4099b00,0x44a1f80,0x49c0800,0x4dc8c80,0x52e7500,0x56ef980,0x27ac500,0x5d73200,0x5d73200,0x5d73200,0x5d73200,0x5d73200,0x5d73200,0x5d73200,0x5d73200,0x5d73200,0x5d73200,0x5d73200,0x5d73200,327,1319,2311,3119,4111,4919,5911,6719,7711,8519,9511,10503,11311,12303,13111,14088,14896,15873,16681,17416,17947,17947,17947,17947,21,20,0,0,0,0,0,0,0x32df00,0x856500,0xd7eb00,0x1186f80,0x16af580,0x1ab7a00,0x1fe0000,0x23e8480,0x2910a80,0x2d18f00,0x3241500,0x3769b00,0x3b71f80,0x409a580,0x44a2a00,0x49c1280,0x4dc9700,0x52e7f80,0x56f0400,0x5ac8a00,0x5d73c80,0x5d73c80,0x5d73c80,0x5d73c80,0x5d73c80,0x5d73c80,0x5d73c80,0x5d73c80,0x5d73c80,0x5d73c80,0x5d73c80,0x5d73c80,512,1320,2312,3120,4112,4920,5912,6720,7712,8704,9512,10504,11312,12304,13112,14089,14897,15874,16682,17417,17948,17948,17948,17948,21,20,0,0,0,0,0,0,0x44eb00,0x856f80,0xd7f580,0x1187a00,0x16b0000,0x1ab8480,0x1fe0a80,0x23e8f00,0x2911500,0x2e39b00,0x3241f80,0x376a580,0x3b72a00,0x409b000,0x44a3480,0x49c1d00,0x4dca180,0x52e8a00,0x56f0e80,0x5ac9480,0x5d74700,0x5d74700,0x5d74700,0x5d74700,0x5d74700,0x5d74700,0x5d74700,0x5d74700,0x5d74700,0x5d74700,0x5d74700,0x5d74700,513,1321,2313,3121,4113,4921,5913,6721,7713,8705,9513,10505,11313,12305,13113,14090,14898,15875,16683,7752,7752,7752,7752,7752,20,19,0,0,0,0,0,0,0x44f580,0x857a00,0xd80000,0x1188480,0x16b0a80,0x1ab8f00,0x1fe1500,0x23e9980,0x2911f80,0x2e3a580,0x3242a00,0x376b000,0x3b73480,0x409ba80,0x44a3f00,0x49c2780,0x4dcac00,0x52e9480,0x56f1900,0x28fba80,0x28fba80,0x28fba80,0x28fba80,0x28fba80,0x28fba80,0x28fba80,0x28fba80,0x28fba80,0x28fba80,0x28fba80,0x28fba80,0x28fba80,514,1322,2314,3122,4114,4922,5914,6722,7714,8706,9514,10506,11314,12306,13114,14091,14899,15876,16684,17418,17949,18234,18234,18234,22,21,0,0,0,0,0,0,0x450000,0x858480,0xd80a80,0x1188f00,0x16b1500,0x1ab9980,0x1fe1f80,0x23ea400,0x2912a00,0x2e3b000,0x3243480,0x376ba80,0x3b73f00,0x409c500,0x44a4980,0x49c3200,0x4dcb680,0x52e9f00,0x56f2380,0x5ac9f00,0x5d75180,0x5ed7780,0x5ed7780,0x5ed7780,0x5ed7780,0x5ed7780,0x5ed7780,0x5ed7780,0x5ed7780,0x5ed7780,0x5ed7780,0x5ed7780,515,1323,2315,3123,4115,4923,5915,6723,7715,8707,9515,10507,11315,12307,13115,14092,14900,15877,16685,17419,17950,17950,17950,17950,21,20,0,0,0,0,0,0,0x450a80,0x858f00,0xd81500,0x1189980,0x16b1f80,0x1aba400,0x1fe2a00,0x23eae80,0x2913480,0x2e3ba80,0x3243f00,0x376c500,0x3b74980,0x409cf80,0x44a5400,0x49c3c80,0x4dcc100,0x52ea980,0x56f2e00,0x5aca980,0x5d75c00,0x5d75c00,0x5d75c00,0x5d75c00,0x5d75c00,0x5d75c00,0x5d75c00,0x5d75c00,0x5d75c00,0x5d75c00,0x5d75c00,0x5d75c00,516,1324,2316,3124,4116,4924,5916,6724,7716,8708,9516,10508,11316,12308,13116,14093,14901,15878,16686,17420,17951,17951,17951,17951,21,20,0,0,0,0,0,0,0x451500,0x859980,0xd81f80,0x118a400,0x16b2a00,0x1abae80,0x1fe3480,0x23eb900,0x2913f00,0x2e3c500,0x3244980,0x376cf80,0x3b75400,0x409da00,0x44a5e80,0x49c4700,0x4dccb80,0x52eb400,0x56f3880,0x5acb400,0x5d76680,0x5d76680,0x5d76680,0x5d76680,0x5d76680,0x5d76680,0x5d76680,0x5d76680,0x5d76680,0x5d76680,0x5d76680,0x5d76680,517,1325,2317,3125,4117,4925,5917,6725,7717,8709,9517,10509,11317,12309,13117,14094,14902,15879,16687,8008,8008,8008,8008,8008,20,19,0,0,0,0,0,0,0x451f80,0x85a400,0xd82a00,0x118ae80,0x16b3480,0x1abb900,0x1fe3f00,0x23ec380,0x2914980,0x2e3cf80,0x3245400,0x376da00,0x3b75e80,0x409e480,0x44a6900,0x49c5180,0x4dcd600,0x52ebe80,0x56f4300,0x2a4b000,0x2a4b000,0x2a4b000,0x2a4b000,0x2a4b000,0x2a4b000,0x2a4b000,0x2a4b000,0x2a4b000,0x2a4b000,0x2a4b000,0x2a4b000,0x2a4b000,518,1326,2318,3126,4118,4926,5918,6726,7718,8710,9518,10510,11318,12310,13118,14095,14903,15880,16688,8264,17952,17952,17952,17952,21,20,0,0,0,0,0,0,0x452a00,0x85ae80,0xd83480,0x118b900,0x16b3f00,0x1abc380,0x1fe4980,0x23ece00,0x2915400,0x2e3da00,0x3245e80,0x376e480,0x3b76900,0x409ef00,0x44a7380,0x49c5c00,0x4dce080,0x52ec900,0x56f4d80,0x2b9a580,0x5d77100,0x5d77100,0x5d77100,0x5d77100,0x5d77100,0x5d77100,0x5d77100,0x5d77100,0x5d77100,0x5d77100,0x5d77100,0x5d77100,519,1327,2319,3127,4119,4927,5919,6727,7719,8711,9519,10511,11319,12311,13119,14096,14904,15881,16689,17421,17953,17953,17953,17953,21,20,0,0,0,0,0,0,0x453480,0x85b900,0xd83f00,0x118c380,0x16b4980,0x1abce00,0x1fe5400,0x23ed880,0x2915e80,0x2e3e480,0x3246900,0x376ef00,0x3b77380,0x409f980,0x44a7e00,0x49c6680,0x4dceb00,0x52ed380,0x56f5800,0x5acbe80,0x5d77b80,0x5d77b80,0x5d77b80,0x5d77b80,0x5d77b80,0x5d77b80,0x5d77b80,0x5d77b80,0x5d77b80,0x5d77b80,0x5d77b80,0x5d77b80,520,1328,2320,3128,4120,4928,5920,6912,7720,8712,9520,10512,11320,12312,13120,14097,14905,15882,16690,17422,17954,17954,17954,17954,21,20,0,0,0,0,0,0,0x453f00,0x85c380,0xd84980,0x118ce00,0x16b5400,0x1abd880,0x1fe5e80,0x250e480,0x2916900,0x2e3ef00,0x3247380,0x376f980,0x3b77e00,0x40a0400,0x44a8880,0x49c7100,0x4dcf580,0x52ede00,0x56f6280,0x5acc900,0x5d78600,0x5d78600,0x5d78600,0x5d78600,0x5d78600,0x5d78600,0x5d78600,0x5d78600,0x5d78600,0x5d78600,0x5d78600,0x5d78600,521,1329,2321,3129,4121,4929,5921,6913,7721,8713,9521,10513,11321,12313,13121,14098,14906,15883,16691,8520,8520,8520,8520,8520,20,19,0,0,0,0,0,0,0x454980,0x85ce00,0xd85400,0x118d880,0x16b5e80,0x1abe300,0x1fe6900,0x250ef00,0x2917380,0x2e3f980,0x3247e00,0x3770400,0x3b78880,0x40a0e80,0x44a9300,0x49c7b80,0x4dd0000,0x52ee880,0x56f6d00,0x2ce9b00,0x2ce9b00,0x2ce9b00,0x2ce9b00,0x2ce9b00,0x2ce9b00,0x2ce9b00,0x2ce9b00,0x2ce9b00,0x2ce9b00,0x2ce9b00,0x2ce9b00,0x2ce9b00,522,1330,2322,3130,4122,4930,5922,6914,7722,8714,9522,10514,11322,12314,13122,14099,14907,15884,16692,17423,17955,18235,18235,18235,22,21,0,0,0,0,0,0,0x455400,0x85d880,0xd85e80,0x118e300,0x16b6900,0x1abed80,0x1fe7380,0x250f980,0x2917e00,0x2e40400,0x3248880,0x3770e80,0x3b79300,0x40a1900,0x44a9d80,0x49c8600,0x4dd0a80,0x52ef300,0x56f7780,0x5acd380,0x5d79080,0x5ed8200,0x5ed8200,0x5ed8200,0x5ed8200,0x5ed8200,0x5ed8200,0x5ed8200,0x5ed8200,0x5ed8200,0x5ed8200,0x5ed8200,523,1331,2323,3131,4123,4931,5923,6915,7723,8715,9523,10515,11323,12315,13123,14100,14908,15885,16693,17424,17956,17956,17956,17956,21,20,0,0,0,0,0,0,0x455e80,0x85e300,0xd86900,0x118ed80,0x16b7380,0x1abf800,0x1fe7e00,0x2510400,0x2918880,0x2e40e80,0x3249300,0x3771900,0x3b79d80,0x40a2380,0x44aa800,0x49c9080,0x4dd1500,0x52efd80,0x56f8200,0x5acde00,0x5d79b00,0x5d79b00,0x5d79b00,0x5d79b00,0x5d79b00,0x5d79b00,0x5d79b00,0x5d79b00,0x5d79b00,0x5d79b00,0x5d79b00,0x5d79b00,524,1332,2324,3132,4124,4932,5924,6916,7724,8716,9524,10516,11324,12316,13124,14101,14909,15886,16694,17425,17957,17957,17957,17957,21,20,0,0,0,0,0,0,0x456900,0x85ed80,0xd87380,0x118f800,0x16b7e00,0x1ac0280,0x1fe8880,0x2510e80,0x2919300,0x2e41900,0x3249d80,0x3772380,0x3b7a800,0x40a2e00,0x44ab280,0x49c9b00,0x4dd1f80,0x52f0800,0x56f8c80,0x5ace880,0x5d7a580,0x5d7a580,0x5d7a580,0x5d7a580,0x5d7a580,0x5d7a580,0x5d7a580,0x5d7a580,0x5d7a580,0x5d7a580,0x5d7a580,0x5d7a580,525,1333,2325,3133,4125,4933,5925,6917,7725,8717,9525,10517,11325,12317,13125,14102,14910,15887,16695,8776,8776,8776,8776,8776,20,19,0,0,0,0,0,0,0x457380,0x85f800,0xd87e00,0x1190280,0x16b8880,0x1ac0d00,0x1fe9300,0x2511900,0x2919d80,0x2e42380,0x324a800,0x3772e00,0x3b7b280,0x40a3880,0x44abd00,0x49ca580,0x4dd2a00,0x52f1280,0x56f9700,0x2e39080,0x2e39080,0x2e39080,0x2e39080,0x2e39080,0x2e39080,0x2e39080,0x2e39080,0x2e39080,0x2e39080,0x2e39080,0x2e39080,0x2e39080,526,1334,2326,3134,4126,4934,5926,6918,7726,8718,9526,10518,11326,12318,13126,14103,14911,15888,16696,9032,17958,17958,17958,17958,21,20,0,0,0,0,0,0,0x457e00,0x860280,0xd88880,0x1190d00,0x16b9300,0x1ac1780,0x1fe9d80,0x2512380,0x291a800,0x2e42e00,0x324b280,0x3773880,0x3b7bd00,0x40a4300,0x44ac780,0x49cb000,0x4dd3480,0x52f1d00,0x56fa180,0x2f88600,0x5d7b000,0x5d7b000,0x5d7b000,0x5d7b000,0x5d7b000,0x5d7b000,0x5d7b000,0x5d7b000,0x5d7b000,0x5d7b000,0x5d7b000,0x5d7b000,527,1335,2327,3135,4127,4935,5927,6919,7727,8719,9527,10519,11327,12319,13127,14104,14912,15889,16697,17426,17959,17959,17959,17959,21,20,0,0,0,0,0,0,0x458880,0x860d00,0xd89300,0x1191780,0x16b9d80,0x1ac2200,0x1fea800,0x2512e00,0x291b280,0x2e43880,0x324bd00,0x3774300,0x3b7c780,0x40a4d80,0x44ad200,0x49cba80,0x4dd3f00,0x52f2780,0x56fac00,0x5acf300,0x5d7ba80,0x5d7ba80,0x5d7ba80,0x5d7ba80,0x5d7ba80,0x5d7ba80,0x5d7ba80,0x5d7ba80,0x5d7ba80,0x5d7ba80,0x5d7ba80,0x5d7ba80,528,1336,2328,3136,4128,5120,5928,6920,7728,8720,9528,10520,11328,12320,13312,14105,14913,15890,16698,17427,17960,17960,17960,17960,21,20,0,0,0,0,0,0,0x459300,0x861780,0xd89d80,0x1192200,0x16ba800,0x1be2e00,0x1feb280,0x2513880,0x291bd00,0x2e44300,0x324c780,0x3774d80,0x3b7d200,0x40a5800,0x45cde00,0x49cc500,0x4dd4980,0x52f3200,0x56fb680,0x5acfd80,0x5d7c500,0x5d7c500,0x5d7c500,0x5d7c500,0x5d7c500,0x5d7c500,0x5d7c500,0x5d7c500,0x5d7c500,0x5d7c500,0x5d7c500,0x5d7c500,529,1337,2329,3137,4129,5121,5929,6921,7729,8721,9529,10521,11329,12321,13313,14106,14914,15891,16699,9288,9288,9288,9288,9288,20,19,0,0,0,0,0,0,0x459d80,0x862200,0xd8a800,0x1192c80,0x16bb280,0x1be3880,0x1febd00,0x2514300,0x291c780,0x2e44d80,0x324d200,0x3775800,0x3b7dc80,0x40a6280,0x45ce880,0x49ccf80,0x4dd5400,0x52f3c80,0x56fc100,0x30d7b80,0x30d7b80,0x30d7b80,0x30d7b80,0x30d7b80,0x30d7b80,0x30d7b80,0x30d7b80,0x30d7b80,0x30d7b80,0x30d7b80,0x30d7b80,0x30d7b80,530,1338,2330,3138,4130,5122,5930,6922,7730,8722,9530,10522,11330,12322,13314,14107,14915,15892,16700,17428,17961,18236,18236,18236,22,21,0,0,0,0,0,0,0x45a800,0x862c80,0xd8b280,0x1193700,0x16bbd00,0x1be4300,0x1fec780,0x2514d80,0x291d200,0x2e45800,0x324dc80,0x3776280,0x3b7e700,0x40a6d00,0x45cf300,0x49cda00,0x4dd5e80,0x52f4700,0x56fcb80,0x5ad0800,0x5d7cf80,0x5ed8c80,0x5ed8c80,0x5ed8c80,0x5ed8c80,0x5ed8c80,0x5ed8c80,0x5ed8c80,0x5ed8c80,0x5ed8c80,0x5ed8c80,0x5ed8c80,531,1339,2331,3139,4131,5123,5931,6923,7731,8723,9531,10523,11331,12323,13315,14108,14916,15893,16701,17429,17962,17962,17962,17962,21,20,0,0,0,0,0,0,0x45b280,0x863700,0xd8bd00,0x1194180,0x16bc780,0x1be4d80,0x1fed200,0x2515800,0x291dc80,0x2e46280,0x324e700,0x3776d00,0x3b7f180,0x40a7780,0x45cfd80,0x49ce480,0x4dd6900,0x52f5180,0x56fd600,0x5ad1280,0x5d7da00,0x5d7da00,0x5d7da00,0x5d7da00,0x5d7da00,0x5d7da00,0x5d7da00,0x5d7da00,0x5d7da00,0x5d7da00,0x5d7da00,0x5d7da00,532,1340,2332,3140,4132,5124,5932,6924,7732,8724,9532,10524,11332,12324,13316,14109,14917,15894,16702,17430,17963,17963,17963,17963,21,20,0,0,0,0,0,0,0x45bd00,0x864180,0xd8c780,0x1194c00,0x16bd200,0x1be5800,0x1fedc80,0x2516280,0x291e700,0x2e46d00,0x324f180,0x3777780,0x3b7fc00,0x40a8200,0x45d0800,0x49cef00,0x4dd7380,0x52f5c00,0x56fe080,0x5ad1d00,0x5d7e480,0x5d7e480,0x5d7e480,0x5d7e480,0x5d7e480,0x5d7e480,0x5d7e480,0x5d7e480,0x5d7e480,0x5d7e480,0x5d7e480,0x5d7e480,533,1341,2333,3141,4133,5125,5933,6925,7733,8725,9533,10525,11333,12325,13317,14110,14918,15895,16703,9544,9544,9544,9544,9544,20,19,0,0,0,0,0,0,0x45c780,0x864c00,0xd8d200,0x1195680,0x16bdc80,0x1be6280,0x1fee700,0x2516d00,0x291f180,0x2e47780,0x324fc00,0x3778200,0x3b80680,0x40a8c80,0x45d1280,0x49cf980,0x4dd7e00,0x52f6680,0x56feb00,0x3227100,0x3227100,0x3227100,0x3227100,0x3227100,0x3227100,0x3227100,0x3227100,0x3227100,0x3227100,0x3227100,0x3227100,0x3227100,534,1342,2334,3142,4134,5126,5934,6926,7734,8726,9534,10526,11334,12326,13318,14111,14919,15896,16704,9800,17964,17964,17964,17964,21,20,0,0,0,0,0,0,0x45d200,0x865680,0xd8dc80,0x1196100,0x16be700,0x1be6d00,0x1fef180,0x2517780,0x291fc00,0x2e48200,0x3250680,0x3778c80,0x3b81100,0x40a9700,0x45d1d00,0x49d0400,0x4dd8880,0x52f7100,0x56ff580,0x3376680,0x5d7ef00,0x5d7ef00,0x5d7ef00,0x5d7ef00,0x5d7ef00,0x5d7ef00,0x5d7ef00,0x5d7ef00,0x5d7ef00,0x5d7ef00,0x5d7ef00,0x5d7ef00,535,1343,2335,3143,4135,5127,5935,6927,7735,8727,9535,10527,11335,12327,13319,14112,15104,15897,16705,17431,17965,17965,17965,17965,21,20,0,0,0,0,0,0,0x45dc80,0x866100,0xd8e700,0x1196b80,0x16bf180,0x1be7780,0x1fefc00,0x2518200,0x2920680,0x2e48c80,0x3251100,0x3779700,0x3b81b80,0x40aa180,0x45d2780,0x49d0e80,0x4ef9480,0x52f7b80,0x5700000,0x5ad2780,0x5d7f980,0x5d7f980,0x5d7f980,0x5d7f980,0x5d7f980,0x5d7f980,0x5d7f980,0x5d7f980,0x5d7f980,0x5d7f980,0x5d7f980,0x5d7f980,536,1344,2336,3328,4136,5128,5936,6928,7736,8728,9536,10528,11520,12328,13320,14113,15105,15898,16706,17432,17966,17966,17966,17966,21,20,0,0,0,0,0,0,0x45e700,0x866b80,0xd8f180,0x12b7780,0x16bfc00,0x1be8200,0x1ff0680,0x2518c80,0x2921100,0x2e49700,0x3251b80,0x377a180,0x3ca2780,0x40aac00,0x45d3200,0x49d1900,0x4ef9f00,0x52f8600,0x5700a80,0x5ad3200,0x5d80400,0x5d80400,0x5d80400,0x5d80400,0x5d80400,0x5d80400,0x5d80400,0x5d80400,0x5d80400,0x5d80400,0x5d80400,0x5d80400,537,1345,2337,3329,4137,5129,5937,6929,7737,8729,9537,10529,11521,12329,13321,14114,15106,15899,16707,10056,10056,10056,10056,10056,20,19,0,0,0,0,0,0,0x45f180,0x867600,0xd8fc00,0x12b8200,0x16c0680,0x1be8c80,0x1ff1100,0x2519700,0x2921b80,0x2e4a180,0x3252600,0x377ac00,0x3ca3200,0x40ab680,0x45d3c80,0x49d2380,0x4efa980,0x52f9080,0x5701500,0x34c5c00,0x34c5c00,0x34c5c00,0x34c5c00,0x34c5c00,0x34c5c00,0x34c5c00,0x34c5c00,0x34c5c00,0x34c5c00,0x34c5c00,0x34c5c00,0x34c5c00,538,1346,2338,3330,4138,5130,5938,6930,7738,8730,9538,10530,11522,12330,13322,14115,15107,15900,16708,17433,17967,18237,18237,18237,22,21,0,0,0,0,0,0,0x45fc00,0x868080,0xd90680,0x12b8c80,0x16c1100,0x1be9700,0x1ff1b80,0x251a180,0x2922600,0x2e4ac00,0x3253080,0x377b680,0x3ca3c80,0x40ac100,0x45d4700,0x49d2e00,0x4efb400,0x52f9b00,0x5701f80,0x5ad3c80,0x5d80e80,0x5ed9700,0x5ed9700,0x5ed9700,0x5ed9700,0x5ed9700,0x5ed9700,0x5ed9700,0x5ed9700,0x5ed9700,0x5ed9700,0x5ed9700,539,1347,2339,3331,4139,5131,5939,6931,7739,8731,9539,10531,11523,12331,13323,14116,15108,15901,16709,17434,17968,17968,17968,17968,21,20,0,0,0,0,0,0,0x460680,0x868b00,0xd91100,0x12b9700,0x16c1b80,0x1bea180,0x1ff2600,0x251ac00,0x2923080,0x2e4b680,0x3253b00,0x377c100,0x3ca4700,0x40acb80,0x45d5180,0x49d3880,0x4efbe80,0x52fa580,0x5702a00,0x5ad4700,0x5d81900,0x5d81900,0x5d81900,0x5d81900,0x5d81900,0x5d81900,0x5d81900,0x5d81900,0x5d81900,0x5d81900,0x5d81900,0x5d81900,540,1348,2340,3332,4140,5132,5940,6932,7740,8732,9540,10532,11524,12332,13324,14117,15109,15902,16710,17435,17969,17969,17969,17969,21,20,0,0,0,0,0,0,0x461100,0x869580,0xd91b80,0x12ba180,0x16c2600,0x1beac00,0x1ff3080,0x251b680,0x2923b00,0x2e4c100,0x3254580,0x377cb80,0x3ca5180,0x40ad600,0x45d5c00,0x49d4300,0x4efc900,0x52fb000,0x5703480,0x5ad5180,0x5d82380,0x5d82380,0x5d82380,0x5d82380,0x5d82380,0x5d82380,0x5d82380,0x5d82380,0x5d82380,0x5d82380,0x5d82380,0x5d82380,541,1349,2341,3333,4141,5133,5941,6933,7741,8733,9541,10533,11525,12333,13325,14118,15110,15903,16711,10312,10312,10312,10312,10312,20,19,0,0,0,0,0,0,0x461b80,0x86a000,0xd92600,0x12bac00,0x16c3080,0x1beb680,0x1ff3b00,0x251c100,0x2924580,0x2e4cb80,0x3255000,0x377d600,0x3ca5c00,0x40ae080,0x45d6680,0x49d4d80,0x4efd380,0x52fba80,0x5703f00,0x3615180,0x3615180,0x3615180,0x3615180,0x3615180,0x3615180,0x3615180,0x3615180,0x3615180,0x3615180,0x3615180,0x3615180,0x3615180,542,1350,2342,3334,4142,5134,5942,6934,7742,8734,9542,10534,11526,12334,13326,14119,15111,15904,16896,10568,17970,17970,17970,17970,21,20,0,0,0,0,0,0,0x462600,0x86aa80,0xd93080,0x12bb680,0x16c3b00,0x1bec100,0x1ff4580,0x251cb80,0x2925000,0x2e4d600,0x3255a80,0x377e080,0x3ca6680,0x40aeb00,0x45d7100,0x49d5800,0x4efde00,0x52fc500,0x5824b00,0x3764700,0x5d82e00,0x5d82e00,0x5d82e00,0x5d82e00,0x5d82e00,0x5d82e00,0x5d82e00,0x5d82e00,0x5d82e00,0x5d82e00,0x5d82e00,0x5d82e00,543,1351,2343,3335,4143,5135,5943,6935,7743,8735,9543,10535,11527,12335,13327,14120,15112,15905,16897,17436,17971,17971,17971,17971,21,20,0,0,0,0,0,0,0x463080,0x86b500,0xd93b00,0x12bc100,0x16c4580,0x1becb80,0x1ff5000,0x251d600,0x2925a80,0x2e4e080,0x3256500,0x377eb00,0x3ca7100,0x40af580,0x45d7b80,0x49d6280,0x4efe880,0x52fcf80,0x5825580,0x5ad5c00,0x5d83880,0x5d83880,0x5d83880,0x5d83880,0x5d83880,0x5d83880,0x5d83880,0x5d83880,0x5d83880,0x5d83880,0x5d83880,0x5d83880,544,1536,2344,3336,4144,5136,5944,6936,7744,8736,9728,10536,11528,12336,13328,14121,15113,15906,16898,17437,17972,17972,17972,17972,21,20,0,0,0,0,0,0,0x463b00,0x98c100,0xd94580,0x12bcb80,0x16c5000,0x1bed600,0x1ff5a80,0x251e080,0x2926500,0x2e4eb00,0x3377100,0x377f580,0x3ca7b80,0x40b0000,0x45d8600,0x49d6d00,0x4eff300,0x52fda00,0x5826000,0x5ad6680,0x5d84300,0x5d84300,0x5d84300,0x5d84300,0x5d84300,0x5d84300,0x5d84300,0x5d84300,0x5d84300,0x5d84300,0x5d84300,0x5d84300,545,1537,2345,3337,4145,5137,5945,6937,7745,8737,9729,10537,11529,12337,13329,14122,15114,15907,16899,10824,10824,10824,10824,10824,20,19,0,0,0,0,0,0,0x464580,0x98cb80,0xd95000,0x12bd600,0x16c5a80,0x1bee080,0x1ff6500,0x251eb00,0x2926f80,0x2e4f580,0x3377b80,0x3780000,0x3ca8600,0x40b0a80,0x45d9080,0x49d7780,0x4effd80,0x52fe480,0x5826a80,0x38b3c80,0x38b3c80,0x38b3c80,0x38b3c80,0x38b3c80,0x38b3c80,0x38b3c80,0x38b3c80,0x38b3c80,0x38b3c80,0x38b3c80,0x38b3c80,0x38b3c80,546,1538,2346,3338,4146,5138,5946,6938,7746,8738,9730,10538,11530,12338,13330,14123,15115,15908,16900,17438,17973,18238,18238,18238,22,21,0,0,0,0,0,0,0x465000,0x98d600,0xd95a80,0x12be080,0x16c6500,0x1beeb00,0x1ff6f80,0x251f580,0x2927a00,0x2e50000,0x3378600,0x3780a80,0x3ca9080,0x40b1500,0x45d9b00,0x49d8200,0x4f00800,0x52fef00,0x5827500,0x5ad7100,0x5d84d80,0x5eda180,0x5eda180,0x5eda180,0x5eda180,0x5eda180,0x5eda180,0x5eda180,0x5eda180,0x5eda180,0x5eda180,0x5eda180,547,1539,2347,3339,4147,5139,5947,6939,7747,8739,9731,10539,11531,12339,13331,14124,15116,15909,16901,17439,17974,17974,17974,17974,21,20,0,0,0,0,0,0,0x465a80,0x98e080,0xd96500,0x12beb00,0x16c6f80,0x1bef580,0x1ff7a00,0x2520000,0x2928480,0x2e50a80,0x3379080,0x3781500,0x3ca9b00,0x40b1f80,0x45da580,0x49d8c80,0x4f01280,0x52ff980,0x5827f80,0x5ad7b80,0x5d85800,0x5d85800,0x5d85800,0x5d85800,0x5d85800,0x5d85800,0x5d85800,0x5d85800,0x5d85800,0x5d85800,0x5d85800,0x5d85800,548,1540,2348,3340,4148,5140,5948,6940,7748,8740,9732,10540,11532,12340,13332,14125,15117,15910,16902,17440,17975,17975,17975,17975,21,20,0,0,0,0,0,0,0x466500,0x98eb00,0xd96f80,0x12bf580,0x16c7a00,0x1bf0000,0x1ff8480,0x2520a80,0x2928f00,0x2e51500,0x3379b00,0x3781f80,0x3caa580,0x40b2a00,0x45db000,0x49d9700,0x4f01d00,0x5300400,0x5828a00,0x5ad8600,0x5d86280,0x5d86280,0x5d86280,0x5d86280,0x5d86280,0x5d86280,0x5d86280,0x5d86280,0x5d86280,0x5d86280,0x5d86280,0x5d86280,549,1541,2349,3341,4149,5141,5949,6941,7749,8741,9733,10541,11533,12341,13333,14126,15118,15911,16903,11080,11080,11080,11080,11080,20,19,0,0,0,0,0,0,0x466f80,0x98f580,0xd97a00,0x12c0000,0x16c8480,0x1bf0a80,0x1ff8f00,0x2521500,0x2929980,0x2e51f80,0x337a580,0x3782a00,0x3cab000,0x40b3480,0x45dba80,0x49da180,0x4f02780,0x5300e80,0x5829480,0x3a03200,0x3a03200,0x3a03200,0x3a03200,0x3a03200,0x3a03200,0x3a03200,0x3a03200,0x3a03200,0x3a03200,0x3a03200,0x3a03200,0x3a03200,550,1542,2350,3342,4150,5142,5950,6942,7750,8742,9734,10542,11534,12342,13334,14127,15119,15912,16904,11336,17976,17976,17976,17976,21,20,0,0,0,0,0,0,0x467a00,0x990000,0xd98480,0x12c0a80,0x16c8f00,0x1bf1500,0x1ff9980,0x2521f80,0x292a400,0x2e52a00,0x337b000,0x3783480,0x3caba80,0x40b3f00,0x45dc500,0x49dac00,0x4f03200,0x5301900,0x5829f00,0x3b52780,0x5d86d00,0x5d86d00,0x5d86d00,0x5d86d00,0x5d86d00,0x5d86d00,0x5d86d00,0x5d86d00,0x5d86d00,0x5d86d00,0x5d86d00,0x5d86d00,551,1543,2351,3343,4151,5143,5951,6943,7751,8743,9735,10543,11535,12343,13335,14128,15120,15913,16905,17441,17977,17977,17977,17977,21,20,0,0,0,0,0,0,0x468480,0x990a80,0xd98f00,0x12c1500,0x16c9980,0x1bf1f80,0x1ffa400,0x2522a00,0x292ae80,0x2e53480,0x337ba80,0x3783f00,0x3cac500,0x40b4980,0x45dcf80,0x49db680,0x4f03c80,0x5302380,0x582a980,0x5ad9080,0x5d87780,0x5d87780,0x5d87780,0x5d87780,0x5d87780,0x5d87780,0x5d87780,0x5d87780,0x5d87780,0x5d87780,0x5d87780,0x5d87780,552,1544,2352,3344,4152,5144,5952,6944,7936,8744,9736,10544,11536,12344,13336,14129,15121,15914,16906,17442,17978,17978,17978,17978,21,20,0,0,0,0,0,0,0x468f00,0x991500,0xd99980,0x12c1f80,0x16ca400,0x1bf2a00,0x1ffae80,0x2523480,0x2a4ba80,0x2e53f00,0x337c500,0x3784980,0x3cacf80,0x40b5400,0x45dda00,0x49dc100,0x4f04700,0x5302e00,0x582b400,0x5ad9b00,0x5d88200,0x5d88200,0x5d88200,0x5d88200,0x5d88200,0x5d88200,0x5d88200,0x5d88200,0x5d88200,0x5d88200,0x5d88200,0x5d88200,553,1545,2353,3345,4153,5145,5953,6945,7937,8745,9737,10545,11537,12345,13337,14130,15122,15915,16907,11592,11592,11592,11592,11592,20,19,0,0,0,0,0,0,0x469980,0x991f80,0xd9a400,0x12c2a00,0x16cae80,0x1bf3480,0x1ffb900,0x2523f00,0x2a4c500,0x2e54980,0x337cf80,0x3785400,0x3cada00,0x40b5e80,0x45de480,0x49dcb80,0x4f05180,0x5303880,0x582be80,0x3ca1d00,0x3ca1d00,0x3ca1d00,0x3ca1d00,0x3ca1d00,0x3ca1d00,0x3ca1d00,0x3ca1d00,0x3ca1d00,0x3ca1d00,0x3ca1d00,0x3ca1d00,0x3ca1d00,554,1546,2354,3346,4154,5146,5954,6946,7938,8746,9738,10546,11538,12346,13338,14131,15123,15916,16908,17443,17979,18239,18239,18239,22,21,0,0,0,0,0,0,0x46a400,0x992a00,0xd9ae80,0x12c3480,0x16cb900,0x1bf3f00,0x1ffc380,0x2524980,0x2a4cf80,0x2e55400,0x337da00,0x3785e80,0x3cae480,0x40b6900,0x45def00,0x49dd600,0x4f05c00,0x5304300,0x582c900,0x5ada580,0x5d88c80,0x5edac00,0x5edac00,0x5edac00,0x5edac00,0x5edac00,0x5edac00,0x5edac00,0x5edac00,0x5edac00,0x5edac00,0x5edac00,555,1547,2355,3347,4155,5147,5955,6947,7939,8747,9739,10547,11539,12347,13339,14132,15124,15917,16909,17444,17980,17980,17980,17980,21,20,0,0,0,0,0,0,0x46ae80,0x993480,0xd9b900,0x12c3f00,0x16cc380,0x1bf4980,0x1ffce00,0x2525400,0x2a4da00,0x2e55e80,0x337e480,0x3786900,0x3caef00,0x40b7380,0x45df980,0x49de080,0x4f06680,0x5304d80,0x582d380,0x5adb000,0x5d89700,0x5d89700,0x5d89700,0x5d89700,0x5d89700,0x5d89700,0x5d89700,0x5d89700,0x5d89700,0x5d89700,0x5d89700,0x5d89700,556,1548,2356,3348,4156,5148,5956,6948,7940,8748,9740,10548,11540,12348,13340,14133,15125,15918,16910,17445,17981,17981,17981,17981,21,20,0,0,0,0,0,0,0x46b900,0x993f00,0xd9c380,0x12c4980,0x16cce00,0x1bf5400,0x1ffd880,0x2525e80,0x2a4e480,0x2e56900,0x337ef00,0x3787380,0x3caf980,0x40b7e00,0x45e0400,0x49deb00,0x4f07100,0x5305800,0x582de00,0x5adba80,0x5d8a180,0x5d8a180,0x5d8a180,0x5d8a180,0x5d8a180,0x5d8a180,0x5d8a180,0x5d8a180,0x5d8a180,0x5d8a180,0x5d8a180,0x5d8a180,557,1549,2357,3349,4157,5149,5957,6949,7941,8749,9741,10549,11541,12349,13341,14134,15126,15919,16911,11848,11848,11848,11848,11848,20,19,0,0,0,0,0,0,0x46c380,0x994980,0xd9ce00,0x12c5400,0x16cd880,0x1bf5e80,0x1ffe300,0x2526900,0x2a4ef00,0x2e57380,0x337f980,0x3787e00,0x3cb0400,0x40b8880,0x45e0e80,0x49df580,0x4f07b80,0x5306280,0x582e880,0x3df1280,0x3df1280,0x3df1280,0x3df1280,0x3df1280,0x3df1280,0x3df1280,0x3df1280,0x3df1280,0x3df1280,0x3df1280,0x3df1280,0x3df1280,558,1550,2358,3350,4158,5150,5958,6950,7942,8750,9742,10550,11542,12350,13342,14135,15127,15920,16912,12104,17982,17982,17982,17982,21,20,0,0,0,0,0,0,0x46ce00,0x995400,0xd9d880,0x12c5e80,0x16ce300,0x1bf6900,0x1ffed80,0x2527380,0x2a4f980,0x2e57e00,0x3380400,0x3788880,0x3cb0e80,0x40b9300,0x45e1900,0x49e0000,0x4f08600,0x5306d00,0x582f300,0x3f40800,0x5d8ac00,0x5d8ac00,0x5d8ac00,0x5d8ac00,0x5d8ac00,0x5d8ac00,0x5d8ac00,0x5d8ac00,0x5d8ac00,0x5d8ac00,0x5d8ac00,0x5d8ac00,559,1551,2359,3351,4159,5151,5959,6951,7943,8751,9743,10551,11543,12351,13343,14136,15128,15921,16913,17446,17983,17983,17983,17983,21,20,0,0,0,0,0,0,0x46d880,0x995e80,0xd9e300,0x12c6900,0x16ced80,0x1bf7380,0x1fff800,0x2527e00,0x2a50400,0x2e58880,0x3380e80,0x3789300,0x3cb1900,0x40b9d80,0x45e2380,0x49e0a80,0x4f09080,0x5307780,0x582fd80,0x5adc500,0x5d8b680,0x5d8b680,0x5d8b680,0x5d8b680,0x5d8b680,0x5d8b680,0x5d8b680,0x5d8b680,0x5d8b680,0x5d8b680,0x5d8b680,0x5d8b680,560,1552,2360,3352,4160,5152,6144,6952,7944,8752,9744,10552,11544,12352,13344,14137,15129,15922,16914,17447,17984,17984,17984,17984,21,20,0,0,0,0,0,0,0x46e300,0x996900,0xd9ed80,0x12c7380,0x16cf800,0x1bf7e00,0x2120400,0x2528880,0x2a50e80,0x2e59300,0x3381900,0x3789d80,0x3cb2380,0x40ba800,0x45e2e00,0x49e1500,0x4f09b00,0x5308200,0x5830800,0x5adcf80,0x5d8c100,0x5d8c100,0x5d8c100,0x5d8c100,0x5d8c100,0x5d8c100,0x5d8c100,0x5d8c100,0x5d8c100,0x5d8c100,0x5d8c100,0x5d8c100,561,1553,2361,3353,4161,5153,6145,6953,7945,8753,9745,10553,11545,12353,13345,14138,15130,15923,16915,12360,12360,12360,12360,12360,20,19,0,0,0,0,0,0,0x46ed80,0x997380,0xd9f800,0x12c7e00,0x16d0280,0x1bf8880,0x2120e80,0x2529300,0x2a51900,0x2e59d80,0x3382380,0x378a800,0x3cb2e00,0x40bb280,0x45e3880,0x49e1f80,0x4f0a580,0x5308c80,0x5831280,0x408fd80,0x408fd80,0x408fd80,0x408fd80,0x408fd80,0x408fd80,0x408fd80,0x408fd80,0x408fd80,0x408fd80,0x408fd80,0x408fd80,0x408fd80,562,1554,2362,3354,4162,5154,6146,6954,7946,8754,9746,10554,11546,12354,13346,14139,15131,15924,16916,17448,17985,18240,18240,18240,22,21,0,0,0,0,0,0,0x46f800,0x997e00,0xda0280,0x12c8880,0x16d0d00,0x1bf9300,0x2121900,0x2529d80,0x2a52380,0x2e5a800,0x3382e00,0x378b280,0x3cb3880,0x40bbd00,0x45e4300,0x49e2a00,0x4f0b000,0x5309700,0x5831d00,0x5adda00,0x5d8cb80,0x5edb680,0x5edb680,0x5edb680,0x5edb680,0x5edb680,0x5edb680,0x5edb680,0x5edb680,0x5edb680,0x5edb680,0x5edb680,563,1555,2363,3355,4163,5155,6147,6955,7947,8755,9747,10555,11547,12355,13347,14140,15132,15925,16917,17449,17986,17986,17986,17986,21,20,0,0,0,0,0,0,0x470280,0x998880,0xda0d00,0x12c9300,0x16d1780,0x1bf9d80,0x2122380,0x252a800,0x2a52e00,0x2e5b280,0x3383880,0x378bd00,0x3cb4300,0x40bc780,0x45e4d80,0x49e3480,0x4f0ba80,0x530a180,0x5832780,0x5ade480,0x5d8d600,0x5d8d600,0x5d8d600,0x5d8d600,0x5d8d600,0x5d8d600,0x5d8d600,0x5d8d600,0x5d8d600,0x5d8d600,0x5d8d600,0x5d8d600,564,1556,2364,3356,4164,5156,6148,6956,7948,8756,9748,10556,11548,12356,13348,14141,15133,15926,16918,17450,17987,17987,17987,17987,21,20,0,0,0,0,0,0,0x470d00,0x999300,0xda1780,0x12c9d80,0x16d2200,0x1bfa800,0x2122e00,0x252b280,0x2a53880,0x2e5bd00,0x3384300,0x378c780,0x3cb4d80,0x40bd200,0x45e5800,0x49e3f00,0x4f0c500,0x530ac00,0x5833200,0x5adef00,0x5d8e080,0x5d8e080,0x5d8e080,0x5d8e080,0x5d8e080,0x5d8e080,0x5d8e080,0x5d8e080,0x5d8e080,0x5d8e080,0x5d8e080,0x5d8e080,565,1557,2365,3357,4165,5157,6149,6957,7949,8757,9749,10557,11549,12357,13349,14142,15134,15927,16919,12616,12616,12616,12616,12616,20,19,0,0,0,0,0,0,0x471780,0x999d80,0xda2200,0x12ca800,0x16d2c80,0x1bfb280,0x2123880,0x252bd00,0x2a54300,0x2e5c780,0x3384d80,0x378d200,0x3cb5800,0x40bdc80,0x45e6280,0x49e4980,0x4f0cf80,0x530b680,0x5833c80,0x41df300,0x41df300,0x41df300,0x41df300,0x41df300,0x41df300,0x41df300,0x41df300,0x41df300,0x41df300,0x41df300,0x41df300,0x41df300,566,1558,2366,3358,4166,5158,6150,6958,7950,8758,9750,10558,11550,12358,13350,14143,15135,15928,16920,12872,17988,17988,17988,17988,21,20,0,0,0,0,0,0,0x472200,0x99a800,0xda2c80,0x12cb280,0x16d3700,0x1bfbd00,0x2124300,0x252c780,0x2a54d80,0x2e5d200,0x3385800,0x378dc80,0x3cb6280,0x40be700,0x45e6d00,0x49e5400,0x4f0da00,0x530c100,0x5834700,0x432e880,0x5d8eb00,0x5d8eb00,0x5d8eb00,0x5d8eb00,0x5d8eb00,0x5d8eb00,0x5d8eb00,0x5d8eb00,0x5d8eb00,0x5d8eb00,0x5d8eb00,0x5d8eb00,567,1559,2367,3359,4167,5159,6151,6959,7951,8759,9751,10559,11551,12359,13351,14144,15136,15929,16921,17451,17989,17989,17989,17989,21,20,0,0,0,0,0,0,0x472c80,0x99b280,0xda3700,0x12cbd00,0x16d4180,0x1bfc780,0x2124d80,0x252d200,0x2a55800,0x2e5dc80,0x3386280,0x378e700,0x3cb6d00,0x40bf180,0x45e7780,0x49e5e80,0x4f0e480,0x530cb80,0x5835180,0x5adf980,0x5d8f580,0x5d8f580,0x5d8f580,0x5d8f580,0x5d8f580,0x5d8f580,0x5d8f580,0x5d8f580,0x5d8f580,0x5d8f580,0x5d8f580,0x5d8f580,568,1560,2368,3360,4352,5160,6152,6960,7952,8760,9752,10560,11552,12544,13352,14145,15137,15930,16922,17452,17990,17990,17990,17990,21,20,0,0,0,0,0,0,0x473700,0x99bd00,0xda4180,0x12cc780,0x17f4d80,0x1bfd200,0x2125800,0x252dc80,0x2a56280,0x2e5e700,0x3386d00,0x378f180,0x3cb7780,0x41dfd80,0x45e8200,0x49e6900,0x4f0ef00,0x530d600,0x5835c00,0x5ae0400,0x5d90000,0x5d90000,0x5d90000,0x5d90000,0x5d90000,0x5d90000,0x5d90000,0x5d90000,0x5d90000,0x5d90000,0x5d90000,0x5d90000,569,1561,2369,3361,4353,5161,6153,6961,7953,8761,9753,10561,11553,12545,13353,14146,15138,15931,16923,13128,13128,13128,13128,13128,20,19,0,0,0,0,0,0,0x474180,0x99c780,0xda4c00,0x12cd200,0x17f5800,0x1bfdc80,0x2126280,0x252e700,0x2a56d00,0x2e5f180,0x3387780,0x378fc00,0x3cb8200,0x41e0800,0x45e8c80,0x49e7380,0x4f0f980,0x530e080,0x5836680,0x447de00,0x447de00,0x447de00,0x447de00,0x447de00,0x447de00,0x447de00,0x447de00,0x447de00,0x447de00,0x447de00,0x447de00,0x447de00,570,1562,2370,3362,4354,5162,6154,6962,7954,8762,9754,10562,11554,12546,13354,14147,15139,15932,16924,17453,17991,18241,18241,18241,22,21,0,0,0,0,0,0,0x474c00,0x99d200,0xda5680,0x12cdc80,0x17f6280,0x1bfe700,0x2126d00,0x252f180,0x2a57780,0x2e5fc00,0x3388200,0x3790680,0x3cb8c80,0x41e1280,0x45e9700,0x49e7e00,0x4f10400,0x530eb00,0x5837100,0x5ae0e80,0x5d90a80,0x5edc100,0x5edc100,0x5edc100,0x5edc100,0x5edc100,0x5edc100,0x5edc100,0x5edc100,0x5edc100,0x5edc100,0x5edc100,571,1563,2371,3363,4355,5163,6155,6963,7955,8763,9755,10563,11555,12547,13355,14148,15140,15933,16925,17454,18176,18176,18176,18176,21,20,0,0,0,0,0,0,0x475680,0x99dc80,0xda6100,0x12ce700,0x17f6d00,0x1bff180,0x2127780,0x252fc00,0x2a58200,0x2e60680,0x3388c80,0x3791100,0x3cb9700,0x41e1d00,0x45ea180,0x49e8880,0x4f10e80,0x530f580,0x5837b80,0x5ae1900,0x5eb1680,0x5eb1680,0x5eb1680,0x5eb1680,0x5eb1680,0x5eb1680,0x5eb1680,0x5eb1680,0x5eb1680,0x5eb1680,0x5eb1680,0x5eb1680,572,1564,2372,3364,4356,5164,6156,6964,7956,8764,9756,10564,11556,12548,13356,14149,15141,15934,16926,17455,18177,18177,18177,18177,21,20,0,0,0,0,0,0,0x476100,0x99e700,0xda6b80,0x12cf180,0x17f7780,0x1bffc00,0x2128200,0x2530680,0x2a58c80,0x2e61100,0x3389700,0x3791b80,0x3cba180,0x41e2780,0x45eac00,0x49e9300,0x4f11900,0x5310000,0x5838600,0x5ae2380,0x5eb2100,0x5eb2100,0x5eb2100,0x5eb2100,0x5eb2100,0x5eb2100,0x5eb2100,0x5eb2100,0x5eb2100,0x5eb2100,0x5eb2100,0x5eb2100,573,1565,2373,3365,4357,5165,6157,6965,7957,8765,9757,10565,11557,12549,13357,14150,15142,15935,16927,13384,13384,13384,13384,13384,20,19,0,0,0,0,0,0,0x476b80,0x99f180,0xda7600,0x12cfc00,0x17f8200,0x1c00680,0x2128c80,0x2531100,0x2a59700,0x2e61b80,0x338a180,0x3792600,0x3cbac00,0x41e3200,0x45eb680,0x49e9d80,0x4f12380,0x5310a80,0x5839080,0x45cd380,0x45cd380,0x45cd380,0x45cd380,0x45cd380,0x45cd380,0x45cd380,0x45cd380,0x45cd380,0x45cd380,0x45cd380,0x45cd380,0x45cd380,574,1566,2374,3366,4358,5166,6158,6966,7958,8766,9758,10566,11558,12550,13358,14151,15143,15936,16928,13640,18178,18178,18178,18178,21,20,0,0,0,0,0,0,0x477600,0x99fc00,0xda8080,0x12d0680,0x17f8c80,0x1c01100,0x2129700,0x2531b80,0x2a5a180,0x2e62600,0x338ac00,0x3793080,0x3cbb680,0x41e3c80,0x45ec100,0x49ea800,0x4f12e00,0x5311500,0x5839b00,0x471c900,0x5eb2b80,0x5eb2b80,0x5eb2b80,0x5eb2b80,0x5eb2b80,0x5eb2b80,0x5eb2b80,0x5eb2b80,0x5eb2b80,0x5eb2b80,0x5eb2b80,0x5eb2b80,575,1567,2375,3367,4359,5167,6159,6967,7959,8767,9759,10567,11559,12551,13359,14336,15144,15937,16929,17456,18179,18179,18179,18179,21,20,0,0,0,0,0,0,0x478080,0x9a0680,0xda8b00,0x12d1100,0x17f9700,0x1c01b80,0x212a180,0x2532600,0x2a5ac00,0x2e63080,0x338b680,0x3793b00,0x3cbc100,0x41e4700,0x45ecb80,0x4b0b400,0x4f13880,0x5311f80,0x583a580,0x5ae2e00,0x5eb3600,0x5eb3600,0x5eb3600,0x5eb3600,0x5eb3600,0x5eb3600,0x5eb3600,0x5eb3600,0x5eb3600,0x5eb3600,0x5eb3600,0x5eb3600,576,1568,2560,3368,4360,5168,6160,6968,7960,8768,9760,10752,11560,12552,13360,14337,15145,15938,16930,17457,18180,18180,18180,18180,21,20,0,0,0,0,0,0,0x478b00,0x9a1100,0xec9700,0x12d1b80,0x17fa180,0x1c02600,0x212ac00,0x2533080,0x2a5b680,0x2e63b00,0x338c100,0x38b4700,0x3cbcb80,0x41e5180,0x45ed600,0x4b0be80,0x4f14300,0x5312a00,0x583b000,0x5ae3880,0x5eb4080,0x5eb4080,0x5eb4080,0x5eb4080,0x5eb4080,0x5eb4080,0x5eb4080,0x5eb4080,0x5eb4080,0x5eb4080,0x5eb4080,0x5eb4080,577,1569,2561,3369,4361,5169,6161,6969,7961,8769,9761,10753,11561,12553,13361,14338,15146,15939,16931,13896,13896,13896,13896,13896,20,19,0,0,0,0,0,0,0x479580,0x9a1b80,0xeca180,0x12d2600,0x17fac00,0x1c03080,0x212b680,0x2533b00,0x2a5c100,0x2e64580,0x338cb80,0x38b5180,0x3cbd600,0x41e5c00,0x45ee080,0x4b0c900,0x4f14d80,0x5313480,0x583ba80,0x486be80,0x486be80,0x486be80,0x486be80,0x486be80,0x486be80,0x486be80,0x486be80,0x486be80,0x486be80,0x486be80,0x486be80,0x486be80,578,1570,2562,3370,4362,5170,6162,6970,7962,8770,9762,10754,11562,12554,13362,14339,15147,15940,16932,17458,18181,18242,18242,18242,22,21,0,0,0,0,0,0,0x47a000,0x9a2600,0xecac00,0x12d3080,0x17fb680,0x1c03b00,0x212c100,0x2534580,0x2a5cb80,0x2e65000,0x338d600,0x38b5c00,0x3cbe080,0x41e6680,0x45eeb00,0x4b0d380,0x4f15800,0x5313f00,0x583c500,0x5ae4300,0x5eb4b00,0x5edcb80,0x5edcb80,0x5edcb80,0x5edcb80,0x5edcb80,0x5edcb80,0x5edcb80,0x5edcb80,0x5edcb80,0x5edcb80,0x5edcb80,579,1571,2563,3371,4363,5171,6163,6971,7963,8771,9763,10755,11563,12555,13363,14340,15148,15941,16933,17459,18182,18182,18182,18182,21,20,0,0,0,0,0,0,0x47aa80,0x9a3080,0xecb680,0x12d3b00,0x17fc100,0x1c04580,0x212cb80,0x2535000,0x2a5d600,0x2e65a80,0x338e080,0x38b6680,0x3cbeb00,0x41e7100,0x45ef580,0x4b0de00,0x4f16280,0x5314980,0x583cf80,0x5ae4d80,0x5eb5580,0x5eb5580,0x5eb5580,0x5eb5580,0x5eb5580,0x5eb5580,0x5eb5580,0x5eb5580,0x5eb5580,0x5eb5580,0x5eb5580,0x5eb5580,580,1572,2564,3372,4364,5172,6164,6972,7964,8772,9764,10756,11564,12556,13364,14341,15149,15942,16934,17460,18183,18183,18183,18183,21,20,0,0,0,0,0,0,0x47b500,0x9a3b00,0xecc100,0x12d4580,0x17fcb80,0x1c05000,0x212d600,0x2535a80,0x2a5e080,0x2e66500,0x338eb00,0x38b7100,0x3cbf580,0x41e7b80,0x45f0000,0x4b0e880,0x4f16d00,0x5315400,0x583da00,0x5ae5800,0x5eb6000,0x5eb6000,0x5eb6000,0x5eb6000,0x5eb6000,0x5eb6000,0x5eb6000,0x5eb6000,0x5eb6000,0x5eb6000,0x5eb6000,0x5eb6000,581,1573,2565,3373,4365,5173,6165,6973,7965,8773,9765,10757,11565,12557,13365,14342,15150,15943,16935,14152,14152,14152,14152,14152,20,19,0,0,0,0,0,0,0x47bf80,0x9a4580,0xeccb80,0x12d5000,0x17fd600,0x1c05a80,0x212e080,0x2536500,0x2a5eb00,0x2e66f80,0x338f580,0x38b7b80,0x3cc0000,0x41e8600,0x45f0a80,0x4b0f300,0x4f17780,0x5315e80,0x583e480,0x49bb400,0x49bb400,0x49bb400,0x49bb400,0x49bb400,0x49bb400,0x49bb400,0x49bb400,0x49bb400,0x49bb400,0x49bb400,0x49bb400,0x49bb400,582,1574,2566,3374,4366,5174,6166,6974,7966,8774,9766,10758,11566,12558,13366,14343,15151,16128,16936,14408,18184,18184,18184,18184,21,20,0,0,0,0,0,0,0x47ca00,0x9a5000,0xecd600,0x12d5a80,0x17fe080,0x1c06500,0x212eb00,0x2536f80,0x2a5f580,0x2e67a00,0x3390000,0x38b8600,0x3cc0a80,0x41e9080,0x45f1500,0x4b0fd80,0x4f18200,0x5436a80,0x583ef00,0x4b0a980,0x5eb6a80,0x5eb6a80,0x5eb6a80,0x5eb6a80,0x5eb6a80,0x5eb6a80,0x5eb6a80,0x5eb6a80,0x5eb6a80,0x5eb6a80,0x5eb6a80,0x5eb6a80,583,1575,2567,3375,4367,5175,6167,6975,7967,8775,9767,10759,11567,12559,13367,14344,15152,16129,16937,17461,18185,18185,18185,18185,21,20,0,0,0,0,0,0,0x47d480,0x9a5a80,0xece080,0x12d6500,0x17feb00,0x1c06f80,0x212f580,0x2537a00,0x2a60000,0x2e68480,0x3390a80,0x38b9080,0x3cc1500,0x41e9b00,0x45f1f80,0x4b10800,0x4f18c80,0x5437500,0x583f980,0x5ae6280,0x5eb7500,0x5eb7500,0x5eb7500,0x5eb7500,0x5eb7500,0x5eb7500,0x5eb7500,0x5eb7500,0x5eb7500,0x5eb7500,0x5eb7500,0x5eb7500,768,1576,2568,3376,4368,5176,6168,6976,7968,8960,9768,10760,11568,12560,13368,14345,15153,16130,16938,17462,18186,18186,18186,18186,21,20,0,0,0,0,0,0,0x59e080,0x9a6500,0xeceb00,0x12d6f80,0x17ff580,0x1c07a00,0x2130000,0x2538480,0x2a60a80,0x2f89080,0x3391500,0x38b9b00,0x3cc1f80,0x41ea580,0x45f2a00,0x4b11280,0x4f19700,0x5437f80,0x5840400,0x5ae6d00,0x5eb7f80,0x5eb7f80,0x5eb7f80,0x5eb7f80,0x5eb7f80,0x5eb7f80,0x5eb7f80,0x5eb7f80,0x5eb7f80,0x5eb7f80,0x5eb7f80,0x5eb7f80,769,1577,2569,3377,4369,5177,6169,6977,7969,8961,9769,10761,11569,12561,13369,14346,15154,16131,16939,14664,14664,14664,14664,14664,20,19,0,0,0,0,0,0,0x59eb00,0x9a6f80,0xecf580,0x12d7a00,0x1800000,0x1c08480,0x2130a80,0x2538f00,0x2a61500,0x2f89b00,0x3391f80,0x38ba580,0x3cc2a00,0x41eb000,0x45f3480,0x4b11d00,0x4f1a180,0x5438a00,0x5840e80,0x4c59f00,0x4c59f00,0x4c59f00,0x4c59f00,0x4c59f00,0x4c59f00,0x4c59f00,0x4c59f00,0x4c59f00,0x4c59f00,0x4c59f00,0x4c59f00,0x4c59f00,770,1578,2570,3378,4370,5178,6170,6978,7970,8962,9770,10762,11570,12562,13370,14347,15155,16132,16940,17463,18187,18243,18243,18243,22,21,0,0,0,0,0,0,0x59f580,0x9a7a00,0xed0000,0x12d8480,0x1800a80,0x1c08f00,0x2131500,0x2539980,0x2a61f80,0x2f8a580,0x3392a00,0x38bb000,0x3cc3480,0x41eba80,0x45f3f00,0x4b12780,0x4f1ac00,0x5439480,0x5841900,0x5ae7780,0x5eb8a00,0x5edd600,0x5edd600,0x5edd600,0x5edd600,0x5edd600,0x5edd600,0x5edd600,0x5edd600,0x5edd600,0x5edd600,0x5edd600,771,1579,2571,3379,4371,5179,6171,6979,7971,8963,9771,10763,11571,12563,13371,14348,15156,16133,16941,17464,18188,18188,18188,18188,21,20,0,0,0,0,0,0,0x5a0000,0x9a8480,0xed0a80,0x12d8f00,0x1801500,0x1c09980,0x2131f80,0x253a400,0x2a62a00,0x2f8b000,0x3393480,0x38bba80,0x3cc3f00,0x41ec500,0x45f4980,0x4b13200,0x4f1b680,0x5439f00,0x5842380,0x5ae8200,0x5eb9480,0x5eb9480,0x5eb9480,0x5eb9480,0x5eb9480,0x5eb9480,0x5eb9480,0x5eb9480,0x5eb9480,0x5eb9480,0x5eb9480,0x5eb9480,772,1580,2572,3380,4372,5180,6172,6980,7972,8964,9772,10764,11572,12564,13372,14349,15157,16134,16942,17465,18189,18189,18189,18189,21,20,0,0,0,0,0,0,0x5a0a80,0x9a8f00,0xed1500,0x12d9980,0x1801f80,0x1c0a400,0x2132a00,0x253ae80,0x2a63480,0x2f8ba80,0x3393f00,0x38bc500,0x3cc4980,0x41ecf80,0x45f5400,0x4b13c80,0x4f1c100,0x543a980,0x5842e00,0x5ae8c80,0x5eb9f00,0x5eb9f00,0x5eb9f00,0x5eb9f00,0x5eb9f00,0x5eb9f00,0x5eb9f00,0x5eb9f00,0x5eb9f00,0x5eb9f00,0x5eb9f00,0x5eb9f00,773,1581,2573,3381,4373,5181,6173,6981,7973,8965,9773,10765,11573,12565,13373,14350,15158,16135,16943,14920,14920,14920,14920,14920,20,19,0,0,0,0,0,0,0x5a1500,0x9a9980,0xed1f80,0x12da400,0x1802a00,0x1c0ae80,0x2133480,0x253b900,0x2a63f00,0x2f8c500,0x3394980,0x38bcf80,0x3cc5400,0x41eda00,0x45f5e80,0x4b14700,0x4f1cb80,0x543b400,0x5843880,0x4da9480,0x4da9480,0x4da9480,0x4da9480,0x4da9480,0x4da9480,0x4da9480,0x4da9480,0x4da9480,0x4da9480,0x4da9480,0x4da9480,0x4da9480,774,1582,2574,3382,4374,5182,6174,6982,7974,8966,9774,10766,11574,12566,13374,14351,15159,16136,16944,15176,18190,18190,18190,18190,21,20,0,0,0,0,0,0,0x5a1f80,0x9aa400,0xed2a00,0x12dae80,0x1803480,0x1c0b900,0x2133f00,0x253c380,0x2a64980,0x2f8cf80,0x3395400,0x38bda00,0x3cc5e80,0x41ee480,0x45f6900,0x4b15180,0x4f1d600,0x543be80,0x5844300,0x4ef8a00,0x5eba980,0x5eba980,0x5eba980,0x5eba980,0x5eba980,0x5eba980,0x5eba980,0x5eba980,0x5eba980,0x5eba980,0x5eba980,0x5eba980,775,1583,2575,3383,4375,5183,6175,6983,7975,8967,9775,10767,11575,12567,13375,14352,15160,16137,16945,17466,18191,18191,18191,18191,21,20,0,0,0,0,0,0,0x5a2a00,0x9aae80,0xed3480,0x12db900,0x1803f00,0x1c0c380,0x2134980,0x253ce00,0x2a65400,0x2f8da00,0x3395e80,0x38be480,0x3cc6900,0x41eef00,0x45f7380,0x4b15c00,0x4f1e080,0x543c900,0x5844d80,0x5ae9700,0x5ebb400,0x5ebb400,0x5ebb400,0x5ebb400,0x5ebb400,0x5ebb400,0x5ebb400,0x5ebb400,0x5ebb400,0x5ebb400,0x5ebb400,0x5ebb400,776,1584,2576,3384,4376,5184,6176,7168,7976,8968,9776,10768,11576,12568,13376,14353,15161,16138,16946,17467,18192,18192,18192,18192,21,20,0,0,0,0,0,0,0x5a3480,0x9ab900,0xed3f00,0x12dc380,0x1804980,0x1c0ce00,0x2135400,0x265da00,0x2a65e80,0x2f8e480,0x3396900,0x38bef00,0x3cc7380,0x41ef980,0x45f7e00,0x4b16680,0x4f1eb00,0x543d380,0x5845800,0x5aea180,0x5ebbe80,0x5ebbe80,0x5ebbe80,0x5ebbe80,0x5ebbe80,0x5ebbe80,0x5ebbe80,0x5ebbe80,0x5ebbe80,0x5ebbe80,0x5ebbe80,0x5ebbe80,777,1585,2577,3385,4377,5185,6177,7169,7977,8969,9777,10769,11577,12569,13377,14354,15162,16139,16947,15432,15432,15432,15432,15432,20,19,0,0,0,0,0,0,0x5a3f00,0x9ac380,0xed4980,0x12dce00,0x1805400,0x1c0d880,0x2135e80,0x265e480,0x2a66900,0x2f8ef00,0x3397380,0x38bf980,0x3cc7e00,0x41f0400,0x45f8880,0x4b17100,0x4f1f580,0x543de00,0x5846280,0x5047f80,0x5047f80,0x5047f80,0x5047f80,0x5047f80,0x5047f80,0x5047f80,0x5047f80,0x5047f80,0x5047f80,0x5047f80,0x5047f80,0x5047f80,778,1586,2578,3386,4378,5186,6178,7170,7978,8970,9778,10770,11578,12570,13378,14355,15163,16140,16948,17468,18193,18244,18244,18244,22,21,0,0,0,0,0,0,0x5a4980,0x9ace00,0xed5400,0x12dd880,0x1805e80,0x1c0e300,0x2136900,0x265ef00,0x2a67380,0x2f8f980,0x3397e00,0x38c0400,0x3cc8880,0x41f0e80,0x45f9300,0x4b17b80,0x4f20000,0x543e880,0x5846d00,0x5aeac00,0x5ebc900,0x5ede080,0x5ede080,0x5ede080,0x5ede080,0x5ede080,0x5ede080,0x5ede080,0x5ede080,0x5ede080,0x5ede080,0x5ede080,779,1587,2579,3387,4379,5187,6179,7171,7979,8971,9779,10771,11579,12571,13379,14356,15164,16141,16949,17469,18194,18194,18194,18194,21,20,0,0,0,0,0,0,0x5a5400,0x9ad880,0xed5e80,0x12de300,0x1806900,0x1c0ed80,0x2137380,0x265f980,0x2a67e00,0x2f90400,0x3398880,0x38c0e80,0x3cc9300,0x41f1900,0x45f9d80,0x4b18600,0x4f20a80,0x543f300,0x5847780,0x5aeb680,0x5ebd380,0x5ebd380,0x5ebd380,0x5ebd380,0x5ebd380,0x5ebd380,0x5ebd380,0x5ebd380,0x5ebd380,0x5ebd380,0x5ebd380,0x5ebd380,780,1588,2580,3388,4380,5188,6180,7172,7980,8972,9780,10772,11580,12572,13380,14357,15165,16142,16950,17470,18195,18195,18195,18195,21,20,0,0,0,0,0,0,0x5a5e80,0x9ae300,0xed6900,0x12ded80,0x1807380,0x1c0f800,0x2137e00,0x2660400,0x2a68880,0x2f90e80,0x3399300,0x38c1900,0x3cc9d80,0x41f2380,0x45fa800,0x4b19080,0x4f21500,0x543fd80,0x5848200,0x5aec100,0x5ebde00,0x5ebde00,0x5ebde00,0x5ebde00,0x5ebde00,0x5ebde00,0x5ebde00,0x5ebde00,0x5ebde00,0x5ebde00,0x5ebde00,0x5ebde00,781,1589,2581,3389,4381,5189,6181,7173,7981,8973,9781,10773,11581,12573,13381,14358,15166,16143,16951,15688,15688,15688,15688,15688,20,19,0,0,0,0,0,0,0x5a6900,0x9aed80,0xed7380,0x12df800,0x1807e00,0x1c10280,0x2138880,0x2660e80,0x2a69300,0x2f91900,0x3399d80,0x38c2380,0x3cca800,0x41f2e00,0x45fb280,0x4b19b00,0x4f21f80,0x5440800,0x5848c80,0x5197500,0x5197500,0x5197500,0x5197500,0x5197500,0x5197500,0x5197500,0x5197500,0x5197500,0x5197500,0x5197500,0x5197500,0x5197500,782,1590,2582,3390,4382,5190,6182,7174,7982,8974,9782,10774,11582,12574,13382,14359,15167,16144,16952,15944,18196,18196,18196,18196,21,20,0,0,0,0,0,0,0x5a7380,0x9af800,0xed7e00,0x12e0280,0x1808880,0x1c10d00,0x2139300,0x2661900,0x2a69d80,0x2f92380,0x339a800,0x38c2e00,0x3ccb280,0x41f3880,0x45fbd00,0x4b1a580,0x4f22a00,0x5441280,0x5849700,0x52e6a80,0x5ebe880,0x5ebe880,0x5ebe880,0x5ebe880,0x5ebe880,0x5ebe880,0x5ebe880,0x5ebe880,0x5ebe880,0x5ebe880,0x5ebe880,0x5ebe880,783,1591,2583,3391,4383,5191,6183,7175,7983,8975,9783,10775,11583,12575,13383,14360,15168,16145,16953,17471,18197,18197,18197,18197,21,20,0,0,0,0,0,0,0x5a7e00,0x9b0280,0xed8880,0x12e0d00,0x1809300,0x1c11780,0x2139d80,0x2662380,0x2a6a800,0x2f92e00,0x339b280,0x38c3880,0x3ccbd00,0x41f4300,0x45fc780,0x4b1b000,0x4f23480,0x5441d00,0x584a180,0x5aecb80,0x5ebf300,0x5ebf300,0x5ebf300,0x5ebf300,0x5ebf300,0x5ebf300,0x5ebf300,0x5ebf300,0x5ebf300,0x5ebf300,0x5ebf300,0x5ebf300,784,1592,2584,3392,4384,5376,6184,7176,7984,8976,9784,10776,11584,12576,13568,14361,15169,16146,16954,17472,18198,18198,18198,18198,21,20,0,0,0,0,0,0,0x5a8880,0x9b0d00,0xed9300,0x12e1780,0x1809d80,0x1d32380,0x213a800,0x2662e00,0x2a6b280,0x2f93880,0x339bd00,0x38c4300,0x3ccc780,0x41f4d80,0x471d380,0x4b1ba80,0x4f23f00,0x5442780,0x584ac00,0x5aed600,0x5ebfd80,0x5ebfd80,0x5ebfd80,0x5ebfd80,0x5ebfd80,0x5ebfd80,0x5ebfd80,0x5ebfd80,0x5ebfd80,0x5ebfd80,0x5ebfd80,0x5ebfd80,785,1593,2585,3393,4385,5377,6185,7177,7985,8977,9785,10777,11585,12577,13569,14362,15170,16147,16955,16200,16200,16200,16200,16200,20,19,0,0,0,0,0,0,0x5a9300,0x9b1780,0xed9d80,0x12e2200,0x180a800,0x1d32e00,0x213b280,0x2663880,0x2a6bd00,0x2f94300,0x339c780,0x38c4d80,0x3ccd200,0x41f5800,0x471de00,0x4b1c500,0x4f24980,0x5443200,0x584b680,0x5436000,0x5436000,0x5436000,0x5436000,0x5436000,0x5436000,0x5436000,0x5436000,0x5436000,0x5436000,0x5436000,0x5436000,0x5436000,786,1594,2586,3394,4386,5378,6186,7178,7986,8978,9786,10778,11586,12578,13570,14363,15171,16148,16956,17473,18199,18245,18245,18245,22,21,0,0,0,0,0,0,0x5a9d80,0x9b2200,0xeda800,0x12e2c80,0x180b280,0x1d33880,0x213bd00,0x2664300,0x2a6c780,0x2f94d80,0x339d200,0x38c5800,0x3ccdc80,0x41f6280,0x471e880,0x4b1cf80,0x4f25400,0x5443c80,0x584c100,0x5aee080,0x5ec0800,0x5edeb00,0x5edeb00,0x5edeb00,0x5edeb00,0x5edeb00,0x5edeb00,0x5edeb00,0x5edeb00,0x5edeb00,0x5edeb00,0x5edeb00,787,1595,2587,3395,4387,5379,6187,7179,7987,8979,9787,10779,11587,12579,13571,14364,15172,16149,16957,17474,18200,18200,18200,18200,21,20,0,0,0,0,0,0,0x5aa800,0x9b2c80,0xedb280,0x12e3700,0x180bd00,0x1d34300,0x213c780,0x2664d80,0x2a6d200,0x2f95800,0x339dc80,0x38c6280,0x3cce700,0x41f6d00,0x471f300,0x4b1da00,0x4f25e80,0x5444700,0x584cb80,0x5aeeb00,0x5ec1280,0x5ec1280,0x5ec1280,0x5ec1280,0x5ec1280,0x5ec1280,0x5ec1280,0x5ec1280,0x5ec1280,0x5ec1280,0x5ec1280,0x5ec1280,788,1596,2588,3396,4388,5380,6188,7180,7988,8980,9788,10780,11588,12580,13572,14365,15173,16150,16958,17475,18201,18201,18201,18201,21,20,0,0,0,0,0,0,0x5ab280,0x9b3700,0xedbd00,0x12e4180,0x180c780,0x1d34d80,0x213d200,0x2665800,0x2a6dc80,0x2f96280,0x339e700,0x38c6d00,0x3ccf180,0x41f7780,0x471fd80,0x4b1e480,0x4f26900,0x5445180,0x584d600,0x5aef580,0x5ec1d00,0x5ec1d00,0x5ec1d00,0x5ec1d00,0x5ec1d00,0x5ec1d00,0x5ec1d00,0x5ec1d00,0x5ec1d00,0x5ec1d00,0x5ec1d00,0x5ec1d00,789,1597,2589,3397,4389,5381,6189,7181,7989,8981,9789,10781,11589,12581,13573,14366,15174,16151,16959,16456,16456,16456,16456,16456,20,19,0,0,0,0,0,0,0x5abd00,0x9b4180,0xedc780,0x12e4c00,0x180d200,0x1d35800,0x213dc80,0x2666280,0x2a6e700,0x2f96d00,0x339f180,0x38c7780,0x3ccfc00,0x41f8200,0x4720800,0x4b1ef00,0x4f27380,0x5445c00,0x584e080,0x5585580,0x5585580,0x5585580,0x5585580,0x5585580,0x5585580,0x5585580,0x5585580,0x5585580,0x5585580,0x5585580,0x5585580,0x5585580,790,1598,2590,3398,4390,5382,6190,7182,7990,8982,9790,10782,11590,12582,13574,14367,15175,16152,16960,16712,18202,18202,18202,18202,21,20,0,0,0,0,0,0,0x5ac780,0x9b4c00,0xedd200,0x12e5680,0x180dc80,0x1d36280,0x213e700,0x2666d00,0x2a6f180,0x2f97780,0x339fc00,0x38c8200,0x3cd0680,0x41f8c80,0x4721280,0x4b1f980,0x4f27e00,0x5446680,0x584eb00,0x56d4b00,0x5ec2780,0x5ec2780,0x5ec2780,0x5ec2780,0x5ec2780,0x5ec2780,0x5ec2780,0x5ec2780,0x5ec2780,0x5ec2780,0x5ec2780,0x5ec2780,791,1599,2591,3399,4391,5383,6191,7183,7991,8983,9791,10783,11591,12583,13575,14368,15360,16153,16961,17476,18203,18203,18203,18203,21,20,0,0,0,0,0,0,0x5ad200,0x9b5680,0xeddc80,0x12e6100,0x180e700,0x1d36d00,0x213f180,0x2667780,0x2a6fc00,0x2f98200,0x33a0680,0x38c8c80,0x3cd1100,0x41f9700,0x4721d00,0x4b20400,0x5048a00,0x5447100,0x584f580,0x5af0000,0x5ec3200,0x5ec3200,0x5ec3200,0x5ec3200,0x5ec3200,0x5ec3200,0x5ec3200,0x5ec3200,0x5ec3200,0x5ec3200,0x5ec3200,0x5ec3200,792,1600,2592,3584,4392,5384,6192,7184,7992,8984,9792,10784,11776,12584,13576,14369,15361,16154,16962,17477,18204,18204,18204,18204,21,20,0,0,0,0,0,0,0x5adc80,0x9b6100,0xede700,0x1406d00,0x180f180,0x1d37780,0x213fc00,0x2668200,0x2a70680,0x2f98c80,0x33a1100,0x38c9700,0x3df1d00,0x41fa180,0x4722780,0x4b20e80,0x5049480,0x5447b80,0x5850000,0x5af0a80,0x5ec3c80,0x5ec3c80,0x5ec3c80,0x5ec3c80,0x5ec3c80,0x5ec3c80,0x5ec3c80,0x5ec3c80,0x5ec3c80,0x5ec3c80,0x5ec3c80,0x5ec3c80,793,1601,2593,3585,4393,5385,6193,7185,7993,8985,9793,10785,11777,12585,13577,14370,15362,16155,16963,16968,16968,16968,16968,16968,20,19,0,0,0,0,0,0,0x5ae700,0x9b6b80,0xedf180,0x1407780,0x180fc00,0x1d38200,0x2140680,0x2668c80,0x2a71100,0x2f99700,0x33a1b80,0x38ca180,0x3df2780,0x41fac00,0x4723200,0x4b21900,0x5049f00,0x5448600,0x5850a80,0x5824080,0x5824080,0x5824080,0x5824080,0x5824080,0x5824080,0x5824080,0x5824080,0x5824080,0x5824080,0x5824080,0x5824080,0x5824080,794,1602,2594,3586,4394,5386,6194,7186,7994,8986,9794,10786,11778,12586,13578,14371,15363,16156,16964,17478,18205,18246,18246,18246,22,21,0,0,0,0,0,0,0x5af180,0x9b7600,0xedfc00,0x1408200,0x1810680,0x1d38c80,0x2141100,0x2669700,0x2a71b80,0x2f9a180,0x33a2600,0x38cac00,0x3df3200,0x41fb680,0x4723c80,0x4b22380,0x504a980,0x5449080,0x5851500,0x5af1500,0x5ec4700,0x5edf580,0x5edf580,0x5edf580,0x5edf580,0x5edf580,0x5edf580,0x5edf580,0x5edf580,0x5edf580,0x5edf580,0x5edf580,795,1603,2595,3587,4395,5387,6195,7187,7995,8987,9795,10787,11779,12587,13579,14372,15364,16157,16965,17479,18206,18206,18206,18206,21,20,0,0,0,0,0,0,0x5afc00,0x9b8080,0xee0680,0x1408c80,0x1811100,0x1d39700,0x2141b80,0x266a180,0x2a72600,0x2f9ac00,0x33a3080,0x38cb680,0x3df3c80,0x41fc100,0x4724700,0x4b22e00,0x504b400,0x5449b00,0x5851f80,0x5af1f80,0x5ec5180,0x5ec5180,0x5ec5180,0x5ec5180,0x5ec5180,0x5ec5180,0x5ec5180,0x5ec5180,0x5ec5180,0x5ec5180,0x5ec5180,0x5ec5180,796,1604,2596,3588,4396,5388,6196,7188,7996,8988,9796,10788,11780,12588,13580,14373,15365,16158,16966,17664,18207,18207,18207,18207,21,20,0,0,0,0,0,0,0x5b0680,0x9b8b00,0xee1100,0x1409700,0x1811b80,0x1d3a180,0x2142600,0x266ac00,0x2a73080,0x2f9b680,0x33a3b00,0x38cc100,0x3df4700,0x41fcb80,0x4725180,0x4b23880,0x504be80,0x544a580,0x5852a00,0x5c12b80,0x5ec5c00,0x5ec5c00,0x5ec5c00,0x5ec5c00,0x5ec5c00,0x5ec5c00,0x5ec5c00,0x5ec5c00,0x5ec5c00,0x5ec5c00,0x5ec5c00,0x5ec5c00,797,1605,2597,3589,4397,5389,6197,7189,7997,8989,9797,10789,11781,12589,13581,14374,15366,16159,16967,17224,17224,17224,17224,17224,20,19,0,0,0,0,0,0,0x5b1100,0x9b9580,0xee1b80,0x140a180,0x1812600,0x1d3ac00,0x2143080,0x266b680,0x2a73b00,0x2f9c100,0x33a4580,0x38ccb80,0x3df5180,0x41fd600,0x4725c00,0x4b24300,0x504c900,0x544b000,0x5853480,0x5973600,0x5973600,0x5973600,0x5973600,0x5973600,0x5973600,0x5973600,0x5973600,0x5973600,0x5973600,0x5973600,0x5973600,0x5973600,798,1606,2598,3590,4398,5390,6198,7190,7998,8990,9798,10790,11782,12590,13582,14375,15367,16160,17152,17480,18208,18208,18208,18208,21,20,0,0,0,0,0,0,0x5b1b80,0x9ba000,0xee2600,0x140ac00,0x1813080,0x1d3b680,0x2143b00,0x266c100,0x2a74580,0x2f9cb80,0x33a5000,0x38cd600,0x3df5c00,0x41fe080,0x4726680,0x4b24d80,0x504d380,0x544ba80,0x5974080,0x5ac2b80,0x5ec6680,0x5ec6680,0x5ec6680,0x5ec6680,0x5ec6680,0x5ec6680,0x5ec6680,0x5ec6680,0x5ec6680,0x5ec6680,0x5ec6680,0x5ec6680,799,1607,2599,3591,4399,5391,6199,7191,7999,8991,9799,10791,11783,12591,13583,14376,15368,16161,17153,17665,18209,18209,18209,18209,21,20,0,0,0,0,0,0,0x5b2600,0x9baa80,0xee3080,0x140b680,0x1813b00,0x1d3c100,0x2144580,0x266cb80,0x2a75000,0x2f9d600,0x33a5a80,0x38ce080,0x3df6680,0x41feb00,0x4727100,0x4b25800,0x504de00,0x544c500,0x5974b00,0x5c13600,0x5ec7100,0x5ec7100,0x5ec7100,0x5ec7100,0x5ec7100,0x5ec7100,0x5ec7100,0x5ec7100,0x5ec7100,0x5ec7100,0x5ec7100,0x5ec7100,800,1792,2600,3592,4400,5392,6200,7192,8000,8992,9984,10792,11784,12592,13584,14377,15369,16162,17154,17666,18210,18210,18210,18210,21,20,0,0,0,0,0,0,0x5b3080,0xadb680,0xee3b00,0x140c100,0x1814580,0x1d3cb80,0x2145000,0x266d600,0x2a75a80,0x2f9e080,0x34c6680,0x38ceb00,0x3df7100,0x41ff580,0x4727b80,0x4b26280,0x504e880,0x544cf80,0x5975580,0x5c14080,0x5ec7b80,0x5ec7b80,0x5ec7b80,0x5ec7b80,0x5ec7b80,0x5ec7b80,0x5ec7b80,0x5ec7b80,0x5ec7b80,0x5ec7b80,0x5ec7b80,0x5ec7b80,801,1793,2601,3593,4401,5393,6201,7193,8001,8993,9985,10793,11785,12593,13585,14378,15370,16163,17155,17736,17736,17736,17736,17736,20,19,0,0,0,0,0,0,0x5b3b00,0xadc100,0xee4580,0x140cb80,0x1815000,0x1d3d600,0x2145a80,0x266e080,0x2a76500,0x2f9eb00,0x34c7100,0x38cf580,0x3df7b80,0x4200000,0x4728600,0x4b26d00,0x504f300,0x544da00,0x5976000,0x5c12100,0x5c12100,0x5c12100,0x5c12100,0x5c12100,0x5c12100,0x5c12100,0x5c12100,0x5c12100,0x5c12100,0x5c12100,0x5c12100,0x5c12100,802,1794,2602,3594,4402,5394,6202,7194,8002,8994,9986,10794,11786,12594,13586,14379,15371,16164,17156,17667,18211,18247,18247,18247,22,21,0,0,0,0,0,0,0x5b4580,0xadcb80,0xee5000,0x140d600,0x1815a80,0x1d3e080,0x2146500,0x266eb00,0x2a76f80,0x2f9f580,0x34c7b80,0x38d0000,0x3df8600,0x4200a80,0x4729080,0x4b27780,0x504fd80,0x544e480,0x5976a80,0x5c14b00,0x5ec8600,0x5ee0000,0x5ee0000,0x5ee0000,0x5ee0000,0x5ee0000,0x5ee0000,0x5ee0000,0x5ee0000,0x5ee0000,0x5ee0000,0x5ee0000,803,1795,2603,3595,4403,5395,6203,7195,8003,8995,9987,10795,11787,12595,13587,14380,15372,16165,17157,17668,18212,18212,18212,18212,21,20,0,0,0,0,0,0,0x5b5000,0xadd600,0xee5a80,0x140e080,0x1816500,0x1d3eb00,0x2146f80,0x266f580,0x2a77a00,0x2fa0000,0x34c8600,0x38d0a80,0x3df9080,0x4201500,0x4729b00,0x4b28200,0x5050800,0x544ef00,0x5977500,0x5c15580,0x5ec9080,0x5ec9080,0x5ec9080,0x5ec9080,0x5ec9080,0x5ec9080,0x5ec9080,0x5ec9080,0x5ec9080,0x5ec9080,0x5ec9080,0x5ec9080,804,1796,2604,3596,4404,5396,6204,7196,8004,8996,9988,10796,11788,12596,13588,14381,15373,16166,17158,17669,18213,18213,18213,18213,21,20,0,0,0,0,0,0,0x5b5a80,0xade080,0xee6500,0x140eb00,0x1816f80,0x1d3f580,0x2147a00,0x2670000,0x2a78480,0x2fa0a80,0x34c9080,0x38d1500,0x3df9b00,0x4201f80,0x472a580,0x4b28c80,0x5051280,0x544f980,0x5977f80,0x5c16000,0x5ec9b00,0x5ec9b00,0x5ec9b00,0x5ec9b00,0x5ec9b00,0x5ec9b00,0x5ec9b00,0x5ec9b00,0x5ec9b00,0x5ec9b00,0x5ec9b00,0x5ec9b00,805,1797,2605,3597,4405,5397,6205,7197,8005,8997,9989,10797,11789,12597,13589,14382,15374,16167,17159,17992,17992,17992,17992,17992,20,19,0,0,0,0,0,0,0x5b6500,0xadeb00,0xee6f80,0x140f580,0x1817a00,0x1d40000,0x2148480,0x2670a80,0x2a78f00,0x2fa1500,0x34c9b00,0x38d1f80,0x3dfa580,0x4202a00,0x472b000,0x4b29700,0x5051d00,0x5450400,0x5978a00,0x5d61680,0x5d61680,0x5d61680,0x5d61680,0x5d61680,0x5d61680,0x5d61680,0x5d61680,0x5d61680,0x5d61680,0x5d61680,0x5d61680,0x5d61680,806,1798,2606,3598,4406,5398,6206,7198,8006,8998,9990,10798,11790,12598,13590,14383,15375,16168,17160,18248,18214,18214,18214,18214,21,20,0,0,0,0,0,0,0x5b6f80,0xadf580,0xee7a00,0x1410000,0x1818480,0x1d40a80,0x2148f00,0x2671500,0x2a79980,0x2fa1f80,0x34ca580,0x38d2a00,0x3dfb000,0x4203480,0x472ba80,0x4b2a180,0x5052780,0x5450e80,0x5979480,0x5eb0c00,0x5eca580,0x5eca580,0x5eca580,0x5eca580,0x5eca580,0x5eca580,0x5eca580,0x5eca580,0x5eca580,0x5eca580,0x5eca580,0x5eca580,807,1799,2607,3599,4407,5399,6207,7199,8007,8999,9991,10799,11791,12599,13591,14384,15376,16169,17161,17670,18215,18215,18215,18215,21,20,0,0,0,0,0,0,0x5b7a00,0xae0000,0xee8480,0x1410a80,0x1818f00,0x1d41500,0x2149980,0x2671f80,0x2a7a400,0x2fa2a00,0x34cb000,0x38d3480,0x3dfba80,0x4203f00,0x472c500,0x4b2ac00,0x5053200,0x5451900,0x5979f00,0x5c16a80,0x5ecb000,0x5ecb000,0x5ecb000,0x5ecb000,0x5ecb000,0x5ecb000,0x5ecb000,0x5ecb000,0x5ecb000,0x5ecb000,0x5ecb000,0x5ecb000};

__global__ __launch_bounds__(256, 1) void swin_main(const uint16_t* x, const uint16_t* mask, char* ws, uint16_t* out,
                                                    const uint16_t* wqkv, const uint16_t* wo, const uint16_t* ls,
                                                    const uint16_t* w1, const uint16_t* w2, const uint16_t* rc,
                                                    const int* ridx) {
  __shared__ __attribute__((aligned(16))) char smem[LDS_BYTES];
  const int wave = __builtin_amdgcn_readfirstlane(threadIdx.x >> 6);
  const unsigned lds0 = (unsigned)(size_t)smem;
  asm volatile(
      ".p2align 5\n"
      "s_load_dwordx8 s[16:23], %0, 0\n"
      "s_load_dwordx4 s[4:7], %0, 32\n"
      "s_load_dwordx8 s[80:87], %0, 48\n"
      "s_load_dwordx2 s[90:91], %0, 80\n"
      "s_mov_b32 s24, %1\n"
      "s_mov_b32 s25, %2\n"
      "s_mov_b64 s[60:61], %3\n"
      "s_getpc_b64 s[12:13]\n"
      "L_ipf0_%=:\n"
      "v_mbcnt_lo_u32_b32 v168, -1, 0\n"
      "v_mbcnt_hi_u32_b32 v168, -1, v168\n"
      "s_lshr_b32 s28, s25, 3\n"
      "s_lshl_b32 s29, s28, 2\n"
      "s_add_u32 s29, s29, s24\n"
      "s_mul_i32 s29, s29, 14\n"
      "v_min_u32_e64 v169, 13, v168\n"
      "v_add_lshl_u32 v169, v169, s29, 7\n"
      "v_min_u32 v169, 221056, v169\n"
      "v_lshlrev_b32_e64 v171, 4, v168\n"
      "s_lshl_b32 s29, s24, 6\n"
      "s_add_u32 s54, s60, s29\n"
      "s_addc_u32 s55, s61, 0\n"
      "s_and_b32 s29, s29, 64\n"
      "v_xor_b32_e64 v170, s29, v171\n"
      "s_mul_i32 s30, s24, 9216\n"
      "v_add_u32_e64 v170, s30, v170\n"
      "s_add_u32 s2, s30, 36864\n"
      "v_xor_b32 v195, 32, v170\n"
      "s_mov_b64 s[34:35], 0xffff\n"
      "v_cndmask_b32 v193, v195, v170, s[34:35]\n"
      "s_lshl_b64 s[34:35], s[34:35], 48\n"
      "v_cndmask_b32 v192, v170, v195, s[34:35]\n"
      "s_lshl_b64 s[34:35], -1, 32\n"
      "v_cndmask_b32 v194, v195, v170, s[34:35]\n"
      "s_waitcnt lgkmcnt(0)\n"
      "global_load_dword v31, v169, s[4:5]\n"
      "s_add_u32 m0, s2, 0\n"
      "s_nop 0\n"
      "global_load_lds_dwordx4 v170, s[4:5]\n"
      "global_load_lds_dwordx4 v170, s[4:5] offset:1024\n"
      "global_load_lds_dwordx4 v193, s[4:5] offset:2048\n"
      "global_load_lds_dwordx4 v195, s[4:5] offset:3072\n"
      "s_add_u32 s4, s4, 4096\n"
      "s_addc_u32 s5, s5, 0\n"
      "s_add_u32 m0, s2, 4096\n"
      "s_nop 0\n"
      "global_load_lds_dwordx4 v194, s[4:5]\n"
      "global_load_lds_dwordx4 v170, s[4:5] offset:1024\n"
      "global_load_lds_dwordx4 v192, s[4:5] offset:2048\n"
      "global_load_lds_dwordx4 v195, s[4:5] offset:3072\n"
      "s_add_u32 s4, s4, 4096\n"
      "s_addc_u32 s5, s5, 0\n"
      "s_add_u32 m0, s2, 8192\n"
      "s_nop 0\n"
      "global_load_lds_dwordx4 v195, s[4:5]\n"
      "s_add_u32 s4, s4, 28672\n"
      "s_addc_u32 s5, s5, 0\n"
      "s_load_dwordx16 s[36:51], s[82:83], 0\n"
      "s_load_dwordx2 s[8:9], s[80:81], 0\n"
      "s_load_dwordx16 s[64:79], s[84:85], 0\n"
      "s_lshl_b32 s29, s24, 8\n"
      "v_lshl_add_u32 v196, v168, 2, s29\n"
      "v_min_u32 v196, 672, v196\n"
      "v_add_u32 v199, 150640, v196\n"
      "s_lshl_b32 s29, s28, 2\n"
      "s_add_u32 s29, s29, s24\n"
      "s_mov_b64 s[34:35], s[16:17]\n"
      "s_mov_b32 s30, s29\n"
      "s_sub_u32 s31, s29, 48\n"
      "s_cmp_ge_u32 s29, 48\n"
      "s_cselect_b64 s[34:35], s[22:23], s[34:35]\n"
      "s_cselect_b32 s30, s31, s30\n"
      "s_sub_u32 s31, s29, 96\n"
      "s_cmp_ge_u32 s29, 96\n"
      "s_cselect_b64 s[34:35], s[18:19], s[34:35]\n"
      "s_cselect_b32 s30, s31, s30\n"
      "s_sub_u32 s31, s29, 54\n"
      "s_cmp_ge_u32 s29, 109\n"
      "s_cselect_b64 s[34:35], s[20:21], s[34:35]\n"
      "s_cselect_b32 s30, s31, s30\n"
      "s_lshl_b32 s30, s30, 21\n"
      "s_load_dword s100, s[34:35], s30\n"
      "s_and_b32 s29, s29, 7\n"
      "s_cmp_eq_u32 s29, 0\n"
      "s_cselect_b64 s[34:35], s[6:7], s[34:35]\n"
      "s_cmp_eq_u32 s29, 1\n"
      "s_cselect_b64 s[34:35], s[90:91], s[34:35]\n"
      "s_cmp_eq_u32 s29, 2\n"
      "s_cselect_b64 s[34:35], s[86:87], s[34:35]\n"
      "s_cmp_eq_u32 s29, 3\n"
      "s_cselect_b64 s[34:35], s[60:61], s[34:35]\n"
      "s_cmp_eq_u32 s29, 4\n"
      "s_cselect_b64 s[34:35], s[12:13], s[34:35]\n"
      "s_load_dword s100, s[34:35], 0\n"
      "s_add_u32 m0, s2, 36864\n"
      "s_nop 0\n"
      "global_load_lds_dwordx4 v170, s[4:5]\n"
      "global_load_lds_dwordx4 v170, s[4:5] offset:1024\n"
      "global_load_lds_dwordx4 v193, s[4:5] offset:2048\n"
      "global_load_lds_dwordx4 v195, s[4:5] offset:3072\n"
      "s_add_u32 s4, s4, 4096\n"
      "s_addc_u32 s5, s5, 0\n"
      "s_add_u32 m0, s2, 40960\n"
      "s_nop 0\n"
      "global_load_lds_dwordx4 v194, s[4:5]\n"
      "global_load_lds_dwordx4 v170, s[4:5] offset:1024\n"
      "global_load_lds_dwordx4 v192, s[4:5] offset:2048\n"
      "global_load_lds_dwordx4 v195, s[4:5] offset:3072\n"
      "s_add_u32 s4, s4, 4096\n"
      "s_addc_u32 s5, s5, 0\n"
      "s_add_u32 m0, s2, 45056\n"
      "s_nop 0\n"
      "global_load_lds_dwordx4 v195, s[4:5]\n"
      "s_add_u32 s4, s4, 32768\n"
      "s_addc_u32 s5, s5, 0\n"
      "v_add_u32 v197, 4096, v195\n"
      "v_add_u32 v229, s2, v171\n"
      "v_lshrrev_b32 v230, 2, v168\n"
      "global_load_dwordx4 a[72:75], v170, s[4:5] offset:-4096\n"
      "v_and_b32 v198, 3, v230\n"
      "v_lshrrev_b32_e64 v228, 4, v168\n"
      "global_load_dwordx4 a[76:79], v170, s[4:5] offset:-3072\n"
      "v_lshl_add_u32 v198, v228, 3, v198\n"
      "v_mul_u32_u24 v198, 1152, v198\n"
      "global_load_dwordx4 a[80:83], v193, s[4:5] offset:-2048\n"
      "v_and_b32_e64 v228, 1, v168\n"
      "v_lshl_add_u32 v198, v228, 3, v198\n"
      "global_load_dwordx4 a[84:87], v195, s[4:5] offset:-1024\n"
      "v_add_u32 v198, 36864, v198\n"
      "v_and_b32_e64 v230, 6, v230\n"
      "global_load_dwordx4 a[88:91], v194, s[4:5]\n"
      "v_bfe_u32 v228, v168, 1, 1\n"
      "s_mul_i32 s29, s24, 6\n"
      "global_load_dwordx4 a[92:95], v170, s[4:5] offset:1024\n"
      "v_add_u32 v228, s29, v228\n"
      "v_xor_b32_e64 v148, v228, v230\n"
      "global_load_dwordx4 a[96:99], v192, s[4:5] offset:2048\n"
      "v_lshl_add_u32 v148, v148, 4, v198\n"
      "v_add_u32_e64 v228, 2, v228\n"
      "v_xor_b32_e64 v149, v228, v230\n"
      "global_load_dwordx4 a[100:103], v195, s[4:5] offset:3072\n"
      "v_lshl_add_u32 v149, v149, 4, v198\n"
      "v_add_u32_e64 v228, 2, v228\n"
      "global_load_dwordx4 a[104:107], v197, s[4:5]\n"
      "v_xor_b32_e64 v231, v228, v230\n"
      "v_lshl_add_u32 v231, v231, 4, v198\n"
      "s_movk_i32 s29, 18\n"
      "s_mov_b32 s30, 73600\n"
      "s_cmp_eq_u32 s24, 1\n"
      "v_add_u32 v170, 36864, v170\n"
      "v_add_u32 v195, 36864, v195\n"
      "v_add_u32 v193, 36864, v193\n"
      "v_add_u32 v194, 36864, v194\n"
      "v_add_u32 v192, 36864, v192\n"
      "v_add_u32 v197, 36864, v197\n"
      "global_load_dwordx4 a[108:111], v170, s[4:5] offset:-4096\n"
      "s_cselect_b64 s[12:13], s[6:7], s[12:13]\n"
      "s_cmp_eq_u32 s24, 2\n"
      "s_cselect_b32 s29, 16, s29\n"
      "s_cselect_b32 s30, L_cend_%=-L_ipf0_%=-128, s30\n"
      "s_cmp_eq_u32 s24, 3\n"
      "global_load_dwordx4 a[112:115], v170, s[4:5] offset:-3072\n"
      "s_cselect_b64 s[12:13], s[60:61], s[12:13]\n"
      "s_cselect_b32 s29, 10, s29\n"
      "s_cselect_b32 s30, 40832, s30\n"
      "global_load_dwordx4 a[116:119], v193, s[4:5] offset:-2048\n"
      "s_cmp_eq_u32 s24, 0\n"
      "s_cselect_b64 s[12:13], s[90:91], s[12:13]\n"
      "global_load_dwordx4 a[120:123], v195, s[4:5] offset:-1024\n"
      "s_cselect_b32 s29, 3, s29\n"
      "s_cselect_b32 s30, 9476, s30\n"
      "global_load_dwordx4 a[124:127], v194, s[4:5]\n"
      "s_mul_i32 s28, s28, s29\n"
      "s_sub_u32 s29, s29, 1\n"
      "global_load_dwordx4 a[128:131], v170, s[4:5] offset:1024\n"
      "v_min_u32 v169, s29, v168\n"
      "v_add_lshl_u32 v169, v169, s28, 7\n"
      "global_load_dwordx4 a[132:135], v192, s[4:5] offset:2048\n"
      "v_min_u32_e64 v169, s30, v169\n"
      "s_mul_i32 s28, s24, 10240\n"
      "global_load_dwordx4 a[136:139], v195, s[4:5] offset:3072\n"
      "s_add_u32 s28, s28, 4096\n"
      "v_add_u32 v198, s28, v171\n"
      "global_load_dwordx4 a[140:143], v197, s[4:5]\n"
      "v_add_u32 v230, 8192, v198\n"
      "global_load_dword v196, v196, s[86:87]\n"
      "s_mov_b64 s[56:57], s[90:91]\n"
      "s_load_dwordx16 s[84:99], s[84:85], 64\n"
      "s_lshl_b32 s29, s25, 8\n"
      "s_add_u32 s52, s60, s29\n"
      "s_addc_u32 s53, s61, 0\n"
      "s_lshl_b32 s29, s24, 5\n"
      "v_lshrrev_b32_e64 v151, 3, v168\n"
      "v_lshl_add_u32 v228, v151, 2, s29\n"
      "v_add_u32 v228, 41408, v228\n"
      "global_load_dword v228, v228, s[52:53]\n"
      "global_load_dword v30, v169, s[12:13]\n"
      "v_xor_b32 v151, v151, v168\n"
      "v_and_b32 v151, 7, v151\n"
      "s_waitcnt vmcnt(21)\n"
      "s_barrier\n"
      "ds_read_b64_tr_b16 a[0:1], v148\n"
      "ds_read_b64_tr_b16 a[2:3], v148 offset:4608\n"
      "ds_read_b64_tr_b16 a[4:5], v149\n"
      "ds_read_b64_tr_b16 a[6:7], v149 offset:4608\n"
      "ds_read_b64_tr_b16 a[8:9], v231\n"
      "ds_read_b64_tr_b16 a[10:11], v231 offset:4608\n"
      "ds_read_b64_tr_b16 a[12:13], v148 offset:384\n"
      "ds_read_b64_tr_b16 a[14:15], v148 offset:4992\n"
      "ds_read_b64_tr_b16 a[16:17], v149 offset:384\n"
      "v_add_u32 v170, 36864, v170\n"
      "v_add_u32 v195, 36864, v195\n"
      "v_add_u32 v193, 36864, v193\n"
      "v_add_u32 v194, 36864, v194\n"
      "v_add_u32 v192, 36864, v192\n"
      "v_add_u32 v197, 36864, v197\n"
      "global_load_dwordx4 a[144:147], v170, s[4:5] offset:-4096\n"
      "ds_read_b64_tr_b16 a[18:19], v149 offset:4992\n"
      "ds_read_b64_tr_b16 a[20:21], v231 offset:384\n"
      "ds_read_b64_tr_b16 a[22:23], v231 offset:4992\n"
      "ds_read_b64_tr_b16 a[24:25], v148 offset:768\n"
      "ds_read_b64_tr_b16 a[26:27], v148 offset:5376\n"
      "ds_read_b64_tr_b16 a[28:29], v149 offset:768\n"
      "ds_read_b64_tr_b16 a[30:31], v149 offset:5376\n"
      "ds_read_b64_tr_b16 a[32:33], v231 offset:768\n"
      "ds_read_b64_tr_b16 a[34:35], v231 offset:5376\n"
      "ds_read_b64_tr_b16 a[36:37], v148 offset:36864\n"
      "ds_read_b64_tr_b16 a[38:39], v148 offset:41472\n"
      "global_load_dwordx4 a[148:151], v170, s[4:5] offset:-3072\n"
      "ds_read_b64_tr_b16 a[40:41], v149 offset:36864\n"
      "ds_read_b64_tr_b16 a[42:43], v149 offset:41472\n"
      "ds_read_b64_tr_b16 a[44:45], v231 offset:36864\n"
      "ds_read_b64_tr_b16 a[46:47], v231 offset:41472\n"
      "global_load_dwordx4 a[152:155], v193, s[4:5] offset:-2048\n"
      "ds_read_b64_tr_b16 a[48:49], v148 offset:37248\n"
      "ds_read_b64_tr_b16 a[50:51], v148 offset:41856\n"
      "ds_read_b64_tr_b16 a[52:53], v149 offset:37248\n"
      "ds_read_b64_tr_b16 a[54:55], v149 offset:41856\n"
      "global_load_dwordx4 a[156:159], v195, s[4:5] offset:-1024\n"
      "ds_read_b64_tr_b16 a[56:57], v231 offset:37248\n"
      "ds_read_b64_tr_b16 a[58:59], v231 offset:41856\n"
      "ds_read_b64_tr_b16 a[60:61], v148 offset:37632\n"
      "ds_read_b64_tr_b16 a[62:63], v148 offset:42240\n"
      "global_load_dwordx4 a[160:163], v194, s[4:5]\n"
      "ds_read_b64_tr_b16 a[64:65], v149 offset:37632\n"
      "ds_read_b64_tr_b16 a[66:67], v149 offset:42240\n"
      "ds_read_b64_tr_b16 a[68:69], v231 offset:37632\n"
      "ds_read_b64_tr_b16 a[70:71], v231 offset:42240\n"
      "s_waitcnt lgkmcnt(0)\n"
      "s_pack_ll_b32_b16 s28, s36, s44\n"
      "s_waitcnt vmcnt(7)\n"
      "v_dot2_f32_bf16 v150, s28, v196, 0\n"
      "s_pack_hh_b32_b16 s29, s36, s44\n"
      "v_dot2_f32_bf16 v168, s29, v196, 0\n"
      "s_pack_ll_b32_b16 s30, s37, s45\n"
      "v_dot2_f32_bf16 v240, s30, v196, 0\n"
      "s_pack_hh_b32_b16 s31, s37, s45\n"
      "v_dot2_f32_bf16 v241, s31, v196, 0\n"
      "s_pack_ll_b32_b16 s28, s38, s46\n"
      "v_dot2_f32_bf16 v243, s28, v196, 0\n"
      "s_pack_hh_b32_b16 s29, s38, s46\n"
      "v_dot2_f32_bf16 v242, s29, v196, 0\n"
      "s_pack_ll_b32_b16 s30, s39, s47\n"
      "v_dot2_f32_bf16 v169, s30, v196, 0\n"
      "s_pack_hh_b32_b16 s31, s39, s47\n"
      "v_dot2_f32_bf16 v249, s31, v196, 0\n"
      "s_pack_ll_b32_b16 s28, s40, s48\n"
      "v_dot2_f32_bf16 v248, s28, v196, 0\n"
      "s_waitcnt vmcnt(6)\n"
      "v_lshl_add_u32 v228, v151, 4, v228\n"
      "s_lshl_b32 s10, s24, 10\n"
      "s_add_u32 m0, s10, 18432\n"
      "s_nop 0\n"
      "global_load_lds_dwordx4 v228, s[16:17]\n"
      "s_add_u32 m0, s10, 24448\n"
      "s_nop 0\n"
      "global_load_lds_dwordx4 v228, s[16:17] offset:128\n"
      "s_add_u32 m0, s10, 30464\n"
      "s_nop 0\n"
      "global_load_lds_dwordx4 v228, s[16:17] offset:256\n"
      "v_add_u32_e64 v151, s10, v171\n"
      "s_add_u32 m0, s10, 124928\n"
      "s_nop 0\n"
      "global_load_lds_dwordx4 v151, s[56:57]\n"
      "v_add_u32 v151, 4096, v151\n"
      "s_add_u32 m0, s10, 129024\n"
      "s_nop 0\n"
      "global_load_lds_dwordx4 v151, s[56:57]\n"
      "v_add_u32 v151, 4096, v151\n"
      "v_min_u32 v151, 9600, v151\n"
      "s_add_u32 m0, s10, 133120\n"
      "s_nop 0\n"
      "global_load_lds_dwordx4 v151, s[56:57]\n"
      "s_pack_hh_b32_b16 s29, s40, s48\n"
      "v_dot2_f32_bf16 v204, s29, v196, 0\n"
      "s_pack_ll_b32_b16 s30, s41, s49\n"
      "v_dot2_f32_bf16 v171, s30, v196, 0\n"
      "s_pack_hh_b32_b16 s31, s41, s49\n"
      "v_dot2_f32_bf16 v207, s31, v196, 0\n"
      "s_pack_ll_b32_b16 s28, s42, s50\n"
      "v_dot2_f32_bf16 v151, s28, v196, 0\n"
      "s_pack_hh_b32_b16 s29, s42, s50\n"
      "v_dot2_f32_bf16 v206, s29, v196, 0\n"
      "s_pack_ll_b32_b16 s30, s43, s51\n"
      "v_dot2_f32_bf16 v250, s30, v196, 0\n"
      "s_pack_hh_b32_b16 s31, s43, s51\n"
      "v_dot2_f32_bf16 v205, s31, v196, 0\n"
      "s_load_dwordx8 s[36:43], s[60:61], 41216\n"
      "s_load_dwordx4 s[44:47], s[60:61], 41248\n"
      "s_mov_b64 s[48:49], s[60:61]\n"
      "v_max_f32 v150, 0, v150\n"
      "v_max_f32 v168, 0, v168\n"
      "v_cvt_pk_bf16_f32 v150, v150, v168\n"
      "v_max_f32 v240, 0, v240\n"
      "v_max_f32 v241, 0, v241\n"
      "global_load_dwordx4 v[114:117], v198, s[60:61] offset:-4096\n"
      "v_cvt_pk_bf16_f32 v240, v240, v241\n"
      "v_max_f32 v243, 0, v243\n"
      "v_max_f32_e64 v242, 0, v242\n"
      "v_cvt_pk_bf16_f32 v243, v243, v242\n"
      "v_max_f32 v169, 0, v169\n"
      "global_load_dwordx4 a[164:167], v170, s[4:5] offset:1024\n"
      "v_max_f32 v249, 0, v249\n"
      "v_cvt_pk_bf16_f32 v169, v169, v249\n"
      "v_max_f32 v248, 0, v248\n"
      "v_max_f32 v204, 0, v204\n"
      "global_load_dwordx4 a[168:171], v192, s[4:5] offset:2048\n"
      "v_cvt_pk_bf16_f32 v248, v248, v204\n"
      "v_max_f32 v171, 0, v171\n"
      "v_max_f32 v207, 0, v207\n"
      "v_cvt_pk_bf16_f32 v171, v171, v207\n"
      "global_load_dwordx4 a[172:175], v195, s[4:5] offset:3072\n"
      "v_max_f32 v151, 0, v151\n"
      "v_max_f32 v206, 0, v206\n"
      "v_cvt_pk_bf16_f32 v151, v151, v206\n"
      "v_max_f32 v250, 0, v250\n"
      "global_load_dwordx4 a[176:179], v197, s[4:5]\n"
      "v_max_f32 v205, 0, v205\n"
      "v_cvt_pk_bf16_f32 v250, v250, v205\n"
      "s_waitcnt lgkmcnt(0)\n"
      "s_barrier\n"
      "s_pack_ll_b32_b16 s28, s64, s66\n"
      "v_dot2_f32_bf16 v204, s28, v150, 0\n"
      "s_pack_hh_b32_b16 s29, s64, s66\n"
      "v_dot2_f32_bf16 v205, s29, v150, 0\n"
      "s_pack_ll_b32_b16 s30, s65, s67\n"
      "v_dot2_f32_bf16 v206, s30, v150, 0\n"
      "s_pack_hh_b32_b16 s31, s65, s67\n"
      "v_dot2_f32_bf16 v207, s31, v150, 0\n"
      "s_pack_ll_b32_b16 s28, s68, s70\n"
      "v_dot2_f32_bf16 v204, s28, v240, v204\n"
      "s_pack_hh_b32_b16 s29, s68, s70\n"
      "v_dot2_f32_bf16 v205, s29, v240, v205\n"
      "s_pack_ll_b32_b16 s30, s69, s71\n"
      "v_dot2_f32_bf16 v206, s30, v240, v206\n"
      "s_pack_hh_b32_b16 s31, s69, s71\n"
      "v_dot2_f32_bf16 v207, s31, v240, v207\n"
      "v_add_u32 v170, 36864, v170\n"
      "v_add_u32 v195, 36864, v195\n"
      "v_add_u32 v193, 36864, v193\n"
      "v_add_u32 v194, 36864, v194\n"
      "v_add_u32 v192, 36864, v192\n"
      "v_add_u32 v197, 36864, v197\n"
      "global_load_dwordx4 a[180:183], v170, s[4:5] offset:-4096\n"
      "s_pack_ll_b32_b16 s28, s72, s74\n"
      "v_dot2_f32_bf16 v204, s28, v243, v204\n"
      "s_pack_hh_b32_b16 s29, s72, s74\n"
      "v_dot2_f32_bf16 v205, s29, v243, v205\n"
      "s_pack_ll_b32_b16 s30, s73, s75\n"
      "v_dot2_f32_bf16 v206, s30, v243, v206\n"
      "s_pack_hh_b32_b16 s31, s73, s75\n"
      "v_dot2_f32_bf16 v207, s31, v243, v207\n"
      "s_pack_ll_b32_b16 s28, s76, s78\n"
      "v_dot2_f32_bf16 v204, s28, v169, v204\n"
      "s_pack_hh_b32_b16 s29, s76, s78\n"
      "v_dot2_f32_bf16 v205, s29, v169, v205\n"
      "s_pack_ll_b32_b16 s30, s77, s79\n"
      "v_dot2_f32_bf16 v206, s30, v169, v206\n"
      "s_pack_hh_b32_b16 s31, s77, s79\n"
      "v_dot2_f32_bf16 v207, s31, v169, v207\n"
      "s_load_dwordx16 s[64:79], s[54:55], 40960\n"
      "s_lshl_b32 s28, s24, 4\n"
      "global_load_dwordx4 a[184:187], v170, s[4:5] offset:-3072\n"
      "s_lshr_b64 s[8:9], s[8:9], s28\n"
      "s_lshl_b32 s8, s8, 16\n"
      "v_mov_b32 v196, s8\n"
      "v_max_f32 v196, 0xc2c80000, v196\n"
      "v_min_f32 v196, 0x40930000, v196\n"
      "v_mul_f32 v196, 0x3fb8aa3b, v196\n"
      "v_exp_f32 v196, v196\n"
      "s_nop 0\n"
      "v_cvt_pk_bf16_f32 v196, v196, v196\n"
      "global_load_dwordx4 a[188:191], v193, s[4:5] offset:-2048\n"
      "v_lshlrev_b32_e64 v196, 16, v196\n"
      "s_pack_ll_b32_b16 s28, s84, s86\n"
      "v_dot2_f32_bf16 v204, s28, v248, v204\n"
      "s_pack_hh_b32_b16 s29, s84, s86\n"
      "v_dot2_f32_bf16 v205, s29, v248, v205\n"
      "s_pack_ll_b32_b16 s30, s85, s87\n"
      "v_dot2_f32_bf16 v206, s30, v248, v206\n"
      "global_load_dwordx4 a[192:195], v195, s[4:5] offset:-1024\n"
      "s_pack_hh_b32_b16 s31, s85, s87\n"
      "v_dot2_f32_bf16 v207, s31, v248, v207\n"
      "s_pack_ll_b32_b16 s28, s88, s90\n"
      "v_dot2_f32_bf16 v204, s28, v171, v204\n"
      "s_pack_hh_b32_b16 s29, s88, s90\n"
      "v_dot2_f32_bf16 v205, s29, v171, v205\n"
      "s_pack_ll_b32_b16 s30, s89, s91\n"
      "v_dot2_f32_bf16 v206, s30, v171, v206\n"
      "global_load_dwordx4 a[196:199], v194, s[4:5]\n"
      "s_pack_hh_b32_b16 s31, s89, s91\n"
      "v_dot2_f32_bf16 v207, s31, v171, v207\n"
      "s_pack_ll_b32_b16 s28, s92, s94\n"
      "v_dot2_f32_bf16 v204, s28, v151, v204\n"
      "s_pack_hh_b32_b16 s29, s92, s94\n"
      "v_dot2_f32_bf16 v205, s29, v151, v205\n"
      "s_pack_ll_b32_b16 s30, s93, s95\n"
      "v_dot2_f32_bf16 v206, s30, v151, v206\n"
      "global_load_dwordx4 a[200:203], v170, s[4:5] offset:1024\n"
      "s_pack_hh_b32_b16 s31, s93, s95\n"
      "v_dot2_f32_bf16 v207, s31, v151, v207\n"
      "s_pack_ll_b32_b16 s28, s96, s98\n"
      "v_dot2_f32_bf16 v204, s28, v250, v204\n"
      "s_pack_hh_b32_b16 s29, s96, s98\n"
      "v_dot2_f32_bf16 v205, s29, v250, v205\n"
      "s_pack_ll_b32_b16 s30, s97, s99\n"
      "global_load_dwordx4 a[204:207], v192, s[4:5] offset:2048\n"
      "v_dot2_f32_bf16 v206, s30, v250, v206\n"
      "s_pack_hh_b32_b16 s31, s97, s99\n"
      "v_dot2_f32_bf16 v207, s31, v250, v207\n"
      "s_load_dwordx16 s[80:95], s[52:53], 41280\n"
      "s_load_dwordx4 s[96:99], s[52:53], 41344\n"
      "s_load_dwordx2 s[100:101], s[52:53], 41360\n"
      "s_load_dword s26, s[52:53], 41376\n"
      "s_load_dword s3, s[52:53], 41380\n"
      "global_load_dwordx4 a[208:211], v195, s[4:5] offset:3072\n"
      "ds_write_b128 v229, a[72:75]\n"
      "ds_write_b128 v229, a[76:79] offset:1024\n"
      "ds_write_b128 v229, a[80:83] offset:2048\n"
      "ds_write_b128 v229, a[84:87] offset:3072\n"
      "ds_write_b128 v229, a[88:91] offset:4096\n"
      "ds_write_b128 v229, a[92:95] offset:5120\n"
      "ds_write_b128 v229, a[96:99] offset:6144\n"
      "global_load_dwordx4 a[212:215], v197, s[4:5]\n"
      "ds_write_b128 v229, a[100:103] offset:7168\n"
      "ds_write_b128 v229, a[104:107] offset:8192\n"
      "ds_write_b128 v229, a[108:111] offset:36864\n"
      "ds_write_b128 v229, a[112:115] offset:37888\n"
      "ds_write_b128 v229, a[116:119] offset:38912\n"
      "ds_write_b128 v229, a[120:123] offset:39936\n"
      "ds_write_b128 v229, a[124:127] offset:40960\n"
      "ds_write_b128 v229, a[128:131] offset:41984\n"
      "global_load_dwordx4 v[168:171], v198, s[60:61] offset:3072\n"
      "ds_write_b128 v229, a[132:135] offset:43008\n"
      "ds_write_b128 v229, a[136:139] offset:44032\n"
      "ds_write_b128 v229, a[140:143] offset:45056\n"
      "s_waitcnt vmcnt(15)\n"
      "s_waitcnt lgkmcnt(0)\n"
      "s_barrier\n"
      "L_sched_%=:\n"
      "ds_read_b64_tr_b16 a[76:77], v149\n"
      "s_waitcnt vmcnt(14)\n"
      "ds_read_b128 v[248:251], v114 offset:18432\n"
      "ds_read_b128 v[192:195], v115 offset:18432\n"
      "ds_read_b64_tr_b16 a[82:83], v231 offset:4608\n"
      "ds_read_b64_tr_b16 a[88:89], v149 offset:384\n"
      "v_cvt_pk_bf16_f32 v207, v206, v207\n"
      "v_cmp_nge_f32 vcc, 0x42480000, v196\n"
      "v_lshlrev_b32 v206, 16, v207\n"
      "s_waitcnt vmcnt(0)\n"
      "v_add_u32 v197, 3072, v171\n"
      "v_cvt_pk_bf16_f32 v205, v204, v205\n"
      "v_lshlrev_b32 v204, 16, v205\n"
      "s_mov_b64 s[50:51], vcc\n"
      "ds_read_b128 v[240:243], v114 offset:20480\n"
      "ds_read_b128 v[152:155], v115 offset:20480\n"
      "ds_read_b64_tr_b16 a[80:81], v231\n"
      "v_and_b32 v207, 0xffff0000, v207\n"
      "v_mul_f32 v207, 0xbfb8aa3b, v207\n"
      "v_and_b32 v205, 0xffff0000, v205\n"
      "v_mul_f32 v206, 0xbfb8aa3b, v206\n"
      "v_mul_f32 v204, 0xbfb8aa3b, v204\n"
      "v_mul_f32 v205, 0xbfb8aa3b, v205\n"
      "v_exp_f32 v207, v207\n"
      "v_exp_f32 v206, v206\n"
      "v_exp_f32 v204, v204\n"
      "v_exp_f32 v205, v205\n"
      "s_waitcnt lgkmcnt(5)\n"
      "v_mfma_f32_16x16x32_bf16 v[200:203], a[0:3], v[248:251], 0\n"
      "ds_read_b64_tr_b16 a[90:91], v149 offset:4992\n"
      "v_add_f32 v204, 1.0, v204\n"
      "v_mfma_f32_16x16x32_bf16 v[200:203], a[36:39], v[192:195], v[200:203]\n"
      "global_load_dwordx4 a[216:219], v197, s[6:7] offset:-3072\n"
      "v_mfma_f32_16x16x32_bf16 v[176:179], a[4:7], v[248:251], 0\n"
      "ds_read_b64_tr_b16 a[94:95], v231 offset:4992\n"
      "v_add_f32_e64 v205, 1.0, v205\n"
      "v_mfma_f32_16x16x32_bf16 v[176:179], a[40:43], v[192:195], v[176:179]\n"
      "ds_read_b64_tr_b16 a[100:101], v149 offset:768\n"
      "ds_read_b64_tr_b16 a[84:85], v148 offset:384\n"
      "ds_read_b64_tr_b16 a[96:97], v148 offset:768\n"
      "s_waitcnt lgkmcnt(5)\n"
      "v_mfma_f32_16x16x32_bf16 v[212:215], a[8:11], v[248:251], 0\n"
      "ds_read_b64_tr_b16 a[98:99], v148 offset:5376\n"
      "v_add_f32 v206, 1.0, v206\n"
      "v_mfma_f32_16x16x32_bf16 v[212:215], a[44:47], v[192:195], v[212:215]\n"
      "ds_read_b64_tr_b16 a[92:93], v231 offset:384\n"
      "ds_read_b64_tr_b16 a[86:87], v148 offset:4992\n"
      "v_mfma_f32_16x16x32_bf16 v[184:187], a[12:15], v[248:251], 0\n"
      "ds_read_b64_tr_b16 a[102:103], v149 offset:5376\n"
      "v_mul_f32 v196, 0x3fb8aa3b, v196\n"
      "v_mfma_f32_16x16x32_bf16 v[184:187], a[48:51], v[192:195], v[184:187]\n"
      "global_load_dwordx4 a[220:223], v197, s[6:7] offset:-2944\n"
      "v_rcp_f32_e64 v205, v205\n"
      "v_mfma_f32_16x16x32_bf16 v[244:247], a[16:19], v[248:251], 0\n"
      "ds_read_b64_tr_b16 a[78:79], v149 offset:4608\n"
      "v_add_f32_e64 v207, 1.0, v207\n"
      "v_mfma_f32_16x16x32_bf16 v[244:247], a[52:55], v[192:195], v[244:247]\n"
      "ds_read_b64_tr_b16 a[104:105], v231 offset:768\n"
      "v_rcp_f32_e64 v204, v204\n"
      "v_mfma_f32_16x16x32_bf16 v[232:235], a[20:23], v[248:251], 0\n"
      "ds_read_b64_tr_b16 a[74:75], v148 offset:4608\n"
      "ds_read_b64_tr_b16 a[72:73], v148\n"
      "v_mfma_f32_16x16x32_bf16 v[232:235], a[56:59], v[192:195], v[232:235]\n"
      "ds_read_b64_tr_b16 a[106:107], v231 offset:5376\n"
      "ds_read_b64_tr_b16 a[110:111], v148 offset:41472\n"
      "v_mfma_f32_16x16x32_bf16 v[216:219], a[24:27], v[248:251], 0\n"
      "ds_read_b64_tr_b16 a[112:113], v149 offset:36864\n"
      "v_cvt_pk_bf16_f32 v205, v204, v205\n"
      "v_mfma_f32_16x16x32_bf16 v[216:219], a[60:63], v[192:195], v[216:219]\n"
      "ds_read_b64_tr_b16 a[114:115], v149 offset:41472\n"
      "v_rcp_f32 v206, v206\n"
      "v_mfma_f32_16x16x32_bf16 v[188:191], a[28:31], v[248:251], 0\n"
      "ds_read_b64_tr_b16 a[116:117], v231 offset:36864\n"
      "v_lshlrev_b32 v204, 16, v205\n"
      "v_mfma_f32_16x16x32_bf16 v[188:191], a[64:67], v[192:195], v[188:191]\n"
      "ds_read_b64_tr_b16 a[118:119], v231 offset:41472\n"
      "v_and_b32 v205, 0xffff0000, v205\n"
      "v_mfma_f32_16x16x32_bf16 v[180:183], a[32:35], v[248:251], 0\n"
      "global_load_dwordx4 a[224:227], v197, s[6:7] offset:-2816\n"
      "v_add_f32 v204, -0.5, v204\n"
      "v_mfma_f32_16x16x32_bf16 v[180:183], a[68:71], v[192:195], v[180:183]\n"
      "ds_read_b64_tr_b16 a[108:109], v148 offset:36864\n"
      "v_rcp_f32 v207, v207\n"
      "v_mfma_f32_16x16x32_bf16 v[192:195], a[0:3], v[240:243], 0\n"
      "ds_read_b64_tr_b16 a[122:123], v148 offset:41856\n"
      "v_cvt_pk_bf16_f32 v207, v206, v207\n"
      "v_mfma_f32_16x16x32_bf16 v[192:195], a[36:39], v[152:155], v[192:195]\n"
      "ds_read_b64_tr_b16 a[120:121], v148 offset:37248\n"
      "ds_read_b64_tr_b16 a[124:125], v149 offset:37248\n"
      "v_mfma_f32_16x16x32_bf16 v[248:251], a[4:7], v[240:243], 0\n"
      "ds_read_b64_tr_b16 a[126:127], v149 offset:41856\n"
      "v_lshlrev_b32 v206, 16, v207\n"
      "v_mfma_f32_16x16x32_bf16 v[248:251], a[40:43], v[152:155], v[248:251]\n"
      "ds_read_b64_tr_b16 a[128:129], v231 offset:37248\n"
      "v_add_f32 v205, -0.5, v205\n"
      "v_mfma_f32_16x16x32_bf16 v[144:147], a[8:11], v[240:243], 0\n"
      "ds_read_b64_tr_b16 a[130:131], v231 offset:41856\n"
      "v_mul_f32 v204, 0x41b8aa3b, v204\n"
      "v_mfma_f32_16x16x32_bf16 v[144:147], a[44:47], v[152:155], v[144:147]\n"
      "global_load_dwordx4 a[228:231], v197, s[6:7]\n"
      "v_mfma_f32_16x16x32_bf16 v[140:143], a[12:15], v[240:243], 0\n"
      "ds_read_b64_tr_b16 a[134:135], v148 offset:42240\n"
      "v_add_f32_e64 v206, -0.5, v206\n"
      "v_mfma_f32_16x16x32_bf16 v[140:143], a[48:51], v[152:155], v[140:143]\n"
      "global_load_dwordx4 a[232:235], v197, s[6:7] offset:128\n"
      "v_mfma_f32_16x16x32_bf16 v[164:167], a[16:19], v[240:243], 0\n"
      "ds_read_b64_tr_b16 a[136:137], v149 offset:37632\n"
      "v_mul_f32 v206, 0x41b8aa3b, v206\n"
      "v_mfma_f32_16x16x32_bf16 v[208:211], a[20:23], v[240:243], 0\n"
      "ds_read_b64_tr_b16 a[138:139], v149 offset:42240\n"
      "v_and_b32 v207, 0xffff0000, v207\n"
      "v_mfma_f32_16x16x32_bf16 v[208:211], a[56:59], v[152:155], v[208:211]\n"
      "ds_read_b64_tr_b16 a[140:141], v231 offset:37632\n"
      "v_add_f32_e64 v207, -0.5, v207\n"
      "v_mul_f32 v205, 0x41b8aa3b, v205\n"
      "v_mul_f32 v207, 0x41b8aa3b, v207\n"
      "v_mfma_f32_16x16x32_bf16 v[224:227], a[24:27], v[240:243], 0\n"
      "ds_read_b64_tr_b16 a[132:133], v148 offset:37632\n"
      "v_mfma_f32_16x16x32_bf16 v[164:167], a[52:55], v[152:155], v[164:167]\n"
      "global_load_dwordx4 a[236:239], v197, s[6:7] offset:256\n"
      "v_add_u32 v197, 9216, v171\n"
      "v_mfma_f32_16x16x32_bf16 v[136:139], a[28:31], v[240:243], 0\n"
      "global_load_dwordx4 a[240:243], v197, s[6:7] offset:-3072\n"
      "ds_write_b32 v199, v205 offset:768\n"
      "ds_write_b32 v199, v206 offset:1536\n"
      "v_mfma_f32_16x16x32_bf16 v[136:139], a[64:67], v[152:155], v[136:139]\n"
      "ds_write_b32 v199, v207 offset:2304\n"
      "v_mfma_f32_16x16x32_bf16 v[224:227], a[60:63], v[152:155], v[224:227]\n"
      "ds_write_b32 v199, v204\n"
      "v_mfma_f32_16x16x32_bf16 v[252:255], a[32:35], v[240:243], 0\n"
      "ds_read_b64_tr_b16 a[142:143], v231 offset:42240\n"
      "v_mfma_f32_16x16x32_bf16 v[252:255], a[68:71], v[152:155], v[252:255]\n"
      "global_load_dwordx4 a[244:247], v197, s[6:7] offset:-2944\n"
      "global_load_dwordx4 a[248:251], v197, s[6:7] offset:-2816\n"
      "s_waitcnt lgkmcnt(0)\n"
      "s_barrier\n"
      "ds_read_b128 v[204:207], v114 offset:24576\n"
      "ds_read_b128 v[152:155], v115 offset:24576\n"
      "v_mov_b32 v43, 0\n"
      "v_readfirstlane_b32 s36, v196\n"
      "v_mov_b32 v65, 0\n"
      "v_mov_b32 v57, 0\n"
      "v_mov_b32 v42, 0\n"
      "v_mov_b32 v61, 0\n"
      "s_mov_b32 s11, 100466688\n"
      "v_add_u32 v196, 3136, v168\n"
      "s_sub_u32 s16, s16, 99876864\n"
      "ds_read2_b32 v[98:99], v196 offset0:32 offset1:33\n"
      "global_load_dwordx4 v[118:121], v198, s[48:49] offset:-3072\n"
      "v_mov_b32 v36, 0\n"
      "s_subb_u32 s17, s17, 0\n"
      "v_mov_b32 v199, 134528\n"
      "v_mov_b32_e64 v49, 0\n"
      "s_sub_u32 s22, s22, 99876864\n"
      "ds_read2_b32 v[86:87], v168 offset0:32 offset1:33\n"
      "v_mov_b32 v53, 0\n"
      "s_subb_u32 s23, s23, 0\n"
      "ds_read2_b32 v[96:97], v196 offset0:18 offset1:19\n"
      "ds_read2_b32 v[78:79], v168 offset0:0 offset1:1\n"
      "ds_read_b128 v[160:163], v115 offset:26624\n"
      "s_add_u32 s12, s20, 16777216\n"
      "s_waitcnt lgkmcnt(5)\n"
      "v_mfma_f32_16x16x32_bf16 v[200:203], a[72:75], v[204:207], v[200:203]\n"
      "ds_read2_b32 v[82:83], v168 offset0:16 offset1:17\n"
      "v_add_u32 v150, 6272, v168\n"
      "v_mfma_f32_16x16x32_bf16 v[200:203], a[108:111], v[152:155], v[200:203]\n"
      "ds_read_b128 v[240:243], v114 offset:26624\n"
      "ds_read2_b32 v[84:85], v168 offset0:18 offset1:19\n"
      "v_mfma_f32_16x16x32_bf16 v[176:179], a[76:79], v[204:207], v[176:179]\n"
      "ds_read2_b32 v[88:89], v168 offset0:34 offset1:35\n"
      "s_addc_u32 s13, s21, 0\n"
      "v_mfma_f32_16x16x32_bf16 v[176:179], a[112:115], v[152:155], v[176:179]\n"
      "global_load_dwordx4 v[122:125], v198, s[48:49] offset:-2048\n"
      "v_mfma_f32_16x16x32_bf16 v[212:215], a[80:83], v[204:207], v[212:215]\n"
      "ds_read2_b32 v[100:101], v196 offset0:34 offset1:35\n"
      "s_mul_i32 s2, s24, 768\n"
      "v_mfma_f32_16x16x32_bf16 v[212:215], a[116:119], v[152:155], v[212:215]\n"
      "ds_read2_b32 v[94:95], v196 offset0:16 offset1:17\n"
      "ds_read2_b32 v[90:91], v196 offset0:0 offset1:1\n"
      "v_mfma_f32_16x16x32_bf16 v[184:187], a[84:87], v[204:207], v[184:187]\n"
      "ds_read2_b32 v[80:81], v168 offset0:2 offset1:3\n"
      "v_mov_b32 v48, 0\n"
      "v_mfma_f32_16x16x32_bf16 v[184:187], a[120:123], v[152:155], v[184:187]\n"
      "ds_read2_b32 v[106:107], v150 offset0:16 offset1:17\n"
      "ds_read2_b32 v[92:93], v196 offset0:2 offset1:3\n"
      "v_mfma_f32_16x16x32_bf16 v[244:247], a[88:91], v[204:207], v[244:247]\n"
      "ds_read_b32 v73, v170 offset:64\n"
      "ds_read_b32 v70, v169 offset:192\n"
      "v_mfma_f32_16x16x32_bf16 v[244:247], a[124:127], v[152:155], v[244:247]\n"
      "ds_read2_b32 v[108:109], v150 offset0:18 offset1:19\n"
      "ds_read2_b32 v[110:111], v150 offset0:32 offset1:33\n"
      "v_mfma_f32_16x16x32_bf16 v[232:235], a[92:95], v[204:207], v[232:235]\n"
      "ds_read2_b32 v[104:105], v150 offset0:2 offset1:3\n"
      "ds_read_b32 v72, v169 offset:3328\n"
      "v_mfma_f32_16x16x32_bf16 v[232:235], a[128:131], v[152:155], v[232:235]\n"
      "s_waitcnt lgkmcnt(12)\n"
      "ds_read2_b32 v[102:103], v150 offset0:0 offset1:1\n"
      "v_mfma_f32_16x16x32_bf16 v[216:219], a[96:99], v[204:207], v[216:219]\n"
      "global_load_dwordx4 v[236:239], v230, s[48:49] offset:-3072\n"
      "v_mfma_f32_16x16x32_bf16 v[216:219], a[132:135], v[152:155], v[216:219]\n"
      "ds_write_b128 v229, a[192:195] offset:39936\n"
      "v_mfma_f32_16x16x32_bf16 v[188:191], a[100:103], v[204:207], v[188:191]\n"
      "s_add_u32 s2, s2, 150640\n"
      "ds_read_b32 v74, v169 offset:6464\n"
      "v_mfma_f32_16x16x32_bf16 v[188:191], a[136:139], v[152:155], v[188:191]\n"
      "ds_write_b128 v229, a[160:163] offset:4096\n"
      "v_mfma_f32_16x16x32_bf16 v[180:183], a[104:107], v[204:207], v[180:183]\n"
      "ds_read_b32 v77, v199\n"
      "v_mov_b32_e64 v37, 0\n"
      "v_mfma_f32_16x16x32_bf16 v[180:183], a[140:143], v[152:155], v[180:183]\n"
      "ds_read2_b32 v[112:113], v150 offset0:34 offset1:35\n"
      "ds_read_b32 v75, v170 offset:128\n"
      "v_mfma_f32_16x16x32_bf16 v[192:195], a[72:75], v[240:243], v[192:195]\n"
      "ds_write_b128 v229, a[168:171] offset:6144\n"
      "v_mfma_f32_16x16x32_bf16 v[192:195], a[108:111], v[160:163], v[192:195]\n"
      "ds_write_b128 v229, a[144:147]\n"
      "v_mfma_f32_16x16x32_bf16 v[248:251], a[76:79], v[240:243], v[248:251]\n"
      "global_load_dwordx4 v[204:207], v230, s[48:49] offset:-4096\n"
      "v_mfma_f32_16x16x32_bf16 v[248:251], a[112:115], v[160:163], v[248:251]\n"
      "ds_write_b128 v229, a[148:151] offset:1024\n"
      "v_mfma_f32_16x16x32_bf16 v[144:147], a[80:83], v[240:243], v[144:147]\n"
      "ds_read_b32 v71, v170\n"
      "v_mfma_f32_16x16x32_bf16 v[144:147], a[116:119], v[160:163], v[144:147]\n"
      "ds_write_b128 v229, a[152:155] offset:2048\n"
      "v_mfma_f32_16x16x32_bf16 v[140:143], a[84:87], v[240:243], v[140:143]\n"
      "ds_write_b128 v229, a[204:207] offset:43008\n"
      "v_mfma_f32_16x16x32_bf16 v[140:143], a[120:123], v[160:163], v[140:143]\n"
      "ds_write_b128 v229, a[156:159] offset:3072\n"
      "v_mfma_f32_16x16x32_bf16 v[208:211], a[92:95], v[240:243], v[208:211]\n"
      "ds_write_b128 v229, a[164:167] offset:5120\n"
      "v_mfma_f32_16x16x32_bf16 v[224:227], a[96:99], v[240:243], v[224:227]\n"
      "ds_write_b128 v229, a[200:203] offset:41984\n"
      "v_mfma_f32_16x16x32_bf16 v[136:139], a[100:103], v[240:243], v[136:139]\n"
      "ds_write_b128 v229, a[196:199] offset:40960\n"
      "v_mfma_f32_16x16x32_bf16 v[224:227], a[132:135], v[160:163], v[224:227]\n"
      "ds_write_b128 v229, a[212:215] offset:45056\n"
      "v_mfma_f32_16x16x32_bf16 v[252:255], a[104:107], v[240:243], v[252:255]\n"
      "ds_write_b128 v229, a[180:183] offset:36864\n"
      "v_mfma_f32_16x16x32_bf16 v[164:167], a[88:91], v[240:243], v[164:167]\n"
      "ds_write_b128 v229, a[188:191] offset:38912\n"
      "v_mfma_f32_16x16x32_bf16 v[136:139], a[136:139], v[160:163], v[136:139]\n"
      "ds_write_b128 v229, a[172:175] offset:7168\n"
      "v_mfma_f32_16x16x32_bf16 v[164:167], a[124:127], v[160:163], v[164:167]\n"
      "ds_write_b128 v229, a[176:179] offset:8192\n"
      "v_mfma_f32_16x16x32_bf16 v[252:255], a[140:143], v[160:163], v[252:255]\n"
      "ds_write_b128 v229, a[208:211] offset:44032\n"
      "v_mfma_f32_16x16x32_bf16 v[208:211], a[128:131], v[160:163], v[208:211]\n"
      "ds_write_b128 v229, a[184:187] offset:37888\n"
      "s_waitcnt lgkmcnt(0)\n"
      "s_barrier\n"
      "v_lshl_add_u32 v85, v85, 2, s2\n"
      "s_mov_b32 s60, 100270464\n"
      "v_lshl_add_u32 v87, v87, 2, s2\n"
      "s_mov_b32 s61, 196224\n"
      "v_lshl_add_u32 v78, v78, 2, s2\n"
      "s_lshr_b32 s8, s80, 8\n"
      "s_and_b32 s9, s80, 0xff\n"
      "v_lshl_add_u32 v86, v86, 2, s2\n"
      "s_waitcnt vmcnt(1)\n"
      "v_mov_b32 v153, v237\n"
      "s_mul_i32 s10, s8, 73\n"
      "ds_read_b64_tr_b16 a[148:149], v149\n"
      "s_add_u32 s10, s10, s9\n"
      "ds_read_b64_tr_b16 a[172:173], v149 offset:768\n"
      "v_lshl_add_u32 v80, v80, 2, s2\n"
      "v_lshl_add_u32 v79, v79, 2, s2\n"
      "v_mov_b32 v68, v236\n"
      "v_mov_b32 v155, v237\n"
      "v_mov_b32_e64 v152, v237\n"
      "v_mov_b32_e64 v67, v236\n"
      "v_mov_b32_e64 v66, v236\n"
      "v_mov_b32_e64 v69, v236\n"
      "v_lshl_add_u32 v89, v89, 2, s2\n"
      "s_mul_i32 s28, s10, 4802\n"
      "v_mov_b32 v154, v237\n"
      "s_add_u32 s56, s18, s28\n"
      "ds_read_b64_tr_b16 a[210:211], v149 offset:42240\n"
      "ds_read_b64_tr_b16 a[206:207], v148 offset:42240\n"
      "v_lshl_add_u32 v84, v84, 2, s2\n"
      "v_mov_b32 v237, 0\n"
      "s_addc_u32 s57, s19, 0\n"
      "ds_read_b64_tr_b16 a[146:147], v148 offset:4608\n"
      "s_and_b32 s56, s56, -16\n"
      "s_sub_u32 s58, s56, 110592\n"
      "ds_read_b64_tr_b16 a[188:189], v231 offset:36864\n"
      "ds_read_b64_tr_b16 a[144:145], v148\n"
      "s_subb_u32 s59, s57, 0\n"
      "ds_read_b128 v[172:175], v114 offset:32768\n"
      "ds_read_b128 v[156:159], v115 offset:30720\n"
      "s_sub_u32 s56, s56, 73728\n"
      "ds_read_b128 v[160:163], v114 offset:30720\n"
      "s_subb_u32 s57, s57, 0\n"
      "ds_read_b64_tr_b16 a[154:155], v231 offset:4608\n"
      "ds_read_b64_tr_b16 a[164:165], v231 offset:384\n"
      "v_lshl_add_u32 v81, v81, 2, s2\n"
      "s_mul_i32 s28, s8, 1373568\n"
      "v_lshl_add_u32 v82, v82, 2, s2\n"
      "s_mul_i32 s29, s9, 2688\n"
      "s_add_u32 s28, s28, s29\n"
      "ds_read_b64_tr_b16 a[184:185], v149 offset:36864\n"
      "s_cmp_eq_u32 s8, 72\n"
      "ds_read_b64_tr_b16 a[166:167], v231 offset:4992\n"
      "ds_read_b64_tr_b16 a[212:213], v231 offset:37632\n"
      "s_cselect_b32 s8, 0, 100270464\n"
      "v_mov_b32 v236, 0\n"
      "s_cmp_eq_u32 s9, 72\n"
      "ds_read_b64_tr_b16 a[196:197], v149 offset:37248\n"
      "s_cselect_b32 s9, 0, 196224\n"
      "ds_read_b64_tr_b16 a[150:151], v149 offset:4608\n"
      "ds_read_b64_tr_b16 a[208:209], v149 offset:37632\n"
      "ds_read_b64_tr_b16 a[194:195], v148 offset:41856\n"
      "ds_read_b64_tr_b16 a[156:157], v148 offset:384\n"
      "s_add_u32 s10, s8, s9\n"
      "ds_read_b64_tr_b16 a[198:199], v149 offset:41856\n"
      "v_lshl_add_u32 v83, v83, 2, s2\n"
      "s_add_u32 s28, s28, s10\n"
      "ds_read_b64_tr_b16 a[170:171], v148 offset:5376\n"
      "ds_read_b64_tr_b16 a[192:193], v148 offset:37248\n"
      "ds_read_b64_tr_b16 a[182:183], v148 offset:41472\n"
      "ds_read_b64_tr_b16 a[200:201], v231 offset:37248\n"
      "ds_read_b64_tr_b16 a[186:187], v149 offset:41472\n"
      "ds_read_b64_tr_b16 a[158:159], v148 offset:4992\n"
      "ds_read_b64_tr_b16 a[180:181], v148 offset:36864\n"
      "s_add_u32 s52, s16, s28\n"
      "v_lshl_add_u32 v88, v88, 2, s2\n"
      "v_lshl_add_u32 v91, v91, 2, s2\n"
      "s_addc_u32 s53, s17, 0\n"
      "s_cmp_lg_u32 s10, s11\n"
      "v_add_u32 v228, 99876864, v228\n"
      "s_cbranch_scc1 L_edge_1_%=\n"
      "L_eb_2_%=:\n"
      "s_add_u32 m0, s76, 0\n"
      "ds_read_b64_tr_b16 a[204:205], v148 offset:37632\n"
      "ds_read_b64_tr_b16 a[190:191], v231 offset:41472\n"
      "ds_read_b64_tr_b16 a[174:175], v149 offset:5376\n"
      "global_load_lds_dwordx4 v116, s[52:53]\n"
      "ds_read_b64_tr_b16 a[152:153], v231\n"
      "ds_read_b64_tr_b16 a[202:203], v231 offset:41856\n"
      "ds_read_b32 v84, v84\n"
      "ds_read_b64_tr_b16 a[178:179], v231 offset:5376\n"
      "v_lshl_add_u32 v90, v90, 2, s2\n"
      "s_add_u32 m0, s76, 6016\n"
      "ds_read_b32 v89, v89\n"
      "ds_read_b32 v87, v87\n"
      "ds_read_b64_tr_b16 a[160:161], v149 offset:384\n"
      "ds_read_b64_tr_b16 a[176:177], v231 offset:768\n"
      "ds_read_b64_tr_b16 a[168:169], v148 offset:768\n"
      "v_mov_b32 v240, 0\n"
      "v_mov_b32_e64 v241, 0\n"
      "s_waitcnt lgkmcnt(12)\n"
      "ds_read_b64_tr_b16 a[162:163], v149 offset:4992\n"
      "v_mfma_f32_16x16x32_bf16 v[200:203], a[144:147], v[160:163], v[200:203]\n"
      "ds_read_b128 v[148:151], v115 offset:32768\n"
      "ds_read_b64_tr_b16 a[214:215], v231 offset:42240\n"
      "global_load_lds_dwordx4 v116, s[52:53] offset:128\n"
      "v_lshl_add_u32 v75, v75, 2, s2\n"
      "v_mfma_f32_16x16x32_bf16 v[200:203], a[180:183], v[156:159], v[200:203]\n"
      "ds_read_b32 v80, v80\n"
      "ds_read_b32 v79, v79\n"
      "ds_read_b32 v82, v82\n"
      "ds_read_b32 v83, v83\n"
      "v_lshl_add_u32 v99, v99, 2, s2\n"
      "v_mfma_f32_16x16x32_bf16 v[176:179], a[148:151], v[160:163], v[176:179]\n"
      "s_waitcnt lgkmcnt(10)\n"
      "ds_read_b32 v85, v85\n"
      "v_mfma_f32_16x16x32_bf16 v[176:179], a[184:187], v[156:159], v[176:179]\n"
      "s_waitcnt vmcnt(2)\n"
      "ds_write_b32 v205, v228\n"
      "ds_read_b32 v81, v81\n"
      "v_mfma_f32_16x16x32_bf16 v[212:215], a[152:155], v[160:163], v[212:215]\n"
      "s_add_u32 m0, s76, 12032\n"
      "v_lshl_add_u32 v92, v92, 2, s2\n"
      "v_lshl_add_u32 v93, v93, 2, s2\n"
      "v_mfma_f32_16x16x32_bf16 v[212:215], a[188:191], v[156:159], v[212:215]\n"
      "global_load_lds_dwordx4 v116, s[52:53] offset:256\n"
      "s_waitcnt lgkmcnt(7)\n"
      "v_mfma_f32_16x16x32_bf16 v[184:187], a[156:159], v[160:163], v[184:187]\n"
      "ds_read_b32 v86, v86\n"
      "v_lshl_add_u32 v94, v94, 2, s2\n"
      "v_mfma_f32_16x16x32_bf16 v[184:187], a[192:195], v[156:159], v[184:187]\n"
      "s_add_u32 m0, s77, 0\n"
      "ds_read_b32 v88, v88\n"
      "global_load_lds_dwordx4 v117, s[52:53]\n"
      "v_lshl_add_u32 v112, v112, 2, s2\n"
      "v_lshl_add_u32 v108, v108, 2, s2\n"
      "v_lshl_add_u32 v101, v101, 2, s2\n"
      "v_lshl_add_u32 v95, v95, 2, s2\n"
      "s_add_u32 m0, s78, 0\n"
      "v_lshl_add_u32 v100, v100, 2, s2\n"
      "s_mov_b32 exec_hi, 0\n"
      "global_load_lds_dwordx4 v118, s[52:53]\n"
      "s_mov_b32 exec_hi, -1\n"
      "v_mfma_f32_16x16x32_bf16 v[244:247], a[160:163], v[160:163], v[244:247]\n"
      "ds_read_b32 v90, v90\n"
      "s_add_u32 m0, s76, 110592\n"
      "v_mfma_f32_16x16x32_bf16 v[244:247], a[196:199], v[156:159], v[244:247]\n"
      "ds_read_b32 v78, v78\n"
      "v_lshl_add_u32 v98, v98, 2, s2\n"
      "v_mfma_f32_16x16x32_bf16 v[188:191], a[172:175], v[160:163], v[188:191]\n"
      "global_load_lds_dwordx4 v119, s[56:57]\n"
      "s_add_u32 m0, s79, 0\n"
      "v_mfma_f32_16x16x32_bf16 v[232:235], a[164:167], v[160:163], v[232:235]\n"
      "ds_read_b32 v94, v94\n"
      "global_load_lds_dword v122, s[58:59]\n"
      "global_load_dwordx4 v[126:129], v198, s[48:49] offset:-1024\n"
      "v_mfma_f32_16x16x32_bf16 v[216:219], a[168:171], v[160:163], v[216:219]\n"
      "ds_read_b32 v95, v95\n"
      "v_lshl_add_u32 v73, v73, 2, s2\n"
      "v_mfma_f32_16x16x32_bf16 v[216:219], a[204:207], v[156:159], v[216:219]\n"
      "global_load_dwordx4 v[130:133], v198, s[48:49]\n"
      "ds_read_b32 v93, v93\n"
      "v_lshl_add_u32 v74, v74, 2, s2\n"
      "v_lshl_add_u32 v71, v71, 2, s2\n"
      "v_mfma_f32_16x16x32_bf16 v[232:235], a[200:203], v[156:159], v[232:235]\n"
      "global_load_dwordx4 v[228:231], v198, s[48:49] offset:1024\n"
      "global_load_dwordx4 a[252:255], v197, s[6:7]\n"
      "v_lshl_add_u32 v109, v109, 2, s2\n"
      "v_lshl_add_u32 v103, v103, 2, s2\n"
      "v_lshl_add_u32 v70, v70, 2, s2\n"
      "v_lshl_add_u32 v106, v106, 2, s2\n"
      "v_lshl_add_u32 v72, v72, 2, s2\n"
      "v_mfma_f32_16x16x32_bf16 v[180:183], a[176:179], v[160:163], v[180:183]\n"
      "ds_read_b32 v92, v92\n"
      "v_lshl_add_u32 v105, v105, 2, s2\n"
      "v_mfma_f32_16x16x32_bf16 v[180:183], a[212:215], v[156:159], v[180:183]\n"
      "ds_read_b32 v91, v91\n"
      "v_lshl_add_u32 v102, v102, 2, s2\n"
      "v_lshl_add_u32 v110, v110, 2, s2\n"
      "v_lshl_add_u32 v111, v111, 2, s2\n"
      "v_lshl_add_u32 v97, v97, 2, s2\n"
      "v_lshl_add_u32 v107, v107, 2, s2\n"
      "v_fma_f32 v205, v184, v184, s38\n"
      "v_lshl_add_u32 v96, v96, 2, s2\n"
      "v_mfma_f32_16x16x32_bf16 v[188:191], a[208:211], v[156:159], v[188:191]\n"
      "global_load_dwordx4 v[0:3], v197, s[6:7] offset:128\n"
      "v_mfma_f32_16x16x32_bf16 v[192:195], a[144:147], v[172:175], v[192:195]\n"
      "ds_read_b32 v96, v96\n"
      "v_fma_f32 v242, v200, v200, s38\n"
      "v_mfma_f32_16x16x32_bf16 v[192:195], a[180:183], v[148:151], v[192:195]\n"
      "global_load_dwordx4 v[4:7], v197, s[6:7] offset:256\n"
      "v_fmac_f32_e64 v205, v185, v185\n"
      "v_mfma_f32_16x16x32_bf16 v[248:251], a[148:151], v[172:175], v[248:251]\n"
      "ds_read_b32 v97, v97\n"
      "v_add_u32 v197, 15360, v171\n"
      "v_mfma_f32_16x16x32_bf16 v[248:251], a[184:187], v[148:151], v[248:251]\n"
      "ds_read_b32 v99, v99\n"
      "v_fmac_f32 v242, v201, v201\n"
      "v_fmac_f32 v242, v202, v202\n"
      "v_lshl_add_u32 v104, v104, 2, s2\n"
      "v_fmac_f32 v242, v203, v203\n"
      "v_fmac_f32 v242, v176, v176\n"
      "v_fmac_f32 v242, v177, v177\n"
      "v_fmac_f32 v205, v186, v186\n"
      "v_fmac_f32_e64 v242, v178, v178\n"
      "v_mfma_f32_16x16x32_bf16 v[144:147], a[152:155], v[172:175], v[144:147]\n"
      "ds_read_b32 v105, v105\n"
      "ds_read_b32 v98, v98\n"
      "ds_read_b32 v111, v111\n"
      "global_load_dwordx4 v[8:11], v197, s[6:7] offset:-3072\n"
      "v_fmac_f32 v205, v187, v187\n"
      "v_fmac_f32 v205, v244, v244\n"
      "v_fmac_f32 v205, v245, v245\n"
      "v_fmac_f32 v242, v179, v179\n"
      "v_fmac_f32 v205, v246, v246\n"
      "v_lshl_add_u32 v113, v113, 2, s2\n"
      "v_fmac_f32 v205, v247, v247\n"
      "v_fmac_f32 v205, v232, v232\n"
      "v_fmac_f32 v242, v212, v212\n"
      "v_mfma_f32_16x16x32_bf16 v[140:143], a[156:159], v[172:175], v[140:143]\n"
      "ds_read_b32 v71, v71\n"
      "v_fmac_f32 v205, v233, v233\n"
      "v_fmac_f32 v205, v234, v234\n"
      "v_fmac_f32 v242, v213, v213\n"
      "v_fmac_f32 v242, v214, v214\n"
      "v_fmac_f32 v242, v215, v215\n"
      "v_fmac_f32 v205, v235, v235\n"
      "v_mfma_f32_16x16x32_bf16 v[140:143], a[192:195], v[148:151], v[140:143]\n"
      "ds_read_b32 v104, v104\n"
      "v_permlane16_swap_b32 v242, v205\n"
      "v_add_f32 v242, v242, v205\n"
      "v_mov_b32_e64 v205, v242\n"
      "v_mfma_f32_16x16x32_bf16 v[144:147], a[188:191], v[148:151], v[144:147]\n"
      "ds_read_b32 v101, v101\n"
      "ds_read_b32 v108, v108\n"
      "ds_read_b32 v102, v102\n"
      "ds_read_b32 v106, v106\n"
      "ds_read_b32 v107, v107\n"
      "global_load_dwordx4 v[12:15], v197, s[6:7] offset:-2944\n"
      "v_permlane32_swap_b32 v242, v205\n"
      "v_add_f32 v242, v242, v205\n"
      "v_rsq_f32 v242, v242\n"
      "v_lshl_add_u32 v77, v77, 2, s2\n"
      "v_mov_b32 v205, v242\n"
      "v_mfma_f32_16x16x32_bf16 v[164:167], a[160:163], v[172:175], v[164:167]\n"
      "ds_read_b32 v109, v109\n"
      "ds_read_b32 v103, v103\n"
      "ds_read_b32 v74, v74\n"
      "v_permlane16_swap_b32 v242, v205\n"
      "v_mul_f32 v244, v205, v244\n"
      "v_mul_f32 v247, v205, v247\n"
      "v_mul_f32 v234, v205, v234\n"
      "v_mul_f32 v187, v205, v187\n"
      "v_mul_f32 v185, v205, v185\n"
      "v_mul_f32 v245, v205, v245\n"
      "v_mul_f32 v242, s36, v242\n"
      "v_mul_f32 v215, v242, v215\n"
      "v_mul_f32 v184, v205, v184\n"
      "v_mfma_f32_16x16x32_bf16 v[164:167], a[196:199], v[148:151], v[164:167]\n"
      "ds_read_b32 v110, v110\n"
      "global_load_dwordx4 v[16:19], v197, s[6:7] offset:-2816\n"
      "v_mul_f32 v233, v205, v233\n"
      "v_mul_f32 v177, v242, v177\n"
      "v_cvt_pk_bf16_f32 v184, v184, v185\n"
      "v_mul_f32 v186, v205, v186\n"
      "v_cvt_pk_bf16_f32 v185, v186, v187\n"
      "v_mul_f32 v200, v242, v200\n"
      "v_mul_f32 v212, v242, v212\n"
      "v_mul_f32 v246, v205, v246\n"
      "v_mul_f32 v202, v242, v202\n"
      "v_mul_f32 v232, v205, v232\n"
      "v_mul_f32 v176, v242, v176\n"
      "v_mul_f32 v201, v242, v201\n"
      "v_mfma_f32_16x16x32_bf16 v[208:211], a[164:167], v[172:175], v[208:211]\n"
      "ds_read_b32 v70, v70\n"
      "v_cvt_pk_bf16_f32 v187, v246, v247\n"
      "v_mfma_f32_16x16x32_bf16 v[208:211], a[200:203], v[148:151], v[208:211]\n"
      "ds_read_b32 v113, v113\n"
      "ds_read_b32 v72, v72\n"
      "ds_read_b32 v112, v112\n"
      "ds_read_b32 v77, v77\n"
      "ds_read_b32 v73, v73\n"
      "ds_read_b32 v75, v75\n"
      "global_load_dwordx4 v[20:23], v197, s[6:7]\n"
      "ds_read_b32 v100, v100\n"
      "global_load_dwordx4 v[24:27], v197, s[6:7] offset:128\n"
      "global_load_dwordx4 v[28:31], v197, s[6:7] offset:256\n"
      "s_waitcnt vmcnt(9)\n"
      "v_mov_b32 v135, v229\n"
      "v_mul_f32 v235, v205, v235\n"
      "v_mov_b32 v134, v228\n"
      "v_mfma_f32_16x16x32_bf16 v[224:227], a[168:171], v[172:175], v[224:227]\n"
      "ds_read_b32 v205, v122\n"
      "v_mul_f32 v179, v242, v179\n"
      "s_waitcnt lgkmcnt(6)\n"
      "ds_read_b128 v[168:171], v119 offset:36864\n"
      "v_mul_f32 v178, v242, v178\n"
      "s_waitcnt lgkmcnt(5)\n"
      "v_cvt_pk_bf16_f32 v186, v244, v245\n"
      "v_cvt_pk_bf16_f32 v200, v200, v201\n"
      "v_mul_f32 v213, v242, v213\n"
      "v_mul_f32 v203, v242, v203\n"
      "v_add_u32 v228, 36864, v231\n"
      "v_mfma_f32_16x16x32_bf16 v[224:227], a[204:207], v[148:151], v[224:227]\n"
      "s_mov_b32 exec_lo, 0xffff0000\n"
      "s_mov_b32 exec_hi, 0xffffffff\n"
      "v_mov_b32 v70, 0xf149f2ca\n"
      "v_mov_b32 v72, 0xf149f2ca\n"
      "v_mov_b32 v74, 0xf149f2ca\n"
      "s_mov_b64 exec, -1\n"
      "v_mul_f32 v214, v242, v214\n"
      "s_mov_b32 exec_lo, 0x0\n"
      "s_mov_b32 exec_hi, 0xfffe0000\n"
      "v_mov_b32 v77, 0xf149f2ca\n"
      "s_mov_b64 exec, -1\n"
      "v_cvt_pk_bf16_f32 v33, v214, v215\n"
      "v_cvt_pk_bf16_f32 v35, v234, v235\n"
      "v_mfma_f32_16x16x32_bf16 v[136:139], a[172:175], v[172:175], v[136:139]\n"
      "s_mov_b32 s4, 0\n"
      "v_cvt_pk_bf16_f32 v32, v212, v213\n"
      "v_mov_b32_e64 v242, -1\n"
      "v_mfma_f32_16x16x32_bf16 v[136:139], a[208:211], v[148:151], v[136:139]\n"
      "ds_write_b32 v129, v242 offset:1024\n"
      "v_cvt_pk_bf16_f32 v34, v232, v233\n"
      "v_cvt_pk_bf16_f32 v201, v202, v203\n"
      "v_add_u32 v229, 36864, v230\n"
      "v_cvt_pk_bf16_f32 v203, v178, v179\n"
      "v_cvt_pk_bf16_f32 v202, v176, v177\n"
      "v_mfma_f32_16x16x32_bf16 v[252:255], a[176:179], v[172:175], v[252:255]\n"
      "s_waitcnt lgkmcnt(1)\n"
      "s_mov_b32 s5, 0\n"
      "ds_write_b32 v129, v242 offset:3072\n"
      "v_or3_b32 v168, v168, v169, v170\n"
      "v_and_b32_e64 v168, s40, v168\n"
      "v_bitop3_b32 v205, v205, v171, s40 bitop3:0xa8\n"
      "v_or3_b32 v168, v168, v205, s4\n"
      "v_mfma_f32_16x16x32_bf16 v[252:255], a[212:215], v[148:151], v[252:255]\n"
      "ds_write_b32 v129, v242\n"
      "ds_write_b32 v129, v168 offset:2048\n"
      "s_waitcnt lgkmcnt(0)\n"
      "s_barrier\n"
      "v_fma_f32 v242, v192, v192, s38\n"
      "v_fma_f32 v205, v140, v140, s38\n"
      "v_fmac_f32 v205, v141, v141\n"
      "v_fmac_f32 v205, v142, v142\n"
      "v_fmac_f32 v242, v193, v193\n"
      "v_fmac_f32 v205, v143, v143\n"
      "v_fmac_f32 v205, v164, v164\n"
      "v_fmac_f32 v242, v194, v194\n"
      "v_fmac_f32 v242, v195, v195\n"
      "v_cvt_pk_bf16_f32 v216, v216, v217\n"
      "v_fmac_f32 v242, v248, v248\n"
      "v_fmac_f32 v242, v249, v249\n"
      "v_fmac_f32 v242, v250, v250\n"
      "v_fmac_f32 v205, v165, v165\n"
      "v_fmac_f32 v242, v251, v251\n"
      "v_cvt_pk_bf16_f32 v180, v180, v181\n"
      "v_fmac_f32 v242, v144, v144\n"
      "v_fmac_f32 v205, v166, v166\n"
      "v_fmac_f32 v242, v145, v145\n"
      "v_fmac_f32 v205, v167, v167\n"
      "v_fmac_f32 v242, v146, v146\n"
      "v_fmac_f32 v242, v147, v147\n"
      "v_fmac_f32 v205, v208, v208\n"
      "v_fmac_f32 v205, v209, v209\n"
      "v_fmac_f32 v205, v210, v210\n"
      "v_fmac_f32 v205, v211, v211\n"
      "v_cvt_pk_bf16_f32 v217, v218, v219\n"
      "v_cvt_pk_bf16_f32 v188, v188, v189\n"
      "v_permlane16_swap_b32 v242, v205\n"
      "v_add_f32 v242, v242, v205\n"
      "v_mov_b32 v205, v242\n"
      "v_cvt_pk_bf16_f32 v189, v190, v191\n"
      "v_cvt_pk_bf16_f32 v181, v182, v183\n"
      "v_permlane32_swap_b32 v242, v205\n"
      "v_add_f32 v242, v242, v205\n"
      "v_rsq_f32 v242, v242\n"
      "s_mov_b32 s27, 0\n"
      "ds_read_b128 v[196:199], v115\n"
      "v_mov_b32 v205, v242\n"
      "s_add_u32 s28, s27, 1\n"
      "s_min_u32 m0, s28, s3\n"
      "ds_read_b128 v[148:151], v114\n"
      "v_permlane16_swap_b32 v242, v205\n"
      "v_mul_f32 v165, v205, v165\n"
      "v_mul_f32 v143, v205, v143\n"
      "v_mul_f32 v140, v205, v140\n"
      "v_mul_f32 v242, s36, v242\n"
      "v_mul_f32 v164, v205, v164\n"
      "v_mul_f32 v142, v205, v142\n"
      "v_mul_f32 v141, v205, v141\n"
      "v_mul_f32 v192, v242, v192\n"
      "v_cvt_pk_bf16_f32 v140, v140, v141\n"
      "v_mul_f32 v195, v242, v195\n"
      "v_cvt_pk_bf16_f32 v141, v142, v143\n"
      "s_movrels_b32 s9, s80\n"
      "ds_write_b128 v206, v[200:203]\n"
      "ds_read_b128 v[168:171], v128 offset:2048\n"
      "ds_write_b128 v206, v[32:35] offset:16\n"
      "v_mul_f32 v193, v242, v193\n"
      "v_mul_f32 v208, v205, v208\n"
      "v_mul_f32 v194, v242, v194\n"
      "s_lshr_b32 s8, s9, 8\n"
      "v_mul_f32 v209, v205, v209\n"
      "v_mul_f32 v167, v205, v167\n"
      "s_and_b32 s9, s9, 0xff\n"
      "v_mul_f32 v210, v205, v210\n"
      "v_mul_f32_e64 v166, v205, v166\n"
      "v_cvt_pk_bf16_f32 v143, v166, v167\n"
      "s_mul_i32 s10, s8, 73\n"
      "ds_write_b128 v206, v[184:187] offset:32\n"
      "v_cvt_pk_bf16_f32 v142, v164, v165\n"
      "v_mul_f32 v248, v242, v248\n"
      "s_waitcnt lgkmcnt(4)\n"
      "v_cvt_pk_bf16_f32 v40, v208, v209\n"
      "v_cvt_pk_bf16_f32 v192, v192, v193\n"
      "v_mul_f32 v211, v205, v211\n"
      "s_add_u32 s10, s10, s9\n"
      "v_cvt_pk_bf16_f32 v41, v210, v211\n"
      "v_mfma_f32_16x16x32_bf16 v[184:187], a[0:3], v[148:151], 0\n"
      "ds_read_b128 v[232:235], v114 offset:6144\n"
      "v_mul_f32 v249, v242, v249\n"
      "v_mfma_f32_16x16x32_bf16 v[184:187], a[36:39], v[196:199], v[184:187]\n"
      "ds_write_b64 v207, v[216:217]\n"
      "s_mul_i32 s28, s10, 4802\n"
      "v_mfma_f32_16x16x32_bf16 v[200:203], a[4:7], v[148:151], 0\n"
      "s_add_u32 s56, s18, s28\n"
      "ds_read_b128 v[212:215], v115 offset:6144\n"
      "v_mfma_f32_16x16x32_bf16 v[200:203], a[40:43], v[196:199], v[200:203]\n"
      "ds_write_b64 v207, v[188:189] offset:32\n"
      "v_mfma_f32_16x16x32_bf16 v[188:191], a[8:11], v[148:151], 0\n"
      "s_addc_u32 s57, s19, 0\n"
      "s_and_b32 s56, s56, -16\n"
      "s_sub_u32 s58, s56, 110592\n"
      "v_mfma_f32_16x16x32_bf16 v[188:191], a[44:47], v[196:199], v[188:191]\n"
      "ds_write_b64 v207, v[180:181] offset:64\n"
      "v_cvt_pk_bf16_f32 v193, v194, v195\n"
      "v_mfma_f32_16x16x32_bf16 v[180:183], a[12:15], v[148:151], 0\n"
      "v_mul_f32_e64 v250, v242, v250\n"
      "v_cvt_pk_bf16_f32 v194, v248, v249\n"
      "v_mfma_f32_16x16x32_bf16 v[180:183], a[48:51], v[196:199], v[180:183]\n"
      "s_subb_u32 s59, s57, 0\n"
      "v_mul_f32 v251, v242, v251\n"
      "v_cvt_pk_bf16_f32 v195, v250, v251\n"
      "v_mfma_f32_16x16x32_bf16 v[248:251], a[16:19], v[148:151], 0\n"
      "s_sub_u32 s56, s56, 73728\n"
      "v_mul_f32_e64 v144, v242, v144\n"
      "v_mul_f32_e64 v145, v242, v145\n"
      "v_mfma_f32_16x16x32_bf16 v[248:251], a[52:55], v[196:199], v[248:251]\n"
      "ds_read_b128 v[244:247], v114 offset:12288\n"
      "s_waitcnt lgkmcnt(3)\n"
      "v_mfma_f32_16x16x32_bf16 v[176:179], a[20:23], v[148:151], 0\n"
      "s_subb_u32 s57, s57, 0\n"
      "v_mul_f32_e64 v146, v242, v146\n"
      "v_cvt_pk_bf16_f32 v38, v144, v145\n"
      "v_mfma_f32_16x16x32_bf16 v[176:179], a[56:59], v[196:199], v[176:179]\n"
      "ds_read_b128 v[164:167], v115 offset:12288\n"
      "v_mul_f32 v147, v242, v147\n"
      "v_mfma_f32_16x16x32_bf16 v[216:219], v[148:151], a[24:27], 0\n"
      "s_mul_i32 s28, s8, 1373568\n"
      "v_cvt_pk_bf16_f32 v39, v146, v147\n"
      "s_mul_i32 s29, s9, 2688\n"
      "v_mfma_f32_16x16x32_bf16 v[216:219], v[196:199], a[60:63], v[216:219]\n"
      "s_add_u32 s28, s28, s29\n"
      "v_cvt_pk_bf16_f32 v224, v224, v225\n"
      "v_cvt_pk_bf16_f32 v136, v136, v137\n"
      "v_mfma_f32_16x16x32_bf16 v[172:175], v[148:151], a[28:31], 0\n"
      "v_cvt_pk_bf16_f32 v225, v226, v227\n"
      "s_cmp_eq_u32 s8, 72\n"
      "v_mfma_f32_16x16x32_bf16 v[172:175], v[196:199], a[64:67], v[172:175]\n"
      "s_cselect_b32 s8, 0, 100270464\n"
      "v_cvt_pk_bf16_f32 v252, v252, v253\n"
      "v_cvt_pk_bf16_f32 v253, v254, v255\n"
      "v_mfma_f32_16x16x32_bf16 v[144:147], v[148:151], a[32:35], 0\n"
      "s_cmp_eq_u32 s9, 72\n"
      "s_cselect_b32 s9, 0, 196224\n"
      "s_add_u32 s10, s8, s9\n"
      "v_mfma_f32_16x16x32_bf16 v[144:147], v[196:199], a[68:71], v[144:147]\n"
      "s_add_u32 s28, s28, s10\n"
      "v_bitop3_b32 v170, v170, v171, s4 bitop3:0x7e\n"
      "s_add_u32 s52, s16, s28\n"
      "v_mfma_f32_16x16x32_bf16 v[184:187], a[72:75], v[232:235], v[184:187]\n"
      "s_addc_u32 s53, s17, 0\n"
      "s_cmp_lg_u32 s10, s11\n"
      "v_bitop3_b32 v168, v168, v169, s4 bitop3:0x7e\n"
      "v_mfma_f32_16x16x32_bf16 v[184:187], a[108:111], v[212:215], v[184:187]\n"
      "s_cbranch_scc1 L_edge_6_%=\n"
      "L_eb_7_%=:\n"
      "v_mfma_f32_16x16x32_bf16 v[200:203], a[76:79], v[232:235], v[200:203]\n"
      "s_add_u32 m0, s76, 18432\n"
      "v_or_b32 v168, v168, v170\n"
      "v_cvt_pk_bf16_f32 v137, v138, v139\n"
      "v_mfma_f32_16x16x32_bf16 v[200:203], a[112:115], v[212:215], v[200:203]\n"
      "v_cmp_ne_u32 vcc, 0, v168\n"
      "v_mfma_f32_16x16x32_bf16 v[188:191], a[80:83], v[232:235], v[188:191]\n"
      "global_load_lds_dwordx4 v116, s[52:53]\n"
      "v_mfma_f32_16x16x32_bf16 v[188:191], a[116:119], v[212:215], v[188:191]\n"
      "s_mov_b32 exec_lo, 0x003f003f\n"
      "s_mov_b32 exec_hi, 0x003f003f\n"
      "ds_write_b128 v206, v[192:195] offset:18688\n"
      "ds_write_b128 v206, v[38:41] offset:18704\n"
      "ds_write_b128 v206, v[140:143] offset:18720\n"
      "ds_write_b64 v207, v[224:225] offset:18688\n"
      "ds_write_b64 v207, v[136:137] offset:18720\n"
      "ds_write_b64 v207, v[252:253] offset:18752\n"
      "s_mov_b64 exec, -1\n"
      "v_mfma_f32_16x16x32_bf16 v[180:183], a[84:87], v[232:235], v[180:183]\n"
      "s_mov_b64 s[48:49], vcc\n"
      "s_add_u32 m0, s76, 24448\n"
      "v_mfma_f32_16x16x32_bf16 v[180:183], a[120:123], v[212:215], v[180:183]\n"
      "global_load_lds_dwordx4 v116, s[52:53] offset:128\n"
      "v_mfma_f32_16x16x32_bf16 v[248:251], a[88:91], v[232:235], v[248:251]\n"
      "s_add_u32 m0, s76, 30464\n"
      "v_mfma_f32_16x16x32_bf16 v[248:251], a[124:127], v[212:215], v[248:251]\n"
      "v_mfma_f32_16x16x32_bf16 v[176:179], a[92:95], v[232:235], v[176:179]\n"
      "ds_read_b128 v[192:195], v114 offset:2048\n"
      "v_mfma_f32_16x16x32_bf16 v[176:179], a[128:131], v[212:215], v[176:179]\n"
      "s_cbranch_vccnz L_flg_3_%=\n"
      "L_fb_4_%=:\n"
      "v_mfma_f32_16x16x32_bf16 v[168:171], v[152:155], a[216:219], 0\n"
      "v_mfma_f32_16x16x32_bf16 v[168:171], v[152:155], a[220:223], v[168:171]\n"
      "s_nop 0\n"
      "v_mfma_f32_16x16x32_bf16 v[168:171], v[152:155], a[224:227], v[168:171]\n"
      "ds_read_b128 v[196:199], v115 offset:2048\n"
      "v_mfma_f32_16x16x32_bf16 v[216:219], v[232:235], a[96:99], v[216:219]\n"
      "v_mfma_f32_16x16x32_bf16 v[216:219], v[212:215], a[132:135], v[216:219]\n"
      "global_load_lds_dwordx4 v116, s[52:53] offset:256\n"
      "v_mfma_f32_16x16x32_bf16 v[172:175], v[232:235], a[100:103], v[172:175]\n"
      "s_nop 0\n"
      "v_mfma_f32_16x16x32_bf16 v[172:175], v[212:215], a[136:139], v[172:175]\n"
      "s_waitcnt lgkmcnt(2)\n"
      "s_nop 0\n"
      "v_add_f32_dpp v168, v168, v168 quad_perm:[1,0,3,2] row_mask:0xf bank_mask:0xf\n"
      "v_mfma_f32_16x16x32_bf16 v[144:147], v[232:235], a[104:107], v[144:147]\n"
      "s_nop 0\n"
      "v_add_f32_dpp v168, v168, v168 quad_perm:[2,3,0,1] row_mask:0xf bank_mask:0xf\n"
      "v_mfma_f32_16x16x32_bf16 v[144:147], v[212:215], a[140:143], v[144:147]\n"
      "s_nop 0\n"
      "v_add_f32_dpp v168, v168, v168 row_half_mirror row_mask:0xf bank_mask:0xf\n"
      "v_mfma_f32_16x16x32_bf16 v[184:187], a[144:147], v[244:247], v[184:187]\n"
      "s_nop 0\n"
      "v_mul_f32 v171, 0xbbaaaaab, v168\n"
      "v_mfma_f32_16x16x32_bf16 v[184:187], a[180:183], v[164:167], v[184:187]\n"
      "v_mul_f32 v170, 0xbbaaaaab, v168\n"
      "v_mfma_f32_16x16x32_bf16 v[200:203], a[148:151], v[244:247], v[200:203]\n"
      "s_nop 0\n"
      "v_mul_f32 v169, 0xbbaaaaab, v168\n"
      "v_mul_f32 v168, 0xbbaaaaab, v168\n"
      "v_mfma_f32_16x16x32_bf16 v[200:203], a[184:187], v[164:167], v[200:203]\n"
      "s_nop 0\n"
      "v_mfma_f32_16x16x32_bf16 v[148:151], v[238:241], a[216:219], v[168:171]\n"
      "v_mfma_f32_16x16x32_bf16 v[232:235], v[236:239], a[216:219], v[168:171]\n"
      "v_fma_f32 v242, v184, v184, s38\n"
      "v_mfma_f32_16x16x32_bf16 v[206:209], v[238:241], a[220:223], v[168:171]\n"
      "s_add_u32 m0, s77, 18432\n"
      "s_waitcnt lgkmcnt(0)\n"
      "v_mfma_f32_16x16x32_bf16 v[212:215], v[236:239], a[220:223], v[168:171]\n"
      "s_nop 1\n"
      "v_cvt_pk_bf16_f32 v148, v148, v149\n"
      "v_mfma_f32_16x16x32_bf16 v[136:139], v[238:241], a[224:227], v[168:171]\n"
      "v_cvt_pk_bf16_f32 v149, v150, v151\n"
      "v_cvt_pk_bf16_f32 v150, v232, v233\n"
      "v_mfma_f32_16x16x32_bf16 v[224:227], v[236:239], a[224:227], v[168:171]\n"
      "v_cvt_pk_bf16_f32 v206, v206, v207\n"
      "v_cvt_pk_bf16_f32 v151, v234, v235\n"
      "v_mfma_f32_16x16x32_bf16 v[188:191], a[152:155], v[244:247], v[188:191]\n"
      "v_cvt_pk_bf16_f32 v207, v208, v209\n"
      "v_cvt_pk_bf16_f32 v208, v212, v213\n"
      "v_mfma_f32_16x16x32_bf16 v[188:191], a[188:191], v[164:167], v[188:191]\n"
      "ds_write_b128 v204, v[148:151]\n"
      "v_mfma_f32_16x16x32_bf16 v[168:171], v[152:155], a[228:231], 0\n"
      "global_load_lds_dwordx4 v117, s[52:53]\n"
      "v_mfma_f32_16x16x32_bf16 v[168:171], v[152:155], a[232:235], v[168:171]\n"
      "v_cvt_pk_bf16_f32 v209, v214, v215\n"
      "v_cvt_pk_bf16_f32 v136, v136, v137\n"
      "v_mfma_f32_16x16x32_bf16 v[180:183], a[156:159], v[244:247], v[180:183]\n"
      "v_cvt_pk_bf16_f32 v137, v138, v139\n"
      "v_cvt_pk_bf16_f32 v138, v224, v225\n"
      "v_mfma_f32_16x16x32_bf16 v[180:183], a[192:195], v[164:167], v[180:183]\n"
      "ds_write_b128 v204, v[206:209] offset:128\n"
      "v_mfma_f32_16x16x32_bf16 v[168:171], v[152:155], a[236:239], v[168:171]\n"
      "v_cvt_pk_bf16_f32 v139, v226, v227\n"
      "v_fmac_f32_e64 v242, v185, v185\n"
      "v_mfma_f32_16x16x32_bf16 v[248:251], a[160:163], v[244:247], v[248:251]\n"
      "v_fmac_f32 v242, v186, v186\n"
      "v_fmac_f32 v242, v187, v187\n"
      "v_mfma_f32_16x16x32_bf16 v[248:251], a[196:199], v[164:167], v[248:251]\n"
      "ds_write_b128 v204, v[136:139] offset:256\n"
      "v_mfma_f32_16x16x32_bf16 v[176:179], a[164:167], v[244:247], v[176:179]\n"
      "v_add_f32_dpp v168, v168, v168 quad_perm:[1,0,3,2] row_mask:0xf bank_mask:0xf\n"
      "v_fma_f32 v243, v180, v180, s38\n"
      "v_mfma_f32_16x16x32_bf16 v[176:179], a[200:203], v[164:167], v[176:179]\n"
      "v_add_f32_dpp v168, v168, v168 quad_perm:[2,3,0,1] row_mask:0xf bank_mask:0xf\n"
      "v_fmac_f32_e64 v243, v181, v181\n"
      "v_mfma_f32_16x16x32_bf16 v[216:219], v[244:247], a[168:171], v[216:219]\n"
      "v_add_f32_dpp v168, v168, v168 row_half_mirror row_mask:0xf bank_mask:0xf\n"
      "v_mul_f32 v171, 0xbbaaaaab, v168\n"
      "v_mfma_f32_16x16x32_bf16 v[216:219], v[164:167], a[204:207], v[216:219]\n"
      "v_mul_f32 v169, 0xbbaaaaab, v168\n"
      "v_mul_f32 v170, 0xbbaaaaab, v168\n"
      "v_mfma_f32_16x16x32_bf16 v[172:175], v[244:247], a[172:175], v[172:175]\n"
      "v_mul_f32 v168, 0xbbaaaaab, v168\n"
      "v_fmac_f32 v243, v182, v182\n"
      "v_mfma_f32_16x16x32_bf16 v[172:175], v[164:167], a[208:211], v[172:175]\n"
      "v_fmac_f32 v242, v200, v200\n"
      "v_fmac_f32 v243, v183, v183\n"
      "v_mfma_f32_16x16x32_bf16 v[212:215], v[238:241], a[228:231], v[168:171]\n"
      "v_fmac_f32 v243, v248, v248\n"
      "v_fmac_f32_e64 v242, v201, v201\n"
      "v_mfma_f32_16x16x32_bf16 v[206:209], v[236:239], a[228:231], v[168:171]\n"
      "ds_read_b128 v[148:151], v114 offset:8192\n"
      "v_fmac_f32 v243, v249, v249\n"
      "v_mfma_f32_16x16x32_bf16 v[232:235], v[238:241], a[232:235], v[168:171]\n"
      "v_fmac_f32 v243, v250, v250\n"
      "v_mfma_f32_16x16x32_bf16 v[252:255], v[236:239], a[232:235], v[168:171]\n"
      "s_add_u32 m0, s78, 18432\n"
      "v_cvt_pk_bf16_f32 v212, v212, v213\n"
      "v_cvt_pk_bf16_f32 v213, v214, v215\n"
      "v_mfma_f32_16x16x32_bf16 v[136:139], v[238:241], a[236:239], v[168:171]\n"
      "v_cvt_pk_bf16_f32 v215, v208, v209\n"
      "v_cvt_pk_bf16_f32 v214, v206, v207\n"
      "v_mfma_f32_16x16x32_bf16 v[206:209], v[236:239], a[236:239], v[168:171]\n"
      "ds_write_b128 v204, v[212:215] offset:3072\n"
      "v_mfma_f32_16x16x32_bf16 v[144:147], v[244:247], a[176:179], v[144:147]\n"
      "ds_read_b128 v[212:215], v115 offset:8192\n"
      "v_cvt_pk_bf16_f32 v232, v232, v233\n"
      "v_mfma_f32_16x16x32_bf16 v[144:147], v[164:167], a[212:215], v[144:147]\n"
      "v_cvt_pk_bf16_f32 v233, v234, v235\n"
      "v_cvt_pk_bf16_f32 v234, v252, v253\n"
      "v_mfma_f32_16x16x32_bf16 v[168:171], a[0:3], v[192:195], 0\n"
      "v_cvt_pk_bf16_f32 v136, v136, v137\n"
      "v_cvt_pk_bf16_f32 v235, v254, v255\n"
      "v_mfma_f32_16x16x32_bf16 v[168:171], a[36:39], v[196:199], v[168:171]\n"
      "ds_write_b128 v204, v[232:235] offset:3200\n"
      "v_mfma_f32_16x16x32_bf16 v[232:235], a[4:7], v[192:195], 0\n"
      "v_fmac_f32 v242, v202, v202\n"
      "v_cvt_pk_bf16_f32 v137, v138, v139\n"
      "v_mfma_f32_16x16x32_bf16 v[232:235], a[40:43], v[196:199], v[232:235]\n"
      "s_mov_b32 exec_hi, 0\n"
      "global_load_lds_dwordx4 v118, s[52:53]\n"
      "s_mov_b32 exec_hi, -1\n"
      "v_mfma_f32_16x16x32_bf16 v[244:247], a[8:11], v[192:195], 0\n"
      "v_cvt_pk_bf16_f32 v139, v208, v209\n"
      "v_fmac_f32 v242, v203, v203\n"
      "v_mfma_f32_16x16x32_bf16 v[244:247], a[44:47], v[196:199], v[244:247]\n"
      "v_fmac_f32 v243, v251, v251\n"
      "v_cvt_pk_bf16_f32 v138, v206, v207\n"
      "v_mfma_f32_16x16x32_bf16 v[164:167], a[12:15], v[192:195], 0\n"
      "v_fmac_f32 v242, v188, v188\n"
      "v_fmac_f32_e64 v243, v176, v176\n"
      "v_mfma_f32_16x16x32_bf16 v[164:167], a[48:51], v[196:199], v[164:167]\n"
      "ds_write_b128 v204, v[136:139] offset:3328\n"
      "v_mfma_f32_16x16x32_bf16 v[206:209], a[16:19], v[192:195], 0\n"
      "v_fmac_f32 v242, v189, v189\n"
      "v_fmac_f32 v243, v177, v177\n"
      "v_mfma_f32_16x16x32_bf16 v[206:209], a[52:55], v[196:199], v[206:209]\n"
      "v_fmac_f32 v243, v178, v178\n"
      "v_fmac_f32 v242, v190, v190\n"
      "v_mfma_f32_16x16x32_bf16 v[136:139], a[20:23], v[192:195], 0\n"
      "s_add_u32 m0, s76, 115456\n"
      "v_fmac_f32 v243, v179, v179\n"
      "v_fmac_f32 v242, v191, v191\n"
      "v_mfma_f32_16x16x32_bf16 v[136:139], a[56:59], v[196:199], v[136:139]\n"
      "s_waitcnt lgkmcnt(1)\n"
      "v_permlane16_swap_b32 v242, v243\n"
      "v_mfma_f32_16x16x32_bf16 v[252:255], v[192:195], a[24:27], 0\n"
      "v_add_f32 v242, v242, v243\n"
      "v_mov_b32 v243, v242\n"
      "v_mfma_f32_16x16x32_bf16 v[252:255], v[196:199], a[60:63], v[252:255]\n"
      "s_nop 0\n"
      "v_permlane32_swap_b32 v242, v243\n"
      "v_mfma_f32_16x16x32_bf16 v[156:159], v[192:195], a[28:31], 0\n"
      "v_add_f32 v242, v242, v243\n"
      "v_mfma_f32_16x16x32_bf16 v[156:159], v[196:199], a[64:67], v[156:159]\n"
      "v_rsq_f32 v242, v242\n"
      "v_mfma_f32_16x16x32_bf16 v[168:171], a[72:75], v[148:151], v[168:171]\n"
      "v_mov_b32_e64 v243, v242\n"
      "v_mfma_f32_16x16x32_bf16 v[168:171], a[108:111], v[212:215], v[168:171]\n"
      "s_nop 0\n"
      "v_permlane16_swap_b32 v242, v243\n"
      "v_mfma_f32_16x16x32_bf16 v[232:235], a[76:79], v[148:151], v[232:235]\n"
      "global_load_lds_dwordx4 v119, s[56:57]\n"
      "v_mfma_f32_16x16x32_bf16 v[232:235], a[112:115], v[212:215], v[232:235]\n"
      "v_mul_f32 v181, v243, v181\n"
      "v_mul_f32 v180, v243, v180\n"
      "v_mfma_f32_16x16x32_bf16 v[244:247], a[80:83], v[148:151], v[244:247]\n"
      "v_cvt_pk_bf16_f32 v180, v180, v181\n"
      "v_mul_f32 v182, v243, v182\n"
      "v_mfma_f32_16x16x32_bf16 v[244:247], a[116:119], v[212:215], v[244:247]\n"
      "s_add_u32 m0, s79, 4864\n"
      "v_mul_f32 v183, v243, v183\n"
      "v_cvt_pk_bf16_f32 v181, v182, v183\n"
      "v_mfma_f32_16x16x32_bf16 v[164:167], a[84:87], v[148:151], v[164:167]\n"
      "v_mul_f32 v249, v243, v249\n"
      "v_mul_f32 v248, v243, v248\n"
      "v_mfma_f32_16x16x32_bf16 v[164:167], a[120:123], v[212:215], v[164:167]\n"
      "v_mul_f32 v250, v243, v250\n"
      "v_mul_f32 v251, v243, v251\n"
      "v_mfma_f32_16x16x32_bf16 v[140:143], v[192:195], a[32:35], 0\n"
      "v_cvt_pk_bf16_f32 v182, v248, v249\n"
      "v_mul_f32_e64 v179, v243, v179\n"
      "v_mfma_f32_16x16x32_bf16 v[140:143], v[196:199], a[68:71], v[140:143]\n"
      "ds_read_b128 v[192:195], v115 offset:14336\n"
      "v_mul_f32 v176, v243, v176\n"
      "v_mfma_f32_16x16x32_bf16 v[206:209], a[88:91], v[148:151], v[206:209]\n"
      "ds_read_b128 v[220:223], v114 offset:14336\n"
      "v_mul_f32 v177, v243, v177\n"
      "v_mfma_f32_16x16x32_bf16 v[206:209], a[124:127], v[212:215], v[206:209]\n"
      "v_mfma_f32_16x16x32_bf16 v[196:199], v[152:155], a[240:243], 0\n"
      "v_mul_f32 v242, s36, v242\n"
      "v_mul_f32 v190, v242, v190\n"
      "v_mfma_f32_16x16x32_bf16 v[196:199], v[152:155], a[244:247], v[196:199]\n"
      "v_mul_f32 v186, v242, v186\n"
      "v_mul_f32 v187, v242, v187\n"
      "v_mfma_f32_16x16x32_bf16 v[196:199], v[152:155], a[248:251], v[196:199]\n"
      "v_cvt_pk_bf16_f32 v34, v176, v177\n"
      "v_mul_f32_e64 v188, v242, v188\n"
      "v_mfma_f32_16x16x32_bf16 v[136:139], a[92:95], v[148:151], v[136:139]\n"
      "v_mul_f32 v185, v242, v185\n"
      "v_mul_f32 v178, v243, v178\n"
      "v_mfma_f32_16x16x32_bf16 v[136:139], a[128:131], v[212:215], v[136:139]\n"
      "v_cvt_pk_bf16_f32 v183, v250, v251\n"
      "v_mul_f32_e64 v184, v242, v184\n"
      "v_mfma_f32_16x16x32_bf16 v[252:255], v[148:151], a[96:99], v[252:255]\n"
      "v_add_f32_dpp v196, v196, v196 quad_perm:[1,0,3,2] row_mask:0xf bank_mask:0xf\n"
      "v_cvt_pk_bf16_f32 v184, v184, v185\n"
      "v_mfma_f32_16x16x32_bf16 v[252:255], v[212:215], a[132:135], v[252:255]\n"
      "s_nop 0\n"
      "v_cvt_pk_bf16_f32 v35, v178, v179\n"
      "s_waitcnt lgkmcnt(0)\n"
      "v_mfma_f32_16x16x32_bf16 v[156:159], v[148:151], a[100:103], v[156:159]\n"
      "ds_read_b128 v[248:251], v114 offset:4096\n"
      "v_mul_f32 v202, v242, v202\n"
      "v_mfma_f32_16x16x32_bf16 v[156:159], v[212:215], a[136:139], v[156:159]\n"
      "v_mul_f32 v203, v242, v203\n"
      "v_cvt_pk_bf16_f32 v185, v186, v187\n"
      "v_mfma_f32_16x16x32_bf16 v[140:143], v[148:151], a[104:107], v[140:143]\n"
      "v_mfma_f32_16x16x32_bf16 v[140:143], v[212:215], a[140:143], v[140:143]\n"
      "v_cvt_pk_bf16_f32 v187, v202, v203\n"
      "v_mfma_f32_16x16x32_bf16 v[168:171], a[144:147], v[220:223], v[168:171]\n"
      "v_mul_f32_e64 v200, v242, v200\n"
      "v_add_f32_dpp v196, v196, v196 quad_perm:[2,3,0,1] row_mask:0xf bank_mask:0xf\n"
      "v_mfma_f32_16x16x32_bf16 v[168:171], a[180:183], v[192:195], v[168:171]\n"
      "v_mul_f32_e64 v191, v242, v191\n"
      "v_mfma_f32_16x16x32_bf16 v[232:235], a[148:151], v[220:223], v[232:235]\n"
      "v_add_f32_dpp v196, v196, v196 row_half_mirror row_mask:0xf bank_mask:0xf\n"
      "v_mul_f32 v198, 0xbbaaaaab, v196\n"
      "v_mfma_f32_16x16x32_bf16 v[232:235], a[184:187], v[192:195], v[232:235]\n"
      "v_mul_f32 v197, 0xbbaaaaab, v196\n"
      "s_waitcnt vmcnt(14)\n"
      "v_mfma_f32_16x16x32_bf16 v[148:151], v[152:155], a[252:255], 0\n"
      "v_cvt_pk_bf16_f32 v216, v216, v217\n"
      "s_waitcnt vmcnt(13)\n"
      "v_mfma_f32_16x16x32_bf16 v[148:151], v[152:155], v[0:3], v[148:151]\n"
      "v_mul_f32 v189, v242, v189\n"
      "v_mul_f32 v199, 0xbbaaaaab, v196\n"
      "s_waitcnt vmcnt(12)\n"
      "v_mfma_f32_16x16x32_bf16 v[148:151], v[152:155], v[4:7], v[148:151]\n"
      "v_cvt_pk_bf16_f32 v33, v190, v191\n"
      "v_mul_f32 v196, 0xbbaaaaab, v196\n"
      "v_mfma_f32_16x16x32_bf16 v[244:247], a[152:155], v[220:223], v[244:247]\n"
      "v_cvt_pk_bf16_f32 v32, v188, v189\n"
      "v_mul_f32_e64 v201, v242, v201\n"
      "v_mfma_f32_16x16x32_bf16 v[244:247], a[188:191], v[192:195], v[244:247]\n"
      "v_cvt_pk_bf16_f32 v186, v200, v201\n"
      "v_mfma_f32_16x16x32_bf16 v[188:191], v[236:239], a[240:243], v[196:199]\n"
      "global_load_lds_dword v122, s[58:59]\n"
      "v_mfma_f32_16x16x32_bf16 v[212:215], v[238:241], a[240:243], v[196:199]\n"
      "v_mfma_f32_16x16x32_bf16 v[164:167], a[156:159], v[220:223], v[164:167]\n"
      "v_mfma_f32_16x16x32_bf16 v[164:167], a[192:195], v[192:195], v[164:167]\n"
      "v_mfma_f32_16x16x32_bf16 v[206:209], a[160:163], v[220:223], v[206:209]\n"
      "ds_read_b128 v[200:203], v115 offset:4096\n"
      "v_add_f32_dpp v148, v148, v148 quad_perm:[1,0,3,2] row_mask:0xf bank_mask:0xf\n"
      "v_mfma_f32_16x16x32_bf16 v[206:209], a[196:199], v[192:195], v[206:209]\n"
      "s_nop 1\n"
      "v_cvt_pk_bf16_f32 v212, v212, v213\n"
      "v_mfma_f32_16x16x32_bf16 v[136:139], a[164:167], v[220:223], v[136:139]\n"
      "s_nop 0\n"
      "v_cvt_pk_bf16_f32 v213, v214, v215\n"
      "v_cvt_pk_bf16_f32 v215, v190, v191\n"
      "v_mfma_f32_16x16x32_bf16 v[136:139], a[200:203], v[192:195], v[136:139]\n"
      "v_add_f32_dpp v148, v148, v148 quad_perm:[2,3,0,1] row_mask:0xf bank_mask:0xf\n"
      "v_cvt_pk_bf16_f32 v214, v188, v189\n"
      "v_mfma_f32_16x16x32_bf16 v[188:191], v[238:241], a[244:247], v[196:199]\n"
      "ds_write_b128 v204, v[212:215] offset:6144\n"
      "v_mfma_f32_16x16x32_bf16 v[212:215], v[236:239], a[244:247], v[196:199]\n"
      "v_add_f32_dpp v148, v148, v148 row_half_mirror row_mask:0xf bank_mask:0xf\n"
      "v_mul_f32 v151, 0xbbaaaaab, v148\n"
      "v_mfma_f32_16x16x32_bf16 v[252:255], v[220:223], a[168:171], v[252:255]\n"
      "ds_read_b128 v[160:163], v115 offset:10240\n"
      "v_mfma_f32_16x16x32_bf16 v[252:255], v[192:195], a[204:207], v[252:255]\n"
      "s_nop 0\n"
      "v_cvt_pk_bf16_f32 v188, v188, v189\n"
      "v_cvt_pk_bf16_f32 v189, v190, v191\n"
      "v_mfma_f32_16x16x32_bf16 v[156:159], v[220:223], a[172:175], v[156:159]\n"
      "s_waitcnt lgkmcnt(2)\n"
      "v_cvt_pk_bf16_f32 v190, v212, v213\n"
      "v_cvt_pk_bf16_f32 v191, v214, v215\n"
      "v_mfma_f32_16x16x32_bf16 v[156:159], v[192:195], a[208:211], v[156:159]\n"
      "ds_write_b128 v204, v[188:191] offset:6272\n"
      "v_mfma_f32_16x16x32_bf16 v[188:191], v[238:241], a[248:251], v[196:199]\n"
      "v_mul_f32 v150, 0xbbaaaaab, v148\n"
      "v_mul_f32 v149, 0xbbaaaaab, v148\n"
      "v_mfma_f32_16x16x32_bf16 v[212:215], v[236:239], a[248:251], v[196:199]\n"
      "v_mul_f32 v148, 0xbbaaaaab, v148\n"
      "v_cvt_pk_bf16_f32 v217, v218, v219\n"
      "v_mfma_f32_16x16x32_bf16 v[140:143], v[220:223], a[176:179], v[140:143]\n"
      "v_cvt_pk_bf16_f32 v218, v252, v253\n"
      "v_cvt_pk_bf16_f32 v219, v254, v255\n"
      "v_mfma_f32_16x16x32_bf16 v[140:143], v[192:195], a[212:215], v[140:143]\n"
      "v_cvt_pk_bf16_f32 v188, v188, v189\n"
      "v_cvt_pk_bf16_f32 v189, v190, v191\n"
      "v_mfma_f32_16x16x32_bf16 v[196:199], v[238:241], a[252:255], v[148:151]\n"
      "v_cvt_pk_bf16_f32 v190, v212, v213\n"
      "v_cvt_pk_bf16_f32 v191, v214, v215\n"
      "v_mfma_f32_16x16x32_bf16 v[176:179], v[236:239], a[252:255], v[148:151]\n"
      "ds_write_b128 v204, v[188:191] offset:6400\n"
      "v_mfma_f32_16x16x32_bf16 v[212:215], v[238:241], v[0:3], v[148:151]\n"
      "v_cvt_pk_bf16_f32 v172, v172, v173\n"
      "v_cvt_pk_bf16_f32 v173, v174, v175\n"
      "v_mfma_f32_16x16x32_bf16 v[188:191], v[236:239], v[0:3], v[148:151]\n"
      "v_cvt_pk_bf16_f32 v196, v196, v197\n"
      "v_cvt_pk_bf16_f32 v197, v198, v199\n"
      "v_mfma_f32_16x16x32_bf16 v[192:195], v[238:241], v[4:7], v[148:151]\n"
      "v_cvt_pk_bf16_f32 v198, v176, v177\n"
      "v_cvt_pk_bf16_f32 v199, v178, v179\n"
      "v_mfma_f32_16x16x32_bf16 v[176:179], v[236:239], v[4:7], v[148:151]\n"
      "ds_write_b128 v204, v[196:199] offset:9216\n"
      "v_mfma_f32_16x16x32_bf16 v[252:255], a[0:3], v[248:251], 0\n"
      "ds_read_b128 v[220:223], v114 offset:10240\n"
      "v_cvt_pk_bf16_f32 v212, v212, v213\n"
      "v_mfma_f32_16x16x32_bf16 v[252:255], a[36:39], v[200:203], v[252:255]\n"
      "v_cvt_pk_bf16_f32 v213, v214, v215\n"
      "v_cvt_pk_bf16_f32 v215, v190, v191\n"
      "v_cvt_pk_bf16_f32 v214, v188, v189\n"
      "v_mfma_f32_16x16x32_bf16 v[196:199], a[4:7], v[248:251], 0\n"
      "v_cvt_pk_bf16_f32 v192, v192, v193\n"
      "v_cvt_pk_bf16_f32 v193, v194, v195\n"
      "v_mfma_f32_16x16x32_bf16 v[196:199], a[40:43], v[200:203], v[196:199]\n"
      "ds_write_b128 v204, v[212:215] offset:9344\n"
      "v_mfma_f32_16x16x32_bf16 v[148:151], a[8:11], v[248:251], 0\n"
      "v_cvt_pk_bf16_f32 v194, v176, v177\n"
      "v_cvt_pk_bf16_f32 v195, v178, v179\n"
      "v_mfma_f32_16x16x32_bf16 v[148:151], a[44:47], v[200:203], v[148:151]\n"
      "ds_write_b128 v204, v[192:195] offset:9472\n"
      "v_mfma_f32_16x16x32_bf16 v[212:215], a[12:15], v[248:251], 0\n"
      "v_cvt_pk_bf16_f32 v175, v158, v159\n"
      "v_cvt_pk_bf16_f32 v174, v156, v157\n"
      "v_mfma_f32_16x16x32_bf16 v[212:215], a[48:51], v[200:203], v[212:215]\n"
      "v_cvt_pk_bf16_f32 v144, v144, v145\n"
      "v_cvt_pk_bf16_f32 v145, v146, v147\n"
      "v_mfma_f32_16x16x32_bf16 v[192:195], a[16:19], v[248:251], 0\n"
      "s_nop 0\n"
      "v_cvt_pk_bf16_f32 v146, v140, v141\n"
      "v_cvt_pk_bf16_f32 v147, v142, v143\n"
      "v_mfma_f32_16x16x32_bf16 v[192:195], a[52:55], v[200:203], v[192:195]\n"
      "s_waitcnt lgkmcnt(2)\n"
      "v_fma_f32 v243, v164, v164, s38\n"
      "v_fma_f32 v205, v168, v168, s38\n"
      "v_mfma_f32_16x16x32_bf16 v[188:191], a[20:23], v[248:251], 0\n"
      "v_fmac_f32 v205, v169, v169\n"
      "v_fmac_f32 v243, v165, v165\n"
      "v_mfma_f32_16x16x32_bf16 v[188:191], a[56:59], v[200:203], v[188:191]\n"
      "v_fmac_f32 v205, v170, v170\n"
      "v_fmac_f32 v243, v166, v166\n"
      "v_mfma_f32_16x16x32_bf16 v[252:255], a[72:75], v[220:223], v[252:255]\n"
      "v_fmac_f32 v205, v171, v171\n"
      "v_fmac_f32 v243, v167, v167\n"
      "v_mfma_f32_16x16x32_bf16 v[252:255], a[108:111], v[160:163], v[252:255]\n"
      "v_fmac_f32 v243, v206, v206\n"
      "v_fmac_f32 v205, v232, v232\n"
      "v_mfma_f32_16x16x32_bf16 v[196:199], a[76:79], v[220:223], v[196:199]\n"
      "v_fmac_f32 v243, v207, v207\n"
      "v_fmac_f32 v205, v233, v233\n"
      "v_mfma_f32_16x16x32_bf16 v[196:199], a[112:115], v[160:163], v[196:199]\n"
      "v_fmac_f32 v205, v234, v234\n"
      "v_fmac_f32 v243, v208, v208\n"
      "v_mfma_f32_16x16x32_bf16 v[148:151], a[80:83], v[220:223], v[148:151]\n"
      "v_fmac_f32 v243, v209, v209\n"
      "v_fmac_f32 v205, v235, v235\n"
      "v_mfma_f32_16x16x32_bf16 v[148:151], a[116:119], v[160:163], v[148:151]\n"
      "v_fmac_f32 v243, v136, v136\n"
      "v_fmac_f32 v205, v244, v244\n"
      "v_mfma_f32_16x16x32_bf16 v[212:215], a[84:87], v[220:223], v[212:215]\n"
      "v_fmac_f32 v243, v137, v137\n"
      "v_fmac_f32 v205, v245, v245\n"
      "v_mfma_f32_16x16x32_bf16 v[212:215], a[120:123], v[160:163], v[212:215]\n"
      "v_fmac_f32 v243, v138, v138\n"
      "v_fmac_f32 v205, v246, v246\n"
      "v_mfma_f32_16x16x32_bf16 v[192:195], a[88:91], v[220:223], v[192:195]\n"
      "v_fmac_f32 v243, v139, v139\n"
      "v_fmac_f32 v205, v247, v247\n"
      "v_mfma_f32_16x16x32_bf16 v[192:195], a[124:127], v[160:163], v[192:195]\n"
      "s_nop 0\n"
      "v_permlane16_swap_b32 v205, v243\n"
      "v_mfma_f32_16x16x32_bf16 v[188:191], a[92:95], v[220:223], v[188:191]\n"
      "v_add_f32 v205, v205, v243\n"
      "v_mov_b32 v243, v205\n"
      "v_mfma_f32_16x16x32_bf16 v[188:191], a[128:131], v[160:163], v[188:191]\n"
      "s_nop 0\n"
      "v_permlane32_swap_b32 v205, v243\n"
      "v_mfma_f32_16x16x32_bf16 v[176:179], v[248:251], a[24:27], 0\n"
      "v_add_f32_e64 v205, v205, v243\n"
      "v_mfma_f32_16x16x32_bf16 v[176:179], v[200:203], a[60:63], v[176:179]\n"
      "v_rsq_f32 v205, v205\n"
      "v_mfma_f32_16x16x32_bf16 v[176:179], v[220:223], a[96:99], v[176:179]\n"
      "v_mov_b32 v243, v205\n"
      "v_mfma_f32_16x16x32_bf16 v[176:179], v[160:163], a[132:135], v[176:179]\n"
      "s_nop 0\n"
      "v_permlane16_swap_b32 v205, v243\n"
      "v_mul_f32 v207, v243, v207\n"
      "v_mfma_f32_16x16x32_bf16 v[156:159], v[248:251], a[28:31], 0\n"
      "v_mul_f32 v164, v243, v164\n"
      "v_mul_f32_e64 v165, v243, v165\n"
      "v_mfma_f32_16x16x32_bf16 v[156:159], v[200:203], a[64:67], v[156:159]\n"
      "v_cvt_pk_bf16_f32 v164, v164, v165\n"
      "v_mul_f32 v166, v243, v166\n"
      "v_mfma_f32_16x16x32_bf16 v[156:159], v[220:223], a[100:103], v[156:159]\n"
      "v_mul_f32 v167, v243, v167\n"
      "v_cvt_pk_bf16_f32 v165, v166, v167\n"
      "v_mfma_f32_16x16x32_bf16 v[156:159], v[160:163], a[136:139], v[156:159]\n"
      "v_mul_f32 v206, v243, v206\n"
      "v_mul_f32 v209, v243, v209\n"
      "v_mfma_f32_16x16x32_bf16 v[140:143], v[248:251], a[32:35], 0\n"
      "v_mul_f32_e64 v208, v243, v208\n"
      "v_cvt_pk_bf16_f32 v166, v206, v207\n"
      "v_mfma_f32_16x16x32_bf16 v[140:143], v[200:203], a[68:71], v[140:143]\n"
      "ds_read_b128 v[200:203], v115 offset:16384\n"
      "v_cvt_pk_bf16_f32 v167, v208, v209\n"
      "v_mfma_f32_16x16x32_bf16 v[140:143], v[220:223], a[104:107], v[140:143]\n"
      "ds_read_b128 v[206:209], v114 offset:16384\n"
      "v_mul_f32_e64 v136, v243, v136\n"
      "v_mfma_f32_16x16x32_bf16 v[140:143], v[160:163], a[140:143], v[140:143]\n"
      "v_mul_f32 v137, v243, v137\n"
      "v_cvt_pk_bf16_f32 v40, v136, v137\n"
      "s_waitcnt vmcnt(12)\n"
      "v_mfma_f32_16x16x32_bf16 v[248:251], v[152:155], v[8:11], 0\n"
      "v_mul_f32 v138, v243, v138\n"
      "v_mul_f32 v139, v243, v139\n"
      "s_waitcnt vmcnt(11)\n"
      "v_mfma_f32_16x16x32_bf16 v[248:251], v[152:155], v[12:15], v[248:251]\n"
      "v_mul_f32 v205, s36, v205\n"
      "v_cvt_pk_bf16_f32 v41, v138, v139\n"
      "s_waitcnt vmcnt(10)\n"
      "v_mfma_f32_16x16x32_bf16 v[248:251], v[152:155], v[16:19], v[248:251]\n"
      "v_mul_f32 v235, v205, v235\n"
      "v_mul_f32 v246, v205, v246\n"
      "v_mul_f32 v168, v205, v168\n"
      "v_mul_f32 v169, v205, v169\n"
      "v_mul_f32 v247, v205, v247\n"
      "v_mul_f32 v232, v205, v232\n"
      "s_waitcnt vmcnt(9)\n"
      "v_mfma_f32_16x16x32_bf16 v[160:163], v[152:155], v[20:23], 0\n"
      "v_cvt_pk_bf16_f32 v168, v168, v169\n"
      "v_mul_f32 v170, v205, v170\n"
      "s_waitcnt vmcnt(8)\n"
      "v_mfma_f32_16x16x32_bf16 v[160:163], v[152:155], v[24:27], v[160:163]\n"
      "v_mul_f32 v171, v205, v171\n"
      "v_cvt_pk_bf16_f32 v169, v170, v171\n"
      "s_waitcnt vmcnt(7)\n"
      "v_mfma_f32_16x16x32_bf16 v[160:163], v[152:155], v[28:31], v[160:163]\n"
      "s_waitcnt lgkmcnt(0)\n"
      "v_mul_f32 v244, v205, v244\n"
      "v_mul_f32_e64 v234, v205, v234\n"
      "v_mfma_f32_16x16x32_bf16 v[252:255], a[144:147], v[206:209], v[252:255]\n"
      "v_mul_f32_e64 v245, v205, v245\n"
      "v_cvt_pk_bf16_f32 v171, v234, v235\n"
      "v_mfma_f32_16x16x32_bf16 v[252:255], a[180:183], v[200:203], v[252:255]\n"
      "v_mul_f32_e64 v233, v205, v233\n"
      "v_add_f32_dpp v160, v160, v160 quad_perm:[1,0,3,2] row_mask:0xf bank_mask:0xf\n"
      "v_mfma_f32_16x16x32_bf16 v[196:199], a[148:151], v[206:209], v[196:199]\n"
      "v_add_f32_dpp v248, v248, v248 quad_perm:[1,0,3,2] row_mask:0xf bank_mask:0xf\n"
      "v_cvt_pk_bf16_f32 v39, v246, v247\n"
      "v_mfma_f32_16x16x32_bf16 v[196:199], a[184:187], v[200:203], v[196:199]\n"
      "s_nop 0\n"
      "v_cvt_pk_bf16_f32 v170, v232, v233\n"
      "v_add_f32_dpp v248, v248, v248 quad_perm:[2,3,0,1] row_mask:0xf bank_mask:0xf\n"
      "v_mfma_f32_16x16x32_bf16 v[148:151], a[152:155], v[206:209], v[148:151]\n"
      "s_nop 0\n"
      "v_add_f32_dpp v248, v248, v248 row_half_mirror row_mask:0xf bank_mask:0xf\n"
      "v_mul_f32 v251, 0xbbaaaaab, v248\n"
      "v_mfma_f32_16x16x32_bf16 v[148:151], a[188:191], v[200:203], v[148:151]\n"
      "v_mul_f32 v250, 0xbbaaaaab, v248\n"
      "v_mul_f32 v249, 0xbbaaaaab, v248\n"
      "v_mfma_f32_16x16x32_bf16 v[212:215], a[156:159], v[206:209], v[212:215]\n"
      "s_nop 0\n"
      "v_mul_f32 v248, 0xbbaaaaab, v248\n"
      "v_add_f32_dpp v160, v160, v160 quad_perm:[2,3,0,1] row_mask:0xf bank_mask:0xf\n"
      "v_mfma_f32_16x16x32_bf16 v[212:215], a[192:195], v[200:203], v[212:215]\n"
      "s_nop 0\n"
      "v_add_f32_dpp v160, v160, v160 row_half_mirror row_mask:0xf bank_mask:0xf\n"
      "v_cvt_pk_bf16_f32 v38, v244, v245\n"
      "v_mfma_f32_16x16x32_bf16 v[232:235], v[236:239], v[8:11], v[248:251]\n"
      "v_mfma_f32_16x16x32_bf16 v[244:247], v[238:241], v[8:11], v[248:251]\n"
      "ds_read_b64 v[242:243], v133 offset:16\n"
      "v_mul_f32 v163, 0xbbaaaaab, v160\n"
      "v_mfma_f32_16x16x32_bf16 v[224:227], v[236:239], v[16:19], v[248:251]\n"
      "ds_read_u16 v56, v134 offset:32\n"
      "v_mul_f32 v162, 0xbbaaaaab, v160\n"
      "v_mfma_f32_16x16x32_bf16 v[152:155], v[238:241], v[12:15], v[248:251]\n"
      "s_nop 1\n"
      "v_cvt_pk_bf16_f32 v244, v244, v245\n"
      "v_mfma_f32_16x16x32_bf16 v[192:195], a[160:163], v[206:209], v[192:195]\n"
      "s_nop 0\n"
      "v_cvt_pk_bf16_f32 v245, v246, v247\n"
      "v_mfma_f32_16x16x32_bf16 v[192:195], a[196:199], v[200:203], v[192:195]\n"
      "v_cvt_pk_bf16_f32 v247, v234, v235\n"
      "v_mfma_f32_16x16x32_bf16 v[188:191], a[164:167], v[206:209], v[188:191]\n"
      "v_cvt_pk_bf16_f32 v246, v232, v233\n"
      "v_mul_f32 v161, 0xbbaaaaab, v160\n"
      "v_mfma_f32_16x16x32_bf16 v[188:191], a[200:203], v[200:203], v[188:191]\n"
      "v_mfma_f32_16x16x32_bf16 v[232:235], v[238:241], v[16:19], v[248:251]\n"
      "ds_write_b128 v204, v[244:247] offset:12288\n"
      "v_mfma_f32_16x16x32_bf16 v[244:247], v[236:239], v[12:15], v[248:251]\n"
      "ds_read_u16 v60, v134 offset:64\n"
      "v_mfma_f32_16x16x32_bf16 v[176:179], v[206:209], a[168:171], v[176:179]\n"
      "v_mfma_f32_16x16x32_bf16 v[176:179], v[200:203], a[204:207], v[176:179]\n"
      "v_cvt_pk_bf16_f32 v152, v152, v153\n"
      "v_mfma_f32_16x16x32_bf16 v[156:159], v[206:209], a[172:175], v[156:159]\n"
      "v_mfma_f32_16x16x32_bf16 v[156:159], v[200:203], a[208:211], v[156:159]\n"
      "v_mul_f32 v160, 0xbbaaaaab, v160\n"
      "v_cvt_pk_bf16_f32 v232, v232, v233\n"
      "v_mfma_f32_16x16x32_bf16 v[140:143], v[206:209], a[176:179], v[140:143]\n"
      "v_cvt_pk_bf16_f32 v153, v154, v155\n"
      "v_cvt_pk_bf16_f32 v155, v246, v247\n"
      "v_mfma_f32_16x16x32_bf16 v[140:143], v[200:203], a[212:215], v[140:143]\n"
      "v_cvt_pk_bf16_f32 v233, v234, v235\n"
      "v_cvt_pk_bf16_f32 v154, v244, v245\n"
      "v_mfma_f32_16x16x32_bf16 v[200:203], v[238:241], v[20:23], v[160:163]\n"
      "ds_write_b128 v204, v[152:155] offset:12416\n"
      "v_mfma_f32_16x16x32_bf16 v[136:139], v[236:239], v[24:27], v[160:163]\n"
      "v_cvt_pk_bf16_f32 v234, v224, v225\n"
      "v_cvt_pk_bf16_f32 v235, v226, v227\n"
      "v_mfma_f32_16x16x32_bf16 v[244:247], v[236:239], v[20:23], v[160:163]\n"
      "ds_read_b128 v[248:251], v132 offset:32\n"
      "v_mfma_f32_16x16x32_bf16 v[206:209], v[238:241], v[28:31], v[160:163]\n"
      "ds_write_b128 v204, v[232:235] offset:12544\n"
      "v_mfma_f32_16x16x32_bf16 v[152:155], v[238:241], v[24:27], v[160:163]\n"
      "ds_read_b64 v[240:241], v132 offset:24\n"
      "v_cvt_pk_bf16_f32 v200, v200, v201\n"
      "v_mfma_f32_16x16x32_bf16 v[232:235], v[236:239], v[28:31], v[160:163]\n"
      "ds_read_b128 v[236:239], v133\n"
      "v_cvt_pk_bf16_f32 v201, v202, v203\n"
      "v_cvt_pk_bf16_f32 v202, v244, v245\n"
      "v_cvt_pk_bf16_f32 v206, v206, v207\n"
      "v_cvt_pk_bf16_f32 v207, v208, v209\n"
      "v_cvt_pk_bf16_f32 v203, v246, v247\n"
      "v_cvt_pk_bf16_f32 v152, v152, v153\n"
      "v_cvt_pk_bf16_f32 v153, v154, v155\n"
      "v_cvt_pk_bf16_f32 v208, v232, v233\n"
      "v_cvt_pk_bf16_f32 v155, v138, v139\n"
      "v_cvt_pk_bf16_f32 v209, v234, v235\n"
      "v_cvt_pk_bf16_f32 v154, v136, v137\n"
      "s_waitcnt vmcnt(0)\n"
      "ds_write_b128 v204, v[152:155] offset:15488\n"
      "ds_write_b128 v204, v[206:209] offset:15616\n"
      "ds_write_b128 v204, v[200:203] offset:15360\n"
      "ds_read_b128 v[204:207], v119 offset:41728\n"
      "ds_read_b32 v234, v122 offset:4864\n"
      "ds_read_u16 v52, v134\n"
      "s_waitcnt lgkmcnt(2)\n"
      "v_or3_b32 v204, v204, v205, v206\n"
      "v_and_b32 v204, s40, v204\n"
      "s_waitcnt lgkmcnt(1)\n"
      "v_bitop3_b32 v234, v234, v207, s40 bitop3:0xa8\n"
      "v_or3_b32 v204, v204, v234, s4\n"
      "ds_write_b32 v129, v204 offset:3072\n"
      "s_waitcnt lgkmcnt(0)\n"
      "s_barrier\n"
      "v_mfma_f32_16x16x32_bf16 v[204:207], v[180:183], v[184:187], v[78:81]\n"
      "ds_read_b64_tr_b16 a[216:217], v230\n"
      "v_fma_f32 v155, v252, v252, s38\n"
      "v_mfma_f32_16x16x32_bf16 v[204:207], v[34:37], v[32:35], v[204:207]\n"
      "ds_read_b64_tr_b16 a[220:221], v230 offset:8\n"
      "v_fma_f32 v154, v212, v212, s38\n"
      "v_mfma_f32_16x16x32_bf16 v[232:235], v[164:167], v[184:187], v[82:85]\n"
      "ds_read_b64_tr_b16 a[224:225], v231\n"
      "v_fmac_f32 v155, v253, v253\n"
      "v_mfma_f32_16x16x32_bf16 v[232:235], v[40:43], v[32:35], v[232:235]\n"
      "ds_read_b64_tr_b16 a[218:219], v230 offset:6144\n"
      "v_fmac_f32 v154, v213, v213\n"
      "v_mfma_f32_16x16x32_bf16 v[200:203], v[248:251], v[184:187], v[70:73]\n"
      "ds_read_b64_tr_b16 a[222:223], v230 offset:6152\n"
      "v_fmac_f32_e64 v155, v254, v254\n"
      "v_mfma_f32_16x16x32_bf16 v[200:203], v[236:239], v[180:183], v[200:203]\n"
      "ds_read_b64_tr_b16 a[226:227], v231 offset:6144\n"
      "v_fmac_f32 v154, v214, v214\n"
      "v_mfma_f32_16x16x32_bf16 v[200:203], v[240:243], v[32:35], v[200:203]\n"
      "ds_read_b64_tr_b16 a[228:229], v230 offset:18432\n"
      "v_fmac_f32 v155, v255, v255\n"
      "v_fmac_f32 v154, v215, v215\n"
      "v_fmac_f32 v155, v196, v196\n"
      "v_fmac_f32 v154, v192, v192\n"
      "v_fmac_f32 v155, v197, v197\n"
      "v_fmac_f32 v154, v193, v193\n"
      "v_fmac_f32 v155, v198, v198\n"
      "v_fmac_f32 v154, v194, v194\n"
      "v_fmac_f32 v155, v199, v199\n"
      "v_fmac_f32 v154, v195, v195\n"
      "v_fmac_f32 v155, v148, v148\n"
      "v_fmac_f32 v154, v188, v188\n"
      "v_fmac_f32 v155, v149, v149\n"
      "v_fmac_f32 v154, v189, v189\n"
      "v_fmac_f32 v155, v150, v150\n"
      "v_fmac_f32 v154, v190, v190\n"
      "v_fmac_f32 v155, v151, v151\n"
      "v_fmac_f32 v154, v191, v191\n"
      "s_nop 1\n"
      "v_permlane16_swap_b32 v155, v154\n"
      "v_add_f32 v155, v155, v154\n"
      "v_mov_b32 v154, v155\n"
      "s_nop 1\n"
      "v_permlane32_swap_b32 v155, v154\n"
      "v_add_f32 v155, v155, v154\n"
      "v_rsq_f32 v155, v155\n"
      "v_cvt_pk_bf16_f32 v50, v176, v177\n"
      "v_mov_b32 v154, v155\n"
      "v_cvt_pk_bf16_f32 v51, v178, v179\n"
      "s_nop 0\n"
      "v_permlane16_swap_b32 v155, v154\n"
      "v_mul_f32 v212, v154, v212\n"
      "v_mul_f32 v213, v154, v213\n"
      "v_cvt_pk_bf16_f32 v212, v212, v213\n"
      "v_mul_f32 v214, v154, v214\n"
      "v_mul_f32 v215, v154, v215\n"
      "v_cvt_pk_bf16_f32 v213, v214, v215\n"
      "v_mul_f32 v192, v154, v192\n"
      "v_mul_f32 v193, v154, v193\n"
      "v_cvt_pk_bf16_f32 v214, v192, v193\n"
      "v_mul_f32 v194, v154, v194\n"
      "v_mul_f32 v195, v154, v195\n"
      "v_cvt_pk_bf16_f32 v215, v194, v195\n"
      "v_mul_f32 v188, v154, v188\n"
      "v_mul_f32 v189, v154, v189\n"
      "v_cvt_pk_bf16_f32 v46, v188, v189\n"
      "v_mul_f32 v190, v154, v190\n"
      "v_mul_f32 v191, v154, v191\n"
      "v_cvt_pk_bf16_f32 v47, v190, v191\n"
      "v_mul_f32 v155, s36, v155\n"
      "v_mul_f32 v252, v155, v252\n"
      "v_mfma_f32_16x16x32_bf16 v[192:195], v[212:215], v[184:187], v[86:89]\n"
      "ds_read_b64_tr_b16 a[232:233], v230 offset:18440\n"
      "v_mul_f32_e64 v253, v155, v253\n"
      "v_mfma_f32_16x16x32_bf16 v[192:195], v[46:49], v[32:35], v[192:195]\n"
      "ds_read_b64_tr_b16 a[236:237], v231 offset:18432\n"
      "ds_read_b64_tr_b16 a[230:231], v230 offset:24576\n"
      "ds_read_b64_tr_b16 a[234:235], v230 offset:24584\n"
      "ds_read_b64_tr_b16 a[238:239], v231 offset:24576\n"
      "ds_read_b64_tr_b16 a[240:241], v229\n"
      "ds_read_b64_tr_b16 a[244:245], v229 offset:8\n"
      "ds_read_b64_tr_b16 a[248:249], v228\n"
      "ds_read_b64_tr_b16 a[242:243], v229 offset:6144\n"
      "ds_read_b64_tr_b16 a[246:247], v229 offset:6152\n"
      "ds_read_b64_tr_b16 a[250:251], v228 offset:6144\n"
      "ds_read_b64_tr_b16 a[252:253], v229 offset:18432\n"
      "ds_read_b64_tr_b16 v[0:1], v229 offset:18440\n"
      "ds_read_b64_tr_b16 v[4:5], v228 offset:18432\n"
      "ds_read_b64_tr_b16 a[254:255], v229 offset:24576\n"
      "ds_read_b64_tr_b16 v[2:3], v229 offset:24584\n"
      "ds_read_b64_tr_b16 v[6:7], v228 offset:24576\n"
      "ds_read_b64_tr_b16 v[8:9], v230 offset:12288\n"
      "ds_read_b64_tr_b16 v[12:13], v230 offset:12296\n"
      "ds_read_b64_tr_b16 v[16:17], v231 offset:12288\n"
      "ds_read_b64_tr_b16 v[10:11], v230 offset:30720\n"
      "ds_read_b64_tr_b16 v[14:15], v230 offset:30728\n"
      "ds_read_b64_tr_b16 v[18:19], v231 offset:30720\n"
      "ds_read_b64_tr_b16 v[20:21], v229 offset:12288\n"
      "ds_read_b64_tr_b16 v[24:25], v229 offset:12296\n"
      "ds_read_b64_tr_b16 v[28:29], v228 offset:12288\n"
      "ds_read_b64_tr_b16 v[22:23], v229 offset:30720\n"
      "ds_read_b64_tr_b16 v[26:27], v229 offset:30728\n"
      "ds_read_b64_tr_b16 v[30:31], v228 offset:30720\n"
      "v_cvt_pk_bf16_f32 v252, v252, v253\n"
      "v_mul_f32 v254, v155, v254\n"
      "v_mul_f32 v255, v155, v255\n"
      "v_cvt_pk_bf16_f32 v253, v254, v255\n"
      "v_mul_f32 v196, v155, v196\n"
      "v_mul_f32 v197, v155, v197\n"
      "v_cvt_pk_bf16_f32 v254, v196, v197\n"
      "v_mul_f32 v198, v155, v198\n"
      "v_mul_f32 v199, v155, v199\n"
      "v_cvt_pk_bf16_f32 v255, v198, v199\n"
      "v_mul_f32 v148, v155, v148\n"
      "v_mul_f32 v149, v155, v149\n"
      "v_cvt_pk_bf16_f32 v44, v148, v149\n"
      "v_mul_f32 v150, v155, v150\n"
      "v_mul_f32 v151, v155, v151\n"
      "v_cvt_pk_bf16_f32 v45, v150, v151\n"
      "v_cvt_pk_bf16_f32 v54, v156, v157\n"
      "v_cvt_pk_bf16_f32 v55, v158, v159\n"
      "v_cvt_pk_bf16_f32 v58, v140, v141\n"
      "v_cvt_pk_bf16_f32 v59, v142, v143\n"
      "s_waitcnt lgkmcnt(0)\n"
      "s_barrier\n"
      "s_or_b64 s[34:35], s[48:49], s[50:51]\n"
      "s_cbranch_scc1 L_gen_8_%=\n"
      "L_fast_11_%=:\n"
      "v_mfma_f32_16x16x32_bf16 v[196:199], v[180:183], v[168:171], v[90:93]\n"
      "v_exp_f32_e64 v204, v204\n"
      "v_mfma_f32_16x16x32_bf16 v[196:199], v[34:37], v[38:41], v[196:199]\n"
      "ds_read_b128 v[148:151], v128 offset:3072\n"
      "v_exp_f32 v233, v233\n"
      "v_mfma_f32_16x16x32_bf16 v[184:187], v[164:167], v[168:171], v[94:97]\n"
      "v_exp_f32 v205, v205\n"
      "v_mfma_f32_16x16x32_bf16 v[184:187], v[40:43], v[38:41], v[184:187]\n"
      "v_exp_f32 v235, v235\n"
      "v_exp_f32 v206, v206\n"
      "v_mfma_f32_16x16x32_bf16 v[228:231], v[248:251], v[168:171], v[72:75]\n"
      "v_exp_f32_e64 v193, v193\n"
      "v_mfma_f32_16x16x32_bf16 v[228:231], v[236:239], v[164:167], v[228:231]\n"
      "v_exp_f32 v207, v207\n"
      "v_mfma_f32_16x16x32_bf16 v[228:231], v[240:243], v[38:41], v[228:231]\n"
      "v_exp_f32 v232, v232\n"
      "v_mfma_f32_16x16x32_bf16 v[188:191], v[212:215], v[168:171], v[98:101]\n"
      "v_exp_f32 v197, v197\n"
      "v_mfma_f32_16x16x32_bf16 v[188:191], v[46:49], v[38:41], v[188:191]\n"
      "v_cvt_pk_bf16_f32 v204, v204, v205\n"
      "v_exp_f32 v196, v196\n"
      "v_mfma_f32_16x16x32_bf16 v[176:179], v[180:183], v[252:255], v[102:105]\n"
      "v_exp_f32_e64 v234, v234\n"
      "v_mfma_f32_16x16x32_bf16 v[176:179], v[34:37], v[44:47], v[176:179]\n"
      "v_cvt_pk_bf16_f32 v205, v206, v207\n"
      "v_cvt_pk_bf16_f32 v206, v232, v233\n"
      "v_cvt_pk_bf16_f32 v207, v234, v235\n"
      "v_mfma_f32_16x16x32_bf16 v[180:183], v[164:167], v[252:255], v[106:109]\n"
      "v_exp_f32 v192, v192\n"
      "v_mfma_f32_16x16x32_bf16 v[180:183], v[40:43], v[44:47], v[180:183]\n"
      "s_waitcnt lgkmcnt(0)\n"
      "v_exp_f32 v191, v191\n"
      "v_mfma_f32_16x16x32_bf16 v[232:235], v[248:251], v[252:255], v[74:77]\n"
      "v_exp_f32 v194, v194\n"
      "v_mfma_f32_16x16x32_bf16 v[232:235], v[236:239], v[212:215], v[232:235]\n"
      "v_exp_f32_e64 v195, v195\n"
      "v_mfma_f32_16x16x32_bf16 v[232:235], v[240:243], v[44:47], v[232:235]\n"
      "v_exp_f32 v200, v200\n"
      "v_exp_f32 v228, v228\n"
      "v_bitop3_b32 v148, v148, v149, s4 bitop3:0x7e\n"
      "v_mfma_f32_16x16x32_bf16 v[152:155], v[212:215], v[252:255], v[110:113]\n"
      "v_cvt_pk_bf16_f32 v62, v192, v193\n"
      "v_cvt_pk_bf16_f32 v63, v194, v195\n"
      "v_mfma_f32_16x16x32_bf16 v[152:155], v[46:49], v[44:47], v[152:155]\n"
      "v_exp_f32 v189, v189\n"
      "v_exp_f32 v190, v190\n"
      "v_exp_f32 v185, v185\n"
      "v_mfma_f32_16x16x32_bf16 v[212:215], v[236:239], v[248:251], v[76:79]\n"
      "v_bitop3_b32 v150, v150, v151, s4 bitop3:0x7e\n"
      "v_or_b32 v148, v148, v150\n"
      "v_cvt_pk_bf16_f32 v64, v200, 0\n"
      "v_mfma_f32_16x16x32_bf16 v[244:247], v[66:69], v[204:207], 0\n"
      "v_cmp_ne_u32 vcc, 0, v148\n"
      "v_cndmask_b32 v201, v201, v229, s[42:43]\n"
      "v_mfma_f32_16x16x32_bf16 v[244:247], v[66:69], v[62:65], v[244:247]\n"
      "s_mov_b64 s[48:49], vcc\n"
      "v_exp_f32 v199, v199\n"
      "v_exp_f32 v186, v186\n"
      "v_mfma_f32_16x16x32_bf16 v[248:251], v[216:219], v[204:207], 0\n"
      "s_cbranch_vccnz L_flg_12_%=\n"
      "L_fb_13_%=:\n"
      "v_mfma_f32_16x16x32_bf16 v[248:251], v[50:53], v[62:65], v[248:251]\n"
      "s_add_u32 s28, s27, 2\n"
      "v_exp_f32 v188, v188\n"
      "v_cvt_pk_bf16_f32 v196, v196, v197\n"
      "v_mfma_f32_16x16x32_bf16 v[148:151], v[172:175], v[204:207], 0\n"
      "s_min_u32 m0, s28, s3\n"
      "v_exp_f32_e64 v198, v198\n"
      "v_mfma_f32_16x16x32_bf16 v[148:151], v[54:57], v[62:65], v[148:151]\n"
      "s_movrels_b32 s9, s80\n"
      "v_exp_f32 v179, v179\n"
      "v_mfma_f32_16x16x32_bf16 v[234:237], v[144:147], v[204:207], 0\n"
      "s_lshr_b32 s8, s9, 8\n"
      "v_exp_f32 v184, v184\n"
      "v_mfma_f32_16x16x32_bf16 v[234:237], v[58:61], v[62:65], v[234:237]\n"
      "s_and_b32 s9, s9, 0xff\n"
      "v_exp_f32_e64 v187, v187\n"
      "v_cvt_pk_bf16_f32 v197, v198, v199\n"
      "v_cvt_pk_bf16_f32 v64, v228, 0\n"
      "v_cvt_pk_bf16_f32 v198, v184, v185\n"
      "v_cvt_pk_bf16_f32 v199, v186, v187\n"
      "v_rcp_f32_e64 v244, v244\n"
      "v_cvt_pk_bf16_f32 v62, v188, v189\n"
      "v_mfma_f32_16x16x16_bf16 v[212:215], v[242:243], v[240:241], v[212:215]\n"
      "s_mul_i32 s10, s8, 73\n"
      "v_cvt_pk_bf16_f32 v63, v190, v191\n"
      "v_mul_f32 v251, v244, v251\n"
      "v_mul_f32 v249, v244, v249\n"
      "v_mfma_f32_16x16x32_bf16 v[184:187], v[66:69], v[196:199], 0\n"
      "ds_read_b128 v[168:171], v114 offset:18432\n"
      "v_mul_f32_e64 v248, v244, v248\n"
      "v_mfma_f32_16x16x32_bf16 v[184:187], v[66:69], v[62:65], v[184:187]\n"
      "ds_read_b128 v[192:195], v115 offset:18432\n"
      "v_exp_f32 v152, v152\n"
      "v_mul_f32 v150, v244, v150\n"
      "v_mfma_f32_16x16x32_bf16 v[188:191], v[216:219], v[196:199], 0\n"
      "s_add_u32 s10, s10, s9\n"
      "v_mul_f32 v250, v244, v250\n"
      "v_cvt_pk_bf16_f32 v248, v248, v249\n"
      "v_mfma_f32_16x16x32_bf16 v[188:191], v[50:53], v[62:65], v[188:191]\n"
      "s_mul_i32 s28, s10, 4802\n"
      "v_rcp_f32 v184, v184\n"
      "v_mfma_f32_16x16x32_bf16 v[252:255], v[172:175], v[196:199], 0\n"
      "s_add_u32 s56, s18, s28\n"
      "v_mul_f32 v148, v244, v148\n"
      "v_mul_f32 v149, v244, v149\n"
      "v_mfma_f32_16x16x32_bf16 v[252:255], v[54:57], v[62:65], v[252:255]\n"
      "s_addc_u32 s57, s19, 0\n"
      "v_exp_f32 v178, v178\n"
      "v_exp_f32 v182, v182\n"
      "v_exp_f32 v155, v155\n"
      "v_cvt_pk_bf16_f32 v249, v250, v251\n"
      "v_exp_f32_e64 v181, v181\n"
      "v_cvt_pk_bf16_f32 v250, v148, v149\n"
      "v_mfma_f32_16x16x32_bf16 v[164:167], v[144:147], v[196:199], 0\n"
      "s_and_b32 s56, s56, -16\n"
      "v_mul_f32 v151, v244, v151\n"
      "v_cvt_pk_bf16_f32 v251, v150, v151\n"
      "v_mfma_f32_16x16x32_bf16 v[164:167], v[58:61], v[62:65], v[164:167]\n"
      "ds_write_b128 v124, v[248:251]\n"
      "v_exp_f32_e64 v154, v154\n"
      "v_cndmask_b32 v201, v201, v233, s[44:45]\n"
      "v_cndmask_b32 v201, v201, v213, s[46:47]\n"
      "s_waitcnt lgkmcnt(1)\n"
      "v_mul_f32 v235, v244, v235\n"
      "v_mfma_f32_16x16x32_bf16 v[196:199], a[0:3], v[168:171], 0\n"
      "s_sub_u32 s58, s56, 110592\n"
      "v_mul_f32 v237, v244, v237\n"
      "v_mul_f32 v236, v244, v236\n"
      "v_mfma_f32_16x16x32_bf16 v[196:199], a[36:39], v[192:195], v[196:199]\n"
      "s_subb_u32 s59, s57, 0\n"
      "v_exp_f32 v177, v177\n"
      "v_exp_f32 v232, v232\n"
      "v_mul_f32 v190, v184, v190\n"
      "v_exp_f32 v176, v176\n"
      "v_mul_f32 v234, v244, v234\n"
      "v_cvt_pk_bf16_f32 v176, v176, v177\n"
      "v_exp_f32 v180, v180\n"
      "v_exp_f32 v183, v183\n"
      "v_mfma_f32_16x16x32_bf16 v[228:231], a[4:7], v[168:171], 0\n"
      "s_sub_u32 s56, s56, 73728\n"
      "v_exp_f32 v201, v201\n"
      "v_mfma_f32_16x16x32_bf16 v[228:231], a[40:43], v[192:195], v[228:231]\n"
      "s_subb_u32 s57, s57, 0\n"
      "v_cvt_pk_bf16_f32 v177, v178, v179\n"
      "v_exp_f32_e64 v153, v153\n"
      "v_cvt_pk_bf16_f32 v178, v180, v181\n"
      "v_cvt_pk_bf16_f32 v201, v201, v201\n"
      "v_cvt_pk_bf16_f32 v62, v152, v153\n"
      "v_cvt_pk_bf16_f32 v234, v234, v235\n"
      "v_cvt_pk_bf16_f32 v179, v182, v183\n"
      "v_cvt_pk_bf16_f32 v235, v236, v237\n"
      "v_mul_f32_e64 v166, v184, v166\n"
      "v_cvt_pk_bf16_f32 v63, v154, v155\n"
      "v_cvt_pk_bf16_f32 v64, v232, 0\n"
      "v_mfma_f32_16x16x32_bf16 v[148:151], v[66:69], v[176:179], 0\n"
      "s_mul_i32 s28, s8, 1373568\n"
      "v_mul_f32 v189, v184, v189\n"
      "v_mul_f32 v188, v184, v188\n"
      "v_mfma_f32_16x16x32_bf16 v[148:151], v[66:69], v[62:65], v[148:151]\n"
      "ds_write_b16 v130, v201\n"
      "v_mfma_f32_16x16x32_bf16 v[248:251], v[216:219], v[176:179], 0\n"
      "ds_read_b128 v[152:155], v131\n"
      "v_cvt_pk_bf16_f32 v188, v188, v189\n"
      "v_mfma_f32_16x16x32_bf16 v[248:251], v[50:53], v[62:65], v[248:251]\n"
      "ds_read_b128 v[180:183], v131 offset:64\n"
      "v_mul_f32 v255, v184, v255\n"
      "v_mov_b32 v232, 0\n"
      "v_mul_f32 v165, v184, v165\n"
      "v_mfma_f32_16x16x32_bf16 v[240:243], v[172:175], v[176:179], 0\n"
      "s_mul_i32 s29, s9, 2688\n"
      "v_mul_f32 v191, v184, v191\n"
      "v_cvt_pk_bf16_f32 v189, v190, v191\n"
      "v_mfma_f32_16x16x32_bf16 v[240:243], v[54:57], v[62:65], v[240:243]\n"
      "s_add_u32 s28, s28, s29\n"
      "v_mul_f32 v252, v184, v252\n"
      "v_mul_f32 v164, v184, v164\n"
      "v_mul_f32 v253, v184, v253\n"
      "v_mfma_f32_16x16x32_bf16 v[204:207], v[144:147], v[176:179], 0\n"
      "s_cmp_eq_u32 s8, 72\n"
      "v_mul_f32_e64 v254, v184, v254\n"
      "v_cvt_pk_bf16_f32 v190, v252, v253\n"
      "v_mfma_f32_16x16x32_bf16 v[204:207], v[58:61], v[62:65], v[204:207]\n"
      "s_cselect_b32 s8, 0, 100270464\n"
      "v_mul_f32_e64 v167, v184, v167\n"
      "v_cvt_pk_bf16_f32 v164, v164, v165\n"
      "v_cvt_pk_bf16_f32 v191, v254, v255\n"
      "v_cvt_pk_bf16_f32 v165, v166, v167\n"
      "s_cmp_eq_u32 s9, 72\n"
      "ds_write_b128 v124, v[188:191] offset:1024\n"
      "ds_write2st64_b64 v125, v[234:235], v[164:165] offset0:0 offset1:2\n"
      "s_waitcnt lgkmcnt(2)\n"
      "v_mfma_f32_16x16x32_bf16 v[188:191], v[152:155], v[66:69], 0\n"
      "s_cselect_b32 s9, 0, 196224\n"
      "s_add_u32 s10, s8, s9\n"
      "s_add_u32 s28, s28, s10\n"
      "v_mfma_f32_16x16x32_bf16 v[188:191], v[180:183], v[66:69], v[188:191]\n"
      "ds_write_b32 v129, v232\n"
      "v_mfma_f32_16x16x32_bf16 v[176:179], v[152:155], v[216:219], 0\n"
      "s_add_u32 s52, s16, s28\n"
      "s_addc_u32 s53, s17, 0\n"
      "s_cmp_lg_u32 s10, s11\n"
      "v_mfma_f32_16x16x32_bf16 v[176:179], v[180:183], v[50:53], v[176:179]\n"
      "s_cbranch_scc1 L_edge_15_%=\n"
      "L_eb_16_%=:\n"
      "v_mfma_f32_16x16x32_bf16 v[164:167], v[152:155], v[172:175], 0\n"
      "s_nop 0\n"
      "v_mov_b32_dpp v188, v188 quad_perm:[0,0,0,0] row_mask:0xf bank_mask:0xf\n"
      "v_mfma_f32_16x16x32_bf16 v[164:167], v[180:183], v[54:57], v[164:167]\n"
      "v_rcp_f32_e64 v188, v188\n"
      "v_mfma_f32_16x16x32_bf16 v[236:239], v[152:155], v[144:147], 0\n"
      "v_mfma_f32_16x16x32_bf16 v[236:239], v[180:183], v[58:61], v[236:239]\n"
      "s_nop 4\n"
      "v_cndmask_b32 v176, v176, v164, s[42:43]\n"
      "s_nop 1\n"
      "v_cndmask_b32 v176, v176, v236, s[44:45]\n"
      "v_mul_f32 v176, v188, v176\n"
      "v_cvt_pk_bf16_f32 v176, v176, v176\n"
      "ds_write_b16 v135, v176\n"
      "L_join_10_%=:\n"
      "s_mov_b32 s27, 1\n"
      ".p2align 6\n"
      "L_loop_%=:\n"
      "v_mfma_f32_16x16x32_bf16 v[172:175], a[8:11], v[168:171], 0\n"
      "ds_read_b128 v[236:239], v114 offset:24576\n"
      "s_add_u32 m0, s76, 0\n"
      "v_mfma_f32_16x16x32_bf16 v[172:175], a[44:47], v[192:195], v[172:175]\n"
      "ds_read_b128 v[164:167], v115 offset:24576\n"
      "v_rcp_f32 v148, v148\n"
      "v_mfma_f32_16x16x32_bf16 v[252:255], a[12:15], v[168:171], 0\n"
      "v_mul_f32 v249, v148, v249\n"
      "v_mul_f32 v248, v148, v248\n"
      "v_mfma_f32_16x16x32_bf16 v[252:255], a[48:51], v[192:195], v[252:255]\n"
      "ds_read_b128 v[216:219], v114 offset:30720\n"
      "v_cvt_pk_bf16_f32 v248, v248, v249\n"
      "v_mfma_f32_16x16x32_bf16 v[144:147], a[16:19], v[168:171], 0\n"
      "v_mul_f32 v251, v148, v251\n"
      "v_mul_f32 v250, v148, v250\n"
      "v_mfma_f32_16x16x32_bf16 v[144:147], a[52:55], v[192:195], v[144:147]\n"
      "s_waitcnt lgkmcnt(3)\n"
      "s_barrier\n"
      "v_mfma_f32_16x16x32_bf16 v[156:159], a[20:23], v[168:171], 0\n"
      "ds_read_b128 v[160:163], v128\n"
      "v_cvt_pk_bf16_f32 v249, v250, v251\n"
      "v_mfma_f32_16x16x32_bf16 v[156:159], a[56:59], v[192:195], v[156:159]\n"
      "global_load_lds_dwordx4 v116, s[52:53]\n"
      "v_mfma_f32_16x16x32_bf16 v[140:143], v[168:171], a[24:27], 0\n"
      "v_mul_f32 v240, v148, v240\n"
      "v_mul_f32 v241, v148, v241\n"
      "v_mfma_f32_16x16x32_bf16 v[140:143], v[192:195], a[60:63], v[140:143]\n"
      "v_mul_f32_e64 v242, v148, v242\n"
      "v_cvt_pk_bf16_f32 v250, v240, v241\n"
      "v_mfma_f32_16x16x32_bf16 v[224:227], v[168:171], a[28:31], 0\n"
      "s_add_u32 m0, s76, 6016\n"
      "v_mul_f32_e64 v243, v148, v243\n"
      "v_cvt_pk_bf16_f32 v251, v242, v243\n"
      "v_mfma_f32_16x16x32_bf16 v[224:227], v[192:195], a[64:67], v[224:227]\n"
      "ds_write_b128 v124, v[248:251] offset:2048\n"
      "v_mfma_f32_16x16x32_bf16 v[136:139], v[168:171], a[32:35], 0\n"
      "ds_read_b128 v[248:251], v115 offset:30720\n"
      "v_mul_f32 v204, v148, v204\n"
      "v_mfma_f32_16x16x32_bf16 v[136:139], v[192:195], a[68:71], v[136:139]\n"
      "s_waitcnt lgkmcnt(3)\n"
      "v_mul_f32_e64 v205, v148, v205\n"
      "v_cvt_pk_bf16_f32 v204, v204, v205\n"
      "v_mfma_f32_16x16x32_bf16 v[196:199], a[72:75], v[236:239], v[196:199]\n"
      "s_waitcnt lgkmcnt(2)\n"
      "v_mul_f32 v206, v148, v206\n"
      "v_mul_f32_e64 v207, v148, v207\n"
      "v_mfma_f32_16x16x32_bf16 v[196:199], a[108:111], v[164:167], v[196:199]\n"
      "global_load_lds_dwordx4 v116, s[52:53] offset:128\n"
      "v_mfma_f32_16x16x32_bf16 v[228:231], a[76:79], v[236:239], v[228:231]\n"
      "v_cvt_pk_bf16_f32 v205, v206, v207\n"
      "v_add_f32 v160, |v160|, |v161|\n"
      "v_mfma_f32_16x16x32_bf16 v[228:231], a[112:115], v[164:167], v[228:231]\n"
      "ds_write_b64 v125, v[204:205] offset:2048\n"
      "v_mfma_f32_16x16x32_bf16 v[172:175], a[80:83], v[236:239], v[172:175]\n"
      "v_add_f32 v162, |v162|, |v163|\n"
      "v_add_f32 v160, v160, v162\n"
      "v_mfma_f32_16x16x32_bf16 v[172:175], a[116:119], v[164:167], v[172:175]\n"
      "s_add_u32 m0, s76, 12032\n"
      "v_rsq_f32 v160, v160\n"
      "v_mfma_f32_16x16x32_bf16 v[252:255], a[84:87], v[236:239], v[252:255]\n"
      "v_mul_f32 v160, s41, v160\n"
      "v_mov_b32 v150, v160\n"
      "v_mfma_f32_16x16x32_bf16 v[252:255], a[120:123], v[164:167], v[252:255]\n"
      "s_nop 0\n"
      "v_permlane32_swap_b32 v160, v150\n"
      "v_mfma_f32_16x16x32_bf16 v[144:147], a[88:91], v[236:239], v[144:147]\n"
      "ds_read_b128 v[192:195], v114 offset:20480\n"
      "v_mov_b32_e64 v148, v160\n"
      "v_mfma_f32_16x16x32_bf16 v[144:147], a[124:127], v[164:167], v[144:147]\n"
      "global_load_lds_dwordx4 v116, s[52:53] offset:256\n"
      "v_mfma_f32_16x16x32_bf16 v[156:159], a[92:95], v[236:239], v[156:159]\n"
      "s_add_u32 m0, s77, 0\n"
      "v_permlane16_swap_b32 v160, v148\n"
      "v_mfma_f32_16x16x32_bf16 v[156:159], a[128:131], v[164:167], v[156:159]\n"
      "ds_read_b128 v[240:243], v115 offset:20480\n"
      "v_mul_f32 v232, v160, v232\n"
      "v_mfma_f32_16x16x32_bf16 v[140:143], v[236:239], a[96:99], v[140:143]\n"
      "v_mul_f32 v233, v160, v233\n"
      "v_cvt_pk_bf16_f32 v232, v232, v233\n"
      "v_mfma_f32_16x16x32_bf16 v[140:143], v[164:167], a[132:135], v[140:143]\n"
      "v_mul_f32 v234, v160, v234\n"
      "v_mul_f32 v235, v160, v235\n"
      "v_mfma_f32_16x16x32_bf16 v[224:227], v[236:239], a[100:103], v[224:227]\n"
      "v_mul_f32_e64 v152, v160, v152\n"
      "v_cvt_pk_bf16_f32 v233, v234, v235\n"
      "v_mfma_f32_16x16x32_bf16 v[224:227], v[164:167], a[136:139], v[224:227]\n"
      "s_waitcnt lgkmcnt(2)\n"
      "v_mul_f32 v153, v160, v153\n"
      "v_cvt_pk_bf16_f32 v234, v152, v153\n"
      "v_mfma_f32_16x16x32_bf16 v[136:139], v[236:239], a[104:107], v[136:139]\n"
      "v_mul_f32 v154, v160, v154\n"
      "v_mul_f32 v155, v160, v155\n"
      "v_mfma_f32_16x16x32_bf16 v[136:139], v[164:167], a[140:143], v[136:139]\n"
      "v_cvt_pk_bf16_f32 v235, v154, v155\n"
      "v_mul_f32_e64 v180, v160, v180\n"
      "v_mfma_f32_16x16x32_bf16 v[196:199], a[144:147], v[216:219], v[196:199]\n"
      "global_load_lds_dwordx4 v117, s[52:53]\n"
      "v_mfma_f32_16x16x32_bf16 v[196:199], a[180:183], v[248:251], v[196:199]\n"
      "v_mul_f32_e64 v181, v160, v181\n"
      "v_cvt_pk_bf16_f32 v180, v180, v181\n"
      "v_mfma_f32_16x16x32_bf16 v[228:231], a[148:151], v[216:219], v[228:231]\n"
      "s_waitcnt lgkmcnt(0)\n"
      "v_mul_f32 v182, v160, v182\n"
      "v_mul_f32_e64 v183, v160, v183\n"
      "v_mfma_f32_16x16x32_bf16 v[228:231], a[184:187], v[248:251], v[228:231]\n"
      "ds_write_b128 v126, v[232:235] offset:18432\n"
      "v_mfma_f32_16x16x32_bf16 v[172:175], a[152:155], v[216:219], v[172:175]\n"
      "v_cvt_pk_bf16_f32 v181, v182, v183\n"
      "v_mul_f32 v176, v148, v176\n"
      "v_mfma_f32_16x16x32_bf16 v[172:175], a[188:191], v[248:251], v[172:175]\n"
      "v_mul_f32 v177, v148, v177\n"
      "v_cvt_pk_bf16_f32 v176, v176, v177\n"
      "v_mfma_f32_16x16x32_bf16 v[252:255], a[156:159], v[216:219], v[252:255]\n"
      "v_mul_f32 v178, v148, v178\n"
      "v_mul_f32 v179, v148, v179\n"
      "v_mfma_f32_16x16x32_bf16 v[252:255], a[192:195], v[248:251], v[252:255]\n"
      "s_add_u32 m0, s78, 0\n"
      "v_mul_f32 v244, v148, v244\n"
      "v_cvt_pk_bf16_f32 v177, v178, v179\n"
      "v_mfma_f32_16x16x32_bf16 v[144:147], a[160:163], v[216:219], v[144:147]\n"
      "ds_read_b128 v[232:235], v114 offset:26624\n"
      "v_mul_f32_e64 v245, v148, v245\n"
      "v_mfma_f32_16x16x32_bf16 v[144:147], a[196:199], v[248:251], v[144:147]\n"
      "ds_read_b128 v[152:155], v115 offset:26624\n"
      "v_cvt_pk_bf16_f32 v178, v244, v245\n"
      "v_mfma_f32_16x16x32_bf16 v[156:159], a[164:167], v[216:219], v[156:159]\n"
      "v_mul_f32 v246, v148, v246\n"
      "v_mul_f32 v247, v148, v247\n"
      "v_mfma_f32_16x16x32_bf16 v[156:159], a[200:203], v[248:251], v[156:159]\n"
      "s_mov_b32 exec_hi, 0\n"
      "global_load_lds_dwordx4 v118, s[52:53]\n"
      "s_mov_b32 exec_hi, -1\n"
      "v_mfma_f32_16x16x32_bf16 v[140:143], v[216:219], a[168:171], v[140:143]\n"
      "s_add_u32 m0, s76, 110592\n"
      "v_cvt_pk_bf16_f32 v179, v246, v247\n"
      "v_mul_f32_e64 v200, v148, v200\n"
      "v_mfma_f32_16x16x32_bf16 v[140:143], v[248:251], a[204:207], v[140:143]\n"
      "ds_write_b128 v126, v[176:179] offset:20480\n"
      "v_mfma_f32_16x16x32_bf16 v[224:227], v[216:219], a[172:175], v[224:227]\n"
      "v_mul_f32_e64 v201, v148, v201\n"
      "v_cvt_pk_bf16_f32 v200, v200, v201\n"
      "v_mfma_f32_16x16x32_bf16 v[224:227], v[248:251], a[208:211], v[224:227]\n"
      "v_mul_f32 v203, v148, v203\n"
      "v_mul_f32_e64 v202, v148, v202\n"
      "v_mfma_f32_16x16x32_bf16 v[136:139], v[216:219], a[176:179], v[136:139]\n"
      "s_waitcnt lgkmcnt(1)\n"
      "v_cvt_pk_bf16_f32 v201, v202, v203\n"
      "v_mul_f32_e64 v184, v150, v184\n"
      "v_mfma_f32_16x16x32_bf16 v[136:139], v[248:251], a[212:215], v[136:139]\n"
      "ds_write2st64_b64 v127, v[180:181], v[200:201] offset0:36 offset1:40\n"
      "v_mfma_f32_16x16x32_bf16 v[200:203], a[0:3], v[192:195], 0\n"
      "global_load_lds_dwordx4 v119, s[56:57]\n"
      "v_mfma_f32_16x16x32_bf16 v[200:203], a[36:39], v[240:243], v[200:203]\n"
      "s_add_u32 m0, s79, 0\n"
      "v_mul_f32 v185, v150, v185\n"
      "v_cvt_pk_bf16_f32 v184, v184, v185\n"
      "v_mfma_f32_16x16x32_bf16 v[180:183], a[4:7], v[192:195], 0\n"
      "v_mul_f32 v187, v150, v187\n"
      "v_mul_f32 v186, v150, v186\n"
      "v_mfma_f32_16x16x32_bf16 v[180:183], a[40:43], v[240:243], v[180:183]\n"
      "s_sub_u32 s28, s27, 3\n"
      "v_mul_f32 v212, v150, v212\n"
      "v_cvt_pk_bf16_f32 v185, v186, v187\n"
      "v_mfma_f32_16x16x32_bf16 v[244:247], a[8:11], v[192:195], 0\n"
      "s_cselect_b64 s[54:55], s[12:13], s[22:23]\n"
      "v_mul_f32 v213, v150, v213\n"
      "v_cvt_pk_bf16_f32 v186, v212, v213\n"
      "v_mfma_f32_16x16x32_bf16 v[244:247], a[44:47], v[240:243], v[244:247]\n"
      "v_mul_f32 v214, v150, v214\n"
      "v_mul_f32 v215, v150, v215\n"
      "v_mfma_f32_16x16x32_bf16 v[248:251], a[12:15], v[192:195], 0\n"
      "ds_read_b128 v[168:171], v114 offset:32768\n"
      "v_cvt_pk_bf16_f32 v187, v214, v215\n"
      "v_mfma_f32_16x16x32_bf16 v[248:251], a[48:51], v[240:243], v[248:251]\n"
      "ds_write_b128 v126, v[184:187] offset:22528\n"
      "v_mfma_f32_16x16x32_bf16 v[176:179], a[16:19], v[192:195], 0\n"
      "global_load_lds_dword v122, s[58:59]\n"
      "v_mfma_f32_16x16x32_bf16 v[176:179], a[52:55], v[240:243], v[176:179]\n"
      "ds_read_b128 v[212:215], v115 offset:32768\n"
      "v_mul_f32 v188, v150, v188\n"
      "v_mfma_f32_16x16x32_bf16 v[184:187], a[20:23], v[192:195], 0\n"
      "s_max_i32 m0, s28, 0\n"
      "v_mul_f32_e64 v189, v150, v189\n"
      "v_cvt_pk_bf16_f32 v188, v188, v189\n"
      "v_mfma_f32_16x16x32_bf16 v[184:187], a[56:59], v[240:243], v[184:187]\n"
      "s_movrels_b32 s9, s80\n"
      "v_mul_f32 v190, v150, v190\n"
      "v_mul_f32 v191, v150, v191\n"
      "v_mfma_f32_16x16x32_bf16 v[148:151], v[192:195], a[24:27], 0\n"
      "s_lshr_b32 s8, s9, 8\n"
      "v_fma_f32 v236, v196, v196, s38\n"
      "v_cvt_pk_bf16_f32 v189, v190, v191\n"
      "v_mfma_f32_16x16x32_bf16 v[148:151], v[240:243], a[60:63], v[148:151]\n"
      "ds_write_b64 v127, v[188:189] offset:22528\n"
      "v_mfma_f32_16x16x32_bf16 v[188:191], v[192:195], a[28:31], 0\n"
      "s_and_b32 s9, s9, 0xff\n"
      "v_fma_f32 v238, v252, v252, s38\n"
      "v_fmac_f32_e64 v236, v197, v197\n"
      "v_mfma_f32_16x16x32_bf16 v[188:191], v[240:243], a[64:67], v[188:191]\n"
      "s_mul_i32 s28, s8, 1373568\n"
      "v_fmac_f32 v238, v253, v253\n"
      "v_fmac_f32 v236, v198, v198\n"
      "v_mfma_f32_16x16x32_bf16 v[204:207], v[192:195], a[32:35], 0\n"
      "s_mul_i32 s29, s9, 2688\n"
      "v_fmac_f32 v238, v254, v254\n"
      "v_fmac_f32 v236, v199, v199\n"
      "v_mfma_f32_16x16x32_bf16 v[204:207], v[240:243], a[68:71], v[204:207]\n"
      "s_waitcnt lgkmcnt(1)\n"
      "v_fmac_f32 v238, v255, v255\n"
      "v_fmac_f32 v236, v228, v228\n"
      "v_mfma_f32_16x16x32_bf16 v[200:203], a[72:75], v[232:235], v[200:203]\n"
      "s_add_u32 s28, s28, s29\n"
      "v_fmac_f32 v236, v229, v229\n"
      "v_fmac_f32 v238, v144, v144\n"
      "v_mfma_f32_16x16x32_bf16 v[200:203], a[108:111], v[152:155], v[200:203]\n"
      "s_cmp_eq_u32 s8, 72\n"
      "v_fmac_f32 v238, v145, v145\n"
      "v_fmac_f32 v236, v230, v230\n"
      "v_mfma_f32_16x16x32_bf16 v[180:183], a[76:79], v[232:235], v[180:183]\n"
      "s_cselect_b32 s8, 0, 100270464\n"
      "v_fmac_f32 v238, v146, v146\n"
      "v_fmac_f32 v236, v231, v231\n"
      "v_mfma_f32_16x16x32_bf16 v[180:183], a[112:115], v[152:155], v[180:183]\n"
      "s_cmp_eq_u32 s9, 72\n"
      "v_fmac_f32 v236, v172, v172\n"
      "v_fmac_f32 v238, v147, v147\n"
      "v_mfma_f32_16x16x32_bf16 v[244:247], a[80:83], v[232:235], v[244:247]\n"
      "ds_read_b128 v[240:243], v114 offset:22528\n"
      "v_fmac_f32_e64 v238, v156, v156\n"
      "v_mfma_f32_16x16x32_bf16 v[244:247], a[116:119], v[152:155], v[244:247]\n"
      "s_cselect_b32 s9, 0, 196224\n"
      "v_fmac_f32 v236, v173, v173\n"
      "v_fmac_f32 v238, v157, v157\n"
      "v_mfma_f32_16x16x32_bf16 v[248:251], a[84:87], v[232:235], v[248:251]\n"
      "ds_read_b128 v[192:195], v115 offset:22528\n"
      "v_fmac_f32 v236, v174, v174\n"
      "v_mfma_f32_16x16x32_bf16 v[248:251], a[120:123], v[152:155], v[248:251]\n"
      "s_add_u32 s10, s8, s9\n"
      "v_fmac_f32 v236, v175, v175\n"
      "v_fmac_f32 v238, v158, v158\n"
      "v_mfma_f32_16x16x32_bf16 v[176:179], a[88:91], v[232:235], v[176:179]\n"
      "s_add_u32 s28, s28, s10\n"
      "v_cvt_pk_bf16_f32 v140, v140, v141\n"
      "v_fmac_f32 v238, v159, v159\n"
      "v_mfma_f32_16x16x32_bf16 v[176:179], a[124:127], v[152:155], v[176:179]\n"
      "s_add_u32 s54, s54, s28\n"
      "v_permlane16_swap_b32 v236, v238\n"
      "v_mfma_f32_16x16x32_bf16 v[184:187], a[92:95], v[232:235], v[184:187]\n"
      "s_addc_u32 s55, s55, 0\n"
      "v_add_f32 v236, v236, v238\n"
      "v_mov_b32 v238, v236\n"
      "v_mfma_f32_16x16x32_bf16 v[184:187], a[128:131], v[152:155], v[184:187]\n"
      "s_cmp_lg_u32 s10, s11\n"
      "v_permlane32_swap_b32 v236, v238\n"
      "v_add_f32 v236, v236, v238\n"
      "v_mfma_f32_16x16x32_bf16 v[148:151], v[232:235], a[96:99], v[148:151]\n"
      "s_cbranch_scc1 L_edge_17_%=\n"
      "L_eb_18_%=:\n"
      "v_mfma_f32_16x16x32_bf16 v[148:151], v[152:155], a[132:135], v[148:151]\n"
      "v_rsq_f32 v236, v236\n"
      "v_mfma_f32_16x16x32_bf16 v[188:191], v[232:235], a[100:103], v[188:191]\n"
      "s_mov_b32 s5, s4\n"
      "v_mov_b32 v238, v236\n"
      "v_cvt_pk_bf16_f32 v141, v142, v143\n"
      "v_mfma_f32_16x16x32_bf16 v[188:191], v[152:155], a[136:139], v[188:191]\n"
      "s_add_u32 s4, s27, 1\n"
      "v_permlane16_swap_b32 v236, v238\n"
      "v_mfma_f32_16x16x32_bf16 v[204:207], v[232:235], a[104:107], v[204:207]\n"
      "s_waitcnt lgkmcnt(0)\n"
      "v_mul_f32 v252, v238, v252\n"
      "v_mul_f32_e64 v253, v238, v253\n"
      "v_mfma_f32_16x16x32_bf16 v[204:207], v[152:155], a[140:143], v[204:207]\n"
      "s_lshl_b32 s4, s4, 30\n"
      "v_cvt_pk_bf16_f32 v252, v252, v253\n"
      "v_mul_f32 v254, v238, v254\n"
      "v_mfma_f32_16x16x32_bf16 v[200:203], a[144:147], v[168:171], v[200:203]\n"
      "v_mul_f32 v255, v238, v255\n"
      "v_cvt_pk_bf16_f32 v253, v254, v255\n"
      "v_mfma_f32_16x16x32_bf16 v[200:203], a[180:183], v[212:215], v[200:203]\n"
      "ds_read_b128 v[232:235], v119\n"
      "v_mul_f32 v144, v238, v144\n"
      "v_mfma_f32_16x16x32_bf16 v[180:183], a[148:151], v[168:171], v[180:183]\n"
      "v_mul_f32 v145, v238, v145\n"
      "v_cvt_pk_bf16_f32 v254, v144, v145\n"
      "v_mfma_f32_16x16x32_bf16 v[180:183], a[184:187], v[212:215], v[180:183]\n"
      "v_mul_f32 v146, v238, v146\n"
      "v_mul_f32 v147, v238, v147\n"
      "v_mfma_f32_16x16x32_bf16 v[244:247], a[152:155], v[168:171], v[244:247]\n"
      "s_and_b32 s4, s4, 0x80000000\n"
      "v_cvt_pk_bf16_f32 v255, v146, v147\n"
      "v_mul_f32 v156, v238, v156\n"
      "v_mfma_f32_16x16x32_bf16 v[244:247], a[188:191], v[212:215], v[244:247]\n"
      "v_mul_f32_e64 v157, v238, v157\n"
      "v_cvt_pk_bf16_f32 v34, v156, v157\n"
      "v_mfma_f32_16x16x32_bf16 v[248:251], a[156:159], v[168:171], v[248:251]\n"
      "ds_read_b128 v[164:167], v114 offset:28672\n"
      "v_mul_f32 v158, v238, v158\n"
      "v_mfma_f32_16x16x32_bf16 v[248:251], a[192:195], v[212:215], v[248:251]\n"
      "v_mul_f32 v159, v238, v159\n"
      "v_cvt_pk_bf16_f32 v35, v158, v159\n"
      "v_mfma_f32_16x16x32_bf16 v[176:179], a[160:163], v[168:171], v[176:179]\n"
      "ds_read_b128 v[152:155], v115 offset:28672\n"
      "v_mul_f32 v236, s36, v236\n"
      "v_mfma_f32_16x16x32_bf16 v[176:179], a[196:199], v[212:215], v[176:179]\n"
      "s_waitcnt lgkmcnt(2)\n"
      "v_mul_f32 v196, v236, v196\n"
      "v_mul_f32 v197, v236, v197\n"
      "v_mfma_f32_16x16x32_bf16 v[184:187], a[164:167], v[168:171], v[184:187]\n"
      "global_store_dwordx4 v116, v[232:235], s[54:55] sc1\n"
      "v_mfma_f32_16x16x32_bf16 v[184:187], a[200:203], v[212:215], v[184:187]\n"
      "v_mul_f32 v198, v236, v198\n"
      "v_cvt_pk_bf16_f32 v196, v196, v197\n"
      "v_mfma_f32_16x16x32_bf16 v[148:151], v[168:171], a[168:171], v[148:151]\n"
      "v_mul_f32 v199, v236, v199\n"
      "v_cvt_pk_bf16_f32 v197, v198, v199\n"
      "v_mfma_f32_16x16x32_bf16 v[148:151], v[212:215], a[204:207], v[148:151]\n"
      "v_mul_f32 v229, v236, v229\n"
      "v_mul_f32 v228, v236, v228\n"
      "v_mfma_f32_16x16x32_bf16 v[188:191], v[168:171], a[172:175], v[188:191]\n"
      "v_cvt_pk_bf16_f32 v198, v228, v229\n"
      "v_mul_f32 v230, v236, v230\n"
      "v_mfma_f32_16x16x32_bf16 v[188:191], v[212:215], a[208:211], v[188:191]\n"
      "v_mul_f32 v231, v236, v231\n"
      "v_cvt_pk_bf16_f32 v199, v230, v231\n"
      "v_mfma_f32_16x16x32_bf16 v[204:207], v[168:171], a[176:179], v[204:207]\n"
      "v_mul_f32 v172, v236, v172\n"
      "v_mul_f32 v173, v236, v173\n"
      "v_mfma_f32_16x16x32_bf16 v[204:207], v[212:215], a[212:215], v[204:207]\n"
      "v_mul_f32_e64 v174, v236, v174\n"
      "v_cvt_pk_bf16_f32 v32, v172, v173\n"
      "v_mfma_f32_16x16x32_bf16 v[168:171], a[0:3], v[240:243], 0\n"
      "v_mul_f32_e64 v175, v236, v175\n"
      "v_cvt_pk_bf16_f32 v33, v174, v175\n"
      "v_mfma_f32_16x16x32_bf16 v[168:171], a[36:39], v[192:195], v[168:171]\n"
      "s_nop 0\n"
      "v_cvt_pk_bf16_f32 v143, v150, v151\n"
      "v_cvt_pk_bf16_f32 v142, v148, v149\n"
      "v_mfma_f32_16x16x32_bf16 v[212:215], a[4:7], v[240:243], 0\n"
      "s_waitcnt lgkmcnt(0)\n"
      "v_cvt_pk_bf16_f32 v224, v224, v225\n"
      "v_cvt_pk_bf16_f32 v225, v226, v227\n"
      "v_mfma_f32_16x16x32_bf16 v[212:215], a[40:43], v[192:195], v[212:215]\n"
      "ds_read_b128 v[232:235], v119 offset:6144\n"
      "v_cvt_pk_bf16_f32 v226, v188, v189\n"
      "v_mfma_f32_16x16x32_bf16 v[148:151], a[8:11], v[240:243], 0\n"
      "v_cvt_pk_bf16_f32 v227, v190, v191\n"
      "v_cvt_pk_bf16_f32 v136, v136, v137\n"
      "v_mfma_f32_16x16x32_bf16 v[148:151], a[44:47], v[192:195], v[148:151]\n"
      "v_cvt_pk_bf16_f32 v137, v138, v139\n"
      "v_cvt_pk_bf16_f32 v138, v204, v205\n"
      "v_mfma_f32_16x16x32_bf16 v[188:191], a[12:15], v[240:243], 0\n"
      "v_fma_f32 v145, v200, v200, s38\n"
      "v_cvt_pk_bf16_f32 v139, v206, v207\n"
      "v_mfma_f32_16x16x32_bf16 v[188:191], a[48:51], v[192:195], v[188:191]\n"
      "v_fmac_f32_e64 v145, v201, v201\n"
      "v_fma_f32 v146, v248, v248, s38\n"
      "v_mfma_f32_16x16x32_bf16 v[228:231], a[16:19], v[240:243], 0\n"
      "v_fmac_f32 v145, v202, v202\n"
      "v_fmac_f32 v146, v249, v249\n"
      "v_mfma_f32_16x16x32_bf16 v[228:231], a[52:55], v[192:195], v[228:231]\n"
      "v_fmac_f32 v145, v203, v203\n"
      "v_fmac_f32 v146, v250, v250\n"
      "v_mfma_f32_16x16x32_bf16 v[204:207], a[20:23], v[240:243], 0\n"
      "v_fmac_f32 v146, v251, v251\n"
      "v_fmac_f32 v145, v180, v180\n"
      "v_mfma_f32_16x16x32_bf16 v[204:207], a[56:59], v[192:195], v[204:207]\n"
      "s_waitcnt lgkmcnt(0)\n"
      "v_fmac_f32 v146, v176, v176\n"
      "v_fmac_f32_e64 v145, v181, v181\n"
      "v_mfma_f32_16x16x32_bf16 v[172:175], v[240:243], a[24:27], 0\n"
      "global_store_dwordx4 v116, v[232:235], s[54:55] offset:128 sc1\n"
      "v_mfma_f32_16x16x32_bf16 v[172:175], v[192:195], a[60:63], v[172:175]\n"
      "ds_read_b128 v[156:159], v114 offset:34816\n"
      "v_fmac_f32 v146, v177, v177\n"
      "v_mfma_f32_16x16x32_bf16 v[236:239], v[240:243], a[28:31], 0\n"
      "v_fmac_f32 v146, v178, v178\n"
      "v_fmac_f32 v146, v179, v179\n"
      "v_mfma_f32_16x16x32_bf16 v[236:239], v[192:195], a[64:67], v[236:239]\n"
      "v_fmac_f32 v145, v182, v182\n"
      "v_fmac_f32 v145, v183, v183\n"
      "v_mfma_f32_16x16x32_bf16 v[220:223], v[240:243], a[32:35], 0\n"
      "v_fmac_f32 v146, v184, v184\n"
      "v_fmac_f32 v145, v244, v244\n"
      "v_mfma_f32_16x16x32_bf16 v[220:223], v[192:195], a[68:71], v[220:223]\n"
      "v_fmac_f32 v145, v245, v245\n"
      "v_fmac_f32_e64 v145, v246, v246\n"
      "v_mfma_f32_16x16x32_bf16 v[168:171], a[72:75], v[164:167], v[168:171]\n"
      "ds_read_b128 v[232:235], v115 offset:34816\n"
      "v_fmac_f32 v146, v185, v185\n"
      "v_mfma_f32_16x16x32_bf16 v[168:171], a[108:111], v[152:155], v[168:171]\n"
      "v_fmac_f32 v146, v186, v186\n"
      "v_mfma_f32_16x16x32_bf16 v[212:215], a[76:79], v[164:167], v[212:215]\n"
      "v_fmac_f32 v146, v187, v187\n"
      "v_fmac_f32 v145, v247, v247\n"
      "v_mfma_f32_16x16x32_bf16 v[212:215], a[112:115], v[152:155], v[212:215]\n"
      "s_nop 0\n"
      "v_permlane16_swap_b32 v145, v146\n"
      "v_mfma_f32_16x16x32_bf16 v[148:151], a[80:83], v[164:167], v[148:151]\n"
      "v_add_f32 v145, v145, v146\n"
      "v_mov_b32 v146, v145\n"
      "v_mfma_f32_16x16x32_bf16 v[148:151], a[116:119], v[152:155], v[148:151]\n"
      "s_nop 0\n"
      "v_permlane32_swap_b32 v145, v146\n"
      "v_mfma_f32_16x16x32_bf16 v[188:191], a[84:87], v[164:167], v[188:191]\n"
      "ds_read_b128 v[240:243], v132 offset:1200\n"
      "v_add_f32 v145, v145, v146\n"
      "v_mfma_f32_16x16x32_bf16 v[188:191], a[120:123], v[152:155], v[188:191]\n"
      "v_rsq_f32 v145, v145\n"
      "v_mfma_f32_16x16x32_bf16 v[228:231], a[88:91], v[164:167], v[228:231]\n"
      "v_mov_b32 v146, v145\n"
      "v_mfma_f32_16x16x32_bf16 v[228:231], a[124:127], v[152:155], v[228:231]\n"
      "s_nop 0\n"
      "v_permlane16_swap_b32 v145, v146\n"
      "v_mfma_f32_16x16x32_bf16 v[204:207], a[92:95], v[164:167], v[204:207]\n"
      "v_mul_f32 v250, v146, v250\n"
      "v_mul_f32 v248, v146, v248\n"
      "v_mfma_f32_16x16x32_bf16 v[204:207], a[128:131], v[152:155], v[204:207]\n"
      "v_mul_f32 v251, v146, v251\n"
      "v_mul_f32 v249, v146, v249\n"
      "v_mfma_f32_16x16x32_bf16 v[172:175], v[164:167], a[96:99], v[172:175]\n"
      "s_waitcnt lgkmcnt(1)\n"
      "v_mul_f32_e64 v177, v146, v177\n"
      "v_cvt_pk_bf16_f32 v248, v248, v249\n"
      "v_mfma_f32_16x16x32_bf16 v[172:175], v[152:155], a[132:135], v[172:175]\n"
      "ds_read_b64 v[192:193], v132 offset:1192\n"
      "v_cvt_pk_bf16_f32 v249, v250, v251\n"
      "v_mfma_f32_16x16x32_bf16 v[236:239], v[164:167], a[100:103], v[236:239]\n"
      "ds_read_u16 v52, v134 offset:1168\n"
      "v_mul_f32_e64 v176, v146, v176\n"
      "v_mfma_f32_16x16x32_bf16 v[236:239], v[152:155], a[136:139], v[236:239]\n"
      "ds_read_u16 v56, v134 offset:1200\n"
      "v_mul_f32_e64 v178, v146, v178\n"
      "v_mfma_f32_16x16x32_bf16 v[220:223], v[164:167], a[104:107], v[220:223]\n"
      "ds_read_b128 v[216:219], v133 offset:1168\n"
      "v_cvt_pk_bf16_f32 v250, v176, v177\n"
      "v_mfma_f32_16x16x32_bf16 v[220:223], v[152:155], a[140:143], v[220:223]\n"
      "ds_read_b64 v[194:195], v133 offset:1184\n"
      "v_mfma_f32_16x16x32_bf16 v[168:171], a[144:147], v[156:159], v[168:171]\n"
      "ds_read_u16 v60, v134 offset:1232\n"
      "v_mul_f32_e64 v179, v146, v179\n"
      "v_mfma_f32_16x16x32_bf16 v[168:171], a[180:183], v[232:235], v[168:171]\n"
      "ds_read_b128 v[164:167], v123\n"
      "v_cvt_pk_bf16_f32 v251, v178, v179\n"
      "v_mfma_f32_16x16x32_bf16 v[212:215], a[148:151], v[156:159], v[212:215]\n"
      "v_mul_f32 v185, v146, v185\n"
      "v_mul_f32 v184, v146, v184\n"
      "v_mfma_f32_16x16x32_bf16 v[212:215], a[184:187], v[232:235], v[212:215]\n"
      "v_mul_f32_e64 v186, v146, v186\n"
      "v_cvt_pk_bf16_f32 v40, v184, v185\n"
      "v_mfma_f32_16x16x32_bf16 v[148:151], a[152:155], v[156:159], v[148:151]\n"
      "ds_read_b128 v[176:179], v123 offset:3072\n"
      "v_mul_f32 v187, v146, v187\n"
      "v_mfma_f32_16x16x32_bf16 v[148:151], a[188:191], v[232:235], v[148:151]\n"
      "v_cvt_pk_bf16_f32 v41, v186, v187\n"
      "v_mul_f32 v145, s36, v145\n"
      "v_mfma_f32_16x16x32_bf16 v[188:191], a[156:159], v[156:159], v[188:191]\n"
      "v_mul_f32 v201, v145, v201\n"
      "v_mul_f32 v200, v145, v200\n"
      "v_mfma_f32_16x16x32_bf16 v[188:191], a[192:195], v[232:235], v[188:191]\n"
      "v_cvt_pk_bf16_f32 v200, v200, v201\n"
      "v_mul_f32 v202, v145, v202\n"
      "v_mfma_f32_16x16x32_bf16 v[228:231], a[160:163], v[156:159], v[228:231]\n"
      "v_mul_f32 v203, v145, v203\n"
      "v_cvt_pk_bf16_f32 v201, v202, v203\n"
      "v_mfma_f32_16x16x32_bf16 v[228:231], a[196:199], v[232:235], v[228:231]\n"
      "v_mul_f32 v181, v145, v181\n"
      "v_mul_f32 v180, v145, v180\n"
      "v_mfma_f32_16x16x32_bf16 v[204:207], a[164:167], v[156:159], v[204:207]\n"
      "ds_read_b128 v[160:163], v123 offset:9216\n"
      "v_cvt_pk_bf16_f32 v202, v180, v181\n"
      "v_mfma_f32_16x16x32_bf16 v[204:207], a[200:203], v[232:235], v[204:207]\n"
      "ds_read_b128 v[184:187], v123 offset:6144\n"
      "v_mul_f32 v244, v145, v244\n"
      "v_mfma_f32_16x16x32_bf16 v[172:175], v[156:159], a[168:171], v[172:175]\n"
      "v_mul_f32 v183, v145, v183\n"
      "v_mul_f32_e64 v182, v145, v182\n"
      "v_mfma_f32_16x16x32_bf16 v[172:175], v[232:235], a[204:207], v[172:175]\n"
      "v_mul_f32_e64 v245, v145, v245\n"
      "v_cvt_pk_bf16_f32 v203, v182, v183\n"
      "v_mfma_f32_16x16x32_bf16 v[236:239], v[156:159], a[172:175], v[236:239]\n"
      "v_mul_f32 v246, v145, v246\n"
      "v_mfma_f32_16x16x32_bf16 v[236:239], v[232:235], a[208:211], v[236:239]\n"
      "s_or_b64 s[34:35], s[48:49], s[50:51]\n"
      "v_cvt_pk_bf16_f32 v38, v244, v245\n"
      "v_mul_f32 v247, v145, v247\n"
      "v_mfma_f32_16x16x32_bf16 v[220:223], v[156:159], a[176:179], v[220:223]\n"
      "s_waitcnt lgkmcnt(2)\n"
      "v_fma_f32 v156, v188, v188, s38\n"
      "v_mfma_f32_16x16x32_bf16 v[220:223], v[232:235], a[212:215], v[220:223]\n"
      "v_fma_f32 v158, v168, v168, s38\n"
      "v_fmac_f32 v158, v169, v169\n"
      "v_mfma_f32_16x16x32_bf16 v[180:183], a[216:219], v[164:167], 0\n"
      "v_fmac_f32 v156, v189, v189\n"
      "v_cvt_pk_bf16_f32 v39, v246, v247\n"
      "v_mfma_f32_16x16x32_bf16 v[180:183], a[228:231], v[176:179], v[180:183]\n"
      "v_fmac_f32 v156, v190, v190\n"
      "v_fmac_f32 v158, v170, v170\n"
      "v_mfma_f32_16x16x32_bf16 v[232:235], a[220:223], v[164:167], 0\n"
      "v_fmac_f32 v156, v191, v191\n"
      "v_fmac_f32 v158, v171, v171\n"
      "v_mfma_f32_16x16x32_bf16 v[232:235], a[232:235], v[176:179], v[232:235]\n"
      "v_fmac_f32 v158, v212, v212\n"
      "v_fmac_f32 v156, v228, v228\n"
      "v_mfma_f32_16x16x32_bf16 v[152:155], a[224:227], v[164:167], 0\n"
      "v_fmac_f32 v156, v229, v229\n"
      "v_fmac_f32 v158, v213, v213\n"
      "v_mfma_f32_16x16x32_bf16 v[152:155], a[236:239], v[176:179], v[152:155]\n"
      "s_waitcnt lgkmcnt(0)\n"
      "v_fmac_f32 v158, v214, v214\n"
      "v_fmac_f32_e64 v156, v230, v230\n"
      "v_mfma_f32_16x16x32_bf16 v[180:183], a[240:243], v[184:187], v[180:183]\n"
      "ds_read_b128 v[244:247], v123 offset:12288\n"
      "v_fmac_f32 v158, v215, v215\n"
      "v_mfma_f32_16x16x32_bf16 v[180:183], a[252:255], v[160:163], v[180:183]\n"
      "ds_read_b128 v[176:179], v123 offset:15360\n"
      "v_fmac_f32 v156, v231, v231\n"
      "v_mfma_f32_16x16x32_bf16 v[232:235], a[244:247], v[184:187], v[232:235]\n"
      "v_fmac_f32 v156, v204, v204\n"
      "v_fmac_f32 v158, v148, v148\n"
      "v_mfma_f32_16x16x32_bf16 v[232:235], v[0:3], v[160:163], v[232:235]\n"
      "v_fmac_f32 v156, v205, v205\n"
      "v_fmac_f32 v158, v149, v149\n"
      "v_mfma_f32_16x16x32_bf16 v[152:155], a[248:251], v[184:187], v[152:155]\n"
      "v_fmac_f32 v156, v206, v206\n"
      "v_fmac_f32 v158, v150, v150\n"
      "v_mfma_f32_16x16x32_bf16 v[152:155], v[4:7], v[160:163], v[152:155]\n"
      "v_fmac_f32 v156, v207, v207\n"
      "v_fmac_f32 v158, v151, v151\n"
      "v_mfma_f32_16x16x32_bf16 v[184:187], v[252:255], v[196:199], v[78:81]\n"
      "s_nop 0\n"
      "v_permlane16_swap_b32 v158, v156\n"
      "v_mfma_f32_16x16x32_bf16 v[184:187], v[34:37], v[32:35], v[184:187]\n"
      "v_add_f32 v158, v158, v156\n"
      "v_mov_b32 v156, v158\n"
      "v_add_u32 v132, 2336, v132\n"
      "v_add_u32 v133, 2336, v133\n"
      "v_permlane32_swap_b32 v158, v156\n"
      "v_add_f32 v158, v158, v156\n"
      "v_rsq_f32 v158, v158\n"
      "s_waitcnt lgkmcnt(0)\n"
      "v_mfma_f32_16x16x32_bf16 v[180:183], v[8:11], v[244:247], v[180:183]\n"
      "v_add_u32 v134, 2336, v134\n"
      "v_mov_b32 v156, v158\n"
      "v_mfma_f32_16x16x32_bf16 v[180:183], v[20:23], v[176:179], v[180:183]\n"
      "s_nop 0\n"
      "v_permlane16_swap_b32 v158, v156\n"
      "v_mul_f32 v190, v156, v190\n"
      "v_mfma_f32_16x16x32_bf16 v[232:235], v[12:15], v[244:247], v[232:235]\n"
      "v_mul_f32 v189, v156, v189\n"
      "v_mul_f32 v188, v156, v188\n"
      "v_mfma_f32_16x16x32_bf16 v[232:235], v[24:27], v[176:179], v[232:235]\n"
      "v_mul_f32 v207, v156, v207\n"
      "v_cvt_pk_bf16_f32 v188, v188, v189\n"
      "v_mfma_f32_16x16x32_bf16 v[152:155], v[16:19], v[244:247], v[152:155]\n"
      "v_mul_f32 v191, v156, v191\n"
      "v_cvt_pk_bf16_f32 v189, v190, v191\n"
      "v_mfma_f32_16x16x32_bf16 v[152:155], v[28:31], v[176:179], v[152:155]\n"
      "v_mul_f32 v231, v156, v231\n"
      "v_mul_f32 v229, v156, v229\n"
      "v_mul_f32_e64 v228, v156, v228\n"
      "v_mfma_f32_16x16x32_bf16 v[244:247], v[248:251], v[196:199], v[82:85]\n"
      "v_cvt_pk_bf16_f32 v190, v228, v229\n"
      "v_mul_f32 v230, v156, v230\n"
      "v_mfma_f32_16x16x32_bf16 v[244:247], v[40:43], v[32:35], v[244:247]\n"
      "v_cvt_pk_bf16_f32 v191, v230, v231\n"
      "v_mul_f32 v158, s36, v158\n"
      "v_mfma_f32_16x16x32_bf16 v[228:231], v[240:243], v[196:199], v[70:73]\n"
      "v_mul_f32 v204, v156, v204\n"
      "v_mul_f32 v205, v156, v205\n"
      "v_mfma_f32_16x16x32_bf16 v[228:231], v[216:219], v[252:255], v[228:231]\n"
      "v_cvt_pk_bf16_f32 v46, v204, v205\n"
      "v_mul_f32 v206, v156, v206\n"
      "v_mfma_f32_16x16x32_bf16 v[228:231], v[192:195], v[32:35], v[228:231]\n"
      "v_mul_f32 v168, v158, v168\n"
      "v_cvt_pk_bf16_f32 v47, v206, v207\n"
      "v_mfma_f32_16x16x32_bf16 v[176:179], v[188:191], v[196:199], v[86:89]\n"
      "s_waitcnt vmcnt(2)\n"
      "v_mul_f32 v169, v158, v169\n"
      "v_cvt_pk_bf16_f32 v168, v168, v169\n"
      "v_mfma_f32_16x16x32_bf16 v[176:179], v[46:49], v[32:35], v[176:179]\n"
      "ds_read_b128 v[204:207], v119 offset:36864\n"
      "ds_read_b32 v231, v122\n"
      "v_mul_f32 v150, v158, v150\n"
      "v_cvt_pk_bf16_f32 v51, v174, v175\n"
      "v_fma_f32 v230, v180, v180, s39\n"
      "v_mul_f32 v148, v158, v148\n"
      "v_fmac_f32 v230, v181, v181\n"
      "v_mul_f32 v170, v158, v170\n"
      "v_mul_f32 v171, v158, v171\n"
      "v_cvt_pk_bf16_f32 v59, v222, v223\n"
      "v_cvt_pk_bf16_f32 v54, v236, v237\n"
      "v_mul_f32 v212, v158, v212\n"
      "v_mul_f32 v149, v158, v149\n"
      "v_fmac_f32 v230, v182, v182\n"
      "v_mul_f32 v215, v158, v215\n"
      "v_fmac_f32 v230, v183, v183\n"
      "v_fmac_f32 v230, v232, v232\n"
      "v_fmac_f32 v230, v233, v233\n"
      "v_fmac_f32 v230, v234, v234\n"
      "v_mul_f32 v214, v158, v214\n"
      "v_cvt_pk_bf16_f32 v169, v170, v171\n"
      "v_fmac_f32 v230, v235, v235\n"
      "v_mul_f32 v213, v158, v213\n"
      "v_mul_f32 v151, v158, v151\n"
      "v_cvt_pk_bf16_f32 v58, v220, v221\n"
      "v_fmac_f32 v230, v152, v152\n"
      "v_cvt_pk_bf16_f32 v171, v214, v215\n"
      "v_cvt_pk_bf16_f32 v45, v150, v151\n"
      "v_cvt_pk_bf16_f32 v170, v212, v213\n"
      "v_fmac_f32 v230, v153, v153\n"
      "v_fmac_f32 v230, v154, v154\n"
      "v_cvt_pk_bf16_f32 v55, v238, v239\n"
      "v_fmac_f32 v230, v155, v155\n"
      "s_waitcnt lgkmcnt(0)\n"
      "v_cvt_pk_bf16_f32 v44, v148, v149\n"
      "v_cvt_pk_bf16_f32 v50, v172, v173\n"
      "v_or3_b32 v204, v204, v205, v206\n"
      "v_and_b32_e64 v204, s40, v204\n"
      "v_bitop3_b32 v231, v231, v207, s40 bitop3:0xa8\n"
      "v_or3_b32 v204, v204, v231, s4\n"
      "ds_write_b32 v129, v204 offset:2048\n"
      "s_cbranch_scc1 L_gen_19_%=\n"
      "L_fast_22_%=:\n"
      "v_mfma_f32_16x16x32_bf16 v[148:151], v[252:255], v[200:203], v[90:93]\n"
      "ds_read_b128 v[204:207], v119 offset:12288\n"
      "v_exp_f32 v246, v246\n"
      "v_mfma_f32_16x16x32_bf16 v[148:151], v[34:37], v[38:41], v[148:151]\n"
      "ds_read_b128 v[156:159], v123 offset:1024\n"
      "v_exp_f32 v184, v184\n"
      "v_mfma_f32_16x16x32_bf16 v[208:211], v[248:251], v[200:203], v[94:97]\n"
      "ds_read_b128 v[212:215], v123 offset:4096\n"
      "v_exp_f32 v176, v176\n"
      "v_exp_f32 v228, v228\n"
      "v_exp_f32 v187, v187\n"
      "v_mfma_f32_16x16x32_bf16 v[208:211], v[40:43], v[38:41], v[208:211]\n"
      "ds_read_b128 v[196:199], v128 offset:2048\n"
      "v_exp_f32 v177, v177\n"
      "v_exp_f32 v185, v185\n"
      "v_cvt_pk_bf16_f32 v62, v176, v177\n"
      "v_mfma_f32_16x16x32_bf16 v[220:223], v[240:243], v[200:203], v[72:75]\n"
      "ds_read_b128 v[172:175], v123 offset:7168\n"
      "v_cvt_pk_bf16_f32 v184, v184, v185\n"
      "v_mfma_f32_16x16x32_bf16 v[220:223], v[216:219], v[248:251], v[220:223]\n"
      "v_exp_f32_e64 v186, v186\n"
      "v_mfma_f32_16x16x32_bf16 v[220:223], v[192:195], v[38:41], v[220:223]\n"
      "ds_read_b128 v[236:239], v123 offset:10240\n"
      "v_cvt_pk_bf16_f32 v64, v228, 0\n"
      "v_exp_f32_e64 v178, v178\n"
      "v_cvt_pk_bf16_f32 v185, v186, v187\n"
      "v_mfma_f32_16x16x32_bf16 v[164:167], v[188:191], v[200:203], v[98:101]\n"
      "v_exp_f32 v179, v179\n"
      "v_mfma_f32_16x16x32_bf16 v[164:167], v[46:49], v[38:41], v[164:167]\n"
      "s_waitcnt lgkmcnt(3)\n"
      "v_exp_f32_e64 v247, v247\n"
      "v_mfma_f32_16x16x32_bf16 v[200:203], a[216:219], v[156:159], 0\n"
      "v_exp_f32 v244, v244\n"
      "v_mfma_f32_16x16x32_bf16 v[200:203], a[228:231], v[212:215], v[200:203]\n"
      "global_store_dwordx4 v116, v[204:207], s[54:55] offset:256 sc1\n"
      "v_exp_f32 v245, v245\n"
      "v_cvt_pk_bf16_f32 v63, v178, v179\n"
      "v_cvt_pk_bf16_f32 v186, v244, v245\n"
      "v_cvt_pk_bf16_f32 v187, v246, v247\n"
      "v_mfma_f32_16x16x32_bf16 v[244:247], a[220:223], v[156:159], 0\n"
      "s_waitcnt lgkmcnt(1)\n"
      "v_bitop3_b32 v196, v196, v197, s4 bitop3:0x7e\n"
      "v_bitop3_b32 v198, v198, v199, s4 bitop3:0x7e\n"
      "v_mfma_f32_16x16x32_bf16 v[244:247], a[232:235], v[212:215], v[244:247]\n"
      "s_waitcnt lgkmcnt(0)\n"
      "v_or_b32_e64 v196, v196, v198\n"
      "v_cmp_ne_u32 vcc, 0, v196\n"
      "v_mfma_f32_16x16x32_bf16 v[176:179], a[224:227], v[156:159], 0\n"
      "s_mov_b64 s[48:49], vcc\n"
      "ds_read_b128 v[204:207], v123 offset:13312\n"
      "v_mfma_f32_16x16x32_bf16 v[176:179], a[236:239], v[212:215], v[176:179]\n"
      "ds_read_b128 v[212:215], v123 offset:16384\n"
      "v_exp_f32 v149, v149\n"
      "v_mfma_f32_16x16x32_bf16 v[200:203], a[240:243], v[172:175], v[200:203]\n"
      "s_cbranch_vccnz L_flg_23_%=\n"
      "L_fb_24_%=:\n"
      "v_exp_f32 v208, v208\n"
      "v_mfma_f32_16x16x32_bf16 v[200:203], a[252:255], v[236:239], v[200:203]\n"
      "ds_read_b128 v[156:159], v120\n"
      "v_exp_f32 v148, v148\n"
      "v_mfma_f32_16x16x32_bf16 v[244:247], a[244:247], v[172:175], v[244:247]\n"
      "v_exp_f32 v150, v150\n"
      "v_mfma_f32_16x16x32_bf16 v[244:247], v[0:3], v[236:239], v[244:247]\n"
      "v_exp_f32 v151, v151\n"
      "v_exp_f32_e64 v209, v209\n"
      "v_cvt_pk_bf16_f32 v148, v148, v149\n"
      "v_mfma_f32_16x16x32_bf16 v[176:179], a[248:251], v[172:175], v[176:179]\n"
      "ds_read_b128 v[172:175], v123 offset:2048\n"
      "v_cvt_pk_bf16_f32 v149, v150, v151\n"
      "v_mfma_f32_16x16x32_bf16 v[176:179], v[4:7], v[236:239], v[176:179]\n"
      "s_waitcnt lgkmcnt(2)\n"
      "v_cvt_pk_bf16_f32 v150, v208, v209\n"
      "v_mfma_f32_16x16x32_bf16 v[200:203], v[8:11], v[204:207], v[200:203]\n"
      "v_exp_f32 v210, v210\n"
      "v_mfma_f32_16x16x32_bf16 v[200:203], v[20:23], v[212:215], v[200:203]\n"
      "ds_read_b128 v[196:199], v123 offset:5120\n"
      "v_exp_f32 v211, v211\n"
      "v_mfma_f32_16x16x32_bf16 v[244:247], v[12:15], v[204:207], v[244:247]\n"
      "s_waitcnt lgkmcnt(2)\n"
      "v_exp_f32_e64 v164, v164\n"
      "v_mfma_f32_16x16x32_bf16 v[244:247], v[24:27], v[212:215], v[244:247]\n"
      "global_store_dwordx4 v117, v[156:159], s[54:55] sc1\n"
      "v_mfma_f32_16x16x32_bf16 v[176:179], v[16:19], v[204:207], v[176:179]\n"
      "v_fma_f32 v228, v200, v200, s39\n"
      "v_cvt_pk_bf16_f32 v151, v210, v211\n"
      "v_mfma_f32_16x16x32_bf16 v[176:179], v[28:31], v[212:215], v[176:179]\n"
      "v_exp_f32 v165, v165\n"
      "v_mfma_f32_16x16x32_bf16 v[212:215], v[66:69], v[184:187], 0\n"
      "v_exp_f32 v166, v166\n"
      "v_mfma_f32_16x16x32_bf16 v[212:215], v[66:69], v[62:65], v[212:215]\n"
      "v_exp_f32 v167, v167\n"
      "v_mfma_f32_16x16x32_bf16 v[208:211], v[140:143], v[184:187], 0\n"
      "v_exp_f32 v220, v220\n"
      "v_mfma_f32_16x16x32_bf16 v[208:211], v[50:53], v[62:65], v[208:211]\n"
      "v_fmac_f32 v228, v201, v201\n"
      "v_fmac_f32 v228, v202, v202\n"
      "v_mfma_f32_16x16x32_bf16 v[204:207], v[224:227], v[184:187], 0\n"
      "v_fmac_f32 v228, v203, v203\n"
      "v_fmac_f32 v228, v244, v244\n"
      "v_mfma_f32_16x16x32_bf16 v[204:207], v[54:57], v[62:65], v[204:207]\n"
      "v_fmac_f32 v228, v245, v245\n"
      "v_fmac_f32 v228, v246, v246\n"
      "v_mfma_f32_16x16x32_bf16 v[236:239], v[136:139], v[184:187], 0\n"
      "v_fmac_f32 v228, v247, v247\n"
      "v_fmac_f32 v228, v176, v176\n"
      "v_mfma_f32_16x16x32_bf16 v[236:239], v[58:61], v[62:65], v[236:239]\n"
      "v_fmac_f32 v228, v177, v177\n"
      "v_fmac_f32 v228, v178, v178\n"
      "v_mfma_f32_16x16x32_bf16 v[156:159], v[252:255], v[168:171], v[102:105]\n"
      "v_cvt_pk_bf16_f32 v62, v164, v165\n"
      "v_fmac_f32_e64 v228, v179, v179\n"
      "v_mfma_f32_16x16x32_bf16 v[156:159], v[34:37], v[44:47], v[156:159]\n"
      "ds_read_b128 v[184:187], v121\n"
      "v_cvt_pk_bf16_f32 v63, v166, v167\n"
      "v_mfma_f32_16x16x32_bf16 v[164:167], v[248:251], v[168:171], v[106:109]\n"
      "v_cvt_pk_bf16_f32 v64, v220, 0\n"
      "v_cndmask_b32 v229, v229, v221, s[42:43]\n"
      "v_mfma_f32_16x16x32_bf16 v[164:167], v[40:43], v[44:47], v[164:167]\n"
      "v_rcp_f32_e64 v212, v212\n"
      "v_mfma_f32_16x16x32_bf16 v[248:251], v[240:243], v[168:171], v[74:77]\n"
      "v_mul_f32 v208, v212, v208\n"
      "v_mul_f32 v209, v212, v209\n"
      "v_mfma_f32_16x16x32_bf16 v[248:251], v[216:219], v[188:191], v[248:251]\n"
      "v_mul_f32_e64 v210, v212, v210\n"
      "v_cvt_pk_bf16_f32 v208, v208, v209\n"
      "v_mfma_f32_16x16x32_bf16 v[248:251], v[192:195], v[44:47], v[248:251]\n"
      "v_mul_f32_e64 v211, v212, v211\n"
      "v_cvt_pk_bf16_f32 v209, v210, v211\n"
      "v_mfma_f32_16x16x32_bf16 v[220:223], v[188:191], v[168:171], v[110:113]\n"
      "ds_read_b128 v[168:171], v123 offset:8192\n"
      "v_mul_f32 v204, v212, v204\n"
      "v_mfma_f32_16x16x32_bf16 v[220:223], v[46:49], v[44:47], v[220:223]\n"
      "v_mul_f32 v205, v212, v205\n"
      "v_cvt_pk_bf16_f32 v210, v204, v205\n"
      "v_mfma_f32_16x16x32_bf16 v[188:191], v[216:219], v[240:243], v[76:79]\n"
      "ds_read_b128 v[216:219], v123 offset:11264\n"
      "v_mul_f32_e64 v206, v212, v206\n"
      "v_mfma_f32_16x16x32_bf16 v[250:253], v[66:69], v[148:151], 0\n"
      "ds_read_b128 v[240:243], v123 offset:14336\n"
      "v_mul_f32 v207, v212, v207\n"
      "v_mfma_f32_16x16x32_bf16 v[250:253], v[66:69], v[62:65], v[250:253]\n"
      "s_waitcnt lgkmcnt(3)\n"
      "v_cvt_pk_bf16_f32 v211, v206, v207\n"
      "v_mul_f32_e64 v236, v212, v236\n"
      "v_mfma_f32_16x16x16_bf16 v[188:191], v[194:195], v[192:193], v[188:191]\n"
      "ds_write_b128 v124, v[208:211] offset:18432\n"
      "v_mfma_f32_16x16x32_bf16 v[204:207], v[140:143], v[148:151], 0\n"
      "v_mul_f32 v237, v212, v237\n"
      "v_cvt_pk_bf16_f32 v236, v236, v237\n"
      "v_mfma_f32_16x16x32_bf16 v[204:207], v[50:53], v[62:65], v[204:207]\n"
      "s_mov_b32 exec_hi, 0\n"
      "global_store_dwordx4 v118, v[184:187], s[54:55] sc1\n"
      "s_mov_b32 exec_hi, -1\n"
      "v_mfma_f32_16x16x32_bf16 v[192:195], v[224:227], v[148:151], 0\n"
      "s_add_u32 s28, s27, 2\n"
      "v_mul_f32_e64 v238, v212, v238\n"
      "v_mul_f32 v239, v212, v239\n"
      "v_mfma_f32_16x16x32_bf16 v[192:195], v[54:57], v[62:65], v[192:195]\n"
      "s_waitcnt lgkmcnt(2)\n"
      "v_cvt_pk_bf16_f32 v237, v238, v239\n"
      "v_cndmask_b32 v229, v229, v249, s[44:45]\n"
      "v_mfma_f32_16x16x32_bf16 v[208:211], v[136:139], v[148:151], 0\n"
      "s_min_u32 m0, s28, s3\n"
      "v_cndmask_b32 v229, v229, v189, s[46:47]\n"
      "s_movrels_b32 s9, s80\n"
      "v_mfma_f32_16x16x32_bf16 v[208:211], v[58:61], v[62:65], v[208:211]\n"
      "s_lshr_b32 s8, s9, 8\n"
      "v_exp_f32 v229, v229\n"
      "v_mfma_f32_16x16x32_bf16 v[212:215], a[216:219], v[172:175], 0\n"
      "s_and_b32 s9, s9, 0xff\n"
      "v_cvt_pk_bf16_f32 v229, v229, v229\n"
      "s_mul_i32 s10, s8, 73\n"
      "v_mfma_f32_16x16x32_bf16 v[212:215], a[228:231], v[196:199], v[212:215]\n"
      "ds_write_b16 v130, v229\n"
      "v_mfma_f32_16x16x32_bf16 v[184:187], a[220:223], v[172:175], 0\n"
      "s_add_u32 s10, s10, s9\n"
      "ds_read_b128 v[160:163], v131\n"
      "v_mfma_f32_16x16x32_bf16 v[184:187], a[232:235], v[196:199], v[184:187]\n"
      "ds_read_b128 v[252:255], v131 offset:64\n"
      "v_exp_f32 v158, v158\n"
      "v_exp_f32 v159, v159\n"
      "v_exp_f32 v156, v156\n"
      "v_mfma_f32_16x16x32_bf16 v[212:215], a[240:243], v[168:171], v[212:215]\n"
      "s_mul_i32 s28, s10, 4802\n"
      "v_exp_f32 v157, v157\n"
      "v_mfma_f32_16x16x32_bf16 v[212:215], a[252:255], v[216:219], v[212:215]\n"
      "s_add_u32 s56, s18, s28\n"
      "v_exp_f32 v165, v165\n"
      "v_mfma_f32_16x16x32_bf16 v[184:187], a[244:247], v[168:171], v[184:187]\n"
      "s_addc_u32 s57, s19, 0\n"
      "v_cvt_pk_bf16_f32 v156, v156, v157\n"
      "s_and_b32 s56, s56, -16\n"
      "v_mfma_f32_16x16x32_bf16 v[184:187], v[0:3], v[216:219], v[184:187]\n"
      "s_sub_u32 s58, s56, 110592\n"
      "v_exp_f32 v166, v166\n"
      "v_exp_f32_e64 v164, v164\n"
      "v_cvt_pk_bf16_f32 v157, v158, v159\n"
      "v_cvt_pk_bf16_f32 v158, v164, v165\n"
      "v_mfma_f32_16x16x32_bf16 v[188:191], a[224:227], v[172:175], 0\n"
      "ds_read_b128 v[148:151], v123 offset:17408\n"
      "s_subb_u32 s59, s57, 0\n"
      "v_mfma_f32_16x16x32_bf16 v[188:191], a[236:239], v[196:199], v[188:191]\n"
      "s_waitcnt lgkmcnt(1)\n"
      "v_exp_f32_e64 v167, v167\n"
      "v_mfma_f32_16x16x32_bf16 v[188:191], a[248:251], v[168:171], v[188:191]\n"
      "ds_read_b128 v[168:171], v114\n"
      "v_cvt_pk_bf16_f32 v159, v166, v167\n"
      "v_mfma_f32_16x16x32_bf16 v[188:191], v[4:7], v[216:219], v[188:191]\n"
      "ds_read_b128 v[172:175], v115\n"
      "v_exp_f32 v220, v220\n"
      "v_mfma_f32_16x16x32_bf16 v[196:199], v[160:163], v[66:69], 0\n"
      "s_sub_u32 s56, s56, 73728\n"
      "v_exp_f32 v221, v221\n"
      "v_mfma_f32_16x16x32_bf16 v[196:199], v[252:255], v[66:69], v[196:199]\n"
      "s_subb_u32 s57, s57, 0\n"
      "v_exp_f32 v222, v222\n"
      "v_mfma_f32_16x16x32_bf16 v[164:167], v[160:163], v[140:143], 0\n"
      "s_mul_i32 s28, s8, 1373568\n"
      "v_exp_f32_e64 v223, v223\n"
      "v_mfma_f32_16x16x32_bf16 v[164:167], v[252:255], v[50:53], v[164:167]\n"
      "s_mul_i32 s29, s9, 2688\n"
      "v_exp_f32 v248, v248\n"
      "v_mfma_f32_16x16x32_bf16 v[216:219], v[160:163], v[224:227], 0\n"
      "s_waitcnt lgkmcnt(2)\n"
      "v_cvt_pk_bf16_f32 v63, v222, v223\n"
      "v_cvt_pk_bf16_f32 v62, v220, v221\n"
      "v_mfma_f32_16x16x32_bf16 v[216:219], v[252:255], v[54:57], v[216:219]\n"
      "s_add_u32 s28, s28, s29\n"
      "v_rcp_f32 v250, v250\n"
      "v_mfma_f32_16x16x32_bf16 v[212:215], v[8:11], v[240:243], v[212:215]\n"
      "s_cmp_eq_u32 s8, 72\n"
      "v_mul_f32 v204, v250, v204\n"
      "v_mul_f32 v205, v250, v205\n"
      "v_mfma_f32_16x16x32_bf16 v[212:215], v[20:23], v[148:151], v[212:215]\n"
      "s_waitcnt lgkmcnt(0)\n"
      "v_mul_f32 v206, v250, v206\n"
      "v_cvt_pk_bf16_f32 v64, v248, 0\n"
      "v_mul_f32_e64 v195, v250, v195\n"
      "v_cvt_pk_bf16_f32 v204, v204, v205\n"
      "v_mfma_f32_16x16x32_bf16 v[184:187], v[12:15], v[240:243], v[184:187]\n"
      "s_cselect_b32 s8, 0, 100270464\n"
      "v_mul_f32_e64 v207, v250, v207\n"
      "v_cvt_pk_bf16_f32 v205, v206, v207\n"
      "v_mfma_f32_16x16x32_bf16 v[184:187], v[24:27], v[148:151], v[184:187]\n"
      "s_cmp_eq_u32 s9, 72\n"
      "v_mul_f32 v192, v250, v192\n"
      "v_mul_f32 v193, v250, v193\n"
      "v_mfma_f32_16x16x32_bf16 v[188:191], v[16:19], v[240:243], v[188:191]\n"
      "s_cselect_b32 s9, 0, 196224\n"
      "v_mul_f32 v194, v250, v194\n"
      "v_cvt_pk_bf16_f32 v206, v192, v193\n"
      "v_mfma_f32_16x16x32_bf16 v[188:191], v[28:31], v[148:151], v[188:191]\n"
      "s_add_u32 s10, s8, s9\n"
      "v_cvt_pk_bf16_f32 v207, v194, v195\n"
      "v_cndmask_b32 v164, v164, v216, s[42:43]\n"
      "v_mfma_f32_16x16x32_bf16 v[192:195], v[66:69], v[156:159], 0\n"
      "s_add_u32 s28, s28, s10\n"
      "v_mul_f32 v208, v250, v208\n"
      "v_mul_f32 v209, v250, v209\n"
      "v_mfma_f32_16x16x32_bf16 v[192:195], v[66:69], v[62:65], v[192:195]\n"
      "ds_write_b128 v124, v[204:207] offset:19456\n"
      "v_mfma_f32_16x16x32_bf16 v[240:243], v[140:143], v[156:159], 0\n"
      "s_add_u32 s52, s16, s28\n"
      "v_mul_f32 v210, v250, v210\n"
      "v_cvt_pk_bf16_f32 v208, v208, v209\n"
      "v_mfma_f32_16x16x32_bf16 v[240:243], v[50:53], v[62:65], v[240:243]\n"
      "s_addc_u32 s53, s17, 0\n"
      "v_mul_f32_e64 v211, v250, v211\n"
      "v_cvt_pk_bf16_f32 v209, v210, v211\n"
      "v_mfma_f32_16x16x32_bf16 v[248:251], v[224:227], v[156:159], 0\n"
      "s_add_u32 s27, s27, 1\n"
      "v_fma_f32 v229, v212, v212, s39\n"
      "v_fmac_f32 v229, v213, v213\n"
      "v_mfma_f32_16x16x32_bf16 v[248:251], v[54:57], v[62:65], v[248:251]\n"
      "ds_write2st64_b64 v125, v[236:237], v[208:209] offset0:36 offset1:38\n"
      "v_mfma_f32_16x16x32_bf16 v[148:151], v[136:139], v[156:159], 0\n"
      "v_fmac_f32 v229, v214, v214\n"
      "v_fmac_f32 v229, v215, v215\n"
      "v_mfma_f32_16x16x32_bf16 v[148:151], v[58:61], v[62:65], v[148:151]\n"
      "s_cmp_ge_u32 s27, s26\n"
      "v_mov_b32_dpp v196, v196 quad_perm:[0,0,0,0] row_mask:0xf bank_mask:0xf\n"
      "v_fmac_f32 v229, v184, v184\n"
      "v_fmac_f32_e64 v229, v185, v185\n"
      "v_mfma_f32_16x16x32_bf16 v[224:227], v[160:163], v[136:139], 0\n"
      "v_fmac_f32 v229, v186, v186\n"
      "v_fmac_f32 v229, v187, v187\n"
      "v_mfma_f32_16x16x32_bf16 v[224:227], v[252:255], v[58:61], v[224:227]\n"
      "v_fmac_f32 v229, v188, v188\n"
      "v_fmac_f32 v229, v189, v189\n"
      "v_mfma_f32_16x16x32_bf16 v[204:207], a[0:3], v[168:171], 0\n"
      "s_cselect_b32 s28, 1, 0\n"
      "v_fmac_f32 v229, v190, v190\n"
      "v_fmac_f32 v229, v191, v191\n"
      "v_mfma_f32_16x16x32_bf16 v[204:207], a[36:39], v[172:175], v[204:207]\n"
      "s_xor_b32 s29, s10, s11\n"
      "v_permlane16_swap_b32 v230, v228\n"
      "v_mfma_f32_16x16x32_bf16 v[160:163], a[4:7], v[168:171], 0\n"
      "v_add_f32 v230, v230, v228\n"
      "v_mov_b32 v231, v229\n"
      "v_mfma_f32_16x16x32_bf16 v[160:163], a[40:43], v[172:175], v[160:163]\n"
      "s_or_b32 s28, s28, s29\n"
      "v_permlane16_swap_b32 v229, v231\n"
      "v_add_f32 v229, v229, v231\n"
      "v_cndmask_b32 v164, v164, v224, s[44:45]\n"
      "v_rcp_f32 v196, v196\n"
      "v_permlane32_swap_b32 v230, v229\n"
      "v_mul_f32_e64 v164, v196, v164\n"
      "v_add_f32 v230, v230, v229\n"
      "v_cvt_pk_bf16_f32 v164, v164, v164\n"
      "ds_write_b32 v129, v230 offset:1024\n"
      "ds_write_b16 v135, v164 offset:16\n"
      "L_join_21_%=:\n"
      "s_cbranch_scc1 L_tail1_%=\n"
      "L_mid_%=:\n"
      "v_mfma_f32_16x16x32_bf16 v[196:199], a[8:11], v[168:171], 0\n"
      "s_add_u32 m0, s76, 18432\n"
      "ds_read_b128 v[228:231], v114 offset:6144\n"
      "v_mfma_f32_16x16x32_bf16 v[196:199], a[44:47], v[172:175], v[196:199]\n"
      "ds_read_b128 v[208:211], v115 offset:6144\n"
      "v_rcp_f32 v192, v192\n"
      "v_mfma_f32_16x16x32_bf16 v[164:167], a[12:15], v[168:171], 0\n"
      "v_mul_f32 v241, v192, v241\n"
      "v_mul_f32_e64 v240, v192, v240\n"
      "v_mfma_f32_16x16x32_bf16 v[164:167], a[48:51], v[172:175], v[164:167]\n"
      "ds_read_b128 v[224:227], v114 offset:12288\n"
      "v_cvt_pk_bf16_f32 v240, v240, v241\n"
      "v_mfma_f32_16x16x32_bf16 v[156:159], a[16:19], v[168:171], 0\n"
      "v_mul_f32 v242, v192, v242\n"
      "v_mul_f32 v243, v192, v243\n"
      "v_mfma_f32_16x16x32_bf16 v[156:159], a[52:55], v[172:175], v[156:159]\n"
      "s_waitcnt lgkmcnt(3)\n"
      "s_barrier\n"
      "v_mfma_f32_16x16x32_bf16 v[136:139], a[20:23], v[168:171], 0\n"
      "ds_read_b128 v[140:143], v128 offset:1024\n"
      "v_cvt_pk_bf16_f32 v241, v242, v243\n"
      "v_mfma_f32_16x16x32_bf16 v[136:139], a[56:59], v[172:175], v[136:139]\n"
      "global_load_lds_dwordx4 v116, s[52:53]\n"
      "v_mul_f32 v248, v192, v248\n"
      "v_mfma_f32_16x16x32_bf16 v[220:223], v[168:171], a[24:27], 0\n"
      "s_add_u32 m0, s76, 24448\n"
      "v_mul_f32 v250, v192, v250\n"
      "v_mul_f32_e64 v251, v192, v251\n"
      "v_mfma_f32_16x16x32_bf16 v[220:223], v[172:175], a[60:63], v[220:223]\n"
      "v_mul_f32_e64 v249, v192, v249\n"
      "v_cvt_pk_bf16_f32 v242, v248, v249\n"
      "v_mfma_f32_16x16x32_bf16 v[236:239], v[168:171], a[28:31], 0\n"
      "v_cvt_pk_bf16_f32 v243, v250, v251\n"
      "v_mul_f32_e64 v148, v192, v148\n"
      "v_mfma_f32_16x16x32_bf16 v[236:239], v[172:175], a[64:67], v[236:239]\n"
      "ds_write_b128 v124, v[240:243] offset:20480\n"
      "v_mfma_f32_16x16x32_bf16 v[252:255], v[168:171], a[32:35], 0\n"
      "s_waitcnt lgkmcnt(2)\n"
      "ds_read_b128 v[168:171], v115 offset:12288\n"
      "v_mfma_f32_16x16x32_bf16 v[252:255], v[172:175], a[68:71], v[252:255]\n"
      "v_mul_f32 v149, v192, v149\n"
      "v_cvt_pk_bf16_f32 v148, v148, v149\n"
      "v_mfma_f32_16x16x32_bf16 v[204:207], a[72:75], v[228:231], v[204:207]\n"
      "s_waitcnt lgkmcnt(2)\n"
      "v_mul_f32 v150, v192, v150\n"
      "v_mul_f32_e64 v151, v192, v151\n"
      "v_mfma_f32_16x16x32_bf16 v[204:207], a[108:111], v[208:211], v[204:207]\n"
      "global_load_lds_dwordx4 v116, s[52:53] offset:128\n"
      "v_mfma_f32_16x16x32_bf16 v[160:163], a[76:79], v[228:231], v[160:163]\n"
      "v_cvt_pk_bf16_f32 v149, v150, v151\n"
      "v_add_f32 v140, |v140|, |v141|\n"
      "v_mfma_f32_16x16x32_bf16 v[160:163], a[112:115], v[208:211], v[160:163]\n"
      "ds_write_b64 v125, v[148:149] offset:20480\n"
      "v_mfma_f32_16x16x32_bf16 v[196:199], a[80:83], v[228:231], v[196:199]\n"
      "v_add_f32 v142, |v142|, |v143|\n"
      "v_add_f32 v140, v140, v142\n"
      "v_mfma_f32_16x16x32_bf16 v[196:199], a[116:119], v[208:211], v[196:199]\n"
      "s_add_u32 m0, s76, 30464\n"
      "v_rsq_f32 v140, v140\n"
      "v_mfma_f32_16x16x32_bf16 v[164:167], a[84:87], v[228:231], v[164:167]\n"
      "v_mul_f32 v140, s41, v140\n"
      "v_mov_b32 v249, v140\n"
      "v_mfma_f32_16x16x32_bf16 v[164:167], a[120:123], v[208:211], v[164:167]\n"
      "s_nop 0\n"
      "v_permlane32_swap_b32 v140, v249\n"
      "v_mfma_f32_16x16x32_bf16 v[156:159], a[88:91], v[228:231], v[156:159]\n"
      "ds_read_b128 v[192:195], v114 offset:2048\n"
      "v_mov_b32_e64 v251, v140\n"
      "v_mfma_f32_16x16x32_bf16 v[156:159], a[124:127], v[208:211], v[156:159]\n"
      "global_load_lds_dwordx4 v116, s[52:53] offset:256\n"
      "v_mfma_f32_16x16x32_bf16 v[136:139], a[92:95], v[228:231], v[136:139]\n"
      "v_permlane16_swap_b32_e64 v140, v251\n"
      "v_mfma_f32_16x16x32_bf16 v[136:139], a[128:131], v[208:211], v[136:139]\n"
      "ds_read_b128 v[148:151], v115 offset:2048\n"
      "v_mul_f32 v180, v140, v180\n"
      "v_mfma_f32_16x16x32_bf16 v[220:223], v[228:231], a[96:99], v[220:223]\n"
      "s_waitcnt lgkmcnt(2)\n"
      "v_mul_f32_e64 v181, v140, v181\n"
      "v_cvt_pk_bf16_f32 v180, v180, v181\n"
      "v_mfma_f32_16x16x32_bf16 v[220:223], v[208:211], a[132:135], v[220:223]\n"
      "v_mul_f32 v182, v140, v182\n"
      "v_mul_f32 v183, v140, v183\n"
      "v_mfma_f32_16x16x32_bf16 v[236:239], v[228:231], a[100:103], v[236:239]\n"
      "v_cvt_pk_bf16_f32 v181, v182, v183\n"
      "v_mul_f32 v232, v140, v232\n"
      "v_mfma_f32_16x16x32_bf16 v[236:239], v[208:211], a[136:139], v[236:239]\n"
      "s_add_u32 m0, s77, 18432\n"
      "v_mul_f32 v233, v140, v233\n"
      "v_cvt_pk_bf16_f32 v182, v232, v233\n"
      "v_mfma_f32_16x16x32_bf16 v[252:255], v[228:231], a[104:107], v[252:255]\n"
      "v_mul_f32 v235, v140, v235\n"
      "v_mul_f32 v234, v140, v234\n"
      "v_mfma_f32_16x16x32_bf16 v[252:255], v[208:211], a[140:143], v[252:255]\n"
      "v_mul_f32_e64 v152, v140, v152\n"
      "v_cvt_pk_bf16_f32 v183, v234, v235\n"
      "v_mfma_f32_16x16x32_bf16 v[204:207], a[144:147], v[224:227], v[204:207]\n"
      "global_load_lds_dwordx4 v117, s[52:53]\n"
      "v_mfma_f32_16x16x32_bf16 v[204:207], a[180:183], v[168:171], v[204:207]\n"
      "v_mul_f32 v153, v140, v153\n"
      "v_cvt_pk_bf16_f32 v152, v152, v153\n"
      "v_mfma_f32_16x16x32_bf16 v[160:163], a[148:151], v[224:227], v[160:163]\n"
      "v_mul_f32 v155, v140, v155\n"
      "v_mul_f32_e64 v154, v140, v154\n"
      "v_mfma_f32_16x16x32_bf16 v[160:163], a[184:187], v[168:171], v[160:163]\n"
      "ds_write_b128 v126, v[180:183]\n"
      "v_mfma_f32_16x16x32_bf16 v[196:199], a[152:155], v[224:227], v[196:199]\n"
      "v_cvt_pk_bf16_f32 v153, v154, v155\n"
      "v_mul_f32 v200, v251, v200\n"
      "v_mfma_f32_16x16x32_bf16 v[196:199], a[188:191], v[168:171], v[196:199]\n"
      "v_mul_f32 v201, v251, v201\n"
      "v_cvt_pk_bf16_f32 v200, v200, v201\n"
      "v_mfma_f32_16x16x32_bf16 v[164:167], a[156:159], v[224:227], v[164:167]\n"
      "s_waitcnt lgkmcnt(1)\n"
      "v_mul_f32 v203, v251, v203\n"
      "v_mul_f32_e64 v202, v251, v202\n"
      "v_mfma_f32_16x16x32_bf16 v[164:167], a[192:195], v[168:171], v[164:167]\n"
      "s_add_u32 m0, s78, 18432\n"
      "v_mul_f32_e64 v244, v251, v244\n"
      "v_cvt_pk_bf16_f32 v201, v202, v203\n"
      "v_mfma_f32_16x16x32_bf16 v[156:159], a[160:163], v[224:227], v[156:159]\n"
      "ds_read_b128 v[232:235], v114 offset:8192\n"
      "v_mul_f32_e64 v245, v251, v245\n"
      "v_mfma_f32_16x16x32_bf16 v[156:159], a[196:199], v[168:171], v[156:159]\n"
      "ds_read_b128 v[180:183], v115 offset:8192\n"
      "v_cvt_pk_bf16_f32 v202, v244, v245\n"
      "v_mfma_f32_16x16x32_bf16 v[136:139], a[164:167], v[224:227], v[136:139]\n"
      "v_mul_f32 v246, v251, v246\n"
      "v_mul_f32 v247, v251, v247\n"
      "v_mfma_f32_16x16x32_bf16 v[136:139], a[200:203], v[168:171], v[136:139]\n"
      "s_mov_b32 exec_hi, 0\n"
      "global_load_lds_dwordx4 v118, s[52:53]\n"
      "s_mov_b32 exec_hi, -1\n"
      "v_mfma_f32_16x16x32_bf16 v[220:223], v[224:227], a[168:171], v[220:223]\n"
      "v_mul_f32_e64 v176, v251, v176\n"
      "v_cvt_pk_bf16_f32 v203, v246, v247\n"
      "v_mfma_f32_16x16x32_bf16 v[220:223], v[168:171], a[204:207], v[220:223]\n"
      "ds_write_b128 v126, v[200:203] offset:2048\n"
      "v_mfma_f32_16x16x32_bf16 v[236:239], v[224:227], a[172:175], v[236:239]\n"
      "v_mul_f32 v177, v251, v177\n"
      "v_cvt_pk_bf16_f32 v176, v176, v177\n"
      "v_mfma_f32_16x16x32_bf16 v[236:239], v[168:171], a[208:211], v[236:239]\n"
      "s_add_u32 m0, s76, 115456\n"
      "v_mul_f32 v179, v251, v179\n"
      "v_mul_f32 v178, v251, v178\n"
      "v_mfma_f32_16x16x32_bf16 v[252:255], v[224:227], a[176:179], v[252:255]\n"
      "s_waitcnt lgkmcnt(1)\n"
      "v_mul_f32_e64 v212, v249, v212\n"
      "v_cvt_pk_bf16_f32 v177, v178, v179\n"
      "v_mfma_f32_16x16x32_bf16 v[252:255], v[168:171], a[212:215], v[252:255]\n"
      "ds_write2st64_b64 v127, v[152:153], v[176:177] offset0:0 offset1:4\n"
      "v_mfma_f32_16x16x32_bf16 v[176:179], a[0:3], v[192:195], 0\n"
      "global_load_lds_dwordx4 v119, s[56:57]\n"
      "v_mfma_f32_16x16x32_bf16 v[176:179], a[36:39], v[148:151], v[176:179]\n"
      "s_add_u32 m0, s79, 4864\n"
      "v_mul_f32_e64 v213, v249, v213\n"
      "v_cvt_pk_bf16_f32 v212, v212, v213\n"
      "v_mfma_f32_16x16x32_bf16 v[152:155], a[4:7], v[192:195], 0\n"
      "v_mul_f32 v215, v249, v215\n"
      "v_mul_f32 v214, v249, v214\n"
      "v_mfma_f32_16x16x32_bf16 v[152:155], a[40:43], v[148:151], v[152:155]\n"
      "s_sub_u32 s28, s27, 3\n"
      "v_mul_f32 v184, v249, v184\n"
      "v_cvt_pk_bf16_f32 v213, v214, v215\n"
      "v_mfma_f32_16x16x32_bf16 v[200:203], a[8:11], v[192:195], 0\n"
      "s_cselect_b64 s[54:55], s[12:13], s[22:23]\n"
      "v_mul_f32 v185, v249, v185\n"
      "v_cvt_pk_bf16_f32 v214, v184, v185\n"
      "v_mfma_f32_16x16x32_bf16 v[200:203], a[44:47], v[148:151], v[200:203]\n"
      "v_mul_f32 v187, v249, v187\n"
      "v_mul_f32 v186, v249, v186\n"
      "v_mfma_f32_16x16x32_bf16 v[168:171], a[12:15], v[192:195], 0\n"
      "ds_read_b128 v[244:247], v114 offset:14336\n"
      "v_cvt_pk_bf16_f32 v215, v186, v187\n"
      "v_mfma_f32_16x16x32_bf16 v[168:171], a[48:51], v[148:151], v[168:171]\n"
      "ds_write_b128 v126, v[212:215] offset:4096\n"
      "v_mfma_f32_16x16x32_bf16 v[240:243], a[16:19], v[192:195], 0\n"
      "global_load_lds_dword v122, s[58:59]\n"
      "v_mfma_f32_16x16x32_bf16 v[240:243], a[52:55], v[148:151], v[240:243]\n"
      "ds_read_b128 v[184:187], v115 offset:14336\n"
      "v_mul_f32_e64 v188, v249, v188\n"
      "v_mfma_f32_16x16x32_bf16 v[212:215], a[20:23], v[192:195], 0\n"
      "s_max_i32 m0, s28, 0\n"
      "v_mul_f32 v189, v249, v189\n"
      "v_cvt_pk_bf16_f32 v188, v188, v189\n"
      "v_mfma_f32_16x16x32_bf16 v[212:215], a[56:59], v[148:151], v[212:215]\n"
      "s_movrels_b32 s9, s80\n"
      "v_mul_f32 v190, v249, v190\n"
      "v_mul_f32 v191, v249, v191\n"
      "v_mfma_f32_16x16x32_bf16 v[248:251], v[192:195], a[24:27], 0\n"
      "s_lshr_b32 s8, s9, 8\n"
      "v_cvt_pk_bf16_f32 v189, v190, v191\n"
      "v_fma_f32 v209, v204, v204, s38\n"
      "v_mfma_f32_16x16x32_bf16 v[248:251], v[148:151], a[60:63], v[248:251]\n"
      "ds_write_b64 v127, v[188:189] offset:4096\n"
      "v_mfma_f32_16x16x32_bf16 v[188:191], v[192:195], a[28:31], 0\n"
      "s_and_b32 s9, s9, 0xff\n"
      "v_fmac_f32_e64 v209, v205, v205\n"
      "v_fma_f32 v211, v164, v164, s38\n"
      "v_mfma_f32_16x16x32_bf16 v[188:191], v[148:151], a[64:67], v[188:191]\n"
      "s_mul_i32 s28, s8, 1373568\n"
      "v_fmac_f32 v211, v165, v165\n"
      "v_fmac_f32 v209, v206, v206\n"
      "v_mfma_f32_16x16x32_bf16 v[228:231], v[192:195], a[32:35], 0\n"
      "s_mul_i32 s29, s9, 2688\n"
      "v_fmac_f32 v209, v207, v207\n"
      "v_fmac_f32 v211, v166, v166\n"
      "v_mfma_f32_16x16x32_bf16 v[228:231], v[148:151], a[68:71], v[228:231]\n"
      "s_waitcnt lgkmcnt(1)\n"
      "v_fmac_f32 v209, v160, v160\n"
      "v_fmac_f32_e64 v211, v167, v167\n"
      "v_mfma_f32_16x16x32_bf16 v[176:179], a[72:75], v[232:235], v[176:179]\n"
      "s_add_u32 s28, s28, s29\n"
      "v_fmac_f32 v211, v156, v156\n"
      "v_fmac_f32_e64 v209, v161, v161\n"
      "v_mfma_f32_16x16x32_bf16 v[176:179], a[108:111], v[180:183], v[176:179]\n"
      "s_cmp_eq_u32 s8, 72\n"
      "v_fmac_f32 v211, v157, v157\n"
      "v_fmac_f32 v209, v162, v162\n"
      "v_mfma_f32_16x16x32_bf16 v[152:155], a[76:79], v[232:235], v[152:155]\n"
      "s_cselect_b32 s8, 0, 100270464\n"
      "v_fmac_f32 v211, v158, v158\n"
      "v_fmac_f32 v209, v163, v163\n"
      "v_mfma_f32_16x16x32_bf16 v[152:155], a[112:115], v[180:183], v[152:155]\n"
      "s_cmp_eq_u32 s9, 72\n"
      "v_fmac_f32 v209, v196, v196\n"
      "v_fmac_f32 v211, v159, v159\n"
      "v_mfma_f32_16x16x32_bf16 v[200:203], a[80:83], v[232:235], v[200:203]\n"
      "ds_read_b128 v[192:195], v114 offset:4096\n"
      "v_fmac_f32_e64 v211, v136, v136\n"
      "v_mfma_f32_16x16x32_bf16 v[200:203], a[116:119], v[180:183], v[200:203]\n"
      "s_cselect_b32 s9, 0, 196224\n"
      "v_fmac_f32 v209, v197, v197\n"
      "v_fmac_f32 v211, v137, v137\n"
      "v_mfma_f32_16x16x32_bf16 v[168:171], a[84:87], v[232:235], v[168:171]\n"
      "ds_read_b128 v[148:151], v115 offset:4096\n"
      "v_fmac_f32 v209, v198, v198\n"
      "v_mfma_f32_16x16x32_bf16 v[168:171], a[120:123], v[180:183], v[168:171]\n"
      "s_add_u32 s10, s8, s9\n"
      "v_fmac_f32 v211, v138, v138\n"
      "v_fmac_f32 v209, v199, v199\n"
      "v_mfma_f32_16x16x32_bf16 v[240:243], a[88:91], v[232:235], v[240:243]\n"
      "s_add_u32 s28, s28, s10\n"
      "v_cvt_pk_bf16_f32 v220, v220, v221\n"
      "v_fmac_f32 v211, v139, v139\n"
      "v_mfma_f32_16x16x32_bf16 v[240:243], a[124:127], v[180:183], v[240:243]\n"
      "s_add_u32 s54, s54, s28\n"
      "v_permlane16_swap_b32 v209, v211\n"
      "v_mfma_f32_16x16x32_bf16 v[212:215], a[92:95], v[232:235], v[212:215]\n"
      "s_addc_u32 s55, s55, 0\n"
      "v_add_f32 v209, v209, v211\n"
      "v_mov_b32 v211, v209\n"
      "v_mfma_f32_16x16x32_bf16 v[212:215], a[128:131], v[180:183], v[212:215]\n"
      "s_cmp_lg_u32 s10, s11\n"
      "v_permlane32_swap_b32 v209, v211\n"
      "v_mfma_f32_16x16x32_bf16 v[248:251], v[232:235], a[96:99], v[248:251]\n"
      "s_cbranch_scc1 L_edge_26_%=\n"
      "L_eb_27_%=:\n"
      "v_add_f32 v209, v209, v211\n"
      "v_mfma_f32_16x16x32_bf16 v[248:251], v[180:183], a[132:135], v[248:251]\n"
      "v_rsq_f32 v209, v209\n"
      "v_mfma_f32_16x16x32_bf16 v[188:191], v[232:235], a[100:103], v[188:191]\n"
      "s_mov_b32 s5, s4\n"
      "v_cvt_pk_bf16_f32 v221, v222, v223\n"
      "v_mov_b32 v211, v209\n"
      "v_mfma_f32_16x16x32_bf16 v[188:191], v[180:183], a[136:139], v[188:191]\n"
      "s_add_u32 s4, s27, 1\n"
      "v_permlane16_swap_b32 v209, v211\n"
      "v_mfma_f32_16x16x32_bf16 v[228:231], v[232:235], a[104:107], v[228:231]\n"
      "s_waitcnt lgkmcnt(0)\n"
      "v_mul_f32 v164, v211, v164\n"
      "v_mul_f32_e64 v165, v211, v165\n"
      "v_mfma_f32_16x16x32_bf16 v[228:231], v[180:183], a[140:143], v[228:231]\n"
      "s_lshl_b32 s4, s4, 30\n"
      "v_mul_f32 v166, v211, v166\n"
      "v_cvt_pk_bf16_f32 v164, v164, v165\n"
      "v_mfma_f32_16x16x32_bf16 v[176:179], a[144:147], v[244:247], v[176:179]\n"
      "v_mul_f32 v167, v211, v167\n"
      "v_cvt_pk_bf16_f32 v165, v166, v167\n"
      "v_mfma_f32_16x16x32_bf16 v[176:179], a[180:183], v[184:187], v[176:179]\n"
      "ds_read_b128 v[232:235], v119 offset:18432\n"
      "v_mul_f32 v156, v211, v156\n"
      "v_mfma_f32_16x16x32_bf16 v[152:155], a[148:151], v[244:247], v[152:155]\n"
      "v_mul_f32 v157, v211, v157\n"
      "v_cvt_pk_bf16_f32 v166, v156, v157\n"
      "v_mfma_f32_16x16x32_bf16 v[152:155], a[184:187], v[184:187], v[152:155]\n"
      "v_mul_f32 v158, v211, v158\n"
      "v_mul_f32 v159, v211, v159\n"
      "v_mfma_f32_16x16x32_bf16 v[200:203], a[152:155], v[244:247], v[200:203]\n"
      "v_cvt_pk_bf16_f32 v167, v158, v159\n"
      "v_mul_f32 v136, v211, v136\n"
      "v_mfma_f32_16x16x32_bf16 v[200:203], a[188:191], v[184:187], v[200:203]\n"
      "s_and_b32 s4, s4, 0x80000000\n"
      "v_mul_f32 v137, v211, v137\n"
      "v_cvt_pk_bf16_f32 v34, v136, v137\n"
      "v_mfma_f32_16x16x32_bf16 v[168:171], a[156:159], v[244:247], v[168:171]\n"
      "v_mul_f32 v139, v211, v139\n"
      "v_mul_f32_e64 v138, v211, v138\n"
      "v_mfma_f32_16x16x32_bf16 v[168:171], a[192:195], v[184:187], v[168:171]\n"
      "ds_read_b128 v[180:183], v114 offset:10240\n"
      "v_cvt_pk_bf16_f32 v35, v138, v139\n"
      "v_mfma_f32_16x16x32_bf16 v[240:243], a[160:163], v[244:247], v[240:243]\n"
      "ds_read_b128 v[136:139], v115 offset:10240\n"
      "v_mul_f32 v209, s36, v209\n"
      "v_mfma_f32_16x16x32_bf16 v[240:243], a[196:199], v[184:187], v[240:243]\n"
      "s_waitcnt lgkmcnt(2)\n"
      "v_mul_f32 v205, v209, v205\n"
      "v_mul_f32 v204, v209, v204\n"
      "v_mfma_f32_16x16x32_bf16 v[212:215], a[164:167], v[244:247], v[212:215]\n"
      "global_store_dwordx4 v116, v[232:235], s[54:55] sc1\n"
      "v_mfma_f32_16x16x32_bf16 v[212:215], a[200:203], v[184:187], v[212:215]\n"
      "v_cvt_pk_bf16_f32 v204, v204, v205\n"
      "v_mul_f32 v206, v209, v206\n"
      "v_mfma_f32_16x16x32_bf16 v[248:251], v[244:247], a[168:171], v[248:251]\n"
      "v_mul_f32 v207, v209, v207\n"
      "v_cvt_pk_bf16_f32 v205, v206, v207\n"
      "v_mfma_f32_16x16x32_bf16 v[248:251], v[184:187], a[204:207], v[248:251]\n"
      "v_mul_f32 v160, v209, v160\n"
      "v_mul_f32 v161, v209, v161\n"
      "v_mfma_f32_16x16x32_bf16 v[188:191], v[244:247], a[172:175], v[188:191]\n"
      "v_mul_f32_e64 v162, v209, v162\n"
      "v_cvt_pk_bf16_f32 v206, v160, v161\n"
      "v_mfma_f32_16x16x32_bf16 v[188:191], v[184:187], a[208:211], v[188:191]\n"
      "v_mul_f32_e64 v163, v209, v163\n"
      "v_cvt_pk_bf16_f32 v207, v162, v163\n"
      "v_mfma_f32_16x16x32_bf16 v[228:231], v[244:247], a[176:179], v[228:231]\n"
      "v_mul_f32 v196, v209, v196\n"
      "v_mul_f32_e64 v197, v209, v197\n"
      "v_mfma_f32_16x16x32_bf16 v[228:231], v[184:187], a[212:215], v[228:231]\n"
      "s_waitcnt lgkmcnt(0)\n"
      "v_cvt_pk_bf16_f32 v32, v196, v197\n"
      "v_mul_f32 v198, v209, v198\n"
      "v_mfma_f32_16x16x32_bf16 v[184:187], a[0:3], v[192:195], 0\n"
      "v_mul_f32 v199, v209, v199\n"
      "v_cvt_pk_bf16_f32 v33, v198, v199\n"
      "v_mfma_f32_16x16x32_bf16 v[184:187], a[36:39], v[148:151], v[184:187]\n"
      "v_cvt_pk_bf16_f32 v222, v248, v249\n"
      "v_cvt_pk_bf16_f32 v223, v250, v251\n"
      "v_mfma_f32_16x16x32_bf16 v[244:247], a[4:7], v[192:195], 0\n"
      "v_cvt_pk_bf16_f32 v236, v236, v237\n"
      "v_cvt_pk_bf16_f32 v237, v238, v239\n"
      "v_mfma_f32_16x16x32_bf16 v[244:247], a[40:43], v[148:151], v[244:247]\n"
      "v_cvt_pk_bf16_f32 v238, v188, v189\n"
      "v_cvt_pk_bf16_f32 v239, v190, v191\n"
      "v_mfma_f32_16x16x32_bf16 v[196:199], a[8:11], v[192:195], 0\n"
      "ds_read_b128 v[232:235], v119 offset:24576\n"
      "v_cvt_pk_bf16_f32 v252, v252, v253\n"
      "v_mfma_f32_16x16x32_bf16 v[196:199], a[44:47], v[148:151], v[196:199]\n"
      "v_cvt_pk_bf16_f32 v253, v254, v255\n"
      "v_cvt_pk_bf16_f32 v254, v228, v229\n"
      "v_mfma_f32_16x16x32_bf16 v[248:251], a[12:15], v[192:195], 0\n"
      "v_fma_f32 v209, v176, v176, s38\n"
      "v_cvt_pk_bf16_f32 v255, v230, v231\n"
      "v_mfma_f32_16x16x32_bf16 v[248:251], a[48:51], v[148:151], v[248:251]\n"
      "v_fma_f32 v211, v168, v168, s38\n"
      "v_fmac_f32 v209, v177, v177\n"
      "v_mfma_f32_16x16x32_bf16 v[188:191], a[16:19], v[192:195], 0\n"
      "v_fmac_f32 v209, v178, v178\n"
      "v_fmac_f32 v211, v169, v169\n"
      "v_mfma_f32_16x16x32_bf16 v[188:191], a[52:55], v[148:151], v[188:191]\n"
      "v_fmac_f32 v209, v179, v179\n"
      "v_fmac_f32 v211, v170, v170\n"
      "v_mfma_f32_16x16x32_bf16 v[228:231], a[20:23], v[192:195], 0\n"
      "v_fmac_f32 v211, v171, v171\n"
      "v_fmac_f32 v209, v152, v152\n"
      "v_mfma_f32_16x16x32_bf16 v[228:231], a[56:59], v[148:151], v[228:231]\n"
      "v_fmac_f32 v209, v153, v153\n"
      "v_fmac_f32 v211, v240, v240\n"
      "v_mfma_f32_16x16x32_bf16 v[160:163], v[192:195], a[24:27], 0\n"
      "s_waitcnt lgkmcnt(0)\n"
      "v_fmac_f32_e64 v211, v241, v241\n"
      "v_mfma_f32_16x16x32_bf16 v[160:163], v[148:151], a[60:63], v[160:163]\n"
      "global_store_dwordx4 v116, v[232:235], s[54:55] offset:128 sc1\n"
      "v_mfma_f32_16x16x32_bf16 v[156:159], v[192:195], a[28:31], 0\n"
      "ds_read_b128 v[224:227], v114 offset:16384\n"
      "v_fmac_f32_e64 v209, v154, v154\n"
      "v_mfma_f32_16x16x32_bf16 v[156:159], v[148:151], a[64:67], v[156:159]\n"
      "v_fmac_f32 v211, v242, v242\n"
      "v_fmac_f32 v209, v155, v155\n"
      "v_mfma_f32_16x16x32_bf16 v[172:175], v[192:195], a[32:35], 0\n"
      "ds_read_b128 v[232:235], v115 offset:16384\n"
      "v_fmac_f32 v211, v243, v243\n"
      "v_mfma_f32_16x16x32_bf16 v[172:175], v[148:151], a[68:71], v[172:175]\n"
      "v_fmac_f32 v209, v200, v200\n"
      "v_fmac_f32 v211, v212, v212\n"
      "v_mfma_f32_16x16x32_bf16 v[184:187], a[72:75], v[180:183], v[184:187]\n"
      "v_fmac_f32 v211, v213, v213\n"
      "v_fmac_f32 v209, v201, v201\n"
      "v_mfma_f32_16x16x32_bf16 v[184:187], a[108:111], v[136:139], v[184:187]\n"
      "v_fmac_f32 v211, v214, v214\n"
      "v_fmac_f32 v209, v202, v202\n"
      "v_mfma_f32_16x16x32_bf16 v[244:247], a[76:79], v[180:183], v[244:247]\n"
      "v_fmac_f32 v209, v203, v203\n"
      "v_fmac_f32 v211, v215, v215\n"
      "v_mfma_f32_16x16x32_bf16 v[244:247], a[112:115], v[136:139], v[244:247]\n"
      "s_nop 0\n"
      "v_permlane16_swap_b32 v209, v211\n"
      "v_mfma_f32_16x16x32_bf16 v[196:199], a[80:83], v[180:183], v[196:199]\n"
      "v_add_f32 v209, v209, v211\n"
      "v_mov_b32 v211, v209\n"
      "v_mfma_f32_16x16x32_bf16 v[196:199], a[116:119], v[136:139], v[196:199]\n"
      "s_nop 0\n"
      "v_permlane32_swap_b32 v209, v211\n"
      "v_mfma_f32_16x16x32_bf16 v[248:251], a[84:87], v[180:183], v[248:251]\n"
      "v_add_f32 v209, v209, v211\n"
      "v_mfma_f32_16x16x32_bf16 v[248:251], a[120:123], v[136:139], v[248:251]\n"
      "v_rsq_f32 v209, v209\n"
      "v_mfma_f32_16x16x32_bf16 v[188:191], a[88:91], v[180:183], v[188:191]\n"
      "v_mov_b32 v211, v209\n"
      "v_add_u32_e64 v135, 32, v135\n"
      "v_mfma_f32_16x16x32_bf16 v[188:191], a[124:127], v[136:139], v[188:191]\n"
      "v_permlane16_swap_b32 v209, v211\n"
      "v_mfma_f32_16x16x32_bf16 v[228:231], a[92:95], v[180:183], v[228:231]\n"
      "v_mul_f32 v168, v211, v168\n"
      "v_mul_f32_e64 v169, v211, v169\n"
      "v_mfma_f32_16x16x32_bf16 v[228:231], a[128:131], v[136:139], v[228:231]\n"
      "ds_read_b128 v[192:195], v132 offset:32\n"
      "v_cvt_pk_bf16_f32 v168, v168, v169\n"
      "v_mfma_f32_16x16x32_bf16 v[160:163], v[180:183], a[96:99], v[160:163]\n"
      "s_waitcnt lgkmcnt(1)\n"
      "v_mul_f32 v171, v211, v171\n"
      "v_mul_f32_e64 v170, v211, v170\n"
      "v_mfma_f32_16x16x32_bf16 v[160:163], v[136:139], a[132:135], v[160:163]\n"
      "ds_read_b64 v[148:149], v132 offset:24\n"
      "v_cvt_pk_bf16_f32 v169, v170, v171\n"
      "v_mfma_f32_16x16x32_bf16 v[156:159], v[180:183], a[100:103], v[156:159]\n"
      "ds_read_u16 v52, v134\n"
      "v_mul_f32_e64 v240, v211, v240\n"
      "v_mfma_f32_16x16x32_bf16 v[156:159], v[136:139], a[136:139], v[156:159]\n"
      "ds_read_u16 v56, v134 offset:32\n"
      "v_mul_f32_e64 v241, v211, v241\n"
      "v_mfma_f32_16x16x32_bf16 v[172:175], v[180:183], a[104:107], v[172:175]\n"
      "ds_read_b128 v[140:143], v133\n"
      "v_cvt_pk_bf16_f32 v170, v240, v241\n"
      "v_mfma_f32_16x16x32_bf16 v[172:175], v[136:139], a[140:143], v[172:175]\n"
      "ds_read_b64 v[150:151], v133 offset:16\n"
      "v_mul_f32 v242, v211, v242\n"
      "v_mfma_f32_16x16x32_bf16 v[184:187], a[144:147], v[224:227], v[184:187]\n"
      "ds_read_u16 v60, v134 offset:64\n"
      "v_mul_f32 v243, v211, v243\n"
      "v_mfma_f32_16x16x32_bf16 v[184:187], a[180:183], v[232:235], v[184:187]\n"
      "ds_read_b128 v[136:139], v123 offset:18432\n"
      "v_cvt_pk_bf16_f32 v171, v242, v243\n"
      "v_mfma_f32_16x16x32_bf16 v[244:247], a[148:151], v[224:227], v[244:247]\n"
      "v_mul_f32 v213, v211, v213\n"
      "v_mul_f32 v212, v211, v212\n"
      "v_mfma_f32_16x16x32_bf16 v[244:247], a[184:187], v[232:235], v[244:247]\n"
      "v_mul_f32_e64 v214, v211, v214\n"
      "v_cvt_pk_bf16_f32 v40, v212, v213\n"
      "v_mfma_f32_16x16x32_bf16 v[196:199], a[152:155], v[224:227], v[196:199]\n"
      "ds_read_b128 v[240:243], v123 offset:21504\n"
      "v_mul_f32 v215, v211, v215\n"
      "v_mfma_f32_16x16x32_bf16 v[196:199], a[188:191], v[232:235], v[196:199]\n"
      "v_mul_f32 v209, s36, v209\n"
      "v_cvt_pk_bf16_f32 v41, v214, v215\n"
      "v_mfma_f32_16x16x32_bf16 v[248:251], a[156:159], v[224:227], v[248:251]\n"
      "v_mul_f32 v176, v209, v176\n"
      "v_mul_f32 v177, v209, v177\n"
      "v_mfma_f32_16x16x32_bf16 v[248:251], a[192:195], v[232:235], v[248:251]\n"
      "v_cvt_pk_bf16_f32 v176, v176, v177\n"
      "v_mul_f32 v178, v209, v178\n"
      "v_mfma_f32_16x16x32_bf16 v[188:191], a[160:163], v[224:227], v[188:191]\n"
      "v_mul_f32 v179, v209, v179\n"
      "v_cvt_pk_bf16_f32 v177, v178, v179\n"
      "v_mfma_f32_16x16x32_bf16 v[188:191], a[196:199], v[232:235], v[188:191]\n"
      "v_mul_f32 v153, v209, v153\n"
      "v_mul_f32 v152, v209, v152\n"
      "v_mfma_f32_16x16x32_bf16 v[228:231], a[164:167], v[224:227], v[228:231]\n"
      "s_or_b64 s[34:35], s[48:49], s[50:51]\n"
      "v_mul_f32 v202, v209, v202\n"
      "v_mfma_f32_16x16x32_bf16 v[228:231], a[200:203], v[232:235], v[228:231]\n"
      "ds_read_b128 v[212:215], v123 offset:24576\n"
      "v_mul_f32 v154, v209, v154\n"
      "v_mfma_f32_16x16x32_bf16 v[160:163], v[224:227], a[168:171], v[160:163]\n"
      "v_mul_f32 v155, v209, v155\n"
      "v_mul_f32_e64 v201, v209, v201\n"
      "v_mfma_f32_16x16x32_bf16 v[160:163], v[232:235], a[204:207], v[160:163]\n"
      "v_cvt_pk_bf16_f32 v179, v154, v155\n"
      "v_mul_f32 v200, v209, v200\n"
      "v_mfma_f32_16x16x32_bf16 v[156:159], v[224:227], a[172:175], v[156:159]\n"
      "s_waitcnt lgkmcnt(1)\n"
      "v_cvt_pk_bf16_f32 v178, v152, v153\n"
      "v_cvt_pk_bf16_f32 v38, v200, v201\n"
      "v_mfma_f32_16x16x32_bf16 v[156:159], v[232:235], a[208:211], v[156:159]\n"
      "ds_read_b128 v[144:147], v123 offset:27648\n"
      "v_mul_f32_e64 v203, v209, v203\n"
      "v_mfma_f32_16x16x32_bf16 v[172:175], v[224:227], a[176:179], v[172:175]\n"
      "v_fma_f32 v226, v248, v248, s38\n"
      "v_cvt_pk_bf16_f32 v39, v202, v203\n"
      "v_mfma_f32_16x16x32_bf16 v[172:175], v[232:235], a[212:215], v[172:175]\n"
      "v_fmac_f32 v226, v249, v249\n"
      "v_fmac_f32 v226, v250, v250\n"
      "v_mfma_f32_16x16x32_bf16 v[232:235], a[216:219], v[136:139], 0\n"
      "v_fma_f32 v225, v184, v184, s38\n"
      "v_fmac_f32 v225, v185, v185\n"
      "v_mfma_f32_16x16x32_bf16 v[232:235], a[228:231], v[240:243], v[232:235]\n"
      "v_fmac_f32 v225, v186, v186\n"
      "v_cvt_pk_bf16_f32 v50, v160, v161\n"
      "v_mfma_f32_16x16x32_bf16 v[152:155], a[220:223], v[136:139], 0\n"
      "v_fmac_f32 v226, v251, v251\n"
      "v_fmac_f32 v225, v187, v187\n"
      "v_mfma_f32_16x16x32_bf16 v[152:155], a[232:235], v[240:243], v[152:155]\n"
      "v_cvt_pk_bf16_f32 v55, v158, v159\n"
      "v_fmac_f32 v225, v244, v244\n"
      "v_fmac_f32 v226, v188, v188\n"
      "v_mfma_f32_16x16x32_bf16 v[180:183], a[224:227], v[136:139], 0\n"
      "v_fmac_f32 v225, v245, v245\n"
      "v_fmac_f32 v226, v189, v189\n"
      "v_mfma_f32_16x16x32_bf16 v[180:183], a[236:239], v[240:243], v[180:183]\n"
      "s_waitcnt lgkmcnt(0)\n"
      "v_fmac_f32 v225, v246, v246\n"
      "v_fmac_f32 v226, v190, v190\n"
      "v_mfma_f32_16x16x32_bf16 v[232:235], a[240:243], v[212:215], v[232:235]\n"
      "ds_read_b128 v[240:243], v123 offset:30720\n"
      "v_fmac_f32 v225, v247, v247\n"
      "v_mfma_f32_16x16x32_bf16 v[232:235], a[252:255], v[144:147], v[232:235]\n"
      "ds_read_b128 v[200:203], v123 offset:33792\n"
      "v_fmac_f32 v226, v191, v191\n"
      "v_mfma_f32_16x16x32_bf16 v[152:155], a[244:247], v[212:215], v[152:155]\n"
      "v_fmac_f32 v225, v196, v196\n"
      "v_fmac_f32_e64 v226, v228, v228\n"
      "v_mfma_f32_16x16x32_bf16 v[152:155], v[0:3], v[144:147], v[152:155]\n"
      "v_fmac_f32 v225, v197, v197\n"
      "v_fmac_f32 v226, v229, v229\n"
      "v_mfma_f32_16x16x32_bf16 v[180:183], a[248:251], v[212:215], v[180:183]\n"
      "v_fmac_f32 v225, v198, v198\n"
      "v_fmac_f32 v226, v230, v230\n"
      "v_mfma_f32_16x16x32_bf16 v[180:183], v[4:7], v[144:147], v[180:183]\n"
      "v_fmac_f32 v225, v199, v199\n"
      "v_fmac_f32 v226, v231, v231\n"
      "v_mfma_f32_16x16x32_bf16 v[212:215], v[164:167], v[204:207], v[78:81]\n"
      "s_nop 0\n"
      "v_permlane16_swap_b32 v225, v226\n"
      "v_mfma_f32_16x16x32_bf16 v[212:215], v[34:37], v[32:35], v[212:215]\n"
      "v_add_f32 v225, v225, v226\n"
      "v_mov_b32 v226, v225\n"
      "v_mfma_f32_16x16x32_bf16 v[144:147], v[168:171], v[204:207], v[82:85]\n"
      "s_nop 0\n"
      "v_permlane32_swap_b32 v225, v226\n"
      "v_add_f32 v225, v225, v226\n"
      "v_mfma_f32_16x16x32_bf16 v[144:147], v[40:43], v[32:35], v[144:147]\n"
      "s_waitcnt lgkmcnt(0)\n"
      "v_rsq_f32 v225, v225\n"
      "v_mfma_f32_16x16x32_bf16 v[232:235], v[8:11], v[240:243], v[232:235]\n"
      "v_mov_b32 v226, v225\n"
      "v_cvt_pk_bf16_f32 v51, v162, v163\n"
      "v_mfma_f32_16x16x32_bf16 v[232:235], v[20:23], v[200:203], v[232:235]\n"
      "v_permlane16_swap_b32 v225, v226\n"
      "v_mul_f32 v188, v226, v188\n"
      "v_mul_f32 v230, v226, v230\n"
      "v_mul_f32 v250, v226, v250\n"
      "v_mul_f32 v189, v226, v189\n"
      "v_mul_f32 v225, s36, v225\n"
      "v_mfma_f32_16x16x32_bf16 v[152:155], v[12:15], v[240:243], v[152:155]\n"
      "v_mul_f32 v248, v226, v248\n"
      "v_mul_f32 v249, v226, v249\n"
      "v_mfma_f32_16x16x32_bf16 v[152:155], v[24:27], v[200:203], v[152:155]\n"
      "v_cvt_pk_bf16_f32 v248, v248, v249\n"
      "v_mul_f32 v228, v226, v228\n"
      "v_mfma_f32_16x16x32_bf16 v[180:183], v[16:19], v[240:243], v[180:183]\n"
      "v_mul_f32 v251, v226, v251\n"
      "v_cvt_pk_bf16_f32 v249, v250, v251\n"
      "v_mfma_f32_16x16x32_bf16 v[180:183], v[28:31], v[200:203], v[180:183]\n"
      "v_mul_f32_e64 v231, v226, v231\n"
      "v_cvt_pk_bf16_f32 v47, v230, v231\n"
      "v_mfma_f32_16x16x32_bf16 v[240:243], v[192:195], v[204:207], v[70:73]\n"
      "v_cvt_pk_bf16_f32 v250, v188, v189\n"
      "v_mul_f32 v190, v226, v190\n"
      "v_mfma_f32_16x16x32_bf16 v[240:243], v[140:143], v[164:167], v[240:243]\n"
      "v_mul_f32 v191, v226, v191\n"
      "v_cvt_pk_bf16_f32 v251, v190, v191\n"
      "v_mfma_f32_16x16x32_bf16 v[240:243], v[148:151], v[32:35], v[240:243]\n"
      "v_mul_f32 v184, v225, v184\n"
      "v_mul_f32 v229, v226, v229\n"
      "v_cvt_pk_bf16_f32 v46, v228, v229\n"
      "v_mfma_f32_16x16x32_bf16 v[200:203], v[248:251], v[204:207], v[86:89]\n"
      "s_waitcnt vmcnt(2)\n"
      "v_mul_f32 v185, v225, v185\n"
      "v_cvt_pk_bf16_f32 v184, v184, v185\n"
      "v_mfma_f32_16x16x32_bf16 v[200:203], v[46:49], v[32:35], v[200:203]\n"
      "ds_read_b32 v243, v122 offset:4864\n"
      "ds_read_b128 v[188:191], v119 offset:41728\n"
      "v_fma_f32 v159, v232, v232, s39\n"
      "v_mul_f32 v187, v225, v187\n"
      "v_mul_f32 v199, v225, v199\n"
      "v_fmac_f32_e64 v159, v233, v233\n"
      "v_cvt_pk_bf16_f32 v59, v174, v175\n"
      "v_mul_f32 v246, v225, v246\n"
      "v_fmac_f32 v159, v234, v234\n"
      "v_mul_f32 v247, v225, v247\n"
      "v_mul_f32 v186, v225, v186\n"
      "v_mul_f32 v244, v225, v244\n"
      "v_fmac_f32 v159, v235, v235\n"
      "v_fmac_f32 v159, v152, v152\n"
      "v_fmac_f32 v159, v153, v153\n"
      "v_mul_f32 v197, v225, v197\n"
      "v_fmac_f32 v159, v154, v154\n"
      "v_fmac_f32 v159, v155, v155\n"
      "v_mul_f32 v245, v225, v245\n"
      "v_mul_f32 v196, v225, v196\n"
      "v_fmac_f32 v159, v180, v180\n"
      "v_cvt_pk_bf16_f32 v44, v196, v197\n"
      "v_mul_f32_e64 v198, v225, v198\n"
      "v_cvt_pk_bf16_f32 v45, v198, v199\n"
      "v_cvt_pk_bf16_f32 v58, v172, v173\n"
      "v_fmac_f32_e64 v159, v181, v181\n"
      "v_cvt_pk_bf16_f32 v185, v186, v187\n"
      "v_cvt_pk_bf16_f32 v187, v246, v247\n"
      "v_cvt_pk_bf16_f32 v54, v156, v157\n"
      "v_fmac_f32 v159, v182, v182\n"
      "v_fmac_f32_e64 v159, v183, v183\n"
      "v_cvt_pk_bf16_f32 v186, v244, v245\n"
      "s_waitcnt lgkmcnt(0)\n"
      "v_or3_b32 v188, v188, v189, v190\n"
      "v_and_b32 v188, s40, v188\n"
      "v_bitop3_b32 v243, v243, v191, s40 bitop3:0xa8\n"
      "v_or3_b32 v188, v188, v243, s4\n"
      "ds_write_b32 v129, v188 offset:3072\n"
      "s_cbranch_scc1 L_gen_28_%=\n"
      "L_fast_31_%=:\n"
      "v_mfma_f32_16x16x32_bf16 v[196:199], v[164:167], v[176:179], v[90:93]\n"
      "ds_read_b128 v[244:247], v119 offset:30720\n"
      "v_exp_f32 v200, v200\n"
      "v_exp_f32 v146, v146\n"
      "v_mfma_f32_16x16x32_bf16 v[196:199], v[34:37], v[38:41], v[196:199]\n"
      "ds_read_b128 v[204:207], v123 offset:19456\n"
      "v_exp_f32 v213, v213\n"
      "v_mfma_f32_16x16x32_bf16 v[188:191], v[168:171], v[176:179], v[94:97]\n"
      "ds_read_b128 v[160:163], v123 offset:22528\n"
      "v_exp_f32 v202, v202\n"
      "v_mfma_f32_16x16x32_bf16 v[188:191], v[40:43], v[38:41], v[188:191]\n"
      "ds_read_b128 v[172:175], v128 offset:3072\n"
      "v_exp_f32 v203, v203\n"
      "v_exp_f32 v215, v215\n"
      "v_exp_f32_e64 v212, v212\n"
      "v_mfma_f32_16x16x32_bf16 v[228:231], v[192:195], v[176:179], v[72:75]\n"
      "ds_read_b128 v[136:139], v123 offset:25600\n"
      "v_cvt_pk_bf16_f32 v212, v212, v213\n"
      "v_mfma_f32_16x16x32_bf16 v[228:231], v[140:143], v[168:171], v[228:231]\n"
      "v_exp_f32 v214, v214\n"
      "v_mfma_f32_16x16x32_bf16 v[228:231], v[148:151], v[38:41], v[228:231]\n"
      "ds_read_b128 v[208:211], v123 offset:28672\n"
      "v_exp_f32 v147, v147\n"
      "v_exp_f32 v145, v145\n"
      "v_exp_f32 v240, v240\n"
      "v_mfma_f32_16x16x32_bf16 v[224:227], v[248:251], v[176:179], v[98:101]\n"
      "s_waitcnt lgkmcnt(4)\n"
      "v_exp_f32 v201, v201\n"
      "v_mfma_f32_16x16x32_bf16 v[224:227], v[46:49], v[38:41], v[224:227]\n"
      "s_waitcnt lgkmcnt(3)\n"
      "v_exp_f32 v144, v144\n"
      "v_mfma_f32_16x16x32_bf16 v[176:179], a[216:219], v[204:207], 0\n"
      "global_store_dwordx4 v116, v[244:247], s[54:55] offset:256 sc1\n"
      "v_exp_f32 v196, v196\n"
      "v_mfma_f32_16x16x32_bf16 v[176:179], a[228:231], v[160:163], v[176:179]\n"
      "s_waitcnt lgkmcnt(2)\n"
      "v_cvt_pk_bf16_f32 v62, v200, v201\n"
      "v_cvt_pk_bf16_f32 v63, v202, v203\n"
      "v_cvt_pk_bf16_f32 v213, v214, v215\n"
      "v_mfma_f32_16x16x32_bf16 v[244:247], a[220:223], v[204:207], 0\n"
      "v_bitop3_b32 v174, v174, v175, s4 bitop3:0x7e\n"
      "v_bitop3_b32 v172, v172, v173, s4 bitop3:0x7e\n"
      "v_mfma_f32_16x16x32_bf16 v[244:247], a[232:235], v[160:163], v[244:247]\n"
      "s_waitcnt lgkmcnt(0)\n"
      "v_or_b32 v172, v172, v174\n"
      "v_cmp_ne_u32 vcc, 0, v172\n"
      "v_mfma_f32_16x16x32_bf16 v[200:203], a[224:227], v[204:207], 0\n"
      "ds_read_b128 v[204:207], v123 offset:31744\n"
      "s_mov_b64 s[48:49], vcc\n"
      "v_mfma_f32_16x16x32_bf16 v[200:203], a[236:239], v[160:163], v[200:203]\n"
      "ds_read_b128 v[160:163], v123 offset:34816\n"
      "v_exp_f32 v197, v197\n"
      "v_exp_f32 v199, v199\n"
      "v_cvt_pk_bf16_f32 v214, v144, v145\n"
      "v_cvt_pk_bf16_f32 v215, v146, v147\n"
      "v_mfma_f32_16x16x32_bf16 v[176:179], a[240:243], v[136:139], v[176:179]\n"
      "v_exp_f32 v189, v189\n"
      "v_mfma_f32_16x16x32_bf16 v[176:179], a[252:255], v[208:211], v[176:179]\n"
      "ds_read_b128 v[144:147], v120 offset:18432\n"
      "v_cvt_pk_bf16_f32 v196, v196, v197\n"
      "v_exp_f32 v191, v191\n"
      "v_exp_f32 v188, v188\n"
      "v_mfma_f32_16x16x32_bf16 v[244:247], a[244:247], v[136:139], v[244:247]\n"
      "v_exp_f32 v198, v198\n"
      "v_mfma_f32_16x16x32_bf16 v[244:247], v[0:3], v[208:211], v[244:247]\n"
      "s_cbranch_vccnz L_flg_32_%=\n"
      "L_fb_33_%=:\n"
      "v_mfma_f32_16x16x32_bf16 v[200:203], a[248:251], v[136:139], v[200:203]\n"
      "ds_read_b128 v[136:139], v123 offset:20480\n"
      "v_mfma_f32_16x16x32_bf16 v[200:203], v[4:7], v[208:211], v[200:203]\n"
      "s_waitcnt lgkmcnt(2)\n"
      "v_cvt_pk_bf16_f32 v197, v198, v199\n"
      "v_cvt_pk_bf16_f32 v64, v240, 0\n"
      "v_mfma_f32_16x16x32_bf16 v[176:179], v[8:11], v[204:207], v[176:179]\n"
      "v_exp_f32_e64 v190, v190\n"
      "v_mfma_f32_16x16x32_bf16 v[176:179], v[20:23], v[160:163], v[176:179]\n"
      "ds_read_b128 v[208:211], v123 offset:23552\n"
      "v_cvt_pk_bf16_f32 v198, v188, v189\n"
      "v_mfma_f32_16x16x32_bf16 v[244:247], v[12:15], v[204:207], v[244:247]\n"
      "s_waitcnt lgkmcnt(2)\n"
      "v_exp_f32 v224, v224\n"
      "v_mfma_f32_16x16x32_bf16 v[244:247], v[24:27], v[160:163], v[244:247]\n"
      "global_store_dwordx4 v117, v[144:147], s[54:55] sc1\n"
      "v_mfma_f32_16x16x32_bf16 v[200:203], v[16:19], v[204:207], v[200:203]\n"
      "v_cvt_pk_bf16_f32 v199, v190, v191\n"
      "v_fma_f32 v156, v176, v176, s39\n"
      "v_mfma_f32_16x16x32_bf16 v[200:203], v[28:31], v[160:163], v[200:203]\n"
      "v_exp_f32_e64 v225, v225\n"
      "v_mfma_f32_16x16x32_bf16 v[188:191], v[66:69], v[212:215], 0\n"
      "v_exp_f32 v226, v226\n"
      "v_mfma_f32_16x16x32_bf16 v[188:191], v[66:69], v[62:65], v[188:191]\n"
      "v_exp_f32 v227, v227\n"
      "v_mfma_f32_16x16x32_bf16 v[204:207], v[220:223], v[212:215], 0\n"
      "v_exp_f32 v228, v228\n"
      "v_mfma_f32_16x16x32_bf16 v[204:207], v[50:53], v[62:65], v[204:207]\n"
      "v_fmac_f32 v156, v177, v177\n"
      "v_fmac_f32 v156, v178, v178\n"
      "v_mfma_f32_16x16x32_bf16 v[160:163], v[236:239], v[212:215], 0\n"
      "v_fmac_f32 v156, v179, v179\n"
      "v_fmac_f32 v156, v244, v244\n"
      "v_mfma_f32_16x16x32_bf16 v[160:163], v[54:57], v[62:65], v[160:163]\n"
      "v_fmac_f32 v156, v245, v245\n"
      "v_fmac_f32 v156, v246, v246\n"
      "v_mfma_f32_16x16x32_bf16 v[144:147], v[252:255], v[212:215], 0\n"
      "v_fmac_f32 v156, v247, v247\n"
      "v_fmac_f32 v156, v200, v200\n"
      "v_mfma_f32_16x16x32_bf16 v[144:147], v[58:61], v[62:65], v[144:147]\n"
      "v_fmac_f32 v156, v201, v201\n"
      "v_fmac_f32 v156, v202, v202\n"
      "v_mfma_f32_16x16x32_bf16 v[216:219], v[164:167], v[184:187], v[102:105]\n"
      "v_fmac_f32 v156, v203, v203\n"
      "v_cvt_pk_bf16_f32 v62, v224, v225\n"
      "v_mfma_f32_16x16x32_bf16 v[216:219], v[34:37], v[44:47], v[216:219]\n"
      "ds_read_b128 v[212:215], v121 offset:18432\n"
      "v_cvt_pk_bf16_f32 v63, v226, v227\n"
      "v_mfma_f32_16x16x32_bf16 v[164:167], v[168:171], v[184:187], v[106:109]\n"
      "v_cndmask_b32 v241, v241, v229, s[42:43]\n"
      "v_cvt_pk_bf16_f32 v64, v228, 0\n"
      "v_mfma_f32_16x16x32_bf16 v[164:167], v[40:43], v[44:47], v[164:167]\n"
      "v_rcp_f32 v188, v188\n"
      "v_mfma_f32_16x16x32_bf16 v[228:231], v[192:195], v[184:187], v[74:77]\n"
      "s_waitcnt lgkmcnt(1)\n"
      "v_mul_f32 v205, v188, v205\n"
      "v_mul_f32 v204, v188, v204\n"
      "v_mfma_f32_16x16x32_bf16 v[228:231], v[140:143], v[248:251], v[228:231]\n"
      "v_cvt_pk_bf16_f32 v204, v204, v205\n"
      "v_mul_f32 v206, v188, v206\n"
      "v_mfma_f32_16x16x32_bf16 v[228:231], v[148:151], v[44:47], v[228:231]\n"
      "v_mul_f32 v207, v188, v207\n"
      "v_cvt_pk_bf16_f32 v205, v206, v207\n"
      "v_mfma_f32_16x16x32_bf16 v[224:227], v[248:251], v[184:187], v[110:113]\n"
      "ds_read_b128 v[168:171], v123 offset:26624\n"
      "v_mul_f32 v160, v188, v160\n"
      "v_mfma_f32_16x16x32_bf16 v[224:227], v[46:49], v[44:47], v[224:227]\n"
      "v_mul_f32 v161, v188, v161\n"
      "v_cvt_pk_bf16_f32 v206, v160, v161\n"
      "v_mfma_f32_16x16x32_bf16 v[184:187], v[140:143], v[192:195], v[76:79]\n"
      "ds_read_b128 v[192:195], v123 offset:29696\n"
      "v_mul_f32_e64 v162, v188, v162\n"
      "v_mfma_f32_16x16x32_bf16 v[140:143], v[66:69], v[196:199], 0\n"
      "ds_read_b128 v[248:251], v123 offset:32768\n"
      "v_mul_f32 v163, v188, v163\n"
      "v_mfma_f32_16x16x32_bf16 v[140:143], v[66:69], v[62:65], v[140:143]\n"
      "s_waitcnt lgkmcnt(3)\n"
      "v_mul_f32_e64 v144, v188, v144\n"
      "v_cvt_pk_bf16_f32 v207, v162, v163\n"
      "v_mfma_f32_16x16x16_bf16 v[184:187], v[150:151], v[148:149], v[184:187]\n"
      "ds_write_b128 v124, v[204:207]\n"
      "v_mfma_f32_16x16x32_bf16 v[204:207], v[220:223], v[196:199], 0\n"
      "v_mul_f32 v145, v188, v145\n"
      "v_cvt_pk_bf16_f32 v144, v144, v145\n"
      "v_mfma_f32_16x16x32_bf16 v[204:207], v[50:53], v[62:65], v[204:207]\n"
      "s_mov_b32 exec_hi, 0\n"
      "global_store_dwordx4 v118, v[212:215], s[54:55] sc1\n"
      "s_mov_b32 exec_hi, -1\n"
      "v_mfma_f32_16x16x32_bf16 v[148:151], v[236:239], v[196:199], 0\n"
      "s_add_u32 s28, s27, 2\n"
      "v_mul_f32_e64 v146, v188, v146\n"
      "v_mul_f32 v147, v188, v147\n"
      "v_mfma_f32_16x16x32_bf16 v[148:151], v[54:57], v[62:65], v[148:151]\n"
      "s_waitcnt lgkmcnt(2)\n"
      "v_cndmask_b32 v241, v241, v229, s[44:45]\n"
      "v_cvt_pk_bf16_f32 v145, v146, v147\n"
      "v_mfma_f32_16x16x32_bf16 v[160:163], v[252:255], v[196:199], 0\n"
      "s_min_u32 m0, s28, s3\n"
      "v_cndmask_b32 v241, v241, v185, s[46:47]\n"
      "s_movrels_b32 s9, s80\n"
      "v_mfma_f32_16x16x32_bf16 v[160:163], v[58:61], v[62:65], v[160:163]\n"
      "s_lshr_b32 s8, s9, 8\n"
      "v_exp_f32 v241, v241\n"
      "v_mfma_f32_16x16x32_bf16 v[184:187], a[216:219], v[136:139], 0\n"
      "s_and_b32 s9, s9, 0xff\n"
      "v_cvt_pk_bf16_f32 v241, v241, v241\n"
      "s_mul_i32 s10, s8, 73\n"
      "v_mfma_f32_16x16x32_bf16 v[184:187], a[228:231], v[208:211], v[184:187]\n"
      "ds_write_b16 v130, v241\n"
      "v_mfma_f32_16x16x32_bf16 v[212:215], a[220:223], v[136:139], 0\n"
      "s_add_u32 s10, s10, s9\n"
      "ds_read_b128 v[196:199], v131\n"
      "v_mfma_f32_16x16x32_bf16 v[212:215], a[232:235], v[208:211], v[212:215]\n"
      "ds_read_b128 v[172:175], v131 offset:64\n"
      "v_exp_f32 v165, v165\n"
      "v_exp_f32 v216, v216\n"
      "v_exp_f32 v166, v166\n"
      "v_exp_f32 v218, v218\n"
      "v_mfma_f32_16x16x32_bf16 v[184:187], a[240:243], v[168:171], v[184:187]\n"
      "s_mul_i32 s28, s10, 4802\n"
      "v_exp_f32 v217, v217\n"
      "v_mfma_f32_16x16x32_bf16 v[184:187], a[252:255], v[192:195], v[184:187]\n"
      "s_add_u32 s56, s18, s28\n"
      "v_exp_f32 v219, v219\n"
      "v_mfma_f32_16x16x32_bf16 v[212:215], a[244:247], v[168:171], v[212:215]\n"
      "s_addc_u32 s57, s19, 0\n"
      "s_and_b32 s56, s56, -16\n"
      "v_cvt_pk_bf16_f32 v216, v216, v217\n"
      "v_mfma_f32_16x16x32_bf16 v[212:215], v[0:3], v[192:195], v[212:215]\n"
      "s_sub_u32 s58, s56, 110592\n"
      "v_exp_f32 v164, v164\n"
      "v_cvt_pk_bf16_f32 v217, v218, v219\n"
      "v_cvt_pk_bf16_f32 v218, v164, v165\n"
      "v_mfma_f32_16x16x32_bf16 v[188:191], a[224:227], v[136:139], 0\n"
      "ds_read_b128 v[240:243], v123 offset:35840\n"
      "s_subb_u32 s59, s57, 0\n"
      "v_mfma_f32_16x16x32_bf16 v[188:191], a[236:239], v[208:211], v[188:191]\n"
      "s_waitcnt lgkmcnt(1)\n"
      "v_exp_f32 v167, v167\n"
      "v_mfma_f32_16x16x32_bf16 v[188:191], a[248:251], v[168:171], v[188:191]\n"
      "ds_read_b128 v[168:171], v114 offset:18432\n"
      "v_cvt_pk_bf16_f32 v219, v166, v167\n"
      "v_mfma_f32_16x16x32_bf16 v[188:191], v[4:7], v[192:195], v[188:191]\n"
      "ds_read_b128 v[192:195], v115 offset:18432\n"
      "v_exp_f32_e64 v224, v224\n"
      "v_mfma_f32_16x16x32_bf16 v[136:139], v[196:199], v[66:69], 0\n"
      "s_sub_u32 s56, s56, 73728\n"
      "v_exp_f32 v225, v225\n"
      "v_mfma_f32_16x16x32_bf16 v[136:139], v[172:175], v[66:69], v[136:139]\n"
      "s_subb_u32 s57, s57, 0\n"
      "v_exp_f32 v226, v226\n"
      "v_mfma_f32_16x16x32_bf16 v[208:211], v[196:199], v[220:223], 0\n"
      "s_mul_i32 s28, s8, 1373568\n"
      "v_exp_f32 v227, v227\n"
      "v_mfma_f32_16x16x32_bf16 v[208:211], v[172:175], v[50:53], v[208:211]\n"
      "s_mul_i32 s29, s9, 2688\n"
      "v_exp_f32 v228, v228\n"
      "v_mfma_f32_16x16x32_bf16 v[164:167], v[196:199], v[236:239], 0\n"
      "s_waitcnt lgkmcnt(2)\n"
      "v_cvt_pk_bf16_f32 v63, v226, v227\n"
      "v_cvt_pk_bf16_f32 v62, v224, v225\n"
      "v_mfma_f32_16x16x32_bf16 v[164:167], v[172:175], v[54:57], v[164:167]\n"
      "s_add_u32 s28, s28, s29\n"
      "v_rcp_f32 v140, v140\n"
      "v_mfma_f32_16x16x32_bf16 v[184:187], v[8:11], v[248:251], v[184:187]\n"
      "s_cmp_eq_u32 s8, 72\n"
      "v_mul_f32 v204, v140, v204\n"
      "v_mul_f32 v205, v140, v205\n"
      "v_mfma_f32_16x16x32_bf16 v[184:187], v[20:23], v[240:243], v[184:187]\n"
      "s_waitcnt lgkmcnt(0)\n"
      "v_mul_f32 v149, v140, v149\n"
      "v_mul_f32 v206, v140, v206\n"
      "v_cvt_pk_bf16_f32 v204, v204, v205\n"
      "v_mfma_f32_16x16x32_bf16 v[212:215], v[12:15], v[248:251], v[212:215]\n"
      "s_cselect_b32 s8, 0, 100270464\n"
      "v_mul_f32 v207, v140, v207\n"
      "v_cvt_pk_bf16_f32 v205, v206, v207\n"
      "v_mfma_f32_16x16x32_bf16 v[212:215], v[24:27], v[240:243], v[212:215]\n"
      "s_cmp_eq_u32 s9, 72\n"
      "v_cvt_pk_bf16_f32 v64, v228, 0\n"
      "v_mul_f32 v148, v140, v148\n"
      "v_mfma_f32_16x16x32_bf16 v[188:191], v[16:19], v[248:251], v[188:191]\n"
      "s_cselect_b32 s9, 0, 196224\n"
      "v_mul_f32 v150, v140, v150\n"
      "v_cvt_pk_bf16_f32 v206, v148, v149\n"
      "v_mfma_f32_16x16x32_bf16 v[188:191], v[28:31], v[240:243], v[188:191]\n"
      "s_add_u32 s10, s8, s9\n"
      "v_mul_f32 v151, v140, v151\n"
      "v_cvt_pk_bf16_f32 v207, v150, v151\n"
      "v_cndmask_b32 v208, v208, v164, s[42:43]\n"
      "v_mfma_f32_16x16x32_bf16 v[148:151], v[66:69], v[216:219], 0\n"
      "s_add_u32 s28, s28, s10\n"
      "v_mul_f32 v161, v140, v161\n"
      "v_mul_f32_e64 v160, v140, v160\n"
      "v_mfma_f32_16x16x32_bf16 v[148:151], v[66:69], v[62:65], v[148:151]\n"
      "ds_write_b128 v124, v[204:207] offset:1024\n"
      "v_mfma_f32_16x16x32_bf16 v[248:251], v[220:223], v[216:219], 0\n"
      "s_add_u32 s52, s16, s28\n"
      "v_mul_f32 v162, v140, v162\n"
      "v_cvt_pk_bf16_f32 v160, v160, v161\n"
      "v_mfma_f32_16x16x32_bf16 v[248:251], v[50:53], v[62:65], v[248:251]\n"
      "s_addc_u32 s53, s17, 0\n"
      "v_mul_f32 v163, v140, v163\n"
      "v_cvt_pk_bf16_f32 v161, v162, v163\n"
      "v_mfma_f32_16x16x32_bf16 v[240:243], v[236:239], v[216:219], 0\n"
      "s_add_u32 s27, s27, 1\n"
      "v_fma_f32 v147, v184, v184, s39\n"
      "v_fmac_f32 v147, v185, v185\n"
      "v_mfma_f32_16x16x32_bf16 v[240:243], v[54:57], v[62:65], v[240:243]\n"
      "ds_write2st64_b64 v125, v[144:145], v[160:161] offset0:0 offset1:2\n"
      "v_mov_b32_dpp v136, v136 quad_perm:[0,0,0,0] row_mask:0xf bank_mask:0xf\n"
      "v_mfma_f32_16x16x32_bf16 v[204:207], v[252:255], v[216:219], 0\n"
      "s_cmp_ge_u32 s27, s26\n"
      "v_fmac_f32 v147, v186, v186\n"
      "v_fmac_f32_e64 v147, v187, v187\n"
      "v_mfma_f32_16x16x32_bf16 v[204:207], v[58:61], v[62:65], v[204:207]\n"
      "s_cselect_b32 s28, 1, 0\n"
      "v_rcp_f32 v136, v136\n"
      "v_fmac_f32 v147, v212, v212\n"
      "v_fmac_f32 v147, v213, v213\n"
      "v_mfma_f32_16x16x32_bf16 v[236:239], v[196:199], v[252:255], 0\n"
      "s_xor_b32 s29, s10, s11\n"
      "v_fmac_f32 v147, v214, v214\n"
      "v_fmac_f32 v147, v215, v215\n"
      "v_mfma_f32_16x16x32_bf16 v[236:239], v[172:175], v[58:61], v[236:239]\n"
      "v_fmac_f32 v147, v188, v188\n"
      "v_fmac_f32_e64 v147, v189, v189\n"
      "v_mfma_f32_16x16x32_bf16 v[196:199], a[0:3], v[168:171], 0\n"
      "v_fmac_f32 v147, v190, v190\n"
      "v_fmac_f32 v147, v191, v191\n"
      "v_mfma_f32_16x16x32_bf16 v[196:199], a[36:39], v[192:195], v[196:199]\n"
      "v_permlane16_swap_b32 v159, v156\n"
      "v_mov_b32 v138, v147\n"
      "v_mfma_f32_16x16x32_bf16 v[228:231], a[4:7], v[168:171], 0\n"
      "s_or_b32 s28, s28, s29\n"
      "v_permlane16_swap_b32 v147, v138\n"
      "v_add_f32 v159, v159, v156\n"
      "v_add_f32 v147, v147, v138\n"
      "v_cndmask_b32 v208, v208, v236, s[44:45]\n"
      "v_mul_f32 v208, v136, v208\n"
      "v_permlane32_swap_b32 v159, v147\n"
      "v_cvt_pk_bf16_f32 v208, v208, v208\n"
      "v_add_f32_e64 v159, v159, v147\n"
      "v_mfma_f32_16x16x32_bf16 v[228:231], a[40:43], v[192:195], v[228:231]\n"
      "ds_write_b32 v129, v159\n"
      "ds_write_b16 v135, v208\n"
      "L_join_30_%=:\n"
      "s_cbranch_scc0 L_loop_%=\n"
      "L_tail0_%=:\n"
      "s_cmp_eq_u32 s10, s11\n"
      "s_cbranch_scc1 L_sk_35_%=\n"
      "s_mov_b32 s11, s10\n"
      "s_sub_u32 s28, s60, s8\n"
      "s_sub_u32 s29, s61, s9\n"
      "s_mov_b32 s60, s8\n"
      "s_mov_b32 s61, s9\n"
      "s_mov_b64 exec, s[64:65]\n"
      "v_add_u32 v116, s28, v116\n"
      "s_mov_b64 exec, s[70:71]\n"
      "v_add_u32 v116, s29, v116\n"
      "s_mov_b64 exec, s[66:67]\n"
      "v_add_u32 v117, s28, v117\n"
      "s_mov_b64 exec, s[72:73]\n"
      "v_add_u32 v117, s29, v117\n"
      "s_mov_b64 exec, s[68:69]\n"
      "v_add_u32_e64 v118, s28, v118\n"
      "s_mov_b64 exec, s[74:75]\n"
      "v_add_u32 v118, s29, v118\n"
      "s_mov_b64 exec, -1\n"
      "s_nop 4\n"
      "L_sk_35_%=:\n"
      "s_cmp_lt_u32 s27, s26\n"
      "s_cbranch_scc1 L_loop_%=\n"
      "s_nop 7\n"
      "v_mbcnt_lo_u32_b32 v208, -1, 0\n"
      "v_mbcnt_hi_u32_b32 v208, -1, v208\n"
      "v_rcp_f32 v148, v148\n"
      "v_and_b32 v149, 15, v208\n"
      "v_pk_mul_f32 v[240:241], v[240:241], v[148:149] op_sel_hi:[1,0]\n"
      "v_pk_mul_f32 v[204:205], v[204:205], v[148:149] op_sel_hi:[1,0]\n"
      "v_cvt_pk_bf16_f32 v204, v204, v205\n"
      "v_pk_mul_f32 v[206:207], v[206:207], v[148:149] op_sel_hi:[1,0]\n"
      "v_cvt_pk_bf16_f32 v205, v206, v207\n"
      "v_pk_mul_f32 v[242:243], v[242:243], v[148:149] op_sel_hi:[1,0]\n"
      "v_lshrrev_b32_e64 v196, 4, v208\n"
      "v_pk_mul_f32 v[248:249], v[248:249], v[148:149] op_sel_hi:[1,0]\n"
      "v_mul_u32_u24 v196, 352, v196\n"
      "v_cvt_pk_bf16_f32 v248, v248, v249\n"
      "v_pk_mul_f32 v[250:251], v[250:251], v[148:149] op_sel_hi:[1,0]\n"
      "v_cvt_pk_bf16_f32 v249, v250, v251\n"
      "v_lshl_add_u32 v196, v149, 4, v196\n"
      "v_cvt_pk_bf16_f32 v251, v242, v243\n"
      "v_cvt_pk_bf16_f32 v250, v240, v241\n"
      "v_add_u32 v196, 150640, v196\n"
      "ds_write_b64 v125, v[204:205] offset:2048\n"
      "ds_write_b128 v124, v[248:251] offset:2048\n"
      "s_waitcnt lgkmcnt(0)\n"
      "s_barrier\n"
      "ds_read_b128 v[240:243], v123 offset:3072\n"
      "ds_read_b128 v[148:151], v123\n"
      "ds_read_b128 v[248:251], v128\n"
      "ds_read_b128 v[228:231], v120\n"
      "ds_read_b128 v[192:195], v119\n"
      "s_sub_u32 s28, s27, 3\n"
      "s_cselect_b64 s[54:55], s[12:13], s[22:23]\n"
      "ds_read_b128 v[172:175], v123 offset:6144\n"
      "ds_read_b128 v[236:239], v119 offset:12288\n"
      "s_max_i32 m0, s28, 0\n"
      "ds_read_b128 v[220:223], v123 offset:9216\n"
      "s_movrels_b32 s9, s80\n"
      "s_lshr_b32 s8, s9, 8\n"
      "s_and_b32 s9, s9, 0xff\n"
      "s_mul_i32 s28, s8, 1373568\n"
      "s_mul_i32 s29, s9, 2688\n"
      "ds_read_b128 v[168:171], v119 offset:6144\n"
      "s_add_u32 s28, s28, s29\n"
      "s_cmp_eq_u32 s8, 72\n"
      "ds_read_b128 v[160:163], v121\n"
      "s_cselect_b32 s8, 0, 100270464\n"
      "s_cmp_eq_u32 s9, 72\n"
      "s_cselect_b32 s9, 0, 196224\n"
      "s_add_u32 s10, s8, s9\n"
      "s_add_u32 s28, s28, s10\n"
      "s_waitcnt lgkmcnt(8)\n"
      "v_mfma_f32_16x16x32_bf16 v[204:207], a[216:219], v[148:151], 0\n"
      "s_add_u32 s54, s54, s28\n"
      "s_waitcnt lgkmcnt(7)\n"
      "v_add_f32 v250, |v250|, |v251|\n"
      "v_mfma_f32_16x16x32_bf16 v[204:207], a[228:231], v[240:243], v[204:207]\n"
      "ds_read_b128 v[156:159], v123 offset:12288\n"
      "v_add_f32 v248, |v248|, |v249|\n"
      "v_mfma_f32_16x16x32_bf16 v[164:167], a[220:223], v[148:151], 0\n"
      "ds_read_b128 v[144:147], v123 offset:15360\n"
      "v_add_f32 v248, v248, v250\n"
      "v_mfma_f32_16x16x32_bf16 v[164:167], a[232:235], v[240:243], v[164:167]\n"
      "ds_read_b128 v[250:253], v123 offset:1024\n"
      "v_rsq_f32 v248, v248\n"
      "v_mfma_f32_16x16x32_bf16 v[224:227], a[224:227], v[148:151], 0\n"
      "s_waitcnt lgkmcnt(5)\n"
      "v_mul_f32 v248, s41, v248\n"
      "v_mov_b32 v249, v248\n"
      "v_mfma_f32_16x16x32_bf16 v[224:227], a[236:239], v[240:243], v[224:227]\n"
      "s_addc_u32 s55, s55, 0\n"
      "v_permlane32_swap_b32 v248, v249\n"
      "v_mov_b32_e64 v254, v248\n"
      "v_mfma_f32_16x16x32_bf16 v[164:167], a[244:247], v[172:175], v[164:167]\n"
      "ds_read_b128 v[148:151], v123 offset:4096\n"
      "s_cmp_lg_u32 s10, s11\n"
      "v_permlane16_swap_b32_e64 v248, v254\n"
      "v_mfma_f32_16x16x32_bf16 v[204:207], a[240:243], v[172:175], v[204:207]\n"
      "s_cbranch_scc1 L_edge_36_%=\n"
      "L_eb_37_%=:\n"
      "v_mul_f32_e64 v233, v248, v233\n"
      "v_mfma_f32_16x16x32_bf16 v[204:207], a[252:255], v[220:223], v[204:207]\n"
      "global_store_dwordx4 v116, v[192:195], s[54:55] sc1\n"
      "v_mfma_f32_16x16x32_bf16 v[164:167], v[0:3], v[220:223], v[164:167]\n"
      "ds_read_b128 v[140:143], v123 offset:10240\n"
      "v_mul_f32_e64 v232, v248, v232\n"
      "v_mfma_f32_16x16x32_bf16 v[224:227], a[248:251], v[172:175], v[224:227]\n"
      "ds_read_b128 v[192:195], v123 offset:13312\n"
      "v_mul_f32 v155, v248, v155\n"
      "v_mfma_f32_16x16x32_bf16 v[224:227], v[4:7], v[220:223], v[224:227]\n"
      "ds_read_b128 v[220:223], v123 offset:16384\n"
      "v_cvt_pk_bf16_f32 v232, v232, v233\n"
      "s_waitcnt lgkmcnt(4)\n"
      "v_mfma_f32_16x16x32_bf16 v[204:207], v[8:11], v[156:159], v[204:207]\n"
      "ds_read_b128 v[240:243], v123 offset:7168\n"
      "v_mul_f32 v234, v248, v234\n"
      "v_mfma_f32_16x16x32_bf16 v[204:207], v[20:23], v[144:147], v[204:207]\n"
      "v_mul_f32 v235, v248, v235\n"
      "v_cvt_pk_bf16_f32 v233, v234, v235\n"
      "v_mfma_f32_16x16x32_bf16 v[164:167], v[12:15], v[156:159], v[164:167]\n"
      "s_waitcnt lgkmcnt(4)\n"
      "v_mul_f32 v154, v248, v154\n"
      "v_mul_f32_e64 v153, v248, v153\n"
      "v_mfma_f32_16x16x32_bf16 v[164:167], v[24:27], v[144:147], v[164:167]\n"
      "global_store_dwordx4 v116, v[168:171], s[54:55] offset:128 sc1\n"
      "v_mfma_f32_16x16x32_bf16 v[224:227], v[16:19], v[156:159], v[224:227]\n"
      "v_mul_f32 v180, v248, v180\n"
      "v_mul_f32 v152, v248, v152\n"
      "v_mfma_f32_16x16x32_bf16 v[224:227], v[28:31], v[144:147], v[224:227]\n"
      "v_cvt_pk_bf16_f32 v234, v152, v153\n"
      "v_cvt_pk_bf16_f32 v235, v154, v155\n"
      "v_mfma_f32_16x16x32_bf16 v[156:159], a[216:219], v[250:253], 0\n"
      "ds_read_b128 v[152:155], v123 offset:2048\n"
      "v_mul_f32_e64 v181, v248, v181\n"
      "v_mfma_f32_16x16x32_bf16 v[156:159], a[228:231], v[148:151], v[156:159]\n"
      "ds_write_b128 v126, v[232:235] offset:18432\n"
      "v_mfma_f32_16x16x32_bf16 v[168:171], a[220:223], v[250:253], 0\n"
      "ds_read_b128 v[232:235], v123 offset:5120\n"
      "v_cvt_pk_bf16_f32 v180, v180, v181\n"
      "v_mfma_f32_16x16x32_bf16 v[168:171], a[232:235], v[148:151], v[168:171]\n"
      "s_waitcnt lgkmcnt(3)\n"
      "v_mul_f32 v183, v248, v183\n"
      "v_mul_f32 v182, v248, v182\n"
      "v_mfma_f32_16x16x32_bf16 v[136:139], a[224:227], v[250:253], 0\n"
      "s_lshl_b32 s29, s24, 8\n"
      "v_mul_f32_e64 v176, v254, v176\n"
      "v_cvt_pk_bf16_f32 v181, v182, v183\n"
      "v_mfma_f32_16x16x32_bf16 v[136:139], a[236:239], v[148:151], v[136:139]\n"
      "ds_write_b64 v127, v[180:181] offset:18432\n"
      "v_mfma_f32_16x16x32_bf16 v[156:159], a[240:243], v[240:243], v[156:159]\n"
      "ds_read_b128 v[148:151], v123 offset:8192\n"
      "v_mul_f32_e64 v177, v254, v177\n"
      "v_mfma_f32_16x16x32_bf16 v[156:159], a[252:255], v[140:143], v[156:159]\n"
      "ds_read_b128 v[180:183], v123 offset:11264\n"
      "v_cvt_pk_bf16_f32 v176, v176, v177\n"
      "v_mfma_f32_16x16x32_bf16 v[168:171], a[244:247], v[240:243], v[168:171]\n"
      "v_mul_f32 v179, v254, v179\n"
      "v_mul_f32 v178, v254, v178\n"
      "v_mfma_f32_16x16x32_bf16 v[168:171], v[0:3], v[140:143], v[168:171]\n"
      "v_mul_f32 v244, v254, v244\n"
      "v_cvt_pk_bf16_f32 v177, v178, v179\n"
      "v_mfma_f32_16x16x32_bf16 v[136:139], a[248:251], v[240:243], v[136:139]\n"
      "v_mul_f32 v245, v254, v245\n"
      "v_cvt_pk_bf16_f32 v178, v244, v245\n"
      "v_mfma_f32_16x16x32_bf16 v[136:139], v[4:7], v[140:143], v[136:139]\n"
      "v_mul_f32 v246, v254, v246\n"
      "v_mul_f32 v247, v254, v247\n"
      "v_mfma_f32_16x16x32_bf16 v[156:159], v[8:11], v[192:195], v[156:159]\n"
      "s_add_u32 s29, s29, 159088\n"
      "v_cvt_pk_bf16_f32 v179, v246, v247\n"
      "v_mul_f32_e64 v200, v254, v200\n"
      "v_mfma_f32_16x16x32_bf16 v[156:159], v[20:23], v[220:223], v[156:159]\n"
      "ds_write_b128 v126, v[176:179] offset:20480\n"
      "v_mfma_f32_16x16x32_bf16 v[168:171], v[12:15], v[192:195], v[168:171]\n"
      "ds_read_b128 v[176:179], v123 offset:14336\n"
      "v_mul_f32_e64 v201, v254, v201\n"
      "v_mfma_f32_16x16x32_bf16 v[168:171], v[24:27], v[220:223], v[168:171]\n"
      "ds_read_b128 v[244:247], v123 offset:17408\n"
      "v_cvt_pk_bf16_f32 v200, v200, v201\n"
      "v_mfma_f32_16x16x32_bf16 v[136:139], v[16:19], v[192:195], v[136:139]\n"
      "s_waitcnt lgkmcnt(3)\n"
      "v_mul_f32 v202, v254, v202\n"
      "v_mul_f32_e64 v203, v254, v203\n"
      "v_mfma_f32_16x16x32_bf16 v[136:139], v[28:31], v[220:223], v[136:139]\n"
      "v_fma_f32 v248, v204, v204, s39\n"
      "v_cvt_pk_bf16_f32 v201, v202, v203\n"
      "v_mfma_f32_16x16x32_bf16 v[240:243], a[216:219], v[152:155], 0\n"
      "global_store_dwordx4 v116, v[236:239], s[54:55] offset:256 sc1\n"
      "v_mfma_f32_16x16x32_bf16 v[240:243], a[228:231], v[232:235], v[240:243]\n"
      "ds_write_b64 v127, v[200:201] offset:20480\n"
      "v_mfma_f32_16x16x32_bf16 v[192:195], a[220:223], v[152:155], 0\n"
      "ds_read_b128 v[250:253], v196\n"
      "v_fmac_f32_e64 v248, v205, v205\n"
      "v_mfma_f32_16x16x32_bf16 v[192:195], a[232:235], v[232:235], v[192:195]\n"
      "ds_read_b128 v[200:203], v196 offset:1408\n"
      "v_fmac_f32 v248, v206, v206\n"
      "v_mfma_f32_16x16x32_bf16 v[236:239], a[224:227], v[152:155], 0\n"
      "v_fmac_f32 v248, v207, v207\n"
      "v_fmac_f32_e64 v248, v164, v164\n"
      "v_mfma_f32_16x16x32_bf16 v[236:239], a[236:239], v[232:235], v[236:239]\n"
      "v_fmac_f32 v248, v165, v165\n"
      "v_fmac_f32 v248, v166, v166\n"
      "v_mfma_f32_16x16x32_bf16 v[240:243], a[240:243], v[148:151], v[240:243]\n"
      "s_waitcnt lgkmcnt(3)\n"
      "v_fmac_f32 v248, v167, v167\n"
      "v_fmac_f32 v248, v224, v224\n"
      "v_mfma_f32_16x16x32_bf16 v[240:243], a[252:255], v[180:183], v[240:243]\n"
      "v_fmac_f32 v248, v225, v225\n"
      "v_fmac_f32_e64 v248, v226, v226\n"
      "v_mfma_f32_16x16x32_bf16 v[192:195], a[244:247], v[148:151], v[192:195]\n"
      "global_store_dwordx4 v117, v[228:231], s[54:55] sc1\n"
      "v_mfma_f32_16x16x32_bf16 v[192:195], v[0:3], v[180:183], v[192:195]\n"
      "v_mul_f32 v184, v249, v184\n"
      "v_fmac_f32 v248, v227, v227\n"
      "v_mfma_f32_16x16x32_bf16 v[236:239], a[248:251], v[148:151], v[236:239]\n"
      "ds_read_b128 v[152:155], v196 offset:2816\n"
      "v_mul_f32 v185, v249, v185\n"
      "v_mfma_f32_16x16x32_bf16 v[236:239], v[4:7], v[180:183], v[236:239]\n"
      "v_mul_f32 v186, v249, v186\n"
      "v_cvt_pk_bf16_f32 v184, v184, v185\n"
      "v_mfma_f32_16x16x32_bf16 v[240:243], v[8:11], v[176:179], v[240:243]\n"
      "ds_read_b128 v[148:151], v196 offset:4224\n"
      "v_mul_f32 v187, v249, v187\n"
      "v_mfma_f32_16x16x32_bf16 v[240:243], v[20:23], v[244:247], v[240:243]\n"
      "s_waitcnt lgkmcnt(2)\n"
      "v_cvt_pk_bf16_f32 v185, v186, v187\n"
      "v_mul_f32 v212, v249, v212\n"
      "v_mfma_f32_16x16x32_bf16 v[192:195], v[12:15], v[176:179], v[192:195]\n"
      "v_mul_f32 v213, v249, v213\n"
      "v_cvt_pk_bf16_f32 v186, v212, v213\n"
      "v_mfma_f32_16x16x32_bf16 v[192:195], v[24:27], v[244:247], v[192:195]\n"
      "ds_read_b128 v[180:183], v196 offset:5632\n"
      "v_mul_f32 v214, v249, v214\n"
      "v_mfma_f32_16x16x32_bf16 v[236:239], v[16:19], v[176:179], v[236:239]\n"
      "v_mul_f32 v215, v249, v215\n"
      "v_cvt_pk_bf16_f32 v187, v214, v215\n"
      "v_mfma_f32_16x16x32_bf16 v[236:239], v[28:31], v[244:247], v[236:239]\n"
      "ds_write_b128 v126, v[184:187] offset:22528\n"
      "v_mfma_f32_16x16x32_bf16 v[184:187], a[216:219], v[250:253], 0\n"
      "ds_read_b128 v[212:215], v196 offset:7040\n"
      "v_mul_f32 v190, v249, v190\n"
      "v_mfma_f32_16x16x32_bf16 v[184:187], a[228:231], v[200:203], v[184:187]\n"
      "v_mul_f32 v189, v249, v189\n"
      "v_mfma_f32_16x16x32_bf16 v[244:247], a[220:223], v[250:253], 0\n"
      "s_waitcnt lgkmcnt(3)\n"
      "v_mul_f32 v188, v249, v188\n"
      "v_cvt_pk_bf16_f32 v188, v188, v189\n"
      "v_mfma_f32_16x16x32_bf16 v[244:247], a[232:235], v[200:203], v[244:247]\n"
      "ds_read_b128 v[176:179], v196 offset:256\n"
      "v_mul_f32_e64 v191, v249, v191\n"
      "v_mfma_f32_16x16x32_bf16 v[232:235], a[224:227], v[250:253], 0\n"
      "v_cvt_pk_bf16_f32 v189, v190, v191\n"
      "v_fma_f32 v249, v156, v156, s39\n"
      "v_mfma_f32_16x16x32_bf16 v[232:235], a[236:239], v[200:203], v[232:235]\n"
      "ds_write_b64 v127, v[188:189] offset:22528\n"
      "v_mfma_f32_16x16x32_bf16 v[184:187], a[240:243], v[152:155], v[184:187]\n"
      "ds_read_b128 v[188:191], v196 offset:1664\n"
      "v_fmac_f32 v249, v157, v157\n"
      "v_mfma_f32_16x16x32_bf16 v[184:187], a[252:255], v[148:151], v[184:187]\n"
      "v_fmac_f32 v249, v158, v158\n"
      "v_fmac_f32 v249, v159, v159\n"
      "v_mfma_f32_16x16x32_bf16 v[244:247], a[244:247], v[152:155], v[244:247]\n"
      "s_waitcnt lgkmcnt(3)\n"
      "v_fmac_f32 v249, v168, v168\n"
      "v_fmac_f32 v249, v169, v169\n"
      "v_mfma_f32_16x16x32_bf16 v[244:247], v[0:3], v[148:151], v[244:247]\n"
      "ds_read_b128 v[200:203], v196 offset:3072\n"
      "v_fmac_f32_e64 v249, v170, v170\n"
      "v_mfma_f32_16x16x32_bf16 v[232:235], a[248:251], v[152:155], v[232:235]\n"
      "ds_read_b128 v[152:155], v196 offset:4480\n"
      "v_fmac_f32 v249, v171, v171\n"
      "v_mfma_f32_16x16x32_bf16 v[232:235], v[4:7], v[148:151], v[232:235]\n"
      "v_fmac_f32 v249, v136, v136\n"
      "v_lshl_add_u32 v199, v208, 2, s29\n"
      "v_mfma_f32_16x16x32_bf16 v[184:187], v[8:11], v[180:183], v[184:187]\n"
      "v_fmac_f32 v249, v137, v137\n"
      "v_fmac_f32 v249, v138, v138\n"
      "v_mfma_f32_16x16x32_bf16 v[184:187], v[20:23], v[212:215], v[184:187]\n"
      "s_lshl_b32 s29, s24, 3\n"
      "v_fmac_f32 v249, v139, v139\n"
      "v_mfma_f32_16x16x32_bf16 v[244:247], v[12:15], v[180:183], v[244:247]\n"
      "ds_read_b128 v[148:151], v196 offset:5888\n"
      "v_mfma_f32_16x16x32_bf16 v[244:247], v[24:27], v[212:215], v[244:247]\n"
      "ds_read_b32 v199, v199\n"
      "v_fma_f32 v197, v240, v240, s39\n"
      "v_mfma_f32_16x16x32_bf16 v[232:235], v[16:19], v[180:183], v[232:235]\n"
      "s_waitcnt lgkmcnt(4)\n"
      "v_fmac_f32 v197, v241, v241\n"
      "v_lshrrev_b32_e64 v251, 3, v208\n"
      "v_mfma_f32_16x16x32_bf16 v[232:235], v[28:31], v[212:215], v[232:235]\n"
      "v_fmac_f32_e64 v197, v242, v242\n"
      "v_add_u32_e64 v251, s29, v251\n"
      "v_mfma_f32_16x16x32_bf16 v[220:223], a[216:219], v[176:179], 0\n"
      "ds_read_b128 v[180:183], v196 offset:7296\n"
      "v_cmp_gt_u32 s[48:49], s26, v251\n"
      "v_mfma_f32_16x16x32_bf16 v[220:223], a[228:231], v[188:191], v[220:223]\n"
      "s_mov_b32 exec_hi, 0\n"
      "global_store_dwordx4 v118, v[160:163], s[54:55] sc1\n"
      "s_mov_b32 exec_hi, -1\n"
      "v_mfma_f32_16x16x32_bf16 v[250:253], a[220:223], v[176:179], 0\n"
      "s_waitcnt lgkmcnt(3)\n"
      "v_fmac_f32 v197, v243, v243\n"
      "v_fmac_f32 v197, v192, v192\n"
      "v_mfma_f32_16x16x32_bf16 v[250:253], a[232:235], v[188:191], v[250:253]\n"
      "v_fmac_f32 v197, v193, v193\n"
      "v_fmac_f32 v197, v194, v194\n"
      "v_mfma_f32_16x16x32_bf16 v[212:215], a[224:227], v[176:179], 0\n"
      "v_fmac_f32 v197, v195, v195\n"
      "v_fmac_f32 v197, v236, v236\n"
      "v_mfma_f32_16x16x32_bf16 v[212:215], a[236:239], v[188:191], v[212:215]\n"
      "v_fmac_f32 v197, v237, v237\n"
      "v_fmac_f32 v197, v238, v238\n"
      "v_mfma_f32_16x16x32_bf16 v[220:223], a[240:243], v[200:203], v[220:223]\n"
      "v_fmac_f32 v197, v239, v239\n"
      "v_mov_b32 v177, v197\n"
      "v_mfma_f32_16x16x32_bf16 v[220:223], a[252:255], v[152:155], v[220:223]\n"
      "v_permlane16_swap_b32 v248, v249\n"
      "v_mfma_f32_16x16x32_bf16 v[250:253], a[244:247], v[200:203], v[250:253]\n"
      "v_fma_f32 v176, v184, v184, s39\n"
      "v_add_f32 v248, v248, v249\n"
      "v_mfma_f32_16x16x32_bf16 v[250:253], v[0:3], v[152:155], v[250:253]\n"
      "v_permlane16_swap_b32 v197, v177\n"
      "v_mfma_f32_16x16x32_bf16 v[212:215], a[248:251], v[200:203], v[212:215]\n"
      "v_add_f32 v197, v197, v177\n"
      "v_fmac_f32 v176, v185, v185\n"
      "v_mfma_f32_16x16x32_bf16 v[212:215], v[4:7], v[152:155], v[212:215]\n"
      "s_waitcnt lgkmcnt(0)\n"
      "v_permlane32_swap_b32 v248, v197\n"
      "v_mfma_f32_16x16x32_bf16 v[220:223], v[8:11], v[148:151], v[220:223]\n"
      "v_add_f32_e64 v248, v248, v197\n"
      "v_mfma_f32_16x16x32_bf16 v[220:223], v[20:23], v[180:183], v[220:223]\n"
      "ds_write_b32 v129, v248 offset:1024\n"
      "v_mfma_f32_16x16x32_bf16 v[250:253], v[12:15], v[148:151], v[250:253]\n"
      "v_fmac_f32 v176, v186, v186\n"
      "v_fmac_f32 v176, v187, v187\n"
      "v_mfma_f32_16x16x32_bf16 v[250:253], v[24:27], v[180:183], v[250:253]\n"
      "v_fmac_f32 v176, v244, v244\n"
      "v_fmac_f32 v176, v245, v245\n"
      "v_mfma_f32_16x16x32_bf16 v[212:215], v[16:19], v[148:151], v[212:215]\n"
      "v_fmac_f32 v176, v246, v246\n"
      "v_fmac_f32 v176, v247, v247\n"
      "v_mfma_f32_16x16x32_bf16 v[212:215], v[28:31], v[180:183], v[212:215]\n"
      "v_fmac_f32 v176, v232, v232\n"
      "v_fmac_f32 v176, v233, v233\n"
      "v_fmac_f32 v176, v234, v234\n"
      "v_fmac_f32 v176, v235, v235\n"
      "v_pk_mul_f32 v[248:249], v[220:221], v[220:221]\n"
      "v_pk_fma_f32 v[248:249], v[222:223], v[222:223], v[248:249]\n"
      "v_pk_fma_f32 v[248:249], v[250:251], v[250:251], v[248:249]\n"
      "v_pk_fma_f32 v[248:249], v[252:253], v[252:253], v[248:249]\n"
      "v_pk_fma_f32 v[248:249], v[212:213], v[212:213], v[248:249]\n"
      "v_pk_fma_f32 v[248:249], v[214:215], v[214:215], v[248:249]\n"
      "v_add_f32 v248, s39, v248\n"
      "v_add_f32 v149, v248, v249\n"
      "v_mov_b32 v148, v149\n"
      "v_mov_b32 v151, v148\n"
      "v_permlane16_swap_b32 v176, v149\n"
      "v_add_f32 v176, v176, v149\n"
      "v_permlane16_swap_b32 v148, v151\n"
      "v_add_f32 v148, v148, v151\n"
      "s_nop 1\n"
      "v_permlane32_swap_b32 v176, v148\n"
      "v_add_f32_e64 v176, v176, v148\n"
      "ds_write_b32 v129, v176 offset:2048\n"
      "s_waitcnt lgkmcnt(0)\n"
      "s_barrier\n"
      "s_add_u32 s27, s27, 1\n"
      "ds_read_b128 v[148:151], v128 offset:1024\n"
      "ds_read_b128 v[180:183], v128 offset:2048\n"
      "s_sub_u32 s28, s27, 3\n"
      "ds_read_b128 v[152:155], v119 offset:18432\n"
      "ds_read_b128 v[188:191], v120 offset:18432\n"
      "s_cselect_b64 s[54:55], s[12:13], s[22:23]\n"
      "s_max_i32 m0, s28, 0\n"
      "ds_read_b128 v[200:203], v121 offset:18432\n"
      "ds_read_b128 v[228:231], v119 offset:24576\n"
      "ds_read_b128 v[176:179], v119 offset:30720\n"
      "s_movrels_b32 s9, s80\n"
      "s_lshr_b32 s8, s9, 8\n"
      "s_and_b32 s9, s9, 0xff\n"
      "s_mul_i32 s28, s8, 1373568\n"
      "s_mul_i32 s29, s9, 2688\n"
      "s_add_u32 s28, s28, s29\n"
      "s_cmp_eq_u32 s8, 72\n"
      "s_cselect_b32 s8, 0, 100270464\n"
      "s_cmp_eq_u32 s9, 72\n"
      "s_cselect_b32 s9, 0, 196224\n"
      "s_add_u32 s10, s8, s9\n"
      "s_add_u32 s28, s28, s10\n"
      "s_add_u32 s54, s54, s28\n"
      "s_addc_u32 s55, s55, 0\n"
      "s_cmp_lg_u32 s10, s11\n"
      "s_cbranch_scc1 L_edge_38_%=\n"
      "L_eb_39_%=:\n"
      "s_waitcnt lgkmcnt(5)\n"
      "v_add_f32 v150, |v150|, |v151|\n"
      "v_add_f32 v148, |v148|, |v149|\n"
      "v_add_f32 v148, v148, v150\n"
      "v_rsq_f32 v148, v148\n"
      "v_add_f32 v180, |v180|, |v181|\n"
      "v_mul_f32 v148, s41, v148\n"
      "v_mov_b32 v249, v148\n"
      "v_add_f32 v182, |v182|, |v183|\n"
      "v_add_f32 v180, v180, v182\n"
      "v_rsq_f32 v180, v180\n"
      "v_permlane32_swap_b32 v148, v249\n"
      "v_pk_mul_f32 v[238:239], v[238:239], v[248:249] op_sel:[0,1]\n"
      "v_mov_b32 v181, v148\n"
      "v_mul_f32_e64 v180, s41, v180\n"
      "v_subrev_u32 v182, 73728, v127\n"
      "v_permlane16_swap_b32 v148, v181\n"
      "v_pk_mul_f32 v[236:237], v[236:237], v[248:249] op_sel:[0,1]\n"
      "v_pk_mul_f32 v[138:139], v[138:139], v[180:181] op_sel:[0,1]\n"
      "v_mov_b32 v248, v180\n"
      "v_pk_mul_f32 v[226:227], v[226:227], v[148:149] op_sel_hi:[1,0]\n"
      "v_pk_mul_f32 v[224:225], v[224:225], v[148:149] op_sel_hi:[1,0]\n"
      "v_cvt_pk_bf16_f32 v236, v236, v237\n"
      "v_pk_mul_f32 v[156:157], v[156:157], v[180:181] op_sel:[0,1]\n"
      "v_pk_mul_f32 v[242:243], v[242:243], v[248:249] op_sel:[0,1]\n"
      "v_pk_mul_f32 v[204:205], v[204:205], v[148:149] op_sel_hi:[1,0]\n"
      "v_pk_mul_f32 v[158:159], v[158:159], v[180:181] op_sel:[0,1]\n"
      "v_permlane32_swap_b32 v180, v248\n"
      "v_mov_b32 v248, v180\n"
      "s_waitcnt lgkmcnt(0)\n"
      "v_cvt_pk_bf16_f32 v156, v156, v157\n"
      "v_pk_mul_f32 v[240:241], v[240:241], v[248:249] op_sel:[0,1]\n"
      "v_permlane16_swap_b32 v180, v248\n"
      "v_pk_mul_f32 v[136:137], v[136:137], v[180:181] op_sel:[0,1]\n"
      "v_pk_mul_f32 v[186:187], v[186:187], v[180:181] op_sel_hi:[1,0]\n"
      "v_cvt_pk_bf16_f32 v237, v238, v239\n"
      "v_pk_mul_f32 v[214:215], v[214:215], v[248:249] op_sel_hi:[1,0]\n"
      "v_pk_mul_f32 v[212:213], v[212:213], v[248:249] op_sel_hi:[1,0]\n"
      "v_pk_mul_f32 v[164:165], v[164:165], v[148:149] op_sel_hi:[1,0]\n"
      "v_cvt_pk_bf16_f32 v240, v240, v241\n"
      "v_cvt_pk_bf16_f32 v136, v136, v137\n"
      "v_cvt_pk_bf16_f32 v224, v224, v225\n"
      "v_pk_mul_f32 v[170:171], v[170:171], v[180:181] op_sel:[0,1]\n"
      "v_pk_mul_f32 v[232:233], v[232:233], v[180:181] op_sel_hi:[1,0]\n"
      "v_cvt_pk_bf16_f32 v212, v212, v213\n"
      "v_cvt_pk_bf16_f32 v241, v242, v243\n"
      "v_pk_mul_f32 v[194:195], v[194:195], v[248:249] op_sel:[0,1]\n"
      "v_pk_mul_f32 v[234:235], v[234:235], v[180:181] op_sel_hi:[1,0]\n"
      "v_cvt_pk_bf16_f32 v225, v226, v227\n"
      "v_cvt_pk_bf16_f32 v157, v158, v159\n"
      "v_pk_mul_f32 v[252:253], v[252:253], v[248:249] op_sel_hi:[1,0]\n"
      "v_cvt_pk_bf16_f32 v137, v138, v139\n"
      "v_pk_mul_f32 v[244:245], v[244:245], v[180:181] op_sel_hi:[1,0]\n"
      "v_pk_mul_f32 v[166:167], v[166:167], v[148:149] op_sel_hi:[1,0]\n"
      "global_store_dwordx4 v116, v[152:155], s[54:55] sc1\n"
      "v_pk_mul_f32 v[184:185], v[184:185], v[180:181] op_sel_hi:[1,0]\n"
      "v_pk_mul_f32 v[192:193], v[192:193], v[248:249] op_sel:[0,1]\n"
      "v_cvt_pk_bf16_f32 v184, v184, v185\n"
      "v_cvt_pk_bf16_f32 v185, v186, v187\n"
      "v_pk_mul_f32 v[168:169], v[168:169], v[180:181] op_sel:[0,1]\n"
      "v_pk_mul_f32 v[222:223], v[222:223], v[248:249] op_sel_hi:[1,0]\n"
      "v_cvt_pk_bf16_f32 v158, v168, v169\n"
      "v_cvt_pk_bf16_f32 v232, v232, v233\n"
      "v_pk_mul_f32 v[220:221], v[220:221], v[248:249] op_sel_hi:[1,0]\n"
      "v_cvt_pk_bf16_f32 v233, v234, v235\n"
      "v_pk_mul_f32 v[206:207], v[206:207], v[148:149] op_sel_hi:[1,0]\n"
      "v_pk_mul_f32 v[246:247], v[246:247], v[180:181] op_sel_hi:[1,0]\n"
      "v_subrev_u32 v249, 73728, v126\n"
      "v_cvt_pk_bf16_f32 v159, v170, v171\n"
      "v_cvt_pk_bf16_f32 v204, v204, v205\n"
      "v_cvt_pk_bf16_f32 v205, v206, v207\n"
      "v_cvt_pk_bf16_f32 v206, v164, v165\n"
      "v_cvt_pk_bf16_f32 v243, v194, v195\n"
      "v_cvt_pk_bf16_f32 v220, v220, v221\n"
      "v_cvt_pk_bf16_f32 v213, v214, v215\n"
      "v_cvt_pk_bf16_f32 v207, v166, v167\n"
      "v_cvt_pk_bf16_f32 v221, v222, v223\n"
      "v_cvt_pk_bf16_f32 v186, v244, v245\n"
      "v_cvt_pk_bf16_f32 v242, v192, v193\n"
      "v_pk_mul_f32 v[250:251], v[250:251], v[248:249] op_sel_hi:[1,0]\n"
      "v_cvt_pk_bf16_f32 v187, v246, v247\n"
      "ds_write_b64 v127, v[136:137] offset:2048\n"
      "global_store_dwordx4 v116, v[228:231], s[54:55] offset:128 sc1\n"
      "global_store_dwordx4 v116, v[176:179], s[54:55] offset:256 sc1\n"
      "ds_write_b128 v249, v[184:187]\n"
      "ds_write_b64 v127, v[236:237] offset:4096\n"
      "ds_write_b128 v126, v[204:207]\n"
      "ds_write_b64 v127, v[224:225]\n"
      "global_store_dwordx4 v117, v[188:191], s[54:55] sc1\n"
      "ds_write_b64 v182, v[232:233]\n"
      "v_cvt_pk_bf16_f32 v222, v250, v251\n"
      "v_cvt_pk_bf16_f32 v223, v252, v253\n"
      "ds_write_b128 v126, v[156:159] offset:2048\n"
      "ds_write_b128 v249, v[220:223] offset:2048\n"
      "ds_write_b128 v126, v[240:243] offset:4096\n"
      "s_mov_b32 exec_hi, 0\n"
      "global_store_dwordx4 v118, v[200:203], s[54:55] sc1\n"
      "s_mov_b32 exec_hi, -1\n"
      "ds_write_b64 v182, v[212:213] offset:2048\n"
      "s_waitcnt lgkmcnt(0)\n"
      "s_barrier\n"
      "ds_read_b128 v[184:187], v119 offset:6144\n"
      "ds_read_b128 v[232:235], v119\n"
      "s_add_u32 s27, s27, 1\n"
      "v_subrev_u32 v182, 73728, v119\n"
      "s_sub_u32 s28, s27, 3\n"
      "ds_read_b128 v[176:179], v182\n"
      "ds_read_b128 v[244:247], v121\n"
      "ds_read_b128 v[212:215], v182 offset:6144\n"
      "s_cselect_b64 s[54:55], s[12:13], s[22:23]\n"
      "ds_read_b128 v[188:191], v182 offset:12288\n"
      "ds_read_b128 v[200:203], v120\n"
      "s_max_i32 m0, s28, 0\n"
      "ds_read_b128 v[248:251], v119 offset:12288\n"
      "s_movrels_b32 s9, s80\n"
      "s_lshr_b32 s8, s9, 8\n"
      "s_and_b32 s9, s9, 0xff\n"
      "s_mul_i32 s28, s8, 1373568\n"
      "s_mul_i32 s29, s9, 2688\n"
      "s_add_u32 s28, s28, s29\n"
      "s_cmp_eq_u32 s8, 72\n"
      "s_cselect_b32 s8, 0, 100270464\n"
      "s_cmp_eq_u32 s9, 72\n"
      "s_cselect_b32 s9, 0, 196224\n"
      "s_add_u32 s10, s8, s9\n"
      "s_add_u32 s28, s28, s10\n"
      "s_add_u32 s54, s54, s28\n"
      "s_addc_u32 s55, s55, 0\n"
      "s_cmp_lg_u32 s10, s11\n"
      "s_waitcnt lgkmcnt(6)\n"
      "s_cbranch_scc1 L_edge_40_%=\n"
      "L_eb_41_%=:\n"
      "global_store_dwordx4 v116, v[232:235], s[54:55] sc1\n"
      "global_store_dwordx4 v116, v[184:187], s[54:55] offset:128 sc1\n"
      "s_waitcnt lgkmcnt(2)\n"
      "s_waitcnt lgkmcnt(1)\n"
      "s_waitcnt lgkmcnt(0)\n"
      "global_store_dwordx4 v116, v[248:251], s[54:55] offset:256 sc1\n"
      "global_store_dwordx4 v117, v[200:203], s[54:55] sc1\n"
      "s_mov_b32 exec_hi, 0\n"
      "global_store_dwordx4 v118, v[244:247], s[54:55] sc1\n"
      "s_mov_b32 exec_hi, -1\n"
      "s_mov_b64 exec, s[48:49]\n"
      "s_cbranch_execz L_np_42_%=\n"
      "global_store_dwordx4 v199, v[176:179], s[22:23] sc1\n"
      "global_store_dwordx4 v199, v[212:215], s[22:23] offset:128 sc1\n"
      "global_store_dwordx4 v199, v[188:191], s[22:23] offset:256 sc1\n"
      "L_np_42_%=:\n"
      "s_mov_b64 exec, -1\n"
      "s_nop 0\n"
      "s_endpgm\n"
      "L_tail1_%=:\n"
      "s_cmp_eq_u32 s10, s11\n"
      "s_cbranch_scc1 L_sk_43_%=\n"
      "s_mov_b32 s11, s10\n"
      "s_sub_u32 s28, s60, s8\n"
      "s_sub_u32 s29, s61, s9\n"
      "s_mov_b32 s60, s8\n"
      "s_mov_b32 s61, s9\n"
      "s_mov_b64 exec, s[64:65]\n"
      "v_add_u32 v116, s28, v116\n"
      "s_mov_b64 exec, s[70:71]\n"
      "v_add_u32 v116, s29, v116\n"
      "s_mov_b64 exec, s[66:67]\n"
      "v_add_u32 v117, s28, v117\n"
      "s_mov_b64 exec, s[72:73]\n"
      "v_add_u32 v117, s29, v117\n"
      "s_mov_b64 exec, s[68:69]\n"
      "v_add_u32 v118, s28, v118\n"
      "s_mov_b64 exec, s[74:75]\n"
      "v_add_u32 v118, s29, v118\n"
      "s_mov_b64 exec, -1\n"
      "s_nop 4\n"
      "L_sk_43_%=:\n"
      "s_cmp_lt_u32 s27, s26\n"
      "s_cbranch_scc1 L_mid_%=\n"
      "s_nop 7\n"
      "v_mbcnt_lo_u32_b32 v193, -1, 0\n"
      "v_mbcnt_hi_u32_b32 v193, -1, v193\n"
      "v_rcp_f32 v192, v192\n"
      "v_and_b32 v221, 15, v193\n"
      "v_pk_mul_f32 v[248:249], v[248:249], v[192:193] op_sel_hi:[1,0]\n"
      "v_pk_mul_f32 v[148:149], v[148:149], v[192:193] op_sel_hi:[1,0]\n"
      "v_cvt_pk_bf16_f32 v148, v148, v149\n"
      "v_pk_mul_f32 v[150:151], v[150:151], v[192:193] op_sel_hi:[1,0]\n"
      "v_cvt_pk_bf16_f32 v149, v150, v151\n"
      "v_pk_mul_f32 v[250:251], v[250:251], v[192:193] op_sel_hi:[1,0]\n"
      "v_lshrrev_b32_e64 v194, 4, v193\n"
      "v_pk_mul_f32 v[240:241], v[240:241], v[192:193] op_sel_hi:[1,0]\n"
      "v_mul_u32_u24 v194, 352, v194\n"
      "v_cvt_pk_bf16_f32 v240, v240, v241\n"
      "v_pk_mul_f32 v[242:243], v[242:243], v[192:193] op_sel_hi:[1,0]\n"
      "v_cvt_pk_bf16_f32 v241, v242, v243\n"
      "v_lshl_add_u32 v194, v221, 4, v194\n"
      "v_cvt_pk_bf16_f32 v243, v250, v251\n"
      "v_cvt_pk_bf16_f32 v242, v248, v249\n"
      "v_add_u32 v194, 150640, v194\n"
      "ds_write_b64 v125, v[148:149] offset:20480\n"
      "ds_write_b128 v124, v[240:243] offset:20480\n"
      "s_waitcnt lgkmcnt(0)\n"
      "s_barrier\n"
      "ds_read_b128 v[228:231], v123 offset:21504\n"
      "ds_read_b128 v[220:223], v123 offset:18432\n"
      "s_sub_u32 s28, s27, 3\n"
      "s_cselect_b64 s[54:55], s[12:13], s[22:23]\n"
      "ds_read_b128 v[148:151], v128 offset:1024\n"
      "ds_read_b128 v[248:251], v119 offset:30720\n"
      "s_max_i32 m0, s28, 0\n"
      "ds_read_b128 v[156:159], v119 offset:18432\n"
      "ds_read_b128 v[240:243], v119 offset:24576\n"
      "ds_read_b128 v[140:143], v123 offset:24576\n"
      "ds_read_b128 v[168:171], v123 offset:27648\n"
      "s_movrels_b32 s9, s80\n"
      "s_lshr_b32 s8, s9, 8\n"
      "s_and_b32 s9, s9, 0xff\n"
      "s_mul_i32 s28, s8, 1373568\n"
      "s_mul_i32 s29, s9, 2688\n"
      "s_add_u32 s28, s28, s29\n"
      "s_cmp_eq_u32 s8, 72\n"
      "ds_read_b128 v[204:207], v120 offset:18432\n"
      "s_cselect_b32 s8, 0, 100270464\n"
      "s_cmp_eq_u32 s9, 72\n"
      "s_cselect_b32 s9, 0, 196224\n"
      "s_add_u32 s10, s8, s9\n"
      "ds_read_b128 v[224:227], v121 offset:18432\n"
      "s_add_u32 s28, s28, s10\n"
      "s_waitcnt lgkmcnt(8)\n"
      "v_mfma_f32_16x16x32_bf16 v[252:255], a[216:219], v[220:223], 0\n"
      "ds_read_b128 v[144:147], v123 offset:30720\n"
      "s_waitcnt lgkmcnt(8)\n"
      "v_mfma_f32_16x16x32_bf16 v[252:255], a[228:231], v[228:231], v[252:255]\n"
      "s_add_u32 s54, s54, s28\n"
      "v_add_f32 v150, |v150|, |v151|\n"
      "v_add_f32 v148, |v148|, |v149|\n"
      "v_mfma_f32_16x16x32_bf16 v[136:139], a[220:223], v[220:223], 0\n"
      "ds_read_b128 v[236:239], v123 offset:33792\n"
      "v_add_f32 v148, v148, v150\n"
      "v_mfma_f32_16x16x32_bf16 v[136:139], a[232:235], v[228:231], v[136:139]\n"
      "ds_read_b128 v[160:163], v123 offset:19456\n"
      "v_rsq_f32 v148, v148\n"
      "s_waitcnt lgkmcnt(5)\n"
      "v_mul_f32 v148, s41, v148\n"
      "v_mfma_f32_16x16x32_bf16 v[208:211], a[224:227], v[220:223], 0\n"
      "ds_read_b128 v[220:223], v123 offset:22528\n"
      "v_mov_b32 v195, v148\n"
      "v_mfma_f32_16x16x32_bf16 v[208:211], a[236:239], v[228:231], v[208:211]\n"
      "s_addc_u32 s55, s55, 0\n"
      "v_permlane32_swap_b32 v148, v195\n"
      "s_cmp_lg_u32 s10, s11\n"
      "v_mov_b32_e64 v192, v148\n"
      "v_mfma_f32_16x16x32_bf16 v[252:255], a[240:243], v[140:143], v[252:255]\n"
      "s_cbranch_scc1 L_edge_44_%=\n"
      "L_eb_45_%=:\n"
      "v_mfma_f32_16x16x32_bf16 v[252:255], a[252:255], v[168:171], v[252:255]\n"
      "global_store_dwordx4 v116, v[156:159], s[54:55] sc1\n"
      "v_permlane16_swap_b32 v148, v192\n"
      "v_mfma_f32_16x16x32_bf16 v[136:139], a[244:247], v[140:143], v[136:139]\n"
      "v_mul_f32 v183, v148, v183\n"
      "v_mul_f32 v180, v148, v180\n"
      "v_mfma_f32_16x16x32_bf16 v[136:139], v[0:3], v[168:171], v[136:139]\n"
      "ds_read_b128 v[196:199], v123 offset:28672\n"
      "v_mul_f32 v181, v148, v181\n"
      "v_mfma_f32_16x16x32_bf16 v[208:211], a[248:251], v[140:143], v[208:211]\n"
      "ds_read_b128 v[140:143], v123 offset:31744\n"
      "v_mul_f32 v235, v148, v235\n"
      "v_mfma_f32_16x16x32_bf16 v[208:211], v[4:7], v[168:171], v[208:211]\n"
      "s_waitcnt lgkmcnt(3)\n"
      "ds_read_b128 v[168:171], v123 offset:34816\n"
      "v_mfma_f32_16x16x32_bf16 v[252:255], v[8:11], v[144:147], v[252:255]\n"
      "ds_read_b128 v[228:231], v123 offset:25600\n"
      "v_mul_f32_e64 v182, v148, v182\n"
      "v_mfma_f32_16x16x32_bf16 v[252:255], v[20:23], v[236:239], v[252:255]\n"
      "v_cvt_pk_bf16_f32 v180, v180, v181\n"
      "v_cvt_pk_bf16_f32 v181, v182, v183\n"
      "v_mfma_f32_16x16x32_bf16 v[136:139], v[12:15], v[144:147], v[136:139]\n"
      "v_mul_f32 v234, v148, v234\n"
      "v_mul_f32 v233, v148, v233\n"
      "v_mfma_f32_16x16x32_bf16 v[136:139], v[24:27], v[236:239], v[136:139]\n"
      "global_store_dwordx4 v116, v[240:243], s[54:55] offset:128 sc1\n"
      "v_mfma_f32_16x16x32_bf16 v[208:211], v[16:19], v[144:147], v[208:211]\n"
      "v_mul_f32_e64 v152, v148, v152\n"
      "v_mul_f32 v232, v148, v232\n"
      "v_mfma_f32_16x16x32_bf16 v[208:211], v[28:31], v[236:239], v[208:211]\n"
      "s_waitcnt lgkmcnt(4)\n"
      "v_cvt_pk_bf16_f32 v182, v232, v233\n"
      "v_cvt_pk_bf16_f32 v183, v234, v235\n"
      "v_mfma_f32_16x16x32_bf16 v[240:243], a[216:219], v[160:163], 0\n"
      "ds_read_b128 v[144:147], v123 offset:20480\n"
      "v_mul_f32_e64 v153, v148, v153\n"
      "v_mfma_f32_16x16x32_bf16 v[240:243], a[228:231], v[220:223], v[240:243]\n"
      "ds_write_b128 v126, v[180:183]\n"
      "v_mfma_f32_16x16x32_bf16 v[180:183], a[220:223], v[160:163], 0\n"
      "ds_read_b128 v[232:235], v123 offset:23552\n"
      "v_cvt_pk_bf16_f32 v152, v152, v153\n"
      "v_mfma_f32_16x16x32_bf16 v[180:183], a[232:235], v[220:223], v[180:183]\n"
      "s_waitcnt lgkmcnt(3)\n"
      "v_mul_f32 v155, v148, v155\n"
      "v_mul_f32 v154, v148, v154\n"
      "v_mfma_f32_16x16x32_bf16 v[156:159], a[224:227], v[160:163], 0\n"
      "s_lshl_b32 s29, s24, 8\n"
      "v_cvt_pk_bf16_f32 v153, v154, v155\n"
      "v_mul_f32_e64 v200, v192, v200\n"
      "v_mfma_f32_16x16x32_bf16 v[156:159], a[236:239], v[220:223], v[156:159]\n"
      "ds_write_b64 v127, v[152:153]\n"
      "v_mfma_f32_16x16x32_bf16 v[240:243], a[240:243], v[228:231], v[240:243]\n"
      "ds_read_b128 v[148:151], v123 offset:26624\n"
      "v_mul_f32_e64 v201, v192, v201\n"
      "v_mfma_f32_16x16x32_bf16 v[240:243], a[252:255], v[196:199], v[240:243]\n"
      "ds_read_b128 v[236:239], v123 offset:29696\n"
      "v_cvt_pk_bf16_f32 v200, v200, v201\n"
      "v_mfma_f32_16x16x32_bf16 v[180:183], a[244:247], v[228:231], v[180:183]\n"
      "s_add_u32 s29, s29, 159088\n"
      "v_mul_f32 v203, v192, v203\n"
      "v_mul_f32 v202, v192, v202\n"
      "v_mfma_f32_16x16x32_bf16 v[180:183], v[0:3], v[196:199], v[180:183]\n"
      "v_mul_f32_e64 v244, v192, v244\n"
      "v_cvt_pk_bf16_f32 v201, v202, v203\n"
      "v_mfma_f32_16x16x32_bf16 v[156:159], a[248:251], v[228:231], v[156:159]\n"
      "v_mul_f32 v245, v192, v245\n"
      "v_cvt_pk_bf16_f32 v202, v244, v245\n"
      "v_mfma_f32_16x16x32_bf16 v[156:159], v[4:7], v[196:199], v[156:159]\n"
      "v_mul_f32 v247, v192, v247\n"
      "v_mul_f32 v246, v192, v246\n"
      "v_mfma_f32_16x16x32_bf16 v[240:243], v[8:11], v[140:143], v[240:243]\n"
      "v_mul_f32 v176, v192, v176\n"
      "v_cvt_pk_bf16_f32 v203, v246, v247\n"
      "v_mfma_f32_16x16x32_bf16 v[240:243], v[20:23], v[168:171], v[240:243]\n"
      "ds_write_b128 v126, v[200:203] offset:2048\n"
      "v_mfma_f32_16x16x32_bf16 v[180:183], v[12:15], v[140:143], v[180:183]\n"
      "ds_read_b128 v[200:203], v123 offset:32768\n"
      "v_mul_f32_e64 v177, v192, v177\n"
      "v_mfma_f32_16x16x32_bf16 v[180:183], v[24:27], v[168:171], v[180:183]\n"
      "ds_read_b128 v[160:163], v123 offset:35840\n"
      "v_cvt_pk_bf16_f32 v176, v176, v177\n"
      "v_mfma_f32_16x16x32_bf16 v[156:159], v[16:19], v[140:143], v[156:159]\n"
      "s_waitcnt lgkmcnt(3)\n"
      "v_mul_f32 v179, v192, v179\n"
      "v_mul_f32_e64 v178, v192, v178\n"
      "v_mfma_f32_16x16x32_bf16 v[156:159], v[28:31], v[168:171], v[156:159]\n"
      "v_cvt_pk_bf16_f32 v177, v178, v179\n"
      "v_fma_f32 v192, v252, v252, s39\n"
      "v_mfma_f32_16x16x32_bf16 v[220:223], a[216:219], v[144:147], 0\n"
      "global_store_dwordx4 v116, v[248:251], s[54:55] offset:256 sc1\n"
      "v_mfma_f32_16x16x32_bf16 v[220:223], a[228:231], v[232:235], v[220:223]\n"
      "ds_write_b64 v127, v[176:177] offset:2048\n"
      "v_mfma_f32_16x16x32_bf16 v[244:247], a[220:223], v[144:147], 0\n"
      "ds_read_b128 v[248:251], v194\n"
      "v_fmac_f32_e64 v192, v253, v253\n"
      "v_mfma_f32_16x16x32_bf16 v[244:247], a[232:235], v[232:235], v[244:247]\n"
      "ds_read_b128 v[152:155], v194 offset:1408\n"
      "v_fmac_f32 v192, v254, v254\n"
      "v_mfma_f32_16x16x32_bf16 v[168:171], a[224:227], v[144:147], 0\n"
      "v_fmac_f32 v192, v255, v255\n"
      "v_fmac_f32 v192, v136, v136\n"
      "v_mfma_f32_16x16x32_bf16 v[168:171], a[236:239], v[232:235], v[168:171]\n"
      "v_fmac_f32 v192, v137, v137\n"
      "v_fmac_f32 v192, v138, v138\n"
      "v_mfma_f32_16x16x32_bf16 v[220:223], a[240:243], v[148:151], v[220:223]\n"
      "v_fmac_f32 v192, v139, v139\n"
      "v_fmac_f32 v192, v208, v208\n"
      "v_mfma_f32_16x16x32_bf16 v[220:223], a[252:255], v[236:239], v[220:223]\n"
      "v_fmac_f32 v192, v209, v209\n"
      "v_fmac_f32_e64 v192, v210, v210\n"
      "v_mfma_f32_16x16x32_bf16 v[244:247], a[244:247], v[148:151], v[244:247]\n"
      "global_store_dwordx4 v117, v[204:207], s[54:55] sc1\n"
      "v_mfma_f32_16x16x32_bf16 v[244:247], v[0:3], v[236:239], v[244:247]\n"
      "s_waitcnt lgkmcnt(3)\n"
      "v_fmac_f32 v192, v211, v211\n"
      "v_mul_f32_e64 v212, v195, v212\n"
      "v_mfma_f32_16x16x32_bf16 v[168:171], a[248:251], v[148:151], v[168:171]\n"
      "ds_read_b128 v[232:235], v194 offset:2816\n"
      "v_mul_f32_e64 v213, v195, v213\n"
      "v_mfma_f32_16x16x32_bf16 v[168:171], v[4:7], v[236:239], v[168:171]\n"
      "v_cvt_pk_bf16_f32 v212, v212, v213\n"
      "v_mul_f32 v214, v195, v214\n"
      "v_mfma_f32_16x16x32_bf16 v[220:223], v[8:11], v[200:203], v[220:223]\n"
      "ds_read_b128 v[148:151], v194 offset:4224\n"
      "v_mul_f32 v215, v195, v215\n"
      "v_mfma_f32_16x16x32_bf16 v[220:223], v[20:23], v[160:163], v[220:223]\n"
      "v_mul_f32 v184, v195, v184\n"
      "v_cvt_pk_bf16_f32 v213, v214, v215\n"
      "v_mfma_f32_16x16x32_bf16 v[244:247], v[12:15], v[200:203], v[244:247]\n"
      "s_waitcnt lgkmcnt(2)\n"
      "v_mul_f32 v185, v195, v185\n"
      "v_cvt_pk_bf16_f32 v214, v184, v185\n"
      "v_mfma_f32_16x16x32_bf16 v[244:247], v[24:27], v[160:163], v[244:247]\n"
      "ds_read_b128 v[144:147], v194 offset:5632\n"
      "v_mul_f32 v186, v195, v186\n"
      "v_mfma_f32_16x16x32_bf16 v[168:171], v[16:19], v[200:203], v[168:171]\n"
      "v_mul_f32_e64 v187, v195, v187\n"
      "v_cvt_pk_bf16_f32 v215, v186, v187\n"
      "v_mfma_f32_16x16x32_bf16 v[168:171], v[28:31], v[160:163], v[168:171]\n"
      "ds_write_b128 v126, v[212:215] offset:4096\n"
      "v_mfma_f32_16x16x32_bf16 v[160:163], a[216:219], v[248:251], 0\n"
      "ds_read_b128 v[236:239], v194 offset:7040\n"
      "v_mul_f32_e64 v188, v195, v188\n"
      "v_mfma_f32_16x16x32_bf16 v[160:163], a[228:231], v[152:155], v[160:163]\n"
      "v_mul_f32 v190, v195, v190\n"
      "v_mfma_f32_16x16x32_bf16 v[184:187], a[220:223], v[248:251], 0\n"
      "v_mul_f32 v189, v195, v189\n"
      "v_cvt_pk_bf16_f32 v188, v188, v189\n"
      "v_mfma_f32_16x16x32_bf16 v[184:187], a[232:235], v[152:155], v[184:187]\n"
      "ds_read_b128 v[196:199], v194 offset:256\n"
      "v_mul_f32 v191, v195, v191\n"
      "v_mfma_f32_16x16x32_bf16 v[176:179], a[224:227], v[248:251], 0\n"
      "s_waitcnt lgkmcnt(4)\n"
      "v_cvt_pk_bf16_f32 v189, v190, v191\n"
      "v_fma_f32 v195, v240, v240, s39\n"
      "v_mfma_f32_16x16x32_bf16 v[176:179], a[236:239], v[152:155], v[176:179]\n"
      "ds_write_b64 v127, v[188:189] offset:4096\n"
      "v_mfma_f32_16x16x32_bf16 v[160:163], a[240:243], v[232:235], v[160:163]\n"
      "ds_read_b128 v[152:155], v194 offset:1664\n"
      "v_fmac_f32 v195, v241, v241\n"
      "v_mfma_f32_16x16x32_bf16 v[160:163], a[252:255], v[148:151], v[160:163]\n"
      "v_fmac_f32 v195, v242, v242\n"
      "v_fmac_f32 v195, v243, v243\n"
      "v_mfma_f32_16x16x32_bf16 v[184:187], a[244:247], v[232:235], v[184:187]\n"
      "s_waitcnt lgkmcnt(3)\n"
      "v_fmac_f32 v195, v180, v180\n"
      "v_fmac_f32 v195, v181, v181\n"
      "v_mfma_f32_16x16x32_bf16 v[184:187], v[0:3], v[148:151], v[184:187]\n"
      "ds_read_b128 v[248:251], v194 offset:3072\n"
      "v_fmac_f32 v195, v182, v182\n"
      "v_mfma_f32_16x16x32_bf16 v[176:179], a[248:251], v[232:235], v[176:179]\n"
      "ds_read_b128 v[228:231], v194 offset:4480\n"
      "v_fmac_f32 v195, v183, v183\n"
      "v_mfma_f32_16x16x32_bf16 v[176:179], v[4:7], v[148:151], v[176:179]\n"
      "v_fmac_f32 v195, v156, v156\n"
      "v_fmac_f32 v195, v157, v157\n"
      "v_mfma_f32_16x16x32_bf16 v[160:163], v[8:11], v[144:147], v[160:163]\n"
      "v_fmac_f32 v195, v158, v158\n"
      "v_fmac_f32 v195, v159, v159\n"
      "v_mfma_f32_16x16x32_bf16 v[160:163], v[20:23], v[236:239], v[160:163]\n"
      "ds_read_b128 v[232:235], v194 offset:5888\n"
      "v_lshl_add_u32 v141, v193, 2, s29\n"
      "v_mfma_f32_16x16x32_bf16 v[184:187], v[12:15], v[144:147], v[184:187]\n"
      "s_waitcnt lgkmcnt(3)\n"
      "v_lshrrev_b32 v150, 3, v193\n"
      "v_mfma_f32_16x16x32_bf16 v[184:187], v[24:27], v[236:239], v[184:187]\n"
      "ds_read_b32 v141, v141\n"
      "v_fma_f32 v193, v220, v220, s39\n"
      "v_mfma_f32_16x16x32_bf16 v[176:179], v[16:19], v[144:147], v[176:179]\n"
      "ds_read_b128 v[212:215], v194 offset:7296\n"
      "v_fmac_f32 v193, v221, v221\n"
      "v_mfma_f32_16x16x32_bf16 v[176:179], v[28:31], v[236:239], v[176:179]\n"
      "s_lshl_b32 s29, s24, 3\n"
      "v_add_u32 v150, s29, v150\n"
      "v_cmp_gt_u32 s[48:49], s26, v150\n"
      "v_mfma_f32_16x16x32_bf16 v[148:151], a[216:219], v[196:199], 0\n"
      "v_fmac_f32 v193, v222, v222\n"
      "v_mfma_f32_16x16x32_bf16 v[148:151], a[228:231], v[152:155], v[148:151]\n"
      "s_mov_b32 exec_hi, 0\n"
      "global_store_dwordx4 v118, v[224:227], s[54:55] sc1\n"
      "s_mov_b32 exec_hi, -1\n"
      "v_mfma_f32_16x16x32_bf16 v[188:191], a[220:223], v[196:199], 0\n"
      "v_fmac_f32 v193, v223, v223\n"
      "v_fmac_f32 v193, v244, v244\n"
      "v_mfma_f32_16x16x32_bf16 v[188:191], a[232:235], v[152:155], v[188:191]\n"
      "v_fmac_f32 v193, v245, v245\n"
      "v_fmac_f32 v193, v246, v246\n"
      "v_mfma_f32_16x16x32_bf16 v[200:203], a[224:227], v[196:199], 0\n"
      "v_fmac_f32 v193, v247, v247\n"
      "v_fmac_f32 v193, v168, v168\n"
      "v_mfma_f32_16x16x32_bf16 v[200:203], a[236:239], v[152:155], v[200:203]\n"
      "s_waitcnt lgkmcnt(1)\n"
      "v_fmac_f32 v193, v169, v169\n"
      "v_fmac_f32 v193, v170, v170\n"
      "v_mfma_f32_16x16x32_bf16 v[148:151], a[240:243], v[248:251], v[148:151]\n"
      "v_fmac_f32 v193, v171, v171\n"
      "v_mov_b32 v143, v193\n"
      "v_mfma_f32_16x16x32_bf16 v[148:151], a[252:255], v[228:231], v[148:151]\n"
      "v_permlane16_swap_b32 v192, v195\n"
      "v_mfma_f32_16x16x32_bf16 v[188:191], a[244:247], v[248:251], v[188:191]\n"
      "v_fma_f32 v194, v160, v160, s39\n"
      "v_add_f32 v192, v192, v195\n"
      "v_mfma_f32_16x16x32_bf16 v[188:191], v[0:3], v[228:231], v[188:191]\n"
      "v_permlane16_swap_b32 v193, v143\n"
      "v_mfma_f32_16x16x32_bf16 v[200:203], a[248:251], v[248:251], v[200:203]\n"
      "s_waitcnt lgkmcnt(0)\n"
      "v_fmac_f32 v194, v161, v161\n"
      "v_add_f32 v193, v193, v143\n"
      "v_mfma_f32_16x16x32_bf16 v[200:203], v[4:7], v[228:231], v[200:203]\n"
      "s_nop 0\n"
      "v_permlane32_swap_b32 v192, v193\n"
      "v_mfma_f32_16x16x32_bf16 v[148:151], v[8:11], v[232:235], v[148:151]\n"
      "v_fmac_f32 v194, v162, v162\n"
      "v_add_f32 v192, v192, v193\n"
      "v_mfma_f32_16x16x32_bf16 v[148:151], v[20:23], v[212:215], v[148:151]\n"
      "ds_write_b32 v129, v192\n"
      "v_mfma_f32_16x16x32_bf16 v[188:191], v[12:15], v[232:235], v[188:191]\n"
      "v_fmac_f32 v194, v163, v163\n"
      "v_fmac_f32 v194, v184, v184\n"
      "v_mfma_f32_16x16x32_bf16 v[188:191], v[24:27], v[212:215], v[188:191]\n"
      "v_fmac_f32 v194, v185, v185\n"
      "v_fmac_f32 v194, v186, v186\n"
      "v_mfma_f32_16x16x32_bf16 v[200:203], v[16:19], v[232:235], v[200:203]\n"
      "v_fmac_f32 v194, v187, v187\n"
      "v_mfma_f32_16x16x32_bf16 v[200:203], v[28:31], v[212:215], v[200:203]\n"
      "v_fmac_f32 v194, v176, v176\n"
      "v_fmac_f32 v194, v177, v177\n"
      "v_fmac_f32 v194, v178, v178\n"
      "v_fmac_f32 v194, v179, v179\n"
      "v_pk_mul_f32 v[142:143], v[148:149], v[148:149]\n"
      "v_pk_fma_f32 v[142:143], v[150:151], v[150:151], v[142:143]\n"
      "v_pk_fma_f32 v[142:143], v[188:189], v[188:189], v[142:143]\n"
      "v_pk_fma_f32 v[142:143], v[190:191], v[190:191], v[142:143]\n"
      "v_pk_fma_f32 v[142:143], v[200:201], v[200:201], v[142:143]\n"
      "v_pk_fma_f32 v[142:143], v[202:203], v[202:203], v[142:143]\n"
      "v_add_f32 v142, s39, v142\n"
      "v_add_f32 v193, v142, v143\n"
      "v_mov_b32 v192, v193\n"
      "v_mov_b32 v195, v192\n"
      "v_permlane16_swap_b32 v194, v193\n"
      "v_add_f32 v194, v194, v193\n"
      "v_permlane16_swap_b32 v192, v195\n"
      "v_add_f32 v192, v192, v195\n"
      "s_nop 1\n"
      "v_permlane32_swap_b32 v194, v192\n"
      "v_add_f32 v194, v194, v192\n"
      "ds_write_b32 v129, v194 offset:2048\n"
      "s_waitcnt lgkmcnt(0)\n"
      "s_barrier\n"
      "s_add_u32 s27, s27, 1\n"
      "ds_read_b128 v[232:235], v128 offset:2048\n"
      "ds_read_b128 v[192:195], v128\n"
      "s_sub_u32 s28, s27, 3\n"
      "s_cselect_b64 s[54:55], s[12:13], s[22:23]\n"
      "s_max_i32 m0, s28, 0\n"
      "ds_read_b128 v[248:251], v119 offset:12288\n"
      "s_movrels_b32 s9, s80\n"
      "s_lshr_b32 s8, s9, 8\n"
      "ds_read_b128 v[144:147], v119\n"
      "s_and_b32 s9, s9, 0xff\n"
      "s_mul_i32 s28, s8, 1373568\n"
      "s_mul_i32 s29, s9, 2688\n"
      "s_add_u32 s28, s28, s29\n"
      "s_cmp_eq_u32 s8, 72\n"
      "ds_read_b128 v[228:231], v120\n"
      "s_cselect_b32 s8, 0, 100270464\n"
      "ds_read_b128 v[212:215], v119 offset:6144\n"
      "s_cmp_eq_u32 s9, 72\n"
      "s_cselect_b32 s9, 0, 196224\n"
      "ds_read_b128 v[152:155], v121\n"
      "s_add_u32 s10, s8, s9\n"
      "s_add_u32 s28, s28, s10\n"
      "s_add_u32 s54, s54, s28\n"
      "s_addc_u32 s55, s55, 0\n"
      "s_cmp_lg_u32 s10, s11\n"
      "s_cbranch_scc1 L_edge_46_%=\n"
      "L_eb_47_%=:\n"
      "s_waitcnt lgkmcnt(5)\n"
      "v_add_f32 v194, |v194|, |v195|\n"
      "v_add_f32 v192, |v192|, |v193|\n"
      "v_add_f32 v192, v192, v194\n"
      "v_add_f32 v232, |v232|, |v233|\n"
      "v_rsq_f32 v192, v192\n"
      "v_subrev_u32 v233, 73728, v127\n"
      "v_mul_f32 v192, s41, v192\n"
      "v_mov_b32_e64 v195, v192\n"
      "v_add_f32 v234, |v234|, |v235|\n"
      "v_add_f32 v232, v232, v234\n"
      "v_permlane32_swap_b32 v192, v195\n"
      "v_mov_b32 v235, v192\n"
      "v_rsq_f32 v232, v232\n"
      "v_pk_mul_f32 v[220:221], v[220:221], v[194:195] op_sel:[0,1]\n"
      "v_pk_mul_f32 v[168:169], v[168:169], v[194:195] op_sel:[0,1]\n"
      "v_permlane16_swap_b32_e64 v192, v235\n"
      "v_pk_mul_f32 v[240:241], v[240:241], v[234:235] op_sel:[0,1]\n"
      "v_pk_mul_f32 v[252:253], v[252:253], v[192:193] op_sel_hi:[1,0]\n"
      "v_pk_mul_f32 v[136:137], v[136:137], v[192:193] op_sel_hi:[1,0]\n"
      "v_cvt_pk_bf16_f32 v252, v252, v253\n"
      "v_mul_f32 v232, s41, v232\n"
      "v_pk_mul_f32 v[158:159], v[158:159], v[234:235] op_sel:[0,1]\n"
      "v_mov_b32 v234, v232\n"
      "s_waitcnt lgkmcnt(0)\n"
      "global_store_dwordx4 v116, v[144:147], s[54:55] sc1\n"
      "v_pk_mul_f32 v[254:255], v[254:255], v[192:193] op_sel_hi:[1,0]\n"
      "v_pk_mul_f32 v[156:157], v[156:157], v[234:235] op_sel:[0,1]\n"
      "v_permlane32_swap_b32 v232, v234\n"
      "v_pk_mul_f32 v[170:171], v[170:171], v[194:195] op_sel:[0,1]\n"
      "v_mov_b32_e64 v234, v232\n"
      "v_pk_mul_f32 v[208:209], v[208:209], v[192:193] op_sel_hi:[1,0]\n"
      "v_cvt_pk_bf16_f32 v253, v254, v255\n"
      "v_pk_mul_f32 v[222:223], v[222:223], v[194:195] op_sel:[0,1]\n"
      "v_permlane16_swap_b32_e64 v232, v234\n"
      "v_pk_mul_f32 v[138:139], v[138:139], v[192:193] op_sel_hi:[1,0]\n"
      "v_cvt_pk_bf16_f32 v220, v220, v221\n"
      "v_pk_mul_f32 v[186:187], v[186:187], v[232:233] op_sel_hi:[1,0]\n"
      "v_pk_mul_f32 v[162:163], v[162:163], v[232:233] op_sel_hi:[1,0]\n"
      "v_cvt_pk_bf16_f32 v240, v240, v241\n"
      "v_pk_mul_f32 v[178:179], v[178:179], v[232:233] op_sel_hi:[1,0]\n"
      "v_pk_mul_f32 v[190:191], v[190:191], v[234:235] op_sel_hi:[1,0]\n"
      "v_pk_mul_f32 v[148:149], v[148:149], v[234:235] op_sel_hi:[1,0]\n"
      "v_pk_mul_f32 v[184:185], v[184:185], v[232:233] op_sel_hi:[1,0]\n"
      "v_pk_mul_f32 v[160:161], v[160:161], v[232:233] op_sel_hi:[1,0]\n"
      "v_cvt_pk_bf16_f32 v254, v136, v137\n"
      "v_cvt_pk_bf16_f32 v148, v148, v149\n"
      "v_pk_mul_f32 v[150:151], v[150:151], v[234:235] op_sel_hi:[1,0]\n"
      "v_cvt_pk_bf16_f32 v149, v150, v151\n"
      "v_pk_mul_f32 v[180:181], v[180:181], v[234:235] op_sel:[0,1]\n"
      "v_pk_mul_f32 v[176:177], v[176:177], v[232:233] op_sel_hi:[1,0]\n"
      "v_cvt_pk_bf16_f32 v160, v160, v161\n"
      "v_pk_mul_f32 v[188:189], v[188:189], v[234:235] op_sel_hi:[1,0]\n"
      "v_pk_mul_f32 v[246:247], v[246:247], v[194:195] op_sel:[0,1]\n"
      "v_pk_mul_f32 v[182:183], v[182:183], v[234:235] op_sel:[0,1]\n"
      "v_cvt_pk_bf16_f32 v151, v190, v191\n"
      "v_pk_mul_f32 v[242:243], v[242:243], v[234:235] op_sel:[0,1]\n"
      "v_pk_mul_f32 v[200:201], v[200:201], v[234:235] op_sel_hi:[1,0]\n"
      "v_cvt_pk_bf16_f32 v156, v156, v157\n"
      "v_pk_mul_f32 v[210:211], v[210:211], v[192:193] op_sel_hi:[1,0]\n"
      "v_cvt_pk_bf16_f32 v161, v162, v163\n"
      "v_pk_mul_f32 v[202:203], v[202:203], v[234:235] op_sel_hi:[1,0]\n"
      "v_cvt_pk_bf16_f32 v255, v138, v139\n"
      "v_cvt_pk_bf16_f32 v162, v184, v185\n"
      "v_cvt_pk_bf16_f32 v157, v158, v159\n"
      "v_subrev_u32 v235, 73728, v126\n"
      "v_cvt_pk_bf16_f32 v208, v208, v209\n"
      "v_cvt_pk_bf16_f32 v168, v168, v169\n"
      "v_cvt_pk_bf16_f32 v163, v186, v187\n"
      "v_cvt_pk_bf16_f32 v200, v200, v201\n"
      "v_cvt_pk_bf16_f32 v169, v170, v171\n"
      "v_cvt_pk_bf16_f32 v209, v210, v211\n"
      "ds_write_b64 v127, v[156:157] offset:20480\n"
      "global_store_dwordx4 v116, v[212:215], s[54:55] offset:128 sc1\n"
      "ds_write_b64 v127, v[208:209] offset:18432\n"
      "v_cvt_pk_bf16_f32 v221, v222, v223\n"
      "v_pk_mul_f32 v[244:245], v[244:245], v[194:195] op_sel:[0,1]\n"
      "v_cvt_pk_bf16_f32 v222, v244, v245\n"
      "v_cvt_pk_bf16_f32 v241, v242, v243\n"
      "v_cvt_pk_bf16_f32 v201, v202, v203\n"
      "v_cvt_pk_bf16_f32 v242, v180, v181\n"
      "v_cvt_pk_bf16_f32 v223, v246, v247\n"
      "v_cvt_pk_bf16_f32 v176, v176, v177\n"
      "v_cvt_pk_bf16_f32 v243, v182, v183\n"
      "v_cvt_pk_bf16_f32 v177, v178, v179\n"
      "v_cvt_pk_bf16_f32 v150, v188, v189\n"
      "ds_write_b128 v235, v[148:151] offset:20480\n"
      "ds_write_b64 v233, v[176:177] offset:18432\n"
      "global_store_dwordx4 v116, v[248:251], s[54:55] offset:256 sc1\n"
      "ds_write_b128 v126, v[240:243] offset:20480\n"
      "ds_write_b64 v127, v[168:169] offset:22528\n"
      "ds_write_b64 v233, v[200:201] offset:20480\n"
      "ds_write_b128 v126, v[220:223] offset:22528\n"
      "ds_write_b128 v126, v[252:255] offset:18432\n"
      "ds_write_b128 v235, v[160:163] offset:18432\n"
      "global_store_dwordx4 v117, v[228:231], s[54:55] sc1\n"
      "s_mov_b32 exec_hi, 0\n"
      "global_store_dwordx4 v118, v[152:155], s[54:55] sc1\n"
      "s_mov_b32 exec_hi, -1\n"
      "s_waitcnt lgkmcnt(0)\n"
      "s_barrier\n"
      "ds_read_b128 v[200:203], v119 offset:24576\n"
      "ds_read_b128 v[228:231], v119 offset:18432\n"
      "s_add_u32 s27, s27, 1\n"
      "v_subrev_u32 v143, 73728, v119\n"
      "s_sub_u32 s28, s27, 3\n"
      "ds_read_b128 v[208:211], v143 offset:18432\n"
      "ds_read_b128 v[160:163], v121 offset:18432\n"
      "ds_read_b128 v[248:251], v143 offset:24576\n"
      "s_cselect_b64 s[54:55], s[12:13], s[22:23]\n"
      "ds_read_b128 v[152:155], v143 offset:30720\n"
      "ds_read_b128 v[168:171], v120 offset:18432\n"
      "s_max_i32 m0, s28, 0\n"
      "ds_read_b128 v[212:215], v119 offset:30720\n"
      "s_movrels_b32 s9, s80\n"
      "s_lshr_b32 s8, s9, 8\n"
      "s_and_b32 s9, s9, 0xff\n"
      "s_mul_i32 s28, s8, 1373568\n"
      "s_mul_i32 s29, s9, 2688\n"
      "s_add_u32 s28, s28, s29\n"
      "s_cmp_eq_u32 s8, 72\n"
      "s_cselect_b32 s8, 0, 100270464\n"
      "s_cmp_eq_u32 s9, 72\n"
      "s_cselect_b32 s9, 0, 196224\n"
      "s_add_u32 s10, s8, s9\n"
      "s_add_u32 s28, s28, s10\n"
      "s_add_u32 s54, s54, s28\n"
      "s_addc_u32 s55, s55, 0\n"
      "s_cmp_lg_u32 s10, s11\n"
      "s_waitcnt lgkmcnt(6)\n"
      "s_cbranch_scc1 L_edge_48_%=\n"
      "L_eb_49_%=:\n"
      "global_store_dwordx4 v116, v[228:231], s[54:55] sc1\n"
      "global_store_dwordx4 v116, v[200:203], s[54:55] offset:128 sc1\n"
      "s_waitcnt lgkmcnt(2)\n"
      "s_waitcnt lgkmcnt(1)\n"
      "s_waitcnt lgkmcnt(0)\n"
      "global_store_dwordx4 v116, v[212:215], s[54:55] offset:256 sc1\n"
      "global_store_dwordx4 v117, v[168:171], s[54:55] sc1\n"
      "s_mov_b32 exec_hi, 0\n"
      "global_store_dwordx4 v118, v[160:163], s[54:55] sc1\n"
      "s_mov_b32 exec_hi, -1\n"
      "s_mov_b64 exec, s[48:49]\n"
      "s_cbranch_execz L_np_50_%=\n"
      "global_store_dwordx4 v141, v[208:211], s[22:23] sc1\n"
      "global_store_dwordx4 v141, v[248:251], s[22:23] offset:128 sc1\n"
      "global_store_dwordx4 v141, v[152:155], s[22:23] offset:256 sc1\n"
      "L_np_50_%=:\n"
      "s_mov_b64 exec, -1\n"
      "s_nop 0\n"
      "s_endpgm\n"
      "L_gen_8_%=:\n"
      "s_waitcnt lgkmcnt(0)\n"
      "s_nop 5\n"
      "s_cmp_eq_u64 s[48:49], 0\n"
      "s_cbranch_scc1 L_go_63_%=\n"
      "s_mov_b32 m0, s27\n"
      "s_nop 0\n"
      "s_movrels_b32 s9, s80\n"
      "s_lshr_b32 s8, s9, 8\n"
      "s_and_b32 s9, s9, 0xff\n"
      "s_mul_i32 s10, s8, 73\n"
      "s_add_u32 s10, s10, s9\n"
      "s_mul_i32 s30, s10, 4802\n"
      "s_add_u32 s30, s30, s18\n"
      "s_and_b32 s29, s30, 15\n"
      "s_add_u32 s30, s29, 110592\n"
      "s_lshl_b64 s[34:35], s[48:49], 4\n"
      "s_lshr_b64 s[34:35], s[34:35], 5\n"
      "s_cbranch_scc1 L_gen_9_%=\n"
      "v_mbcnt_lo_u32_b32 v148, -1, 0\n"
      "v_mbcnt_hi_u32_b32 v148, -1, v148\n"
      "v_lshlrev_b32 v149, 1, v148\n"
      "v_cmp_gt_u32 vcc, s29, v149\n"
      "v_add_u32 v150, 4802, v149\n"
      "s_nop 0\n"
      "v_cndmask_b32 v149, v150, v149, vcc\n"
      "v_mov_b32_e64 v150, 0\n"
      "v_add_u32 v149, 110592, v149\n"
      "s_mov_b64 exec, 0x7f\n"
      "ds_write_b16 v149, v150\n"
      "s_mov_b64 exec, -1\n"
      "v_lshlrev_b32_e64 v149, 4, v148\n"
      "v_add_u32 v149, 110592, v149\n"
      "v_min_u32 v148, 111296, v149\n"
      "ds_read_b128 v[196:199], v149\n"
      "ds_read_b128 v[150:153], v149 offset:2048\n"
      "ds_read_b128 v[228:231], v149 offset:1024\n"
      "ds_read_b128 v[140:143], v148 offset:4096\n"
      "ds_read_b128 v[136:139], v149 offset:3072\n"
      "s_waitcnt lgkmcnt(4)\n"
      "v_or3_b32 v196, v196, v197, v198\n"
      "s_waitcnt lgkmcnt(2)\n"
      "v_or3_b32 v196, v196, v199, v228\n"
      "v_or3_b32 v196, v196, v229, v230\n"
      "v_or3_b32 v196, v196, v231, v150\n"
      "s_waitcnt lgkmcnt(0)\n"
      "v_or3_b32 v196, v196, v151, v152\n"
      "v_or3_b32 v196, v196, v153, v136\n"
      "v_or3_b32 v196, v196, v137, v138\n"
      "v_or3_b32 v196, v196, v139, v140\n"
      "v_or3_b32 v196, v196, v141, v142\n"
      "v_bitop3_b32 v196, v196, v143, s40 bitop3:0xa8\n"
      "v_cmp_ne_u32 s[48:49], 0, v196\n"
      "s_cmp_lg_u64 s[48:49], 0\n"
      "s_cbranch_scc1 L_gen_9_%=\n"
      "s_cmp_eq_u64 s[50:51], 0\n"
      "s_cbranch_scc1 L_fast_11_%=\n"
      "L_go_63_%=:\n"
      "v_mfma_f32_16x16x32_bf16 v[148:151], v[180:183], v[168:171], v[90:93]\n"
      "ds_read_b128 v[196:199], v128 offset:3072\n"
      "v_max3_f32 v231, v204, v205, v206\n"
      "v_mfma_f32_16x16x32_bf16 v[148:151], v[34:37], v[38:41], v[148:151]\n"
      "v_max3_f32 v229, v207, v232, v233\n"
      "v_max3_f32 v231, v231, v234, v235\n"
      "v_mfma_f32_16x16x32_bf16 v[140:143], v[164:167], v[168:171], v[94:97]\n"
      "v_max3_f32 v229, v229, v192, v193\n"
      "v_max3_f32 v231, v231, v194, v195\n"
      "v_mfma_f32_16x16x32_bf16 v[140:143], v[40:43], v[38:41], v[140:143]\n"
      "v_max3_f32 v231, v231, v229, v200\n"
      "v_mov_b32 v229, v231\n"
      "v_mfma_f32_16x16x32_bf16 v[136:139], v[248:251], v[168:171], v[72:75]\n"
      "s_nop 0\n"
      "v_permlane16_swap_b32 v231, v229\n"
      "v_mfma_f32_16x16x32_bf16 v[136:139], v[236:239], v[164:167], v[136:139]\n"
      "v_max_f32 v231, v231, v229\n"
      "v_mov_b32 v229, v231\n"
      "v_mfma_f32_16x16x32_bf16 v[136:139], v[240:243], v[38:41], v[136:139]\n"
      "s_nop 0\n"
      "v_permlane32_swap_b32 v231, v229\n"
      "v_mfma_f32_16x16x32_bf16 v[152:155], v[212:215], v[168:171], v[98:101]\n"
      "v_max_f32 v231, v231, v229\n"
      "v_sub_f32 v204, v204, v231\n"
      "v_mfma_f32_16x16x32_bf16 v[152:155], v[46:49], v[38:41], v[152:155]\n"
      "v_sub_f32 v205, v205, v231\n"
      "v_sub_f32 v206, v206, v231\n"
      "v_mfma_f32_16x16x32_bf16 v[226:229], v[180:183], v[252:255], v[102:105]\n"
      "v_sub_f32 v232, v232, v231\n"
      "v_sub_f32 v207, v207, v231\n"
      "v_mfma_f32_16x16x32_bf16 v[226:229], v[34:37], v[44:47], v[226:229]\n"
      "s_waitcnt lgkmcnt(0)\n"
      "v_sub_f32 v194, v194, v231\n"
      "v_sub_f32 v193, v193, v231\n"
      "v_sub_f32_e64 v234, v234, v231\n"
      "v_mfma_f32_16x16x32_bf16 v[168:171], v[164:167], v[252:255], v[106:109]\n"
      "v_sub_f32 v235, v235, v231\n"
      "v_sub_f32 v192, v192, v231\n"
      "v_mfma_f32_16x16x32_bf16 v[168:171], v[40:43], v[44:47], v[168:171]\n"
      "v_exp_f32_e64 v232, v232\n"
      "v_mfma_f32_16x16x32_bf16 v[164:167], v[248:251], v[252:255], v[74:77]\n"
      "v_sub_f32 v195, v195, v231\n"
      "v_sub_f32 v200, v200, v231\n"
      "v_mfma_f32_16x16x32_bf16 v[164:167], v[236:239], v[212:215], v[164:167]\n"
      "v_exp_f32_e64 v204, v204\n"
      "v_mfma_f32_16x16x32_bf16 v[164:167], v[240:243], v[44:47], v[164:167]\n"
      "v_exp_f32 v195, v195\n"
      "v_exp_f32 v205, v205\n"
      "v_mfma_f32_16x16x32_bf16 v[180:183], v[212:215], v[252:255], v[110:113]\n"
      "v_exp_f32_e64 v206, v206\n"
      "v_mfma_f32_16x16x32_bf16 v[180:183], v[46:49], v[44:47], v[180:183]\n"
      "v_exp_f32 v207, v207\n"
      "v_mfma_f32_16x16x32_bf16 v[212:215], v[236:239], v[248:251], v[76:79]\n"
      "v_sub_f32 v233, v233, v231\n"
      "v_exp_f32 v234, v234\n"
      "v_exp_f32 v192, v192\n"
      "v_exp_f32 v200, v200\n"
      "v_exp_f32 v233, v233\n"
      "v_cvt_pk_bf16_f32 v204, v204, v205\n"
      "v_exp_f32 v194, v194\n"
      "v_exp_f32 v235, v235\n"
      "v_cvt_pk_bf16_f32 v205, v206, v207\n"
      "v_bitop3_b32 v196, v196, v197, s4 bitop3:0x7e\n"
      "v_cvt_pk_bf16_f32 v63, v194, v195\n"
      "v_cvt_pk_bf16_f32 v206, v232, v233\n"
      "v_exp_f32_e64 v193, v193\n"
      "v_cvt_pk_bf16_f32 v207, v234, v235\n"
      "v_bitop3_b32 v198, v198, v199, s4 bitop3:0x7e\n"
      "v_mfma_f32_16x16x16_bf16 v[212:215], v[242:243], v[240:241], v[212:215]\n"
      "v_cvt_pk_bf16_f32 v62, v192, v193\n"
      "v_cvt_pk_bf16_f32 v64, v200, 0\n"
      "v_mfma_f32_16x16x32_bf16 v[192:195], v[66:69], v[204:207], 0\n"
      "v_or_b32 v196, v196, v198\n"
      "v_cmp_ne_u32 vcc, 0, v196\n"
      "v_mfma_f32_16x16x32_bf16 v[192:195], v[66:69], v[62:65], v[192:195]\n"
      "s_mov_b64 s[48:49], vcc\n"
      "v_max3_f32 v230, v151, v140, v141\n"
      "v_max3_f32 v231, v148, v149, v150\n"
      "v_mfma_f32_16x16x32_bf16 v[248:251], v[216:219], v[204:207], 0\n"
      "s_cbranch_vccnz L_flg_64_%=\n"
      "L_fb_65_%=:\n"
      "v_mfma_f32_16x16x32_bf16 v[248:251], v[50:53], v[62:65], v[248:251]\n"
      "v_max3_f32 v231, v231, v142, v143\n"
      "v_max3_f32 v230, v230, v152, v153\n"
      "v_mfma_f32_16x16x32_bf16 v[240:243], v[172:175], v[204:207], 0\n"
      "v_max3_f32 v231, v231, v154, v155\n"
      "v_max3_f32 v231, v231, v230, v136\n"
      "v_mfma_f32_16x16x32_bf16 v[240:243], v[54:57], v[62:65], v[240:243]\n"
      "v_rcp_f32 v192, v192\n"
      "v_mov_b32 v230, v231\n"
      "v_mfma_f32_16x16x32_bf16 v[232:235], v[144:147], v[204:207], 0\n"
      "s_nop 0\n"
      "v_permlane16_swap_b32 v231, v230\n"
      "v_mfma_f32_16x16x32_bf16 v[232:235], v[58:61], v[62:65], v[232:235]\n"
      "v_max_f32 v231, v231, v230\n"
      "v_mov_b32 v230, v231\n"
      "v_mul_f32 v248, v192, v248\n"
      "v_mul_f32 v249, v192, v249\n"
      "v_permlane32_swap_b32 v231, v230\n"
      "v_max_f32 v231, v231, v230\n"
      "v_sub_f32 v148, v148, v231\n"
      "v_sub_f32 v149, v149, v231\n"
      "v_sub_f32 v155, v155, v231\n"
      "v_max3_f32 v230, v229, v168, v169\n"
      "v_sub_f32 v153, v153, v231\n"
      "v_sub_f32 v152, v152, v231\n"
      "v_sub_f32 v154, v154, v231\n"
      "v_sub_f32 v136, v136, v231\n"
      "v_sub_f32 v141, v141, v231\n"
      "v_sub_f32 v140, v140, v231\n"
      "v_sub_f32 v150, v150, v231\n"
      "v_exp_f32 v136, v136\n"
      "v_exp_f32 v155, v155\n"
      "v_exp_f32 v152, v152\n"
      "v_exp_f32 v153, v153\n"
      "v_sub_f32 v142, v142, v231\n"
      "v_sub_f32 v151, v151, v231\n"
      "v_exp_f32 v150, v150\n"
      "v_exp_f32 v142, v142\n"
      "v_mul_f32 v240, v192, v240\n"
      "v_exp_f32 v148, v148\n"
      "v_exp_f32 v149, v149\n"
      "v_exp_f32 v154, v154\n"
      "v_exp_f32 v140, v140\n"
      "v_exp_f32 v151, v151\n"
      "v_sub_f32 v143, v143, v231\n"
      "v_exp_f32 v143, v143\n"
      "v_exp_f32 v141, v141\n"
      "v_cvt_pk_bf16_f32 v148, v148, v149\n"
      "v_cvt_pk_bf16_f32 v63, v154, v155\n"
      "v_mul_f32 v250, v192, v250\n"
      "v_cvt_pk_bf16_f32 v149, v150, v151\n"
      "v_cvt_pk_bf16_f32 v150, v140, v141\n"
      "v_cvt_pk_bf16_f32 v62, v152, v153\n"
      "v_cvt_pk_bf16_f32 v151, v142, v143\n"
      "v_cvt_pk_bf16_f32 v248, v248, v249\n"
      "v_cvt_pk_bf16_f32 v64, v136, 0\n"
      "v_mfma_f32_16x16x32_bf16 v[202:205], v[66:69], v[148:151], 0\n"
      "s_add_u32 s28, s27, 2\n"
      "v_mul_f32 v251, v192, v251\n"
      "v_cvt_pk_bf16_f32 v249, v250, v251\n"
      "v_mfma_f32_16x16x32_bf16 v[202:205], v[66:69], v[62:65], v[202:205]\n"
      "s_min_u32 m0, s28, s3\n"
      "v_mul_f32 v243, v192, v243\n"
      "v_mul_f32 v241, v192, v241\n"
      "v_mfma_f32_16x16x32_bf16 v[152:155], v[216:219], v[148:151], 0\n"
      "s_movrels_b32 s9, s80\n"
      "v_cvt_pk_bf16_f32 v250, v240, v241\n"
      "v_mul_f32 v242, v192, v242\n"
      "v_mfma_f32_16x16x32_bf16 v[152:155], v[50:53], v[62:65], v[152:155]\n"
      "s_lshr_b32 s8, s9, 8\n"
      "v_cvt_pk_bf16_f32 v251, v242, v243\n"
      "v_mul_f32 v235, v192, v235\n"
      "v_mfma_f32_16x16x32_bf16 v[140:143], v[172:175], v[148:151], 0\n"
      "s_and_b32 s9, s9, 0xff\n"
      "v_mul_f32 v233, v192, v233\n"
      "v_mul_f32_e64 v232, v192, v232\n"
      "v_mfma_f32_16x16x32_bf16 v[140:143], v[54:57], v[62:65], v[140:143]\n"
      "ds_write_b128 v124, v[248:251]\n"
      "v_max3_f32 v231, v226, v227, v228\n"
      "v_mfma_f32_16x16x32_bf16 v[236:239], v[144:147], v[148:151], 0\n"
      "s_mul_i32 s10, s8, 73\n"
      "v_mul_f32 v234, v192, v234\n"
      "v_cvt_pk_bf16_f32 v232, v232, v233\n"
      "v_mfma_f32_16x16x32_bf16 v[236:239], v[58:61], v[62:65], v[236:239]\n"
      "s_add_u32 s10, s10, s9\n"
      "v_max3_f32 v231, v231, v170, v171\n"
      "v_max3_f32 v230, v230, v180, v181\n"
      "v_max3_f32 v231, v231, v182, v183\n"
      "v_max3_f32 v231, v231, v230, v164\n"
      "v_rcp_f32 v202, v202\n"
      "v_mov_b32 v230, v231\n"
      "v_cndmask_b32 v201, v201, v137, s[42:43]\n"
      "v_cndmask_b32 v201, v201, v165, s[44:45]\n"
      "v_permlane16_swap_b32 v231, v230\n"
      "v_max_f32 v231, v231, v230\n"
      "v_mov_b32 v230, v231\n"
      "v_cvt_pk_bf16_f32 v233, v234, v235\n"
      "v_cndmask_b32 v201, v201, v213, s[46:47]\n"
      "v_permlane32_swap_b32 v231, v230\n"
      "v_max_f32 v231, v231, v230\n"
      "v_sub_f32 v170, v170, v231\n"
      "v_sub_f32 v182, v182, v231\n"
      "v_sub_f32 v183, v183, v231\n"
      "v_sub_f32 v169, v169, v231\n"
      "v_sub_f32 v180, v180, v231\n"
      "v_sub_f32 v226, v226, v231\n"
      "v_exp_f32 v182, v182\n"
      "v_exp_f32 v170, v170\n"
      "v_exp_f32 v226, v226\n"
      "v_exp_f32 v180, v180\n"
      "v_sub_f32 v227, v227, v231\n"
      "v_mul_f32 v152, v202, v152\n"
      "v_exp_f32 v227, v227\n"
      "v_sub_f32 v168, v168, v231\n"
      "v_sub_f32 v164, v164, v231\n"
      "v_sub_f32 v181, v181, v231\n"
      "v_sub_f32 v229, v229, v231\n"
      "v_exp_f32 v169, v169\n"
      "v_sub_f32 v228, v228, v231\n"
      "v_exp_f32 v229, v229\n"
      "v_exp_f32 v164, v164\n"
      "v_exp_f32 v183, v183\n"
      "v_cvt_pk_bf16_f32 v226, v226, v227\n"
      "v_sub_f32 v171, v171, v231\n"
      "v_exp_f32 v228, v228\n"
      "v_exp_f32 v171, v171\n"
      "v_exp_f32 v168, v168\n"
      "v_cvt_pk_bf16_f32 v227, v228, v229\n"
      "v_exp_f32 v181, v181\n"
      "v_cvt_pk_bf16_f32 v228, v168, v169\n"
      "v_cvt_pk_bf16_f32 v229, v170, v171\n"
      "v_cvt_pk_bf16_f32 v64, v164, 0\n"
      "v_max_f32_dpp v231, v201, v201 row_ror:8 row_mask:0xf bank_mask:0xf\n"
      "v_cvt_pk_bf16_f32 v63, v182, v183\n"
      "v_cvt_pk_bf16_f32 v62, v180, v181\n"
      "v_mfma_f32_16x16x32_bf16 v[148:151], v[66:69], v[226:229], 0\n"
      "ds_read_b128 v[168:171], v114 offset:18432\n"
      "v_max_f32_dpp v231, v231, v231 row_ror:4 row_mask:0xf bank_mask:0xf\n"
      "v_mfma_f32_16x16x32_bf16 v[148:151], v[66:69], v[62:65], v[148:151]\n"
      "ds_read_b128 v[192:195], v115 offset:18432\n"
      "v_max_f32_dpp v231, v231, v231 row_ror:2 row_mask:0xf bank_mask:0xf\n"
      "v_mfma_f32_16x16x32_bf16 v[248:251], v[216:219], v[226:229], 0\n"
      "s_mul_i32 s28, s10, 4802\n"
      "v_mul_f32 v153, v202, v153\n"
      "v_max_f32_dpp v231, v231, v231 row_ror:1 row_mask:0xf bank_mask:0xf\n"
      "v_mfma_f32_16x16x32_bf16 v[248:251], v[50:53], v[62:65], v[248:251]\n"
      "s_add_u32 s56, s18, s28\n"
      "v_mul_f32 v140, v202, v140\n"
      "v_cvt_pk_bf16_f32 v152, v152, v153\n"
      "v_mul_f32 v142, v202, v142\n"
      "v_max_f32_dpp v231, v231, v231 row_bcast:15 row_mask:0xa bank_mask:0xf\n"
      "v_mfma_f32_16x16x32_bf16 v[240:243], v[172:175], v[226:229], 0\n"
      "s_addc_u32 s57, s19, 0\n"
      "v_max_f32_dpp v231, v231, v231 row_bcast:31 row_mask:0xc bank_mask:0xf\n"
      "v_mul_f32_e64 v154, v202, v154\n"
      "v_mfma_f32_16x16x32_bf16 v[240:243], v[54:57], v[62:65], v[240:243]\n"
      "s_and_b32 s56, s56, -16\n"
      "v_readlane_b32 s31, v231, 63\n"
      "v_mul_f32_e64 v155, v202, v155\n"
      "v_mfma_f32_16x16x32_bf16 v[204:207], v[144:147], v[226:229], 0\n"
      "s_sub_u32 s58, s56, 110592\n"
      "v_cvt_pk_bf16_f32 v153, v154, v155\n"
      "v_subrev_f32 v201, s31, v201\n"
      "v_mfma_f32_16x16x32_bf16 v[204:207], v[58:61], v[62:65], v[204:207]\n"
      "s_subb_u32 s59, s57, 0\n"
      "v_exp_f32 v201, v201\n"
      "v_mul_f32 v143, v202, v143\n"
      "v_cvt_pk_bf16_f32 v201, v201, v201\n"
      "v_mul_f32 v141, v202, v141\n"
      "v_cvt_pk_bf16_f32 v154, v140, v141\n"
      "s_waitcnt lgkmcnt(0)\n"
      "v_mfma_f32_16x16x32_bf16 v[196:199], a[0:3], v[168:171], 0\n"
      "s_sub_u32 s56, s56, 73728\n"
      "v_cvt_pk_bf16_f32 v155, v142, v143\n"
      "v_mul_f32_e64 v236, v202, v236\n"
      "v_mfma_f32_16x16x32_bf16 v[196:199], a[36:39], v[192:195], v[196:199]\n"
      "ds_write_b16 v130, v201\n"
      "v_mfma_f32_16x16x32_bf16 v[228:231], a[4:7], v[168:171], 0\n"
      "ds_read_b128 v[140:143], v131\n"
      "v_mul_f32_e64 v237, v202, v237\n"
      "v_mfma_f32_16x16x32_bf16 v[228:231], a[40:43], v[192:195], v[228:231]\n"
      "ds_read_b128 v[180:183], v131 offset:64\n"
      "v_mul_f32 v239, v202, v239\n"
      "v_mul_f32 v238, v202, v238\n"
      "v_cvt_pk_bf16_f32 v236, v236, v237\n"
      "v_cvt_pk_bf16_f32 v237, v238, v239\n"
      "ds_write_b128 v124, v[152:155] offset:1024\n"
      "s_subb_u32 s57, s57, 0\n"
      "ds_write2st64_b64 v125, v[232:233], v[236:237] offset0:0 offset1:2\n"
      "s_mul_i32 s28, s8, 1373568\n"
      "v_mov_b32 v238, 0\n"
      "s_mul_i32 s29, s9, 2688\n"
      "s_add_u32 s28, s28, s29\n"
      "s_cmp_eq_u32 s8, 72\n"
      "ds_write_b32 v129, v238\n"
      "s_cselect_b32 s8, 0, 100270464\n"
      "s_cmp_eq_u32 s9, 72\n"
      "s_cselect_b32 s9, 0, 196224\n"
      "s_add_u32 s10, s8, s9\n"
      "s_waitcnt lgkmcnt(3)\n"
      "v_mfma_f32_16x16x32_bf16 v[236:239], v[140:143], v[66:69], 0\n"
      "s_add_u32 s28, s28, s10\n"
      "v_mfma_f32_16x16x32_bf16 v[236:239], v[180:183], v[66:69], v[236:239]\n"
      "s_add_u32 s52, s16, s28\n"
      "s_addc_u32 s53, s17, 0\n"
      "v_mfma_f32_16x16x32_bf16 v[152:155], v[140:143], v[216:219], 0\n"
      "s_cmp_lg_u32 s10, s11\n"
      "v_mfma_f32_16x16x32_bf16 v[152:155], v[180:183], v[50:53], v[152:155]\n"
      "s_cbranch_scc1 L_edge_67_%=\n"
      "L_eb_68_%=:\n"
      "v_mfma_f32_16x16x32_bf16 v[216:219], v[140:143], v[172:175], 0\n"
      "s_nop 0\n"
      "v_mov_b32_dpp v236, v236 quad_perm:[0,0,0,0] row_mask:0xf bank_mask:0xf\n"
      "v_mfma_f32_16x16x32_bf16 v[216:219], v[180:183], v[54:57], v[216:219]\n"
      "v_mfma_f32_16x16x32_bf16 v[172:175], v[140:143], v[144:147], 0\n"
      "v_rcp_f32 v236, v236\n"
      "v_mfma_f32_16x16x32_bf16 v[172:175], v[180:183], v[58:61], v[172:175]\n"
      "s_nop 4\n"
      "v_cndmask_b32 v152, v152, v216, s[42:43]\n"
      "s_nop 1\n"
      "v_cndmask_b32 v152, v152, v172, s[44:45]\n"
      "v_mul_f32 v152, v236, v152\n"
      "v_cvt_pk_bf16_f32 v152, v152, v152\n"
      "ds_write_b16 v135, v152\n"
      "s_branch L_join_10_%=\n"
      "L_flg_64_%=:\n"
      "v_cmp_gt_i32 vcc, 0, v196\n"
      "s_cbranch_vccz L_fb_65_%=\n"
      "s_sleep 1\n"
      "ds_read_b128 v[196:199], v128 offset:3072\n"
      "s_waitcnt lgkmcnt(0)\n"
      "v_bitop3_b32 v196, v196, v197, s4 bitop3:0x7e\n"
      "v_bitop3_b32 v198, v198, v199, s4 bitop3:0x7e\n"
      "v_or_b32 v196, v196, v198\n"
      "v_cmp_ne_u32 s[48:49], 0, v196\n"
      "s_branch L_flg_64_%=\n"
      "L_edge_67_%=:\n"
      "s_mov_b32 s11, s10\n"
      "s_sub_u32 s28, s60, s8\n"
      "s_sub_u32 s29, s61, s9\n"
      "s_mov_b32 s60, s8\n"
      "s_mov_b32 s61, s9\n"
      "s_mov_b64 exec, s[64:65]\n"
      "v_add_u32 v116, s28, v116\n"
      "s_mov_b64 exec, s[70:71]\n"
      "v_add_u32 v116, s29, v116\n"
      "s_mov_b64 exec, s[66:67]\n"
      "v_add_u32 v117, s28, v117\n"
      "s_mov_b64 exec, s[72:73]\n"
      "v_add_u32 v117, s29, v117\n"
      "s_mov_b64 exec, s[68:69]\n"
      "v_add_u32 v118, s28, v118\n"
      "s_mov_b64 exec, s[74:75]\n"
      "v_add_u32 v118, s29, v118\n"
      "s_mov_b64 exec, -1\n"
      "s_nop 4\n"
      "s_branch L_eb_68_%=\n"
      "L_gen_9_%=:\n"
      "s_nop 0\n"
      "s_nop 0\n"
      "v_mfma_f32_16x16x32_bf16 v[148:151], v[180:183], v[168:171], v[90:93]\n"
      "v_mbcnt_lo_u32_b32 v196, -1, 0\n"
      "v_mbcnt_hi_u32_b32 v196, -1, v196\n"
      "v_mfma_f32_16x16x32_bf16 v[148:151], v[34:37], v[38:41], v[148:151]\n"
      "v_lshrrev_b32 v198, 4, v196\n"
      "v_and_b32 v197, 15, v196\n"
      "v_mfma_f32_16x16x32_bf16 v[228:231], v[164:167], v[168:171], v[94:97]\n"
      "v_mul_u32_u24 v199, 0x62, v197\n"
      "v_lshl_add_u32 v199, v198, 3, v199\n"
      "v_mfma_f32_16x16x32_bf16 v[228:231], v[40:43], v[38:41], v[228:231]\n"
      "s_movk_i32 s31, 0x3fb9\n"
      "v_lshlrev_b32 v198, 2, v198\n"
      "v_sub_u32 v197, v197, v198\n"
      "v_and_b32 v198, 1, v197\n"
      "v_lshlrev_b32 v198, 4, v198\n"
      "v_lshrrev_b32 v197, 1, v197\n"
      "v_add_u32 v199, s30, v199\n"
      "v_lshlrev_b32 v198, v198, s31\n"
      "v_and_b32_e64 v196, -4, v199\n"
      "v_cmp_eq_u32 vcc, 0, v197\n"
      "v_mfma_f32_16x16x32_bf16 v[136:139], v[248:251], v[168:171], v[72:75]\n"
      "ds_read2_b32 v[140:141], v196 offset0:0 offset1:1\n"
      "ds_read2_b32 v[154:155], v196 offset0:2 offset1:10\n"
      "ds_read2_b32 v[152:153], v196 offset0:16 offset1:17\n"
      "ds_read2_b32 v[142:143], v196 offset0:8 offset1:9\n"
      "v_and_b32_e64 v199, 2, v199\n"
      "v_mfma_f32_16x16x32_bf16 v[136:139], v[236:239], v[164:167], v[136:139]\n"
      "ds_read2_b32 v[156:157], v196 offset0:18 offset1:24\n"
      "v_add_u32 v196, 1568, v196\n"
      "v_mfma_f32_16x16x32_bf16 v[136:139], v[240:243], v[38:41], v[136:139]\n"
      "ds_read2_b32 v[176:177], v196 offset0:2 offset1:10\n"
      "ds_read2_b32 v[158:159], v196 offset0:0 offset1:1\n"
      "ds_read2_b32 v[160:161], v196 offset0:8 offset1:9\n"
      "ds_read2_b32 v[178:179], v196 offset0:18 offset1:24\n"
      "v_cndmask_b32 v202, 0, v198, vcc\n"
      "ds_read2_b32 v[162:163], v196 offset0:16 offset1:17\n"
      "v_cmp_eq_u32 vcc, 1, v197\n"
      "v_add_u32 v196, 1568, v196\n"
      "ds_read2_b32 v[188:189], v196 offset0:2 offset1:10\n"
      "ds_read2_b32 v[138:139], v196 offset0:0 offset1:1\n"
      "ds_read2_b32 v[184:185], v196 offset0:8 offset1:9\n"
      "ds_read2_b32 v[186:187], v196 offset0:16 offset1:17\n"
      "ds_read2_b32 v[190:191], v196 offset0:18 offset1:24\n"
      "v_cndmask_b32 v203, 0, v198, vcc\n"
      "s_waitcnt lgkmcnt(11)\n"
      "v_alignbyte_b32 v152, v153, v152, v199\n"
      "v_alignbyte_b32 v142, v143, v142, v199\n"
      "v_alignbyte_b32 v143, v155, v143, v199\n"
      "v_alignbyte_b32 v140, v141, v140, v199\n"
      "v_mfma_f32_16x16x32_bf16 v[208:211], v[212:215], v[168:171], v[98:101]\n"
      "s_waitcnt lgkmcnt(10)\n"
      "v_alignbyte_b32 v157, v157, v157, v199\n"
      "v_alignbyte_b32 v153, v156, v153, v199\n"
      "v_mfma_f32_16x16x32_bf16 v[208:211], v[46:49], v[38:41], v[208:211]\n"
      "s_waitcnt lgkmcnt(8)\n"
      "v_alignbyte_b32 v141, v154, v141, v199\n"
      "v_lshlrev_b32_e64 v157, 16, v157\n"
      "v_fmac_f32_e64 v200, s37, v157\n"
      "v_mfma_f32_16x16x16_bf16 v[204:207], v[202:203], v[140:141], v[204:207]\n"
      "ds_read_b128 v[168:171], v128 offset:3072\n"
      "v_alignbyte_b32 v158, v159, v158, v199\n"
      "v_mfma_f32_16x16x16_bf16 v[232:235], v[202:203], v[142:143], v[232:235]\n"
      "v_alignbyte_b32 v159, v176, v159, v199\n"
      "s_waitcnt lgkmcnt(6)\n"
      "v_mfma_f32_16x16x16_bf16 v[192:195], v[202:203], v[152:153], v[192:195]\n"
      "s_nop 0\n"
      "v_alignbyte_b32 v160, v161, v160, v199\n"
      "v_alignbyte_b32 v161, v177, v161, v199\n"
      "v_mfma_f32_16x16x32_bf16 v[140:143], v[180:183], v[252:255], v[102:105]\n"
      "v_max3_f32 v198, v204, v205, v206\n"
      "v_alignbyte_b32 v162, v163, v162, v199\n"
      "v_mfma_f32_16x16x32_bf16 v[140:143], v[34:37], v[44:47], v[140:143]\n"
      "s_waitcnt lgkmcnt(1)\n"
      "v_alignbyte_b32 v163, v178, v163, v199\n"
      "v_alignbyte_b32 v179, v179, v179, v199\n"
      "v_mfma_f32_16x16x32_bf16 v[180:183], v[164:167], v[252:255], v[106:109]\n"
      "v_lshlrev_b32 v179, 16, v179\n"
      "v_alignbyte_b32 v138, v139, v138, v199\n"
      "v_mfma_f32_16x16x32_bf16 v[180:183], v[40:43], v[44:47], v[180:183]\n"
      "v_max3_f32 v197, v207, v232, v233\n"
      "v_alignbyte_b32 v139, v188, v139, v199\n"
      "v_mfma_f32_16x16x32_bf16 v[164:167], v[248:251], v[252:255], v[74:77]\n"
      "v_alignbyte_b32 v184, v185, v184, v199\n"
      "v_alignbyte_b32 v185, v189, v185, v199\n"
      "v_mfma_f32_16x16x32_bf16 v[164:167], v[236:239], v[212:215], v[164:167]\n"
      "v_max3_f32 v198, v198, v234, v235\n"
      "v_alignbyte_b32 v186, v187, v186, v199\n"
      "v_mfma_f32_16x16x32_bf16 v[164:167], v[240:243], v[44:47], v[164:167]\n"
      "v_alignbyte_b32 v191, v191, v191, v199\n"
      "v_alignbyte_b32 v187, v190, v187, v199\n"
      "v_mfma_f32_16x16x32_bf16 v[152:155], v[212:215], v[252:255], v[110:113]\n"
      "s_waitcnt lgkmcnt(0)\n"
      "v_max3_f32 v197, v197, v192, v193\n"
      "v_lshlrev_b32 v191, 16, v191\n"
      "v_mfma_f32_16x16x32_bf16 v[152:155], v[46:49], v[44:47], v[152:155]\n"
      "v_max3_f32 v198, v198, v194, v195\n"
      "v_max3_f32 v198, v198, v197, v200\n"
      "v_mfma_f32_16x16x32_bf16 v[212:215], v[236:239], v[248:251], v[76:79]\n"
      "v_fmac_f32 v136, s37, v179\n"
      "v_mov_b32 v197, v198\n"
      "v_mfma_f32_16x16x16_bf16 v[148:151], v[202:203], v[158:159], v[148:151]\n"
      "s_nop 0\n"
      "v_permlane16_swap_b32 v198, v197\n"
      "v_max_f32 v198, v198, v197\n"
      "v_mov_b32 v197, v198\n"
      "v_mfma_f32_16x16x16_bf16 v[212:215], v[242:243], v[240:241], v[212:215]\n"
      "s_nop 0\n"
      "v_permlane32_swap_b32 v198, v197\n"
      "v_max_f32 v198, v198, v197\n"
      "v_sub_f32 v204, v204, v198\n"
      "v_bitop3_b32 v170, v170, v171, s4 bitop3:0x7e\n"
      "v_sub_f32 v206, v206, v198\n"
      "v_sub_f32 v192, v192, v198\n"
      "v_sub_f32 v193, v193, v198\n"
      "v_sub_f32 v207, v207, v198\n"
      "v_sub_f32 v235, v235, v198\n"
      "v_sub_f32 v233, v233, v198\n"
      "v_sub_f32 v195, v195, v198\n"
      "v_exp_f32 v192, v192\n"
      "v_sub_f32 v205, v205, v198\n"
      "v_sub_f32 v234, v234, v198\n"
      "v_exp_f32 v205, v205\n"
      "v_sub_f32 v200, v200, v198\n"
      "v_exp_f32 v206, v206\n"
      "v_exp_f32 v207, v207\n"
      "v_mfma_f32_16x16x16_bf16 v[140:143], v[202:203], v[138:139], v[140:143]\n"
      "v_sub_f32 v232, v232, v198\n"
      "v_exp_f32 v235, v235\n"
      "v_exp_f32 v200, v200\n"
      "v_exp_f32 v234, v234\n"
      "v_exp_f32 v204, v204\n"
      "v_sub_f32 v194, v194, v198\n"
      "v_exp_f32 v195, v195\n"
      "v_exp_f32 v233, v233\n"
      "v_mfma_f32_16x16x16_bf16 v[180:183], v[202:203], v[184:185], v[180:183]\n"
      "v_exp_f32_e64 v193, v193\n"
      "v_cvt_pk_bf16_f32 v204, v204, v205\n"
      "v_mfma_f32_16x16x16_bf16 v[208:211], v[202:203], v[162:163], v[208:211]\n"
      "v_bitop3_b32 v168, v168, v169, s4 bitop3:0x7e\n"
      "v_exp_f32 v232, v232\n"
      "v_exp_f32 v194, v194\n"
      "v_mfma_f32_16x16x16_bf16 v[228:231], v[202:203], v[160:161], v[228:231]\n"
      "v_cvt_pk_bf16_f32 v205, v206, v207\n"
      "v_cvt_pk_bf16_f32 v207, v234, v235\n"
      "v_cvt_pk_bf16_f32 v206, v232, v233\n"
      "v_max3_f32 v198, v148, v149, v150\n"
      "v_mfma_f32_16x16x16_bf16 v[152:155], v[202:203], v[186:187], v[152:155]\n"
      "v_cvt_pk_bf16_f32 v64, v200, 0\n"
      "v_cvt_pk_bf16_f32 v63, v194, v195\n"
      "v_cvt_pk_bf16_f32 v62, v192, v193\n"
      "v_max3_f32 v243, v140, v141, v142\n"
      "v_mfma_f32_16x16x32_bf16 v[192:195], v[66:69], v[204:207], 0\n"
      "v_or_b32_e64 v168, v168, v170\n"
      "v_cmp_ne_u32 vcc, 0, v168\n"
      "v_mfma_f32_16x16x32_bf16 v[192:195], v[66:69], v[62:65], v[192:195]\n"
      "s_mov_b64 s[48:49], vcc\n"
      "v_max3_f32 v197, v151, v228, v229\n"
      "v_max3_f32 v197, v197, v208, v209\n"
      "v_max3_f32 v198, v198, v230, v231\n"
      "v_mfma_f32_16x16x32_bf16 v[248:251], v[216:219], v[204:207], 0\n"
      "s_cbranch_vccnz L_flg_85_%=\n"
      "L_fb_86_%=:\n"
      "v_mfma_f32_16x16x32_bf16 v[168:171], v[172:175], v[204:207], 0\n"
      "s_nop 0\n"
      "v_max3_f32 v198, v198, v210, v211\n"
      "v_max3_f32 v198, v198, v197, v136\n"
      "v_mfma_f32_16x16x32_bf16 v[168:171], v[54:57], v[62:65], v[168:171]\n"
      "v_mov_b32 v197, v198\n"
      "v_fmac_f32 v164, s37, v191\n"
      "v_mfma_f32_16x16x32_bf16 v[238:241], v[144:147], v[204:207], 0\n"
      "v_permlane16_swap_b32_e64 v198, v197\n"
      "v_mfma_f32_16x16x32_bf16 v[238:241], v[58:61], v[62:65], v[238:241]\n"
      "v_max_f32 v198, v198, v197\n"
      "v_mov_b32 v242, v198\n"
      "v_mfma_f32_16x16x32_bf16 v[248:251], v[50:53], v[62:65], v[248:251]\n"
      "s_nop 0\n"
      "v_permlane32_swap_b32 v198, v242\n"
      "v_max_f32 v198, v198, v242\n"
      "v_sub_f32 v148, v148, v198\n"
      "v_sub_f32 v230, v230, v198\n"
      "v_sub_f32 v149, v149, v198\n"
      "v_sub_f32 v208, v208, v198\n"
      "v_sub_f32 v210, v210, v198\n"
      "v_sub_f32 v136, v136, v198\n"
      "v_exp_f32 v136, v136\n"
      "v_sub_f32 v228, v228, v198\n"
      "v_sub_f32 v211, v211, v198\n"
      "v_rcp_f32 v192, v192\n"
      "v_exp_f32 v148, v148\n"
      "v_sub_f32 v150, v150, v198\n"
      "v_exp_f32 v230, v230\n"
      "v_sub_f32 v229, v229, v198\n"
      "v_exp_f32 v150, v150\n"
      "v_exp_f32 v229, v229\n"
      "v_exp_f32 v149, v149\n"
      "v_sub_f32 v209, v209, v198\n"
      "v_sub_f32 v151, v151, v198\n"
      "v_exp_f32 v208, v208\n"
      "v_exp_f32 v151, v151\n"
      "v_cvt_pk_bf16_f32 v148, v148, v149\n"
      "v_cvt_pk_bf16_f32 v149, v150, v151\n"
      "v_exp_f32 v210, v210\n"
      "v_mul_f32 v249, v192, v249\n"
      "v_exp_f32 v211, v211\n"
      "v_exp_f32 v228, v228\n"
      "v_exp_f32 v209, v209\n"
      "v_sub_f32 v231, v231, v198\n"
      "v_exp_f32_e64 v231, v231\n"
      "v_cvt_pk_bf16_f32 v64, v136, 0\n"
      "v_cvt_pk_bf16_f32 v151, v230, v231\n"
      "v_cvt_pk_bf16_f32 v63, v210, v211\n"
      "v_cvt_pk_bf16_f32 v150, v228, v229\n"
      "v_cvt_pk_bf16_f32 v62, v208, v209\n"
      "v_mul_f32 v248, v192, v248\n"
      "v_mfma_f32_16x16x32_bf16 v[202:205], v[66:69], v[148:151], 0\n"
      "s_add_u32 s28, s27, 2\n"
      "v_mul_f32 v250, v192, v250\n"
      "v_cvt_pk_bf16_f32 v248, v248, v249\n"
      "v_mfma_f32_16x16x32_bf16 v[202:205], v[66:69], v[62:65], v[202:205]\n"
      "s_min_u32 m0, s28, s3\n"
      "v_mul_f32_e64 v251, v192, v251\n"
      "v_cvt_pk_bf16_f32 v249, v250, v251\n"
      "v_mfma_f32_16x16x32_bf16 v[228:231], v[216:219], v[148:151], 0\n"
      "s_movrels_b32 s9, s80\n"
      "v_mul_f32 v168, v192, v168\n"
      "v_mul_f32_e64 v169, v192, v169\n"
      "v_mfma_f32_16x16x32_bf16 v[228:231], v[50:53], v[62:65], v[228:231]\n"
      "s_lshr_b32 s8, s9, 8\n"
      "v_cvt_pk_bf16_f32 v250, v168, v169\n"
      "v_mul_f32 v170, v192, v170\n"
      "v_mul_f32_e64 v240, v192, v240\n"
      "v_mfma_f32_16x16x32_bf16 v[208:211], v[172:175], v[148:151], 0\n"
      "s_and_b32 s9, s9, 0xff\n"
      "v_mul_f32_e64 v171, v192, v171\n"
      "v_cvt_pk_bf16_f32 v251, v170, v171\n"
      "v_mfma_f32_16x16x32_bf16 v[208:211], v[54:57], v[62:65], v[208:211]\n"
      "ds_write_b128 v124, v[248:251]\n"
      "v_mul_f32_e64 v241, v192, v241\n"
      "v_mfma_f32_16x16x32_bf16 v[232:235], v[144:147], v[148:151], 0\n"
      "s_mul_i32 s10, s8, 73\n"
      "v_mul_f32 v239, v192, v239\n"
      "v_mul_f32_e64 v238, v192, v238\n"
      "v_mfma_f32_16x16x32_bf16 v[232:235], v[58:61], v[62:65], v[232:235]\n"
      "s_add_u32 s10, s10, s9\n"
      "v_cvt_pk_bf16_f32 v238, v238, v239\n"
      "v_cvt_pk_bf16_f32 v239, v240, v241\n"
      "v_max3_f32 v240, v143, v180, v181\n"
      "v_max3_f32 v240, v240, v152, v153\n"
      "v_max3_f32 v243, v243, v182, v183\n"
      "v_max3_f32 v243, v243, v154, v155\n"
      "v_max3_f32 v243, v243, v240, v164\n"
      "v_mov_b32_e64 v240, v243\n"
      "v_cndmask_b32 v201, v201, v137, s[42:43]\n"
      "v_cndmask_b32 v201, v201, v165, s[44:45]\n"
      "v_permlane16_swap_b32 v243, v240\n"
      "v_max_f32 v243, v243, v240\n"
      "v_mov_b32_e64 v240, v243\n"
      "v_cndmask_b32 v201, v201, v213, s[46:47]\n"
      "v_mbcnt_lo_u32_b32 v196, -1, 0\n"
      "v_permlane32_swap_b32 v243, v240\n"
      "v_max_f32 v243, v243, v240\n"
      "v_sub_f32 v142, v142, v243\n"
      "v_sub_f32 v141, v141, v243\n"
      "v_sub_f32 v164, v164, v243\n"
      "v_sub_f32 v180, v180, v243\n"
      "v_sub_f32 v143, v143, v243\n"
      "v_exp_f32 v143, v143\n"
      "v_sub_f32 v183, v183, v243\n"
      "v_sub_f32 v153, v153, v243\n"
      "v_sub_f32 v140, v140, v243\n"
      "v_exp_f32 v140, v140\n"
      "v_sub_f32 v152, v152, v243\n"
      "v_sub_f32 v182, v182, v243\n"
      "v_exp_f32 v183, v183\n"
      "v_exp_f32 v142, v142\n"
      "v_sub_f32 v154, v154, v243\n"
      "v_sub_f32 v181, v181, v243\n"
      "v_mbcnt_hi_u32_b32 v196, -1, v196\n"
      "v_sub_f32 v155, v155, v243\n"
      "v_exp_f32 v141, v141\n"
      "v_exp_f32 v155, v155\n"
      "v_cvt_pk_bf16_f32 v140, v140, v141\n"
      "v_cvt_pk_bf16_f32 v141, v142, v143\n"
      "v_exp_f32 v152, v152\n"
      "v_exp_f32 v180, v180\n"
      "v_exp_f32 v164, v164\n"
      "v_min_u32 v196, 48, v196\n"
      "v_exp_f32 v181, v181\n"
      "v_exp_f32 v154, v154\n"
      "v_exp_f32 v153, v153\n"
      "v_exp_f32_e64 v182, v182\n"
      "v_cvt_pk_bf16_f32 v142, v180, v181\n"
      "v_cvt_pk_bf16_f32 v143, v182, v183\n"
      "v_cvt_pk_bf16_f32 v64, v164, 0\n"
      "v_cvt_pk_bf16_f32 v62, v152, v153\n"
      "v_cvt_pk_bf16_f32 v63, v154, v155\n"
      "v_mfma_f32_16x16x32_bf16 v[148:151], v[66:69], v[140:143], 0\n"
      "ds_read_b128 v[168:171], v114 offset:18432\n"
      "v_lshlrev_b32_e64 v196, 1, v196\n"
      "v_mfma_f32_16x16x32_bf16 v[148:151], v[66:69], v[62:65], v[148:151]\n"
      "ds_read_b128 v[192:195], v115 offset:18432\n"
      "v_add_u32 v196, s30, v196\n"
      "v_mov_b32 v200, 0\n"
      "v_mfma_f32_16x16x32_bf16 v[248:251], v[216:219], v[140:143], 0\n"
      "ds_read_u16 v203, v196 offset:4704\n"
      "v_mfma_f32_16x16x32_bf16 v[248:251], v[50:53], v[62:65], v[248:251]\n"
      "s_mul_i32 s28, s10, 4802\n"
      "v_rcp_f32_e64 v202, v202\n"
      "v_mfma_f32_16x16x32_bf16 v[240:243], v[172:175], v[140:143], 0\n"
      "s_add_u32 s56, s18, s28\n"
      "v_mul_f32 v228, v202, v228\n"
      "v_mul_f32 v229, v202, v229\n"
      "v_mfma_f32_16x16x32_bf16 v[240:243], v[54:57], v[62:65], v[240:243]\n"
      "s_addc_u32 s57, s19, 0\n"
      "v_mul_f32 v209, v202, v209\n"
      "v_mul_f32 v210, v202, v210\n"
      "v_mul_f32 v233, v202, v233\n"
      "v_mul_f32 v230, v202, v230\n"
      "v_cvt_pk_bf16_f32 v228, v228, v229\n"
      "v_mfma_f32_16x16x32_bf16 v[204:207], v[144:147], v[140:143], 0\n"
      "s_and_b32 s56, s56, -16\n"
      "v_mul_f32 v231, v202, v231\n"
      "v_cvt_pk_bf16_f32 v229, v230, v231\n"
      "v_mfma_f32_16x16x32_bf16 v[204:207], v[58:61], v[62:65], v[204:207]\n"
      "s_sub_u32 s58, s56, 110592\n"
      "v_mul_f32 v208, v202, v208\n"
      "v_cvt_pk_bf16_f32 v230, v208, v209\n"
      "v_mul_f32 v211, v202, v211\n"
      "s_waitcnt lgkmcnt(1)\n"
      "v_mfma_f32_16x16x32_bf16 v[196:199], a[0:3], v[168:171], 0\n"
      "s_subb_u32 s59, s57, 0\n"
      "v_mul_f32_e64 v234, v202, v234\n"
      "v_cvt_pk_bf16_f32 v231, v210, v211\n"
      "v_mfma_f32_16x16x32_bf16 v[196:199], a[36:39], v[192:195], v[196:199]\n"
      "ds_write_b128 v124, v[228:231] offset:1024\n"
      "v_mfma_f32_16x16x32_bf16 v[228:231], a[4:7], v[168:171], 0\n"
      "s_waitcnt lgkmcnt(1)\n"
      "v_lshlrev_b32 v203, 16, v203\n"
      "v_fmac_f32_e64 v201, s37, v203\n"
      "v_mfma_f32_16x16x32_bf16 v[228:231], a[40:43], v[192:195], v[228:231]\n"
      "s_sub_u32 s56, s56, 73728\n"
      "v_max_f32_dpp v203, v201, v201 row_ror:8 row_mask:0xf bank_mask:0xf\n"
      "v_mul_f32 v235, v202, v235\n"
      "s_subb_u32 s57, s57, 0\n"
      "v_max_f32_dpp v203, v203, v203 row_ror:4 row_mask:0xf bank_mask:0xf\n"
      "s_mul_i32 s28, s8, 1373568\n"
      "v_mul_f32 v232, v202, v232\n"
      "v_max_f32_dpp v203, v203, v203 row_ror:2 row_mask:0xf bank_mask:0xf\n"
      "s_nop 1\n"
      "v_max_f32_dpp v203, v203, v203 row_ror:1 row_mask:0xf bank_mask:0xf\n"
      "s_mul_i32 s29, s9, 2688\n"
      "v_cvt_pk_bf16_f32 v232, v232, v233\n"
      "v_max_f32_dpp v203, v203, v203 row_bcast:15 row_mask:0xa bank_mask:0xf\n"
      "s_add_u32 s28, s28, s29\n"
      "v_cvt_pk_bf16_f32 v233, v234, v235\n"
      "v_max_f32_dpp v203, v203, v203 row_bcast:31 row_mask:0xc bank_mask:0xf\n"
      "s_nop 0\n"
      "v_readlane_b32 s31, v203, 63\n"
      "s_cmp_eq_u32 s8, 72\n"
      "s_nop 0\n"
      "v_subrev_f32 v201, s31, v201\n"
      "v_exp_f32_e64 v201, v201\n"
      "s_cselect_b32 s8, 0, 100270464\n"
      "v_cvt_pk_bf16_f32 v201, v201, v201\n"
      "ds_write_b16 v130, v201\n"
      "ds_read_b128 v[140:143], v131 offset:64\n"
      "ds_read_b128 v[208:211], v131\n"
      "s_cmp_eq_u32 s9, 72\n"
      "ds_write2st64_b64 v125, v[238:239], v[232:233] offset0:0 offset1:2\n"
      "s_cselect_b32 s9, 0, 196224\n"
      "ds_write_b32 v129, v200\n"
      "s_add_u32 s10, s8, s9\n"
      "s_add_u32 s28, s28, s10\n"
      "s_add_u32 s52, s16, s28\n"
      "s_addc_u32 s53, s17, 0\n"
      "s_cmp_lg_u32 s10, s11\n"
      "s_cbranch_scc1 L_edge_88_%=\n"
      "L_eb_89_%=:\n"
      "s_waitcnt lgkmcnt(2)\n"
      "v_mfma_f32_16x16x32_bf16 v[200:203], v[208:211], v[66:69], 0\n"
      "s_nop 0\n"
      "v_mfma_f32_16x16x32_bf16 v[200:203], v[140:143], v[66:69], v[200:203]\n"
      "v_mfma_f32_16x16x32_bf16 v[232:235], v[208:211], v[216:219], 0\n"
      "v_mfma_f32_16x16x32_bf16 v[232:235], v[140:143], v[50:53], v[232:235]\n"
      "v_mfma_f32_16x16x32_bf16 v[216:219], v[208:211], v[172:175], 0\n"
      "v_mfma_f32_16x16x32_bf16 v[216:219], v[140:143], v[54:57], v[216:219]\n"
      "v_mfma_f32_16x16x32_bf16 v[172:175], v[208:211], v[144:147], 0\n"
      "s_nop 0\n"
      "v_mfma_f32_16x16x32_bf16 v[172:175], v[140:143], v[58:61], v[172:175]\n"
      "s_nop 1\n"
      "v_mov_b32_dpp v200, v200 quad_perm:[0,0,0,0] row_mask:0xf bank_mask:0xf\n"
      "s_nop 2\n"
      "v_cndmask_b32 v232, v232, v216, s[42:43]\n"
      "v_rcp_f32 v200, v200\n"
      "v_cndmask_b32 v232, v232, v172, s[44:45]\n"
      "v_mul_f32 v232, v200, v232\n"
      "v_cvt_pk_bf16_f32 v232, v232, v232\n"
      "ds_write_b16 v135, v232\n"
      "s_branch L_join_10_%=\n"
      "L_flg_85_%=:\n"
      "v_cmp_gt_i32 vcc, 0, v168\n"
      "s_cbranch_vccz L_fb_86_%=\n"
      "s_sleep 1\n"
      "ds_read_b128 v[168:171], v128 offset:3072\n"
      "s_waitcnt lgkmcnt(0)\n"
      "v_bitop3_b32 v168, v168, v169, s4 bitop3:0x7e\n"
      "v_bitop3_b32 v170, v170, v171, s4 bitop3:0x7e\n"
      "v_or_b32 v168, v168, v170\n"
      "v_cmp_ne_u32 s[48:49], 0, v168\n"
      "s_branch L_flg_85_%=\n"
      "L_edge_88_%=:\n"
      "s_mov_b32 s11, s10\n"
      "s_sub_u32 s28, s60, s8\n"
      "s_sub_u32 s29, s61, s9\n"
      "s_mov_b32 s60, s8\n"
      "s_mov_b32 s61, s9\n"
      "s_mov_b64 exec, s[64:65]\n"
      "v_add_u32 v116, s28, v116\n"
      "s_mov_b64 exec, s[70:71]\n"
      "v_add_u32 v116, s29, v116\n"
      "s_mov_b64 exec, s[66:67]\n"
      "v_add_u32 v117, s28, v117\n"
      "s_mov_b64 exec, s[72:73]\n"
      "v_add_u32 v117, s29, v117\n"
      "s_mov_b64 exec, s[68:69]\n"
      "v_add_u32 v118, s28, v118\n"
      "s_mov_b64 exec, s[74:75]\n"
      "v_add_u32 v118, s29, v118\n"
      "s_mov_b64 exec, -1\n"
      "s_nop 4\n"
      "s_branch L_eb_89_%=\n"
      "L_gen_19_%=:\n"
      "s_waitcnt lgkmcnt(0)\n"
      "s_nop 5\n"
      "s_cmp_eq_u64 s[48:49], 0\n"
      "s_cbranch_scc1 L_go_107_%=\n"
      "s_mov_b32 m0, s27\n"
      "s_nop 0\n"
      "s_movrels_b32 s9, s80\n"
      "s_lshr_b32 s8, s9, 8\n"
      "s_and_b32 s9, s9, 0xff\n"
      "s_mul_i32 s10, s8, 73\n"
      "s_add_u32 s10, s10, s9\n"
      "s_mul_i32 s30, s10, 4802\n"
      "s_add_u32 s30, s30, s18\n"
      "s_and_b32 s29, s30, 15\n"
      "s_add_u32 s30, s29, 115456\n"
      "s_lshl_b64 s[34:35], s[48:49], 4\n"
      "s_lshr_b64 s[34:35], s[34:35], 5\n"
      "s_cbranch_scc1 L_gen_20_%=\n"
      "v_mbcnt_lo_u32_b32 v148, -1, 0\n"
      "v_mbcnt_hi_u32_b32 v148, -1, v148\n"
      "v_lshlrev_b32 v149, 1, v148\n"
      "v_cmp_gt_u32 vcc, s29, v149\n"
      "v_add_u32 v150, 4802, v149\n"
      "s_nop 0\n"
      "v_cndmask_b32 v149, v150, v149, vcc\n"
      "v_mov_b32 v150, 0\n"
      "v_add_u32 v149, 115456, v149\n"
      "s_mov_b64 exec, 0x7f\n"
      "ds_write_b16 v149, v150\n"
      "s_mov_b64 exec, -1\n"
      "v_lshlrev_b32 v149, 4, v148\n"
      "v_add_u32 v149, 115456, v149\n"
      "v_min_u32 v148, 116160, v149\n"
      "ds_read_b128 v[212:215], v149\n"
      "ds_read_b128 v[204:207], v149 offset:2048\n"
      "ds_read_b128 v[172:175], v149 offset:1024\n"
      "ds_read_b128 v[144:147], v148 offset:4096\n"
      "ds_read_b128 v[160:163], v149 offset:3072\n"
      "s_waitcnt lgkmcnt(4)\n"
      "v_or3_b32 v212, v212, v213, v214\n"
      "s_waitcnt lgkmcnt(2)\n"
      "v_or3_b32 v212, v212, v215, v172\n"
      "v_or3_b32 v212, v212, v173, v174\n"
      "v_or3_b32 v212, v212, v175, v204\n"
      "s_waitcnt lgkmcnt(0)\n"
      "v_or3_b32 v212, v212, v205, v206\n"
      "v_or3_b32 v212, v212, v207, v160\n"
      "v_or3_b32 v212, v212, v161, v162\n"
      "v_or3_b32 v212, v212, v163, v144\n"
      "v_or3_b32 v212, v212, v145, v146\n"
      "v_bitop3_b32 v212, v212, v147, s40 bitop3:0xa8\n"
      "v_cmp_ne_u32 s[48:49], 0, v212\n"
      "s_cmp_lg_u64 s[48:49], 0\n"
      "s_cbranch_scc1 L_gen_20_%=\n"
      "s_cmp_eq_u64 s[50:51], 0\n"
      "s_cbranch_scc1 L_fast_22_%=\n"
      "L_go_107_%=:\n"
      "v_mfma_f32_16x16x32_bf16 v[148:151], v[252:255], v[200:203], v[90:93]\n"
      "ds_read_b128 v[212:215], v128 offset:2048\n"
      "v_max3_f32 v175, v184, v185, v186\n"
      "v_mfma_f32_16x16x32_bf16 v[148:151], v[34:37], v[38:41], v[148:151]\n"
      "v_max3_f32 v175, v175, v246, v247\n"
      "v_max3_f32 v173, v187, v244, v245\n"
      "v_mfma_f32_16x16x32_bf16 v[204:207], v[248:251], v[200:203], v[94:97]\n"
      "v_max3_f32 v173, v173, v176, v177\n"
      "v_max3_f32 v175, v175, v178, v179\n"
      "v_mfma_f32_16x16x32_bf16 v[204:207], v[40:43], v[38:41], v[204:207]\n"
      "v_max3_f32 v175, v175, v173, v228\n"
      "v_mov_b32 v173, v175\n"
      "v_mfma_f32_16x16x32_bf16 v[160:163], v[240:243], v[200:203], v[72:75]\n"
      "s_nop 0\n"
      "v_permlane16_swap_b32_e64 v175, v173\n"
      "v_mfma_f32_16x16x32_bf16 v[160:163], v[216:219], v[248:251], v[160:163]\n"
      "v_max_f32 v175, v175, v173\n"
      "v_mov_b32 v173, v175\n"
      "v_mfma_f32_16x16x32_bf16 v[160:163], v[192:195], v[38:41], v[160:163]\n"
      "s_nop 0\n"
      "v_permlane32_swap_b32 v175, v173\n"
      "v_mfma_f32_16x16x32_bf16 v[144:147], v[188:191], v[200:203], v[98:101]\n"
      "v_max_f32 v175, v175, v173\n"
      "v_sub_f32 v184, v184, v175\n"
      "v_mfma_f32_16x16x32_bf16 v[144:147], v[46:49], v[38:41], v[144:147]\n"
      "v_sub_f32 v185, v185, v175\n"
      "v_sub_f32 v228, v228, v175\n"
      "v_mfma_f32_16x16x32_bf16 v[200:203], v[252:255], v[168:171], v[102:105]\n"
      "s_waitcnt lgkmcnt(0)\n"
      "v_sub_f32 v187, v187, v175\n"
      "v_sub_f32 v244, v244, v175\n"
      "v_mfma_f32_16x16x32_bf16 v[200:203], v[34:37], v[44:47], v[200:203]\n"
      "v_exp_f32 v187, v187\n"
      "v_exp_f32 v184, v184\n"
      "v_sub_f32 v186, v186, v175\n"
      "v_sub_f32 v245, v245, v175\n"
      "v_bitop3_b32 v214, v214, v215, s4 bitop3:0x7e\n"
      "v_bitop3_b32 v212, v212, v213, s4 bitop3:0x7e\n"
      "v_sub_f32 v179, v179, v175\n"
      "v_exp_f32 v185, v185\n"
      "v_sub_f32 v247, v247, v175\n"
      "v_sub_f32 v176, v176, v175\n"
      "v_sub_f32 v177, v177, v175\n"
      "v_exp_f32 v244, v244\n"
      "v_exp_f32 v186, v186\n"
      "v_exp_f32 v179, v179\n"
      "v_sub_f32 v246, v246, v175\n"
      "v_cvt_pk_bf16_f32 v184, v184, v185\n"
      "v_cvt_pk_bf16_f32 v185, v186, v187\n"
      "v_exp_f32 v245, v245\n"
      "v_sub_f32 v178, v178, v175\n"
      "v_exp_f32 v178, v178\n"
      "v_exp_f32 v247, v247\n"
      "v_exp_f32 v177, v177\n"
      "v_exp_f32 v228, v228\n"
      "v_exp_f32 v246, v246\n"
      "v_cvt_pk_bf16_f32 v186, v244, v245\n"
      "v_cvt_pk_bf16_f32 v63, v178, v179\n"
      "v_exp_f32 v176, v176\n"
      "v_cvt_pk_bf16_f32 v187, v246, v247\n"
      "v_cvt_pk_bf16_f32 v62, v176, v177\n"
      "v_cvt_pk_bf16_f32 v64, v228, 0\n"
      "v_mfma_f32_16x16x32_bf16 v[244:247], v[66:69], v[184:187], 0\n"
      "ds_read_b128 v[176:179], v119 offset:12288\n"
      "v_or_b32_e64 v212, v212, v214\n"
      "v_mfma_f32_16x16x32_bf16 v[244:247], v[66:69], v[62:65], v[244:247]\n"
      "v_max3_f32 v162, v148, v149, v150\n"
      "v_cmp_ne_u32 vcc, 0, v212\n"
      "v_mfma_f32_16x16x32_bf16 v[172:175], v[140:143], v[184:187], 0\n"
      "s_mov_b64 s[48:49], vcc\n"
      "v_max3_f32 v163, v151, v204, v205\n"
      "v_max3_f32 v162, v162, v206, v207\n"
      "v_mfma_f32_16x16x32_bf16 v[172:175], v[50:53], v[62:65], v[172:175]\n"
      "s_cbranch_vccnz L_flg_108_%=\n"
      "L_fb_109_%=:\n"
      "v_max3_f32 v163, v163, v144, v145\n"
      "v_mfma_f32_16x16x32_bf16 v[212:215], v[224:227], v[184:187], 0\n"
      "s_nop 0\n"
      "v_max3_f32 v162, v162, v146, v147\n"
      "v_max3_f32 v162, v162, v163, v160\n"
      "v_mfma_f32_16x16x32_bf16 v[212:215], v[54:57], v[62:65], v[212:215]\n"
      "v_rcp_f32 v244, v244\n"
      "v_mov_b32 v163, v162\n"
      "v_mfma_f32_16x16x32_bf16 v[252:255], v[136:139], v[184:187], 0\n"
      "s_nop 0\n"
      "v_permlane16_swap_b32 v162, v163\n"
      "v_mfma_f32_16x16x32_bf16 v[252:255], v[58:61], v[62:65], v[252:255]\n"
      "v_max_f32 v162, v162, v163\n"
      "v_mov_b32 v163, v162\n"
      "v_mul_f32 v173, v244, v173\n"
      "s_waitcnt lgkmcnt(0)\n"
      "v_permlane32_swap_b32 v162, v163\n"
      "v_mul_f32 v172, v244, v172\n"
      "v_max_f32 v162, v162, v163\n"
      "v_sub_f32 v149, v149, v162\n"
      "v_sub_f32 v151, v151, v162\n"
      "v_sub_f32 v148, v148, v162\n"
      "v_sub_f32 v205, v205, v162\n"
      "v_sub_f32 v147, v147, v162\n"
      "v_sub_f32 v145, v145, v162\n"
      "v_sub_f32 v146, v146, v162\n"
      "v_sub_f32 v206, v206, v162\n"
      "v_exp_f32 v148, v148\n"
      "v_sub_f32 v204, v204, v162\n"
      "v_sub_f32 v144, v144, v162\n"
      "v_sub_f32 v160, v160, v162\n"
      "v_exp_f32 v206, v206\n"
      "v_exp_f32 v145, v145\n"
      "v_sub_f32 v207, v207, v162\n"
      "v_exp_f32 v149, v149\n"
      "v_sub_f32 v150, v150, v162\n"
      "v_exp_f32_e64 v151, v151\n"
      "v_mfma_f32_16x16x32_bf16 v[184:187], v[248:251], v[168:171], v[106:109]\n"
      "v_exp_f32_e64 v150, v150\n"
      "v_mfma_f32_16x16x32_bf16 v[184:187], v[40:43], v[44:47], v[184:187]\n"
      "v_cvt_pk_bf16_f32 v148, v148, v149\n"
      "v_cvt_pk_bf16_f32 v149, v150, v151\n"
      "v_mfma_f32_16x16x32_bf16 v[162:165], v[240:243], v[168:171], v[74:77]\n"
      "v_exp_f32 v204, v204\n"
      "v_mfma_f32_16x16x32_bf16 v[162:165], v[216:219], v[188:191], v[162:165]\n"
      "v_exp_f32 v205, v205\n"
      "v_mfma_f32_16x16x32_bf16 v[162:165], v[192:195], v[44:47], v[162:165]\n"
      "global_store_dwordx4 v116, v[176:179], s[54:55] offset:256 sc1\n"
      "v_mfma_f32_16x16x32_bf16 v[248:251], v[188:191], v[168:171], v[110:113]\n"
      "v_exp_f32_e64 v144, v144\n"
      "v_mfma_f32_16x16x32_bf16 v[248:251], v[46:49], v[44:47], v[248:251]\n"
      "v_exp_f32 v146, v146\n"
      "v_exp_f32 v207, v207\n"
      "v_mfma_f32_16x16x32_bf16 v[168:171], v[216:219], v[240:243], v[76:79]\n"
      "v_exp_f32 v147, v147\n"
      "v_exp_f32 v160, v160\n"
      "v_cvt_pk_bf16_f32 v150, v204, v205\n"
      "v_cvt_pk_bf16_f32 v151, v206, v207\n"
      "v_cvt_pk_bf16_f32 v64, v160, 0\n"
      "v_cvt_pk_bf16_f32 v62, v144, v145\n"
      "v_mfma_f32_16x16x16_bf16 v[168:171], v[194:195], v[192:193], v[168:171]\n"
      "v_mul_f32_e64 v174, v244, v174\n"
      "v_cvt_pk_bf16_f32 v63, v146, v147\n"
      "v_cvt_pk_bf16_f32 v172, v172, v173\n"
      "v_mfma_f32_16x16x32_bf16 v[176:179], v[66:69], v[148:151], 0\n"
      "v_mul_f32 v175, v244, v175\n"
      "v_cvt_pk_bf16_f32 v173, v174, v175\n"
      "v_mfma_f32_16x16x32_bf16 v[176:179], v[66:69], v[62:65], v[176:179]\n"
      "v_mul_f32 v213, v244, v213\n"
      "v_mul_f32 v212, v244, v212\n"
      "v_mfma_f32_16x16x32_bf16 v[188:191], v[140:143], v[148:151], 0\n"
      "v_cvt_pk_bf16_f32 v174, v212, v213\n"
      "v_mul_f32 v214, v244, v214\n"
      "v_mfma_f32_16x16x32_bf16 v[188:191], v[50:53], v[62:65], v[188:191]\n"
      "v_mul_f32 v215, v244, v215\n"
      "v_cvt_pk_bf16_f32 v175, v214, v215\n"
      "v_mfma_f32_16x16x32_bf16 v[212:215], v[224:227], v[148:151], 0\n"
      "ds_read_b128 v[204:207], v120\n"
      "v_mul_f32 v252, v244, v252\n"
      "v_mfma_f32_16x16x32_bf16 v[212:215], v[54:57], v[62:65], v[212:215]\n"
      "ds_write_b128 v124, v[172:175] offset:18432\n"
      "v_rcp_f32_e64 v176, v176\n"
      "v_mfma_f32_16x16x32_bf16 v[172:175], v[136:139], v[148:151], 0\n"
      "ds_read_b128 v[144:147], v123 offset:1024\n"
      "v_mul_f32_e64 v255, v244, v255\n"
      "v_max3_f32 v160, v200, v201, v202\n"
      "v_max3_f32 v177, v203, v184, v185\n"
      "v_max3_f32 v177, v177, v248, v249\n"
      "v_mul_f32_e64 v254, v244, v254\n"
      "v_max3_f32 v160, v160, v186, v187\n"
      "v_max3_f32 v160, v160, v250, v251\n"
      "v_max3_f32 v160, v160, v177, v162\n"
      "v_mov_b32 v177, v160\n"
      "v_mul_f32 v253, v244, v253\n"
      "v_cvt_pk_bf16_f32 v252, v252, v253\n"
      "v_permlane16_swap_b32 v160, v177\n"
      "v_max_f32 v160, v160, v177\n"
      "v_mov_b32 v177, v160\n"
      "v_mul_f32 v188, v176, v188\n"
      "v_cvt_pk_bf16_f32 v253, v254, v255\n"
      "v_mul_f32 v189, v176, v189\n"
      "v_permlane32_swap_b32 v160, v177\n"
      "v_max_f32 v160, v160, v177\n"
      "v_sub_f32 v251, v251, v160\n"
      "v_sub_f32 v203, v203, v160\n"
      "v_sub_f32 v248, v248, v160\n"
      "v_sub_f32 v201, v201, v160\n"
      "v_sub_f32 v186, v186, v160\n"
      "v_sub_f32 v200, v200, v160\n"
      "v_sub_f32 v185, v185, v160\n"
      "v_exp_f32_e64 v201, v201\n"
      "v_mfma_f32_16x16x32_bf16 v[172:175], v[58:61], v[62:65], v[172:175]\n"
      "v_exp_f32 v248, v248\n"
      "v_sub_f32 v202, v202, v160\n"
      "v_sub_f32 v162, v162, v160\n"
      "v_sub_f32 v187, v187, v160\n"
      "v_exp_f32 v186, v186\n"
      "v_exp_f32 v203, v203\n"
      "v_exp_f32 v200, v200\n"
      "v_exp_f32 v202, v202\n"
      "v_cvt_pk_bf16_f32 v200, v200, v201\n"
      "v_cvt_pk_bf16_f32 v201, v202, v203\n"
      "v_sub_f32 v249, v249, v160\n"
      "v_exp_f32 v249, v249\n"
      "v_exp_f32 v187, v187\n"
      "v_exp_f32 v185, v185\n"
      "v_sub_f32 v184, v184, v160\n"
      "v_exp_f32 v251, v251\n"
      "v_exp_f32_e64 v184, v184\n"
      "v_cvt_pk_bf16_f32 v188, v188, v189\n"
      "v_cvt_pk_bf16_f32 v203, v186, v187\n"
      "v_mul_f32 v213, v176, v213\n"
      "v_sub_f32 v250, v250, v160\n"
      "v_cvt_pk_bf16_f32 v62, v248, v249\n"
      "v_exp_f32_e64 v162, v162\n"
      "v_cvt_pk_bf16_f32 v202, v184, v185\n"
      "v_exp_f32_e64 v250, v250\n"
      "v_cvt_pk_bf16_f32 v64, v162, 0\n"
      "v_cvt_pk_bf16_f32 v63, v250, v251\n"
      "v_mfma_f32_16x16x32_bf16 v[192:195], v[66:69], v[200:203], 0\n"
      "ds_read_b128 v[184:187], v123 offset:4096\n"
      "v_mul_f32_e64 v191, v176, v191\n"
      "v_mfma_f32_16x16x32_bf16 v[192:195], v[66:69], v[62:65], v[192:195]\n"
      "ds_read_b128 v[216:219], v123 offset:7168\n"
      "v_mul_f32_e64 v190, v176, v190\n"
      "v_cvt_pk_bf16_f32 v189, v190, v191\n"
      "v_mfma_f32_16x16x32_bf16 v[240:243], v[140:143], v[200:203], 0\n"
      "ds_read_b128 v[164:167], v121\n"
      "v_mul_f32 v212, v176, v212\n"
      "v_mfma_f32_16x16x32_bf16 v[240:243], v[50:53], v[62:65], v[240:243]\n"
      "v_cvt_pk_bf16_f32 v190, v212, v213\n"
      "v_mul_f32 v214, v176, v214\n"
      "s_waitcnt lgkmcnt(3)\n"
      "v_mul_f32 v215, v176, v215\n"
      "v_mfma_f32_16x16x32_bf16 v[248:251], v[224:227], v[200:203], 0\n"
      "global_store_dwordx4 v117, v[204:207], s[54:55] sc1\n"
      "v_mfma_f32_16x16x32_bf16 v[248:251], v[54:57], v[62:65], v[248:251]\n"
      "ds_read_b128 v[210:213], v123 offset:10240\n"
      "v_cvt_pk_bf16_f32 v191, v214, v215\n"
      "v_mfma_f32_16x16x32_bf16 v[148:151], v[136:139], v[200:203], 0\n"
      "ds_read_b128 v[204:207], v123 offset:13312\n"
      "v_mul_f32 v172, v176, v172\n"
      "v_mfma_f32_16x16x32_bf16 v[148:151], v[58:61], v[62:65], v[148:151]\n"
      "ds_write_b128 v124, v[188:191] offset:19456\n"
      "s_waitcnt lgkmcnt(4)\n"
      "v_mfma_f32_16x16x32_bf16 v[200:203], a[216:219], v[144:147], 0\n"
      "ds_read_b128 v[188:191], v123 offset:16384\n"
      "v_mul_f32 v173, v176, v173\n"
      "v_mfma_f32_16x16x32_bf16 v[200:203], a[228:231], v[184:187], v[200:203]\n"
      "v_cvt_pk_bf16_f32 v172, v172, v173\n"
      "v_mul_f32 v174, v176, v174\n"
      "v_mfma_f32_16x16x32_bf16 v[244:247], a[220:223], v[144:147], 0\n"
      "v_mul_f32_e64 v175, v176, v175\n"
      "v_cvt_pk_bf16_f32 v173, v174, v175\n"
      "v_mfma_f32_16x16x32_bf16 v[244:247], a[232:235], v[184:187], v[244:247]\n"
      "ds_write2st64_b64 v125, v[252:253], v[172:173] offset0:36 offset1:38\n"
      "v_mfma_f32_16x16x32_bf16 v[176:179], a[224:227], v[144:147], 0\n"
      "ds_read_b128 v[172:175], v123 offset:2048\n"
      "v_cndmask_b32 v229, v229, v161, s[42:43]\n"
      "v_mfma_f32_16x16x32_bf16 v[176:179], a[236:239], v[184:187], v[176:179]\n"
      "ds_read_b128 v[158:161], v123 offset:5120\n"
      "s_waitcnt lgkmcnt(5)\n"
      "v_cndmask_b32 v229, v229, v163, s[44:45]\n"
      "v_mfma_f32_16x16x32_bf16 v[200:203], a[240:243], v[216:219], v[200:203]\n"
      "ds_read_b128 v[144:147], v123 offset:8192\n"
      "v_cndmask_b32 v229, v229, v169, s[46:47]\n"
      "v_mfma_f32_16x16x32_bf16 v[200:203], a[252:255], v[210:213], v[200:203]\n"
      "s_waitcnt lgkmcnt(4)\n"
      "v_max_f32_dpp v214, v229, v229 row_ror:8 row_mask:0xf bank_mask:0xf\n"
      "v_mfma_f32_16x16x32_bf16 v[244:247], a[244:247], v[216:219], v[244:247]\n"
      "ds_read_b128 v[252:255], v123 offset:11264\n"
      "v_max_f32_dpp v214, v214, v214 row_ror:4 row_mask:0xf bank_mask:0xf\n"
      "v_mfma_f32_16x16x32_bf16 v[244:247], v[0:3], v[210:213], v[244:247]\n"
      "s_mov_b32 exec_hi, 0\n"
      "global_store_dwordx4 v118, v[164:167], s[54:55] sc1\n"
      "s_mov_b32 exec_hi, -1\n"
      "v_mfma_f32_16x16x32_bf16 v[176:179], a[248:251], v[216:219], v[176:179]\n"
      "ds_read_b128 v[216:219], v123 offset:14336\n"
      "v_max_f32_dpp v214, v214, v214 row_ror:2 row_mask:0xf bank_mask:0xf\n"
      "v_mfma_f32_16x16x32_bf16 v[176:179], v[4:7], v[210:213], v[176:179]\n"
      "ds_read_b128 v[162:165], v123 offset:17408\n"
      "v_max_f32_dpp v214, v214, v214 row_ror:1 row_mask:0xf bank_mask:0xf\n"
      "v_mfma_f32_16x16x32_bf16 v[200:203], v[8:11], v[204:207], v[200:203]\n"
      "s_add_u32 s28, s27, 2\n"
      "v_max_f32_dpp v214, v214, v214 row_bcast:15 row_mask:0xa bank_mask:0xf\n"
      "s_min_u32 m0, s28, s3\n"
      "v_mfma_f32_16x16x32_bf16 v[200:203], v[20:23], v[188:191], v[200:203]\n"
      "s_nop 0\n"
      "v_max_f32_dpp v214, v214, v214 row_bcast:31 row_mask:0xc bank_mask:0xf\n"
      "v_mfma_f32_16x16x32_bf16 v[244:247], v[12:15], v[204:207], v[244:247]\n"
      "s_movrels_b32 s9, s80\n"
      "v_readlane_b32 s31, v214, 63\n"
      "s_lshr_b32 s8, s9, 8\n"
      "v_mfma_f32_16x16x32_bf16 v[244:247], v[24:27], v[188:191], v[244:247]\n"
      "s_and_b32 s9, s9, 0xff\n"
      "v_subrev_f32 v229, s31, v229\n"
      "v_exp_f32 v229, v229\n"
      "v_mfma_f32_16x16x32_bf16 v[176:179], v[16:19], v[204:207], v[176:179]\n"
      "s_waitcnt lgkmcnt(2)\n"
      "v_fma_f32 v204, v200, v200, s39\n"
      "v_cvt_pk_bf16_f32 v229, v229, v229\n"
      "v_mfma_f32_16x16x32_bf16 v[176:179], v[28:31], v[188:191], v[176:179]\n"
      "ds_write_b16 v130, v229\n"
      "v_mfma_f32_16x16x32_bf16 v[212:215], a[216:219], v[172:175], 0\n"
      "ds_read_b128 v[206:209], v131\n"
      "v_fmac_f32 v204, v201, v201\n"
      "v_mfma_f32_16x16x32_bf16 v[212:215], a[228:231], v[158:161], v[212:215]\n"
      "ds_read_b128 v[196:199], v131 offset:64\n"
      "v_fmac_f32 v204, v202, v202\n"
      "v_mfma_f32_16x16x32_bf16 v[184:187], a[220:223], v[172:175], 0\n"
      "ds_read_b128 v[168:171], v114\n"
      "v_fmac_f32 v204, v203, v203\n"
      "v_mfma_f32_16x16x32_bf16 v[184:187], a[232:235], v[158:161], v[184:187]\n"
      "s_waitcnt lgkmcnt(4)\n"
      "v_fmac_f32 v204, v244, v244\n"
      "v_fmac_f32 v204, v245, v245\n"
      "v_mfma_f32_16x16x32_bf16 v[188:191], a[224:227], v[172:175], 0\n"
      "ds_read_b128 v[172:175], v115\n"
      "v_fmac_f32 v204, v246, v246\n"
      "v_mfma_f32_16x16x32_bf16 v[188:191], a[236:239], v[158:161], v[188:191]\n"
      "v_fmac_f32 v204, v247, v247\n"
      "v_fmac_f32 v204, v176, v176\n"
      "v_mfma_f32_16x16x32_bf16 v[212:215], a[240:243], v[144:147], v[212:215]\n"
      "s_mul_i32 s10, s8, 73\n"
      "v_fmac_f32 v204, v177, v177\n"
      "v_fmac_f32_e64 v204, v178, v178\n"
      "v_mfma_f32_16x16x32_bf16 v[212:215], a[252:255], v[252:255], v[212:215]\n"
      "s_add_u32 s10, s10, s9\n"
      "v_fmac_f32 v204, v179, v179\n"
      "s_mul_i32 s28, s10, 4802\n"
      "v_mfma_f32_16x16x32_bf16 v[184:187], a[244:247], v[144:147], v[184:187]\n"
      "v_permlane16_swap_b32 v230, v204\n"
      "v_mfma_f32_16x16x32_bf16 v[184:187], v[0:3], v[252:255], v[184:187]\n"
      "s_add_u32 s56, s18, s28\n"
      "v_mfma_f32_16x16x32_bf16 v[188:191], a[248:251], v[144:147], v[188:191]\n"
      "s_addc_u32 s57, s19, 0\n"
      "v_add_f32 v230, v230, v204\n"
      "s_and_b32 s56, s56, -16\n"
      "v_mfma_f32_16x16x32_bf16 v[188:191], v[4:7], v[252:255], v[188:191]\n"
      "s_sub_u32 s58, s56, 110592\n"
      "s_subb_u32 s59, s57, 0\n"
      "v_mfma_f32_16x16x32_bf16 v[212:215], v[8:11], v[216:219], v[212:215]\n"
      "s_sub_u32 s56, s56, 73728\n"
      "s_waitcnt lgkmcnt(0)\n"
      "s_subb_u32 s57, s57, 0\n"
      "v_mfma_f32_16x16x32_bf16 v[212:215], v[20:23], v[162:165], v[212:215]\n"
      "s_mul_i32 s28, s8, 1373568\n"
      "s_mul_i32 s29, s9, 2688\n"
      "s_add_u32 s28, s28, s29\n"
      "v_mfma_f32_16x16x32_bf16 v[184:187], v[12:15], v[216:219], v[184:187]\n"
      "s_cmp_eq_u32 s8, 72\n"
      "s_cselect_b32 s8, 0, 100270464\n"
      "v_mfma_f32_16x16x32_bf16 v[184:187], v[24:27], v[162:165], v[184:187]\n"
      "s_cmp_eq_u32 s9, 72\n"
      "v_fma_f32 v204, v212, v212, s39\n"
      "v_fmac_f32 v204, v213, v213\n"
      "v_mfma_f32_16x16x32_bf16 v[188:191], v[16:19], v[216:219], v[188:191]\n"
      "s_cselect_b32 s9, 0, 196224\n"
      "v_fmac_f32 v204, v214, v214\n"
      "v_fmac_f32 v204, v215, v215\n"
      "v_mfma_f32_16x16x32_bf16 v[188:191], v[28:31], v[162:165], v[188:191]\n"
      "s_add_u32 s10, s8, s9\n"
      "v_fmac_f32 v204, v184, v184\n"
      "v_fmac_f32 v204, v185, v185\n"
      "v_mfma_f32_16x16x32_bf16 v[158:161], v[206:209], v[66:69], 0\n"
      "s_add_u32 s28, s28, s10\n"
      "v_fmac_f32 v204, v186, v186\n"
      "v_fmac_f32 v204, v187, v187\n"
      "v_mfma_f32_16x16x32_bf16 v[158:161], v[196:199], v[66:69], v[158:161]\n"
      "s_add_u32 s52, s16, s28\n"
      "v_fmac_f32 v204, v188, v188\n"
      "v_fmac_f32 v204, v189, v189\n"
      "v_mfma_f32_16x16x32_bf16 v[216:219], v[206:209], v[140:143], 0\n"
      "s_addc_u32 s53, s17, 0\n"
      "v_fmac_f32 v204, v190, v190\n"
      "v_fmac_f32 v204, v191, v191\n"
      "v_mfma_f32_16x16x32_bf16 v[216:219], v[196:199], v[50:53], v[216:219]\n"
      "s_add_u32 s27, s27, 1\n"
      "v_mov_b32_dpp v158, v158 quad_perm:[0,0,0,0] row_mask:0xf bank_mask:0xf\n"
      "v_mov_b32 v205, v204\n"
      "v_mfma_f32_16x16x32_bf16 v[160:163], v[206:209], v[224:227], 0\n"
      "s_cmp_ge_u32 s27, s26\n"
      "v_permlane16_swap_b32 v204, v205\n"
      "v_mfma_f32_16x16x32_bf16 v[160:163], v[196:199], v[54:57], v[160:163]\n"
      "v_add_f32_e64 v204, v204, v205\n"
      "s_cselect_b32 s28, 1, 0\n"
      "v_mfma_f32_16x16x32_bf16 v[224:227], v[206:209], v[136:139], 0\n"
      "s_xor_b32 s29, s10, s11\n"
      "v_permlane32_swap_b32_e64 v230, v204\n"
      "v_mfma_f32_16x16x32_bf16 v[224:227], v[196:199], v[58:61], v[224:227]\n"
      "v_add_f32 v230, v230, v204\n"
      "v_rcp_f32_e64 v158, v158\n"
      "v_mfma_f32_16x16x32_bf16 v[204:207], a[0:3], v[168:171], 0\n"
      "s_or_b32 s28, s28, s29\n"
      "v_cndmask_b32 v216, v216, v160, s[42:43]\n"
      "v_mfma_f32_16x16x32_bf16 v[204:207], a[36:39], v[172:175], v[204:207]\n"
      "ds_write_b32 v129, v230 offset:1024\n"
      "v_mfma_f32_16x16x32_bf16 v[160:163], a[4:7], v[168:171], 0\n"
      "v_cndmask_b32 v216, v216, v224, s[44:45]\n"
      "v_mul_f32 v216, v158, v216\n"
      "v_cvt_pk_bf16_f32 v216, v216, v216\n"
      "v_mfma_f32_16x16x32_bf16 v[160:163], a[40:43], v[172:175], v[160:163]\n"
      "ds_write_b16 v135, v216 offset:16\n"
      "s_branch L_join_21_%=\n"
      "L_flg_108_%=:\n"
      "v_cmp_gt_i32 vcc, 0, v212\n"
      "s_cbranch_vccz L_fb_109_%=\n"
      "s_sleep 1\n"
      "ds_read_b128 v[212:215], v128 offset:2048\n"
      "s_waitcnt lgkmcnt(0)\n"
      "v_bitop3_b32 v212, v212, v213, s4 bitop3:0x7e\n"
      "v_bitop3_b32 v214, v214, v215, s4 bitop3:0x7e\n"
      "v_or_b32 v212, v212, v214\n"
      "v_cmp_ne_u32 s[48:49], 0, v212\n"
      "s_branch L_flg_108_%=\n"
      "L_gen_20_%=:\n"
      "s_nop 0\n"
      "s_nop 0\n"
      "v_mbcnt_lo_u32_b32 v231, -1, 0\n"
      "v_mbcnt_hi_u32_b32 v231, -1, v231\n"
      "v_and_b32 v220, 15, v231\n"
      "v_mul_u32_u24 v156, 0x62, v220\n"
      "v_lshrrev_b32 v221, 4, v231\n"
      "v_lshl_add_u32 v156, v221, 3, v156\n"
      "v_add_u32 v156, s30, v156\n"
      "v_lshlrev_b32 v221, 2, v221\n"
      "v_sub_u32 v220, v220, v221\n"
      "v_and_b32 v221, 1, v220\n"
      "v_lshlrev_b32_e64 v221, 4, v221\n"
      "v_and_b32_e64 v231, -4, v156\n"
      "v_lshrrev_b32_e64 v220, 1, v220\n"
      "v_cmp_eq_u32 vcc, 0, v220\n"
      "s_movk_i32 s31, 0x3fb9\n"
      "ds_read2_b32 v[160:161], v231 offset0:2 offset1:10\n"
      "ds_read2_b32 v[150:151], v231 offset0:18 offset1:24\n"
      "ds_read2_b32 v[214:215], v231 offset0:0 offset1:1\n"
      "ds_read2_b32 v[172:173], v231 offset0:8 offset1:9\n"
      "ds_read2_b32 v[210:211], v231 offset0:16 offset1:17\n"
      "v_add_u32 v231, 1568, v231\n"
      "v_lshlrev_b32 v221, v221, s31\n"
      "v_cndmask_b32 v198, 0, v221, vcc\n"
      "ds_read2_b32 v[212:213], v231 offset0:8 offset1:9\n"
      "ds_read2_b32 v[206:207], v231 offset0:18 offset1:24\n"
      "ds_read2_b32 v[148:149], v231 offset0:0 offset1:1\n"
      "ds_read2_b32 v[238:239], v231 offset0:2 offset1:10\n"
      "ds_read2_b32 v[204:205], v231 offset0:16 offset1:17\n"
      "v_and_b32 v156, 2, v156\n"
      "v_add_u32 v231, 1568, v231\n"
      "v_mfma_f32_16x16x32_bf16 v[164:167], v[188:191], v[200:203], v[98:101]\n"
      "ds_read2_b32 v[174:175], v231 offset0:0 offset1:1\n"
      "ds_read2_b32 v[236:237], v231 offset0:16 offset1:17\n"
      "v_cmp_eq_u32 vcc, 1, v220\n"
      "v_mfma_f32_16x16x32_bf16 v[144:147], v[248:251], v[200:203], v[94:97]\n"
      "ds_read2_b32 v[196:197], v231 offset0:18 offset1:24\n"
      "v_cndmask_b32 v199, 0, v221, vcc\n"
      "v_mfma_f32_16x16x32_bf16 v[220:223], v[252:255], v[200:203], v[90:93]\n"
      "s_waitcnt lgkmcnt(10)\n"
      "ds_read2_b32 v[208:209], v231 offset0:2 offset1:10\n"
      "v_mfma_f32_16x16x32_bf16 v[220:223], v[34:37], v[38:41], v[220:223]\n"
      "s_waitcnt lgkmcnt(10)\n"
      "ds_read2_b32 v[162:163], v231 offset0:8 offset1:9\n"
      "v_alignbyte_b32 v172, v173, v172, v156\n"
      "v_alignbyte_b32 v173, v161, v173, v156\n"
      "v_alignbyte_b32 v214, v215, v214, v156\n"
      "v_alignbyte_b32 v215, v160, v215, v156\n"
      "v_alignbyte_b32 v151, v151, v151, v156\n"
      "v_mfma_f32_16x16x32_bf16 v[144:147], v[40:43], v[38:41], v[144:147]\n"
      "s_waitcnt lgkmcnt(7)\n"
      "v_alignbyte_b32 v210, v211, v210, v156\n"
      "v_lshlrev_b32 v151, 16, v151\n"
      "v_mfma_f32_16x16x32_bf16 v[158:161], v[240:243], v[200:203], v[72:75]\n"
      "v_alignbyte_b32 v148, v149, v148, v156\n"
      "v_fmac_f32 v228, s37, v151\n"
      "v_mfma_f32_16x16x32_bf16 v[158:161], v[216:219], v[248:251], v[158:161]\n"
      "s_waitcnt lgkmcnt(5)\n"
      "v_alignbyte_b32 v212, v213, v212, v156\n"
      "v_alignbyte_b32 v213, v239, v213, v156\n"
      "v_mfma_f32_16x16x32_bf16 v[158:161], v[192:195], v[38:41], v[158:161]\n"
      "v_alignbyte_b32 v211, v150, v211, v156\n"
      "v_alignbyte_b32 v204, v205, v204, v156\n"
      "v_mfma_f32_16x16x16_bf16 v[184:187], v[198:199], v[214:215], v[184:187]\n"
      "s_waitcnt lgkmcnt(2)\n"
      "v_alignbyte_b32 v207, v207, v207, v156\n"
      "v_lshlrev_b32 v207, 16, v207\n"
      "v_mfma_f32_16x16x16_bf16 v[176:179], v[198:199], v[210:211], v[176:179]\n"
      "ds_read_b128 v[200:203], v128 offset:2048\n"
      "v_alignbyte_b32 v149, v238, v149, v156\n"
      "v_mfma_f32_16x16x16_bf16 v[244:247], v[198:199], v[172:173], v[244:247]\n"
      "s_nop 0\n"
      "v_max3_f32 v151, v184, v185, v186\n"
      "v_alignbyte_b32 v205, v206, v205, v156\n"
      "v_fmac_f32 v158, s37, v207\n"
      "v_mfma_f32_16x16x32_bf16 v[164:167], v[46:49], v[38:41], v[164:167]\n"
      "s_nop 2\n"
      "v_max3_f32 v173, v187, v244, v245\n"
      "v_mfma_f32_16x16x16_bf16 v[144:147], v[198:199], v[212:213], v[144:147]\n"
      "s_nop 0\n"
      "v_max3_f32 v173, v173, v176, v177\n"
      "v_max3_f32 v151, v151, v246, v247\n"
      "v_max3_f32 v151, v151, v178, v179\n"
      "v_max3_f32 v151, v151, v173, v228\n"
      "v_alignbyte_b32 v174, v175, v174, v156\n"
      "v_mov_b32 v173, v151\n"
      "v_mfma_f32_16x16x16_bf16 v[220:223], v[198:199], v[148:149], v[220:223]\n"
      "s_nop 0\n"
      "v_permlane16_swap_b32 v151, v173\n"
      "v_max_f32 v151, v151, v173\n"
      "v_mov_b32_e64 v172, v151\n"
      "v_mfma_f32_16x16x16_bf16 v[164:167], v[198:199], v[204:205], v[164:167]\n"
      "s_nop 0\n"
      "v_permlane32_swap_b32 v151, v172\n"
      "v_max_f32 v151, v151, v172\n"
      "v_sub_f32 v179, v179, v151\n"
      "v_max3_f32 v172, v220, v221, v222\n"
      "v_sub_f32 v184, v184, v151\n"
      "v_sub_f32 v178, v178, v151\n"
      "v_sub_f32 v244, v244, v151\n"
      "v_sub_f32 v245, v245, v151\n"
      "v_sub_f32 v228, v228, v151\n"
      "v_sub_f32 v176, v176, v151\n"
      "v_exp_f32 v244, v244\n"
      "v_exp_f32 v179, v179\n"
      "v_sub_f32 v186, v186, v151\n"
      "v_exp_f32 v184, v184\n"
      "v_max3_f32 v172, v172, v146, v147\n"
      "v_exp_f32 v245, v245\n"
      "v_sub_f32 v247, v247, v151\n"
      "v_sub_f32 v185, v185, v151\n"
      "v_exp_f32 v185, v185\n"
      "v_max3_f32 v173, v223, v144, v145\n"
      "v_sub_f32 v187, v187, v151\n"
      "v_sub_f32 v177, v177, v151\n"
      "v_exp_f32 v177, v177\n"
      "v_exp_f32 v247, v247\n"
      "v_exp_f32 v187, v187\n"
      "v_exp_f32 v186, v186\n"
      "v_max3_f32 v173, v173, v164, v165\n"
      "v_cvt_pk_bf16_f32 v184, v184, v185\n"
      "v_cvt_pk_bf16_f32 v185, v186, v187\n"
      "v_sub_f32_e64 v246, v246, v151\n"
      "v_max3_f32 v172, v172, v166, v167\n"
      "s_waitcnt lgkmcnt(0)\n"
      "v_exp_f32 v176, v176\n"
      "v_cvt_pk_bf16_f32 v186, v244, v245\n"
      "v_cvt_pk_bf16_f32 v62, v176, v177\n"
      "v_exp_f32 v178, v178\n"
      "v_exp_f32 v228, v228\n"
      "v_alignbyte_b32 v162, v163, v162, v156\n"
      "v_max3_f32 v172, v172, v173, v158\n"
      "v_mov_b32_e64 v173, v172\n"
      "v_alignbyte_b32 v175, v208, v175, v156\n"
      "v_exp_f32 v246, v246\n"
      "v_alignbyte_b32 v236, v237, v236, v156\n"
      "v_permlane16_swap_b32 v172, v173\n"
      "v_max_f32 v172, v172, v173\n"
      "v_mov_b32 v173, v172\n"
      "v_alignbyte_b32 v237, v196, v237, v156\n"
      "v_bitop3_b32 v200, v200, v201, s4 bitop3:0x7e\n"
      "v_permlane32_swap_b32 v172, v173\n"
      "v_max_f32 v172, v172, v173\n"
      "v_cvt_pk_bf16_f32 v187, v246, v247\n"
      "v_cvt_pk_bf16_f32 v63, v178, v179\n"
      "v_sub_f32 v222, v222, v172\n"
      "v_cvt_pk_bf16_f32 v64, v228, 0\n"
      "v_alignbyte_b32 v163, v209, v163, v156\n"
      "v_sub_f32 v147, v147, v172\n"
      "v_sub_f32 v166, v166, v172\n"
      "v_bitop3_b32 v202, v202, v203, s4 bitop3:0x7e\n"
      "v_sub_f32 v144, v144, v172\n"
      "v_sub_f32_e64 v220, v220, v172\n"
      "v_mfma_f32_16x16x32_bf16 v[244:247], v[66:69], v[184:187], 0\n"
      "v_alignbyte_b32 v197, v197, v197, v156\n"
      "v_lshlrev_b32 v197, 16, v197\n"
      "v_mfma_f32_16x16x32_bf16 v[244:247], v[66:69], v[62:65], v[244:247]\n"
      "v_sub_f32 v158, v158, v172\n"
      "v_sub_f32 v145, v145, v172\n"
      "v_sub_f32 v164, v164, v172\n"
      "v_sub_f32 v146, v146, v172\n"
      "v_sub_f32 v223, v223, v172\n"
      "v_sub_f32 v165, v165, v172\n"
      "v_mfma_f32_16x16x32_bf16 v[176:179], v[140:143], v[184:187], 0\n"
      "v_or_b32_e64 v200, v200, v202\n"
      "v_cmp_ne_u32 vcc, 0, v200\n"
      "v_mfma_f32_16x16x32_bf16 v[176:179], v[50:53], v[62:65], v[176:179]\n"
      "s_mov_b64 s[48:49], vcc\n"
      "v_sub_f32 v221, v221, v172\n"
      "v_exp_f32 v221, v221\n"
      "v_mfma_f32_16x16x32_bf16 v[212:215], v[136:139], v[184:187], 0\n"
      "s_cbranch_vccnz L_flg_114_%=\n"
      "L_fb_115_%=:\n"
      "v_exp_f32 v220, v220\n"
      "v_sub_f32 v167, v167, v172\n"
      "v_mfma_f32_16x16x32_bf16 v[200:203], v[224:227], v[184:187], 0\n"
      "v_exp_f32_e64 v222, v222\n"
      "v_mfma_f32_16x16x32_bf16 v[200:203], v[54:57], v[62:65], v[200:203]\n"
      "ds_read_b128 v[148:151], v119 offset:12288\n"
      "v_exp_f32 v144, v144\n"
      "v_mfma_f32_16x16x32_bf16 v[212:215], v[58:61], v[62:65], v[212:215]\n"
      "v_exp_f32 v166, v166\n"
      "v_mfma_f32_16x16x32_bf16 v[184:187], v[252:255], v[168:171], v[102:105]\n"
      "v_exp_f32 v146, v146\n"
      "v_mfma_f32_16x16x32_bf16 v[184:187], v[34:37], v[44:47], v[184:187]\n"
      "v_cvt_pk_bf16_f32 v220, v220, v221\n"
      "v_exp_f32 v223, v223\n"
      "v_exp_f32_e64 v147, v147\n"
      "v_cvt_pk_bf16_f32 v221, v222, v223\n"
      "v_exp_f32 v145, v145\n"
      "v_mfma_f32_16x16x32_bf16 v[204:207], v[248:251], v[168:171], v[106:109]\n"
      "v_exp_f32 v164, v164\n"
      "v_mfma_f32_16x16x32_bf16 v[204:207], v[40:43], v[44:47], v[204:207]\n"
      "v_cvt_pk_bf16_f32 v222, v144, v145\n"
      "v_cvt_pk_bf16_f32 v223, v146, v147\n"
      "v_exp_f32 v165, v165\n"
      "v_mfma_f32_16x16x16_bf16 v[184:187], v[198:199], v[174:175], v[184:187]\n"
      "s_waitcnt lgkmcnt(0)\n"
      "v_exp_f32 v167, v167\n"
      "v_mfma_f32_16x16x16_bf16 v[204:207], v[198:199], v[162:163], v[204:207]\n"
      "global_store_dwordx4 v116, v[148:151], s[54:55] offset:256 sc1\n"
      "v_mfma_f32_16x16x32_bf16 v[172:175], v[240:243], v[168:171], v[74:77]\n"
      "v_exp_f32 v158, v158\n"
      "v_mfma_f32_16x16x32_bf16 v[172:175], v[216:219], v[188:191], v[172:175]\n"
      "v_cvt_pk_bf16_f32 v63, v166, v167\n"
      "v_cvt_pk_bf16_f32 v62, v164, v165\n"
      "v_mfma_f32_16x16x32_bf16 v[172:175], v[192:195], v[44:47], v[172:175]\n"
      "v_rcp_f32 v244, v244\n"
      "v_mfma_f32_16x16x32_bf16 v[148:151], v[188:191], v[168:171], v[110:113]\n"
      "v_mul_f32 v176, v244, v176\n"
      "v_mul_f32 v177, v244, v177\n"
      "v_mfma_f32_16x16x32_bf16 v[148:151], v[46:49], v[44:47], v[148:151]\n"
      "v_mul_f32 v178, v244, v178\n"
      "v_cvt_pk_bf16_f32 v176, v176, v177\n"
      "v_mfma_f32_16x16x32_bf16 v[188:191], v[216:219], v[240:243], v[76:79]\n"
      "v_mul_f32 v213, v244, v213\n"
      "v_mul_f32 v179, v244, v179\n"
      "v_mul_f32_e64 v200, v244, v200\n"
      "v_cvt_pk_bf16_f32 v177, v178, v179\n"
      "v_mul_f32 v202, v244, v202\n"
      "v_mul_f32 v212, v244, v212\n"
      "v_cvt_pk_bf16_f32 v64, v158, 0\n"
      "v_mfma_f32_16x16x16_bf16 v[148:151], v[198:199], v[236:237], v[148:151]\n"
      "v_fmac_f32 v172, s37, v197\n"
      "v_mul_f32 v201, v244, v201\n"
      "v_cvt_pk_bf16_f32 v178, v200, v201\n"
      "v_mfma_f32_16x16x32_bf16 v[168:171], v[66:69], v[220:223], 0\n"
      "v_mul_f32_e64 v203, v244, v203\n"
      "v_cvt_pk_bf16_f32 v179, v202, v203\n"
      "v_mfma_f32_16x16x32_bf16 v[168:171], v[66:69], v[62:65], v[168:171]\n"
      "ds_write_b128 v124, v[176:179] offset:18432\n"
      "v_mul_f32_e64 v214, v244, v214\n"
      "v_cvt_pk_bf16_f32 v212, v212, v213\n"
      "v_mfma_f32_16x16x32_bf16 v[200:203], v[224:227], v[220:223], 0\n"
      "v_mul_f32_e64 v215, v244, v215\n"
      "v_cvt_pk_bf16_f32 v213, v214, v215\n"
      "v_mfma_f32_16x16x32_bf16 v[200:203], v[54:57], v[62:65], v[200:203]\n"
      "ds_read_b128 v[244:247], v120\n"
      "v_max3_f32 v215, v187, v204, v205\n"
      "v_mfma_f32_16x16x32_bf16 v[176:179], v[140:143], v[220:223], 0\n"
      "v_rcp_f32_e64 v168, v168\n"
      "v_max3_f32 v214, v184, v185, v186\n"
      "v_mfma_f32_16x16x16_bf16 v[188:191], v[194:195], v[192:193], v[188:191]\n"
      "v_max3_f32 v214, v214, v206, v207\n"
      "v_max3_f32 v215, v215, v148, v149\n"
      "v_max3_f32 v214, v214, v150, v151\n"
      "v_mfma_f32_16x16x32_bf16 v[160:163], v[136:139], v[220:223], 0\n"
      "v_max3_f32 v214, v214, v215, v172\n"
      "v_mov_b32 v215, v214\n"
      "v_mfma_f32_16x16x32_bf16 v[160:163], v[58:61], v[62:65], v[160:163]\n"
      "s_nop 0\n"
      "v_permlane16_swap_b32 v214, v215\n"
      "v_max_f32 v214, v214, v215\n"
      "v_mov_b32_e64 v215, v214\n"
      "v_mfma_f32_16x16x32_bf16 v[176:179], v[50:53], v[62:65], v[176:179]\n"
      "s_nop 0\n"
      "v_permlane32_swap_b32 v214, v215\n"
      "v_max_f32 v214, v214, v215\n"
      "v_sub_f32 v206, v206, v214\n"
      "v_sub_f32 v172, v172, v214\n"
      "v_sub_f32 v205, v205, v214\n"
      "v_sub_f32 v151, v151, v214\n"
      "v_sub_f32 v187, v187, v214\n"
      "v_sub_f32 v186, v186, v214\n"
      "v_sub_f32 v149, v149, v214\n"
      "v_sub_f32 v207, v207, v214\n"
      "v_exp_f32 v187, v187\n"
      "v_exp_f32 v206, v206\n"
      "v_exp_f32 v149, v149\n"
      "v_sub_f32 v184, v184, v214\n"
      "v_exp_f32 v184, v184\n"
      "v_exp_f32 v205, v205\n"
      "v_sub_f32 v185, v185, v214\n"
      "v_sub_f32 v148, v148, v214\n"
      "v_exp_f32 v172, v172\n"
      "v_exp_f32 v207, v207\n"
      "v_exp_f32 v151, v151\n"
      "v_sub_f32 v204, v204, v214\n"
      "v_exp_f32 v185, v185\n"
      "v_exp_f32 v186, v186\n"
      "v_exp_f32 v148, v148\n"
      "v_mul_f32 v177, v168, v177\n"
      "v_exp_f32 v204, v204\n"
      "v_cvt_pk_bf16_f32 v64, v172, 0\n"
      "v_mul_f32 v176, v168, v176\n"
      "v_cvt_pk_bf16_f32 v184, v184, v185\n"
      "v_sub_f32_e64 v150, v150, v214\n"
      "v_cvt_pk_bf16_f32 v185, v186, v187\n"
      "s_waitcnt lgkmcnt(0)\n"
      "v_cvt_pk_bf16_f32 v186, v204, v205\n"
      "v_cvt_pk_bf16_f32 v187, v206, v207\n"
      "v_exp_f32_e64 v150, v150\n"
      "v_cvt_pk_bf16_f32 v62, v148, v149\n"
      "v_cvt_pk_bf16_f32 v63, v150, v151\n"
      "v_mfma_f32_16x16x32_bf16 v[192:195], v[66:69], v[184:187], 0\n"
      "ds_read_b128 v[204:207], v123 offset:1024\n"
      "v_cvt_pk_bf16_f32 v176, v176, v177\n"
      "v_mfma_f32_16x16x32_bf16 v[192:195], v[66:69], v[62:65], v[192:195]\n"
      "ds_read_b128 v[196:199], v123 offset:4096\n"
      "v_mul_f32 v178, v168, v178\n"
      "v_mul_f32 v161, v168, v161\n"
      "v_mfma_f32_16x16x32_bf16 v[240:243], v[140:143], v[184:187], 0\n"
      "v_mul_f32 v179, v168, v179\n"
      "v_cvt_pk_bf16_f32 v177, v178, v179\n"
      "v_mfma_f32_16x16x32_bf16 v[240:243], v[50:53], v[62:65], v[240:243]\n"
      "global_store_dwordx4 v117, v[244:247], s[54:55] sc1\n"
      "v_mul_f32 v163, v168, v163\n"
      "v_mfma_f32_16x16x32_bf16 v[248:251], v[224:227], v[184:187], 0\n"
      "ds_read_b128 v[214:217], v121\n"
      "v_mul_f32_e64 v200, v168, v200\n"
      "v_mfma_f32_16x16x32_bf16 v[248:251], v[54:57], v[62:65], v[248:251]\n"
      "ds_read_b128 v[252:255], v123 offset:7168\n"
      "v_mul_f32_e64 v201, v168, v201\n"
      "v_cvt_pk_bf16_f32 v178, v200, v201\n"
      "v_mfma_f32_16x16x32_bf16 v[148:151], v[136:139], v[184:187], 0\n"
      "ds_read_b128 v[184:187], v123 offset:10240\n"
      "v_mul_f32 v202, v168, v202\n"
      "v_mfma_f32_16x16x32_bf16 v[148:151], v[58:61], v[62:65], v[148:151]\n"
      "v_mul_f32 v162, v168, v162\n"
      "v_mul_f32_e64 v203, v168, v203\n"
      "v_mul_f32 v160, v168, v160\n"
      "v_cvt_pk_bf16_f32 v160, v160, v161\n"
      "v_cvt_pk_bf16_f32 v179, v202, v203\n"
      "s_waitcnt lgkmcnt(3)\n"
      "v_mfma_f32_16x16x32_bf16 v[200:203], a[216:219], v[204:207], 0\n"
      "ds_read_b128 v[168:171], v123 offset:13312\n"
      "v_cvt_pk_bf16_f32 v161, v162, v163\n"
      "v_mfma_f32_16x16x32_bf16 v[200:203], a[228:231], v[196:199], v[200:203]\n"
      "ds_write_b128 v124, v[176:179] offset:19456\n"
      "v_mfma_f32_16x16x32_bf16 v[244:247], a[220:223], v[204:207], 0\n"
      "ds_read_b128 v[162:165], v123 offset:16384\n"
      "v_cndmask_b32 v229, v229, v159, s[42:43]\n"
      "v_mfma_f32_16x16x32_bf16 v[244:247], a[232:235], v[196:199], v[244:247]\n"
      "ds_write2st64_b64 v125, v[212:213], v[160:161] offset0:36 offset1:38\n"
      "s_waitcnt lgkmcnt(4)\n"
      "v_mfma_f32_16x16x32_bf16 v[176:179], a[224:227], v[204:207], 0\n"
      "ds_read_b128 v[204:207], v123 offset:2048\n"
      "v_cndmask_b32 v229, v229, v173, s[44:45]\n"
      "v_mfma_f32_16x16x32_bf16 v[176:179], a[236:239], v[196:199], v[176:179]\n"
      "ds_read_b128 v[172:175], v123 offset:5120\n"
      "v_cndmask_b32 v229, v229, v189, s[46:47]\n"
      "v_mfma_f32_16x16x32_bf16 v[200:203], a[240:243], v[252:255], v[200:203]\n"
      "v_mbcnt_lo_u32_b32 v231, -1, 0\n"
      "v_mbcnt_hi_u32_b32 v231, -1, v231\n"
      "v_mfma_f32_16x16x32_bf16 v[200:203], a[252:255], v[184:187], v[200:203]\n"
      "s_mov_b32 exec_hi, 0\n"
      "global_store_dwordx4 v118, v[214:217], s[54:55] sc1\n"
      "s_mov_b32 exec_hi, -1\n"
      "v_mfma_f32_16x16x32_bf16 v[244:247], a[244:247], v[252:255], v[244:247]\n"
      "s_add_u32 s28, s27, 2\n"
      "v_min_u32_e64 v231, 48, v231\n"
      "v_lshlrev_b32_e64 v231, 1, v231\n"
      "v_mfma_f32_16x16x32_bf16 v[244:247], v[0:3], v[184:187], v[244:247]\n"
      "s_waitcnt lgkmcnt(3)\n"
      "v_add_u32_e64 v231, s30, v231\n"
      "v_mfma_f32_16x16x32_bf16 v[176:179], a[248:251], v[252:255], v[176:179]\n"
      "ds_read_u16 v191, v231 offset:4704\n"
      "s_min_u32 m0, s28, s3\n"
      "v_mfma_f32_16x16x32_bf16 v[176:179], v[4:7], v[184:187], v[176:179]\n"
      "ds_read_b128 v[252:255], v123 offset:8192\n"
      "v_mfma_f32_16x16x32_bf16 v[200:203], v[8:11], v[168:171], v[200:203]\n"
      "ds_read_b128 v[158:161], v123 offset:11264\n"
      "s_movrels_b32 s9, s80\n"
      "v_mfma_f32_16x16x32_bf16 v[200:203], v[20:23], v[162:165], v[200:203]\n"
      "s_lshr_b32 s8, s9, 8\n"
      "s_waitcnt lgkmcnt(3)\n"
      "s_and_b32 s9, s9, 0xff\n"
      "v_mfma_f32_16x16x32_bf16 v[244:247], v[12:15], v[168:171], v[244:247]\n"
      "s_mul_i32 s10, s8, 73\n"
      "s_add_u32 s10, s10, s9\n"
      "s_mul_i32 s28, s10, 4802\n"
      "v_mfma_f32_16x16x32_bf16 v[244:247], v[24:27], v[162:165], v[244:247]\n"
      "s_add_u32 s56, s18, s28\n"
      "v_mfma_f32_16x16x32_bf16 v[176:179], v[16:19], v[168:171], v[176:179]\n"
      "s_addc_u32 s57, s19, 0\n"
      "s_and_b32 s56, s56, -16\n"
      "s_sub_u32 s58, s56, 110592\n"
      "v_mfma_f32_16x16x32_bf16 v[176:179], v[28:31], v[162:165], v[176:179]\n"
      "s_subb_u32 s59, s57, 0\n"
      "s_sub_u32 s56, s56, 73728\n"
      "v_mfma_f32_16x16x32_bf16 v[212:215], a[216:219], v[204:207], 0\n"
      "s_waitcnt lgkmcnt(2)\n"
      "v_lshlrev_b32 v191, 16, v191\n"
      "v_mfma_f32_16x16x32_bf16 v[212:215], a[228:231], v[172:175], v[212:215]\n"
      "ds_read_b128 v[168:171], v123 offset:14336\n"
      "v_fmac_f32 v229, s37, v191\n"
      "v_mfma_f32_16x16x32_bf16 v[184:187], a[220:223], v[204:207], 0\n"
      "s_nop 0\n"
      "v_max_f32_dpp v147, v229, v229 row_ror:8 row_mask:0xf bank_mask:0xf\n"
      "s_subb_u32 s57, s57, 0\n"
      "v_mfma_f32_16x16x32_bf16 v[184:187], a[232:235], v[172:175], v[184:187]\n"
      "ds_read_b128 v[162:165], v123 offset:17408\n"
      "v_max_f32_dpp v147, v147, v147 row_ror:4 row_mask:0xf bank_mask:0xf\n"
      "v_mfma_f32_16x16x32_bf16 v[188:191], a[224:227], v[204:207], 0\n"
      "s_waitcnt lgkmcnt(2)\n"
      "v_max_f32_dpp v147, v147, v147 row_ror:2 row_mask:0xf bank_mask:0xf\n"
      "v_fma_f32 v219, v200, v200, s39\n"
      "v_mfma_f32_16x16x32_bf16 v[188:191], a[236:239], v[172:175], v[188:191]\n"
      "s_mul_i32 s28, s8, 1373568\n"
      "v_fmac_f32_e64 v219, v201, v201\n"
      "v_max_f32_dpp v147, v147, v147 row_ror:1 row_mask:0xf bank_mask:0xf\n"
      "v_mfma_f32_16x16x32_bf16 v[212:215], a[240:243], v[252:255], v[212:215]\n"
      "s_mul_i32 s29, s9, 2688\n"
      "v_max_f32_dpp v147, v147, v147 row_bcast:15 row_mask:0xa bank_mask:0xf\n"
      "v_fmac_f32 v219, v202, v202\n"
      "v_mfma_f32_16x16x32_bf16 v[212:215], a[252:255], v[158:161], v[212:215]\n"
      "s_add_u32 s28, s28, s29\n"
      "v_fmac_f32_e64 v219, v203, v203\n"
      "v_max_f32_dpp v147, v147, v147 row_bcast:31 row_mask:0xc bank_mask:0xf\n"
      "v_mfma_f32_16x16x32_bf16 v[184:187], a[244:247], v[252:255], v[184:187]\n"
      "s_cmp_eq_u32 s8, 72\n"
      "v_readlane_b32 s31, v147, 63\n"
      "v_fmac_f32 v219, v244, v244\n"
      "v_mfma_f32_16x16x32_bf16 v[184:187], v[0:3], v[158:161], v[184:187]\n"
      "s_cselect_b32 s8, 0, 100270464\n"
      "v_subrev_f32 v229, s31, v229\n"
      "v_exp_f32_e64 v229, v229\n"
      "v_mfma_f32_16x16x32_bf16 v[188:191], a[248:251], v[252:255], v[188:191]\n"
      "s_cmp_eq_u32 s9, 72\n"
      "v_fmac_f32_e64 v219, v245, v245\n"
      "v_cvt_pk_bf16_f32 v229, v229, v229\n"
      "v_mfma_f32_16x16x32_bf16 v[188:191], v[4:7], v[158:161], v[188:191]\n"
      "s_waitcnt lgkmcnt(0)\n"
      "v_fmac_f32 v219, v246, v246\n"
      "v_fmac_f32_e64 v219, v247, v247\n"
      "v_mfma_f32_16x16x32_bf16 v[212:215], v[8:11], v[168:171], v[212:215]\n"
      "s_cselect_b32 s9, 0, 196224\n"
      "v_fmac_f32 v219, v176, v176\n"
      "v_fmac_f32 v219, v177, v177\n"
      "v_mfma_f32_16x16x32_bf16 v[212:215], v[20:23], v[162:165], v[212:215]\n"
      "ds_write_b16 v130, v229\n"
      "v_mfma_f32_16x16x32_bf16 v[184:187], v[12:15], v[168:171], v[184:187]\n"
      "ds_read_b128 v[204:207], v131\n"
      "v_fmac_f32_e64 v219, v178, v178\n"
      "v_mfma_f32_16x16x32_bf16 v[184:187], v[24:27], v[162:165], v[184:187]\n"
      "ds_read_b128 v[158:161], v131 offset:64\n"
      "v_fmac_f32_e64 v219, v179, v179\n"
      "v_mfma_f32_16x16x32_bf16 v[188:191], v[16:19], v[168:171], v[188:191]\n"
      "ds_read_b128 v[168:171], v114\n"
      "v_fma_f32 v217, v212, v212, s39\n"
      "v_mfma_f32_16x16x32_bf16 v[188:191], v[28:31], v[162:165], v[188:191]\n"
      "ds_read_b128 v[172:175], v115\n"
      "v_fmac_f32 v217, v213, v213\n"
      "v_fmac_f32 v217, v214, v214\n"
      "v_fmac_f32 v217, v215, v215\n"
      "v_fmac_f32 v217, v184, v184\n"
      "v_fmac_f32 v217, v185, v185\n"
      "v_fmac_f32 v217, v186, v186\n"
      "v_fmac_f32 v217, v187, v187\n"
      "v_fmac_f32 v217, v188, v188\n"
      "v_fmac_f32 v217, v189, v189\n"
      "v_fmac_f32 v217, v190, v190\n"
      "v_fmac_f32 v217, v191, v191\n"
      "v_mov_b32 v163, v217\n"
      "v_permlane16_swap_b32 v230, v219\n"
      "v_add_f32 v230, v230, v219\n"
      "v_permlane16_swap_b32 v217, v163\n"
      "v_add_f32 v217, v217, v163\n"
      "s_add_u32 s10, s8, s9\n"
      "s_add_u32 s28, s28, s10\n"
      "v_permlane32_swap_b32 v230, v217\n"
      "s_waitcnt lgkmcnt(2)\n"
      "v_mfma_f32_16x16x32_bf16 v[144:147], v[204:207], v[66:69], 0\n"
      "v_add_f32 v230, v230, v217\n"
      "s_add_u32 s52, s16, s28\n"
      "v_mfma_f32_16x16x32_bf16 v[144:147], v[158:161], v[66:69], v[144:147]\n"
      "ds_write_b32 v129, v230 offset:1024\n"
      "s_waitcnt lgkmcnt(1)\n"
      "v_mfma_f32_16x16x32_bf16 v[216:219], v[204:207], v[140:143], 0\n"
      "s_addc_u32 s53, s17, 0\n"
      "s_add_u32 s27, s27, 1\n"
      "s_cmp_ge_u32 s27, s26\n"
      "v_mfma_f32_16x16x32_bf16 v[216:219], v[158:161], v[50:53], v[216:219]\n"
      "v_mfma_f32_16x16x32_bf16 v[140:143], v[204:207], v[224:227], 0\n"
      "s_cselect_b32 s28, 1, 0\n"
      "s_xor_b32 s29, s10, s11\n"
      "v_mfma_f32_16x16x32_bf16 v[140:143], v[158:161], v[54:57], v[140:143]\n"
      "v_mfma_f32_16x16x32_bf16 v[208:211], v[204:207], v[136:139], 0\n"
      "v_mov_b32_dpp v144, v144 quad_perm:[0,0,0,0] row_mask:0xf bank_mask:0xf\n"
      "v_mfma_f32_16x16x32_bf16 v[208:211], v[158:161], v[58:61], v[208:211]\n"
      "s_or_b32 s28, s28, s29\n"
      "v_rcp_f32 v144, v144\n"
      "v_mfma_f32_16x16x32_bf16 v[204:207], a[0:3], v[168:171], 0\n"
      "s_nop 1\n"
      "v_cndmask_b32 v216, v216, v140, s[42:43]\n"
      "v_mfma_f32_16x16x32_bf16 v[204:207], a[36:39], v[172:175], v[204:207]\n"
      "s_nop 0\n"
      "v_cndmask_b32 v216, v216, v208, s[44:45]\n"
      "v_mul_f32_e64 v216, v144, v216\n"
      "v_mfma_f32_16x16x32_bf16 v[160:163], a[4:7], v[168:171], 0\n"
      "v_cvt_pk_bf16_f32 v216, v216, v216\n"
      "v_mfma_f32_16x16x32_bf16 v[160:163], a[40:43], v[172:175], v[160:163]\n"
      "ds_write_b16 v135, v216 offset:16\n"
      "s_branch L_join_21_%=\n"
      "L_flg_114_%=:\n"
      "v_cmp_gt_i32 vcc, 0, v200\n"
      "s_cbranch_vccz L_fb_115_%=\n"
      "s_sleep 1\n"
      "ds_read_b128 v[200:203], v128 offset:2048\n"
      "s_waitcnt lgkmcnt(0)\n"
      "v_bitop3_b32 v200, v200, v201, s4 bitop3:0x7e\n"
      "v_bitop3_b32 v202, v202, v203, s4 bitop3:0x7e\n"
      "v_or_b32 v200, v200, v202\n"
      "v_cmp_ne_u32 s[48:49], 0, v200\n"
      "s_branch L_flg_114_%=\n"
      "L_gen_28_%=:\n"
      "s_waitcnt lgkmcnt(0)\n"
      "s_nop 5\n"
      "s_cmp_eq_u64 s[48:49], 0\n"
      "s_cbranch_scc1 L_go_135_%=\n"
      "s_mov_b32 m0, s27\n"
      "s_nop 0\n"
      "s_movrels_b32 s9, s80\n"
      "s_lshr_b32 s8, s9, 8\n"
      "s_and_b32 s9, s9, 0xff\n"
      "s_mul_i32 s10, s8, 73\n"
      "s_add_u32 s10, s10, s9\n"
      "s_mul_i32 s30, s10, 4802\n"
      "s_add_u32 s30, s30, s18\n"
      "s_and_b32 s29, s30, 15\n"
      "s_add_u32 s30, s29, 110592\n"
      "s_lshl_b64 s[34:35], s[48:49], 4\n"
      "s_lshr_b64 s[34:35], s[34:35], 5\n"
      "s_cbranch_scc1 L_gen_29_%=\n"
      "v_mbcnt_lo_u32_b32 v242, -1, 0\n"
      "v_mbcnt_hi_u32_b32 v242, -1, v242\n"
      "v_lshlrev_b32_e64 v243, 1, v242\n"
      "v_cmp_gt_u32 vcc, s29, v243\n"
      "v_add_u32 v204, 4802, v243\n"
      "s_nop 0\n"
      "v_cndmask_b32 v243, v204, v243, vcc\n"
      "v_mov_b32_e64 v204, 0\n"
      "v_add_u32 v243, 110592, v243\n"
      "s_mov_b64 exec, 0x7f\n"
      "ds_write_b16 v243, v204\n"
      "s_mov_b64 exec, -1\n"
      "v_lshlrev_b32_e64 v243, 4, v242\n"
      "v_add_u32 v243, 110592, v243\n"
      "v_min_u32 v242, 111296, v243\n"
      "ds_read_b128 v[204:207], v243\n"
      "ds_read_b128 v[188:191], v243 offset:2048\n"
      "ds_read_b128 v[244:247], v243 offset:1024\n"
      "ds_read_b128 v[228:231], v242 offset:4096\n"
      "ds_read_b128 v[196:199], v243 offset:3072\n"
      "s_waitcnt lgkmcnt(4)\n"
      "v_or3_b32 v204, v204, v205, v206\n"
      "s_waitcnt lgkmcnt(2)\n"
      "v_or3_b32 v204, v204, v207, v244\n"
      "v_or3_b32 v204, v204, v245, v246\n"
      "v_or3_b32 v204, v204, v247, v188\n"
      "s_waitcnt lgkmcnt(0)\n"
      "v_or3_b32 v204, v204, v189, v190\n"
      "v_or3_b32 v204, v204, v191, v196\n"
      "v_or3_b32 v204, v204, v197, v198\n"
      "v_or3_b32 v204, v204, v199, v228\n"
      "v_or3_b32 v204, v204, v229, v230\n"
      "v_bitop3_b32 v204, v204, v231, s40 bitop3:0xa8\n"
      "v_cmp_ne_u32 s[48:49], 0, v204\n"
      "s_cmp_lg_u64 s[48:49], 0\n"
      "s_cbranch_scc1 L_gen_29_%=\n"
      "s_cmp_eq_u64 s[50:51], 0\n"
      "s_cbranch_scc1 L_fast_31_%=\n"
      "L_go_135_%=:\n"
      "v_mfma_f32_16x16x32_bf16 v[204:207], v[164:167], v[176:179], v[90:93]\n"
      "ds_read_b128 v[244:247], v128 offset:3072\n"
      "v_max3_f32 v242, v212, v213, v214\n"
      "v_mfma_f32_16x16x32_bf16 v[204:207], v[34:37], v[38:41], v[204:207]\n"
      "v_max3_f32 v242, v242, v146, v147\n"
      "v_max3_f32 v243, v215, v144, v145\n"
      "v_mfma_f32_16x16x32_bf16 v[188:191], v[168:171], v[176:179], v[94:97]\n"
      "v_max3_f32 v243, v243, v200, v201\n"
      "v_max3_f32 v242, v242, v202, v203\n"
      "v_mfma_f32_16x16x32_bf16 v[188:191], v[40:43], v[38:41], v[188:191]\n"
      "v_max3_f32 v242, v242, v243, v240\n"
      "v_mov_b32 v243, v242\n"
      "v_mfma_f32_16x16x32_bf16 v[196:199], v[192:195], v[176:179], v[72:75]\n"
      "s_nop 0\n"
      "v_permlane16_swap_b32 v242, v243\n"
      "v_mfma_f32_16x16x32_bf16 v[196:199], v[140:143], v[168:171], v[196:199]\n"
      "v_max_f32 v242, v242, v243\n"
      "v_mov_b32 v243, v242\n"
      "v_mfma_f32_16x16x32_bf16 v[196:199], v[148:151], v[38:41], v[196:199]\n"
      "s_nop 0\n"
      "v_permlane32_swap_b32 v242, v243\n"
      "v_mfma_f32_16x16x32_bf16 v[228:231], v[248:251], v[176:179], v[98:101]\n"
      "v_max_f32 v242, v242, v243\n"
      "v_sub_f32 v212, v212, v242\n"
      "v_mfma_f32_16x16x32_bf16 v[228:231], v[46:49], v[38:41], v[228:231]\n"
      "v_sub_f32 v146, v146, v242\n"
      "v_sub_f32 v214, v214, v242\n"
      "v_sub_f32 v200, v200, v242\n"
      "v_exp_f32_e64 v212, v212\n"
      "v_mfma_f32_16x16x32_bf16 v[136:139], v[164:167], v[184:187], v[102:105]\n"
      "v_sub_f32 v144, v144, v242\n"
      "v_sub_f32 v215, v215, v242\n"
      "v_mfma_f32_16x16x32_bf16 v[136:139], v[34:37], v[44:47], v[136:139]\n"
      "v_exp_f32_e64 v215, v215\n"
      "v_mfma_f32_16x16x32_bf16 v[176:179], v[168:171], v[184:187], v[106:109]\n"
      "ds_read_b128 v[168:171], v119 offset:30720\n"
      "v_sub_f32 v147, v147, v242\n"
      "v_mfma_f32_16x16x32_bf16 v[176:179], v[40:43], v[44:47], v[176:179]\n"
      "s_waitcnt lgkmcnt(1)\n"
      "v_sub_f32 v202, v202, v242\n"
      "v_sub_f32 v213, v213, v242\n"
      "v_exp_f32 v144, v144\n"
      "v_exp_f32 v213, v213\n"
      "v_exp_f32 v214, v214\n"
      "v_exp_f32 v202, v202\n"
      "v_exp_f32 v146, v146\n"
      "v_sub_f32 v203, v203, v242\n"
      "v_sub_f32 v201, v201, v242\n"
      "v_exp_f32 v203, v203\n"
      "v_cvt_pk_bf16_f32 v212, v212, v213\n"
      "v_sub_f32 v145, v145, v242\n"
      "v_cvt_pk_bf16_f32 v213, v214, v215\n"
      "v_sub_f32 v240, v240, v242\n"
      "v_exp_f32 v147, v147\n"
      "v_exp_f32 v145, v145\n"
      "v_exp_f32_e64 v201, v201\n"
      "v_cvt_pk_bf16_f32 v214, v144, v145\n"
      "v_exp_f32_e64 v200, v200\n"
      "v_cvt_pk_bf16_f32 v63, v202, v203\n"
      "v_exp_f32 v240, v240\n"
      "v_cvt_pk_bf16_f32 v62, v200, v201\n"
      "v_cvt_pk_bf16_f32 v215, v146, v147\n"
      "v_bitop3_b32 v246, v246, v247, s4 bitop3:0x7e\n"
      "v_cvt_pk_bf16_f32 v64, v240, 0\n"
      "v_bitop3_b32 v244, v244, v245, s4 bitop3:0x7e\n"
      "v_mfma_f32_16x16x32_bf16 v[200:203], v[66:69], v[212:215], 0\n"
      "s_waitcnt lgkmcnt(0)\n"
      "v_or_b32 v244, v244, v246\n"
      "v_cmp_ne_u32 vcc, 0, v244\n"
      "v_mfma_f32_16x16x32_bf16 v[200:203], v[66:69], v[62:65], v[200:203]\n"
      "global_store_dwordx4 v116, v[168:171], s[54:55] offset:256 sc1\n"
      "v_mfma_f32_16x16x32_bf16 v[144:147], v[220:223], v[212:215], 0\n"
      "s_mov_b64 s[48:49], vcc\n"
      "v_max3_f32 v199, v207, v188, v189\n"
      "v_max3_f32 v198, v204, v205, v206\n"
      "v_mfma_f32_16x16x32_bf16 v[144:147], v[50:53], v[62:65], v[144:147]\n"
      "s_cbranch_vccnz L_flg_136_%=\n"
      "L_fb_137_%=:\n"
      "v_max3_f32 v198, v198, v190, v191\n"
      "v_mfma_f32_16x16x32_bf16 v[244:247], v[236:239], v[212:215], 0\n"
      "v_max3_f32 v198, v198, v230, v231\n"
      "v_max3_f32 v199, v199, v228, v229\n"
      "v_mfma_f32_16x16x32_bf16 v[244:247], v[54:57], v[62:65], v[244:247]\n"
      "v_max3_f32 v198, v198, v199, v196\n"
      "v_mov_b32 v199, v198\n"
      "v_mfma_f32_16x16x32_bf16 v[168:171], v[252:255], v[212:215], 0\n"
      "s_nop 0\n"
      "v_permlane16_swap_b32 v198, v199\n"
      "v_mfma_f32_16x16x32_bf16 v[168:171], v[58:61], v[62:65], v[168:171]\n"
      "v_max_f32 v198, v198, v199\n"
      "v_mov_b32 v199, v198\n"
      "v_mfma_f32_16x16x32_bf16 v[212:215], v[192:195], v[184:187], v[74:77]\n"
      "s_nop 0\n"
      "v_permlane32_swap_b32 v198, v199\n"
      "v_mfma_f32_16x16x32_bf16 v[212:215], v[140:143], v[248:251], v[212:215]\n"
      "v_max_f32 v198, v198, v199\n"
      "v_sub_f32 v204, v204, v198\n"
      "v_mfma_f32_16x16x32_bf16 v[212:215], v[148:151], v[44:47], v[212:215]\n"
      "v_sub_f32 v206, v206, v198\n"
      "v_sub_f32 v190, v190, v198\n"
      "v_sub_f32 v231, v231, v198\n"
      "v_sub_f32 v205, v205, v198\n"
      "v_exp_f32 v205, v205\n"
      "v_sub_f32 v191, v191, v198\n"
      "v_sub_f32 v196, v196, v198\n"
      "v_sub_f32 v228, v228, v198\n"
      "v_sub_f32 v189, v189, v198\n"
      "v_exp_f32 v189, v189\n"
      "v_sub_f32 v230, v230, v198\n"
      "v_exp_f32 v231, v231\n"
      "v_exp_f32 v206, v206\n"
      "v_mfma_f32_16x16x32_bf16 v[214:217], v[248:251], v[184:187], v[110:113]\n"
      "v_exp_f32_e64 v204, v204\n"
      "v_mfma_f32_16x16x32_bf16 v[214:217], v[46:49], v[44:47], v[214:217]\n"
      "v_exp_f32 v196, v196\n"
      "v_sub_f32 v207, v207, v198\n"
      "v_exp_f32 v228, v228\n"
      "v_cvt_pk_bf16_f32 v204, v204, v205\n"
      "v_exp_f32 v207, v207\n"
      "v_sub_f32 v229, v229, v198\n"
      "v_cvt_pk_bf16_f32 v205, v206, v207\n"
      "v_max3_f32 v201, v136, v137, v138\n"
      "v_sub_f32 v188, v188, v198\n"
      "v_exp_f32_e64 v230, v230\n"
      "v_mfma_f32_16x16x32_bf16 v[184:187], v[140:143], v[192:195], v[76:79]\n"
      "ds_read_b128 v[248:251], v120 offset:18432\n"
      "v_exp_f32 v229, v229\n"
      "v_exp_f32 v191, v191\n"
      "v_exp_f32 v190, v190\n"
      "v_exp_f32 v188, v188\n"
      "v_cvt_pk_bf16_f32 v62, v228, v229\n"
      "v_max3_f32 v201, v201, v178, v179\n"
      "v_cvt_pk_bf16_f32 v206, v188, v189\n"
      "v_cvt_pk_bf16_f32 v63, v230, v231\n"
      "v_cvt_pk_bf16_f32 v64, v196, 0\n"
      "v_cvt_pk_bf16_f32 v207, v190, v191\n"
      "v_mfma_f32_16x16x16_bf16 v[184:187], v[150:151], v[148:149], v[184:187]\n"
      "v_rcp_f32_e64 v200, v200\n"
      "v_mfma_f32_16x16x32_bf16 v[192:195], v[66:69], v[204:207], 0\n"
      "v_mul_f32 v144, v200, v144\n"
      "v_mul_f32 v145, v200, v145\n"
      "v_mfma_f32_16x16x32_bf16 v[192:195], v[66:69], v[62:65], v[192:195]\n"
      "v_mul_f32_e64 v146, v200, v146\n"
      "v_cvt_pk_bf16_f32 v144, v144, v145\n"
      "v_mfma_f32_16x16x32_bf16 v[188:191], v[220:223], v[204:207], 0\n"
      "v_mul_f32_e64 v147, v200, v147\n"
      "v_cvt_pk_bf16_f32 v145, v146, v147\n"
      "v_mfma_f32_16x16x32_bf16 v[188:191], v[50:53], v[62:65], v[188:191]\n"
      "v_mul_f32 v170, v200, v170\n"
      "v_mul_f32 v171, v200, v171\n"
      "v_mul_f32 v169, v200, v169\n"
      "v_mul_f32 v168, v200, v168\n"
      "v_cvt_pk_bf16_f32 v168, v168, v169\n"
      "v_mul_f32 v245, v200, v245\n"
      "v_mul_f32 v244, v200, v244\n"
      "v_cvt_pk_bf16_f32 v146, v244, v245\n"
      "v_max3_f32 v201, v201, v216, v217\n"
      "v_mfma_f32_16x16x32_bf16 v[228:231], v[236:239], v[204:207], 0\n"
      "s_waitcnt lgkmcnt(0)\n"
      "v_mul_f32 v246, v200, v246\n"
      "v_mul_f32_e64 v247, v200, v247\n"
      "v_mfma_f32_16x16x32_bf16 v[228:231], v[54:57], v[62:65], v[228:231]\n"
      "global_store_dwordx4 v117, v[248:251], s[54:55] sc1\n"
      "v_mfma_f32_16x16x32_bf16 v[242:245], v[252:255], v[204:207], 0\n"
      "ds_read_b128 v[140:143], v123 offset:19456\n"
      "v_cvt_pk_bf16_f32 v147, v246, v247\n"
      "v_mfma_f32_16x16x32_bf16 v[242:245], v[58:61], v[62:65], v[242:245]\n"
      "ds_write_b128 v124, v[144:147]\n"
      "v_max3_f32 v240, v139, v176, v177\n"
      "v_max3_f32 v240, v240, v214, v215\n"
      "v_max3_f32 v201, v201, v240, v212\n"
      "v_mov_b32 v240, v201\n"
      "v_rcp_f32 v192, v192\n"
      "v_cvt_pk_bf16_f32 v169, v170, v171\n"
      "v_permlane16_swap_b32 v201, v240\n"
      "v_max_f32 v201, v201, v240\n"
      "v_mov_b32 v240, v201\n"
      "v_mul_f32 v188, v192, v188\n"
      "v_mul_f32 v189, v192, v189\n"
      "v_permlane32_swap_b32 v201, v240\n"
      "v_max_f32 v201, v201, v240\n"
      "v_sub_f32 v136, v136, v201\n"
      "v_sub_f32 v177, v177, v201\n"
      "v_sub_f32 v138, v138, v201\n"
      "v_sub_f32 v216, v216, v201\n"
      "v_sub_f32 v137, v137, v201\n"
      "v_sub_f32 v212, v212, v201\n"
      "v_exp_f32 v137, v137\n"
      "v_sub_f32 v176, v176, v201\n"
      "v_exp_f32 v136, v136\n"
      "v_exp_f32 v176, v176\n"
      "v_sub_f32 v179, v179, v201\n"
      "v_sub_f32 v217, v217, v201\n"
      "v_sub_f32 v139, v139, v201\n"
      "v_cvt_pk_bf16_f32 v136, v136, v137\n"
      "v_exp_f32 v179, v179\n"
      "v_exp_f32 v139, v139\n"
      "v_sub_f32 v214, v214, v201\n"
      "v_exp_f32 v212, v212\n"
      "v_exp_f32 v138, v138\n"
      "v_sub_f32 v215, v215, v201\n"
      "v_cvt_pk_bf16_f32 v137, v138, v139\n"
      "v_exp_f32 v216, v216\n"
      "v_cvt_pk_bf16_f32 v64, v212, 0\n"
      "v_sub_f32 v178, v178, v201\n"
      "v_exp_f32 v178, v178\n"
      "v_exp_f32 v217, v217\n"
      "v_exp_f32 v177, v177\n"
      "v_cvt_pk_bf16_f32 v188, v188, v189\n"
      "v_cvt_pk_bf16_f32 v138, v176, v177\n"
      "v_exp_f32 v214, v214\n"
      "v_exp_f32_e64 v215, v215\n"
      "v_cvt_pk_bf16_f32 v139, v178, v179\n"
      "v_mul_f32_e64 v190, v192, v190\n"
      "v_cvt_pk_bf16_f32 v63, v216, v217\n"
      "v_cvt_pk_bf16_f32 v62, v214, v215\n"
      "v_mfma_f32_16x16x32_bf16 v[148:151], v[66:69], v[136:139], 0\n"
      "ds_read_b128 v[214:217], v123 offset:22528\n"
      "v_mul_f32_e64 v191, v192, v191\n"
      "v_mfma_f32_16x16x32_bf16 v[148:151], v[66:69], v[62:65], v[148:151]\n"
      "ds_read_b128 v[170:173], v123 offset:25600\n"
      "v_cvt_pk_bf16_f32 v189, v190, v191\n"
      "v_mfma_f32_16x16x32_bf16 v[248:251], v[220:223], v[136:139], 0\n"
      "ds_read_b128 v[144:147], v121 offset:18432\n"
      "v_mul_f32 v228, v192, v228\n"
      "v_mfma_f32_16x16x32_bf16 v[248:251], v[50:53], v[62:65], v[248:251]\n"
      "ds_read_b128 v[164:167], v123 offset:28672\n"
      "v_mul_f32 v242, v192, v242\n"
      "v_mfma_f32_16x16x32_bf16 v[204:207], v[252:255], v[136:139], 0\n"
      "v_mul_f32 v231, v192, v231\n"
      "v_mul_f32 v230, v192, v230\n"
      "v_mfma_f32_16x16x32_bf16 v[204:207], v[58:61], v[62:65], v[204:207]\n"
      "v_mul_f32 v245, v192, v245\n"
      "v_cndmask_b32 v241, v241, v197, s[42:43]\n"
      "v_mul_f32 v243, v192, v243\n"
      "v_mul_f32_e64 v229, v192, v229\n"
      "v_mul_f32_e64 v244, v192, v244\n"
      "v_cndmask_b32 v241, v241, v213, s[44:45]\n"
      "v_cvt_pk_bf16_f32 v191, v230, v231\n"
      "v_cndmask_b32 v241, v241, v185, s[46:47]\n"
      "v_cvt_pk_bf16_f32 v190, v228, v229\n"
      "v_cvt_pk_bf16_f32 v242, v242, v243\n"
      "v_cvt_pk_bf16_f32 v243, v244, v245\n"
      "s_waitcnt lgkmcnt(3)\n"
      "v_mfma_f32_16x16x32_bf16 v[176:179], a[216:219], v[140:143], 0\n"
      "ds_read_b128 v[184:187], v123 offset:31744\n"
      "v_max_f32_dpp v240, v241, v241 row_ror:8 row_mask:0xf bank_mask:0xf\n"
      "v_mfma_f32_16x16x32_bf16 v[176:179], a[228:231], v[214:217], v[176:179]\n"
      "ds_write_b128 v124, v[188:191] offset:1024\n"
      "s_waitcnt lgkmcnt(2)\n"
      "v_mfma_f32_16x16x32_bf16 v[244:247], a[220:223], v[140:143], 0\n"
      "ds_read_b128 v[188:191], v123 offset:34816\n"
      "v_max_f32_dpp v240, v240, v240 row_ror:4 row_mask:0xf bank_mask:0xf\n"
      "v_mfma_f32_16x16x32_bf16 v[244:247], a[232:235], v[214:217], v[244:247]\n"
      "ds_write2st64_b64 v125, v[168:169], v[242:243] offset0:0 offset1:2\n"
      "v_mfma_f32_16x16x32_bf16 v[200:203], a[224:227], v[140:143], 0\n"
      "ds_read_b128 v[228:231], v123 offset:20480\n"
      "v_max_f32_dpp v240, v240, v240 row_ror:2 row_mask:0xf bank_mask:0xf\n"
      "v_mfma_f32_16x16x32_bf16 v[200:203], a[236:239], v[214:217], v[200:203]\n"
      "ds_read_b128 v[196:199], v123 offset:23552\n"
      "v_max_f32_dpp v240, v240, v240 row_ror:1 row_mask:0xf bank_mask:0xf\n"
      "v_mfma_f32_16x16x32_bf16 v[176:179], a[240:243], v[170:173], v[176:179]\n"
      "ds_read_b128 v[140:143], v123 offset:26624\n"
      "v_max_f32_dpp v240, v240, v240 row_bcast:15 row_mask:0xa bank_mask:0xf\n"
      "v_mfma_f32_16x16x32_bf16 v[176:179], a[252:255], v[164:167], v[176:179]\n"
      "s_nop 0\n"
      "v_max_f32_dpp v240, v240, v240 row_bcast:31 row_mask:0xc bank_mask:0xf\n"
      "v_mfma_f32_16x16x32_bf16 v[244:247], a[244:247], v[170:173], v[244:247]\n"
      "s_nop 0\n"
      "v_readlane_b32 s31, v240, 63\n"
      "v_mfma_f32_16x16x32_bf16 v[244:247], v[0:3], v[164:167], v[244:247]\n"
      "s_mov_b32 exec_hi, 0\n"
      "global_store_dwordx4 v118, v[144:147], s[54:55] sc1\n"
      "s_mov_b32 exec_hi, -1\n"
      "v_mfma_f32_16x16x32_bf16 v[200:203], a[248:251], v[170:173], v[200:203]\n"
      "ds_read_b128 v[144:147], v123 offset:29696\n"
      "v_subrev_f32 v241, s31, v241\n"
      "v_mfma_f32_16x16x32_bf16 v[200:203], v[4:7], v[164:167], v[200:203]\n"
      "v_exp_f32 v241, v241\n"
      "s_waitcnt lgkmcnt(3)\n"
      "v_mfma_f32_16x16x32_bf16 v[176:179], v[8:11], v[184:187], v[176:179]\n"
      "v_cvt_pk_bf16_f32 v241, v241, v241\n"
      "v_mfma_f32_16x16x32_bf16 v[176:179], v[20:23], v[188:191], v[176:179]\n"
      "ds_write_b16 v130, v241\n"
      "v_mfma_f32_16x16x32_bf16 v[244:247], v[12:15], v[184:187], v[244:247]\n"
      "s_waitcnt lgkmcnt(2)\n"
      "s_add_u32 s28, s27, 2\n"
      "s_min_u32 m0, s28, s3\n"
      "v_mfma_f32_16x16x32_bf16 v[244:247], v[24:27], v[188:191], v[244:247]\n"
      "s_movrels_b32 s9, s80\n"
      "ds_read_b128 v[164:167], v123 offset:32768\n"
      "v_fma_f32 v156, v176, v176, s39\n"
      "v_mfma_f32_16x16x32_bf16 v[240:243], v[236:239], v[136:139], 0\n"
      "s_lshr_b32 s8, s9, 8\n"
      "ds_read_b128 v[136:139], v131\n"
      "v_mfma_f32_16x16x32_bf16 v[240:243], v[54:57], v[62:65], v[240:243]\n"
      "s_and_b32 s9, s9, 0xff\n"
      "v_fmac_f32 v156, v177, v177\n"
      "v_fmac_f32 v156, v178, v178\n"
      "v_mfma_f32_16x16x32_bf16 v[200:203], v[16:19], v[184:187], v[200:203]\n"
      "ds_read_b128 v[172:175], v131 offset:64\n"
      "v_fmac_f32 v156, v179, v179\n"
      "v_mfma_f32_16x16x32_bf16 v[200:203], v[28:31], v[188:191], v[200:203]\n"
      "s_waitcnt lgkmcnt(4)\n"
      "v_fmac_f32_e64 v156, v244, v244\n"
      "v_fmac_f32_e64 v156, v245, v245\n"
      "v_mfma_f32_16x16x32_bf16 v[184:187], a[216:219], v[228:231], 0\n"
      "ds_read_b128 v[168:171], v114 offset:18432\n"
      "v_fmac_f32_e64 v156, v246, v246\n"
      "v_mfma_f32_16x16x32_bf16 v[184:187], a[228:231], v[196:199], v[184:187]\n"
      "ds_read_b128 v[192:195], v115 offset:18432\n"
      "v_fmac_f32_e64 v156, v247, v247\n"
      "v_mfma_f32_16x16x32_bf16 v[212:215], a[220:223], v[228:231], 0\n"
      "s_mul_i32 s10, s8, 73\n"
      "v_fmac_f32_e64 v156, v200, v200\n"
      "v_fmac_f32_e64 v156, v201, v201\n"
      "v_mfma_f32_16x16x32_bf16 v[212:215], a[232:235], v[196:199], v[212:215]\n"
      "s_add_u32 s10, s10, s9\n"
      "v_fmac_f32 v156, v202, v202\n"
      "v_fmac_f32_e64 v156, v203, v203\n"
      "v_mfma_f32_16x16x32_bf16 v[188:191], a[224:227], v[228:231], 0\n"
      "s_mul_i32 s28, s10, 4802\n"
      "ds_read_b128 v[228:231], v123 offset:35840\n"
      "v_mfma_f32_16x16x32_bf16 v[188:191], a[236:239], v[196:199], v[188:191]\n"
      "s_add_u32 s56, s18, s28\n"
      "v_permlane16_swap_b32 v159, v156\n"
      "v_mfma_f32_16x16x32_bf16 v[184:187], a[240:243], v[140:143], v[184:187]\n"
      "s_addc_u32 s57, s19, 0\n"
      "s_and_b32 s56, s56, -16\n"
      "v_mfma_f32_16x16x32_bf16 v[184:187], a[252:255], v[144:147], v[184:187]\n"
      "v_mfma_f32_16x16x32_bf16 v[212:215], a[244:247], v[140:143], v[212:215]\n"
      "s_sub_u32 s58, s56, 110592\n"
      "s_subb_u32 s59, s57, 0\n"
      "s_sub_u32 s56, s56, 73728\n"
      "v_mfma_f32_16x16x32_bf16 v[212:215], v[0:3], v[144:147], v[212:215]\n"
      "s_waitcnt lgkmcnt(1)\n"
      "s_subb_u32 s57, s57, 0\n"
      "v_mfma_f32_16x16x32_bf16 v[188:191], a[248:251], v[140:143], v[188:191]\n"
      "s_mul_i32 s28, s8, 1373568\n"
      "s_mul_i32 s29, s9, 2688\n"
      "s_add_u32 s28, s28, s29\n"
      "v_mfma_f32_16x16x32_bf16 v[188:191], v[4:7], v[144:147], v[188:191]\n"
      "s_cmp_eq_u32 s8, 72\n"
      "v_mfma_f32_16x16x32_bf16 v[144:147], v[136:139], v[66:69], 0\n"
      "s_cselect_b32 s8, 0, 100270464\n"
      "s_cmp_eq_u32 s9, 72\n"
      "v_add_f32 v159, v159, v156\n"
      "v_mfma_f32_16x16x32_bf16 v[144:147], v[172:175], v[66:69], v[144:147]\n"
      "s_cselect_b32 s9, 0, 196224\n"
      "s_add_u32 s10, s8, s9\n"
      "s_waitcnt lgkmcnt(0)\n"
      "v_mfma_f32_16x16x32_bf16 v[184:187], v[8:11], v[164:167], v[184:187]\n"
      "s_add_u32 s28, s28, s10\n"
      "s_add_u32 s52, s16, s28\n"
      "s_addc_u32 s53, s17, 0\n"
      "v_mfma_f32_16x16x32_bf16 v[184:187], v[20:23], v[228:231], v[184:187]\n"
      "s_add_u32 s27, s27, 1\n"
      "v_mov_b32_dpp v144, v144 quad_perm:[0,0,0,0] row_mask:0xf bank_mask:0xf\n"
      "v_rcp_f32 v144, v144\n"
      "v_mfma_f32_16x16x32_bf16 v[212:215], v[12:15], v[164:167], v[212:215]\n"
      "s_cmp_ge_u32 s27, s26\n"
      "s_cselect_b32 s28, 1, 0\n"
      "s_xor_b32 s29, s10, s11\n"
      "v_mfma_f32_16x16x32_bf16 v[212:215], v[24:27], v[228:231], v[212:215]\n"
      "s_or_b32 s28, s28, s29\n"
      "v_fma_f32 v145, v184, v184, s39\n"
      "v_fmac_f32_e64 v145, v185, v185\n"
      "v_mfma_f32_16x16x32_bf16 v[188:191], v[16:19], v[164:167], v[188:191]\n"
      "v_fmac_f32 v145, v186, v186\n"
      "v_fmac_f32 v145, v187, v187\n"
      "v_mfma_f32_16x16x32_bf16 v[188:191], v[28:31], v[228:231], v[188:191]\n"
      "s_nop 0\n"
      "v_fmac_f32 v145, v212, v212\n"
      "v_fmac_f32 v145, v213, v213\n"
      "v_mfma_f32_16x16x32_bf16 v[226:229], v[136:139], v[220:223], 0\n"
      "v_fmac_f32 v145, v214, v214\n"
      "v_fmac_f32 v145, v215, v215\n"
      "v_mfma_f32_16x16x32_bf16 v[226:229], v[172:175], v[50:53], v[226:229]\n"
      "s_nop 0\n"
      "v_fmac_f32 v145, v188, v188\n"
      "v_mfma_f32_16x16x32_bf16 v[220:223], v[136:139], v[236:239], 0\n"
      "v_fmac_f32 v145, v189, v189\n"
      "v_fmac_f32 v145, v190, v190\n"
      "v_mfma_f32_16x16x32_bf16 v[220:223], v[172:175], v[54:57], v[220:223]\n"
      "v_fmac_f32 v145, v191, v191\n"
      "v_mov_b32 v230, v145\n"
      "v_mfma_f32_16x16x32_bf16 v[236:239], v[136:139], v[252:255], 0\n"
      "s_nop 0\n"
      "v_permlane16_swap_b32_e64 v145, v230\n"
      "v_mfma_f32_16x16x32_bf16 v[236:239], v[172:175], v[58:61], v[236:239]\n"
      "v_add_f32 v145, v145, v230\n"
      "v_mfma_f32_16x16x32_bf16 v[196:199], a[0:3], v[168:171], 0\n"
      "s_nop 0\n"
      "v_permlane32_swap_b32_e64 v159, v145\n"
      "v_cndmask_b32 v226, v226, v220, s[42:43]\n"
      "v_add_f32 v159, v159, v145\n"
      "v_mfma_f32_16x16x32_bf16 v[228:231], a[4:7], v[168:171], 0\n"
      "s_nop 0\n"
      "v_cndmask_b32 v226, v226, v236, s[44:45]\n"
      "v_mul_f32_e64 v226, v144, v226\n"
      "v_mfma_f32_16x16x32_bf16 v[228:231], a[40:43], v[192:195], v[228:231]\n"
      "ds_write_b32 v129, v159\n"
      "v_cvt_pk_bf16_f32 v226, v226, v226\n"
      "v_mfma_f32_16x16x32_bf16 v[196:199], a[36:39], v[192:195], v[196:199]\n"
      "ds_write_b16 v135, v226\n"
      "s_branch L_join_30_%=\n"
      "L_flg_136_%=:\n"
      "v_cmp_gt_i32 vcc, 0, v244\n"
      "s_cbranch_vccz L_fb_137_%=\n"
      "s_sleep 1\n"
      "ds_read_b128 v[244:247], v128 offset:3072\n"
      "s_waitcnt lgkmcnt(0)\n"
      "v_bitop3_b32 v244, v244, v245, s4 bitop3:0x7e\n"
      "v_bitop3_b32 v246, v246, v247, s4 bitop3:0x7e\n"
      "v_or_b32 v244, v244, v246\n"
      "v_cmp_ne_u32 s[48:49], 0, v244\n"
      "s_branch L_flg_136_%=\n"
      "L_gen_29_%=:\n"
      "s_nop 0\n"
      "s_nop 0\n"
      "v_mbcnt_lo_u32_b32 v242, -1, 0\n"
      "v_mbcnt_hi_u32_b32 v242, -1, v242\n"
      "v_and_b32 v243, 15, v242\n"
      "v_mul_u32_u24 v205, 0x62, v243\n"
      "v_lshrrev_b32 v204, 4, v242\n"
      "v_lshl_add_u32 v205, v204, 3, v205\n"
      "v_lshlrev_b32 v204, 2, v204\n"
      "v_sub_u32 v243, v243, v204\n"
      "v_add_u32 v205, s30, v205\n"
      "v_and_b32 v204, 1, v243\n"
      "v_and_b32 v242, -4, v205\n"
      "v_and_b32 v205, 2, v205\n"
      "v_lshrrev_b32_e64 v243, 1, v243\n"
      "s_movk_i32 s31, 0x3fb9\n"
      "ds_read2_b32 v[206:207], v242 offset0:0 offset1:1\n"
      "ds_read2_b32 v[188:189], v242 offset0:2 offset1:10\n"
      "ds_read2_b32 v[190:191], v242 offset0:18 offset1:24\n"
      "ds_read2_b32 v[244:245], v242 offset0:8 offset1:9\n"
      "ds_read2_b32 v[246:247], v242 offset0:16 offset1:17\n"
      "v_lshlrev_b32 v204, 4, v204\n"
      "v_cmp_eq_u32 vcc, 0, v243\n"
      "v_add_u32 v242, 1568, v242\n"
      "v_mfma_f32_16x16x32_bf16 v[224:227], v[192:195], v[176:179], v[72:75]\n"
      "ds_read2_b32 v[230:231], v242 offset0:2 offset1:10\n"
      "ds_read2_b32 v[198:199], v242 offset0:8 offset1:9\n"
      "ds_read2_b32 v[156:157], v242 offset0:18 offset1:24\n"
      "ds_read2_b32 v[196:197], v242 offset0:0 offset1:1\n"
      "ds_read2_b32 v[228:229], v242 offset0:16 offset1:17\n"
      "v_lshlrev_b32 v204, v204, s31\n"
      "v_add_u32 v242, 1568, v242\n"
      "v_mfma_f32_16x16x32_bf16 v[208:211], v[164:167], v[176:179], v[90:93]\n"
      "ds_read2_b32 v[136:137], v242 offset0:0 offset1:1\n"
      "v_cndmask_b32 v174, 0, v204, vcc\n"
      "v_mfma_f32_16x16x32_bf16 v[208:211], v[34:37], v[38:41], v[208:211]\n"
      "ds_read2_b32 v[162:163], v242 offset0:2 offset1:10\n"
      "ds_read2_b32 v[138:139], v242 offset0:8 offset1:9\n"
      "ds_read2_b32 v[172:173], v242 offset0:18 offset1:24\n"
      "ds_read2_b32 v[160:161], v242 offset0:16 offset1:17\n"
      "v_mfma_f32_16x16x32_bf16 v[224:227], v[140:143], v[168:171], v[224:227]\n"
      "s_waitcnt lgkmcnt(11)\n"
      "v_alignbyte_b32 v206, v207, v206, v205\n"
      "v_cmp_eq_u32 vcc, 1, v243\n"
      "s_waitcnt lgkmcnt(10)\n"
      "v_alignbyte_b32 v207, v188, v207, v205\n"
      "v_alignbyte_b32 v244, v245, v244, v205\n"
      "v_mfma_f32_16x16x32_bf16 v[216:219], v[168:171], v[176:179], v[94:97]\n"
      "s_waitcnt lgkmcnt(9)\n"
      "v_alignbyte_b32 v246, v247, v246, v205\n"
      "v_alignbyte_b32 v191, v191, v191, v205\n"
      "v_alignbyte_b32 v245, v189, v245, v205\n"
      "v_alignbyte_b32 v247, v190, v247, v205\n"
      "v_lshlrev_b32 v191, 16, v191\n"
      "v_cndmask_b32 v175, 0, v204, vcc\n"
      "v_mfma_f32_16x16x32_bf16 v[216:219], v[40:43], v[38:41], v[216:219]\n"
      "s_waitcnt lgkmcnt(6)\n"
      "v_alignbyte_b32 v196, v197, v196, v205\n"
      "v_alignbyte_b32 v198, v199, v198, v205\n"
      "v_mfma_f32_16x16x32_bf16 v[224:227], v[148:151], v[38:41], v[224:227]\n"
      "s_waitcnt lgkmcnt(5)\n"
      "v_fmac_f32 v240, s37, v191\n"
      "v_alignbyte_b32 v199, v231, v199, v205\n"
      "v_mfma_f32_16x16x32_bf16 v[188:191], v[248:251], v[176:179], v[98:101]\n"
      "v_alignbyte_b32 v228, v229, v228, v205\n"
      "v_alignbyte_b32 v157, v157, v157, v205\n"
      "v_mfma_f32_16x16x32_bf16 v[188:191], v[46:49], v[38:41], v[188:191]\n"
      "v_lshlrev_b32 v157, 16, v157\n"
      "v_alignbyte_b32 v229, v156, v229, v205\n"
      "v_mfma_f32_16x16x16_bf16 v[212:215], v[174:175], v[206:207], v[212:215]\n"
      "s_waitcnt lgkmcnt(0)\n"
      "v_alignbyte_b32 v136, v137, v136, v205\n"
      "v_alignbyte_b32 v137, v162, v137, v205\n"
      "v_mfma_f32_16x16x16_bf16 v[144:147], v[174:175], v[244:245], v[144:147]\n"
      "v_fmac_f32 v224, s37, v157\n"
      "v_alignbyte_b32 v197, v230, v197, v205\n"
      "v_mfma_f32_16x16x16_bf16 v[200:203], v[174:175], v[246:247], v[200:203]\n"
      "ds_read_b128 v[176:179], v128 offset:3072\n"
      "v_alignbyte_b32 v138, v139, v138, v205\n"
      "v_max3_f32 v243, v212, v213, v214\n"
      "v_mfma_f32_16x16x16_bf16 v[208:211], v[174:175], v[196:197], v[208:211]\n"
      "s_nop 0\n"
      "v_max3_f32 v206, v215, v144, v145\n"
      "v_alignbyte_b32 v139, v163, v139, v205\n"
      "v_mfma_f32_16x16x16_bf16 v[216:219], v[174:175], v[198:199], v[216:219]\n"
      "v_max3_f32 v206, v206, v200, v201\n"
      "v_max3_f32 v243, v243, v146, v147\n"
      "v_max3_f32 v243, v243, v202, v203\n"
      "v_max3_f32 v243, v243, v206, v240\n"
      "v_alignbyte_b32 v173, v173, v173, v205\n"
      "v_mov_b32 v206, v243\n"
      "v_alignbyte_b32 v160, v161, v160, v205\n"
      "v_alignbyte_b32 v161, v172, v161, v205\n"
      "v_permlane16_swap_b32 v243, v206\n"
      "v_max_f32 v243, v243, v206\n"
      "v_mov_b32 v206, v243\n"
      "v_lshlrev_b32 v173, 16, v173\n"
      "v_mfma_f32_16x16x16_bf16 v[188:191], v[174:175], v[228:229], v[188:191]\n"
      "v_permlane32_swap_b32 v243, v206\n"
      "v_max_f32 v243, v243, v206\n"
      "v_sub_f32 v144, v144, v243\n"
      "v_sub_f32 v147, v147, v243\n"
      "v_sub_f32 v202, v202, v243\n"
      "v_sub_f32 v212, v212, v243\n"
      "v_sub_f32 v213, v213, v243\n"
      "v_sub_f32 v240, v240, v243\n"
      "v_sub_f32 v215, v215, v243\n"
      "v_exp_f32 v240, v240\n"
      "v_sub_f32 v203, v203, v243\n"
      "v_exp_f32 v202, v202\n"
      "v_sub_f32 v214, v214, v243\n"
      "v_exp_f32 v215, v215\n"
      "v_exp_f32 v214, v214\n"
      "s_waitcnt lgkmcnt(0)\n"
      "v_exp_f32 v213, v213\n"
      "v_exp_f32 v147, v147\n"
      "v_exp_f32 v212, v212\n"
      "v_sub_f32 v200, v200, v243\n"
      "v_sub_f32 v145, v145, v243\n"
      "v_sub_f32 v146, v146, v243\n"
      "v_exp_f32 v144, v144\n"
      "v_exp_f32 v146, v146\n"
      "v_sub_f32 v201, v201, v243\n"
      "v_exp_f32_e64 v201, v201\n"
      "v_cvt_pk_bf16_f32 v212, v212, v213\n"
      "v_exp_f32_e64 v145, v145\n"
      "v_cvt_pk_bf16_f32 v213, v214, v215\n"
      "v_cvt_pk_bf16_f32 v215, v146, v147\n"
      "v_cvt_pk_bf16_f32 v214, v144, v145\n"
      "v_exp_f32 v203, v203\n"
      "v_exp_f32 v200, v200\n"
      "v_cvt_pk_bf16_f32 v63, v202, v203\n"
      "v_cvt_pk_bf16_f32 v64, v240, 0\n"
      "v_max3_f32 v240, v208, v209, v210\n"
      "v_max3_f32 v243, v211, v216, v217\n"
      "v_cvt_pk_bf16_f32 v62, v200, v201\n"
      "v_mfma_f32_16x16x32_bf16 v[204:207], v[66:69], v[212:215], 0\n"
      "v_bitop3_b32 v176, v176, v177, s4 bitop3:0x7e\n"
      "v_bitop3_b32 v178, v178, v179, s4 bitop3:0x7e\n"
      "v_mfma_f32_16x16x32_bf16 v[204:207], v[66:69], v[62:65], v[204:207]\n"
      "v_or_b32_e64 v176, v176, v178\n"
      "v_cmp_ne_u32 vcc, 0, v176\n"
      "v_mfma_f32_16x16x32_bf16 v[200:203], v[220:223], v[212:215], 0\n"
      "s_mov_b64 s[48:49], vcc\n"
      "v_max3_f32 v240, v240, v218, v219\n"
      "v_max3_f32 v243, v243, v188, v189\n"
      "v_mfma_f32_16x16x32_bf16 v[200:203], v[50:53], v[62:65], v[200:203]\n"
      "s_cbranch_vccnz L_flg_148_%=\n"
      "L_fb_149_%=:\n"
      "v_max3_f32 v240, v240, v190, v191\n"
      "v_mfma_f32_16x16x32_bf16 v[176:179], v[236:239], v[212:215], 0\n"
      "s_nop 0\n"
      "v_max3_f32 v240, v240, v243, v224\n"
      "v_mov_b32 v243, v240\n"
      "v_mfma_f32_16x16x32_bf16 v[176:179], v[54:57], v[62:65], v[176:179]\n"
      "s_nop 0\n"
      "v_permlane16_swap_b32 v240, v243\n"
      "v_max_f32 v240, v240, v243\n"
      "v_mov_b32 v243, v240\n"
      "v_rcp_f32 v204, v204\n"
      "s_nop 0\n"
      "v_permlane32_swap_b32 v240, v243\n"
      "v_max_f32 v240, v240, v243\n"
      "v_sub_f32 v211, v211, v240\n"
      "v_sub_f32 v209, v209, v240\n"
      "v_sub_f32 v189, v189, v240\n"
      "v_sub_f32 v210, v210, v240\n"
      "v_exp_f32 v210, v210\n"
      "v_exp_f32 v209, v209\n"
      "v_sub_f32 v224, v224, v240\n"
      "v_sub_f32 v188, v188, v240\n"
      "v_sub_f32 v218, v218, v240\n"
      "v_sub_f32 v208, v208, v240\n"
      "v_exp_f32 v208, v208\n"
      "v_sub_f32 v219, v219, v240\n"
      "v_exp_f32 v211, v211\n"
      "v_sub_f32 v217, v217, v240\n"
      "v_exp_f32 v218, v218\n"
      "v_sub_f32 v190, v190, v240\n"
      "v_sub_f32 v216, v216, v240\n"
      "v_exp_f32_e64 v216, v216\n"
      "v_cvt_pk_bf16_f32 v208, v208, v209\n"
      "v_exp_f32 v217, v217\n"
      "v_cvt_pk_bf16_f32 v209, v210, v211\n"
      "v_exp_f32 v188, v188\n"
      "v_cvt_pk_bf16_f32 v210, v216, v217\n"
      "v_exp_f32 v219, v219\n"
      "v_mfma_f32_16x16x32_bf16 v[244:247], v[252:255], v[212:215], 0\n"
      "ds_read_b128 v[212:215], v119 offset:30720\n"
      "v_mul_f32 v200, v204, v200\n"
      "v_mfma_f32_16x16x32_bf16 v[244:247], v[58:61], v[62:65], v[244:247]\n"
      "v_exp_f32 v189, v189\n"
      "v_mfma_f32_16x16x32_bf16 v[196:199], v[164:167], v[184:187], v[102:105]\n"
      "v_exp_f32 v190, v190\n"
      "v_mfma_f32_16x16x32_bf16 v[196:199], v[34:37], v[44:47], v[196:199]\n"
      "v_sub_f32 v191, v191, v240\n"
      "v_exp_f32 v191, v191\n"
      "v_mfma_f32_16x16x32_bf16 v[228:231], v[168:171], v[184:187], v[106:109]\n"
      "v_exp_f32_e64 v224, v224\n"
      "v_mfma_f32_16x16x32_bf16 v[228:231], v[40:43], v[44:47], v[228:231]\n"
      "v_cvt_pk_bf16_f32 v63, v190, v191\n"
      "v_cvt_pk_bf16_f32 v62, v188, v189\n"
      "v_mul_f32_e64 v201, v204, v201\n"
      "v_mfma_f32_16x16x16_bf16 v[196:199], v[174:175], v[136:137], v[196:199]\n"
      "v_cvt_pk_bf16_f32 v64, v224, 0\n"
      "v_cvt_pk_bf16_f32 v200, v200, v201\n"
      "v_mfma_f32_16x16x16_bf16 v[228:231], v[174:175], v[138:139], v[228:231]\n"
      "v_mul_f32 v202, v204, v202\n"
      "v_mul_f32 v203, v204, v203\n"
      "v_mfma_f32_16x16x32_bf16 v[188:191], v[192:195], v[184:187], v[74:77]\n"
      "s_waitcnt lgkmcnt(0)\n"
      "v_cvt_pk_bf16_f32 v201, v202, v203\n"
      "v_mul_f32 v176, v204, v176\n"
      "v_mfma_f32_16x16x32_bf16 v[188:191], v[140:143], v[248:251], v[188:191]\n"
      "global_store_dwordx4 v116, v[212:215], s[54:55] offset:256 sc1\n"
      "v_mfma_f32_16x16x32_bf16 v[188:191], v[148:151], v[44:47], v[188:191]\n"
      "v_cvt_pk_bf16_f32 v211, v218, v219\n"
      "v_mul_f32_e64 v177, v204, v177\n"
      "v_cvt_pk_bf16_f32 v202, v176, v177\n"
      "v_mfma_f32_16x16x32_bf16 v[212:215], v[248:251], v[184:187], v[110:113]\n"
      "v_mul_f32 v178, v204, v178\n"
      "v_mul_f32 v179, v204, v179\n"
      "v_mfma_f32_16x16x32_bf16 v[212:215], v[46:49], v[44:47], v[212:215]\n"
      "v_mul_f32_e64 v244, v204, v244\n"
      "v_cvt_pk_bf16_f32 v203, v178, v179\n"
      "v_mfma_f32_16x16x32_bf16 v[184:187], v[140:143], v[192:195], v[76:79]\n"
      "ds_write_b128 v124, v[200:203]\n"
      "v_mfma_f32_16x16x32_bf16 v[176:179], v[66:69], v[208:211], 0\n"
      "ds_read_b128 v[248:251], v120 offset:18432\n"
      "v_fmac_f32 v188, s37, v173\n"
      "v_mfma_f32_16x16x32_bf16 v[176:179], v[66:69], v[62:65], v[176:179]\n"
      "v_mul_f32 v245, v204, v245\n"
      "v_cvt_pk_bf16_f32 v244, v244, v245\n"
      "v_mfma_f32_16x16x16_bf16 v[184:187], v[150:151], v[148:149], v[184:187]\n"
      "v_mul_f32 v246, v204, v246\n"
      "v_mfma_f32_16x16x16_bf16 v[212:215], v[174:175], v[160:161], v[212:215]\n"
      "v_mul_f32 v247, v204, v247\n"
      "v_cvt_pk_bf16_f32 v245, v246, v247\n"
      "v_mfma_f32_16x16x32_bf16 v[200:203], v[220:223], v[208:211], 0\n"
      "v_max3_f32 v246, v196, v197, v198\n"
      "v_max3_f32 v246, v246, v230, v231\n"
      "v_mfma_f32_16x16x32_bf16 v[200:203], v[50:53], v[62:65], v[200:203]\n"
      "v_rcp_f32_e64 v176, v176\n"
      "v_mfma_f32_16x16x32_bf16 v[168:171], v[236:239], v[208:211], 0\n"
      "ds_read_b128 v[192:195], v123 offset:19456\n"
      "v_mfma_f32_16x16x32_bf16 v[168:171], v[54:57], v[62:65], v[168:171]\n"
      "s_nop 0\n"
      "v_max3_f32 v246, v246, v214, v215\n"
      "v_max3_f32 v247, v199, v228, v229\n"
      "v_mfma_f32_16x16x32_bf16 v[140:143], v[252:255], v[208:211], 0\n"
      "s_waitcnt lgkmcnt(1)\n"
      "v_max3_f32 v247, v247, v212, v213\n"
      "v_mfma_f32_16x16x32_bf16 v[140:143], v[58:61], v[62:65], v[140:143]\n"
      "global_store_dwordx4 v117, v[248:251], s[54:55] sc1\n"
      "v_max3_f32 v246, v246, v247, v188\n"
      "v_mov_b32 v247, v246\n"
      "v_mul_f32 v201, v176, v201\n"
      "v_mul_f32 v200, v176, v200\n"
      "v_permlane16_swap_b32 v246, v247\n"
      "v_max_f32 v246, v246, v247\n"
      "v_mov_b32 v247, v246\n"
      "v_mul_f32_e64 v202, v176, v202\n"
      "v_cvt_pk_bf16_f32 v200, v200, v201\n"
      "v_permlane32_swap_b32 v246, v247\n"
      "v_max_f32 v246, v246, v247\n"
      "v_sub_f32 v199, v199, v246\n"
      "v_mul_f32 v203, v176, v203\n"
      "v_sub_f32 v197, v197, v246\n"
      "v_sub_f32 v196, v196, v246\n"
      "v_sub_f32 v212, v212, v246\n"
      "v_exp_f32 v199, v199\n"
      "v_exp_f32 v196, v196\n"
      "v_sub_f32 v198, v198, v246\n"
      "v_sub_f32 v231, v231, v246\n"
      "v_sub_f32 v228, v228, v246\n"
      "v_sub_f32 v214, v214, v246\n"
      "v_exp_f32 v228, v228\n"
      "v_exp_f32 v198, v198\n"
      "v_sub_f32 v230, v230, v246\n"
      "v_sub_f32 v215, v215, v246\n"
      "v_sub_f32 v213, v213, v246\n"
      "v_exp_f32 v215, v215\n"
      "v_exp_f32 v231, v231\n"
      "v_exp_f32 v197, v197\n"
      "v_exp_f32 v213, v213\n"
      "v_cvt_pk_bf16_f32 v196, v196, v197\n"
      "v_cvt_pk_bf16_f32 v197, v198, v199\n"
      "v_sub_f32 v229, v229, v246\n"
      "v_exp_f32 v230, v230\n"
      "v_exp_f32 v229, v229\n"
      "v_sub_f32 v188, v188, v246\n"
      "v_cvt_pk_bf16_f32 v198, v228, v229\n"
      "v_exp_f32 v214, v214\n"
      "v_cvt_pk_bf16_f32 v199, v230, v231\n"
      "v_exp_f32 v212, v212\n"
      "v_exp_f32_e64 v188, v188\n"
      "v_cvt_pk_bf16_f32 v63, v214, v215\n"
      "v_cvt_pk_bf16_f32 v201, v202, v203\n"
      "v_cvt_pk_bf16_f32 v64, v188, 0\n"
      "v_cvt_pk_bf16_f32 v62, v212, v213\n"
      "v_mfma_f32_16x16x32_bf16 v[148:151], v[66:69], v[196:199], 0\n"
      "ds_read_b128 v[212:215], v123 offset:22528\n"
      "v_mul_f32_e64 v168, v176, v168\n"
      "v_mfma_f32_16x16x32_bf16 v[148:151], v[66:69], v[62:65], v[148:151]\n"
      "ds_read_b128 v[228:231], v121 offset:18432\n"
      "v_mul_f32 v170, v176, v170\n"
      "v_mul_f32 v169, v176, v169\n"
      "v_mfma_f32_16x16x32_bf16 v[248:251], v[220:223], v[196:199], 0\n"
      "ds_read_b128 v[208:211], v123 offset:25600\n"
      "v_cvt_pk_bf16_f32 v202, v168, v169\n"
      "v_mfma_f32_16x16x32_bf16 v[248:251], v[50:53], v[62:65], v[248:251]\n"
      "ds_read_b128 v[166:169], v123 offset:28672\n"
      "v_mul_f32 v141, v176, v141\n"
      "v_mul_f32 v140, v176, v140\n"
      "v_mfma_f32_16x16x32_bf16 v[204:207], v[252:255], v[196:199], 0\n"
      "v_mul_f32_e64 v171, v176, v171\n"
      "v_cvt_pk_bf16_f32 v203, v170, v171\n"
      "v_mfma_f32_16x16x32_bf16 v[204:207], v[58:61], v[62:65], v[204:207]\n"
      "ds_write_b128 v124, v[200:203] offset:1024\n"
      "v_mul_f32_e64 v143, v176, v143\n"
      "v_cvt_pk_bf16_f32 v140, v140, v141\n"
      "v_cndmask_b32 v241, v241, v225, s[42:43]\n"
      "v_mul_f32 v142, v176, v142\n"
      "v_cvt_pk_bf16_f32 v141, v142, v143\n"
      "s_waitcnt lgkmcnt(4)\n"
      "v_mfma_f32_16x16x32_bf16 v[176:179], a[216:219], v[192:195], 0\n"
      "ds_read_b128 v[170:173], v123 offset:31744\n"
      "v_cndmask_b32 v241, v241, v189, s[44:45]\n"
      "v_mfma_f32_16x16x32_bf16 v[176:179], a[228:231], v[212:215], v[176:179]\n"
      "ds_write2st64_b64 v125, v[244:245], v[140:141] offset0:0 offset1:2\n"
      "v_mfma_f32_16x16x32_bf16 v[244:247], a[220:223], v[192:195], 0\n"
      "ds_read_b128 v[188:191], v123 offset:34816\n"
      "v_cndmask_b32 v241, v241, v185, s[46:47]\n"
      "v_mfma_f32_16x16x32_bf16 v[244:247], a[232:235], v[212:215], v[244:247]\n"
      "ds_read_b128 v[140:143], v123 offset:20480\n"
      "v_mbcnt_lo_u32_b32 v242, -1, 0\n"
      "v_mfma_f32_16x16x32_bf16 v[200:203], a[224:227], v[192:195], 0\n"
      "ds_read_b128 v[192:195], v123 offset:23552\n"
      "v_mbcnt_hi_u32_b32 v242, -1, v242\n"
      "v_mfma_f32_16x16x32_bf16 v[200:203], a[236:239], v[212:215], v[200:203]\n"
      "s_waitcnt lgkmcnt(6)\n"
      "v_min_u32 v242, 48, v242\n"
      "v_lshlrev_b32 v242, 1, v242\n"
      "v_mfma_f32_16x16x32_bf16 v[176:179], a[240:243], v[208:211], v[176:179]\n"
      "v_add_u32 v242, s30, v242\n"
      "v_mfma_f32_16x16x32_bf16 v[176:179], a[252:255], v[166:169], v[176:179]\n"
      "ds_read_u16 v240, v242 offset:4704\n"
      "v_mfma_f32_16x16x32_bf16 v[244:247], a[244:247], v[208:211], v[244:247]\n"
      "ds_read_b128 v[136:139], v123 offset:26624\n"
      "v_mfma_f32_16x16x32_bf16 v[244:247], v[0:3], v[166:169], v[244:247]\n"
      "s_mov_b32 exec_hi, 0\n"
      "global_store_dwordx4 v118, v[228:231], s[54:55] sc1\n"
      "s_mov_b32 exec_hi, -1\n"
      "v_mfma_f32_16x16x32_bf16 v[200:203], a[248:251], v[208:211], v[200:203]\n"
      "ds_read_b128 v[228:231], v123 offset:29696\n"
      "v_mfma_f32_16x16x32_bf16 v[200:203], v[4:7], v[166:169], v[200:203]\n"
      "ds_read_b128 v[208:211], v123 offset:32768\n"
      "s_add_u32 s28, s27, 2\n"
      "s_waitcnt lgkmcnt(4)\n"
      "v_mfma_f32_16x16x32_bf16 v[176:179], v[8:11], v[170:173], v[176:179]\n"
      "s_min_u32 m0, s28, s3\n"
      "s_nop 0\n"
      "s_movrels_b32 s9, s80\n"
      "v_mfma_f32_16x16x32_bf16 v[176:179], v[20:23], v[188:191], v[176:179]\n"
      "s_lshr_b32 s8, s9, 8\n"
      "s_and_b32 s9, s9, 0xff\n"
      "v_mfma_f32_16x16x32_bf16 v[244:247], v[12:15], v[170:173], v[244:247]\n"
      "s_mul_i32 s10, s8, 73\n"
      "s_waitcnt lgkmcnt(3)\n"
      "s_add_u32 s10, s10, s9\n"
      "v_mfma_f32_16x16x32_bf16 v[244:247], v[24:27], v[188:191], v[244:247]\n"
      "s_mul_i32 s28, s10, 4802\n"
      "v_lshlrev_b32 v240, 16, v240\n"
      "v_fmac_f32 v241, s37, v240\n"
      "v_mfma_f32_16x16x32_bf16 v[200:203], v[16:19], v[170:173], v[200:203]\n"
      "s_add_u32 s56, s18, s28\n"
      "v_max_f32_dpp v240, v241, v241 row_ror:8 row_mask:0xf bank_mask:0xf\n"
      "v_fma_f32 v156, v176, v176, s39\n"
      "v_mfma_f32_16x16x32_bf16 v[200:203], v[28:31], v[188:191], v[200:203]\n"
      "s_addc_u32 s57, s19, 0\n"
      "v_fmac_f32 v156, v177, v177\n"
      "v_max_f32_dpp v240, v240, v240 row_ror:4 row_mask:0xf bank_mask:0xf\n"
      "v_mfma_f32_16x16x32_bf16 v[184:187], a[216:219], v[140:143], 0\n"
      "s_waitcnt lgkmcnt(1)\n"
      "v_max_f32_dpp v240, v240, v240 row_ror:2 row_mask:0xf bank_mask:0xf\n"
      "s_and_b32 s56, s56, -16\n"
      "v_mfma_f32_16x16x32_bf16 v[184:187], a[228:231], v[192:195], v[184:187]\n"
      "v_max_f32_dpp v240, v240, v240 row_ror:1 row_mask:0xf bank_mask:0xf\n"
      "v_fmac_f32 v156, v178, v178\n"
      "v_mfma_f32_16x16x32_bf16 v[212:215], a[220:223], v[140:143], 0\n"
      "s_sub_u32 s58, s56, 110592\n"
      "v_fmac_f32 v156, v179, v179\n"
      "v_max_f32_dpp v240, v240, v240 row_bcast:15 row_mask:0xa bank_mask:0xf\n"
      "v_mfma_f32_16x16x32_bf16 v[212:215], a[232:235], v[192:195], v[212:215]\n"
      "s_subb_u32 s59, s57, 0\n"
      "v_max_f32_dpp v240, v240, v240 row_bcast:31 row_mask:0xc bank_mask:0xf\n"
      "v_fmac_f32_e64 v156, v244, v244\n"
      "v_mfma_f32_16x16x32_bf16 v[188:191], a[224:227], v[140:143], 0\n"
      "ds_read_b128 v[140:143], v123 offset:35840\n"
      "v_readlane_b32 s31, v240, 63\n"
      "v_mfma_f32_16x16x32_bf16 v[188:191], a[236:239], v[192:195], v[188:191]\n"
      "s_sub_u32 s56, s56, 73728\n"
      "v_subrev_f32 v241, s31, v241\n"
      "v_exp_f32 v241, v241\n"
      "v_mfma_f32_16x16x32_bf16 v[184:187], a[240:243], v[136:139], v[184:187]\n"
      "s_subb_u32 s57, s57, 0\n"
      "v_cvt_pk_bf16_f32 v241, v241, v241\n"
      "v_fmac_f32 v156, v245, v245\n"
      "v_mfma_f32_16x16x32_bf16 v[184:187], a[252:255], v[228:231], v[184:187]\n"
      "ds_write_b16 v130, v241\n"
      "v_mfma_f32_16x16x32_bf16 v[240:243], v[236:239], v[196:199], 0\n"
      "ds_read_b128 v[196:199], v131\n"
      "v_fmac_f32 v156, v246, v246\n"
      "v_mfma_f32_16x16x32_bf16 v[240:243], v[54:57], v[62:65], v[240:243]\n"
      "ds_read_b128 v[172:175], v131 offset:64\n"
      "v_fmac_f32 v156, v247, v247\n"
      "v_mfma_f32_16x16x32_bf16 v[212:215], a[244:247], v[136:139], v[212:215]\n"
      "ds_read_b128 v[168:171], v114 offset:18432\n"
      "v_fmac_f32 v156, v200, v200\n"
      "v_mfma_f32_16x16x32_bf16 v[212:215], v[0:3], v[228:231], v[212:215]\n"
      "ds_read_b128 v[192:195], v115 offset:18432\n"
      "v_fmac_f32 v156, v201, v201\n"
      "v_mfma_f32_16x16x32_bf16 v[188:191], a[248:251], v[136:139], v[188:191]\n"
      "s_waitcnt lgkmcnt(5)\n"
      "v_fmac_f32 v156, v202, v202\n"
      "v_fmac_f32 v156, v203, v203\n"
      "v_mfma_f32_16x16x32_bf16 v[188:191], v[4:7], v[228:231], v[188:191]\n"
      "s_mul_i32 s28, s8, 1373568\n"
      "v_permlane16_swap_b32 v159, v156\n"
      "v_mfma_f32_16x16x32_bf16 v[184:187], v[8:11], v[208:211], v[184:187]\n"
      "s_mul_i32 s29, s9, 2688\n"
      "v_add_f32 v159, v159, v156\n"
      "s_add_u32 s28, s28, s29\n"
      "v_mfma_f32_16x16x32_bf16 v[184:187], v[20:23], v[140:143], v[184:187]\n"
      "s_cmp_eq_u32 s8, 72\n"
      "s_cselect_b32 s8, 0, 100270464\n"
      "v_mfma_f32_16x16x32_bf16 v[212:215], v[12:15], v[208:211], v[212:215]\n"
      "s_cmp_eq_u32 s9, 72\n"
      "s_cselect_b32 s9, 0, 196224\n"
      "s_add_u32 s10, s8, s9\n"
      "v_mfma_f32_16x16x32_bf16 v[212:215], v[24:27], v[140:143], v[212:215]\n"
      "s_waitcnt lgkmcnt(2)\n"
      "v_fma_f32 v228, v184, v184, s39\n"
      "v_fmac_f32 v228, v185, v185\n"
      "v_mfma_f32_16x16x32_bf16 v[188:191], v[16:19], v[208:211], v[188:191]\n"
      "s_add_u32 s28, s28, s10\n"
      "v_fmac_f32 v228, v186, v186\n"
      "v_fmac_f32 v228, v187, v187\n"
      "v_mfma_f32_16x16x32_bf16 v[188:191], v[28:31], v[140:143], v[188:191]\n"
      "s_waitcnt lgkmcnt(0)\n"
      "v_fmac_f32 v228, v212, v212\n"
      "v_fmac_f32 v228, v213, v213\n"
      "v_mfma_f32_16x16x32_bf16 v[140:143], v[196:199], v[66:69], 0\n"
      "s_add_u32 s52, s16, s28\n"
      "v_fmac_f32 v228, v214, v214\n"
      "v_fmac_f32 v228, v215, v215\n"
      "v_mfma_f32_16x16x32_bf16 v[140:143], v[172:175], v[66:69], v[140:143]\n"
      "s_addc_u32 s53, s17, 0\n"
      "v_fmac_f32 v228, v188, v188\n"
      "v_fmac_f32 v228, v189, v189\n"
      "v_mfma_f32_16x16x32_bf16 v[208:211], v[196:199], v[220:223], 0\n"
      "s_add_u32 s27, s27, 1\n"
      "v_fmac_f32 v228, v190, v190\n"
      "v_fmac_f32_e64 v228, v191, v191\n"
      "v_mfma_f32_16x16x32_bf16 v[208:211], v[172:175], v[50:53], v[208:211]\n"
      "s_cmp_ge_u32 s27, s26\n"
      "v_mov_b32_dpp v140, v140 quad_perm:[0,0,0,0] row_mask:0xf bank_mask:0xf\n"
      "v_mov_b32 v229, v228\n"
      "v_mfma_f32_16x16x32_bf16 v[220:223], v[196:199], v[236:239], 0\n"
      "s_cselect_b32 s28, 1, 0\n"
      "v_permlane16_swap_b32_e64 v228, v229\n"
      "v_mfma_f32_16x16x32_bf16 v[220:223], v[172:175], v[54:57], v[220:223]\n"
      "s_xor_b32 s29, s10, s11\n"
      "v_add_f32_e64 v228, v228, v229\n"
      "v_rcp_f32_e64 v140, v140\n"
      "v_mfma_f32_16x16x32_bf16 v[236:239], v[196:199], v[252:255], 0\n"
      "s_or_b32 s28, s28, s29\n"
      "v_permlane32_swap_b32 v159, v228\n"
      "v_mfma_f32_16x16x32_bf16 v[236:239], v[172:175], v[58:61], v[236:239]\n"
      "s_nop 0\n"
      "v_cndmask_b32 v208, v208, v220, s[42:43]\n"
      "v_mfma_f32_16x16x32_bf16 v[196:199], a[0:3], v[168:171], 0\n"
      "v_add_f32 v159, v159, v228\n"
      "v_mfma_f32_16x16x32_bf16 v[196:199], a[36:39], v[192:195], v[196:199]\n"
      "ds_write_b32 v129, v159\n"
      "v_mfma_f32_16x16x32_bf16 v[228:231], a[4:7], v[168:171], 0\n"
      "s_nop 0\n"
      "v_cndmask_b32 v208, v208, v236, s[44:45]\n"
      "v_mul_f32_e64 v208, v140, v208\n"
      "v_cvt_pk_bf16_f32 v208, v208, v208\n"
      "v_mfma_f32_16x16x32_bf16 v[228:231], a[40:43], v[192:195], v[228:231]\n"
      "ds_write_b16 v135, v208\n"
      "s_branch L_join_30_%=\n"
      "L_flg_148_%=:\n"
      "v_cmp_gt_i32 vcc, 0, v176\n"
      "s_cbranch_vccz L_fb_149_%=\n"
      "s_sleep 1\n"
      "ds_read_b128 v[176:179], v128 offset:3072\n"
      "s_waitcnt lgkmcnt(0)\n"
      "v_bitop3_b32 v176, v176, v177, s4 bitop3:0x7e\n"
      "v_bitop3_b32 v178, v178, v179, s4 bitop3:0x7e\n"
      "v_or_b32 v176, v176, v178\n"
      "v_cmp_ne_u32 s[48:49], 0, v176\n"
      "s_branch L_flg_148_%=\n"
      "L_edge_1_%=:\n"
      "s_mov_b32 s11, s10\n"
      "s_sub_u32 s28, s60, s8\n"
      "s_sub_u32 s29, s61, s9\n"
      "s_mov_b32 s60, s8\n"
      "s_mov_b32 s61, s9\n"
      "s_mov_b64 exec, s[64:65]\n"
      "v_add_u32 v116, s28, v116\n"
      "s_mov_b64 exec, s[70:71]\n"
      "v_add_u32 v116, s29, v116\n"
      "s_mov_b64 exec, s[66:67]\n"
      "v_add_u32 v117, s28, v117\n"
      "s_mov_b64 exec, s[72:73]\n"
      "v_add_u32 v117, s29, v117\n"
      "s_mov_b64 exec, s[68:69]\n"
      "v_add_u32 v118, s28, v118\n"
      "s_mov_b64 exec, s[74:75]\n"
      "v_add_u32 v118, s29, v118\n"
      "s_mov_b64 exec, -1\n"
      "s_nop 4\n"
      "s_branch L_eb_2_%=\n"
      "L_flg_3_%=:\n"
      "v_cmp_gt_i32 vcc, 0, v168\n"
      "s_cbranch_vccz L_fb_4_%=\n"
      "s_sleep 1\n"
      "ds_read_b128 v[168:171], v128 offset:2048\n"
      "s_waitcnt lgkmcnt(0)\n"
      "v_bitop3_b32 v168, v168, v169, s4 bitop3:0x7e\n"
      "v_bitop3_b32 v170, v170, v171, s4 bitop3:0x7e\n"
      "v_or_b32 v168, v168, v170\n"
      "v_cmp_ne_u32 s[48:49], 0, v168\n"
      "s_branch L_flg_3_%=\n"
      "L_edge_6_%=:\n"
      "s_mov_b32 s11, s10\n"
      "s_sub_u32 s28, s60, s8\n"
      "s_sub_u32 s29, s61, s9\n"
      "s_mov_b32 s60, s8\n"
      "s_mov_b32 s61, s9\n"
      "s_mov_b64 exec, s[64:65]\n"
      "v_add_u32 v116, s28, v116\n"
      "s_mov_b64 exec, s[70:71]\n"
      "v_add_u32 v116, s29, v116\n"
      "s_mov_b64 exec, s[66:67]\n"
      "v_add_u32 v117, s28, v117\n"
      "s_mov_b64 exec, s[72:73]\n"
      "v_add_u32 v117, s29, v117\n"
      "s_mov_b64 exec, s[68:69]\n"
      "v_add_u32 v118, s28, v118\n"
      "s_mov_b64 exec, s[74:75]\n"
      "v_add_u32_e64 v118, s29, v118\n"
      "s_mov_b64 exec, -1\n"
      "s_nop 4\n"
      "s_branch L_eb_7_%=\n"
      "L_flg_12_%=:\n"
      "v_cmp_gt_i32 vcc, 0, v148\n"
      "s_cbranch_vccz L_fb_13_%=\n"
      "s_sleep 1\n"
      "ds_read_b128 v[148:151], v128 offset:3072\n"
      "s_waitcnt lgkmcnt(0)\n"
      "v_bitop3_b32 v148, v148, v149, s4 bitop3:0x7e\n"
      "v_bitop3_b32 v150, v150, v151, s4 bitop3:0x7e\n"
      "v_or_b32 v148, v148, v150\n"
      "v_cmp_ne_u32 s[48:49], 0, v148\n"
      "s_branch L_flg_12_%=\n"
      "L_edge_15_%=:\n"
      "s_mov_b32 s11, s10\n"
      "s_sub_u32 s28, s60, s8\n"
      "s_sub_u32 s29, s61, s9\n"
      "s_mov_b32 s60, s8\n"
      "s_mov_b32 s61, s9\n"
      "s_mov_b64 exec, s[64:65]\n"
      "v_add_u32 v116, s28, v116\n"
      "s_mov_b64 exec, s[70:71]\n"
      "v_add_u32 v116, s29, v116\n"
      "s_mov_b64 exec, s[66:67]\n"
      "v_add_u32 v117, s28, v117\n"
      "s_mov_b64 exec, s[72:73]\n"
      "v_add_u32 v117, s29, v117\n"
      "s_mov_b64 exec, s[68:69]\n"
      "v_add_u32 v118, s28, v118\n"
      "s_mov_b64 exec, s[74:75]\n"
      "v_add_u32 v118, s29, v118\n"
      "s_mov_b64 exec, -1\n"
      "s_nop 4\n"
      "s_branch L_eb_16_%=\n"
      "L_edge_17_%=:\n"
      "s_mov_b32 s11, s10\n"
      "s_sub_u32 s28, s60, s8\n"
      "s_sub_u32 s29, s61, s9\n"
      "s_mov_b32 s60, s8\n"
      "s_mov_b32 s61, s9\n"
      "s_mov_b64 exec, s[64:65]\n"
      "v_add_u32 v116, s28, v116\n"
      "s_mov_b64 exec, s[70:71]\n"
      "v_add_u32 v116, s29, v116\n"
      "s_mov_b64 exec, s[66:67]\n"
      "v_add_u32 v117, s28, v117\n"
      "s_mov_b64 exec, s[72:73]\n"
      "v_add_u32 v117, s29, v117\n"
      "s_mov_b64 exec, s[68:69]\n"
      "v_add_u32 v118, s28, v118\n"
      "s_mov_b64 exec, s[74:75]\n"
      "v_add_u32 v118, s29, v118\n"
      "s_mov_b64 exec, -1\n"
      "s_nop 4\n"
      "s_branch L_eb_18_%=\n"
      "L_flg_23_%=:\n"
      "v_cmp_gt_i32 vcc, 0, v196\n"
      "s_cbranch_vccz L_fb_24_%=\n"
      "s_sleep 1\n"
      "ds_read_b128 v[196:199], v128 offset:2048\n"
      "s_waitcnt lgkmcnt(0)\n"
      "v_bitop3_b32 v196, v196, v197, s4 bitop3:0x7e\n"
      "v_bitop3_b32 v198, v198, v199, s4 bitop3:0x7e\n"
      "v_or_b32 v196, v196, v198\n"
      "v_cmp_ne_u32 s[48:49], 0, v196\n"
      "s_branch L_flg_23_%=\n"
      "L_edge_26_%=:\n"
      "s_mov_b32 s11, s10\n"
      "s_sub_u32 s28, s60, s8\n"
      "s_sub_u32 s29, s61, s9\n"
      "s_mov_b32 s60, s8\n"
      "s_mov_b32 s61, s9\n"
      "s_mov_b64 exec, s[64:65]\n"
      "v_add_u32 v116, s28, v116\n"
      "s_mov_b64 exec, s[70:71]\n"
      "v_add_u32 v116, s29, v116\n"
      "s_mov_b64 exec, s[66:67]\n"
      "v_add_u32 v117, s28, v117\n"
      "s_mov_b64 exec, s[72:73]\n"
      "v_add_u32 v117, s29, v117\n"
      "s_mov_b64 exec, s[68:69]\n"
      "v_add_u32 v118, s28, v118\n"
      "s_mov_b64 exec, s[74:75]\n"
      "v_add_u32 v118, s29, v118\n"
      "s_mov_b64 exec, -1\n"
      "s_nop 4\n"
      "s_branch L_eb_27_%=\n"
      "L_flg_32_%=:\n"
      "v_cmp_gt_i32 vcc, 0, v172\n"
      "s_cbranch_vccz L_fb_33_%=\n"
      "s_sleep 1\n"
      "ds_read_b128 v[172:175], v128 offset:3072\n"
      "s_waitcnt lgkmcnt(0)\n"
      "v_bitop3_b32 v172, v172, v173, s4 bitop3:0x7e\n"
      "v_bitop3_b32 v174, v174, v175, s4 bitop3:0x7e\n"
      "v_or_b32 v172, v172, v174\n"
      "v_cmp_ne_u32 s[48:49], 0, v172\n"
      "s_branch L_flg_32_%=\n"
      "L_edge_36_%=:\n"
      "s_mov_b32 s11, s10\n"
      "s_sub_u32 s28, s60, s8\n"
      "s_sub_u32 s29, s61, s9\n"
      "s_mov_b32 s60, s8\n"
      "s_mov_b32 s61, s9\n"
      "s_mov_b64 exec, s[64:65]\n"
      "v_add_u32 v116, s28, v116\n"
      "s_mov_b64 exec, s[70:71]\n"
      "v_add_u32 v116, s29, v116\n"
      "s_mov_b64 exec, s[66:67]\n"
      "v_add_u32 v117, s28, v117\n"
      "s_mov_b64 exec, s[72:73]\n"
      "v_add_u32 v117, s29, v117\n"
      "s_mov_b64 exec, s[68:69]\n"
      "v_add_u32 v118, s28, v118\n"
      "s_mov_b64 exec, s[74:75]\n"
      "v_add_u32 v118, s29, v118\n"
      "s_mov_b64 exec, -1\n"
      "s_nop 4\n"
      "s_branch L_eb_37_%=\n"
      "L_edge_38_%=:\n"
      "s_mov_b32 s11, s10\n"
      "s_sub_u32 s28, s60, s8\n"
      "s_sub_u32 s29, s61, s9\n"
      "s_mov_b32 s60, s8\n"
      "s_mov_b32 s61, s9\n"
      "s_mov_b64 exec, s[64:65]\n"
      "v_add_u32 v116, s28, v116\n"
      "s_mov_b64 exec, s[70:71]\n"
      "v_add_u32 v116, s29, v116\n"
      "s_mov_b64 exec, s[66:67]\n"
      "v_add_u32 v117, s28, v117\n"
      "s_mov_b64 exec, s[72:73]\n"
      "v_add_u32 v117, s29, v117\n"
      "s_mov_b64 exec, s[68:69]\n"
      "v_add_u32 v118, s28, v118\n"
      "s_mov_b64 exec, s[74:75]\n"
      "v_add_u32 v118, s29, v118\n"
      "s_mov_b64 exec, -1\n"
      "s_nop 4\n"
      "s_branch L_eb_39_%=\n"
      "L_edge_40_%=:\n"
      "s_mov_b32 s11, s10\n"
      "s_sub_u32 s28, s60, s8\n"
      "s_sub_u32 s29, s61, s9\n"
      "s_mov_b32 s60, s8\n"
      "s_mov_b32 s61, s9\n"
      "s_mov_b64 exec, s[64:65]\n"
      "v_add_u32 v116, s28, v116\n"
      "s_mov_b64 exec, s[70:71]\n"
      "v_add_u32 v116, s29, v116\n"
      "s_mov_b64 exec, s[66:67]\n"
      "v_add_u32 v117, s28, v117\n"
      "s_mov_b64 exec, s[72:73]\n"
      "v_add_u32 v117, s29, v117\n"
      "s_mov_b64 exec, s[68:69]\n"
      "v_add_u32 v118, s28, v118\n"
      "s_mov_b64 exec, s[74:75]\n"
      "v_add_u32 v118, s29, v118\n"
      "s_mov_b64 exec, -1\n"
      "s_nop 4\n"
      "s_branch L_eb_41_%=\n"
      "L_edge_44_%=:\n"
      "s_mov_b32 s11, s10\n"
      "s_sub_u32 s28, s60, s8\n"
      "s_sub_u32 s29, s61, s9\n"
      "s_mov_b32 s60, s8\n"
      "s_mov_b32 s61, s9\n"
      "s_mov_b64 exec, s[64:65]\n"
      "v_add_u32 v116, s28, v116\n"
      "s_mov_b64 exec, s[70:71]\n"
      "v_add_u32 v116, s29, v116\n"
      "s_mov_b64 exec, s[66:67]\n"
      "v_add_u32 v117, s28, v117\n"
      "s_mov_b64 exec, s[72:73]\n"
      "v_add_u32 v117, s29, v117\n"
      "s_mov_b64 exec, s[68:69]\n"
      "v_add_u32 v118, s28, v118\n"
      "s_mov_b64 exec, s[74:75]\n"
      "v_add_u32 v118, s29, v118\n"
      "s_mov_b64 exec, -1\n"
      "s_nop 4\n"
      "s_branch L_eb_45_%=\n"
      "L_edge_46_%=:\n"
      "s_mov_b32 s11, s10\n"
      "s_sub_u32 s28, s60, s8\n"
      "s_sub_u32 s29, s61, s9\n"
      "s_mov_b32 s60, s8\n"
      "s_mov_b32 s61, s9\n"
      "s_mov_b64 exec, s[64:65]\n"
      "v_add_u32 v116, s28, v116\n"
      "s_mov_b64 exec, s[70:71]\n"
      "v_add_u32 v116, s29, v116\n"
      "s_mov_b64 exec, s[66:67]\n"
      "v_add_u32 v117, s28, v117\n"
      "s_mov_b64 exec, s[72:73]\n"
      "v_add_u32 v117, s29, v117\n"
      "s_mov_b64 exec, s[68:69]\n"
      "v_add_u32 v118, s28, v118\n"
      "s_mov_b64 exec, s[74:75]\n"
      "v_add_u32 v118, s29, v118\n"
      "s_mov_b64 exec, -1\n"
      "s_nop 4\n"
      "s_branch L_eb_47_%=\n"
      "L_edge_48_%=:\n"
      "s_mov_b32 s11, s10\n"
      "s_sub_u32 s28, s60, s8\n"
      "s_sub_u32 s29, s61, s9\n"
      "s_mov_b32 s60, s8\n"
      "s_mov_b32 s61, s9\n"
      "s_mov_b64 exec, s[64:65]\n"
      "v_add_u32 v116, s28, v116\n"
      "s_mov_b64 exec, s[70:71]\n"
      "v_add_u32 v116, s29, v116\n"
      "s_mov_b64 exec, s[66:67]\n"
      "v_add_u32 v117, s28, v117\n"
      "s_mov_b64 exec, s[72:73]\n"
      "v_add_u32 v117, s29, v117\n"
      "s_mov_b64 exec, s[68:69]\n"
      "v_add_u32 v118, s28, v118\n"
      "s_mov_b64 exec, s[74:75]\n"
      "v_add_u32 v118, s29, v118\n"
      "s_mov_b64 exec, -1\n"
      "s_nop 4\n"
      "s_branch L_eb_49_%=\n"
      "L_cend_%=:\n"
      :
      : "s"(__builtin_amdgcn_kernarg_segment_ptr()), "s"(wave), "s"(blockIdx.x), "s"(CT_TAB), "s"(lds0)
      : "v0","v1","v2","v3","v4","v5","v6","v7","v8","v9","v10","v11","v12","v13","v14","v15","v16","v17","v18","v19","v20","v21","v22","v23","v24","v25","v26","v27","v28","v29","v30","v31","v32","v33","v34","v35","v36","v37","v38","v39","v40","v41","v42","v43","v44","v45","v46","v47","v48","v49","v50","v51","v52","v53","v54","v55","v56","v57","v58","v59","v60","v61","v62","v63","v64","v65","v66","v67","v68","v69","v70","v71","v72","v73","v74","v75","v76","v77","v78","v79","v80","v81","v82","v83","v84","v85","v86","v87","v88","v89","v90","v91","v92","v93","v94","v95","v96","v97","v98","v99","v100","v101","v102","v103","v104","v105","v106","v107","v108","v109","v110","v111","v112","v113","v114","v115","v116","v117","v118","v119","v120","v121","v122","v123","v124","v125","v126","v127","v128","v129","v130","v131","v132","v133","v134","v135","v136","v137","v138","v139","v140","v141","v142","v143","v144","v145","v146","v147","v148","v149","v150","v151","v152","v153","v154","v155","v156","v157","v158","v159","v160","v161","v162","v163","v164","v165","v166","v167","v168","v169","v170","v171","v172","v173","v174","v175","v176","v177","v178","v179","v180","v181","v182","v183","v184","v185","v186","v187","v188","v189","v190","v191","v192","v193","v194","v195","v196","v197","v198","v199","v200","v201","v202","v203","v204","v205","v206","v207","v208","v209","v210","v211","v212","v213","v214","v215","v216","v217","v218","v219","v220","v221","v222","v223","v224","v225","v226","v227","v228","v229","v230","v231","v232","v233","v234","v235","v236","v237","v238","v239","v240","v241","v242","v243","v244","v245","v246","v247","v248","v249","v250","v251","v252","v253","v254","v255","a0","a1","a2","a3","a4","a5","a6","a7","a8","a9","a10","a11","a12","a13","a14","a15","a16","a17","a18","a19","a20","a21","a22","a23","a24","a25","a26","a27","a28","a29","a30","a31","a32","a33","a34","a35","a36","a37","a38","a39","a40","a41","a42","a43","a44","a45","a46","a47","a48","a49","a50","a51","a52","a53","a54","a55","a56","a57","a58","a59","a60","a61","a62","a63","a64","a65","a66","a67","a68","a69","a70","a71","a72","a73","a74","a75","a76","a77","a78","a79","a80","a81","a82","a83","a84","a85","a86","a87","a88","a89","a90","a91","a92","a93","a94","a95","a96","a97","a98","a99","a100","a101","a102","a103","a104","a105","a106","a107","a108","a109","a110","a111","a112","a113","a114","a115","a116","a117","a118","a119","a120","a121","a122","a123","a124","a125","a126","a127","a128","a129","a130","a131","a132","a133","a134","a135","a136","a137","a138","a139","a140","a141","a142","a143","a144","a145","a146","a147","a148","a149","a150","a151","a152","a153","a154","a155","a156","a157","a158","a159","a160","a161","a162","a163","a164","a165","a166","a167","a168","a169","a170","a171","a172","a173","a174","a175","a176","a177","a178","a179","a180","a181","a182","a183","a184","a185","a186","a187","a188","a189","a190","a191","a192","a193","a194","a195","a196","a197","a198","a199","a200","a201","a202","a203","a204","a205","a206","a207","a208","a209","a210","a211","a212","a213","a214","a215","a216","a217","a218","a219","a220","a221","a222","a223","a224","a225","a226","a227","a228","a229","a230","a231","a232","a233","a234","a235","a236","a237","a238","a239","a240","a241","a242","a243","a244","a245","a246","a247","a248","a249","a250","a251","a252","a253","a254","a255","s2","s3","s4","s5","s6","s7","s8","s9","s10","s11","s12","s13","s16","s17","s18","s19","s20","s21","s22","s23","s24","s25","s26","s27","s28","s29","s30","s31","s34","s35","s36","s37","s38","s39","s40","s41","s42","s43","s44","s45","s46","s47","s48","s49","s50","s51","s52","s53","s54","s55","s60","s61","s62","s63","s64","s65","s66","s67","s68","s69","s70","s71","s72","s73","s74","s75","s76","s77","s78","s79","s80","s81","s82","s83","s84","s85","s86","s87","s88","s89","s90","s91","s92","s93","s94","s95","s96","s97","s98","s99","s100","s101", "vcc", "scc", "memory");
}

}

extern "C" void kernel_launch(void* const* d_in, const int* in_sizes, int n_in, void* d_out, int out_size, void* d_ws,
                              size_t ws_size, hipStream_t stream) {
  const uint16_t* x = (const uint16_t*)d_in[0];
  const uint16_t* wqkv = (const uint16_t*)d_in[1];
  const uint16_t* wo = (const uint16_t*)d_in[2];
  const uint16_t* ls = (const uint16_t*)d_in[3];
  const uint16_t* w1 = (const uint16_t*)d_in[4];
  const uint16_t* w2 = (const uint16_t*)d_in[5];
  const uint16_t* rc = (const uint16_t*)d_in[6];
  const int* ridx = (const int*)d_in[7];
  const uint16_t* mask = (const uint16_t*)d_in[8];
  swin::swin_main<<<256, 256, 0, stream>>>(x, mask, (char*)d_ws, (uint16_t*)d_out, wqkv, wo, ls, w1, w2, rc, ridx);
}
```

```cpp
#include <hip/hip_runtime.h>
#include <hip/hip_bf16.h>
#include <stdint.h>

namespace swin {

constexpr unsigned LDS_BYTES = 163840u;

__device__ __attribute__((aligned(256))) const unsigned CT_TAB[26704] = {0,64,0,0xc0000,144,208,16,0xc0010,288,352,32,0xc0020,432,496,48,0xc0030,576,512,64,0xc0040,720,656,80,0xc0050,864,800,96,0xc0060,1008,944,112,0xc0070,1024,1088,400,0xc0190,1168,1232,384,0xc0180,1312,1376,432,0xc01b0,1456,1520,416,0xc01a0,1600,1536,464,0xc01d0,1744,1680,448,0xc01c0,1888,1824,496,0xc01f0,2032,1968,480,0xc01e0,16,80,800,0xc0320,128,192,816,0xc0330,304,368,768,0xc0300,416,480,784,0xc0310,592,528,864,0xc0360,704,640,880,0xc0370,880,816,832,0xc0340,992,928,848,0xc0350,1040,1104,1200,0xef8b0,1152,1216,1184,0xef8a0,1328,1392,1168,0xef890,1440,1504,1152,0xef880,1616,1552,1264,0xef8f0,1728,1664,1248,0xef8e0,1904,1840,1232,0xef8d0,2016,1952,1216,0xef8c0,32,96,1600,0xefa40,176,240,1616,0xefa50,256,320,1632,0xefa60,400,464,1648,0xefa70,608,544,1536,0xefa00,752,688,1552,0xefa10,832,768,1568,0xefa20,976,912,1584,0xefa30,1056,1120,2000,0xefbd0,1200,1264,1984,0xefbc0,1280,1344,2032,0xefbf0,1424,1488,2016,0xefbe0,1632,1568,1936,0xefb90,1776,1712,1920,0xefb80,1856,1792,1968,0xefbb0,2000,1936,1952,0xefba0,48,112,2400,0xefd60,160,224,2416,0xefd70,272,336,2368,0xefd40,384,448,2384,0xefd50,624,560,2336,0xefd20,736,672,2352,0xefd30,848,784,2304,0xefd00,960,896,2320,0xefd10,1072,1136,0x2fef0,0xefef0,1184,1248,0x2fee0,0xefee0,1296,1360,0x2fed0,0xefed0,1408,1472,0x2fec0,0xefec0,1648,1584,0x2feb0,0xefeb0,1760,1696,0x2fea0,0xefea0,1872,1808,0x2fe90,0xefe90,1984,1920,0x2fe80,0xefe80,0xc0100,73728,77824,90112,0xc0110,73744,77840,90128,0xc0120,73760,77856,90144,0xc0130,73776,77872,90160,0xc0140,73792,77888,90176,0xc0150,73808,77904,90192,0xc0160,73824,77920,90208,0xc0170,73840,77936,90224,0xc0290,73856,77952,90240,0xc0280,73872,77968,90256,0xc02b0,73888,77984,90272,0xc02a0,73904,78000,90288,0xc02d0,73920,78016,90304,0xc02c0,73936,78032,90320,0xc02f0,73952,78048,90336,0xc02e0,73968,78064,90352,0xc0420,73984,78080,90368,0xc0430,74000,78096,90384,0xc0400,74016,78112,90400,0xc0410,74032,78128,90416,0xc0460,74048,78144,90432,0xc0470,74064,78160,90448,0xc0440,74080,78176,90464,0xc0450,74096,78192,90480,0xef9b0,74112,78208,90496,0xef9a0,74128,78224,90512,0xef990,74144,78240,90528,0xef980,74160,78256,90544,0xef9f0,74176,78272,90560,0xef9e0,74192,78288,90576,0xef9d0,74208,78304,90592,0xef9c0,74224,78320,90608,0xc0100,74240,78336,90624,0xc0110,74256,78352,90640,0xc0120,74272,78368,90656,0xc0130,74288,78384,90672,0xc0140,74304,78400,90688,0xc0150,74320,78416,90704,0xc0160,74336,78432,90720,0xc0170,74352,78448,90736,0xc0290,74368,78464,90752,0xc0280,74384,78480,90768,0xc02b0,74400,78496,90784,0xc02a0,74416,78512,90800,0xc02d0,74432,78528,90816,0xc02c0,74448,78544,90832,0xc02f0,74464,78560,90848,0xc02e0,74480,78576,90864,0xc0420,74496,78592,90880,0xc0430,74512,78608,90896,0xc0400,74528,78624,90912,0xc0410,74544,78640,90928,0xc0460,74560,78656,90944,0xc0470,74576,78672,90960,0xc0440,74592,78688,90976,0xc0450,74608,78704,90992,0xef9b0,74624,78720,91008,0xef9a0,74640,78736,91024,0xef990,74656,78752,91040,0xef980,74672,78768,91056,0xef9f0,74688,78784,91072,0xef9e0,74704,78800,91088,0xef9d0,74720,78816,91104,0xef9c0,74736,78832,91120,0x1c000,36864,36864,49152,0x1c004,36880,36880,49168,0x1c008,36896,36896,49184,0x1c00c,36912,36912,49200,0x1c010,36928,36928,49216,0x1c014,36944,36944,49232,0x1c018,36960,36960,49248,0x1c01c,36976,36976,49264,0x1c020,36992,36992,49280,0x1c024,37008,37008,49296,0x1c028,37024,37024,49312,0x1c02c,37040,37040,49328,0x1c030,37056,37056,49344,0x1c034,37072,37072,49360,0x1c038,37088,37088,49376,0x1c03c,37104,37104,49392,0x1c040,37120,37120,49408,0x1c044,37136,37136,49424,0x1c048,37152,37152,49440,0x1c04c,37168,37168,49456,0x1c050,37184,37184,49472,0x1c054,37200,37200,49488,0x1c058,37216,37216,49504,0x1c05c,37232,37232,49520,0x1c060,37248,37248,49536,0x1c064,37264,37264,49552,0x1c068,37280,37280,49568,0x1c06c,37296,37296,49584,0x1c070,37312,37312,49600,0x1c074,37328,37328,49616,0x1c078,37344,37344,49632,0x1c07c,37360,37360,49648,0x1c080,37376,37376,49664,0x1c084,37392,37392,49680,0x1c088,37408,37408,49696,0x1c08c,37424,37424,49712,0x1c090,37440,37440,49728,0x1c094,37456,37456,49744,0x1c098,37472,37472,49760,0x1c09c,37488,37488,49776,0x1c0a0,37504,37504,49792,0x1c0a4,37520,37520,49808,0x1c0a8,37536,37536,49824,0x1c0ac,37552,37552,49840,0x1c0b0,37568,37568,49856,0x1c0b4,37584,37584,49872,0x1c0b8,37600,37600,49888,0x1c0bc,37616,37616,49904,0x1c0c0,37632,37632,49920,0x1c0c4,37648,37648,49936,0x1c0c8,37664,37664,49952,0x1c0cc,37680,37680,49968,0x1c0d0,37696,37696,49984,0x1c0d4,37712,37712,50000,0x1c0d8,37728,37728,50016,0x1c0dc,37744,37744,50032,0x1c0e0,37760,37760,50048,0x1c0e4,37776,37776,50064,0x1c0e8,37792,37792,50080,0x1c0ec,37808,37808,50096,0x1c0f0,37824,37824,50112,0x1c0f4,37840,37840,50128,0x1c0f8,37856,37856,50144,0x1c0fc,37872,37872,50160,73728,73792,0x1d600,0x1d600,73872,73936,0x1d610,0x1d610,74016,74080,0x1d620,0x1d620,74160,74224,0x1d630,0x1d630,74304,74240,0x1d640,0x1d640,74448,74384,0x1d650,0x1d650,74592,74528,0x1d660,0x1d660,74736,74672,0x1d670,0x1d670,74752,74816,0x1d680,0x1d680,74896,74960,0x1d690,0x1d690,75040,75104,0x1d6a0,0x1d6a0,75184,75248,0x1d6b0,0x1d6b0,75328,75264,0x1d6c0,0x1d6c0,75472,75408,0x1d6d0,0x1d6d0,75616,75552,0x1d6e0,0x1d6e0,75760,75696,0x1d6f0,0x1d6f0,73744,73800,0x1d700,0x1d700,73856,73944,0x1d710,0x1d710,74032,74088,0x1d720,0x1d720,74144,74232,0x1d730,0x1d730,74320,74248,0x1d740,0x1d740,74432,74392,0x1d750,0x1d750,74608,74536,0x1d760,0x1d760,74720,74680,0x1d770,0x1d770,74768,74824,0x1d780,0x1d780,74880,74968,0x1d790,0x1d790,75056,75112,0x1d7a0,0x1d7a0,75168,75256,0x1d7b0,0x1d7b0,75344,75272,0x1d7c0,0x1d7c0,75456,75416,0x1d7d0,0x1d7d0,75632,75560,0x1d7e0,0x1d7e0,75744,75704,0x1d7f0,0x1d7f0,73760,73808,0x1d800,0x1d800,73904,73920,0x1d810,0x1d810,73984,74096,0x1d820,0x1d820,74128,74208,0x1d830,0x1d830,74336,74256,0x1d840,0x1d840,74480,74368,0x1d850,0x1d850,74560,74544,0x1d860,0x1d860,74704,74656,0x1d870,0x1d870,74784,74832,0x1d880,0x1d880,74928,74944,0x1d890,0x1d890,75008,75120,0x1d8a0,0x1d8a0,75152,75232,0x1d8b0,0x1d8b0,75360,75280,0x1d8c0,0x1d8c0,75504,75392,0x1d8d0,0x1d8d0,75584,75568,0x1d8e0,0x1d8e0,75728,75680,0x1d8f0,0x1d8f0,73776,73816,0x1d900,0x1d900,73888,73928,0x1d910,0x1d910,74000,74104,0x1d920,0x1d920,74112,74216,0x1d930,0x1d930,74352,74264,0x1d940,0x1d940,74464,74376,0x1d950,0x1d950,74576,74552,0x1d960,0x1d960,74688,74664,0x1d970,0x1d970,74800,74840,0x1d980,0x1d980,74912,74952,0x1d990,0x1d990,75024,75128,0x1d9a0,0x1d9a0,75136,75240,0x1d9b0,0x1d9b0,75376,75288,0x1d9c0,0x1d9c0,75488,75400,0x1d9d0,0x1d9d0,75600,75576,0x1d9e0,0x1d9e0,75712,75688,0x1d9f0,0x1d9f0,0x1e600,0x1e600,0x1e800,0x100000,0x1e602,0x100000,0x100000,0x1e800,0x1e604,0x100000,0x100000,0x100000,0x1e606,0x100000,0x100000,0x100000,0x1e610,0x1e600,0x100000,0x100000,0x1e612,0x100000,0x100000,0x1e800,0x1e614,0x100000,0x100000,0x100000,0x1e616,0x100000,0x100000,0x100000,0x1e620,0x1e600,0x100000,0x100000,0x1e622,0x100000,0x100000,0x1e800,0x1e624,0x100000,0x100000,0x100000,0x1e626,0x100000,0x100000,0x100000,0x1e630,0x1e600,0x100000,0x100000,0x1e632,0x100000,0x100000,0x1e800,0x1e634,0x100000,0x100000,0x100000,0x1e636,0x100000,0x100000,0x100000,0x1e608,0x1e610,0x1e830,0x100000,0x1e60a,0x100000,0x100000,0x1e830,0x1e60c,0x100000,0x100000,0x100000,0x1e60e,0x100000,0x100000,0x100000,0x1e618,0x1e610,0x100000,0x100000,0x1e61a,0x100000,0x100000,0x1e830,0x1e61c,0x100000,0x100000,0x100000,0x1e61e,0x100000,0x100000,0x100000,0x1e628,0x1e610,0x100000,0x100000,0x1e62a,0x100000,0x100000,0x1e830,0x1e62c,0x100000,0x100000,0x100000,0x1e62e,0x100000,0x100000,0x100000,0x1e638,0x1e610,0x100000,0x100000,0x1e63a,0x100000,0x100000,0x1e830,0x1e63c,0x100000,0x100000,0x100000,0x1e63e,0x100000,0x100000,0x100000,0x1e640,0x1e620,0x1e860,0x100000,0x1e642,0x100000,0x100000,0x1e860,0x1e644,0x100000,0x100000,0x100000,0x1e646,0x100000,0x100000,0x100000,0x1e650,0x1e620,0x100000,0x100000,0x1e652,0x100000,0x100000,0x1e860,0x1e654,0x100000,0x100000,0x100000,0x1e656,0x100000,0x100000,0x100000,0x1e660,0x1e620,0x100000,0x100000,0x1e662,0x100000,0x100000,0x1e860,0x1e664,0x100000,0x100000,0x100000,0x1e666,0x100000,0x100000,0x100000,0x1e670,0x1e620,0x100000,0x100000,0x1e672,0x100000,0x100000,0x1e860,0x1e674,0x100000,0x100000,0x100000,0x1e676,0x100000,0x100000,0x100000,0x1e648,0x1e630,0x1e890,0x100000,0x1e64a,0x100000,0x100000,0x1e890,0x1e64c,0x100000,0x100000,0x100000,0x1e64e,0x100000,0x100000,0x100000,0x1e658,0x1e630,0x100000,0x100000,0x1e65a,0x100000,0x100000,0x1e890,0x1e65c,0x100000,0x100000,0x100000,0x1e65e,0x100000,0x100000,0x100000,0x1e668,0x1e630,0x100000,0x100000,0x1e66a,0x100000,0x100000,0x1e890,0x1e66c,0x100000,0x100000,0x100000,0x1e66e,0x100000,0x100000,0x100000,0x1e678,0x1e630,0x100000,0x100000,0x1e67a,0x100000,0x100000,0x1e890,0x1e67c,0x100000,0x100000,0x100000,0x1e67e,0x100000,0x100000,0x100000,0x1eb00,0x24c70,36864,36928,0x1eb02,0x24c72,36880,36936,0x1eb04,0x24c74,36896,36944,0x1eb06,0x24c76,36912,36952,0x1eb08,0x24dd0,37248,37312,0x1eb0a,0x24dd2,37264,37320,0x1eb0c,0x24dd4,37280,37328,0x1eb0e,0x24dd6,37296,37336,0x1eb10,0x24f30,37664,37728,0x1eb12,0x24f32,37680,37736,0x1eb14,0x24f34,37632,37744,0x1eb16,0x24f36,37648,37752,0x1eb18,0x25090,38048,38112,0x1eb1a,0x25092,38064,38120,0x1eb1c,0x25094,38016,38128,0x1eb1e,0x25096,38032,38136,0x1eb00,0x24c78,38464,38400,0x1eb02,0x24c7a,38480,38408,0x1eb04,0x24c7c,38496,38416,0x1eb06,0x24c7e,38512,38424,0x1eb08,0x24dd8,38848,38784,0x1eb0a,0x24dda,38864,38792,0x1eb0c,0x24ddc,38880,38800,0x1eb0e,0x24dde,38896,38808,0x1eb10,0x24f38,39264,39200,0x1eb12,0x24f3a,39280,39208,0x1eb14,0x24f3c,39232,39216,0x1eb16,0x24f3e,39248,39224,0x1eb18,0x25098,39648,39584,0x1eb1a,0x2509a,39664,39592,0x1eb1c,0x2509c,39616,39600,0x1eb1e,0x2509e,39632,39608,0x1eb00,0x26270,39936,40000,0x1eb02,0x26272,39952,40008,0x1eb04,0x26274,39968,40016,0x1eb06,0x26276,39984,40024,0x1eb08,0x263d0,40320,40384,0x1eb0a,0x263d2,40336,40392,0x1eb0c,0x263d4,40352,40400,0x1eb0e,0x263d6,40368,40408,0x1eb10,0x26530,40736,40800,0x1eb12,0x26532,40752,40808,0x1eb14,0x26534,40704,40816,0x1eb16,0x26536,40720,40824,0x1eb18,0x26690,41120,41184,0x1eb1a,0x26692,41136,41192,0x1eb1c,0x26694,41088,41200,0x1eb1e,0x26696,41104,41208,0x1eb00,0x100000,41536,41472,0x1eb02,0x100000,41552,41480,0x1eb04,0x100000,41568,41488,0x1eb06,0x100000,41584,41496,0x1eb08,0x100000,41920,41856,0x1eb0a,0x100000,41936,41864,0x1eb0c,0x100000,41952,41872,0x1eb0e,0x100000,41968,41880,0x1eb10,0x100000,42336,42272,0x1eb12,0x100000,42352,42280,0x1eb14,0x100000,42304,42288,0x1eb16,0x100000,42320,42296,0x1eb18,0x100000,42720,42656,0x1eb1a,0x100000,42736,42664,0x1eb1c,0x100000,42688,42672,0x1eb1e,0x100000,42704,42680,36864,36896,36928,36864,36872,36904,36936,36880,36880,36912,36944,36896,36888,36920,36952,36912,38016,38048,38080,36928,38024,38056,38088,36944,38032,38064,38096,36960,38040,38072,38104,36976,39200,39168,39264,36992,39208,39176,39272,37008,39216,39184,39280,37024,39224,39192,39288,37040,40352,40320,40416,37056,40360,40328,40424,37072,40368,40336,40432,37088,40376,40344,40440,37104,46144,46176,46080,37120,46152,46184,46088,37136,46160,46192,46096,37152,46168,46200,46104,37168,47296,47328,47232,37184,47304,47336,47240,37200,47312,47344,47248,37216,47320,47352,47256,37232,48480,48448,48416,37248,48488,48456,48424,37264,48496,48464,48432,37280,48504,48472,48440,37296,49632,49600,49568,37312,49640,49608,49576,37328,49648,49616,49584,37344,49656,49624,49592,37360,55296,55328,55360,37376,55304,55336,55368,37392,55312,55344,55376,37408,55320,55352,55384,37424,56448,56480,56512,37440,56456,56488,56520,37456,56464,56496,56528,37472,56472,56504,56536,37488,57632,57600,57696,37504,57640,57608,57704,37520,57648,57616,57712,37536,57656,57624,57720,37552,58784,58752,58848,37568,58792,58760,58856,37584,58800,58768,58864,37600,58808,58776,58872,37616,64576,64608,64512,37632,64584,64616,64520,37648,64592,64624,64528,37664,64600,64632,64536,37680,65728,65760,65664,37696,65736,65768,65672,37712,65744,65776,65680,37728,65752,65784,65688,37744,66912,66880,66848,37760,66920,66888,66856,37776,66928,66896,66864,37792,66936,66904,66872,37808,68064,68032,68000,37824,68072,68040,68008,37840,68080,68048,68016,37856,68088,68056,68024,37872,0x1e800,0x1e800,0x20cc0,0,0x1e8c4,0x1e8c4,0x20cc4,16,0x1e988,0x1e988,0x20cc8,32,0x1ea4c,0x1ea4c,0x20ccc,48,0x1eb10,0x1eb10,0x20cd0,64,0x1ebd4,0x1ebd4,0x20cd4,80,0x1ec98,0x1ec98,0x20cd8,96,0x1ed5c,0x1ed5c,0x20cdc,112,0x1ee20,0x1ee20,0x20ce0,384,0x1eee4,0x1eee4,0x20ce4,400,0x1efa8,0x1efa8,0x20ce8,416,0x1f06c,0x1f06c,0x20cec,432,0x1f130,0x1f130,0x20cf0,448,0x1f1f4,0x1f1f4,0x20cf4,464,0x1f2b8,0x1f2b8,0x20cf8,480,0x1f37c,0x1f37c,0x20cfc,496,0x1e810,0x1e800,0x20cc0,768,0x1e8d4,0x1e8c4,0x20cc4,784,0x1e998,0x1e988,0x20cc8,800,0x1ea5c,0x1ea4c,0x20ccc,816,0x1eb20,0x1eb10,0x20cd0,832,0x1ebe4,0x1ebd4,0x20cd4,848,0x1eca8,0x1ec98,0x20cd8,864,0x1ed6c,0x1ed5c,0x20cdc,880,0x1ee30,0x1ee20,0x20ce0,1152,0x1eef4,0x1eee4,0x20ce4,1168,0x1efb8,0x1efa8,0x20ce8,1184,0x1f07c,0x1f06c,0x20cec,1200,0x1f140,0x1f130,0x20cf0,1216,0x1f204,0x1f1f4,0x20cf4,1232,0x1f2c8,0x1f2b8,0x20cf8,1248,0x1f38c,0x1f37c,0x20cfc,1264,0x1e820,0x1e800,0x20cc0,1536,0x1e8e4,0x1e8c4,0x20cc4,1552,0x1e9a8,0x1e988,0x20cc8,1568,0x1ea6c,0x1ea4c,0x20ccc,1584,0x1eb30,0x1eb10,0x20cd0,1600,0x1ebf4,0x1ebd4,0x20cd4,1616,0x1ecb8,0x1ec98,0x20cd8,1632,0x1ed7c,0x1ed5c,0x20cdc,1648,0x1ee40,0x1ee20,0x20ce0,1920,0x1ef04,0x1eee4,0x20ce4,1936,0x1efc8,0x1efa8,0x20ce8,1952,0x1f08c,0x1f06c,0x20cec,1968,0x1f150,0x1f130,0x20cf0,1984,0x1f214,0x1f1f4,0x20cf4,2000,0x1f2d8,0x1f2b8,0x20cf8,2016,0x1f39c,0x1f37c,0x20cfc,2032,0x1e830,0x1e800,0x20cc0,2304,0x1e8f4,0x1e8c4,0x20cc4,2320,0x1e9b8,0x1e988,0x20cc8,2336,0x1ea7c,0x1ea4c,0x20ccc,2352,0x1eb40,0x1eb10,0x20cd0,2368,0x1ec04,0x1ebd4,0x20cd4,2384,0x1ecc8,0x1ec98,0x20cd8,2400,0x1ed8c,0x1ed5c,0x20cdc,2416,0x1ee50,0x1ee20,0x20ce0,2688,0x1ef14,0x1eee4,0x20ce4,2704,0x1efd8,0x1efa8,0x20ce8,2720,0x1f09c,0x1f06c,0x20cec,2736,0x1f160,0x1f130,0x20cf0,2752,0x1f224,0x1f1f4,0x20cf4,2768,0x1f2e8,0x1f2b8,0x20cf8,2784,0x1f3ac,0x1f37c,0x20cfc,2800,36864,0x26d70,0x1e800,0x1eb00,36880,0x26d74,0x1ec90,0x1ef90,36896,0x26d78,0x1f120,0x1f420,36912,0x26d7c,0x1f5b0,0x1f8b0,36928,0x26d80,0x1fa40,0x1fd40,36944,0x26d84,0x1fed0,0x201d0,36960,0x26d88,0x20360,0x20660,36976,0x26d8c,0x207f0,0x20af0,37248,0x26d90,0x20c80,0x20f80,37264,0x26d94,0x21110,0x21410,37280,0x26d98,0x215a0,0x218a0,37296,0x26d9c,0x21a30,0x21d30,37312,0x26da0,0x21ec0,0x221c0,37328,0x26da4,0x22350,0x22650,37344,0x26da8,0x227e0,0x22ae0,37360,0x26dac,0x22c70,0x22f70,37664,0x26db0,0x1e830,0x1eb08,37680,0x26db4,0x1ecc0,0x1ef98,37632,0x26db8,0x1f150,0x1f428,37648,0x26dbc,0x1f5e0,0x1f8b8,37728,0x26dc0,0x1fa70,0x1fd48,37744,0x26dc4,0x1ff00,0x201d8,37696,0x26dc8,0x20390,0x20668,37712,0x26dcc,0x20820,0x20af8,38048,0x26dd0,0x20cb0,0x20f88,38064,0x26dd4,0x21140,0x21418,38016,0x26dd8,0x215d0,0x218a8,38032,0x26ddc,0x21a60,0x21d38,38112,0x26de0,0x21ef0,0x221c8,38128,0x26de4,0x22380,0x22658,38080,0x26de8,0x22810,0x22ae8,38096,0x26dec,0x22ca0,0x22f78,38464,0x26df0,0x1e860,0x1eb10,38480,0x26df4,0x1ecf0,0x1efa0,38496,0x26df8,0x1f180,0x1f430,38512,0x26dfc,0x1f610,0x1f8c0,38400,0x26e00,0x1faa0,0x1fd50,38416,0x26e04,0x1ff30,0x201e0,38432,0x26e08,0x203c0,0x20670,38448,0x26e0c,0x20850,0x20b00,38848,0x26e10,0x20ce0,0x20f90,38864,0x26e14,0x21170,0x21420,38880,0x26e18,0x21600,0x218b0,38896,0x26e1c,0x21a90,0x21d40,38784,0x26e20,0x21f20,0x221d0,38800,0x26e24,0x223b0,0x22660,38816,0x26e28,0x22840,0x22af0,38832,0x26e2c,0x22cd0,0x22f80,39264,0x26e30,0x1e890,0x1eb18,39280,0x26e34,0x1ed20,0x1efa8,39232,0x26e38,0x1f1b0,0x1f438,39248,0x26e3c,0x1f640,0x1f8c8,39200,0x26e40,0x1fad0,0x1fd58,39216,0x26e44,0x1ff60,0x201e8,39168,0x26e48,0x203f0,0x20678,39184,0x26e4c,0x20880,0x20b08,39648,0x26e50,0x20d10,0x20f98,39664,0x26e54,0x211a0,0x21428,39616,0x26e58,0x21630,0x218b8,39632,0x26e5c,0x21ac0,0x21d48,39584,0x26e60,0x21f50,0x221d8,39600,0x26e64,0x223e0,0x22668,39552,0x26e68,0x22870,0x22af8,39568,0x26e6c,0x22d00,0x22f88,0x3f803f80,0x3f803f80,16256,0,0,0x3f803f80,0x3f800000,0,0,0x3f803f80,0,16256,0,0x3f803f80,0,0x3f800000,0x3f803f80,0,0,0,0,0,0,0,0,0,0,0,0,0,0,0,0x3f803f80,0,0,0,0,0,0,0,0,0,0,0,0,0,0,0,0x3f803f80,0,0,0,0,0,0,0,0,0,0,0,0,0,0,0,0x3f803f80,0,0,0,0,0,0,0,0,0,0,0,0,0,0,0,0x3f803f80,0x3f803f80,16256,0,0,0x3f803f80,0x3f800000,0,0,0x3f803f80,0,16256,0,0x3f803f80,0,0x3f800000,0x3f803f80,0,0,0,0,0,0,0,0,0,0,0,0,0,0,0,0x3f803f80,0,0,0,0,0,0,0,0,0,0,0,0,0,0,0,0x3f803f80,0,0,0,0,0,0,0,0,0,0,0,0,0,0,0,0x3f803f80,0,0,0,0,0,0,0,0,0,0,0,0,0,0,0,0x3f803f80,0x3f803f80,16256,0,0,0x3f803f80,0x3f800000,0,0,0x3f803f80,0,16256,0,0x3f803f80,0,0x3f800000,0x3f803f80,0,0,0,0,0,0,0,0,0,0,0,0,0,0,0,0x3f803f80,0,0,0,0,0,0,0,0,0,0,0,0,0,0,0,0x3f803f80,0,0,0,0,0,0,0,0,0,0,0,0,0,0,0,0x3f803f80,0,0,0,0,0,0,0,0,0,0,0,0,0,0,0,0x3f803f80,0x3f803f80,16256,0,0,0x3f803f80,0x3f800000,0,0,0x3f803f80,0,16256,0,0x3f803f80,0,0x3f800000,0,64,0x30000,0xf0000,144,208,0x30010,0xf0010,288,352,0x30020,0xf0020,432,496,0x30030,0xf0030,576,512,0x30040,0xf0040,720,656,0x30050,0xf0050,864,800,0x30060,0xf0060,1008,944,0x30070,0xf0070,1024,1088,0x30190,0xf0190,1168,1232,0x30180,0xf0180,1312,1376,0x301b0,0xf01b0,1456,1520,0x301a0,0xf01a0,1600,1536,0x301d0,0xf01d0,1744,1680,0x301c0,0xf01c0,1888,1824,0x301f0,0xf01f0,2032,1968,0x301e0,0xf01e0,16,80,0x30320,0x11f720,128,192,0x30330,0x11f730,304,368,0x30300,0x11f700,416,480,0x30310,0x11f710,592,528,0x30360,0x11f760,704,640,0x30370,0x11f770,880,816,0x30340,0x11f740,992,928,0x30350,0x11f750,1040,1104,0x304b0,0x11f8b0,1152,1216,0x304a0,0x11f8a0,1328,1392,0x30490,0x11f890,1440,1504,0x30480,0x11f880,1616,1552,0x304f0,0x11f8f0,1728,1664,0x304e0,0x11f8e0,1904,1840,0x304d0,0x11f8d0,2016,1952,0x304c0,0x11f8c0,32,96,0x30640,0x11fa40,176,240,0x30650,0x11fa50,256,320,0x30660,0x11fa60,400,464,0x30670,0x11fa70,608,544,0x30600,0x11fa00,752,688,0x30610,0x11fa10,832,768,0x30620,0x11fa20,976,912,0x30630,0x11fa30,1056,1120,0x307d0,0x11fbd0,1200,1264,0x307c0,0x11fbc0,1280,1344,0x307f0,0x11fbf0,1424,1488,0x307e0,0x11fbe0,1632,1568,0x30790,0x11fb90,1776,1712,0x30780,0x11fb80,1856,1792,0x307b0,0x11fbb0,2000,1936,0x307a0,0x11fba0,48,112,0x5fd60,0x11fd60,160,224,0x5fd70,0x11fd70,272,336,0x5fd40,0x11fd40,384,448,0x5fd50,0x11fd50,624,560,0x5fd20,0x11fd20,736,672,0x5fd30,0x11fd30,848,784,0x5fd00,0x11fd00,960,896,0x5fd10,0x11fd10,1072,1136,0x5fef0,0x11fef0,1184,1248,0x5fee0,0x11fee0,1296,1360,0x5fed0,0x11fed0,1408,1472,0x5fec0,0x11fec0,1648,1584,0x5feb0,0x11feb0,1760,1696,0x5fea0,0x11fea0,1872,1808,0x5fe90,0x11fe90,1984,1920,0x5fe80,0x11fe80,0xefb40,74752,78848,90624,0xefb50,74768,78864,90640,0xefb60,74784,78880,90656,0xefb70,74800,78896,90672,0xefb00,74816,78912,90688,0xefb10,74832,78928,90704,0xefb20,74848,78944,90720,0xefb30,74864,78960,90736,0xefcd0,74880,78976,90752,0xefcc0,74896,78992,90768,0xefcf0,74912,79008,90784,0xefce0,74928,79024,90800,0xefc90,74944,79040,90816,0xefc80,74960,79056,90832,0xefcb0,74976,79072,90848,0xefca0,74992,79088,90864,0xefe60,75008,79104,90880,0xefe70,75024,79120,90896,0xefe40,75040,79136,90912,0xefe50,75056,79152,90928,0xefe20,75072,79168,90944,0xefe30,75088,79184,90960,0xefe00,75104,79200,90976,0xefe10,75120,79216,90992,0xefff0,75136,79232,91008,0xeffe0,75152,79248,91024,0xeffd0,75168,79264,91040,0xeffc0,75184,79280,91056,0xeffb0,75200,79296,91072,0xeffa0,75216,79312,91088,0xeff90,75232,79328,91104,0xeff80,75248,79344,91120,0xefb40,75264,79360,91136,0xefb50,75280,79376,91152,0xefb60,75296,79392,91168,0xefb70,75312,79408,91184,0xefb00,75328,79424,91200,0xefb10,75344,79440,91216,0xefb20,75360,79456,91232,0xefb30,75376,79472,91248,0xefcd0,75392,79488,91264,0xefcc0,75408,79504,91280,0xefcf0,75424,79520,91296,0xefce0,75440,79536,91312,0xefc90,75456,79552,91328,0xefc80,75472,79568,91344,0xefcb0,75488,79584,91360,0xefca0,75504,79600,91376,0xefe60,75520,79616,91392,0xefe70,75536,79632,91408,0xefe40,75552,79648,91424,0xefe50,75568,79664,91440,0xefe20,75584,79680,91456,0xefe30,75600,79696,91472,0xefe00,75616,79712,91488,0xefe10,75632,79728,91504,0xefff0,75648,79744,91520,0xeffe0,75664,79760,91536,0xeffd0,75680,79776,91552,0xeffc0,75696,79792,91568,0xeffb0,75712,79808,91584,0xeffa0,75728,79824,91600,0xeff90,75744,79840,91616,0xeff80,75760,79856,91632,0x1c0a0,36864,39936,49160,0x1c0a4,36880,39952,49176,0x1c0a8,36896,39968,49192,0x1c0ac,36912,39984,49208,0x1c0b0,36928,40000,49224,0x1c0b4,36944,40016,49240,0x1c0b8,36960,40032,49256,0x1c0bc,36976,40048,49272,0x1c0c0,36992,40064,49288,0x1c0c4,37008,40080,49304,0x1c0c8,37024,40096,49320,0x1c0cc,37040,40112,49336,0x1c0d0,37056,40128,49352,0x1c0d4,37072,40144,49368,0x1c0d8,37088,40160,49384,0x1c0dc,37104,40176,49400,0x1c0e0,37120,40192,49416,0x1c0e4,37136,40208,49432,0x1c0e8,37152,40224,49448,0x1c0ec,37168,40240,49464,0x1c0f0,37184,40256,49480,0x1c0f4,37200,40272,49496,0x1c0f8,37216,40288,49512,0x1c0fc,37232,40304,49528,0x1c100,37248,40320,49544,0x1c104,37264,40336,49560,0x1c108,37280,40352,49576,0x1c10c,37296,40368,49592,0x1c110,37312,40384,49608,0x1c114,37328,40400,49624,0x1c118,37344,40416,49640,0x1c11c,37360,40432,49656,0x1c120,37376,40448,49672,0x1c124,37392,40464,49688,0x1c128,37408,40480,49704,0x1c12c,37424,40496,49720,0x1c130,37440,40512,49736,0x1c134,37456,40528,49752,0x1c138,37472,40544,49768,0x1c13c,37488,40560,49784,0x1c140,37504,40576,49800,0x1c144,37520,40592,49816,0x1c148,37536,40608,49832,0x1c14c,37552,40624,49848,0x1c150,37568,40640,49864,0x1c154,37584,40656,49880,0x1c158,37600,40672,49896,0x1c15c,37616,40688,49912,0x1c160,37632,40704,49928,0x1c164,37648,40720,49944,0x1c168,37664,40736,49960,0x1c16c,37680,40752,49976,0x1c170,37696,40768,49992,0x1c174,37712,40784,50008,0x1c178,37728,40800,50024,0x1c17c,37744,40816,50040,0x1c180,37760,40832,50056,0x1c184,37776,40848,50072,0x1c188,37792,40864,50088,0x1c18c,37808,40880,50104,0x1c190,37824,40896,50120,0x1c194,37840,40912,50136,0x1c198,37856,40928,50152,0x1c19c,37872,40944,50168,73824,79904,0x1d600,0x1d604,73968,80048,0x1d610,0x1d614,74048,80128,0x1d620,0x1d624,74192,80272,0x1d630,0x1d634,74272,80480,0x1d640,0x1d644,74416,80624,0x1d650,0x1d654,74496,80704,0x1d660,0x1d664,74640,80848,0x1d670,0x1d674,74848,80928,0x1d680,0x1d684,74992,81072,0x1d690,0x1d694,75072,81152,0x1d6a0,0x1d6a4,75216,81296,0x1d6b0,0x1d6b4,75296,81504,0x1d6c0,0x1d6c4,75440,81648,0x1d6d0,0x1d6d4,75520,81728,0x1d6e0,0x1d6e4,75664,81872,0x1d6f0,0x1d6f4,73840,79912,0x1d700,0x1d704,73952,80056,0x1d710,0x1d714,74064,80136,0x1d720,0x1d724,74176,80280,0x1d730,0x1d734,74288,80488,0x1d740,0x1d744,74400,80632,0x1d750,0x1d754,74512,80712,0x1d760,0x1d764,74624,80856,0x1d770,0x1d774,74864,80936,0x1d780,0x1d784,74976,81080,0x1d790,0x1d794,75088,81160,0x1d7a0,0x1d7a4,75200,81304,0x1d7b0,0x1d7b4,75312,81512,0x1d7c0,0x1d7c4,75424,81656,0x1d7d0,0x1d7d4,75536,81736,0x1d7e0,0x1d7e4,75648,81880,0x1d7f0,0x1d7f4,79872,79920,0x1d800,0x1d804,80016,80032,0x1d810,0x1d814,80160,80144,0x1d820,0x1d824,80304,80256,0x1d830,0x1d834,80448,80496,0x1d840,0x1d844,80592,80608,0x1d850,0x1d854,80736,80720,0x1d860,0x1d864,80880,80832,0x1d870,0x1d874,80896,80944,0x1d880,0x1d884,81040,81056,0x1d890,0x1d894,81184,81168,0x1d8a0,0x1d8a4,81328,81280,0x1d8b0,0x1d8b4,81472,81520,0x1d8c0,0x1d8c4,81616,81632,0x1d8d0,0x1d8d4,81760,81744,0x1d8e0,0x1d8e4,81904,81856,0x1d8f0,0x1d8f4,79888,79928,0x1d900,0x1d904,80000,80040,0x1d910,0x1d914,80176,80152,0x1d920,0x1d924,80288,80264,0x1d930,0x1d934,80464,80504,0x1d940,0x1d944,80576,80616,0x1d950,0x1d954,80752,80728,0x1d960,0x1d964,80864,80840,0x1d970,0x1d974,80912,80952,0x1d980,0x1d984,81024,81064,0x1d990,0x1d994,81200,81176,0x1d9a0,0x1d9a4,81312,81288,0x1d9b0,0x1d9b4,81488,81528,0x1d9c0,0x1d9c4,81600,81640,0x1d9d0,0x1d9d4,81776,81752,0x1d9e0,0x1d9e4,81888,81864,0x1d9f0,0x1d9f4,0x1e680,0x1e680,0x1e8c0,0x100000,0x1e682,0x100000,0x100000,0x1e8c0,0x1e684,0x100000,0x100000,0x100000,0x1e686,0x100000,0x100000,0x100000,0x1e690,0x1e680,0x100000,0x100000,0x1e692,0x100000,0x100000,0x1e8c0,0x1e694,0x100000,0x100000,0x100000,0x1e696,0x100000,0x100000,0x100000,0x1e6a0,0x1e680,0x100000,0x100000,0x1e6a2,0x100000,0x100000,0x1e8c0,0x1e6a4,0x100000,0x100000,0x100000,0x1e6a6,0x100000,0x100000,0x100000,0x1e6b0,0x1e680,0x100000,0x100000,0x1e6b2,0x100000,0x100000,0x1e8c0,0x1e6b4,0x100000,0x100000,0x100000,0x1e6b6,0x100000,0x100000,0x100000,0x1e688,0x1e690,0x1e8f0,0x100000,0x1e68a,0x100000,0x100000,0x1e8f0,0x1e68c,0x100000,0x100000,0x100000,0x1e68e,0x100000,0x100000,0x100000,0x1e698,0x1e690,0x100000,0x100000,0x1e69a,0x100000,0x100000,0x1e8f0,0x1e69c,0x100000,0x100000,0x100000,0x1e69e,0x100000,0x100000,0x100000,0x1e6a8,0x1e690,0x100000,0x100000,0x1e6aa,0x100000,0x100000,0x1e8f0,0x1e6ac,0x100000,0x100000,0x100000,0x1e6ae,0x100000,0x100000,0x100000,0x1e6b8,0x1e690,0x100000,0x100000,0x1e6ba,0x100000,0x100000,0x1e8f0,0x1e6bc,0x100000,0x100000,0x100000,0x1e6be,0x100000,0x100000,0x100000,0x1e6c0,0x1e6a0,0x1e920,0x100000,0x1e6c2,0x100000,0x100000,0x1e920,0x1e6c4,0x100000,0x100000,0x100000,0x1e6c6,0x100000,0x100000,0x100000,0x1e6d0,0x1e6a0,0x100000,0x100000,0x1e6d2,0x100000,0x100000,0x1e920,0x1e6d4,0x100000,0x100000,0x100000,0x1e6d6,0x100000,0x100000,0x100000,0x1e6e0,0x1e6a0,0x100000,0x100000,0x1e6e2,0x100000,0x100000,0x1e920,0x1e6e4,0x100000,0x100000,0x100000,0x1e6e6,0x100000,0x100000,0x100000,0x1e6f0,0x1e6a0,0x100000,0x100000,0x1e6f2,0x100000,0x100000,0x1e920,0x1e6f4,0x100000,0x100000,0x100000,0x1e6f6,0x100000,0x100000,0x100000,0x1e6c8,0x1e6b0,0x1e950,0x100000,0x1e6ca,0x100000,0x100000,0x1e950,0x1e6cc,0x100000,0x100000,0x100000,0x1e6ce,0x100000,0x100000,0x100000,0x1e6d8,0x1e6b0,0x100000,0x100000,0x1e6da,0x100000,0x100000,0x1e950,0x1e6dc,0x100000,0x100000,0x100000,0x1e6de,0x100000,0x100000,0x100000,0x1e6e8,0x1e6b0,0x100000,0x100000,0x1e6ea,0x100000,0x100000,0x1e950,0x1e6ec,0x100000,0x100000,0x100000,0x1e6ee,0x100000,0x100000,0x100000,0x1e6f8,0x1e6b0,0x100000,0x100000,0x1e6fa,0x100000,0x100000,0x1e950,0x1e6fc,0x100000,0x100000,0x100000,0x1e6fe,0x100000,0x100000,0x100000,0x1eb60,0x251f0,36960,37024,0x1eb62,0x251f2,36976,37032,0x1eb64,0x251f4,36992,37040,0x1eb66,0x251f6,37008,37048,0x1eb68,0x25350,37344,37408,0x1eb6a,0x25352,37360,37416,0x1eb6c,0x25354,37376,37424,0x1eb6e,0x25356,37392,37432,0x1eb70,0x254b0,37696,37760,0x1eb72,0x254b2,37712,37768,0x1eb74,0x254b4,37792,37776,0x1eb76,0x254b6,37808,37784,0x1eb78,0x25610,38080,38144,0x1eb7a,0x25612,38096,38152,0x1eb7c,0x25614,38176,38160,0x1eb7e,0x25616,38192,38168,0x1eb60,0x251f8,38432,38624,0x1eb62,0x251fa,38448,38632,0x1eb64,0x251fc,38592,38640,0x1eb66,0x251fe,38608,38648,0x1eb68,0x25358,38816,39008,0x1eb6a,0x2535a,38832,39016,0x1eb6c,0x2535c,38976,39024,0x1eb6e,0x2535e,38992,39032,0x1eb70,0x254b8,39168,39360,0x1eb72,0x254ba,39184,39368,0x1eb74,0x254bc,39392,39376,0x1eb76,0x254be,39408,39384,0x1eb78,0x25618,39552,39744,0x1eb7a,0x2561a,39568,39752,0x1eb7c,0x2561c,39776,39760,0x1eb7e,0x2561e,39792,39768,0x1eb60,0x26278,40032,40096,0x1eb62,0x2627a,40048,40104,0x1eb64,0x2627c,40064,40112,0x1eb66,0x2627e,40080,40120,0x1eb68,0x263d8,40416,40480,0x1eb6a,0x263da,40432,40488,0x1eb6c,0x263dc,40448,40496,0x1eb6e,0x263de,40464,40504,0x1eb70,0x26538,40768,40832,0x1eb72,0x2653a,40784,40840,0x1eb74,0x2653c,40864,40848,0x1eb76,0x2653e,40880,40856,0x1eb78,0x26698,41152,41216,0x1eb7a,0x2669a,41168,41224,0x1eb7c,0x2669c,41248,41232,0x1eb7e,0x2669e,41264,41240,0x1eb60,0x100000,41504,41696,0x1eb62,0x100000,41520,41704,0x1eb64,0x100000,41664,41712,0x1eb66,0x100000,41680,41720,0x1eb68,0x100000,41888,42080,0x1eb6a,0x100000,41904,42088,0x1eb6c,0x100000,42048,42096,0x1eb6e,0x100000,42064,42104,0x1eb70,0x100000,42240,42432,0x1eb72,0x100000,42256,42440,0x1eb74,0x100000,42464,42448,0x1eb76,0x100000,42480,42456,0x1eb78,0x100000,42624,42816,0x1eb7a,0x100000,42640,42824,0x1eb7c,0x100000,42848,42832,0x1eb7e,0x100000,42864,42840,36960,36992,37024,46080,36968,37000,37032,46096,36976,37008,37040,46112,36984,37016,37048,46128,38112,38144,38176,46144,38120,38152,38184,46160,38128,38160,38192,46176,38136,38168,38200,46192,39232,39328,39296,46208,39240,39336,39304,46224,39248,39344,39312,46240,39256,39352,39320,46256,40384,40480,40448,46272,40392,40488,40456,46288,40400,40496,40464,46304,40408,40504,40472,46320,46112,46272,46304,46336,46120,46280,46312,46352,46128,46288,46320,46368,46136,46296,46328,46384,47264,47424,47456,46400,47272,47432,47464,46416,47280,47440,47472,46432,47288,47448,47480,46448,48384,48608,48576,46464,48392,48616,48584,46480,48400,48624,48592,46496,48408,48632,48600,46512,49536,49760,49728,46528,49544,49768,49736,46544,49552,49776,49744,46560,49560,49784,49752,46576,55392,55424,55456,46592,55400,55432,55464,46608,55408,55440,55472,46624,55416,55448,55480,46640,56544,56576,56608,46656,56552,56584,56616,46672,56560,56592,56624,46688,56568,56600,56632,46704,57664,57760,57728,46720,57672,57768,57736,46736,57680,57776,57744,46752,57688,57784,57752,46768,58816,58912,58880,46784,58824,58920,58888,46800,58832,58928,58896,46816,58840,58936,58904,46832,64544,64704,64736,46848,64552,64712,64744,46864,64560,64720,64752,46880,64568,64728,64760,46896,65696,65856,65888,46912,65704,65864,65896,46928,65712,65872,65904,46944,65720,65880,65912,46960,66816,67040,67008,46976,66824,67048,67016,46992,66832,67056,67024,47008,66840,67064,67032,47024,67968,68192,68160,47040,67976,68200,68168,47056,67984,68208,68176,47072,67992,68216,68184,47088,0x1e800,0x1e800,0x20cc0,18432,0x1e8c4,0x1e8c4,0x20cc4,18448,0x1e988,0x1e988,0x20cc8,18464,0x1ea4c,0x1ea4c,0x20ccc,18480,0x1eb10,0x1eb10,0x20cd0,18496,0x1ebd4,0x1ebd4,0x20cd4,18512,0x1ec98,0x1ec98,0x20cd8,18528,0x1ed5c,0x1ed5c,0x20cdc,18544,0x1ee20,0x1ee20,0x20ce0,18816,0x1eee4,0x1eee4,0x20ce4,18832,0x1efa8,0x1efa8,0x20ce8,18848,0x1f06c,0x1f06c,0x20cec,18864,0x1f130,0x1f130,0x20cf0,18880,0x1f1f4,0x1f1f4,0x20cf4,18896,0x1f2b8,0x1f2b8,0x20cf8,18912,0x1f37c,0x1f37c,0x20cfc,18928,0x1e810,0x1e800,0x20cc0,19200,0x1e8d4,0x1e8c4,0x20cc4,19216,0x1e998,0x1e988,0x20cc8,19232,0x1ea5c,0x1ea4c,0x20ccc,19248,0x1eb20,0x1eb10,0x20cd0,19264,0x1ebe4,0x1ebd4,0x20cd4,19280,0x1eca8,0x1ec98,0x20cd8,19296,0x1ed6c,0x1ed5c,0x20cdc,19312,0x1ee30,0x1ee20,0x20ce0,19584,0x1eef4,0x1eee4,0x20ce4,19600,0x1efb8,0x1efa8,0x20ce8,19616,0x1f07c,0x1f06c,0x20cec,19632,0x1f140,0x1f130,0x20cf0,19648,0x1f204,0x1f1f4,0x20cf4,19664,0x1f2c8,0x1f2b8,0x20cf8,19680,0x1f38c,0x1f37c,0x20cfc,19696,0x1e820,0x1e800,0x20cc0,19968,0x1e8e4,0x1e8c4,0x20cc4,19984,0x1e9a8,0x1e988,0x20cc8,20000,0x1ea6c,0x1ea4c,0x20ccc,20016,0x1eb30,0x1eb10,0x20cd0,20032,0x1ebf4,0x1ebd4,0x20cd4,20048,0x1ecb8,0x1ec98,0x20cd8,20064,0x1ed7c,0x1ed5c,0x20cdc,20080,0x1ee40,0x1ee20,0x20ce0,20352,0x1ef04,0x1eee4,0x20ce4,20368,0x1efc8,0x1efa8,0x20ce8,20384,0x1f08c,0x1f06c,0x20cec,20400,0x1f150,0x1f130,0x20cf0,20416,0x1f214,0x1f1f4,0x20cf4,20432,0x1f2d8,0x1f2b8,0x20cf8,20448,0x1f39c,0x1f37c,0x20cfc,20464,0x1e830,0x1e800,0x20cc0,20736,0x1e8f4,0x1e8c4,0x20cc4,20752,0x1e9b8,0x1e988,0x20cc8,20768,0x1ea7c,0x1ea4c,0x20ccc,20784,0x1eb40,0x1eb10,0x20cd0,20800,0x1ec04,0x1ebd4,0x20cd4,20816,0x1ecc8,0x1ec98,0x20cd8,20832,0x1ed8c,0x1ed5c,0x20cdc,20848,0x1ee50,0x1ee20,0x20ce0,21120,0x1ef14,0x1eee4,0x20ce4,21136,0x1efd8,0x1efa8,0x20ce8,21152,0x1f09c,0x1f06c,0x20cec,21168,0x1f160,0x1f130,0x20cf0,21184,0x1f224,0x1f1f4,0x20cf4,21200,0x1f2e8,0x1f2b8,0x20cf8,21216,0x1f3ac,0x1f37c,0x20cfc,21232,55296,0x26e70,0x1e8c0,0x1eb60,55312,0x26e74,0x1ed50,0x1eff0,55328,0x26e78,0x1f1e0,0x1f480,55344,0x26e7c,0x1f670,0x1f910,55360,0x26e80,0x1fb00,0x1fda0,55376,0x26e84,0x1ff90,0x20230,55392,0x26e88,0x20420,0x206c0,55408,0x26e8c,0x208b0,0x20b50,55680,0x26e90,0x20d40,0x20fe0,55696,0x26e94,0x211d0,0x21470,55712,0x26e98,0x21660,0x21900,55728,0x26e9c,0x21af0,0x21d90,55744,0x26ea0,0x21f80,0x22220,55760,0x26ea4,0x22410,0x226b0,55776,0x26ea8,0x228a0,0x22b40,55792,0x26eac,0x22d30,0x22fd0,56096,0x26eb0,0x1e8f0,0x1eb68,56112,0x26eb4,0x1ed80,0x1eff8,56064,0x26eb8,0x1f210,0x1f488,56080,0x26ebc,0x1f6a0,0x1f918,56160,0x26ec0,0x1fb30,0x1fda8,56176,0x26ec4,0x1ffc0,0x20238,56128,0x26ec8,0x20450,0x206c8,56144,0x26ecc,0x208e0,0x20b58,56480,0x26ed0,0x20d70,0x20fe8,56496,0x26ed4,0x21200,0x21478,56448,0x26ed8,0x21690,0x21908,56464,0x26edc,0x21b20,0x21d98,56544,0x26ee0,0x21fb0,0x22228,56560,0x26ee4,0x22440,0x226b8,56512,0x26ee8,0x228d0,0x22b48,56528,0x26eec,0x22d60,0x22fd8,56896,0x26ef0,0x1e920,0x1eb70,56912,0x26ef4,0x1edb0,0x1f000,56928,0x26ef8,0x1f240,0x1f490,56944,0x26efc,0x1f6d0,0x1f920,56832,0x26f00,0x1fb60,0x1fdb0,56848,0x26f04,0x1fff0,0x20240,56864,0x26f08,0x20480,0x206d0,56880,0x26f0c,0x20910,0x20b60,57280,0x26f10,0x20da0,0x20ff0,57296,0x26f14,0x21230,0x21480,57312,0x26f18,0x216c0,0x21910,57328,0x26f1c,0x21b50,0x21da0,57216,0x26f20,0x21fe0,0x22230,57232,0x26f24,0x22470,0x226c0,57248,0x26f28,0x22900,0x22b50,57264,0x26f2c,0x22d90,0x22fe0,57696,0x26f30,0x1e950,0x1eb78,57712,0x26f34,0x1ede0,0x1f008,57664,0x26f38,0x1f270,0x1f498,57680,0x26f3c,0x1f700,0x1f928,57632,0x26f40,0x1fb90,0x1fdb8,57648,0x26f44,0x20020,0x20248,57600,0x26f48,0x204b0,0x206d8,57616,0x26f4c,0x20940,0x20b68,58080,0x26f50,0x20dd0,0x20ff8,58096,0x26f54,0x21260,0x21488,58048,0x26f58,0x216f0,0x21918,58064,0x26f5c,0x21b80,0x21da8,58016,0x26f60,0x22010,0x22238,58032,0x26f64,0x224a0,0x226c8,57984,0x26f68,0x22930,0x22b58,58000,0x26f6c,0x22dc0,0x22fe8,0x3f803f80,0x3f803f80,16256,0,0,0x3f803f80,0x3f800000,0,0,0x3f803f80,0,16256,0,0x3f803f80,0,0x3f800000,0x3f803f80,0,0,0,0,0,0,0,0,0,0,0,0,0,0,0,0x3f803f80,0,0,0,0,0,0,0,0,0,0,0,0,0,0,0,0x3f803f80,0,0,0,0,0,0,0,0,0,0,0,0,0,0,0,0x3f803f80,0,0,0,0,0,0,0,0,0,0,0,0,0,0,0,0x3f803f80,0x3f803f80,16256,0,0,0x3f803f80,0x3f800000,0,0,0x3f803f80,0,16256,0,0x3f803f80,0,0x3f800000,0x3f803f80,0,0,0,0,0,0,0,0,0,0,0,0,0,0,0,0x3f803f80,0,0,0,0,0,0,0,0,0,0,0,0,0,0,0,0x3f803f80,0,0,0,0,0,0,0,0,0,0,0,0,0,0,0,0x3f803f80,0,0,0,0,0,0,0,0,0,0,0,0,0,0,0,0x3f803f80,0x3f803f80,16256,0,0,0x3f803f80,0x3f800000,0,0,0x3f803f80,0,16256,0,0x3f803f80,0,0x3f800000,0x3f803f80,0,0,0,0,0,0,0,0,0,0,0,0,0,0,0,0x3f803f80,0,0,0,0,0,0,0,0,0,0,0,0,0,0,0,0x3f803f80,0,0,0,0,0,0,0,0,0,0,0,0,0,0,0,0x3f803f80,0,0,0,0,0,0,0,0,0,0,0,0,0,0,0,0x3f803f80,0x3f803f80,16256,0,0,0x3f803f80,0x3f800000,0,0,0x3f803f80,0,16256,0,0x3f803f80,0,0x3f800000,0,64,0x60000,0xc0080,144,208,0x60010,0xc0090,288,352,0x60020,0xc00a0,432,496,0x60030,0xc00b0,576,512,0x60040,0xc00c0,720,656,0x60050,0xc00d0,864,800,0x60060,0xc00e0,1008,944,0x60070,0xc00f0,1024,1088,0x60190,0xc0210,1168,1232,0x60180,0xc0200,1312,1376,0x601b0,0xc0230,1456,1520,0x601a0,0xc0220,1600,1536,0x601d0,0xc0250,1744,1680,0x601c0,0xc0240,1888,1824,0x601f0,0xc0270,2032,1968,0x601e0,0xc0260,16,80,0x60320,0xc03a0,128,192,0x60330,0xc03b0,304,368,0x60300,0xc0380,416,480,0x60310,0xc0390,592,528,0x60360,0xc03e0,704,640,0x60370,0xc03f0,880,816,0x60340,0xc03c0,992,928,0x60350,0xc03d0,1040,1104,0x604b0,0xef930,1152,1216,0x604a0,0xef920,1328,1392,0x60490,0xef910,1440,1504,0x60480,0xef900,1616,1552,0x604f0,0xef970,1728,1664,0x604e0,0xef960,1904,1840,0x604d0,0xef950,2016,1952,0x604c0,0xef940,32,96,0x60640,0xefac0,176,240,0x60650,0xefad0,256,320,0x60660,0xefae0,400,464,0x60670,0xefaf0,608,544,0x60600,0xefa80,752,688,0x60610,0xefa90,832,768,0x60620,0xefaa0,976,912,0x60630,0xefab0,1056,1120,0x8fbd0,0xefc50,1200,1264,0x8fbc0,0xefc40,1280,1344,0x8fbf0,0xefc70,1424,1488,0x8fbe0,0xefc60,1632,1568,0x8fb90,0xefc10,1776,1712,0x8fb80,0xefc00,1856,1792,0x8fbb0,0xefc30,2000,1936,0x8fba0,0xefc20,48,112,0x8fd60,0xefde0,160,224,0x8fd70,0xefdf0,272,336,0x8fd40,0xefdc0,384,448,0x8fd50,0xefdd0,624,560,0x8fd20,0xefda0,736,672,0x8fd30,0xefdb0,848,784,0x8fd00,0xefd80,960,896,0x8fd10,0xefd90,1072,1136,0x8fef0,0xeff70,1184,1248,0x8fee0,0xeff60,1296,1360,0x8fed0,0xeff50,1408,1472,0x8fec0,0xeff40,1648,1584,0x8feb0,0xeff30,1760,1696,0x8fea0,0xeff20,1872,1808,0x8fe90,0xeff10,1984,1920,0x8fe80,0xeff00,0xf0100,75776,83968,91136,0xf0110,75792,83984,91152,0xf0120,75808,84000,91168,0xf0130,75824,84016,91184,0xf0140,75840,84032,91200,0xf0150,75856,84048,91216,0xf0160,75872,84064,91232,0xf0170,75888,84080,91248,0xf0290,75904,84096,91264,0xf0280,75920,84112,91280,0xf02b0,75936,84128,91296,0xf02a0,75952,84144,91312,0xf02d0,75968,84160,91328,0xf02c0,75984,84176,91344,0xf02f0,76000,84192,91360,0xf02e0,76016,84208,91376,0x11f820,76032,84224,91392,0x11f830,76048,84240,91408,0x11f800,76064,84256,91424,0x11f810,76080,84272,91440,0x11f860,76096,84288,91456,0x11f870,76112,84304,91472,0x11f840,76128,84320,91488,0x11f850,76144,84336,91504,0x11f9b0,76160,84352,91520,0x11f9a0,76176,84368,91536,0x11f990,76192,84384,91552,0x11f980,76208,84400,91568,0x11f9f0,76224,84416,91584,0x11f9e0,76240,84432,91600,0x11f9d0,76256,84448,91616,0x11f9c0,76272,84464,91632,0xf0100,76288,84480,91648,0xf0110,76304,84496,91664,0xf0120,76320,84512,91680,0xf0130,76336,84528,91696,0xf0140,76352,84544,91712,0xf0150,76368,84560,91728,0xf0160,76384,84576,91744,0xf0170,76400,84592,91760,0xf0290,76416,84608,91776,0xf0280,76432,84624,91792,0xf02b0,76448,84640,91808,0xf02a0,76464,84656,91824,0xf02d0,76480,84672,91840,0xf02c0,76496,84688,91856,0xf02f0,76512,84704,91872,0xf02e0,76528,84720,91888,0x11f820,76544,84736,91904,0x11f830,76560,84752,91920,0x11f800,76576,84768,91936,0x11f810,76592,84784,91952,0x11f860,76608,84800,91968,0x11f870,76624,84816,91984,0x11f840,76640,84832,92000,0x11f850,76656,84848,92016,0x11f9b0,76672,84864,92032,0x11f9a0,76688,84880,92048,0x11f990,76704,84896,92064,0x11f980,76720,84912,92080,0x11f9f0,76736,84928,92096,0x11f9e0,76752,84944,92112,0x11f9d0,76768,84960,92128,0x11f9c0,76784,84976,92144,0x1c140,36864,43008,52224,0x1c144,36880,43024,52240,0x1c148,36896,43040,52256,0x1c14c,36912,43056,52272,0x1c150,36928,43072,52288,0x1c154,36944,43088,52304,0x1c158,36960,43104,52320,0x1c15c,36976,43120,52336,0x1c160,36992,43136,52352,0x1c164,37008,43152,52368,0x1c168,37024,43168,52384,0x1c16c,37040,43184,52400,0x1c170,37056,43200,52416,0x1c174,37072,43216,52432,0x1c178,37088,43232,52448,0x1c17c,37104,43248,52464,0x1c180,37120,43264,52480,0x1c184,37136,43280,52496,0x1c188,37152,43296,52512,0x1c18c,37168,43312,52528,0x1c190,37184,43328,52544,0x1c194,37200,43344,52560,0x1c198,37216,43360,52576,0x1c19c,37232,43376,52592,0x1c1a0,37248,43392,52608,0x1c1a4,37264,43408,52624,0x1c1a8,37280,43424,52640,0x1c1ac,37296,43440,52656,0x1c1b0,37312,43456,52672,0x1c1b4,37328,43472,52688,0x1c1b8,37344,43488,52704,0x1c1bc,37360,43504,52720,0x1c1c0,37376,43520,52736,0x1c1c4,37392,43536,52752,0x1c1c8,37408,43552,52768,0x1c1cc,37424,43568,52784,0x1c1d0,37440,43584,52800,0x1c1d4,37456,43600,52816,0x1c1d8,37472,43616,52832,0x1c1dc,37488,43632,52848,0x1c1e0,37504,43648,52864,0x1c1e4,37520,43664,52880,0x1c1e8,37536,43680,52896,0x1c1ec,37552,43696,52912,0x1c1f0,37568,43712,52928,0x1c1f4,37584,43728,52944,0x1c1f8,37600,43744,52960,0x1c1fc,37616,43760,52976,0x1c200,37632,43776,52992,0x1c204,37648,43792,53008,0x1c208,37664,43808,53024,0x1c20c,37680,43824,53040,0x1c210,37696,43840,53056,0x1c214,37712,43856,53072,0x1c218,37728,43872,53088,0x1c21c,37744,43888,53104,0x1c220,37760,43904,53120,0x1c224,37776,43920,53136,0x1c228,37792,43936,53152,0x1c22c,37808,43952,53168,0x1c230,37824,43968,53184,0x1c234,37840,43984,53200,0x1c238,37856,44000,53216,0x1c23c,37872,44016,53232,79936,86016,0x1d600,0x1d608,80080,86160,0x1d610,0x1d618,80224,86304,0x1d620,0x1d628,80368,86448,0x1d630,0x1d638,80384,86592,0x1d640,0x1d648,80528,86736,0x1d650,0x1d658,80672,86880,0x1d660,0x1d668,80816,87024,0x1d670,0x1d678,80960,87040,0x1d680,0x1d688,81104,87184,0x1d690,0x1d698,81248,87328,0x1d6a0,0x1d6a8,81392,87472,0x1d6b0,0x1d6b8,81408,87616,0x1d6c0,0x1d6c8,81552,87760,0x1d6d0,0x1d6d8,81696,87904,0x1d6e0,0x1d6e8,81840,88048,0x1d6f0,0x1d6f8,79952,86024,0x1d700,0x1d708,80064,86168,0x1d710,0x1d718,80240,86312,0x1d720,0x1d728,80352,86456,0x1d730,0x1d738,80400,86600,0x1d740,0x1d748,80512,86744,0x1d750,0x1d758,80688,86888,0x1d760,0x1d768,80800,87032,0x1d770,0x1d778,80976,87048,0x1d780,0x1d788,81088,87192,0x1d790,0x1d798,81264,87336,0x1d7a0,0x1d7a8,81376,87480,0x1d7b0,0x1d7b8,81424,87624,0x1d7c0,0x1d7c8,81536,87768,0x1d7d0,0x1d7d8,81712,87912,0x1d7e0,0x1d7e8,81824,88056,0x1d7f0,0x1d7f8,79968,86032,0x1d800,0x1d808,80112,86144,0x1d810,0x1d818,80192,86320,0x1d820,0x1d828,80336,86432,0x1d830,0x1d838,80416,86608,0x1d840,0x1d848,80560,86720,0x1d850,0x1d858,80640,86896,0x1d860,0x1d868,80784,87008,0x1d870,0x1d878,80992,87056,0x1d880,0x1d888,81136,87168,0x1d890,0x1d898,81216,87344,0x1d8a0,0x1d8a8,81360,87456,0x1d8b0,0x1d8b8,81440,87632,0x1d8c0,0x1d8c8,81584,87744,0x1d8d0,0x1d8d8,81664,87920,0x1d8e0,0x1d8e8,81808,88032,0x1d8f0,0x1d8f8,79984,86040,0x1d900,0x1d908,80096,86152,0x1d910,0x1d918,80208,86328,0x1d920,0x1d928,80320,86440,0x1d930,0x1d938,80432,86616,0x1d940,0x1d948,80544,86728,0x1d950,0x1d958,80656,86904,0x1d960,0x1d968,80768,87016,0x1d970,0x1d978,81008,87064,0x1d980,0x1d988,81120,87176,0x1d990,0x1d998,81232,87352,0x1d9a0,0x1d9a8,81344,87464,0x1d9b0,0x1d9b8,81456,87640,0x1d9c0,0x1d9c8,81568,87752,0x1d9d0,0x1d9d8,81680,87928,0x1d9e0,0x1d9e8,81792,88040,0x1d9f0,0x1d9f8,0x1e700,0x1e700,0x1e980,0x100000,0x1e702,0x100000,0x100000,0x1e980,0x1e704,0x100000,0x100000,0x100000,0x1e706,0x100000,0x100000,0x100000,0x1e710,0x1e700,0x100000,0x100000,0x1e712,0x100000,0x100000,0x1e980,0x1e714,0x100000,0x100000,0x100000,0x1e716,0x100000,0x100000,0x100000,0x1e720,0x1e700,0x100000,0x100000,0x1e722,0x100000,0x100000,0x1e980,0x1e724,0x100000,0x100000,0x100000,0x1e726,0x100000,0x100000,0x100000,0x1e730,0x1e700,0x100000,0x100000,0x1e732,0x100000,0x100000,0x1e980,0x1e734,0x100000,0x100000,0x100000,0x1e736,0x100000,0x100000,0x100000,0x1e708,0x1e710,0x1e9b0,0x100000,0x1e70a,0x100000,0x100000,0x1e9b0,0x1e70c,0x100000,0x100000,0x100000,0x1e70e,0x100000,0x100000,0x100000,0x1e718,0x1e710,0x100000,0x100000,0x1e71a,0x100000,0x100000,0x1e9b0,0x1e71c,0x100000,0x100000,0x100000,0x1e71e,0x100000,0x100000,0x100000,0x1e728,0x1e710,0x100000,0x100000,0x1e72a,0x100000,0x100000,0x1e9b0,0x1e72c,0x100000,0x100000,0x100000,0x1e72e,0x100000,0x100000,0x100000,0x1e738,0x1e710,0x100000,0x100000,0x1e73a,0x100000,0x100000,0x1e9b0,0x1e73c,0x100000,0x100000,0x100000,0x1e73e,0x100000,0x100000,0x100000,0x1e740,0x1e720,0x1e9e0,0x100000,0x1e742,0x100000,0x100000,0x1e9e0,0x1e744,0x100000,0x100000,0x100000,0x1e746,0x100000,0x100000,0x100000,0x1e750,0x1e720,0x100000,0x100000,0x1e752,0x100000,0x100000,0x1e9e0,0x1e754,0x100000,0x100000,0x100000,0x1e756,0x100000,0x100000,0x100000,0x1e760,0x1e720,0x100000,0x100000,0x1e762,0x100000,0x100000,0x1e9e0,0x1e764,0x100000,0x100000,0x100000,0x1e766,0x100000,0x100000,0x100000,0x1e770,0x1e720,0x100000,0x100000,0x1e772,0x100000,0x100000,0x1e9e0,0x1e774,0x100000,0x100000,0x100000,0x1e776,0x100000,0x100000,0x100000,0x1e748,0x1e730,0x1ea10,0x100000,0x1e74a,0x100000,0x100000,0x1ea10,0x1e74c,0x100000,0x100000,0x100000,0x1e74e,0x100000,0x100000,0x100000,0x1e758,0x1e730,0x100000,0x100000,0x1e75a,0x100000,0x100000,0x1ea10,0x1e75c,0x100000,0x100000,0x100000,0x1e75e,0x100000,0x100000,0x100000,0x1e768,0x1e730,0x100000,0x100000,0x1e76a,0x100000,0x100000,0x1ea10,0x1e76c,0x100000,0x100000,0x100000,0x1e76e,0x100000,0x100000,0x100000,0x1e778,0x1e730,0x100000,0x100000,0x1e77a,0x100000,0x100000,0x1ea10,0x1e77c,0x100000,0x100000,0x100000,0x1e77e,0x100000,0x100000,0x100000,0x1ebc0,0x25770,37056,37120,0x1ebc2,0x25772,37072,37128,0x1ebc4,0x25774,37088,37136,0x1ebc6,0x25776,37104,37144,0x1ebc8,0x258d0,37440,37504,0x1ebca,0x258d2,37456,37512,0x1ebcc,0x258d4,37472,37520,0x1ebce,0x258d6,37488,37528,0x1ebd0,0x25a30,37856,37920,0x1ebd2,0x25a32,37872,37928,0x1ebd4,0x25a34,37824,37936,0x1ebd6,0x25a36,37840,37944,0x1ebd8,0x25b90,38240,38304,0x1ebda,0x25b92,38256,38312,0x1ebdc,0x25b94,38208,38320,0x1ebde,0x25b96,38224,38328,0x1ebc0,0x25778,38528,38720,0x1ebc2,0x2577a,38544,38728,0x1ebc4,0x2577c,38560,38736,0x1ebc6,0x2577e,38576,38744,0x1ebc8,0x258d8,38912,39104,0x1ebca,0x258da,38928,39112,0x1ebcc,0x258dc,38944,39120,0x1ebce,0x258de,38960,39128,0x1ebd0,0x25a38,39328,39520,0x1ebd2,0x25a3a,39344,39528,0x1ebd4,0x25a3c,39296,39536,0x1ebd6,0x25a3e,39312,39544,0x1ebd8,0x25b98,39712,39904,0x1ebda,0x25b9a,39728,39912,0x1ebdc,0x25b9c,39680,39920,0x1ebde,0x25b9e,39696,39928,0x1ebc0,0x267f0,40128,40192,0x1ebc2,0x267f2,40144,40200,0x1ebc4,0x267f4,40160,40208,0x1ebc6,0x267f6,40176,40216,0x1ebc8,0x26950,40512,40576,0x1ebca,0x26952,40528,40584,0x1ebcc,0x26954,40544,40592,0x1ebce,0x26956,40560,40600,0x1ebd0,0x26ab0,40928,40992,0x1ebd2,0x26ab2,40944,41000,0x1ebd4,0x26ab4,40896,41008,0x1ebd6,0x26ab6,40912,41016,0x1ebd8,0x26c10,41312,41376,0x1ebda,0x26c12,41328,41384,0x1ebdc,0x26c14,41280,41392,0x1ebde,0x26c16,41296,41400,0x1ebc0,0x100000,41600,41792,0x1ebc2,0x100000,41616,41800,0x1ebc4,0x100000,41632,41808,0x1ebc6,0x100000,41648,41816,0x1ebc8,0x100000,41984,42176,0x1ebca,0x100000,42000,42184,0x1ebcc,0x100000,42016,42192,0x1ebce,0x100000,42032,42200,0x1ebd0,0x100000,42400,42592,0x1ebd2,0x100000,42416,42600,0x1ebd4,0x100000,42368,42608,0x1ebd6,0x100000,42384,42616,0x1ebd8,0x100000,42784,42976,0x1ebda,0x100000,42800,42984,0x1ebdc,0x100000,42752,42992,0x1ebde,0x100000,42768,43000,37056,37088,37120,55296,37064,37096,37128,55312,37072,37104,37136,55328,37080,37112,37144,55344,38208,38240,38272,55360,38216,38248,38280,55376,38224,38256,38288,55392,38232,38264,38296,55408,39392,39360,39456,55424,39400,39368,39464,55440,39408,39376,39472,55456,39416,39384,39480,55472,40544,40512,40608,55488,40552,40520,40616,55504,40560,40528,40624,55520,40568,40536,40632,55536,46208,46240,46400,55552,46216,46248,46408,55568,46224,46256,46416,55584,46232,46264,46424,55600,47360,47392,47552,55616,47368,47400,47560,55632,47376,47408,47568,55648,47384,47416,47576,55664,48544,48512,48736,55680,48552,48520,48744,55696,48560,48528,48752,55712,48568,48536,48760,55728,49696,49664,49888,55744,49704,49672,49896,55760,49712,49680,49904,55776,49720,49688,49912,55792,55488,55520,55552,55808,55496,55528,55560,55824,55504,55536,55568,55840,55512,55544,55576,55856,56640,56672,56704,55872,56648,56680,56712,55888,56656,56688,56720,55904,56664,56696,56728,55920,57824,57792,57888,55936,57832,57800,57896,55952,57840,57808,57904,55968,57848,57816,57912,55984,58976,58944,59040,56000,58984,58952,59048,56016,58992,58960,59056,56032,59000,58968,59064,56048,64640,64672,64832,56064,64648,64680,64840,56080,64656,64688,64848,56096,64664,64696,64856,56112,65792,65824,65984,56128,65800,65832,65992,56144,65808,65840,66000,56160,65816,65848,66008,56176,66976,66944,67168,56192,66984,66952,67176,56208,66992,66960,67184,56224,67000,66968,67192,56240,68128,68096,68320,56256,68136,68104,68328,56272,68144,68112,68336,56288,68152,68120,68344,56304,0x1e800,0x1e800,0x20cc0,36864,0x1e8c4,0x1e8c4,0x20cc4,36880,0x1e988,0x1e988,0x20cc8,36896,0x1ea4c,0x1ea4c,0x20ccc,36912,0x1eb10,0x1eb10,0x20cd0,36928,0x1ebd4,0x1ebd4,0x20cd4,36944,0x1ec98,0x1ec98,0x20cd8,36960,0x1ed5c,0x1ed5c,0x20cdc,36976,0x1ee20,0x1ee20,0x20ce0,37248,0x1eee4,0x1eee4,0x20ce4,37264,0x1efa8,0x1efa8,0x20ce8,37280,0x1f06c,0x1f06c,0x20cec,37296,0x1f130,0x1f130,0x20cf0,37312,0x1f1f4,0x1f1f4,0x20cf4,37328,0x1f2b8,0x1f2b8,0x20cf8,37344,0x1f37c,0x1f37c,0x20cfc,37360,0x1e810,0x1e800,0x20cc0,37632,0x1e8d4,0x1e8c4,0x20cc4,37648,0x1e998,0x1e988,0x20cc8,37664,0x1ea5c,0x1ea4c,0x20ccc,37680,0x1eb20,0x1eb10,0x20cd0,37696,0x1ebe4,0x1ebd4,0x20cd4,37712,0x1eca8,0x1ec98,0x20cd8,37728,0x1ed6c,0x1ed5c,0x20cdc,37744,0x1ee30,0x1ee20,0x20ce0,38016,0x1eef4,0x1eee4,0x20ce4,38032,0x1efb8,0x1efa8,0x20ce8,38048,0x1f07c,0x1f06c,0x20cec,38064,0x1f140,0x1f130,0x20cf0,38080,0x1f204,0x1f1f4,0x20cf4,38096,0x1f2c8,0x1f2b8,0x20cf8,38112,0x1f38c,0x1f37c,0x20cfc,38128,0x1e820,0x1e800,0x20cc0,38400,0x1e8e4,0x1e8c4,0x20cc4,38416,0x1e9a8,0x1e988,0x20cc8,38432,0x1ea6c,0x1ea4c,0x20ccc,38448,0x1eb30,0x1eb10,0x20cd0,38464,0x1ebf4,0x1ebd4,0x20cd4,38480,0x1ecb8,0x1ec98,0x20cd8,38496,0x1ed7c,0x1ed5c,0x20cdc,38512,0x1ee40,0x1ee20,0x20ce0,38784,0x1ef04,0x1eee4,0x20ce4,38800,0x1efc8,0x1efa8,0x20ce8,38816,0x1f08c,0x1f06c,0x20cec,38832,0x1f150,0x1f130,0x20cf0,38848,0x1f214,0x1f1f4,0x20cf4,38864,0x1f2d8,0x1f2b8,0x20cf8,38880,0x1f39c,0x1f37c,0x20cfc,38896,0x1e830,0x1e800,0x20cc0,39168,0x1e8f4,0x1e8c4,0x20cc4,39184,0x1e9b8,0x1e988,0x20cc8,39200,0x1ea7c,0x1ea4c,0x20ccc,39216,0x1eb40,0x1eb10,0x20cd0,39232,0x1ec04,0x1ebd4,0x20cd4,39248,0x1ecc8,0x1ec98,0x20cd8,39264,0x1ed8c,0x1ed5c,0x20cdc,39280,0x1ee50,0x1ee20,0x20ce0,39552,0x1ef14,0x1eee4,0x20ce4,39568,0x1efd8,0x1efa8,0x20ce8,39584,0x1f09c,0x1f06c,0x20cec,39600,0x1f160,0x1f130,0x20cf0,39616,0x1f224,0x1f1f4,0x20cf4,39632,0x1f2e8,0x1f2b8,0x20cf8,39648,0x1f3ac,0x1f37c,0x20cfc,39664,73728,0x26f70,0x1e980,0x1ebc0,73744,0x26f74,0x1ee10,0x1f050,73760,0x26f78,0x1f2a0,0x1f4e0,73776,0x26f7c,0x1f730,0x1f970,73792,0x26f80,0x1fbc0,0x1fe00,73808,0x26f84,0x20050,0x20290,73824,0x26f88,0x204e0,0x20720,73840,0x26f8c,0x20970,0x20bb0,74112,0x26f90,0x20e00,0x21040,74128,0x26f94,0x21290,0x214d0,74144,0x26f98,0x21720,0x21960,74160,0x26f9c,0x21bb0,0x21df0,74176,0x26fa0,0x22040,0x22280,74192,0x26fa4,0x224d0,0x22710,74208,0x26fa8,0x22960,0x22ba0,74224,0x26fac,0x22df0,0x23030,74528,0x26fb0,0x1e9b0,0x1ebc8,74544,0x26fb4,0x1ee40,0x1f058,74496,0x26fb8,0x1f2d0,0x1f4e8,74512,0x26fbc,0x1f760,0x1f978,74592,0x26fc0,0x1fbf0,0x1fe08,74608,0x26fc4,0x20080,0x20298,74560,0x26fc8,0x20510,0x20728,74576,0x26fcc,0x209a0,0x20bb8,74912,0x26fd0,0x20e30,0x21048,74928,0x26fd4,0x212c0,0x214d8,74880,0x26fd8,0x21750,0x21968,74896,0x26fdc,0x21be0,0x21df8,74976,0x26fe0,0x22070,0x22288,74992,0x26fe4,0x22500,0x22718,74944,0x26fe8,0x22990,0x22ba8,74960,0x26fec,0x22e20,0x23038,75328,0x26ff0,0x1e9e0,0x1ebd0,75344,0x26ff4,0x1ee70,0x1f060,75360,0x26ff8,0x1f300,0x1f4f0,75376,0x26ffc,0x1f790,0x1f980,75264,0x27000,0x1fc20,0x1fe10,75280,0x27004,0x200b0,0x202a0,75296,0x27008,0x20540,0x20730,75312,0x2700c,0x209d0,0x20bc0,75712,0x27010,0x20e60,0x21050,75728,0x27014,0x212f0,0x214e0,75744,0x27018,0x21780,0x21970,75760,0x2701c,0x21c10,0x21e00,75648,0x27020,0x220a0,0x22290,75664,0x27024,0x22530,0x22720,75680,0x27028,0x229c0,0x22bb0,75696,0x2702c,0x22e50,0x23040,76128,0x27030,0x1ea10,0x1ebd8,76144,0x27034,0x1eea0,0x1f068,76096,0x27038,0x1f330,0x1f4f8,76112,0x2703c,0x1f7c0,0x1f988,76064,0x27040,0x1fc50,0x1fe18,76080,0x27044,0x200e0,0x202a8,76032,0x27048,0x20570,0x20738,76048,0x2704c,0x20a00,0x20bc8,76512,0x27050,0x20e90,0x21058,76528,0x27054,0x21320,0x214e8,76480,0x27058,0x217b0,0x21978,76496,0x2705c,0x21c40,0x21e08,76448,0x27060,0x220d0,0x22298,76464,0x27064,0x22560,0x22728,76416,0x27068,0x229f0,0x22bb8,76432,0x2706c,0x22e80,0x23048,0x3f803f80,0x3f803f80,16256,0,0,0x3f803f80,0x3f800000,0,0,0x3f803f80,0,16256,0,0x3f803f80,0,0x3f800000,0x3f803f80,0,0,0,0,0,0,0,0,0,0,0,0,0,0,0,0x3f803f80,0,0,0,0,0,0,0,0,0,0,0,0,0,0,0,0x3f803f80,0,0,0,0,0,0,0,0,0,0,0,0,0,0,0,0x3f803f80,0,0,0,0,0,0,0,0,0,0,0,0,0,0,0,0x3f803f80,0x3f803f80,16256,0,0,0x3f803f80,0x3f800000,0,0,0x3f803f80,0,16256,0,0x3f803f80,0,0x3f800000,0x3f803f80,0,0,0,0,0,0,0,0,0,0,0,0,0,0,0,0x3f803f80,0,0,0,0,0,0,0,0,0,0,0,0,0,0,0,0x3f803f80,0,0,0,0,0,0,0,0,0,0,0,0,0,0,0,0x3f803f80,0,0,0,0,0,0,0,0,0,0,0,0,0,0,0,0x3f803f80,0x3f803f80,16256,0,0,0x3f803f80,0x3f800000,0,0,0x3f803f80,0,16256,0,0x3f803f80,0,0x3f800000,0x3f803f80,0,0,0,0,0,0,0,0,0,0,0,0,0,0,0,0x3f803f80,0,0,0,0,0,0,0,0,0,0,0,0,0,0,0,0x3f803f80,0,0,0,0,0,0,0,0,0,0,0,0,0,0,0,0x3f803f80,0,0,0,0,0,0,0,0,0,0,0,0,0,0,0,0x3f803f80,0x3f803f80,16256,0,0,0x3f803f80,0x3f800000,0,0,0x3f803f80,0,16256,0,0x3f803f80,0,0x3f800000,0,64,0x90000,0xf0080,144,208,0x90010,0xf0090,288,352,0x90020,0xf00a0,432,496,0x90030,0xf00b0,576,512,0x90040,0xf00c0,720,656,0x90050,0xf00d0,864,800,0x90060,0xf00e0,1008,944,0x90070,0xf00f0,1024,1088,0x90190,0xf0210,1168,1232,0x90180,0xf0200,1312,1376,0x901b0,0xf0230,1456,1520,0x901a0,0xf0220,1600,1536,0x901d0,0xf0250,1744,1680,0x901c0,0xf0240,1888,1824,0x901f0,0xf0270,2032,1968,0x901e0,0xf0260,16,80,0x90320,0x11f7a0,128,192,0x90330,0x11f7b0,304,368,0x90300,0x11f780,416,480,0x90310,0x11f790,592,528,0x90360,0x11f7e0,704,640,0x90370,0x11f7f0,880,816,0x90340,0x11f7c0,992,928,0x90350,0x11f7d0,1040,1104,0x904b0,0x11f930,1152,1216,0x904a0,0x11f920,1328,1392,0x90490,0x11f910,1440,1504,0x90480,0x11f900,1616,1552,0x904f0,0x11f970,1728,1664,0x904e0,0x11f960,1904,1840,0x904d0,0x11f950,2016,1952,0x904c0,0x11f940,32,96,0xbfa40,0x11fac0,176,240,0xbfa50,0x11fad0,256,320,0xbfa60,0x11fae0,400,464,0xbfa70,0x11faf0,608,544,0xbfa00,0x11fa80,752,688,0xbfa10,0x11fa90,832,768,0xbfa20,0x11faa0,976,912,0xbfa30,0x11fab0,1056,1120,0xbfbd0,0x11fc50,1200,1264,0xbfbc0,0x11fc40,1280,1344,0xbfbf0,0x11fc70,1424,1488,0xbfbe0,0x11fc60,1632,1568,0xbfb90,0x11fc10,1776,1712,0xbfb80,0x11fc00,1856,1792,0xbfbb0,0x11fc30,2000,1936,0xbfba0,0x11fc20,48,112,0xbfd60,0x11fde0,160,224,0xbfd70,0x11fdf0,272,336,0xbfd40,0x11fdc0,384,448,0xbfd50,0x11fdd0,624,560,0xbfd20,0x11fda0,736,672,0xbfd30,0x11fdb0,848,784,0xbfd00,0x11fd80,960,896,0xbfd10,0x11fd90,1072,1136,0xbfef0,0x11ff70,1184,1248,0xbfee0,0x11ff60,1296,1360,0xbfed0,0x11ff50,1408,1472,0xbfec0,0x11ff40,1648,1584,0xbfeb0,0x11ff30,1760,1696,0xbfea0,0x11ff20,1872,1808,0xbfe90,0x11ff10,1984,1920,0xbfe80,0x11ff00,0x11fb40,76800,84992,91648,0x11fb50,76816,85008,91664,0x11fb60,76832,85024,91680,0x11fb70,76848,85040,91696,0x11fb00,76864,85056,91712,0x11fb10,76880,85072,91728,0x11fb20,76896,85088,91744,0x11fb30,76912,85104,91760,0x11fcd0,76928,85120,91776,0x11fcc0,76944,85136,91792,0x11fcf0,76960,85152,91808,0x11fce0,76976,85168,91824,0x11fc90,76992,85184,91840,0x11fc80,77008,85200,91856,0x11fcb0,77024,85216,91872,0x11fca0,77040,85232,91888,0x11fe60,77056,85248,91904,0x11fe70,77072,85264,91920,0x11fe40,77088,85280,91936,0x11fe50,77104,85296,91952,0x11fe20,77120,85312,91968,0x11fe30,77136,85328,91984,0x11fe00,77152,85344,92000,0x11fe10,77168,85360,92016,0x11fff0,77184,85376,92032,0x11ffe0,77200,85392,92048,0x11ffd0,77216,85408,92064,0x11ffc0,77232,85424,92080,0x11ffb0,77248,85440,92096,0x11ffa0,77264,85456,92112,0x11ff90,77280,85472,92128,0x11ff80,77296,85488,92144,0x11fb40,77312,85504,92160,0x11fb50,77328,85520,92176,0x11fb60,77344,85536,92192,0x11fb70,77360,85552,92208,0x11fb00,77376,85568,92224,0x11fb10,77392,85584,92240,0x11fb20,77408,85600,92256,0x11fb30,77424,85616,92272,0x11fcd0,77440,85632,92288,0x11fcc0,77456,85648,92304,0x11fcf0,77472,85664,92320,0x11fce0,77488,85680,92336,0x11fc90,77504,85696,92352,0x11fc80,77520,85712,92368,0x11fcb0,77536,85728,92384,0x11fca0,77552,85744,92400,0x11fe60,77568,85760,92416,0x11fe70,77584,85776,92432,0x11fe40,77600,85792,92448,0x11fe50,77616,85808,92464,0x11fe20,77632,85824,92480,0x11fe30,77648,85840,92496,0x11fe00,77664,85856,92512,0x11fe10,77680,85872,92528,0x11fff0,77696,85888,92544,0x11ffe0,77712,85904,92560,0x11ffd0,77728,85920,92576,0x11ffc0,77744,85936,92592,0x11ffb0,77760,85952,92608,0x11ffa0,77776,85968,92624,0x11ff90,77792,85984,92640,0x11ff80,77808,86000,92656,0x1c1d0,36864,46080,52232,0x1c1d4,36880,46096,52248,0x1c1d8,36896,46112,52264,0x1c1dc,36912,46128,52280,0x1c1e0,36928,46144,52296,0x1c1e4,36944,46160,52312,0x1c1e8,36960,46176,52328,0x1c1ec,36976,46192,52344,0x1c1f0,36992,46208,52360,0x1c1f4,37008,46224,52376,0x1c1f8,37024,46240,52392,0x1c1fc,37040,46256,52408,0x1c200,37056,46272,52424,0x1c204,37072,46288,52440,0x1c208,37088,46304,52456,0x1c20c,37104,46320,52472,0x1c210,37120,46336,52488,0x1c214,37136,46352,52504,0x1c218,37152,46368,52520,0x1c21c,37168,46384,52536,0x1c220,37184,46400,52552,0x1c224,37200,46416,52568,0x1c228,37216,46432,52584,0x1c22c,37232,46448,52600,0x1c230,37248,46464,52616,0x1c234,37264,46480,52632,0x1c238,37280,46496,52648,0x1c23c,37296,46512,52664,0x1c240,37312,46528,52680,0x1c244,37328,46544,52696,0x1c248,37344,46560,52712,0x1c24c,37360,46576,52728,0x1c250,37376,46592,52744,0x1c254,37392,46608,52760,0x1c258,37408,46624,52776,0x1c25c,37424,46640,52792,0x1c260,37440,46656,52808,0x1c264,37456,46672,52824,0x1c268,37472,46688,52840,0x1c26c,37488,46704,52856,0x1c270,37504,46720,52872,0x1c274,37520,46736,52888,0x1c278,37536,46752,52904,0x1c27c,37552,46768,52920,0x1c280,37568,46784,52936,0x1c284,37584,46800,52952,0x1c288,37600,46816,52968,0x1c28c,37616,46832,52984,0x1c290,37632,46848,53000,0x1c294,37648,46864,53016,0x1c298,37664,46880,53032,0x1c29c,37680,46896,53048,0x1c2a0,37696,46912,53064,0x1c2a4,37712,46928,53080,0x1c2a8,37728,46944,53096,0x1c2ac,37744,46960,53112,0x1c2b0,37760,46976,53128,0x1c2b4,37776,46992,53144,0x1c2b8,37792,47008,53160,0x1c2bc,37808,47024,53176,0x1c2c0,37824,47040,53192,0x1c2c4,37840,47056,53208,0x1c2c8,37856,47072,53224,0x1c2cc,37872,47088,53240,86048,86112,0x1d600,0x1d60c,86192,86256,0x1d610,0x1d61c,86272,86336,0x1d620,0x1d62c,86416,86480,0x1d630,0x1d63c,86624,86560,0x1d640,0x1d64c,86768,86704,0x1d650,0x1d65c,86848,86784,0x1d660,0x1d66c,86992,86928,0x1d670,0x1d67c,87072,87136,0x1d680,0x1d68c,87216,87280,0x1d690,0x1d69c,87296,87360,0x1d6a0,0x1d6ac,87440,87504,0x1d6b0,0x1d6bc,87648,87584,0x1d6c0,0x1d6cc,87792,87728,0x1d6d0,0x1d6dc,87872,87808,0x1d6e0,0x1d6ec,88016,87952,0x1d6f0,0x1d6fc,86064,86120,0x1d700,0x1d70c,86176,86264,0x1d710,0x1d71c,86288,86344,0x1d720,0x1d72c,86400,86488,0x1d730,0x1d73c,86640,86568,0x1d740,0x1d74c,86752,86712,0x1d750,0x1d75c,86864,86792,0x1d760,0x1d76c,86976,86936,0x1d770,0x1d77c,87088,87144,0x1d780,0x1d78c,87200,87288,0x1d790,0x1d79c,87312,87368,0x1d7a0,0x1d7ac,87424,87512,0x1d7b0,0x1d7bc,87664,87592,0x1d7c0,0x1d7cc,87776,87736,0x1d7d0,0x1d7dc,87888,87816,0x1d7e0,0x1d7ec,88000,87960,0x1d7f0,0x1d7fc,86080,86128,0x1d800,0x1d80c,86224,86240,0x1d810,0x1d81c,86368,86352,0x1d820,0x1d82c,86512,86464,0x1d830,0x1d83c,86528,86576,0x1d840,0x1d84c,86672,86688,0x1d850,0x1d85c,86816,86800,0x1d860,0x1d86c,86960,86912,0x1d870,0x1d87c,87104,87152,0x1d880,0x1d88c,87248,87264,0x1d890,0x1d89c,87392,87376,0x1d8a0,0x1d8ac,87536,87488,0x1d8b0,0x1d8bc,87552,87600,0x1d8c0,0x1d8cc,87696,87712,0x1d8d0,0x1d8dc,87840,87824,0x1d8e0,0x1d8ec,87984,87936,0x1d8f0,0x1d8fc,86096,86136,0x1d900,0x1d90c,86208,86248,0x1d910,0x1d91c,86384,86360,0x1d920,0x1d92c,86496,86472,0x1d930,0x1d93c,86544,86584,0x1d940,0x1d94c,86656,86696,0x1d950,0x1d95c,86832,86808,0x1d960,0x1d96c,86944,86920,0x1d970,0x1d97c,87120,87160,0x1d980,0x1d98c,87232,87272,0x1d990,0x1d99c,87408,87384,0x1d9a0,0x1d9ac,87520,87496,0x1d9b0,0x1d9bc,87568,87608,0x1d9c0,0x1d9cc,87680,87720,0x1d9d0,0x1d9dc,87856,87832,0x1d9e0,0x1d9ec,87968,87944,0x1d9f0,0x1d9fc,0x1e780,0x1e780,0x1ea40,0x100000,0x1e782,0x100000,0x100000,0x1ea40,0x1e784,0x100000,0x100000,0x100000,0x1e786,0x100000,0x100000,0x100000,0x1e790,0x1e780,0x100000,0x100000,0x1e792,0x100000,0x100000,0x1ea40,0x1e794,0x100000,0x100000,0x100000,0x1e796,0x100000,0x100000,0x100000,0x1e7a0,0x1e780,0x100000,0x100000,0x1e7a2,0x100000,0x100000,0x1ea40,0x1e7a4,0x100000,0x100000,0x100000,0x1e7a6,0x100000,0x100000,0x100000,0x1e7b0,0x1e780,0x100000,0x100000,0x1e7b2,0x100000,0x100000,0x1ea40,0x1e7b4,0x100000,0x100000,0x100000,0x1e7b6,0x100000,0x100000,0x100000,0x1e788,0x1e790,0x1ea70,0x100000,0x1e78a,0x100000,0x100000,0x1ea70,0x1e78c,0x100000,0x100000,0x100000,0x1e78e,0x100000,0x100000,0x100000,0x1e798,0x1e790,0x100000,0x100000,0x1e79a,0x100000,0x100000,0x1ea70,0x1e79c,0x100000,0x100000,0x100000,0x1e79e,0x100000,0x100000,0x100000,0x1e7a8,0x1e790,0x100000,0x100000,0x1e7aa,0x100000,0x100000,0x1ea70,0x1e7ac,0x100000,0x100000,0x100000,0x1e7ae,0x100000,0x100000,0x100000,0x1e7b8,0x1e790,0x100000,0x100000,0x1e7ba,0x100000,0x100000,0x1ea70,0x1e7bc,0x100000,0x100000,0x100000,0x1e7be,0x100000,0x100000,0x100000,0x1e7c0,0x1e7a0,0x1eaa0,0x100000,0x1e7c2,0x100000,0x100000,0x1eaa0,0x1e7c4,0x100000,0x100000,0x100000,0x1e7c6,0x100000,0x100000,0x100000,0x1e7d0,0x1e7a0,0x100000,0x100000,0x1e7d2,0x100000,0x100000,0x1eaa0,0x1e7d4,0x100000,0x100000,0x100000,0x1e7d6,0x100000,0x100000,0x100000,0x1e7e0,0x1e7a0,0x100000,0x100000,0x1e7e2,0x100000,0x100000,0x1eaa0,0x1e7e4,0x100000,0x100000,0x100000,0x1e7e6,0x100000,0x100000,0x100000,0x1e7f0,0x1e7a0,0x100000,0x100000,0x1e7f2,0x100000,0x100000,0x1eaa0,0x1e7f4,0x100000,0x100000,0x100000,0x1e7f6,0x100000,0x100000,0x100000,0x1e7c8,0x1e7b0,0x1ead0,0x100000,0x1e7ca,0x100000,0x100000,0x1ead0,0x1e7cc,0x100000,0x100000,0x100000,0x1e7ce,0x100000,0x100000,0x100000,0x1e7d8,0x1e7b0,0x100000,0x100000,0x1e7da,0x100000,0x100000,0x1ead0,0x1e7dc,0x100000,0x100000,0x100000,0x1e7de,0x100000,0x100000,0x100000,0x1e7e8,0x1e7b0,0x100000,0x100000,0x1e7ea,0x100000,0x100000,0x1ead0,0x1e7ec,0x100000,0x100000,0x100000,0x1e7ee,0x100000,0x100000,0x100000,0x1e7f8,0x1e7b0,0x100000,0x100000,0x1e7fa,0x100000,0x100000,0x1ead0,0x1e7fc,0x100000,0x100000,0x100000,0x1e7fe,0x100000,0x100000,0x100000,0x1ec20,0x25cf0,37152,37216,0x1ec22,0x25cf2,37168,37224,0x1ec24,0x25cf4,37184,37232,0x1ec26,0x25cf6,37200,37240,0x1ec28,0x25e50,37536,37600,0x1ec2a,0x25e52,37552,37608,0x1ec2c,0x25e54,37568,37616,0x1ec2e,0x25e56,37584,37624,0x1ec30,0x25fb0,37888,37952,0x1ec32,0x25fb2,37904,37960,0x1ec34,0x25fb4,37984,37968,0x1ec36,0x25fb6,38000,37976,0x1ec38,0x26110,38272,38336,0x1ec3a,0x26112,38288,38344,0x1ec3c,0x26114,38368,38352,0x1ec3e,0x26116,38384,38360,0x1ec20,0x25cf8,38752,38688,0x1ec22,0x25cfa,38768,38696,0x1ec24,0x25cfc,38656,38704,0x1ec26,0x25cfe,38672,38712,0x1ec28,0x25e58,39136,39072,0x1ec2a,0x25e5a,39152,39080,0x1ec2c,0x25e5c,39040,39088,0x1ec2e,0x25e5e,39056,39096,0x1ec30,0x25fb8,39488,39424,0x1ec32,0x25fba,39504,39432,0x1ec34,0x25fbc,39456,39440,0x1ec36,0x25fbe,39472,39448,0x1ec38,0x26118,39872,39808,0x1ec3a,0x2611a,39888,39816,0x1ec3c,0x2611c,39840,39824,0x1ec3e,0x2611e,39856,39832,0x1ec20,0x267f8,40224,40288,0x1ec22,0x267fa,40240,40296,0x1ec24,0x267fc,40256,40304,0x1ec26,0x267fe,40272,40312,0x1ec28,0x26958,40608,40672,0x1ec2a,0x2695a,40624,40680,0x1ec2c,0x2695c,40640,40688,0x1ec2e,0x2695e,40656,40696,0x1ec30,0x26ab8,40960,41024,0x1ec32,0x26aba,40976,41032,0x1ec34,0x26abc,41056,41040,0x1ec36,0x26abe,41072,41048,0x1ec38,0x26c18,41344,41408,0x1ec3a,0x26c1a,41360,41416,0x1ec3c,0x26c1c,41440,41424,0x1ec3e,0x26c1e,41456,41432,0x1ec20,0x100000,41824,41760,0x1ec22,0x100000,41840,41768,0x1ec24,0x100000,41728,41776,0x1ec26,0x100000,41744,41784,0x1ec28,0x100000,42208,42144,0x1ec2a,0x100000,42224,42152,0x1ec2c,0x100000,42112,42160,0x1ec2e,0x100000,42128,42168,0x1ec30,0x100000,42560,42496,0x1ec32,0x100000,42576,42504,0x1ec34,0x100000,42528,42512,0x1ec36,0x100000,42544,42520,0x1ec38,0x100000,42944,42880,0x1ec3a,0x100000,42960,42888,0x1ec3c,0x100000,42912,42896,0x1ec3e,0x100000,42928,42904,37152,37184,37216,64512,37160,37192,37224,64528,37168,37200,37232,64544,37176,37208,37240,64560,38304,38336,38368,64576,38312,38344,38376,64592,38320,38352,38384,64608,38328,38360,38392,64624,39424,39520,39488,64640,39432,39528,39496,64656,39440,39536,39504,64672,39448,39544,39512,64688,40576,40672,40640,64704,40584,40680,40648,64720,40592,40688,40656,64736,40600,40696,40664,64752,46432,46336,46368,64768,46440,46344,46376,64784,46448,46352,46384,64800,46456,46360,46392,64816,47584,47488,47520,64832,47592,47496,47528,64848,47600,47504,47536,64864,47608,47512,47544,64880,48704,48672,48640,64896,48712,48680,48648,64912,48720,48688,48656,64928,48728,48696,48664,64944,49856,49824,49792,64960,49864,49832,49800,64976,49872,49840,49808,64992,49880,49848,49816,65008,55584,55616,55648,65024,55592,55624,55656,65040,55600,55632,55664,65056,55608,55640,55672,65072,56736,56768,56800,65088,56744,56776,56808,65104,56752,56784,56816,65120,56760,56792,56824,65136,57856,57952,57920,65152,57864,57960,57928,65168,57872,57968,57936,65184,57880,57976,57944,65200,59008,59104,59072,65216,59016,59112,59080,65232,59024,59120,59088,65248,59032,59128,59096,65264,64864,64768,64800,65280,64872,64776,64808,65296,64880,64784,64816,65312,64888,64792,64824,65328,66016,65920,65952,65344,66024,65928,65960,65360,66032,65936,65968,65376,66040,65944,65976,65392,67136,67104,67072,65408,67144,67112,67080,65424,67152,67120,67088,65440,67160,67128,67096,65456,68288,68256,68224,65472,68296,68264,68232,65488,68304,68272,68240,65504,68312,68280,68248,65520,0x1e800,0x1e800,0x20cc0,55296,0x1e8c4,0x1e8c4,0x20cc4,55312,0x1e988,0x1e988,0x20cc8,55328,0x1ea4c,0x1ea4c,0x20ccc,55344,0x1eb10,0x1eb10,0x20cd0,55360,0x1ebd4,0x1ebd4,0x20cd4,55376,0x1ec98,0x1ec98,0x20cd8,55392,0x1ed5c,0x1ed5c,0x20cdc,55408,0x1ee20,0x1ee20,0x20ce0,55680,0x1eee4,0x1eee4,0x20ce4,55696,0x1efa8,0x1efa8,0x20ce8,55712,0x1f06c,0x1f06c,0x20cec,55728,0x1f130,0x1f130,0x20cf0,55744,0x1f1f4,0x1f1f4,0x20cf4,55760,0x1f2b8,0x1f2b8,0x20cf8,55776,0x1f37c,0x1f37c,0x20cfc,55792,0x1e810,0x1e800,0x20cc0,56064,0x1e8d4,0x1e8c4,0x20cc4,56080,0x1e998,0x1e988,0x20cc8,56096,0x1ea5c,0x1ea4c,0x20ccc,56112,0x1eb20,0x1eb10,0x20cd0,56128,0x1ebe4,0x1ebd4,0x20cd4,56144,0x1eca8,0x1ec98,0x20cd8,56160,0x1ed6c,0x1ed5c,0x20cdc,56176,0x1ee30,0x1ee20,0x20ce0,56448,0x1eef4,0x1eee4,0x20ce4,56464,0x1efb8,0x1efa8,0x20ce8,56480,0x1f07c,0x1f06c,0x20cec,56496,0x1f140,0x1f130,0x20cf0,56512,0x1f204,0x1f1f4,0x20cf4,56528,0x1f2c8,0x1f2b8,0x20cf8,56544,0x1f38c,0x1f37c,0x20cfc,56560,0x1e820,0x1e800,0x20cc0,56832,0x1e8e4,0x1e8c4,0x20cc4,56848,0x1e9a8,0x1e988,0x20cc8,56864,0x1ea6c,0x1ea4c,0x20ccc,56880,0x1eb30,0x1eb10,0x20cd0,56896,0x1ebf4,0x1ebd4,0x20cd4,56912,0x1ecb8,0x1ec98,0x20cd8,56928,0x1ed7c,0x1ed5c,0x20cdc,56944,0x1ee40,0x1ee20,0x20ce0,57216,0x1ef04,0x1eee4,0x20ce4,57232,0x1efc8,0x1efa8,0x20ce8,57248,0x1f08c,0x1f06c,0x20cec,57264,0x1f150,0x1f130,0x20cf0,57280,0x1f214,0x1f1f4,0x20cf4,57296,0x1f2d8,0x1f2b8,0x20cf8,57312,0x1f39c,0x1f37c,0x20cfc,57328,0x1e830,0x1e800,0x20cc0,57600,0x1e8f4,0x1e8c4,0x20cc4,57616,0x1e9b8,0x1e988,0x20cc8,57632,0x1ea7c,0x1ea4c,0x20ccc,57648,0x1eb40,0x1eb10,0x20cd0,57664,0x1ec04,0x1ebd4,0x20cd4,57680,0x1ecc8,0x1ec98,0x20cd8,57696,0x1ed8c,0x1ed5c,0x20cdc,57712,0x1ee50,0x1ee20,0x20ce0,57984,0x1ef14,0x1eee4,0x20ce4,58000,0x1efd8,0x1efa8,0x20ce8,58016,0x1f09c,0x1f06c,0x20cec,58032,0x1f160,0x1f130,0x20cf0,58048,0x1f224,0x1f1f4,0x20cf4,58064,0x1f2e8,0x1f2b8,0x20cf8,58080,0x1f3ac,0x1f37c,0x20cfc,58096,92160,0x27070,0x1ea40,0x1ec20,92176,0x27074,0x1eed0,0x1f0b0,92192,0x27078,0x1f360,0x1f540,92208,0x2707c,0x1f7f0,0x1f9d0,92224,0x27080,0x1fc80,0x1fe60,92240,0x27084,0x20110,0x202f0,92256,0x27088,0x205a0,0x20780,92272,0x2708c,0x20a30,0x20c10,92544,0x27090,0x20ec0,0x210a0,92560,0x27094,0x21350,0x21530,92576,0x27098,0x217e0,0x219c0,92592,0x2709c,0x21c70,0x21e50,92608,0x270a0,0x22100,0x222e0,92624,0x270a4,0x22590,0x22770,92640,0x270a8,0x22a20,0x22c00,92656,0x270ac,0x22eb0,0x23090,92960,0x270b0,0x1ea70,0x1ec28,92976,0x270b4,0x1ef00,0x1f0b8,92928,0x270b8,0x1f390,0x1f548,92944,0x270bc,0x1f820,0x1f9d8,93024,0x270c0,0x1fcb0,0x1fe68,93040,0x270c4,0x20140,0x202f8,92992,0x270c8,0x205d0,0x20788,93008,0x270cc,0x20a60,0x20c18,93344,0x270d0,0x20ef0,0x210a8,93360,0x270d4,0x21380,0x21538,93312,0x270d8,0x21810,0x219c8,93328,0x270dc,0x21ca0,0x21e58,93408,0x270e0,0x22130,0x222e8,93424,0x270e4,0x225c0,0x22778,93376,0x270e8,0x22a50,0x22c08,93392,0x270ec,0x22ee0,0x23098,93760,0x270f0,0x1eaa0,0x1ec30,93776,0x270f4,0x1ef30,0x1f0c0,93792,0x270f8,0x1f3c0,0x1f550,93808,0x270fc,0x1f850,0x1f9e0,93696,0x27100,0x1fce0,0x1fe70,93712,0x27104,0x20170,0x20300,93728,0x27108,0x20600,0x20790,93744,0x2710c,0x20a90,0x20c20,94144,0x27110,0x20f20,0x210b0,94160,0x27114,0x213b0,0x21540,94176,0x27118,0x21840,0x219d0,94192,0x2711c,0x21cd0,0x21e60,94080,0x27120,0x22160,0x222f0,94096,0x27124,0x225f0,0x22780,94112,0x27128,0x22a80,0x22c10,94128,0x2712c,0x22f10,0x230a0,94560,0x27130,0x1ead0,0x1ec38,94576,0x27134,0x1ef60,0x1f0c8,94528,0x27138,0x1f3f0,0x1f558,94544,0x2713c,0x1f880,0x1f9e8,94496,0x27140,0x1fd10,0x1fe78,94512,0x27144,0x201a0,0x20308,94464,0x27148,0x20630,0x20798,94480,0x2714c,0x20ac0,0x20c28,94944,0x27150,0x20f50,0x210b8,94960,0x27154,0x213e0,0x21548,94912,0x27158,0x21870,0x219d8,94928,0x2715c,0x21d00,0x21e68,94880,0x27160,0x22190,0x222f8,94896,0x27164,0x22620,0x22788,94848,0x27168,0x22ab0,0x22c18,94864,0x2716c,0x22f40,0x230a8,0x3f803f80,0x3f803f80,16256,0,0,0x3f803f80,0x3f800000,0,0,0x3f803f80,0,16256,0,0x3f803f80,0,0x3f800000,0x3f803f80,0,0,0,0,0,0,0,0,0,0,0,0,0,0,0,0x3f803f80,0,0,0,0,0,0,0,0,0,0,0,0,0,0,0,0x3f803f80,0,0,0,0,0,0,0,0,0,0,0,0,0,0,0,0x3f803f80,0,0,0,0,0,0,0,0,0,0,0,0,0,0,0,0x3f803f80,0x3f803f80,16256,0,0,0x3f803f80,0x3f800000,0,0,0x3f803f80,0,16256,0,0x3f803f80,0,0x3f800000,0x3f803f80,0,0,0,0,0,0,0,0,0,0,0,0,0,0,0,0x3f803f80,0,0,0,0,0,0,0,0,0,0,0,0,0,0,0,0x3f803f80,0,0,0,0,0,0,0,0,0,0,0,0,0,0,0,0x3f803f80,0,0,0,0,0,0,0,0,0,0,0,0,0,0,0,0x3f803f80,0x3f803f80,16256,0,0,0x3f803f80,0x3f800000,0,0,0x3f803f80,0,16256,0,0x3f803f80,0,0x3f800000,0x3f803f80,0,0,0,0,0,0,0,0,0,0,0,0,0,0,0,0x3f803f80,0,0,0,0,0,0,0,0,0,0,0,0,0,0,0,0x3f803f80,0,0,0,0,0,0,0,0,0,0,0,0,0,0,0,0x3f803f80,0,0,0,0,0,0,0,0,0,0,0,0,0,0,0,0x3f803f80,0x3f803f80,16256,0,0,0x3f803f80,0x3f800000,0,0,0x3f803f80,0,16256,0,0x3f803f80,0,0x3f800000,0xffffffff,0xffffffff,0,0,0,0,0xffffffff,0xff000000,0xff000000,0xffffff,0xff000000,0xff000000,0,4096,16384,0x1c000,0xffffffff,0xffffffff,0,0,0,0,0xffffff,0xffff0000,0xffff0000,65535,0xffffff,0xffffff,1024,5120,16896,0x1c0a0,0xffffffff,0xffffffff,0,0,0,0,65535,0xffffff00,0xff000000,0xffffff,0xffff0000,0xffff0000,2048,10240,17408,0x1c140,0xffffffff,0,0,0,0,0,255,0xffffffff,0xffff0000,65535,65535,65535,3072,11264,17920,0x1c1d0,0,0x3fb8aa3b,0x2a8cbccc,0x38fba882,0x7fff7fff,0x415db3d7,0xffff0000,0,0,65535,0,0xffff0000,0,0,0,0,0,808,1800,2608,3600,4408,5400,6208,7200,8192,9000,9992,10800,11792,12600,18432,14385,18447,16170,18462,18462,18462,18462,18462,20,19,0,0,0,0,0,0,0x1b0000,0x5b8480,0xae0a80,0xee8f00,0x1411500,0x1819980,0x1d41f80,0x214a400,0x2672a00,0x2b9b000,0x2fa3480,0x34cba80,0x38d3f00,0x3dfc500,0x4204980,0x60a80,0x4b2b680,0x6a800,0x5452380,0x74580,0x74580,0x74580,0x74580,0x74580,0x74580,0x74580,0x74580,0x74580,0x74580,0x74580,0x74580,0x74580,1,809,1801,2609,3601,4409,5401,6209,7201,8193,9001,9993,10801,11793,12601,13592,14386,15377,16171,18463,18463,18463,18463,18463,20,19,0,0,0,0,0,0,0x1b0a80,0x5b8f00,0xae1500,0xee9980,0x1411f80,0x181a400,0x1d42a00,0x214ae80,0x2673480,0x2b9ba80,0x2fa3f00,0x34cc500,0x38d4980,0x3dfcf80,0x4205400,0x472cf80,0x4b2c100,0x5053c80,0x5452e00,0x75000,0x75000,0x75000,0x75000,0x75000,0x75000,0x75000,0x75000,0x75000,0x75000,0x75000,0x75000,0x75000,2,810,1802,2610,3602,4410,5402,6210,7202,8194,9002,9994,10802,11794,12602,13593,14387,15378,16172,17162,17671,18216,18216,18216,22,21,0,0,0,0,0,0,0x1b1500,0x5b9980,0xae1f80,0xeea400,0x1412a00,0x181ae80,0x1d43480,0x214b900,0x2673f00,0x2b9c500,0x2fa4980,0x34ccf80,0x38d5400,0x3dfda00,0x4205e80,0x472da00,0x4b2cb80,0x5054700,0x5453880,0x597a980,0x5c17500,0x5ecba80,0x5ecba80,0x5ecba80,0x5ecba80,0x5ecba80,0x5ecba80,0x5ecba80,0x5ecba80,0x5ecba80,0x5ecba80,0x5ecba80,3,811,1803,2611,3603,4411,5403,6211,7203,8195,9003,9995,10803,11795,12603,13594,14388,15379,16173,17163,17672,17672,17672,17672,21,20,0,0,0,0,0,0,0x1b1f80,0x5ba400,0xae2a00,0xeeae80,0x1413480,0x181b900,0x1d43f00,0x214c380,0x2674980,0x2b9cf80,0x2fa5400,0x34cda00,0x38d5e80,0x3dfe480,0x4206900,0x472e480,0x4b2d600,0x5055180,0x5454300,0x597b400,0x5c17f80,0x5c17f80,0x5c17f80,0x5c17f80,0x5c17f80,0x5c17f80,0x5c17f80,0x5c17f80,0x5c17f80,0x5c17f80,0x5c17f80,0x5c17f80,4,812,1804,2612,3604,4412,5404,6212,7204,8196,9004,9996,10804,11796,12604,13595,14389,15380,16174,18464,17673,17673,17673,17673,21,20,0,0,0,0,0,0,0x1b2a00,0x5bae80,0xae3480,0xeeb900,0x1413f00,0x181c380,0x1d44980,0x214ce00,0x2675400,0x2b9da00,0x2fa5e80,0x34ce480,0x38d6900,0x3dfef00,0x4207380,0x472ef00,0x4b2e080,0x5055c00,0x5454d80,0x75a80,0x5c18a00,0x5c18a00,0x5c18a00,0x5c18a00,0x5c18a00,0x5c18a00,0x5c18a00,0x5c18a00,0x5c18a00,0x5c18a00,0x5c18a00,0x5c18a00,5,813,1805,2613,3605,4413,5405,6213,7205,8197,9005,9997,10805,11797,12605,13596,14390,15381,16175,18465,18465,18465,18465,18465,20,19,0,0,0,0,0,0,0x1b3480,0x5bb900,0xae3f00,0xeec380,0x1414980,0x181ce00,0x1d45400,0x214d880,0x2675e80,0x2b9e480,0x2fa6900,0x34cef00,0x38d7380,0x3dff980,0x4207e00,0x472f980,0x4b2eb00,0x5056680,0x5455800,0x76500,0x76500,0x76500,0x76500,0x76500,0x76500,0x76500,0x76500,0x76500,0x76500,0x76500,0x76500,0x76500,6,814,1806,2614,3606,4414,5406,6214,7206,8198,9006,9998,10806,11798,12606,13597,14391,15382,16176,18466,17674,17674,17674,17674,21,20,0,0,0,0,0,0,0x1b3f00,0x5bc380,0xae4980,0xeece00,0x1415400,0x181d880,0x1d45e80,0x214e300,0x2676900,0x2b9ef00,0x2fa7380,0x34cf980,0x38d7e00,0x3e00400,0x4208880,0x4730400,0x4b2f580,0x5057100,0x5456280,0x76f80,0x5c19480,0x5c19480,0x5c19480,0x5c19480,0x5c19480,0x5c19480,0x5c19480,0x5c19480,0x5c19480,0x5c19480,0x5c19480,0x5c19480,7,815,1807,2615,3607,4415,5407,6215,7207,8199,9007,9999,10807,11799,12607,13598,14392,15383,16177,17164,17675,17675,17675,17675,21,20,0,0,0,0,0,0,0x1b4980,0x5bce00,0xae5400,0xeed880,0x1415e80,0x181e300,0x1d46900,0x214ed80,0x2677380,0x2b9f980,0x2fa7e00,0x34d0400,0x38d8880,0x3e00e80,0x4209300,0x4730e80,0x4b30000,0x5057b80,0x5456d00,0x597be80,0x5c19f00,0x5c19f00,0x5c19f00,0x5c19f00,0x5c19f00,0x5c19f00,0x5c19f00,0x5c19f00,0x5c19f00,0x5c19f00,0x5c19f00,0x5c19f00,8,816,1808,2616,3608,4416,5408,6400,7208,8200,9008,10000,10808,11800,12608,18433,14393,18448,16178,18467,18467,18467,18467,18467,20,19,0,0,0,0,0,0,0x1b5400,0x5bd880,0xae5e80,0xeee300,0x1416900,0x181ed80,0x1d47380,0x226f980,0x2677e00,0x2ba0400,0x2fa8880,0x34d0e80,0x38d9300,0x3e01900,0x4209d80,0x61500,0x4b30a80,0x6b280,0x5457780,0x77a00,0x77a00,0x77a00,0x77a00,0x77a00,0x77a00,0x77a00,0x77a00,0x77a00,0x77a00,0x77a00,0x77a00,0x77a00,9,817,1809,2617,3609,4417,5409,6401,7209,8201,9009,10001,10809,11801,12609,13599,14394,15384,16179,18468,18468,18468,18468,18468,20,19,0,0,0,0,0,0,0x1b5e80,0x5be300,0xae6900,0xeeed80,0x1417380,0x181f800,0x1d47e00,0x2270400,0x2678880,0x2ba0e80,0x2fa9300,0x34d1900,0x38d9d80,0x3e02380,0x420a800,0x4731900,0x4b31500,0x5058600,0x5458200,0x78480,0x78480,0x78480,0x78480,0x78480,0x78480,0x78480,0x78480,0x78480,0x78480,0x78480,0x78480,0x78480,10,818,1810,2618,3610,4418,5410,6402,7210,8202,9010,10002,10810,11802,12610,13600,14395,15385,16180,17165,17676,18217,18217,18217,22,21,0,0,0,0,0,0,0x1b6900,0x5bed80,0xae7380,0xeef800,0x1417e00,0x1820280,0x1d48880,0x2270e80,0x2679300,0x2ba1900,0x2fa9d80,0x34d2380,0x38da800,0x3e02e00,0x420b280,0x4732380,0x4b31f80,0x5059080,0x5458c80,0x597c900,0x5c1a980,0x5ecc500,0x5ecc500,0x5ecc500,0x5ecc500,0x5ecc500,0x5ecc500,0x5ecc500,0x5ecc500,0x5ecc500,0x5ecc500,0x5ecc500,11,819,1811,2619,3611,4419,5411,6403,7211,8203,9011,10003,10811,11803,12611,13601,14396,15386,16181,17166,17677,17677,17677,17677,21,20,0,0,0,0,0,0,0x1b7380,0x5bf800,0xae7e00,0xef0280,0x1418880,0x1820d00,0x1d49300,0x2271900,0x2679d80,0x2ba2380,0x2faa800,0x34d2e00,0x38db280,0x3e03880,0x420bd00,0x4732e00,0x4b32a00,0x5059b00,0x5459700,0x597d380,0x5c1b400,0x5c1b400,0x5c1b400,0x5c1b400,0x5c1b400,0x5c1b400,0x5c1b400,0x5c1b400,0x5c1b400,0x5c1b400,0x5c1b400,0x5c1b400,12,820,1812,2620,3612,4420,5412,6404,7212,8204,9012,10004,10812,11804,12612,13602,14397,15387,16182,18469,17678,17678,17678,17678,21,20,0,0,0,0,0,0,0x1b7e00,0x5c0280,0xae8880,0xef0d00,0x1419300,0x1821780,0x1d49d80,0x2272380,0x267a800,0x2ba2e00,0x2fab280,0x34d3880,0x38dbd00,0x3e04300,0x420c780,0x4733880,0x4b33480,0x505a580,0x545a180,0x78f00,0x5c1be80,0x5c1be80,0x5c1be80,0x5c1be80,0x5c1be80,0x5c1be80,0x5c1be80,0x5c1be80,0x5c1be80,0x5c1be80,0x5c1be80,0x5c1be80,13,821,1813,2621,3613,4421,5413,6405,7213,8205,9013,10005,10813,11805,12613,13603,14398,15388,16183,18470,18470,18470,18470,18470,20,19,0,0,0,0,0,0,0x1b8880,0x5c0d00,0xae9300,0xef1780,0x1419d80,0x1822200,0x1d4a800,0x2272e00,0x267b280,0x2ba3880,0x2fabd00,0x34d4300,0x38dc780,0x3e04d80,0x420d200,0x4734300,0x4b33f00,0x505b000,0x545ac00,0x79980,0x79980,0x79980,0x79980,0x79980,0x79980,0x79980,0x79980,0x79980,0x79980,0x79980,0x79980,0x79980,14,822,1814,2622,3614,4422,5414,6406,7214,8206,9014,10006,10814,11806,12614,13604,14399,15389,16184,18471,17679,17679,17679,17679,21,20,0,0,0,0,0,0,0x1b9300,0x5c1780,0xae9d80,0xef2200,0x141a800,0x1822c80,0x1d4b280,0x2273880,0x267bd00,0x2ba4300,0x2fac780,0x34d4d80,0x38dd200,0x3e05800,0x420dc80,0x4734d80,0x4b34980,0x505ba80,0x545b680,0x7a400,0x5c1c900,0x5c1c900,0x5c1c900,0x5c1c900,0x5c1c900,0x5c1c900,0x5c1c900,0x5c1c900,0x5c1c900,0x5c1c900,0x5c1c900,0x5c1c900,15,823,1815,2623,3615,4423,5415,6407,7215,8207,9015,10007,10815,11807,12615,13605,14400,15390,16185,17167,17680,17680,17680,17680,21,20,0,0,0,0,0,0,0x1b9d80,0x5c2200,0xaea800,0xef2c80,0x141b280,0x1823700,0x1d4bd00,0x2274300,0x267c780,0x2ba4d80,0x2fad200,0x34d5800,0x38ddc80,0x3e06280,0x420e700,0x4735800,0x4b35400,0x505c500,0x545c100,0x597de00,0x5c1d380,0x5c1d380,0x5c1d380,0x5c1d380,0x5c1d380,0x5c1d380,0x5c1d380,0x5c1d380,0x5c1d380,0x5c1d380,0x5c1d380,0x5c1d380,16,824,1816,2624,3616,4608,5416,6408,7216,8208,9016,10008,10816,11808,12800,18434,14401,18449,16186,18472,18472,18472,18472,18472,20,19,0,0,0,0,0,0,0x1ba800,0x5c2c80,0xaeb280,0xef3700,0x141bd00,0x1944300,0x1d4c780,0x2274d80,0x267d200,0x2ba5800,0x2fadc80,0x34d6280,0x38de700,0x3e06d00,0x432f300,0x61f80,0x4b35e80,0x6bd00,0x545cb80,0x7ae80,0x7ae80,0x7ae80,0x7ae80,0x7ae80,0x7ae80,0x7ae80,0x7ae80,0x7ae80,0x7ae80,0x7ae80,0x7ae80,0x7ae80,17,825,1817,2625,3617,4609,5417,6409,7217,8209,9017,10009,10817,11809,12801,13606,14402,15391,16187,18473,18473,18473,18473,18473,20,19,0,0,0,0,0,0,0x1bb280,0x5c3700,0xaebd00,0xef4180,0x141c780,0x1944d80,0x1d4d200,0x2275800,0x267dc80,0x2ba6280,0x2fae700,0x34d6d00,0x38df180,0x3e07780,0x432fd80,0x4736280,0x4b36900,0x505cf80,0x545d600,0x7b900,0x7b900,0x7b900,0x7b900,0x7b900,0x7b900,0x7b900,0x7b900,0x7b900,0x7b900,0x7b900,0x7b900,0x7b900,18,826,1818,2626,3618,4610,5418,6410,7218,8210,9018,10010,10818,11810,12802,13607,14403,15392,16188,17168,17681,18218,18218,18218,22,21,0,0,0,0,0,0,0x1bbd00,0x5c4180,0xaec780,0xef4c00,0x141d200,0x1945800,0x1d4dc80,0x2276280,0x267e700,0x2ba6d00,0x2faf180,0x34d7780,0x38dfc00,0x3e08200,0x4330800,0x4736d00,0x4b37380,0x505da00,0x545e080,0x597e880,0x5c1de00,0x5eccf80,0x5eccf80,0x5eccf80,0x5eccf80,0x5eccf80,0x5eccf80,0x5eccf80,0x5eccf80,0x5eccf80,0x5eccf80,0x5eccf80,19,827,1819,2627,3619,4611,5419,6411,7219,8211,9019,10011,10819,11811,12803,13608,14404,15393,16189,17169,17682,17682,17682,17682,21,20,0,0,0,0,0,0,0x1bc780,0x5c4c00,0xaed200,0xef5680,0x141dc80,0x1946280,0x1d4e700,0x2276d00,0x267f180,0x2ba7780,0x2fafc00,0x34d8200,0x38e0680,0x3e08c80,0x4331280,0x4737780,0x4b37e00,0x505e480,0x545eb00,0x597f300,0x5c1e880,0x5c1e880,0x5c1e880,0x5c1e880,0x5c1e880,0x5c1e880,0x5c1e880,0x5c1e880,0x5c1e880,0x5c1e880,0x5c1e880,0x5c1e880,20,828,1820,2628,3620,4612,5420,6412,7220,8212,9020,10012,10820,11812,12804,13609,14405,15394,16190,18474,17683,17683,17683,17683,21,20,0,0,0,0,0,0,0x1bd200,0x5c5680,0xaedc80,0xef6100,0x141e700,0x1946d00,0x1d4f180,0x2277780,0x267fc00,0x2ba8200,0x2fb0680,0x34d8c80,0x38e1100,0x3e09700,0x4331d00,0x4738200,0x4b38880,0x505ef00,0x545f580,0x7c380,0x5c1f300,0x5c1f300,0x5c1f300,0x5c1f300,0x5c1f300,0x5c1f300,0x5c1f300,0x5c1f300,0x5c1f300,0x5c1f300,0x5c1f300,0x5c1f300,21,829,1821,2629,3621,4613,5421,6413,7221,8213,9021,10013,10821,11813,12805,13610,14406,15395,16191,18475,18475,18475,18475,18475,20,19,0,0,0,0,0,0,0x1bdc80,0x5c6100,0xaee700,0xef6b80,0x141f180,0x1947780,0x1d4fc00,0x2278200,0x2680680,0x2ba8c80,0x2fb1100,0x34d9700,0x38e1b80,0x3e0a180,0x4332780,0x4738c80,0x4b39300,0x505f980,0x5460000,0x7ce00,0x7ce00,0x7ce00,0x7ce00,0x7ce00,0x7ce00,0x7ce00,0x7ce00,0x7ce00,0x7ce00,0x7ce00,0x7ce00,0x7ce00,22,830,1822,2630,3622,4614,5422,6414,7222,8214,9022,10014,10822,11814,12806,13611,14407,15396,16192,18476,17684,17684,17684,17684,21,20,0,0,0,0,0,0,0x1be700,0x5c6b80,0xaef180,0xef7600,0x141fc00,0x1948200,0x1d50680,0x2278c80,0x2681100,0x2ba9700,0x2fb1b80,0x34da180,0x38e2600,0x3e0ac00,0x4333200,0x4739700,0x4b39d80,0x5060400,0x5460a80,0x7d880,0x5c1fd80,0x5c1fd80,0x5c1fd80,0x5c1fd80,0x5c1fd80,0x5c1fd80,0x5c1fd80,0x5c1fd80,0x5c1fd80,0x5c1fd80,0x5c1fd80,0x5c1fd80,23,831,1823,2631,3623,4615,5423,6415,7223,8215,9023,10015,10823,11815,12807,13612,14592,15397,16193,17170,17685,17685,17685,17685,21,20,0,0,0,0,0,0,0x1bf180,0x5c7600,0xaefc00,0xef8080,0x1420680,0x1948c80,0x1d51100,0x2279700,0x2681b80,0x2baa180,0x2fb2600,0x34dac00,0x38e3080,0x3e0b680,0x4333c80,0x473a180,0x4c5a980,0x5060e80,0x5461500,0x597fd80,0x5c20800,0x5c20800,0x5c20800,0x5c20800,0x5c20800,0x5c20800,0x5c20800,0x5c20800,0x5c20800,0x5c20800,0x5c20800,0x5c20800,24,832,1824,2816,3624,4616,5424,6416,7224,8216,9024,10016,11008,11816,12808,18435,14593,18450,16194,18477,18477,18477,18477,18477,20,19,0,0,0,0,0,0,0x1bfc00,0x5c8080,0xaf0680,0x1018c80,0x1421100,0x1949700,0x1d51b80,0x227a180,0x2682600,0x2baac00,0x2fb3080,0x34db680,0x3a03c80,0x3e0c100,0x4334700,0x62a00,0x4c5b400,0x6c780,0x5461f80,0x7e300,0x7e300,0x7e300,0x7e300,0x7e300,0x7e300,0x7e300,0x7e300,0x7e300,0x7e300,0x7e300,0x7e300,0x7e300,25,833,1825,2817,3625,4617,5425,6417,7225,8217,9025,10017,11009,11817,12809,13613,14594,15398,16195,18478,18478,18478,18478,18478,20,19,0,0,0,0,0,0,0x1c0680,0x5c8b00,0xaf1100,0x1019700,0x1421b80,0x194a180,0x1d52600,0x227ac00,0x2683080,0x2bab680,0x2fb3b00,0x34dc100,0x3a04700,0x3e0cb80,0x4335180,0x473ac00,0x4c5be80,0x5061900,0x5462a00,0x7ed80,0x7ed80,0x7ed80,0x7ed80,0x7ed80,0x7ed80,0x7ed80,0x7ed80,0x7ed80,0x7ed80,0x7ed80,0x7ed80,0x7ed80,26,834,1826,2818,3626,4618,5426,6418,7226,8218,9026,10018,11010,11818,12810,13614,14595,15399,16196,17171,17686,18219,18219,18219,22,21,0,0,0,0,0,0,0x1c1100,0x5c9580,0xaf1b80,0x101a180,0x1422600,0x194ac00,0x1d53080,0x227b680,0x2683b00,0x2bac100,0x2fb4580,0x34dcb80,0x3a05180,0x3e0d600,0x4335c00,0x473b680,0x4c5c900,0x5062380,0x5463480,0x5980800,0x5c21280,0x5ecda00,0x5ecda00,0x5ecda00,0x5ecda00,0x5ecda00,0x5ecda00,0x5ecda00,0x5ecda00,0x5ecda00,0x5ecda00,0x5ecda00,27,835,1827,2819,3627,4619,5427,6419,7227,8219,9027,10019,11011,11819,12811,13615,14596,15400,16197,17172,17687,17687,17687,17687,21,20,0,0,0,0,0,0,0x1c1b80,0x5ca000,0xaf2600,0x101ac00,0x1423080,0x194b680,0x1d53b00,0x227c100,0x2684580,0x2bacb80,0x2fb5000,0x34dd600,0x3a05c00,0x3e0e080,0x4336680,0x473c100,0x4c5d380,0x5062e00,0x5463f00,0x5981280,0x5c21d00,0x5c21d00,0x5c21d00,0x5c21d00,0x5c21d00,0x5c21d00,0x5c21d00,0x5c21d00,0x5c21d00,0x5c21d00,0x5c21d00,0x5c21d00,28,836,1828,2820,3628,4620,5428,6420,7228,8220,9028,10020,11012,11820,12812,13616,14597,15401,16198,18479,17688,17688,17688,17688,21,20,0,0,0,0,0,0,0x1c2600,0x5caa80,0xaf3080,0x101b680,0x1423b00,0x194c100,0x1d54580,0x227cb80,0x2685000,0x2bad600,0x2fb5a80,0x34de080,0x3a06680,0x3e0eb00,0x4337100,0x473cb80,0x4c5de00,0x5063880,0x5464980,0x7f800,0x5c22780,0x5c22780,0x5c22780,0x5c22780,0x5c22780,0x5c22780,0x5c22780,0x5c22780,0x5c22780,0x5c22780,0x5c22780,0x5c22780,29,837,1829,2821,3629,4621,5429,6421,7229,8221,9029,10021,11013,11821,12813,13617,14598,15402,16199,18480,18480,18480,18480,18480,20,19,0,0,0,0,0,0,0x1c3080,0x5cb500,0xaf3b00,0x101c100,0x1424580,0x194cb80,0x1d55000,0x227d600,0x2685a80,0x2bae080,0x2fb6500,0x34deb00,0x3a07100,0x3e0f580,0x4337b80,0x473d600,0x4c5e880,0x5064300,0x5465400,0x80280,0x80280,0x80280,0x80280,0x80280,0x80280,0x80280,0x80280,0x80280,0x80280,0x80280,0x80280,0x80280,30,838,1830,2822,3630,4622,5430,6422,7230,8222,9030,10022,11014,11822,12814,13618,14599,15403,16384,18481,17689,17689,17689,17689,21,20,0,0,0,0,0,0,0x1c3b00,0x5cbf80,0xaf4580,0x101cb80,0x1425000,0x194d600,0x1d55a80,0x227e080,0x2686500,0x2baeb00,0x2fb6f80,0x34df580,0x3a07b80,0x3e10000,0x4338600,0x473e080,0x4c5f300,0x5064d80,0x5586000,0x80d00,0x5c23200,0x5c23200,0x5c23200,0x5c23200,0x5c23200,0x5c23200,0x5c23200,0x5c23200,0x5c23200,0x5c23200,0x5c23200,0x5c23200,31,839,1831,2823,3631,4623,5431,6423,7231,8223,9031,10023,11015,11823,12815,13619,14600,15404,16385,17173,17690,17690,17690,17690,21,20,0,0,0,0,0,0,0x1c4580,0x5cca00,0xaf5000,0x101d600,0x1425a80,0x194e080,0x1d56500,0x227eb00,0x2686f80,0x2baf580,0x2fb7a00,0x34e0000,0x3a08600,0x3e10a80,0x4339080,0x473eb00,0x4c5fd80,0x5065800,0x5586a80,0x5981d00,0x5c23c80,0x5c23c80,0x5c23c80,0x5c23c80,0x5c23c80,0x5c23c80,0x5c23c80,0x5c23c80,0x5c23c80,0x5c23c80,0x5c23c80,0x5c23c80,32,1024,1832,2824,3632,4624,5432,6424,7232,8224,9216,10024,11016,11824,12816,18436,14601,18451,16386,18482,18482,18482,18482,18482,20,19,0,0,0,0,0,0,0x1c5000,0x6ed600,0xaf5a80,0x101e080,0x1426500,0x194eb00,0x1d56f80,0x227f580,0x2687a00,0x2bb0000,0x30d8600,0x34e0a80,0x3a09080,0x3e11500,0x4339b00,0x63480,0x4c60800,0x6d200,0x5587500,0x81780,0x81780,0x81780,0x81780,0x81780,0x81780,0x81780,0x81780,0x81780,0x81780,0x81780,0x81780,0x81780,33,1025,1833,2825,3633,4625,5433,6425,7233,8225,9217,10025,11017,11825,12817,13620,14602,15405,16387,18483,18483,18483,18483,18483,20,19,0,0,0,0,0,0,0x1c5a80,0x6ee080,0xaf6500,0x101eb00,0x1426f80,0x194f580,0x1d57a00,0x2280000,0x2688480,0x2bb0a80,0x30d9080,0x34e1500,0x3a09b00,0x3e11f80,0x433a580,0x473f580,0x4c61280,0x5066280,0x5587f80,0x82200,0x82200,0x82200,0x82200,0x82200,0x82200,0x82200,0x82200,0x82200,0x82200,0x82200,0x82200,0x82200,34,1026,1834,2826,3634,4626,5434,6426,7234,8226,9218,10026,11018,11826,12818,13621,14603,15406,16388,17174,17691,18220,18220,18220,22,21,0,0,0,0,0,0,0x1c6500,0x6eeb00,0xaf6f80,0x101f580,0x1427a00,0x1950000,0x1d58480,0x2280a80,0x2688f00,0x2bb1500,0x30d9b00,0x34e1f80,0x3a0a580,0x3e12a00,0x433b000,0x4740000,0x4c61d00,0x5066d00,0x5588a00,0x5982780,0x5c24700,0x5ece480,0x5ece480,0x5ece480,0x5ece480,0x5ece480,0x5ece480,0x5ece480,0x5ece480,0x5ece480,0x5ece480,0x5ece480,35,1027,1835,2827,3635,4627,5435,6427,7235,8227,9219,10027,11019,11827,12819,13622,14604,15407,16389,17175,17692,17692,17692,17692,21,20,0,0,0,0,0,0,0x1c6f80,0x6ef580,0xaf7a00,0x1020000,0x1428480,0x1950a80,0x1d58f00,0x2281500,0x2689980,0x2bb1f80,0x30da580,0x34e2a00,0x3a0b000,0x3e13480,0x433ba80,0x4740a80,0x4c62780,0x5067780,0x5589480,0x5983200,0x5c25180,0x5c25180,0x5c25180,0x5c25180,0x5c25180,0x5c25180,0x5c25180,0x5c25180,0x5c25180,0x5c25180,0x5c25180,0x5c25180,36,1028,1836,2828,3636,4628,5436,6428,7236,8228,9220,10028,11020,11828,12820,13623,14605,15408,16390,17176,17693,17693,17693,17693,21,20,0,0,0,0,0,0,0x1c7a00,0x6f0000,0xaf8480,0x1020a80,0x1428f00,0x1951500,0x1d59980,0x2281f80,0x268a400,0x2bb2a00,0x30db000,0x34e3480,0x3a0ba80,0x3e13f00,0x433c500,0x4741500,0x4c63200,0x5068200,0x5589f00,0x5983c80,0x5c25c00,0x5c25c00,0x5c25c00,0x5c25c00,0x5c25c00,0x5c25c00,0x5c25c00,0x5c25c00,0x5c25c00,0x5c25c00,0x5c25c00,0x5c25c00,37,1029,1837,2829,3637,4629,5437,6429,7237,8229,9221,10029,11021,11829,12821,13624,14606,15409,16391,18484,18484,18484,18484,18484,20,19,0,0,0,0,0,0,0x1c8480,0x6f0a80,0xaf8f00,0x1021500,0x1429980,0x1951f80,0x1d5a400,0x2282a00,0x268ae80,0x2bb3480,0x30dba80,0x34e3f00,0x3a0c500,0x3e14980,0x433cf80,0x4741f80,0x4c63c80,0x5068c80,0x558a980,0x82c80,0x82c80,0x82c80,0x82c80,0x82c80,0x82c80,0x82c80,0x82c80,0x82c80,0x82c80,0x82c80,0x82c80,0x82c80,38,1030,1838,2830,3638,4630,5438,6430,7238,8230,9222,10030,11022,11830,12822,13625,14607,15410,16392,18485,17694,17694,17694,17694,21,20,0,0,0,0,0,0,0x1c8f00,0x6f1500,0xaf9980,0x1021f80,0x142a400,0x1952a00,0x1d5ae80,0x2283480,0x268b900,0x2bb3f00,0x30dc500,0x34e4980,0x3a0cf80,0x3e15400,0x433da00,0x4742a00,0x4c64700,0x5069700,0x558b400,0x83700,0x5c26680,0x5c26680,0x5c26680,0x5c26680,0x5c26680,0x5c26680,0x5c26680,0x5c26680,0x5c26680,0x5c26680,0x5c26680,0x5c26680,39,1031,1839,2831,3639,4631,5439,6431,7239,8231,9223,10031,11023,11831,12823,13626,14608,15411,16393,17177,17695,17695,17695,17695,21,20,0,0,0,0,0,0,0x1c9980,0x6f1f80,0xafa400,0x1022a00,0x142ae80,0x1953480,0x1d5b900,0x2283f00,0x268c380,0x2bb4980,0x30dcf80,0x34e5400,0x3a0da00,0x3e15e80,0x433e480,0x4743480,0x4c65180,0x506a180,0x558be80,0x5984700,0x5c27100,0x5c27100,0x5c27100,0x5c27100,0x5c27100,0x5c27100,0x5c27100,0x5c27100,0x5c27100,0x5c27100,0x5c27100,0x5c27100,40,1032,1840,2832,3640,4632,5440,6432,7424,8232,9224,10032,11024,11832,12824,18437,14609,18452,16394,18486,18486,18486,18486,18486,20,19,0,0,0,0,0,0,0x1ca400,0x6f2a00,0xafae80,0x1023480,0x142b900,0x1953f00,0x1d5c380,0x2284980,0x27acf80,0x2bb5400,0x30dda00,0x34e5e80,0x3a0e480,0x3e16900,0x433ef00,0x63f00,0x4c65c00,0x6dc80,0x558c900,0x84180,0x84180,0x84180,0x84180,0x84180,0x84180,0x84180,0x84180,0x84180,0x84180,0x84180,0x84180,0x84180,41,1033,1841,2833,3641,4633,5441,6433,7425,8233,9225,10033,11025,11833,12825,13627,14610,15412,16395,18487,18487,18487,18487,18487,20,19,0,0,0,0,0,0,0x1cae80,0x6f3480,0xafb900,0x1023f00,0x142c380,0x1954980,0x1d5ce00,0x2285400,0x27ada00,0x2bb5e80,0x30de480,0x34e6900,0x3a0ef00,0x3e17380,0x433f980,0x4743f00,0x4c66680,0x506ac00,0x558d380,0x84c00,0x84c00,0x84c00,0x84c00,0x84c00,0x84c00,0x84c00,0x84c00,0x84c00,0x84c00,0x84c00,0x84c00,0x84c00,42,1034,1842,2834,3642,4634,5442,6434,7426,8234,9226,10034,11026,11834,12826,13628,14611,15413,16396,17178,17696,18221,18221,18221,22,21,0,0,0,0,0,0,0x1cb900,0x6f3f00,0xafc380,0x1024980,0x142ce00,0x1955400,0x1d5d880,0x2285e80,0x27ae480,0x2bb6900,0x30def00,0x34e7380,0x3a0f980,0x3e17e00,0x4340400,0x4744980,0x4c67100,0x506b680,0x558de00,0x5985180,0x5c27b80,0x5ecef00,0x5ecef00,0x5ecef00,0x5ecef00,0x5ecef00,0x5ecef00,0x5ecef00,0x5ecef00,0x5ecef00,0x5ecef00,0x5ecef00,43,1035,1843,2835,3643,4635,5443,6435,7427,8235,9227,10035,11027,11835,12827,13629,14612,15414,16397,17179,17697,17697,17697,17697,21,20,0,0,0,0,0,0,0x1cc380,0x6f4980,0xafce00,0x1025400,0x142d880,0x1955e80,0x1d5e300,0x2286900,0x27aef00,0x2bb7380,0x30df980,0x34e7e00,0x3a10400,0x3e18880,0x4340e80,0x4745400,0x4c67b80,0x506c100,0x558e880,0x5985c00,0x5c28600,0x5c28600,0x5c28600,0x5c28600,0x5c28600,0x5c28600,0x5c28600,0x5c28600,0x5c28600,0x5c28600,0x5c28600,0x5c28600,44,1036,1844,2836,3644,4636,5444,6436,7428,8236,9228,10036,11028,11836,12828,13630,14613,15415,16398,17180,17698,17698,17698,17698,21,20,0,0,0,0,0,0,0x1cce00,0x6f5400,0xafd880,0x1025e80,0x142e300,0x1956900,0x1d5ed80,0x2287380,0x27af980,0x2bb7e00,0x30e0400,0x34e8880,0x3a10e80,0x3e19300,0x4341900,0x4745e80,0x4c68600,0x506cb80,0x558f300,0x5986680,0x5c29080,0x5c29080,0x5c29080,0x5c29080,0x5c29080,0x5c29080,0x5c29080,0x5c29080,0x5c29080,0x5c29080,0x5c29080,0x5c29080,45,1037,1845,2837,3645,4637,5445,6437,7429,8237,9229,10037,11029,11837,12829,13631,14614,15416,16399,18488,18488,18488,18488,18488,20,19,0,0,0,0,0,0,0x1cd880,0x6f5e80,0xafe300,0x1026900,0x142ed80,0x1957380,0x1d5f800,0x2287e00,0x27b0400,0x2bb8880,0x30e0e80,0x34e9300,0x3a11900,0x3e19d80,0x4342380,0x4746900,0x4c69080,0x506d600,0x558fd80,0x85680,0x85680,0x85680,0x85680,0x85680,0x85680,0x85680,0x85680,0x85680,0x85680,0x85680,0x85680,0x85680,46,1038,1846,2838,3646,4638,5446,6438,7430,8238,9230,10038,11030,11838,12830,13632,14615,15417,16400,18489,17699,17699,17699,17699,21,20,0,0,0,0,0,0,0x1ce300,0x6f6900,0xafed80,0x1027380,0x142f800,0x1957e00,0x1d60280,0x2288880,0x27b0e80,0x2bb9300,0x30e1900,0x34e9d80,0x3a12380,0x3e1a800,0x4342e00,0x4747380,0x4c69b00,0x506e080,0x5590800,0x86100,0x5c29b00,0x5c29b00,0x5c29b00,0x5c29b00,0x5c29b00,0x5c29b00,0x5c29b00,0x5c29b00,0x5c29b00,0x5c29b00,0x5c29b00,0x5c29b00,47,1039,1847,2839,3647,4639,5447,6439,7431,8239,9231,10039,11031,11839,12831,13633,14616,15418,16401,17181,17700,17700,17700,17700,21,20,0,0,0,0,0,0,0x1ced80,0x6f7380,0xaff800,0x1027e00,0x1430280,0x1958880,0x1d60d00,0x2289300,0x27b1900,0x2bb9d80,0x30e2380,0x34ea800,0x3a12e00,0x3e1b280,0x4343880,0x4747e00,0x4c6a580,0x506eb00,0x5591280,0x5987100,0x5c2a580,0x5c2a580,0x5c2a580,0x5c2a580,0x5c2a580,0x5c2a580,0x5c2a580,0x5c2a580,0x5c2a580,0x5c2a580,0x5c2a580,0x5c2a580,48,1040,1848,2840,3648,4640,5632,6440,7432,8240,9232,10040,11032,11840,12832,18438,14617,18453,16402,18490,18490,18490,18490,18490,20,19,0,0,0,0,0,0,0x1cf800,0x6f7e00,0xb00280,0x1028880,0x1430d00,0x1959300,0x1e81900,0x2289d80,0x27b2380,0x2bba800,0x30e2e00,0x34eb280,0x3a13880,0x3e1bd00,0x4344300,0x64980,0x4c6b000,0x6e700,0x5591d00,0x86b80,0x86b80,0x86b80,0x86b80,0x86b80,0x86b80,0x86b80,0x86b80,0x86b80,0x86b80,0x86b80,0x86b80,0x86b80,49,1041,1849,2841,3649,4641,5633,6441,7433,8241,9233,10041,11033,11841,12833,13634,14618,15419,16403,18491,18491,18491,18491,18491,20,19,0,0,0,0,0,0,0x1d0280,0x6f8880,0xb00d00,0x1029300,0x1431780,0x1959d80,0x1e82380,0x228a800,0x27b2e00,0x2bbb280,0x30e3880,0x34ebd00,0x3a14300,0x3e1c780,0x4344d80,0x4748880,0x4c6ba80,0x506f580,0x5592780,0x87600,0x87600,0x87600,0x87600,0x87600,0x87600,0x87600,0x87600,0x87600,0x87600,0x87600,0x87600,0x87600,50,1042,1850,2842,3650,4642,5634,6442,7434,8242,9234,10042,11034,11842,12834,13635,14619,15420,16404,17182,17701,18222,18222,18222,22,21,0,0,0,0,0,0,0x1d0d00,0x6f9300,0xb01780,0x1029d80,0x1432200,0x195a800,0x1e82e00,0x228b280,0x27b3880,0x2bbbd00,0x30e4300,0x34ec780,0x3a14d80,0x3e1d200,0x4345800,0x4749300,0x4c6c500,0x5070000,0x5593200,0x5987b80,0x5c2b000,0x5ecf980,0x5ecf980,0x5ecf980,0x5ecf980,0x5ecf980,0x5ecf980,0x5ecf980,0x5ecf980,0x5ecf980,0x5ecf980,0x5ecf980,51,1043,1851,2843,3651,4643,5635,6443,7435,8243,9235,10043,11035,11843,12835,13636,14620,15421,16405,17183,17702,17702,17702,17702,21,20,0,0,0,0,0,0,0x1d1780,0x6f9d80,0xb02200,0x102a800,0x1432c80,0x195b280,0x1e83880,0x228bd00,0x27b4300,0x2bbc780,0x30e4d80,0x34ed200,0x3a15800,0x3e1dc80,0x4346280,0x4749d80,0x4c6cf80,0x5070a80,0x5593c80,0x5988600,0x5c2ba80,0x5c2ba80,0x5c2ba80,0x5c2ba80,0x5c2ba80,0x5c2ba80,0x5c2ba80,0x5c2ba80,0x5c2ba80,0x5c2ba80,0x5c2ba80,0x5c2ba80,52,1044,1852,2844,3652,4644,5636,6444,7436,8244,9236,10044,11036,11844,12836,13637,14621,15422,16406,17184,17703,17703,17703,17703,21,20,0,0,0,0,0,0,0x1d2200,0x6fa800,0xb02c80,0x102b280,0x1433700,0x195bd00,0x1e84300,0x228c780,0x27b4d80,0x2bbd200,0x30e5800,0x34edc80,0x3a16280,0x3e1e700,0x4346d00,0x474a800,0x4c6da00,0x5071500,0x5594700,0x5989080,0x5c2c500,0x5c2c500,0x5c2c500,0x5c2c500,0x5c2c500,0x5c2c500,0x5c2c500,0x5c2c500,0x5c2c500,0x5c2c500,0x5c2c500,0x5c2c500,53,1045,1853,2845,3653,4645,5637,6445,7437,8245,9237,10045,11037,11845,12837,13638,14622,15423,16407,18492,18492,18492,18492,18492,20,19,0,0,0,0,0,0,0x1d2c80,0x6fb280,0xb03700,0x102bd00,0x1434180,0x195c780,0x1e84d80,0x228d200,0x27b5800,0x2bbdc80,0x30e6280,0x34ee700,0x3a16d00,0x3e1f180,0x4347780,0x474b280,0x4c6e480,0x5071f80,0x5595180,0x88080,0x88080,0x88080,0x88080,0x88080,0x88080,0x88080,0x88080,0x88080,0x88080,0x88080,0x88080,0x88080,54,1046,1854,2846,3654,4646,5638,6446,7438,8246,9238,10046,11038,11846,12838,13639,14623,15424,16408,18493,17704,17704,17704,17704,21,20,0,0,0,0,0,0,0x1d3700,0x6fbd00,0xb04180,0x102c780,0x1434c00,0x195d200,0x1e85800,0x228dc80,0x27b6280,0x2bbe700,0x30e6d00,0x34ef180,0x3a17780,0x3e1fc00,0x4348200,0x474bd00,0x4c6ef00,0x5072a00,0x5595c00,0x88b00,0x5c2cf80,0x5c2cf80,0x5c2cf80,0x5c2cf80,0x5c2cf80,0x5c2cf80,0x5c2cf80,0x5c2cf80,0x5c2cf80,0x5c2cf80,0x5c2cf80,0x5c2cf80,55,1047,1855,2847,3655,4647,5639,6447,7439,8247,9239,10047,11039,11847,12839,13824,14624,15425,16409,17185,17705,17705,17705,17705,21,20,0,0,0,0,0,0,0x1d4180,0x6fc780,0xb04c00,0x102d200,0x1435680,0x195dc80,0x1e86280,0x228e700,0x27b6d00,0x2bbf180,0x30e7780,0x34efc00,0x3a18200,0x3e20680,0x4348c80,0x486c900,0x4c6f980,0x5073480,0x5596680,0x5989b00,0x5c2da00,0x5c2da00,0x5c2da00,0x5c2da00,0x5c2da00,0x5c2da00,0x5c2da00,0x5c2da00,0x5c2da00,0x5c2da00,0x5c2da00,0x5c2da00,56,1048,1856,2848,3840,4648,5640,6448,7440,8248,9240,10048,11040,12032,12840,18439,14625,18454,16410,18494,18494,18494,18494,18494,20,19,0,0,0,0,0,0,0x1d4c00,0x6fd200,0xb05680,0x102dc80,0x1556280,0x195e700,0x1e86d00,0x228f180,0x27b7780,0x2bbfc00,0x30e8200,0x34f0680,0x3a18c80,0x3f41280,0x4349700,0x65400,0x4c70400,0x6f180,0x5597100,0x89580,0x89580,0x89580,0x89580,0x89580,0x89580,0x89580,0x89580,0x89580,0x89580,0x89580,0x89580,0x89580,57,1049,1857,2849,3841,4649,5641,6449,7441,8249,9241,10049,11041,12033,12841,13825,14626,15426,16411,18495,18495,18495,18495,18495,20,19,0,0,0,0,0,0,0x1d5680,0x6fdc80,0xb06100,0x102e700,0x1556d00,0x195f180,0x1e87780,0x228fc00,0x27b8200,0x2bc0680,0x30e8c80,0x34f1100,0x3a19700,0x3f41d00,0x434a180,0x486d380,0x4c70e80,0x5073f00,0x5597b80,0x8a000,0x8a000,0x8a000,0x8a000,0x8a000,0x8a000,0x8a000,0x8a000,0x8a000,0x8a000,0x8a000,0x8a000,0x8a000,58,1050,1858,2850,3842,4650,5642,6450,7442,8250,9242,10050,11042,12034,12842,13826,14627,15427,16412,17186,17706,18223,18223,18223,22,21,0,0,0,0,0,0,0x1d6100,0x6fe700,0xb06b80,0x102f180,0x1557780,0x195fc00,0x1e88200,0x2290680,0x27b8c80,0x2bc1100,0x30e9700,0x34f1b80,0x3a1a180,0x3f42780,0x434ac00,0x486de00,0x4c71900,0x5074980,0x5598600,0x598a580,0x5c2e480,0x5ed0400,0x5ed0400,0x5ed0400,0x5ed0400,0x5ed0400,0x5ed0400,0x5ed0400,0x5ed0400,0x5ed0400,0x5ed0400,0x5ed0400,59,1051,1859,2851,3843,4651,5643,6451,7443,8251,9243,10051,11043,12035,12843,13827,14628,15428,16413,17187,17707,17707,17707,17707,21,20,0,0,0,0,0,0,0x1d6b80,0x6ff180,0xb07600,0x102fc00,0x1558200,0x1960680,0x1e88c80,0x2291100,0x27b9700,0x2bc1b80,0x30ea180,0x34f2600,0x3a1ac00,0x3f43200,0x434b680,0x486e880,0x4c72380,0x5075400,0x5599080,0x598b000,0x5c2ef00,0x5c2ef00,0x5c2ef00,0x5c2ef00,0x5c2ef00,0x5c2ef00,0x5c2ef00,0x5c2ef00,0x5c2ef00,0x5c2ef00,0x5c2ef00,0x5c2ef00,60,1052,1860,2852,3844,4652,5644,6452,7444,8252,9244,10052,11044,12036,12844,13828,14629,15429,16414,17188,17708,17708,17708,17708,21,20,0,0,0,0,0,0,0x1d7600,0x6ffc00,0xb08080,0x1030680,0x1558c80,0x1961100,0x1e89700,0x2291b80,0x27ba180,0x2bc2600,0x30eac00,0x34f3080,0x3a1b680,0x3f43c80,0x434c100,0x486f300,0x4c72e00,0x5075e80,0x5599b00,0x598ba80,0x5c2f980,0x5c2f980,0x5c2f980,0x5c2f980,0x5c2f980,0x5c2f980,0x5c2f980,0x5c2f980,0x5c2f980,0x5c2f980,0x5c2f980,0x5c2f980,61,1053,1861,2853,3845,4653,5645,6453,7445,8253,9245,10053,11045,12037,12845,13829,14630,15430,16415,18496,18496,18496,18496,18496,20,19,0,0,0,0,0,0,0x1d8080,0x700680,0xb08b00,0x1031100,0x1559700,0x1961b80,0x1e8a180,0x2292600,0x27bac00,0x2bc3080,0x30eb680,0x34f3b00,0x3a1c100,0x3f44700,0x434cb80,0x486fd80,0x4c73880,0x5076900,0x559a580,0x8aa80,0x8aa80,0x8aa80,0x8aa80,0x8aa80,0x8aa80,0x8aa80,0x8aa80,0x8aa80,0x8aa80,0x8aa80,0x8aa80,0x8aa80,62,1054,1862,2854,3846,4654,5646,6454,7446,8254,9246,10054,11046,12038,12846,13830,14631,15431,16416,18497,17709,17709,17709,17709,21,20,0,0,0,0,0,0,0x1d8b00,0x701100,0xb09580,0x1031b80,0x155a180,0x1962600,0x1e8ac00,0x2293080,0x27bb680,0x2bc3b00,0x30ec100,0x34f4580,0x3a1cb80,0x3f45180,0x434d600,0x4870800,0x4c74300,0x5077380,0x559b000,0x8b500,0x5c30400,0x5c30400,0x5c30400,0x5c30400,0x5c30400,0x5c30400,0x5c30400,0x5c30400,0x5c30400,0x5c30400,0x5c30400,0x5c30400,63,1055,1863,2855,3847,4655,5647,6455,7447,8255,9247,10055,11047,12039,12847,13831,14632,15616,16417,17189,17710,17710,17710,17710,21,20,0,0,0,0,0,0,0x1d9580,0x701b80,0xb0a000,0x1032600,0x155ac00,0x1963080,0x1e8b680,0x2293b00,0x27bc100,0x2bc4580,0x30ecb80,0x34f5000,0x3a1d600,0x3f45c00,0x434e080,0x4871280,0x4c74d80,0x5197f80,0x559ba80,0x598c500,0x5c30e80,0x5c30e80,0x5c30e80,0x5c30e80,0x5c30e80,0x5c30e80,0x5c30e80,0x5c30e80,0x5c30e80,0x5c30e80,0x5c30e80,0x5c30e80,64,1056,2048,2856,3848,4656,5648,6456,7448,8256,9248,10240,11048,12040,12848,18440,14633,18455,16418,18498,18498,18498,18498,18498,20,19,0,0,0,0,0,0,0x1da000,0x702600,0xc2ac00,0x1033080,0x155b680,0x1963b00,0x1e8c100,0x2294580,0x27bcb80,0x2bc5000,0x30ed600,0x3615c00,0x3a1e080,0x3f46680,0x434eb00,0x65e80,0x4c75800,0x6fc00,0x559c500,0x8bf80,0x8bf80,0x8bf80,0x8bf80,0x8bf80,0x8bf80,0x8bf80,0x8bf80,0x8bf80,0x8bf80,0x8bf80,0x8bf80,0x8bf80,65,1057,2049,2857,3849,4657,5649,6457,7449,8257,9249,10241,11049,12041,12849,13832,14634,15617,16419,18499,18499,18499,18499,18499,20,19,0,0,0,0,0,0,0x1daa80,0x703080,0xc2b680,0x1033b00,0x155c100,0x1964580,0x1e8cb80,0x2295000,0x27bd600,0x2bc5a80,0x30ee080,0x3616680,0x3a1eb00,0x3f47100,0x434f580,0x4871d00,0x4c76280,0x5198a00,0x559cf80,0x8ca00,0x8ca00,0x8ca00,0x8ca00,0x8ca00,0x8ca00,0x8ca00,0x8ca00,0x8ca00,0x8ca00,0x8ca00,0x8ca00,0x8ca00,66,1058,2050,2858,3850,4658,5650,6458,7450,8258,9250,10242,11050,12042,12850,13833,14635,15618,16420,17190,17711,18224,18224,18224,22,21,0,0,0,0,0,0,0x1db500,0x703b00,0xc2c100,0x1034580,0x155cb80,0x1965000,0x1e8d600,0x2295a80,0x27be080,0x2bc6500,0x30eeb00,0x3617100,0x3a1f580,0x3f47b80,0x4350000,0x4872780,0x4c76d00,0x5199480,0x559da00,0x598cf80,0x5c31900,0x5ed0e80,0x5ed0e80,0x5ed0e80,0x5ed0e80,0x5ed0e80,0x5ed0e80,0x5ed0e80,0x5ed0e80,0x5ed0e80,0x5ed0e80,0x5ed0e80,67,1059,2051,2859,3851,4659,5651,6459,7451,8259,9251,10243,11051,12043,12851,13834,14636,15619,16421,17191,17712,17712,17712,17712,21,20,0,0,0,0,0,0,0x1dbf80,0x704580,0xc2cb80,0x1035000,0x155d600,0x1965a80,0x1e8e080,0x2296500,0x27beb00,0x2bc6f80,0x30ef580,0x3617b80,0x3a20000,0x3f48600,0x4350a80,0x4873200,0x4c77780,0x5199f00,0x559e480,0x598da00,0x5c32380,0x5c32380,0x5c32380,0x5c32380,0x5c32380,0x5c32380,0x5c32380,0x5c32380,0x5c32380,0x5c32380,0x5c32380,0x5c32380,68,1060,2052,2860,3852,4660,5652,6460,7452,8260,9252,10244,11052,12044,12852,13835,14637,15620,16422,17192,17713,17713,17713,17713,21,20,0,0,0,0,0,0,0x1dca00,0x705000,0xc2d600,0x1035a80,0x155e080,0x1966500,0x1e8eb00,0x2296f80,0x27bf580,0x2bc7a00,0x30f0000,0x3618600,0x3a20a80,0x3f49080,0x4351500,0x4873c80,0x4c78200,0x519a980,0x559ef00,0x598e480,0x5c32e00,0x5c32e00,0x5c32e00,0x5c32e00,0x5c32e00,0x5c32e00,0x5c32e00,0x5c32e00,0x5c32e00,0x5c32e00,0x5c32e00,0x5c32e00,69,1061,2053,2861,3853,4661,5653,6461,7453,8261,9253,10245,11053,12045,12853,13836,14638,15621,16423,18500,18500,18500,18500,18500,20,19,0,0,0,0,0,0,0x1dd480,0x705a80,0xc2e080,0x1036500,0x155eb00,0x1966f80,0x1e8f580,0x2297a00,0x27c0000,0x2bc8480,0x30f0a80,0x3619080,0x3a21500,0x3f49b00,0x4351f80,0x4874700,0x4c78c80,0x519b400,0x559f980,0x8d480,0x8d480,0x8d480,0x8d480,0x8d480,0x8d480,0x8d480,0x8d480,0x8d480,0x8d480,0x8d480,0x8d480,0x8d480,70,1062,2054,2862,3854,4662,5654,6462,7454,8262,9254,10246,11054,12046,12854,13837,14639,15622,16424,18501,17714,17714,17714,17714,21,20,0,0,0,0,0,0,0x1ddf00,0x706500,0xc2eb00,0x1036f80,0x155f580,0x1967a00,0x1e90000,0x2298480,0x27c0a80,0x2bc8f00,0x30f1500,0x3619b00,0x3a21f80,0x3f4a580,0x4352a00,0x4875180,0x4c79700,0x519be80,0x55a0400,0x8df00,0x5c33880,0x5c33880,0x5c33880,0x5c33880,0x5c33880,0x5c33880,0x5c33880,0x5c33880,0x5c33880,0x5c33880,0x5c33880,0x5c33880,71,1063,2055,2863,3855,4663,5655,6463,7455,8263,9255,10247,11055,12047,12855,13838,14640,15623,16425,17193,17715,17715,17715,17715,21,20,0,0,0,0,0,0,0x1de980,0x706f80,0xc2f580,0x1037a00,0x1560000,0x1968480,0x1e90a80,0x2298f00,0x27c1500,0x2bc9980,0x30f1f80,0x361a580,0x3a22a00,0x3f4b000,0x4353480,0x4875c00,0x4c7a180,0x519c900,0x55a0e80,0x598ef00,0x5c34300,0x5c34300,0x5c34300,0x5c34300,0x5c34300,0x5c34300,0x5c34300,0x5c34300,0x5c34300,0x5c34300,0x5c34300,0x5c34300,256,1064,2056,2864,3856,4664,5656,6464,7456,8448,9256,10248,11056,12048,12856,18441,14641,18456,16426,18502,18502,18502,18502,18502,20,19,0,0,0,0,0,0,0x2ff580,0x707a00,0xc30000,0x1038480,0x1560a80,0x1968f00,0x1e91500,0x2299980,0x27c1f80,0x2cea580,0x30f2a00,0x361b000,0x3a23480,0x3f4ba80,0x4353f00,0x66900,0x4c7ac00,0x70680,0x55a1900,0x8e980,0x8e980,0x8e980,0x8e980,0x8e980,0x8e980,0x8e980,0x8e980,0x8e980,0x8e980,0x8e980,0x8e980,0x8e980,257,1065,2057,2865,3857,4665,5657,6465,7457,8449,9257,10249,11057,12049,12857,13839,14642,15624,16427,18503,18503,18503,18503,18503,20,19,0,0,0,0,0,0,0x300000,0x708480,0xc30a80,0x1038f00,0x1561500,0x1969980,0x1e91f80,0x229a400,0x27c2a00,0x2ceb000,0x30f3480,0x361ba80,0x3a23f00,0x3f4c500,0x4354980,0x4876680,0x4c7b680,0x519d380,0x55a2380,0x8f400,0x8f400,0x8f400,0x8f400,0x8f400,0x8f400,0x8f400,0x8f400,0x8f400,0x8f400,0x8f400,0x8f400,0x8f400,258,1066,2058,2866,3858,4666,5658,6466,7458,8450,9258,10250,11058,12050,12858,13840,14643,15625,16428,17194,17716,18225,18225,18225,22,21,0,0,0,0,0,0,0x300a80,0x708f00,0xc31500,0x1039980,0x1561f80,0x196a400,0x1e92a00,0x229ae80,0x27c3480,0x2ceba80,0x30f3f00,0x361c500,0x3a24980,0x3f4cf80,0x4355400,0x4877100,0x4c7c100,0x519de00,0x55a2e00,0x598f980,0x5c34d80,0x5ed1900,0x5ed1900,0x5ed1900,0x5ed1900,0x5ed1900,0x5ed1900,0x5ed1900,0x5ed1900,0x5ed1900,0x5ed1900,0x5ed1900,259,1067,2059,2867,3859,4667,5659,6467,7459,8451,9259,10251,11059,12051,12859,13841,14644,15626,16429,17195,17717,17717,17717,17717,21,20,0,0,0,0,0,0,0x301500,0x709980,0xc31f80,0x103a400,0x1562a00,0x196ae80,0x1e93480,0x229b900,0x27c3f00,0x2cec500,0x30f4980,0x361cf80,0x3a25400,0x3f4da00,0x4355e80,0x4877b80,0x4c7cb80,0x519e880,0x55a3880,0x5990400,0x5c35800,0x5c35800,0x5c35800,0x5c35800,0x5c35800,0x5c35800,0x5c35800,0x5c35800,0x5c35800,0x5c35800,0x5c35800,0x5c35800,260,1068,2060,2868,3860,4668,5660,6468,7460,8452,9260,10252,11060,12052,12860,13842,14645,15627,16430,17196,17718,17718,17718,17718,21,20,0,0,0,0,0,0,0x301f80,0x70a400,0xc32a00,0x103ae80,0x1563480,0x196b900,0x1e93f00,0x229c380,0x27c4980,0x2cecf80,0x30f5400,0x361da00,0x3a25e80,0x3f4e480,0x4356900,0x4878600,0x4c7d600,0x519f300,0x55a4300,0x5990e80,0x5c36280,0x5c36280,0x5c36280,0x5c36280,0x5c36280,0x5c36280,0x5c36280,0x5c36280,0x5c36280,0x5c36280,0x5c36280,0x5c36280,261,1069,2061,2869,3861,4669,5661,6469,7461,8453,9261,10253,11061,12053,12861,13843,14646,15628,16431,18504,18504,18504,18504,18504,20,19,0,0,0,0,0,0,0x302a00,0x70ae80,0xc33480,0x103b900,0x1563f00,0x196c380,0x1e94980,0x229ce00,0x27c5400,0x2ceda00,0x30f5e80,0x361e480,0x3a26900,0x3f4ef00,0x4357380,0x4879080,0x4c7e080,0x519fd80,0x55a4d80,0x60000,0x60000,0x60000,0x60000,0x60000,0x60000,0x60000,0x60000,0x60000,0x60000,0x60000,0x60000,0x60000,262,1070,2062,2870,3862,4670,5662,6470,7462,8454,9262,10254,11062,12054,12862,13844,14647,15629,16432,72,17719,17719,17719,17719,21,20,0,0,0,0,0,0,0x303480,0x70b900,0xc33f00,0x103c380,0x1564980,0x196ce00,0x1e95400,0x229d880,0x27c5e80,0x2cee480,0x30f6900,0x361ef00,0x3a27380,0x3f4f980,0x4357e00,0x4879b00,0x4c7eb00,0x51a0800,0x55a5800,0x1af580,0x5c36d00,0x5c36d00,0x5c36d00,0x5c36d00,0x5c36d00,0x5c36d00,0x5c36d00,0x5c36d00,0x5c36d00,0x5c36d00,0x5c36d00,0x5c36d00,263,1071,2063,2871,3863,4671,5663,6471,7463,8455,9263,10255,11063,12055,12863,13845,14648,15630,16433,17197,17720,17720,17720,17720,21,20,0,0,0,0,0,0,0x303f00,0x70c380,0xc34980,0x103ce00,0x1565400,0x196d880,0x1e95e80,0x229e300,0x27c6900,0x2ceef00,0x30f7380,0x361f980,0x3a27e00,0x3f50400,0x4358880,0x487a580,0x4c7f580,0x51a1280,0x55a6280,0x5991900,0x5c37780,0x5c37780,0x5c37780,0x5c37780,0x5c37780,0x5c37780,0x5c37780,0x5c37780,0x5c37780,0x5c37780,0x5c37780,0x5c37780,264,1072,2064,2872,3864,4672,5664,6656,7464,8456,9264,10256,11064,12056,12864,18442,14649,18457,16434,328,328,328,328,328,20,19,0,0,0,0,0,0,0x304980,0x70ce00,0xc35400,0x103d880,0x1565e80,0x196e300,0x1e96900,0x23bef00,0x27c7380,0x2cef980,0x30f7e00,0x3620400,0x3a28880,0x3f50e80,0x4359300,0x67380,0x4c80000,0x71100,0x55a6d00,0x2feb00,0x2feb00,0x2feb00,0x2feb00,0x2feb00,0x2feb00,0x2feb00,0x2feb00,0x2feb00,0x2feb00,0x2feb00,0x2feb00,0x2feb00,265,1073,2065,2873,3865,4673,5665,6657,7465,8457,9265,10257,11065,12057,12865,13846,14650,15631,16435,584,584,584,584,584,20,19,0,0,0,0,0,0,0x305400,0x70d880,0xc35e80,0x103e300,0x1566900,0x196ed80,0x1e97380,0x23bf980,0x27c7e00,0x2cf0400,0x30f8880,0x3620e80,0x3a29300,0x3f51900,0x4359d80,0x487b000,0x4c80a80,0x51a1d00,0x55a7780,0x44e080,0x44e080,0x44e080,0x44e080,0x44e080,0x44e080,0x44e080,0x44e080,0x44e080,0x44e080,0x44e080,0x44e080,0x44e080,266,1074,2066,2874,3866,4674,5666,6658,7466,8458,9266,10258,11066,12058,12866,13847,14651,15632,16436,17198,17721,18226,18226,18226,22,21,0,0,0,0,0,0,0x305e80,0x70e300,0xc36900,0x103ed80,0x1567380,0x196f800,0x1e97e00,0x23c0400,0x27c8880,0x2cf0e80,0x30f9300,0x3621900,0x3a29d80,0x3f52380,0x435a800,0x487ba80,0x4c81500,0x51a2780,0x55a8200,0x5992380,0x5c38200,0x5ed2380,0x5ed2380,0x5ed2380,0x5ed2380,0x5ed2380,0x5ed2380,0x5ed2380,0x5ed2380,0x5ed2380,0x5ed2380,0x5ed2380,267,1075,2067,2875,3867,4675,5667,6659,7467,8459,9267,10259,11067,12059,12867,13848,14652,15633,16437,17199,17722,17722,17722,17722,21,20,0,0,0,0,0,0,0x306900,0x70ed80,0xc37380,0x103f800,0x1567e00,0x1970280,0x1e98880,0x23c0e80,0x27c9300,0x2cf1900,0x30f9d80,0x3622380,0x3a2a800,0x3f52e00,0x435b280,0x487c500,0x4c81f80,0x51a3200,0x55a8c80,0x5992e00,0x5c38c80,0x5c38c80,0x5c38c80,0x5c38c80,0x5c38c80,0x5c38c80,0x5c38c80,0x5c38c80,0x5c38c80,0x5c38c80,0x5c38c80,0x5c38c80,268,1076,2068,2876,3868,4676,5668,6660,7468,8460,9268,10260,11068,12060,12868,13849,14653,15634,16438,17200,17723,17723,17723,17723,21,20,0,0,0,0,0,0,0x307380,0x70f800,0xc37e00,0x1040280,0x1568880,0x1970d00,0x1e99300,0x23c1900,0x27c9d80,0x2cf2380,0x30fa800,0x3622e00,0x3a2b280,0x3f53880,0x435bd00,0x487cf80,0x4c82a00,0x51a3c80,0x55a9700,0x5993880,0x5c39700,0x5c39700,0x5c39700,0x5c39700,0x5c39700,0x5c39700,0x5c39700,0x5c39700,0x5c39700,0x5c39700,0x5c39700,0x5c39700,269,1077,2069,2877,3869,4677,5669,6661,7469,8461,9269,10261,11069,12061,12869,13850,14654,15635,16439,840,840,840,840,840,20,19,0,0,0,0,0,0,0x307e00,0x710280,0xc38880,0x1040d00,0x1569300,0x1971780,0x1e99d80,0x23c2380,0x27ca800,0x2cf2e00,0x30fb280,0x3623880,0x3a2bd00,0x3f54300,0x435c780,0x487da00,0x4c83480,0x51a4700,0x55aa180,0x59d600,0x59d600,0x59d600,0x59d600,0x59d600,0x59d600,0x59d600,0x59d600,0x59d600,0x59d600,0x59d600,0x59d600,0x59d600,270,1078,2070,2878,3870,4678,5670,6662,7470,8462,9270,10262,11070,12062,12870,13851,14655,15636,16440,1096,17724,17724,17724,17724,21,20,0,0,0,0,0,0,0x308880,0x710d00,0xc39300,0x1041780,0x1569d80,0x1972200,0x1e9a800,0x23c2e00,0x27cb280,0x2cf3880,0x30fbd00,0x3624300,0x3a2c780,0x3f54d80,0x435d200,0x487e480,0x4c83f00,0x51a5180,0x55aac00,0x6ecb80,0x5c3a180,0x5c3a180,0x5c3a180,0x5c3a180,0x5c3a180,0x5c3a180,0x5c3a180,0x5c3a180,0x5c3a180,0x5c3a180,0x5c3a180,0x5c3a180,271,1079,2071,2879,3871,4679,5671,6663,7471,8463,9271,10263,11071,12063,12871,13852,14656,15637,16441,17201,17725,17725,17725,17725,21,20,0,0,0,0,0,0,0x309300,0x711780,0xc39d80,0x1042200,0x156a800,0x1972c80,0x1e9b280,0x23c3880,0x27cbd00,0x2cf4300,0x30fc780,0x3624d80,0x3a2d200,0x3f55800,0x435dc80,0x487ef00,0x4c84980,0x51a5c00,0x55ab680,0x5994300,0x5c3ac00,0x5c3ac00,0x5c3ac00,0x5c3ac00,0x5c3ac00,0x5c3ac00,0x5c3ac00,0x5c3ac00,0x5c3ac00,0x5c3ac00,0x5c3ac00,0x5c3ac00,272,1080,2072,2880,3872,4864,5672,6664,7472,8464,9272,10264,11072,12064,13056,18443,14657,18458,16442,1352,1352,1352,1352,1352,20,19,0,0,0,0,0,0,0x309d80,0x712200,0xc3a800,0x1042c80,0x156b280,0x1a93880,0x1e9bd00,0x23c4300,0x27cc780,0x2cf4d80,0x30fd200,0x3625800,0x3a2dc80,0x3f56280,0x447e880,0x67e00,0x4c85400,0x71b80,0x55ac100,0x83c100,0x83c100,0x83c100,0x83c100,0x83c100,0x83c100,0x83c100,0x83c100,0x83c100,0x83c100,0x83c100,0x83c100,0x83c100,273,1081,2073,2881,3873,4865,5673,6665,7473,8465,9273,10265,11073,12065,13057,13853,14658,15638,16443,1608,1608,1608,1608,1608,20,19,0,0,0,0,0,0,0x30a800,0x712c80,0xc3b280,0x1043700,0x156bd00,0x1a94300,0x1e9c780,0x23c4d80,0x27cd200,0x2cf5800,0x30fdc80,0x3626280,0x3a2e700,0x3f56d00,0x447f300,0x487f980,0x4c85e80,0x51a6680,0x55acb80,0x98b680,0x98b680,0x98b680,0x98b680,0x98b680,0x98b680,0x98b680,0x98b680,0x98b680,0x98b680,0x98b680,0x98b680,0x98b680,274,1082,2074,2882,3874,4866,5674,6666,7474,8466,9274,10266,11074,12066,13058,13854,14659,15639,16444,17202,17726,18227,18227,18227,22,21,0,0,0,0,0,0,0x30b280,0x713700,0xc3bd00,0x1044180,0x156c780,0x1a94d80,0x1e9d200,0x23c5800,0x27cdc80,0x2cf6280,0x30fe700,0x3626d00,0x3a2f180,0x3f57780,0x447fd80,0x4880400,0x4c86900,0x51a7100,0x55ad600,0x5994d80,0x5c3b680,0x5ed2e00,0x5ed2e00,0x5ed2e00,0x5ed2e00,0x5ed2e00,0x5ed2e00,0x5ed2e00,0x5ed2e00,0x5ed2e00,0x5ed2e00,0x5ed2e00,275,1083,2075,2883,3875,4867,5675,6667,7475,8467,9275,10267,11075,12067,13059,13855,14660,15640,16445,17203,17727,17727,17727,17727,21,20,0,0,0,0,0,0,0x30bd00,0x714180,0xc3c780,0x1044c00,0x156d200,0x1a95800,0x1e9dc80,0x23c6280,0x27ce700,0x2cf6d00,0x30ff180,0x3627780,0x3a2fc00,0x3f58200,0x4480800,0x4880e80,0x4c87380,0x51a7b80,0x55ae080,0x5995800,0x5c3c100,0x5c3c100,0x5c3c100,0x5c3c100,0x5c3c100,0x5c3c100,0x5c3c100,0x5c3c100,0x5c3c100,0x5c3c100,0x5c3c100,0x5c3c100,276,1084,2076,2884,3876,4868,5676,6668,7476,8468,9276,10268,11076,12068,13060,13856,14661,15641,16446,17204,17728,17728,17728,17728,21,20,0,0,0,0,0,0,0x30c780,0x714c00,0xc3d200,0x1045680,0x156dc80,0x1a96280,0x1e9e700,0x23c6d00,0x27cf180,0x2cf7780,0x30ffc00,0x3628200,0x3a30680,0x3f58c80,0x4481280,0x4881900,0x4c87e00,0x51a8600,0x55aeb00,0x5996280,0x5c3cb80,0x5c3cb80,0x5c3cb80,0x5c3cb80,0x5c3cb80,0x5c3cb80,0x5c3cb80,0x5c3cb80,0x5c3cb80,0x5c3cb80,0x5c3cb80,0x5c3cb80,277,1085,2077,2885,3877,4869,5677,6669,7477,8469,9277,10269,11077,12069,13061,13857,14662,15642,16447,1864,1864,1864,1864,1864,20,19,0,0,0,0,0,0,0x30d200,0x715680,0xc3dc80,0x1046100,0x156e700,0x1a96d00,0x1e9f180,0x23c7780,0x27cfc00,0x2cf8200,0x3100680,0x3628c80,0x3a31100,0x3f59700,0x4481d00,0x4882380,0x4c88880,0x51a9080,0x55af580,0xadac00,0xadac00,0xadac00,0xadac00,0xadac00,0xadac00,0xadac00,0xadac00,0xadac00,0xadac00,0xadac00,0xadac00,0xadac00,278,1086,2078,2886,3878,4870,5678,6670,7478,8470,9278,10270,11078,12070,13062,13858,14663,15643,16448,2120,17729,17729,17729,17729,21,20,0,0,0,0,0,0,0x30dc80,0x716100,0xc3e700,0x1046b80,0x156f180,0x1a97780,0x1e9fc00,0x23c8200,0x27d0680,0x2cf8c80,0x3101100,0x3629700,0x3a31b80,0x3f5a180,0x4482780,0x4882e00,0x4c89300,0x51a9b00,0x55b0000,0xc2a180,0x5c3d600,0x5c3d600,0x5c3d600,0x5c3d600,0x5c3d600,0x5c3d600,0x5c3d600,0x5c3d600,0x5c3d600,0x5c3d600,0x5c3d600,0x5c3d600,279,1087,2079,2887,3879,4871,5679,6671,7479,8471,9279,10271,11079,12071,13063,13859,14848,15644,16449,17205,17730,17730,17730,17730,21,20,0,0,0,0,0,0,0x30e700,0x716b80,0xc3f180,0x1047600,0x156fc00,0x1a98200,0x1ea0680,0x23c8c80,0x27d1100,0x2cf9700,0x3101b80,0x362a180,0x3a32600,0x3f5ac00,0x4483200,0x4883880,0x4da9f00,0x51aa580,0x55b0a80,0x5996d00,0x5c3e080,0x5c3e080,0x5c3e080,0x5c3e080,0x5c3e080,0x5c3e080,0x5c3e080,0x5c3e080,0x5c3e080,0x5c3e080,0x5c3e080,0x5c3e080,280,1088,2080,3072,3880,4872,5680,6672,7480,8472,9280,10272,11264,12072,13064,18444,14849,18459,16450,2376,2376,2376,2376,2376,20,19,0,0,0,0,0,0,0x30f180,0x717600,0xc3fc00,0x1168200,0x1570680,0x1a98c80,0x1ea1100,0x23c9700,0x27d1b80,0x2cfa180,0x3102600,0x362ac00,0x3b53200,0x3f5b680,0x4483c80,0x68880,0x4daa980,0x72600,0x55b1500,0xd79700,0xd79700,0xd79700,0xd79700,0xd79700,0xd79700,0xd79700,0xd79700,0xd79700,0xd79700,0xd79700,0xd79700,0xd79700,281,1089,2081,3073,3881,4873,5681,6673,7481,8473,9281,10273,11265,12073,13065,13860,14850,15645,16451,2632,2632,2632,2632,2632,20,19,0,0,0,0,0,0,0x30fc00,0x718080,0xc40680,0x1168c80,0x1571100,0x1a99700,0x1ea1b80,0x23ca180,0x27d2600,0x2cfac00,0x3103080,0x362b680,0x3b53c80,0x3f5c100,0x4484700,0x4884300,0x4dab400,0x51ab000,0x55b1f80,0xec8c80,0xec8c80,0xec8c80,0xec8c80,0xec8c80,0xec8c80,0xec8c80,0xec8c80,0xec8c80,0xec8c80,0xec8c80,0xec8c80,0xec8c80,282,1090,2082,3074,3882,4874,5682,6674,7482,8474,9282,10274,11266,12074,13066,13861,14851,15646,16452,17206,17731,18228,18228,18228,22,21,0,0,0,0,0,0,0x310680,0x718b00,0xc41100,0x1169700,0x1571b80,0x1a9a180,0x1ea2600,0x23cac00,0x27d3080,0x2cfb680,0x3103b00,0x362c100,0x3b54700,0x3f5cb80,0x4485180,0x4884d80,0x4dabe80,0x51aba80,0x55b2a00,0x5997780,0x5c3eb00,0x5ed3880,0x5ed3880,0x5ed3880,0x5ed3880,0x5ed3880,0x5ed3880,0x5ed3880,0x5ed3880,0x5ed3880,0x5ed3880,0x5ed3880,283,1091,2083,3075,3883,4875,5683,6675,7483,8475,9283,10275,11267,12075,13067,13862,14852,15647,16453,17207,17732,17732,17732,17732,21,20,0,0,0,0,0,0,0x311100,0x719580,0xc41b80,0x116a180,0x1572600,0x1a9ac00,0x1ea3080,0x23cb680,0x27d3b00,0x2cfc100,0x3104580,0x362cb80,0x3b55180,0x3f5d600,0x4485c00,0x4885800,0x4dac900,0x51ac500,0x55b3480,0x5998200,0x5c3f580,0x5c3f580,0x5c3f580,0x5c3f580,0x5c3f580,0x5c3f580,0x5c3f580,0x5c3f580,0x5c3f580,0x5c3f580,0x5c3f580,0x5c3f580,284,1092,2084,3076,3884,4876,5684,6676,7484,8476,9284,10276,11268,12076,13068,13863,14853,15648,16454,17208,17733,17733,17733,17733,21,20,0,0,0,0,0,0,0x311b80,0x71a000,0xc42600,0x116ac00,0x1573080,0x1a9b680,0x1ea3b00,0x23cc100,0x27d4580,0x2cfcb80,0x3105000,0x362d600,0x3b55c00,0x3f5e080,0x4486680,0x4886280,0x4dad380,0x51acf80,0x55b3f00,0x5998c80,0x5c40000,0x5c40000,0x5c40000,0x5c40000,0x5c40000,0x5c40000,0x5c40000,0x5c40000,0x5c40000,0x5c40000,0x5c40000,0x5c40000,285,1093,2085,3077,3885,4877,5685,6677,7485,8477,9285,10277,11269,12077,13069,13864,14854,15649,16455,2888,2888,2888,2888,2888,20,19,0,0,0,0,0,0,0x312600,0x71aa80,0xc43080,0x116b680,0x1573b00,0x1a9c100,0x1ea4580,0x23ccb80,0x27d5000,0x2cfd600,0x3105a80,0x362e080,0x3b56680,0x3f5eb00,0x4487100,0x4886d00,0x4dade00,0x51ada00,0x55b4980,0x1018200,0x1018200,0x1018200,0x1018200,0x1018200,0x1018200,0x1018200,0x1018200,0x1018200,0x1018200,0x1018200,0x1018200,0x1018200,286,1094,2086,3078,3886,4878,5686,6678,7486,8478,9286,10278,11270,12078,13070,13865,14855,15650,16640,3144,17734,17734,17734,17734,21,20,0,0,0,0,0,0,0x313080,0x71b500,0xc43b00,0x116c100,0x1574580,0x1a9cb80,0x1ea5000,0x23cd600,0x27d5a80,0x2cfe080,0x3106500,0x362eb00,0x3b57100,0x3f5f580,0x4487b80,0x4887780,0x4dae880,0x51ae480,0x56d5580,0x1167780,0x5c40a80,0x5c40a80,0x5c40a80,0x5c40a80,0x5c40a80,0x5c40a80,0x5c40a80,0x5c40a80,0x5c40a80,0x5c40a80,0x5c40a80,0x5c40a80,287,1095,2087,3079,3887,4879,5687,6679,7487,8479,9287,10279,11271,12079,13071,13866,14856,15651,16641,17209,17735,17735,17735,17735,21,20,0,0,0,0,0,0,0x313b00,0x71bf80,0xc44580,0x116cb80,0x1575000,0x1a9d600,0x1ea5a80,0x23ce080,0x27d6500,0x2cfeb00,0x3106f80,0x362f580,0x3b57b80,0x3f60000,0x4488600,0x4888200,0x4daf300,0x51aef00,0x56d6000,0x5999700,0x5c41500,0x5c41500,0x5c41500,0x5c41500,0x5c41500,0x5c41500,0x5c41500,0x5c41500,0x5c41500,0x5c41500,0x5c41500,0x5c41500,288,1280,2088,3080,3888,4880,5688,6680,7488,8480,9472,10280,11272,12080,13072,18445,14857,18460,16642,3400,3400,3400,3400,3400,20,19,0,0,0,0,0,0,0x314580,0x83cb80,0xc45000,0x116d600,0x1575a80,0x1a9e080,0x1ea6500,0x23ceb00,0x27d6f80,0x2cff580,0x3227b80,0x3630000,0x3b58600,0x3f60a80,0x4489080,0x69300,0x4dafd80,0x73080,0x56d6a80,0x12b6d00,0x12b6d00,0x12b6d00,0x12b6d00,0x12b6d00,0x12b6d00,0x12b6d00,0x12b6d00,0x12b6d00,0x12b6d00,0x12b6d00,0x12b6d00,0x12b6d00,289,1281,2089,3081,3889,4881,5689,6681,7489,8481,9473,10281,11273,12081,13073,13867,14858,15652,16643,3656,3656,3656,3656,3656,20,19,0,0,0,0,0,0,0x315000,0x83d600,0xc45a80,0x116e080,0x1576500,0x1a9eb00,0x1ea6f80,0x23cf580,0x27d7a00,0x2d00000,0x3228600,0x3630a80,0x3b59080,0x3f61500,0x4489b00,0x4888c80,0x4db0800,0x51af980,0x56d7500,0x1406280,0x1406280,0x1406280,0x1406280,0x1406280,0x1406280,0x1406280,0x1406280,0x1406280,0x1406280,0x1406280,0x1406280,0x1406280,290,1282,2090,3082,3890,4882,5690,6682,7490,8482,9474,10282,11274,12082,13074,13868,14859,15653,16644,17210,17920,18229,18229,18229,22,21,0,0,0,0,0,0,0x315a80,0x83e080,0xc46500,0x116eb00,0x1576f80,0x1a9f580,0x1ea7a00,0x23d0000,0x27d8480,0x2d00a80,0x3229080,0x3631500,0x3b59b00,0x3f61f80,0x448a580,0x4889700,0x4db1280,0x51b0400,0x56d7f80,0x599a180,0x5d62100,0x5ed4300,0x5ed4300,0x5ed4300,0x5ed4300,0x5ed4300,0x5ed4300,0x5ed4300,0x5ed4300,0x5ed4300,0x5ed4300,0x5ed4300,291,1283,2091,3083,3891,4883,5691,6683,7491,8483,9475,10283,11275,12083,13075,13869,14860,15654,16645,17211,17921,17921,17921,17921,21,20,0,0,0,0,0,0,0x316500,0x83eb00,0xc46f80,0x116f580,0x1577a00,0x1aa0000,0x1ea8480,0x23d0a80,0x27d8f00,0x2d01500,0x3229b00,0x3631f80,0x3b5a580,0x3f62a00,0x448b000,0x488a180,0x4db1d00,0x51b0e80,0x56d8a00,0x599ac00,0x5d62b80,0x5d62b80,0x5d62b80,0x5d62b80,0x5d62b80,0x5d62b80,0x5d62b80,0x5d62b80,0x5d62b80,0x5d62b80,0x5d62b80,0x5d62b80,292,1284,2092,3084,3892,4884,5692,6684,7492,8484,9476,10284,11276,12084,13076,13870,14861,15655,16646,17212,17922,17922,17922,17922,21,20,0,0,0,0,0,0,0x316f80,0x83f580,0xc47a00,0x1170000,0x1578480,0x1aa0a80,0x1ea8f00,0x23d1500,0x27d9980,0x2d01f80,0x322a580,0x3632a00,0x3b5b000,0x3f63480,0x448ba80,0x488ac00,0x4db2780,0x51b1900,0x56d9480,0x599b680,0x5d63600,0x5d63600,0x5d63600,0x5d63600,0x5d63600,0x5d63600,0x5d63600,0x5d63600,0x5d63600,0x5d63600,0x5d63600,0x5d63600,293,1285,2093,3085,3893,4885,5693,6685,7493,8485,9477,10285,11277,12085,13077,13871,14862,15656,16647,3912,3912,3912,3912,3912,20,19,0,0,0,0,0,0,0x317a00,0x840000,0xc48480,0x1170a80,0x1578f00,0x1aa1500,0x1ea9980,0x23d1f80,0x27da400,0x2d02a00,0x322b000,0x3633480,0x3b5ba80,0x3f63f00,0x448c500,0x488b680,0x4db3200,0x51b2380,0x56d9f00,0x1555800,0x1555800,0x1555800,0x1555800,0x1555800,0x1555800,0x1555800,0x1555800,0x1555800,0x1555800,0x1555800,0x1555800,0x1555800,294,1286,2094,3086,3894,4886,5694,6686,7494,8486,9478,10286,11278,12086,13078,13872,14863,15657,16648,4168,17923,17923,17923,17923,21,20,0,0,0,0,0,0,0x318480,0x840a80,0xc48f00,0x1171500,0x1579980,0x1aa1f80,0x1eaa400,0x23d2a00,0x27dae80,0x2d03480,0x322ba80,0x3633f00,0x3b5c500,0x3f64980,0x448cf80,0x488c100,0x4db3c80,0x51b2e00,0x56da980,0x16a4d80,0x5d64080,0x5d64080,0x5d64080,0x5d64080,0x5d64080,0x5d64080,0x5d64080,0x5d64080,0x5d64080,0x5d64080,0x5d64080,0x5d64080,295,1287,2095,3087,3895,4887,5695,6687,7495,8487,9479,10287,11279,12087,13079,13873,14864,15658,16649,17213,17924,17924,17924,17924,21,20,0,0,0,0,0,0,0x318f00,0x841500,0xc49980,0x1171f80,0x157a400,0x1aa2a00,0x1eaae80,0x23d3480,0x27db900,0x2d03f00,0x322c500,0x3634980,0x3b5cf80,0x3f65400,0x448da00,0x488cb80,0x4db4700,0x51b3880,0x56db400,0x599c100,0x5d64b00,0x5d64b00,0x5d64b00,0x5d64b00,0x5d64b00,0x5d64b00,0x5d64b00,0x5d64b00,0x5d64b00,0x5d64b00,0x5d64b00,0x5d64b00,296,1288,2096,3088,3896,4888,5696,6688,7680,8488,9480,10288,11280,12088,13080,18446,14865,18461,16650,4424,4424,4424,4424,4424,20,19,0,0,0,0,0,0,0x319980,0x841f80,0xc4a400,0x1172a00,0x157ae80,0x1aa3480,0x1eab900,0x23d3f00,0x28fc500,0x2d04980,0x322cf80,0x3635400,0x3b5da00,0x3f65e80,0x448e480,0x69d80,0x4db5180,0x73b00,0x56dbe80,0x17f4300,0x17f4300,0x17f4300,0x17f4300,0x17f4300,0x17f4300,0x17f4300,0x17f4300,0x17f4300,0x17f4300,0x17f4300,0x17f4300,0x17f4300,297,1289,2097,3089,3897,4889,5697,6689,7681,8489,9481,10289,11281,12089,13081,13874,14866,15659,16651,4680,4680,4680,4680,4680,20,19,0,0,0,0,0,0,0x31a400,0x842a00,0xc4ae80,0x1173480,0x157b900,0x1aa3f00,0x1eac380,0x23d4980,0x28fcf80,0x2d05400,0x322da00,0x3635e80,0x3b5e480,0x3f66900,0x448ef00,0x488d600,0x4db5c00,0x51b4300,0x56dc900,0x1943880,0x1943880,0x1943880,0x1943880,0x1943880,0x1943880,0x1943880,0x1943880,0x1943880,0x1943880,0x1943880,0x1943880,0x1943880,298,1290,2098,3090,3898,4890,5698,6690,7682,8490,9482,10290,11282,12090,13082,13875,14867,15660,16652,17214,17925,18230,18230,18230,22,21,0,0,0,0,0,0,0x31ae80,0x843480,0xc4b900,0x1173f00,0x157c380,0x1aa4980,0x1eace00,0x23d5400,0x28fda00,0x2d05e80,0x322e480,0x3636900,0x3b5ef00,0x3f67380,0x448f980,0x488e080,0x4db6680,0x51b4d80,0x56dd380,0x599cb80,0x5d65580,0x5ed4d80,0x5ed4d80,0x5ed4d80,0x5ed4d80,0x5ed4d80,0x5ed4d80,0x5ed4d80,0x5ed4d80,0x5ed4d80,0x5ed4d80,0x5ed4d80,299,1291,2099,3091,3899,4891,5699,6691,7683,8491,9483,10291,11283,12091,13083,13876,14868,15661,16653,17215,17926,17926,17926,17926,21,20,0,0,0,0,0,0,0x31b900,0x843f00,0xc4c380,0x1174980,0x157ce00,0x1aa5400,0x1ead880,0x23d5e80,0x28fe480,0x2d06900,0x322ef00,0x3637380,0x3b5f980,0x3f67e00,0x4490400,0x488eb00,0x4db7100,0x51b5800,0x56dde00,0x599d600,0x5d66000,0x5d66000,0x5d66000,0x5d66000,0x5d66000,0x5d66000,0x5d66000,0x5d66000,0x5d66000,0x5d66000,0x5d66000,0x5d66000,300,1292,2100,3092,3900,4892,5700,6692,7684,8492,9484,10292,11284,12092,13084,13877,14869,15662,16654,17216,17927,17927,17927,17927,21,20,0,0,0,0,0,0,0x31c380,0x844980,0xc4ce00,0x1175400,0x157d880,0x1aa5e80,0x1eae300,0x23d6900,0x28fef00,0x2d07380,0x322f980,0x3637e00,0x3b60400,0x3f68880,0x4490e80,0x488f580,0x4db7b80,0x51b6280,0x56de880,0x599e080,0x5d66a80,0x5d66a80,0x5d66a80,0x5d66a80,0x5d66a80,0x5d66a80,0x5d66a80,0x5d66a80,0x5d66a80,0x5d66a80,0x5d66a80,0x5d66a80,301,1293,2101,3093,3901,4893,5701,6693,7685,8493,9485,10293,11285,12093,13085,13878,14870,15663,16655,4936,4936,4936,4936,4936,20,19,0,0,0,0,0,0,0x31ce00,0x845400,0xc4d880,0x1175e80,0x157e300,0x1aa6900,0x1eaed80,0x23d7380,0x28ff980,0x2d07e00,0x3230400,0x3638880,0x3b60e80,0x3f69300,0x4491900,0x4890000,0x4db8600,0x51b6d00,0x56df300,0x1a92e00,0x1a92e00,0x1a92e00,0x1a92e00,0x1a92e00,0x1a92e00,0x1a92e00,0x1a92e00,0x1a92e00,0x1a92e00,0x1a92e00,0x1a92e00,0x1a92e00,302,1294,2102,3094,3902,4894,5702,6694,7686,8494,9486,10294,11286,12094,13086,13879,14871,15664,16656,5192,17928,17928,17928,17928,21,20,0,0,0,0,0,0,0x31d880,0x845e80,0xc4e300,0x1176900,0x157ed80,0x1aa7380,0x1eaf800,0x23d7e00,0x2900400,0x2d08880,0x3230e80,0x3639300,0x3b61900,0x3f69d80,0x4492380,0x4890a80,0x4db9080,0x51b7780,0x56dfd80,0x1be2380,0x5d67500,0x5d67500,0x5d67500,0x5d67500,0x5d67500,0x5d67500,0x5d67500,0x5d67500,0x5d67500,0x5d67500,0x5d67500,0x5d67500,303,1295,2103,3095,3903,4895,5703,6695,7687,8495,9487,10295,11287,12095,13087,13880,14872,15665,16657,17217,17929,17929,17929,17929,21,20,0,0,0,0,0,0,0x31e300,0x846900,0xc4ed80,0x1177380,0x157f800,0x1aa7e00,0x1eb0280,0x23d8880,0x2900e80,0x2d09300,0x3231900,0x3639d80,0x3b62380,0x3f6a800,0x4492e00,0x4891500,0x4db9b00,0x51b8200,0x56e0800,0x599eb00,0x5d67f80,0x5d67f80,0x5d67f80,0x5d67f80,0x5d67f80,0x5d67f80,0x5d67f80,0x5d67f80,0x5d67f80,0x5d67f80,0x5d67f80,0x5d67f80,304,1296,2104,3096,3904,4896,5888,6696,7688,8496,9488,10296,11288,12096,13088,13881,14873,15666,16658,17218,17930,17930,17930,17930,21,20,0,0,0,0,0,0,0x31ed80,0x847380,0xc4f800,0x1177e00,0x1580280,0x1aa8880,0x1fd0e80,0x23d9300,0x2901900,0x2d09d80,0x3232380,0x363a800,0x3b62e00,0x3f6b280,0x4493880,0x4891f80,0x4dba580,0x51b8c80,0x56e1280,0x599f580,0x5d68a00,0x5d68a00,0x5d68a00,0x5d68a00,0x5d68a00,0x5d68a00,0x5d68a00,0x5d68a00,0x5d68a00,0x5d68a00,0x5d68a00,0x5d68a00,305,1297,2105,3097,3905,4897,5889,6697,7689,8497,9489,10297,11289,12097,13089,13882,14874,15667,16659,5448,5448,5448,5448,5448,20,19,0,0,0,0,0,0,0x31f800,0x847e00,0xc50280,0x1178880,0x1580d00,0x1aa9300,0x1fd1900,0x23d9d80,0x2902380,0x2d0a800,0x3232e00,0x363b280,0x3b63880,0x3f6bd00,0x4494300,0x4892a00,0x4dbb000,0x51b9700,0x56e1d00,0x1d31900,0x1d31900,0x1d31900,0x1d31900,0x1d31900,0x1d31900,0x1d31900,0x1d31900,0x1d31900,0x1d31900,0x1d31900,0x1d31900,0x1d31900,306,1298,2106,3098,3906,4898,5890,6698,7690,8498,9490,10298,11290,12098,13090,13883,14875,15668,16660,17219,17931,18231,18231,18231,22,21,0,0,0,0,0,0,0x320280,0x848880,0xc50d00,0x1179300,0x1581780,0x1aa9d80,0x1fd2380,0x23da800,0x2902e00,0x2d0b280,0x3233880,0x363bd00,0x3b64300,0x3f6c780,0x4494d80,0x4893480,0x4dbba80,0x51ba180,0x56e2780,0x59a0000,0x5d69480,0x5ed5800,0x5ed5800,0x5ed5800,0x5ed5800,0x5ed5800,0x5ed5800,0x5ed5800,0x5ed5800,0x5ed5800,0x5ed5800,0x5ed5800,307,1299,2107,3099,3907,4899,5891,6699,7691,8499,9491,10299,11291,12099,13091,13884,14876,15669,16661,17220,17932,17932,17932,17932,21,20,0,0,0,0,0,0,0x320d00,0x849300,0xc51780,0x1179d80,0x1582200,0x1aaa800,0x1fd2e00,0x23db280,0x2903880,0x2d0bd00,0x3234300,0x363c780,0x3b64d80,0x3f6d200,0x4495800,0x4893f00,0x4dbc500,0x51bac00,0x56e3200,0x59a0a80,0x5d69f00,0x5d69f00,0x5d69f00,0x5d69f00,0x5d69f00,0x5d69f00,0x5d69f00,0x5d69f00,0x5d69f00,0x5d69f00,0x5d69f00,0x5d69f00,308,1300,2108,3100,3908,4900,5892,6700,7692,8500,9492,10300,11292,12100,13092,13885,14877,15670,16662,17221,17933,17933,17933,17933,21,20,0,0,0,0,0,0,0x321780,0x849d80,0xc52200,0x117a800,0x1582c80,0x1aab280,0x1fd3880,0x23dbd00,0x2904300,0x2d0c780,0x3234d80,0x363d200,0x3b65800,0x3f6dc80,0x4496280,0x4894980,0x4dbcf80,0x51bb680,0x56e3c80,0x59a1500,0x5d6a980,0x5d6a980,0x5d6a980,0x5d6a980,0x5d6a980,0x5d6a980,0x5d6a980,0x5d6a980,0x5d6a980,0x5d6a980,0x5d6a980,0x5d6a980,309,1301,2109,3101,3909,4901,5893,6701,7693,8501,9493,10301,11293,12101,13093,13886,14878,15671,16663,5704,5704,5704,5704,5704,20,19,0,0,0,0,0,0,0x322200,0x84a800,0xc52c80,0x117b280,0x1583700,0x1aabd00,0x1fd4300,0x23dc780,0x2904d80,0x2d0d200,0x3235800,0x363dc80,0x3b66280,0x3f6e700,0x4496d00,0x4895400,0x4dbda00,0x51bc100,0x56e4700,0x1e80e80,0x1e80e80,0x1e80e80,0x1e80e80,0x1e80e80,0x1e80e80,0x1e80e80,0x1e80e80,0x1e80e80,0x1e80e80,0x1e80e80,0x1e80e80,0x1e80e80,310,1302,2110,3102,3910,4902,5894,6702,7694,8502,9494,10302,11294,12102,13094,13887,14879,15672,16664,5960,17934,17934,17934,17934,21,20,0,0,0,0,0,0,0x322c80,0x84b280,0xc53700,0x117bd00,0x1584180,0x1aac780,0x1fd4d80,0x23dd200,0x2905800,0x2d0dc80,0x3236280,0x363e700,0x3b66d00,0x3f6f180,0x4497780,0x4895e80,0x4dbe480,0x51bcb80,0x56e5180,0x1fd0400,0x5d6b400,0x5d6b400,0x5d6b400,0x5d6b400,0x5d6b400,0x5d6b400,0x5d6b400,0x5d6b400,0x5d6b400,0x5d6b400,0x5d6b400,0x5d6b400,311,1303,2111,3103,3911,4903,5895,6703,7695,8503,9495,10303,11295,12103,13095,13888,14880,15673,16665,17222,17935,17935,17935,17935,21,20,0,0,0,0,0,0,0x323700,0x84bd00,0xc54180,0x117c780,0x1584c00,0x1aad200,0x1fd5800,0x23ddc80,0x2906280,0x2d0e700,0x3236d00,0x363f180,0x3b67780,0x3f6fc00,0x4498200,0x4896900,0x4dbef00,0x51bd600,0x56e5c00,0x59a1f80,0x5d6be80,0x5d6be80,0x5d6be80,0x5d6be80,0x5d6be80,0x5d6be80,0x5d6be80,0x5d6be80,0x5d6be80,0x5d6be80,0x5d6be80,0x5d6be80,312,1304,2112,3104,4096,4904,5896,6704,7696,8504,9496,10304,11296,12288,13096,13889,14881,15674,16666,17223,17936,17936,17936,17936,21,20,0,0,0,0,0,0,0x324180,0x84c780,0xc54c00,0x117d200,0x16a5800,0x1aadc80,0x1fd6280,0x23de700,0x2906d00,0x2d0f180,0x3237780,0x363fc00,0x3b68200,0x4090800,0x4498c80,0x4897380,0x4dbf980,0x51be080,0x56e6680,0x59a2a00,0x5d6c900,0x5d6c900,0x5d6c900,0x5d6c900,0x5d6c900,0x5d6c900,0x5d6c900,0x5d6c900,0x5d6c900,0x5d6c900,0x5d6c900,0x5d6c900,313,1305,2113,3105,4097,4905,5897,6705,7697,8505,9497,10305,11297,12289,13097,13890,14882,15675,16667,6216,6216,6216,6216,6216,20,19,0,0,0,0,0,0,0x324c00,0x84d200,0xc55680,0x117dc80,0x16a6280,0x1aae700,0x1fd6d00,0x23df180,0x2907780,0x2d0fc00,0x3238200,0x3640680,0x3b68c80,0x4091280,0x4499700,0x4897e00,0x4dc0400,0x51beb00,0x56e7100,0x211f980,0x211f980,0x211f980,0x211f980,0x211f980,0x211f980,0x211f980,0x211f980,0x211f980,0x211f980,0x211f980,0x211f980,0x211f980,314,1306,2114,3106,4098,4906,5898,6706,7698,8506,9498,10306,11298,12290,13098,13891,14883,15676,16668,17408,17937,18232,18232,18232,22,21,0,0,0,0,0,0,0x325680,0x84dc80,0xc56100,0x117e700,0x16a6d00,0x1aaf180,0x1fd7780,0x23dfc00,0x2908200,0x2d10680,0x3238c80,0x3641100,0x3b69700,0x4091d00,0x449a180,0x4898880,0x4dc0e80,0x51bf580,0x56e7b80,0x5ac3600,0x5d6d380,0x5ed6280,0x5ed6280,0x5ed6280,0x5ed6280,0x5ed6280,0x5ed6280,0x5ed6280,0x5ed6280,0x5ed6280,0x5ed6280,0x5ed6280,315,1307,2115,3107,4099,4907,5899,6707,7699,8507,9499,10307,11299,12291,13099,13892,14884,15677,16669,17409,17938,17938,17938,17938,21,20,0,0,0,0,0,0,0x326100,0x84e700,0xc56b80,0x117f180,0x16a7780,0x1aafc00,0x1fd8200,0x23e0680,0x2908c80,0x2d11100,0x3239700,0x3641b80,0x3b6a180,0x4092780,0x449ac00,0x4899300,0x4dc1900,0x51c0000,0x56e8600,0x5ac4080,0x5d6de00,0x5d6de00,0x5d6de00,0x5d6de00,0x5d6de00,0x5d6de00,0x5d6de00,0x5d6de00,0x5d6de00,0x5d6de00,0x5d6de00,0x5d6de00,316,1308,2116,3108,4100,4908,5900,6708,7700,8508,9500,10308,11300,12292,13100,13893,14885,15678,16670,17410,17939,17939,17939,17939,21,20,0,0,0,0,0,0,0x326b80,0x84f180,0xc57600,0x117fc00,0x16a8200,0x1ab0680,0x1fd8c80,0x23e1100,0x2909700,0x2d11b80,0x323a180,0x3642600,0x3b6ac00,0x4093200,0x449b680,0x4899d80,0x4dc2380,0x51c0a80,0x56e9080,0x5ac4b00,0x5d6e880,0x5d6e880,0x5d6e880,0x5d6e880,0x5d6e880,0x5d6e880,0x5d6e880,0x5d6e880,0x5d6e880,0x5d6e880,0x5d6e880,0x5d6e880,317,1309,2117,3109,4101,4909,5901,6709,7701,8509,9501,10309,11301,12293,13101,13894,14886,15679,16671,6472,6472,6472,6472,6472,20,19,0,0,0,0,0,0,0x327600,0x84fc00,0xc58080,0x1180680,0x16a8c80,0x1ab1100,0x1fd9700,0x23e1b80,0x290a180,0x2d12600,0x323ac00,0x3643080,0x3b6b680,0x4093c80,0x449c100,0x489a800,0x4dc2e00,0x51c1500,0x56e9b00,0x226ef00,0x226ef00,0x226ef00,0x226ef00,0x226ef00,0x226ef00,0x226ef00,0x226ef00,0x226ef00,0x226ef00,0x226ef00,0x226ef00,0x226ef00,318,1310,2118,3110,4102,4910,5902,6710,7702,8510,9502,10310,11302,12294,13102,13895,14887,15680,16672,6728,17940,17940,17940,17940,21,20,0,0,0,0,0,0,0x328080,0x850680,0xc58b00,0x1181100,0x16a9700,0x1ab1b80,0x1fda180,0x23e2600,0x290ac00,0x2d13080,0x323b680,0x3643b00,0x3b6c100,0x4094700,0x449cb80,0x489b280,0x4dc3880,0x51c1f80,0x56ea580,0x23be480,0x5d6f300,0x5d6f300,0x5d6f300,0x5d6f300,0x5d6f300,0x5d6f300,0x5d6f300,0x5d6f300,0x5d6f300,0x5d6f300,0x5d6f300,0x5d6f300,319,1311,2119,3111,4103,4911,5903,6711,7703,8511,9503,10311,11303,12295,13103,14080,14888,15681,16673,17411,17941,17941,17941,17941,21,20,0,0,0,0,0,0,0x328b00,0x851100,0xc59580,0x1181b80,0x16aa180,0x1ab2600,0x1fdac00,0x23e3080,0x290b680,0x2d13b00,0x323c100,0x3644580,0x3b6cb80,0x4095180,0x449d600,0x49bbe80,0x4dc4300,0x51c2a00,0x56eb000,0x5ac5580,0x5d6fd80,0x5d6fd80,0x5d6fd80,0x5d6fd80,0x5d6fd80,0x5d6fd80,0x5d6fd80,0x5d6fd80,0x5d6fd80,0x5d6fd80,0x5d6fd80,0x5d6fd80,320,1312,2304,3112,4104,4912,5904,6712,7704,8512,9504,10496,11304,12296,13104,14081,14889,15682,16674,17412,17942,17942,17942,17942,21,20,0,0,0,0,0,0,0x329580,0x851b80,0xd7a180,0x1182600,0x16aac00,0x1ab3080,0x1fdb680,0x23e3b00,0x290c100,0x2d14580,0x323cb80,0x3765180,0x3b6d600,0x4095c00,0x449e080,0x49bc900,0x4dc4d80,0x51c3480,0x56eba80,0x5ac6000,0x5d70800,0x5d70800,0x5d70800,0x5d70800,0x5d70800,0x5d70800,0x5d70800,0x5d70800,0x5d70800,0x5d70800,0x5d70800,0x5d70800,321,1313,2305,3113,4105,4913,5905,6713,7705,8513,9505,10497,11305,12297,13105,14082,14890,15683,16675,6984,6984,6984,6984,6984,20,19,0,0,0,0,0,0,0x32a000,0x852600,0xd7ac00,0x1183080,0x16ab680,0x1ab3b00,0x1fdc100,0x23e4580,0x290cb80,0x2d15000,0x323d600,0x3765c00,0x3b6e080,0x4096680,0x449eb00,0x49bd380,0x4dc5800,0x51c3f00,0x56ec500,0x250da00,0x250da00,0x250da00,0x250da00,0x250da00,0x250da00,0x250da00,0x250da00,0x250da00,0x250da00,0x250da00,0x250da00,0x250da00,322,1314,2306,3114,4106,4914,5906,6714,7706,8514,9506,10498,11306,12298,13106,14083,14891,15684,16676,17413,17943,18233,18233,18233,22,21,0,0,0,0,0,0,0x32aa80,0x853080,0xd7b680,0x1183b00,0x16ac100,0x1ab4580,0x1fdcb80,0x23e5000,0x290d600,0x2d15a80,0x323e080,0x3766680,0x3b6eb00,0x4097100,0x449f580,0x49bde00,0x4dc6280,0x51c4980,0x56ecf80,0x5ac6a80,0x5d71280,0x5ed6d00,0x5ed6d00,0x5ed6d00,0x5ed6d00,0x5ed6d00,0x5ed6d00,0x5ed6d00,0x5ed6d00,0x5ed6d00,0x5ed6d00,0x5ed6d00,323,1315,2307,3115,4107,4915,5907,6715,7707,8515,9507,10499,11307,12299,13107,14084,14892,15685,16677,17414,17944,17944,17944,17944,21,20,0,0,0,0,0,0,0x32b500,0x853b00,0xd7c100,0x1184580,0x16acb80,0x1ab5000,0x1fdd600,0x23e5a80,0x290e080,0x2d16500,0x323eb00,0x3767100,0x3b6f580,0x4097b80,0x44a0000,0x49be880,0x4dc6d00,0x51c5400,0x56eda00,0x5ac7500,0x5d71d00,0x5d71d00,0x5d71d00,0x5d71d00,0x5d71d00,0x5d71d00,0x5d71d00,0x5d71d00,0x5d71d00,0x5d71d00,0x5d71d00,0x5d71d00,324,1316,2308,3116,4108,4916,5908,6716,7708,8516,9508,10500,11308,12300,13108,14085,14893,15686,16678,17415,17945,17945,17945,17945,21,20,0,0,0,0,0,0,0x32bf80,0x854580,0xd7cb80,0x1185000,0x16ad600,0x1ab5a80,0x1fde080,0x23e6500,0x290eb00,0x2d16f80,0x323f580,0x3767b80,0x3b70000,0x4098600,0x44a0a80,0x49bf300,0x4dc7780,0x51c5e80,0x56ee480,0x5ac7f80,0x5d72780,0x5d72780,0x5d72780,0x5d72780,0x5d72780,0x5d72780,0x5d72780,0x5d72780,0x5d72780,0x5d72780,0x5d72780,0x5d72780,325,1317,2309,3117,4109,4917,5909,6717,7709,8517,9509,10501,11309,12301,13109,14086,14894,15687,16679,7240,7240,7240,7240,7240,20,19,0,0,0,0,0,0,0x32ca00,0x855000,0xd7d600,0x1185a80,0x16ae080,0x1ab6500,0x1fdeb00,0x23e6f80,0x290f580,0x2d17a00,0x3240000,0x3768600,0x3b70a80,0x4099080,0x44a1500,0x49bfd80,0x4dc8200,0x51c6900,0x56eef00,0x265cf80,0x265cf80,0x265cf80,0x265cf80,0x265cf80,0x265cf80,0x265cf80,0x265cf80,0x265cf80,0x265cf80,0x265cf80,0x265cf80,0x265cf80,326,1318,2310,3118,4110,4918,5910,6718,7710,8518,9510,10502,11310,12302,13110,14087,14895,15872,16680,7496,17946,17946,17946,17946,21,20,0,0,0,0,0,0,0x32d480,0x855a80,0xd7e080,0x1186500,0x16aeb00,0x1ab6f80,0x1fdf580,0x23e7a00,0x2910000,0x2d18480,0x3240a80,0x3769080,0x3b71500,0x4099b00,0x44a1f80,0x49c0800,0x4dc8c80,0x52e7500,0x56ef980,0x27ac500,0x5d73200,0x5d73200,0x5d73200,0x5d73200,0x5d73200,0x5d73200,0x5d73200,0x5d73200,0x5d73200,0x5d73200,0x5d73200,0x5d73200,327,1319,2311,3119,4111,4919,5911,6719,7711,8519,9511,10503,11311,12303,13111,14088,14896,15873,16681,17416,17947,17947,17947,17947,21,20,0,0,0,0,0,0,0x32df00,0x856500,0xd7eb00,0x1186f80,0x16af580,0x1ab7a00,0x1fe0000,0x23e8480,0x2910a80,0x2d18f00,0x3241500,0x3769b00,0x3b71f80,0x409a580,0x44a2a00,0x49c1280,0x4dc9700,0x52e7f80,0x56f0400,0x5ac8a00,0x5d73c80,0x5d73c80,0x5d73c80,0x5d73c80,0x5d73c80,0x5d73c80,0x5d73c80,0x5d73c80,0x5d73c80,0x5d73c80,0x5d73c80,0x5d73c80,512,1320,2312,3120,4112,4920,5912,6720,7712,8704,9512,10504,11312,12304,13112,14089,14897,15874,16682,17417,17948,17948,17948,17948,21,20,0,0,0,0,0,0,0x44eb00,0x856f80,0xd7f580,0x1187a00,0x16b0000,0x1ab8480,0x1fe0a80,0x23e8f00,0x2911500,0x2e39b00,0x3241f80,0x376a580,0x3b72a00,0x409b000,0x44a3480,0x49c1d00,0x4dca180,0x52e8a00,0x56f0e80,0x5ac9480,0x5d74700,0x5d74700,0x5d74700,0x5d74700,0x5d74700,0x5d74700,0x5d74700,0x5d74700,0x5d74700,0x5d74700,0x5d74700,0x5d74700,513,1321,2313,3121,4113,4921,5913,6721,7713,8705,9513,10505,11313,12305,13113,14090,14898,15875,16683,7752,7752,7752,7752,7752,20,19,0,0,0,0,0,0,0x44f580,0x857a00,0xd80000,0x1188480,0x16b0a80,0x1ab8f00,0x1fe1500,0x23e9980,0x2911f80,0x2e3a580,0x3242a00,0x376b000,0x3b73480,0x409ba80,0x44a3f00,0x49c2780,0x4dcac00,0x52e9480,0x56f1900,0x28fba80,0x28fba80,0x28fba80,0x28fba80,0x28fba80,0x28fba80,0x28fba80,0x28fba80,0x28fba80,0x28fba80,0x28fba80,0x28fba80,0x28fba80,514,1322,2314,3122,4114,4922,5914,6722,7714,8706,9514,10506,11314,12306,13114,14091,14899,15876,16684,17418,17949,18234,18234,18234,22,21,0,0,0,0,0,0,0x450000,0x858480,0xd80a80,0x1188f00,0x16b1500,0x1ab9980,0x1fe1f80,0x23ea400,0x2912a00,0x2e3b000,0x3243480,0x376ba80,0x3b73f00,0x409c500,0x44a4980,0x49c3200,0x4dcb680,0x52e9f00,0x56f2380,0x5ac9f00,0x5d75180,0x5ed7780,0x5ed7780,0x5ed7780,0x5ed7780,0x5ed7780,0x5ed7780,0x5ed7780,0x5ed7780,0x5ed7780,0x5ed7780,0x5ed7780,515,1323,2315,3123,4115,4923,5915,6723,7715,8707,9515,10507,11315,12307,13115,14092,14900,15877,16685,17419,17950,17950,17950,17950,21,20,0,0,0,0,0,0,0x450a80,0x858f00,0xd81500,0x1189980,0x16b1f80,0x1aba400,0x1fe2a00,0x23eae80,0x2913480,0x2e3ba80,0x3243f00,0x376c500,0x3b74980,0x409cf80,0x44a5400,0x49c3c80,0x4dcc100,0x52ea980,0x56f2e00,0x5aca980,0x5d75c00,0x5d75c00,0x5d75c00,0x5d75c00,0x5d75c00,0x5d75c00,0x5d75c00,0x5d75c00,0x5d75c00,0x5d75c00,0x5d75c00,0x5d75c00,516,1324,2316,3124,4116,4924,5916,6724,7716,8708,9516,10508,11316,12308,13116,14093,14901,15878,16686,17420,17951,17951,17951,17951,21,20,0,0,0,0,0,0,0x451500,0x859980,0xd81f80,0x118a400,0x16b2a00,0x1abae80,0x1fe3480,0x23eb900,0x2913f00,0x2e3c500,0x3244980,0x376cf80,0x3b75400,0x409da00,0x44a5e80,0x49c4700,0x4dccb80,0x52eb400,0x56f3880,0x5acb400,0x5d76680,0x5d76680,0x5d76680,0x5d76680,0x5d76680,0x5d76680,0x5d76680,0x5d76680,0x5d76680,0x5d76680,0x5d76680,0x5d76680,517,1325,2317,3125,4117,4925,5917,6725,7717,8709,9517,10509,11317,12309,13117,14094,14902,15879,16687,8008,8008,8008,8008,8008,20,19,0,0,0,0,0,0,0x451f80,0x85a400,0xd82a00,0x118ae80,0x16b3480,0x1abb900,0x1fe3f00,0x23ec380,0x2914980,0x2e3cf80,0x3245400,0x376da00,0x3b75e80,0x409e480,0x44a6900,0x49c5180,0x4dcd600,0x52ebe80,0x56f4300,0x2a4b000,0x2a4b000,0x2a4b000,0x2a4b000,0x2a4b000,0x2a4b000,0x2a4b000,0x2a4b000,0x2a4b000,0x2a4b000,0x2a4b000,0x2a4b000,0x2a4b000,518,1326,2318,3126,4118,4926,5918,6726,7718,8710,9518,10510,11318,12310,13118,14095,14903,15880,16688,8264,17952,17952,17952,17952,21,20,0,0,0,0,0,0,0x452a00,0x85ae80,0xd83480,0x118b900,0x16b3f00,0x1abc380,0x1fe4980,0x23ece00,0x2915400,0x2e3da00,0x3245e80,0x376e480,0x3b76900,0x409ef00,0x44a7380,0x49c5c00,0x4dce080,0x52ec900,0x56f4d80,0x2b9a580,0x5d77100,0x5d77100,0x5d77100,0x5d77100,0x5d77100,0x5d77100,0x5d77100,0x5d77100,0x5d77100,0x5d77100,0x5d77100,0x5d77100,519,1327,2319,3127,4119,4927,5919,6727,7719,8711,9519,10511,11319,12311,13119,14096,14904,15881,16689,17421,17953,17953,17953,17953,21,20,0,0,0,0,0,0,0x453480,0x85b900,0xd83f00,0x118c380,0x16b4980,0x1abce00,0x1fe5400,0x23ed880,0x2915e80,0x2e3e480,0x3246900,0x376ef00,0x3b77380,0x409f980,0x44a7e00,0x49c6680,0x4dceb00,0x52ed380,0x56f5800,0x5acbe80,0x5d77b80,0x5d77b80,0x5d77b80,0x5d77b80,0x5d77b80,0x5d77b80,0x5d77b80,0x5d77b80,0x5d77b80,0x5d77b80,0x5d77b80,0x5d77b80,520,1328,2320,3128,4120,4928,5920,6912,7720,8712,9520,10512,11320,12312,13120,14097,14905,15882,16690,17422,17954,17954,17954,17954,21,20,0,0,0,0,0,0,0x453f00,0x85c380,0xd84980,0x118ce00,0x16b5400,0x1abd880,0x1fe5e80,0x250e480,0x2916900,0x2e3ef00,0x3247380,0x376f980,0x3b77e00,0x40a0400,0x44a8880,0x49c7100,0x4dcf580,0x52ede00,0x56f6280,0x5acc900,0x5d78600,0x5d78600,0x5d78600,0x5d78600,0x5d78600,0x5d78600,0x5d78600,0x5d78600,0x5d78600,0x5d78600,0x5d78600,0x5d78600,521,1329,2321,3129,4121,4929,5921,6913,7721,8713,9521,10513,11321,12313,13121,14098,14906,15883,16691,8520,8520,8520,8520,8520,20,19,0,0,0,0,0,0,0x454980,0x85ce00,0xd85400,0x118d880,0x16b5e80,0x1abe300,0x1fe6900,0x250ef00,0x2917380,0x2e3f980,0x3247e00,0x3770400,0x3b78880,0x40a0e80,0x44a9300,0x49c7b80,0x4dd0000,0x52ee880,0x56f6d00,0x2ce9b00,0x2ce9b00,0x2ce9b00,0x2ce9b00,0x2ce9b00,0x2ce9b00,0x2ce9b00,0x2ce9b00,0x2ce9b00,0x2ce9b00,0x2ce9b00,0x2ce9b00,0x2ce9b00,522,1330,2322,3130,4122,4930,5922,6914,7722,8714,9522,10514,11322,12314,13122,14099,14907,15884,16692,17423,17955,18235,18235,18235,22,21,0,0,0,0,0,0,0x455400,0x85d880,0xd85e80,0x118e300,0x16b6900,0x1abed80,0x1fe7380,0x250f980,0x2917e00,0x2e40400,0x3248880,0x3770e80,0x3b79300,0x40a1900,0x44a9d80,0x49c8600,0x4dd0a80,0x52ef300,0x56f7780,0x5acd380,0x5d79080,0x5ed8200,0x5ed8200,0x5ed8200,0x5ed8200,0x5ed8200,0x5ed8200,0x5ed8200,0x5ed8200,0x5ed8200,0x5ed8200,0x5ed8200,523,1331,2323,3131,4123,4931,5923,6915,7723,8715,9523,10515,11323,12315,13123,14100,14908,15885,16693,17424,17956,17956,17956,17956,21,20,0,0,0,0,0,0,0x455e80,0x85e300,0xd86900,0x118ed80,0x16b7380,0x1abf800,0x1fe7e00,0x2510400,0x2918880,0x2e40e80,0x3249300,0x3771900,0x3b79d80,0x40a2380,0x44aa800,0x49c9080,0x4dd1500,0x52efd80,0x56f8200,0x5acde00,0x5d79b00,0x5d79b00,0x5d79b00,0x5d79b00,0x5d79b00,0x5d79b00,0x5d79b00,0x5d79b00,0x5d79b00,0x5d79b00,0x5d79b00,0x5d79b00,524,1332,2324,3132,4124,4932,5924,6916,7724,8716,9524,10516,11324,12316,13124,14101,14909,15886,16694,17425,17957,17957,17957,17957,21,20,0,0,0,0,0,0,0x456900,0x85ed80,0xd87380,0x118f800,0x16b7e00,0x1ac0280,0x1fe8880,0x2510e80,0x2919300,0x2e41900,0x3249d80,0x3772380,0x3b7a800,0x40a2e00,0x44ab280,0x49c9b00,0x4dd1f80,0x52f0800,0x56f8c80,0x5ace880,0x5d7a580,0x5d7a580,0x5d7a580,0x5d7a580,0x5d7a580,0x5d7a580,0x5d7a580,0x5d7a580,0x5d7a580,0x5d7a580,0x5d7a580,0x5d7a580,525,1333,2325,3133,4125,4933,5925,6917,7725,8717,9525,10517,11325,12317,13125,14102,14910,15887,16695,8776,8776,8776,8776,8776,20,19,0,0,0,0,0,0,0x457380,0x85f800,0xd87e00,0x1190280,0x16b8880,0x1ac0d00,0x1fe9300,0x2511900,0x2919d80,0x2e42380,0x324a800,0x3772e00,0x3b7b280,0x40a3880,0x44abd00,0x49ca580,0x4dd2a00,0x52f1280,0x56f9700,0x2e39080,0x2e39080,0x2e39080,0x2e39080,0x2e39080,0x2e39080,0x2e39080,0x2e39080,0x2e39080,0x2e39080,0x2e39080,0x2e39080,0x2e39080,526,1334,2326,3134,4126,4934,5926,6918,7726,8718,9526,10518,11326,12318,13126,14103,14911,15888,16696,9032,17958,17958,17958,17958,21,20,0,0,0,0,0,0,0x457e00,0x860280,0xd88880,0x1190d00,0x16b9300,0x1ac1780,0x1fe9d80,0x2512380,0x291a800,0x2e42e00,0x324b280,0x3773880,0x3b7bd00,0x40a4300,0x44ac780,0x49cb000,0x4dd3480,0x52f1d00,0x56fa180,0x2f88600,0x5d7b000,0x5d7b000,0x5d7b000,0x5d7b000,0x5d7b000,0x5d7b000,0x5d7b000,0x5d7b000,0x5d7b000,0x5d7b000,0x5d7b000,0x5d7b000,527,1335,2327,3135,4127,4935,5927,6919,7727,8719,9527,10519,11327,12319,13127,14104,14912,15889,16697,17426,17959,17959,17959,17959,21,20,0,0,0,0,0,0,0x458880,0x860d00,0xd89300,0x1191780,0x16b9d80,0x1ac2200,0x1fea800,0x2512e00,0x291b280,0x2e43880,0x324bd00,0x3774300,0x3b7c780,0x40a4d80,0x44ad200,0x49cba80,0x4dd3f00,0x52f2780,0x56fac00,0x5acf300,0x5d7ba80,0x5d7ba80,0x5d7ba80,0x5d7ba80,0x5d7ba80,0x5d7ba80,0x5d7ba80,0x5d7ba80,0x5d7ba80,0x5d7ba80,0x5d7ba80,0x5d7ba80,528,1336,2328,3136,4128,5120,5928,6920,7728,8720,9528,10520,11328,12320,13312,14105,14913,15890,16698,17427,17960,17960,17960,17960,21,20,0,0,0,0,0,0,0x459300,0x861780,0xd89d80,0x1192200,0x16ba800,0x1be2e00,0x1feb280,0x2513880,0x291bd00,0x2e44300,0x324c780,0x3774d80,0x3b7d200,0x40a5800,0x45cde00,0x49cc500,0x4dd4980,0x52f3200,0x56fb680,0x5acfd80,0x5d7c500,0x5d7c500,0x5d7c500,0x5d7c500,0x5d7c500,0x5d7c500,0x5d7c500,0x5d7c500,0x5d7c500,0x5d7c500,0x5d7c500,0x5d7c500,529,1337,2329,3137,4129,5121,5929,6921,7729,8721,9529,10521,11329,12321,13313,14106,14914,15891,16699,9288,9288,9288,9288,9288,20,19,0,0,0,0,0,0,0x459d80,0x862200,0xd8a800,0x1192c80,0x16bb280,0x1be3880,0x1febd00,0x2514300,0x291c780,0x2e44d80,0x324d200,0x3775800,0x3b7dc80,0x40a6280,0x45ce880,0x49ccf80,0x4dd5400,0x52f3c80,0x56fc100,0x30d7b80,0x30d7b80,0x30d7b80,0x30d7b80,0x30d7b80,0x30d7b80,0x30d7b80,0x30d7b80,0x30d7b80,0x30d7b80,0x30d7b80,0x30d7b80,0x30d7b80,530,1338,2330,3138,4130,5122,5930,6922,7730,8722,9530,10522,11330,12322,13314,14107,14915,15892,16700,17428,17961,18236,18236,18236,22,21,0,0,0,0,0,0,0x45a800,0x862c80,0xd8b280,0x1193700,0x16bbd00,0x1be4300,0x1fec780,0x2514d80,0x291d200,0x2e45800,0x324dc80,0x3776280,0x3b7e700,0x40a6d00,0x45cf300,0x49cda00,0x4dd5e80,0x52f4700,0x56fcb80,0x5ad0800,0x5d7cf80,0x5ed8c80,0x5ed8c80,0x5ed8c80,0x5ed8c80,0x5ed8c80,0x5ed8c80,0x5ed8c80,0x5ed8c80,0x5ed8c80,0x5ed8c80,0x5ed8c80,531,1339,2331,3139,4131,5123,5931,6923,7731,8723,9531,10523,11331,12323,13315,14108,14916,15893,16701,17429,17962,17962,17962,17962,21,20,0,0,0,0,0,0,0x45b280,0x863700,0xd8bd00,0x1194180,0x16bc780,0x1be4d80,0x1fed200,0x2515800,0x291dc80,0x2e46280,0x324e700,0x3776d00,0x3b7f180,0x40a7780,0x45cfd80,0x49ce480,0x4dd6900,0x52f5180,0x56fd600,0x5ad1280,0x5d7da00,0x5d7da00,0x5d7da00,0x5d7da00,0x5d7da00,0x5d7da00,0x5d7da00,0x5d7da00,0x5d7da00,0x5d7da00,0x5d7da00,0x5d7da00,532,1340,2332,3140,4132,5124,5932,6924,7732,8724,9532,10524,11332,12324,13316,14109,14917,15894,16702,17430,17963,17963,17963,17963,21,20,0,0,0,0,0,0,0x45bd00,0x864180,0xd8c780,0x1194c00,0x16bd200,0x1be5800,0x1fedc80,0x2516280,0x291e700,0x2e46d00,0x324f180,0x3777780,0x3b7fc00,0x40a8200,0x45d0800,0x49cef00,0x4dd7380,0x52f5c00,0x56fe080,0x5ad1d00,0x5d7e480,0x5d7e480,0x5d7e480,0x5d7e480,0x5d7e480,0x5d7e480,0x5d7e480,0x5d7e480,0x5d7e480,0x5d7e480,0x5d7e480,0x5d7e480,533,1341,2333,3141,4133,5125,5933,6925,7733,8725,9533,10525,11333,12325,13317,14110,14918,15895,16703,9544,9544,9544,9544,9544,20,19,0,0,0,0,0,0,0x45c780,0x864c00,0xd8d200,0x1195680,0x16bdc80,0x1be6280,0x1fee700,0x2516d00,0x291f180,0x2e47780,0x324fc00,0x3778200,0x3b80680,0x40a8c80,0x45d1280,0x49cf980,0x4dd7e00,0x52f6680,0x56feb00,0x3227100,0x3227100,0x3227100,0x3227100,0x3227100,0x3227100,0x3227100,0x3227100,0x3227100,0x3227100,0x3227100,0x3227100,0x3227100,534,1342,2334,3142,4134,5126,5934,6926,7734,8726,9534,10526,11334,12326,13318,14111,14919,15896,16704,9800,17964,17964,17964,17964,21,20,0,0,0,0,0,0,0x45d200,0x865680,0xd8dc80,0x1196100,0x16be700,0x1be6d00,0x1fef180,0x2517780,0x291fc00,0x2e48200,0x3250680,0x3778c80,0x3b81100,0x40a9700,0x45d1d00,0x49d0400,0x4dd8880,0x52f7100,0x56ff580,0x3376680,0x5d7ef00,0x5d7ef00,0x5d7ef00,0x5d7ef00,0x5d7ef00,0x5d7ef00,0x5d7ef00,0x5d7ef00,0x5d7ef00,0x5d7ef00,0x5d7ef00,0x5d7ef00,535,1343,2335,3143,4135,5127,5935,6927,7735,8727,9535,10527,11335,12327,13319,14112,15104,15897,16705,17431,17965,17965,17965,17965,21,20,0,0,0,0,0,0,0x45dc80,0x866100,0xd8e700,0x1196b80,0x16bf180,0x1be7780,0x1fefc00,0x2518200,0x2920680,0x2e48c80,0x3251100,0x3779700,0x3b81b80,0x40aa180,0x45d2780,0x49d0e80,0x4ef9480,0x52f7b80,0x5700000,0x5ad2780,0x5d7f980,0x5d7f980,0x5d7f980,0x5d7f980,0x5d7f980,0x5d7f980,0x5d7f980,0x5d7f980,0x5d7f980,0x5d7f980,0x5d7f980,0x5d7f980,536,1344,2336,3328,4136,5128,5936,6928,7736,8728,9536,10528,11520,12328,13320,14113,15105,15898,16706,17432,17966,17966,17966,17966,21,20,0,0,0,0,0,0,0x45e700,0x866b80,0xd8f180,0x12b7780,0x16bfc00,0x1be8200,0x1ff0680,0x2518c80,0x2921100,0x2e49700,0x3251b80,0x377a180,0x3ca2780,0x40aac00,0x45d3200,0x49d1900,0x4ef9f00,0x52f8600,0x5700a80,0x5ad3200,0x5d80400,0x5d80400,0x5d80400,0x5d80400,0x5d80400,0x5d80400,0x5d80400,0x5d80400,0x5d80400,0x5d80400,0x5d80400,0x5d80400,537,1345,2337,3329,4137,5129,5937,6929,7737,8729,9537,10529,11521,12329,13321,14114,15106,15899,16707,10056,10056,10056,10056,10056,20,19,0,0,0,0,0,0,0x45f180,0x867600,0xd8fc00,0x12b8200,0x16c0680,0x1be8c80,0x1ff1100,0x2519700,0x2921b80,0x2e4a180,0x3252600,0x377ac00,0x3ca3200,0x40ab680,0x45d3c80,0x49d2380,0x4efa980,0x52f9080,0x5701500,0x34c5c00,0x34c5c00,0x34c5c00,0x34c5c00,0x34c5c00,0x34c5c00,0x34c5c00,0x34c5c00,0x34c5c00,0x34c5c00,0x34c5c00,0x34c5c00,0x34c5c00,538,1346,2338,3330,4138,5130,5938,6930,7738,8730,9538,10530,11522,12330,13322,14115,15107,15900,16708,17433,17967,18237,18237,18237,22,21,0,0,0,0,0,0,0x45fc00,0x868080,0xd90680,0x12b8c80,0x16c1100,0x1be9700,0x1ff1b80,0x251a180,0x2922600,0x2e4ac00,0x3253080,0x377b680,0x3ca3c80,0x40ac100,0x45d4700,0x49d2e00,0x4efb400,0x52f9b00,0x5701f80,0x5ad3c80,0x5d80e80,0x5ed9700,0x5ed9700,0x5ed9700,0x5ed9700,0x5ed9700,0x5ed9700,0x5ed9700,0x5ed9700,0x5ed9700,0x5ed9700,0x5ed9700,539,1347,2339,3331,4139,5131,5939,6931,7739,8731,9539,10531,11523,12331,13323,14116,15108,15901,16709,17434,17968,17968,17968,17968,21,20,0,0,0,0,0,0,0x460680,0x868b00,0xd91100,0x12b9700,0x16c1b80,0x1bea180,0x1ff2600,0x251ac00,0x2923080,0x2e4b680,0x3253b00,0x377c100,0x3ca4700,0x40acb80,0x45d5180,0x49d3880,0x4efbe80,0x52fa580,0x5702a00,0x5ad4700,0x5d81900,0x5d81900,0x5d81900,0x5d81900,0x5d81900,0x5d81900,0x5d81900,0x5d81900,0x5d81900,0x5d81900,0x5d81900,0x5d81900,540,1348,2340,3332,4140,5132,5940,6932,7740,8732,9540,10532,11524,12332,13324,14117,15109,15902,16710,17435,17969,17969,17969,17969,21,20,0,0,0,0,0,0,0x461100,0x869580,0xd91b80,0x12ba180,0x16c2600,0x1beac00,0x1ff3080,0x251b680,0x2923b00,0x2e4c100,0x3254580,0x377cb80,0x3ca5180,0x40ad600,0x45d5c00,0x49d4300,0x4efc900,0x52fb000,0x5703480,0x5ad5180,0x5d82380,0x5d82380,0x5d82380,0x5d82380,0x5d82380,0x5d82380,0x5d82380,0x5d82380,0x5d82380,0x5d82380,0x5d82380,0x5d82380,541,1349,2341,3333,4141,5133,5941,6933,7741,8733,9541,10533,11525,12333,13325,14118,15110,15903,16711,10312,10312,10312,10312,10312,20,19,0,0,0,0,0,0,0x461b80,0x86a000,0xd92600,0x12bac00,0x16c3080,0x1beb680,0x1ff3b00,0x251c100,0x2924580,0x2e4cb80,0x3255000,0x377d600,0x3ca5c00,0x40ae080,0x45d6680,0x49d4d80,0x4efd380,0x52fba80,0x5703f00,0x3615180,0x3615180,0x3615180,0x3615180,0x3615180,0x3615180,0x3615180,0x3615180,0x3615180,0x3615180,0x3615180,0x3615180,0x3615180,542,1350,2342,3334,4142,5134,5942,6934,7742,8734,9542,10534,11526,12334,13326,14119,15111,15904,16896,10568,17970,17970,17970,17970,21,20,0,0,0,0,0,0,0x462600,0x86aa80,0xd93080,0x12bb680,0x16c3b00,0x1bec100,0x1ff4580,0x251cb80,0x2925000,0x2e4d600,0x3255a80,0x377e080,0x3ca6680,0x40aeb00,0x45d7100,0x49d5800,0x4efde00,0x52fc500,0x5824b00,0x3764700,0x5d82e00,0x5d82e00,0x5d82e00,0x5d82e00,0x5d82e00,0x5d82e00,0x5d82e00,0x5d82e00,0x5d82e00,0x5d82e00,0x5d82e00,0x5d82e00,543,1351,2343,3335,4143,5135,5943,6935,7743,8735,9543,10535,11527,12335,13327,14120,15112,15905,16897,17436,17971,17971,17971,17971,21,20,0,0,0,0,0,0,0x463080,0x86b500,0xd93b00,0x12bc100,0x16c4580,0x1becb80,0x1ff5000,0x251d600,0x2925a80,0x2e4e080,0x3256500,0x377eb00,0x3ca7100,0x40af580,0x45d7b80,0x49d6280,0x4efe880,0x52fcf80,0x5825580,0x5ad5c00,0x5d83880,0x5d83880,0x5d83880,0x5d83880,0x5d83880,0x5d83880,0x5d83880,0x5d83880,0x5d83880,0x5d83880,0x5d83880,0x5d83880,544,1536,2344,3336,4144,5136,5944,6936,7744,8736,9728,10536,11528,12336,13328,14121,15113,15906,16898,17437,17972,17972,17972,17972,21,20,0,0,0,0,0,0,0x463b00,0x98c100,0xd94580,0x12bcb80,0x16c5000,0x1bed600,0x1ff5a80,0x251e080,0x2926500,0x2e4eb00,0x3377100,0x377f580,0x3ca7b80,0x40b0000,0x45d8600,0x49d6d00,0x4eff300,0x52fda00,0x5826000,0x5ad6680,0x5d84300,0x5d84300,0x5d84300,0x5d84300,0x5d84300,0x5d84300,0x5d84300,0x5d84300,0x5d84300,0x5d84300,0x5d84300,0x5d84300,545,1537,2345,3337,4145,5137,5945,6937,7745,8737,9729,10537,11529,12337,13329,14122,15114,15907,16899,10824,10824,10824,10824,10824,20,19,0,0,0,0,0,0,0x464580,0x98cb80,0xd95000,0x12bd600,0x16c5a80,0x1bee080,0x1ff6500,0x251eb00,0x2926f80,0x2e4f580,0x3377b80,0x3780000,0x3ca8600,0x40b0a80,0x45d9080,0x49d7780,0x4effd80,0x52fe480,0x5826a80,0x38b3c80,0x38b3c80,0x38b3c80,0x38b3c80,0x38b3c80,0x38b3c80,0x38b3c80,0x38b3c80,0x38b3c80,0x38b3c80,0x38b3c80,0x38b3c80,0x38b3c80,546,1538,2346,3338,4146,5138,5946,6938,7746,8738,9730,10538,11530,12338,13330,14123,15115,15908,16900,17438,17973,18238,18238,18238,22,21,0,0,0,0,0,0,0x465000,0x98d600,0xd95a80,0x12be080,0x16c6500,0x1beeb00,0x1ff6f80,0x251f580,0x2927a00,0x2e50000,0x3378600,0x3780a80,0x3ca9080,0x40b1500,0x45d9b00,0x49d8200,0x4f00800,0x52fef00,0x5827500,0x5ad7100,0x5d84d80,0x5eda180,0x5eda180,0x5eda180,0x5eda180,0x5eda180,0x5eda180,0x5eda180,0x5eda180,0x5eda180,0x5eda180,0x5eda180,547,1539,2347,3339,4147,5139,5947,6939,7747,8739,9731,10539,11531,12339,13331,14124,15116,15909,16901,17439,17974,17974,17974,17974,21,20,0,0,0,0,0,0,0x465a80,0x98e080,0xd96500,0x12beb00,0x16c6f80,0x1bef580,0x1ff7a00,0x2520000,0x2928480,0x2e50a80,0x3379080,0x3781500,0x3ca9b00,0x40b1f80,0x45da580,0x49d8c80,0x4f01280,0x52ff980,0x5827f80,0x5ad7b80,0x5d85800,0x5d85800,0x5d85800,0x5d85800,0x5d85800,0x5d85800,0x5d85800,0x5d85800,0x5d85800,0x5d85800,0x5d85800,0x5d85800,548,1540,2348,3340,4148,5140,5948,6940,7748,8740,9732,10540,11532,12340,13332,14125,15117,15910,16902,17440,17975,17975,17975,17975,21,20,0,0,0,0,0,0,0x466500,0x98eb00,0xd96f80,0x12bf580,0x16c7a00,0x1bf0000,0x1ff8480,0x2520a80,0x2928f00,0x2e51500,0x3379b00,0x3781f80,0x3caa580,0x40b2a00,0x45db000,0x49d9700,0x4f01d00,0x5300400,0x5828a00,0x5ad8600,0x5d86280,0x5d86280,0x5d86280,0x5d86280,0x5d86280,0x5d86280,0x5d86280,0x5d86280,0x5d86280,0x5d86280,0x5d86280,0x5d86280,549,1541,2349,3341,4149,5141,5949,6941,7749,8741,9733,10541,11533,12341,13333,14126,15118,15911,16903,11080,11080,11080,11080,11080,20,19,0,0,0,0,0,0,0x466f80,0x98f580,0xd97a00,0x12c0000,0x16c8480,0x1bf0a80,0x1ff8f00,0x2521500,0x2929980,0x2e51f80,0x337a580,0x3782a00,0x3cab000,0x40b3480,0x45dba80,0x49da180,0x4f02780,0x5300e80,0x5829480,0x3a03200,0x3a03200,0x3a03200,0x3a03200,0x3a03200,0x3a03200,0x3a03200,0x3a03200,0x3a03200,0x3a03200,0x3a03200,0x3a03200,0x3a03200,550,1542,2350,3342,4150,5142,5950,6942,7750,8742,9734,10542,11534,12342,13334,14127,15119,15912,16904,11336,17976,17976,17976,17976,21,20,0,0,0,0,0,0,0x467a00,0x990000,0xd98480,0x12c0a80,0x16c8f00,0x1bf1500,0x1ff9980,0x2521f80,0x292a400,0x2e52a00,0x337b000,0x3783480,0x3caba80,0x40b3f00,0x45dc500,0x49dac00,0x4f03200,0x5301900,0x5829f00,0x3b52780,0x5d86d00,0x5d86d00,0x5d86d00,0x5d86d00,0x5d86d00,0x5d86d00,0x5d86d00,0x5d86d00,0x5d86d00,0x5d86d00,0x5d86d00,0x5d86d00,551,1543,2351,3343,4151,5143,5951,6943,7751,8743,9735,10543,11535,12343,13335,14128,15120,15913,16905,17441,17977,17977,17977,17977,21,20,0,0,0,0,0,0,0x468480,0x990a80,0xd98f00,0x12c1500,0x16c9980,0x1bf1f80,0x1ffa400,0x2522a00,0x292ae80,0x2e53480,0x337ba80,0x3783f00,0x3cac500,0x40b4980,0x45dcf80,0x49db680,0x4f03c80,0x5302380,0x582a980,0x5ad9080,0x5d87780,0x5d87780,0x5d87780,0x5d87780,0x5d87780,0x5d87780,0x5d87780,0x5d87780,0x5d87780,0x5d87780,0x5d87780,0x5d87780,552,1544,2352,3344,4152,5144,5952,6944,7936,8744,9736,10544,11536,12344,13336,14129,15121,15914,16906,17442,17978,17978,17978,17978,21,20,0,0,0,0,0,0,0x468f00,0x991500,0xd99980,0x12c1f80,0x16ca400,0x1bf2a00,0x1ffae80,0x2523480,0x2a4ba80,0x2e53f00,0x337c500,0x3784980,0x3cacf80,0x40b5400,0x45dda00,0x49dc100,0x4f04700,0x5302e00,0x582b400,0x5ad9b00,0x5d88200,0x5d88200,0x5d88200,0x5d88200,0x5d88200,0x5d88200,0x5d88200,0x5d88200,0x5d88200,0x5d88200,0x5d88200,0x5d88200,553,1545,2353,3345,4153,5145,5953,6945,7937,8745,9737,10545,11537,12345,13337,14130,15122,15915,16907,11592,11592,11592,11592,11592,20,19,0,0,0,0,0,0,0x469980,0x991f80,0xd9a400,0x12c2a00,0x16cae80,0x1bf3480,0x1ffb900,0x2523f00,0x2a4c500,0x2e54980,0x337cf80,0x3785400,0x3cada00,0x40b5e80,0x45de480,0x49dcb80,0x4f05180,0x5303880,0x582be80,0x3ca1d00,0x3ca1d00,0x3ca1d00,0x3ca1d00,0x3ca1d00,0x3ca1d00,0x3ca1d00,0x3ca1d00,0x3ca1d00,0x3ca1d00,0x3ca1d00,0x3ca1d00,0x3ca1d00,554,1546,2354,3346,4154,5146,5954,6946,7938,8746,9738,10546,11538,12346,13338,14131,15123,15916,16908,17443,17979,18239,18239,18239,22,21,0,0,0,0,0,0,0x46a400,0x992a00,0xd9ae80,0x12c3480,0x16cb900,0x1bf3f00,0x1ffc380,0x2524980,0x2a4cf80,0x2e55400,0x337da00,0x3785e80,0x3cae480,0x40b6900,0x45def00,0x49dd600,0x4f05c00,0x5304300,0x582c900,0x5ada580,0x5d88c80,0x5edac00,0x5edac00,0x5edac00,0x5edac00,0x5edac00,0x5edac00,0x5edac00,0x5edac00,0x5edac00,0x5edac00,0x5edac00,555,1547,2355,3347,4155,5147,5955,6947,7939,8747,9739,10547,11539,12347,13339,14132,15124,15917,16909,17444,17980,17980,17980,17980,21,20,0,0,0,0,0,0,0x46ae80,0x993480,0xd9b900,0x12c3f00,0x16cc380,0x1bf4980,0x1ffce00,0x2525400,0x2a4da00,0x2e55e80,0x337e480,0x3786900,0x3caef00,0x40b7380,0x45df980,0x49de080,0x4f06680,0x5304d80,0x582d380,0x5adb000,0x5d89700,0x5d89700,0x5d89700,0x5d89700,0x5d89700,0x5d89700,0x5d89700,0x5d89700,0x5d89700,0x5d89700,0x5d89700,0x5d89700,556,1548,2356,3348,4156,5148,5956,6948,7940,8748,9740,10548,11540,12348,13340,14133,15125,15918,16910,17445,17981,17981,17981,17981,21,20,0,0,0,0,0,0,0x46b900,0x993f00,0xd9c380,0x12c4980,0x16cce00,0x1bf5400,0x1ffd880,0x2525e80,0x2a4e480,0x2e56900,0x337ef00,0x3787380,0x3caf980,0x40b7e00,0x45e0400,0x49deb00,0x4f07100,0x5305800,0x582de00,0x5adba80,0x5d8a180,0x5d8a180,0x5d8a180,0x5d8a180,0x5d8a180,0x5d8a180,0x5d8a180,0x5d8a180,0x5d8a180,0x5d8a180,0x5d8a180,0x5d8a180,557,1549,2357,3349,4157,5149,5957,6949,7941,8749,9741,10549,11541,12349,13341,14134,15126,15919,16911,11848,11848,11848,11848,11848,20,19,0,0,0,0,0,0,0x46c380,0x994980,0xd9ce00,0x12c5400,0x16cd880,0x1bf5e80,0x1ffe300,0x2526900,0x2a4ef00,0x2e57380,0x337f980,0x3787e00,0x3cb0400,0x40b8880,0x45e0e80,0x49df580,0x4f07b80,0x5306280,0x582e880,0x3df1280,0x3df1280,0x3df1280,0x3df1280,0x3df1280,0x3df1280,0x3df1280,0x3df1280,0x3df1280,0x3df1280,0x3df1280,0x3df1280,0x3df1280,558,1550,2358,3350,4158,5150,5958,6950,7942,8750,9742,10550,11542,12350,13342,14135,15127,15920,16912,12104,17982,17982,17982,17982,21,20,0,0,0,0,0,0,0x46ce00,0x995400,0xd9d880,0x12c5e80,0x16ce300,0x1bf6900,0x1ffed80,0x2527380,0x2a4f980,0x2e57e00,0x3380400,0x3788880,0x3cb0e80,0x40b9300,0x45e1900,0x49e0000,0x4f08600,0x5306d00,0x582f300,0x3f40800,0x5d8ac00,0x5d8ac00,0x5d8ac00,0x5d8ac00,0x5d8ac00,0x5d8ac00,0x5d8ac00,0x5d8ac00,0x5d8ac00,0x5d8ac00,0x5d8ac00,0x5d8ac00,559,1551,2359,3351,4159,5151,5959,6951,7943,8751,9743,10551,11543,12351,13343,14136,15128,15921,16913,17446,17983,17983,17983,17983,21,20,0,0,0,0,0,0,0x46d880,0x995e80,0xd9e300,0x12c6900,0x16ced80,0x1bf7380,0x1fff800,0x2527e00,0x2a50400,0x2e58880,0x3380e80,0x3789300,0x3cb1900,0x40b9d80,0x45e2380,0x49e0a80,0x4f09080,0x5307780,0x582fd80,0x5adc500,0x5d8b680,0x5d8b680,0x5d8b680,0x5d8b680,0x5d8b680,0x5d8b680,0x5d8b680,0x5d8b680,0x5d8b680,0x5d8b680,0x5d8b680,0x5d8b680,560,1552,2360,3352,4160,5152,6144,6952,7944,8752,9744,10552,11544,12352,13344,14137,15129,15922,16914,17447,17984,17984,17984,17984,21,20,0,0,0,0,0,0,0x46e300,0x996900,0xd9ed80,0x12c7380,0x16cf800,0x1bf7e00,0x2120400,0x2528880,0x2a50e80,0x2e59300,0x3381900,0x3789d80,0x3cb2380,0x40ba800,0x45e2e00,0x49e1500,0x4f09b00,0x5308200,0x5830800,0x5adcf80,0x5d8c100,0x5d8c100,0x5d8c100,0x5d8c100,0x5d8c100,0x5d8c100,0x5d8c100,0x5d8c100,0x5d8c100,0x5d8c100,0x5d8c100,0x5d8c100,561,1553,2361,3353,4161,5153,6145,6953,7945,8753,9745,10553,11545,12353,13345,14138,15130,15923,16915,12360,12360,12360,12360,12360,20,19,0,0,0,0,0,0,0x46ed80,0x997380,0xd9f800,0x12c7e00,0x16d0280,0x1bf8880,0x2120e80,0x2529300,0x2a51900,0x2e59d80,0x3382380,0x378a800,0x3cb2e00,0x40bb280,0x45e3880,0x49e1f80,0x4f0a580,0x5308c80,0x5831280,0x408fd80,0x408fd80,0x408fd80,0x408fd80,0x408fd80,0x408fd80,0x408fd80,0x408fd80,0x408fd80,0x408fd80,0x408fd80,0x408fd80,0x408fd80,562,1554,2362,3354,4162,5154,6146,6954,7946,8754,9746,10554,11546,12354,13346,14139,15131,15924,16916,17448,17985,18240,18240,18240,22,21,0,0,0,0,0,0,0x46f800,0x997e00,0xda0280,0x12c8880,0x16d0d00,0x1bf9300,0x2121900,0x2529d80,0x2a52380,0x2e5a800,0x3382e00,0x378b280,0x3cb3880,0x40bbd00,0x45e4300,0x49e2a00,0x4f0b000,0x5309700,0x5831d00,0x5adda00,0x5d8cb80,0x5edb680,0x5edb680,0x5edb680,0x5edb680,0x5edb680,0x5edb680,0x5edb680,0x5edb680,0x5edb680,0x5edb680,0x5edb680,563,1555,2363,3355,4163,5155,6147,6955,7947,8755,9747,10555,11547,12355,13347,14140,15132,15925,16917,17449,17986,17986,17986,17986,21,20,0,0,0,0,0,0,0x470280,0x998880,0xda0d00,0x12c9300,0x16d1780,0x1bf9d80,0x2122380,0x252a800,0x2a52e00,0x2e5b280,0x3383880,0x378bd00,0x3cb4300,0x40bc780,0x45e4d80,0x49e3480,0x4f0ba80,0x530a180,0x5832780,0x5ade480,0x5d8d600,0x5d8d600,0x5d8d600,0x5d8d600,0x5d8d600,0x5d8d600,0x5d8d600,0x5d8d600,0x5d8d600,0x5d8d600,0x5d8d600,0x5d8d600,564,1556,2364,3356,4164,5156,6148,6956,7948,8756,9748,10556,11548,12356,13348,14141,15133,15926,16918,17450,17987,17987,17987,17987,21,20,0,0,0,0,0,0,0x470d00,0x999300,0xda1780,0x12c9d80,0x16d2200,0x1bfa800,0x2122e00,0x252b280,0x2a53880,0x2e5bd00,0x3384300,0x378c780,0x3cb4d80,0x40bd200,0x45e5800,0x49e3f00,0x4f0c500,0x530ac00,0x5833200,0x5adef00,0x5d8e080,0x5d8e080,0x5d8e080,0x5d8e080,0x5d8e080,0x5d8e080,0x5d8e080,0x5d8e080,0x5d8e080,0x5d8e080,0x5d8e080,0x5d8e080,565,1557,2365,3357,4165,5157,6149,6957,7949,8757,9749,10557,11549,12357,13349,14142,15134,15927,16919,12616,12616,12616,12616,12616,20,19,0,0,0,0,0,0,0x471780,0x999d80,0xda2200,0x12ca800,0x16d2c80,0x1bfb280,0x2123880,0x252bd00,0x2a54300,0x2e5c780,0x3384d80,0x378d200,0x3cb5800,0x40bdc80,0x45e6280,0x49e4980,0x4f0cf80,0x530b680,0x5833c80,0x41df300,0x41df300,0x41df300,0x41df300,0x41df300,0x41df300,0x41df300,0x41df300,0x41df300,0x41df300,0x41df300,0x41df300,0x41df300,566,1558,2366,3358,4166,5158,6150,6958,7950,8758,9750,10558,11550,12358,13350,14143,15135,15928,16920,12872,17988,17988,17988,17988,21,20,0,0,0,0,0,0,0x472200,0x99a800,0xda2c80,0x12cb280,0x16d3700,0x1bfbd00,0x2124300,0x252c780,0x2a54d80,0x2e5d200,0x3385800,0x378dc80,0x3cb6280,0x40be700,0x45e6d00,0x49e5400,0x4f0da00,0x530c100,0x5834700,0x432e880,0x5d8eb00,0x5d8eb00,0x5d8eb00,0x5d8eb00,0x5d8eb00,0x5d8eb00,0x5d8eb00,0x5d8eb00,0x5d8eb00,0x5d8eb00,0x5d8eb00,0x5d8eb00,567,1559,2367,3359,4167,5159,6151,6959,7951,8759,9751,10559,11551,12359,13351,14144,15136,15929,16921,17451,17989,17989,17989,17989,21,20,0,0,0,0,0,0,0x472c80,0x99b280,0xda3700,0x12cbd00,0x16d4180,0x1bfc780,0x2124d80,0x252d200,0x2a55800,0x2e5dc80,0x3386280,0x378e700,0x3cb6d00,0x40bf180,0x45e7780,0x49e5e80,0x4f0e480,0x530cb80,0x5835180,0x5adf980,0x5d8f580,0x5d8f580,0x5d8f580,0x5d8f580,0x5d8f580,0x5d8f580,0x5d8f580,0x5d8f580,0x5d8f580,0x5d8f580,0x5d8f580,0x5d8f580,568,1560,2368,3360,4352,5160,6152,6960,7952,8760,9752,10560,11552,12544,13352,14145,15137,15930,16922,17452,17990,17990,17990,17990,21,20,0,0,0,0,0,0,0x473700,0x99bd00,0xda4180,0x12cc780,0x17f4d80,0x1bfd200,0x2125800,0x252dc80,0x2a56280,0x2e5e700,0x3386d00,0x378f180,0x3cb7780,0x41dfd80,0x45e8200,0x49e6900,0x4f0ef00,0x530d600,0x5835c00,0x5ae0400,0x5d90000,0x5d90000,0x5d90000,0x5d90000,0x5d90000,0x5d90000,0x5d90000,0x5d90000,0x5d90000,0x5d90000,0x5d90000,0x5d90000,569,1561,2369,3361,4353,5161,6153,6961,7953,8761,9753,10561,11553,12545,13353,14146,15138,15931,16923,13128,13128,13128,13128,13128,20,19,0,0,0,0,0,0,0x474180,0x99c780,0xda4c00,0x12cd200,0x17f5800,0x1bfdc80,0x2126280,0x252e700,0x2a56d00,0x2e5f180,0x3387780,0x378fc00,0x3cb8200,0x41e0800,0x45e8c80,0x49e7380,0x4f0f980,0x530e080,0x5836680,0x447de00,0x447de00,0x447de00,0x447de00,0x447de00,0x447de00,0x447de00,0x447de00,0x447de00,0x447de00,0x447de00,0x447de00,0x447de00,570,1562,2370,3362,4354,5162,6154,6962,7954,8762,9754,10562,11554,12546,13354,14147,15139,15932,16924,17453,17991,18241,18241,18241,22,21,0,0,0,0,0,0,0x474c00,0x99d200,0xda5680,0x12cdc80,0x17f6280,0x1bfe700,0x2126d00,0x252f180,0x2a57780,0x2e5fc00,0x3388200,0x3790680,0x3cb8c80,0x41e1280,0x45e9700,0x49e7e00,0x4f10400,0x530eb00,0x5837100,0x5ae0e80,0x5d90a80,0x5edc100,0x5edc100,0x5edc100,0x5edc100,0x5edc100,0x5edc100,0x5edc100,0x5edc100,0x5edc100,0x5edc100,0x5edc100,571,1563,2371,3363,4355,5163,6155,6963,7955,8763,9755,10563,11555,12547,13355,14148,15140,15933,16925,17454,18176,18176,18176,18176,21,20,0,0,0,0,0,0,0x475680,0x99dc80,0xda6100,0x12ce700,0x17f6d00,0x1bff180,0x2127780,0x252fc00,0x2a58200,0x2e60680,0x3388c80,0x3791100,0x3cb9700,0x41e1d00,0x45ea180,0x49e8880,0x4f10e80,0x530f580,0x5837b80,0x5ae1900,0x5eb1680,0x5eb1680,0x5eb1680,0x5eb1680,0x5eb1680,0x5eb1680,0x5eb1680,0x5eb1680,0x5eb1680,0x5eb1680,0x5eb1680,0x5eb1680,572,1564,2372,3364,4356,5164,6156,6964,7956,8764,9756,10564,11556,12548,13356,14149,15141,15934,16926,17455,18177,18177,18177,18177,21,20,0,0,0,0,0,0,0x476100,0x99e700,0xda6b80,0x12cf180,0x17f7780,0x1bffc00,0x2128200,0x2530680,0x2a58c80,0x2e61100,0x3389700,0x3791b80,0x3cba180,0x41e2780,0x45eac00,0x49e9300,0x4f11900,0x5310000,0x5838600,0x5ae2380,0x5eb2100,0x5eb2100,0x5eb2100,0x5eb2100,0x5eb2100,0x5eb2100,0x5eb2100,0x5eb2100,0x5eb2100,0x5eb2100,0x5eb2100,0x5eb2100,573,1565,2373,3365,4357,5165,6157,6965,7957,8765,9757,10565,11557,12549,13357,14150,15142,15935,16927,13384,13384,13384,13384,13384,20,19,0,0,0,0,0,0,0x476b80,0x99f180,0xda7600,0x12cfc00,0x17f8200,0x1c00680,0x2128c80,0x2531100,0x2a59700,0x2e61b80,0x338a180,0x3792600,0x3cbac00,0x41e3200,0x45eb680,0x49e9d80,0x4f12380,0x5310a80,0x5839080,0x45cd380,0x45cd380,0x45cd380,0x45cd380,0x45cd380,0x45cd380,0x45cd380,0x45cd380,0x45cd380,0x45cd380,0x45cd380,0x45cd380,0x45cd380,574,1566,2374,3366,4358,5166,6158,6966,7958,8766,9758,10566,11558,12550,13358,14151,15143,15936,16928,13640,18178,18178,18178,18178,21,20,0,0,0,0,0,0,0x477600,0x99fc00,0xda8080,0x12d0680,0x17f8c80,0x1c01100,0x2129700,0x2531b80,0x2a5a180,0x2e62600,0x338ac00,0x3793080,0x3cbb680,0x41e3c80,0x45ec100,0x49ea800,0x4f12e00,0x5311500,0x5839b00,0x471c900,0x5eb2b80,0x5eb2b80,0x5eb2b80,0x5eb2b80,0x5eb2b80,0x5eb2b80,0x5eb2b80,0x5eb2b80,0x5eb2b80,0x5eb2b80,0x5eb2b80,0x5eb2b80,575,1567,2375,3367,4359,5167,6159,6967,7959,8767,9759,10567,11559,12551,13359,14336,15144,15937,16929,17456,18179,18179,18179,18179,21,20,0,0,0,0,0,0,0x478080,0x9a0680,0xda8b00,0x12d1100,0x17f9700,0x1c01b80,0x212a180,0x2532600,0x2a5ac00,0x2e63080,0x338b680,0x3793b00,0x3cbc100,0x41e4700,0x45ecb80,0x4b0b400,0x4f13880,0x5311f80,0x583a580,0x5ae2e00,0x5eb3600,0x5eb3600,0x5eb3600,0x5eb3600,0x5eb3600,0x5eb3600,0x5eb3600,0x5eb3600,0x5eb3600,0x5eb3600,0x5eb3600,0x5eb3600,576,1568,2560,3368,4360,5168,6160,6968,7960,8768,9760,10752,11560,12552,13360,14337,15145,15938,16930,17457,18180,18180,18180,18180,21,20,0,0,0,0,0,0,0x478b00,0x9a1100,0xec9700,0x12d1b80,0x17fa180,0x1c02600,0x212ac00,0x2533080,0x2a5b680,0x2e63b00,0x338c100,0x38b4700,0x3cbcb80,0x41e5180,0x45ed600,0x4b0be80,0x4f14300,0x5312a00,0x583b000,0x5ae3880,0x5eb4080,0x5eb4080,0x5eb4080,0x5eb4080,0x5eb4080,0x5eb4080,0x5eb4080,0x5eb4080,0x5eb4080,0x5eb4080,0x5eb4080,0x5eb4080,577,1569,2561,3369,4361,5169,6161,6969,7961,8769,9761,10753,11561,12553,13361,14338,15146,15939,16931,13896,13896,13896,13896,13896,20,19,0,0,0,0,0,0,0x479580,0x9a1b80,0xeca180,0x12d2600,0x17fac00,0x1c03080,0x212b680,0x2533b00,0x2a5c100,0x2e64580,0x338cb80,0x38b5180,0x3cbd600,0x41e5c00,0x45ee080,0x4b0c900,0x4f14d80,0x5313480,0x583ba80,0x486be80,0x486be80,0x486be80,0x486be80,0x486be80,0x486be80,0x486be80,0x486be80,0x486be80,0x486be80,0x486be80,0x486be80,0x486be80,578,1570,2562,3370,4362,5170,6162,6970,7962,8770,9762,10754,11562,12554,13362,14339,15147,15940,16932,17458,18181,18242,18242,18242,22,21,0,0,0,0,0,0,0x47a000,0x9a2600,0xecac00,0x12d3080,0x17fb680,0x1c03b00,0x212c100,0x2534580,0x2a5cb80,0x2e65000,0x338d600,0x38b5c00,0x3cbe080,0x41e6680,0x45eeb00,0x4b0d380,0x4f15800,0x5313f00,0x583c500,0x5ae4300,0x5eb4b00,0x5edcb80,0x5edcb80,0x5edcb80,0x5edcb80,0x5edcb80,0x5edcb80,0x5edcb80,0x5edcb80,0x5edcb80,0x5edcb80,0x5edcb80,579,1571,2563,3371,4363,5171,6163,6971,7963,8771,9763,10755,11563,12555,13363,14340,15148,15941,16933,17459,18182,18182,18182,18182,21,20,0,0,0,0,0,0,0x47aa80,0x9a3080,0xecb680,0x12d3b00,0x17fc100,0x1c04580,0x212cb80,0x2535000,0x2a5d600,0x2e65a80,0x338e080,0x38b6680,0x3cbeb00,0x41e7100,0x45ef580,0x4b0de00,0x4f16280,0x5314980,0x583cf80,0x5ae4d80,0x5eb5580,0x5eb5580,0x5eb5580,0x5eb5580,0x5eb5580,0x5eb5580,0x5eb5580,0x5eb5580,0x5eb5580,0x5eb5580,0x5eb5580,0x5eb5580,580,1572,2564,3372,4364,5172,6164,6972,7964,8772,9764,10756,11564,12556,13364,14341,15149,15942,16934,17460,18183,18183,18183,18183,21,20,0,0,0,0,0,0,0x47b500,0x9a3b00,0xecc100,0x12d4580,0x17fcb80,0x1c05000,0x212d600,0x2535a80,0x2a5e080,0x2e66500,0x338eb00,0x38b7100,0x3cbf580,0x41e7b80,0x45f0000,0x4b0e880,0x4f16d00,0x5315400,0x583da00,0x5ae5800,0x5eb6000,0x5eb6000,0x5eb6000,0x5eb6000,0x5eb6000,0x5eb6000,0x5eb6000,0x5eb6000,0x5eb6000,0x5eb6000,0x5eb6000,0x5eb6000,581,1573,2565,3373,4365,5173,6165,6973,7965,8773,9765,10757,11565,12557,13365,14342,15150,15943,16935,14152,14152,14152,14152,14152,20,19,0,0,0,0,0,0,0x47bf80,0x9a4580,0xeccb80,0x12d5000,0x17fd600,0x1c05a80,0x212e080,0x2536500,0x2a5eb00,0x2e66f80,0x338f580,0x38b7b80,0x3cc0000,0x41e8600,0x45f0a80,0x4b0f300,0x4f17780,0x5315e80,0x583e480,0x49bb400,0x49bb400,0x49bb400,0x49bb400,0x49bb400,0x49bb400,0x49bb400,0x49bb400,0x49bb400,0x49bb400,0x49bb400,0x49bb400,0x49bb400,582,1574,2566,3374,4366,5174,6166,6974,7966,8774,9766,10758,11566,12558,13366,14343,15151,16128,16936,14408,18184,18184,18184,18184,21,20,0,0,0,0,0,0,0x47ca00,0x9a5000,0xecd600,0x12d5a80,0x17fe080,0x1c06500,0x212eb00,0x2536f80,0x2a5f580,0x2e67a00,0x3390000,0x38b8600,0x3cc0a80,0x41e9080,0x45f1500,0x4b0fd80,0x4f18200,0x5436a80,0x583ef00,0x4b0a980,0x5eb6a80,0x5eb6a80,0x5eb6a80,0x5eb6a80,0x5eb6a80,0x5eb6a80,0x5eb6a80,0x5eb6a80,0x5eb6a80,0x5eb6a80,0x5eb6a80,0x5eb6a80,583,1575,2567,3375,4367,5175,6167,6975,7967,8775,9767,10759,11567,12559,13367,14344,15152,16129,16937,17461,18185,18185,18185,18185,21,20,0,0,0,0,0,0,0x47d480,0x9a5a80,0xece080,0x12d6500,0x17feb00,0x1c06f80,0x212f580,0x2537a00,0x2a60000,0x2e68480,0x3390a80,0x38b9080,0x3cc1500,0x41e9b00,0x45f1f80,0x4b10800,0x4f18c80,0x5437500,0x583f980,0x5ae6280,0x5eb7500,0x5eb7500,0x5eb7500,0x5eb7500,0x5eb7500,0x5eb7500,0x5eb7500,0x5eb7500,0x5eb7500,0x5eb7500,0x5eb7500,0x5eb7500,768,1576,2568,3376,4368,5176,6168,6976,7968,8960,9768,10760,11568,12560,13368,14345,15153,16130,16938,17462,18186,18186,18186,18186,21,20,0,0,0,0,0,0,0x59e080,0x9a6500,0xeceb00,0x12d6f80,0x17ff580,0x1c07a00,0x2130000,0x2538480,0x2a60a80,0x2f89080,0x3391500,0x38b9b00,0x3cc1f80,0x41ea580,0x45f2a00,0x4b11280,0x4f19700,0x5437f80,0x5840400,0x5ae6d00,0x5eb7f80,0x5eb7f80,0x5eb7f80,0x5eb7f80,0x5eb7f80,0x5eb7f80,0x5eb7f80,0x5eb7f80,0x5eb7f80,0x5eb7f80,0x5eb7f80,0x5eb7f80,769,1577,2569,3377,4369,5177,6169,6977,7969,8961,9769,10761,11569,12561,13369,14346,15154,16131,16939,14664,14664,14664,14664,14664,20,19,0,0,0,0,0,0,0x59eb00,0x9a6f80,0xecf580,0x12d7a00,0x1800000,0x1c08480,0x2130a80,0x2538f00,0x2a61500,0x2f89b00,0x3391f80,0x38ba580,0x3cc2a00,0x41eb000,0x45f3480,0x4b11d00,0x4f1a180,0x5438a00,0x5840e80,0x4c59f00,0x4c59f00,0x4c59f00,0x4c59f00,0x4c59f00,0x4c59f00,0x4c59f00,0x4c59f00,0x4c59f00,0x4c59f00,0x4c59f00,0x4c59f00,0x4c59f00,770,1578,2570,3378,4370,5178,6170,6978,7970,8962,9770,10762,11570,12562,13370,14347,15155,16132,16940,17463,18187,18243,18243,18243,22,21,0,0,0,0,0,0,0x59f580,0x9a7a00,0xed0000,0x12d8480,0x1800a80,0x1c08f00,0x2131500,0x2539980,0x2a61f80,0x2f8a580,0x3392a00,0x38bb000,0x3cc3480,0x41eba80,0x45f3f00,0x4b12780,0x4f1ac00,0x5439480,0x5841900,0x5ae7780,0x5eb8a00,0x5edd600,0x5edd600,0x5edd600,0x5edd600,0x5edd600,0x5edd600,0x5edd600,0x5edd600,0x5edd600,0x5edd600,0x5edd600,771,1579,2571,3379,4371,5179,6171,6979,7971,8963,9771,10763,11571,12563,13371,14348,15156,16133,16941,17464,18188,18188,18188,18188,21,20,0,0,0,0,0,0,0x5a0000,0x9a8480,0xed0a80,0x12d8f00,0x1801500,0x1c09980,0x2131f80,0x253a400,0x2a62a00,0x2f8b000,0x3393480,0x38bba80,0x3cc3f00,0x41ec500,0x45f4980,0x4b13200,0x4f1b680,0x5439f00,0x5842380,0x5ae8200,0x5eb9480,0x5eb9480,0x5eb9480,0x5eb9480,0x5eb9480,0x5eb9480,0x5eb9480,0x5eb9480,0x5eb9480,0x5eb9480,0x5eb9480,0x5eb9480,772,1580,2572,3380,4372,5180,6172,6980,7972,8964,9772,10764,11572,12564,13372,14349,15157,16134,16942,17465,18189,18189,18189,18189,21,20,0,0,0,0,0,0,0x5a0a80,0x9a8f00,0xed1500,0x12d9980,0x1801f80,0x1c0a400,0x2132a00,0x253ae80,0x2a63480,0x2f8ba80,0x3393f00,0x38bc500,0x3cc4980,0x41ecf80,0x45f5400,0x4b13c80,0x4f1c100,0x543a980,0x5842e00,0x5ae8c80,0x5eb9f00,0x5eb9f00,0x5eb9f00,0x5eb9f00,0x5eb9f00,0x5eb9f00,0x5eb9f00,0x5eb9f00,0x5eb9f00,0x5eb9f00,0x5eb9f00,0x5eb9f00,773,1581,2573,3381,4373,5181,6173,6981,7973,8965,9773,10765,11573,12565,13373,14350,15158,16135,16943,14920,14920,14920,14920,14920,20,19,0,0,0,0,0,0,0x5a1500,0x9a9980,0xed1f80,0x12da400,0x1802a00,0x1c0ae80,0x2133480,0x253b900,0x2a63f00,0x2f8c500,0x3394980,0x38bcf80,0x3cc5400,0x41eda00,0x45f5e80,0x4b14700,0x4f1cb80,0x543b400,0x5843880,0x4da9480,0x4da9480,0x4da9480,0x4da9480,0x4da9480,0x4da9480,0x4da9480,0x4da9480,0x4da9480,0x4da9480,0x4da9480,0x4da9480,0x4da9480,774,1582,2574,3382,4374,5182,6174,6982,7974,8966,9774,10766,11574,12566,13374,14351,15159,16136,16944,15176,18190,18190,18190,18190,21,20,0,0,0,0,0,0,0x5a1f80,0x9aa400,0xed2a00,0x12dae80,0x1803480,0x1c0b900,0x2133f00,0x253c380,0x2a64980,0x2f8cf80,0x3395400,0x38bda00,0x3cc5e80,0x41ee480,0x45f6900,0x4b15180,0x4f1d600,0x543be80,0x5844300,0x4ef8a00,0x5eba980,0x5eba980,0x5eba980,0x5eba980,0x5eba980,0x5eba980,0x5eba980,0x5eba980,0x5eba980,0x5eba980,0x5eba980,0x5eba980,775,1583,2575,3383,4375,5183,6175,6983,7975,8967,9775,10767,11575,12567,13375,14352,15160,16137,16945,17466,18191,18191,18191,18191,21,20,0,0,0,0,0,0,0x5a2a00,0x9aae80,0xed3480,0x12db900,0x1803f00,0x1c0c380,0x2134980,0x253ce00,0x2a65400,0x2f8da00,0x3395e80,0x38be480,0x3cc6900,0x41eef00,0x45f7380,0x4b15c00,0x4f1e080,0x543c900,0x5844d80,0x5ae9700,0x5ebb400,0x5ebb400,0x5ebb400,0x5ebb400,0x5ebb400,0x5ebb400,0x5ebb400,0x5ebb400,0x5ebb400,0x5ebb400,0x5ebb400,0x5ebb400,776,1584,2576,3384,4376,5184,6176,7168,7976,8968,9776,10768,11576,12568,13376,14353,15161,16138,16946,17467,18192,18192,18192,18192,21,20,0,0,0,0,0,0,0x5a3480,0x9ab900,0xed3f00,0x12dc380,0x1804980,0x1c0ce00,0x2135400,0x265da00,0x2a65e80,0x2f8e480,0x3396900,0x38bef00,0x3cc7380,0x41ef980,0x45f7e00,0x4b16680,0x4f1eb00,0x543d380,0x5845800,0x5aea180,0x5ebbe80,0x5ebbe80,0x5ebbe80,0x5ebbe80,0x5ebbe80,0x5ebbe80,0x5ebbe80,0x5ebbe80,0x5ebbe80,0x5ebbe80,0x5ebbe80,0x5ebbe80,777,1585,2577,3385,4377,5185,6177,7169,7977,8969,9777,10769,11577,12569,13377,14354,15162,16139,16947,15432,15432,15432,15432,15432,20,19,0,0,0,0,0,0,0x5a3f00,0x9ac380,0xed4980,0x12dce00,0x1805400,0x1c0d880,0x2135e80,0x265e480,0x2a66900,0x2f8ef00,0x3397380,0x38bf980,0x3cc7e00,0x41f0400,0x45f8880,0x4b17100,0x4f1f580,0x543de00,0x5846280,0x5047f80,0x5047f80,0x5047f80,0x5047f80,0x5047f80,0x5047f80,0x5047f80,0x5047f80,0x5047f80,0x5047f80,0x5047f80,0x5047f80,0x5047f80,778,1586,2578,3386,4378,5186,6178,7170,7978,8970,9778,10770,11578,12570,13378,14355,15163,16140,16948,17468,18193,18244,18244,18244,22,21,0,0,0,0,0,0,0x5a4980,0x9ace00,0xed5400,0x12dd880,0x1805e80,0x1c0e300,0x2136900,0x265ef00,0x2a67380,0x2f8f980,0x3397e00,0x38c0400,0x3cc8880,0x41f0e80,0x45f9300,0x4b17b80,0x4f20000,0x543e880,0x5846d00,0x5aeac00,0x5ebc900,0x5ede080,0x5ede080,0x5ede080,0x5ede080,0x5ede080,0x5ede080,0x5ede080,0x5ede080,0x5ede080,0x5ede080,0x5ede080,779,1587,2579,3387,4379,5187,6179,7171,7979,8971,9779,10771,11579,12571,13379,14356,15164,16141,16949,17469,18194,18194,18194,18194,21,20,0,0,0,0,0,0,0x5a5400,0x9ad880,0xed5e80,0x12de300,0x1806900,0x1c0ed80,0x2137380,0x265f980,0x2a67e00,0x2f90400,0x3398880,0x38c0e80,0x3cc9300,0x41f1900,0x45f9d80,0x4b18600,0x4f20a80,0x543f300,0x5847780,0x5aeb680,0x5ebd380,0x5ebd380,0x5ebd380,0x5ebd380,0x5ebd380,0x5ebd380,0x5ebd380,0x5ebd380,0x5ebd380,0x5ebd380,0x5ebd380,0x5ebd380,780,1588,2580,3388,4380,5188,6180,7172,7980,8972,9780,10772,11580,12572,13380,14357,15165,16142,16950,17470,18195,18195,18195,18195,21,20,0,0,0,0,0,0,0x5a5e80,0x9ae300,0xed6900,0x12ded80,0x1807380,0x1c0f800,0x2137e00,0x2660400,0x2a68880,0x2f90e80,0x3399300,0x38c1900,0x3cc9d80,0x41f2380,0x45fa800,0x4b19080,0x4f21500,0x543fd80,0x5848200,0x5aec100,0x5ebde00,0x5ebde00,0x5ebde00,0x5ebde00,0x5ebde00,0x5ebde00,0x5ebde00,0x5ebde00,0x5ebde00,0x5ebde00,0x5ebde00,0x5ebde00,781,1589,2581,3389,4381,5189,6181,7173,7981,8973,9781,10773,11581,12573,13381,14358,15166,16143,16951,15688,15688,15688,15688,15688,20,19,0,0,0,0,0,0,0x5a6900,0x9aed80,0xed7380,0x12df800,0x1807e00,0x1c10280,0x2138880,0x2660e80,0x2a69300,0x2f91900,0x3399d80,0x38c2380,0x3cca800,0x41f2e00,0x45fb280,0x4b19b00,0x4f21f80,0x5440800,0x5848c80,0x5197500,0x5197500,0x5197500,0x5197500,0x5197500,0x5197500,0x5197500,0x5197500,0x5197500,0x5197500,0x5197500,0x5197500,0x5197500,782,1590,2582,3390,4382,5190,6182,7174,7982,8974,9782,10774,11582,12574,13382,14359,15167,16144,16952,15944,18196,18196,18196,18196,21,20,0,0,0,0,0,0,0x5a7380,0x9af800,0xed7e00,0x12e0280,0x1808880,0x1c10d00,0x2139300,0x2661900,0x2a69d80,0x2f92380,0x339a800,0x38c2e00,0x3ccb280,0x41f3880,0x45fbd00,0x4b1a580,0x4f22a00,0x5441280,0x5849700,0x52e6a80,0x5ebe880,0x5ebe880,0x5ebe880,0x5ebe880,0x5ebe880,0x5ebe880,0x5ebe880,0x5ebe880,0x5ebe880,0x5ebe880,0x5ebe880,0x5ebe880,783,1591,2583,3391,4383,5191,6183,7175,7983,8975,9783,10775,11583,12575,13383,14360,15168,16145,16953,17471,18197,18197,18197,18197,21,20,0,0,0,0,0,0,0x5a7e00,0x9b0280,0xed8880,0x12e0d00,0x1809300,0x1c11780,0x2139d80,0x2662380,0x2a6a800,0x2f92e00,0x339b280,0x38c3880,0x3ccbd00,0x41f4300,0x45fc780,0x4b1b000,0x4f23480,0x5441d00,0x584a180,0x5aecb80,0x5ebf300,0x5ebf300,0x5ebf300,0x5ebf300,0x5ebf300,0x5ebf300,0x5ebf300,0x5ebf300,0x5ebf300,0x5ebf300,0x5ebf300,0x5ebf300,784,1592,2584,3392,4384,5376,6184,7176,7984,8976,9784,10776,11584,12576,13568,14361,15169,16146,16954,17472,18198,18198,18198,18198,21,20,0,0,0,0,0,0,0x5a8880,0x9b0d00,0xed9300,0x12e1780,0x1809d80,0x1d32380,0x213a800,0x2662e00,0x2a6b280,0x2f93880,0x339bd00,0x38c4300,0x3ccc780,0x41f4d80,0x471d380,0x4b1ba80,0x4f23f00,0x5442780,0x584ac00,0x5aed600,0x5ebfd80,0x5ebfd80,0x5ebfd80,0x5ebfd80,0x5ebfd80,0x5ebfd80,0x5ebfd80,0x5ebfd80,0x5ebfd80,0x5ebfd80,0x5ebfd80,0x5ebfd80,785,1593,2585,3393,4385,5377,6185,7177,7985,8977,9785,10777,11585,12577,13569,14362,15170,16147,16955,16200,16200,16200,16200,16200,20,19,0,0,0,0,0,0,0x5a9300,0x9b1780,0xed9d80,0x12e2200,0x180a800,0x1d32e00,0x213b280,0x2663880,0x2a6bd00,0x2f94300,0x339c780,0x38c4d80,0x3ccd200,0x41f5800,0x471de00,0x4b1c500,0x4f24980,0x5443200,0x584b680,0x5436000,0x5436000,0x5436000,0x5436000,0x5436000,0x5436000,0x5436000,0x5436000,0x5436000,0x5436000,0x5436000,0x5436000,0x5436000,786,1594,2586,3394,4386,5378,6186,7178,7986,8978,9786,10778,11586,12578,13570,14363,15171,16148,16956,17473,18199,18245,18245,18245,22,21,0,0,0,0,0,0,0x5a9d80,0x9b2200,0xeda800,0x12e2c80,0x180b280,0x1d33880,0x213bd00,0x2664300,0x2a6c780,0x2f94d80,0x339d200,0x38c5800,0x3ccdc80,0x41f6280,0x471e880,0x4b1cf80,0x4f25400,0x5443c80,0x584c100,0x5aee080,0x5ec0800,0x5edeb00,0x5edeb00,0x5edeb00,0x5edeb00,0x5edeb00,0x5edeb00,0x5edeb00,0x5edeb00,0x5edeb00,0x5edeb00,0x5edeb00,787,1595,2587,3395,4387,5379,6187,7179,7987,8979,9787,10779,11587,12579,13571,14364,15172,16149,16957,17474,18200,18200,18200,18200,21,20,0,0,0,0,0,0,0x5aa800,0x9b2c80,0xedb280,0x12e3700,0x180bd00,0x1d34300,0x213c780,0x2664d80,0x2a6d200,0x2f95800,0x339dc80,0x38c6280,0x3cce700,0x41f6d00,0x471f300,0x4b1da00,0x4f25e80,0x5444700,0x584cb80,0x5aeeb00,0x5ec1280,0x5ec1280,0x5ec1280,0x5ec1280,0x5ec1280,0x5ec1280,0x5ec1280,0x5ec1280,0x5ec1280,0x5ec1280,0x5ec1280,0x5ec1280,788,1596,2588,3396,4388,5380,6188,7180,7988,8980,9788,10780,11588,12580,13572,14365,15173,16150,16958,17475,18201,18201,18201,18201,21,20,0,0,0,0,0,0,0x5ab280,0x9b3700,0xedbd00,0x12e4180,0x180c780,0x1d34d80,0x213d200,0x2665800,0x2a6dc80,0x2f96280,0x339e700,0x38c6d00,0x3ccf180,0x41f7780,0x471fd80,0x4b1e480,0x4f26900,0x5445180,0x584d600,0x5aef580,0x5ec1d00,0x5ec1d00,0x5ec1d00,0x5ec1d00,0x5ec1d00,0x5ec1d00,0x5ec1d00,0x5ec1d00,0x5ec1d00,0x5ec1d00,0x5ec1d00,0x5ec1d00,789,1597,2589,3397,4389,5381,6189,7181,7989,8981,9789,10781,11589,12581,13573,14366,15174,16151,16959,16456,16456,16456,16456,16456,20,19,0,0,0,0,0,0,0x5abd00,0x9b4180,0xedc780,0x12e4c00,0x180d200,0x1d35800,0x213dc80,0x2666280,0x2a6e700,0x2f96d00,0x339f180,0x38c7780,0x3ccfc00,0x41f8200,0x4720800,0x4b1ef00,0x4f27380,0x5445c00,0x584e080,0x5585580,0x5585580,0x5585580,0x5585580,0x5585580,0x5585580,0x5585580,0x5585580,0x5585580,0x5585580,0x5585580,0x5585580,0x5585580,790,1598,2590,3398,4390,5382,6190,7182,7990,8982,9790,10782,11590,12582,13574,14367,15175,16152,16960,16712,18202,18202,18202,18202,21,20,0,0,0,0,0,0,0x5ac780,0x9b4c00,0xedd200,0x12e5680,0x180dc80,0x1d36280,0x213e700,0x2666d00,0x2a6f180,0x2f97780,0x339fc00,0x38c8200,0x3cd0680,0x41f8c80,0x4721280,0x4b1f980,0x4f27e00,0x5446680,0x584eb00,0x56d4b00,0x5ec2780,0x5ec2780,0x5ec2780,0x5ec2780,0x5ec2780,0x5ec2780,0x5ec2780,0x5ec2780,0x5ec2780,0x5ec2780,0x5ec2780,0x5ec2780,791,1599,2591,3399,4391,5383,6191,7183,7991,8983,9791,10783,11591,12583,13575,14368,15360,16153,16961,17476,18203,18203,18203,18203,21,20,0,0,0,0,0,0,0x5ad200,0x9b5680,0xeddc80,0x12e6100,0x180e700,0x1d36d00,0x213f180,0x2667780,0x2a6fc00,0x2f98200,0x33a0680,0x38c8c80,0x3cd1100,0x41f9700,0x4721d00,0x4b20400,0x5048a00,0x5447100,0x584f580,0x5af0000,0x5ec3200,0x5ec3200,0x5ec3200,0x5ec3200,0x5ec3200,0x5ec3200,0x5ec3200,0x5ec3200,0x5ec3200,0x5ec3200,0x5ec3200,0x5ec3200,792,1600,2592,3584,4392,5384,6192,7184,7992,8984,9792,10784,11776,12584,13576,14369,15361,16154,16962,17477,18204,18204,18204,18204,21,20,0,0,0,0,0,0,0x5adc80,0x9b6100,0xede700,0x1406d00,0x180f180,0x1d37780,0x213fc00,0x2668200,0x2a70680,0x2f98c80,0x33a1100,0x38c9700,0x3df1d00,0x41fa180,0x4722780,0x4b20e80,0x5049480,0x5447b80,0x5850000,0x5af0a80,0x5ec3c80,0x5ec3c80,0x5ec3c80,0x5ec3c80,0x5ec3c80,0x5ec3c80,0x5ec3c80,0x5ec3c80,0x5ec3c80,0x5ec3c80,0x5ec3c80,0x5ec3c80,793,1601,2593,3585,4393,5385,6193,7185,7993,8985,9793,10785,11777,12585,13577,14370,15362,16155,16963,16968,16968,16968,16968,16968,20,19,0,0,0,0,0,0,0x5ae700,0x9b6b80,0xedf180,0x1407780,0x180fc00,0x1d38200,0x2140680,0x2668c80,0x2a71100,0x2f99700,0x33a1b80,0x38ca180,0x3df2780,0x41fac00,0x4723200,0x4b21900,0x5049f00,0x5448600,0x5850a80,0x5824080,0x5824080,0x5824080,0x5824080,0x5824080,0x5824080,0x5824080,0x5824080,0x5824080,0x5824080,0x5824080,0x5824080,0x5824080,794,1602,2594,3586,4394,5386,6194,7186,7994,8986,9794,10786,11778,12586,13578,14371,15363,16156,16964,17478,18205,18246,18246,18246,22,21,0,0,0,0,0,0,0x5af180,0x9b7600,0xedfc00,0x1408200,0x1810680,0x1d38c80,0x2141100,0x2669700,0x2a71b80,0x2f9a180,0x33a2600,0x38cac00,0x3df3200,0x41fb680,0x4723c80,0x4b22380,0x504a980,0x5449080,0x5851500,0x5af1500,0x5ec4700,0x5edf580,0x5edf580,0x5edf580,0x5edf580,0x5edf580,0x5edf580,0x5edf580,0x5edf580,0x5edf580,0x5edf580,0x5edf580,795,1603,2595,3587,4395,5387,6195,7187,7995,8987,9795,10787,11779,12587,13579,14372,15364,16157,16965,17479,18206,18206,18206,18206,21,20,0,0,0,0,0,0,0x5afc00,0x9b8080,0xee0680,0x1408c80,0x1811100,0x1d39700,0x2141b80,0x266a180,0x2a72600,0x2f9ac00,0x33a3080,0x38cb680,0x3df3c80,0x41fc100,0x4724700,0x4b22e00,0x504b400,0x5449b00,0x5851f80,0x5af1f80,0x5ec5180,0x5ec5180,0x5ec5180,0x5ec5180,0x5ec5180,0x5ec5180,0x5ec5180,0x5ec5180,0x5ec5180,0x5ec5180,0x5ec5180,0x5ec5180,796,1604,2596,3588,4396,5388,6196,7188,7996,8988,9796,10788,11780,12588,13580,14373,15365,16158,16966,17664,18207,18207,18207,18207,21,20,0,0,0,0,0,0,0x5b0680,0x9b8b00,0xee1100,0x1409700,0x1811b80,0x1d3a180,0x2142600,0x266ac00,0x2a73080,0x2f9b680,0x33a3b00,0x38cc100,0x3df4700,0x41fcb80,0x4725180,0x4b23880,0x504be80,0x544a580,0x5852a00,0x5c12b80,0x5ec5c00,0x5ec5c00,0x5ec5c00,0x5ec5c00,0x5ec5c00,0x5ec5c00,0x5ec5c00,0x5ec5c00,0x5ec5c00,0x5ec5c00,0x5ec5c00,0x5ec5c00,797,1605,2597,3589,4397,5389,6197,7189,7997,8989,9797,10789,11781,12589,13581,14374,15366,16159,16967,17224,17224,17224,17224,17224,20,19,0,0,0,0,0,0,0x5b1100,0x9b9580,0xee1b80,0x140a180,0x1812600,0x1d3ac00,0x2143080,0x266b680,0x2a73b00,0x2f9c100,0x33a4580,0x38ccb80,0x3df5180,0x41fd600,0x4725c00,0x4b24300,0x504c900,0x544b000,0x5853480,0x5973600,0x5973600,0x5973600,0x5973600,0x5973600,0x5973600,0x5973600,0x5973600,0x5973600,0x5973600,0x5973600,0x5973600,0x5973600,798,1606,2598,3590,4398,5390,6198,7190,7998,8990,9798,10790,11782,12590,13582,14375,15367,16160,17152,17480,18208,18208,18208,18208,21,20,0,0,0,0,0,0,0x5b1b80,0x9ba000,0xee2600,0x140ac00,0x1813080,0x1d3b680,0x2143b00,0x266c100,0x2a74580,0x2f9cb80,0x33a5000,0x38cd600,0x3df5c00,0x41fe080,0x4726680,0x4b24d80,0x504d380,0x544ba80,0x5974080,0x5ac2b80,0x5ec6680,0x5ec6680,0x5ec6680,0x5ec6680,0x5ec6680,0x5ec6680,0x5ec6680,0x5ec6680,0x5ec6680,0x5ec6680,0x5ec6680,0x5ec6680,799,1607,2599,3591,4399,5391,6199,7191,7999,8991,9799,10791,11783,12591,13583,14376,15368,16161,17153,17665,18209,18209,18209,18209,21,20,0,0,0,0,0,0,0x5b2600,0x9baa80,0xee3080,0x140b680,0x1813b00,0x1d3c100,0x2144580,0x266cb80,0x2a75000,0x2f9d600,0x33a5a80,0x38ce080,0x3df6680,0x41feb00,0x4727100,0x4b25800,0x504de00,0x544c500,0x5974b00,0x5c13600,0x5ec7100,0x5ec7100,0x5ec7100,0x5ec7100,0x5ec7100,0x5ec7100,0x5ec7100,0x5ec7100,0x5ec7100,0x5ec7100,0x5ec7100,0x5ec7100,800,1792,2600,3592,4400,5392,6200,7192,8000,8992,9984,10792,11784,12592,13584,14377,15369,16162,17154,17666,18210,18210,18210,18210,21,20,0,0,0,0,0,0,0x5b3080,0xadb680,0xee3b00,0x140c100,0x1814580,0x1d3cb80,0x2145000,0x266d600,0x2a75a80,0x2f9e080,0x34c6680,0x38ceb00,0x3df7100,0x41ff580,0x4727b80,0x4b26280,0x504e880,0x544cf80,0x5975580,0x5c14080,0x5ec7b80,0x5ec7b80,0x5ec7b80,0x5ec7b80,0x5ec7b80,0x5ec7b80,0x5ec7b80,0x5ec7b80,0x5ec7b80,0x5ec7b80,0x5ec7b80,0x5ec7b80,801,1793,2601,3593,4401,5393,6201,7193,8001,8993,9985,10793,11785,12593,13585,14378,15370,16163,17155,17736,17736,17736,17736,17736,20,19,0,0,0,0,0,0,0x5b3b00,0xadc100,0xee4580,0x140cb80,0x1815000,0x1d3d600,0x2145a80,0x266e080,0x2a76500,0x2f9eb00,0x34c7100,0x38cf580,0x3df7b80,0x4200000,0x4728600,0x4b26d00,0x504f300,0x544da00,0x5976000,0x5c12100,0x5c12100,0x5c12100,0x5c12100,0x5c12100,0x5c12100,0x5c12100,0x5c12100,0x5c12100,0x5c12100,0x5c12100,0x5c12100,0x5c12100,802,1794,2602,3594,4402,5394,6202,7194,8002,8994,9986,10794,11786,12594,13586,14379,15371,16164,17156,17667,18211,18247,18247,18247,22,21,0,0,0,0,0,0,0x5b4580,0xadcb80,0xee5000,0x140d600,0x1815a80,0x1d3e080,0x2146500,0x266eb00,0x2a76f80,0x2f9f580,0x34c7b80,0x38d0000,0x3df8600,0x4200a80,0x4729080,0x4b27780,0x504fd80,0x544e480,0x5976a80,0x5c14b00,0x5ec8600,0x5ee0000,0x5ee0000,0x5ee0000,0x5ee0000,0x5ee0000,0x5ee0000,0x5ee0000,0x5ee0000,0x5ee0000,0x5ee0000,0x5ee0000,803,1795,2603,3595,4403,5395,6203,7195,8003,8995,9987,10795,11787,12595,13587,14380,15372,16165,17157,17668,18212,18212,18212,18212,21,20,0,0,0,0,0,0,0x5b5000,0xadd600,0xee5a80,0x140e080,0x1816500,0x1d3eb00,0x2146f80,0x266f580,0x2a77a00,0x2fa0000,0x34c8600,0x38d0a80,0x3df9080,0x4201500,0x4729b00,0x4b28200,0x5050800,0x544ef00,0x5977500,0x5c15580,0x5ec9080,0x5ec9080,0x5ec9080,0x5ec9080,0x5ec9080,0x5ec9080,0x5ec9080,0x5ec9080,0x5ec9080,0x5ec9080,0x5ec9080,0x5ec9080,804,1796,2604,3596,4404,5396,6204,7196,8004,8996,9988,10796,11788,12596,13588,14381,15373,16166,17158,17669,18213,18213,18213,18213,21,20,0,0,0,0,0,0,0x5b5a80,0xade080,0xee6500,0x140eb00,0x1816f80,0x1d3f580,0x2147a00,0x2670000,0x2a78480,0x2fa0a80,0x34c9080,0x38d1500,0x3df9b00,0x4201f80,0x472a580,0x4b28c80,0x5051280,0x544f980,0x5977f80,0x5c16000,0x5ec9b00,0x5ec9b00,0x5ec9b00,0x5ec9b00,0x5ec9b00,0x5ec9b00,0x5ec9b00,0x5ec9b00,0x5ec9b00,0x5ec9b00,0x5ec9b00,0x5ec9b00,805,1797,2605,3597,4405,5397,6205,7197,8005,8997,9989,10797,11789,12597,13589,14382,15374,16167,17159,17992,17992,17992,17992,17992,20,19,0,0,0,0,0,0,0x5b6500,0xadeb00,0xee6f80,0x140f580,0x1817a00,0x1d40000,0x2148480,0x2670a80,0x2a78f00,0x2fa1500,0x34c9b00,0x38d1f80,0x3dfa580,0x4202a00,0x472b000,0x4b29700,0x5051d00,0x5450400,0x5978a00,0x5d61680,0x5d61680,0x5d61680,0x5d61680,0x5d61680,0x5d61680,0x5d61680,0x5d61680,0x5d61680,0x5d61680,0x5d61680,0x5d61680,0x5d61680,806,1798,2606,3598,4406,5398,6206,7198,8006,8998,9990,10798,11790,12598,13590,14383,15375,16168,17160,18248,18214,18214,18214,18214,21,20,0,0,0,0,0,0,0x5b6f80,0xadf580,0xee7a00,0x1410000,0x1818480,0x1d40a80,0x2148f00,0x2671500,0x2a79980,0x2fa1f80,0x34ca580,0x38d2a00,0x3dfb000,0x4203480,0x472ba80,0x4b2a180,0x5052780,0x5450e80,0x5979480,0x5eb0c00,0x5eca580,0x5eca580,0x5eca580,0x5eca580,0x5eca580,0x5eca580,0x5eca580,0x5eca580,0x5eca580,0x5eca580,0x5eca580,0x5eca580,807,1799,2607,3599,4407,5399,6207,7199,8007,8999,9991,10799,11791,12599,13591,14384,15376,16169,17161,17670,18215,18215,18215,18215,21,20,0,0,0,0,0,0,0x5b7a00,0xae0000,0xee8480,0x1410a80,0x1818f00,0x1d41500,0x2149980,0x2671f80,0x2a7a400,0x2fa2a00,0x34cb000,0x38d3480,0x3dfba80,0x4203f00,0x472c500,0x4b2ac00,0x5053200,0x5451900,0x5979f00,0x5c16a80,0x5ecb000,0x5ecb000,0x5ecb000,0x5ecb000,0x5ecb000,0x5ecb000,0x5ecb000,0x5ecb000,0x5ecb000,0x5ecb000,0x5ecb000,0x5ecb000};

__global__ __launch_bounds__(256, 1) void swin_main(const uint16_t* x, const uint16_t* mask, char* ws, uint16_t* out,
                                                    const uint16_t* wqkv, const uint16_t* wo, const uint16_t* ls,
                                                    const uint16_t* w1, const uint16_t* w2, const uint16_t* rc,
                                                    const int* ridx) {
  __shared__ __attribute__((aligned(16))) char smem[LDS_BYTES];
  const int wave = __builtin_amdgcn_readfirstlane(threadIdx.x >> 6);
  const unsigned lds0 = (unsigned)(size_t)smem;
  asm volatile(
      ".p2align 5\n"
      "s_load_dwordx8 s[16:23], %0, 0\n"
      "s_load_dwordx4 s[4:7], %0, 32\n"
      "s_load_dwordx8 s[80:87], %0, 48\n"
      "s_load_dwordx2 s[90:91], %0, 80\n"
      "s_mov_b32 s24, %1\n"
      "s_mov_b32 s25, %2\n"
      "s_mov_b64 s[60:61], %3\n"
      "s_getpc_b64 s[12:13]\n"
      "L_ipf0_%=:\n"
      "v_mbcnt_lo_u32_b32 v136, -1, 0\n"
      "v_mbcnt_hi_u32_b32 v136, -1, v136\n"
      "s_lshr_b32 s28, s25, 3\n"
      "s_lshl_b32 s29, s28, 2\n"
      "s_add_u32 s29, s29, s24\n"
      "s_mul_i32 s29, s29, 14\n"
      "v_min_u32_e64 v137, 13, v136\n"
      "v_add_lshl_u32 v137, v137, s29, 7\n"
      "v_min_u32 v137, 221056, v137\n"
      "v_lshlrev_b32_e64 v138, 4, v136\n"
      "s_lshl_b32 s29, s24, 6\n"
      "s_add_u32 s54, s60, s29\n"
      "s_addc_u32 s55, s61, 0\n"
      "s_and_b32 s29, s29, 64\n"
      "v_xor_b32_e64 v139, s29, v138\n"
      "s_mul_i32 s30, s24, 9216\n"
      "v_add_u32_e64 v139, s30, v139\n"
      "s_add_u32 s2, s30, 36864\n"
      "v_xor_b32 v140, 32, v139\n"
      "s_mov_b64 s[34:35], 0xffff\n"
      "v_cndmask_b32 v141, v140, v139, s[34:35]\n"
      "s_lshl_b64 s[34:35], s[34:35], 48\n"
      "v_cndmask_b32 v142, v139, v140, s[34:35]\n"
      "s_lshl_b64 s[34:35], -1, 32\n"
      "v_cndmask_b32 v143, v140, v139, s[34:35]\n"
      "s_waitcnt lgkmcnt(0)\n"
      "global_load_dword v31, v137, s[4:5]\n"
      "s_add_u32 m0, s2, 0\n"
      "s_nop 0\n"
      "global_load_lds_dwordx4 v139, s[4:5]\n"
      "global_load_lds_dwordx4 v139, s[4:5] offset:1024\n"
      "global_load_lds_dwordx4 v141, s[4:5] offset:2048\n"
      "global_load_lds_dwordx4 v140, s[4:5] offset:3072\n"
      "s_add_u32 s4, s4, 4096\n"
      "s_addc_u32 s5, s5, 0\n"
      "s_add_u32 m0, s2, 4096\n"
      "s_nop 0\n"
      "global_load_lds_dwordx4 v143, s[4:5]\n"
      "global_load_lds_dwordx4 v139, s[4:5] offset:1024\n"
      "global_load_lds_dwordx4 v142, s[4:5] offset:2048\n"
      "global_load_lds_dwordx4 v140, s[4:5] offset:3072\n"
      "s_add_u32 s4, s4, 4096\n"
      "s_addc_u32 s5, s5, 0\n"
      "s_add_u32 m0, s2, 8192\n"
      "s_nop 0\n"
      "global_load_lds_dwordx4 v140, s[4:5]\n"
      "s_add_u32 s4, s4, 28672\n"
      "s_addc_u32 s5, s5, 0\n"
      "s_load_dwordx16 s[36:51], s[82:83], 0\n"
      "s_load_dwordx2 s[8:9], s[80:81], 0\n"
      "s_load_dwordx16 s[64:79], s[84:85], 0\n"
      "s_lshl_b32 s29, s24, 8\n"
      "v_lshl_add_u32 v144, v136, 2, s29\n"
      "v_min_u32 v144, 672, v144\n"
      "v_add_u32 v145, 150640, v144\n"
      "s_lshl_b32 s29, s28, 2\n"
      "s_add_u32 s29, s29, s24\n"
      "s_mov_b64 s[34:35], s[16:17]\n"
      "s_mov_b32 s30, s29\n"
      "s_sub_u32 s31, s29, 48\n"
      "s_cmp_ge_u32 s29, 48\n"
      "s_cselect_b64 s[34:35], s[22:23], s[34:35]\n"
      "s_cselect_b32 s30, s31, s30\n"
      "s_sub_u32 s31, s29, 96\n"
      "s_cmp_ge_u32 s29, 96\n"
      "s_cselect_b64 s[34:35], s[18:19], s[34:35]\n"
      "s_cselect_b32 s30, s31, s30\n"
      "s_sub_u32 s31, s29, 54\n"
      "s_cmp_ge_u32 s29, 109\n"
      "s_cselect_b64 s[34:35], s[20:21], s[34:35]\n"
      "s_cselect_b32 s30, s31, s30\n"
      "s_lshl_b32 s30, s30, 21\n"
      "s_load_dword s100, s[34:35], s30\n"
      "s_and_b32 s29, s29, 7\n"
      "s_cmp_eq_u32 s29, 0\n"
      "s_cselect_b64 s[34:35], s[6:7], s[34:35]\n"
      "s_cmp_eq_u32 s29, 1\n"
      "s_cselect_b64 s[34:35], s[90:91], s[34:35]\n"
      "s_cmp_eq_u32 s29, 2\n"
      "s_cselect_b64 s[34:35], s[86:87], s[34:35]\n"
      "s_cmp_eq_u32 s29, 3\n"
      "s_cselect_b64 s[34:35], s[60:61], s[34:35]\n"
      "s_cmp_eq_u32 s29, 4\n"
      "s_cselect_b64 s[34:35], s[12:13], s[34:35]\n"
      "s_load_dword s100, s[34:35], 0\n"
      "s_add_u32 m0, s2, 36864\n"
      "s_nop 0\n"
      "global_load_lds_dwordx4 v139, s[4:5]\n"
      "global_load_lds_dwordx4 v139, s[4:5] offset:1024\n"
      "global_load_lds_dwordx4 v141, s[4:5] offset:2048\n"
      "global_load_lds_dwordx4 v140, s[4:5] offset:3072\n"
      "s_add_u32 s4, s4, 4096\n"
      "s_addc_u32 s5, s5, 0\n"
      "s_add_u32 m0, s2, 40960\n"
      "s_nop 0\n"
      "global_load_lds_dwordx4 v143, s[4:5]\n"
      "global_load_lds_dwordx4 v139, s[4:5] offset:1024\n"
      "global_load_lds_dwordx4 v142, s[4:5] offset:2048\n"
      "global_load_lds_dwordx4 v140, s[4:5] offset:3072\n"
      "s_add_u32 s4, s4, 4096\n"
      "s_addc_u32 s5, s5, 0\n"
      "s_add_u32 m0, s2, 45056\n"
      "s_nop 0\n"
      "global_load_lds_dwordx4 v140, s[4:5]\n"
      "s_add_u32 s4, s4, 32768\n"
      "s_addc_u32 s5, s5, 0\n"
      "v_add_u32 v146, 4096, v140\n"
      "v_add_u32 v147, s2, v138\n"
      "v_lshrrev_b32 v148, 2, v136\n"
      "global_load_dwordx4 a[72:75], v139, s[4:5] offset:-4096\n"
      "v_and_b32 v149, 3, v148\n"
      "v_lshrrev_b32_e64 v150, 4, v136\n"
      "global_load_dwordx4 a[76:79], v139, s[4:5] offset:-3072\n"
      "v_lshl_add_u32 v149, v150, 3, v149\n"
      "v_mul_u32_u24 v149, 1152, v149\n"
      "global_load_dwordx4 a[80:83], v141, s[4:5] offset:-2048\n"
      "v_and_b32_e64 v150, 1, v136\n"
      "v_lshl_add_u32 v149, v150, 3, v149\n"
      "global_load_dwordx4 a[84:87], v140, s[4:5] offset:-1024\n"
      "v_add_u32 v149, 36864, v149\n"
      "v_and_b32_e64 v148, 6, v148\n"
      "global_load_dwordx4 a[88:91], v143, s[4:5]\n"
      "v_bfe_u32 v150, v136, 1, 1\n"
      "s_mul_i32 s29, s24, 6\n"
      "global_load_dwordx4 a[92:95], v139, s[4:5] offset:1024\n"
      "v_add_u32 v150, s29, v150\n"
      "v_xor_b32_e64 v151, v150, v148\n"
      "global_load_dwordx4 a[96:99], v142, s[4:5] offset:2048\n"
      "v_lshl_add_u32 v151, v151, 4, v149\n"
      "v_add_u32_e64 v150, 2, v150\n"
      "v_xor_b32_e64 v152, v150, v148\n"
      "global_load_dwordx4 a[100:103], v140, s[4:5] offset:3072\n"
      "v_lshl_add_u32 v152, v152, 4, v149\n"
      "v_add_u32_e64 v150, 2, v150\n"
      "global_load_dwordx4 a[104:107], v146, s[4:5]\n"
      "v_xor_b32_e64 v153, v150, v148\n"
      "v_lshl_add_u32 v153, v153, 4, v149\n"
      "s_movk_i32 s29, 18\n"
      "s_mov_b32 s30, 73600\n"
      "s_cmp_eq_u32 s24, 1\n"
      "v_add_u32 v139, 36864, v139\n"
      "v_add_u32 v140, 36864, v140\n"
      "v_add_u32 v141, 36864, v141\n"
      "v_add_u32 v143, 36864, v143\n"
      "v_add_u32 v142, 36864, v142\n"
      "v_add_u32 v146, 36864, v146\n"
      "global_load_dwordx4 a[108:111], v139, s[4:5] offset:-4096\n"
      "s_cselect_b64 s[12:13], s[6:7], s[12:13]\n"
      "s_cmp_eq_u32 s24, 2\n"
      "s_cselect_b32 s29, 16, s29\n"
      "s_cselect_b32 s30, L_cend_%=-L_ipf0_%=-128, s30\n"
      "s_cmp_eq_u32 s24, 3\n"
      "global_load_dwordx4 a[112:115], v139, s[4:5] offset:-3072\n"
      "s_cselect_b64 s[12:13], s[60:61], s[12:13]\n"
      "s_cselect_b32 s29, 10, s29\n"
      "s_cselect_b32 s30, 40832, s30\n"
      "global_load_dwordx4 a[116:119], v141, s[4:5] offset:-2048\n"
      "s_cmp_eq_u32 s24, 0\n"
      "s_cselect_b64 s[12:13], s[90:91], s[12:13]\n"
      "global_load_dwordx4 a[120:123], v140, s[4:5] offset:-1024\n"
      "s_cselect_b32 s29, 3, s29\n"
      "s_cselect_b32 s30, 9476, s30\n"
      "global_load_dwordx4 a[124:127], v143, s[4:5]\n"
      "s_mul_i32 s28, s28, s29\n"
      "s_sub_u32 s29, s29, 1\n"
      "global_load_dwordx4 a[128:131], v139, s[4:5] offset:1024\n"
      "v_min_u32 v137, s29, v136\n"
      "v_add_lshl_u32 v137, v137, s28, 7\n"
      "global_load_dwordx4 a[132:135], v142, s[4:5] offset:2048\n"
      "v_min_u32_e64 v137, s30, v137\n"
      "s_mul_i32 s28, s24, 10240\n"
      "global_load_dwordx4 a[136:139], v140, s[4:5] offset:3072\n"
      "s_add_u32 s28, s28, 4096\n"
      "v_add_u32 v154, s28, v138\n"
      "global_load_dwordx4 a[140:143], v146, s[4:5]\n"
      "v_add_u32 v155, 8192, v154\n"
      "global_load_dword v144, v144, s[86:87]\n"
      "s_mov_b64 s[56:57], s[90:91]\n"
      "s_load_dwordx16 s[84:99], s[84:85], 64\n"
      "s_lshl_b32 s29, s25, 8\n"
      "s_add_u32 s52, s60, s29\n"
      "s_addc_u32 s53, s61, 0\n"
      "s_lshl_b32 s29, s24, 5\n"
      "v_lshrrev_b32_e64 v149, 3, v136\n"
      "v_lshl_add_u32 v148, v149, 2, s29\n"
      "v_add_u32 v148, 41408, v148\n"
      "global_load_dword v148, v148, s[52:53]\n"
      "global_load_dword v30, v137, s[12:13]\n"
      "v_xor_b32 v149, v149, v136\n"
      "v_and_b32 v149, 7, v149\n"
      "s_waitcnt vmcnt(21)\n"
      "s_barrier\n"
      "ds_read_b64_tr_b16 a[0:1], v151\n"
      "ds_read_b64_tr_b16 a[2:3], v151 offset:4608\n"
      "ds_read_b64_tr_b16 a[4:5], v152\n"
      "ds_read_b64_tr_b16 a[6:7], v152 offset:4608\n"
      "ds_read_b64_tr_b16 a[8:9], v153\n"
      "ds_read_b64_tr_b16 a[10:11], v153 offset:4608\n"
      "ds_read_b64_tr_b16 a[12:13], v151 offset:384\n"
      "ds_read_b64_tr_b16 a[14:15], v151 offset:4992\n"
      "ds_read_b64_tr_b16 a[16:17], v152 offset:384\n"
      "v_add_u32 v139, 36864, v139\n"
      "v_add_u32 v140, 36864, v140\n"
      "v_add_u32 v141, 36864, v141\n"
      "v_add_u32 v143, 36864, v143\n"
      "v_add_u32 v142, 36864, v142\n"
      "v_add_u32 v146, 36864, v146\n"
      "global_load_dwordx4 a[144:147], v139, s[4:5] offset:-4096\n"
      "ds_read_b64_tr_b16 a[18:19], v152 offset:4992\n"
      "ds_read_b64_tr_b16 a[20:21], v153 offset:384\n"
      "ds_read_b64_tr_b16 a[22:23], v153 offset:4992\n"
      "ds_read_b64_tr_b16 a[24:25], v151 offset:768\n"
      "ds_read_b64_tr_b16 a[26:27], v151 offset:5376\n"
      "ds_read_b64_tr_b16 a[28:29], v152 offset:768\n"
      "ds_read_b64_tr_b16 a[30:31], v152 offset:5376\n"
      "ds_read_b64_tr_b16 a[32:33], v153 offset:768\n"
      "ds_read_b64_tr_b16 a[34:35], v153 offset:5376\n"
      "ds_read_b64_tr_b16 a[36:37], v151 offset:36864\n"
      "ds_read_b64_tr_b16 a[38:39], v151 offset:41472\n"
      "global_load_dwordx4 a[148:151], v139, s[4:5] offset:-3072\n"
      "ds_read_b64_tr_b16 a[40:41], v152 offset:36864\n"
      "ds_read_b64_tr_b16 a[42:43], v152 offset:41472\n"
      "ds_read_b64_tr_b16 a[44:45], v153 offset:36864\n"
      "ds_read_b64_tr_b16 a[46:47], v153 offset:41472\n"
      "global_load_dwordx4 a[152:155], v141, s[4:5] offset:-2048\n"
      "ds_read_b64_tr_b16 a[48:49], v151 offset:37248\n"
      "ds_read_b64_tr_b16 a[50:51], v151 offset:41856\n"
      "ds_read_b64_tr_b16 a[52:53], v152 offset:37248\n"
      "ds_read_b64_tr_b16 a[54:55], v152 offset:41856\n"
      "global_load_dwordx4 a[156:159], v140, s[4:5] offset:-1024\n"
      "ds_read_b64_tr_b16 a[56:57], v153 offset:37248\n"
      "ds_read_b64_tr_b16 a[58:59], v153 offset:41856\n"
      "ds_read_b64_tr_b16 a[60:61], v151 offset:37632\n"
      "ds_read_b64_tr_b16 a[62:63], v151 offset:42240\n"
      "global_load_dwordx4 a[160:163], v143, s[4:5]\n"
      "ds_read_b64_tr_b16 a[64:65], v152 offset:37632\n"
      "ds_read_b64_tr_b16 a[66:67], v152 offset:42240\n"
      "ds_read_b64_tr_b16 a[68:69], v153 offset:37632\n"
      "ds_read_b64_tr_b16 a[70:71], v153 offset:42240\n"
      "s_waitcnt lgkmcnt(0)\n"
      "s_pack_ll_b32_b16 s28, s36, s44\n"
      "s_waitcnt vmcnt(7)\n"
      "v_dot2_f32_bf16 v150, s28, v144, 0\n"
      "s_pack_hh_b32_b16 s29, s36, s44\n"
      "v_dot2_f32_bf16 v136, s29, v144, 0\n"
      "s_pack_ll_b32_b16 s30, s37, s45\n"
      "v_dot2_f32_bf16 v137, s30, v144, 0\n"
      "s_pack_hh_b32_b16 s31, s37, s45\n"
      "v_dot2_f32_bf16 v156, s31, v144, 0\n"
      "s_pack_ll_b32_b16 s28, s38, s46\n"
      "v_dot2_f32_bf16 v157, s28, v144, 0\n"
      "s_pack_hh_b32_b16 s29, s38, s46\n"
      "v_dot2_f32_bf16 v158, s29, v144, 0\n"
      "s_pack_ll_b32_b16 s30, s39, s47\n"
      "v_dot2_f32_bf16 v159, s30, v144, 0\n"
      "s_pack_hh_b32_b16 s31, s39, s47\n"
      "v_dot2_f32_bf16 v160, s31, v144, 0\n"
      "s_pack_ll_b32_b16 s28, s40, s48\n"
      "v_dot2_f32_bf16 v161, s28, v144, 0\n"
      "s_waitcnt vmcnt(6)\n"
      "v_lshl_add_u32 v148, v149, 4, v148\n"
      "s_lshl_b32 s10, s24, 10\n"
      "s_add_u32 m0, s10, 18432\n"
      "s_nop 0\n"
      "global_load_lds_dwordx4 v148, s[16:17]\n"
      "s_add_u32 m0, s10, 24448\n"
      "s_nop 0\n"
      "global_load_lds_dwordx4 v148, s[16:17] offset:128\n"
      "s_add_u32 m0, s10, 30464\n"
      "s_nop 0\n"
      "global_load_lds_dwordx4 v148, s[16:17] offset:256\n"
      "v_add_u32_e64 v149, s10, v138\n"
      "s_add_u32 m0, s10, 124928\n"
      "s_nop 0\n"
      "global_load_lds_dwordx4 v149, s[56:57]\n"
      "v_add_u32 v149, 4096, v149\n"
      "s_add_u32 m0, s10, 129024\n"
      "s_nop 0\n"
      "global_load_lds_dwordx4 v149, s[56:57]\n"
      "v_add_u32 v149, 4096, v149\n"
      "v_min_u32 v149, 9600, v149\n"
      "s_add_u32 m0, s10, 133120\n"
      "s_nop 0\n"
      "global_load_lds_dwordx4 v149, s[56:57]\n"
      "s_pack_hh_b32_b16 s29, s40, s48\n"
      "v_dot2_f32_bf16 v138, s29, v144, 0\n"
      "s_pack_ll_b32_b16 s30, s41, s49\n"
      "v_dot2_f32_bf16 v149, s30, v144, 0\n"
      "s_pack_hh_b32_b16 s31, s41, s49\n"
      "v_dot2_f32_bf16 v162, s31, v144, 0\n"
      "s_pack_ll_b32_b16 s28, s42, s50\n"
      "v_dot2_f32_bf16 v163, s28, v144, 0\n"
      "s_pack_hh_b32_b16 s29, s42, s50\n"
      "v_dot2_f32_bf16 v164, s29, v144, 0\n"
      "s_pack_ll_b32_b16 s30, s43, s51\n"
      "v_dot2_f32_bf16 v165, s30, v144, 0\n"
      "s_pack_hh_b32_b16 s31, s43, s51\n"
      "v_dot2_f32_bf16 v166, s31, v144, 0\n"
      "s_load_dwordx8 s[36:43], s[60:61], 41216\n"
      "s_load_dwordx4 s[44:47], s[60:61], 41248\n"
      "s_mov_b64 s[48:49], s[60:61]\n"
      "v_max_f32 v150, 0, v150\n"
      "v_max_f32 v136, 0, v136\n"
      "v_cvt_pk_bf16_f32 v150, v150, v136\n"
      "v_max_f32 v137, 0, v137\n"
      "v_max_f32 v156, 0, v156\n"
      "global_load_dwordx4 v[114:117], v154, s[60:61] offset:-4096\n"
      "v_cvt_pk_bf16_f32 v137, v137, v156\n"
      "v_max_f32 v157, 0, v157\n"
      "v_max_f32_e64 v158, 0, v158\n"
      "v_cvt_pk_bf16_f32 v157, v157, v158\n"
      "v_max_f32 v159, 0, v159\n"
      "global_load_dwordx4 a[164:167], v139, s[4:5] offset:1024\n"
      "v_max_f32 v160, 0, v160\n"
      "v_cvt_pk_bf16_f32 v159, v159, v160\n"
      "v_max_f32 v161, 0, v161\n"
      "v_max_f32 v138, 0, v138\n"
      "global_load_dwordx4 a[168:171], v142, s[4:5] offset:2048\n"
      "v_cvt_pk_bf16_f32 v161, v161, v138\n"
      "v_max_f32 v149, 0, v149\n"
      "v_max_f32 v162, 0, v162\n"
      "v_cvt_pk_bf16_f32 v149, v149, v162\n"
      "global_load_dwordx4 a[172:175], v140, s[4:5] offset:3072\n"
      "v_max_f32 v163, 0, v163\n"
      "v_max_f32 v164, 0, v164\n"
      "v_cvt_pk_bf16_f32 v163, v163, v164\n"
      "v_max_f32 v165, 0, v165\n"
      "global_load_dwordx4 a[176:179], v146, s[4:5]\n"
      "v_max_f32 v166, 0, v166\n"
      "v_cvt_pk_bf16_f32 v165, v165, v166\n"
      "s_waitcnt lgkmcnt(0)\n"
      "s_barrier\n"
      "s_pack_ll_b32_b16 s28, s64, s66\n"
      "v_dot2_f32_bf16 v168, s28, v150, 0\n"
      "s_pack_hh_b32_b16 s29, s64, s66\n"
      "v_dot2_f32_bf16 v169, s29, v150, 0\n"
      "s_pack_ll_b32_b16 s30, s65, s67\n"
      "v_dot2_f32_bf16 v170, s30, v150, 0\n"
      "s_pack_hh_b32_b16 s31, s65, s67\n"
      "v_dot2_f32_bf16 v171, s31, v150, 0\n"
      "s_pack_ll_b32_b16 s28, s68, s70\n"
      "v_dot2_f32_bf16 v168, s28, v137, v168\n"
      "s_pack_hh_b32_b16 s29, s68, s70\n"
      "v_dot2_f32_bf16 v169, s29, v137, v169\n"
      "s_pack_ll_b32_b16 s30, s69, s71\n"
      "v_dot2_f32_bf16 v170, s30, v137, v170\n"
      "s_pack_hh_b32_b16 s31, s69, s71\n"
      "v_dot2_f32_bf16 v171, s31, v137, v171\n"
      "v_add_u32 v139, 36864, v139\n"
      "v_add_u32 v140, 36864, v140\n"
      "v_add_u32 v141, 36864, v141\n"
      "v_add_u32 v143, 36864, v143\n"
      "v_add_u32 v142, 36864, v142\n"
      "v_add_u32 v146, 36864, v146\n"
      "global_load_dwordx4 a[180:183], v139, s[4:5] offset:-4096\n"
      "s_pack_ll_b32_b16 s28, s72, s74\n"
      "v_dot2_f32_bf16 v168, s28, v157, v168\n"
      "s_pack_hh_b32_b16 s29, s72, s74\n"
      "v_dot2_f32_bf16 v169, s29, v157, v169\n"
      "s_pack_ll_b32_b16 s30, s73, s75\n"
      "v_dot2_f32_bf16 v170, s30, v157, v170\n"
      "s_pack_hh_b32_b16 s31, s73, s75\n"
      "v_dot2_f32_bf16 v171, s31, v157, v171\n"
      "s_pack_ll_b32_b16 s28, s76, s78\n"
      "v_dot2_f32_bf16 v168, s28, v159, v168\n"
      "s_pack_hh_b32_b16 s29, s76, s78\n"
      "v_dot2_f32_bf16 v169, s29, v159, v169\n"
      "s_pack_ll_b32_b16 s30, s77, s79\n"
      "v_dot2_f32_bf16 v170, s30, v159, v170\n"
      "s_pack_hh_b32_b16 s31, s77, s79\n"
      "v_dot2_f32_bf16 v171, s31, v159, v171\n"
      "s_load_dwordx16 s[64:79], s[54:55], 40960\n"
      "s_lshl_b32 s28, s24, 4\n"
      "global_load_dwordx4 a[184:187], v139, s[4:5] offset:-3072\n"
      "s_lshr_b64 s[8:9], s[8:9], s28\n"
      "s_lshl_b32 s8, s8, 16\n"
      "v_mov_b32 v150, s8\n"
      "v_max_f32 v150, 0xc2c80000, v150\n"
      "v_min_f32 v150, 0x40930000, v150\n"
      "v_mul_f32 v150, 0x3fb8aa3b, v150\n"
      "v_exp_f32 v150, v150\n"
      "s_nop 0\n"
      "v_cvt_pk_bf16_f32 v150, v150, v150\n"
      "global_load_dwordx4 a[188:191], v141, s[4:5] offset:-2048\n"
      "v_lshlrev_b32_e64 v150, 16, v150\n"
      "s_pack_ll_b32_b16 s28, s84, s86\n"
      "v_dot2_f32_bf16 v168, s28, v161, v168\n"
      "s_pack_hh_b32_b16 s29, s84, s86\n"
      "v_dot2_f32_bf16 v169, s29, v161, v169\n"
      "s_pack_ll_b32_b16 s30, s85, s87\n"
      "v_dot2_f32_bf16 v170, s30, v161, v170\n"
      "global_load_dwordx4 a[192:195], v140, s[4:5] offset:-1024\n"
      "s_pack_hh_b32_b16 s31, s85, s87\n"
      "v_dot2_f32_bf16 v171, s31, v161, v171\n"
      "s_pack_ll_b32_b16 s28, s88, s90\n"
      "v_dot2_f32_bf16 v168, s28, v149, v168\n"
      "s_pack_hh_b32_b16 s29, s88, s90\n"
      "v_dot2_f32_bf16 v169, s29, v149, v169\n"
      "s_pack_ll_b32_b16 s30, s89, s91\n"
      "v_dot2_f32_bf16 v170, s30, v149, v170\n"
      "global_load_dwordx4 a[196:199], v143, s[4:5]\n"
      "s_pack_hh_b32_b16 s31, s89, s91\n"
      "v_dot2_f32_bf16 v171, s31, v149, v171\n"
      "s_pack_ll_b32_b16 s28, s92, s94\n"
      "v_dot2_f32_bf16 v168, s28, v163, v168\n"
      "s_pack_hh_b32_b16 s29, s92, s94\n"
      "v_dot2_f32_bf16 v169, s29, v163, v169\n"
      "s_pack_ll_b32_b16 s30, s93, s95\n"
      "v_dot2_f32_bf16 v170, s30, v163, v170\n"
      "global_load_dwordx4 a[200:203], v139, s[4:5] offset:1024\n"
      "s_pack_hh_b32_b16 s31, s93, s95\n"
      "v_dot2_f32_bf16 v171, s31, v163, v171\n"
      "s_pack_ll_b32_b16 s28, s96, s98\n"
      "v_dot2_f32_bf16 v168, s28, v165, v168\n"
      "s_pack_hh_b32_b16 s29, s96, s98\n"
      "v_dot2_f32_bf16 v169, s29, v165, v169\n"
      "s_pack_ll_b32_b16 s30, s97, s99\n"
      "global_load_dwordx4 a[204:207], v142, s[4:5] offset:2048\n"
      "v_dot2_f32_bf16 v170, s30, v165, v170\n"
      "s_pack_hh_b32_b16 s31, s97, s99\n"
      "v_dot2_f32_bf16 v171, s31, v165, v171\n"
      "s_load_dwordx16 s[80:95], s[52:53], 41280\n"
      "s_load_dwordx4 s[96:99], s[52:53], 41344\n"
      "s_load_dwordx2 s[100:101], s[52:53], 41360\n"
      "s_load_dword s26, s[52:53], 41376\n"
      "s_load_dword s3, s[52:53], 41380\n"
      "global_load_dwordx4 a[208:211], v140, s[4:5] offset:3072\n"
      "ds_write_b128 v147, a[72:75]\n"
      "ds_write_b128 v147, a[76:79] offset:1024\n"
      "ds_write_b128 v147, a[80:83] offset:2048\n"
      "ds_write_b128 v147, a[84:87] offset:3072\n"
      "ds_write_b128 v147, a[88:91] offset:4096\n"
      "ds_write_b128 v147, a[92:95] offset:5120\n"
      "ds_write_b128 v147, a[96:99] offset:6144\n"
      "global_load_dwordx4 a[212:215], v146, s[4:5]\n"
      "ds_write_b128 v147, a[100:103] offset:7168\n"
      "ds_write_b128 v147, a[104:107] offset:8192\n"
      "ds_write_b128 v147, a[108:111] offset:36864\n"
      "ds_write_b128 v147, a[112:115] offset:37888\n"
      "ds_write_b128 v147, a[116:119] offset:38912\n"
      "ds_write_b128 v147, a[120:123] offset:39936\n"
      "ds_write_b128 v147, a[124:127] offset:40960\n"
      "ds_write_b128 v147, a[128:131] offset:41984\n"
      "global_load_dwordx4 v[140:143], v154, s[60:61] offset:3072\n"
      "ds_write_b128 v147, a[132:135] offset:43008\n"
      "ds_write_b128 v147, a[136:139] offset:44032\n"
      "ds_write_b128 v147, a[140:143] offset:45056\n"
      "s_waitcnt vmcnt(15)\n"
      "s_waitcnt lgkmcnt(0)\n"
      "s_barrier\n"
      "L_sched_%=:\n"
      "v_cvt_pk_bf16_f32 v171, v170, v171\n"
      "v_cvt_pk_bf16_f32 v169, v168, v169\n"
      "v_lshlrev_b32 v168, 16, v169\n"
      "v_lshlrev_b32 v170, 16, v171\n"
      "v_and_b32 v169, 0xffff0000, v169\n"
      "v_cmp_nge_f32 vcc, 0x42480000, v150\n"
      "v_mul_f32 v169, 0xbfb8aa3b, v169\n"
      "s_mov_b64 s[50:51], vcc\n"
      "s_waitcnt vmcnt(14)\n"
      "ds_read_b128 v[164:167], v114 offset:20480\n"
      "ds_read_b128 v[156:159], v115 offset:18432\n"
      "ds_read_b128 v[160:163], v114 offset:18432\n"
      "ds_read_b128 v[136:139], v115 offset:20480\n"
      "v_mul_f32 v168, 0xbfb8aa3b, v168\n"
      "s_waitcnt vmcnt(0)\n"
      "v_add_u32 v149, 3072, v143\n"
      "v_mul_f32 v170, 0xbfb8aa3b, v170\n"
      "v_exp_f32_e64 v168, v168\n"
      "v_and_b32 v171, 0xffff0000, v171\n"
      "global_load_dwordx4 a[216:219], v149, s[6:7] offset:-3072\n"
      "ds_read_b64_tr_b16 a[82:83], v153 offset:4608\n"
      "ds_read_b64_tr_b16 a[96:97], v151 offset:768\n"
      "ds_read_b64_tr_b16 a[72:73], v151\n"
      "ds_read_b64_tr_b16 a[76:77], v152\n"
      "ds_read_b64_tr_b16 a[80:81], v153\n"
      "ds_read_b64_tr_b16 a[86:87], v151 offset:4992\n"
      "ds_read_b64_tr_b16 a[78:79], v152 offset:4608\n"
      "ds_read_b64_tr_b16 a[84:85], v151 offset:384\n"
      "v_mul_f32 v150, 0x3fb8aa3b, v150\n"
      "v_exp_f32 v169, v169\n"
      "v_mul_f32 v171, 0xbfb8aa3b, v171\n"
      "s_waitcnt lgkmcnt(8)\n"
      "v_mfma_f32_16x16x32_bf16 v[172:175], a[0:3], v[160:163], 0\n"
      "ds_read_b64_tr_b16 a[90:91], v152 offset:4992\n"
      "v_add_f32 v168, 1.0, v168\n"
      "v_mfma_f32_16x16x32_bf16 v[172:175], a[36:39], v[156:159], v[172:175]\n"
      "ds_read_b64_tr_b16 a[88:89], v152 offset:384\n"
      "ds_read_b64_tr_b16 a[92:93], v153 offset:384\n"
      "v_exp_f32 v171, v171\n"
      "v_mfma_f32_16x16x32_bf16 v[176:179], a[4:7], v[160:163], 0\n"
      "ds_read_b64_tr_b16 a[94:95], v153 offset:4992\n"
      "v_add_f32_e64 v169, 1.0, v169\n"
      "v_mfma_f32_16x16x32_bf16 v[176:179], a[40:43], v[156:159], v[176:179]\n"
      "ds_read_b64_tr_b16 a[74:75], v151 offset:4608\n"
      "v_exp_f32 v170, v170\n"
      "v_mfma_f32_16x16x32_bf16 v[180:183], a[8:11], v[160:163], 0\n"
      "ds_read_b64_tr_b16 a[98:99], v151 offset:5376\n"
      "v_add_f32 v171, 1.0, v171\n"
      "v_mfma_f32_16x16x32_bf16 v[180:183], a[44:47], v[156:159], v[180:183]\n"
      "global_load_dwordx4 a[220:223], v149, s[6:7] offset:-2944\n"
      "v_mfma_f32_16x16x32_bf16 v[184:187], a[12:15], v[160:163], 0\n"
      "ds_read_b64_tr_b16 a[104:105], v153 offset:768\n"
      "v_mfma_f32_16x16x32_bf16 v[184:187], a[48:51], v[156:159], v[184:187]\n"
      "ds_read_b64_tr_b16 a[112:113], v152 offset:36864\n"
      "v_rcp_f32_e64 v168, v168\n"
      "v_mfma_f32_16x16x32_bf16 v[188:191], a[16:19], v[160:163], 0\n"
      "ds_read_b64_tr_b16 a[102:103], v152 offset:5376\n"
      "v_add_f32 v170, 1.0, v170\n"
      "v_mfma_f32_16x16x32_bf16 v[188:191], a[52:55], v[156:159], v[188:191]\n"
      "ds_read_b64_tr_b16 a[108:109], v151 offset:36864\n"
      "v_rcp_f32 v169, v169\n"
      "v_mfma_f32_16x16x32_bf16 v[192:195], a[20:23], v[160:163], 0\n"
      "ds_read_b64_tr_b16 a[106:107], v153 offset:5376\n"
      "v_cvt_pk_bf16_f32 v169, v168, v169\n"
      "v_mfma_f32_16x16x32_bf16 v[192:195], a[56:59], v[156:159], v[192:195]\n"
      "ds_read_b64_tr_b16 a[100:101], v152 offset:768\n"
      "v_lshlrev_b32_e64 v168, 16, v169\n"
      "v_mfma_f32_16x16x32_bf16 v[196:199], a[24:27], v[160:163], 0\n"
      "ds_read_b64_tr_b16 a[110:111], v151 offset:41472\n"
      "v_and_b32 v169, 0xffff0000, v169\n"
      "v_mfma_f32_16x16x32_bf16 v[196:199], a[60:63], v[156:159], v[196:199]\n"
      "ds_read_b64_tr_b16 a[116:117], v153 offset:36864\n"
      "v_rcp_f32_e64 v171, v171\n"
      "v_mfma_f32_16x16x32_bf16 v[200:203], a[28:31], v[160:163], 0\n"
      "global_load_dwordx4 a[224:227], v149, s[6:7] offset:-2816\n"
      "v_mfma_f32_16x16x32_bf16 v[200:203], a[64:67], v[156:159], v[200:203]\n"
      "ds_read_b64_tr_b16 a[114:115], v152 offset:41472\n"
      "v_rcp_f32_e64 v170, v170\n"
      "v_mfma_f32_16x16x32_bf16 v[204:207], a[32:35], v[160:163], 0\n"
      "ds_read_b64_tr_b16 a[118:119], v153 offset:41472\n"
      "v_cvt_pk_bf16_f32 v171, v170, v171\n"
      "v_mfma_f32_16x16x32_bf16 v[204:207], a[68:71], v[156:159], v[204:207]\n"
      "ds_read_b64_tr_b16 a[122:123], v151 offset:41856\n"
      "v_add_f32_e64 v169, -0.5, v169\n"
      "v_mfma_f32_16x16x32_bf16 v[156:159], a[0:3], v[164:167], 0\n"
      "ds_read_b64_tr_b16 a[126:127], v152 offset:41856\n"
      "v_lshlrev_b32 v170, 16, v171\n"
      "v_mfma_f32_16x16x32_bf16 v[156:159], a[36:39], v[136:139], v[156:159]\n"
      "ds_read_b64_tr_b16 a[120:121], v151 offset:37248\n"
      "v_add_f32 v168, -0.5, v168\n"
      "v_mfma_f32_16x16x32_bf16 v[160:163], a[4:7], v[164:167], 0\n"
      "v_mul_f32 v168, 0x41b8aa3b, v168\n"
      "v_add_f32_e64 v170, -0.5, v170\n"
      "v_mfma_f32_16x16x32_bf16 v[160:163], a[40:43], v[136:139], v[160:163]\n"
      "ds_read_b64_tr_b16 a[128:129], v153 offset:37248\n"
      "ds_read_b64_tr_b16 a[136:137], v152 offset:37632\n"
      "v_mfma_f32_16x16x32_bf16 v[208:211], a[8:11], v[164:167], 0\n"
      "ds_read_b64_tr_b16 a[130:131], v153 offset:41856\n"
      "v_and_b32 v171, 0xffff0000, v171\n"
      "v_mfma_f32_16x16x32_bf16 v[208:211], a[44:47], v[136:139], v[208:211]\n"
      "global_load_dwordx4 a[228:231], v149, s[6:7]\n"
      "v_mfma_f32_16x16x32_bf16 v[212:215], a[12:15], v[164:167], 0\n"
      "ds_read_b64_tr_b16 a[138:139], v152 offset:42240\n"
      "v_mul_f32 v169, 0x41b8aa3b, v169\n"
      "v_mfma_f32_16x16x32_bf16 v[212:215], a[48:51], v[136:139], v[212:215]\n"
      "ds_read_b64_tr_b16 a[124:125], v152 offset:37248\n"
      "ds_read_b64_tr_b16 a[132:133], v151 offset:37632\n"
      "v_mfma_f32_16x16x32_bf16 v[216:219], a[16:19], v[164:167], 0\n"
      "ds_read_b64_tr_b16 a[134:135], v151 offset:42240\n"
      "v_add_f32_e64 v171, -0.5, v171\n"
      "v_mfma_f32_16x16x32_bf16 v[216:219], a[52:55], v[136:139], v[216:219]\n"
      "ds_write_b32 v145, v169 offset:768\n"
      "v_mul_f32 v171, 0x41b8aa3b, v171\n"
      "v_mfma_f32_16x16x32_bf16 v[220:223], a[20:23], v[164:167], 0\n"
      "ds_read_b64_tr_b16 a[140:141], v153 offset:37632\n"
      "v_mul_f32 v170, 0x41b8aa3b, v170\n"
      "v_mfma_f32_16x16x32_bf16 v[220:223], a[56:59], v[136:139], v[220:223]\n"
      "ds_write_b32 v145, v170 offset:1536\n"
      "v_mfma_f32_16x16x32_bf16 v[224:227], a[24:27], v[164:167], 0\n"
      "global_load_dwordx4 a[232:235], v149, s[6:7] offset:128\n"
      "global_load_dwordx4 a[236:239], v149, s[6:7] offset:256\n"
      "v_add_u32 v149, 9216, v143\n"
      "v_mfma_f32_16x16x32_bf16 v[228:231], a[28:31], v[164:167], 0\n"
      "global_load_dwordx4 a[240:243], v149, s[6:7] offset:-3072\n"
      "v_mfma_f32_16x16x32_bf16 v[232:235], a[32:35], v[164:167], 0\n"
      "ds_write_b32 v145, v168\n"
      "global_load_dwordx4 a[244:247], v149, s[6:7] offset:-2944\n"
      "v_mfma_f32_16x16x32_bf16 v[224:227], a[60:63], v[136:139], v[224:227]\n"
      "ds_write_b32 v145, v171 offset:2304\n"
      "v_mfma_f32_16x16x32_bf16 v[232:235], a[68:71], v[136:139], v[232:235]\n"
      "global_load_dwordx4 a[248:251], v149, s[6:7] offset:-2816\n"
      "v_mfma_f32_16x16x32_bf16 v[228:231], a[64:67], v[136:139], v[228:231]\n"
      "ds_read_b64_tr_b16 a[142:143], v153 offset:42240\n"
      "s_waitcnt lgkmcnt(0)\n"
      "s_barrier\n"
      "ds_read_b128 v[136:139], v114 offset:24576\n"
      "s_mov_b32 s11, 100466688\n"
      "ds_read_b128 v[164:167], v115 offset:26624\n"
      "ds_read_b128 v[168:171], v114 offset:26624\n"
      "ds_read_b128 v[236:239], v115 offset:24576\n"
      "ds_read2_b32 v[78:79], v140 offset0:0 offset1:1\n"
      "s_sub_u32 s16, s16, 99876864\n"
      "v_add_u32 v145, 3136, v140\n"
      "v_readfirstlane_b32 s36, v150\n"
      "v_mov_b32 v57, 0\n"
      "s_subb_u32 s17, s17, 0\n"
      "ds_read2_b32 v[94:95], v145 offset0:16 offset1:17\n"
      "global_load_dwordx4 v[118:121], v154, s[48:49] offset:-3072\n"
      "v_mov_b32 v48, 0\n"
      "v_mov_b32_e64 v42, 0\n"
      "ds_read2_b32 v[84:85], v140 offset0:18 offset1:19\n"
      "ds_read2_b32 v[98:99], v145 offset0:32 offset1:33\n"
      "ds_read2_b32 v[92:93], v145 offset0:2 offset1:3\n"
      "ds_read2_b32 v[90:91], v145 offset0:0 offset1:1\n"
      "v_mov_b32_e64 v53, 0\n"
      "v_mov_b32_e64 v49, 0\n"
      "v_mov_b32_e64 v61, 0\n"
      "v_mov_b32 v36, 0\n"
      "s_sub_u32 s22, s22, 99876864\n"
      "ds_read2_b32 v[86:87], v140 offset0:32 offset1:33\n"
      "s_subb_u32 s23, s23, 0\n"
      "s_add_u32 s12, s20, 16777216\n"
      "s_waitcnt lgkmcnt(7)\n"
      "v_mfma_f32_16x16x32_bf16 v[156:159], a[72:75], v[168:171], v[156:159]\n"
      "ds_read2_b32 v[82:83], v140 offset0:16 offset1:17\n"
      "s_addc_u32 s13, s21, 0\n"
      "v_mfma_f32_16x16x32_bf16 v[156:159], a[108:111], v[164:167], v[156:159]\n"
      "ds_read2_b32 v[96:97], v145 offset0:18 offset1:19\n"
      "ds_read2_b32 v[80:81], v140 offset0:2 offset1:3\n"
      "v_mfma_f32_16x16x32_bf16 v[172:175], a[72:75], v[136:139], v[172:175]\n"
      "global_load_dwordx4 v[122:125], v154, s[48:49] offset:-2048\n"
      "v_mfma_f32_16x16x32_bf16 v[172:175], a[108:111], v[236:239], v[172:175]\n"
      "ds_read2_b32 v[100:101], v145 offset0:34 offset1:35\n"
      "v_add_u32 v145, 6272, v140\n"
      "v_mfma_f32_16x16x32_bf16 v[176:179], a[76:79], v[136:139], v[176:179]\n"
      "ds_read2_b32 v[102:103], v145 offset0:0 offset1:1\n"
      "s_mul_i32 s2, s24, 768\n"
      "v_mfma_f32_16x16x32_bf16 v[176:179], a[112:115], v[236:239], v[176:179]\n"
      "ds_read2_b32 v[88:89], v140 offset0:34 offset1:35\n"
      "v_mov_b32 v43, 0\n"
      "v_mfma_f32_16x16x32_bf16 v[180:183], a[80:83], v[136:139], v[180:183]\n"
      "ds_read2_b32 v[104:105], v145 offset0:2 offset1:3\n"
      "v_mov_b32 v65, 0\n"
      "v_mfma_f32_16x16x32_bf16 v[180:183], a[116:119], v[236:239], v[180:183]\n"
      "ds_read2_b32 v[112:113], v145 offset0:34 offset1:35\n"
      "v_mov_b32_e64 v37, 0\n"
      "v_mfma_f32_16x16x32_bf16 v[184:187], a[84:87], v[136:139], v[184:187]\n"
      "ds_read_b32 v75, v142 offset:128\n"
      "v_mov_b32 v150, 134528\n"
      "v_mfma_f32_16x16x32_bf16 v[184:187], a[120:123], v[236:239], v[184:187]\n"
      "ds_read_b32 v71, v142\n"
      "ds_read2_b32 v[110:111], v145 offset0:32 offset1:33\n"
      "v_mfma_f32_16x16x32_bf16 v[188:191], a[88:91], v[136:139], v[188:191]\n"
      "ds_read_b32 v70, v141 offset:192\n"
      "v_mfma_f32_16x16x32_bf16 v[188:191], a[124:127], v[236:239], v[188:191]\n"
      "ds_read2_b32 v[108:109], v145 offset0:18 offset1:19\n"
      "s_add_u32 s2, s2, 150640\n"
      "v_mfma_f32_16x16x32_bf16 v[192:195], a[92:95], v[136:139], v[192:195]\n"
      "global_load_dwordx4 v[240:243], v155, s[48:49] offset:-3072\n"
      "v_mfma_f32_16x16x32_bf16 v[192:195], a[128:131], v[236:239], v[192:195]\n"
      "ds_read_b32 v72, v141 offset:3328\n"
      "ds_read2_b32 v[106:107], v145 offset0:16 offset1:17\n"
      "v_mfma_f32_16x16x32_bf16 v[196:199], a[96:99], v[136:139], v[196:199]\n"
      "ds_read_b32 v74, v141 offset:6464\n"
      "v_mfma_f32_16x16x32_bf16 v[196:199], a[132:135], v[236:239], v[196:199]\n"
      "ds_write_b128 v147, a[184:187] offset:37888\n"
      "v_mfma_f32_16x16x32_bf16 v[200:203], a[100:103], v[136:139], v[200:203]\n"
      "ds_read_b32 v77, v150\n"
      "v_mfma_f32_16x16x32_bf16 v[200:203], a[136:139], v[236:239], v[200:203]\n"
      "ds_write_b128 v147, a[144:147]\n"
      "v_mfma_f32_16x16x32_bf16 v[204:207], a[104:107], v[136:139], v[204:207]\n"
      "ds_read_b32 v73, v142 offset:64\n"
      "v_mfma_f32_16x16x32_bf16 v[204:207], a[140:143], v[236:239], v[204:207]\n"
      "ds_write_b128 v147, a[152:155] offset:2048\n"
      "v_mfma_f32_16x16x32_bf16 v[160:163], a[76:79], v[168:171], v[160:163]\n"
      "global_load_dwordx4 v[136:139], v155, s[48:49] offset:-4096\n"
      "v_mfma_f32_16x16x32_bf16 v[160:163], a[112:115], v[164:167], v[160:163]\n"
      "ds_write_b128 v147, a[148:151] offset:1024\n"
      "v_mfma_f32_16x16x32_bf16 v[208:211], a[80:83], v[168:171], v[208:211]\n"
      "ds_write_b128 v147, a[168:171] offset:6144\n"
      "v_mfma_f32_16x16x32_bf16 v[208:211], a[116:119], v[164:167], v[208:211]\n"
      "ds_write_b128 v147, a[156:159] offset:3072\n"
      "v_mfma_f32_16x16x32_bf16 v[212:215], a[84:87], v[168:171], v[212:215]\n"
      "ds_write_b128 v147, a[212:215] offset:45056\n"
      "v_mfma_f32_16x16x32_bf16 v[212:215], a[120:123], v[164:167], v[212:215]\n"
      "ds_write_b128 v147, a[160:163] offset:4096\n"
      "v_mfma_f32_16x16x32_bf16 v[216:219], a[88:91], v[168:171], v[216:219]\n"
      "ds_write_b128 v147, a[188:191] offset:38912\n"
      "v_mfma_f32_16x16x32_bf16 v[220:223], a[92:95], v[168:171], v[220:223]\n"
      "ds_write_b128 v147, a[200:203] offset:41984\n"
      "v_mfma_f32_16x16x32_bf16 v[220:223], a[128:131], v[164:167], v[220:223]\n"
      "ds_write_b128 v147, a[192:195] offset:39936\n"
      "v_mfma_f32_16x16x32_bf16 v[224:227], a[96:99], v[168:171], v[224:227]\n"
      "ds_write_b128 v147, a[176:179] offset:8192\n"
      "v_mfma_f32_16x16x32_bf16 v[228:231], a[100:103], v[168:171], v[228:231]\n"
      "ds_write_b128 v147, a[208:211] offset:44032\n"
      "v_mfma_f32_16x16x32_bf16 v[228:231], a[136:139], v[164:167], v[228:231]\n"
      "ds_write_b128 v147, a[172:175] offset:7168\n"
      "v_mfma_f32_16x16x32_bf16 v[216:219], a[124:127], v[164:167], v[216:219]\n"
      "ds_write_b128 v147, a[204:207] offset:43008\n"
      "v_mfma_f32_16x16x32_bf16 v[232:235], a[104:107], v[168:171], v[232:235]\n"
      "ds_write_b128 v147, a[164:167] offset:5120\n"
      "v_mfma_f32_16x16x32_bf16 v[232:235], a[140:143], v[164:167], v[232:235]\n"
      "ds_write_b128 v147, a[196:199] offset:40960\n"
      "v_mfma_f32_16x16x32_bf16 v[224:227], a[132:135], v[164:167], v[224:227]\n"
      "ds_write_b128 v147, a[180:183] offset:36864\n"
      "s_waitcnt lgkmcnt(0)\n"
      "s_barrier\n"
      "ds_read_b64_tr_b16 a[160:161], v152 offset:384\n"
      "s_mov_b32 s60, 100270464\n"
      "s_mov_b32 s61, 196224\n"
      "s_lshr_b32 s8, s80, 8\n"
      "ds_read_b64_tr_b16 a[196:197], v152 offset:37248\n"
      "ds_read_b64_tr_b16 a[202:203], v153 offset:41856\n"
      "s_and_b32 s9, s80, 0xff\n"
      "ds_read_b64_tr_b16 a[178:179], v153 offset:5376\n"
      "ds_read_b64_tr_b16 a[162:163], v152 offset:4992\n"
      "ds_read_b64_tr_b16 a[154:155], v153 offset:4608\n"
      "ds_read_b64_tr_b16 a[198:199], v152 offset:41856\n"
      "v_lshl_add_u32 v89, v89, 2, s2\n"
      "s_waitcnt vmcnt(1)\n"
      "v_mov_b32_e64 v67, v240\n"
      "v_lshl_add_u32 v81, v81, 2, s2\n"
      "v_mov_b32 v68, v240\n"
      "v_mov_b32 v66, v240\n"
      "v_lshl_add_u32 v87, v87, 2, s2\n"
      "s_mul_i32 s10, s8, 73\n"
      "s_add_u32 s10, s10, s9\n"
      "s_mul_i32 s28, s10, 4802\n"
      "s_add_u32 s56, s18, s28\n"
      "ds_read_b64_tr_b16 a[148:149], v152\n"
      "v_mov_b32 v69, v240\n"
      "s_addc_u32 s57, s19, 0\n"
      "ds_read_b64_tr_b16 a[192:193], v151 offset:37248\n"
      "s_and_b32 s56, s56, -16\n"
      "ds_read_b64_tr_b16 a[150:151], v152 offset:4608\n"
      "ds_read_b64_tr_b16 a[146:147], v151 offset:4608\n"
      "v_mov_b32 v240, 0\n"
      "s_sub_u32 s58, s56, 110592\n"
      "ds_read_b128 v[168:171], v115 offset:30720\n"
      "v_add_u32 v148, 99876864, v148\n"
      "v_mov_b32_e64 v145, v241\n"
      "v_lshl_add_u32 v79, v79, 2, s2\n"
      "s_subb_u32 s59, s57, 0\n"
      "s_sub_u32 s56, s56, 73728\n"
      "v_lshl_add_u32 v80, v80, 2, s2\n"
      "v_lshl_add_u32 v82, v82, 2, s2\n"
      "v_lshl_add_u32 v84, v84, 2, s2\n"
      "s_subb_u32 s57, s57, 0\n"
      "v_lshl_add_u32 v85, v85, 2, s2\n"
      "v_lshl_add_u32 v83, v83, 2, s2\n"
      "s_mul_i32 s28, s8, 1373568\n"
      "v_lshl_add_u32 v78, v78, 2, s2\n"
      "s_mul_i32 s29, s9, 2688\n"
      "ds_read_b64_tr_b16 a[174:175], v152 offset:5376\n"
      "ds_read_b64_tr_b16 a[158:159], v151 offset:4992\n"
      "s_add_u32 s28, s28, s29\n"
      "s_cmp_eq_u32 s8, 72\n"
      "s_cselect_b32 s8, 0, 100270464\n"
      "ds_read_b32 v85, v85\n"
      "v_mov_b32 v147, v241\n"
      "s_cmp_eq_u32 s9, 72\n"
      "ds_read_b64_tr_b16 a[180:181], v151 offset:36864\n"
      "ds_read_b64_tr_b16 a[172:173], v152 offset:768\n"
      "ds_read_b64_tr_b16 a[144:145], v151\n"
      "v_lshl_add_u32 v88, v88, 2, s2\n"
      "v_mov_b32_e64 v144, v241\n"
      "s_cselect_b32 s9, 0, 196224\n"
      "ds_read_b64_tr_b16 a[190:191], v153 offset:41472\n"
      "ds_read_b64_tr_b16 a[210:211], v152 offset:42240\n"
      "ds_read_b128 v[164:167], v114 offset:30720\n"
      "ds_read_b64_tr_b16 a[152:153], v153\n"
      "ds_read_b64_tr_b16 a[206:207], v151 offset:42240\n"
      "ds_read_b64_tr_b16 a[188:189], v153 offset:36864\n"
      "ds_read_b64_tr_b16 a[200:201], v153 offset:37248\n"
      "ds_read_b64_tr_b16 a[186:187], v152 offset:41472\n"
      "ds_read_b64_tr_b16 a[168:169], v151 offset:768\n"
      "ds_read_b64_tr_b16 a[176:177], v153 offset:768\n"
      "ds_read_b32 v79, v79\n"
      "ds_read_b64_tr_b16 a[182:183], v151 offset:41472\n"
      "ds_read_b64_tr_b16 a[166:167], v153 offset:4992\n"
      "ds_read_b64_tr_b16 a[184:185], v152 offset:36864\n"
      "ds_read_b64_tr_b16 a[170:171], v151 offset:5376\n"
      "s_add_u32 s10, s8, s9\n"
      "s_add_u32 s28, s28, s10\n"
      "s_add_u32 s52, s16, s28\n"
      "v_mov_b32 v146, v241\n"
      "s_addc_u32 s53, s17, 0\n"
      "ds_read_b64_tr_b16 a[194:195], v151 offset:41856\n"
      "ds_read_b64_tr_b16 a[214:215], v153 offset:42240\n"
      "ds_read_b64_tr_b16 a[204:205], v151 offset:37632\n"
      "ds_read_b32 v82, v82\n"
      "ds_read_b32 v80, v80\n"
      "s_cmp_lg_u32 s10, s11\n"
      "ds_read_b64_tr_b16 a[164:165], v153 offset:384\n"
      "s_cbranch_scc1 L_edge_1_%=\n"
      "L_eb_2_%=:\n"
      "ds_read_b64_tr_b16 a[212:213], v153 offset:37632\n"
      "ds_read_b64_tr_b16 a[208:209], v152 offset:37632\n"
      "ds_read_b128 v[236:239], v115 offset:32768\n"
      "v_mov_b32 v241, 0\n"
      "v_lshl_add_u32 v86, v86, 2, s2\n"
      "s_waitcnt lgkmcnt(10)\n"
      "v_lshl_add_u32 v91, v91, 2, s2\n"
      "v_mfma_f32_16x16x32_bf16 v[172:175], a[144:147], v[164:167], v[172:175]\n"
      "s_add_u32 m0, s76, 0\n"
      "ds_read_b32 v78, v78\n"
      "v_mfma_f32_16x16x32_bf16 v[172:175], a[180:183], v[168:171], v[172:175]\n"
      "global_load_lds_dwordx4 v116, s[52:53]\n"
      "v_lshl_add_u32 v92, v92, 2, s2\n"
      "v_mfma_f32_16x16x32_bf16 v[176:179], a[148:151], v[164:167], v[176:179]\n"
      "ds_read_b32 v86, v86\n"
      "ds_read_b64_tr_b16 a[156:157], v151 offset:384\n"
      "ds_read_b32 v83, v83\n"
      "ds_read_b128 v[150:153], v114 offset:32768\n"
      "v_mfma_f32_16x16x32_bf16 v[180:183], a[152:155], v[164:167], v[180:183]\n"
      "s_nop 0\n"
      "v_lshl_add_u32 v93, v93, 2, s2\n"
      "v_lshl_add_u32 v96, v96, 2, s2\n"
      "v_mfma_f32_16x16x32_bf16 v[180:183], a[188:191], v[168:171], v[180:183]\n"
      "ds_read_b32 v84, v84\n"
      "v_lshl_add_u32 v90, v90, 2, s2\n"
      "s_add_u32 m0, s76, 6016\n"
      "ds_read_b32 v81, v81\n"
      "s_waitcnt vmcnt(1)\n"
      "ds_write_b32 v137, v148\n"
      "global_load_lds_dwordx4 v116, s[52:53] offset:128\n"
      "s_add_u32 m0, s76, 12032\n"
      "ds_read_b32 v87, v87\n"
      "ds_read_b32 v88, v88\n"
      "global_load_lds_dwordx4 v116, s[52:53] offset:256\n"
      "s_add_u32 m0, s77, 0\n"
      "s_waitcnt lgkmcnt(7)\n"
      "v_mfma_f32_16x16x32_bf16 v[176:179], a[184:187], v[168:171], v[176:179]\n"
      "global_load_lds_dwordx4 v117, s[52:53]\n"
      "v_mfma_f32_16x16x32_bf16 v[184:187], a[156:159], v[164:167], v[184:187]\n"
      "ds_read_b32 v89, v89\n"
      "v_lshl_add_u32 v94, v94, 2, s2\n"
      "v_mfma_f32_16x16x32_bf16 v[184:187], a[192:195], v[168:171], v[184:187]\n"
      "ds_read_b32 v91, v91\n"
      "v_lshl_add_u32 v99, v99, 2, s2\n"
      "v_fma_f32 v137, v172, v172, s38\n"
      "v_mfma_f32_16x16x32_bf16 v[188:191], a[160:163], v[164:167], v[188:191]\n"
      "ds_read_b32 v94, v94\n"
      "v_lshl_add_u32 v97, v97, 2, s2\n"
      "v_mfma_f32_16x16x32_bf16 v[188:191], a[196:199], v[168:171], v[188:191]\n"
      "s_add_u32 m0, s78, 0\n"
      "v_lshl_add_u32 v111, v111, 2, s2\n"
      "v_lshl_add_u32 v95, v95, 2, s2\n"
      "v_lshl_add_u32 v106, v106, 2, s2\n"
      "v_lshl_add_u32 v98, v98, 2, s2\n"
      "s_mov_b32 exec_hi, 0\n"
      "global_load_lds_dwordx4 v118, s[52:53]\n"
      "s_mov_b32 exec_hi, -1\n"
      "v_lshl_add_u32 v107, v107, 2, s2\n"
      "v_lshl_add_u32 v105, v105, 2, s2\n"
      "v_lshl_add_u32 v110, v110, 2, s2\n"
      "s_add_u32 m0, s76, 110592\n"
      "ds_read_b32 v95, v95\n"
      "global_load_lds_dwordx4 v119, s[56:57]\n"
      "s_add_u32 m0, s79, 0\n"
      "ds_read_b32 v96, v96\n"
      "global_load_lds_dword v122, s[58:59]\n"
      "v_lshl_add_u32 v74, v74, 2, s2\n"
      "v_lshl_add_u32 v100, v100, 2, s2\n"
      "v_mfma_f32_16x16x32_bf16 v[192:195], a[164:167], v[164:167], v[192:195]\n"
      "ds_read_b32 v97, v97\n"
      "v_lshl_add_u32 v102, v102, 2, s2\n"
      "v_mfma_f32_16x16x32_bf16 v[192:195], a[200:203], v[168:171], v[192:195]\n"
      "ds_read_b32 v90, v90\n"
      "ds_read_b32 v92, v92\n"
      "v_mfma_f32_16x16x32_bf16 v[200:203], a[172:175], v[164:167], v[200:203]\n"
      "ds_read_b32 v93, v93\n"
      "v_lshl_add_u32 v112, v112, 2, s2\n"
      "v_mov_b32 v244, 0\n"
      "v_fma_f32 v148, v184, v184, s38\n"
      "v_lshl_add_u32 v101, v101, 2, s2\n"
      "v_mfma_f32_16x16x32_bf16 v[196:199], a[168:171], v[164:167], v[196:199]\n"
      "global_load_dwordx4 v[126:129], v154, s[48:49] offset:-1024\n"
      "v_lshl_add_u32 v72, v72, 2, s2\n"
      "v_mfma_f32_16x16x32_bf16 v[196:199], a[204:207], v[168:171], v[196:199]\n"
      "s_waitcnt lgkmcnt(5)\n"
      "global_load_dwordx4 v[130:133], v154, s[48:49]\n"
      "global_load_dwordx4 v[248:251], v154, s[48:49] offset:1024\n"
      "v_mfma_f32_16x16x32_bf16 v[204:207], a[176:179], v[164:167], v[204:207]\n"
      "global_load_dwordx4 a[252:255], v149, s[6:7]\n"
      "v_mfma_f32_16x16x32_bf16 v[200:203], a[208:211], v[168:171], v[200:203]\n"
      "global_load_dwordx4 v[0:3], v149, s[6:7] offset:128\n"
      "v_mfma_f32_16x16x32_bf16 v[156:159], a[144:147], v[150:153], v[156:159]\n"
      "global_load_dwordx4 v[4:7], v149, s[6:7] offset:256\n"
      "v_lshl_add_u32 v108, v108, 2, s2\n"
      "v_fmac_f32 v137, v173, v173\n"
      "v_fmac_f32 v148, v185, v185\n"
      "v_fmac_f32 v148, v186, v186\n"
      "v_fmac_f32 v137, v174, v174\n"
      "v_mfma_f32_16x16x32_bf16 v[156:159], a[180:183], v[236:239], v[156:159]\n"
      "ds_read_b32 v106, v106\n"
      "ds_read_b32 v99, v99\n"
      "v_add_u32 v149, 15360, v143\n"
      "v_mov_b32_e64 v245, 0\n"
      "v_lshl_add_u32 v109, v109, 2, s2\n"
      "v_mfma_f32_16x16x32_bf16 v[160:163], a[148:151], v[150:153], v[160:163]\n"
      "ds_read_b32 v98, v98\n"
      "v_lshl_add_u32 v113, v113, 2, s2\n"
      "v_mfma_f32_16x16x32_bf16 v[160:163], a[184:187], v[236:239], v[160:163]\n"
      "ds_read_b32 v100, v100\n"
      "v_lshl_add_u32 v77, v77, 2, s2\n"
      "v_fmac_f32_e64 v148, v187, v187\n"
      "v_mfma_f32_16x16x32_bf16 v[204:207], a[212:215], v[168:171], v[204:207]\n"
      "global_load_dwordx4 v[8:11], v149, s[6:7] offset:-3072\n"
      "ds_read_b32 v109, v109\n"
      "v_fmac_f32 v137, v175, v175\n"
      "v_fmac_f32 v148, v188, v188\n"
      "v_lshl_add_u32 v71, v71, 2, s2\n"
      "v_fmac_f32 v137, v176, v176\n"
      "v_fmac_f32 v148, v189, v189\n"
      "v_lshl_add_u32 v103, v103, 2, s2\n"
      "v_fmac_f32_e64 v148, v190, v190\n"
      "v_lshl_add_u32 v75, v75, 2, s2\n"
      "v_lshl_add_u32 v73, v73, 2, s2\n"
      "v_fmac_f32 v137, v177, v177\n"
      "v_fmac_f32 v137, v178, v178\n"
      "v_fmac_f32_e64 v137, v179, v179\n"
      "v_lshl_add_u32 v104, v104, 2, s2\n"
      "v_mfma_f32_16x16x32_bf16 v[208:211], a[152:155], v[150:153], v[208:211]\n"
      "ds_read_b32 v102, v102\n"
      "v_fmac_f32_e64 v148, v191, v191\n"
      "v_mfma_f32_16x16x32_bf16 v[208:211], a[188:191], v[236:239], v[208:211]\n"
      "ds_read_b32 v103, v103\n"
      "ds_read_b32 v108, v108\n"
      "global_load_dwordx4 v[12:15], v149, s[6:7] offset:-2944\n"
      "v_fmac_f32 v137, v180, v180\n"
      "v_fmac_f32 v137, v181, v181\n"
      "v_fmac_f32 v148, v192, v192\n"
      "v_fmac_f32 v137, v182, v182\n"
      "v_fmac_f32 v148, v193, v193\n"
      "v_lshl_add_u32 v70, v70, 2, s2\n"
      "v_fmac_f32 v148, v194, v194\n"
      "v_fmac_f32_e64 v148, v195, v195\n"
      "v_mfma_f32_16x16x32_bf16 v[212:215], a[156:159], v[150:153], v[212:215]\n"
      "ds_read_b32 v107, v107\n"
      "v_fmac_f32_e64 v137, v183, v183\n"
      "v_mfma_f32_16x16x32_bf16 v[212:215], a[192:195], v[236:239], v[212:215]\n"
      "ds_read_b32 v74, v74\n"
      "ds_read_b32 v101, v101\n"
      "v_permlane16_swap_b32 v137, v148\n"
      "v_add_f32 v137, v137, v148\n"
      "v_mov_b32_e64 v148, v137\n"
      "ds_read_b32 v111, v111\n"
      "global_load_dwordx4 v[16:19], v149, s[6:7] offset:-2816\n"
      "ds_read_b32 v104, v104\n"
      "ds_read_b32 v70, v70\n"
      "v_permlane32_swap_b32 v137, v148\n"
      "v_add_f32 v137, v137, v148\n"
      "v_rsq_f32 v137, v137\n"
      "v_mfma_f32_16x16x32_bf16 v[216:219], a[160:163], v[150:153], v[216:219]\n"
      "ds_read_b32 v112, v112\n"
      "v_mov_b32 v148, v137\n"
      "v_mfma_f32_16x16x32_bf16 v[216:219], a[196:199], v[236:239], v[216:219]\n"
      "ds_read_b32 v72, v72\n"
      "v_permlane16_swap_b32 v137, v148\n"
      "v_mul_f32 v187, v148, v187\n"
      "v_mul_f32 v190, v148, v190\n"
      "v_mul_f32 v186, v148, v186\n"
      "v_mul_f32 v184, v148, v184\n"
      "v_mul_f32 v191, v148, v191\n"
      "v_mul_f32 v137, s36, v137\n"
      "v_mul_f32 v185, v148, v185\n"
      "v_mul_f32_e64 v172, v137, v172\n"
      "v_mfma_f32_16x16x32_bf16 v[220:223], a[164:167], v[150:153], v[220:223]\n"
      "ds_read_b32 v110, v110\n"
      "global_load_dwordx4 v[20:23], v149, s[6:7]\n"
      "v_cvt_pk_f16_f32 v184, v184, v185\n"
      "v_mul_f32 v193, v148, v193\n"
      "v_mul_f32 v174, v137, v174\n"
      "v_mul_f32 v180, v137, v180\n"
      "v_mul_f32 v188, v148, v188\n"
      "v_mul_f32 v176, v137, v176\n"
      "v_mfma_f32_16x16x32_bf16 v[220:223], a[200:203], v[236:239], v[220:223]\n"
      "ds_read_b32 v113, v113\n"
      "v_mul_f32 v195, v148, v195\n"
      "v_mul_f32 v194, v148, v194\n"
      "v_mov_b32 v141, -1\n"
      "v_mul_f32 v192, v148, v192\n"
      "v_mul_f32 v177, v137, v177\n"
      "v_cvt_pk_f16_f32 v185, v186, v187\n"
      "v_cvt_pk_f16_f32 v35, v194, v195\n"
      "v_mul_f32 v173, v137, v173\n"
      "v_mfma_f32_16x16x32_bf16 v[224:227], a[168:171], v[150:153], v[224:227]\n"
      "ds_read_b32 v77, v77\n"
      "v_mul_f32 v175, v137, v175\n"
      "v_mfma_f32_16x16x32_bf16 v[224:227], a[204:207], v[236:239], v[224:227]\n"
      "ds_read_b32 v105, v105\n"
      "global_load_dwordx4 v[24:27], v149, s[6:7] offset:128\n"
      "ds_read_b32 v71, v71\n"
      "global_load_dwordx4 v[28:31], v149, s[6:7] offset:256\n"
      "v_mul_f32 v189, v148, v189\n"
      "s_waitcnt lgkmcnt(4)\n"
      "v_mfma_f32_16x16x32_bf16 v[232:235], a[176:179], v[150:153], v[232:235]\n"
      "ds_read_b32 v73, v73\n"
      "v_cvt_pk_f16_f32 v186, v188, v189\n"
      "v_mfma_f32_16x16x32_bf16 v[232:235], a[212:215], v[236:239], v[232:235]\n"
      "s_waitcnt vmcnt(12)\n"
      "ds_read_b32 v143, v122\n"
      "v_cvt_pk_f16_f32 v172, v172, v173\n"
      "v_mul_f32 v182, v137, v182\n"
      "v_mfma_f32_16x16x32_bf16 v[228:231], a[172:175], v[150:153], v[228:231]\n"
      "ds_read_b128 v[148:151], v119 offset:36864\n"
      "s_waitcnt vmcnt(9)\n"
      "v_mov_b32 v135, v249\n"
      "v_mul_f32 v181, v137, v181\n"
      "v_cvt_pk_f16_f32 v187, v190, v191\n"
      "s_waitcnt lgkmcnt(5)\n"
      "ds_write_b32 v129, v141 offset:1024\n"
      "ds_write_b32 v129, v141\n"
      "s_mov_b32 exec_lo, 0xffff0000\n"
      "s_mov_b32 exec_hi, 0xffffffff\n"
      "v_mov_b32 v70, 0xf149f2ca\n"
      "v_mov_b32 v72, 0xf149f2ca\n"
      "v_mov_b32 v74, 0xf149f2ca\n"
      "s_mov_b64 exec, -1\n"
      "v_add_u32 v140, 36864, v251\n"
      "v_cvt_pk_f16_f32 v34, v192, v193\n"
      "v_cvt_pk_f16_f32 v173, v174, v175\n"
      "v_mul_f32_e64 v179, v137, v179\n"
      "s_mov_b32 exec_lo, 0x0\n"
      "s_mov_b32 exec_hi, 0xfffe0000\n"
      "v_mov_b32 v77, 0xf149f2ca\n"
      "s_mov_b64 exec, -1\n"
      "ds_write_b32 v129, v141 offset:3072\n"
      "s_mov_b32 s4, 0\n"
      "ds_read_b32 v75, v75\n"
      "v_cvt_pk_f16_f32 v174, v176, v177\n"
      "s_waitcnt lgkmcnt(4)\n"
      "v_mul_f32_e64 v178, v137, v178\n"
      "v_or3_b32 v148, v148, v149, v150\n"
      "v_bitop3_b32 v143, v143, v151, s40 bitop3:0xa8\n"
      "v_cvt_pk_f16_f32 v175, v178, v179\n"
      "v_mul_f32_e64 v183, v137, v183\n"
      "v_and_b32 v148, s40, v148\n"
      "v_add_u32 v137, 36864, v250\n"
      "s_mov_b32 s5, 0\n"
      "v_or3_b32 v148, v148, v143, s4\n"
      "v_mfma_f32_16x16x32_bf16 v[228:231], a[208:211], v[236:239], v[228:231]\n"
      "ds_write_b32 v129, v148 offset:2048\n"
      "v_cvt_pk_f16_f32 v33, v182, v183\n"
      "v_cvt_pk_f16_f32 v32, v180, v181\n"
      "v_mov_b32_e64 v134, v248\n"
      "s_waitcnt lgkmcnt(0)\n"
      "s_barrier\n"
      "v_cvt_pk_bf16_f32 v204, v204, v205\n"
      "v_cvt_pk_bf16_f32 v205, v206, v207\n"
      "v_fma_f32 v207, v212, v212, s38\n"
      "v_fma_f32 v206, v156, v156, s38\n"
      "v_fmac_f32 v207, v213, v213\n"
      "v_fmac_f32 v207, v214, v214\n"
      "v_fmac_f32 v206, v157, v157\n"
      "v_fmac_f32 v206, v158, v158\n"
      "v_fmac_f32 v206, v159, v159\n"
      "v_fmac_f32 v206, v160, v160\n"
      "v_fmac_f32 v207, v215, v215\n"
      "v_fmac_f32 v207, v216, v216\n"
      "v_fmac_f32 v207, v217, v217\n"
      "v_fmac_f32 v207, v218, v218\n"
      "v_fmac_f32 v207, v219, v219\n"
      "v_cvt_pk_bf16_f32 v200, v200, v201\n"
      "v_fmac_f32 v206, v161, v161\n"
      "v_fmac_f32 v206, v162, v162\n"
      "v_fmac_f32 v207, v220, v220\n"
      "v_fmac_f32 v206, v163, v163\n"
      "v_fmac_f32 v206, v208, v208\n"
      "v_fmac_f32 v206, v209, v209\n"
      "v_fmac_f32 v207, v221, v221\n"
      "v_fmac_f32 v206, v210, v210\n"
      "v_fmac_f32 v207, v222, v222\n"
      "v_fmac_f32 v207, v223, v223\n"
      "v_fmac_f32 v206, v211, v211\n"
      "s_mov_b32 s27, 0\n"
      "s_nop 0\n"
      "v_permlane16_swap_b32 v206, v207\n"
      "v_add_f32 v206, v206, v207\n"
      "v_mov_b32 v207, v206\n"
      "s_add_u32 s28, s27, 1\n"
      "v_cvt_pk_bf16_f32 v201, v202, v203\n"
      "v_permlane32_swap_b32 v206, v207\n"
      "v_add_f32 v206, v206, v207\n"
      "v_rsq_f32 v206, v206\n"
      "v_cvt_pk_bf16_f32 v196, v196, v197\n"
      "v_mov_b32 v207, v206\n"
      "s_min_u32 m0, s28, s3\n"
      "ds_read_b128 v[180:183], v114\n"
      "ds_read_b128 v[148:151], v128 offset:2048\n"
      "v_permlane16_swap_b32 v206, v207\n"
      "v_mul_f32 v206, s36, v206\n"
      "v_mul_f32 v159, v206, v159\n"
      "s_movrels_b32 s9, s80\n"
      "ds_read_b128 v[152:155], v115\n"
      "v_mul_f32 v221, v207, v221\n"
      "v_mul_f32 v212, v207, v212\n"
      "v_cvt_pk_bf16_f32 v197, v198, v199\n"
      "v_mul_f32 v158, v206, v158\n"
      "v_mul_f32 v160, v206, v160\n"
      "v_mul_f32 v217, v207, v217\n"
      "v_mul_f32 v215, v207, v215\n"
      "v_mul_f32 v214, v207, v214\n"
      "s_lshr_b32 s8, s9, 8\n"
      "ds_write_b128 v138, v[172:175]\n"
      "v_mul_f32 v157, v206, v157\n"
      "v_mul_f32 v156, v206, v156\n"
      "v_mul_f32 v213, v207, v213\n"
      "v_mul_f32 v219, v207, v219\n"
      "v_mul_f32 v218, v207, v218\n"
      "v_cvt_pk_f16_f32 v212, v212, v213\n"
      "v_cvt_pk_f16_f32 v213, v214, v215\n"
      "v_mul_f32_e64 v161, v206, v161\n"
      "s_and_b32 s9, s9, 0xff\n"
      "ds_write_b128 v138, v[32:35] offset:16\n"
      "ds_write_b128 v138, v[184:187] offset:32\n"
      "s_mul_i32 s10, s8, 73\n"
      "v_mul_f32_e64 v216, v207, v216\n"
      "v_cvt_pk_f16_f32 v156, v156, v157\n"
      "v_mul_f32_e64 v220, v207, v220\n"
      "s_waitcnt lgkmcnt(3)\n"
      "v_mfma_f32_16x16x32_bf16 v[172:175], a[0:3], v[180:183], 0\n"
      "ds_read_b128 v[184:187], v114 offset:6144\n"
      "s_add_u32 s10, s10, s9\n"
      "v_mfma_f32_16x16x32_bf16 v[172:175], a[36:39], v[152:155], v[172:175]\n"
      "ds_write_b64 v139, v[196:197]\n"
      "v_mul_f32 v223, v207, v223\n"
      "v_mul_f32 v222, v207, v222\n"
      "v_cvt_pk_f16_f32 v214, v216, v217\n"
      "v_mfma_f32_16x16x32_bf16 v[196:199], a[4:7], v[180:183], 0\n"
      "ds_read_b128 v[176:179], v115 offset:6144\n"
      "s_mul_i32 s28, s10, 4802\n"
      "v_mfma_f32_16x16x32_bf16 v[196:199], a[40:43], v[152:155], v[196:199]\n"
      "ds_write_b64 v139, v[200:201] offset:32\n"
      "v_cvt_pk_f16_f32 v40, v220, v221\n"
      "v_cvt_pk_f16_f32 v41, v222, v223\n"
      "v_mfma_f32_16x16x32_bf16 v[200:203], a[8:11], v[180:183], 0\n"
      "s_add_u32 s56, s18, s28\n"
      "s_addc_u32 s57, s19, 0\n"
      "s_and_b32 s56, s56, -16\n"
      "v_mfma_f32_16x16x32_bf16 v[200:203], a[44:47], v[152:155], v[200:203]\n"
      "ds_write_b64 v139, v[204:205] offset:64\n"
      "v_cvt_pk_f16_f32 v157, v158, v159\n"
      "v_mfma_f32_16x16x32_bf16 v[164:167], a[12:15], v[180:183], 0\n"
      "s_sub_u32 s58, s56, 110592\n"
      "v_mul_f32 v208, v206, v208\n"
      "v_cvt_pk_f16_f32 v158, v160, v161\n"
      "v_mfma_f32_16x16x32_bf16 v[164:167], a[48:51], v[152:155], v[164:167]\n"
      "s_subb_u32 s59, s57, 0\n"
      "v_mul_f32 v163, v206, v163\n"
      "v_mul_f32 v162, v206, v162\n"
      "v_mfma_f32_16x16x32_bf16 v[192:195], a[16:19], v[180:183], 0\n"
      "s_sub_u32 s56, s56, 73728\n"
      "v_mul_f32 v209, v206, v209\n"
      "v_cvt_pk_f16_f32 v159, v162, v163\n"
      "v_mfma_f32_16x16x32_bf16 v[192:195], a[52:55], v[152:155], v[192:195]\n"
      "s_subb_u32 s57, s57, 0\n"
      "v_mul_f32 v210, v206, v210\n"
      "v_cvt_pk_f16_f32 v38, v208, v209\n"
      "v_mfma_f32_16x16x32_bf16 v[160:163], a[20:23], v[180:183], 0\n"
      "ds_read_b128 v[168:171], v114 offset:12288\n"
      "v_cvt_pk_bf16_f32 v224, v224, v225\n"
      "v_mfma_f32_16x16x32_bf16 v[160:163], a[56:59], v[152:155], v[160:163]\n"
      "s_mul_i32 s28, s8, 1373568\n"
      "v_mul_f32_e64 v211, v206, v211\n"
      "v_cvt_pk_f16_f32 v39, v210, v211\n"
      "v_mfma_f32_16x16x32_bf16 v[208:211], v[180:183], a[24:27], 0\n"
      "v_cvt_pk_bf16_f32 v228, v228, v229\n"
      "s_waitcnt lgkmcnt(2)\n"
      "v_mfma_f32_16x16x32_bf16 v[208:211], v[152:155], a[60:63], v[208:211]\n"
      "s_mul_i32 s29, s9, 2688\n"
      "v_cvt_pk_f16_f32 v215, v218, v219\n"
      "v_cvt_pk_bf16_f32 v225, v226, v227\n"
      "v_mfma_f32_16x16x32_bf16 v[188:191], v[180:183], a[28:31], 0\n"
      "s_add_u32 s28, s28, s29\n"
      "ds_read_b128 v[204:207], v115 offset:12288\n"
      "v_mfma_f32_16x16x32_bf16 v[188:191], v[152:155], a[64:67], v[188:191]\n"
      "s_cmp_eq_u32 s8, 72\n"
      "s_cselect_b32 s8, 0, 100270464\n"
      "v_cvt_pk_bf16_f32 v232, v232, v233\n"
      "v_mfma_f32_16x16x32_bf16 v[220:223], v[180:183], a[32:35], 0\n"
      "v_cvt_pk_bf16_f32 v229, v230, v231\n"
      "s_cmp_eq_u32 s9, 72\n"
      "v_mfma_f32_16x16x32_bf16 v[220:223], v[152:155], a[68:71], v[220:223]\n"
      "s_cselect_b32 s9, 0, 196224\n"
      "s_add_u32 s10, s8, s9\n"
      "s_add_u32 s28, s28, s10\n"
      "v_mfma_f32_16x16x32_bf16 v[172:175], a[72:75], v[184:187], v[172:175]\n"
      "s_add_u32 s52, s16, s28\n"
      "s_addc_u32 s53, s17, 0\n"
      "s_cmp_lg_u32 s10, s11\n"
      "v_mfma_f32_16x16x32_bf16 v[172:175], a[108:111], v[176:179], v[172:175]\n"
      "s_cbranch_scc1 L_edge_6_%=\n"
      "L_eb_7_%=:\n"
      "v_mfma_f32_16x16x32_bf16 v[196:199], a[76:79], v[184:187], v[196:199]\n"
      "v_cvt_pk_bf16_f32 v233, v234, v235\n"
      "s_add_u32 m0, s76, 18432\n"
      "v_mfma_f32_16x16x32_bf16 v[196:199], a[112:115], v[176:179], v[196:199]\n"
      "global_load_lds_dwordx4 v116, s[52:53]\n"
      "v_mfma_f32_16x16x32_bf16 v[200:203], a[80:83], v[184:187], v[200:203]\n"
      "v_mfma_f32_16x16x32_bf16 v[200:203], a[116:119], v[176:179], v[200:203]\n"
      "s_mov_b32 exec_lo, 0x003f003f\n"
      "s_mov_b32 exec_hi, 0x003f003f\n"
      "ds_write_b128 v138, v[156:159] offset:18688\n"
      "ds_write_b128 v138, v[38:41] offset:18704\n"
      "ds_write_b128 v138, v[212:215] offset:18720\n"
      "ds_write_b64 v139, v[224:225] offset:18688\n"
      "ds_write_b64 v139, v[228:229] offset:18720\n"
      "ds_write_b64 v139, v[232:233] offset:18752\n"
      "s_mov_b64 exec, -1\n"
      "v_mfma_f32_16x16x32_bf16 v[164:167], a[84:87], v[184:187], v[164:167]\n"
      "s_nop 0\n"
      "v_bitop3_b32 v150, v150, v151, s4 bitop3:0x7e\n"
      "v_bitop3_b32 v148, v148, v149, s4 bitop3:0x7e\n"
      "v_mfma_f32_16x16x32_bf16 v[164:167], a[120:123], v[176:179], v[164:167]\n"
      "v_or_b32 v148, v148, v150\n"
      "v_cmp_ne_u32 vcc, 0, v148\n"
      "v_mfma_f32_16x16x32_bf16 v[192:195], a[88:91], v[184:187], v[192:195]\n"
      "s_mov_b64 s[48:49], vcc\n"
      "s_add_u32 m0, s76, 24448\n"
      "v_mfma_f32_16x16x32_bf16 v[192:195], a[124:127], v[176:179], v[192:195]\n"
      "s_cbranch_vccnz L_flg_3_%=\n"
      "L_fb_4_%=:\n"
      "v_mfma_f32_16x16x32_bf16 v[148:151], v[144:147], a[216:219], 0\n"
      "global_load_lds_dwordx4 v116, s[52:53] offset:128\n"
      "v_mfma_f32_16x16x32_bf16 v[160:163], a[92:95], v[184:187], v[160:163]\n"
      "s_add_u32 m0, s76, 30464\n"
      "v_mfma_f32_16x16x32_bf16 v[160:163], a[128:131], v[176:179], v[160:163]\n"
      "v_mfma_f32_16x16x32_bf16 v[148:151], v[144:147], a[220:223], v[148:151]\n"
      "s_waitcnt lgkmcnt(1)\n"
      "v_mfma_f32_16x16x32_bf16 v[148:151], v[144:147], a[224:227], v[148:151]\n"
      "s_nop 0\n"
      "v_mfma_f32_16x16x32_bf16 v[188:191], v[184:187], a[100:103], v[188:191]\n"
      "ds_read_b128 v[152:155], v114 offset:2048\n"
      "v_mfma_f32_16x16x32_bf16 v[188:191], v[176:179], a[136:139], v[188:191]\n"
      "v_mfma_f32_16x16x32_bf16 v[208:211], v[184:187], a[96:99], v[208:211]\n"
      "v_mfma_f32_16x16x32_bf16 v[208:211], v[176:179], a[132:135], v[208:211]\n"
      "v_mfma_f32_16x16x32_bf16 v[220:223], v[184:187], a[104:107], v[220:223]\n"
      "s_nop 1\n"
      "v_add_f32_dpp v148, v148, v148 quad_perm:[1,0,3,2] row_mask:0xf bank_mask:0xf\n"
      "v_mfma_f32_16x16x32_bf16 v[220:223], v[176:179], a[140:143], v[220:223]\n"
      "ds_read_b128 v[156:159], v115 offset:2048\n"
      "v_add_f32_dpp v148, v148, v148 quad_perm:[2,3,0,1] row_mask:0xf bank_mask:0xf\n"
      "v_mfma_f32_16x16x32_bf16 v[172:175], a[144:147], v[168:171], v[172:175]\n"
      "s_nop 0\n"
      "v_add_f32_dpp v148, v148, v148 row_half_mirror row_mask:0xf bank_mask:0xf\n"
      "v_mul_f32 v149, 0xbbaaaaab, v148\n"
      "v_mfma_f32_16x16x32_bf16 v[172:175], a[180:183], v[204:207], v[172:175]\n"
      "v_mul_f32 v151, 0xbbaaaaab, v148\n"
      "v_mul_f32 v150, 0xbbaaaaab, v148\n"
      "v_mfma_f32_16x16x32_bf16 v[196:199], a[148:151], v[168:171], v[196:199]\n"
      "v_mul_f32 v148, 0xbbaaaaab, v148\n"
      "v_mfma_f32_16x16x32_bf16 v[196:199], a[184:187], v[204:207], v[196:199]\n"
      "s_nop 0\n"
      "v_mfma_f32_16x16x32_bf16 v[184:187], v[242:245], a[216:219], v[148:151]\n"
      "s_nop 0\n"
      "v_fma_f32 v139, v172, v172, s38\n"
      "v_mfma_f32_16x16x32_bf16 v[212:215], v[242:245], a[220:223], v[148:151]\n"
      "global_load_lds_dwordx4 v116, s[52:53] offset:256\n"
      "v_mfma_f32_16x16x32_bf16 v[176:179], v[240:243], a[216:219], v[148:151]\n"
      "s_nop 0\n"
      "v_mfma_f32_16x16x32_bf16 v[180:183], v[240:243], a[220:223], v[148:151]\n"
      "s_nop 1\n"
      "v_cvt_pk_bf16_f32 v184, v184, v185\n"
      "v_mfma_f32_16x16x32_bf16 v[200:203], a[152:155], v[168:171], v[200:203]\n"
      "s_add_u32 m0, s77, 18432\n"
      "v_mfma_f32_16x16x32_bf16 v[200:203], a[188:191], v[204:207], v[200:203]\n"
      "v_cvt_pk_bf16_f32 v185, v186, v187\n"
      "v_cvt_pk_bf16_f32 v212, v212, v213\n"
      "v_mfma_f32_16x16x32_bf16 v[228:231], v[240:243], a[224:227], v[148:151]\n"
      "v_cvt_pk_bf16_f32 v187, v178, v179\n"
      "v_mfma_f32_16x16x32_bf16 v[232:235], v[242:245], a[224:227], v[148:151]\n"
      "v_cvt_pk_bf16_f32 v213, v214, v215\n"
      "v_mfma_f32_16x16x32_bf16 v[148:151], v[144:147], a[228:231], 0\n"
      "s_nop 0\n"
      "v_cvt_pk_bf16_f32 v186, v176, v177\n"
      "s_waitcnt lgkmcnt(0)\n"
      "v_mfma_f32_16x16x32_bf16 v[148:151], v[144:147], a[232:235], v[148:151]\n"
      "ds_write_b128 v136, v[184:187]\n"
      "v_mfma_f32_16x16x32_bf16 v[148:151], v[144:147], a[236:239], v[148:151]\n"
      "global_load_lds_dwordx4 v117, s[52:53]\n"
      "v_mfma_f32_16x16x32_bf16 v[164:167], a[156:159], v[168:171], v[164:167]\n"
      "v_cvt_pk_bf16_f32 v215, v182, v183\n"
      "v_cvt_pk_bf16_f32 v214, v180, v181\n"
      "v_mfma_f32_16x16x32_bf16 v[164:167], a[192:195], v[204:207], v[164:167]\n"
      "v_cvt_pk_bf16_f32 v232, v232, v233\n"
      "v_mfma_f32_16x16x32_bf16 v[192:195], a[160:163], v[168:171], v[192:195]\n"
      "v_cvt_pk_bf16_f32 v233, v234, v235\n"
      "v_cvt_pk_bf16_f32 v234, v228, v229\n"
      "v_mfma_f32_16x16x32_bf16 v[192:195], a[196:199], v[204:207], v[192:195]\n"
      "ds_write_b128 v136, v[212:215] offset:128\n"
      "v_mfma_f32_16x16x32_bf16 v[160:163], a[164:167], v[168:171], v[160:163]\n"
      "v_add_f32_dpp v148, v148, v148 quad_perm:[1,0,3,2] row_mask:0xf bank_mask:0xf\n"
      "v_cvt_pk_bf16_f32 v235, v230, v231\n"
      "v_mfma_f32_16x16x32_bf16 v[160:163], a[200:203], v[204:207], v[160:163]\n"
      "ds_write_b128 v136, v[232:235] offset:256\n"
      "v_mfma_f32_16x16x32_bf16 v[208:211], v[168:171], a[168:171], v[208:211]\n"
      "s_nop 0\n"
      "v_fma_f32 v138, v164, v164, s38\n"
      "v_add_f32_dpp v148, v148, v148 quad_perm:[2,3,0,1] row_mask:0xf bank_mask:0xf\n"
      "v_mfma_f32_16x16x32_bf16 v[208:211], v[204:207], a[204:207], v[208:211]\n"
      "s_nop 0\n"
      "v_add_f32_dpp v148, v148, v148 row_half_mirror row_mask:0xf bank_mask:0xf\n"
      "v_mul_f32 v149, 0xbbaaaaab, v148\n"
      "v_mfma_f32_16x16x32_bf16 v[188:191], v[168:171], a[172:175], v[188:191]\n"
      "v_mul_f32 v150, 0xbbaaaaab, v148\n"
      "v_mul_f32 v151, 0xbbaaaaab, v148\n"
      "v_mfma_f32_16x16x32_bf16 v[188:191], v[204:207], a[208:211], v[188:191]\n"
      "v_mul_f32 v148, 0xbbaaaaab, v148\n"
      "v_fmac_f32_e64 v138, v165, v165\n"
      "v_mfma_f32_16x16x32_bf16 v[220:223], v[168:171], a[176:179], v[220:223]\n"
      "ds_read_b128 v[168:171], v114 offset:8192\n"
      "v_fmac_f32 v138, v166, v166\n"
      "v_mfma_f32_16x16x32_bf16 v[220:223], v[204:207], a[212:215], v[220:223]\n"
      "v_fmac_f32 v139, v173, v173\n"
      "v_fmac_f32 v139, v174, v174\n"
      "v_mfma_f32_16x16x32_bf16 v[204:207], v[240:243], a[228:231], v[148:151]\n"
      "v_fmac_f32 v139, v175, v175\n"
      "v_fmac_f32 v139, v196, v196\n"
      "v_mfma_f32_16x16x32_bf16 v[180:183], v[242:245], a[228:231], v[148:151]\n"
      "v_fmac_f32 v138, v167, v167\n"
      "v_fmac_f32 v138, v192, v192\n"
      "v_mfma_f32_16x16x32_bf16 v[184:187], v[242:245], a[232:235], v[148:151]\n"
      "v_fmac_f32 v139, v197, v197\n"
      "v_fmac_f32 v138, v193, v193\n"
      "v_mfma_f32_16x16x32_bf16 v[176:179], v[240:243], a[232:235], v[148:151]\n"
      "v_fmac_f32 v139, v198, v198\n"
      "v_fmac_f32_e64 v138, v194, v194\n"
      "v_mfma_f32_16x16x32_bf16 v[232:235], v[242:245], a[236:239], v[148:151]\n"
      "v_cvt_pk_bf16_f32 v180, v180, v181\n"
      "v_cvt_pk_bf16_f32 v181, v182, v183\n"
      "v_mfma_f32_16x16x32_bf16 v[212:215], v[240:243], a[236:239], v[148:151]\n"
      "ds_read_b128 v[228:231], v115 offset:8192\n"
      "v_cvt_pk_bf16_f32 v183, v206, v207\n"
      "v_mfma_f32_16x16x32_bf16 v[224:227], a[4:7], v[152:155], 0\n"
      "v_cvt_pk_bf16_f32 v182, v204, v205\n"
      "v_cvt_pk_bf16_f32 v184, v184, v185\n"
      "v_mfma_f32_16x16x32_bf16 v[224:227], a[40:43], v[156:159], v[224:227]\n"
      "ds_write_b128 v136, v[180:183] offset:3072\n"
      "v_mfma_f32_16x16x32_bf16 v[148:151], a[0:3], v[152:155], 0\n"
      "v_cvt_pk_bf16_f32 v185, v186, v187\n"
      "v_cvt_pk_bf16_f32 v187, v178, v179\n"
      "v_mfma_f32_16x16x32_bf16 v[148:151], a[36:39], v[156:159], v[148:151]\n"
      "v_cvt_pk_bf16_f32 v186, v176, v177\n"
      "v_cvt_pk_bf16_f32 v232, v232, v233\n"
      "v_mfma_f32_16x16x32_bf16 v[176:179], a[8:11], v[152:155], 0\n"
      "v_cvt_pk_bf16_f32 v233, v234, v235\n"
      "v_cvt_pk_bf16_f32 v234, v212, v213\n"
      "v_mfma_f32_16x16x32_bf16 v[176:179], a[44:47], v[156:159], v[176:179]\n"
      "ds_write_b128 v136, v[184:187] offset:3200\n"
      "v_mfma_f32_16x16x32_bf16 v[184:187], a[12:15], v[152:155], 0\n"
      "v_fmac_f32_e64 v138, v195, v195\n"
      "v_cvt_pk_bf16_f32 v235, v214, v215\n"
      "v_mfma_f32_16x16x32_bf16 v[184:187], a[48:51], v[156:159], v[184:187]\n"
      "ds_write_b128 v136, v[232:235] offset:3328\n"
      "v_mfma_f32_16x16x32_bf16 v[180:183], a[16:19], v[152:155], 0\n"
      "v_fmac_f32 v138, v160, v160\n"
      "v_fmac_f32 v139, v199, v199\n"
      "v_mfma_f32_16x16x32_bf16 v[180:183], a[52:55], v[156:159], v[180:183]\n"
      "v_fmac_f32 v139, v200, v200\n"
      "v_fmac_f32 v139, v201, v201\n"
      "v_mfma_f32_16x16x32_bf16 v[204:207], a[20:23], v[152:155], 0\n"
      "v_fmac_f32 v139, v202, v202\n"
      "v_fmac_f32 v138, v161, v161\n"
      "v_mfma_f32_16x16x32_bf16 v[204:207], a[56:59], v[156:159], v[204:207]\n"
      "v_fmac_f32 v138, v162, v162\n"
      "v_fmac_f32 v139, v203, v203\n"
      "v_mfma_f32_16x16x32_bf16 v[232:235], v[152:155], a[24:27], 0\n"
      "s_waitcnt lgkmcnt(2)\n"
      "v_cvt_pk_f16_f32 v164, v164, v165\n"
      "v_fmac_f32 v138, v163, v163\n"
      "v_mfma_f32_16x16x32_bf16 v[232:235], v[156:159], a[60:63], v[232:235]\n"
      "s_nop 0\n"
      "v_permlane16_swap_b32 v139, v138\n"
      "v_mfma_f32_16x16x32_bf16 v[212:215], v[152:155], a[28:31], 0\n"
      "v_add_f32 v139, v139, v138\n"
      "v_mov_b32 v138, v139\n"
      "v_mfma_f32_16x16x32_bf16 v[212:215], v[156:159], a[64:67], v[212:215]\n"
      "s_nop 0\n"
      "v_permlane32_swap_b32 v139, v138\n"
      "v_cvt_pk_f16_f32 v34, v160, v161\n"
      "v_mfma_f32_16x16x32_bf16 v[148:151], a[72:75], v[168:171], v[148:151]\n"
      "v_cvt_pk_f16_f32 v165, v166, v167\n"
      "v_cvt_pk_f16_f32 v166, v192, v193\n"
      "v_mfma_f32_16x16x32_bf16 v[148:151], a[108:111], v[228:231], v[148:151]\n"
      "v_cvt_pk_f16_f32 v167, v194, v195\n"
      "v_cvt_pk_f16_f32 v172, v172, v173\n"
      "v_mfma_f32_16x16x32_bf16 v[192:195], v[152:155], a[32:35], 0\n"
      "ds_read_b128 v[152:155], v114 offset:14336\n"
      "v_add_f32_e64 v139, v139, v138\n"
      "v_mfma_f32_16x16x32_bf16 v[192:195], v[156:159], a[68:71], v[192:195]\n"
      "v_rsq_f32_e64 v139, v139\n"
      "v_mfma_f32_16x16x32_bf16 v[224:227], a[76:79], v[168:171], v[224:227]\n"
      "v_cvt_pk_f16_f32 v35, v162, v163\n"
      "v_mov_b32_e64 v138, v139\n"
      "v_mfma_f32_16x16x32_bf16 v[224:227], a[112:115], v[228:231], v[224:227]\n"
      "ds_read_b128 v[156:159], v115 offset:14336\n"
      "v_cmp_nge_f32 vcc, 0x43800000, v138\n"
      "v_mfma_f32_16x16x32_bf16 v[160:163], v[144:147], a[240:243], 0\n"
      "s_mov_b64 s[34:35], vcc\n"
      "v_cvt_pk_f16_f32 v173, v174, v175\n"
      "v_cmp_nle_f32 vcc, 0x3a000000, v138\n"
      "v_mfma_f32_16x16x32_bf16 v[176:179], a[80:83], v[168:171], v[176:179]\n"
      "s_or_b64 s[34:35], s[34:35], vcc\n"
      "v_cvt_pk_f16_f32 v175, v198, v199\n"
      "v_mfma_f32_16x16x32_bf16 v[176:179], a[116:119], v[228:231], v[176:179]\n"
      "s_add_u32 m0, s78, 18432\n"
      "v_permlane16_swap_b32_e64 v139, v138\n"
      "v_mfma_f32_16x16x32_bf16 v[160:163], v[144:147], a[244:247], v[160:163]\n"
      "s_mov_b32 exec_hi, 0\n"
      "global_load_lds_dwordx4 v118, s[52:53]\n"
      "s_mov_b32 exec_hi, -1\n"
      "v_mfma_f32_16x16x32_bf16 v[160:163], v[144:147], a[248:251], v[160:163]\n"
      "v_mfma_f32_16x16x32_bf16 v[184:187], a[84:87], v[168:171], v[184:187]\n"
      "v_mul_f32_e64 v139, s36, v139\n"
      "v_cvt_pk_f16_f32 v139, v139, v138\n"
      "v_mfma_f32_16x16x32_bf16 v[184:187], a[120:123], v[228:231], v[184:187]\n"
      "s_nop 0\n"
      "v_cvt_pk_f16_f32 v174, v196, v197\n"
      "v_mfma_f32_16x16x32_bf16 v[180:183], a[88:91], v[168:171], v[180:183]\n"
      "s_waitcnt lgkmcnt(0)\n"
      "v_pk_mul_f16 v35, v35, v139 op_sel:[0,1] op_sel_hi:[1,1]\n"
      "v_cvt_pk_f16_f32 v33, v202, v203\n"
      "v_mfma_f32_16x16x32_bf16 v[180:183], a[124:127], v[228:231], v[180:183]\n"
      "v_pk_mul_f16 v172, v172, v139 op_sel_hi:[1,0]\n"
      "v_add_f32_dpp v160, v160, v160 quad_perm:[1,0,3,2] row_mask:0xf bank_mask:0xf\n"
      "v_mfma_f32_16x16x32_bf16 v[204:207], a[92:95], v[168:171], v[204:207]\n"
      "v_pk_mul_f16 v174, v174, v139 op_sel_hi:[1,0]\n"
      "v_pk_mul_f16 v33, v33, v139 op_sel_hi:[1,0]\n"
      "v_mfma_f32_16x16x32_bf16 v[204:207], a[128:131], v[228:231], v[204:207]\n"
      "s_add_u32 m0, s76, 115456\n"
      "v_cvt_pk_f16_f32 v32, v200, v201\n"
      "v_mfma_f32_16x16x32_bf16 v[232:235], v[168:171], a[96:99], v[232:235]\n"
      "v_pk_mul_f16 v164, v164, v139 op_sel:[0,1] op_sel_hi:[1,1]\n"
      "v_add_f32_dpp v160, v160, v160 quad_perm:[2,3,0,1] row_mask:0xf bank_mask:0xf\n"
      "v_mfma_f32_16x16x32_bf16 v[232:235], v[228:231], a[132:135], v[232:235]\n"
      "global_load_lds_dwordx4 v119, s[56:57]\n"
      "v_mfma_f32_16x16x32_bf16 v[192:195], v[168:171], a[104:107], v[192:195]\n"
      "v_add_f32_dpp v160, v160, v160 row_half_mirror row_mask:0xf bank_mask:0xf\n"
      "v_pk_mul_f16 v165, v165, v139 op_sel:[0,1] op_sel_hi:[1,1]\n"
      "v_mfma_f32_16x16x32_bf16 v[192:195], v[228:231], a[140:143], v[192:195]\n"
      "v_mul_f32 v163, 0xbbaaaaab, v160\n"
      "v_mfma_f32_16x16x32_bf16 v[212:215], v[168:171], a[100:103], v[212:215]\n"
      "v_pk_mul_f16 v166, v166, v139 op_sel:[0,1] op_sel_hi:[1,1]\n"
      "v_mul_f32 v162, 0xbbaaaaab, v160\n"
      "v_mfma_f32_16x16x32_bf16 v[212:215], v[228:231], a[136:139], v[212:215]\n"
      "v_mul_f32 v161, 0xbbaaaaab, v160\n"
      "s_waitcnt vmcnt(14)\n"
      "v_mfma_f32_16x16x32_bf16 v[168:171], v[144:147], a[252:255], 0\n"
      "ds_read_b128 v[200:203], v114 offset:4096\n"
      "s_waitcnt vmcnt(13)\n"
      "v_mfma_f32_16x16x32_bf16 v[168:171], v[144:147], v[0:3], v[168:171]\n"
      "s_nop 0\n"
      "v_mul_f32 v160, 0xbbaaaaab, v160\n"
      "v_pk_mul_f16 v167, v167, v139 op_sel:[0,1] op_sel_hi:[1,1]\n"
      "s_waitcnt vmcnt(12)\n"
      "v_mfma_f32_16x16x32_bf16 v[168:171], v[144:147], v[4:7], v[168:171]\n"
      "v_mfma_f32_16x16x32_bf16 v[196:199], v[240:243], a[240:243], v[160:163]\n"
      "v_pk_mul_f16 v173, v173, v139 op_sel_hi:[1,0]\n"
      "s_add_u32 m0, s79, 4864\n"
      "v_mfma_f32_16x16x32_bf16 v[228:231], v[242:245], a[240:243], v[160:163]\n"
      "v_cvt_pk_bf16_f32 v208, v208, v209\n"
      "v_mfma_f32_16x16x32_bf16 v[148:151], a[144:147], v[152:155], v[148:151]\n"
      "v_pk_mul_f16 v175, v175, v139 op_sel_hi:[1,0]\n"
      "v_mfma_f32_16x16x32_bf16 v[148:151], a[180:183], v[156:159], v[148:151]\n"
      "global_load_lds_dword v122, s[58:59]\n"
      "v_mfma_f32_16x16x32_bf16 v[224:227], a[148:151], v[152:155], v[224:227]\n"
      "v_mfma_f32_16x16x32_bf16 v[224:227], a[184:187], v[156:159], v[224:227]\n"
      "s_nop 0\n"
      "v_cvt_pk_bf16_f32 v228, v228, v229\n"
      "v_cvt_pk_bf16_f32 v229, v230, v231\n"
      "v_mfma_f32_16x16x32_bf16 v[176:179], a[152:155], v[152:155], v[176:179]\n"
      "s_nop 0\n"
      "v_pk_mul_f16 v34, v34, v139 op_sel:[0,1] op_sel_hi:[1,1]\n"
      "v_cvt_pk_bf16_f32 v230, v196, v197\n"
      "v_mfma_f32_16x16x32_bf16 v[176:179], a[188:191], v[156:159], v[176:179]\n"
      "v_add_f32_dpp v168, v168, v168 quad_perm:[1,0,3,2] row_mask:0xf bank_mask:0xf\n"
      "v_cvt_pk_bf16_f32 v231, v198, v199\n"
      "v_mfma_f32_16x16x32_bf16 v[196:199], v[242:245], a[244:247], v[160:163]\n"
      "ds_write_b128 v136, v[228:231] offset:6144\n"
      "v_mfma_f32_16x16x32_bf16 v[228:231], v[240:243], a[244:247], v[160:163]\n"
      "v_add_f32_dpp v168, v168, v168 quad_perm:[2,3,0,1] row_mask:0xf bank_mask:0xf\n"
      "v_pk_mul_f16 v32, v32, v139 op_sel_hi:[1,0]\n"
      "v_mfma_f32_16x16x32_bf16 v[184:187], a[156:159], v[152:155], v[184:187]\n"
      "v_add_f32_dpp v168, v168, v168 row_half_mirror row_mask:0xf bank_mask:0xf\n"
      "v_mul_f32 v170, 0xbbaaaaab, v168\n"
      "v_mfma_f32_16x16x32_bf16 v[184:187], a[192:195], v[156:159], v[184:187]\n"
      "v_cvt_pk_bf16_f32 v196, v196, v197\n"
      "v_cvt_pk_bf16_f32 v197, v198, v199\n"
      "v_mfma_f32_16x16x32_bf16 v[204:207], a[164:167], v[152:155], v[204:207]\n"
      "v_cvt_pk_bf16_f32 v198, v228, v229\n"
      "v_cvt_pk_bf16_f32 v199, v230, v231\n"
      "v_mfma_f32_16x16x32_bf16 v[204:207], a[200:203], v[156:159], v[204:207]\n"
      "ds_write_b128 v136, v[196:199] offset:6272\n"
      "v_mfma_f32_16x16x32_bf16 v[196:199], v[240:243], a[248:251], v[160:163]\n"
      "ds_read_b128 v[228:231], v115 offset:4096\n"
      "v_mul_f32 v169, 0xbbaaaaab, v168\n"
      "v_mfma_f32_16x16x32_bf16 v[216:219], v[242:245], a[248:251], v[160:163]\n"
      "v_mul_f32 v171, 0xbbaaaaab, v168\n"
      "v_cvt_pk_bf16_f32 v220, v220, v221\n"
      "v_mfma_f32_16x16x32_bf16 v[180:183], a[160:163], v[152:155], v[180:183]\n"
      "v_mul_f32 v168, 0xbbaaaaab, v168\n"
      "v_mfma_f32_16x16x32_bf16 v[180:183], a[196:199], v[156:159], v[180:183]\n"
      "v_cvt_pk_bf16_f32 v209, v210, v211\n"
      "v_cvt_pk_bf16_f32 v188, v188, v189\n"
      "v_mfma_f32_16x16x32_bf16 v[232:235], v[152:155], a[168:171], v[232:235]\n"
      "v_cvt_pk_bf16_f32 v216, v216, v217\n"
      "v_cvt_pk_bf16_f32 v217, v218, v219\n"
      "v_mfma_f32_16x16x32_bf16 v[232:235], v[156:159], a[204:207], v[232:235]\n"
      "v_cvt_pk_bf16_f32 v218, v196, v197\n"
      "v_cvt_pk_bf16_f32 v219, v198, v199\n"
      "v_mfma_f32_16x16x32_bf16 v[160:163], v[242:245], a[252:255], v[168:171]\n"
      "ds_write_b128 v136, v[216:219] offset:6400\n"
      "v_mfma_f32_16x16x32_bf16 v[196:199], v[240:243], a[252:255], v[168:171]\n"
      "v_cvt_pk_bf16_f32 v221, v222, v223\n"
      "v_cvt_pk_bf16_f32 v189, v190, v191\n"
      "v_mfma_f32_16x16x32_bf16 v[212:215], v[152:155], a[172:175], v[212:215]\n"
      "s_nop 0\n"
      "v_cvt_pk_bf16_f32 v211, v234, v235\n"
      "v_cvt_pk_bf16_f32 v210, v232, v233\n"
      "v_mfma_f32_16x16x32_bf16 v[212:215], v[156:159], a[208:211], v[212:215]\n"
      "s_waitcnt lgkmcnt(1)\n"
      "v_cvt_pk_bf16_f32 v160, v160, v161\n"
      "v_cvt_pk_bf16_f32 v161, v162, v163\n"
      "v_mfma_f32_16x16x32_bf16 v[232:235], v[242:245], v[0:3], v[168:171]\n"
      "v_cvt_pk_bf16_f32 v162, v196, v197\n"
      "v_cvt_pk_bf16_f32 v163, v198, v199\n"
      "v_mfma_f32_16x16x32_bf16 v[196:199], v[240:243], v[0:3], v[168:171]\n"
      "ds_write_b128 v136, v[160:163] offset:9216\n"
      "v_mfma_f32_16x16x32_bf16 v[160:163], v[240:243], v[4:7], v[168:171]\n"
      "ds_read_b128 v[216:219], v114 offset:10240\n"
      "v_cvt_pk_bf16_f32 v190, v212, v213\n"
      "v_mfma_f32_16x16x32_bf16 v[236:239], v[242:245], v[4:7], v[168:171]\n"
      "v_cvt_pk_bf16_f32 v232, v232, v233\n"
      "v_cvt_pk_bf16_f32 v233, v234, v235\n"
      "v_mfma_f32_16x16x32_bf16 v[192:195], v[152:155], a[176:179], v[192:195]\n"
      "ds_read_b128 v[152:155], v115 offset:10240\n"
      "v_cvt_pk_bf16_f32 v235, v198, v199\n"
      "v_mfma_f32_16x16x32_bf16 v[192:195], v[156:159], a[212:215], v[192:195]\n"
      "v_cvt_pk_bf16_f32 v234, v196, v197\n"
      "v_cvt_pk_bf16_f32 v191, v214, v215\n"
      "v_mfma_f32_16x16x32_bf16 v[156:159], a[0:3], v[200:203], 0\n"
      "v_cvt_pk_bf16_f32 v236, v236, v237\n"
      "v_cvt_pk_bf16_f32 v237, v238, v239\n"
      "v_mfma_f32_16x16x32_bf16 v[156:159], a[36:39], v[228:231], v[156:159]\n"
      "ds_write_b128 v136, v[232:235] offset:9344\n"
      "v_mfma_f32_16x16x32_bf16 v[168:171], a[4:7], v[200:203], 0\n"
      "v_cvt_pk_bf16_f32 v238, v160, v161\n"
      "v_cvt_pk_bf16_f32 v239, v162, v163\n"
      "v_mfma_f32_16x16x32_bf16 v[168:171], a[40:43], v[228:231], v[168:171]\n"
      "ds_write_b128 v136, v[236:239] offset:9472\n"
      "v_mfma_f32_16x16x32_bf16 v[160:163], a[8:11], v[200:203], 0\n"
      "v_cvt_pk_bf16_f32 v222, v192, v193\n"
      "v_cvt_pk_bf16_f32 v223, v194, v195\n"
      "v_mfma_f32_16x16x32_bf16 v[160:163], a[44:47], v[228:231], v[160:163]\n"
      "v_fma_f32 v139, v184, v184, s38\n"
      "v_fma_f32 v138, v148, v148, s38\n"
      "v_mfma_f32_16x16x32_bf16 v[192:195], a[12:15], v[200:203], 0\n"
      "v_fmac_f32 v139, v185, v185\n"
      "v_fmac_f32 v138, v149, v149\n"
      "v_mfma_f32_16x16x32_bf16 v[192:195], a[48:51], v[228:231], v[192:195]\n"
      "v_fmac_f32 v138, v150, v150\n"
      "v_fmac_f32 v138, v151, v151\n"
      "v_mfma_f32_16x16x32_bf16 v[196:199], a[16:19], v[200:203], 0\n"
      "s_waitcnt lgkmcnt(2)\n"
      "v_fmac_f32 v138, v224, v224\n"
      "v_fmac_f32 v139, v186, v186\n"
      "v_mfma_f32_16x16x32_bf16 v[196:199], a[52:55], v[228:231], v[196:199]\n"
      "v_fmac_f32 v139, v187, v187\n"
      "v_fmac_f32_e64 v139, v180, v180\n"
      "v_mfma_f32_16x16x32_bf16 v[236:239], a[20:23], v[200:203], 0\n"
      "v_fmac_f32 v139, v181, v181\n"
      "v_fmac_f32 v138, v225, v225\n"
      "v_mfma_f32_16x16x32_bf16 v[236:239], a[56:59], v[228:231], v[236:239]\n"
      "v_fmac_f32 v138, v226, v226\n"
      "v_fmac_f32 v138, v227, v227\n"
      "v_mfma_f32_16x16x32_bf16 v[156:159], a[72:75], v[216:219], v[156:159]\n"
      "v_fmac_f32 v138, v176, v176\n"
      "v_fmac_f32 v139, v182, v182\n"
      "v_mfma_f32_16x16x32_bf16 v[156:159], a[108:111], v[152:155], v[156:159]\n"
      "v_fmac_f32 v139, v183, v183\n"
      "v_fmac_f32 v139, v204, v204\n"
      "v_mfma_f32_16x16x32_bf16 v[168:171], a[76:79], v[216:219], v[168:171]\n"
      "v_fmac_f32 v139, v205, v205\n"
      "v_fmac_f32 v139, v206, v206\n"
      "v_mfma_f32_16x16x32_bf16 v[168:171], a[112:115], v[152:155], v[168:171]\n"
      "v_fmac_f32 v138, v177, v177\n"
      "v_fmac_f32 v138, v178, v178\n"
      "v_mfma_f32_16x16x32_bf16 v[160:163], a[80:83], v[216:219], v[160:163]\n"
      "v_fmac_f32 v139, v207, v207\n"
      "v_fmac_f32 v138, v179, v179\n"
      "v_mfma_f32_16x16x32_bf16 v[160:163], a[116:119], v[152:155], v[160:163]\n"
      "s_nop 0\n"
      "v_permlane16_swap_b32 v138, v139\n"
      "v_mfma_f32_16x16x32_bf16 v[192:195], a[84:87], v[216:219], v[192:195]\n"
      "v_add_f32_e64 v138, v138, v139\n"
      "v_cvt_pk_f16_f32 v184, v184, v185\n"
      "v_mfma_f32_16x16x32_bf16 v[192:195], a[120:123], v[152:155], v[192:195]\n"
      "v_cvt_pk_f16_f32 v185, v186, v187\n"
      "v_cvt_pk_f16_f32 v186, v180, v181\n"
      "v_mfma_f32_16x16x32_bf16 v[232:235], v[200:203], a[24:27], 0\n"
      "v_mov_b32_e64 v139, v138\n"
      "v_cvt_pk_f16_f32 v187, v182, v183\n"
      "v_mfma_f32_16x16x32_bf16 v[232:235], v[228:231], a[60:63], v[232:235]\n"
      "v_permlane32_swap_b32_e64 v138, v139\n"
      "v_mfma_f32_16x16x32_bf16 v[180:183], v[200:203], a[28:31], 0\n"
      "v_cvt_pk_f16_f32 v40, v204, v205\n"
      "v_add_f32_e64 v138, v138, v139\n"
      "v_mfma_f32_16x16x32_bf16 v[180:183], v[228:231], a[64:67], v[180:183]\n"
      "v_rsq_f32_e64 v138, v138\n"
      "v_mfma_f32_16x16x32_bf16 v[196:199], a[88:91], v[216:219], v[196:199]\n"
      "v_cvt_pk_f16_f32 v148, v148, v149\n"
      "v_mov_b32_e64 v139, v138\n"
      "v_mfma_f32_16x16x32_bf16 v[196:199], a[124:127], v[152:155], v[196:199]\n"
      "s_nop 0\n"
      "v_cmp_nge_f32 vcc, 0x43800000, v139\n"
      "v_cvt_pk_f16_f32 v41, v206, v207\n"
      "v_mfma_f32_16x16x32_bf16 v[204:207], v[200:203], a[32:35], 0\n"
      "s_or_b64 s[34:35], s[34:35], vcc\n"
      "v_cvt_pk_f16_f32 v149, v150, v151\n"
      "v_cmp_nle_f32 vcc, 0x3a000000, v139\n"
      "v_mfma_f32_16x16x32_bf16 v[204:207], v[228:231], a[68:71], v[204:207]\n"
      "v_cvt_pk_f16_f32 v150, v224, v225\n"
      "s_waitcnt vmcnt(12)\n"
      "v_mfma_f32_16x16x32_bf16 v[200:203], v[144:147], v[8:11], 0\n"
      "v_permlane16_swap_b32 v138, v139\n"
      "s_waitcnt vmcnt(11)\n"
      "v_mfma_f32_16x16x32_bf16 v[200:203], v[144:147], v[12:15], v[200:203]\n"
      "s_or_b64 s[34:35], s[34:35], vcc\n"
      "v_cvt_pk_f16_f32 v38, v176, v177\n"
      "v_mul_f32 v138, s36, v138\n"
      "s_waitcnt vmcnt(10)\n"
      "v_mfma_f32_16x16x32_bf16 v[200:203], v[144:147], v[16:19], v[200:203]\n"
      "v_cvt_pk_f16_f32 v138, v138, v139\n"
      "v_mfma_f32_16x16x32_bf16 v[236:239], a[92:95], v[216:219], v[236:239]\n"
      "v_pk_mul_f16 v187, v187, v138 op_sel:[0,1] op_sel_hi:[1,1]\n"
      "v_cvt_pk_f16_f32 v151, v226, v227\n"
      "v_mfma_f32_16x16x32_bf16 v[236:239], a[128:131], v[152:155], v[236:239]\n"
      "v_cvt_pk_f16_f32 v39, v178, v179\n"
      "v_pk_mul_f16 v185, v185, v138 op_sel:[0,1] op_sel_hi:[1,1]\n"
      "v_mfma_f32_16x16x32_bf16 v[232:235], v[216:219], a[96:99], v[232:235]\n"
      "v_add_f32_dpp v200, v200, v200 quad_perm:[1,0,3,2] row_mask:0xf bank_mask:0xf\n"
      "v_pk_mul_f16 v149, v149, v138 op_sel_hi:[1,0]\n"
      "v_mfma_f32_16x16x32_bf16 v[232:235], v[152:155], a[132:135], v[232:235]\n"
      "v_pk_mul_f16 v150, v150, v138 op_sel_hi:[1,0]\n"
      "v_mfma_f32_16x16x32_bf16 v[180:183], v[216:219], a[100:103], v[180:183]\n"
      "v_pk_mul_f16 v39, v39, v138 op_sel_hi:[1,0]\n"
      "v_add_f32_dpp v200, v200, v200 quad_perm:[2,3,0,1] row_mask:0xf bank_mask:0xf\n"
      "v_mfma_f32_16x16x32_bf16 v[180:183], v[152:155], a[136:139], v[180:183]\n"
      "v_pk_mul_f16 v40, v40, v138 op_sel:[0,1] op_sel_hi:[1,1]\n"
      "v_pk_mul_f16 v186, v186, v138 op_sel:[0,1] op_sel_hi:[1,1]\n"
      "v_mfma_f32_16x16x32_bf16 v[204:207], v[216:219], a[104:107], v[204:207]\n"
      "ds_read_b128 v[176:179], v114 offset:16384\n"
      "v_pk_mul_f16 v184, v184, v138 op_sel:[0,1] op_sel_hi:[1,1]\n"
      "v_mfma_f32_16x16x32_bf16 v[204:207], v[152:155], a[140:143], v[204:207]\n"
      "ds_read_b128 v[152:155], v115 offset:16384\n"
      "v_add_f32_dpp v200, v200, v200 row_half_mirror row_mask:0xf bank_mask:0xf\n"
      "v_mul_f32 v203, 0xbbaaaaab, v200\n"
      "v_mul_f32 v201, 0xbbaaaaab, v200\n"
      "v_mul_f32 v202, 0xbbaaaaab, v200\n"
      "v_mul_f32 v200, 0xbbaaaaab, v200\n"
      "v_pk_mul_f16 v151, v151, v138 op_sel_hi:[1,0]\n"
      "v_pk_mul_f16 v41, v41, v138 op_sel:[0,1] op_sel_hi:[1,1]\n"
      "v_mfma_f32_16x16x32_bf16 v[216:219], v[242:245], v[8:11], v[200:203]\n"
      "v_pk_mul_f16 v38, v38, v138 op_sel_hi:[1,0]\n"
      "v_pk_mul_f16 v148, v148, v138 op_sel_hi:[1,0]\n"
      "v_mfma_f32_16x16x32_bf16 v[224:227], v[240:243], v[8:11], v[200:203]\n"
      "s_nop 4\n"
      "v_cvt_pk_bf16_f32 v216, v216, v217\n"
      "v_mfma_f32_16x16x32_bf16 v[228:231], v[242:245], v[12:15], v[200:203]\n"
      "s_nop 0\n"
      "v_cvt_pk_bf16_f32 v217, v218, v219\n"
      "v_cvt_pk_bf16_f32 v218, v224, v225\n"
      "v_cvt_pk_bf16_f32 v219, v226, v227\n"
      "v_mfma_f32_16x16x32_bf16 v[212:215], v[240:243], v[12:15], v[200:203]\n"
      "ds_write_b128 v136, v[216:219] offset:12288\n"
      "s_waitcnt lgkmcnt(1)\n"
      "v_mfma_f32_16x16x32_bf16 v[156:159], a[144:147], v[176:179], v[156:159]\n"
      "v_mfma_f32_16x16x32_bf16 v[156:159], a[180:183], v[152:155], v[156:159]\n"
      "s_nop 0\n"
      "v_mfma_f32_16x16x32_bf16 v[168:171], a[148:151], v[176:179], v[168:171]\n"
      "v_cvt_pk_bf16_f32 v228, v228, v229\n"
      "v_cvt_pk_bf16_f32 v229, v230, v231\n"
      "v_mfma_f32_16x16x32_bf16 v[168:171], a[184:187], v[152:155], v[168:171]\n"
      "s_nop 0\n"
      "v_cvt_pk_bf16_f32 v231, v214, v215\n"
      "v_cvt_pk_bf16_f32 v230, v212, v213\n"
      "v_mfma_f32_16x16x32_bf16 v[224:227], v[242:245], v[16:19], v[200:203]\n"
      "ds_write_b128 v136, v[228:231] offset:12416\n"
      "v_mfma_f32_16x16x32_bf16 v[212:215], v[240:243], v[16:19], v[200:203]\n"
      "ds_read_u16 v52, v134\n"
      "s_waitcnt vmcnt(9)\n"
      "v_mfma_f32_16x16x32_bf16 v[216:219], v[144:147], v[20:23], 0\n"
      "ds_read_b64 v[230:231], v133 offset:16\n"
      "s_waitcnt vmcnt(8)\n"
      "v_mfma_f32_16x16x32_bf16 v[216:219], v[144:147], v[24:27], v[216:219]\n"
      "s_waitcnt vmcnt(7)\n"
      "v_mfma_f32_16x16x32_bf16 v[216:219], v[144:147], v[28:31], v[216:219]\n"
      "s_nop 0\n"
      "v_cvt_pk_bf16_f32 v224, v224, v225\n"
      "v_cvt_pk_bf16_f32 v225, v226, v227\n"
      "v_mfma_f32_16x16x32_bf16 v[160:163], a[152:155], v[176:179], v[160:163]\n"
      "s_nop 0\n"
      "v_cvt_pk_bf16_f32 v226, v212, v213\n"
      "v_cvt_pk_bf16_f32 v227, v214, v215\n"
      "v_mfma_f32_16x16x32_bf16 v[160:163], a[188:191], v[152:155], v[160:163]\n"
      "ds_write_b128 v136, v[224:227] offset:12544\n"
      "v_mfma_f32_16x16x32_bf16 v[192:195], a[156:159], v[176:179], v[192:195]\n"
      "ds_read_u16 v56, v134 offset:32\n"
      "v_add_f32_dpp v216, v216, v216 quad_perm:[1,0,3,2] row_mask:0xf bank_mask:0xf\n"
      "v_mfma_f32_16x16x32_bf16 v[192:195], a[192:195], v[152:155], v[192:195]\n"
      "s_nop 0\n"
      "v_add_f32_dpp v216, v216, v216 quad_perm:[2,3,0,1] row_mask:0xf bank_mask:0xf\n"
      "v_mfma_f32_16x16x32_bf16 v[196:199], a[160:163], v[176:179], v[196:199]\n"
      "s_nop 0\n"
      "v_add_f32_dpp v216, v216, v216 row_half_mirror row_mask:0xf bank_mask:0xf\n"
      "v_mul_f32 v217, 0xbbaaaaab, v216\n"
      "v_mfma_f32_16x16x32_bf16 v[196:199], a[196:199], v[152:155], v[196:199]\n"
      "v_mul_f32 v218, 0xbbaaaaab, v216\n"
      "v_mfma_f32_16x16x32_bf16 v[204:207], v[176:179], a[176:179], v[204:207]\n"
      "v_mul_f32 v219, 0xbbaaaaab, v216\n"
      "v_mfma_f32_16x16x32_bf16 v[236:239], a[164:167], v[176:179], v[236:239]\n"
      "v_mul_f32 v216, 0xbbaaaaab, v216\n"
      "v_mfma_f32_16x16x32_bf16 v[232:235], v[176:179], a[168:171], v[232:235]\n"
      "s_nop 0\n"
      "v_mfma_f32_16x16x32_bf16 v[224:227], v[242:245], v[20:23], v[216:219]\n"
      "ds_read_b128 v[144:147], v132 offset:32\n"
      "v_mfma_f32_16x16x32_bf16 v[200:203], v[240:243], v[20:23], v[216:219]\n"
      "s_nop 0\n"
      "v_mfma_f32_16x16x32_bf16 v[180:183], v[176:179], a[172:175], v[180:183]\n"
      "v_mfma_f32_16x16x32_bf16 v[180:183], v[152:155], a[208:211], v[180:183]\n"
      "ds_read_b128 v[176:179], v133\n"
      "v_mfma_f32_16x16x32_bf16 v[204:207], v[152:155], a[212:215], v[204:207]\n"
      "s_nop 1\n"
      "v_cvt_pk_bf16_f32 v224, v224, v225\n"
      "v_mfma_f32_16x16x32_bf16 v[232:235], v[152:155], a[204:207], v[232:235]\n"
      "s_nop 0\n"
      "v_cvt_pk_bf16_f32 v225, v226, v227\n"
      "v_cvt_pk_bf16_f32 v227, v202, v203\n"
      "v_mfma_f32_16x16x32_bf16 v[236:239], a[200:203], v[152:155], v[236:239]\n"
      "v_cvt_pk_bf16_f32 v226, v200, v201\n"
      "v_mfma_f32_16x16x32_bf16 v[152:155], v[240:243], v[24:27], v[216:219]\n"
      "ds_write_b128 v136, v[224:227] offset:15360\n"
      "v_mfma_f32_16x16x32_bf16 v[200:203], v[242:245], v[24:27], v[216:219]\n"
      "s_nop 7\n"
      "v_cvt_pk_bf16_f32 v200, v200, v201\n"
      "v_cvt_pk_bf16_f32 v201, v202, v203\n"
      "v_cvt_pk_bf16_f32 v203, v154, v155\n"
      "v_cvt_pk_bf16_f32 v202, v152, v153\n"
      "s_waitcnt vmcnt(0)\n"
      "ds_read_b128 v[152:155], v119 offset:41728\n"
      "ds_read_b32 v138, v122 offset:4864\n"
      "v_mfma_f32_16x16x32_bf16 v[224:227], v[242:245], v[28:31], v[216:219]\n"
      "ds_read_b64 v[228:229], v132 offset:24\n"
      "ds_write_b128 v136, v[200:203] offset:15488\n"
      "v_mfma_f32_16x16x32_bf16 v[212:215], v[240:243], v[28:31], v[216:219]\n"
      "ds_read_u16 v60, v134 offset:64\n"
      "s_nop 3\n"
      "v_cvt_pk_bf16_f32 v224, v224, v225\n"
      "v_cvt_pk_bf16_f32 v225, v226, v227\n"
      "s_nop 0\n"
      "v_cvt_pk_bf16_f32 v226, v212, v213\n"
      "v_cvt_pk_bf16_f32 v227, v214, v215\n"
      "ds_write_b128 v136, v[224:227] offset:15616\n"
      "s_waitcnt lgkmcnt(5)\n"
      "v_or3_b32 v152, v152, v153, v154\n"
      "v_and_b32_e64 v152, s40, v152\n"
      "s_waitcnt lgkmcnt(4)\n"
      "v_bitop3_b32 v138, v138, v155, s40 bitop3:0xa8\n"
      "v_or3_b32 v152, v152, v138, s4\n"
      "ds_write_b32 v129, v152 offset:3072\n"
      "s_waitcnt lgkmcnt(0)\n"
      "s_barrier\n"
      "v_mfma_f32_16x16x32_f16 v[152:155], v[164:167], v[172:175], v[78:81]\n"
      "ds_read_b64_tr_b16 a[220:221], v250 offset:8\n"
      "v_fma_f32 v138, v192, v192, s38\n"
      "v_mfma_f32_16x16x32_f16 v[152:155], v[34:37], v[32:35], v[152:155]\n"
      "ds_read_b64_tr_b16 a[224:225], v251\n"
      "v_fmac_f32_e64 v138, v193, v193\n"
      "v_mfma_f32_16x16x32_f16 v[200:203], v[184:187], v[172:175], v[82:85]\n"
      "ds_read_b64_tr_b16 a[216:217], v250\n"
      "v_fma_f32 v136, v156, v156, s38\n"
      "v_mfma_f32_16x16x32_f16 v[200:203], v[40:43], v[32:35], v[200:203]\n"
      "ds_read_b64_tr_b16 a[222:223], v250 offset:6152\n"
      "v_fmac_f32_e64 v138, v194, v194\n"
      "v_mfma_f32_16x16x32_f16 v[212:215], v[144:147], v[172:175], v[70:73]\n"
      "ds_read_b64_tr_b16 a[218:219], v250 offset:6144\n"
      "v_fmac_f32 v136, v157, v157\n"
      "v_mfma_f32_16x16x32_f16 v[212:215], v[176:179], v[164:167], v[212:215]\n"
      "ds_read_b64_tr_b16 a[228:229], v250 offset:18432\n"
      "v_fmac_f32 v136, v158, v158\n"
      "v_mfma_f32_16x16x32_f16 v[212:215], v[228:231], v[32:35], v[212:215]\n"
      "ds_read_b64_tr_b16 a[226:227], v251 offset:6144\n"
      "v_fmac_f32 v138, v195, v195\n"
      "v_fmac_f32 v138, v196, v196\n"
      "v_fmac_f32 v136, v159, v159\n"
      "v_fmac_f32 v136, v168, v168\n"
      "v_fmac_f32 v136, v169, v169\n"
      "v_fmac_f32 v136, v170, v170\n"
      "v_fmac_f32 v138, v197, v197\n"
      "v_fmac_f32 v138, v198, v198\n"
      "v_fmac_f32 v136, v171, v171\n"
      "v_fmac_f32 v136, v160, v160\n"
      "v_fmac_f32 v138, v199, v199\n"
      "v_fmac_f32 v138, v236, v236\n"
      "v_fmac_f32 v136, v161, v161\n"
      "v_fmac_f32 v136, v162, v162\n"
      "v_fmac_f32 v136, v163, v163\n"
      "v_fmac_f32 v138, v237, v237\n"
      "v_fmac_f32_e64 v138, v238, v238\n"
      "v_cvt_pk_f16_f32 v192, v192, v193\n"
      "v_cvt_pk_f16_f32 v193, v194, v195\n"
      "v_fmac_f32 v138, v239, v239\n"
      "v_cvt_pk_f16_f32 v194, v196, v197\n"
      "v_cvt_pk_f16_f32 v46, v236, v237\n"
      "v_permlane16_swap_b32 v136, v138\n"
      "v_add_f32 v136, v136, v138\n"
      "v_mov_b32 v138, v136\n"
      "v_cvt_pk_f16_f32 v195, v198, v199\n"
      "v_cvt_pk_f16_f32 v156, v156, v157\n"
      "v_permlane32_swap_b32 v136, v138\n"
      "v_add_f32 v136, v136, v138\n"
      "v_rsq_f32 v136, v136\n"
      "v_cvt_pk_f16_f32 v47, v238, v239\n"
      "v_mov_b32 v138, v136\n"
      "v_cmp_nge_f32 vcc, 0x43800000, v138\n"
      "v_cvt_pk_f16_f32 v157, v158, v159\n"
      "v_cvt_pk_f16_f32 v158, v168, v169\n"
      "v_cvt_pk_f16_f32 v159, v170, v171\n"
      "v_cvt_pk_f16_f32 v44, v160, v161\n"
      "v_cvt_pk_f16_f32 v45, v162, v163\n"
      "v_cvt_pk_bf16_f32 v50, v232, v233\n"
      "v_cvt_pk_bf16_f32 v55, v182, v183\n"
      "v_cvt_pk_bf16_f32 v58, v204, v205\n"
      "v_cvt_pk_bf16_f32 v51, v234, v235\n"
      "v_cvt_pk_bf16_f32 v59, v206, v207\n"
      "v_cvt_pk_bf16_f32 v54, v180, v181\n"
      "s_or_b64 s[34:35], s[34:35], vcc\n"
      "v_cmp_nle_f32 vcc, 0x3a000000, v138\n"
      "v_permlane16_swap_b32 v136, v138\n"
      "v_mul_f32_e64 v136, s36, v136\n"
      "v_cvt_pk_f16_f32 v136, v136, v138\n"
      "v_pk_mul_f16 v192, v192, v136 op_sel:[0,1] op_sel_hi:[1,1]\n"
      "v_pk_mul_f16 v46, v46, v136 op_sel:[0,1] op_sel_hi:[1,1]\n"
      "v_pk_mul_f16 v193, v193, v136 op_sel:[0,1] op_sel_hi:[1,1]\n"
      "v_pk_mul_f16 v195, v195, v136 op_sel:[0,1] op_sel_hi:[1,1]\n"
      "v_pk_mul_f16 v194, v194, v136 op_sel:[0,1] op_sel_hi:[1,1]\n"
      "v_pk_mul_f16 v47, v47, v136 op_sel:[0,1] op_sel_hi:[1,1]\n"
      "v_pk_mul_f16 v157, v157, v136 op_sel_hi:[1,0]\n"
      "v_pk_mul_f16 v156, v156, v136 op_sel_hi:[1,0]\n"
      "v_mfma_f32_16x16x32_f16 v[160:163], v[192:195], v[172:175], v[86:89]\n"
      "ds_read_b64_tr_b16 a[232:233], v250 offset:18440\n"
      "v_pk_mul_f16 v44, v44, v136 op_sel_hi:[1,0]\n"
      "v_mfma_f32_16x16x32_f16 v[160:163], v[46:49], v[32:35], v[160:163]\n"
      "ds_read_b64_tr_b16 a[236:237], v251 offset:18432\n"
      "ds_read_b64_tr_b16 a[234:235], v250 offset:24584\n"
      "ds_read_b64_tr_b16 a[240:241], v137\n"
      "ds_read_b64_tr_b16 a[238:239], v251 offset:24576\n"
      "ds_read_b64_tr_b16 a[230:231], v250 offset:24576\n"
      "ds_read_b64_tr_b16 a[246:247], v137 offset:6152\n"
      "ds_read_b64_tr_b16 a[242:243], v137 offset:6144\n"
      "ds_read_b64_tr_b16 a[244:245], v137 offset:8\n"
      "ds_read_b64_tr_b16 a[248:249], v140\n"
      "ds_read_b64_tr_b16 a[250:251], v140 offset:6144\n"
      "ds_read_b64_tr_b16 a[254:255], v137 offset:24576\n"
      "ds_read_b64_tr_b16 a[252:253], v137 offset:18432\n"
      "ds_read_b64_tr_b16 v[6:7], v140 offset:24576\n"
      "ds_read_b64_tr_b16 v[4:5], v140 offset:18432\n"
      "ds_read_b64_tr_b16 v[8:9], v250 offset:12288\n"
      "ds_read_b64_tr_b16 v[0:1], v137 offset:18440\n"
      "ds_read_b64_tr_b16 v[2:3], v137 offset:24584\n"
      "ds_read_b64_tr_b16 v[16:17], v251 offset:12288\n"
      "ds_read_b64_tr_b16 v[14:15], v250 offset:30728\n"
      "ds_read_b64_tr_b16 v[12:13], v250 offset:12296\n"
      "ds_read_b64_tr_b16 v[10:11], v250 offset:30720\n"
      "ds_read_b64_tr_b16 v[20:21], v137 offset:12288\n"
      "ds_read_b64_tr_b16 v[18:19], v251 offset:30720\n"
      "ds_read_b64_tr_b16 v[26:27], v137 offset:30728\n"
      "ds_read_b64_tr_b16 v[24:25], v137 offset:12296\n"
      "ds_read_b64_tr_b16 v[30:31], v140 offset:30720\n"
      "ds_read_b64_tr_b16 v[22:23], v137 offset:30720\n"
      "ds_read_b64_tr_b16 v[28:29], v140 offset:12288\n"
      "v_pk_mul_f16 v45, v45, v136 op_sel_hi:[1,0]\n"
      "v_pk_mul_f16 v159, v159, v136 op_sel_hi:[1,0]\n"
      "v_pk_mul_f16 v158, v158, v136 op_sel_hi:[1,0]\n"
      "s_or_b64 s[34:35], s[34:35], vcc\n"
      "s_waitcnt lgkmcnt(0)\n"
      "s_barrier\n"
      "s_or_b64 vcc, s[48:49], s[50:51]\n"
      "s_or_b64 vcc, vcc, s[34:35]\n"
      "s_cbranch_scc1 L_gen_8_%=\n"
      "L_fast_11_%=:\n"
      "v_mfma_f32_16x16x32_f16 v[172:175], v[164:167], v[148:151], v[90:93]\n"
      "v_exp_f32 v200, v200\n"
      "v_mfma_f32_16x16x32_f16 v[172:175], v[34:37], v[38:41], v[172:175]\n"
      "ds_read_b128 v[136:139], v128 offset:3072\n"
      "v_exp_f32 v153, v153\n"
      "v_exp_f32 v152, v152\n"
      "v_exp_f32 v212, v212\n"
      "v_mfma_f32_16x16x32_f16 v[140:143], v[184:187], v[148:151], v[94:97]\n"
      "v_exp_f32 v154, v154\n"
      "v_mfma_f32_16x16x32_f16 v[140:143], v[40:43], v[38:41], v[140:143]\n"
      "v_exp_f32 v201, v201\n"
      "v_mfma_f32_16x16x32_f16 v[168:171], v[144:147], v[148:151], v[72:75]\n"
      "v_exp_f32 v203, v203\n"
      "v_mfma_f32_16x16x32_f16 v[168:171], v[176:179], v[184:187], v[168:171]\n"
      "v_exp_f32 v155, v155\n"
      "v_mfma_f32_16x16x32_f16 v[168:171], v[228:231], v[38:41], v[168:171]\n"
      "v_exp_f32 v172, v172\n"
      "v_mfma_f32_16x16x32_f16 v[180:183], v[192:195], v[148:151], v[98:101]\n"
      "v_cvt_pk_bf16_f32 v152, v152, v153\n"
      "v_cvt_pk_bf16_f32 v153, v154, v155\n"
      "v_mfma_f32_16x16x32_f16 v[180:183], v[46:49], v[38:41], v[180:183]\n"
      "s_waitcnt lgkmcnt(0)\n"
      "v_exp_f32 v202, v202\n"
      "v_mfma_f32_16x16x32_f16 v[204:207], v[164:167], v[156:159], v[102:105]\n"
      "v_exp_f32 v160, v160\n"
      "v_mfma_f32_16x16x32_f16 v[204:207], v[34:37], v[44:47], v[204:207]\n"
      "v_exp_f32_e64 v163, v163\n"
      "v_mfma_f32_16x16x32_f16 v[164:167], v[184:187], v[156:159], v[106:109]\n"
      "v_cvt_pk_bf16_f32 v154, v200, v201\n"
      "v_bitop3_b32 v138, v138, v139, s4 bitop3:0x7e\n"
      "v_mfma_f32_16x16x32_f16 v[164:167], v[40:43], v[44:47], v[164:167]\n"
      "v_exp_f32_e64 v161, v161\n"
      "v_mfma_f32_16x16x32_f16 v[184:187], v[144:147], v[156:159], v[74:77]\n"
      "v_bitop3_b32 v136, v136, v137, s4 bitop3:0x7e\n"
      "v_cvt_pk_bf16_f32 v155, v202, v203\n"
      "v_mfma_f32_16x16x32_f16 v[184:187], v[176:179], v[192:195], v[184:187]\n"
      "v_cvt_pk_bf16_f32 v64, v212, 0\n"
      "v_exp_f32_e64 v180, v180\n"
      "v_mfma_f32_16x16x32_f16 v[148:151], v[176:179], v[144:147], v[76:79]\n"
      "v_exp_f32 v182, v182\n"
      "v_mfma_f32_16x16x32_f16 v[200:203], v[192:195], v[156:159], v[110:113]\n"
      "v_exp_f32 v162, v162\n"
      "v_mfma_f32_16x16x32_f16 v[200:203], v[46:49], v[44:47], v[200:203]\n"
      "v_cvt_pk_bf16_f32 v63, v162, v163\n"
      "v_or_b32 v136, v136, v138\n"
      "v_cvt_pk_bf16_f32 v62, v160, v161\n"
      "v_mfma_f32_16x16x32_f16 v[184:187], v[228:231], v[44:47], v[184:187]\n"
      "v_exp_f32 v168, v168\n"
      "v_exp_f32 v183, v183\n"
      "v_mfma_f32_16x16x32_bf16 v[144:147], v[66:69], v[152:155], 0\n"
      "v_cmp_ne_u32 vcc, 0, v136\n"
      "v_cndmask_b32 v213, v213, v169, s[42:43]\n"
      "v_mfma_f32_16x16x32_bf16 v[144:147], v[66:69], v[62:65], v[144:147]\n"
      "s_mov_b64 s[48:49], vcc\n"
      "v_exp_f32 v143, v143\n"
      "v_mfma_f32_16x16x32_bf16 v[156:159], v[208:211], v[152:155], 0\n"
      "v_exp_f32 v140, v140\n"
      "v_mfma_f32_16x16x32_bf16 v[156:159], v[50:53], v[62:65], v[156:159]\n"
      "v_exp_f32 v174, v174\n"
      "v_exp_f32 v142, v142\n"
      "v_mfma_f32_16x16x32_bf16 v[160:163], v[188:191], v[152:155], 0\n"
      "s_add_u32 s28, s27, 2\n"
      "v_exp_f32 v173, v173\n"
      "v_mfma_f32_16x16x32_bf16 v[160:163], v[54:57], v[62:65], v[160:163]\n"
      "s_cbranch_vccnz L_flg_12_%=\n"
      "L_fb_13_%=:\n"
      "v_cvt_pk_bf16_f32 v172, v172, v173\n"
      "v_mfma_f32_16x16x32_bf16 v[176:179], v[220:223], v[152:155], 0\n"
      "s_min_u32 m0, s28, s3\n"
      "v_exp_f32 v175, v175\n"
      "v_mfma_f32_16x16x32_bf16 v[176:179], v[58:61], v[62:65], v[176:179]\n"
      "s_movrels_b32 s9, s80\n"
      "v_exp_f32 v181, v181\n"
      "v_rcp_f32 v144, v144\n"
      "v_cvt_pk_bf16_f32 v173, v174, v175\n"
      "v_mfma_f32_16x16x16_f16 v[148:151], v[230:231], v[228:229], v[148:151]\n"
      "s_lshr_b32 s8, s9, 8\n"
      "v_exp_f32_e64 v141, v141\n"
      "v_cvt_pk_bf16_f32 v63, v182, v183\n"
      "v_cvt_pk_bf16_f32 v62, v180, v181\n"
      "v_cvt_pk_bf16_f32 v174, v140, v141\n"
      "v_cvt_pk_bf16_f32 v175, v142, v143\n"
      "v_mul_f32 v156, v144, v156\n"
      "v_cvt_pk_bf16_f32 v64, v168, 0\n"
      "v_mul_f32 v157, v144, v157\n"
      "v_mfma_f32_16x16x32_bf16 v[180:183], v[66:69], v[172:175], 0\n"
      "ds_read_b128 v[136:139], v114 offset:18432\n"
      "v_cvt_pk_bf16_f32 v156, v156, v157\n"
      "v_mfma_f32_16x16x32_bf16 v[180:183], v[66:69], v[62:65], v[180:183]\n"
      "ds_read_b128 v[140:143], v115 offset:18432\n"
      "v_mul_f32 v162, v144, v162\n"
      "v_mul_f32 v159, v144, v159\n"
      "v_mfma_f32_16x16x32_bf16 v[192:195], v[208:211], v[172:175], 0\n"
      "s_and_b32 s9, s9, 0xff\n"
      "v_mul_f32_e64 v158, v144, v158\n"
      "v_cvt_pk_bf16_f32 v157, v158, v159\n"
      "v_mfma_f32_16x16x32_bf16 v[192:195], v[50:53], v[62:65], v[192:195]\n"
      "s_mul_i32 s10, s8, 73\n"
      "v_mul_f32 v163, v144, v163\n"
      "v_mul_f32 v160, v144, v160\n"
      "v_mfma_f32_16x16x32_bf16 v[168:171], v[188:191], v[172:175], 0\n"
      "s_add_u32 s10, s10, s9\n"
      "v_mul_f32 v161, v144, v161\n"
      "v_cvt_pk_bf16_f32 v158, v160, v161\n"
      "v_mfma_f32_16x16x32_bf16 v[168:171], v[54:57], v[62:65], v[168:171]\n"
      "s_mul_i32 s28, s10, 4802\n"
      "v_cvt_pk_bf16_f32 v159, v162, v163\n"
      "v_mul_f32 v176, v144, v176\n"
      "v_mfma_f32_16x16x32_bf16 v[196:199], v[220:223], v[172:175], 0\n"
      "s_add_u32 s56, s18, s28\n"
      "v_mul_f32 v177, v144, v177\n"
      "v_mul_f32 v179, v144, v179\n"
      "v_mfma_f32_16x16x32_bf16 v[196:199], v[58:61], v[62:65], v[196:199]\n"
      "ds_write_b128 v124, v[156:159]\n"
      "v_cvt_pk_bf16_f32 v176, v176, v177\n"
      "v_mul_f32_e64 v178, v144, v178\n"
      "s_waitcnt lgkmcnt(1)\n"
      "v_mfma_f32_16x16x32_bf16 v[144:147], a[0:3], v[136:139], 0\n"
      "s_addc_u32 s57, s19, 0\n"
      "v_cndmask_b32 v213, v213, v185, s[44:45]\n"
      "v_cvt_pk_bf16_f32 v177, v178, v179\n"
      "v_mfma_f32_16x16x32_bf16 v[144:147], a[36:39], v[140:143], v[144:147]\n"
      "s_and_b32 s56, s56, -16\n"
      "v_cndmask_b32 v213, v213, v149, s[46:47]\n"
      "v_exp_f32 v213, v213\n"
      "v_mfma_f32_16x16x32_bf16 v[148:151], a[4:7], v[136:139], 0\n"
      "s_sub_u32 s58, s56, 110592\n"
      "v_cvt_pk_bf16_f32 v213, v213, v213\n"
      "v_mov_b32 v178, 0\n"
      "v_mfma_f32_16x16x32_bf16 v[148:151], a[40:43], v[140:143], v[148:151]\n"
      "ds_write_b16 v130, v213\n"
      "v_mfma_f32_16x16x32_bf16 v[152:155], a[8:11], v[136:139], 0\n"
      "s_subb_u32 s59, s57, 0\n"
      "ds_read_b128 v[228:231], v131 offset:64\n"
      "v_mfma_f32_16x16x32_bf16 v[152:155], a[44:47], v[140:143], v[152:155]\n"
      "ds_read_b128 v[212:215], v131\n"
      "v_exp_f32 v200, v200\n"
      "v_exp_f32 v204, v204\n"
      "v_exp_f32 v202, v202\n"
      "v_exp_f32 v207, v207\n"
      "v_exp_f32 v205, v205\n"
      "v_mfma_f32_16x16x32_bf16 v[156:159], a[12:15], v[136:139], 0\n"
      "s_sub_u32 s56, s56, 73728\n"
      "v_exp_f32 v206, v206\n"
      "v_mfma_f32_16x16x32_bf16 v[156:159], a[48:51], v[140:143], v[156:159]\n"
      "s_subb_u32 s57, s57, 0\n"
      "v_exp_f32 v166, v166\n"
      "v_cvt_pk_bf16_f32 v204, v204, v205\n"
      "v_cvt_pk_bf16_f32 v205, v206, v207\n"
      "v_exp_f32 v203, v203\n"
      "v_exp_f32 v164, v164\n"
      "v_exp_f32 v165, v165\n"
      "v_exp_f32 v167, v167\n"
      "v_exp_f32 v201, v201\n"
      "v_cvt_pk_bf16_f32 v207, v166, v167\n"
      "s_waitcnt lgkmcnt(0)\n"
      "v_mfma_f32_16x16x32_bf16 v[232:235], v[212:215], v[66:69], 0\n"
      "s_mul_i32 s28, s8, 1373568\n"
      "v_cvt_pk_bf16_f32 v62, v200, v201\n"
      "v_cvt_pk_bf16_f32 v63, v202, v203\n"
      "v_mfma_f32_16x16x32_bf16 v[232:235], v[228:231], v[66:69], v[232:235]\n"
      "s_mul_i32 s29, s9, 2688\n"
      "v_exp_f32 v184, v184\n"
      "v_mfma_f32_16x16x32_bf16 v[200:203], v[212:215], v[208:211], 0\n"
      "s_add_u32 s28, s28, s29\n"
      "v_rcp_f32_e64 v180, v180\n"
      "v_mfma_f32_16x16x32_bf16 v[200:203], v[228:231], v[50:53], v[200:203]\n"
      "s_cmp_eq_u32 s8, 72\n"
      "v_cvt_pk_bf16_f32 v206, v164, v165\n"
      "v_cvt_pk_bf16_f32 v64, v184, 0\n"
      "v_mfma_f32_16x16x32_bf16 v[184:187], v[212:215], v[188:191], 0\n"
      "s_cselect_b32 s8, 0, 100270464\n"
      "v_mul_f32 v193, v180, v193\n"
      "v_mul_f32 v192, v180, v192\n"
      "v_mfma_f32_16x16x32_bf16 v[184:187], v[228:231], v[54:57], v[184:187]\n"
      "s_cmp_eq_u32 s9, 72\n"
      "v_cvt_pk_bf16_f32 v192, v192, v193\n"
      "v_mul_f32 v195, v180, v195\n"
      "v_mul_f32 v194, v180, v194\n"
      "v_mfma_f32_16x16x32_bf16 v[160:163], v[66:69], v[204:207], 0\n"
      "s_cselect_b32 s9, 0, 196224\n"
      "v_mul_f32 v168, v180, v168\n"
      "v_cvt_pk_bf16_f32 v193, v194, v195\n"
      "v_mfma_f32_16x16x32_bf16 v[160:163], v[66:69], v[62:65], v[160:163]\n"
      "s_add_u32 s10, s8, s9\n"
      "v_mul_f32 v171, v180, v171\n"
      "v_mul_f32_e64 v169, v180, v169\n"
      "v_mfma_f32_16x16x32_bf16 v[164:167], v[208:211], v[204:207], 0\n"
      "s_add_u32 s28, s28, s10\n"
      "v_cvt_pk_bf16_f32 v194, v168, v169\n"
      "v_mul_f32_e64 v170, v180, v170\n"
      "v_mfma_f32_16x16x32_bf16 v[164:167], v[50:53], v[62:65], v[164:167]\n"
      "v_mul_f32_e64 v197, v180, v197\n"
      "v_cvt_pk_bf16_f32 v195, v170, v171\n"
      "v_mfma_f32_16x16x32_bf16 v[168:171], v[220:223], v[204:207], 0\n"
      "s_add_u32 s52, s16, s28\n"
      "v_mul_f32 v199, v180, v199\n"
      "v_mul_f32_e64 v196, v180, v196\n"
      "v_mfma_f32_16x16x32_bf16 v[168:171], v[58:61], v[62:65], v[168:171]\n"
      "ds_write_b128 v124, v[192:195] offset:1024\n"
      "v_mfma_f32_16x16x32_bf16 v[172:175], v[188:191], v[204:207], 0\n"
      "s_addc_u32 s53, s17, 0\n"
      "v_mul_f32 v198, v180, v198\n"
      "v_mfma_f32_16x16x32_bf16 v[248:251], v[212:215], v[220:223], 0\n"
      "s_cmp_lg_u32 s10, s11\n"
      "v_cvt_pk_bf16_f32 v196, v196, v197\n"
      "v_cvt_pk_bf16_f32 v197, v198, v199\n"
      "v_mfma_f32_16x16x32_bf16 v[248:251], v[228:231], v[58:61], v[248:251]\n"
      "s_cbranch_scc1 L_edge_15_%=\n"
      "L_eb_16_%=:\n"
      "v_mfma_f32_16x16x32_bf16 v[172:175], v[54:57], v[62:65], v[172:175]\n"
      "ds_write2st64_b64 v125, v[176:177], v[196:197] offset0:0 offset1:2\n"
      "ds_write_b32 v129, v178\n"
      "v_cndmask_b32 v200, v200, v184, s[42:43]\n"
      "v_mov_b32_dpp v232, v232 quad_perm:[0,0,0,0] row_mask:0xf bank_mask:0xf\n"
      "v_rcp_f32 v232, v232\n"
      "s_nop 0\n"
      "v_cndmask_b32 v200, v200, v248, s[44:45]\n"
      "v_mul_f32_e64 v200, v232, v200\n"
      "v_cvt_pk_bf16_f32 v200, v200, v200\n"
      "ds_write_b16 v135, v200\n"
      "L_join_10_%=:\n"
      "s_mov_b32 s27, 1\n"
      ".p2align 6\n"
      "L_loop_%=:\n"
      "v_mfma_f32_16x16x32_bf16 v[248:251], a[16:19], v[136:139], 0\n"
      "ds_read_b128 v[228:231], v115 offset:24576\n"
      "s_add_u32 m0, s76, 0\n"
      "v_mfma_f32_16x16x32_bf16 v[248:251], a[52:55], v[140:143], v[248:251]\n"
      "ds_read_b128 v[212:215], v114 offset:24576\n"
      "v_rcp_f32 v160, v160\n"
      "v_mfma_f32_16x16x32_bf16 v[220:223], a[20:23], v[136:139], 0\n"
      "v_mul_f32 v165, v160, v165\n"
      "v_mul_f32 v164, v160, v164\n"
      "v_mfma_f32_16x16x32_bf16 v[220:223], a[56:59], v[140:143], v[220:223]\n"
      "s_waitcnt lgkmcnt(2)\n"
      "s_barrier\n"
      "v_mfma_f32_16x16x32_bf16 v[232:235], v[136:139], a[24:27], 0\n"
      "ds_read_b128 v[216:219], v128\n"
      "v_cvt_pk_bf16_f32 v164, v164, v165\n"
      "v_mfma_f32_16x16x32_bf16 v[232:235], v[140:143], a[60:63], v[232:235]\n"
      "global_load_lds_dwordx4 v116, s[52:53]\n"
      "v_mfma_f32_16x16x32_bf16 v[224:227], v[136:139], a[28:31], 0\n"
      "v_mul_f32 v167, v160, v167\n"
      "v_mul_f32 v166, v160, v166\n"
      "v_mfma_f32_16x16x32_bf16 v[224:227], v[140:143], a[64:67], v[224:227]\n"
      "s_waitcnt lgkmcnt(1)\n"
      "v_mul_f32 v170, v160, v170\n"
      "v_mul_f32 v172, v160, v172\n"
      "v_mfma_f32_16x16x32_bf16 v[144:147], a[72:75], v[212:215], v[144:147]\n"
      "v_mul_f32 v173, v160, v173\n"
      "v_mul_f32 v174, v160, v174\n"
      "v_mfma_f32_16x16x32_bf16 v[144:147], a[108:111], v[228:231], v[144:147]\n"
      "s_add_u32 m0, s76, 6016\n"
      "v_cvt_pk_bf16_f32 v165, v166, v167\n"
      "v_mul_f32 v175, v160, v175\n"
      "v_mfma_f32_16x16x32_bf16 v[148:151], a[76:79], v[212:215], v[148:151]\n"
      "v_cvt_pk_bf16_f32 v166, v172, v173\n"
      "v_cvt_pk_bf16_f32 v167, v174, v175\n"
      "v_mfma_f32_16x16x32_bf16 v[148:151], a[112:115], v[228:231], v[148:151]\n"
      "ds_write_b128 v124, v[164:167] offset:2048\n"
      "v_mfma_f32_16x16x32_bf16 v[172:175], v[136:139], a[32:35], 0\n"
      "ds_read_b128 v[136:139], v114 offset:30720\n"
      "v_mul_f32_e64 v169, v160, v169\n"
      "v_mfma_f32_16x16x32_bf16 v[172:175], v[140:143], a[68:71], v[172:175]\n"
      "global_load_lds_dwordx4 v116, s[52:53] offset:128\n"
      "v_mfma_f32_16x16x32_bf16 v[152:155], a[80:83], v[212:215], v[152:155]\n"
      "ds_read_b128 v[140:143], v115 offset:30720\n"
      "s_add_u32 m0, s76, 12032\n"
      "v_mfma_f32_16x16x32_bf16 v[152:155], a[116:119], v[228:231], v[152:155]\n"
      "s_waitcnt lgkmcnt(3)\n"
      "v_mul_f32 v168, v160, v168\n"
      "v_cvt_pk_bf16_f32 v168, v168, v169\n"
      "v_mfma_f32_16x16x32_bf16 v[156:159], a[84:87], v[212:215], v[156:159]\n"
      "v_mul_f32_e64 v171, v160, v171\n"
      "v_cvt_pk_bf16_f32 v169, v170, v171\n"
      "v_mfma_f32_16x16x32_bf16 v[156:159], a[120:123], v[228:231], v[156:159]\n"
      "ds_write_b64 v125, v[168:169] offset:2048\n"
      "v_mfma_f32_16x16x32_bf16 v[248:251], a[88:91], v[212:215], v[248:251]\n"
      "v_add_f32 v216, |v216|, |v217|\n"
      "v_add_f32 v218, |v218|, |v219|\n"
      "v_mfma_f32_16x16x32_bf16 v[248:251], a[124:127], v[228:231], v[248:251]\n"
      "ds_read_b128 v[164:167], v114 offset:20480\n"
      "v_add_f32 v216, v216, v218\n"
      "v_mfma_f32_16x16x32_bf16 v[220:223], a[92:95], v[212:215], v[220:223]\n"
      "v_rsq_f32 v216, v216\n"
      "v_mfma_f32_16x16x32_bf16 v[220:223], a[128:131], v[228:231], v[220:223]\n"
      "global_load_lds_dwordx4 v116, s[52:53] offset:256\n"
      "v_mfma_f32_16x16x32_bf16 v[232:235], v[212:215], a[96:99], v[232:235]\n"
      "s_waitcnt lgkmcnt(2)\n"
      "v_mul_f32 v216, s41, v216\n"
      "v_mov_b32 v160, v216\n"
      "v_mfma_f32_16x16x32_bf16 v[232:235], v[228:231], a[132:135], v[232:235]\n"
      "s_nop 0\n"
      "v_permlane32_swap_b32 v216, v160\n"
      "v_mfma_f32_16x16x32_bf16 v[224:227], v[212:215], a[100:103], v[224:227]\n"
      "s_add_u32 m0, s77, 0\n"
      "v_mov_b32 v161, v216\n"
      "v_mul_f32 v200, v160, v200\n"
      "v_mfma_f32_16x16x32_bf16 v[224:227], v[228:231], a[136:139], v[224:227]\n"
      "ds_read_b128 v[168:171], v115 offset:20480\n"
      "v_permlane16_swap_b32 v216, v161\n"
      "v_mfma_f32_16x16x32_bf16 v[172:175], v[212:215], a[104:107], v[172:175]\n"
      "v_mul_f32 v177, v216, v177\n"
      "v_mul_f32 v176, v216, v176\n"
      "v_mfma_f32_16x16x32_bf16 v[172:175], v[228:231], a[140:143], v[172:175]\n"
      "v_mul_f32 v178, v216, v178\n"
      "v_cvt_pk_bf16_f32 v176, v176, v177\n"
      "v_mfma_f32_16x16x32_bf16 v[144:147], a[144:147], v[136:139], v[144:147]\n"
      "v_mul_f32 v180, v216, v180\n"
      "v_mul_f32 v179, v216, v179\n"
      "v_mfma_f32_16x16x32_bf16 v[144:147], a[180:183], v[140:143], v[144:147]\n"
      "global_load_lds_dwordx4 v117, s[52:53]\n"
      "v_mfma_f32_16x16x32_bf16 v[148:151], a[148:151], v[136:139], v[148:151]\n"
      "v_mul_f32 v181, v216, v181\n"
      "v_cvt_pk_bf16_f32 v177, v178, v179\n"
      "v_mfma_f32_16x16x32_bf16 v[148:151], a[184:187], v[140:143], v[148:151]\n"
      "v_mul_f32 v183, v216, v183\n"
      "v_mul_f32 v182, v216, v182\n"
      "v_mfma_f32_16x16x32_bf16 v[152:155], a[152:155], v[136:139], v[152:155]\n"
      "s_add_u32 m0, s78, 0\n"
      "v_cvt_pk_bf16_f32 v178, v180, v181\n"
      "v_cvt_pk_bf16_f32 v179, v182, v183\n"
      "v_mfma_f32_16x16x32_bf16 v[152:155], a[188:191], v[140:143], v[152:155]\n"
      "ds_write_b128 v126, v[176:179] offset:18432\n"
      "v_mfma_f32_16x16x32_bf16 v[156:159], a[156:159], v[136:139], v[156:159]\n"
      "v_mul_f32 v184, v216, v184\n"
      "v_mul_f32 v185, v216, v185\n"
      "v_mfma_f32_16x16x32_bf16 v[156:159], a[192:195], v[140:143], v[156:159]\n"
      "s_waitcnt lgkmcnt(1)\n"
      "v_mul_f32 v186, v216, v186\n"
      "v_cvt_pk_bf16_f32 v184, v184, v185\n"
      "v_mfma_f32_16x16x32_bf16 v[248:251], a[160:163], v[136:139], v[248:251]\n"
      "v_mul_f32 v187, v216, v187\n"
      "v_mul_f32 v188, v161, v188\n"
      "v_mfma_f32_16x16x32_bf16 v[248:251], a[196:199], v[140:143], v[248:251]\n"
      "s_mov_b32 exec_hi, 0\n"
      "global_load_lds_dwordx4 v118, s[52:53]\n"
      "s_mov_b32 exec_hi, -1\n"
      "v_mfma_f32_16x16x32_bf16 v[220:223], a[164:167], v[136:139], v[220:223]\n"
      "v_mul_f32_e64 v189, v161, v189\n"
      "v_cvt_pk_bf16_f32 v185, v186, v187\n"
      "v_mfma_f32_16x16x32_bf16 v[220:223], a[200:203], v[140:143], v[220:223]\n"
      "ds_read_b128 v[176:179], v114 offset:26624\n"
      "v_cvt_pk_bf16_f32 v188, v188, v189\n"
      "v_mfma_f32_16x16x32_bf16 v[232:235], v[136:139], a[168:171], v[232:235]\n"
      "ds_read_b128 v[180:183], v115 offset:26624\n"
      "v_mul_f32 v191, v161, v191\n"
      "v_mfma_f32_16x16x32_bf16 v[232:235], v[140:143], a[204:207], v[232:235]\n"
      "s_add_u32 m0, s76, 110592\n"
      "v_mul_f32 v192, v161, v192\n"
      "v_mul_f32_e64 v190, v161, v190\n"
      "v_mfma_f32_16x16x32_bf16 v[224:227], v[136:139], a[172:175], v[224:227]\n"
      "v_cvt_pk_bf16_f32 v189, v190, v191\n"
      "v_mul_f32 v193, v161, v193\n"
      "v_mfma_f32_16x16x32_bf16 v[224:227], v[140:143], a[208:211], v[224:227]\n"
      "v_mul_f32 v195, v161, v195\n"
      "v_cvt_pk_bf16_f32 v190, v192, v193\n"
      "v_mfma_f32_16x16x32_bf16 v[172:175], v[136:139], a[176:179], v[172:175]\n"
      "v_mul_f32_e64 v194, v161, v194\n"
      "v_cvt_pk_bf16_f32 v191, v194, v195\n"
      "v_mfma_f32_16x16x32_bf16 v[172:175], v[140:143], a[212:215], v[172:175]\n"
      "ds_write_b128 v126, v[188:191] offset:20480\n"
      "v_mfma_f32_16x16x32_bf16 v[188:191], a[0:3], v[164:167], 0\n"
      "global_load_lds_dwordx4 v119, s[56:57]\n"
      "v_mfma_f32_16x16x32_bf16 v[188:191], a[36:39], v[168:171], v[188:191]\n"
      "v_mul_f32 v196, v161, v196\n"
      "v_mul_f32 v198, v161, v198\n"
      "v_mfma_f32_16x16x32_bf16 v[192:195], a[4:7], v[164:167], 0\n"
      "s_waitcnt lgkmcnt(1)\n"
      "v_mul_f32 v202, v160, v202\n"
      "v_mul_f32 v197, v161, v197\n"
      "v_mfma_f32_16x16x32_bf16 v[192:195], a[40:43], v[168:171], v[192:195]\n"
      "v_cvt_pk_bf16_f32 v196, v196, v197\n"
      "v_mul_f32 v199, v161, v199\n"
      "v_mfma_f32_16x16x32_bf16 v[136:139], a[8:11], v[164:167], 0\n"
      "v_cvt_pk_bf16_f32 v197, v198, v199\n"
      "v_mfma_f32_16x16x32_bf16 v[136:139], a[44:47], v[168:171], v[136:139]\n"
      "ds_write2st64_b64 v127, v[184:185], v[196:197] offset0:36 offset1:40\n"
      "v_mfma_f32_16x16x32_bf16 v[140:143], a[12:15], v[164:167], 0\n"
      "v_mul_f32 v203, v160, v203\n"
      "v_mul_f32_e64 v201, v160, v201\n"
      "v_mfma_f32_16x16x32_bf16 v[140:143], a[48:51], v[168:171], v[140:143]\n"
      "s_add_u32 m0, s79, 0\n"
      "v_cvt_pk_bf16_f32 v200, v200, v201\n"
      "v_cvt_pk_bf16_f32 v201, v202, v203\n"
      "v_mfma_f32_16x16x32_bf16 v[184:187], a[16:19], v[164:167], 0\n"
      "global_load_lds_dword v122, s[58:59]\n"
      "v_mfma_f32_16x16x32_bf16 v[184:187], a[52:55], v[168:171], v[184:187]\n"
      "v_mul_f32 v205, v160, v205\n"
      "v_mul_f32 v204, v160, v204\n"
      "v_mfma_f32_16x16x32_bf16 v[196:199], a[20:23], v[164:167], 0\n"
      "ds_read_b128 v[228:231], v114 offset:32768\n"
      "v_cvt_pk_bf16_f32 v202, v204, v205\n"
      "v_mfma_f32_16x16x32_bf16 v[196:199], a[56:59], v[168:171], v[196:199]\n"
      "ds_read_b128 v[212:215], v115 offset:32768\n"
      "v_mul_f32 v206, v160, v206\n"
      "v_mfma_f32_16x16x32_bf16 v[216:219], v[164:167], a[24:27], 0\n"
      "v_mul_f32 v207, v160, v207\n"
      "v_mul_f32 v208, v160, v208\n"
      "v_mfma_f32_16x16x32_bf16 v[216:219], v[168:171], a[60:63], v[216:219]\n"
      "v_cvt_pk_bf16_f32 v203, v206, v207\n"
      "v_mul_f32 v209, v160, v209\n"
      "v_mfma_f32_16x16x32_bf16 v[204:207], v[164:167], a[28:31], 0\n"
      "v_mul_f32 v210, v160, v210\n"
      "v_mul_f32 v211, v160, v211\n"
      "v_mfma_f32_16x16x32_bf16 v[204:207], v[168:171], a[64:67], v[204:207]\n"
      "ds_write_b128 v126, v[200:203] offset:22528\n"
      "v_mfma_f32_16x16x32_bf16 v[200:203], v[164:167], a[32:35], 0\n"
      "v_cvt_pk_bf16_f32 v208, v208, v209\n"
      "v_cvt_pk_bf16_f32 v209, v210, v211\n"
      "v_mfma_f32_16x16x32_bf16 v[200:203], v[168:171], a[68:71], v[200:203]\n"
      "ds_write_b64 v127, v[208:209] offset:22528\n"
      "v_mfma_f32_16x16x32_bf16 v[192:195], a[76:79], v[176:179], v[192:195]\n"
      "v_fma_f32 v208, v144, v144, s38\n"
      "v_mfma_f32_16x16x32_bf16 v[192:195], a[112:115], v[180:183], v[192:195]\n"
      "v_fma_f32 v209, v156, v156, s38\n"
      "v_fmac_f32 v208, v145, v145\n"
      "v_mfma_f32_16x16x32_bf16 v[188:191], a[72:75], v[176:179], v[188:191]\n"
      "v_fmac_f32 v208, v146, v146\n"
      "v_fmac_f32 v209, v157, v157\n"
      "v_mfma_f32_16x16x32_bf16 v[188:191], a[108:111], v[180:183], v[188:191]\n"
      "v_fmac_f32 v209, v158, v158\n"
      "v_fmac_f32 v208, v147, v147\n"
      "v_mfma_f32_16x16x32_bf16 v[136:139], a[80:83], v[176:179], v[136:139]\n"
      "v_fmac_f32 v208, v148, v148\n"
      "v_fmac_f32_e64 v209, v159, v159\n"
      "v_mfma_f32_16x16x32_bf16 v[136:139], a[116:119], v[180:183], v[136:139]\n"
      "ds_read_b128 v[164:167], v114 offset:22528\n"
      "v_fmac_f32 v209, v248, v248\n"
      "v_mfma_f32_16x16x32_bf16 v[140:143], a[84:87], v[176:179], v[140:143]\n"
      "ds_read_b128 v[160:163], v115 offset:22528\n"
      "v_fmac_f32 v209, v249, v249\n"
      "v_mfma_f32_16x16x32_bf16 v[140:143], a[120:123], v[180:183], v[140:143]\n"
      "v_fmac_f32 v208, v149, v149\n"
      "v_fmac_f32 v208, v150, v150\n"
      "v_mfma_f32_16x16x32_bf16 v[184:187], a[88:91], v[176:179], v[184:187]\n"
      "s_waitcnt lgkmcnt(2)\n"
      "v_fmac_f32 v208, v151, v151\n"
      "v_fmac_f32 v209, v250, v250\n"
      "v_mfma_f32_16x16x32_bf16 v[184:187], a[124:127], v[180:183], v[184:187]\n"
      "v_fmac_f32 v208, v152, v152\n"
      "v_fmac_f32 v209, v251, v251\n"
      "v_mfma_f32_16x16x32_bf16 v[196:199], a[92:95], v[176:179], v[196:199]\n"
      "v_fmac_f32 v209, v220, v220\n"
      "v_fmac_f32 v209, v221, v221\n"
      "v_mfma_f32_16x16x32_bf16 v[196:199], a[128:131], v[180:183], v[196:199]\n"
      "v_fmac_f32 v208, v153, v153\n"
      "v_fmac_f32 v208, v154, v154\n"
      "v_mfma_f32_16x16x32_bf16 v[216:219], v[176:179], a[96:99], v[216:219]\n"
      "v_fmac_f32 v209, v222, v222\n"
      "v_cvt_pk_f16_f32 v156, v156, v157\n"
      "v_mfma_f32_16x16x32_bf16 v[216:219], v[180:183], a[132:135], v[216:219]\n"
      "ds_read_b128 v[168:171], v119\n"
      "v_fmac_f32_e64 v209, v223, v223\n"
      "v_mfma_f32_16x16x32_bf16 v[204:207], v[176:179], a[100:103], v[204:207]\n"
      "v_cvt_pk_f16_f32 v157, v158, v159\n"
      "v_fmac_f32 v208, v155, v155\n"
      "v_mfma_f32_16x16x32_bf16 v[204:207], v[180:183], a[136:139], v[204:207]\n"
      "s_nop 0\n"
      "v_permlane16_swap_b32_e64 v208, v209\n"
      "v_mfma_f32_16x16x32_bf16 v[200:203], v[176:179], a[104:107], v[200:203]\n"
      "v_add_f32_e64 v208, v208, v209\n"
      "v_mov_b32_e64 v209, v208\n"
      "v_mfma_f32_16x16x32_bf16 v[200:203], v[180:183], a[140:143], v[200:203]\n"
      "s_nop 0\n"
      "v_permlane32_swap_b32 v208, v209\n"
      "v_mfma_f32_16x16x32_bf16 v[192:195], a[148:151], v[228:231], v[192:195]\n"
      "v_cvt_pk_f16_f32 v34, v220, v221\n"
      "v_add_f32 v208, v208, v209\n"
      "v_mfma_f32_16x16x32_bf16 v[192:195], a[184:187], v[212:215], v[192:195]\n"
      "s_waitcnt lgkmcnt(1)\n"
      "v_cvt_pk_f16_f32 v35, v222, v223\n"
      "v_cvt_pk_f16_f32 v158, v248, v249\n"
      "v_mfma_f32_16x16x32_bf16 v[188:191], a[144:147], v[228:231], v[188:191]\n"
      "v_rsq_f32_e64 v208, v208\n"
      "v_mfma_f32_16x16x32_bf16 v[188:191], a[180:183], v[212:215], v[188:191]\n"
      "ds_read_b128 v[176:179], v114 offset:28672\n"
      "v_mov_b32_e64 v209, v208\n"
      "v_mfma_f32_16x16x32_bf16 v[136:139], a[152:155], v[228:231], v[136:139]\n"
      "v_cmp_nge_f32 vcc, 0x43800000, v209\n"
      "v_cvt_pk_f16_f32 v144, v144, v145\n"
      "v_mfma_f32_16x16x32_bf16 v[136:139], a[188:191], v[212:215], v[136:139]\n"
      "ds_read_b128 v[180:183], v115 offset:28672\n"
      "v_cvt_pk_f16_f32 v145, v146, v147\n"
      "v_mfma_f32_16x16x32_bf16 v[140:143], a[156:159], v[228:231], v[140:143]\n"
      "s_mov_b64 s[34:35], vcc\n"
      "v_cvt_pk_f16_f32 v146, v148, v149\n"
      "v_cmp_nle_f32 vcc, 0x3a000000, v209\n"
      "v_mfma_f32_16x16x32_bf16 v[140:143], a[192:195], v[212:215], v[140:143]\n"
      "s_or_b64 s[34:35], s[34:35], vcc\n"
      "v_cvt_pk_f16_f32 v147, v150, v151\n"
      "v_cvt_pk_f16_f32 v159, v250, v251\n"
      "v_mfma_f32_16x16x32_bf16 v[196:199], a[164:167], v[228:231], v[196:199]\n"
      "s_sub_u32 s28, s27, 3\n"
      "v_cvt_pk_bf16_f32 v232, v232, v233\n"
      "v_cvt_pk_f16_f32 v32, v152, v153\n"
      "v_mfma_f32_16x16x32_bf16 v[196:199], a[200:203], v[212:215], v[196:199]\n"
      "s_cselect_b64 s[54:55], s[12:13], s[22:23]\n"
      "v_permlane16_swap_b32_e64 v208, v209\n"
      "v_mfma_f32_16x16x32_bf16 v[184:187], a[160:163], v[228:231], v[184:187]\n"
      "s_max_i32 m0, s28, 0\n"
      "v_mul_f32 v208, s36, v208\n"
      "v_cvt_pk_f16_f32 v208, v208, v209\n"
      "v_mfma_f32_16x16x32_bf16 v[184:187], a[196:199], v[212:215], v[184:187]\n"
      "s_movrels_b32 s9, s80\n"
      "v_pk_mul_f16 v156, v156, v208 op_sel:[0,1] op_sel_hi:[1,1]\n"
      "v_cvt_pk_f16_f32 v33, v154, v155\n"
      "v_pk_mul_f16 v157, v157, v208 op_sel:[0,1] op_sel_hi:[1,1]\n"
      "v_mfma_f32_16x16x32_bf16 v[216:219], v[228:231], a[168:171], v[216:219]\n"
      "ds_read_b128 v[148:151], v119 offset:6144\n"
      "v_pk_mul_f16 v159, v159, v208 op_sel:[0,1] op_sel_hi:[1,1]\n"
      "v_mfma_f32_16x16x32_bf16 v[216:219], v[212:215], a[204:207], v[216:219]\n"
      "s_lshr_b32 s8, s9, 8\n"
      "v_pk_mul_f16 v35, v35, v208 op_sel:[0,1] op_sel_hi:[1,1]\n"
      "v_pk_mul_f16 v158, v158, v208 op_sel:[0,1] op_sel_hi:[1,1]\n"
      "v_mfma_f32_16x16x32_bf16 v[204:207], v[228:231], a[172:175], v[204:207]\n"
      "s_and_b32 s9, s9, 0xff\n"
      "v_pk_mul_f16 v144, v144, v208 op_sel_hi:[1,0]\n"
      "v_pk_mul_f16 v34, v34, v208 op_sel:[0,1] op_sel_hi:[1,1]\n"
      "v_mfma_f32_16x16x32_bf16 v[204:207], v[212:215], a[208:211], v[204:207]\n"
      "s_mul_i32 s28, s8, 1373568\n"
      "v_pk_mul_f16 v145, v145, v208 op_sel_hi:[1,0]\n"
      "v_pk_mul_f16 v146, v146, v208 op_sel_hi:[1,0]\n"
      "v_mfma_f32_16x16x32_bf16 v[200:203], v[228:231], a[176:179], v[200:203]\n"
      "s_mul_i32 s29, s9, 2688\n"
      "v_pk_mul_f16 v147, v147, v208 op_sel_hi:[1,0]\n"
      "v_pk_mul_f16 v32, v32, v208 op_sel_hi:[1,0]\n"
      "v_mfma_f32_16x16x32_bf16 v[200:203], v[212:215], a[212:215], v[200:203]\n"
      "s_add_u32 s28, s28, s29\n"
      "v_cvt_pk_bf16_f32 v233, v234, v235\n"
      "v_pk_mul_f16 v33, v33, v208 op_sel_hi:[1,0]\n"
      "v_mfma_f32_16x16x32_bf16 v[208:211], a[0:3], v[164:167], 0\n"
      "s_cmp_eq_u32 s8, 72\n"
      "v_cvt_pk_bf16_f32 v234, v216, v217\n"
      "v_cvt_pk_bf16_f32 v235, v218, v219\n"
      "v_mfma_f32_16x16x32_bf16 v[208:211], a[36:39], v[160:163], v[208:211]\n"
      "s_waitcnt lgkmcnt(1)\n"
      "v_cvt_pk_bf16_f32 v224, v224, v225\n"
      "v_cvt_pk_bf16_f32 v225, v226, v227\n"
      "v_mfma_f32_16x16x32_bf16 v[152:155], a[4:7], v[164:167], 0\n"
      "s_cselect_b32 s8, 0, 100270464\n"
      "v_cvt_pk_bf16_f32 v226, v204, v205\n"
      "v_cvt_pk_bf16_f32 v227, v206, v207\n"
      "v_mfma_f32_16x16x32_bf16 v[152:155], a[40:43], v[160:163], v[152:155]\n"
      "s_cmp_eq_u32 s9, 72\n"
      "v_cvt_pk_bf16_f32 v172, v172, v173\n"
      "v_cvt_pk_bf16_f32 v173, v174, v175\n"
      "v_mfma_f32_16x16x32_bf16 v[204:207], a[8:11], v[164:167], 0\n"
      "s_cselect_b32 s9, 0, 196224\n"
      "v_cvt_pk_bf16_f32 v175, v202, v203\n"
      "v_cvt_pk_bf16_f32 v174, v200, v201\n"
      "v_mfma_f32_16x16x32_bf16 v[204:207], a[44:47], v[160:163], v[204:207]\n"
      "s_add_u32 s10, s8, s9\n"
      "v_fma_f32 v203, v188, v188, s38\n"
      "v_fma_f32 v202, v140, v140, s38\n"
      "v_mfma_f32_16x16x32_bf16 v[216:219], a[12:15], v[164:167], 0\n"
      "s_add_u32 s28, s28, s10\n"
      "v_fmac_f32 v203, v189, v189\n"
      "v_fmac_f32 v202, v141, v141\n"
      "v_mfma_f32_16x16x32_bf16 v[216:219], a[48:51], v[160:163], v[216:219]\n"
      "s_add_u32 s54, s54, s28\n"
      "v_fmac_f32 v202, v142, v142\n"
      "v_fmac_f32 v203, v190, v190\n"
      "v_mfma_f32_16x16x32_bf16 v[212:215], a[16:19], v[164:167], 0\n"
      "s_addc_u32 s55, s55, 0\n"
      "v_fmac_f32 v202, v143, v143\n"
      "v_fmac_f32 v203, v191, v191\n"
      "v_mfma_f32_16x16x32_bf16 v[212:215], a[52:55], v[160:163], v[212:215]\n"
      "s_cmp_lg_u32 s10, s11\n"
      "v_fmac_f32 v202, v184, v184\n"
      "v_fmac_f32 v203, v192, v192\n"
      "v_mfma_f32_16x16x32_bf16 v[228:231], a[20:23], v[164:167], 0\n"
      "s_cbranch_scc1 L_edge_17_%=\n"
      "L_eb_18_%=:\n"
      "v_mfma_f32_16x16x32_bf16 v[228:231], a[56:59], v[160:163], v[228:231]\n"
      "s_mov_b32 s5, s4\n"
      "v_fmac_f32 v202, v185, v185\n"
      "v_fmac_f32 v203, v193, v193\n"
      "v_mfma_f32_16x16x32_bf16 v[220:223], v[164:167], a[24:27], 0\n"
      "s_add_u32 s4, s27, 1\n"
      "v_fmac_f32 v203, v194, v194\n"
      "v_fmac_f32 v202, v186, v186\n"
      "v_mfma_f32_16x16x32_bf16 v[220:223], v[160:163], a[60:63], v[220:223]\n"
      "s_lshl_b32 s4, s4, 30\n"
      "v_fmac_f32 v203, v195, v195\n"
      "v_fmac_f32_e64 v203, v136, v136\n"
      "v_mfma_f32_16x16x32_bf16 v[248:251], v[164:167], a[28:31], 0\n"
      "s_waitcnt lgkmcnt(0)\n"
      "v_fmac_f32 v202, v187, v187\n"
      "v_fmac_f32 v202, v196, v196\n"
      "v_mfma_f32_16x16x32_bf16 v[248:251], v[160:163], a[64:67], v[248:251]\n"
      "v_fmac_f32 v203, v137, v137\n"
      "v_fmac_f32_e64 v203, v138, v138\n"
      "v_mfma_f32_16x16x32_bf16 v[208:211], a[72:75], v[176:179], v[208:211]\n"
      "global_store_dwordx4 v116, v[168:171], s[54:55] sc1\n"
      "v_cvt_pk_f16_f32 v140, v140, v141\n"
      "v_mfma_f32_16x16x32_bf16 v[208:211], a[108:111], v[180:183], v[208:211]\n"
      "v_fmac_f32 v202, v197, v197\n"
      "v_fmac_f32 v202, v198, v198\n"
      "v_mfma_f32_16x16x32_bf16 v[168:171], v[164:167], a[32:35], 0\n"
      "ds_read_b128 v[164:167], v114 offset:34816\n"
      "v_fmac_f32 v202, v199, v199\n"
      "v_mfma_f32_16x16x32_bf16 v[168:171], v[160:163], a[68:71], v[168:171]\n"
      "ds_read_b128 v[160:163], v115 offset:34816\n"
      "v_fmac_f32 v203, v139, v139\n"
      "v_mfma_f32_16x16x32_bf16 v[152:155], a[76:79], v[176:179], v[152:155]\n"
      "s_nop 0\n"
      "v_permlane16_swap_b32 v203, v202\n"
      "v_mfma_f32_16x16x32_bf16 v[152:155], a[112:115], v[180:183], v[152:155]\n"
      "s_and_b32 s4, s4, 0x80000000\n"
      "v_cvt_pk_f16_f32 v141, v142, v143\n"
      "v_cvt_pk_f16_f32 v142, v184, v185\n"
      "v_mfma_f32_16x16x32_bf16 v[204:207], a[80:83], v[176:179], v[204:207]\n"
      "v_add_f32 v203, v203, v202\n"
      "v_mov_b32 v202, v203\n"
      "v_mfma_f32_16x16x32_bf16 v[204:207], a[116:119], v[180:183], v[204:207]\n"
      "s_nop 0\n"
      "v_permlane32_swap_b32 v203, v202\n"
      "v_mfma_f32_16x16x32_bf16 v[216:219], a[84:87], v[176:179], v[216:219]\n"
      "global_store_dwordx4 v116, v[148:151], s[54:55] offset:128 sc1\n"
      "v_mfma_f32_16x16x32_bf16 v[216:219], a[120:123], v[180:183], v[216:219]\n"
      "ds_read_b128 v[236:239], v119 offset:12288\n"
      "v_add_f32_e64 v203, v203, v202\n"
      "v_mfma_f32_16x16x32_bf16 v[212:215], a[88:91], v[176:179], v[212:215]\n"
      "ds_read_u16 v52, v134 offset:1168\n"
      "v_cvt_pk_f16_f32 v40, v196, v197\n"
      "v_mfma_f32_16x16x32_bf16 v[212:215], a[124:127], v[180:183], v[212:215]\n"
      "s_waitcnt lgkmcnt(2)\n"
      "v_rsq_f32 v203, v203\n"
      "v_mfma_f32_16x16x32_bf16 v[228:231], a[92:95], v[176:179], v[228:231]\n"
      "ds_read_b128 v[148:151], v132 offset:1200\n"
      "v_cvt_pk_f16_f32 v143, v186, v187\n"
      "v_mfma_f32_16x16x32_bf16 v[228:231], a[128:131], v[180:183], v[228:231]\n"
      "ds_read_b64 v[240:241], v132 offset:1192\n"
      "v_cvt_pk_f16_f32 v41, v198, v199\n"
      "v_mfma_f32_16x16x32_bf16 v[220:223], v[176:179], a[96:99], v[220:223]\n"
      "ds_read_b64 v[242:243], v133 offset:1184\n"
      "v_mov_b32 v202, v203\n"
      "v_mfma_f32_16x16x32_bf16 v[220:223], v[180:183], a[132:135], v[220:223]\n"
      "ds_read_u16 v56, v134 offset:1200\n"
      "v_cvt_pk_f16_f32 v188, v188, v189\n"
      "v_mfma_f32_16x16x32_bf16 v[248:251], v[176:179], a[100:103], v[248:251]\n"
      "ds_read_u16 v60, v134 offset:1232\n"
      "v_cmp_nge_f32 vcc, 0x43800000, v202\n"
      "v_mfma_f32_16x16x32_bf16 v[248:251], v[180:183], a[136:139], v[248:251]\n"
      "s_or_b64 s[34:35], s[34:35], vcc\n"
      "v_cmp_nle_f32 vcc, 0x3a000000, v202\n"
      "v_cvt_pk_f16_f32 v189, v190, v191\n"
      "v_mfma_f32_16x16x32_bf16 v[168:171], v[176:179], a[104:107], v[168:171]\n"
      "ds_read_b128 v[244:247], v133 offset:1168\n"
      "v_cvt_pk_f16_f32 v190, v192, v193\n"
      "v_mfma_f32_16x16x32_bf16 v[168:171], v[180:183], a[140:143], v[168:171]\n"
      "ds_read_b128 v[196:199], v123 offset:3072\n"
      "v_cvt_pk_f16_f32 v191, v194, v195\n"
      "v_mfma_f32_16x16x32_bf16 v[208:211], a[144:147], v[164:167], v[208:211]\n"
      "ds_read_b128 v[192:195], v123 offset:6144\n"
      "v_cvt_pk_f16_f32 v38, v136, v137\n"
      "v_mfma_f32_16x16x32_bf16 v[208:211], a[180:183], v[160:163], v[208:211]\n"
      "s_waitcnt lgkmcnt(7)\n"
      "v_permlane16_swap_b32 v203, v202\n"
      "v_mfma_f32_16x16x32_bf16 v[152:155], a[148:151], v[164:167], v[152:155]\n"
      "v_mul_f32_e64 v203, s36, v203\n"
      "v_cvt_pk_f16_f32 v39, v138, v139\n"
      "v_mfma_f32_16x16x32_bf16 v[152:155], a[184:187], v[160:163], v[152:155]\n"
      "global_store_dwordx4 v116, v[236:239], s[54:55] offset:256 sc1\n"
      "v_mfma_f32_16x16x32_bf16 v[204:207], a[152:155], v[164:167], v[204:207]\n"
      "ds_read_b128 v[136:139], v123\n"
      "v_cvt_pk_f16_f32 v203, v203, v202\n"
      "v_mfma_f32_16x16x32_bf16 v[204:207], a[188:191], v[160:163], v[204:207]\n"
      "ds_read_b128 v[236:239], v123 offset:9216\n"
      "v_pk_mul_f16 v140, v140, v203 op_sel:[0,1] op_sel_hi:[1,1]\n"
      "v_mfma_f32_16x16x32_bf16 v[216:219], a[156:159], v[164:167], v[216:219]\n"
      "s_or_b64 s[34:35], s[34:35], vcc\n"
      "v_pk_mul_f16 v141, v141, v203 op_sel:[0,1] op_sel_hi:[1,1]\n"
      "v_pk_mul_f16 v142, v142, v203 op_sel:[0,1] op_sel_hi:[1,1]\n"
      "v_mfma_f32_16x16x32_bf16 v[216:219], a[192:195], v[160:163], v[216:219]\n"
      "s_waitcnt lgkmcnt(4)\n"
      "v_pk_mul_f16 v40, v40, v203 op_sel:[0,1] op_sel_hi:[1,1]\n"
      "v_pk_mul_f16 v143, v143, v203 op_sel:[0,1] op_sel_hi:[1,1]\n"
      "v_mfma_f32_16x16x32_bf16 v[212:215], a[160:163], v[164:167], v[212:215]\n"
      "v_pk_mul_f16 v188, v188, v203 op_sel_hi:[1,0]\n"
      "v_pk_mul_f16 v41, v41, v203 op_sel:[0,1] op_sel_hi:[1,1]\n"
      "v_mfma_f32_16x16x32_bf16 v[212:215], a[196:199], v[160:163], v[212:215]\n"
      "v_pk_mul_f16 v39, v39, v203 op_sel_hi:[1,0]\n"
      "v_pk_mul_f16 v189, v189, v203 op_sel_hi:[1,0]\n"
      "v_mfma_f32_16x16x32_bf16 v[220:223], v[164:167], a[168:171], v[220:223]\n"
      "v_pk_mul_f16 v190, v190, v203 op_sel_hi:[1,0]\n"
      "v_pk_mul_f16 v191, v191, v203 op_sel_hi:[1,0]\n"
      "v_mfma_f32_16x16x32_bf16 v[220:223], v[160:163], a[204:207], v[220:223]\n"
      "v_pk_mul_f16 v38, v38, v203 op_sel_hi:[1,0]\n"
      "v_fma_f32 v203, v216, v216, s38\n"
      "v_mfma_f32_16x16x32_bf16 v[228:231], a[164:167], v[164:167], v[228:231]\n"
      "v_fma_f32 v202, v208, v208, s38\n"
      "v_fmac_f32 v202, v209, v209\n"
      "v_mfma_f32_16x16x32_bf16 v[228:231], a[200:203], v[160:163], v[228:231]\n"
      "v_fmac_f32 v203, v217, v217\n"
      "v_mfma_f32_16x16x32_bf16 v[248:251], v[164:167], a[172:175], v[248:251]\n"
      "v_fmac_f32 v203, v218, v218\n"
      "v_fmac_f32 v203, v219, v219\n"
      "v_mfma_f32_16x16x32_bf16 v[248:251], v[160:163], a[208:211], v[248:251]\n"
      "v_fmac_f32 v202, v210, v210\n"
      "v_fmac_f32 v202, v211, v211\n"
      "v_mfma_f32_16x16x32_bf16 v[168:171], v[164:167], a[176:179], v[168:171]\n"
      "s_waitcnt lgkmcnt(0)\n"
      "v_fmac_f32 v202, v152, v152\n"
      "v_fmac_f32 v202, v153, v153\n"
      "v_mfma_f32_16x16x32_bf16 v[168:171], v[160:163], a[212:215], v[168:171]\n"
      "v_fmac_f32 v203, v212, v212\n"
      "v_fmac_f32 v203, v213, v213\n"
      "v_mfma_f32_16x16x32_bf16 v[176:179], a[216:219], v[136:139], 0\n"
      "v_fmac_f32 v202, v154, v154\n"
      "v_fmac_f32 v202, v155, v155\n"
      "v_mfma_f32_16x16x32_bf16 v[176:179], a[228:231], v[196:199], v[176:179]\n"
      "v_fmac_f32 v203, v214, v214\n"
      "v_fmac_f32 v203, v215, v215\n"
      "v_mfma_f32_16x16x32_bf16 v[180:183], a[220:223], v[136:139], 0\n"
      "v_fmac_f32 v202, v204, v204\n"
      "v_fmac_f32 v203, v228, v228\n"
      "v_mfma_f32_16x16x32_bf16 v[180:183], a[232:235], v[196:199], v[180:183]\n"
      "v_fmac_f32 v203, v229, v229\n"
      "v_fmac_f32 v203, v230, v230\n"
      "v_mfma_f32_16x16x32_bf16 v[184:187], a[224:227], v[136:139], 0\n"
      "v_fmac_f32 v202, v205, v205\n"
      "v_fmac_f32 v202, v206, v206\n"
      "v_mfma_f32_16x16x32_bf16 v[184:187], a[236:239], v[196:199], v[184:187]\n"
      "ds_read_b128 v[196:199], v123 offset:12288\n"
      "v_fmac_f32 v202, v207, v207\n"
      "v_mfma_f32_16x16x32_bf16 v[176:179], a[240:243], v[192:195], v[176:179]\n"
      "v_fmac_f32 v203, v231, v231\n"
      "v_cvt_pk_f16_f32 v216, v216, v217\n"
      "v_mfma_f32_16x16x32_bf16 v[176:179], a[252:255], v[236:239], v[176:179]\n"
      "ds_read_b128 v[136:139], v123 offset:15360\n"
      "v_permlane16_swap_b32 v202, v203\n"
      "v_mfma_f32_16x16x32_bf16 v[180:183], a[244:247], v[192:195], v[180:183]\n"
      "v_add_f32_e64 v202, v202, v203\n"
      "v_cvt_pk_f16_f32 v217, v218, v219\n"
      "v_mfma_f32_16x16x32_bf16 v[180:183], v[0:3], v[236:239], v[180:183]\n"
      "v_cvt_pk_f16_f32 v44, v204, v205\n"
      "v_cvt_pk_f16_f32 v208, v208, v209\n"
      "v_mov_b32_e64 v203, v202\n"
      "v_cvt_pk_f16_f32 v218, v212, v213\n"
      "v_mfma_f32_16x16x32_bf16 v[184:187], a[248:251], v[192:195], v[184:187]\n"
      "v_permlane32_swap_b32 v202, v203\n"
      "v_mfma_f32_16x16x32_bf16 v[184:187], v[4:7], v[236:239], v[184:187]\n"
      "v_add_f32 v202, v202, v203\n"
      "v_cvt_pk_f16_f32 v219, v214, v215\n"
      "v_mfma_f32_16x16x32_f16 v[160:163], v[156:159], v[144:147], v[78:81]\n"
      "v_cvt_pk_f16_f32 v47, v230, v231\n"
      "v_cvt_pk_f16_f32 v46, v228, v229\n"
      "v_mfma_f32_16x16x32_f16 v[160:163], v[34:37], v[32:35], v[160:163]\n"
      "v_rsq_f32 v202, v202\n"
      "v_mfma_f32_16x16x32_f16 v[192:195], v[140:143], v[144:147], v[82:85]\n"
      "v_mov_b32 v203, v202\n"
      "v_cmp_nge_f32 vcc, 0x43800000, v203\n"
      "v_mfma_f32_16x16x32_f16 v[192:195], v[40:43], v[32:35], v[192:195]\n"
      "s_waitcnt lgkmcnt(0)\n"
      "v_cvt_pk_f16_f32 v209, v210, v211\n"
      "v_cvt_pk_f16_f32 v210, v152, v153\n"
      "v_mfma_f32_16x16x32_bf16 v[176:179], v[8:11], v[196:199], v[176:179]\n"
      "s_or_b64 s[34:35], s[34:35], vcc\n"
      "v_cvt_pk_f16_f32 v211, v154, v155\n"
      "v_cmp_nle_f32 vcc, 0x3a000000, v203\n"
      "v_mfma_f32_16x16x32_bf16 v[176:179], v[20:23], v[136:139], v[176:179]\n"
      "s_or_b64 s[34:35], s[34:35], vcc\n"
      "v_permlane16_swap_b32 v202, v203\n"
      "v_mfma_f32_16x16x32_bf16 v[180:183], v[12:15], v[196:199], v[180:183]\n"
      "s_or_b64 vcc, s[48:49], s[50:51]\n"
      "v_mul_f32 v202, s36, v202\n"
      "v_cvt_pk_f16_f32 v45, v206, v207\n"
      "v_mfma_f32_16x16x32_bf16 v[180:183], v[24:27], v[136:139], v[180:183]\n"
      "v_cvt_pk_f16_f32 v202, v202, v203\n"
      "v_pk_mul_f16 v217, v217, v202 op_sel:[0,1] op_sel_hi:[1,1]\n"
      "v_mfma_f32_16x16x32_bf16 v[184:187], v[16:19], v[196:199], v[184:187]\n"
      "v_pk_mul_f16 v218, v218, v202 op_sel:[0,1] op_sel_hi:[1,1]\n"
      "v_pk_mul_f16 v216, v216, v202 op_sel:[0,1] op_sel_hi:[1,1]\n"
      "v_mfma_f32_16x16x32_bf16 v[184:187], v[28:31], v[136:139], v[184:187]\n"
      "v_pk_mul_f16 v46, v46, v202 op_sel:[0,1] op_sel_hi:[1,1]\n"
      "v_pk_mul_f16 v219, v219, v202 op_sel:[0,1] op_sel_hi:[1,1]\n"
      "v_mfma_f32_16x16x32_f16 v[204:207], v[148:151], v[144:147], v[70:73]\n"
      "v_pk_mul_f16 v208, v208, v202 op_sel_hi:[1,0]\n"
      "v_cvt_pk_bf16_f32 v59, v170, v171\n"
      "v_mfma_f32_16x16x32_f16 v[204:207], v[244:247], v[156:159], v[204:207]\n"
      "s_waitcnt vmcnt(3)\n"
      "ds_read_b32 v203, v122\n"
      "v_mfma_f32_16x16x32_f16 v[204:207], v[240:243], v[32:35], v[204:207]\n"
      "ds_read_b128 v[196:199], v119 offset:36864\n"
      "v_pk_mul_f16 v211, v211, v202 op_sel_hi:[1,0]\n"
      "v_cvt_pk_bf16_f32 v51, v222, v223\n"
      "v_pk_mul_f16 v47, v47, v202 op_sel:[0,1] op_sel_hi:[1,1]\n"
      "v_mfma_f32_16x16x32_f16 v[136:139], v[216:219], v[144:147], v[86:89]\n"
      "s_or_b64 vcc, vcc, s[34:35]\n"
      "v_cvt_pk_bf16_f32 v58, v168, v169\n"
      "v_cvt_pk_bf16_f32 v55, v250, v251\n"
      "v_mfma_f32_16x16x32_f16 v[136:139], v[46:49], v[32:35], v[136:139]\n"
      "v_fma_f32 v168, v176, v176, s39\n"
      "v_fmac_f32 v168, v177, v177\n"
      "v_fmac_f32 v168, v178, v178\n"
      "v_add_u32 v134, 2336, v134\n"
      "v_cvt_pk_bf16_f32 v54, v248, v249\n"
      "v_fmac_f32_e64 v168, v179, v179\n"
      "v_pk_mul_f16 v44, v44, v202 op_sel_hi:[1,0]\n"
      "v_pk_mul_f16 v45, v45, v202 op_sel_hi:[1,0]\n"
      "v_fmac_f32_e64 v168, v180, v180\n"
      "v_pk_mul_f16 v209, v209, v202 op_sel_hi:[1,0]\n"
      "v_pk_mul_f16 v210, v210, v202 op_sel_hi:[1,0]\n"
      "v_fmac_f32 v168, v181, v181\n"
      "v_fmac_f32 v168, v182, v182\n"
      "v_fmac_f32 v168, v183, v183\n"
      "v_cvt_pk_bf16_f32 v50, v220, v221\n"
      "v_fmac_f32 v168, v184, v184\n"
      "v_fmac_f32 v168, v185, v185\n"
      "v_add_u32 v133, 2336, v133\n"
      "v_fmac_f32 v168, v186, v186\n"
      "v_add_u32 v132, 2336, v132\n"
      "v_fmac_f32 v168, v187, v187\n"
      "s_waitcnt lgkmcnt(0)\n"
      "v_or3_b32 v196, v196, v197, v198\n"
      "v_bitop3_b32 v203, v203, v199, s40 bitop3:0xa8\n"
      "v_and_b32_e64 v196, s40, v196\n"
      "v_or3_b32 v196, v196, v203, s4\n"
      "ds_write_b32 v129, v196 offset:2048\n"
      "s_cbranch_scc1 L_gen_19_%=\n"
      "L_fast_22_%=:\n"
      "v_exp_f32 v160, v160\n"
      "v_mfma_f32_16x16x32_f16 v[200:203], v[156:159], v[188:191], v[90:93]\n"
      "ds_read_b128 v[164:167], v123 offset:4096\n"
      "v_exp_f32 v193, v193\n"
      "v_exp_f32 v162, v162\n"
      "v_mfma_f32_16x16x32_f16 v[144:147], v[148:151], v[188:191], v[72:75]\n"
      "ds_read_b128 v[152:155], v123 offset:1024\n"
      "v_exp_f32_e64 v192, v192\n"
      "v_mfma_f32_16x16x32_f16 v[144:147], v[244:247], v[140:143], v[144:147]\n"
      "ds_read_b128 v[196:199], v120\n"
      "v_exp_f32_e64 v161, v161\n"
      "v_mfma_f32_16x16x32_f16 v[220:223], v[140:143], v[188:191], v[94:97]\n"
      "ds_read_b128 v[248:251], v128 offset:2048\n"
      "v_cvt_pk_bf16_f32 v160, v160, v161\n"
      "v_mfma_f32_16x16x32_f16 v[220:223], v[40:43], v[38:41], v[220:223]\n"
      "ds_read_b128 v[228:231], v123 offset:7168\n"
      "v_exp_f32 v139, v139\n"
      "v_exp_f32 v137, v137\n"
      "v_exp_f32 v163, v163\n"
      "v_exp_f32 v194, v194\n"
      "v_mfma_f32_16x16x32_f16 v[144:147], v[240:243], v[38:41], v[144:147]\n"
      "ds_read_b128 v[212:215], v123 offset:10240\n"
      "v_cvt_pk_bf16_f32 v161, v162, v163\n"
      "v_mfma_f32_16x16x32_f16 v[200:203], v[34:37], v[38:41], v[200:203]\n"
      "v_exp_f32_e64 v138, v138\n"
      "v_cvt_pk_bf16_f32 v162, v192, v193\n"
      "v_mfma_f32_16x16x32_f16 v[236:239], v[216:219], v[188:191], v[98:101]\n"
      "s_waitcnt lgkmcnt(4)\n"
      "v_exp_f32 v136, v136\n"
      "v_mfma_f32_16x16x32_f16 v[236:239], v[46:49], v[38:41], v[236:239]\n"
      "v_cvt_pk_bf16_f32 v62, v136, v137\n"
      "v_exp_f32 v204, v204\n"
      "v_exp_f32_e64 v195, v195\n"
      "v_mfma_f32_16x16x32_bf16 v[188:191], a[216:219], v[152:155], 0\n"
      "s_waitcnt lgkmcnt(2)\n"
      "v_cvt_pk_bf16_f32 v163, v194, v195\n"
      "v_cvt_pk_bf16_f32 v64, v204, 0\n"
      "v_mfma_f32_16x16x32_bf16 v[188:191], a[228:231], v[164:167], v[188:191]\n"
      "global_store_dwordx4 v117, v[196:199], s[54:55] sc1\n"
      "v_mfma_f32_16x16x32_bf16 v[192:195], a[220:223], v[152:155], 0\n"
      "v_bitop3_b32 v250, v250, v251, s4 bitop3:0x7e\n"
      "v_bitop3_b32 v248, v248, v249, s4 bitop3:0x7e\n"
      "v_mfma_f32_16x16x32_bf16 v[192:195], a[232:235], v[164:167], v[192:195]\n"
      "v_or_b32 v248, v248, v250\n"
      "v_cmp_ne_u32 vcc, 0, v248\n"
      "v_mfma_f32_16x16x32_bf16 v[196:199], a[224:227], v[152:155], 0\n"
      "s_mov_b64 s[48:49], vcc\n"
      "v_exp_f32 v200, v200\n"
      "v_mfma_f32_16x16x32_bf16 v[196:199], a[236:239], v[164:167], v[196:199]\n"
      "s_waitcnt lgkmcnt(0)\n"
      "v_exp_f32 v201, v201\n"
      "v_cvt_pk_bf16_f32 v63, v138, v139\n"
      "v_mfma_f32_16x16x32_bf16 v[188:191], a[240:243], v[228:231], v[188:191]\n"
      "ds_read_b128 v[136:139], v123 offset:13312\n"
      "v_cvt_pk_bf16_f32 v200, v200, v201\n"
      "v_mfma_f32_16x16x32_bf16 v[188:191], a[252:255], v[212:215], v[188:191]\n"
      "ds_read_b128 v[164:167], v123 offset:16384\n"
      "v_exp_f32 v202, v202\n"
      "v_mfma_f32_16x16x32_bf16 v[192:195], a[244:247], v[228:231], v[192:195]\n"
      "s_cbranch_vccnz L_flg_23_%=\n"
      "L_fb_24_%=:\n"
      "v_mfma_f32_16x16x32_bf16 v[192:195], v[0:3], v[212:215], v[192:195]\n"
      "ds_read_b128 v[152:155], v121\n"
      "v_exp_f32 v221, v221\n"
      "v_exp_f32 v203, v203\n"
      "v_exp_f32_e64 v236, v236\n"
      "v_mfma_f32_16x16x32_bf16 v[196:199], a[248:251], v[228:231], v[196:199]\n"
      "v_exp_f32 v220, v220\n"
      "v_mfma_f32_16x16x32_bf16 v[196:199], v[4:7], v[212:215], v[196:199]\n"
      "ds_read_b128 v[212:215], v123 offset:2048\n"
      "v_exp_f32 v223, v223\n"
      "v_exp_f32 v237, v237\n"
      "v_exp_f32_e64 v222, v222\n"
      "v_cvt_pk_bf16_f32 v201, v202, v203\n"
      "v_cvt_pk_bf16_f32 v202, v220, v221\n"
      "s_waitcnt lgkmcnt(2)\n"
      "v_cvt_pk_bf16_f32 v203, v222, v223\n"
      "v_mfma_f32_16x16x32_bf16 v[188:191], v[8:11], v[136:139], v[188:191]\n"
      "ds_read_b128 v[220:223], v123 offset:5120\n"
      "v_mfma_f32_16x16x32_bf16 v[188:191], v[20:23], v[164:167], v[188:191]\n"
      "v_exp_f32 v238, v238\n"
      "v_mfma_f32_16x16x32_bf16 v[192:195], v[12:15], v[136:139], v[192:195]\n"
      "s_waitcnt lgkmcnt(2)\n"
      "v_exp_f32 v239, v239\n"
      "v_mfma_f32_16x16x32_bf16 v[192:195], v[24:27], v[164:167], v[192:195]\n"
      "s_mov_b32 exec_hi, 0\n"
      "global_store_dwordx4 v118, v[152:155], s[54:55] sc1\n"
      "s_mov_b32 exec_hi, -1\n"
      "v_mfma_f32_16x16x32_bf16 v[196:199], v[16:19], v[136:139], v[196:199]\n"
      "v_fma_f32 v146, v188, v188, s39\n"
      "v_fmac_f32 v146, v189, v189\n"
      "v_mfma_f32_16x16x32_bf16 v[196:199], v[28:31], v[164:167], v[196:199]\n"
      "v_exp_f32 v144, v144\n"
      "v_mfma_f32_16x16x32_bf16 v[136:139], v[66:69], v[160:163], 0\n"
      "v_fmac_f32 v146, v190, v190\n"
      "v_fmac_f32_e64 v146, v191, v191\n"
      "v_mfma_f32_16x16x32_bf16 v[136:139], v[66:69], v[62:65], v[136:139]\n"
      "v_fmac_f32 v146, v192, v192\n"
      "v_fmac_f32 v146, v193, v193\n"
      "v_mfma_f32_16x16x32_bf16 v[164:167], v[232:235], v[160:163], 0\n"
      "v_fmac_f32 v146, v194, v194\n"
      "v_fmac_f32 v146, v195, v195\n"
      "v_mfma_f32_16x16x32_bf16 v[164:167], v[50:53], v[62:65], v[164:167]\n"
      "v_fmac_f32 v146, v196, v196\n"
      "v_fmac_f32 v146, v197, v197\n"
      "v_mfma_f32_16x16x32_bf16 v[152:155], v[224:227], v[160:163], 0\n"
      "v_fmac_f32 v146, v198, v198\n"
      "v_fmac_f32 v146, v199, v199\n"
      "v_mfma_f32_16x16x32_bf16 v[152:155], v[54:57], v[62:65], v[152:155]\n"
      "v_rcp_f32 v136, v136\n"
      "v_mfma_f32_16x16x32_bf16 v[228:231], v[172:175], v[160:163], 0\n"
      "v_mul_f32 v164, v136, v164\n"
      "v_mul_f32_e64 v165, v136, v165\n"
      "v_mfma_f32_16x16x32_bf16 v[228:231], v[58:61], v[62:65], v[228:231]\n"
      "v_cvt_pk_bf16_f32 v62, v236, v237\n"
      "v_cvt_pk_bf16_f32 v63, v238, v239\n"
      "v_mfma_f32_16x16x32_f16 v[236:239], v[156:159], v[208:211], v[102:105]\n"
      "v_cvt_pk_bf16_f32 v64, v144, 0\n"
      "v_cvt_pk_bf16_f32 v164, v164, v165\n"
      "v_mfma_f32_16x16x32_f16 v[236:239], v[34:37], v[44:47], v[236:239]\n"
      "v_mul_f32 v167, v136, v167\n"
      "v_mul_f32 v166, v136, v166\n"
      "v_mfma_f32_16x16x32_f16 v[160:163], v[140:143], v[208:211], v[106:109]\n"
      "v_mul_f32 v152, v136, v152\n"
      "v_cvt_pk_bf16_f32 v165, v166, v167\n"
      "v_mfma_f32_16x16x32_f16 v[160:163], v[40:43], v[44:47], v[160:163]\n"
      "s_waitcnt lgkmcnt(0)\n"
      "v_mul_f32 v153, v136, v153\n"
      "v_mul_f32 v154, v136, v154\n"
      "v_mfma_f32_16x16x32_f16 v[140:143], v[148:151], v[208:211], v[74:77]\n"
      "v_mul_f32_e64 v155, v136, v155\n"
      "v_cvt_pk_bf16_f32 v166, v152, v153\n"
      "v_mfma_f32_16x16x32_f16 v[140:143], v[244:247], v[216:219], v[140:143]\n"
      "ds_read_b128 v[156:159], v123 offset:8192\n"
      "v_cvt_pk_bf16_f32 v167, v154, v155\n"
      "v_mfma_f32_16x16x32_f16 v[140:143], v[240:243], v[44:47], v[140:143]\n"
      "ds_write_b128 v124, v[164:167] offset:18432\n"
      "v_mfma_f32_16x16x32_f16 v[164:167], v[216:219], v[208:211], v[110:113]\n"
      "ds_read_b128 v[152:155], v123 offset:11264\n"
      "v_mul_f32_e64 v229, v136, v229\n"
      "v_mfma_f32_16x16x32_f16 v[164:167], v[46:49], v[44:47], v[164:167]\n"
      "v_mul_f32 v228, v136, v228\n"
      "v_mul_f32 v230, v136, v230\n"
      "v_mfma_f32_16x16x32_f16 v[208:211], v[244:247], v[148:151], v[76:79]\n"
      "v_mul_f32_e64 v231, v136, v231\n"
      "v_cvt_pk_bf16_f32 v228, v228, v229\n"
      "v_mfma_f32_16x16x32_bf16 v[148:151], v[66:69], v[200:203], 0\n"
      "v_cvt_pk_bf16_f32 v229, v230, v231\n"
      "v_cndmask_b32 v205, v205, v145, s[42:43]\n"
      "v_mfma_f32_16x16x32_bf16 v[148:151], v[66:69], v[62:65], v[148:151]\n"
      "v_exp_f32 v237, v237\n"
      "v_cndmask_b32 v205, v205, v141, s[44:45]\n"
      "v_mfma_f32_16x16x16_f16 v[208:211], v[242:243], v[240:241], v[208:211]\n"
      "s_add_u32 s28, s27, 2\n"
      "v_exp_f32 v236, v236\n"
      "v_mfma_f32_16x16x32_bf16 v[142:145], v[232:235], v[200:203], 0\n"
      "s_min_u32 m0, s28, s3\n"
      "v_exp_f32 v238, v238\n"
      "v_mfma_f32_16x16x32_bf16 v[142:145], v[50:53], v[62:65], v[142:145]\n"
      "s_movrels_b32 s9, s80\n"
      "v_cvt_pk_bf16_f32 v236, v236, v237\n"
      "v_cndmask_b32 v205, v205, v209, s[46:47]\n"
      "v_mfma_f32_16x16x32_bf16 v[240:243], v[224:227], v[200:203], 0\n"
      "s_waitcnt lgkmcnt(0)\n"
      "v_exp_f32 v205, v205\n"
      "v_mfma_f32_16x16x32_bf16 v[240:243], v[54:57], v[62:65], v[240:243]\n"
      "s_lshr_b32 s8, s9, 8\n"
      "v_cvt_pk_bf16_f32 v205, v205, v205\n"
      "v_exp_f32 v239, v239\n"
      "v_mfma_f32_16x16x32_bf16 v[244:247], v[172:175], v[200:203], 0\n"
      "s_and_b32 s9, s9, 0xff\n"
      "v_exp_f32_e64 v161, v161\n"
      "v_mfma_f32_16x16x32_bf16 v[244:247], v[58:61], v[62:65], v[244:247]\n"
      "ds_write_b16 v130, v205\n"
      "v_mfma_f32_16x16x32_bf16 v[204:207], a[220:223], v[212:215], 0\n"
      "ds_read_b128 v[216:219], v131\n"
      "v_cvt_pk_bf16_f32 v237, v238, v239\n"
      "v_mfma_f32_16x16x32_bf16 v[204:207], a[232:235], v[220:223], v[204:207]\n"
      "ds_read_b128 v[248:251], v131 offset:64\n"
      "v_exp_f32 v160, v160\n"
      "v_mfma_f32_16x16x32_bf16 v[200:203], a[216:219], v[212:215], 0\n"
      "s_mul_i32 s10, s8, 73\n"
      "v_exp_f32 v162, v162\n"
      "v_mfma_f32_16x16x32_bf16 v[200:203], a[228:231], v[220:223], v[200:203]\n"
      "s_add_u32 s10, s10, s9\n"
      "v_exp_f32 v163, v163\n"
      "v_mfma_f32_16x16x32_bf16 v[208:211], a[224:227], v[212:215], 0\n"
      "ds_read_b128 v[136:139], v123 offset:14336\n"
      "v_cvt_pk_bf16_f32 v238, v160, v161\n"
      "v_mfma_f32_16x16x32_bf16 v[208:211], a[236:239], v[220:223], v[208:211]\n"
      "ds_read_b128 v[220:223], v123 offset:17408\n"
      "v_cvt_pk_bf16_f32 v239, v162, v163\n"
      "v_exp_f32 v164, v164\n"
      "v_mfma_f32_16x16x32_bf16 v[200:203], a[240:243], v[156:159], v[200:203]\n"
      "s_mul_i32 s28, s10, 4802\n"
      "v_exp_f32 v165, v165\n"
      "v_mfma_f32_16x16x32_bf16 v[200:203], a[252:255], v[152:155], v[200:203]\n"
      "s_add_u32 s56, s18, s28\n"
      "v_exp_f32_e64 v166, v166\n"
      "v_mfma_f32_16x16x32_bf16 v[204:207], a[244:247], v[156:159], v[204:207]\n"
      "ds_read_b128 v[212:215], v114\n"
      "s_waitcnt lgkmcnt(3)\n"
      "v_mfma_f32_16x16x32_bf16 v[204:207], v[0:3], v[152:155], v[204:207]\n"
      "s_addc_u32 s57, s19, 0\n"
      "v_exp_f32 v167, v167\n"
      "v_mfma_f32_16x16x32_bf16 v[208:211], a[248:251], v[156:159], v[208:211]\n"
      "ds_read_b128 v[156:159], v115\n"
      "v_cvt_pk_bf16_f32 v62, v164, v165\n"
      "v_mfma_f32_16x16x32_bf16 v[208:211], v[4:7], v[152:155], v[208:211]\n"
      "s_and_b32 s56, s56, -16\n"
      "v_exp_f32 v140, v140\n"
      "v_mfma_f32_16x16x32_bf16 v[152:155], v[216:219], v[66:69], 0\n"
      "s_sub_u32 s58, s56, 110592\n"
      "v_cvt_pk_bf16_f32 v64, v140, 0\n"
      "v_cvt_pk_bf16_f32 v63, v166, v167\n"
      "v_mfma_f32_16x16x32_bf16 v[152:155], v[248:251], v[66:69], v[152:155]\n"
      "s_waitcnt lgkmcnt(2)\n"
      "v_rcp_f32 v148, v148\n"
      "v_mfma_f32_16x16x32_bf16 v[200:203], v[8:11], v[136:139], v[200:203]\n"
      "s_subb_u32 s59, s57, 0\n"
      "v_mul_f32 v143, v148, v143\n"
      "v_mul_f32 v142, v148, v142\n"
      "v_mfma_f32_16x16x32_bf16 v[200:203], v[20:23], v[220:223], v[200:203]\n"
      "s_sub_u32 s56, s56, 73728\n"
      "v_mul_f32 v145, v148, v145\n"
      "v_cvt_pk_bf16_f32 v142, v142, v143\n"
      "v_mfma_f32_16x16x32_bf16 v[204:207], v[12:15], v[136:139], v[204:207]\n"
      "s_subb_u32 s57, s57, 0\n"
      "v_mul_f32 v144, v148, v144\n"
      "v_cvt_pk_bf16_f32 v143, v144, v145\n"
      "v_mfma_f32_16x16x32_bf16 v[204:207], v[24:27], v[220:223], v[204:207]\n"
      "v_mul_f32 v240, v148, v240\n"
      "v_mul_f32 v241, v148, v241\n"
      "v_mfma_f32_16x16x32_bf16 v[208:211], v[16:19], v[136:139], v[208:211]\n"
      "s_mul_i32 s28, s8, 1373568\n"
      "s_waitcnt lgkmcnt(0)\n"
      "v_mul_f32 v243, v148, v243\n"
      "v_mfma_f32_16x16x32_bf16 v[208:211], v[28:31], v[220:223], v[208:211]\n"
      "s_mul_i32 s29, s9, 2688\n"
      "v_mul_f32 v242, v148, v242\n"
      "v_cvt_pk_bf16_f32 v144, v240, v241\n"
      "v_mfma_f32_16x16x32_bf16 v[136:139], v[66:69], v[236:239], 0\n"
      "s_add_u32 s28, s28, s29\n"
      "v_cvt_pk_bf16_f32 v145, v242, v243\n"
      "v_mul_f32_e64 v245, v148, v245\n"
      "v_mfma_f32_16x16x32_bf16 v[136:139], v[66:69], v[62:65], v[136:139]\n"
      "ds_write_b128 v124, v[142:145] offset:19456\n"
      "v_mfma_f32_16x16x32_bf16 v[140:143], v[232:235], v[236:239], 0\n"
      "s_cmp_eq_u32 s8, 72\n"
      "v_mul_f32 v244, v148, v244\n"
      "v_mul_f32 v246, v148, v246\n"
      "v_mfma_f32_16x16x32_bf16 v[140:143], v[50:53], v[62:65], v[140:143]\n"
      "s_cselect_b32 s8, 0, 100270464\n"
      "v_cvt_pk_bf16_f32 v244, v244, v245\n"
      "v_mul_f32_e64 v247, v148, v247\n"
      "v_mfma_f32_16x16x32_bf16 v[160:163], v[224:227], v[236:239], 0\n"
      "s_cmp_eq_u32 s9, 72\n"
      "v_fma_f32 v144, v200, v200, s39\n"
      "v_cvt_pk_bf16_f32 v245, v246, v247\n"
      "v_mfma_f32_16x16x32_bf16 v[160:163], v[54:57], v[62:65], v[160:163]\n"
      "ds_write2st64_b64 v125, v[228:229], v[244:245] offset0:36 offset1:38\n"
      "v_mfma_f32_16x16x32_bf16 v[164:167], v[172:175], v[236:239], 0\n"
      "s_cselect_b32 s9, 0, 196224\n"
      "v_fmac_f32 v144, v201, v201\n"
      "v_fmac_f32 v144, v202, v202\n"
      "v_mfma_f32_16x16x32_bf16 v[164:167], v[58:61], v[62:65], v[164:167]\n"
      "s_add_u32 s10, s8, s9\n"
      "v_fmac_f32 v144, v203, v203\n"
      "v_fmac_f32 v144, v204, v204\n"
      "v_mfma_f32_16x16x32_bf16 v[148:151], v[216:219], v[232:235], 0\n"
      "s_add_u32 s28, s28, s10\n"
      "v_fmac_f32 v144, v205, v205\n"
      "v_fmac_f32 v144, v206, v206\n"
      "v_mfma_f32_16x16x32_bf16 v[148:151], v[248:251], v[50:53], v[148:151]\n"
      "v_fmac_f32 v144, v207, v207\n"
      "v_fmac_f32 v144, v208, v208\n"
      "v_mfma_f32_16x16x32_bf16 v[232:235], v[216:219], v[224:227], 0\n"
      "s_add_u32 s52, s16, s28\n"
      "v_fmac_f32 v144, v209, v209\n"
      "v_fmac_f32 v144, v210, v210\n"
      "v_mfma_f32_16x16x32_bf16 v[232:235], v[248:251], v[54:57], v[232:235]\n"
      "s_addc_u32 s53, s17, 0\n"
      "v_permlane16_swap_b32_e64 v168, v146\n"
      "v_mfma_f32_16x16x32_bf16 v[224:227], v[216:219], v[172:175], 0\n"
      "s_add_u32 s27, s27, 1\n"
      "v_fmac_f32 v144, v211, v211\n"
      "v_mov_b32 v145, v144\n"
      "v_mfma_f32_16x16x32_bf16 v[224:227], v[248:251], v[58:61], v[224:227]\n"
      "s_cmp_ge_u32 s27, s26\n"
      "v_permlane16_swap_b32 v144, v145\n"
      "v_mfma_f32_16x16x32_bf16 v[172:175], a[0:3], v[212:215], 0\n"
      "s_cselect_b32 s28, 1, 0\n"
      "v_add_f32 v144, v144, v145\n"
      "v_add_f32 v168, v168, v146\n"
      "v_mfma_f32_16x16x32_bf16 v[172:175], a[36:39], v[156:159], v[172:175]\n"
      "s_xor_b32 s29, s10, s11\n"
      "v_permlane32_swap_b32 v168, v144\n"
      "v_mfma_f32_16x16x32_bf16 v[248:251], a[4:7], v[212:215], 0\n"
      "v_add_f32_e64 v168, v168, v144\n"
      "v_mov_b32_dpp v152, v152 quad_perm:[0,0,0,0] row_mask:0xf bank_mask:0xf\n"
      "v_mfma_f32_16x16x32_bf16 v[248:251], a[40:43], v[156:159], v[248:251]\n"
      "ds_write_b32 v129, v168 offset:1024\n"
      "v_mfma_f32_16x16x32_bf16 v[144:147], a[8:11], v[212:215], 0\n"
      "v_cndmask_b32 v148, v148, v232, s[42:43]\n"
      "v_cndmask_b32 v148, v148, v224, s[44:45]\n"
      "v_mfma_f32_16x16x32_bf16 v[144:147], a[44:47], v[156:159], v[144:147]\n"
      "s_or_b32 s28, s28, s29\n"
      "v_rcp_f32 v152, v152\n"
      "v_mfma_f32_16x16x32_bf16 v[168:171], a[12:15], v[212:215], 0\n"
      "v_mul_f32 v148, v152, v148\n"
      "v_cvt_pk_bf16_f32 v148, v148, v148\n"
      "v_mfma_f32_16x16x32_bf16 v[168:171], a[48:51], v[156:159], v[168:171]\n"
      "ds_write_b16 v135, v148 offset:16\n"
      "L_join_21_%=:\n"
      "s_cbranch_scc1 L_tail1_%=\n"
      "L_mid_%=:\n"
      "v_mfma_f32_16x16x32_bf16 v[148:151], a[16:19], v[212:215], 0\n"
      "s_add_u32 m0, s76, 18432\n"
      "ds_read_b128 v[152:155], v114 offset:6144\n"
      "v_mfma_f32_16x16x32_bf16 v[148:151], a[52:55], v[156:159], v[148:151]\n"
      "ds_read_b128 v[224:227], v115 offset:6144\n"
      "v_rcp_f32 v136, v136\n"
      "v_mfma_f32_16x16x32_bf16 v[232:235], a[20:23], v[212:215], 0\n"
      "v_mul_f32 v140, v136, v140\n"
      "v_mul_f32_e64 v141, v136, v141\n"
      "v_mfma_f32_16x16x32_bf16 v[232:235], a[56:59], v[156:159], v[232:235]\n"
      "s_waitcnt lgkmcnt(2)\n"
      "s_barrier\n"
      "v_mfma_f32_16x16x32_bf16 v[216:219], v[212:215], a[24:27], 0\n"
      "ds_read_b128 v[236:239], v128 offset:1024\n"
      "v_cvt_pk_bf16_f32 v140, v140, v141\n"
      "v_mfma_f32_16x16x32_bf16 v[216:219], v[156:159], a[60:63], v[216:219]\n"
      "global_load_lds_dwordx4 v116, s[52:53]\n"
      "v_mfma_f32_16x16x32_bf16 v[244:247], v[212:215], a[28:31], 0\n"
      "s_add_u32 m0, s76, 24448\n"
      "v_mul_f32 v142, v136, v142\n"
      "v_mul_f32 v160, v136, v160\n"
      "v_mfma_f32_16x16x32_bf16 v[244:247], v[156:159], a[64:67], v[244:247]\n"
      "s_waitcnt lgkmcnt(1)\n"
      "v_mul_f32 v161, v136, v161\n"
      "v_mul_f32 v143, v136, v143\n"
      "v_mfma_f32_16x16x32_bf16 v[172:175], a[72:75], v[152:155], v[172:175]\n"
      "v_cvt_pk_bf16_f32 v141, v142, v143\n"
      "v_mul_f32 v162, v136, v162\n"
      "v_mfma_f32_16x16x32_bf16 v[172:175], a[108:111], v[224:227], v[172:175]\n"
      "v_cvt_pk_bf16_f32 v142, v160, v161\n"
      "v_mul_f32 v164, v136, v164\n"
      "v_mfma_f32_16x16x32_bf16 v[248:251], a[76:79], v[152:155], v[248:251]\n"
      "v_mul_f32 v163, v136, v163\n"
      "v_cvt_pk_bf16_f32 v143, v162, v163\n"
      "v_mfma_f32_16x16x32_bf16 v[248:251], a[112:115], v[224:227], v[248:251]\n"
      "ds_write_b128 v124, v[140:143] offset:20480\n"
      "v_mfma_f32_16x16x32_bf16 v[240:243], v[212:215], a[32:35], 0\n"
      "ds_read_b128 v[140:143], v114 offset:12288\n"
      "v_mfma_f32_16x16x32_bf16 v[240:243], v[156:159], a[68:71], v[240:243]\n"
      "global_load_lds_dwordx4 v116, s[52:53] offset:128\n"
      "v_mfma_f32_16x16x32_bf16 v[144:147], a[80:83], v[152:155], v[144:147]\n"
      "ds_read_b128 v[160:163], v115 offset:12288\n"
      "v_mul_f32 v166, v136, v166\n"
      "v_mfma_f32_16x16x32_bf16 v[144:147], a[116:119], v[224:227], v[144:147]\n"
      "v_mul_f32 v165, v136, v165\n"
      "v_cvt_pk_bf16_f32 v164, v164, v165\n"
      "v_mfma_f32_16x16x32_bf16 v[168:171], a[84:87], v[152:155], v[168:171]\n"
      "s_waitcnt lgkmcnt(3)\n"
      "v_mul_f32 v167, v136, v167\n"
      "v_cvt_pk_bf16_f32 v165, v166, v167\n"
      "v_mfma_f32_16x16x32_bf16 v[168:171], a[120:123], v[224:227], v[168:171]\n"
      "ds_write_b64 v125, v[164:165] offset:20480\n"
      "v_mfma_f32_16x16x32_bf16 v[148:151], a[88:91], v[152:155], v[148:151]\n"
      "v_add_f32 v238, |v238|, |v239|\n"
      "v_add_f32 v236, |v236|, |v237|\n"
      "v_mfma_f32_16x16x32_bf16 v[148:151], a[124:127], v[224:227], v[148:151]\n"
      "ds_read_b128 v[136:139], v114 offset:2048\n"
      "v_add_f32 v236, v236, v238\n"
      "v_mfma_f32_16x16x32_bf16 v[232:235], a[92:95], v[152:155], v[232:235]\n"
      "s_add_u32 m0, s76, 30464\n"
      "v_rsq_f32 v236, v236\n"
      "v_mfma_f32_16x16x32_bf16 v[232:235], a[128:131], v[224:227], v[232:235]\n"
      "global_load_lds_dwordx4 v116, s[52:53] offset:256\n"
      "v_mfma_f32_16x16x32_bf16 v[216:219], v[152:155], a[96:99], v[216:219]\n"
      "v_mul_f32 v236, s41, v236\n"
      "v_mov_b32 v164, v236\n"
      "v_mfma_f32_16x16x32_bf16 v[216:219], v[224:227], a[132:135], v[216:219]\n"
      "ds_read_b128 v[156:159], v115 offset:2048\n"
      "v_permlane32_swap_b32 v236, v164\n"
      "v_mfma_f32_16x16x32_bf16 v[244:247], v[152:155], a[100:103], v[244:247]\n"
      "s_waitcnt lgkmcnt(3)\n"
      "v_mul_f32 v202, v164, v202\n"
      "v_mov_b32 v165, v236\n"
      "v_mfma_f32_16x16x32_bf16 v[244:247], v[224:227], a[136:139], v[244:247]\n"
      "s_add_u32 m0, s77, 18432\n"
      "v_permlane16_swap_b32_e64 v236, v165\n"
      "v_mfma_f32_16x16x32_bf16 v[240:243], v[152:155], a[104:107], v[240:243]\n"
      "v_mul_f32 v178, v236, v178\n"
      "v_mul_f32 v177, v236, v177\n"
      "v_mfma_f32_16x16x32_bf16 v[240:243], v[224:227], a[140:143], v[240:243]\n"
      "v_mul_f32_e64 v176, v236, v176\n"
      "v_cvt_pk_bf16_f32 v176, v176, v177\n"
      "v_mfma_f32_16x16x32_bf16 v[172:175], a[144:147], v[140:143], v[172:175]\n"
      "v_mul_f32 v181, v236, v181\n"
      "v_mul_f32 v179, v236, v179\n"
      "v_mfma_f32_16x16x32_bf16 v[172:175], a[180:183], v[160:163], v[172:175]\n"
      "global_load_lds_dwordx4 v117, s[52:53]\n"
      "v_mfma_f32_16x16x32_bf16 v[248:251], a[148:151], v[140:143], v[248:251]\n"
      "s_add_u32 m0, s78, 18432\n"
      "v_cvt_pk_bf16_f32 v177, v178, v179\n"
      "v_mul_f32 v182, v236, v182\n"
      "v_mfma_f32_16x16x32_bf16 v[248:251], a[184:187], v[160:163], v[248:251]\n"
      "s_waitcnt lgkmcnt(0)\n"
      "v_mul_f32 v183, v236, v183\n"
      "v_mul_f32 v180, v236, v180\n"
      "v_mfma_f32_16x16x32_bf16 v[144:147], a[152:155], v[140:143], v[144:147]\n"
      "v_cvt_pk_bf16_f32 v179, v182, v183\n"
      "v_cvt_pk_bf16_f32 v178, v180, v181\n"
      "v_mfma_f32_16x16x32_bf16 v[144:147], a[188:191], v[160:163], v[144:147]\n"
      "ds_write_b128 v126, v[176:179]\n"
      "v_mfma_f32_16x16x32_bf16 v[168:171], a[156:159], v[140:143], v[168:171]\n"
      "v_mul_f32 v185, v236, v185\n"
      "v_mul_f32 v184, v236, v184\n"
      "v_mfma_f32_16x16x32_bf16 v[168:171], a[192:195], v[160:163], v[168:171]\n"
      "v_mul_f32 v186, v236, v186\n"
      "v_cvt_pk_bf16_f32 v184, v184, v185\n"
      "v_mfma_f32_16x16x32_bf16 v[148:151], a[160:163], v[140:143], v[148:151]\n"
      "v_mul_f32 v187, v236, v187\n"
      "v_cvt_pk_bf16_f32 v185, v186, v187\n"
      "v_mfma_f32_16x16x32_bf16 v[148:151], a[196:199], v[160:163], v[148:151]\n"
      "s_mov_b32 exec_hi, 0\n"
      "global_load_lds_dwordx4 v118, s[52:53]\n"
      "s_mov_b32 exec_hi, -1\n"
      "v_mfma_f32_16x16x32_bf16 v[232:235], a[164:167], v[140:143], v[232:235]\n"
      "v_mul_f32 v189, v165, v189\n"
      "v_mul_f32 v190, v165, v190\n"
      "v_mfma_f32_16x16x32_bf16 v[232:235], a[200:203], v[160:163], v[232:235]\n"
      "s_add_u32 m0, s76, 115456\n"
      "v_mul_f32_e64 v188, v165, v188\n"
      "v_cvt_pk_bf16_f32 v188, v188, v189\n"
      "v_mfma_f32_16x16x32_bf16 v[216:219], v[140:143], a[168:171], v[216:219]\n"
      "ds_read_b128 v[176:179], v114 offset:8192\n"
      "v_mul_f32_e64 v191, v165, v191\n"
      "v_mfma_f32_16x16x32_bf16 v[216:219], v[160:163], a[204:207], v[216:219]\n"
      "ds_read_b128 v[180:183], v115 offset:8192\n"
      "v_mul_f32 v192, v165, v192\n"
      "v_mfma_f32_16x16x32_bf16 v[244:247], v[140:143], a[172:175], v[244:247]\n"
      "v_mul_f32 v194, v165, v194\n"
      "v_cvt_pk_bf16_f32 v189, v190, v191\n"
      "v_mfma_f32_16x16x32_bf16 v[244:247], v[160:163], a[208:211], v[244:247]\n"
      "v_mul_f32 v193, v165, v193\n"
      "v_cvt_pk_bf16_f32 v190, v192, v193\n"
      "v_mfma_f32_16x16x32_bf16 v[240:243], v[140:143], a[176:179], v[240:243]\n"
      "v_mul_f32 v195, v165, v195\n"
      "v_cvt_pk_bf16_f32 v191, v194, v195\n"
      "v_mfma_f32_16x16x32_bf16 v[240:243], v[160:163], a[212:215], v[240:243]\n"
      "ds_write_b128 v126, v[188:191] offset:2048\n"
      "v_mfma_f32_16x16x32_bf16 v[188:191], a[4:7], v[136:139], 0\n"
      "global_load_lds_dwordx4 v119, s[56:57]\n"
      "v_mfma_f32_16x16x32_bf16 v[188:191], a[40:43], v[156:159], v[188:191]\n"
      "s_add_u32 m0, s79, 4864\n"
      "v_mul_f32 v196, v165, v196\n"
      "v_mul_f32 v197, v165, v197\n"
      "v_mfma_f32_16x16x32_bf16 v[192:195], a[0:3], v[136:139], 0\n"
      "v_mul_f32 v205, v164, v205\n"
      "v_cvt_pk_bf16_f32 v196, v196, v197\n"
      "v_mfma_f32_16x16x32_bf16 v[192:195], a[36:39], v[156:159], v[192:195]\n"
      "v_mul_f32 v199, v165, v199\n"
      "v_mul_f32_e64 v198, v165, v198\n"
      "v_mfma_f32_16x16x32_bf16 v[140:143], a[8:11], v[136:139], 0\n"
      "v_cvt_pk_bf16_f32 v197, v198, v199\n"
      "v_mul_f32_e64 v200, v164, v200\n"
      "v_mfma_f32_16x16x32_bf16 v[140:143], a[44:47], v[156:159], v[140:143]\n"
      "ds_write2st64_b64 v127, v[184:185], v[196:197] offset0:0 offset1:4\n"
      "v_mfma_f32_16x16x32_bf16 v[160:163], a[12:15], v[136:139], 0\n"
      "v_mul_f32_e64 v203, v164, v203\n"
      "v_mul_f32 v201, v164, v201\n"
      "v_mfma_f32_16x16x32_bf16 v[160:163], a[48:51], v[156:159], v[160:163]\n"
      "s_waitcnt lgkmcnt(1)\n"
      "v_cvt_pk_bf16_f32 v200, v200, v201\n"
      "v_cvt_pk_bf16_f32 v201, v202, v203\n"
      "v_mfma_f32_16x16x32_bf16 v[184:187], a[16:19], v[136:139], 0\n"
      "global_load_lds_dword v122, s[58:59]\n"
      "v_mfma_f32_16x16x32_bf16 v[184:187], a[52:55], v[156:159], v[184:187]\n"
      "ds_read_b128 v[224:227], v115 offset:14336\n"
      "v_mul_f32 v204, v164, v204\n"
      "v_mfma_f32_16x16x32_bf16 v[196:199], a[20:23], v[136:139], 0\n"
      "ds_read_b128 v[152:155], v114 offset:14336\n"
      "v_mfma_f32_16x16x32_bf16 v[196:199], a[56:59], v[156:159], v[196:199]\n"
      "v_mul_f32 v206, v164, v206\n"
      "v_cvt_pk_bf16_f32 v202, v204, v205\n"
      "v_mfma_f32_16x16x32_bf16 v[236:239], v[136:139], a[24:27], 0\n"
      "v_mul_f32_e64 v207, v164, v207\n"
      "v_cvt_pk_bf16_f32 v203, v206, v207\n"
      "v_mfma_f32_16x16x32_bf16 v[236:239], v[156:159], a[60:63], v[236:239]\n"
      "ds_write_b128 v126, v[200:203] offset:4096\n"
      "v_mfma_f32_16x16x32_bf16 v[204:207], v[136:139], a[28:31], 0\n"
      "v_mul_f32 v210, v164, v210\n"
      "v_mul_f32 v208, v164, v208\n"
      "v_mfma_f32_16x16x32_bf16 v[204:207], v[156:159], a[64:67], v[204:207]\n"
      "v_mul_f32 v209, v164, v209\n"
      "v_cvt_pk_bf16_f32 v208, v208, v209\n"
      "v_mfma_f32_16x16x32_bf16 v[200:203], v[136:139], a[32:35], 0\n"
      "v_mul_f32 v211, v164, v211\n"
      "v_cvt_pk_bf16_f32 v209, v210, v211\n"
      "v_mfma_f32_16x16x32_bf16 v[200:203], v[156:159], a[68:71], v[200:203]\n"
      "ds_write_b64 v127, v[208:209] offset:4096\n"
      "v_mfma_f32_16x16x32_bf16 v[192:195], a[72:75], v[176:179], v[192:195]\n"
      "v_fma_f32 v208, v168, v168, s38\n"
      "v_fma_f32 v209, v172, v172, s38\n"
      "v_mfma_f32_16x16x32_bf16 v[192:195], a[108:111], v[180:183], v[192:195]\n"
      "v_fmac_f32 v209, v173, v173\n"
      "v_fmac_f32 v208, v169, v169\n"
      "v_mfma_f32_16x16x32_bf16 v[188:191], a[76:79], v[176:179], v[188:191]\n"
      "v_fmac_f32 v209, v174, v174\n"
      "v_fmac_f32 v209, v175, v175\n"
      "v_mfma_f32_16x16x32_bf16 v[188:191], a[112:115], v[180:183], v[188:191]\n"
      "v_fmac_f32 v208, v170, v170\n"
      "v_fmac_f32 v208, v171, v171\n"
      "v_mfma_f32_16x16x32_bf16 v[140:143], a[80:83], v[176:179], v[140:143]\n"
      "v_fmac_f32 v209, v248, v248\n"
      "v_fmac_f32 v209, v249, v249\n"
      "v_mfma_f32_16x16x32_bf16 v[140:143], a[116:119], v[180:183], v[140:143]\n"
      "ds_read_b128 v[136:139], v114 offset:4096\n"
      "v_fmac_f32 v209, v250, v250\n"
      "v_mfma_f32_16x16x32_bf16 v[160:163], a[84:87], v[176:179], v[160:163]\n"
      "ds_read_b128 v[164:167], v115 offset:4096\n"
      "s_waitcnt lgkmcnt(3)\n"
      "v_mfma_f32_16x16x32_bf16 v[160:163], a[120:123], v[180:183], v[160:163]\n"
      "v_fmac_f32 v208, v148, v148\n"
      "v_fmac_f32 v208, v149, v149\n"
      "v_mfma_f32_16x16x32_bf16 v[196:199], a[92:95], v[176:179], v[196:199]\n"
      "v_fmac_f32 v209, v251, v251\n"
      "v_fmac_f32 v208, v150, v150\n"
      "v_mfma_f32_16x16x32_bf16 v[196:199], a[128:131], v[180:183], v[196:199]\n"
      "v_fmac_f32 v208, v151, v151\n"
      "v_fmac_f32 v209, v144, v144\n"
      "v_mfma_f32_16x16x32_bf16 v[184:187], a[88:91], v[176:179], v[184:187]\n"
      "v_fmac_f32 v208, v232, v232\n"
      "v_fmac_f32 v209, v145, v145\n"
      "v_mfma_f32_16x16x32_bf16 v[184:187], a[124:127], v[180:183], v[184:187]\n"
      "v_fmac_f32 v209, v146, v146\n"
      "v_fmac_f32 v208, v233, v233\n"
      "v_mfma_f32_16x16x32_bf16 v[236:239], v[176:179], a[96:99], v[236:239]\n"
      "v_fmac_f32 v208, v234, v234\n"
      "v_fmac_f32 v209, v147, v147\n"
      "v_mfma_f32_16x16x32_bf16 v[236:239], v[180:183], a[132:135], v[236:239]\n"
      "ds_read_b128 v[156:159], v119 offset:18432\n"
      "v_cvt_pk_f16_f32 v168, v168, v169\n"
      "v_mfma_f32_16x16x32_bf16 v[204:207], v[176:179], a[100:103], v[204:207]\n"
      "v_fmac_f32_e64 v208, v235, v235\n"
      "v_cvt_pk_f16_f32 v169, v170, v171\n"
      "v_mfma_f32_16x16x32_bf16 v[204:207], v[180:183], a[136:139], v[204:207]\n"
      "v_permlane16_swap_b32 v209, v208\n"
      "v_mfma_f32_16x16x32_bf16 v[200:203], v[176:179], a[104:107], v[200:203]\n"
      "v_add_f32 v209, v209, v208\n"
      "v_mov_b32_e64 v208, v209\n"
      "v_mfma_f32_16x16x32_bf16 v[200:203], v[180:183], a[140:143], v[200:203]\n"
      "v_cvt_pk_f16_f32 v171, v150, v151\n"
      "v_cvt_pk_f16_f32 v170, v148, v149\n"
      "v_mfma_f32_16x16x32_bf16 v[192:195], a[144:147], v[152:155], v[192:195]\n"
      "v_permlane32_swap_b32 v209, v208\n"
      "v_mfma_f32_16x16x32_bf16 v[192:195], a[180:183], v[224:227], v[192:195]\n"
      "v_add_f32 v209, v209, v208\n"
      "v_cvt_pk_f16_f32 v34, v232, v233\n"
      "v_mfma_f32_16x16x32_bf16 v[188:191], a[148:151], v[152:155], v[188:191]\n"
      "v_cvt_pk_f16_f32 v172, v172, v173\n"
      "v_cvt_pk_f16_f32 v35, v234, v235\n"
      "v_mfma_f32_16x16x32_bf16 v[188:191], a[184:187], v[224:227], v[188:191]\n"
      "v_rsq_f32 v209, v209\n"
      "v_mfma_f32_16x16x32_bf16 v[140:143], a[152:155], v[152:155], v[140:143]\n"
      "ds_read_b128 v[176:179], v114 offset:10240\n"
      "v_mov_b32 v208, v209\n"
      "v_mfma_f32_16x16x32_bf16 v[140:143], a[188:191], v[224:227], v[140:143]\n"
      "s_waitcnt lgkmcnt(1)\n"
      "v_cmp_nge_f32 vcc, 0x43800000, v208\n"
      "v_cvt_pk_f16_f32 v173, v174, v175\n"
      "v_mfma_f32_16x16x32_bf16 v[160:163], a[156:159], v[152:155], v[160:163]\n"
      "ds_read_b128 v[180:183], v115 offset:10240\n"
      "v_cvt_pk_f16_f32 v174, v248, v249\n"
      "v_mfma_f32_16x16x32_bf16 v[160:163], a[192:195], v[224:227], v[160:163]\n"
      "s_mov_b64 s[34:35], vcc\n"
      "v_cvt_pk_f16_f32 v32, v144, v145\n"
      "v_cmp_nle_f32 vcc, 0x3a000000, v208\n"
      "v_mfma_f32_16x16x32_bf16 v[184:187], a[160:163], v[152:155], v[184:187]\n"
      "s_or_b64 s[34:35], s[34:35], vcc\n"
      "v_permlane16_swap_b32 v209, v208\n"
      "v_mfma_f32_16x16x32_bf16 v[184:187], a[196:199], v[224:227], v[184:187]\n"
      "s_sub_u32 s28, s27, 3\n"
      "v_cvt_pk_f16_f32 v175, v250, v251\n"
      "v_cvt_pk_f16_f32 v33, v146, v147\n"
      "v_mfma_f32_16x16x32_bf16 v[196:199], a[164:167], v[152:155], v[196:199]\n"
      "s_cselect_b64 s[54:55], s[12:13], s[22:23]\n"
      "v_mul_f32 v209, s36, v209\n"
      "v_cvt_pk_f16_f32 v209, v209, v208\n"
      "v_mfma_f32_16x16x32_bf16 v[196:199], a[200:203], v[224:227], v[196:199]\n"
      "s_max_i32 m0, s28, 0\n"
      "v_pk_mul_f16 v168, v168, v209 op_sel:[0,1] op_sel_hi:[1,1]\n"
      "v_pk_mul_f16 v169, v169, v209 op_sel:[0,1] op_sel_hi:[1,1]\n"
      "v_mfma_f32_16x16x32_bf16 v[236:239], v[152:155], a[168:171], v[236:239]\n"
      "ds_read_b128 v[144:147], v119 offset:24576\n"
      "v_pk_mul_f16 v171, v171, v209 op_sel:[0,1] op_sel_hi:[1,1]\n"
      "v_mfma_f32_16x16x32_bf16 v[236:239], v[224:227], a[204:207], v[236:239]\n"
      "s_movrels_b32 s9, s80\n"
      "v_pk_mul_f16 v170, v170, v209 op_sel:[0,1] op_sel_hi:[1,1]\n"
      "v_pk_mul_f16 v34, v34, v209 op_sel:[0,1] op_sel_hi:[1,1]\n"
      "v_mfma_f32_16x16x32_bf16 v[204:207], v[152:155], a[172:175], v[204:207]\n"
      "s_lshr_b32 s8, s9, 8\n"
      "v_pk_mul_f16 v172, v172, v209 op_sel_hi:[1,0]\n"
      "v_pk_mul_f16 v35, v35, v209 op_sel:[0,1] op_sel_hi:[1,1]\n"
      "v_mfma_f32_16x16x32_bf16 v[204:207], v[224:227], a[208:211], v[204:207]\n"
      "s_and_b32 s9, s9, 0xff\n"
      "v_pk_mul_f16 v173, v173, v209 op_sel_hi:[1,0]\n"
      "v_pk_mul_f16 v174, v174, v209 op_sel_hi:[1,0]\n"
      "v_mfma_f32_16x16x32_bf16 v[200:203], v[152:155], a[176:179], v[200:203]\n"
      "s_mul_i32 s28, s8, 1373568\n"
      "v_pk_mul_f16 v33, v33, v209 op_sel_hi:[1,0]\n"
      "v_pk_mul_f16 v175, v175, v209 op_sel_hi:[1,0]\n"
      "v_mfma_f32_16x16x32_bf16 v[200:203], v[224:227], a[212:215], v[200:203]\n"
      "s_mul_i32 s29, s9, 2688\n"
      "v_cvt_pk_bf16_f32 v216, v216, v217\n"
      "v_pk_mul_f16 v32, v32, v209 op_sel_hi:[1,0]\n"
      "v_mfma_f32_16x16x32_bf16 v[208:211], a[0:3], v[136:139], 0\n"
      "s_add_u32 s28, s28, s29\n"
      "v_cvt_pk_bf16_f32 v217, v218, v219\n"
      "v_cvt_pk_bf16_f32 v218, v236, v237\n"
      "v_mfma_f32_16x16x32_bf16 v[208:211], a[36:39], v[164:167], v[208:211]\n"
      "s_waitcnt lgkmcnt(1)\n"
      "v_cvt_pk_bf16_f32 v219, v238, v239\n"
      "v_cvt_pk_bf16_f32 v244, v244, v245\n"
      "v_mfma_f32_16x16x32_bf16 v[152:155], a[4:7], v[136:139], 0\n"
      "s_cmp_eq_u32 s8, 72\n"
      "v_cvt_pk_bf16_f32 v245, v246, v247\n"
      "v_cvt_pk_bf16_f32 v246, v204, v205\n"
      "v_mfma_f32_16x16x32_bf16 v[152:155], a[40:43], v[164:167], v[152:155]\n"
      "s_cselect_b32 s8, 0, 100270464\n"
      "v_cvt_pk_bf16_f32 v247, v206, v207\n"
      "v_cvt_pk_bf16_f32 v240, v240, v241\n"
      "v_mfma_f32_16x16x32_bf16 v[204:207], a[8:11], v[136:139], 0\n"
      "s_cmp_eq_u32 s9, 72\n"
      "v_cvt_pk_bf16_f32 v241, v242, v243\n"
      "v_cvt_pk_bf16_f32 v242, v200, v201\n"
      "v_mfma_f32_16x16x32_bf16 v[204:207], a[44:47], v[164:167], v[204:207]\n"
      "s_cselect_b32 s9, 0, 196224\n"
      "v_cvt_pk_bf16_f32 v243, v202, v203\n"
      "v_fma_f32 v202, v192, v192, s38\n"
      "v_mfma_f32_16x16x32_bf16 v[148:151], a[12:15], v[136:139], 0\n"
      "s_add_u32 s10, s8, s9\n"
      "v_fmac_f32 v202, v193, v193\n"
      "v_fma_f32 v203, v160, v160, s38\n"
      "v_mfma_f32_16x16x32_bf16 v[148:151], a[48:51], v[164:167], v[148:151]\n"
      "s_waitcnt lgkmcnt(0)\n"
      "s_add_u32 s28, s28, s10\n"
      "v_fmac_f32 v203, v161, v161\n"
      "v_fmac_f32 v202, v194, v194\n"
      "v_mfma_f32_16x16x32_bf16 v[236:239], a[16:19], v[136:139], 0\n"
      "s_add_u32 s54, s54, s28\n"
      "v_fmac_f32 v202, v195, v195\n"
      "v_fmac_f32 v203, v162, v162\n"
      "v_mfma_f32_16x16x32_bf16 v[236:239], a[52:55], v[164:167], v[236:239]\n"
      "s_addc_u32 s55, s55, 0\n"
      "v_fmac_f32 v203, v163, v163\n"
      "v_fmac_f32 v203, v184, v184\n"
      "v_mfma_f32_16x16x32_bf16 v[224:227], a[20:23], v[136:139], 0\n"
      "s_cmp_lg_u32 s10, s11\n"
      "v_fmac_f32 v203, v185, v185\n"
      "v_fmac_f32_e64 v202, v188, v188\n"
      "v_mfma_f32_16x16x32_bf16 v[224:227], a[56:59], v[164:167], v[224:227]\n"
      "s_cbranch_scc1 L_edge_26_%=\n"
      "L_eb_27_%=:\n"
      "v_fmac_f32 v203, v186, v186\n"
      "v_mfma_f32_16x16x32_bf16 v[248:251], v[136:139], a[24:27], 0\n"
      "global_store_dwordx4 v116, v[156:159], s[54:55] sc1\n"
      "v_mfma_f32_16x16x32_bf16 v[248:251], v[164:167], a[60:63], v[248:251]\n"
      "s_mov_b32 s5, s4\n"
      "v_fmac_f32 v202, v189, v189\n"
      "v_fmac_f32 v202, v190, v190\n"
      "v_mfma_f32_16x16x32_bf16 v[156:159], v[136:139], a[28:31], 0\n"
      "s_add_u32 s4, s27, 1\n"
      "v_fmac_f32 v203, v187, v187\n"
      "v_fmac_f32 v202, v191, v191\n"
      "v_mfma_f32_16x16x32_bf16 v[156:159], v[164:167], a[64:67], v[156:159]\n"
      "s_lshl_b32 s4, s4, 30\n"
      "v_fmac_f32 v203, v196, v196\n"
      "v_fmac_f32_e64 v202, v140, v140\n"
      "v_mfma_f32_16x16x32_bf16 v[232:235], v[136:139], a[32:35], 0\n"
      "ds_read_b128 v[136:139], v114 offset:16384\n"
      "v_fmac_f32_e64 v202, v141, v141\n"
      "v_mfma_f32_16x16x32_bf16 v[232:235], v[164:167], a[68:71], v[232:235]\n"
      "ds_read_b128 v[164:167], v115 offset:16384\n"
      "v_fmac_f32 v202, v142, v142\n"
      "v_mfma_f32_16x16x32_bf16 v[208:211], a[72:75], v[176:179], v[208:211]\n"
      "v_fmac_f32 v203, v197, v197\n"
      "v_fmac_f32_e64 v203, v198, v198\n"
      "v_mfma_f32_16x16x32_bf16 v[208:211], a[108:111], v[180:183], v[208:211]\n"
      "s_and_b32 s4, s4, 0x80000000\n"
      "v_fmac_f32 v202, v143, v143\n"
      "v_fmac_f32 v203, v199, v199\n"
      "v_mfma_f32_16x16x32_bf16 v[152:155], a[76:79], v[176:179], v[152:155]\n"
      "s_nop 0\n"
      "v_permlane16_swap_b32 v202, v203\n"
      "v_mfma_f32_16x16x32_bf16 v[152:155], a[112:115], v[180:183], v[152:155]\n"
      "global_store_dwordx4 v116, v[144:147], s[54:55] offset:128 sc1\n"
      "v_mfma_f32_16x16x32_bf16 v[204:207], a[80:83], v[176:179], v[204:207]\n"
      "ds_read_b128 v[212:215], v119 offset:30720\n"
      "v_add_f32_e64 v202, v202, v203\n"
      "v_mfma_f32_16x16x32_bf16 v[204:207], a[116:119], v[180:183], v[204:207]\n"
      "v_cvt_pk_f16_f32 v160, v160, v161\n"
      "v_cvt_pk_f16_f32 v161, v162, v163\n"
      "v_mfma_f32_16x16x32_bf16 v[148:151], a[84:87], v[176:179], v[148:151]\n"
      "v_cvt_pk_f16_f32 v162, v184, v185\n"
      "v_mov_b32_e64 v203, v202\n"
      "v_mfma_f32_16x16x32_bf16 v[148:151], a[120:123], v[180:183], v[148:151]\n"
      "s_nop 0\n"
      "v_permlane32_swap_b32 v202, v203\n"
      "v_mfma_f32_16x16x32_bf16 v[236:239], a[88:91], v[176:179], v[236:239]\n"
      "ds_read_b64 v[144:145], v132 offset:24\n"
      "v_cvt_pk_f16_f32 v163, v186, v187\n"
      "v_mfma_f32_16x16x32_bf16 v[236:239], a[124:127], v[180:183], v[236:239]\n"
      "ds_read_u16 v56, v134 offset:32\n"
      "v_add_f32_e64 v202, v202, v203\n"
      "v_mfma_f32_16x16x32_bf16 v[224:227], a[92:95], v[176:179], v[224:227]\n"
      "ds_read_u16 v52, v134\n"
      "v_cvt_pk_f16_f32 v40, v196, v197\n"
      "v_mfma_f32_16x16x32_bf16 v[224:227], a[128:131], v[180:183], v[224:227]\n"
      "ds_read_b128 v[220:223], v132 offset:32\n"
      "v_rsq_f32_e64 v202, v202\n"
      "v_mfma_f32_16x16x32_bf16 v[248:251], v[176:179], a[96:99], v[248:251]\n"
      "ds_read_u16 v60, v134 offset:64\n"
      "v_mov_b32 v203, v202\n"
      "v_mfma_f32_16x16x32_bf16 v[248:251], v[180:183], a[132:135], v[248:251]\n"
      "s_waitcnt lgkmcnt(5)\n"
      "v_cvt_pk_f16_f32 v192, v192, v193\n"
      "v_cvt_pk_f16_f32 v41, v198, v199\n"
      "v_mfma_f32_16x16x32_bf16 v[156:159], v[176:179], a[100:103], v[156:159]\n"
      "global_store_dwordx4 v116, v[212:215], s[54:55] offset:256 sc1\n"
      "v_mfma_f32_16x16x32_bf16 v[156:159], v[180:183], a[136:139], v[156:159]\n"
      "ds_read_b128 v[228:231], v133\n"
      "v_cmp_nge_f32 vcc, 0x43800000, v203\n"
      "v_mfma_f32_16x16x32_bf16 v[232:235], v[176:179], a[104:107], v[232:235]\n"
      "ds_read_b64 v[146:147], v133 offset:16\n"
      "v_cvt_pk_f16_f32 v193, v194, v195\n"
      "v_mfma_f32_16x16x32_bf16 v[232:235], v[180:183], a[140:143], v[232:235]\n"
      "ds_read_b128 v[196:199], v123 offset:21504\n"
      "v_cvt_pk_f16_f32 v194, v188, v189\n"
      "v_mfma_f32_16x16x32_bf16 v[208:211], a[144:147], v[136:139], v[208:211]\n"
      "ds_read_b128 v[212:215], v123 offset:18432\n"
      "v_cvt_pk_f16_f32 v195, v190, v191\n"
      "v_mfma_f32_16x16x32_bf16 v[208:211], a[180:183], v[164:167], v[208:211]\n"
      "ds_read_b128 v[188:191], v123 offset:24576\n"
      "v_cvt_pk_f16_f32 v38, v140, v141\n"
      "v_mfma_f32_16x16x32_bf16 v[204:207], a[152:155], v[136:139], v[204:207]\n"
      "s_or_b64 s[34:35], s[34:35], vcc\n"
      "v_cvt_pk_f16_f32 v39, v142, v143\n"
      "v_cmp_nle_f32 vcc, 0x3a000000, v203\n"
      "v_mfma_f32_16x16x32_bf16 v[204:207], a[188:191], v[164:167], v[204:207]\n"
      "ds_read_b128 v[140:143], v123 offset:27648\n"
      "v_permlane16_swap_b32 v202, v203\n"
      "v_mfma_f32_16x16x32_bf16 v[152:155], a[148:151], v[136:139], v[152:155]\n"
      "v_mul_f32_e64 v202, s36, v202\n"
      "v_cvt_pk_f16_f32 v202, v202, v203\n"
      "v_mfma_f32_16x16x32_bf16 v[152:155], a[184:187], v[164:167], v[152:155]\n"
      "v_pk_mul_f16 v161, v161, v202 op_sel:[0,1] op_sel_hi:[1,1]\n"
      "v_pk_mul_f16 v160, v160, v202 op_sel:[0,1] op_sel_hi:[1,1]\n"
      "v_mfma_f32_16x16x32_bf16 v[148:151], a[156:159], v[136:139], v[148:151]\n"
      "v_pk_mul_f16 v163, v163, v202 op_sel:[0,1] op_sel_hi:[1,1]\n"
      "v_pk_mul_f16 v40, v40, v202 op_sel:[0,1] op_sel_hi:[1,1]\n"
      "v_mfma_f32_16x16x32_bf16 v[148:151], a[192:195], v[164:167], v[148:151]\n"
      "v_pk_mul_f16 v193, v193, v202 op_sel_hi:[1,0]\n"
      "v_pk_mul_f16 v162, v162, v202 op_sel:[0,1] op_sel_hi:[1,1]\n"
      "v_mfma_f32_16x16x32_bf16 v[236:239], a[160:163], v[136:139], v[236:239]\n"
      "s_waitcnt lgkmcnt(2)\n"
      "v_pk_mul_f16 v194, v194, v202 op_sel_hi:[1,0]\n"
      "v_pk_mul_f16 v41, v41, v202 op_sel:[0,1] op_sel_hi:[1,1]\n"
      "v_mfma_f32_16x16x32_bf16 v[236:239], a[196:199], v[164:167], v[236:239]\n"
      "s_nop 0\n"
      "v_pk_mul_f16 v192, v192, v202 op_sel_hi:[1,0]\n"
      "v_pk_mul_f16 v195, v195, v202 op_sel_hi:[1,0]\n"
      "v_mfma_f32_16x16x32_bf16 v[224:227], a[164:167], v[136:139], v[224:227]\n"
      "v_pk_mul_f16 v39, v39, v202 op_sel_hi:[1,0]\n"
      "v_pk_mul_f16 v38, v38, v202 op_sel_hi:[1,0]\n"
      "v_mfma_f32_16x16x32_bf16 v[224:227], a[200:203], v[164:167], v[224:227]\n"
      "v_fma_f32 v202, v208, v208, s38\n"
      "v_add_u32_e64 v135, 32, v135\n"
      "v_mfma_f32_16x16x32_bf16 v[248:251], v[136:139], a[168:171], v[248:251]\n"
      "s_or_b64 s[34:35], s[34:35], vcc\n"
      "v_fma_f32 v203, v148, v148, s38\n"
      "v_fmac_f32 v203, v149, v149\n"
      "v_mfma_f32_16x16x32_bf16 v[248:251], v[164:167], a[204:207], v[248:251]\n"
      "v_fmac_f32 v202, v209, v209\n"
      "v_fmac_f32 v202, v210, v210\n"
      "v_mfma_f32_16x16x32_bf16 v[232:235], v[136:139], a[176:179], v[232:235]\n"
      "v_fmac_f32 v203, v150, v150\n"
      "v_fmac_f32 v202, v211, v211\n"
      "v_mfma_f32_16x16x32_bf16 v[232:235], v[164:167], a[212:215], v[232:235]\n"
      "v_fmac_f32 v203, v151, v151\n"
      "v_fmac_f32 v202, v152, v152\n"
      "v_mfma_f32_16x16x32_bf16 v[156:159], v[136:139], a[172:175], v[156:159]\n"
      "v_fmac_f32 v202, v153, v153\n"
      "v_fmac_f32 v203, v236, v236\n"
      "v_mfma_f32_16x16x32_bf16 v[156:159], v[164:167], a[208:211], v[156:159]\n"
      "s_waitcnt lgkmcnt(0)\n"
      "v_fmac_f32 v203, v237, v237\n"
      "v_fmac_f32 v202, v154, v154\n"
      "v_mfma_f32_16x16x32_bf16 v[176:179], a[216:219], v[212:215], 0\n"
      "v_fmac_f32 v202, v155, v155\n"
      "v_fmac_f32_e64 v203, v238, v238\n"
      "v_mfma_f32_16x16x32_bf16 v[176:179], a[228:231], v[196:199], v[176:179]\n"
      "v_fmac_f32 v203, v239, v239\n"
      "v_fmac_f32 v203, v224, v224\n"
      "v_mfma_f32_16x16x32_bf16 v[180:183], a[220:223], v[212:215], 0\n"
      "v_fmac_f32 v202, v204, v204\n"
      "v_fmac_f32 v202, v205, v205\n"
      "v_mfma_f32_16x16x32_bf16 v[180:183], a[232:235], v[196:199], v[180:183]\n"
      "ds_read_b128 v[136:139], v123 offset:33792\n"
      "v_fmac_f32 v202, v206, v206\n"
      "v_fmac_f32 v203, v225, v225\n"
      "v_mfma_f32_16x16x32_bf16 v[184:187], a[224:227], v[212:215], 0\n"
      "v_fmac_f32 v203, v226, v226\n"
      "v_fmac_f32 v203, v227, v227\n"
      "v_mfma_f32_16x16x32_bf16 v[184:187], a[236:239], v[196:199], v[184:187]\n"
      "ds_read_b128 v[196:199], v123 offset:30720\n"
      "v_cvt_pk_f16_f32 v148, v148, v149\n"
      "v_mfma_f32_16x16x32_bf16 v[176:179], a[240:243], v[188:191], v[176:179]\n"
      "v_cvt_pk_f16_f32 v149, v150, v151\n"
      "v_fmac_f32 v202, v207, v207\n"
      "v_mfma_f32_16x16x32_bf16 v[176:179], a[252:255], v[140:143], v[176:179]\n"
      "s_nop 0\n"
      "v_permlane16_swap_b32 v202, v203\n"
      "v_mfma_f32_16x16x32_bf16 v[180:183], a[244:247], v[188:191], v[180:183]\n"
      "v_add_f32 v202, v202, v203\n"
      "v_cvt_pk_f16_f32 v150, v236, v237\n"
      "v_mfma_f32_16x16x32_bf16 v[180:183], v[0:3], v[140:143], v[180:183]\n"
      "v_mov_b32 v203, v202\n"
      "v_cvt_pk_f16_f32 v46, v224, v225\n"
      "v_mfma_f32_16x16x32_bf16 v[184:187], a[248:251], v[188:191], v[184:187]\n"
      "v_permlane32_swap_b32 v202, v203\n"
      "v_mfma_f32_16x16x32_bf16 v[184:187], v[4:7], v[140:143], v[184:187]\n"
      "v_cvt_pk_f16_f32 v151, v238, v239\n"
      "v_add_f32 v202, v202, v203\n"
      "v_mfma_f32_16x16x32_f16 v[140:143], v[168:171], v[172:175], v[78:81]\n"
      "v_rsq_f32 v202, v202\n"
      "v_mfma_f32_16x16x32_f16 v[140:143], v[34:37], v[32:35], v[140:143]\n"
      "v_cvt_pk_f16_f32 v47, v226, v227\n"
      "v_mov_b32_e64 v203, v202\n"
      "v_mfma_f32_16x16x32_f16 v[188:191], v[160:163], v[172:175], v[82:85]\n"
      "v_cvt_pk_f16_f32 v208, v208, v209\n"
      "v_cmp_nge_f32 vcc, 0x43800000, v203\n"
      "v_mfma_f32_16x16x32_f16 v[188:191], v[40:43], v[32:35], v[188:191]\n"
      "s_waitcnt lgkmcnt(0)\n"
      "v_cvt_pk_f16_f32 v209, v210, v211\n"
      "v_cvt_pk_f16_f32 v210, v152, v153\n"
      "v_mfma_f32_16x16x32_bf16 v[176:179], v[8:11], v[196:199], v[176:179]\n"
      "s_or_b64 s[34:35], s[34:35], vcc\n"
      "v_cvt_pk_f16_f32 v211, v154, v155\n"
      "v_cvt_pk_f16_f32 v44, v204, v205\n"
      "v_mfma_f32_16x16x32_bf16 v[176:179], v[20:23], v[136:139], v[176:179]\n"
      "v_cvt_pk_f16_f32 v45, v206, v207\n"
      "v_cmp_nle_f32 vcc, 0x3a000000, v203\n"
      "v_mfma_f32_16x16x32_bf16 v[180:183], v[12:15], v[196:199], v[180:183]\n"
      "v_permlane16_swap_b32 v202, v203\n"
      "v_mfma_f32_16x16x32_bf16 v[180:183], v[24:27], v[136:139], v[180:183]\n"
      "s_or_b64 s[34:35], s[34:35], vcc\n"
      "v_mul_f32 v202, s36, v202\n"
      "v_cvt_pk_f16_f32 v202, v202, v203\n"
      "v_mfma_f32_16x16x32_bf16 v[184:187], v[16:19], v[196:199], v[184:187]\n"
      "v_pk_mul_f16 v149, v149, v202 op_sel:[0,1] op_sel_hi:[1,1]\n"
      "v_pk_mul_f16 v148, v148, v202 op_sel:[0,1] op_sel_hi:[1,1]\n"
      "v_mfma_f32_16x16x32_bf16 v[184:187], v[28:31], v[136:139], v[184:187]\n"
      "s_or_b64 vcc, s[48:49], s[50:51]\n"
      "v_pk_mul_f16 v151, v151, v202 op_sel:[0,1] op_sel_hi:[1,1]\n"
      "v_pk_mul_f16 v209, v209, v202 op_sel_hi:[1,0]\n"
      "v_mfma_f32_16x16x32_f16 v[204:207], v[220:223], v[172:175], v[70:73]\n"
      "s_or_b64 vcc, vcc, s[34:35]\n"
      "v_pk_mul_f16 v150, v150, v202 op_sel:[0,1] op_sel_hi:[1,1]\n"
      "v_pk_mul_f16 v46, v46, v202 op_sel:[0,1] op_sel_hi:[1,1]\n"
      "v_mfma_f32_16x16x32_f16 v[204:207], v[228:231], v[168:171], v[204:207]\n"
      "s_waitcnt vmcnt(3)\n"
      "ds_read_b128 v[196:199], v119 offset:41728\n"
      "v_mfma_f32_16x16x32_f16 v[136:139], v[148:151], v[172:175], v[86:89]\n"
      "ds_read_b32 v203, v122 offset:4864\n"
      "v_pk_mul_f16 v44, v44, v202 op_sel_hi:[1,0]\n"
      "v_cvt_pk_bf16_f32 v51, v250, v251\n"
      "v_pk_mul_f16 v211, v211, v202 op_sel_hi:[1,0]\n"
      "v_cvt_pk_bf16_f32 v55, v158, v159\n"
      "v_cvt_pk_bf16_f32 v50, v248, v249\n"
      "v_fma_f32 v158, v176, v176, s39\n"
      "v_cvt_pk_bf16_f32 v59, v234, v235\n"
      "v_fmac_f32 v158, v177, v177\n"
      "v_fmac_f32 v158, v178, v178\n"
      "v_pk_mul_f16 v47, v47, v202 op_sel:[0,1] op_sel_hi:[1,1]\n"
      "v_fmac_f32_e64 v158, v179, v179\n"
      "v_pk_mul_f16 v45, v45, v202 op_sel_hi:[1,0]\n"
      "v_fmac_f32 v158, v180, v180\n"
      "v_mfma_f32_16x16x32_f16 v[136:139], v[46:49], v[32:35], v[136:139]\n"
      "v_fmac_f32 v158, v181, v181\n"
      "v_pk_mul_f16 v208, v208, v202 op_sel_hi:[1,0]\n"
      "v_fmac_f32 v158, v182, v182\n"
      "v_pk_mul_f16 v210, v210, v202 op_sel_hi:[1,0]\n"
      "v_cvt_pk_bf16_f32 v54, v156, v157\n"
      "v_fmac_f32 v158, v183, v183\n"
      "v_fmac_f32 v158, v184, v184\n"
      "v_fmac_f32 v158, v185, v185\n"
      "v_fmac_f32 v158, v186, v186\n"
      "v_cvt_pk_bf16_f32 v58, v232, v233\n"
      "v_fmac_f32_e64 v158, v187, v187\n"
      "v_mfma_f32_16x16x32_f16 v[204:207], v[144:147], v[32:35], v[204:207]\n"
      "s_waitcnt lgkmcnt(1)\n"
      "v_or3_b32 v196, v196, v197, v198\n"
      "v_and_b32 v196, s40, v196\n"
      "s_waitcnt lgkmcnt(0)\n"
      "v_bitop3_b32 v203, v203, v199, s40 bitop3:0xa8\n"
      "v_or3_b32 v196, v196, v203, s4\n"
      "ds_write_b32 v129, v196 offset:3072\n"
      "s_cbranch_scc1 L_gen_28_%=\n"
      "L_fast_31_%=:\n"
      "v_mfma_f32_16x16x32_f16 v[200:203], v[168:171], v[192:195], v[90:93]\n"
      "ds_read_b128 v[172:175], v123 offset:22528\n"
      "v_exp_f32 v141, v141\n"
      "v_mfma_f32_16x16x32_f16 v[164:167], v[220:223], v[192:195], v[72:75]\n"
      "ds_read_b128 v[152:155], v123 offset:19456\n"
      "v_mfma_f32_16x16x32_f16 v[164:167], v[228:231], v[160:163], v[164:167]\n"
      "ds_read_b128 v[196:199], v120 offset:18432\n"
      "v_exp_f32 v188, v188\n"
      "v_mfma_f32_16x16x32_f16 v[164:167], v[144:147], v[38:41], v[164:167]\n"
      "v_exp_f32 v140, v140\n"
      "v_mfma_f32_16x16x32_f16 v[248:251], v[160:163], v[192:195], v[94:97]\n"
      "ds_read_b128 v[232:235], v128 offset:3072\n"
      "v_cvt_pk_bf16_f32 v140, v140, v141\n"
      "v_mfma_f32_16x16x32_f16 v[248:251], v[40:43], v[38:41], v[248:251]\n"
      "v_exp_f32 v142, v142\n"
      "v_exp_f32 v191, v191\n"
      "v_exp_f32 v143, v143\n"
      "v_exp_f32 v189, v189\n"
      "v_exp_f32_e64 v190, v190\n"
      "v_cvt_pk_bf16_f32 v141, v142, v143\n"
      "v_mfma_f32_16x16x32_f16 v[200:203], v[34:37], v[38:41], v[200:203]\n"
      "ds_read_b128 v[224:227], v123 offset:25600\n"
      "v_cvt_pk_bf16_f32 v142, v188, v189\n"
      "v_mfma_f32_16x16x32_f16 v[236:239], v[148:151], v[192:195], v[98:101]\n"
      "s_waitcnt lgkmcnt(3)\n"
      "v_exp_f32 v136, v136\n"
      "v_mfma_f32_16x16x32_f16 v[236:239], v[46:49], v[38:41], v[236:239]\n"
      "ds_read_b128 v[212:215], v123 offset:28672\n"
      "v_cvt_pk_bf16_f32 v143, v190, v191\n"
      "v_exp_f32 v137, v137\n"
      "v_exp_f32 v204, v204\n"
      "v_cvt_pk_bf16_f32 v62, v136, v137\n"
      "v_mfma_f32_16x16x32_bf16 v[188:191], a[216:219], v[152:155], 0\n"
      "s_waitcnt lgkmcnt(2)\n"
      "v_exp_f32 v138, v138\n"
      "v_mfma_f32_16x16x32_bf16 v[188:191], a[228:231], v[172:175], v[188:191]\n"
      "v_exp_f32_e64 v139, v139\n"
      "v_bitop3_b32 v232, v232, v233, s4 bitop3:0x7e\n"
      "v_mfma_f32_16x16x32_bf16 v[192:195], a[220:223], v[152:155], 0\n"
      "v_bitop3_b32 v234, v234, v235, s4 bitop3:0x7e\n"
      "v_cvt_pk_bf16_f32 v64, v204, 0\n"
      "v_mfma_f32_16x16x32_bf16 v[192:195], a[232:235], v[172:175], v[192:195]\n"
      "global_store_dwordx4 v117, v[196:199], s[54:55] sc1\n"
      "v_or_b32 v232, v232, v234\n"
      "v_cmp_ne_u32 vcc, 0, v232\n"
      "v_cvt_pk_bf16_f32 v63, v138, v139\n"
      "v_mfma_f32_16x16x32_bf16 v[196:199], a[224:227], v[152:155], 0\n"
      "s_mov_b64 s[48:49], vcc\n"
      "v_exp_f32 v200, v200\n"
      "v_mfma_f32_16x16x32_bf16 v[196:199], a[236:239], v[172:175], v[196:199]\n"
      "s_waitcnt lgkmcnt(0)\n"
      "v_exp_f32 v201, v201\n"
      "v_mfma_f32_16x16x32_bf16 v[188:191], a[240:243], v[224:227], v[188:191]\n"
      "ds_read_b128 v[136:139], v123 offset:31744\n"
      "v_cvt_pk_bf16_f32 v200, v200, v201\n"
      "v_mfma_f32_16x16x32_bf16 v[188:191], a[252:255], v[212:215], v[188:191]\n"
      "ds_read_b128 v[172:175], v123 offset:34816\n"
      "v_exp_f32 v202, v202\n"
      "v_mfma_f32_16x16x32_bf16 v[192:195], a[244:247], v[224:227], v[192:195]\n"
      "s_cbranch_vccnz L_flg_32_%=\n"
      "L_fb_33_%=:\n"
      "v_mfma_f32_16x16x32_bf16 v[192:195], v[0:3], v[212:215], v[192:195]\n"
      "ds_read_b128 v[152:155], v121 offset:18432\n"
      "v_exp_f32 v250, v250\n"
      "v_exp_f32 v203, v203\n"
      "v_exp_f32 v236, v236\n"
      "v_mfma_f32_16x16x32_bf16 v[196:199], a[248:251], v[224:227], v[196:199]\n"
      "v_exp_f32 v248, v248\n"
      "v_mfma_f32_16x16x32_bf16 v[196:199], v[4:7], v[212:215], v[196:199]\n"
      "ds_read_b128 v[212:215], v123 offset:20480\n"
      "v_cvt_pk_bf16_f32 v201, v202, v203\n"
      "v_exp_f32 v251, v251\n"
      "v_exp_f32 v237, v237\n"
      "v_exp_f32 v249, v249\n"
      "v_cvt_pk_bf16_f32 v203, v250, v251\n"
      "v_cvt_pk_bf16_f32 v202, v248, v249\n"
      "s_waitcnt lgkmcnt(2)\n"
      "v_mfma_f32_16x16x32_bf16 v[188:191], v[8:11], v[136:139], v[188:191]\n"
      "ds_read_b128 v[248:251], v123 offset:23552\n"
      "v_mfma_f32_16x16x32_bf16 v[188:191], v[20:23], v[172:175], v[188:191]\n"
      "v_exp_f32 v238, v238\n"
      "v_mfma_f32_16x16x32_bf16 v[192:195], v[12:15], v[136:139], v[192:195]\n"
      "s_waitcnt lgkmcnt(2)\n"
      "v_exp_f32_e64 v239, v239\n"
      "v_mfma_f32_16x16x32_bf16 v[192:195], v[24:27], v[172:175], v[192:195]\n"
      "s_mov_b32 exec_hi, 0\n"
      "global_store_dwordx4 v118, v[152:155], s[54:55] sc1\n"
      "s_mov_b32 exec_hi, -1\n"
      "v_mfma_f32_16x16x32_bf16 v[196:199], v[16:19], v[136:139], v[196:199]\n"
      "v_fma_f32 v159, v188, v188, s39\n"
      "v_fmac_f32 v159, v189, v189\n"
      "v_mfma_f32_16x16x32_bf16 v[196:199], v[28:31], v[172:175], v[196:199]\n"
      "v_exp_f32 v164, v164\n"
      "v_mfma_f32_16x16x32_bf16 v[136:139], v[66:69], v[140:143], 0\n"
      "v_fmac_f32 v159, v190, v190\n"
      "v_fmac_f32 v159, v191, v191\n"
      "v_mfma_f32_16x16x32_bf16 v[136:139], v[66:69], v[62:65], v[136:139]\n"
      "v_fmac_f32 v159, v192, v192\n"
      "v_fmac_f32 v159, v193, v193\n"
      "v_mfma_f32_16x16x32_bf16 v[172:175], v[216:219], v[140:143], 0\n"
      "v_fmac_f32 v159, v194, v194\n"
      "v_fmac_f32 v159, v195, v195\n"
      "v_mfma_f32_16x16x32_bf16 v[172:175], v[50:53], v[62:65], v[172:175]\n"
      "v_fmac_f32 v159, v196, v196\n"
      "v_fmac_f32 v159, v197, v197\n"
      "v_mfma_f32_16x16x32_bf16 v[152:155], v[244:247], v[140:143], 0\n"
      "v_fmac_f32 v159, v198, v198\n"
      "v_fmac_f32 v159, v199, v199\n"
      "v_mfma_f32_16x16x32_bf16 v[152:155], v[54:57], v[62:65], v[152:155]\n"
      "v_rcp_f32_e64 v136, v136\n"
      "v_mfma_f32_16x16x32_bf16 v[224:227], v[240:243], v[140:143], 0\n"
      "v_mul_f32 v172, v136, v172\n"
      "v_mul_f32 v173, v136, v173\n"
      "v_mfma_f32_16x16x32_bf16 v[224:227], v[58:61], v[62:65], v[224:227]\n"
      "v_cvt_pk_bf16_f32 v62, v236, v237\n"
      "v_cvt_pk_bf16_f32 v63, v238, v239\n"
      "v_mfma_f32_16x16x32_f16 v[236:239], v[168:171], v[208:211], v[102:105]\n"
      "v_cvt_pk_bf16_f32 v172, v172, v173\n"
      "v_cvt_pk_bf16_f32 v64, v164, 0\n"
      "v_mfma_f32_16x16x32_f16 v[236:239], v[34:37], v[44:47], v[236:239]\n"
      "v_mul_f32 v175, v136, v175\n"
      "v_mul_f32 v174, v136, v174\n"
      "v_mfma_f32_16x16x32_f16 v[140:143], v[160:163], v[208:211], v[106:109]\n"
      "v_cvt_pk_bf16_f32 v173, v174, v175\n"
      "v_mul_f32 v152, v136, v152\n"
      "v_mfma_f32_16x16x32_f16 v[140:143], v[40:43], v[44:47], v[140:143]\n"
      "s_waitcnt lgkmcnt(0)\n"
      "v_mul_f32 v153, v136, v153\n"
      "v_mul_f32 v154, v136, v154\n"
      "v_mfma_f32_16x16x32_f16 v[160:163], v[220:223], v[208:211], v[74:77]\n"
      "v_cvt_pk_bf16_f32 v174, v152, v153\n"
      "v_mul_f32_e64 v155, v136, v155\n"
      "v_mfma_f32_16x16x32_f16 v[160:163], v[228:231], v[148:151], v[160:163]\n"
      "ds_read_b128 v[168:171], v123 offset:26624\n"
      "v_cvt_pk_bf16_f32 v175, v154, v155\n"
      "v_mfma_f32_16x16x32_f16 v[160:163], v[144:147], v[44:47], v[160:163]\n"
      "ds_write_b128 v124, v[172:175]\n"
      "v_mfma_f32_16x16x32_f16 v[172:175], v[148:151], v[208:211], v[110:113]\n"
      "ds_read_b128 v[148:151], v123 offset:29696\n"
      "v_mul_f32_e64 v225, v136, v225\n"
      "v_mfma_f32_16x16x32_f16 v[172:175], v[46:49], v[44:47], v[172:175]\n"
      "v_mul_f32_e64 v226, v136, v226\n"
      "v_mul_f32_e64 v224, v136, v224\n"
      "v_mfma_f32_16x16x32_f16 v[208:211], v[228:231], v[220:223], v[76:79]\n"
      "v_cvt_pk_bf16_f32 v224, v224, v225\n"
      "v_mul_f32_e64 v227, v136, v227\n"
      "v_mfma_f32_16x16x32_bf16 v[152:155], v[66:69], v[200:203], 0\n"
      "v_cvt_pk_bf16_f32 v225, v226, v227\n"
      "v_cndmask_b32 v205, v205, v165, s[42:43]\n"
      "v_mfma_f32_16x16x32_bf16 v[152:155], v[66:69], v[62:65], v[152:155]\n"
      "s_add_u32 s28, s27, 2\n"
      "v_exp_f32 v237, v237\n"
      "v_mfma_f32_16x16x16_f16 v[208:211], v[146:147], v[144:145], v[208:211]\n"
      "v_exp_f32 v236, v236\n"
      "v_mfma_f32_16x16x32_bf16 v[164:167], v[216:219], v[200:203], 0\n"
      "s_min_u32 m0, s28, s3\n"
      "v_exp_f32 v238, v238\n"
      "v_mfma_f32_16x16x32_bf16 v[164:167], v[50:53], v[62:65], v[164:167]\n"
      "s_movrels_b32 s9, s80\n"
      "v_cndmask_b32 v205, v205, v161, s[44:45]\n"
      "v_cvt_pk_bf16_f32 v236, v236, v237\n"
      "v_cndmask_b32 v205, v205, v209, s[46:47]\n"
      "v_mfma_f32_16x16x32_bf16 v[144:147], v[244:247], v[200:203], 0\n"
      "s_waitcnt lgkmcnt(0)\n"
      "v_exp_f32 v205, v205\n"
      "v_mfma_f32_16x16x32_bf16 v[144:147], v[54:57], v[62:65], v[144:147]\n"
      "s_lshr_b32 s8, s9, 8\n"
      "v_cvt_pk_bf16_f32 v205, v205, v205\n"
      "v_exp_f32 v239, v239\n"
      "v_mfma_f32_16x16x32_bf16 v[154:157], v[240:243], v[200:203], 0\n"
      "s_and_b32 s9, s9, 0xff\n"
      "v_exp_f32_e64 v141, v141\n"
      "v_mfma_f32_16x16x32_bf16 v[154:157], v[58:61], v[62:65], v[154:157]\n"
      "ds_write_b16 v130, v205\n"
      "v_mfma_f32_16x16x32_bf16 v[204:207], a[220:223], v[212:215], 0\n"
      "ds_read_b128 v[220:223], v131\n"
      "v_cvt_pk_bf16_f32 v237, v238, v239\n"
      "v_mfma_f32_16x16x32_bf16 v[204:207], a[232:235], v[248:251], v[204:207]\n"
      "ds_read_b128 v[228:231], v131 offset:64\n"
      "v_exp_f32 v140, v140\n"
      "v_mfma_f32_16x16x32_bf16 v[200:203], a[216:219], v[212:215], 0\n"
      "s_mul_i32 s10, s8, 73\n"
      "v_exp_f32 v142, v142\n"
      "v_mfma_f32_16x16x32_bf16 v[200:203], a[228:231], v[248:251], v[200:203]\n"
      "s_add_u32 s10, s10, s9\n"
      "v_exp_f32 v143, v143\n"
      "v_mfma_f32_16x16x32_bf16 v[208:211], a[224:227], v[212:215], 0\n"
      "ds_read_b128 v[212:215], v123 offset:32768\n"
      "v_cvt_pk_bf16_f32 v238, v140, v141\n"
      "v_mfma_f32_16x16x32_bf16 v[208:211], a[236:239], v[248:251], v[208:211]\n"
      "ds_read_b128 v[248:251], v123 offset:35840\n"
      "v_cvt_pk_bf16_f32 v239, v142, v143\n"
      "v_exp_f32_e64 v172, v172\n"
      "v_mfma_f32_16x16x32_bf16 v[200:203], a[240:243], v[168:171], v[200:203]\n"
      "s_mul_i32 s28, s10, 4802\n"
      "v_exp_f32 v173, v173\n"
      "v_mfma_f32_16x16x32_bf16 v[200:203], a[252:255], v[148:151], v[200:203]\n"
      "s_add_u32 s56, s18, s28\n"
      "v_exp_f32 v174, v174\n"
      "v_mfma_f32_16x16x32_bf16 v[204:207], a[244:247], v[168:171], v[204:207]\n"
      "ds_read_b128 v[136:139], v114 offset:18432\n"
      "s_waitcnt lgkmcnt(3)\n"
      "v_mfma_f32_16x16x32_bf16 v[204:207], v[0:3], v[148:151], v[204:207]\n"
      "s_addc_u32 s57, s19, 0\n"
      "v_exp_f32 v175, v175\n"
      "v_mfma_f32_16x16x32_bf16 v[208:211], a[248:251], v[168:171], v[208:211]\n"
      "ds_read_b128 v[140:143], v115 offset:18432\n"
      "v_cvt_pk_bf16_f32 v62, v172, v173\n"
      "v_mfma_f32_16x16x32_bf16 v[208:211], v[4:7], v[148:151], v[208:211]\n"
      "s_and_b32 s56, s56, -16\n"
      "v_exp_f32 v160, v160\n"
      "v_mfma_f32_16x16x32_bf16 v[232:235], v[220:223], v[66:69], 0\n"
      "s_sub_u32 s58, s56, 110592\n"
      "v_cvt_pk_bf16_f32 v64, v160, 0\n"
      "v_cvt_pk_bf16_f32 v63, v174, v175\n"
      "v_mfma_f32_16x16x32_bf16 v[232:235], v[228:231], v[66:69], v[232:235]\n"
      "s_waitcnt lgkmcnt(2)\n"
      "v_rcp_f32 v152, v152\n"
      "v_mfma_f32_16x16x32_bf16 v[200:203], v[8:11], v[212:215], v[200:203]\n"
      "s_subb_u32 s59, s57, 0\n"
      "v_mul_f32 v165, v152, v165\n"
      "v_mul_f32_e64 v164, v152, v164\n"
      "v_mfma_f32_16x16x32_bf16 v[200:203], v[20:23], v[248:251], v[200:203]\n"
      "s_sub_u32 s56, s56, 73728\n"
      "v_cvt_pk_bf16_f32 v164, v164, v165\n"
      "v_mul_f32 v167, v152, v167\n"
      "v_mfma_f32_16x16x32_bf16 v[204:207], v[12:15], v[212:215], v[204:207]\n"
      "s_subb_u32 s57, s57, 0\n"
      "v_mul_f32 v166, v152, v166\n"
      "v_cvt_pk_bf16_f32 v165, v166, v167\n"
      "v_mfma_f32_16x16x32_bf16 v[204:207], v[24:27], v[248:251], v[204:207]\n"
      "s_mul_i32 s28, s8, 1373568\n"
      "v_mul_f32 v144, v152, v144\n"
      "v_mul_f32 v145, v152, v145\n"
      "v_mfma_f32_16x16x32_bf16 v[208:211], v[16:19], v[212:215], v[208:211]\n"
      "s_mul_i32 s29, s9, 2688\n"
      "s_waitcnt lgkmcnt(0)\n"
      "v_mul_f32_e64 v147, v152, v147\n"
      "v_mfma_f32_16x16x32_bf16 v[208:211], v[28:31], v[248:251], v[208:211]\n"
      "v_mul_f32 v146, v152, v146\n"
      "v_cvt_pk_bf16_f32 v166, v144, v145\n"
      "v_mfma_f32_16x16x32_bf16 v[160:163], v[66:69], v[236:239], 0\n"
      "s_add_u32 s28, s28, s29\n"
      "v_mul_f32_e64 v155, v152, v155\n"
      "v_cvt_pk_bf16_f32 v167, v146, v147\n"
      "v_mfma_f32_16x16x32_bf16 v[160:163], v[66:69], v[62:65], v[160:163]\n"
      "ds_write_b128 v124, v[164:167] offset:1024\n"
      "v_mfma_f32_16x16x32_bf16 v[164:167], v[216:219], v[236:239], 0\n"
      "s_cmp_eq_u32 s8, 72\n"
      "v_mul_f32 v156, v152, v156\n"
      "v_mul_f32 v154, v152, v154\n"
      "v_mfma_f32_16x16x32_bf16 v[164:167], v[50:53], v[62:65], v[164:167]\n"
      "s_cselect_b32 s8, 0, 100270464\n"
      "v_cvt_pk_bf16_f32 v154, v154, v155\n"
      "v_mul_f32_e64 v157, v152, v157\n"
      "v_mfma_f32_16x16x32_bf16 v[172:175], v[244:247], v[236:239], 0\n"
      "s_cmp_eq_u32 s9, 72\n"
      "v_cvt_pk_bf16_f32 v155, v156, v157\n"
      "v_fma_f32 v152, v200, v200, s39\n"
      "v_mfma_f32_16x16x32_bf16 v[172:175], v[54:57], v[62:65], v[172:175]\n"
      "ds_write2st64_b64 v125, v[224:225], v[154:155] offset0:0 offset1:2\n"
      "v_mfma_f32_16x16x32_bf16 v[168:171], v[240:243], v[236:239], 0\n"
      "s_cselect_b32 s9, 0, 196224\n"
      "v_fmac_f32 v152, v201, v201\n"
      "v_fmac_f32 v152, v202, v202\n"
      "v_mfma_f32_16x16x32_bf16 v[168:171], v[58:61], v[62:65], v[168:171]\n"
      "s_add_u32 s10, s8, s9\n"
      "v_fmac_f32 v152, v203, v203\n"
      "v_fmac_f32 v152, v204, v204\n"
      "v_mfma_f32_16x16x32_bf16 v[236:239], v[220:223], v[216:219], 0\n"
      "s_add_u32 s28, s28, s10\n"
      "v_fmac_f32 v152, v205, v205\n"
      "v_fmac_f32 v152, v206, v206\n"
      "v_mfma_f32_16x16x32_bf16 v[236:239], v[228:231], v[50:53], v[236:239]\n"
      "s_add_u32 s52, s16, s28\n"
      "v_fmac_f32 v152, v207, v207\n"
      "v_fmac_f32_e64 v152, v208, v208\n"
      "v_mfma_f32_16x16x32_bf16 v[154:157], v[220:223], v[244:247], 0\n"
      "s_addc_u32 s53, s17, 0\n"
      "v_fmac_f32 v152, v209, v209\n"
      "v_fmac_f32 v152, v210, v210\n"
      "v_mfma_f32_16x16x32_bf16 v[154:157], v[228:231], v[54:57], v[154:157]\n"
      "v_fmac_f32 v152, v211, v211\n"
      "v_permlane16_swap_b32 v158, v159\n"
      "v_mfma_f32_16x16x32_bf16 v[244:247], v[220:223], v[240:243], 0\n"
      "s_add_u32 s27, s27, 1\n"
      "v_mov_b32 v153, v152\n"
      "v_add_f32 v158, v158, v159\n"
      "v_mfma_f32_16x16x32_bf16 v[244:247], v[228:231], v[58:61], v[244:247]\n"
      "s_cmp_ge_u32 s27, s26\n"
      "v_permlane16_swap_b32 v152, v153\n"
      "v_mfma_f32_16x16x32_bf16 v[144:147], a[0:3], v[136:139], 0\n"
      "v_add_f32_e64 v152, v152, v153\n"
      "v_mov_b32_dpp v232, v232 quad_perm:[0,0,0,0] row_mask:0xf bank_mask:0xf\n"
      "v_mfma_f32_16x16x32_bf16 v[144:147], a[36:39], v[140:143], v[144:147]\n"
      "v_permlane32_swap_b32 v158, v152\n"
      "v_mfma_f32_16x16x32_bf16 v[148:151], a[4:7], v[136:139], 0\n"
      "s_cselect_b32 s28, 1, 0\n"
      "v_cndmask_b32 v236, v236, v154, s[42:43]\n"
      "v_add_f32_e64 v158, v158, v152\n"
      "v_mfma_f32_16x16x32_bf16 v[148:151], a[40:43], v[140:143], v[148:151]\n"
      "ds_write_b32 v129, v158\n"
      "v_mfma_f32_16x16x32_bf16 v[152:155], a[8:11], v[136:139], 0\n"
      "v_cndmask_b32 v236, v236, v244, s[44:45]\n"
      "s_xor_b32 s29, s10, s11\n"
      "v_mfma_f32_16x16x32_bf16 v[152:155], a[44:47], v[140:143], v[152:155]\n"
      "s_or_b32 s28, s28, s29\n"
      "v_rcp_f32_e64 v232, v232\n"
      "v_mfma_f32_16x16x32_bf16 v[156:159], a[12:15], v[136:139], 0\n"
      "v_mul_f32_e64 v236, v232, v236\n"
      "v_cvt_pk_bf16_f32 v236, v236, v236\n"
      "v_mfma_f32_16x16x32_bf16 v[156:159], a[48:51], v[140:143], v[156:159]\n"
      "ds_write_b16 v135, v236\n"
      "L_join_30_%=:\n"
      "s_cbranch_scc0 L_loop_%=\n"
      "L_tail0_%=:\n"
      "s_cmp_eq_u32 s10, s11\n"
      "s_cbranch_scc1 L_sk_35_%=\n"
      "s_mov_b32 s11, s10\n"
      "s_sub_u32 s28, s60, s8\n"
      "s_sub_u32 s29, s61, s9\n"
      "s_mov_b32 s60, s8\n"
      "s_mov_b32 s61, s9\n"
      "s_mov_b64 exec, s[64:65]\n"
      "v_add_u32 v116, s28, v116\n"
      "s_mov_b64 exec, s[70:71]\n"
      "v_add_u32 v116, s29, v116\n"
      "s_mov_b64 exec, s[66:67]\n"
      "v_add_u32 v117, s28, v117\n"
      "s_mov_b64 exec, s[72:73]\n"
      "v_add_u32 v117, s29, v117\n"
      "s_mov_b64 exec, s[68:69]\n"
      "v_add_u32 v118, s28, v118\n"
      "s_mov_b64 exec, s[74:75]\n"
      "v_add_u32_e64 v118, s29, v118\n"
      "s_mov_b64 exec, -1\n"
      "s_nop 4\n"
      "L_sk_35_%=:\n"
      "s_cmp_lt_u32 s27, s26\n"
      "s_cbranch_scc1 L_loop_%=\n"
      "s_nop 7\n"
      "v_mbcnt_lo_u32_b32 v161, -1, 0\n"
      "v_mbcnt_hi_u32_b32 v161, -1, v161\n"
      "v_lshrrev_b32_e64 v162, 4, v161\n"
      "v_mul_u32_u24 v162, 352, v162\n"
      "v_rcp_f32_e64 v160, v160\n"
      "v_and_b32_e64 v163, 15, v161\n"
      "v_pk_mul_f32 v[164:165], v[164:165], v[160:161] op_sel_hi:[1,0]\n"
      "v_pk_mul_f32 v[172:173], v[172:173], v[160:161] op_sel_hi:[1,0]\n"
      "v_pk_mul_f32 v[174:175], v[174:175], v[160:161] op_sel_hi:[1,0]\n"
      "v_pk_mul_f32 v[166:167], v[166:167], v[160:161] op_sel_hi:[1,0]\n"
      "v_pk_mul_f32 v[168:169], v[168:169], v[160:161] op_sel_hi:[1,0]\n"
      "v_pk_mul_f32 v[170:171], v[170:171], v[160:161] op_sel_hi:[1,0]\n"
      "v_lshl_add_u32 v162, v163, 4, v162\n"
      "v_add_u32 v162, 150640, v162\n"
      "v_cvt_pk_bf16_f32 v164, v164, v165\n"
      "v_cvt_pk_bf16_f32 v165, v166, v167\n"
      "v_cvt_pk_bf16_f32 v167, v174, v175\n"
      "v_cvt_pk_bf16_f32 v166, v172, v173\n"
      "v_cvt_pk_bf16_f32 v168, v168, v169\n"
      "v_cvt_pk_bf16_f32 v169, v170, v171\n"
      "ds_write_b128 v124, v[164:167] offset:2048\n"
      "ds_write_b64 v125, v[168:169] offset:2048\n"
      "s_waitcnt lgkmcnt(0)\n"
      "s_barrier\n"
      "ds_read_b128 v[164:167], v128\n"
      "ds_read_b128 v[136:139], v123\n"
      "ds_read_b128 v[140:143], v123 offset:3072\n"
      "s_sub_u32 s28, s27, 3\n"
      "s_cselect_b64 s[54:55], s[12:13], s[22:23]\n"
      "s_max_i32 m0, s28, 0\n"
      "ds_read_b128 v[168:171], v119\n"
      "s_movrels_b32 s9, s80\n"
      "ds_read_b128 v[156:159], v119 offset:6144\n"
      "s_lshr_b32 s8, s9, 8\n"
      "s_and_b32 s9, s9, 0xff\n"
      "s_mul_i32 s28, s8, 1373568\n"
      "s_mul_i32 s29, s9, 2688\n"
      "s_add_u32 s28, s28, s29\n"
      "ds_read_b128 v[144:147], v119 offset:12288\n"
      "s_cmp_eq_u32 s8, 72\n"
      "s_cselect_b32 s8, 0, 100270464\n"
      "s_cmp_eq_u32 s9, 72\n"
      "s_cselect_b32 s9, 0, 196224\n"
      "s_add_u32 s10, s8, s9\n"
      "s_add_u32 s28, s28, s10\n"
      "s_add_u32 s54, s54, s28\n"
      "ds_read_b128 v[148:151], v123 offset:6144\n"
      "ds_read_b128 v[172:175], v120\n"
      "s_addc_u32 s55, s55, 0\n"
      "s_waitcnt lgkmcnt(7)\n"
      "v_add_f32 v164, |v164|, |v165|\n"
      "v_add_f32 v166, |v166|, |v167|\n"
      "s_cmp_lg_u32 s10, s11\n"
      "s_waitcnt lgkmcnt(5)\n"
      "v_mfma_f32_16x16x32_bf16 v[152:155], a[216:219], v[136:139], 0\n"
      "ds_read_b128 v[244:247], v121\n"
      "v_add_f32 v164, v164, v166\n"
      "v_mfma_f32_16x16x32_bf16 v[152:155], a[228:231], v[140:143], v[152:155]\n"
      "ds_read_b128 v[236:239], v123 offset:9216\n"
      "v_rsq_f32 v164, v164\n"
      "v_mfma_f32_16x16x32_bf16 v[228:231], a[220:223], v[136:139], 0\n"
      "ds_read_b128 v[220:223], v123 offset:12288\n"
      "v_mul_f32 v164, s41, v164\n"
      "v_mfma_f32_16x16x32_bf16 v[228:231], a[232:235], v[140:143], v[228:231]\n"
      "ds_read_b128 v[240:243], v123 offset:15360\n"
      "v_mov_b32 v163, v164\n"
      "v_mfma_f32_16x16x32_bf16 v[216:219], a[224:227], v[136:139], 0\n"
      "s_waitcnt lgkmcnt(6)\n"
      "ds_read_b128 v[136:139], v123 offset:1024\n"
      "v_mfma_f32_16x16x32_bf16 v[216:219], a[236:239], v[140:143], v[216:219]\n"
      "ds_read_b128 v[140:143], v123 offset:4096\n"
      "v_permlane32_swap_b32 v164, v163\n"
      "v_mov_b32 v160, v164\n"
      "s_nop 1\n"
      "v_permlane16_swap_b32 v164, v160\n"
      "v_mul_f32 v179, v164, v179\n"
      "v_mul_f32 v177, v164, v177\n"
      "v_mul_f32_e64 v176, v164, v176\n"
      "v_mul_f32_e64 v178, v164, v178\n"
      "v_mul_f32 v181, v164, v181\n"
      "v_cvt_pk_bf16_f32 v176, v176, v177\n"
      "v_cvt_pk_bf16_f32 v177, v178, v179\n"
      "s_waitcnt lgkmcnt(4)\n"
      "v_mfma_f32_16x16x32_bf16 v[152:155], a[240:243], v[148:151], v[152:155]\n"
      "ds_read_b128 v[232:235], v123 offset:7168\n"
      "v_mul_f32_e64 v180, v164, v180\n"
      "v_mfma_f32_16x16x32_bf16 v[152:155], a[252:255], v[236:239], v[152:155]\n"
      "s_cbranch_scc1 L_edge_36_%=\n"
      "L_eb_37_%=:\n"
      "v_mfma_f32_16x16x32_bf16 v[216:219], a[248:251], v[148:151], v[216:219]\n"
      "global_store_dwordx4 v116, v[168:171], s[54:55] sc1\n"
      "v_mfma_f32_16x16x32_bf16 v[216:219], v[4:7], v[236:239], v[216:219]\n"
      "ds_read_b128 v[248:251], v123 offset:10240\n"
      "s_waitcnt lgkmcnt(2)\n"
      "v_mfma_f32_16x16x32_bf16 v[228:231], a[244:247], v[148:151], v[228:231]\n"
      "ds_read_b128 v[166:169], v123 offset:13312\n"
      "v_cvt_pk_bf16_f32 v178, v180, v181\n"
      "v_mfma_f32_16x16x32_bf16 v[228:231], v[0:3], v[236:239], v[228:231]\n"
      "ds_read_b128 v[236:239], v123 offset:16384\n"
      "v_mul_f32 v182, v164, v182\n"
      "v_mfma_f32_16x16x32_bf16 v[152:155], v[8:11], v[220:223], v[152:155]\n"
      "v_mul_f32 v183, v164, v183\n"
      "v_cvt_pk_bf16_f32 v179, v182, v183\n"
      "v_mfma_f32_16x16x32_bf16 v[152:155], v[20:23], v[240:243], v[152:155]\n"
      "ds_write_b128 v126, v[176:179] offset:18432\n"
      "v_mfma_f32_16x16x32_bf16 v[228:231], v[12:15], v[220:223], v[228:231]\n"
      "ds_read_b128 v[176:179], v123 offset:5120\n"
      "v_mul_f32 v185, v164, v185\n"
      "v_mfma_f32_16x16x32_bf16 v[228:231], v[24:27], v[240:243], v[228:231]\n"
      "ds_read_b128 v[180:183], v123 offset:2048\n"
      "v_mul_f32 v184, v164, v184\n"
      "v_mfma_f32_16x16x32_bf16 v[216:219], v[16:19], v[220:223], v[216:219]\n"
      "global_store_dwordx4 v116, v[156:159], s[54:55] offset:128 sc1\n"
      "v_mfma_f32_16x16x32_bf16 v[216:219], v[28:31], v[240:243], v[216:219]\n"
      "s_waitcnt lgkmcnt(5)\n"
      "v_cvt_pk_bf16_f32 v184, v184, v185\n"
      "v_mul_f32 v186, v164, v186\n"
      "v_mfma_f32_16x16x32_bf16 v[156:159], a[216:219], v[136:139], 0\n"
      "s_lshl_b32 s29, s24, 8\n"
      "v_mul_f32 v187, v164, v187\n"
      "v_cvt_pk_bf16_f32 v185, v186, v187\n"
      "v_mfma_f32_16x16x32_bf16 v[156:159], a[228:231], v[140:143], v[156:159]\n"
      "ds_write_b64 v127, v[184:185] offset:18432\n"
      "v_mfma_f32_16x16x32_bf16 v[184:187], a[220:223], v[136:139], 0\n"
      "ds_read_b128 v[240:243], v123 offset:8192\n"
      "v_mul_f32_e64 v189, v160, v189\n"
      "v_mfma_f32_16x16x32_bf16 v[184:187], a[232:235], v[140:143], v[184:187]\n"
      "ds_read_b128 v[220:223], v123 offset:11264\n"
      "v_mul_f32 v188, v160, v188\n"
      "v_mfma_f32_16x16x32_bf16 v[156:159], a[240:243], v[232:235], v[156:159]\n"
      "v_mul_f32 v191, v160, v191\n"
      "v_cvt_pk_bf16_f32 v188, v188, v189\n"
      "v_mfma_f32_16x16x32_bf16 v[156:159], a[252:255], v[248:251], v[156:159]\n"
      "v_mul_f32_e64 v190, v160, v190\n"
      "v_cvt_pk_bf16_f32 v189, v190, v191\n"
      "v_mfma_f32_16x16x32_bf16 v[184:187], a[244:247], v[232:235], v[184:187]\n"
      "s_add_u32 s29, s29, 159088\n"
      "v_mul_f32 v193, v160, v193\n"
      "v_mul_f32 v192, v160, v192\n"
      "v_mfma_f32_16x16x32_bf16 v[184:187], v[0:3], v[248:251], v[184:187]\n"
      "s_waitcnt lgkmcnt(3)\n"
      "v_cvt_pk_bf16_f32 v190, v192, v193\n"
      "v_mul_f32 v194, v160, v194\n"
      "v_mfma_f32_16x16x32_bf16 v[148:151], a[224:227], v[136:139], 0\n"
      "ds_read_b128 v[136:139], v123 offset:14336\n"
      "v_mul_f32_e64 v195, v160, v195\n"
      "v_mfma_f32_16x16x32_bf16 v[148:151], a[236:239], v[140:143], v[148:151]\n"
      "ds_read_b128 v[140:143], v123 offset:17408\n"
      "v_cvt_pk_bf16_f32 v191, v194, v195\n"
      "v_mfma_f32_16x16x32_bf16 v[148:151], a[248:251], v[232:235], v[148:151]\n"
      "v_mul_f32 v196, v160, v196\n"
      "v_mul_f32 v199, v160, v199\n"
      "v_mfma_f32_16x16x32_bf16 v[148:151], v[4:7], v[248:251], v[148:151]\n"
      "ds_write_b128 v126, v[188:191] offset:20480\n"
      "v_mfma_f32_16x16x32_bf16 v[156:159], v[8:11], v[166:169], v[156:159]\n"
      "global_store_dwordx4 v116, v[144:147], s[54:55] offset:256 sc1\n"
      "v_mfma_f32_16x16x32_bf16 v[156:159], v[20:23], v[236:239], v[156:159]\n"
      "v_mul_f32 v198, v160, v198\n"
      "v_mul_f32 v197, v160, v197\n"
      "v_mfma_f32_16x16x32_bf16 v[184:187], v[12:15], v[166:169], v[184:187]\n"
      "v_cvt_pk_bf16_f32 v196, v196, v197\n"
      "v_cvt_pk_bf16_f32 v197, v198, v199\n"
      "v_mfma_f32_16x16x32_bf16 v[184:187], v[24:27], v[236:239], v[184:187]\n"
      "ds_write_b64 v127, v[196:197] offset:20480\n"
      "v_mfma_f32_16x16x32_bf16 v[148:151], v[16:19], v[166:169], v[148:151]\n"
      "ds_read_b128 v[196:199], v162\n"
      "v_fma_f32 v160, v152, v152, s39\n"
      "v_mfma_f32_16x16x32_bf16 v[148:151], v[28:31], v[236:239], v[148:151]\n"
      "ds_read_b128 v[188:191], v162 offset:1408\n"
      "v_fmac_f32 v160, v153, v153\n"
      "v_mfma_f32_16x16x32_bf16 v[192:195], a[216:219], v[180:183], 0\n"
      "s_waitcnt lgkmcnt(4)\n"
      "v_fmac_f32 v160, v154, v154\n"
      "v_fmac_f32 v160, v155, v155\n"
      "v_mfma_f32_16x16x32_bf16 v[192:195], a[228:231], v[176:179], v[192:195]\n"
      "v_fmac_f32 v160, v228, v228\n"
      "v_fmac_f32 v160, v229, v229\n"
      "v_mfma_f32_16x16x32_bf16 v[164:167], a[220:223], v[180:183], 0\n"
      "global_store_dwordx4 v117, v[172:175], s[54:55] sc1\n"
      "v_mfma_f32_16x16x32_bf16 v[164:167], a[232:235], v[176:179], v[164:167]\n"
      "v_fmac_f32 v160, v230, v230\n"
      "v_fmac_f32 v160, v231, v231\n"
      "v_mfma_f32_16x16x32_bf16 v[172:175], a[224:227], v[180:183], 0\n"
      "ds_read_b128 v[180:183], v162 offset:4224\n"
      "v_fmac_f32 v160, v216, v216\n"
      "v_mfma_f32_16x16x32_bf16 v[172:175], a[236:239], v[176:179], v[172:175]\n"
      "ds_read_b128 v[176:179], v162 offset:2816\n"
      "v_fmac_f32 v160, v217, v217\n"
      "v_mfma_f32_16x16x32_bf16 v[192:195], a[240:243], v[240:243], v[192:195]\n"
      "v_fmac_f32 v160, v218, v218\n"
      "v_fmac_f32 v160, v219, v219\n"
      "v_mfma_f32_16x16x32_bf16 v[192:195], a[252:255], v[220:223], v[192:195]\n"
      "v_mul_f32 v202, v163, v202\n"
      "v_mul_f32 v200, v163, v200\n"
      "v_mfma_f32_16x16x32_bf16 v[164:167], a[244:247], v[240:243], v[164:167]\n"
      "v_mul_f32_e64 v201, v163, v201\n"
      "v_cvt_pk_bf16_f32 v200, v200, v201\n"
      "v_mfma_f32_16x16x32_bf16 v[164:167], v[0:3], v[220:223], v[164:167]\n"
      "ds_read_b128 v[236:239], v162 offset:7040\n"
      "v_mul_f32 v204, v163, v204\n"
      "v_mfma_f32_16x16x32_bf16 v[172:175], a[248:251], v[240:243], v[172:175]\n"
      "ds_read_b128 v[240:243], v162 offset:5632\n"
      "v_mul_f32 v203, v163, v203\n"
      "v_mfma_f32_16x16x32_bf16 v[172:175], v[4:7], v[220:223], v[172:175]\n"
      "v_mul_f32_e64 v205, v163, v205\n"
      "v_cvt_pk_bf16_f32 v201, v202, v203\n"
      "v_mfma_f32_16x16x32_bf16 v[192:195], v[8:11], v[136:139], v[192:195]\n"
      "v_cvt_pk_bf16_f32 v202, v204, v205\n"
      "v_mul_f32 v207, v163, v207\n"
      "v_mfma_f32_16x16x32_bf16 v[192:195], v[20:23], v[140:143], v[192:195]\n"
      "s_waitcnt lgkmcnt(3)\n"
      "v_mul_f32_e64 v206, v163, v206\n"
      "v_cvt_pk_bf16_f32 v203, v206, v207\n"
      "v_mfma_f32_16x16x32_bf16 v[164:167], v[12:15], v[136:139], v[164:167]\n"
      "ds_read_b128 v[204:207], v162 offset:256\n"
      "v_mul_f32_e64 v208, v163, v208\n"
      "v_mfma_f32_16x16x32_bf16 v[164:167], v[24:27], v[140:143], v[164:167]\n"
      "ds_write_b128 v126, v[200:203] offset:22528\n"
      "v_mfma_f32_16x16x32_bf16 v[172:175], v[16:19], v[136:139], v[172:175]\n"
      "ds_read_b128 v[200:203], v162 offset:1664\n"
      "v_mul_f32 v209, v163, v209\n"
      "v_mfma_f32_16x16x32_bf16 v[172:175], v[28:31], v[140:143], v[172:175]\n"
      "v_cvt_pk_bf16_f32 v208, v208, v209\n"
      "v_mul_f32 v211, v163, v211\n"
      "v_mfma_f32_16x16x32_bf16 v[140:143], a[216:219], v[196:199], 0\n"
      "s_waitcnt lgkmcnt(3)\n"
      "v_mul_f32 v210, v163, v210\n"
      "v_cvt_pk_bf16_f32 v209, v210, v211\n"
      "v_mfma_f32_16x16x32_bf16 v[140:143], a[228:231], v[188:191], v[140:143]\n"
      "ds_write_b64 v127, v[208:209] offset:22528\n"
      "v_mfma_f32_16x16x32_bf16 v[208:211], a[220:223], v[196:199], 0\n"
      "ds_read_b128 v[136:139], v162 offset:3072\n"
      "v_fma_f32 v163, v156, v156, s39\n"
      "v_mfma_f32_16x16x32_bf16 v[208:211], a[232:235], v[188:191], v[208:211]\n"
      "ds_read_b128 v[220:223], v162 offset:4480\n"
      "v_fmac_f32 v163, v157, v157\n"
      "v_mfma_f32_16x16x32_bf16 v[144:147], a[224:227], v[196:199], 0\n"
      "v_fmac_f32 v163, v158, v158\n"
      "v_fmac_f32_e64 v163, v159, v159\n"
      "v_mfma_f32_16x16x32_bf16 v[144:147], a[236:239], v[188:191], v[144:147]\n"
      "v_fmac_f32 v163, v184, v184\n"
      "v_fmac_f32 v163, v185, v185\n"
      "v_mfma_f32_16x16x32_bf16 v[140:143], a[240:243], v[176:179], v[140:143]\n"
      "s_waitcnt lgkmcnt(3)\n"
      "v_fmac_f32 v163, v186, v186\n"
      "v_fmac_f32 v163, v187, v187\n"
      "v_mfma_f32_16x16x32_bf16 v[140:143], a[252:255], v[180:183], v[140:143]\n"
      "ds_read_b128 v[188:191], v162 offset:5888\n"
      "v_fmac_f32 v163, v148, v148\n"
      "v_mfma_f32_16x16x32_bf16 v[208:211], a[244:247], v[176:179], v[208:211]\n"
      "ds_read_b128 v[196:199], v162 offset:7296\n"
      "v_fmac_f32 v163, v149, v149\n"
      "v_mfma_f32_16x16x32_bf16 v[208:211], v[0:3], v[180:183], v[208:211]\n"
      "v_fmac_f32 v163, v150, v150\n"
      "v_fmac_f32_e64 v163, v151, v151\n"
      "v_mfma_f32_16x16x32_bf16 v[144:147], a[248:251], v[176:179], v[144:147]\n"
      "v_lshl_add_u32 v162, v161, 2, s29\n"
      "v_lshrrev_b32_e64 v176, 3, v161\n"
      "v_mfma_f32_16x16x32_bf16 v[144:147], v[4:7], v[180:183], v[144:147]\n"
      "ds_read_b32 v162, v162\n"
      "v_fma_f32 v161, v192, v192, s39\n"
      "v_mfma_f32_16x16x32_bf16 v[140:143], v[8:11], v[240:243], v[140:143]\n"
      "s_lshl_b32 s29, s24, 3\n"
      "v_add_u32 v176, s29, v176\n"
      "v_fmac_f32 v161, v193, v193\n"
      "v_mfma_f32_16x16x32_bf16 v[140:143], v[20:23], v[236:239], v[140:143]\n"
      "v_fmac_f32 v161, v194, v194\n"
      "v_cmp_gt_u32 s[48:49], s26, v176\n"
      "v_mfma_f32_16x16x32_bf16 v[208:211], v[12:15], v[240:243], v[208:211]\n"
      "v_fmac_f32 v161, v195, v195\n"
      "v_fmac_f32 v161, v164, v164\n"
      "v_mfma_f32_16x16x32_bf16 v[208:211], v[24:27], v[236:239], v[208:211]\n"
      "s_mov_b32 exec_hi, 0\n"
      "global_store_dwordx4 v118, v[244:247], s[54:55] sc1\n"
      "s_mov_b32 exec_hi, -1\n"
      "v_mfma_f32_16x16x32_bf16 v[144:147], v[16:19], v[240:243], v[144:147]\n"
      "v_fmac_f32 v161, v165, v165\n"
      "v_fmac_f32 v161, v166, v166\n"
      "v_mfma_f32_16x16x32_bf16 v[144:147], v[28:31], v[236:239], v[144:147]\n"
      "s_waitcnt lgkmcnt(1)\n"
      "v_fmac_f32 v161, v167, v167\n"
      "v_fmac_f32 v161, v172, v172\n"
      "v_mfma_f32_16x16x32_bf16 v[176:179], a[216:219], v[204:207], 0\n"
      "v_fmac_f32 v161, v173, v173\n"
      "v_fmac_f32 v161, v174, v174\n"
      "v_mfma_f32_16x16x32_bf16 v[176:179], a[228:231], v[200:203], v[176:179]\n"
      "v_fmac_f32 v161, v175, v175\n"
      "v_mov_b32 v180, v161\n"
      "v_mfma_f32_16x16x32_bf16 v[236:239], a[220:223], v[204:207], 0\n"
      "v_permlane16_swap_b32 v160, v163\n"
      "v_mfma_f32_16x16x32_bf16 v[236:239], a[232:235], v[200:203], v[236:239]\n"
      "v_add_f32 v160, v160, v163\n"
      "v_permlane16_swap_b32 v161, v180\n"
      "v_mfma_f32_16x16x32_bf16 v[244:247], a[224:227], v[204:207], 0\n"
      "v_add_f32 v161, v161, v180\n"
      "v_fma_f32 v163, v140, v140, s39\n"
      "v_mfma_f32_16x16x32_bf16 v[244:247], a[236:239], v[200:203], v[244:247]\n"
      "v_permlane32_swap_b32 v160, v161\n"
      "v_mfma_f32_16x16x32_bf16 v[176:179], a[240:243], v[136:139], v[176:179]\n"
      "v_add_f32 v160, v160, v161\n"
      "v_fmac_f32 v163, v141, v141\n"
      "v_mfma_f32_16x16x32_bf16 v[176:179], a[252:255], v[220:223], v[176:179]\n"
      "ds_write_b32 v129, v160 offset:1024\n"
      "v_mfma_f32_16x16x32_bf16 v[244:247], a[248:251], v[136:139], v[244:247]\n"
      "v_fmac_f32 v163, v142, v142\n"
      "v_fmac_f32 v163, v143, v143\n"
      "v_mfma_f32_16x16x32_bf16 v[244:247], v[4:7], v[220:223], v[244:247]\n"
      "v_fmac_f32 v163, v208, v208\n"
      "v_fmac_f32 v163, v209, v209\n"
      "v_mfma_f32_16x16x32_bf16 v[236:239], a[244:247], v[136:139], v[236:239]\n"
      "v_fmac_f32 v163, v210, v210\n"
      "v_mfma_f32_16x16x32_bf16 v[236:239], v[0:3], v[220:223], v[236:239]\n"
      "v_mfma_f32_16x16x32_bf16 v[236:239], v[12:15], v[188:191], v[236:239]\n"
      "v_fmac_f32 v163, v211, v211\n"
      "v_mfma_f32_16x16x32_bf16 v[176:179], v[8:11], v[188:191], v[176:179]\n"
      "v_fmac_f32 v163, v144, v144\n"
      "v_fmac_f32 v163, v145, v145\n"
      "v_mfma_f32_16x16x32_bf16 v[176:179], v[20:23], v[196:199], v[176:179]\n"
      "v_mfma_f32_16x16x32_bf16 v[236:239], v[24:27], v[196:199], v[236:239]\n"
      "v_fmac_f32 v163, v146, v146\n"
      "v_mfma_f32_16x16x32_bf16 v[244:247], v[16:19], v[188:191], v[244:247]\n"
      "v_mfma_f32_16x16x32_bf16 v[244:247], v[28:31], v[196:199], v[244:247]\n"
      "v_fmac_f32_e64 v163, v147, v147\n"
      "s_nop 2\n"
      "v_pk_mul_f32 v[160:161], v[176:177], v[176:177]\n"
      "v_pk_fma_f32 v[160:161], v[178:179], v[178:179], v[160:161]\n"
      "v_pk_fma_f32 v[160:161], v[236:237], v[236:237], v[160:161]\n"
      "v_pk_fma_f32 v[160:161], v[238:239], v[238:239], v[160:161]\n"
      "v_pk_fma_f32 v[160:161], v[244:245], v[244:245], v[160:161]\n"
      "v_pk_fma_f32 v[160:161], v[246:247], v[246:247], v[160:161]\n"
      "v_add_f32 v160, s39, v160\n"
      "v_add_f32 v196, v160, v161\n"
      "v_mov_b32 v160, v196\n"
      "v_mov_b32 v161, v160\n"
      "v_permlane16_swap_b32 v163, v196\n"
      "v_add_f32 v163, v163, v196\n"
      "v_permlane16_swap_b32 v160, v161\n"
      "v_add_f32 v160, v160, v161\n"
      "s_nop 1\n"
      "v_permlane32_swap_b32 v163, v160\n"
      "v_add_f32_e64 v163, v163, v160\n"
      "ds_write_b32 v129, v163 offset:2048\n"
      "s_waitcnt lgkmcnt(0)\n"
      "s_barrier\n"
      "ds_read_b128 v[188:191], v128 offset:1024\n"
      "ds_read_b128 v[196:199], v128 offset:2048\n"
      "s_add_u32 s27, s27, 1\n"
      "s_sub_u32 s28, s27, 3\n"
      "s_cselect_b64 s[54:55], s[12:13], s[22:23]\n"
      "s_max_i32 m0, s28, 0\n"
      "ds_read_b128 v[180:183], v121 offset:18432\n"
      "ds_read_b128 v[204:207], v119 offset:24576\n"
      "s_movrels_b32 s9, s80\n"
      "s_lshr_b32 s8, s9, 8\n"
      "ds_read_b128 v[136:139], v119 offset:18432\n"
      "ds_read_b128 v[220:223], v120 offset:18432\n"
      "s_and_b32 s9, s9, 0xff\n"
      "s_mul_i32 s28, s8, 1373568\n"
      "s_mul_i32 s29, s9, 2688\n"
      "s_add_u32 s28, s28, s29\n"
      "s_cmp_eq_u32 s8, 72\n"
      "s_cselect_b32 s8, 0, 100270464\n"
      "s_cmp_eq_u32 s9, 72\n"
      "s_cselect_b32 s9, 0, 196224\n"
      "s_add_u32 s10, s8, s9\n"
      "s_add_u32 s28, s28, s10\n"
      "s_add_u32 s54, s54, s28\n"
      "s_addc_u32 s55, s55, 0\n"
      "s_cmp_lg_u32 s10, s11\n"
      "s_cbranch_scc1 L_edge_38_%=\n"
      "L_eb_39_%=:\n"
      "ds_read_b128 v[200:203], v119 offset:30720\n"
      "s_waitcnt lgkmcnt(5)\n"
      "v_add_f32 v188, |v188|, |v189|\n"
      "v_add_f32 v190, |v190|, |v191|\n"
      "v_add_f32 v188, v188, v190\n"
      "v_rsq_f32 v188, v188\n"
      "v_add_f32 v196, |v196|, |v197|\n"
      "v_mul_f32 v188, s41, v188\n"
      "v_mov_b32 v190, v188\n"
      "v_add_f32 v198, |v198|, |v199|\n"
      "v_subrev_u32 v197, 73728, v127\n"
      "v_permlane32_swap_b32 v188, v190\n"
      "v_mov_b32 v199, v188\n"
      "v_pk_mul_f32 v[194:195], v[194:195], v[190:191] op_sel_hi:[1,0]\n"
      "v_add_f32 v196, v196, v198\n"
      "v_permlane16_swap_b32 v188, v199\n"
      "v_pk_mul_f32 v[192:193], v[192:193], v[190:191] op_sel_hi:[1,0]\n"
      "v_pk_mul_f32 v[152:153], v[152:153], v[188:189] op_sel_hi:[1,0]\n"
      "v_pk_mul_f32 v[156:157], v[156:157], v[198:199] op_sel:[0,1]\n"
      "v_pk_mul_f32 v[186:187], v[186:187], v[198:199] op_sel:[0,1]\n"
      "v_pk_mul_f32 v[218:219], v[218:219], v[188:189] op_sel_hi:[1,0]\n"
      "v_rsq_f32_e64 v196, v196\n"
      "v_subrev_u32 v198, 73728, v126\n"
      "v_pk_mul_f32 v[184:185], v[184:185], v[198:199] op_sel:[0,1]\n"
      "v_pk_mul_f32 v[148:149], v[148:149], v[198:199] op_sel:[0,1]\n"
      "v_pk_mul_f32 v[172:173], v[172:173], v[190:191] op_sel_hi:[1,0]\n"
      "v_pk_mul_f32 v[150:151], v[150:151], v[198:199] op_sel:[0,1]\n"
      "v_pk_mul_f32 v[158:159], v[158:159], v[198:199] op_sel:[0,1]\n"
      "v_cvt_pk_bf16_f32 v192, v192, v193\n"
      "v_cvt_pk_bf16_f32 v193, v194, v195\n"
      "v_cvt_pk_bf16_f32 v148, v148, v149\n"
      "v_mul_f32 v196, s41, v196\n"
      "v_mov_b32 v199, v196\n"
      "v_pk_mul_f32 v[174:175], v[174:175], v[190:191] op_sel_hi:[1,0]\n"
      "v_cvt_pk_bf16_f32 v156, v156, v157\n"
      "v_cvt_pk_bf16_f32 v152, v152, v153\n"
      "v_pk_mul_f32 v[154:155], v[154:155], v[188:189] op_sel_hi:[1,0]\n"
      "v_cvt_pk_bf16_f32 v157, v158, v159\n"
      "v_permlane32_swap_b32_e64 v196, v199\n"
      "v_cvt_pk_bf16_f32 v153, v154, v155\n"
      "v_cvt_pk_bf16_f32 v159, v186, v187\n"
      "v_mov_b32_e64 v199, v196\n"
      "v_pk_mul_f32 v[166:167], v[166:167], v[190:191] op_sel_hi:[1,0]\n"
      "v_pk_mul_f32 v[228:229], v[228:229], v[188:189] op_sel_hi:[1,0]\n"
      "v_cvt_pk_bf16_f32 v195, v166, v167\n"
      "s_waitcnt lgkmcnt(0)\n"
      "v_permlane16_swap_b32 v196, v199\n"
      "v_pk_mul_f32 v[208:209], v[208:209], v[196:197] op_sel_hi:[1,0]\n"
      "v_pk_mul_f32 v[210:211], v[210:211], v[196:197] op_sel_hi:[1,0]\n"
      "v_pk_mul_f32 v[216:217], v[216:217], v[188:189] op_sel_hi:[1,0]\n"
      "v_pk_mul_f32 v[176:177], v[176:177], v[198:199] op_sel:[0,1]\n"
      "v_pk_mul_f32 v[140:141], v[140:141], v[196:197] op_sel_hi:[1,0]\n"
      "v_pk_mul_f32 v[238:239], v[238:239], v[198:199] op_sel:[0,1]\n"
      "v_pk_mul_f32 v[244:245], v[244:245], v[198:199] op_sel:[0,1]\n"
      "v_pk_mul_f32 v[142:143], v[142:143], v[196:197] op_sel_hi:[1,0]\n"
      "v_cvt_pk_bf16_f32 v244, v244, v245\n"
      "v_pk_mul_f32 v[144:145], v[144:145], v[196:197] op_sel_hi:[1,0]\n"
      "v_pk_mul_f32 v[230:231], v[230:231], v[188:189] op_sel_hi:[1,0]\n"
      "v_cvt_pk_bf16_f32 v144, v144, v145\n"
      "v_cvt_pk_bf16_f32 v140, v140, v141\n"
      "v_cvt_pk_bf16_f32 v149, v150, v151\n"
      "v_pk_mul_f32 v[146:147], v[146:147], v[196:197] op_sel_hi:[1,0]\n"
      "v_cvt_pk_bf16_f32 v216, v216, v217\n"
      "v_pk_mul_f32 v[236:237], v[236:237], v[198:199] op_sel:[0,1]\n"
      "v_cvt_pk_bf16_f32 v158, v184, v185\n"
      "v_cvt_pk_bf16_f32 v141, v142, v143\n"
      "v_pk_mul_f32 v[246:247], v[246:247], v[198:199] op_sel:[0,1]\n"
      "v_cvt_pk_bf16_f32 v217, v218, v219\n"
      "v_pk_mul_f32 v[164:165], v[164:165], v[190:191] op_sel_hi:[1,0]\n"
      "v_cvt_pk_bf16_f32 v143, v210, v211\n"
      "ds_write_b64 v127, v[216:217]\n"
      "global_store_dwordx4 v116, v[136:139], s[54:55] sc1\n"
      "global_store_dwordx4 v116, v[204:207], s[54:55] offset:128 sc1\n"
      "v_cvt_pk_bf16_f32 v154, v228, v229\n"
      "v_cvt_pk_bf16_f32 v155, v230, v231\n"
      "v_cvt_pk_bf16_f32 v194, v164, v165\n"
      "v_pk_mul_f32 v[178:179], v[178:179], v[198:199] op_sel:[0,1]\n"
      "v_cvt_pk_bf16_f32 v142, v208, v209\n"
      "v_cvt_pk_bf16_f32 v176, v176, v177\n"
      "v_cvt_pk_bf16_f32 v145, v146, v147\n"
      "ds_write_b128 v198, v[140:143]\n"
      "ds_write_b64 v197, v[144:145]\n"
      "ds_write_b64 v127, v[148:149] offset:2048\n"
      "global_store_dwordx4 v116, v[200:203], s[54:55] offset:256 sc1\n"
      "global_store_dwordx4 v117, v[220:223], s[54:55] sc1\n"
      "ds_write_b128 v126, v[152:155]\n"
      "ds_write_b128 v126, v[192:195] offset:4096\n"
      "v_cvt_pk_bf16_f32 v172, v172, v173\n"
      "v_cvt_pk_bf16_f32 v177, v178, v179\n"
      "v_cvt_pk_bf16_f32 v173, v174, v175\n"
      "v_cvt_pk_bf16_f32 v179, v238, v239\n"
      "v_cvt_pk_bf16_f32 v245, v246, v247\n"
      "v_cvt_pk_bf16_f32 v178, v236, v237\n"
      "s_mov_b32 exec_hi, 0\n"
      "global_store_dwordx4 v118, v[180:183], s[54:55] sc1\n"
      "s_mov_b32 exec_hi, -1\n"
      "ds_write_b128 v126, v[156:159] offset:2048\n"
      "ds_write_b128 v198, v[176:179] offset:2048\n"
      "ds_write_b64 v127, v[172:173] offset:4096\n"
      "ds_write_b64 v197, v[244:245] offset:2048\n"
      "s_waitcnt lgkmcnt(0)\n"
      "s_barrier\n"
      "ds_read_b128 v[176:179], v121\n"
      "ds_read_b128 v[204:207], v119 offset:6144\n"
      "s_add_u32 s27, s27, 1\n"
      "ds_read_b128 v[208:211], v119 offset:12288\n"
      "ds_read_b128 v[180:183], v119\n"
      "v_subrev_u32 v163, 73728, v119\n"
      "s_sub_u32 s28, s27, 3\n"
      "ds_read_b128 v[192:195], v163\n"
      "ds_read_b128 v[140:143], v120\n"
      "s_cselect_b64 s[54:55], s[12:13], s[22:23]\n"
      "s_max_i32 m0, s28, 0\n"
      "ds_read_b128 v[164:167], v163 offset:6144\n"
      "s_movrels_b32 s9, s80\n"
      "s_lshr_b32 s8, s9, 8\n"
      "s_and_b32 s9, s9, 0xff\n"
      "s_mul_i32 s28, s8, 1373568\n"
      "s_mul_i32 s29, s9, 2688\n"
      "s_add_u32 s28, s28, s29\n"
      "s_cmp_eq_u32 s8, 72\n"
      "s_cselect_b32 s8, 0, 100270464\n"
      "ds_read_b128 v[200:203], v163 offset:12288\n"
      "s_cmp_eq_u32 s9, 72\n"
      "s_cselect_b32 s9, 0, 196224\n"
      "s_add_u32 s10, s8, s9\n"
      "s_add_u32 s28, s28, s10\n"
      "s_add_u32 s54, s54, s28\n"
      "s_addc_u32 s55, s55, 0\n"
      "s_cmp_lg_u32 s10, s11\n"
      "s_cbranch_scc1 L_edge_40_%=\n"
      "L_eb_41_%=:\n"
      "s_waitcnt lgkmcnt(4)\n"
      "global_store_dwordx4 v116, v[180:183], s[54:55] sc1\n"
      "global_store_dwordx4 v116, v[204:207], s[54:55] offset:128 sc1\n"
      "global_store_dwordx4 v116, v[208:211], s[54:55] offset:256 sc1\n"
      "s_waitcnt lgkmcnt(0)\n"
      "global_store_dwordx4 v117, v[140:143], s[54:55] sc1\n"
      "s_mov_b32 exec_hi, 0\n"
      "global_store_dwordx4 v118, v[176:179], s[54:55] sc1\n"
      "s_mov_b32 exec_hi, -1\n"
      "s_nop 0\n"
      "s_mov_b64 exec, s[48:49]\n"
      "s_cbranch_execz L_np_42_%=\n"
      "global_store_dwordx4 v162, v[192:195], s[22:23] sc1\n"
      "global_store_dwordx4 v162, v[164:167], s[22:23] offset:128 sc1\n"
      "global_store_dwordx4 v162, v[200:203], s[22:23] offset:256 sc1\n"
      "L_np_42_%=:\n"
      "s_mov_b64 exec, -1\n"
      "s_endpgm\n"
      "L_tail1_%=:\n"
      "s_cmp_eq_u32 s10, s11\n"
      "s_cbranch_scc1 L_sk_43_%=\n"
      "s_mov_b32 s11, s10\n"
      "s_sub_u32 s28, s60, s8\n"
      "s_sub_u32 s29, s61, s9\n"
      "s_mov_b32 s60, s8\n"
      "s_mov_b32 s61, s9\n"
      "s_mov_b64 exec, s[64:65]\n"
      "v_add_u32 v116, s28, v116\n"
      "s_mov_b64 exec, s[70:71]\n"
      "v_add_u32 v116, s29, v116\n"
      "s_mov_b64 exec, s[66:67]\n"
      "v_add_u32 v117, s28, v117\n"
      "s_mov_b64 exec, s[72:73]\n"
      "v_add_u32 v117, s29, v117\n"
      "s_mov_b64 exec, s[68:69]\n"
      "v_add_u32 v118, s28, v118\n"
      "s_mov_b64 exec, s[74:75]\n"
      "v_add_u32 v118, s29, v118\n"
      "s_mov_b64 exec, -1\n"
      "s_nop 4\n"
      "L_sk_43_%=:\n"
      "s_cmp_lt_u32 s27, s26\n"
      "s_cbranch_scc1 L_mid_%=\n"
      "s_nop 7\n"
      "v_mbcnt_lo_u32_b32 v137, -1, 0\n"
      "v_mbcnt_hi_u32_b32 v137, -1, v137\n"
      "v_lshrrev_b32 v138, 4, v137\n"
      "v_mul_u32_u24 v138, 352, v138\n"
      "v_rcp_f32 v136, v136\n"
      "v_and_b32_e64 v139, 15, v137\n"
      "v_pk_mul_f32 v[140:141], v[140:141], v[136:137] op_sel_hi:[1,0]\n"
      "v_pk_mul_f32 v[160:161], v[160:161], v[136:137] op_sel_hi:[1,0]\n"
      "v_pk_mul_f32 v[162:163], v[162:163], v[136:137] op_sel_hi:[1,0]\n"
      "v_pk_mul_f32 v[142:143], v[142:143], v[136:137] op_sel_hi:[1,0]\n"
      "v_pk_mul_f32 v[164:165], v[164:165], v[136:137] op_sel_hi:[1,0]\n"
      "v_pk_mul_f32 v[166:167], v[166:167], v[136:137] op_sel_hi:[1,0]\n"
      "v_lshl_add_u32 v138, v139, 4, v138\n"
      "v_add_u32 v138, 150640, v138\n"
      "v_cvt_pk_bf16_f32 v140, v140, v141\n"
      "v_cvt_pk_bf16_f32 v141, v142, v143\n"
      "v_cvt_pk_bf16_f32 v143, v162, v163\n"
      "v_cvt_pk_bf16_f32 v142, v160, v161\n"
      "v_cvt_pk_bf16_f32 v164, v164, v165\n"
      "v_cvt_pk_bf16_f32 v165, v166, v167\n"
      "ds_write_b128 v124, v[140:143] offset:20480\n"
      "ds_write_b64 v125, v[164:165] offset:20480\n"
      "s_waitcnt lgkmcnt(0)\n"
      "s_barrier\n"
      "ds_read_b128 v[140:143], v128 offset:1024\n"
      "ds_read_b128 v[160:163], v123 offset:18432\n"
      "ds_read_b128 v[164:167], v123 offset:21504\n"
      "s_sub_u32 s28, s27, 3\n"
      "s_cselect_b64 s[54:55], s[12:13], s[22:23]\n"
      "s_max_i32 m0, s28, 0\n"
      "ds_read_b128 v[144:147], v119 offset:30720\n"
      "s_movrels_b32 s9, s80\n"
      "ds_read_b128 v[172:175], v123 offset:24576\n"
      "s_lshr_b32 s8, s9, 8\n"
      "s_and_b32 s9, s9, 0xff\n"
      "ds_read_b128 v[220:223], v119 offset:24576\n"
      "s_mul_i32 s28, s8, 1373568\n"
      "s_mul_i32 s29, s9, 2688\n"
      "s_add_u32 s28, s28, s29\n"
      "s_cmp_eq_u32 s8, 72\n"
      "s_cselect_b32 s8, 0, 100270464\n"
      "ds_read_b128 v[244:247], v119 offset:18432\n"
      "s_cmp_eq_u32 s9, 72\n"
      "s_cselect_b32 s9, 0, 196224\n"
      "s_add_u32 s10, s8, s9\n"
      "ds_read_b128 v[236:239], v120 offset:18432\n"
      "s_add_u32 s28, s28, s10\n"
      "s_add_u32 s54, s54, s28\n"
      "s_waitcnt lgkmcnt(7)\n"
      "s_addc_u32 s55, s55, 0\n"
      "v_add_f32 v142, |v142|, |v143|\n"
      "v_add_f32 v140, |v140|, |v141|\n"
      "s_waitcnt lgkmcnt(5)\n"
      "v_mfma_f32_16x16x32_bf16 v[148:151], a[216:219], v[160:163], 0\n"
      "ds_read_b128 v[156:159], v121 offset:18432\n"
      "v_add_f32 v140, v140, v142\n"
      "v_mfma_f32_16x16x32_bf16 v[148:151], a[228:231], v[164:167], v[148:151]\n"
      "ds_read_b128 v[152:155], v123 offset:27648\n"
      "v_rsq_f32 v140, v140\n"
      "v_mfma_f32_16x16x32_bf16 v[216:219], a[220:223], v[160:163], 0\n"
      "ds_read_b128 v[228:231], v123 offset:30720\n"
      "v_mul_f32_e64 v140, s41, v140\n"
      "v_mfma_f32_16x16x32_bf16 v[216:219], a[232:235], v[164:167], v[216:219]\n"
      "ds_read_b128 v[168:171], v123 offset:33792\n"
      "v_mov_b32 v139, v140\n"
      "v_mfma_f32_16x16x32_bf16 v[212:215], a[224:227], v[160:163], 0\n"
      "ds_read_b128 v[160:163], v123 offset:19456\n"
      "s_waitcnt lgkmcnt(6)\n"
      "v_mfma_f32_16x16x32_bf16 v[212:215], a[236:239], v[164:167], v[212:215]\n"
      "ds_read_b128 v[164:167], v123 offset:22528\n"
      "v_permlane32_swap_b32 v140, v139\n"
      "v_mov_b32 v136, v140\n"
      "s_cmp_lg_u32 s10, s11\n"
      "s_nop 0\n"
      "v_permlane16_swap_b32 v140, v136\n"
      "v_mul_f32 v176, v140, v176\n"
      "v_mul_f32 v178, v140, v178\n"
      "v_mul_f32 v179, v140, v179\n"
      "v_mul_f32 v181, v140, v181\n"
      "v_mul_f32 v177, v140, v177\n"
      "v_cvt_pk_bf16_f32 v176, v176, v177\n"
      "v_cvt_pk_bf16_f32 v177, v178, v179\n"
      "s_waitcnt lgkmcnt(4)\n"
      "v_mfma_f32_16x16x32_bf16 v[148:151], a[240:243], v[172:175], v[148:151]\n"
      "ds_read_b128 v[224:227], v123 offset:25600\n"
      "v_mul_f32 v180, v140, v180\n"
      "v_mfma_f32_16x16x32_bf16 v[148:151], a[252:255], v[152:155], v[148:151]\n"
      "s_cbranch_scc1 L_edge_44_%=\n"
      "L_eb_45_%=:\n"
      "v_mfma_f32_16x16x32_bf16 v[212:215], a[248:251], v[172:175], v[212:215]\n"
      "global_store_dwordx4 v116, v[244:247], s[54:55] sc1\n"
      "v_mfma_f32_16x16x32_bf16 v[212:215], v[4:7], v[152:155], v[212:215]\n"
      "ds_read_b128 v[232:235], v123 offset:28672\n"
      "s_waitcnt lgkmcnt(2)\n"
      "v_mfma_f32_16x16x32_bf16 v[216:219], a[244:247], v[172:175], v[216:219]\n"
      "ds_read_b128 v[172:175], v123 offset:31744\n"
      "v_cvt_pk_bf16_f32 v178, v180, v181\n"
      "v_mfma_f32_16x16x32_bf16 v[216:219], v[0:3], v[152:155], v[216:219]\n"
      "ds_read_b128 v[152:155], v123 offset:34816\n"
      "v_mul_f32_e64 v182, v140, v182\n"
      "v_mfma_f32_16x16x32_bf16 v[148:151], v[8:11], v[228:231], v[148:151]\n"
      "v_mul_f32_e64 v183, v140, v183\n"
      "v_cvt_pk_bf16_f32 v179, v182, v183\n"
      "v_mfma_f32_16x16x32_bf16 v[148:151], v[20:23], v[168:171], v[148:151]\n"
      "ds_write_b128 v126, v[176:179]\n"
      "v_mfma_f32_16x16x32_bf16 v[216:219], v[12:15], v[228:231], v[216:219]\n"
      "ds_read_b128 v[176:179], v123 offset:23552\n"
      "v_mul_f32_e64 v185, v140, v185\n"
      "v_mfma_f32_16x16x32_bf16 v[216:219], v[24:27], v[168:171], v[216:219]\n"
      "ds_read_b128 v[180:183], v123 offset:20480\n"
      "v_mul_f32_e64 v184, v140, v184\n"
      "v_mfma_f32_16x16x32_bf16 v[212:215], v[16:19], v[228:231], v[212:215]\n"
      "global_store_dwordx4 v116, v[220:223], s[54:55] offset:128 sc1\n"
      "v_mfma_f32_16x16x32_bf16 v[212:215], v[28:31], v[168:171], v[212:215]\n"
      "s_waitcnt lgkmcnt(5)\n"
      "v_mul_f32 v186, v140, v186\n"
      "v_cvt_pk_bf16_f32 v184, v184, v185\n"
      "v_mfma_f32_16x16x32_bf16 v[220:223], a[216:219], v[160:163], 0\n"
      "s_lshl_b32 s29, s24, 8\n"
      "v_mul_f32 v187, v140, v187\n"
      "v_cvt_pk_bf16_f32 v185, v186, v187\n"
      "v_mfma_f32_16x16x32_bf16 v[220:223], a[228:231], v[164:167], v[220:223]\n"
      "ds_write_b64 v127, v[184:185]\n"
      "v_mfma_f32_16x16x32_bf16 v[184:187], a[220:223], v[160:163], 0\n"
      "ds_read_b128 v[168:171], v123 offset:26624\n"
      "v_mul_f32_e64 v189, v136, v189\n"
      "v_mfma_f32_16x16x32_bf16 v[184:187], a[232:235], v[164:167], v[184:187]\n"
      "ds_read_b128 v[228:231], v123 offset:29696\n"
      "v_mul_f32 v188, v136, v188\n"
      "v_mfma_f32_16x16x32_bf16 v[220:223], a[240:243], v[224:227], v[220:223]\n"
      "v_mul_f32 v191, v136, v191\n"
      "v_cvt_pk_bf16_f32 v188, v188, v189\n"
      "v_mfma_f32_16x16x32_bf16 v[220:223], a[252:255], v[232:235], v[220:223]\n"
      "v_mul_f32_e64 v190, v136, v190\n"
      "v_cvt_pk_bf16_f32 v189, v190, v191\n"
      "v_mfma_f32_16x16x32_bf16 v[184:187], a[244:247], v[224:227], v[184:187]\n"
      "v_mul_f32 v193, v136, v193\n"
      "v_mul_f32 v192, v136, v192\n"
      "v_mfma_f32_16x16x32_bf16 v[184:187], v[0:3], v[232:235], v[184:187]\n"
      "s_add_u32 s29, s29, 159088\n"
      "v_mul_f32 v194, v136, v194\n"
      "v_cvt_pk_bf16_f32 v190, v192, v193\n"
      "v_mfma_f32_16x16x32_bf16 v[244:247], a[224:227], v[160:163], 0\n"
      "ds_read_b128 v[160:163], v123 offset:32768\n"
      "v_mul_f32 v195, v136, v195\n"
      "v_mfma_f32_16x16x32_bf16 v[244:247], a[236:239], v[164:167], v[244:247]\n"
      "ds_read_b128 v[164:167], v123 offset:35840\n"
      "v_cvt_pk_bf16_f32 v191, v194, v195\n"
      "v_mfma_f32_16x16x32_bf16 v[244:247], a[248:251], v[224:227], v[244:247]\n"
      "s_waitcnt lgkmcnt(4)\n"
      "v_mul_f32 v199, v136, v199\n"
      "v_mul_f32_e64 v196, v136, v196\n"
      "v_mfma_f32_16x16x32_bf16 v[244:247], v[4:7], v[232:235], v[244:247]\n"
      "ds_write_b128 v126, v[188:191] offset:2048\n"
      "v_mfma_f32_16x16x32_bf16 v[220:223], v[8:11], v[172:175], v[220:223]\n"
      "global_store_dwordx4 v116, v[144:147], s[54:55] offset:256 sc1\n"
      "v_mfma_f32_16x16x32_bf16 v[220:223], v[20:23], v[152:155], v[220:223]\n"
      "s_waitcnt lgkmcnt(3)\n"
      "v_mul_f32 v198, v136, v198\n"
      "v_mul_f32_e64 v197, v136, v197\n"
      "v_mfma_f32_16x16x32_bf16 v[184:187], v[12:15], v[172:175], v[184:187]\n"
      "v_cvt_pk_bf16_f32 v196, v196, v197\n"
      "v_cvt_pk_bf16_f32 v197, v198, v199\n"
      "v_mfma_f32_16x16x32_bf16 v[184:187], v[24:27], v[152:155], v[184:187]\n"
      "ds_write_b64 v127, v[196:197] offset:2048\n"
      "v_mfma_f32_16x16x32_bf16 v[244:247], v[16:19], v[172:175], v[244:247]\n"
      "ds_read_b128 v[172:175], v138\n"
      "v_fma_f32 v136, v148, v148, s39\n"
      "v_mfma_f32_16x16x32_bf16 v[244:247], v[28:31], v[152:155], v[244:247]\n"
      "ds_read_b128 v[152:155], v138 offset:1408\n"
      "v_fmac_f32_e64 v136, v149, v149\n"
      "v_mfma_f32_16x16x32_bf16 v[196:199], a[216:219], v[180:183], 0\n"
      "v_fmac_f32 v136, v150, v150\n"
      "v_fmac_f32 v136, v151, v151\n"
      "v_mfma_f32_16x16x32_bf16 v[196:199], a[228:231], v[176:179], v[196:199]\n"
      "v_fmac_f32 v136, v216, v216\n"
      "v_fmac_f32 v136, v217, v217\n"
      "v_mfma_f32_16x16x32_bf16 v[144:147], a[220:223], v[180:183], 0\n"
      "global_store_dwordx4 v117, v[236:239], s[54:55] sc1\n"
      "v_mfma_f32_16x16x32_bf16 v[144:147], a[232:235], v[176:179], v[144:147]\n"
      "v_fmac_f32 v136, v218, v218\n"
      "v_fmac_f32 v136, v219, v219\n"
      "v_mfma_f32_16x16x32_bf16 v[236:239], a[224:227], v[180:183], 0\n"
      "ds_read_b128 v[180:183], v138 offset:4224\n"
      "v_fmac_f32_e64 v136, v212, v212\n"
      "v_mfma_f32_16x16x32_bf16 v[236:239], a[236:239], v[176:179], v[236:239]\n"
      "ds_read_b128 v[176:179], v138 offset:2816\n"
      "v_fmac_f32 v136, v213, v213\n"
      "v_mfma_f32_16x16x32_bf16 v[196:199], a[240:243], v[168:171], v[196:199]\n"
      "v_fmac_f32 v136, v214, v214\n"
      "v_fmac_f32_e64 v136, v215, v215\n"
      "v_mfma_f32_16x16x32_bf16 v[196:199], a[252:255], v[228:231], v[196:199]\n"
      "s_waitcnt lgkmcnt(3)\n"
      "v_mul_f32 v200, v139, v200\n"
      "v_mul_f32 v202, v139, v202\n"
      "v_mfma_f32_16x16x32_bf16 v[144:147], a[244:247], v[168:171], v[144:147]\n"
      "v_mul_f32 v201, v139, v201\n"
      "v_cvt_pk_bf16_f32 v200, v200, v201\n"
      "v_mfma_f32_16x16x32_bf16 v[144:147], v[0:3], v[228:231], v[144:147]\n"
      "ds_read_b128 v[188:191], v138 offset:7040\n"
      "v_mul_f32_e64 v204, v139, v204\n"
      "v_mfma_f32_16x16x32_bf16 v[236:239], a[248:251], v[168:171], v[236:239]\n"
      "ds_read_b128 v[168:171], v138 offset:5632\n"
      "v_mul_f32 v203, v139, v203\n"
      "v_mfma_f32_16x16x32_bf16 v[236:239], v[4:7], v[228:231], v[236:239]\n"
      "v_mul_f32 v205, v139, v205\n"
      "v_cvt_pk_bf16_f32 v201, v202, v203\n"
      "v_mfma_f32_16x16x32_bf16 v[196:199], v[8:11], v[160:163], v[196:199]\n"
      "v_cvt_pk_bf16_f32 v202, v204, v205\n"
      "v_mul_f32_e64 v207, v139, v207\n"
      "v_mfma_f32_16x16x32_bf16 v[196:199], v[20:23], v[164:167], v[196:199]\n"
      "v_mul_f32_e64 v206, v139, v206\n"
      "v_cvt_pk_bf16_f32 v203, v206, v207\n"
      "v_mfma_f32_16x16x32_bf16 v[144:147], v[12:15], v[160:163], v[144:147]\n"
      "ds_read_b128 v[204:207], v138 offset:256\n"
      "v_mul_f32_e64 v208, v139, v208\n"
      "v_mfma_f32_16x16x32_bf16 v[144:147], v[24:27], v[164:167], v[144:147]\n"
      "ds_write_b128 v126, v[200:203] offset:4096\n"
      "v_mfma_f32_16x16x32_bf16 v[236:239], v[16:19], v[160:163], v[236:239]\n"
      "ds_read_b128 v[160:163], v138 offset:1664\n"
      "v_mul_f32 v209, v139, v209\n"
      "v_mfma_f32_16x16x32_bf16 v[236:239], v[28:31], v[164:167], v[236:239]\n"
      "s_waitcnt lgkmcnt(4)\n"
      "v_mul_f32 v211, v139, v211\n"
      "v_cvt_pk_bf16_f32 v208, v208, v209\n"
      "v_mfma_f32_16x16x32_bf16 v[164:167], a[216:219], v[172:175], 0\n"
      "v_mul_f32 v210, v139, v210\n"
      "v_cvt_pk_bf16_f32 v209, v210, v211\n"
      "v_mfma_f32_16x16x32_bf16 v[164:167], a[228:231], v[152:155], v[164:167]\n"
      "ds_write_b64 v127, v[208:209] offset:4096\n"
      "v_mfma_f32_16x16x32_bf16 v[208:211], a[220:223], v[172:175], 0\n"
      "ds_read_b128 v[200:203], v138 offset:3072\n"
      "v_fma_f32 v139, v220, v220, s39\n"
      "v_mfma_f32_16x16x32_bf16 v[208:211], a[232:235], v[152:155], v[208:211]\n"
      "ds_read_b128 v[228:231], v138 offset:4480\n"
      "v_fmac_f32_e64 v139, v221, v221\n"
      "v_mfma_f32_16x16x32_bf16 v[232:235], a[224:227], v[172:175], 0\n"
      "v_fmac_f32 v139, v222, v222\n"
      "v_fmac_f32 v139, v223, v223\n"
      "v_mfma_f32_16x16x32_bf16 v[232:235], a[236:239], v[152:155], v[232:235]\n"
      "v_fmac_f32 v139, v184, v184\n"
      "v_fmac_f32 v139, v185, v185\n"
      "v_mfma_f32_16x16x32_bf16 v[164:167], a[240:243], v[176:179], v[164:167]\n"
      "s_waitcnt lgkmcnt(3)\n"
      "v_fmac_f32 v139, v186, v186\n"
      "v_fmac_f32_e64 v139, v187, v187\n"
      "v_mfma_f32_16x16x32_bf16 v[164:167], a[252:255], v[180:183], v[164:167]\n"
      "ds_read_b128 v[152:155], v138 offset:5888\n"
      "v_fmac_f32 v139, v244, v244\n"
      "v_mfma_f32_16x16x32_bf16 v[208:211], a[244:247], v[176:179], v[208:211]\n"
      "ds_read_b128 v[172:175], v138 offset:7296\n"
      "v_fmac_f32 v139, v245, v245\n"
      "v_mfma_f32_16x16x32_bf16 v[208:211], v[0:3], v[180:183], v[208:211]\n"
      "v_fmac_f32 v139, v246, v246\n"
      "v_fmac_f32 v139, v247, v247\n"
      "v_mfma_f32_16x16x32_bf16 v[232:235], a[248:251], v[176:179], v[232:235]\n"
      "v_lshl_add_u32 v138, v137, 2, s29\n"
      "v_lshrrev_b32_e64 v176, 3, v137\n"
      "v_mfma_f32_16x16x32_bf16 v[232:235], v[4:7], v[180:183], v[232:235]\n"
      "ds_read_b32 v138, v138\n"
      "v_fma_f32 v137, v196, v196, s39\n"
      "v_mfma_f32_16x16x32_bf16 v[164:167], v[8:11], v[168:171], v[164:167]\n"
      "s_lshl_b32 s29, s24, 3\n"
      "v_fmac_f32 v137, v197, v197\n"
      "v_add_u32 v176, s29, v176\n"
      "v_mfma_f32_16x16x32_bf16 v[164:167], v[20:23], v[188:191], v[164:167]\n"
      "v_fmac_f32 v137, v198, v198\n"
      "v_cmp_gt_u32 s[48:49], s26, v176\n"
      "v_mfma_f32_16x16x32_bf16 v[208:211], v[12:15], v[168:171], v[208:211]\n"
      "v_fmac_f32 v137, v199, v199\n"
      "v_fmac_f32 v137, v144, v144\n"
      "v_mfma_f32_16x16x32_bf16 v[208:211], v[24:27], v[188:191], v[208:211]\n"
      "s_mov_b32 exec_hi, 0\n"
      "global_store_dwordx4 v118, v[156:159], s[54:55] sc1\n"
      "s_mov_b32 exec_hi, -1\n"
      "v_mfma_f32_16x16x32_bf16 v[232:235], v[16:19], v[168:171], v[232:235]\n"
      "v_fmac_f32 v137, v145, v145\n"
      "v_fmac_f32 v137, v146, v146\n"
      "v_mfma_f32_16x16x32_bf16 v[232:235], v[28:31], v[188:191], v[232:235]\n"
      "s_waitcnt lgkmcnt(1)\n"
      "v_fmac_f32 v137, v147, v147\n"
      "v_fmac_f32 v137, v236, v236\n"
      "v_mfma_f32_16x16x32_bf16 v[188:191], a[216:219], v[204:207], 0\n"
      "v_fmac_f32 v137, v237, v237\n"
      "v_fmac_f32 v137, v238, v238\n"
      "v_mfma_f32_16x16x32_bf16 v[188:191], a[228:231], v[160:163], v[188:191]\n"
      "v_fmac_f32 v137, v239, v239\n"
      "v_mov_b32 v156, v137\n"
      "v_mfma_f32_16x16x32_bf16 v[168:171], a[220:223], v[204:207], 0\n"
      "v_permlane16_swap_b32 v136, v139\n"
      "v_mfma_f32_16x16x32_bf16 v[168:171], a[232:235], v[160:163], v[168:171]\n"
      "v_permlane16_swap_b32 v137, v156\n"
      "v_add_f32 v136, v136, v139\n"
      "v_mfma_f32_16x16x32_bf16 v[176:179], a[224:227], v[204:207], 0\n"
      "v_fma_f32 v139, v164, v164, s39\n"
      "v_add_f32 v137, v137, v156\n"
      "v_mfma_f32_16x16x32_bf16 v[176:179], a[236:239], v[160:163], v[176:179]\n"
      "s_nop 0\n"
      "v_permlane32_swap_b32 v136, v137\n"
      "v_mfma_f32_16x16x32_bf16 v[188:191], a[240:243], v[200:203], v[188:191]\n"
      "v_fmac_f32 v139, v165, v165\n"
      "v_add_f32 v136, v136, v137\n"
      "v_mfma_f32_16x16x32_bf16 v[188:191], a[252:255], v[228:231], v[188:191]\n"
      "ds_write_b32 v129, v136\n"
      "v_mfma_f32_16x16x32_bf16 v[176:179], a[248:251], v[200:203], v[176:179]\n"
      "v_fmac_f32 v139, v166, v166\n"
      "v_fmac_f32 v139, v167, v167\n"
      "v_mfma_f32_16x16x32_bf16 v[176:179], v[4:7], v[228:231], v[176:179]\n"
      "v_mfma_f32_16x16x32_bf16 v[168:171], a[244:247], v[200:203], v[168:171]\n"
      "v_fmac_f32 v139, v208, v208\n"
      "v_fmac_f32_e64 v139, v209, v209\n"
      "v_mfma_f32_16x16x32_bf16 v[168:171], v[0:3], v[228:231], v[168:171]\n"
      "v_fmac_f32 v139, v210, v210\n"
      "v_mfma_f32_16x16x32_bf16 v[168:171], v[12:15], v[152:155], v[168:171]\n"
      "v_fmac_f32 v139, v211, v211\n"
      "v_fmac_f32_e64 v139, v232, v232\n"
      "v_mfma_f32_16x16x32_bf16 v[168:171], v[24:27], v[172:175], v[168:171]\n"
      "v_fmac_f32 v139, v233, v233\n"
      "v_mfma_f32_16x16x32_bf16 v[188:191], v[8:11], v[152:155], v[188:191]\n"
      "v_fmac_f32 v139, v234, v234\n"
      "v_mfma_f32_16x16x32_bf16 v[188:191], v[20:23], v[172:175], v[188:191]\n"
      "v_fmac_f32 v139, v235, v235\n"
      "v_mfma_f32_16x16x32_bf16 v[176:179], v[16:19], v[152:155], v[176:179]\n"
      "v_mfma_f32_16x16x32_bf16 v[176:179], v[28:31], v[172:175], v[176:179]\n"
      "s_nop 4\n"
      "v_pk_mul_f32 v[136:137], v[188:189], v[188:189]\n"
      "v_pk_fma_f32 v[136:137], v[190:191], v[190:191], v[136:137]\n"
      "v_pk_fma_f32 v[136:137], v[168:169], v[168:169], v[136:137]\n"
      "v_pk_fma_f32 v[136:137], v[170:171], v[170:171], v[136:137]\n"
      "v_pk_fma_f32 v[136:137], v[176:177], v[176:177], v[136:137]\n"
      "v_pk_fma_f32 v[136:137], v[178:179], v[178:179], v[136:137]\n"
      "v_add_f32 v136, s39, v136\n"
      "v_add_f32 v172, v136, v137\n"
      "v_mov_b32 v136, v172\n"
      "v_mov_b32 v137, v136\n"
      "v_permlane16_swap_b32 v139, v172\n"
      "v_add_f32 v139, v139, v172\n"
      "v_permlane16_swap_b32 v136, v137\n"
      "v_add_f32 v136, v136, v137\n"
      "s_nop 1\n"
      "v_permlane32_swap_b32 v139, v136\n"
      "v_add_f32_e64 v139, v139, v136\n"
      "ds_write_b32 v129, v139 offset:2048\n"
      "s_waitcnt lgkmcnt(0)\n"
      "s_barrier\n"
      "ds_read_b128 v[152:155], v128\n"
      "ds_read_b128 v[172:175], v128 offset:2048\n"
      "s_add_u32 s27, s27, 1\n"
      "s_sub_u32 s28, s27, 3\n"
      "s_cselect_b64 s[54:55], s[12:13], s[22:23]\n"
      "s_max_i32 m0, s28, 0\n"
      "ds_read_b128 v[160:163], v121\n"
      "ds_read_b128 v[200:203], v119 offset:6144\n"
      "s_movrels_b32 s9, s80\n"
      "s_lshr_b32 s8, s9, 8\n"
      "ds_read_b128 v[204:207], v119\n"
      "ds_read_b128 v[156:159], v120\n"
      "s_and_b32 s9, s9, 0xff\n"
      "s_mul_i32 s28, s8, 1373568\n"
      "s_mul_i32 s29, s9, 2688\n"
      "s_add_u32 s28, s28, s29\n"
      "s_cmp_eq_u32 s8, 72\n"
      "s_cselect_b32 s8, 0, 100270464\n"
      "s_cmp_eq_u32 s9, 72\n"
      "s_cselect_b32 s9, 0, 196224\n"
      "s_add_u32 s10, s8, s9\n"
      "s_add_u32 s28, s28, s10\n"
      "s_add_u32 s54, s54, s28\n"
      "s_addc_u32 s55, s55, 0\n"
      "s_cmp_lg_u32 s10, s11\n"
      "s_cbranch_scc1 L_edge_46_%=\n"
      "L_eb_47_%=:\n"
      "ds_read_b128 v[228:231], v119 offset:12288\n"
      "s_waitcnt lgkmcnt(5)\n"
      "v_add_f32 v152, |v152|, |v153|\n"
      "v_add_f32 v154, |v154|, |v155|\n"
      "v_add_f32 v152, v152, v154\n"
      "v_rsq_f32 v152, v152\n"
      "v_add_f32 v172, |v172|, |v173|\n"
      "v_mul_f32 v152, s41, v152\n"
      "v_mov_b32 v154, v152\n"
      "v_add_f32 v174, |v174|, |v175|\n"
      "v_subrev_u32 v173, 73728, v127\n"
      "v_permlane32_swap_b32 v152, v154\n"
      "v_mov_b32 v175, v152\n"
      "v_pk_mul_f32 v[198:199], v[198:199], v[154:155] op_sel_hi:[1,0]\n"
      "v_add_f32 v172, v172, v174\n"
      "v_permlane16_swap_b32 v152, v175\n"
      "v_pk_mul_f32 v[196:197], v[196:197], v[154:155] op_sel_hi:[1,0]\n"
      "v_pk_mul_f32 v[148:149], v[148:149], v[152:153] op_sel_hi:[1,0]\n"
      "v_pk_mul_f32 v[220:221], v[220:221], v[174:175] op_sel:[0,1]\n"
      "v_pk_mul_f32 v[186:187], v[186:187], v[174:175] op_sel:[0,1]\n"
      "v_pk_mul_f32 v[214:215], v[214:215], v[152:153] op_sel_hi:[1,0]\n"
      "v_rsq_f32_e64 v172, v172\n"
      "v_subrev_u32 v174, 73728, v126\n"
      "v_pk_mul_f32 v[184:185], v[184:185], v[174:175] op_sel:[0,1]\n"
      "v_pk_mul_f32 v[244:245], v[244:245], v[174:175] op_sel:[0,1]\n"
      "v_pk_mul_f32 v[236:237], v[236:237], v[154:155] op_sel_hi:[1,0]\n"
      "v_pk_mul_f32 v[246:247], v[246:247], v[174:175] op_sel:[0,1]\n"
      "v_pk_mul_f32 v[222:223], v[222:223], v[174:175] op_sel:[0,1]\n"
      "v_cvt_pk_bf16_f32 v196, v196, v197\n"
      "v_cvt_pk_bf16_f32 v197, v198, v199\n"
      "v_cvt_pk_bf16_f32 v244, v244, v245\n"
      "v_mul_f32_e64 v172, s41, v172\n"
      "v_mov_b32_e64 v175, v172\n"
      "v_pk_mul_f32 v[238:239], v[238:239], v[154:155] op_sel_hi:[1,0]\n"
      "v_cvt_pk_bf16_f32 v220, v220, v221\n"
      "v_cvt_pk_bf16_f32 v148, v148, v149\n"
      "v_pk_mul_f32 v[150:151], v[150:151], v[152:153] op_sel_hi:[1,0]\n"
      "v_cvt_pk_bf16_f32 v221, v222, v223\n"
      "v_permlane32_swap_b32_e64 v172, v175\n"
      "v_cvt_pk_bf16_f32 v149, v150, v151\n"
      "v_cvt_pk_bf16_f32 v223, v186, v187\n"
      "v_mov_b32_e64 v175, v172\n"
      "v_pk_mul_f32 v[146:147], v[146:147], v[154:155] op_sel_hi:[1,0]\n"
      "v_pk_mul_f32 v[216:217], v[216:217], v[152:153] op_sel_hi:[1,0]\n"
      "v_cvt_pk_bf16_f32 v199, v146, v147\n"
      "s_waitcnt lgkmcnt(0)\n"
      "v_permlane16_swap_b32 v172, v175\n"
      "v_pk_mul_f32 v[208:209], v[208:209], v[172:173] op_sel_hi:[1,0]\n"
      "v_pk_mul_f32 v[210:211], v[210:211], v[172:173] op_sel_hi:[1,0]\n"
      "v_pk_mul_f32 v[212:213], v[212:213], v[152:153] op_sel_hi:[1,0]\n"
      "v_pk_mul_f32 v[188:189], v[188:189], v[174:175] op_sel:[0,1]\n"
      "v_pk_mul_f32 v[164:165], v[164:165], v[172:173] op_sel_hi:[1,0]\n"
      "v_pk_mul_f32 v[170:171], v[170:171], v[174:175] op_sel:[0,1]\n"
      "v_pk_mul_f32 v[176:177], v[176:177], v[174:175] op_sel:[0,1]\n"
      "v_pk_mul_f32 v[166:167], v[166:167], v[172:173] op_sel_hi:[1,0]\n"
      "v_cvt_pk_bf16_f32 v176, v176, v177\n"
      "v_pk_mul_f32 v[232:233], v[232:233], v[172:173] op_sel_hi:[1,0]\n"
      "v_pk_mul_f32 v[218:219], v[218:219], v[152:153] op_sel_hi:[1,0]\n"
      "v_cvt_pk_bf16_f32 v232, v232, v233\n"
      "v_cvt_pk_bf16_f32 v164, v164, v165\n"
      "v_cvt_pk_bf16_f32 v245, v246, v247\n"
      "v_pk_mul_f32 v[234:235], v[234:235], v[172:173] op_sel_hi:[1,0]\n"
      "v_cvt_pk_bf16_f32 v212, v212, v213\n"
      "v_pk_mul_f32 v[168:169], v[168:169], v[174:175] op_sel:[0,1]\n"
      "v_cvt_pk_bf16_f32 v222, v184, v185\n"
      "v_cvt_pk_bf16_f32 v165, v166, v167\n"
      "v_pk_mul_f32 v[178:179], v[178:179], v[174:175] op_sel:[0,1]\n"
      "v_cvt_pk_bf16_f32 v213, v214, v215\n"
      "v_pk_mul_f32 v[144:145], v[144:145], v[154:155] op_sel_hi:[1,0]\n"
      "v_cvt_pk_bf16_f32 v167, v210, v211\n"
      "ds_write_b64 v127, v[212:213] offset:18432\n"
      "global_store_dwordx4 v116, v[204:207], s[54:55] sc1\n"
      "global_store_dwordx4 v116, v[200:203], s[54:55] offset:128 sc1\n"
      "v_cvt_pk_bf16_f32 v150, v216, v217\n"
      "v_cvt_pk_bf16_f32 v151, v218, v219\n"
      "v_cvt_pk_bf16_f32 v198, v144, v145\n"
      "v_pk_mul_f32 v[190:191], v[190:191], v[174:175] op_sel:[0,1]\n"
      "v_cvt_pk_bf16_f32 v166, v208, v209\n"
      "v_cvt_pk_bf16_f32 v188, v188, v189\n"
      "v_cvt_pk_bf16_f32 v233, v234, v235\n"
      "ds_write_b128 v174, v[164:167] offset:18432\n"
      "ds_write_b64 v173, v[232:233] offset:18432\n"
      "ds_write_b64 v127, v[244:245] offset:20480\n"
      "global_store_dwordx4 v116, v[228:231], s[54:55] offset:256 sc1\n"
      "global_store_dwordx4 v117, v[156:159], s[54:55] sc1\n"
      "ds_write_b128 v126, v[148:151] offset:18432\n"
      "ds_write_b128 v126, v[196:199] offset:22528\n"
      "v_cvt_pk_bf16_f32 v236, v236, v237\n"
      "v_cvt_pk_bf16_f32 v189, v190, v191\n"
      "v_cvt_pk_bf16_f32 v237, v238, v239\n"
      "v_cvt_pk_bf16_f32 v191, v170, v171\n"
      "v_cvt_pk_bf16_f32 v177, v178, v179\n"
      "v_cvt_pk_bf16_f32 v190, v168, v169\n"
      "s_mov_b32 exec_hi, 0\n"
      "global_store_dwordx4 v118, v[160:163], s[54:55] sc1\n"
      "s_mov_b32 exec_hi, -1\n"
      "ds_write_b128 v126, v[220:223] offset:20480\n"
      "ds_write_b128 v174, v[188:191] offset:20480\n"
      "ds_write_b64 v127, v[236:237] offset:22528\n"
      "ds_write_b64 v173, v[176:177] offset:20480\n"
      "s_waitcnt lgkmcnt(0)\n"
      "s_barrier\n"
      "ds_read_b128 v[188:191], v121 offset:18432\n"
      "ds_read_b128 v[164:167], v119 offset:24576\n"
      "s_add_u32 s27, s27, 1\n"
      "ds_read_b128 v[168:171], v119 offset:30720\n"
      "ds_read_b128 v[160:163], v119 offset:18432\n"
      "v_subrev_u32 v139, 73728, v119\n"
      "s_sub_u32 s28, s27, 3\n"
      "ds_read_b128 v[232:235], v139 offset:18432\n"
      "ds_read_b128 v[176:179], v120 offset:18432\n"
      "s_cselect_b64 s[54:55], s[12:13], s[22:23]\n"
      "s_max_i32 m0, s28, 0\n"
      "ds_read_b128 v[208:211], v139 offset:24576\n"
      "s_movrels_b32 s9, s80\n"
      "s_lshr_b32 s8, s9, 8\n"
      "s_and_b32 s9, s9, 0xff\n"
      "s_mul_i32 s28, s8, 1373568\n"
      "s_mul_i32 s29, s9, 2688\n"
      "s_add_u32 s28, s28, s29\n"
      "s_cmp_eq_u32 s8, 72\n"
      "s_cselect_b32 s8, 0, 100270464\n"
      "ds_read_b128 v[156:159], v139 offset:30720\n"
      "s_cmp_eq_u32 s9, 72\n"
      "s_cselect_b32 s9, 0, 196224\n"
      "s_add_u32 s10, s8, s9\n"
      "s_add_u32 s28, s28, s10\n"
      "s_add_u32 s54, s54, s28\n"
      "s_addc_u32 s55, s55, 0\n"
      "s_cmp_lg_u32 s10, s11\n"
      "s_cbranch_scc1 L_edge_48_%=\n"
      "L_eb_49_%=:\n"
      "s_waitcnt lgkmcnt(4)\n"
      "global_store_dwordx4 v116, v[160:163], s[54:55] sc1\n"
      "global_store_dwordx4 v116, v[164:167], s[54:55] offset:128 sc1\n"
      "global_store_dwordx4 v116, v[168:171], s[54:55] offset:256 sc1\n"
      "s_waitcnt lgkmcnt(0)\n"
      "global_store_dwordx4 v117, v[176:179], s[54:55] sc1\n"
      "s_mov_b32 exec_hi, 0\n"
      "global_store_dwordx4 v118, v[188:191], s[54:55] sc1\n"
      "s_mov_b32 exec_hi, -1\n"
      "s_nop 0\n"
      "s_mov_b64 exec, s[48:49]\n"
      "s_cbranch_execz L_np_50_%=\n"
      "global_store_dwordx4 v138, v[232:235], s[22:23] sc1\n"
      "global_store_dwordx4 v138, v[208:211], s[22:23] offset:128 sc1\n"
      "global_store_dwordx4 v138, v[156:159], s[22:23] offset:256 sc1\n"
      "L_np_50_%=:\n"
      "s_mov_b64 exec, -1\n"
      "s_endpgm\n"
      "L_gen_8_%=:\n"
      "s_waitcnt lgkmcnt(0)\n"
      "s_nop 5\n"
      "s_cmp_eq_u64 s[34:35], 0\n"
      "s_cbranch_scc1 L_ok_65_%=\n"
      "ds_read_b128 v[136:139], v114\n"
      "ds_read_b128 v[140:143], v115\n"
      "ds_read_b128 v[172:175], v114 offset:6144\n"
      "ds_read_b128 v[168:171], v115 offset:6144\n"
      "s_waitcnt lgkmcnt(2)\n"
      "v_mfma_f32_16x16x32_bf16 v[204:207], a[0:3], v[136:139], 0\n"
      "s_nop 0\n"
      "v_mfma_f32_16x16x32_bf16 v[152:155], a[4:7], v[136:139], 0\n"
      "ds_read_b128 v[180:183], v114 offset:12288\n"
      "v_mfma_f32_16x16x32_bf16 v[152:155], a[40:43], v[140:143], v[152:155]\n"
      "s_waitcnt lgkmcnt(1)\n"
      "ds_read_b128 v[196:199], v115 offset:12288\n"
      "v_mfma_f32_16x16x32_bf16 v[152:155], a[76:79], v[172:175], v[152:155]\n"
      "s_nop 0\n"
      "v_mfma_f32_16x16x32_bf16 v[152:155], a[112:115], v[168:171], v[152:155]\n"
      "v_mfma_f32_16x16x32_bf16 v[216:219], a[20:23], v[136:139], 0\n"
      "v_mfma_f32_16x16x32_bf16 v[200:203], a[8:11], v[136:139], 0\n"
      "v_mfma_f32_16x16x32_bf16 v[200:203], a[44:47], v[140:143], v[200:203]\n"
      "v_mfma_f32_16x16x32_bf16 v[164:167], a[12:15], v[136:139], 0\n"
      "v_mfma_f32_16x16x32_bf16 v[164:167], a[48:51], v[140:143], v[164:167]\n"
      "v_mfma_f32_16x16x32_bf16 v[160:163], a[16:19], v[136:139], 0\n"
      "v_mfma_f32_16x16x32_bf16 v[160:163], a[52:55], v[140:143], v[160:163]\n"
      "v_mfma_f32_16x16x32_bf16 v[204:207], a[36:39], v[140:143], v[204:207]\n"
      "v_mfma_f32_16x16x32_bf16 v[204:207], a[72:75], v[172:175], v[204:207]\n"
      "v_mfma_f32_16x16x32_bf16 v[204:207], a[108:111], v[168:171], v[204:207]\n"
      "v_mfma_f32_16x16x32_bf16 v[164:167], a[84:87], v[172:175], v[164:167]\n"
      "s_waitcnt lgkmcnt(0)\n"
      "ds_read_b128 v[136:139], v115 offset:2048\n"
      "v_mfma_f32_16x16x32_bf16 v[164:167], a[120:123], v[168:171], v[164:167]\n"
      "v_mfma_f32_16x16x32_bf16 v[160:163], a[88:91], v[172:175], v[160:163]\n"
      "s_nop 0\n"
      "v_mfma_f32_16x16x32_bf16 v[160:163], a[124:127], v[168:171], v[160:163]\n"
      "v_mfma_f32_16x16x32_bf16 v[200:203], a[80:83], v[172:175], v[200:203]\n"
      "v_mfma_f32_16x16x32_bf16 v[200:203], a[116:119], v[168:171], v[200:203]\n"
      "v_mfma_f32_16x16x32_bf16 v[216:219], a[56:59], v[140:143], v[216:219]\n"
      "v_mfma_f32_16x16x32_bf16 v[216:219], a[92:95], v[172:175], v[216:219]\n"
      "v_mfma_f32_16x16x32_bf16 v[216:219], a[128:131], v[168:171], v[216:219]\n"
      "v_mfma_f32_16x16x32_bf16 v[204:207], a[144:147], v[180:183], v[204:207]\n"
      "v_mfma_f32_16x16x32_bf16 v[204:207], a[180:183], v[196:199], v[204:207]\n"
      "v_mfma_f32_16x16x32_bf16 v[200:203], a[152:155], v[180:183], v[200:203]\n"
      "v_mfma_f32_16x16x32_bf16 v[200:203], a[188:191], v[196:199], v[200:203]\n"
      "v_mfma_f32_16x16x32_bf16 v[152:155], a[148:151], v[180:183], v[152:155]\n"
      "v_mfma_f32_16x16x32_bf16 v[152:155], a[184:187], v[196:199], v[152:155]\n"
      "v_mfma_f32_16x16x32_bf16 v[164:167], a[156:159], v[180:183], v[164:167]\n"
      "s_nop 0\n"
      "v_mfma_f32_16x16x32_bf16 v[164:167], a[192:195], v[196:199], v[164:167]\n"
      "s_nop 1\n"
      "v_fma_f32 v140, v204, v204, s38\n"
      "v_mfma_f32_16x16x32_bf16 v[216:219], a[164:167], v[180:183], v[216:219]\n"
      "v_fmac_f32 v140, v205, v205\n"
      "v_fmac_f32_e64 v140, v206, v206\n"
      "v_mfma_f32_16x16x32_bf16 v[216:219], a[200:203], v[196:199], v[216:219]\n"
      "s_nop 0\n"
      "v_fma_f32 v141, v164, v164, s38\n"
      "v_fmac_f32 v141, v165, v165\n"
      "v_mfma_f32_16x16x32_bf16 v[160:163], a[160:163], v[180:183], v[160:163]\n"
      "v_fmac_f32 v141, v166, v166\n"
      "v_fmac_f32_e64 v140, v207, v207\n"
      "v_mfma_f32_16x16x32_bf16 v[160:163], a[196:199], v[196:199], v[160:163]\n"
      "v_fmac_f32 v140, v152, v152\n"
      "v_fmac_f32 v140, v153, v153\n"
      "v_fmac_f32 v141, v167, v167\n"
      "v_fmac_f32 v140, v154, v154\n"
      "v_fmac_f32 v140, v155, v155\n"
      "v_fmac_f32 v140, v200, v200\n"
      "v_fmac_f32 v140, v201, v201\n"
      "s_nop 0\n"
      "v_fmac_f32 v141, v160, v160\n"
      "v_fmac_f32 v141, v161, v161\n"
      "v_fmac_f32 v141, v162, v162\n"
      "v_fmac_f32 v141, v163, v163\n"
      "v_fmac_f32 v141, v216, v216\n"
      "v_fmac_f32 v141, v217, v217\n"
      "v_fmac_f32 v141, v218, v218\n"
      "v_fmac_f32 v140, v202, v202\n"
      "v_fmac_f32 v141, v219, v219\n"
      "v_fmac_f32 v140, v203, v203\n"
      "s_nop 1\n"
      "v_permlane16_swap_b32 v140, v141\n"
      "v_add_f32 v140, v140, v141\n"
      "v_mov_b32 v141, v140\n"
      "s_nop 1\n"
      "v_permlane32_swap_b32 v140, v141\n"
      "v_add_f32 v140, v140, v141\n"
      "v_rsq_f32 v140, v140\n"
      "s_nop 0\n"
      "v_mov_b32 v141, v140\n"
      "s_nop 1\n"
      "v_permlane16_swap_b32 v140, v141\n"
      "v_mul_f32 v162, v141, v162\n"
      "v_mul_f32 v140, s36, v140\n"
      "v_mul_f32 v167, v141, v167\n"
      "v_mul_f32 v166, v141, v166\n"
      "v_mul_f32 v164, v141, v164\n"
      "v_mul_f32 v200, v140, v200\n"
      "v_mul_f32 v165, v141, v165\n"
      "v_mul_f32 v202, v140, v202\n"
      "v_mul_f32 v203, v140, v203\n"
      "v_mul_f32 v216, v141, v216\n"
      "v_mul_f32 v217, v141, v217\n"
      "v_mul_f32 v219, v141, v219\n"
      "v_mul_f32 v205, v140, v205\n"
      "v_cvt_pk_f16_f32 v34, v216, v217\n"
      "v_mul_f32 v207, v140, v207\n"
      "v_mul_f32 v161, v141, v161\n"
      "v_mul_f32 v160, v141, v160\n"
      "v_cvt_pk_f16_f32 v164, v164, v165\n"
      "v_mul_f32 v204, v140, v204\n"
      "v_mul_f32 v206, v140, v206\n"
      "v_mul_f32 v153, v140, v153\n"
      "v_mul_f32 v155, v140, v155\n"
      "v_cvt_pk_f16_f32 v165, v166, v167\n"
      "v_mul_f32 v218, v141, v218\n"
      "v_mul_f32 v201, v140, v201\n"
      "v_mul_f32 v163, v141, v163\n"
      "v_mul_f32 v154, v140, v154\n"
      "v_mul_f32_e64 v152, v140, v152\n"
      "v_cvt_pk_f16_f32 v204, v204, v205\n"
      "v_cvt_pk_f16_f32 v205, v206, v207\n"
      "ds_read_b128 v[140:143], v114 offset:2048\n"
      "v_cvt_pk_f16_f32 v167, v162, v163\n"
      "ds_read_b128 v[168:171], v114 offset:8192\n"
      "ds_read_b128 v[172:175], v115 offset:8192\n"
      "v_cvt_pk_f16_f32 v166, v160, v161\n"
      "v_cvt_pk_f16_f32 v206, v152, v153\n"
      "v_cvt_pk_f16_f32 v33, v202, v203\n"
      "v_cvt_pk_f16_f32 v35, v218, v219\n"
      "ds_read_b128 v[216:219], v115 offset:14336\n"
      "v_cvt_pk_f16_f32 v32, v200, v201\n"
      "ds_read_b128 v[196:199], v114 offset:14336\n"
      "s_waitcnt lgkmcnt(4)\n"
      "v_mfma_f32_16x16x32_bf16 v[148:151], a[0:3], v[140:143], 0\n"
      "v_mfma_f32_16x16x32_bf16 v[148:151], a[36:39], v[136:139], v[148:151]\n"
      "s_waitcnt lgkmcnt(2)\n"
      "v_mfma_f32_16x16x32_bf16 v[184:187], a[12:15], v[140:143], 0\n"
      "v_mfma_f32_16x16x32_bf16 v[184:187], a[48:51], v[136:139], v[184:187]\n"
      "v_mfma_f32_16x16x32_bf16 v[200:203], a[8:11], v[140:143], 0\n"
      "v_mfma_f32_16x16x32_bf16 v[200:203], a[44:47], v[136:139], v[200:203]\n"
      "s_waitcnt lgkmcnt(0)\n"
      "v_cvt_pk_f16_f32 v207, v154, v155\n"
      "v_mfma_f32_16x16x32_bf16 v[160:163], a[16:19], v[140:143], 0\n"
      "v_mfma_f32_16x16x32_bf16 v[160:163], a[52:55], v[136:139], v[160:163]\n"
      "s_nop 0\n"
      "v_mfma_f32_16x16x32_bf16 v[180:183], a[20:23], v[140:143], 0\n"
      "v_mfma_f32_16x16x32_bf16 v[180:183], a[56:59], v[136:139], v[180:183]\n"
      "v_mfma_f32_16x16x32_bf16 v[152:155], a[4:7], v[140:143], 0\n"
      "v_mfma_f32_16x16x32_bf16 v[152:155], a[40:43], v[136:139], v[152:155]\n"
      "v_mfma_f32_16x16x32_bf16 v[152:155], a[76:79], v[168:171], v[152:155]\n"
      "v_mfma_f32_16x16x32_bf16 v[152:155], a[112:115], v[172:175], v[152:155]\n"
      "v_mfma_f32_16x16x32_bf16 v[184:187], a[84:87], v[168:171], v[184:187]\n"
      "v_mfma_f32_16x16x32_bf16 v[184:187], a[120:123], v[172:175], v[184:187]\n"
      "v_mfma_f32_16x16x32_bf16 v[148:151], a[72:75], v[168:171], v[148:151]\n"
      "v_mfma_f32_16x16x32_bf16 v[148:151], a[108:111], v[172:175], v[148:151]\n"
      "v_mfma_f32_16x16x32_bf16 v[200:203], a[80:83], v[168:171], v[200:203]\n"
      "v_mfma_f32_16x16x32_bf16 v[200:203], a[116:119], v[172:175], v[200:203]\n"
      "v_mfma_f32_16x16x32_bf16 v[180:183], a[92:95], v[168:171], v[180:183]\n"
      "v_mfma_f32_16x16x32_bf16 v[180:183], a[128:131], v[172:175], v[180:183]\n"
      "v_mfma_f32_16x16x32_bf16 v[160:163], a[88:91], v[168:171], v[160:163]\n"
      "v_mfma_f32_16x16x32_bf16 v[160:163], a[124:127], v[172:175], v[160:163]\n"
      "v_mfma_f32_16x16x32_bf16 v[148:151], a[144:147], v[196:199], v[148:151]\n"
      "v_mfma_f32_16x16x32_bf16 v[148:151], a[180:183], v[216:219], v[148:151]\n"
      "v_mfma_f32_16x16x32_bf16 v[152:155], a[148:151], v[196:199], v[152:155]\n"
      "v_mfma_f32_16x16x32_bf16 v[152:155], a[184:187], v[216:219], v[152:155]\n"
      "v_mfma_f32_16x16x32_bf16 v[200:203], a[152:155], v[196:199], v[200:203]\n"
      "v_mfma_f32_16x16x32_bf16 v[200:203], a[188:191], v[216:219], v[200:203]\n"
      "v_mfma_f32_16x16x32_bf16 v[160:163], a[160:163], v[196:199], v[160:163]\n"
      "ds_read_b128 v[172:175], v115 offset:4096\n"
      "v_mfma_f32_16x16x32_bf16 v[160:163], a[196:199], v[216:219], v[160:163]\n"
      "s_nop 0\n"
      "v_fma_f32 v140, v148, v148, s38\n"
      "v_mfma_f32_16x16x32_bf16 v[184:187], a[156:159], v[196:199], v[184:187]\n"
      "v_fmac_f32 v140, v149, v149\n"
      "v_fmac_f32 v140, v150, v150\n"
      "v_mfma_f32_16x16x32_bf16 v[184:187], a[192:195], v[216:219], v[184:187]\n"
      "v_fmac_f32 v140, v151, v151\n"
      "v_fmac_f32_e64 v140, v152, v152\n"
      "v_mfma_f32_16x16x32_bf16 v[180:183], a[164:167], v[196:199], v[180:183]\n"
      "ds_read_b128 v[136:139], v114 offset:4096\n"
      "v_fmac_f32 v140, v153, v153\n"
      "v_mfma_f32_16x16x32_bf16 v[180:183], a[200:203], v[216:219], v[180:183]\n"
      "v_fmac_f32 v140, v154, v154\n"
      "v_fmac_f32_e64 v140, v155, v155\n"
      "v_fma_f32 v141, v184, v184, s38\n"
      "v_fmac_f32 v141, v185, v185\n"
      "v_fmac_f32 v141, v186, v186\n"
      "v_fmac_f32 v140, v200, v200\n"
      "v_fmac_f32 v141, v187, v187\n"
      "v_fmac_f32 v141, v160, v160\n"
      "v_fmac_f32 v141, v161, v161\n"
      "v_fmac_f32 v141, v162, v162\n"
      "v_fmac_f32 v140, v201, v201\n"
      "v_fmac_f32 v141, v163, v163\n"
      "v_fmac_f32 v140, v202, v202\n"
      "v_fmac_f32 v141, v180, v180\n"
      "v_fmac_f32 v141, v181, v181\n"
      "v_fmac_f32 v141, v182, v182\n"
      "v_fmac_f32 v141, v183, v183\n"
      "v_fmac_f32 v140, v203, v203\n"
      "s_nop 1\n"
      "v_permlane16_swap_b32 v140, v141\n"
      "v_add_f32 v140, v140, v141\n"
      "v_mov_b32 v141, v140\n"
      "s_nop 1\n"
      "v_permlane32_swap_b32 v140, v141\n"
      "v_add_f32 v140, v140, v141\n"
      "v_rsq_f32 v140, v140\n"
      "s_waitcnt lgkmcnt(0)\n"
      "v_mfma_f32_16x16x32_bf16 v[156:159], a[0:3], v[136:139], 0\n"
      "ds_read_b128 v[168:171], v114 offset:10240\n"
      "v_mov_b32_e64 v141, v140\n"
      "v_mfma_f32_16x16x32_bf16 v[156:159], a[36:39], v[172:175], v[156:159]\n"
      "ds_read_b128 v[216:219], v115 offset:10240\n"
      "v_permlane16_swap_b32 v140, v141\n"
      "v_mul_f32 v162, v141, v162\n"
      "v_mfma_f32_16x16x32_bf16 v[192:195], a[12:15], v[136:139], 0\n"
      "ds_read_b128 v[196:199], v115 offset:16384\n"
      "v_mul_f32 v185, v141, v185\n"
      "v_mfma_f32_16x16x32_bf16 v[192:195], a[48:51], v[172:175], v[192:195]\n"
      "ds_read_b128 v[224:227], v114 offset:16384\n"
      "v_mul_f32 v184, v141, v184\n"
      "v_mfma_f32_16x16x32_bf16 v[232:235], a[20:23], v[136:139], 0\n"
      "v_mul_f32 v186, v141, v186\n"
      "v_cvt_pk_f16_f32 v184, v184, v185\n"
      "v_mfma_f32_16x16x32_bf16 v[232:235], a[56:59], v[172:175], v[232:235]\n"
      "v_mul_f32 v187, v141, v187\n"
      "v_mul_f32 v163, v141, v163\n"
      "v_mul_f32 v181, v141, v181\n"
      "v_cvt_pk_f16_f32 v185, v186, v187\n"
      "v_mul_f32 v160, v141, v160\n"
      "v_mul_f32 v161, v141, v161\n"
      "v_cvt_pk_f16_f32 v186, v160, v161\n"
      "v_cvt_pk_f16_f32 v187, v162, v163\n"
      "v_mfma_f32_16x16x32_bf16 v[160:163], a[16:19], v[136:139], 0\n"
      "v_mul_f32 v183, v141, v183\n"
      "v_mul_f32 v180, v141, v180\n"
      "v_mfma_f32_16x16x32_bf16 v[160:163], a[52:55], v[172:175], v[160:163]\n"
      "v_cvt_pk_f16_f32 v40, v180, v181\n"
      "s_waitcnt lgkmcnt(2)\n"
      "v_mfma_f32_16x16x32_bf16 v[156:159], a[72:75], v[168:171], v[156:159]\n"
      "v_mul_f32 v182, v141, v182\n"
      "v_cvt_pk_f16_f32 v41, v182, v183\n"
      "v_mfma_f32_16x16x32_bf16 v[156:159], a[108:111], v[216:219], v[156:159]\n"
      "v_mul_f32 v140, s36, v140\n"
      "v_mul_f32 v149, v140, v149\n"
      "v_mfma_f32_16x16x32_bf16 v[160:163], a[88:91], v[168:171], v[160:163]\n"
      "v_mul_f32 v148, v140, v148\n"
      "v_mul_f32 v152, v140, v152\n"
      "v_mfma_f32_16x16x32_bf16 v[160:163], a[124:127], v[216:219], v[160:163]\n"
      "v_cvt_pk_f16_f32 v148, v148, v149\n"
      "v_mul_f32 v150, v140, v150\n"
      "v_mfma_f32_16x16x32_bf16 v[192:195], a[84:87], v[168:171], v[192:195]\n"
      "v_mul_f32 v151, v140, v151\n"
      "v_mul_f32 v155, v140, v155\n"
      "v_mfma_f32_16x16x32_bf16 v[192:195], a[120:123], v[216:219], v[192:195]\n"
      "v_mul_f32 v154, v140, v154\n"
      "v_cvt_pk_f16_f32 v149, v150, v151\n"
      "v_mfma_f32_16x16x32_bf16 v[232:235], a[92:95], v[168:171], v[232:235]\n"
      "v_cvt_pk_f16_f32 v151, v154, v155\n"
      "v_mul_f32 v200, v140, v200\n"
      "v_mfma_f32_16x16x32_bf16 v[232:235], a[128:131], v[216:219], v[232:235]\n"
      "v_mul_f32_e64 v153, v140, v153\n"
      "s_waitcnt lgkmcnt(0)\n"
      "v_cvt_pk_f16_f32 v150, v152, v153\n"
      "v_mfma_f32_16x16x32_bf16 v[152:155], a[4:7], v[136:139], 0\n"
      "v_mul_f32 v203, v140, v203\n"
      "v_mfma_f32_16x16x32_bf16 v[152:155], a[40:43], v[172:175], v[152:155]\n"
      "v_mul_f32 v202, v140, v202\n"
      "v_mfma_f32_16x16x32_bf16 v[152:155], a[76:79], v[168:171], v[152:155]\n"
      "v_mfma_f32_16x16x32_bf16 v[152:155], a[112:115], v[216:219], v[152:155]\n"
      "v_mul_f32_e64 v201, v140, v201\n"
      "v_cvt_pk_f16_f32 v39, v202, v203\n"
      "v_mfma_f32_16x16x32_bf16 v[152:155], a[148:151], v[224:227], v[152:155]\n"
      "v_cvt_pk_f16_f32 v38, v200, v201\n"
      "v_mfma_f32_16x16x32_bf16 v[152:155], a[184:187], v[196:199], v[152:155]\n"
      "v_mfma_f32_16x16x32_bf16 v[200:203], a[8:11], v[136:139], 0\n"
      "v_mfma_f32_16x16x32_bf16 v[200:203], a[44:47], v[172:175], v[200:203]\n"
      "v_mfma_f32_16x16x32_bf16 v[200:203], a[80:83], v[168:171], v[200:203]\n"
      "v_mfma_f32_16x16x32_bf16 v[156:159], a[144:147], v[224:227], v[156:159]\n"
      "v_mfma_f32_16x16x32_bf16 v[156:159], a[180:183], v[196:199], v[156:159]\n"
      "v_mfma_f32_16x16x32_bf16 v[192:195], a[156:159], v[224:227], v[192:195]\n"
      "v_mfma_f32_16x16x32_bf16 v[192:195], a[192:195], v[196:199], v[192:195]\n"
      "v_mfma_f32_16x16x32_bf16 v[160:163], a[160:163], v[224:227], v[160:163]\n"
      "v_mfma_f32_16x16x32_bf16 v[160:163], a[196:199], v[196:199], v[160:163]\n"
      "v_mfma_f32_16x16x32_bf16 v[200:203], a[116:119], v[216:219], v[200:203]\n"
      "v_mfma_f32_16x16x32_bf16 v[200:203], a[152:155], v[224:227], v[200:203]\n"
      "v_mfma_f32_16x16x32_bf16 v[200:203], a[188:191], v[196:199], v[200:203]\n"
      "s_nop 0\n"
      "v_fma_f32 v140, v156, v156, s38\n"
      "v_mfma_f32_16x16x32_bf16 v[232:235], a[164:167], v[224:227], v[232:235]\n"
      "v_fma_f32 v141, v192, v192, s38\n"
      "v_fmac_f32 v141, v193, v193\n"
      "v_mfma_f32_16x16x32_bf16 v[232:235], a[200:203], v[196:199], v[232:235]\n"
      "v_fmac_f32 v141, v194, v194\n"
      "v_fmac_f32 v140, v157, v157\n"
      "v_fmac_f32 v141, v195, v195\n"
      "v_fmac_f32 v140, v158, v158\n"
      "v_mfma_f32_16x16x32_f16 v[136:139], v[144:147], v[204:207], v[70:73]\n"
      "v_fmac_f32 v140, v159, v159\n"
      "v_fmac_f32 v140, v152, v152\n"
      "v_mfma_f32_16x16x32_f16 v[136:139], v[176:179], v[164:167], v[136:139]\n"
      "v_fmac_f32 v140, v153, v153\n"
      "v_fmac_f32 v141, v160, v160\n"
      "v_mfma_f32_16x16x32_f16 v[136:139], v[228:231], v[32:35], v[136:139]\n"
      "v_fmac_f32 v141, v161, v161\n"
      "v_fmac_f32 v141, v162, v162\n"
      "v_fmac_f32 v141, v163, v163\n"
      "v_fmac_f32 v141, v232, v232\n"
      "v_fmac_f32 v141, v233, v233\n"
      "v_fmac_f32 v141, v234, v234\n"
      "v_fmac_f32 v140, v154, v154\n"
      "v_fmac_f32 v140, v155, v155\n"
      "v_fmac_f32 v141, v235, v235\n"
      "v_fmac_f32 v140, v200, v200\n"
      "v_fmac_f32 v140, v201, v201\n"
      "v_fmac_f32 v140, v202, v202\n"
      "v_fmac_f32 v140, v203, v203\n"
      "v_mov_b32 v212, v136\n"
      "v_mov_b32 v213, v137\n"
      "v_permlane16_swap_b32 v140, v141\n"
      "v_add_f32 v140, v140, v141\n"
      "v_mov_b32 v141, v140\n"
      "s_nop 1\n"
      "v_permlane32_swap_b32 v140, v141\n"
      "v_add_f32 v140, v140, v141\n"
      "v_rsq_f32 v140, v140\n"
      "s_nop 0\n"
      "v_mov_b32 v141, v140\n"
      "s_nop 1\n"
      "v_permlane16_swap_b32 v140, v141\n"
      "v_mul_f32 v232, v141, v232\n"
      "v_mul_f32 v140, s36, v140\n"
      "v_mul_f32 v160, v141, v160\n"
      "v_mul_f32 v193, v141, v193\n"
      "v_mul_f32 v159, v140, v159\n"
      "v_mul_f32 v192, v141, v192\n"
      "v_mul_f32 v156, v140, v156\n"
      "v_mul_f32 v158, v140, v158\n"
      "v_mul_f32 v161, v141, v161\n"
      "v_mul_f32 v163, v141, v163\n"
      "v_mul_f32 v233, v141, v233\n"
      "v_mul_f32 v162, v141, v162\n"
      "v_cvt_pk_f16_f32 v192, v192, v193\n"
      "v_mul_f32 v157, v140, v157\n"
      "v_mul_f32 v155, v140, v155\n"
      "v_mul_f32 v194, v141, v194\n"
      "v_mul_f32 v234, v141, v234\n"
      "v_cvt_pk_f16_f32 v46, v232, v233\n"
      "v_mul_f32 v195, v141, v195\n"
      "v_cvt_pk_f16_f32 v193, v194, v195\n"
      "v_mul_f32 v235, v141, v235\n"
      "v_cvt_pk_f16_f32 v195, v162, v163\n"
      "v_mul_f32 v154, v140, v154\n"
      "v_mul_f32 v152, v140, v152\n"
      "v_cvt_pk_f16_f32 v194, v160, v161\n"
      "v_cvt_pk_f16_f32 v47, v234, v235\n"
      "v_mul_f32_e64 v200, v140, v200\n"
      "v_mfma_f32_16x16x32_f16 v[160:163], v[192:195], v[204:207], v[86:89]\n"
      "v_cvt_pk_f16_f32 v156, v156, v157\n"
      "v_cvt_pk_f16_f32 v157, v158, v159\n"
      "v_mfma_f32_16x16x32_f16 v[160:163], v[46:49], v[32:35], v[160:163]\n"
      "v_mul_f32 v153, v140, v153\n"
      "v_cvt_pk_f16_f32 v159, v154, v155\n"
      "v_cvt_pk_f16_f32 v158, v152, v153\n"
      "v_mul_f32 v202, v140, v202\n"
      "v_mfma_f32_16x16x32_f16 v[152:155], v[164:167], v[204:207], v[78:81]\n"
      "v_mul_f32 v201, v140, v201\n"
      "v_mul_f32 v203, v140, v203\n"
      "v_mfma_f32_16x16x32_f16 v[152:155], v[34:37], v[32:35], v[152:155]\n"
      "v_cvt_pk_f16_f32 v45, v202, v203\n"
      "v_cvt_pk_f16_f32 v44, v200, v201\n"
      "v_mfma_f32_16x16x32_f16 v[200:203], v[184:187], v[204:207], v[82:85]\n"
      "s_nop 0\n"
      "v_mfma_f32_16x16x32_f16 v[200:203], v[40:43], v[32:35], v[200:203]\n"
      "s_nop 7\n"
      "s_or_b64 vcc, s[48:49], s[50:51]\n"
      "s_cbranch_scc0 L_fast_11_%=\n"
      "L_ok_65_%=:\n"
      "s_cmp_eq_u64 s[48:49], 0\n"
      "s_cbranch_scc1 L_go_66_%=\n"
      "s_mov_b32 m0, s27\n"
      "s_nop 0\n"
      "s_movrels_b32 s9, s80\n"
      "s_lshr_b32 s8, s9, 8\n"
      "s_and_b32 s9, s9, 0xff\n"
      "s_mul_i32 s10, s8, 73\n"
      "s_add_u32 s10, s10, s9\n"
      "s_mul_i32 s30, s10, 4802\n"
      "s_add_u32 s30, s30, s18\n"
      "s_and_b32 s29, s30, 15\n"
      "s_add_u32 s30, s29, 110592\n"
      "s_lshl_b64 s[34:35], s[48:49], 4\n"
      "s_lshr_b64 s[34:35], s[34:35], 5\n"
      "s_cbranch_scc1 L_gen_9_%=\n"
      "v_mbcnt_lo_u32_b32 v140, -1, 0\n"
      "v_mbcnt_hi_u32_b32 v140, -1, v140\n"
      "v_lshlrev_b32 v141, 1, v140\n"
      "v_cmp_gt_u32 vcc, s29, v141\n"
      "v_add_u32 v142, 4802, v141\n"
      "s_nop 0\n"
      "v_cndmask_b32 v141, v142, v141, vcc\n"
      "v_add_u32 v141, 110592, v141\n"
      "v_mov_b32 v142, 0\n"
      "s_mov_b64 exec, 0x7f\n"
      "ds_write_b16 v141, v142\n"
      "s_mov_b64 exec, -1\n"
      "v_lshlrev_b32 v141, 4, v140\n"
      "v_add_u32 v141, 110592, v141\n"
      "ds_read_b128 v[136:139], v141 offset:1024\n"
      "ds_read_b128 v[172:175], v141\n"
      "v_min_u32 v140, 111296, v141\n"
      "ds_read_b128 v[168:171], v141 offset:2048\n"
      "ds_read_b128 v[204:207], v141 offset:3072\n"
      "ds_read_b128 v[232:235], v140 offset:4096\n"
      "s_waitcnt lgkmcnt(4)\n"
      "s_waitcnt lgkmcnt(3)\n"
      "v_or3_b32 v172, v172, v173, v174\n"
      "v_or3_b32 v172, v172, v175, v136\n"
      "v_or3_b32 v172, v172, v137, v138\n"
      "s_waitcnt lgkmcnt(2)\n"
      "v_or3_b32 v172, v172, v139, v168\n"
      "v_or3_b32 v172, v172, v169, v170\n"
      "s_waitcnt lgkmcnt(1)\n"
      "s_waitcnt lgkmcnt(0)\n"
      "v_or3_b32 v172, v172, v171, v204\n"
      "v_or3_b32 v172, v172, v205, v206\n"
      "v_or3_b32 v172, v172, v207, v232\n"
      "v_or3_b32 v172, v172, v233, v234\n"
      "v_bitop3_b32 v172, v172, v235, s40 bitop3:0xa8\n"
      "v_cmp_ne_u32 s[48:49], 0, v172\n"
      "s_cmp_lg_u64 s[48:49], 0\n"
      "s_cbranch_scc1 L_gen_9_%=\n"
      "s_cmp_eq_u64 s[50:51], 0\n"
      "s_cbranch_scc1 L_fast_11_%=\n"
      "L_go_66_%=:\n"
      "v_mfma_f32_16x16x32_f16 v[136:139], v[164:167], v[148:151], v[90:93]\n"
      "ds_read_b128 v[172:175], v128 offset:3072\n"
      "v_max3_f32 v140, v152, v153, v154\n"
      "v_mfma_f32_16x16x32_f16 v[136:139], v[34:37], v[38:41], v[136:139]\n"
      "v_max3_f32 v140, v140, v202, v203\n"
      "v_max3_f32 v141, v155, v200, v201\n"
      "v_mfma_f32_16x16x32_f16 v[168:171], v[184:187], v[148:151], v[94:97]\n"
      "v_max3_f32 v141, v141, v160, v161\n"
      "v_max3_f32 v140, v140, v162, v163\n"
      "v_mfma_f32_16x16x32_f16 v[168:171], v[40:43], v[38:41], v[168:171]\n"
      "v_max3_f32 v140, v140, v141, v212\n"
      "v_mov_b32 v141, v140\n"
      "v_mfma_f32_16x16x32_f16 v[232:235], v[144:147], v[148:151], v[72:75]\n"
      "s_nop 0\n"
      "v_permlane16_swap_b32 v140, v141\n"
      "v_mfma_f32_16x16x32_f16 v[232:235], v[176:179], v[184:187], v[232:235]\n"
      "v_max_f32 v140, v140, v141\n"
      "v_mov_b32 v141, v140\n"
      "v_mfma_f32_16x16x32_f16 v[204:207], v[192:195], v[148:151], v[98:101]\n"
      "s_nop 0\n"
      "v_permlane32_swap_b32 v140, v141\n"
      "v_mfma_f32_16x16x32_f16 v[204:207], v[46:49], v[38:41], v[204:207]\n"
      "v_max_f32 v140, v140, v141\n"
      "v_sub_f32 v153, v153, v140\n"
      "v_mfma_f32_16x16x32_f16 v[148:151], v[164:167], v[156:159], v[102:105]\n"
      "v_sub_f32 v200, v200, v140\n"
      "v_sub_f32 v163, v163, v140\n"
      "v_mfma_f32_16x16x32_f16 v[148:151], v[34:37], v[44:47], v[148:151]\n"
      "s_waitcnt lgkmcnt(0)\n"
      "v_sub_f32 v155, v155, v140\n"
      "v_bitop3_b32 v174, v174, v175, s4 bitop3:0x7e\n"
      "v_sub_f32 v201, v201, v140\n"
      "v_mfma_f32_16x16x32_f16 v[232:235], v[228:231], v[38:41], v[232:235]\n"
      "v_exp_f32 v153, v153\n"
      "v_mfma_f32_16x16x32_f16 v[164:167], v[184:187], v[156:159], v[106:109]\n"
      "v_sub_f32 v203, v203, v140\n"
      "v_sub_f32_e64 v162, v162, v140\n"
      "v_mfma_f32_16x16x32_f16 v[164:167], v[40:43], v[44:47], v[164:167]\n"
      "v_sub_f32 v202, v202, v140\n"
      "v_sub_f32 v152, v152, v140\n"
      "v_mfma_f32_16x16x32_f16 v[184:187], v[144:147], v[156:159], v[74:77]\n"
      "v_sub_f32 v160, v160, v140\n"
      "v_sub_f32 v161, v161, v140\n"
      "v_mfma_f32_16x16x32_f16 v[184:187], v[176:179], v[192:195], v[184:187]\n"
      "v_exp_f32 v152, v152\n"
      "v_mfma_f32_16x16x32_f16 v[184:187], v[228:231], v[44:47], v[184:187]\n"
      "v_sub_f32 v154, v154, v140\n"
      "v_exp_f32 v155, v155\n"
      "v_mfma_f32_16x16x32_f16 v[196:199], v[192:195], v[156:159], v[110:113]\n"
      "v_sub_f32 v212, v212, v140\n"
      "v_cvt_pk_bf16_f32 v152, v152, v153\n"
      "v_mfma_f32_16x16x32_f16 v[196:199], v[46:49], v[44:47], v[196:199]\n"
      "v_exp_f32 v202, v202\n"
      "v_exp_f32 v212, v212\n"
      "v_exp_f32 v203, v203\n"
      "v_exp_f32 v200, v200\n"
      "v_mfma_f32_16x16x32_f16 v[140:143], v[176:179], v[144:147], v[76:79]\n"
      "v_exp_f32 v163, v163\n"
      "v_exp_f32 v154, v154\n"
      "v_exp_f32 v161, v161\n"
      "v_exp_f32 v201, v201\n"
      "v_exp_f32 v160, v160\n"
      "v_cvt_pk_bf16_f32 v153, v154, v155\n"
      "v_cvt_pk_bf16_f32 v155, v202, v203\n"
      "v_mfma_f32_16x16x16_f16 v[140:143], v[230:231], v[228:229], v[140:143]\n"
      "v_exp_f32 v162, v162\n"
      "v_bitop3_b32 v172, v172, v173, s4 bitop3:0x7e\n"
      "v_or_b32 v172, v172, v174\n"
      "v_cvt_pk_bf16_f32 v63, v162, v163\n"
      "v_cvt_pk_bf16_f32 v64, v212, 0\n"
      "v_cmp_ne_u32 vcc, 0, v172\n"
      "v_cvt_pk_bf16_f32 v154, v200, v201\n"
      "v_cvt_pk_bf16_f32 v62, v160, v161\n"
      "v_max3_f32 v140, v139, v168, v169\n"
      "v_mfma_f32_16x16x32_bf16 v[160:163], v[66:69], v[152:155], 0\n"
      "s_mov_b64 s[48:49], vcc\n"
      "v_max3_f32 v142, v136, v137, v138\n"
      "v_max3_f32 v142, v142, v170, v171\n"
      "v_mfma_f32_16x16x32_bf16 v[160:163], v[66:69], v[62:65], v[160:163]\n"
      "s_cbranch_vccnz L_flg_67_%=\n"
      "L_fb_68_%=:\n"
      "v_max3_f32 v140, v140, v204, v205\n"
      "v_mfma_f32_16x16x32_bf16 v[172:175], v[208:211], v[152:155], 0\n"
      "v_max3_f32 v142, v142, v206, v207\n"
      "v_max3_f32 v142, v142, v140, v232\n"
      "v_mfma_f32_16x16x32_bf16 v[172:175], v[50:53], v[62:65], v[172:175]\n"
      "v_max3_f32 v143, v151, v164, v165\n"
      "v_mov_b32 v140, v142\n"
      "v_mfma_f32_16x16x32_bf16 v[144:147], v[188:191], v[152:155], 0\n"
      "s_nop 0\n"
      "v_permlane16_swap_b32 v142, v140\n"
      "v_mfma_f32_16x16x32_bf16 v[144:147], v[54:57], v[62:65], v[144:147]\n"
      "v_max_f32 v142, v142, v140\n"
      "v_mov_b32 v140, v142\n"
      "v_mfma_f32_16x16x32_bf16 v[200:203], v[220:223], v[152:155], 0\n"
      "s_nop 0\n"
      "v_permlane32_swap_b32 v142, v140\n"
      "v_mfma_f32_16x16x32_bf16 v[200:203], v[58:61], v[62:65], v[200:203]\n"
      "v_max_f32 v142, v142, v140\n"
      "v_sub_f32 v206, v206, v142\n"
      "v_sub_f32 v170, v170, v142\n"
      "v_sub_f32 v205, v205, v142\n"
      "v_sub_f32 v138, v138, v142\n"
      "v_sub_f32 v232, v232, v142\n"
      "v_rcp_f32 v160, v160\n"
      "v_sub_f32 v169, v169, v142\n"
      "v_sub_f32 v136, v136, v142\n"
      "v_exp_f32 v206, v206\n"
      "v_sub_f32 v207, v207, v142\n"
      "v_sub_f32 v139, v139, v142\n"
      "v_exp_f32 v138, v138\n"
      "v_exp_f32 v232, v232\n"
      "v_sub_f32 v137, v137, v142\n"
      "v_exp_f32 v137, v137\n"
      "v_exp_f32 v207, v207\n"
      "v_exp_f32 v139, v139\n"
      "v_exp_f32 v169, v169\n"
      "v_sub_f32 v171, v171, v142\n"
      "v_exp_f32 v171, v171\n"
      "v_exp_f32 v205, v205\n"
      "v_sub_f32 v168, v168, v142\n"
      "v_exp_f32 v170, v170\n"
      "v_exp_f32 v136, v136\n"
      "v_cvt_pk_bf16_f32 v64, v232, 0\n"
      "v_exp_f32_e64 v168, v168\n"
      "v_cvt_pk_bf16_f32 v136, v136, v137\n"
      "v_sub_f32_e64 v204, v204, v142\n"
      "v_cvt_pk_bf16_f32 v137, v138, v139\n"
      "v_cvt_pk_bf16_f32 v138, v168, v169\n"
      "v_cvt_pk_bf16_f32 v139, v170, v171\n"
      "v_exp_f32 v204, v204\n"
      "v_cvt_pk_bf16_f32 v63, v206, v207\n"
      "v_mul_f32 v173, v160, v173\n"
      "v_cvt_pk_bf16_f32 v62, v204, v205\n"
      "v_mfma_f32_16x16x32_bf16 v[156:159], v[66:69], v[136:139], 0\n"
      "s_add_u32 s28, s27, 2\n"
      "v_mul_f32 v172, v160, v172\n"
      "v_cvt_pk_bf16_f32 v172, v172, v173\n"
      "v_mfma_f32_16x16x32_bf16 v[156:159], v[66:69], v[62:65], v[156:159]\n"
      "s_min_u32 m0, s28, s3\n"
      "v_mul_f32 v175, v160, v175\n"
      "v_mul_f32 v174, v160, v174\n"
      "v_cvt_pk_bf16_f32 v173, v174, v175\n"
      "v_mfma_f32_16x16x32_bf16 v[152:155], v[208:211], v[136:139], 0\n"
      "s_movrels_b32 s9, s80\n"
      "v_mul_f32 v145, v160, v145\n"
      "v_mul_f32 v144, v160, v144\n"
      "v_mfma_f32_16x16x32_bf16 v[152:155], v[50:53], v[62:65], v[152:155]\n"
      "s_lshr_b32 s8, s9, 8\n"
      "v_mul_f32 v146, v160, v146\n"
      "v_mul_f32_e64 v200, v160, v200\n"
      "v_mfma_f32_16x16x32_bf16 v[204:207], v[188:191], v[136:139], 0\n"
      "s_and_b32 s9, s9, 0xff\n"
      "v_mul_f32 v147, v160, v147\n"
      "v_mul_f32 v201, v160, v201\n"
      "v_mfma_f32_16x16x32_bf16 v[204:207], v[54:57], v[62:65], v[204:207]\n"
      "s_mul_i32 s10, s8, 73\n"
      "v_cvt_pk_bf16_f32 v175, v146, v147\n"
      "v_cvt_pk_bf16_f32 v174, v144, v145\n"
      "v_cndmask_b32 v213, v213, v233, s[42:43]\n"
      "v_mfma_f32_16x16x32_bf16 v[228:231], v[220:223], v[136:139], 0\n"
      "s_add_u32 s10, s10, s9\n"
      "v_mul_f32 v202, v160, v202\n"
      "v_cvt_pk_bf16_f32 v200, v200, v201\n"
      "v_mfma_f32_16x16x32_bf16 v[228:231], v[58:61], v[62:65], v[228:231]\n"
      "ds_write_b128 v124, v[172:175]\n"
      "v_max3_f32 v142, v148, v149, v150\n"
      "v_mul_f32_e64 v203, v160, v203\n"
      "v_max3_f32 v142, v142, v166, v167\n"
      "v_max3_f32 v142, v142, v198, v199\n"
      "v_max3_f32 v143, v143, v196, v197\n"
      "v_max3_f32 v142, v142, v143, v184\n"
      "v_mov_b32_e64 v143, v142\n"
      "v_cvt_pk_bf16_f32 v201, v202, v203\n"
      "v_cndmask_b32 v213, v213, v185, s[44:45]\n"
      "v_permlane16_swap_b32 v142, v143\n"
      "v_max_f32 v142, v142, v143\n"
      "v_mov_b32_e64 v143, v142\n"
      "v_cndmask_b32 v213, v213, v141, s[46:47]\n"
      "v_rcp_f32 v156, v156\n"
      "v_permlane32_swap_b32 v142, v143\n"
      "v_max_f32 v142, v142, v143\n"
      "v_sub_f32 v166, v166, v142\n"
      "v_sub_f32 v165, v165, v142\n"
      "v_sub_f32 v148, v148, v142\n"
      "v_sub_f32 v198, v198, v142\n"
      "v_sub_f32 v150, v150, v142\n"
      "v_sub_f32 v184, v184, v142\n"
      "v_sub_f32 v151, v151, v142\n"
      "v_exp_f32 v165, v165\n"
      "v_sub_f32 v196, v196, v142\n"
      "v_sub_f32 v164, v164, v142\n"
      "v_mul_f32 v153, v156, v153\n"
      "v_exp_f32 v150, v150\n"
      "v_exp_f32 v164, v164\n"
      "v_sub_f32 v149, v149, v142\n"
      "v_exp_f32 v148, v148\n"
      "v_sub_f32 v167, v167, v142\n"
      "v_sub_f32 v197, v197, v142\n"
      "v_exp_f32 v149, v149\n"
      "v_exp_f32 v151, v151\n"
      "v_cvt_pk_bf16_f32 v148, v148, v149\n"
      "v_cvt_pk_bf16_f32 v149, v150, v151\n"
      "v_exp_f32 v197, v197\n"
      "v_exp_f32 v196, v196\n"
      "v_exp_f32_e64 v167, v167\n"
      "v_cvt_pk_bf16_f32 v62, v196, v197\n"
      "v_exp_f32_e64 v184, v184\n"
      "v_exp_f32_e64 v166, v166\n"
      "v_max_f32_dpp v157, v213, v213 row_ror:8 row_mask:0xf bank_mask:0xf\n"
      "v_sub_f32_e64 v199, v199, v142\n"
      "v_exp_f32_e64 v199, v199\n"
      "v_cvt_pk_bf16_f32 v150, v164, v165\n"
      "v_max_f32_dpp v157, v157, v157 row_ror:4 row_mask:0xf bank_mask:0xf\n"
      "v_exp_f32_e64 v198, v198\n"
      "v_cvt_pk_bf16_f32 v151, v166, v167\n"
      "v_cvt_pk_bf16_f32 v64, v184, 0\n"
      "v_cvt_pk_bf16_f32 v63, v198, v199\n"
      "v_mfma_f32_16x16x32_bf16 v[160:163], v[66:69], v[148:151], 0\n"
      "ds_read_b128 v[140:143], v115 offset:18432\n"
      "v_max_f32_dpp v157, v157, v157 row_ror:2 row_mask:0xf bank_mask:0xf\n"
      "v_mfma_f32_16x16x32_bf16 v[160:163], v[66:69], v[62:65], v[160:163]\n"
      "ds_read_b128 v[136:139], v114 offset:18432\n"
      "v_max_f32_dpp v157, v157, v157 row_ror:1 row_mask:0xf bank_mask:0xf\n"
      "v_mfma_f32_16x16x32_bf16 v[164:167], v[208:211], v[148:151], 0\n"
      "s_mul_i32 s28, s10, 4802\n"
      "v_mul_f32 v152, v156, v152\n"
      "v_max_f32_dpp v157, v157, v157 row_bcast:15 row_mask:0xa bank_mask:0xf\n"
      "v_mfma_f32_16x16x32_bf16 v[164:167], v[50:53], v[62:65], v[164:167]\n"
      "s_add_u32 s56, s18, s28\n"
      "v_max_f32_dpp v157, v157, v157 row_bcast:31 row_mask:0xc bank_mask:0xf\n"
      "v_cvt_pk_bf16_f32 v152, v152, v153\n"
      "v_mfma_f32_16x16x32_bf16 v[172:175], v[188:191], v[148:151], 0\n"
      "s_addc_u32 s57, s19, 0\n"
      "v_mul_f32 v155, v156, v155\n"
      "v_readlane_b32 s31, v157, 63\n"
      "v_mfma_f32_16x16x32_bf16 v[172:175], v[54:57], v[62:65], v[172:175]\n"
      "s_and_b32 s56, s56, -16\n"
      "v_subrev_f32 v213, s31, v213\n"
      "v_exp_f32_e64 v213, v213\n"
      "v_mfma_f32_16x16x32_bf16 v[168:171], v[220:223], v[148:151], 0\n"
      "s_sub_u32 s58, s56, 110592\n"
      "v_mul_f32_e64 v154, v156, v154\n"
      "v_cvt_pk_bf16_f32 v213, v213, v213\n"
      "v_mfma_f32_16x16x32_bf16 v[168:171], v[58:61], v[62:65], v[168:171]\n"
      "ds_write_b16 v130, v213\n"
      "v_mul_f32 v205, v156, v205\n"
      "v_mul_f32 v204, v156, v204\n"
      "v_cvt_pk_bf16_f32 v153, v154, v155\n"
      "v_mul_f32_e64 v206, v156, v206\n"
      "v_cvt_pk_bf16_f32 v154, v204, v205\n"
      "s_waitcnt lgkmcnt(1)\n"
      "v_mfma_f32_16x16x32_bf16 v[144:147], a[0:3], v[136:139], 0\n"
      "ds_read_b128 v[196:199], v131\n"
      "v_mul_f32 v207, v156, v207\n"
      "v_mfma_f32_16x16x32_bf16 v[144:147], a[36:39], v[140:143], v[144:147]\n"
      "ds_read_b128 v[184:187], v131 offset:64\n"
      "v_cvt_pk_bf16_f32 v155, v206, v207\n"
      "v_mfma_f32_16x16x32_bf16 v[148:151], a[4:7], v[136:139], 0\n"
      "s_subb_u32 s59, s57, 0\n"
      "v_mul_f32 v229, v156, v229\n"
      "v_mul_f32_e64 v228, v156, v228\n"
      "v_mfma_f32_16x16x32_bf16 v[148:151], a[40:43], v[140:143], v[148:151]\n"
      "ds_write_b128 v124, v[152:155] offset:1024\n"
      "v_mfma_f32_16x16x32_bf16 v[152:155], a[8:11], v[136:139], 0\n"
      "s_sub_u32 s56, s56, 73728\n"
      "v_mul_f32_e64 v230, v156, v230\n"
      "v_cvt_pk_bf16_f32 v228, v228, v229\n"
      "v_mfma_f32_16x16x32_bf16 v[152:155], a[44:47], v[140:143], v[152:155]\n"
      "s_subb_u32 s57, s57, 0\n"
      "v_mul_f32 v231, v156, v231\n"
      "v_cvt_pk_bf16_f32 v229, v230, v231\n"
      "v_mfma_f32_16x16x32_bf16 v[156:159], a[12:15], v[136:139], 0\n"
      "s_mul_i32 s28, s8, 1373568\n"
      "v_mov_b32_e64 v230, 0\n"
      "s_mul_i32 s29, s9, 2688\n"
      "v_mfma_f32_16x16x32_bf16 v[156:159], a[48:51], v[140:143], v[156:159]\n"
      "ds_write2st64_b64 v125, v[200:201], v[228:229] offset0:0 offset1:2\n"
      "s_waitcnt lgkmcnt(2)\n"
      "v_mfma_f32_16x16x32_bf16 v[204:207], v[196:199], v[66:69], 0\n"
      "s_add_u32 s28, s28, s29\n"
      "s_cmp_eq_u32 s8, 72\n"
      "s_cselect_b32 s8, 0, 100270464\n"
      "v_mfma_f32_16x16x32_bf16 v[204:207], v[184:187], v[66:69], v[204:207]\n"
      "ds_write_b32 v129, v230\n"
      "v_mfma_f32_16x16x32_bf16 v[228:231], v[196:199], v[208:211], 0\n"
      "s_cmp_eq_u32 s9, 72\n"
      "s_cselect_b32 s9, 0, 196224\n"
      "s_add_u32 s10, s8, s9\n"
      "v_mfma_f32_16x16x32_bf16 v[228:231], v[184:187], v[50:53], v[228:231]\n"
      "s_add_u32 s28, s28, s10\n"
      "s_add_u32 s52, s16, s28\n"
      "v_mov_b32_dpp v204, v204 quad_perm:[0,0,0,0] row_mask:0xf bank_mask:0xf\n"
      "v_mfma_f32_16x16x32_bf16 v[208:211], v[196:199], v[188:191], 0\n"
      "s_addc_u32 s53, s17, 0\n"
      "v_rcp_f32_e64 v204, v204\n"
      "v_mfma_f32_16x16x32_bf16 v[208:211], v[184:187], v[54:57], v[208:211]\n"
      "s_cmp_lg_u32 s10, s11\n"
      "v_mfma_f32_16x16x32_bf16 v[188:191], v[196:199], v[220:223], 0\n"
      "s_cbranch_scc1 L_edge_70_%=\n"
      "L_eb_71_%=:\n"
      "v_mfma_f32_16x16x32_bf16 v[188:191], v[184:187], v[58:61], v[188:191]\n"
      "s_nop 3\n"
      "v_cndmask_b32 v228, v228, v208, s[42:43]\n"
      "s_nop 2\n"
      "v_cndmask_b32 v228, v228, v188, s[44:45]\n"
      "v_mul_f32_e64 v228, v204, v228\n"
      "v_cvt_pk_bf16_f32 v228, v228, v228\n"
      "ds_write_b16 v135, v228\n"
      "s_branch L_join_10_%=\n"
      "L_flg_67_%=:\n"
      "v_cmp_gt_i32 vcc, 0, v172\n"
      "s_cbranch_vccz L_fb_68_%=\n"
      "s_sleep 1\n"
      "ds_read_b128 v[172:175], v128 offset:3072\n"
      "s_waitcnt lgkmcnt(0)\n"
      "v_bitop3_b32 v172, v172, v173, s4 bitop3:0x7e\n"
      "v_bitop3_b32 v174, v174, v175, s4 bitop3:0x7e\n"
      "v_or_b32 v172, v172, v174\n"
      "v_cmp_ne_u32 s[48:49], 0, v172\n"
      "s_branch L_flg_67_%=\n"
      "L_edge_70_%=:\n"
      "s_mov_b32 s11, s10\n"
      "s_sub_u32 s28, s60, s8\n"
      "s_sub_u32 s29, s61, s9\n"
      "s_mov_b32 s60, s8\n"
      "s_mov_b32 s61, s9\n"
      "s_mov_b64 exec, s[64:65]\n"
      "v_add_u32 v116, s28, v116\n"
      "s_mov_b64 exec, s[70:71]\n"
      "v_add_u32 v116, s29, v116\n"
      "s_mov_b64 exec, s[66:67]\n"
      "v_add_u32 v117, s28, v117\n"
      "s_mov_b64 exec, s[72:73]\n"
      "v_add_u32 v117, s29, v117\n"
      "s_mov_b64 exec, s[68:69]\n"
      "v_add_u32 v118, s28, v118\n"
      "s_mov_b64 exec, s[74:75]\n"
      "v_add_u32 v118, s29, v118\n"
      "s_mov_b64 exec, -1\n"
      "s_nop 4\n"
      "s_branch L_eb_71_%=\n"
      "L_gen_9_%=:\n"
      "s_nop 0\n"
      "s_nop 0\n"
      "v_mfma_f32_16x16x32_f16 v[140:143], v[164:167], v[148:151], v[90:93]\n"
      "s_movk_i32 s31, 0x3fb9\n"
      "v_mbcnt_lo_u32_b32 v136, -1, 0\n"
      "v_mbcnt_hi_u32_b32 v136, -1, v136\n"
      "v_mfma_f32_16x16x32_f16 v[140:143], v[34:37], v[38:41], v[140:143]\n"
      "v_lshrrev_b32 v138, 4, v136\n"
      "v_and_b32 v137, 15, v136\n"
      "v_mul_u32_u24 v139, 0x62, v137\n"
      "v_lshl_add_u32 v139, v138, 3, v139\n"
      "v_lshlrev_b32 v138, 2, v138\n"
      "v_add_u32 v139, s30, v139\n"
      "v_sub_u32 v137, v137, v138\n"
      "v_and_b32 v138, 1, v137\n"
      "v_lshrrev_b32 v137, 1, v137\n"
      "v_lshlrev_b32 v138, 4, v138\n"
      "v_and_b32_e64 v136, -4, v139\n"
      "v_cmp_eq_u32 vcc, 0, v137\n"
      "v_lshlrev_b32 v138, v138, s31\n"
      "v_and_b32_e64 v139, 2, v139\n"
      "v_cndmask_b32 v168, 0, v138, vcc\n"
      "v_mfma_f32_16x16x32_f16 v[172:175], v[184:187], v[148:151], v[94:97]\n"
      "ds_read2_b32 v[170:171], v136 offset0:0 offset1:1\n"
      "ds_read2_b32 v[214:215], v136 offset0:2 offset1:10\n"
      "ds_read2_b32 v[180:181], v136 offset0:8 offset1:9\n"
      "ds_read2_b32 v[182:183], v136 offset0:18 offset1:24\n"
      "v_cmp_eq_u32 vcc, 1, v137\n"
      "ds_read2_b32 v[196:197], v136 offset0:16 offset1:17\n"
      "v_add_u32 v136, 1568, v136\n"
      "v_cndmask_b32 v169, 0, v138, vcc\n"
      "v_mfma_f32_16x16x32_f16 v[172:175], v[40:43], v[38:41], v[172:175]\n"
      "ds_read2_b32 v[204:205], v136 offset0:0 offset1:1\n"
      "ds_read2_b32 v[198:199], v136 offset0:8 offset1:9\n"
      "ds_read2_b32 v[216:217], v136 offset0:2 offset1:10\n"
      "ds_read2_b32 v[206:207], v136 offset0:16 offset1:17\n"
      "ds_read2_b32 v[218:219], v136 offset0:18 offset1:24\n"
      "v_add_u32 v136, 1568, v136\n"
      "v_mfma_f32_16x16x32_f16 v[240:243], v[144:147], v[148:151], v[72:75]\n"
      "ds_read2_b32 v[232:233], v136 offset0:2 offset1:10\n"
      "ds_read2_b32 v[234:235], v136 offset0:18 offset1:24\n"
      "ds_read2_b32 v[236:237], v136 offset0:16 offset1:17\n"
      "ds_read2_b32 v[224:225], v136 offset0:8 offset1:9\n"
      "s_waitcnt lgkmcnt(12)\n"
      "v_alignbyte_b32 v170, v171, v170, v139\n"
      "v_alignbyte_b32 v171, v214, v171, v139\n"
      "s_waitcnt lgkmcnt(11)\n"
      "ds_read2_b32 v[226:227], v136 offset0:0 offset1:1\n"
      "v_mfma_f32_16x16x16_bf16 v[152:155], v[168:169], v[170:171], v[152:155]\n"
      "s_waitcnt lgkmcnt(10)\n"
      "v_alignbyte_b32 v180, v181, v180, v139\n"
      "v_alignbyte_b32 v196, v197, v196, v139\n"
      "v_alignbyte_b32 v183, v183, v183, v139\n"
      "v_lshlrev_b32 v183, 16, v183\n"
      "v_mfma_f32_16x16x32_f16 v[240:243], v[176:179], v[184:187], v[240:243]\n"
      "s_waitcnt lgkmcnt(9)\n"
      "v_fmac_f32 v212, s37, v183\n"
      "v_alignbyte_b32 v181, v215, v181, v139\n"
      "v_mfma_f32_16x16x32_f16 v[240:243], v[228:231], v[38:41], v[240:243]\n"
      "s_waitcnt lgkmcnt(7)\n"
      "v_alignbyte_b32 v204, v205, v204, v139\n"
      "v_alignbyte_b32 v197, v182, v197, v139\n"
      "v_mfma_f32_16x16x32_f16 v[244:247], v[192:195], v[148:151], v[98:101]\n"
      "s_waitcnt lgkmcnt(5)\n"
      "v_alignbyte_b32 v198, v199, v198, v139\n"
      "v_alignbyte_b32 v199, v217, v199, v139\n"
      "v_mfma_f32_16x16x32_f16 v[244:247], v[46:49], v[38:41], v[244:247]\n"
      "ds_read_b128 v[148:151], v128 offset:3072\n"
      "v_alignbyte_b32 v206, v207, v206, v139\n"
      "v_mfma_f32_16x16x16_bf16 v[200:203], v[168:169], v[180:181], v[200:203]\n"
      "v_max3_f32 v138, v152, v153, v154\n"
      "v_alignbyte_b32 v207, v218, v207, v139\n"
      "v_mfma_f32_16x16x16_bf16 v[160:163], v[168:169], v[196:197], v[160:163]\n"
      "s_waitcnt lgkmcnt(2)\n"
      "v_alignbyte_b32 v219, v219, v219, v139\n"
      "v_alignbyte_b32 v205, v216, v205, v139\n"
      "v_mfma_f32_16x16x32_f16 v[180:183], v[164:167], v[156:159], v[102:105]\n"
      "s_waitcnt lgkmcnt(1)\n"
      "v_alignbyte_b32 v226, v227, v226, v139\n"
      "v_lshlrev_b32_e64 v219, 16, v219\n"
      "v_mfma_f32_16x16x32_f16 v[180:183], v[34:37], v[44:47], v[180:183]\n"
      "v_alignbyte_b32 v227, v232, v227, v139\n"
      "v_alignbyte_b32 v224, v225, v224, v139\n"
      "v_mfma_f32_16x16x16_bf16 v[172:175], v[168:169], v[198:199], v[172:175]\n"
      "v_max3_f32 v138, v138, v202, v203\n"
      "v_alignbyte_b32 v225, v233, v225, v139\n"
      "v_mfma_f32_16x16x16_bf16 v[140:143], v[168:169], v[204:205], v[140:143]\n"
      "v_max3_f32 v137, v155, v200, v201\n"
      "v_alignbyte_b32 v235, v235, v235, v139\n"
      "v_mfma_f32_16x16x32_f16 v[164:167], v[184:187], v[156:159], v[106:109]\n"
      "v_alignbyte_b32 v236, v237, v236, v139\n"
      "v_alignbyte_b32 v237, v234, v237, v139\n"
      "v_mfma_f32_16x16x32_f16 v[164:167], v[40:43], v[44:47], v[164:167]\n"
      "v_lshlrev_b32_e64 v235, 16, v235\n"
      "v_max3_f32 v137, v137, v160, v161\n"
      "v_mfma_f32_16x16x32_f16 v[184:187], v[144:147], v[156:159], v[74:77]\n"
      "v_max3_f32 v138, v138, v162, v163\n"
      "v_max3_f32 v138, v138, v137, v212\n"
      "v_mfma_f32_16x16x32_f16 v[184:187], v[176:179], v[192:195], v[184:187]\n"
      "v_mov_b32 v137, v138\n"
      "v_fmac_f32 v240, s37, v219\n"
      "v_mfma_f32_16x16x32_f16 v[184:187], v[228:231], v[44:47], v[184:187]\n"
      "s_waitcnt lgkmcnt(0)\n"
      "v_permlane16_swap_b32 v138, v137\n"
      "v_mfma_f32_16x16x32_f16 v[196:199], v[192:195], v[156:159], v[110:113]\n"
      "v_max_f32 v138, v138, v137\n"
      "v_mov_b32 v137, v138\n"
      "v_mfma_f32_16x16x32_f16 v[196:199], v[46:49], v[44:47], v[196:199]\n"
      "s_nop 0\n"
      "v_permlane32_swap_b32 v138, v137\n"
      "v_max_f32 v138, v138, v137\n"
      "v_sub_f32 v200, v200, v138\n"
      "v_sub_f32 v155, v155, v138\n"
      "v_mfma_f32_16x16x32_f16 v[156:159], v[176:179], v[144:147], v[76:79]\n"
      "v_sub_f32 v162, v162, v138\n"
      "v_sub_f32 v153, v153, v138\n"
      "v_sub_f32 v201, v201, v138\n"
      "v_exp_f32 v162, v162\n"
      "v_sub_f32 v152, v152, v138\n"
      "v_exp_f32 v153, v153\n"
      "v_sub_f32 v212, v212, v138\n"
      "v_bitop3_b32 v148, v148, v149, s4 bitop3:0x7e\n"
      "v_exp_f32 v152, v152\n"
      "v_sub_f32 v203, v203, v138\n"
      "v_sub_f32 v154, v154, v138\n"
      "v_sub_f32 v161, v161, v138\n"
      "v_mfma_f32_16x16x16_bf16 v[196:199], v[168:169], v[236:237], v[196:199]\n"
      "v_exp_f32 v212, v212\n"
      "v_exp_f32 v154, v154\n"
      "v_exp_f32 v161, v161\n"
      "v_exp_f32 v200, v200\n"
      "v_sub_f32 v163, v163, v138\n"
      "v_exp_f32 v163, v163\n"
      "v_sub_f32 v160, v160, v138\n"
      "v_mfma_f32_16x16x16_bf16 v[244:247], v[168:169], v[206:207], v[244:247]\n"
      "v_exp_f32 v203, v203\n"
      "v_exp_f32 v155, v155\n"
      "v_exp_f32 v201, v201\n"
      "v_sub_f32 v202, v202, v138\n"
      "v_exp_f32 v202, v202\n"
      "v_mfma_f32_16x16x16_bf16 v[164:167], v[168:169], v[224:225], v[164:167]\n"
      "v_cvt_pk_bf16_f32 v63, v162, v163\n"
      "v_cvt_pk_bf16_f32 v152, v152, v153\n"
      "v_cvt_pk_bf16_f32 v153, v154, v155\n"
      "v_mfma_f32_16x16x16_bf16 v[180:183], v[168:169], v[226:227], v[180:183]\n"
      "v_bitop3_b32 v150, v150, v151, s4 bitop3:0x7e\n"
      "v_exp_f32_e64 v160, v160\n"
      "v_mfma_f32_16x16x16_f16 v[156:159], v[230:231], v[228:229], v[156:159]\n"
      "v_cvt_pk_bf16_f32 v155, v202, v203\n"
      "v_cvt_pk_bf16_f32 v154, v200, v201\n"
      "v_cvt_pk_bf16_f32 v62, v160, v161\n"
      "v_cvt_pk_bf16_f32 v64, v212, 0\n"
      "v_mfma_f32_16x16x32_bf16 v[160:163], v[66:69], v[152:155], 0\n"
      "v_or_b32 v148, v148, v150\n"
      "v_cmp_ne_u32 vcc, 0, v148\n"
      "v_mfma_f32_16x16x32_bf16 v[160:163], v[66:69], v[62:65], v[160:163]\n"
      "s_mov_b64 s[48:49], vcc\n"
      "v_max3_f32 v137, v140, v141, v142\n"
      "v_max3_f32 v138, v143, v172, v173\n"
      "v_max3_f32 v138, v138, v244, v245\n"
      "v_mfma_f32_16x16x32_bf16 v[168:171], v[208:211], v[152:155], 0\n"
      "s_cbranch_vccnz L_flg_89_%=\n"
      "L_fb_90_%=:\n"
      "v_max3_f32 v137, v137, v174, v175\n"
      "v_mfma_f32_16x16x32_bf16 v[148:151], v[188:191], v[152:155], 0\n"
      "v_max3_f32 v137, v137, v246, v247\n"
      "v_max3_f32 v137, v137, v138, v240\n"
      "v_mfma_f32_16x16x32_bf16 v[148:151], v[54:57], v[62:65], v[148:151]\n"
      "v_mov_b32 v138, v137\n"
      "v_fmac_f32 v184, s37, v235\n"
      "v_mfma_f32_16x16x32_bf16 v[200:203], v[220:223], v[152:155], 0\n"
      "v_permlane16_swap_b32 v137, v138\n"
      "v_mfma_f32_16x16x32_bf16 v[200:203], v[58:61], v[62:65], v[200:203]\n"
      "v_max_f32 v137, v137, v138\n"
      "v_mov_b32 v138, v137\n"
      "v_rcp_f32 v160, v160\n"
      "v_max3_f32 v139, v183, v164, v165\n"
      "v_permlane32_swap_b32 v137, v138\n"
      "v_max_f32 v137, v137, v138\n"
      "v_sub_f32 v143, v143, v137\n"
      "v_sub_f32 v140, v140, v137\n"
      "v_sub_f32 v172, v172, v137\n"
      "v_sub_f32 v141, v141, v137\n"
      "v_mfma_f32_16x16x32_bf16 v[168:171], v[50:53], v[62:65], v[168:171]\n"
      "v_sub_f32 v246, v246, v137\n"
      "v_sub_f32 v244, v244, v137\n"
      "v_exp_f32 v143, v143\n"
      "v_sub_f32 v174, v174, v137\n"
      "v_sub_f32 v245, v245, v137\n"
      "v_sub_f32 v142, v142, v137\n"
      "v_exp_f32 v140, v140\n"
      "v_exp_f32 v244, v244\n"
      "v_sub_f32 v175, v175, v137\n"
      "v_exp_f32 v174, v174\n"
      "v_sub_f32 v247, v247, v137\n"
      "v_exp_f32 v172, v172\n"
      "v_exp_f32 v141, v141\n"
      "v_sub_f32 v240, v240, v137\n"
      "v_sub_f32 v173, v173, v137\n"
      "v_cvt_pk_bf16_f32 v140, v140, v141\n"
      "v_exp_f32 v175, v175\n"
      "v_exp_f32 v246, v246\n"
      "v_exp_f32 v173, v173\n"
      "v_exp_f32 v142, v142\n"
      "v_exp_f32 v245, v245\n"
      "v_cvt_pk_bf16_f32 v141, v142, v143\n"
      "v_mul_f32 v170, v160, v170\n"
      "v_cvt_pk_bf16_f32 v143, v174, v175\n"
      "v_cvt_pk_bf16_f32 v62, v244, v245\n"
      "v_exp_f32 v240, v240\n"
      "v_cvt_pk_bf16_f32 v142, v172, v173\n"
      "v_exp_f32 v247, v247\n"
      "v_cvt_pk_bf16_f32 v64, v240, 0\n"
      "v_mul_f32 v169, v160, v169\n"
      "v_cvt_pk_bf16_f32 v63, v246, v247\n"
      "v_mfma_f32_16x16x32_bf16 v[152:155], v[66:69], v[140:143], 0\n"
      "s_add_u32 s28, s27, 2\n"
      "v_mul_f32 v168, v160, v168\n"
      "v_cvt_pk_bf16_f32 v168, v168, v169\n"
      "v_mfma_f32_16x16x32_bf16 v[152:155], v[66:69], v[62:65], v[152:155]\n"
      "v_mul_f32 v171, v160, v171\n"
      "v_cvt_pk_bf16_f32 v169, v170, v171\n"
      "v_mfma_f32_16x16x32_bf16 v[144:147], v[208:211], v[140:143], 0\n"
      "s_min_u32 m0, s28, s3\n"
      "v_mul_f32 v148, v160, v148\n"
      "v_mul_f32 v151, v160, v151\n"
      "v_mfma_f32_16x16x32_bf16 v[144:147], v[50:53], v[62:65], v[144:147]\n"
      "v_max3_f32 v137, v180, v181, v182\n"
      "v_mul_f32_e64 v149, v160, v149\n"
      "v_cvt_pk_bf16_f32 v170, v148, v149\n"
      "v_mfma_f32_16x16x32_bf16 v[244:247], v[188:191], v[140:143], 0\n"
      "s_movrels_b32 s9, s80\n"
      "v_mul_f32 v201, v160, v201\n"
      "v_mul_f32 v150, v160, v150\n"
      "v_mfma_f32_16x16x32_bf16 v[244:247], v[54:57], v[62:65], v[244:247]\n"
      "s_lshr_b32 s8, s9, 8\n"
      "v_cvt_pk_bf16_f32 v171, v150, v151\n"
      "v_max3_f32 v137, v137, v166, v167\n"
      "v_mfma_f32_16x16x32_bf16 v[224:227], v[220:223], v[140:143], 0\n"
      "s_and_b32 s9, s9, 0xff\n"
      "v_mul_f32 v202, v160, v202\n"
      "v_mul_f32 v203, v160, v203\n"
      "v_mfma_f32_16x16x32_bf16 v[224:227], v[58:61], v[62:65], v[224:227]\n"
      "ds_write_b128 v124, v[168:171]\n"
      "v_mul_f32_e64 v200, v160, v200\n"
      "v_max3_f32 v139, v139, v196, v197\n"
      "v_max3_f32 v137, v137, v198, v199\n"
      "v_mbcnt_lo_u32_b32 v136, -1, 0\n"
      "v_max3_f32 v137, v137, v139, v184\n"
      "v_mov_b32 v139, v137\n"
      "v_cndmask_b32 v213, v213, v241, s[42:43]\n"
      "v_mbcnt_hi_u32_b32 v136, -1, v136\n"
      "v_permlane16_swap_b32 v137, v139\n"
      "v_max_f32 v137, v137, v139\n"
      "v_mov_b32 v139, v137\n"
      "v_cndmask_b32 v213, v213, v185, s[44:45]\n"
      "v_cvt_pk_bf16_f32 v200, v200, v201\n"
      "v_permlane32_swap_b32 v137, v139\n"
      "v_max_f32 v137, v137, v139\n"
      "v_min_u32 v136, 48, v136\n"
      "v_sub_f32 v167, v167, v137\n"
      "v_sub_f32 v180, v180, v137\n"
      "v_cvt_pk_bf16_f32 v201, v202, v203\n"
      "v_sub_f32 v166, v166, v137\n"
      "v_exp_f32 v180, v180\n"
      "v_sub_f32 v164, v164, v137\n"
      "v_sub_f32 v184, v184, v137\n"
      "v_sub_f32 v198, v198, v137\n"
      "v_sub_f32 v183, v183, v137\n"
      "v_sub_f32 v181, v181, v137\n"
      "v_exp_f32 v181, v181\n"
      "v_sub_f32 v182, v182, v137\n"
      "v_sub_f32 v197, v197, v137\n"
      "v_exp_f32 v164, v164\n"
      "v_exp_f32_e64 v184, v184\n"
      "v_cvt_pk_bf16_f32 v180, v180, v181\n"
      "v_sub_f32 v165, v165, v137\n"
      "v_exp_f32 v198, v198\n"
      "v_exp_f32 v182, v182\n"
      "v_exp_f32 v197, v197\n"
      "v_sub_f32 v196, v196, v137\n"
      "v_exp_f32 v183, v183\n"
      "v_exp_f32 v196, v196\n"
      "v_exp_f32 v166, v166\n"
      "v_rcp_f32 v152, v152\n"
      "v_cvt_pk_bf16_f32 v64, v184, 0\n"
      "v_exp_f32 v167, v167\n"
      "v_cvt_pk_bf16_f32 v181, v182, v183\n"
      "v_exp_f32 v165, v165\n"
      "v_sub_f32 v199, v199, v137\n"
      "v_lshlrev_b32 v136, 1, v136\n"
      "v_cvt_pk_bf16_f32 v182, v164, v165\n"
      "v_exp_f32 v199, v199\n"
      "v_cvt_pk_bf16_f32 v183, v166, v167\n"
      "v_cvt_pk_bf16_f32 v63, v198, v199\n"
      "v_cvt_pk_bf16_f32 v62, v196, v197\n"
      "v_mfma_f32_16x16x32_bf16 v[160:163], v[66:69], v[180:183], 0\n"
      "ds_read_b128 v[140:143], v115 offset:18432\n"
      "v_add_u32_e64 v136, s30, v136\n"
      "v_mfma_f32_16x16x32_bf16 v[160:163], v[66:69], v[62:65], v[160:163]\n"
      "ds_read_u16 v153, v136 offset:4704\n"
      "v_cndmask_b32 v213, v213, v157, s[46:47]\n"
      "v_mfma_f32_16x16x32_bf16 v[164:167], v[208:211], v[180:183], 0\n"
      "ds_read_b128 v[136:139], v114 offset:18432\n"
      "v_mul_f32_e64 v146, v152, v146\n"
      "v_mfma_f32_16x16x32_bf16 v[164:167], v[50:53], v[62:65], v[164:167]\n"
      "s_mul_i32 s10, s8, 73\n"
      "v_mul_f32 v246, v152, v246\n"
      "v_mul_f32 v144, v152, v144\n"
      "v_mul_f32 v145, v152, v145\n"
      "v_mul_f32 v244, v152, v244\n"
      "v_mfma_f32_16x16x32_bf16 v[172:175], v[188:191], v[180:183], 0\n"
      "s_add_u32 s10, s10, s9\n"
      "v_mul_f32 v147, v152, v147\n"
      "v_cvt_pk_bf16_f32 v144, v144, v145\n"
      "v_mfma_f32_16x16x32_bf16 v[172:175], v[54:57], v[62:65], v[172:175]\n"
      "s_mul_i32 s28, s10, 4802\n"
      "v_mul_f32 v224, v152, v224\n"
      "v_mul_f32 v247, v152, v247\n"
      "v_cvt_pk_bf16_f32 v145, v146, v147\n"
      "v_mfma_f32_16x16x32_bf16 v[168:171], v[220:223], v[180:183], 0\n"
      "s_add_u32 s56, s18, s28\n"
      "v_mul_f32 v245, v152, v245\n"
      "v_cvt_pk_bf16_f32 v146, v244, v245\n"
      "v_mfma_f32_16x16x32_bf16 v[168:171], v[58:61], v[62:65], v[168:171]\n"
      "s_addc_u32 s57, s19, 0\n"
      "v_cvt_pk_bf16_f32 v147, v246, v247\n"
      "v_mul_f32 v225, v152, v225\n"
      "v_mul_f32 v226, v152, v226\n"
      "v_mul_f32_e64 v227, v152, v227\n"
      "v_cvt_pk_bf16_f32 v224, v224, v225\n"
      "v_cvt_pk_bf16_f32 v225, v226, v227\n"
      "s_waitcnt lgkmcnt(1)\n"
      "v_mov_b32_e64 v226, 0\n"
      "v_lshlrev_b32_e64 v153, 16, v153\n"
      "s_waitcnt lgkmcnt(0)\n"
      "v_mfma_f32_16x16x32_bf16 v[148:151], a[4:7], v[136:139], 0\n"
      "s_and_b32 s56, s56, -16\n"
      "s_sub_u32 s58, s56, 110592\n"
      "v_fmac_f32 v213, s37, v153\n"
      "v_mfma_f32_16x16x32_bf16 v[148:151], a[40:43], v[140:143], v[148:151]\n"
      "ds_write_b128 v124, v[144:147] offset:1024\n"
      "v_mfma_f32_16x16x32_bf16 v[144:147], a[0:3], v[136:139], 0\n"
      "s_subb_u32 s59, s57, 0\n"
      "v_max_f32_dpp v227, v213, v213 row_ror:8 row_mask:0xf bank_mask:0xf\n"
      "s_sub_u32 s56, s56, 73728\n"
      "v_mfma_f32_16x16x32_bf16 v[144:147], a[36:39], v[140:143], v[144:147]\n"
      "ds_write2st64_b64 v125, v[200:201], v[224:225] offset0:0 offset1:2\n"
      "v_max_f32_dpp v227, v227, v227 row_ror:4 row_mask:0xf bank_mask:0xf\n"
      "s_subb_u32 s57, s57, 0\n"
      "s_mul_i32 s28, s8, 1373568\n"
      "v_max_f32_dpp v227, v227, v227 row_ror:2 row_mask:0xf bank_mask:0xf\n"
      "v_mfma_f32_16x16x32_bf16 v[156:159], a[12:15], v[136:139], 0\n"
      "s_mul_i32 s29, s9, 2688\n"
      "s_add_u32 s28, s28, s29\n"
      "v_max_f32_dpp v227, v227, v227 row_ror:1 row_mask:0xf bank_mask:0xf\n"
      "v_mfma_f32_16x16x32_bf16 v[152:155], a[8:11], v[136:139], 0\n"
      "s_cmp_eq_u32 s8, 72\n"
      "v_max_f32_dpp v227, v227, v227 row_bcast:15 row_mask:0xa bank_mask:0xf\n"
      "s_cselect_b32 s8, 0, 100270464\n"
      "s_nop 0\n"
      "v_max_f32_dpp v227, v227, v227 row_bcast:31 row_mask:0xc bank_mask:0xf\n"
      "s_nop 0\n"
      "v_readlane_b32 s31, v227, 63\n"
      "s_cmp_eq_u32 s9, 72\n"
      "s_nop 0\n"
      "v_subrev_f32 v213, s31, v213\n"
      "v_exp_f32_e64 v213, v213\n"
      "s_cselect_b32 s9, 0, 196224\n"
      "v_cvt_pk_bf16_f32 v213, v213, v213\n"
      "ds_write_b16 v130, v213\n"
      "ds_read_b128 v[244:247], v131\n"
      "ds_read_b128 v[180:183], v131 offset:64\n"
      "v_mfma_f32_16x16x32_bf16 v[152:155], a[44:47], v[140:143], v[152:155]\n"
      "s_add_u32 s10, s8, s9\n"
      "ds_write_b32 v129, v226\n"
      "v_mfma_f32_16x16x32_bf16 v[156:159], a[48:51], v[140:143], v[156:159]\n"
      "s_add_u32 s28, s28, s10\n"
      "s_add_u32 s52, s16, s28\n"
      "s_addc_u32 s53, s17, 0\n"
      "s_cmp_lg_u32 s10, s11\n"
      "s_cbranch_scc1 L_edge_92_%=\n"
      "L_eb_93_%=:\n"
      "s_waitcnt lgkmcnt(1)\n"
      "v_mfma_f32_16x16x32_bf16 v[224:227], v[244:247], v[188:191], 0\n"
      "v_mfma_f32_16x16x32_bf16 v[224:227], v[180:183], v[54:57], v[224:227]\n"
      "s_nop 0\n"
      "v_mfma_f32_16x16x32_bf16 v[188:191], v[244:247], v[66:69], 0\n"
      "v_mfma_f32_16x16x32_bf16 v[188:191], v[180:183], v[66:69], v[188:191]\n"
      "v_mfma_f32_16x16x32_bf16 v[200:203], v[244:247], v[208:211], 0\n"
      "v_mfma_f32_16x16x32_bf16 v[200:203], v[180:183], v[50:53], v[200:203]\n"
      "v_mfma_f32_16x16x32_bf16 v[208:211], v[244:247], v[220:223], 0\n"
      "v_mfma_f32_16x16x32_bf16 v[208:211], v[180:183], v[58:61], v[208:211]\n"
      "s_nop 3\n"
      "v_mov_b32_dpp v188, v188 quad_perm:[0,0,0,0] row_mask:0xf bank_mask:0xf\n"
      "s_nop 0\n"
      "v_cndmask_b32 v200, v200, v224, s[42:43]\n"
      "v_rcp_f32 v188, v188\n"
      "v_cndmask_b32 v200, v200, v208, s[44:45]\n"
      "v_mul_f32 v200, v188, v200\n"
      "v_cvt_pk_bf16_f32 v200, v200, v200\n"
      "ds_write_b16 v135, v200\n"
      "s_branch L_join_10_%=\n"
      "L_flg_89_%=:\n"
      "v_cmp_gt_i32 vcc, 0, v148\n"
      "s_cbranch_vccz L_fb_90_%=\n"
      "s_sleep 1\n"
      "ds_read_b128 v[148:151], v128 offset:3072\n"
      "s_waitcnt lgkmcnt(0)\n"
      "v_bitop3_b32 v148, v148, v149, s4 bitop3:0x7e\n"
      "v_bitop3_b32 v150, v150, v151, s4 bitop3:0x7e\n"
      "v_or_b32 v148, v148, v150\n"
      "v_cmp_ne_u32 s[48:49], 0, v148\n"
      "s_branch L_flg_89_%=\n"
      "L_edge_92_%=:\n"
      "s_mov_b32 s11, s10\n"
      "s_sub_u32 s28, s60, s8\n"
      "s_sub_u32 s29, s61, s9\n"
      "s_mov_b32 s60, s8\n"
      "s_mov_b32 s61, s9\n"
      "s_mov_b64 exec, s[64:65]\n"
      "v_add_u32 v116, s28, v116\n"
      "s_mov_b64 exec, s[70:71]\n"
      "v_add_u32 v116, s29, v116\n"
      "s_mov_b64 exec, s[66:67]\n"
      "v_add_u32 v117, s28, v117\n"
      "s_mov_b64 exec, s[72:73]\n"
      "v_add_u32 v117, s29, v117\n"
      "s_mov_b64 exec, s[68:69]\n"
      "v_add_u32 v118, s28, v118\n"
      "s_mov_b64 exec, s[74:75]\n"
      "v_add_u32 v118, s29, v118\n"
      "s_mov_b64 exec, -1\n"
      "s_nop 4\n"
      "s_branch L_eb_93_%=\n"
      "L_gen_19_%=:\n"
      "s_waitcnt lgkmcnt(0)\n"
      "s_nop 5\n"
      "s_cmp_eq_u64 s[34:35], 0\n"
      "s_cbranch_scc1 L_ok_114_%=\n"
      "ds_read_b128 v[164:167], v115 offset:18432\n"
      "ds_read_b128 v[196:199], v114 offset:18432\n"
      "s_waitcnt lgkmcnt(0)\n"
      "v_mfma_f32_16x16x32_bf16 v[160:163], a[4:7], v[196:199], 0\n"
      "ds_read_b128 v[248:251], v114 offset:30720\n"
      "v_mfma_f32_16x16x32_bf16 v[160:163], a[40:43], v[164:167], v[160:163]\n"
      "ds_read_b128 v[212:215], v115 offset:24576\n"
      "v_mfma_f32_16x16x32_bf16 v[144:147], a[0:3], v[196:199], 0\n"
      "ds_read_b128 v[200:203], v114 offset:24576\n"
      "v_mfma_f32_16x16x32_bf16 v[144:147], a[36:39], v[164:167], v[144:147]\n"
      "v_mfma_f32_16x16x32_bf16 v[192:195], a[8:11], v[196:199], 0\n"
      "v_mfma_f32_16x16x32_bf16 v[192:195], a[44:47], v[164:167], v[192:195]\n"
      "v_mfma_f32_16x16x32_bf16 v[156:159], a[12:15], v[196:199], 0\n"
      "v_mfma_f32_16x16x32_bf16 v[156:159], a[48:51], v[164:167], v[156:159]\n"
      "v_mfma_f32_16x16x32_bf16 v[152:155], a[20:23], v[196:199], 0\n"
      "v_mfma_f32_16x16x32_bf16 v[152:155], a[56:59], v[164:167], v[152:155]\n"
      "ds_read_b128 v[220:223], v115 offset:30720\n"
      "v_mfma_f32_16x16x32_bf16 v[136:139], a[16:19], v[196:199], 0\n"
      "s_nop 0\n"
      "v_mfma_f32_16x16x32_bf16 v[136:139], a[52:55], v[164:167], v[136:139]\n"
      "s_waitcnt lgkmcnt(1)\n"
      "v_mfma_f32_16x16x32_bf16 v[144:147], a[72:75], v[200:203], v[144:147]\n"
      "v_mfma_f32_16x16x32_bf16 v[144:147], a[108:111], v[212:215], v[144:147]\n"
      "v_mfma_f32_16x16x32_bf16 v[160:163], a[76:79], v[200:203], v[160:163]\n"
      "v_mfma_f32_16x16x32_bf16 v[160:163], a[112:115], v[212:215], v[160:163]\n"
      "v_mfma_f32_16x16x32_bf16 v[156:159], a[84:87], v[200:203], v[156:159]\n"
      "v_mfma_f32_16x16x32_bf16 v[156:159], a[120:123], v[212:215], v[156:159]\n"
      "s_nop 0\n"
      "v_mfma_f32_16x16x32_bf16 v[136:139], a[88:91], v[200:203], v[136:139]\n"
      "s_waitcnt lgkmcnt(0)\n"
      "v_mfma_f32_16x16x32_bf16 v[136:139], a[124:127], v[212:215], v[136:139]\n"
      "v_mfma_f32_16x16x32_bf16 v[152:155], a[92:95], v[200:203], v[152:155]\n"
      "v_mfma_f32_16x16x32_bf16 v[192:195], a[80:83], v[200:203], v[192:195]\n"
      "ds_read_b128 v[164:167], v115 offset:20480\n"
      "v_mfma_f32_16x16x32_bf16 v[192:195], a[116:119], v[212:215], v[192:195]\n"
      "v_mfma_f32_16x16x32_bf16 v[144:147], a[144:147], v[248:251], v[144:147]\n"
      "v_mfma_f32_16x16x32_bf16 v[144:147], a[180:183], v[220:223], v[144:147]\n"
      "v_mfma_f32_16x16x32_bf16 v[192:195], a[152:155], v[248:251], v[192:195]\n"
      "v_mfma_f32_16x16x32_bf16 v[192:195], a[188:191], v[220:223], v[192:195]\n"
      "v_mfma_f32_16x16x32_bf16 v[160:163], a[148:151], v[248:251], v[160:163]\n"
      "v_mfma_f32_16x16x32_bf16 v[160:163], a[184:187], v[220:223], v[160:163]\n"
      "v_mfma_f32_16x16x32_bf16 v[156:159], a[156:159], v[248:251], v[156:159]\n"
      "v_mfma_f32_16x16x32_bf16 v[156:159], a[192:195], v[220:223], v[156:159]\n"
      "v_mfma_f32_16x16x32_bf16 v[152:155], a[128:131], v[212:215], v[152:155]\n"
      "s_nop 0\n"
      "v_fma_f32 v206, v144, v144, s38\n"
      "v_fmac_f32 v206, v145, v145\n"
      "v_mfma_f32_16x16x32_bf16 v[152:155], a[164:167], v[248:251], v[152:155]\n"
      "v_fmac_f32 v206, v146, v146\n"
      "v_mfma_f32_16x16x32_bf16 v[152:155], a[200:203], v[220:223], v[152:155]\n"
      "s_nop 0\n"
      "v_fma_f32 v207, v156, v156, s38\n"
      "v_fmac_f32 v207, v157, v157\n"
      "v_mfma_f32_16x16x32_bf16 v[136:139], a[160:163], v[248:251], v[136:139]\n"
      "v_fmac_f32 v207, v158, v158\n"
      "v_fmac_f32 v206, v147, v147\n"
      "v_mfma_f32_16x16x32_bf16 v[136:139], a[196:199], v[220:223], v[136:139]\n"
      "v_fmac_f32 v206, v160, v160\n"
      "v_fmac_f32 v206, v161, v161\n"
      "v_fmac_f32 v207, v159, v159\n"
      "v_fmac_f32 v206, v162, v162\n"
      "v_fmac_f32 v206, v163, v163\n"
      "v_fmac_f32 v206, v192, v192\n"
      "v_fmac_f32 v206, v193, v193\n"
      "v_fmac_f32 v206, v194, v194\n"
      "v_fmac_f32 v207, v136, v136\n"
      "v_fmac_f32 v207, v137, v137\n"
      "v_fmac_f32 v207, v138, v138\n"
      "v_fmac_f32 v207, v139, v139\n"
      "v_fmac_f32 v207, v152, v152\n"
      "v_fmac_f32 v207, v153, v153\n"
      "v_fmac_f32 v206, v195, v195\n"
      "v_fmac_f32 v207, v154, v154\n"
      "v_fmac_f32 v207, v155, v155\n"
      "s_nop 1\n"
      "v_permlane16_swap_b32 v206, v207\n"
      "v_add_f32 v206, v206, v207\n"
      "v_mov_b32 v207, v206\n"
      "s_nop 1\n"
      "v_permlane32_swap_b32 v206, v207\n"
      "v_add_f32 v206, v206, v207\n"
      "v_rsq_f32 v206, v206\n"
      "ds_read_b128 v[196:199], v114 offset:20480\n"
      "v_mov_b32 v207, v206\n"
      "s_nop 1\n"
      "v_permlane16_swap_b32 v206, v207\n"
      "v_mul_f32 v159, v207, v159\n"
      "v_mul_f32 v157, v207, v157\n"
      "v_mul_f32 v152, v207, v152\n"
      "v_mul_f32 v158, v207, v158\n"
      "v_mul_f32 v156, v207, v156\n"
      "v_mul_f32 v206, s36, v206\n"
      "v_mul_f32 v146, v206, v146\n"
      "v_mul_f32 v139, v207, v139\n"
      "v_mul_f32 v153, v207, v153\n"
      "v_mul_f32 v138, v207, v138\n"
      "v_mul_f32 v145, v206, v145\n"
      "v_mul_f32 v154, v207, v154\n"
      "v_mul_f32 v155, v207, v155\n"
      "v_mul_f32 v136, v207, v136\n"
      "v_cvt_pk_f16_f32 v156, v156, v157\n"
      "v_mul_f32 v194, v206, v194\n"
      "ds_read_b128 v[212:215], v114 offset:26624\n"
      "v_cvt_pk_f16_f32 v34, v152, v153\n"
      "v_cvt_pk_f16_f32 v157, v158, v159\n"
      "v_mul_f32 v137, v207, v137\n"
      "v_mul_f32 v161, v206, v161\n"
      "v_mul_f32 v195, v206, v195\n"
      "v_mul_f32 v160, v206, v160\n"
      "v_cvt_pk_f16_f32 v159, v138, v139\n"
      "v_mul_f32 v192, v206, v192\n"
      "s_waitcnt lgkmcnt(1)\n"
      "v_mfma_f32_16x16x32_bf16 v[140:143], a[12:15], v[196:199], 0\n"
      "v_mul_f32 v144, v206, v144\n"
      "v_mul_f32 v147, v206, v147\n"
      "v_cvt_pk_f16_f32 v144, v144, v145\n"
      "v_mul_f32_e64 v193, v206, v193\n"
      "v_cvt_pk_f16_f32 v145, v146, v147\n"
      "v_cvt_pk_f16_f32 v32, v192, v193\n"
      "v_cvt_pk_f16_f32 v158, v136, v137\n"
      "v_mul_f32 v163, v206, v163\n"
      "v_mfma_f32_16x16x32_bf16 v[188:191], a[0:3], v[196:199], 0\n"
      "ds_read_b128 v[200:203], v115 offset:26624\n"
      "v_mul_f32 v162, v206, v162\n"
      "v_mfma_f32_16x16x32_bf16 v[188:191], a[36:39], v[164:167], v[188:191]\n"
      "v_cvt_pk_f16_f32 v146, v160, v161\n"
      "v_cvt_pk_f16_f32 v35, v154, v155\n"
      "v_cvt_pk_f16_f32 v147, v162, v163\n"
      "v_mfma_f32_16x16x32_bf16 v[160:163], a[4:7], v[196:199], 0\n"
      "v_mfma_f32_16x16x32_bf16 v[160:163], a[40:43], v[164:167], v[160:163]\n"
      "v_mfma_f32_16x16x32_bf16 v[136:139], a[16:19], v[196:199], 0\n"
      "v_mfma_f32_16x16x32_bf16 v[136:139], a[52:55], v[164:167], v[136:139]\n"
      "v_cvt_pk_f16_f32 v33, v194, v195\n"
      "v_mfma_f32_16x16x32_bf16 v[140:143], a[48:51], v[164:167], v[140:143]\n"
      "ds_read_b128 v[248:251], v115 offset:32768\n"
      "v_mfma_f32_16x16x32_bf16 v[192:195], a[8:11], v[196:199], 0\n"
      "v_mfma_f32_16x16x32_bf16 v[192:195], a[44:47], v[164:167], v[192:195]\n"
      "v_mfma_f32_16x16x32_bf16 v[152:155], a[20:23], v[196:199], 0\n"
      "s_waitcnt lgkmcnt(1)\n"
      "v_mfma_f32_16x16x32_bf16 v[152:155], a[56:59], v[164:167], v[152:155]\n"
      "s_nop 0\n"
      "v_mfma_f32_16x16x32_bf16 v[160:163], a[76:79], v[212:215], v[160:163]\n"
      "v_mfma_f32_16x16x32_bf16 v[160:163], a[112:115], v[200:203], v[160:163]\n"
      "v_mfma_f32_16x16x32_bf16 v[140:143], a[84:87], v[212:215], v[140:143]\n"
      "ds_read_b128 v[196:199], v114 offset:32768\n"
      "v_mfma_f32_16x16x32_bf16 v[140:143], a[120:123], v[200:203], v[140:143]\n"
      "v_mfma_f32_16x16x32_bf16 v[188:191], a[72:75], v[212:215], v[188:191]\n"
      "v_mfma_f32_16x16x32_bf16 v[188:191], a[108:111], v[200:203], v[188:191]\n"
      "v_mfma_f32_16x16x32_bf16 v[192:195], a[80:83], v[212:215], v[192:195]\n"
      "v_mfma_f32_16x16x32_bf16 v[192:195], a[116:119], v[200:203], v[192:195]\n"
      "v_mfma_f32_16x16x32_bf16 v[152:155], a[92:95], v[212:215], v[152:155]\n"
      "v_mfma_f32_16x16x32_bf16 v[152:155], a[128:131], v[200:203], v[152:155]\n"
      "v_mfma_f32_16x16x32_bf16 v[136:139], a[88:91], v[212:215], v[136:139]\n"
      "v_mfma_f32_16x16x32_bf16 v[136:139], a[124:127], v[200:203], v[136:139]\n"
      "s_waitcnt lgkmcnt(0)\n"
      "v_mfma_f32_16x16x32_bf16 v[188:191], a[144:147], v[196:199], v[188:191]\n"
      "v_mfma_f32_16x16x32_bf16 v[188:191], a[180:183], v[248:251], v[188:191]\n"
      "s_nop 0\n"
      "v_mfma_f32_16x16x32_bf16 v[160:163], a[148:151], v[196:199], v[160:163]\n"
      "ds_read_b128 v[164:167], v114 offset:22528\n"
      "v_mfma_f32_16x16x32_bf16 v[160:163], a[184:187], v[248:251], v[160:163]\n"
      "v_mfma_f32_16x16x32_bf16 v[192:195], a[152:155], v[196:199], v[192:195]\n"
      "v_mfma_f32_16x16x32_bf16 v[192:195], a[188:191], v[248:251], v[192:195]\n"
      "s_nop 0\n"
      "v_mfma_f32_16x16x32_bf16 v[136:139], a[160:163], v[196:199], v[136:139]\n"
      "s_nop 1\n"
      "v_fma_f32 v206, v188, v188, s38\n"
      "v_mfma_f32_16x16x32_bf16 v[136:139], a[196:199], v[248:251], v[136:139]\n"
      "v_mfma_f32_16x16x32_bf16 v[140:143], a[156:159], v[196:199], v[140:143]\n"
      "v_fmac_f32 v206, v189, v189\n"
      "v_fmac_f32 v206, v190, v190\n"
      "v_mfma_f32_16x16x32_bf16 v[140:143], a[192:195], v[248:251], v[140:143]\n"
      "v_fmac_f32 v206, v191, v191\n"
      "v_fmac_f32 v206, v160, v160\n"
      "v_mfma_f32_16x16x32_bf16 v[152:155], a[164:167], v[196:199], v[152:155]\n"
      "v_fmac_f32_e64 v206, v161, v161\n"
      "v_mfma_f32_16x16x32_bf16 v[152:155], a[200:203], v[248:251], v[152:155]\n"
      "ds_read_b128 v[196:199], v115 offset:22528\n"
      "v_fmac_f32 v206, v162, v162\n"
      "v_fmac_f32 v206, v163, v163\n"
      "v_fma_f32 v207, v140, v140, s38\n"
      "v_fmac_f32 v207, v141, v141\n"
      "v_fmac_f32 v206, v192, v192\n"
      "v_fmac_f32 v207, v142, v142\n"
      "v_fmac_f32 v206, v193, v193\n"
      "v_fmac_f32 v207, v143, v143\n"
      "v_fmac_f32 v207, v136, v136\n"
      "v_fmac_f32 v207, v137, v137\n"
      "v_fmac_f32 v207, v138, v138\n"
      "v_fmac_f32 v207, v139, v139\n"
      "v_fmac_f32 v207, v152, v152\n"
      "v_fmac_f32 v207, v153, v153\n"
      "v_fmac_f32 v206, v194, v194\n"
      "v_fmac_f32 v207, v154, v154\n"
      "v_fmac_f32 v206, v195, v195\n"
      "v_fmac_f32 v207, v155, v155\n"
      "s_nop 1\n"
      "v_permlane16_swap_b32 v206, v207\n"
      "v_add_f32 v206, v206, v207\n"
      "v_mov_b32 v207, v206\n"
      "s_nop 1\n"
      "v_permlane32_swap_b32 v206, v207\n"
      "v_add_f32 v206, v206, v207\n"
      "v_rsq_f32 v206, v206\n"
      "s_waitcnt lgkmcnt(0)\n"
      "v_mfma_f32_16x16x32_bf16 v[208:211], a[0:3], v[164:167], 0\n"
      "ds_read_b128 v[200:203], v114 offset:28672\n"
      "v_mov_b32 v207, v206\n"
      "v_mfma_f32_16x16x32_bf16 v[208:211], a[36:39], v[196:199], v[208:211]\n"
      "ds_read_b128 v[212:215], v115 offset:28672\n"
      "v_permlane16_swap_b32 v206, v207\n"
      "v_mfma_f32_16x16x32_bf16 v[216:219], a[12:15], v[164:167], 0\n"
      "ds_read_b128 v[248:251], v115 offset:34816\n"
      "v_mul_f32 v141, v207, v141\n"
      "v_mfma_f32_16x16x32_bf16 v[216:219], a[48:51], v[196:199], v[216:219]\n"
      "v_mul_f32 v137, v207, v137\n"
      "v_mul_f32 v140, v207, v140\n"
      "v_cvt_pk_f16_f32 v140, v140, v141\n"
      "v_mul_f32 v143, v207, v143\n"
      "v_mul_f32 v142, v207, v142\n"
      "v_mul_f32 v138, v207, v138\n"
      "v_cvt_pk_f16_f32 v141, v142, v143\n"
      "v_mul_f32 v139, v207, v139\n"
      "v_cvt_pk_f16_f32 v143, v138, v139\n"
      "v_mfma_f32_16x16x32_bf16 v[220:223], a[20:23], v[164:167], 0\n"
      "v_mul_f32 v136, v207, v136\n"
      "v_cvt_pk_f16_f32 v142, v136, v137\n"
      "v_mfma_f32_16x16x32_bf16 v[136:139], a[16:19], v[164:167], 0\n"
      "v_mul_f32 v155, v207, v155\n"
      "v_mul_f32 v152, v207, v152\n"
      "v_mfma_f32_16x16x32_bf16 v[136:139], a[52:55], v[196:199], v[136:139]\n"
      "ds_read_b128 v[228:231], v114 offset:34816\n"
      "v_mul_f32 v153, v207, v153\n"
      "v_mul_f32 v206, s36, v206\n"
      "v_mul_f32 v154, v207, v154\n"
      "v_mfma_f32_16x16x32_bf16 v[220:223], a[56:59], v[196:199], v[220:223]\n"
      "s_waitcnt lgkmcnt(2)\n"
      "v_cvt_pk_f16_f32 v41, v154, v155\n"
      "v_cvt_pk_f16_f32 v40, v152, v153\n"
      "v_mfma_f32_16x16x32_bf16 v[208:211], a[72:75], v[200:203], v[208:211]\n"
      "v_mul_f32 v188, v206, v188\n"
      "v_mul_f32 v189, v206, v189\n"
      "v_mfma_f32_16x16x32_bf16 v[208:211], a[108:111], v[212:215], v[208:211]\n"
      "v_mul_f32 v190, v206, v190\n"
      "v_mul_f32 v160, v206, v160\n"
      "v_mfma_f32_16x16x32_bf16 v[136:139], a[88:91], v[200:203], v[136:139]\n"
      "v_mul_f32 v163, v206, v163\n"
      "v_mul_f32 v193, v206, v193\n"
      "v_mfma_f32_16x16x32_bf16 v[136:139], a[124:127], v[212:215], v[136:139]\n"
      "v_mul_f32 v191, v206, v191\n"
      "v_cvt_pk_f16_f32 v188, v188, v189\n"
      "v_mfma_f32_16x16x32_bf16 v[216:219], a[84:87], v[200:203], v[216:219]\n"
      "v_cvt_pk_f16_f32 v189, v190, v191\n"
      "v_mul_f32 v161, v206, v161\n"
      "v_mfma_f32_16x16x32_bf16 v[216:219], a[120:123], v[212:215], v[216:219]\n"
      "v_mul_f32 v192, v206, v192\n"
      "v_mul_f32 v162, v206, v162\n"
      "v_mfma_f32_16x16x32_bf16 v[220:223], a[92:95], v[200:203], v[220:223]\n"
      "v_cvt_pk_f16_f32 v190, v160, v161\n"
      "v_mul_f32 v194, v206, v194\n"
      "v_mfma_f32_16x16x32_bf16 v[220:223], a[128:131], v[212:215], v[220:223]\n"
      "v_mul_f32 v195, v206, v195\n"
      "v_cvt_pk_f16_f32 v191, v162, v163\n"
      "v_mfma_f32_16x16x32_bf16 v[160:163], a[4:7], v[164:167], 0\n"
      "s_waitcnt lgkmcnt(0)\n"
      "v_cvt_pk_f16_f32 v39, v194, v195\n"
      "v_mfma_f32_16x16x32_bf16 v[160:163], a[40:43], v[196:199], v[160:163]\n"
      "s_nop 0\n"
      "v_cvt_pk_f16_f32 v38, v192, v193\n"
      "v_mfma_f32_16x16x32_bf16 v[192:195], a[8:11], v[164:167], 0\n"
      "v_mfma_f32_16x16x32_bf16 v[192:195], a[44:47], v[196:199], v[192:195]\n"
      "v_mfma_f32_16x16x32_bf16 v[192:195], a[80:83], v[200:203], v[192:195]\n"
      "v_mfma_f32_16x16x32_bf16 v[192:195], a[116:119], v[212:215], v[192:195]\n"
      "v_mfma_f32_16x16x32_bf16 v[160:163], a[76:79], v[200:203], v[160:163]\n"
      "v_mfma_f32_16x16x32_bf16 v[160:163], a[112:115], v[212:215], v[160:163]\n"
      "v_mfma_f32_16x16x32_bf16 v[160:163], a[148:151], v[228:231], v[160:163]\n"
      "v_mfma_f32_16x16x32_bf16 v[160:163], a[184:187], v[248:251], v[160:163]\n"
      "v_mfma_f32_16x16x32_bf16 v[208:211], a[144:147], v[228:231], v[208:211]\n"
      "v_mfma_f32_16x16x32_bf16 v[208:211], a[180:183], v[248:251], v[208:211]\n"
      "v_mfma_f32_16x16x32_bf16 v[216:219], a[156:159], v[228:231], v[216:219]\n"
      "v_mfma_f32_16x16x32_bf16 v[216:219], a[192:195], v[248:251], v[216:219]\n"
      "v_mfma_f32_16x16x32_bf16 v[136:139], a[160:163], v[228:231], v[136:139]\n"
      "v_mfma_f32_16x16x32_bf16 v[136:139], a[196:199], v[248:251], v[136:139]\n"
      "v_mfma_f32_16x16x32_bf16 v[192:195], a[152:155], v[228:231], v[192:195]\n"
      "v_mfma_f32_16x16x32_bf16 v[192:195], a[188:191], v[248:251], v[192:195]\n"
      "s_nop 1\n"
      "v_fma_f32 v206, v208, v208, s38\n"
      "v_mfma_f32_16x16x32_bf16 v[220:223], a[164:167], v[228:231], v[220:223]\n"
      "v_fma_f32 v207, v216, v216, s38\n"
      "v_fmac_f32 v207, v217, v217\n"
      "v_mfma_f32_16x16x32_bf16 v[220:223], a[200:203], v[248:251], v[220:223]\n"
      "v_fmac_f32 v206, v209, v209\n"
      "v_fmac_f32 v207, v218, v218\n"
      "v_fmac_f32 v207, v219, v219\n"
      "v_fmac_f32 v206, v210, v210\n"
      "v_mfma_f32_16x16x32_f16 v[164:167], v[148:151], v[144:147], v[70:73]\n"
      "v_fmac_f32 v206, v211, v211\n"
      "v_fmac_f32 v206, v160, v160\n"
      "v_mfma_f32_16x16x32_f16 v[164:167], v[244:247], v[156:159], v[164:167]\n"
      "v_fmac_f32 v206, v161, v161\n"
      "v_fmac_f32 v207, v136, v136\n"
      "v_mfma_f32_16x16x32_f16 v[164:167], v[240:243], v[32:35], v[164:167]\n"
      "v_fmac_f32 v207, v137, v137\n"
      "v_fmac_f32 v207, v138, v138\n"
      "v_fmac_f32 v207, v139, v139\n"
      "v_fmac_f32 v207, v220, v220\n"
      "v_fmac_f32 v206, v162, v162\n"
      "v_fmac_f32 v206, v163, v163\n"
      "v_fmac_f32 v207, v221, v221\n"
      "v_fmac_f32 v207, v222, v222\n"
      "v_fmac_f32 v207, v223, v223\n"
      "v_fmac_f32 v206, v192, v192\n"
      "v_fmac_f32 v206, v193, v193\n"
      "v_fmac_f32 v206, v194, v194\n"
      "v_fmac_f32 v206, v195, v195\n"
      "v_mov_b32 v205, v165\n"
      "s_nop 0\n"
      "v_permlane16_swap_b32 v206, v207\n"
      "v_add_f32 v206, v206, v207\n"
      "v_mov_b32 v207, v206\n"
      "s_nop 1\n"
      "v_permlane32_swap_b32 v206, v207\n"
      "v_add_f32 v206, v206, v207\n"
      "v_rsq_f32 v206, v206\n"
      "s_nop 0\n"
      "v_mov_b32 v207, v206\n"
      "v_mov_b32 v204, v164\n"
      "s_nop 0\n"
      "v_permlane16_swap_b32 v206, v207\n"
      "v_mul_f32 v218, v207, v218\n"
      "v_mul_f32 v220, v207, v220\n"
      "v_mul_f32 v217, v207, v217\n"
      "v_mul_f32 v219, v207, v219\n"
      "v_mul_f32 v221, v207, v221\n"
      "v_mul_f32 v206, s36, v206\n"
      "v_mul_f32 v211, v206, v211\n"
      "v_mul_f32 v216, v207, v216\n"
      "v_cvt_pk_f16_f32 v216, v216, v217\n"
      "v_mul_f32 v138, v207, v138\n"
      "v_mul_f32_e64 v222, v207, v222\n"
      "v_cvt_pk_f16_f32 v217, v218, v219\n"
      "v_mul_f32 v139, v207, v139\n"
      "v_cvt_pk_f16_f32 v46, v220, v221\n"
      "v_mul_f32 v136, v207, v136\n"
      "v_mul_f32_e64 v194, v206, v194\n"
      "v_cvt_pk_f16_f32 v219, v138, v139\n"
      "v_mul_f32 v137, v207, v137\n"
      "v_mul_f32 v223, v207, v223\n"
      "v_cvt_pk_f16_f32 v218, v136, v137\n"
      "v_mul_f32 v163, v206, v163\n"
      "v_mul_f32 v192, v206, v192\n"
      "v_mul_f32 v208, v206, v208\n"
      "v_cvt_pk_f16_f32 v47, v222, v223\n"
      "v_mul_f32 v209, v206, v209\n"
      "v_mul_f32 v161, v206, v161\n"
      "v_mul_f32 v210, v206, v210\n"
      "v_mfma_f32_16x16x32_f16 v[136:139], v[216:219], v[144:147], v[86:89]\n"
      "v_cvt_pk_f16_f32 v208, v208, v209\n"
      "v_cvt_pk_f16_f32 v209, v210, v211\n"
      "v_mfma_f32_16x16x32_f16 v[136:139], v[46:49], v[32:35], v[136:139]\n"
      "v_mul_f32_e64 v160, v206, v160\n"
      "v_mul_f32_e64 v162, v206, v162\n"
      "v_cvt_pk_f16_f32 v210, v160, v161\n"
      "v_cvt_pk_f16_f32 v211, v162, v163\n"
      "v_mfma_f32_16x16x32_f16 v[160:163], v[156:159], v[144:147], v[78:81]\n"
      "v_mul_f32_e64 v195, v206, v195\n"
      "v_mul_f32_e64 v193, v206, v193\n"
      "v_mfma_f32_16x16x32_f16 v[160:163], v[34:37], v[32:35], v[160:163]\n"
      "v_cvt_pk_f16_f32 v45, v194, v195\n"
      "v_cvt_pk_f16_f32 v44, v192, v193\n"
      "v_mfma_f32_16x16x32_f16 v[192:195], v[140:143], v[144:147], v[82:85]\n"
      "s_nop 0\n"
      "v_mfma_f32_16x16x32_f16 v[192:195], v[40:43], v[32:35], v[192:195]\n"
      "s_nop 7\n"
      "s_or_b64 vcc, s[48:49], s[50:51]\n"
      "s_cbranch_scc0 L_fast_22_%=\n"
      "L_ok_114_%=:\n"
      "s_cmp_eq_u64 s[48:49], 0\n"
      "s_cbranch_scc1 L_go_115_%=\n"
      "s_mov_b32 m0, s27\n"
      "s_nop 0\n"
      "s_movrels_b32 s9, s80\n"
      "s_lshr_b32 s8, s9, 8\n"
      "s_and_b32 s9, s9, 0xff\n"
      "s_mul_i32 s10, s8, 73\n"
      "s_add_u32 s10, s10, s9\n"
      "s_mul_i32 s30, s10, 4802\n"
      "s_add_u32 s30, s30, s18\n"
      "s_and_b32 s29, s30, 15\n"
      "s_add_u32 s30, s29, 115456\n"
      "s_lshl_b64 s[34:35], s[48:49], 4\n"
      "s_lshr_b64 s[34:35], s[34:35], 5\n"
      "s_cbranch_scc1 L_gen_20_%=\n"
      "v_mbcnt_lo_u32_b32 v206, -1, 0\n"
      "v_mbcnt_hi_u32_b32 v206, -1, v206\n"
      "v_lshlrev_b32 v207, 1, v206\n"
      "v_cmp_gt_u32 vcc, s29, v207\n"
      "v_add_u32 v165, 4802, v207\n"
      "s_nop 0\n"
      "v_cndmask_b32 v207, v165, v207, vcc\n"
      "v_add_u32 v207, 115456, v207\n"
      "v_mov_b32 v165, 0\n"
      "s_mov_b64 exec, 0x7f\n"
      "ds_write_b16 v207, v165\n"
      "s_mov_b64 exec, -1\n"
      "v_lshlrev_b32 v207, 4, v206\n"
      "v_add_u32 v207, 115456, v207\n"
      "ds_read_b128 v[164:167], v207 offset:1024\n"
      "ds_read_b128 v[196:199], v207\n"
      "v_min_u32 v206, 116160, v207\n"
      "ds_read_b128 v[200:203], v207 offset:2048\n"
      "ds_read_b128 v[212:215], v207 offset:3072\n"
      "ds_read_b128 v[144:147], v206 offset:4096\n"
      "s_waitcnt lgkmcnt(4)\n"
      "s_waitcnt lgkmcnt(3)\n"
      "v_or3_b32 v196, v196, v197, v198\n"
      "v_or3_b32 v196, v196, v199, v164\n"
      "v_or3_b32 v196, v196, v165, v166\n"
      "s_waitcnt lgkmcnt(2)\n"
      "v_or3_b32 v196, v196, v167, v200\n"
      "v_or3_b32 v196, v196, v201, v202\n"
      "s_waitcnt lgkmcnt(1)\n"
      "s_waitcnt lgkmcnt(0)\n"
      "v_or3_b32 v196, v196, v203, v212\n"
      "v_or3_b32 v196, v196, v213, v214\n"
      "v_or3_b32 v196, v196, v215, v144\n"
      "v_or3_b32 v196, v196, v145, v146\n"
      "v_bitop3_b32 v196, v196, v147, s40 bitop3:0xa8\n"
      "v_cmp_ne_u32 s[48:49], 0, v196\n"
      "s_cmp_lg_u64 s[48:49], 0\n"
      "s_cbranch_scc1 L_gen_20_%=\n"
      "s_cmp_eq_u64 s[50:51], 0\n"
      "s_cbranch_scc1 L_fast_22_%=\n"
      "L_go_115_%=:\n"
      "v_mfma_f32_16x16x32_f16 v[164:167], v[156:159], v[188:191], v[90:93]\n"
      "ds_read_b128 v[196:199], v128 offset:2048\n"
      "v_max3_f32 v206, v163, v192, v193\n"
      "v_mfma_f32_16x16x32_f16 v[164:167], v[34:37], v[38:41], v[164:167]\n"
      "v_max3_f32 v206, v206, v136, v137\n"
      "v_max3_f32 v207, v160, v161, v162\n"
      "v_mfma_f32_16x16x32_f16 v[200:203], v[140:143], v[188:191], v[94:97]\n"
      "v_max3_f32 v207, v207, v194, v195\n"
      "v_max3_f32 v207, v207, v138, v139\n"
      "v_mfma_f32_16x16x32_f16 v[200:203], v[40:43], v[38:41], v[200:203]\n"
      "v_max3_f32 v207, v207, v206, v204\n"
      "v_mov_b32 v206, v207\n"
      "v_mfma_f32_16x16x32_f16 v[212:215], v[148:151], v[188:191], v[72:75]\n"
      "s_nop 0\n"
      "v_permlane16_swap_b32 v207, v206\n"
      "v_mfma_f32_16x16x32_f16 v[212:215], v[244:247], v[140:143], v[212:215]\n"
      "v_max_f32 v207, v207, v206\n"
      "v_mov_b32 v206, v207\n"
      "v_mfma_f32_16x16x32_f16 v[212:215], v[240:243], v[38:41], v[212:215]\n"
      "s_nop 0\n"
      "v_permlane32_swap_b32 v207, v206\n"
      "v_max_f32 v207, v207, v206\n"
      "v_mfma_f32_16x16x32_f16 v[144:147], v[216:219], v[188:191], v[98:101]\n"
      "v_sub_f32 v204, v204, v207\n"
      "v_sub_f32 v194, v194, v207\n"
      "v_mfma_f32_16x16x32_f16 v[144:147], v[46:49], v[38:41], v[144:147]\n"
      "s_waitcnt lgkmcnt(0)\n"
      "v_exp_f32 v194, v194\n"
      "v_sub_f32 v137, v137, v207\n"
      "v_sub_f32 v163, v163, v207\n"
      "v_sub_f32 v161, v161, v207\n"
      "v_sub_f32 v160, v160, v207\n"
      "v_exp_f32 v160, v160\n"
      "v_sub_f32 v139, v139, v207\n"
      "v_sub_f32 v136, v136, v207\n"
      "v_bitop3_b32 v196, v196, v197, s4 bitop3:0x7e\n"
      "v_bitop3_b32 v198, v198, v199, s4 bitop3:0x7e\n"
      "v_sub_f32 v162, v162, v207\n"
      "v_exp_f32 v161, v161\n"
      "v_sub_f32 v138, v138, v207\n"
      "v_cvt_pk_bf16_f32 v160, v160, v161\n"
      "v_exp_f32 v162, v162\n"
      "v_exp_f32 v163, v163\n"
      "v_exp_f32 v137, v137\n"
      "v_exp_f32 v138, v138\n"
      "v_cvt_pk_bf16_f32 v161, v162, v163\n"
      "v_exp_f32 v136, v136\n"
      "v_exp_f32 v139, v139\n"
      "v_or_b32 v196, v196, v198\n"
      "v_sub_f32 v195, v195, v207\n"
      "v_sub_f32 v193, v193, v207\n"
      "v_exp_f32 v204, v204\n"
      "v_exp_f32 v193, v193\n"
      "v_sub_f32 v192, v192, v207\n"
      "v_exp_f32 v195, v195\n"
      "v_exp_f32_e64 v192, v192\n"
      "v_cvt_pk_bf16_f32 v62, v136, v137\n"
      "v_cvt_pk_bf16_f32 v63, v138, v139\n"
      "v_cvt_pk_bf16_f32 v163, v194, v195\n"
      "v_cvt_pk_bf16_f32 v162, v192, v193\n"
      "v_cmp_ne_u32 vcc, 0, v196\n"
      "v_cvt_pk_bf16_f32 v64, v204, 0\n"
      "v_mfma_f32_16x16x32_bf16 v[136:139], v[66:69], v[160:163], 0\n"
      "s_mov_b64 s[48:49], vcc\n"
      "v_max3_f32 v215, v167, v200, v201\n"
      "v_max3_f32 v214, v164, v165, v166\n"
      "v_mfma_f32_16x16x32_bf16 v[136:139], v[66:69], v[62:65], v[136:139]\n"
      "s_cbranch_vccnz L_flg_116_%=\n"
      "L_fb_117_%=:\n"
      "v_mfma_f32_16x16x32_bf16 v[192:195], v[232:235], v[160:163], 0\n"
      "v_max3_f32 v214, v214, v202, v203\n"
      "v_max3_f32 v214, v214, v146, v147\n"
      "v_mfma_f32_16x16x32_bf16 v[192:195], v[50:53], v[62:65], v[192:195]\n"
      "v_max3_f32 v215, v215, v144, v145\n"
      "v_max3_f32 v214, v214, v215, v212\n"
      "v_mov_b32 v215, v214\n"
      "v_mfma_f32_16x16x32_bf16 v[188:191], v[224:227], v[160:163], 0\n"
      "s_nop 0\n"
      "v_permlane16_swap_b32 v214, v215\n"
      "v_mfma_f32_16x16x32_bf16 v[188:191], v[54:57], v[62:65], v[188:191]\n"
      "v_max_f32 v214, v214, v215\n"
      "v_mov_b32 v215, v214\n"
      "v_mfma_f32_16x16x32_bf16 v[248:251], v[172:175], v[160:163], 0\n"
      "s_nop 0\n"
      "v_permlane32_swap_b32 v214, v215\n"
      "v_mfma_f32_16x16x32_bf16 v[248:251], v[58:61], v[62:65], v[248:251]\n"
      "v_max_f32 v214, v214, v215\n"
      "v_sub_f32 v203, v203, v214\n"
      "v_mfma_f32_16x16x32_f16 v[196:199], v[156:159], v[208:211], v[102:105]\n"
      "v_sub_f32 v146, v146, v214\n"
      "v_sub_f32 v212, v212, v214\n"
      "v_mfma_f32_16x16x32_f16 v[196:199], v[34:37], v[44:47], v[196:199]\n"
      "v_sub_f32 v202, v202, v214\n"
      "v_sub_f32 v164, v164, v214\n"
      "v_sub_f32 v201, v201, v214\n"
      "v_sub_f32 v165, v165, v214\n"
      "v_sub_f32 v145, v145, v214\n"
      "v_sub_f32 v144, v144, v214\n"
      "v_sub_f32 v166, v166, v214\n"
      "v_exp_f32 v164, v164\n"
      "v_sub_f32 v147, v147, v214\n"
      "v_exp_f32 v165, v165\n"
      "v_sub_f32 v200, v200, v214\n"
      "v_sub_f32_e64 v167, v167, v214\n"
      "v_mfma_f32_16x16x32_f16 v[160:163], v[140:143], v[208:211], v[106:109]\n"
      "ds_read_b128 v[140:143], v120\n"
      "v_cvt_pk_bf16_f32 v164, v164, v165\n"
      "v_mfma_f32_16x16x32_f16 v[160:163], v[40:43], v[44:47], v[160:163]\n"
      "v_exp_f32 v167, v167\n"
      "v_exp_f32 v166, v166\n"
      "v_exp_f32 v200, v200\n"
      "v_mfma_f32_16x16x32_f16 v[156:159], v[148:151], v[208:211], v[74:77]\n"
      "v_exp_f32 v202, v202\n"
      "v_mfma_f32_16x16x32_f16 v[156:159], v[244:247], v[216:219], v[156:159]\n"
      "v_exp_f32 v201, v201\n"
      "v_mfma_f32_16x16x32_f16 v[156:159], v[240:243], v[44:47], v[156:159]\n"
      "v_cvt_pk_bf16_f32 v165, v166, v167\n"
      "v_exp_f32 v203, v203\n"
      "v_exp_f32_e64 v144, v144\n"
      "v_cvt_pk_bf16_f32 v167, v202, v203\n"
      "v_cvt_pk_bf16_f32 v166, v200, v201\n"
      "v_mfma_f32_16x16x32_f16 v[220:223], v[216:219], v[208:211], v[110:113]\n"
      "v_exp_f32_e64 v145, v145\n"
      "v_mfma_f32_16x16x32_f16 v[220:223], v[46:49], v[44:47], v[220:223]\n"
      "s_waitcnt lgkmcnt(0)\n"
      "v_exp_f32 v146, v146\n"
      "v_rcp_f32 v136, v136\n"
      "v_exp_f32 v147, v147\n"
      "v_mfma_f32_16x16x32_f16 v[200:203], v[244:247], v[148:151], v[76:79]\n"
      "global_store_dwordx4 v117, v[140:143], s[54:55] sc1\n"
      "v_exp_f32 v212, v212\n"
      "v_cvt_pk_bf16_f32 v63, v146, v147\n"
      "v_cvt_pk_bf16_f32 v62, v144, v145\n"
      "v_mul_f32 v194, v136, v194\n"
      "v_mul_f32_e64 v189, v136, v189\n"
      "v_mfma_f32_16x16x16_f16 v[200:203], v[242:243], v[240:241], v[200:203]\n"
      "ds_read_b128 v[208:211], v121\n"
      "v_cvt_pk_bf16_f32 v64, v212, 0\n"
      "v_mfma_f32_16x16x32_bf16 v[144:147], v[66:69], v[164:167], 0\n"
      "v_mul_f32 v193, v136, v193\n"
      "v_mul_f32 v192, v136, v192\n"
      "v_mfma_f32_16x16x32_bf16 v[144:147], v[66:69], v[62:65], v[144:147]\n"
      "v_mul_f32_e64 v188, v136, v188\n"
      "v_cvt_pk_bf16_f32 v192, v192, v193\n"
      "v_mfma_f32_16x16x32_bf16 v[214:217], v[232:235], v[164:167], 0\n"
      "v_mul_f32_e64 v195, v136, v195\n"
      "v_cvt_pk_bf16_f32 v193, v194, v195\n"
      "v_mfma_f32_16x16x32_bf16 v[214:217], v[50:53], v[62:65], v[214:217]\n"
      "v_mul_f32 v250, v136, v250\n"
      "v_cvt_pk_bf16_f32 v194, v188, v189\n"
      "v_mfma_f32_16x16x32_bf16 v[240:243], v[224:227], v[164:167], 0\n"
      "v_mul_f32 v191, v136, v191\n"
      "v_mul_f32 v190, v136, v190\n"
      "v_mfma_f32_16x16x32_bf16 v[240:243], v[54:57], v[62:65], v[240:243]\n"
      "v_cvt_pk_bf16_f32 v195, v190, v191\n"
      "v_mul_f32 v249, v136, v249\n"
      "v_mul_f32_e64 v248, v136, v248\n"
      "v_mfma_f32_16x16x32_bf16 v[146:149], v[172:175], v[164:167], 0\n"
      "ds_read_b128 v[244:247], v123 offset:4096\n"
      "v_mul_f32_e64 v251, v136, v251\n"
      "v_mfma_f32_16x16x32_bf16 v[146:149], v[58:61], v[62:65], v[146:149]\n"
      "ds_write_b128 v124, v[192:195] offset:18432\n"
      "v_max3_f32 v145, v196, v197, v198\n"
      "v_max3_f32 v212, v199, v160, v161\n"
      "v_max3_f32 v145, v145, v162, v163\n"
      "v_max3_f32 v212, v212, v220, v221\n"
      "v_max3_f32 v145, v145, v222, v223\n"
      "v_max3_f32 v145, v145, v212, v156\n"
      "v_mov_b32_e64 v212, v145\n"
      "v_cvt_pk_bf16_f32 v248, v248, v249\n"
      "v_rcp_f32 v144, v144\n"
      "v_permlane16_swap_b32 v145, v212\n"
      "v_mul_f32 v216, v144, v216\n"
      "v_max_f32 v145, v145, v212\n"
      "v_mov_b32 v212, v145\n"
      "v_mul_f32 v214, v144, v214\n"
      "v_mul_f32 v217, v144, v217\n"
      "v_permlane32_swap_b32 v145, v212\n"
      "v_max_f32 v145, v145, v212\n"
      "v_sub_f32 v199, v199, v145\n"
      "v_sub_f32 v163, v163, v145\n"
      "v_sub_f32 v221, v221, v145\n"
      "v_sub_f32 v160, v160, v145\n"
      "v_sub_f32 v222, v222, v145\n"
      "v_sub_f32 v197, v197, v145\n"
      "v_sub_f32 v196, v196, v145\n"
      "v_sub_f32 v223, v223, v145\n"
      "v_sub_f32 v198, v198, v145\n"
      "v_exp_f32 v198, v198\n"
      "v_exp_f32 v196, v196\n"
      "v_sub_f32 v161, v161, v145\n"
      "v_exp_f32 v197, v197\n"
      "v_mul_f32 v215, v144, v215\n"
      "v_exp_f32 v199, v199\n"
      "v_exp_f32 v222, v222\n"
      "v_exp_f32 v221, v221\n"
      "v_sub_f32 v220, v220, v145\n"
      "v_sub_f32 v162, v162, v145\n"
      "v_exp_f32 v220, v220\n"
      "v_exp_f32 v162, v162\n"
      "v_cvt_pk_bf16_f32 v196, v196, v197\n"
      "v_exp_f32 v160, v160\n"
      "v_exp_f32 v161, v161\n"
      "v_sub_f32 v156, v156, v145\n"
      "v_cvt_pk_bf16_f32 v62, v220, v221\n"
      "v_exp_f32 v156, v156\n"
      "v_cvt_pk_bf16_f32 v197, v198, v199\n"
      "v_exp_f32 v163, v163\n"
      "v_exp_f32 v223, v223\n"
      "v_cvt_pk_bf16_f32 v198, v160, v161\n"
      "v_cvt_pk_bf16_f32 v199, v162, v163\n"
      "v_cvt_pk_bf16_f32 v249, v250, v251\n"
      "v_mul_f32 v241, v144, v241\n"
      "s_waitcnt lgkmcnt(0)\n"
      "v_cvt_pk_bf16_f32 v64, v156, 0\n"
      "v_cvt_pk_bf16_f32 v63, v222, v223\n"
      "v_mfma_f32_16x16x32_bf16 v[136:139], v[66:69], v[196:199], 0\n"
      "ds_read_b128 v[250:253], v123 offset:1024\n"
      "v_cvt_pk_bf16_f32 v214, v214, v215\n"
      "v_mfma_f32_16x16x32_bf16 v[136:139], v[66:69], v[62:65], v[136:139]\n"
      "ds_read_b128 v[220:223], v123 offset:7168\n"
      "v_mul_f32_e64 v242, v144, v242\n"
      "v_mfma_f32_16x16x32_bf16 v[140:143], v[232:235], v[196:199], 0\n"
      "ds_read_b128 v[152:155], v123 offset:10240\n"
      "v_cvt_pk_bf16_f32 v215, v216, v217\n"
      "v_mfma_f32_16x16x32_bf16 v[140:143], v[50:53], v[62:65], v[140:143]\n"
      "ds_read_b128 v[228:231], v123 offset:16384\n"
      "v_mul_f32_e64 v146, v144, v146\n"
      "v_mfma_f32_16x16x32_bf16 v[160:163], v[224:227], v[196:199], 0\n"
      "v_mul_f32 v240, v144, v240\n"
      "v_cvt_pk_bf16_f32 v216, v240, v241\n"
      "v_mfma_f32_16x16x32_bf16 v[160:163], v[54:57], v[62:65], v[160:163]\n"
      "s_mov_b32 exec_hi, 0\n"
      "global_store_dwordx4 v118, v[208:211], s[54:55] sc1\n"
      "s_mov_b32 exec_hi, -1\n"
      "v_mul_f32 v149, v144, v149\n"
      "v_mfma_f32_16x16x32_bf16 v[164:167], v[172:175], v[196:199], 0\n"
      "ds_read_b128 v[236:239], v123 offset:13312\n"
      "v_mul_f32_e64 v243, v144, v243\n"
      "v_mfma_f32_16x16x32_bf16 v[164:167], v[58:61], v[62:65], v[164:167]\n"
      "s_add_u32 s28, s27, 2\n"
      "s_waitcnt lgkmcnt(4)\n"
      "v_cvt_pk_bf16_f32 v217, v242, v243\n"
      "v_mfma_f32_16x16x32_bf16 v[188:191], a[216:219], v[250:253], 0\n"
      "ds_read_b128 v[240:243], v123 offset:2048\n"
      "v_mul_f32_e64 v147, v144, v147\n"
      "v_mfma_f32_16x16x32_bf16 v[188:191], a[228:231], v[244:247], v[188:191]\n"
      "ds_write_b128 v124, v[214:217] offset:19456\n"
      "v_mfma_f32_16x16x32_bf16 v[192:195], a[220:223], v[250:253], 0\n"
      "ds_read_b128 v[216:219], v123 offset:5120\n"
      "v_cvt_pk_bf16_f32 v146, v146, v147\n"
      "v_mfma_f32_16x16x32_bf16 v[192:195], a[232:235], v[244:247], v[192:195]\n"
      "s_waitcnt lgkmcnt(4)\n"
      "s_min_u32 m0, s28, s3\n"
      "v_mul_f32_e64 v148, v144, v148\n"
      "v_mfma_f32_16x16x32_bf16 v[188:191], a[240:243], v[220:223], v[188:191]\n"
      "s_nop 0\n"
      "v_cvt_pk_bf16_f32 v147, v148, v149\n"
      "v_cndmask_b32 v205, v205, v213, s[42:43]\n"
      "v_mfma_f32_16x16x32_bf16 v[188:191], a[252:255], v[152:155], v[188:191]\n"
      "s_movrels_b32 s9, s80\n"
      "v_cndmask_b32 v205, v205, v157, s[44:45]\n"
      "v_cndmask_b32 v205, v205, v201, s[46:47]\n"
      "v_mfma_f32_16x16x32_bf16 v[196:199], a[224:227], v[250:253], 0\n"
      "ds_read_b128 v[250:253], v123 offset:8192\n"
      "v_max_f32_dpp v144, v205, v205 row_ror:8 row_mask:0xf bank_mask:0xf\n"
      "v_mfma_f32_16x16x32_bf16 v[196:199], a[236:239], v[244:247], v[196:199]\n"
      "ds_write2st64_b64 v125, v[248:249], v[146:147] offset0:36 offset1:38\n"
      "v_mfma_f32_16x16x32_bf16 v[192:195], a[244:247], v[220:223], v[192:195]\n"
      "ds_read_b128 v[246:249], v123 offset:11264\n"
      "v_max_f32_dpp v144, v144, v144 row_ror:4 row_mask:0xf bank_mask:0xf\n"
      "v_mfma_f32_16x16x32_bf16 v[192:195], v[0:3], v[152:155], v[192:195]\n"
      "ds_read_b128 v[146:149], v123 offset:14336\n"
      "s_waitcnt lgkmcnt(4)\n"
      "v_mfma_f32_16x16x32_bf16 v[196:199], a[248:251], v[220:223], v[196:199]\n"
      "ds_read_b128 v[220:223], v123 offset:17408\n"
      "v_max_f32_dpp v144, v144, v144 row_ror:2 row_mask:0xf bank_mask:0xf\n"
      "v_mfma_f32_16x16x32_bf16 v[196:199], v[4:7], v[152:155], v[196:199]\n"
      "s_lshr_b32 s8, s9, 8\n"
      "v_max_f32_dpp v144, v144, v144 row_ror:1 row_mask:0xf bank_mask:0xf\n"
      "v_mfma_f32_16x16x32_bf16 v[188:191], v[8:11], v[236:239], v[188:191]\n"
      "s_and_b32 s9, s9, 0xff\n"
      "s_mul_i32 s10, s8, 73\n"
      "v_max_f32_dpp v144, v144, v144 row_bcast:15 row_mask:0xa bank_mask:0xf\n"
      "v_mfma_f32_16x16x32_bf16 v[188:191], v[20:23], v[228:231], v[188:191]\n"
      "s_add_u32 s10, s10, s9\n"
      "v_max_f32_dpp v144, v144, v144 row_bcast:31 row_mask:0xc bank_mask:0xf\n"
      "s_mul_i32 s28, s10, 4802\n"
      "v_mfma_f32_16x16x32_bf16 v[196:199], v[16:19], v[236:239], v[196:199]\n"
      "s_add_u32 s56, s18, s28\n"
      "v_readlane_b32 s31, v144, 63\n"
      "v_mfma_f32_16x16x32_bf16 v[196:199], v[28:31], v[228:231], v[196:199]\n"
      "s_addc_u32 s57, s19, 0\n"
      "v_subrev_f32 v205, s31, v205\n"
      "v_exp_f32 v205, v205\n"
      "v_mfma_f32_16x16x32_bf16 v[192:195], v[12:15], v[236:239], v[192:195]\n"
      "s_waitcnt lgkmcnt(2)\n"
      "v_fma_f32 v144, v188, v188, s39\n"
      "v_cvt_pk_bf16_f32 v205, v205, v205\n"
      "v_mfma_f32_16x16x32_bf16 v[192:195], v[24:27], v[228:231], v[192:195]\n"
      "ds_write_b16 v130, v205\n"
      "v_mfma_f32_16x16x32_bf16 v[200:203], a[216:219], v[240:243], 0\n"
      "ds_read_b128 v[228:231], v131 offset:64\n"
      "v_fmac_f32_e64 v144, v189, v189\n"
      "v_mfma_f32_16x16x32_bf16 v[200:203], a[228:231], v[216:219], v[200:203]\n"
      "ds_read_b128 v[236:239], v131\n"
      "v_fmac_f32 v144, v190, v190\n"
      "v_mfma_f32_16x16x32_bf16 v[204:207], a[220:223], v[240:243], 0\n"
      "ds_read_b128 v[156:159], v115\n"
      "v_fmac_f32 v144, v191, v191\n"
      "v_mfma_f32_16x16x32_bf16 v[204:207], a[232:235], v[216:219], v[204:207]\n"
      "s_waitcnt lgkmcnt(4)\n"
      "v_fmac_f32 v144, v192, v192\n"
      "v_fmac_f32_e64 v144, v193, v193\n"
      "v_mfma_f32_16x16x32_bf16 v[208:211], a[224:227], v[240:243], 0\n"
      "ds_read_b128 v[212:215], v114\n"
      "v_fmac_f32 v144, v194, v194\n"
      "v_mfma_f32_16x16x32_bf16 v[208:211], a[236:239], v[216:219], v[208:211]\n"
      "s_and_b32 s56, s56, -16\n"
      "v_fmac_f32 v144, v195, v195\n"
      "v_fmac_f32 v144, v196, v196\n"
      "v_mfma_f32_16x16x32_bf16 v[200:203], a[240:243], v[250:253], v[200:203]\n"
      "s_sub_u32 s58, s56, 110592\n"
      "v_fmac_f32 v144, v197, v197\n"
      "v_mfma_f32_16x16x32_bf16 v[200:203], a[252:255], v[246:249], v[200:203]\n"
      "s_subb_u32 s59, s57, 0\n"
      "v_fmac_f32 v144, v198, v198\n"
      "v_fmac_f32 v144, v199, v199\n"
      "v_mfma_f32_16x16x32_bf16 v[204:207], a[244:247], v[250:253], v[204:207]\n"
      "s_sub_u32 s56, s56, 73728\n"
      "v_mfma_f32_16x16x32_bf16 v[204:207], v[0:3], v[246:249], v[204:207]\n"
      "s_subb_u32 s57, s57, 0\n"
      "s_mul_i32 s28, s8, 1373568\n"
      "v_mfma_f32_16x16x32_bf16 v[208:211], a[248:251], v[250:253], v[208:211]\n"
      "v_permlane16_swap_b32 v168, v144\n"
      "v_mfma_f32_16x16x32_bf16 v[208:211], v[4:7], v[246:249], v[208:211]\n"
      "s_mul_i32 s29, s9, 2688\n"
      "s_waitcnt lgkmcnt(1)\n"
      "s_add_u32 s28, s28, s29\n"
      "v_mfma_f32_16x16x32_bf16 v[200:203], v[8:11], v[146:149], v[200:203]\n"
      "s_cmp_eq_u32 s8, 72\n"
      "s_waitcnt lgkmcnt(0)\n"
      "v_add_f32 v168, v168, v144\n"
      "v_mfma_f32_16x16x32_bf16 v[200:203], v[20:23], v[220:223], v[200:203]\n"
      "s_cselect_b32 s8, 0, 100270464\n"
      "s_cmp_eq_u32 s9, 72\n"
      "s_cselect_b32 s9, 0, 196224\n"
      "v_mfma_f32_16x16x32_bf16 v[204:207], v[12:15], v[146:149], v[204:207]\n"
      "s_add_u32 s10, s8, s9\n"
      "s_add_u32 s28, s28, s10\n"
      "s_add_u32 s52, s16, s28\n"
      "v_mfma_f32_16x16x32_bf16 v[204:207], v[24:27], v[220:223], v[204:207]\n"
      "s_addc_u32 s53, s17, 0\n"
      "v_fma_f32 v144, v200, v200, s39\n"
      "v_fmac_f32 v144, v201, v201\n"
      "v_mfma_f32_16x16x32_bf16 v[208:211], v[16:19], v[146:149], v[208:211]\n"
      "s_add_u32 s27, s27, 1\n"
      "v_fmac_f32 v144, v202, v202\n"
      "v_fmac_f32 v144, v203, v203\n"
      "v_mfma_f32_16x16x32_bf16 v[208:211], v[28:31], v[220:223], v[208:211]\n"
      "s_cmp_ge_u32 s27, s26\n"
      "v_fmac_f32 v144, v204, v204\n"
      "v_fmac_f32_e64 v144, v205, v205\n"
      "v_mfma_f32_16x16x32_bf16 v[246:249], v[236:239], v[66:69], 0\n"
      "s_cselect_b32 s28, 1, 0\n"
      "v_fmac_f32 v144, v206, v206\n"
      "v_fmac_f32 v144, v207, v207\n"
      "v_mfma_f32_16x16x32_bf16 v[246:249], v[228:231], v[66:69], v[246:249]\n"
      "s_xor_b32 s29, s10, s11\n"
      "v_fmac_f32 v144, v208, v208\n"
      "v_fmac_f32 v144, v209, v209\n"
      "v_mfma_f32_16x16x32_bf16 v[220:223], v[236:239], v[232:235], 0\n"
      "v_fmac_f32 v144, v210, v210\n"
      "v_fmac_f32 v144, v211, v211\n"
      "v_mfma_f32_16x16x32_bf16 v[220:223], v[228:231], v[50:53], v[220:223]\n"
      "v_mov_b32 v250, v144\n"
      "v_mfma_f32_16x16x32_bf16 v[232:235], v[236:239], v[172:175], 0\n"
      "s_nop 0\n"
      "v_permlane16_swap_b32_e64 v144, v250\n"
      "v_mfma_f32_16x16x32_bf16 v[232:235], v[228:231], v[58:61], v[232:235]\n"
      "v_add_f32 v144, v144, v250\n"
      "s_or_b32 s28, s28, s29\n"
      "v_mfma_f32_16x16x32_bf16 v[216:219], v[236:239], v[224:227], 0\n"
      "v_permlane32_swap_b32_e64 v168, v144\n"
      "v_mfma_f32_16x16x32_bf16 v[216:219], v[228:231], v[54:57], v[216:219]\n"
      "v_mov_b32_dpp v246, v246 quad_perm:[0,0,0,0] row_mask:0xf bank_mask:0xf\n"
      "v_mfma_f32_16x16x32_bf16 v[172:175], a[0:3], v[212:215], 0\n"
      "v_rcp_f32_e64 v246, v246\n"
      "v_mfma_f32_16x16x32_bf16 v[172:175], a[36:39], v[156:159], v[172:175]\n"
      "v_add_f32 v168, v168, v144\n"
      "v_mfma_f32_16x16x32_bf16 v[248:251], a[4:7], v[212:215], 0\n"
      "s_nop 1\n"
      "v_cndmask_b32 v220, v220, v216, s[42:43]\n"
      "v_mfma_f32_16x16x32_bf16 v[248:251], a[40:43], v[156:159], v[248:251]\n"
      "ds_write_b32 v129, v168 offset:1024\n"
      "v_mfma_f32_16x16x32_bf16 v[144:147], a[8:11], v[212:215], 0\n"
      "v_cndmask_b32 v220, v220, v232, s[44:45]\n"
      "v_mfma_f32_16x16x32_bf16 v[144:147], a[44:47], v[156:159], v[144:147]\n"
      "v_mul_f32_e64 v220, v246, v220\n"
      "v_cvt_pk_bf16_f32 v220, v220, v220\n"
      "v_mfma_f32_16x16x32_bf16 v[168:171], a[12:15], v[212:215], 0\n"
      "ds_write_b16 v135, v220 offset:16\n"
      "v_mfma_f32_16x16x32_bf16 v[168:171], a[48:51], v[156:159], v[168:171]\n"
      "s_branch L_join_21_%=\n"
      "L_flg_116_%=:\n"
      "v_cmp_gt_i32 vcc, 0, v196\n"
      "s_cbranch_vccz L_fb_117_%=\n"
      "s_sleep 1\n"
      "ds_read_b128 v[196:199], v128 offset:2048\n"
      "s_waitcnt lgkmcnt(0)\n"
      "v_bitop3_b32 v196, v196, v197, s4 bitop3:0x7e\n"
      "v_bitop3_b32 v198, v198, v199, s4 bitop3:0x7e\n"
      "v_or_b32 v196, v196, v198\n"
      "v_cmp_ne_u32 s[48:49], 0, v196\n"
      "s_branch L_flg_116_%=\n"
      "L_gen_20_%=:\n"
      "s_nop 0\n"
      "s_nop 0\n"
      "v_mbcnt_lo_u32_b32 v169, -1, 0\n"
      "v_mbcnt_hi_u32_b32 v169, -1, v169\n"
      "v_lshrrev_b32 v220, 4, v169\n"
      "v_and_b32 v155, 15, v169\n"
      "v_mul_u32_u24 v228, 0x62, v155\n"
      "v_lshl_add_u32 v228, v220, 3, v228\n"
      "v_add_u32 v228, s30, v228\n"
      "s_movk_i32 s31, 0x3fb9\n"
      "v_lshlrev_b32 v220, 2, v220\n"
      "v_sub_u32_e64 v155, v155, v220\n"
      "v_and_b32_e64 v220, 1, v155\n"
      "v_and_b32_e64 v169, -4, v228\n"
      "v_lshlrev_b32_e64 v220, 4, v220\n"
      "v_and_b32 v228, 2, v228\n"
      "v_lshlrev_b32 v220, v220, s31\n"
      "v_lshrrev_b32_e64 v155, 1, v155\n"
      "v_cmp_eq_u32 vcc, 0, v155\n"
      "ds_read2_b32 v[198:199], v169 offset0:8 offset1:9\n"
      "ds_read2_b32 v[206:207], v169 offset0:0 offset1:1\n"
      "ds_read2_b32 v[166:167], v169 offset0:18 offset1:24\n"
      "ds_read2_b32 v[164:165], v169 offset0:2 offset1:10\n"
      "ds_read2_b32 v[196:197], v169 offset0:16 offset1:17\n"
      "v_cndmask_b32 v154, 0, v220, vcc\n"
      "v_add_u32 v169, 1568, v169\n"
      "ds_read2_b32 v[170:171], v169 offset0:8 offset1:9\n"
      "ds_read2_b32 v[200:201], v169 offset0:0 offset1:1\n"
      "ds_read2_b32 v[202:203], v169 offset0:2 offset1:10\n"
      "ds_read2_b32 v[214:215], v169 offset0:18 offset1:24\n"
      "ds_read2_b32 v[212:213], v169 offset0:16 offset1:17\n"
      "v_cmp_eq_u32 vcc, 1, v155\n"
      "v_add_u32 v169, 1568, v169\n"
      "v_mfma_f32_16x16x32_f16 v[236:239], v[140:143], v[188:191], v[94:97]\n"
      "ds_read2_b32 v[144:145], v169 offset0:0 offset1:1\n"
      "ds_read2_b32 v[250:251], v169 offset0:2 offset1:10\n"
      "ds_read2_b32 v[146:147], v169 offset0:8 offset1:9\n"
      "ds_read2_b32 v[152:153], v169 offset0:18 offset1:24\n"
      "ds_read2_b32 v[248:249], v169 offset0:16 offset1:17\n"
      "v_cndmask_b32 v155, 0, v220, vcc\n"
      "v_mfma_f32_16x16x32_f16 v[220:223], v[156:159], v[188:191], v[90:93]\n"
      "s_waitcnt lgkmcnt(12)\n"
      "v_alignbyte_b32 v198, v199, v198, v228\n"
      "v_alignbyte_b32 v206, v207, v206, v228\n"
      "v_mfma_f32_16x16x32_f16 v[220:223], v[34:37], v[38:41], v[220:223]\n"
      "s_waitcnt lgkmcnt(10)\n"
      "v_alignbyte_b32 v207, v164, v207, v228\n"
      "v_alignbyte_b32 v196, v197, v196, v228\n"
      "v_alignbyte_b32 v197, v166, v197, v228\n"
      "v_mfma_f32_16x16x32_f16 v[236:239], v[40:43], v[38:41], v[236:239]\n"
      "v_alignbyte_b32 v199, v165, v199, v228\n"
      "v_alignbyte_b32 v167, v167, v167, v228\n"
      "v_lshlrev_b32 v167, 16, v167\n"
      "v_mfma_f32_16x16x32_f16 v[252:255], v[148:151], v[188:191], v[72:75]\n"
      "s_waitcnt lgkmcnt(7)\n"
      "s_waitcnt lgkmcnt(6)\n"
      "v_alignbyte_b32 v200, v201, v200, v228\n"
      "v_mfma_f32_16x16x32_f16 v[252:255], v[244:247], v[140:143], v[252:255]\n"
      "v_alignbyte_b32 v170, v171, v170, v228\n"
      "v_alignbyte_b32 v201, v202, v201, v228\n"
      "v_mfma_f32_16x16x32_f16 v[252:255], v[240:243], v[38:41], v[252:255]\n"
      "v_alignbyte_b32 v171, v203, v171, v228\n"
      "v_fmac_f32 v204, s37, v167\n"
      "v_mfma_f32_16x16x32_f16 v[164:167], v[216:219], v[188:191], v[98:101]\n"
      "s_waitcnt lgkmcnt(2)\n"
      "v_alignbyte_b32 v215, v215, v215, v228\n"
      "v_alignbyte_b32 v212, v213, v212, v228\n"
      "v_mfma_f32_16x16x32_f16 v[164:167], v[46:49], v[38:41], v[164:167]\n"
      "s_waitcnt lgkmcnt(0)\n"
      "v_lshlrev_b32 v215, 16, v215\n"
      "v_alignbyte_b32 v144, v145, v144, v228\n"
      "v_mfma_f32_16x16x16_bf16 v[160:163], v[154:155], v[206:207], v[160:163]\n"
      "v_alignbyte_b32 v145, v250, v145, v228\n"
      "v_alignbyte_b32 v213, v214, v213, v228\n"
      "v_mfma_f32_16x16x16_bf16 v[192:195], v[154:155], v[198:199], v[192:195]\n"
      "ds_read_b128 v[188:191], v128 offset:2048\n"
      "v_alignbyte_b32 v146, v147, v146, v228\n"
      "v_mfma_f32_16x16x16_bf16 v[136:139], v[154:155], v[196:197], v[136:139]\n"
      "s_nop 1\n"
      "v_max3_f32 v250, v160, v161, v162\n"
      "v_mfma_f32_16x16x16_bf16 v[220:223], v[154:155], v[200:201], v[220:223]\n"
      "s_nop 0\n"
      "v_max3_f32 v250, v250, v194, v195\n"
      "v_alignbyte_b32 v147, v251, v147, v228\n"
      "v_mfma_f32_16x16x16_bf16 v[164:167], v[154:155], v[212:213], v[164:167]\n"
      "v_max3_f32 v250, v250, v138, v139\n"
      "v_max3_f32 v251, v163, v192, v193\n"
      "v_max3_f32 v251, v251, v136, v137\n"
      "v_max3_f32 v250, v250, v251, v204\n"
      "v_mov_b32 v251, v250\n"
      "v_mfma_f32_16x16x16_bf16 v[236:239], v[154:155], v[170:171], v[236:239]\n"
      "s_nop 0\n"
      "v_permlane16_swap_b32 v250, v251\n"
      "v_max_f32 v250, v250, v251\n"
      "v_mov_b32 v251, v250\n"
      "v_alignbyte_b32 v248, v249, v248, v228\n"
      "v_alignbyte_b32 v249, v152, v249, v228\n"
      "v_permlane32_swap_b32 v250, v251\n"
      "v_max_f32 v250, v250, v251\n"
      "v_sub_f32 v192, v192, v250\n"
      "v_alignbyte_b32 v153, v153, v153, v228\n"
      "v_sub_f32 v161, v161, v250\n"
      "v_lshlrev_b32 v153, 16, v153\n"
      "v_sub_f32 v204, v204, v250\n"
      "v_sub_f32 v137, v137, v250\n"
      "v_sub_f32 v163, v163, v250\n"
      "v_sub_f32 v139, v139, v250\n"
      "v_sub_f32 v160, v160, v250\n"
      "v_sub_f32 v162, v162, v250\n"
      "v_max3_f32 v251, v220, v221, v222\n"
      "v_sub_f32 v193, v193, v250\n"
      "v_sub_f32 v194, v194, v250\n"
      "v_fmac_f32 v252, s37, v215\n"
      "v_max3_f32 v251, v251, v238, v239\n"
      "v_exp_f32 v161, v161\n"
      "v_exp_f32 v137, v137\n"
      "v_exp_f32 v160, v160\n"
      "v_sub_f32 v138, v138, v250\n"
      "v_exp_f32 v163, v163\n"
      "v_exp_f32 v193, v193\n"
      "v_exp_f32 v204, v204\n"
      "v_cvt_pk_bf16_f32 v160, v160, v161\n"
      "v_exp_f32 v162, v162\n"
      "v_sub_f32 v136, v136, v250\n"
      "v_exp_f32 v139, v139\n"
      "v_exp_f32 v194, v194\n"
      "v_sub_f32 v195, v195, v250\n"
      "s_waitcnt lgkmcnt(0)\n"
      "v_cvt_pk_bf16_f32 v161, v162, v163\n"
      "v_max3_f32 v250, v223, v236, v237\n"
      "v_exp_f32_e64 v195, v195\n"
      "v_max3_f32 v251, v251, v166, v167\n"
      "v_max3_f32 v250, v250, v164, v165\n"
      "v_max3_f32 v251, v251, v250, v252\n"
      "v_exp_f32 v136, v136\n"
      "v_exp_f32 v138, v138\n"
      "v_exp_f32 v192, v192\n"
      "v_bitop3_b32 v190, v190, v191, s4 bitop3:0x7e\n"
      "v_cvt_pk_bf16_f32 v162, v192, v193\n"
      "v_mov_b32 v250, v251\n"
      "v_cvt_pk_bf16_f32 v63, v138, v139\n"
      "v_bitop3_b32 v188, v188, v189, s4 bitop3:0x7e\n"
      "v_or_b32 v188, v188, v190\n"
      "v_permlane16_swap_b32 v251, v250\n"
      "v_cmp_ne_u32 vcc, 0, v188\n"
      "v_max_f32 v251, v251, v250\n"
      "v_cvt_pk_bf16_f32 v64, v204, 0\n"
      "v_mov_b32 v250, v251\n"
      "v_cvt_pk_bf16_f32 v62, v136, v137\n"
      "s_mov_b64 s[48:49], vcc\n"
      "v_permlane32_swap_b32 v251, v250\n"
      "v_max_f32 v251, v251, v250\n"
      "v_sub_f32 v236, v236, v251\n"
      "v_sub_f32 v221, v221, v251\n"
      "v_sub_f32 v220, v220, v251\n"
      "v_sub_f32 v252, v252, v251\n"
      "v_cvt_pk_bf16_f32 v163, v194, v195\n"
      "v_sub_f32 v239, v239, v251\n"
      "v_sub_f32 v164, v164, v251\n"
      "v_exp_f32 v239, v239\n"
      "v_sub_f32 v167, v167, v251\n"
      "v_exp_f32 v221, v221\n"
      "v_sub_f32 v223, v223, v251\n"
      "v_sub_f32 v222, v222, v251\n"
      "v_sub_f32 v238, v238, v251\n"
      "v_mfma_f32_16x16x32_bf16 v[200:203], v[172:175], v[160:163], 0\n"
      "v_exp_f32_e64 v220, v220\n"
      "v_mfma_f32_16x16x32_bf16 v[200:203], v[58:61], v[62:65], v[200:203]\n"
      "v_cvt_pk_bf16_f32 v220, v220, v221\n"
      "v_exp_f32 v223, v223\n"
      "v_mfma_f32_16x16x32_bf16 v[192:195], v[66:69], v[160:163], 0\n"
      "s_cbranch_vccnz L_flg_128_%=\n"
      "L_fb_129_%=:\n"
      "v_mfma_f32_16x16x32_bf16 v[192:195], v[66:69], v[62:65], v[192:195]\n"
      "v_sub_f32 v166, v166, v251\n"
      "v_exp_f32 v252, v252\n"
      "v_exp_f32 v167, v167\n"
      "v_sub_f32 v237, v237, v251\n"
      "v_exp_f32_e64 v222, v222\n"
      "v_mfma_f32_16x16x32_bf16 v[188:191], v[232:235], v[160:163], 0\n"
      "v_exp_f32 v164, v164\n"
      "v_mfma_f32_16x16x32_bf16 v[188:191], v[50:53], v[62:65], v[188:191]\n"
      "v_cvt_pk_bf16_f32 v221, v222, v223\n"
      "v_exp_f32 v238, v238\n"
      "v_mfma_f32_16x16x32_bf16 v[136:139], v[224:227], v[160:163], 0\n"
      "v_exp_f32 v166, v166\n"
      "v_mfma_f32_16x16x32_bf16 v[136:139], v[54:57], v[62:65], v[136:139]\n"
      "ds_read_b128 v[196:199], v120\n"
      "v_exp_f32 v237, v237\n"
      "v_sub_f32 v165, v165, v251\n"
      "v_exp_f32 v165, v165\n"
      "v_exp_f32_e64 v236, v236\n"
      "v_mfma_f32_16x16x32_f16 v[212:215], v[156:159], v[208:211], v[102:105]\n"
      "v_cvt_pk_bf16_f32 v62, v164, v165\n"
      "v_cvt_pk_bf16_f32 v64, v252, 0\n"
      "v_mfma_f32_16x16x32_f16 v[212:215], v[34:37], v[44:47], v[212:215]\n"
      "v_rcp_f32 v192, v192\n"
      "v_mfma_f32_16x16x32_f16 v[160:163], v[140:143], v[208:211], v[106:109]\n"
      "v_mul_f32 v188, v192, v188\n"
      "v_mul_f32 v189, v192, v189\n"
      "v_mfma_f32_16x16x32_f16 v[160:163], v[40:43], v[44:47], v[160:163]\n"
      "v_cvt_pk_bf16_f32 v222, v236, v237\n"
      "v_mul_f32 v191, v192, v191\n"
      "v_mul_f32 v137, v192, v137\n"
      "v_mul_f32 v203, v192, v203\n"
      "v_mfma_f32_16x16x16_bf16 v[212:215], v[154:155], v[144:145], v[212:215]\n"
      "v_mul_f32 v190, v192, v190\n"
      "v_cvt_pk_bf16_f32 v223, v238, v239\n"
      "v_cvt_pk_bf16_f32 v63, v166, v167\n"
      "v_mul_f32 v139, v192, v139\n"
      "v_mfma_f32_16x16x16_bf16 v[160:163], v[154:155], v[146:147], v[160:163]\n"
      "s_waitcnt lgkmcnt(0)\n"
      "v_mul_f32 v201, v192, v201\n"
      "v_cvt_pk_bf16_f32 v188, v188, v189\n"
      "v_cvt_pk_bf16_f32 v189, v190, v191\n"
      "v_mul_f32_e64 v138, v192, v138\n"
      "v_cvt_pk_bf16_f32 v191, v138, v139\n"
      "v_mul_f32_e64 v200, v192, v200\n"
      "v_cvt_pk_bf16_f32 v200, v200, v201\n"
      "v_mul_f32_e64 v202, v192, v202\n"
      "v_mul_f32_e64 v136, v192, v136\n"
      "v_mfma_f32_16x16x32_f16 v[156:159], v[148:151], v[208:211], v[74:77]\n"
      "v_cvt_pk_bf16_f32 v190, v136, v137\n"
      "v_mfma_f32_16x16x32_f16 v[156:159], v[244:247], v[216:219], v[156:159]\n"
      "ds_write_b128 v124, v[188:191] offset:18432\n"
      "v_mfma_f32_16x16x32_f16 v[156:159], v[240:243], v[44:47], v[156:159]\n"
      "ds_read_b128 v[188:191], v121\n"
      "v_mfma_f32_16x16x32_f16 v[136:139], v[216:219], v[208:211], v[110:113]\n"
      "v_cvt_pk_bf16_f32 v201, v202, v203\n"
      "v_max3_f32 v203, v212, v213, v214\n"
      "v_mfma_f32_16x16x32_f16 v[136:139], v[46:49], v[44:47], v[136:139]\n"
      "global_store_dwordx4 v117, v[196:199], s[54:55] sc1\n"
      "v_mfma_f32_16x16x32_f16 v[208:211], v[244:247], v[148:151], v[76:79]\n"
      "v_mfma_f32_16x16x32_bf16 v[144:147], v[66:69], v[220:223], 0\n"
      "ds_read_b128 v[140:143], v123 offset:1024\n"
      "v_max3_f32 v203, v203, v162, v163\n"
      "v_mfma_f32_16x16x32_bf16 v[144:147], v[66:69], v[62:65], v[144:147]\n"
      "v_max3_f32 v202, v215, v160, v161\n"
      "v_mfma_f32_16x16x16_bf16 v[136:139], v[154:155], v[248:249], v[136:139]\n"
      "v_fmac_f32 v156, s37, v153\n"
      "v_mfma_f32_16x16x16_f16 v[208:211], v[242:243], v[240:241], v[208:211]\n"
      "v_mfma_f32_16x16x32_bf16 v[248:251], v[232:235], v[220:223], 0\n"
      "s_waitcnt lgkmcnt(1)\n"
      "v_mfma_f32_16x16x32_bf16 v[248:251], v[50:53], v[62:65], v[248:251]\n"
      "s_mov_b32 exec_hi, 0\n"
      "global_store_dwordx4 v118, v[188:191], s[54:55] sc1\n"
      "s_mov_b32 exec_hi, -1\n"
      "v_mfma_f32_16x16x32_bf16 v[164:167], v[224:227], v[220:223], 0\n"
      "ds_read_b128 v[146:149], v123 offset:4096\n"
      "v_max3_f32 v203, v203, v138, v139\n"
      "v_max3_f32 v202, v202, v136, v137\n"
      "v_mfma_f32_16x16x32_bf16 v[240:243], v[172:175], v[220:223], 0\n"
      "v_max3_f32 v203, v203, v202, v156\n"
      "v_mov_b32 v202, v203\n"
      "v_mfma_f32_16x16x32_bf16 v[240:243], v[58:61], v[62:65], v[240:243]\n"
      "s_nop 0\n"
      "v_permlane16_swap_b32 v203, v202\n"
      "v_max_f32 v203, v203, v202\n"
      "v_mov_b32_e64 v202, v203\n"
      "v_mfma_f32_16x16x32_bf16 v[164:167], v[54:57], v[62:65], v[164:167]\n"
      "s_nop 0\n"
      "v_permlane32_swap_b32 v203, v202\n"
      "v_max_f32 v203, v203, v202\n"
      "v_sub_f32 v215, v215, v203\n"
      "v_sub_f32 v214, v214, v203\n"
      "v_sub_f32 v156, v156, v203\n"
      "v_sub_f32 v162, v162, v203\n"
      "v_rcp_f32 v144, v144\n"
      "v_sub_f32 v136, v136, v203\n"
      "v_sub_f32 v213, v213, v203\n"
      "v_sub_f32 v212, v212, v203\n"
      "v_exp_f32 v214, v214\n"
      "v_exp_f32 v212, v212\n"
      "v_sub_f32 v160, v160, v203\n"
      "s_waitcnt lgkmcnt(0)\n"
      "v_mfma_f32_16x16x32_bf16 v[188:191], a[216:219], v[140:143], 0\n"
      "ds_read_b128 v[220:223], v123 offset:7168\n"
      "v_sub_f32 v138, v138, v203\n"
      "v_mfma_f32_16x16x32_bf16 v[188:191], a[228:231], v[146:149], v[188:191]\n"
      "v_exp_f32 v160, v160\n"
      "v_sub_f32 v137, v137, v203\n"
      "v_sub_f32 v139, v139, v203\n"
      "v_sub_f32 v163, v163, v203\n"
      "v_sub_f32 v161, v161, v203\n"
      "v_mfma_f32_16x16x32_bf16 v[192:195], a[220:223], v[140:143], 0\n"
      "v_exp_f32 v213, v213\n"
      "v_mfma_f32_16x16x32_bf16 v[192:195], a[232:235], v[146:149], v[192:195]\n"
      "v_exp_f32 v161, v161\n"
      "v_cvt_pk_bf16_f32 v212, v212, v213\n"
      "v_exp_f32 v137, v137\n"
      "v_exp_f32 v215, v215\n"
      "v_mul_f32 v248, v144, v248\n"
      "v_cvt_pk_bf16_f32 v213, v214, v215\n"
      "v_cvt_pk_bf16_f32 v214, v160, v161\n"
      "v_exp_f32 v136, v136\n"
      "v_mul_f32 v249, v144, v249\n"
      "v_mfma_f32_16x16x32_bf16 v[196:199], a[224:227], v[140:143], 0\n"
      "ds_read_b128 v[244:247], v123 offset:10240\n"
      "v_cvt_pk_bf16_f32 v62, v136, v137\n"
      "v_mfma_f32_16x16x32_bf16 v[196:199], a[236:239], v[146:149], v[196:199]\n"
      "ds_read_b128 v[146:149], v123 offset:13312\n"
      "v_exp_f32 v139, v139\n"
      "v_exp_f32 v163, v163\n"
      "v_exp_f32 v138, v138\n"
      "v_exp_f32 v162, v162\n"
      "v_exp_f32_e64 v156, v156\n"
      "v_cvt_pk_bf16_f32 v215, v162, v163\n"
      "v_cvt_pk_bf16_f32 v64, v156, 0\n"
      "v_cvt_pk_bf16_f32 v63, v138, v139\n"
      "v_mfma_f32_16x16x32_bf16 v[136:139], v[66:69], v[212:215], 0\n"
      "ds_read_b128 v[216:219], v123 offset:16384\n"
      "v_mul_f32 v250, v144, v250\n"
      "v_mfma_f32_16x16x32_bf16 v[136:139], v[66:69], v[62:65], v[136:139]\n"
      "v_mul_f32 v251, v144, v251\n"
      "v_cvt_pk_bf16_f32 v248, v248, v249\n"
      "v_mfma_f32_16x16x32_bf16 v[140:143], v[232:235], v[212:215], 0\n"
      "v_mul_f32_e64 v165, v144, v165\n"
      "v_cvt_pk_bf16_f32 v249, v250, v251\n"
      "v_mfma_f32_16x16x32_bf16 v[140:143], v[50:53], v[62:65], v[140:143]\n"
      "ds_read_b128 v[152:155], v123 offset:2048\n"
      "v_mul_f32 v164, v144, v164\n"
      "v_mfma_f32_16x16x32_bf16 v[160:163], v[224:227], v[212:215], 0\n"
      "s_waitcnt lgkmcnt(3)\n"
      "v_cvt_pk_bf16_f32 v250, v164, v165\n"
      "v_mul_f32_e64 v166, v144, v166\n"
      "v_mfma_f32_16x16x32_bf16 v[160:163], v[54:57], v[62:65], v[160:163]\n"
      "s_add_u32 s28, s27, 2\n"
      "v_mul_f32 v167, v144, v167\n"
      "v_cvt_pk_bf16_f32 v251, v166, v167\n"
      "v_mfma_f32_16x16x32_bf16 v[164:167], v[172:175], v[212:215], 0\n"
      "ds_read_b128 v[212:215], v123 offset:5120\n"
      "v_mul_f32_e64 v242, v144, v242\n"
      "v_mfma_f32_16x16x32_bf16 v[164:167], v[58:61], v[62:65], v[164:167]\n"
      "ds_write_b128 v124, v[248:251] offset:19456\n"
      "v_mfma_f32_16x16x32_bf16 v[188:191], a[240:243], v[220:223], v[188:191]\n"
      "ds_read_b128 v[248:251], v123 offset:8192\n"
      "v_mul_f32_e64 v240, v144, v240\n"
      "v_mfma_f32_16x16x32_bf16 v[188:191], a[252:255], v[244:247], v[188:191]\n"
      "s_waitcnt lgkmcnt(4)\n"
      "v_mul_f32_e64 v241, v144, v241\n"
      "v_cvt_pk_bf16_f32 v240, v240, v241\n"
      "v_mfma_f32_16x16x32_bf16 v[192:195], a[244:247], v[220:223], v[192:195]\n"
      "s_min_u32 m0, s28, s3\n"
      "v_mul_f32 v243, v144, v243\n"
      "v_cvt_pk_bf16_f32 v241, v242, v243\n"
      "v_mfma_f32_16x16x32_bf16 v[192:195], v[0:3], v[244:247], v[192:195]\n"
      "ds_write2st64_b64 v125, v[200:201], v[240:241] offset0:36 offset1:38\n"
      "v_mfma_f32_16x16x32_bf16 v[196:199], a[248:251], v[220:223], v[196:199]\n"
      "ds_read_b128 v[220:223], v123 offset:11264\n"
      "v_cndmask_b32 v205, v205, v253, s[42:43]\n"
      "v_mfma_f32_16x16x32_bf16 v[196:199], v[4:7], v[244:247], v[196:199]\n"
      "s_waitcnt lgkmcnt(4)\n"
      "v_cndmask_b32 v205, v205, v157, s[44:45]\n"
      "v_cndmask_b32 v205, v205, v209, s[46:47]\n"
      "v_mfma_f32_16x16x32_bf16 v[188:191], v[8:11], v[146:149], v[188:191]\n"
      "ds_read_b128 v[244:247], v123 offset:14336\n"
      "v_mbcnt_lo_u32_b32 v169, -1, 0\n"
      "v_mfma_f32_16x16x32_bf16 v[188:191], v[20:23], v[216:219], v[188:191]\n"
      "ds_read_b128 v[240:243], v123 offset:17408\n"
      "v_mbcnt_hi_u32_b32 v169, -1, v169\n"
      "v_mfma_f32_16x16x32_bf16 v[192:195], v[12:15], v[146:149], v[192:195]\n"
      "s_movrels_b32 s9, s80\n"
      "v_min_u32 v169, 48, v169\n"
      "v_lshlrev_b32 v169, 1, v169\n"
      "v_mfma_f32_16x16x32_bf16 v[192:195], v[24:27], v[216:219], v[192:195]\n"
      "s_lshr_b32 s8, s9, 8\n"
      "v_fma_f32 v144, v188, v188, s39\n"
      "v_add_u32 v169, s30, v169\n"
      "v_mfma_f32_16x16x32_bf16 v[196:199], v[16:19], v[146:149], v[196:199]\n"
      "ds_read_u16 v170, v169 offset:4704\n"
      "v_fmac_f32 v144, v189, v189\n"
      "v_mfma_f32_16x16x32_bf16 v[196:199], v[28:31], v[216:219], v[196:199]\n"
      "s_and_b32 s9, s9, 0xff\n"
      "v_fmac_f32 v144, v190, v190\n"
      "v_fmac_f32 v144, v191, v191\n"
      "v_mfma_f32_16x16x32_bf16 v[200:203], a[216:219], v[152:155], 0\n"
      "s_mul_i32 s10, s8, 73\n"
      "v_fmac_f32 v144, v192, v192\n"
      "s_waitcnt lgkmcnt(3)\n"
      "v_mfma_f32_16x16x32_bf16 v[200:203], a[228:231], v[212:215], v[200:203]\n"
      "v_fmac_f32 v144, v193, v193\n"
      "v_fmac_f32 v144, v194, v194\n"
      "v_mfma_f32_16x16x32_bf16 v[200:203], a[240:243], v[248:251], v[200:203]\n"
      "s_add_u32 s10, s10, s9\n"
      "v_fmac_f32 v144, v195, v195\n"
      "v_fmac_f32 v144, v196, v196\n"
      "v_mfma_f32_16x16x32_bf16 v[200:203], a[252:255], v[220:223], v[200:203]\n"
      "s_waitcnt lgkmcnt(1)\n"
      "v_fmac_f32 v144, v197, v197\n"
      "v_fmac_f32_e64 v144, v198, v198\n"
      "v_mfma_f32_16x16x32_bf16 v[208:211], a[224:227], v[152:155], 0\n"
      "s_mul_i32 s28, s10, 4802\n"
      "s_add_u32 s56, s18, s28\n"
      "v_fmac_f32 v144, v199, v199\n"
      "v_mfma_f32_16x16x32_bf16 v[208:211], a[236:239], v[212:215], v[208:211]\n"
      "s_addc_u32 s57, s19, 0\n"
      "s_and_b32 s56, s56, -16\n"
      "v_mfma_f32_16x16x32_bf16 v[208:211], a[248:251], v[248:251], v[208:211]\n"
      "s_waitcnt lgkmcnt(0)\n"
      "v_lshlrev_b32_e64 v170, 16, v170\n"
      "v_fmac_f32_e64 v205, s37, v170\n"
      "v_mfma_f32_16x16x32_bf16 v[208:211], v[4:7], v[220:223], v[208:211]\n"
      "s_sub_u32 s58, s56, 110592\n"
      "v_max_f32_dpp v146, v205, v205 row_ror:8 row_mask:0xf bank_mask:0xf\n"
      "v_mfma_f32_16x16x32_bf16 v[200:203], v[8:11], v[244:247], v[200:203]\n"
      "s_subb_u32 s59, s57, 0\n"
      "s_sub_u32 s56, s56, 73728\n"
      "v_max_f32_dpp v146, v146, v146 row_ror:4 row_mask:0xf bank_mask:0xf\n"
      "v_mfma_f32_16x16x32_bf16 v[200:203], v[20:23], v[240:243], v[200:203]\n"
      "s_subb_u32 s57, s57, 0\n"
      "v_max_f32_dpp v146, v146, v146 row_ror:2 row_mask:0xf bank_mask:0xf\n"
      "s_mul_i32 s28, s8, 1373568\n"
      "v_mfma_f32_16x16x32_bf16 v[208:211], v[16:19], v[244:247], v[208:211]\n"
      "v_max_f32_dpp v146, v146, v146 row_ror:1 row_mask:0xf bank_mask:0xf\n"
      "s_mul_i32 s29, s9, 2688\n"
      "v_mfma_f32_16x16x32_bf16 v[208:211], v[28:31], v[240:243], v[208:211]\n"
      "s_add_u32 s28, s28, s29\n"
      "v_max_f32_dpp v146, v146, v146 row_bcast:15 row_mask:0xa bank_mask:0xf\n"
      "v_fma_f32 v170, v200, v200, s39\n"
      "v_fmac_f32 v170, v201, v201\n"
      "v_max_f32_dpp v146, v146, v146 row_bcast:31 row_mask:0xc bank_mask:0xf\n"
      "v_fmac_f32 v170, v202, v202\n"
      "v_readlane_b32 s31, v146, 63\n"
      "v_permlane16_swap_b32 v168, v144\n"
      "s_nop 0\n"
      "v_subrev_f32 v205, s31, v205\n"
      "v_exp_f32 v205, v205\n"
      "v_add_f32 v168, v168, v144\n"
      "v_cvt_pk_bf16_f32 v205, v205, v205\n"
      "s_cmp_eq_u32 s8, 72\n"
      "ds_write_b16 v130, v205\n"
      "v_mfma_f32_16x16x32_bf16 v[204:207], a[220:223], v[152:155], 0\n"
      "ds_read_b128 v[144:147], v131 offset:64\n"
      "s_cselect_b32 s8, 0, 100270464\n"
      "v_mfma_f32_16x16x32_bf16 v[204:207], a[232:235], v[212:215], v[204:207]\n"
      "ds_read_b128 v[152:155], v131\n"
      "s_cmp_eq_u32 s9, 72\n"
      "v_mfma_f32_16x16x32_bf16 v[204:207], a[244:247], v[248:251], v[204:207]\n"
      "s_cselect_b32 s9, 0, 196224\n"
      "ds_read_b128 v[212:215], v114\n"
      "v_mfma_f32_16x16x32_bf16 v[204:207], v[0:3], v[220:223], v[204:207]\n"
      "s_add_u32 s10, s8, s9\n"
      "ds_read_b128 v[156:159], v115\n"
      "v_mfma_f32_16x16x32_bf16 v[204:207], v[12:15], v[244:247], v[204:207]\n"
      "s_add_u32 s28, s28, s10\n"
      "v_fmac_f32 v170, v203, v203\n"
      "v_mfma_f32_16x16x32_bf16 v[204:207], v[24:27], v[240:243], v[204:207]\n"
      "s_add_u32 s52, s16, s28\n"
      "s_addc_u32 s53, s17, 0\n"
      "s_add_u32 s27, s27, 1\n"
      "s_cmp_ge_u32 s27, s26\n"
      "s_cselect_b32 s28, 1, 0\n"
      "s_xor_b32 s29, s10, s11\n"
      "s_nop 1\n"
      "v_fmac_f32 v170, v204, v204\n"
      "v_fmac_f32 v170, v205, v205\n"
      "v_fmac_f32 v170, v206, v206\n"
      "v_fmac_f32 v170, v207, v207\n"
      "v_fmac_f32 v170, v208, v208\n"
      "v_fmac_f32 v170, v209, v209\n"
      "v_fmac_f32 v170, v210, v210\n"
      "v_fmac_f32 v170, v211, v211\n"
      "v_mov_b32 v169, v170\n"
      "s_waitcnt lgkmcnt(2)\n"
      "v_mfma_f32_16x16x32_bf16 v[248:251], v[152:155], v[66:69], 0\n"
      "v_permlane16_swap_b32_e64 v170, v169\n"
      "v_mfma_f32_16x16x32_bf16 v[248:251], v[144:147], v[66:69], v[248:251]\n"
      "v_add_f32 v170, v170, v169\n"
      "v_mfma_f32_16x16x32_bf16 v[240:243], v[152:155], v[232:235], 0\n"
      "s_waitcnt lgkmcnt(0)\n"
      "v_permlane32_swap_b32_e64 v168, v170\n"
      "v_mfma_f32_16x16x32_bf16 v[240:243], v[144:147], v[50:53], v[240:243]\n"
      "v_add_f32 v168, v168, v170\n"
      "s_or_b32 s28, s28, s29\n"
      "v_mfma_f32_16x16x32_bf16 v[232:235], v[152:155], v[224:227], 0\n"
      "v_mov_b32_dpp v248, v248 quad_perm:[0,0,0,0] row_mask:0xf bank_mask:0xf\n"
      "v_mfma_f32_16x16x32_bf16 v[232:235], v[144:147], v[54:57], v[232:235]\n"
      "ds_write_b32 v129, v168 offset:1024\n"
      "v_mfma_f32_16x16x32_bf16 v[168:171], v[152:155], v[172:175], 0\n"
      "v_rcp_f32 v248, v248\n"
      "v_mfma_f32_16x16x32_bf16 v[168:171], v[144:147], v[58:61], v[168:171]\n"
      "v_mfma_f32_16x16x32_bf16 v[172:175], a[0:3], v[212:215], 0\n"
      "v_mfma_f32_16x16x32_bf16 v[172:175], a[36:39], v[156:159], v[172:175]\n"
      "s_nop 1\n"
      "v_cndmask_b32 v240, v240, v232, s[42:43]\n"
      "v_mfma_f32_16x16x32_bf16 v[144:147], a[8:11], v[212:215], 0\n"
      "s_nop 1\n"
      "v_cndmask_b32 v240, v240, v168, s[44:45]\n"
      "v_mfma_f32_16x16x32_bf16 v[144:147], a[44:47], v[156:159], v[144:147]\n"
      "v_mul_f32 v240, v248, v240\n"
      "v_mfma_f32_16x16x32_bf16 v[248:251], a[4:7], v[212:215], 0\n"
      "v_cvt_pk_bf16_f32 v240, v240, v240\n"
      "v_mfma_f32_16x16x32_bf16 v[248:251], a[40:43], v[156:159], v[248:251]\n"
      "ds_write_b16 v135, v240 offset:16\n"
      "v_mfma_f32_16x16x32_bf16 v[168:171], a[12:15], v[212:215], 0\n"
      "s_nop 0\n"
      "v_mfma_f32_16x16x32_bf16 v[168:171], a[48:51], v[156:159], v[168:171]\n"
      "s_branch L_join_21_%=\n"
      "L_flg_128_%=:\n"
      "v_cmp_gt_i32 vcc, 0, v188\n"
      "s_cbranch_vccz L_fb_129_%=\n"
      "s_sleep 1\n"
      "ds_read_b128 v[188:191], v128 offset:2048\n"
      "s_waitcnt lgkmcnt(0)\n"
      "v_bitop3_b32 v188, v188, v189, s4 bitop3:0x7e\n"
      "v_bitop3_b32 v190, v190, v191, s4 bitop3:0x7e\n"
      "v_or_b32_e64 v188, v188, v190\n"
      "v_cmp_ne_u32 s[48:49], 0, v188\n"
      "s_branch L_flg_128_%=\n"
      "L_gen_28_%=:\n"
      "s_waitcnt lgkmcnt(0)\n"
      "s_nop 5\n"
      "s_cmp_eq_u64 s[34:35], 0\n"
      "s_cbranch_scc1 L_ok_146_%=\n"
      "ds_read_b128 v[164:167], v115\n"
      "ds_read_b128 v[172:175], v114\n"
      "s_waitcnt lgkmcnt(0)\n"
      "v_mfma_f32_16x16x32_bf16 v[140:143], a[4:7], v[172:175], 0\n"
      "ds_read_b128 v[196:199], v114 offset:6144\n"
      "v_mfma_f32_16x16x32_bf16 v[140:143], a[40:43], v[164:167], v[140:143]\n"
      "ds_read_b128 v[200:203], v115 offset:6144\n"
      "v_mfma_f32_16x16x32_bf16 v[212:215], a[0:3], v[172:175], 0\n"
      "v_mfma_f32_16x16x32_bf16 v[212:215], a[36:39], v[164:167], v[212:215]\n"
      "v_mfma_f32_16x16x32_bf16 v[188:191], a[8:11], v[172:175], 0\n"
      "v_mfma_f32_16x16x32_bf16 v[188:191], a[44:47], v[164:167], v[188:191]\n"
      "v_mfma_f32_16x16x32_bf16 v[168:171], a[12:15], v[172:175], 0\n"
      "ds_read_b128 v[232:235], v115 offset:12288\n"
      "v_mfma_f32_16x16x32_bf16 v[168:171], a[48:51], v[164:167], v[168:171]\n"
      "v_mfma_f32_16x16x32_bf16 v[224:227], a[20:23], v[172:175], 0\n"
      "v_mfma_f32_16x16x32_bf16 v[224:227], a[56:59], v[164:167], v[224:227]\n"
      "ds_read_b128 v[152:155], v114 offset:12288\n"
      "v_mfma_f32_16x16x32_bf16 v[136:139], a[16:19], v[172:175], 0\n"
      "s_waitcnt lgkmcnt(2)\n"
      "v_mfma_f32_16x16x32_bf16 v[136:139], a[52:55], v[164:167], v[136:139]\n"
      "s_nop 0\n"
      "v_mfma_f32_16x16x32_bf16 v[212:215], a[72:75], v[196:199], v[212:215]\n"
      "v_mfma_f32_16x16x32_bf16 v[212:215], a[108:111], v[200:203], v[212:215]\n"
      "v_mfma_f32_16x16x32_bf16 v[140:143], a[76:79], v[196:199], v[140:143]\n"
      "v_mfma_f32_16x16x32_bf16 v[140:143], a[112:115], v[200:203], v[140:143]\n"
      "v_mfma_f32_16x16x32_bf16 v[168:171], a[84:87], v[196:199], v[168:171]\n"
      "ds_read_b128 v[164:167], v115 offset:2048\n"
      "v_mfma_f32_16x16x32_bf16 v[168:171], a[120:123], v[200:203], v[168:171]\n"
      "v_mfma_f32_16x16x32_bf16 v[136:139], a[88:91], v[196:199], v[136:139]\n"
      "s_waitcnt lgkmcnt(1)\n"
      "v_mfma_f32_16x16x32_bf16 v[136:139], a[124:127], v[200:203], v[136:139]\n"
      "v_mfma_f32_16x16x32_bf16 v[224:227], a[92:95], v[196:199], v[224:227]\n"
      "v_mfma_f32_16x16x32_bf16 v[188:191], a[80:83], v[196:199], v[188:191]\n"
      "s_nop 0\n"
      "v_mfma_f32_16x16x32_bf16 v[188:191], a[116:119], v[200:203], v[188:191]\n"
      "v_mfma_f32_16x16x32_bf16 v[212:215], a[144:147], v[152:155], v[212:215]\n"
      "v_mfma_f32_16x16x32_bf16 v[212:215], a[180:183], v[232:235], v[212:215]\n"
      "v_mfma_f32_16x16x32_bf16 v[188:191], a[152:155], v[152:155], v[188:191]\n"
      "v_mfma_f32_16x16x32_bf16 v[188:191], a[188:191], v[232:235], v[188:191]\n"
      "v_mfma_f32_16x16x32_bf16 v[140:143], a[148:151], v[152:155], v[140:143]\n"
      "v_mfma_f32_16x16x32_bf16 v[140:143], a[184:187], v[232:235], v[140:143]\n"
      "v_mfma_f32_16x16x32_bf16 v[168:171], a[156:159], v[152:155], v[168:171]\n"
      "v_mfma_f32_16x16x32_bf16 v[168:171], a[192:195], v[232:235], v[168:171]\n"
      "v_mfma_f32_16x16x32_bf16 v[224:227], a[128:131], v[200:203], v[224:227]\n"
      "s_nop 0\n"
      "v_fma_f32 v206, v212, v212, s38\n"
      "v_fmac_f32 v206, v213, v213\n"
      "v_mfma_f32_16x16x32_bf16 v[224:227], a[164:167], v[152:155], v[224:227]\n"
      "v_fmac_f32 v206, v214, v214\n"
      "v_mfma_f32_16x16x32_bf16 v[224:227], a[200:203], v[232:235], v[224:227]\n"
      "s_nop 0\n"
      "v_fma_f32 v207, v168, v168, s38\n"
      "v_fmac_f32 v207, v169, v169\n"
      "v_mfma_f32_16x16x32_bf16 v[136:139], a[160:163], v[152:155], v[136:139]\n"
      "v_fmac_f32 v206, v215, v215\n"
      "v_fmac_f32 v207, v170, v170\n"
      "v_mfma_f32_16x16x32_bf16 v[136:139], a[196:199], v[232:235], v[136:139]\n"
      "v_fmac_f32 v206, v140, v140\n"
      "v_fmac_f32 v206, v141, v141\n"
      "v_fmac_f32 v206, v142, v142\n"
      "v_fmac_f32 v206, v143, v143\n"
      "v_fmac_f32 v206, v188, v188\n"
      "v_fmac_f32 v206, v189, v189\n"
      "v_fmac_f32 v206, v190, v190\n"
      "v_fmac_f32 v207, v171, v171\n"
      "v_fmac_f32 v207, v136, v136\n"
      "v_fmac_f32 v207, v137, v137\n"
      "v_fmac_f32 v207, v138, v138\n"
      "v_fmac_f32 v207, v139, v139\n"
      "v_fmac_f32 v207, v224, v224\n"
      "v_fmac_f32 v207, v225, v225\n"
      "v_fmac_f32 v207, v226, v226\n"
      "v_fmac_f32 v207, v227, v227\n"
      "v_fmac_f32 v206, v191, v191\n"
      "ds_read_b128 v[172:175], v114 offset:2048\n"
      "s_nop 0\n"
      "v_permlane16_swap_b32 v206, v207\n"
      "v_add_f32 v206, v206, v207\n"
      "v_mov_b32 v207, v206\n"
      "s_nop 1\n"
      "v_permlane32_swap_b32 v206, v207\n"
      "v_add_f32 v206, v206, v207\n"
      "v_rsq_f32 v206, v206\n"
      "s_nop 0\n"
      "v_mov_b32 v207, v206\n"
      "s_nop 1\n"
      "v_permlane16_swap_b32 v206, v207\n"
      "v_mul_f32 v224, v207, v224\n"
      "v_mul_f32 v137, v207, v137\n"
      "v_mul_f32 v206, s36, v206\n"
      "v_mul_f32 v170, v207, v170\n"
      "v_mul_f32 v168, v207, v168\n"
      "v_mul_f32 v227, v207, v227\n"
      "v_mul_f32 v214, v206, v214\n"
      "v_mul_f32 v215, v206, v215\n"
      "v_mul_f32 v225, v207, v225\n"
      "v_mul_f32 v141, v206, v141\n"
      "v_mul_f32 v143, v206, v143\n"
      "v_mul_f32 v169, v207, v169\n"
      "v_cvt_pk_f16_f32 v168, v168, v169\n"
      "v_mul_f32 v190, v206, v190\n"
      "v_mul_f32 v191, v206, v191\n"
      "v_mul_f32 v226, v207, v226\n"
      "v_mul_f32 v213, v206, v213\n"
      "v_mul_f32 v171, v207, v171\n"
      "v_cvt_pk_f16_f32 v34, v224, v225\n"
      "v_cvt_pk_f16_f32 v35, v226, v227\n"
      "s_waitcnt lgkmcnt(0)\n"
      "ds_read_b128 v[196:199], v115 offset:8192\n"
      "v_mul_f32 v212, v206, v212\n"
      "v_mul_f32 v140, v206, v140\n"
      "v_mul_f32 v138, v207, v138\n"
      "v_mul_f32 v142, v206, v142\n"
      "v_mul_f32_e64 v136, v207, v136\n"
      "v_cvt_pk_f16_f32 v169, v170, v171\n"
      "v_cvt_pk_f16_f32 v212, v212, v213\n"
      "v_cvt_pk_f16_f32 v213, v214, v215\n"
      "v_mfma_f32_16x16x32_bf16 v[192:195], a[0:3], v[172:175], 0\n"
      "ds_read_b128 v[200:203], v114 offset:8192\n"
      "v_mul_f32_e64 v188, v206, v188\n"
      "v_mfma_f32_16x16x32_bf16 v[192:195], a[36:39], v[164:167], v[192:195]\n"
      "v_cvt_pk_f16_f32 v215, v142, v143\n"
      "v_mul_f32_e64 v189, v206, v189\n"
      "v_mfma_f32_16x16x32_bf16 v[224:227], a[20:23], v[172:175], 0\n"
      "v_cvt_pk_f16_f32 v170, v136, v137\n"
      "v_cvt_pk_f16_f32 v214, v140, v141\n"
      "v_mfma_f32_16x16x32_bf16 v[140:143], a[4:7], v[172:175], 0\n"
      "v_cvt_pk_f16_f32 v32, v188, v189\n"
      "v_mul_f32_e64 v139, v207, v139\n"
      "v_mfma_f32_16x16x32_bf16 v[140:143], a[40:43], v[164:167], v[140:143]\n"
      "v_mfma_f32_16x16x32_bf16 v[160:163], a[12:15], v[172:175], 0\n"
      "v_cvt_pk_f16_f32 v171, v138, v139\n"
      "v_mfma_f32_16x16x32_bf16 v[160:163], a[48:51], v[164:167], v[160:163]\n"
      "ds_read_b128 v[152:155], v115 offset:14336\n"
      "v_cvt_pk_f16_f32 v33, v190, v191\n"
      "v_mfma_f32_16x16x32_bf16 v[224:227], a[56:59], v[164:167], v[224:227]\n"
      "v_mfma_f32_16x16x32_bf16 v[188:191], a[8:11], v[172:175], 0\n"
      "s_nop 0\n"
      "v_mfma_f32_16x16x32_bf16 v[136:139], a[16:19], v[172:175], 0\n"
      "s_waitcnt lgkmcnt(1)\n"
      "v_mfma_f32_16x16x32_bf16 v[136:139], a[52:55], v[164:167], v[136:139]\n"
      "v_mfma_f32_16x16x32_bf16 v[140:143], a[76:79], v[200:203], v[140:143]\n"
      "ds_read_b128 v[172:175], v114 offset:14336\n"
      "v_mfma_f32_16x16x32_bf16 v[140:143], a[112:115], v[196:199], v[140:143]\n"
      "v_mfma_f32_16x16x32_bf16 v[160:163], a[84:87], v[200:203], v[160:163]\n"
      "v_mfma_f32_16x16x32_bf16 v[160:163], a[120:123], v[196:199], v[160:163]\n"
      "v_mfma_f32_16x16x32_bf16 v[192:195], a[72:75], v[200:203], v[192:195]\n"
      "v_mfma_f32_16x16x32_bf16 v[192:195], a[108:111], v[196:199], v[192:195]\n"
      "v_mfma_f32_16x16x32_bf16 v[188:191], a[44:47], v[164:167], v[188:191]\n"
      "v_mfma_f32_16x16x32_bf16 v[188:191], a[80:83], v[200:203], v[188:191]\n"
      "s_nop 0\n"
      "v_mfma_f32_16x16x32_bf16 v[188:191], a[116:119], v[196:199], v[188:191]\n"
      "s_waitcnt lgkmcnt(0)\n"
      "v_mfma_f32_16x16x32_bf16 v[224:227], a[92:95], v[200:203], v[224:227]\n"
      "v_mfma_f32_16x16x32_bf16 v[224:227], a[128:131], v[196:199], v[224:227]\n"
      "v_mfma_f32_16x16x32_bf16 v[136:139], a[88:91], v[200:203], v[136:139]\n"
      "v_mfma_f32_16x16x32_bf16 v[136:139], a[124:127], v[196:199], v[136:139]\n"
      "v_mfma_f32_16x16x32_bf16 v[192:195], a[144:147], v[172:175], v[192:195]\n"
      "v_mfma_f32_16x16x32_bf16 v[192:195], a[180:183], v[152:155], v[192:195]\n"
      "v_mfma_f32_16x16x32_bf16 v[140:143], a[148:151], v[172:175], v[140:143]\n"
      "v_mfma_f32_16x16x32_bf16 v[140:143], a[184:187], v[152:155], v[140:143]\n"
      "ds_read_b128 v[164:167], v115 offset:4096\n"
      "v_mfma_f32_16x16x32_bf16 v[188:191], a[152:155], v[172:175], v[188:191]\n"
      "v_mfma_f32_16x16x32_bf16 v[136:139], a[160:163], v[172:175], v[136:139]\n"
      "v_mfma_f32_16x16x32_bf16 v[136:139], a[196:199], v[152:155], v[136:139]\n"
      "s_nop 1\n"
      "v_fma_f32 v206, v192, v192, s38\n"
      "v_mfma_f32_16x16x32_bf16 v[160:163], a[156:159], v[172:175], v[160:163]\n"
      "v_fmac_f32 v206, v193, v193\n"
      "v_mfma_f32_16x16x32_bf16 v[160:163], a[192:195], v[152:155], v[160:163]\n"
      "v_fmac_f32 v206, v194, v194\n"
      "v_mfma_f32_16x16x32_bf16 v[188:191], a[188:191], v[152:155], v[188:191]\n"
      "v_fmac_f32 v206, v195, v195\n"
      "v_fmac_f32_e64 v206, v140, v140\n"
      "v_mfma_f32_16x16x32_bf16 v[224:227], a[164:167], v[172:175], v[224:227]\n"
      "ds_read_b128 v[172:175], v114 offset:4096\n"
      "v_fmac_f32 v206, v141, v141\n"
      "v_mfma_f32_16x16x32_bf16 v[224:227], a[200:203], v[152:155], v[224:227]\n"
      "v_fmac_f32 v206, v142, v142\n"
      "v_fma_f32 v207, v160, v160, s38\n"
      "v_fmac_f32 v207, v161, v161\n"
      "v_fmac_f32 v207, v162, v162\n"
      "v_fmac_f32 v207, v163, v163\n"
      "v_fmac_f32 v207, v136, v136\n"
      "v_fmac_f32 v206, v143, v143\n"
      "v_fmac_f32 v207, v137, v137\n"
      "v_fmac_f32 v207, v138, v138\n"
      "v_fmac_f32 v207, v139, v139\n"
      "v_fmac_f32 v206, v188, v188\n"
      "v_fmac_f32 v207, v224, v224\n"
      "v_fmac_f32 v207, v225, v225\n"
      "v_fmac_f32 v206, v189, v189\n"
      "v_fmac_f32 v207, v226, v226\n"
      "v_fmac_f32 v207, v227, v227\n"
      "v_fmac_f32 v206, v190, v190\n"
      "v_fmac_f32 v206, v191, v191\n"
      "s_nop 1\n"
      "v_permlane16_swap_b32 v206, v207\n"
      "v_add_f32 v206, v206, v207\n"
      "v_mov_b32 v207, v206\n"
      "s_nop 1\n"
      "v_permlane32_swap_b32 v206, v207\n"
      "v_add_f32 v206, v206, v207\n"
      "v_rsq_f32 v206, v206\n"
      "s_waitcnt lgkmcnt(0)\n"
      "v_mfma_f32_16x16x32_bf16 v[208:211], a[0:3], v[172:175], 0\n"
      "ds_read_b128 v[196:199], v114 offset:10240\n"
      "v_mov_b32 v207, v206\n"
      "v_mfma_f32_16x16x32_bf16 v[208:211], a[36:39], v[164:167], v[208:211]\n"
      "ds_read_b128 v[200:203], v115 offset:10240\n"
      "v_permlane16_swap_b32 v206, v207\n"
      "v_mul_f32 v138, v207, v138\n"
      "v_mfma_f32_16x16x32_bf16 v[148:151], a[12:15], v[172:175], 0\n"
      "ds_read_b128 v[152:155], v115 offset:16384\n"
      "v_mul_f32 v161, v207, v161\n"
      "v_mfma_f32_16x16x32_bf16 v[148:151], a[48:51], v[164:167], v[148:151]\n"
      "v_mul_f32 v225, v207, v225\n"
      "v_mul_f32 v160, v207, v160\n"
      "v_mul_f32 v163, v207, v163\n"
      "v_mul_f32 v136, v207, v136\n"
      "v_mul_f32 v137, v207, v137\n"
      "v_cvt_pk_f16_f32 v160, v160, v161\n"
      "v_mul_f32 v162, v207, v162\n"
      "v_mfma_f32_16x16x32_bf16 v[232:235], a[20:23], v[172:175], 0\n"
      "v_cvt_pk_f16_f32 v161, v162, v163\n"
      "v_mul_f32 v139, v207, v139\n"
      "v_mul_f32 v206, s36, v206\n"
      "v_cvt_pk_f16_f32 v162, v136, v137\n"
      "v_cvt_pk_f16_f32 v163, v138, v139\n"
      "v_mfma_f32_16x16x32_bf16 v[136:139], a[16:19], v[172:175], 0\n"
      "v_mul_f32 v224, v207, v224\n"
      "v_mul_f32 v227, v207, v227\n"
      "v_mfma_f32_16x16x32_bf16 v[136:139], a[52:55], v[164:167], v[136:139]\n"
      "ds_read_b128 v[236:239], v114 offset:16384\n"
      "v_mul_f32 v226, v207, v226\n"
      "v_mfma_f32_16x16x32_bf16 v[232:235], a[56:59], v[164:167], v[232:235]\n"
      "s_waitcnt lgkmcnt(2)\n"
      "v_cvt_pk_f16_f32 v40, v224, v225\n"
      "v_cvt_pk_f16_f32 v41, v226, v227\n"
      "v_mfma_f32_16x16x32_bf16 v[208:211], a[72:75], v[196:199], v[208:211]\n"
      "v_mul_f32 v192, v206, v192\n"
      "v_mul_f32 v193, v206, v193\n"
      "v_mfma_f32_16x16x32_bf16 v[208:211], a[108:111], v[200:203], v[208:211]\n"
      "v_mul_f32 v140, v206, v140\n"
      "v_mul_f32 v194, v206, v194\n"
      "v_mfma_f32_16x16x32_bf16 v[136:139], a[88:91], v[196:199], v[136:139]\n"
      "v_mul_f32 v143, v206, v143\n"
      "v_cvt_pk_f16_f32 v192, v192, v193\n"
      "v_mfma_f32_16x16x32_bf16 v[136:139], a[124:127], v[200:203], v[136:139]\n"
      "v_mul_f32_e64 v195, v206, v195\n"
      "v_cvt_pk_f16_f32 v193, v194, v195\n"
      "v_mfma_f32_16x16x32_bf16 v[148:151], a[84:87], v[196:199], v[148:151]\n"
      "v_mul_f32 v142, v206, v142\n"
      "v_mul_f32 v189, v206, v189\n"
      "v_mfma_f32_16x16x32_bf16 v[148:151], a[120:123], v[200:203], v[148:151]\n"
      "v_mul_f32 v141, v206, v141\n"
      "v_mul_f32 v188, v206, v188\n"
      "v_mfma_f32_16x16x32_bf16 v[232:235], a[92:95], v[196:199], v[232:235]\n"
      "v_mul_f32_e64 v190, v206, v190\n"
      "v_cvt_pk_f16_f32 v194, v140, v141\n"
      "v_mfma_f32_16x16x32_bf16 v[232:235], a[128:131], v[200:203], v[232:235]\n"
      "v_cvt_pk_f16_f32 v195, v142, v143\n"
      "v_mfma_f32_16x16x32_bf16 v[140:143], a[4:7], v[172:175], 0\n"
      "v_mul_f32 v191, v206, v191\n"
      "v_cvt_pk_f16_f32 v39, v190, v191\n"
      "v_mfma_f32_16x16x32_bf16 v[140:143], a[40:43], v[164:167], v[140:143]\n"
      "v_cvt_pk_f16_f32 v38, v188, v189\n"
      "s_waitcnt lgkmcnt(0)\n"
      "v_mfma_f32_16x16x32_bf16 v[188:191], a[8:11], v[172:175], 0\n"
      "v_mfma_f32_16x16x32_bf16 v[188:191], a[44:47], v[164:167], v[188:191]\n"
      "v_mfma_f32_16x16x32_bf16 v[188:191], a[80:83], v[196:199], v[188:191]\n"
      "v_mfma_f32_16x16x32_bf16 v[188:191], a[116:119], v[200:203], v[188:191]\n"
      "v_mfma_f32_16x16x32_bf16 v[140:143], a[76:79], v[196:199], v[140:143]\n"
      "v_mfma_f32_16x16x32_bf16 v[140:143], a[112:115], v[200:203], v[140:143]\n"
      "v_mfma_f32_16x16x32_bf16 v[140:143], a[148:151], v[236:239], v[140:143]\n"
      "v_mfma_f32_16x16x32_bf16 v[140:143], a[184:187], v[152:155], v[140:143]\n"
      "v_mfma_f32_16x16x32_bf16 v[208:211], a[144:147], v[236:239], v[208:211]\n"
      "v_mfma_f32_16x16x32_bf16 v[208:211], a[180:183], v[152:155], v[208:211]\n"
      "v_mfma_f32_16x16x32_bf16 v[148:151], a[156:159], v[236:239], v[148:151]\n"
      "v_mfma_f32_16x16x32_bf16 v[148:151], a[192:195], v[152:155], v[148:151]\n"
      "v_mfma_f32_16x16x32_bf16 v[136:139], a[160:163], v[236:239], v[136:139]\n"
      "v_mfma_f32_16x16x32_bf16 v[136:139], a[196:199], v[152:155], v[136:139]\n"
      "v_mfma_f32_16x16x32_bf16 v[188:191], a[152:155], v[236:239], v[188:191]\n"
      "s_nop 0\n"
      "v_mfma_f32_16x16x32_bf16 v[188:191], a[188:191], v[152:155], v[188:191]\n"
      "s_nop 1\n"
      "v_fma_f32 v206, v208, v208, s38\n"
      "v_mfma_f32_16x16x32_bf16 v[232:235], a[164:167], v[236:239], v[232:235]\n"
      "v_fma_f32 v207, v148, v148, s38\n"
      "v_fmac_f32 v207, v149, v149\n"
      "v_mfma_f32_16x16x32_bf16 v[232:235], a[200:203], v[152:155], v[232:235]\n"
      "v_fmac_f32 v207, v150, v150\n"
      "v_fmac_f32 v206, v209, v209\n"
      "v_fmac_f32 v207, v151, v151\n"
      "v_fmac_f32 v206, v210, v210\n"
      "v_mfma_f32_16x16x32_f16 v[164:167], v[220:223], v[212:215], v[70:73]\n"
      "v_fmac_f32 v206, v211, v211\n"
      "v_fmac_f32_e64 v206, v140, v140\n"
      "v_mfma_f32_16x16x32_f16 v[164:167], v[228:231], v[168:171], v[164:167]\n"
      "v_fmac_f32 v206, v141, v141\n"
      "v_fmac_f32 v207, v136, v136\n"
      "v_mfma_f32_16x16x32_f16 v[164:167], v[144:147], v[32:35], v[164:167]\n"
      "v_fmac_f32 v207, v137, v137\n"
      "v_fmac_f32 v207, v138, v138\n"
      "v_fmac_f32 v206, v142, v142\n"
      "v_fmac_f32 v207, v139, v139\n"
      "v_fmac_f32 v207, v232, v232\n"
      "v_fmac_f32 v206, v143, v143\n"
      "v_fmac_f32 v207, v233, v233\n"
      "v_fmac_f32 v207, v234, v234\n"
      "v_fmac_f32 v206, v188, v188\n"
      "v_fmac_f32 v206, v189, v189\n"
      "v_fmac_f32 v207, v235, v235\n"
      "v_fmac_f32 v206, v190, v190\n"
      "v_fmac_f32 v206, v191, v191\n"
      "v_mov_b32 v204, v164\n"
      "v_mov_b32 v205, v165\n"
      "v_permlane16_swap_b32 v206, v207\n"
      "v_add_f32 v206, v206, v207\n"
      "v_mov_b32 v207, v206\n"
      "s_nop 1\n"
      "v_permlane32_swap_b32 v206, v207\n"
      "v_add_f32 v206, v206, v207\n"
      "v_rsq_f32 v206, v206\n"
      "s_nop 0\n"
      "v_mov_b32 v207, v206\n"
      "s_nop 1\n"
      "v_permlane16_swap_b32 v206, v207\n"
      "v_mul_f32 v233, v207, v233\n"
      "v_mul_f32 v149, v207, v149\n"
      "v_mul_f32 v151, v207, v151\n"
      "v_mul_f32 v206, s36, v206\n"
      "v_mul_f32 v148, v207, v148\n"
      "v_mul_f32 v137, v207, v137\n"
      "v_cvt_pk_f16_f32 v148, v148, v149\n"
      "v_mul_f32 v208, v206, v208\n"
      "v_mul_f32 v232, v207, v232\n"
      "v_mul_f32 v150, v207, v150\n"
      "v_mul_f32 v136, v207, v136\n"
      "v_mul_f32 v190, v206, v190\n"
      "v_cvt_pk_f16_f32 v149, v150, v151\n"
      "v_mul_f32 v235, v207, v235\n"
      "v_mul_f32 v211, v206, v211\n"
      "v_cvt_pk_f16_f32 v150, v136, v137\n"
      "v_cvt_pk_f16_f32 v46, v232, v233\n"
      "v_mul_f32 v139, v207, v139\n"
      "v_mul_f32 v138, v207, v138\n"
      "v_mul_f32 v140, v206, v140\n"
      "v_cvt_pk_f16_f32 v151, v138, v139\n"
      "v_mul_f32 v234, v207, v234\n"
      "v_mul_f32 v142, v206, v142\n"
      "v_cvt_pk_f16_f32 v47, v234, v235\n"
      "v_mul_f32 v143, v206, v143\n"
      "v_mul_f32 v210, v206, v210\n"
      "v_mul_f32_e64 v209, v206, v209\n"
      "v_mfma_f32_16x16x32_f16 v[136:139], v[148:151], v[212:215], v[86:89]\n"
      "v_cvt_pk_f16_f32 v208, v208, v209\n"
      "v_cvt_pk_f16_f32 v209, v210, v211\n"
      "v_mfma_f32_16x16x32_f16 v[136:139], v[46:49], v[32:35], v[136:139]\n"
      "v_mul_f32 v141, v206, v141\n"
      "v_cvt_pk_f16_f32 v210, v140, v141\n"
      "v_mul_f32 v188, v206, v188\n"
      "v_cvt_pk_f16_f32 v211, v142, v143\n"
      "v_mfma_f32_16x16x32_f16 v[140:143], v[168:171], v[212:215], v[78:81]\n"
      "v_mul_f32 v191, v206, v191\n"
      "v_mul_f32 v189, v206, v189\n"
      "v_mfma_f32_16x16x32_f16 v[140:143], v[34:37], v[32:35], v[140:143]\n"
      "v_cvt_pk_f16_f32 v45, v190, v191\n"
      "v_cvt_pk_f16_f32 v44, v188, v189\n"
      "v_mfma_f32_16x16x32_f16 v[188:191], v[160:163], v[212:215], v[82:85]\n"
      "s_nop 0\n"
      "v_mfma_f32_16x16x32_f16 v[188:191], v[40:43], v[32:35], v[188:191]\n"
      "s_nop 7\n"
      "s_or_b64 vcc, s[48:49], s[50:51]\n"
      "s_cbranch_scc0 L_fast_31_%=\n"
      "L_ok_146_%=:\n"
      "s_cmp_eq_u64 s[48:49], 0\n"
      "s_cbranch_scc1 L_go_147_%=\n"
      "s_mov_b32 m0, s27\n"
      "s_nop 0\n"
      "s_movrels_b32 s9, s80\n"
      "s_lshr_b32 s8, s9, 8\n"
      "s_and_b32 s9, s9, 0xff\n"
      "s_mul_i32 s10, s8, 73\n"
      "s_add_u32 s10, s10, s9\n"
      "s_mul_i32 s30, s10, 4802\n"
      "s_add_u32 s30, s30, s18\n"
      "s_and_b32 s29, s30, 15\n"
      "s_add_u32 s30, s29, 110592\n"
      "s_lshl_b64 s[34:35], s[48:49], 4\n"
      "s_lshr_b64 s[34:35], s[34:35], 5\n"
      "s_cbranch_scc1 L_gen_29_%=\n"
      "v_mbcnt_lo_u32_b32 v206, -1, 0\n"
      "v_mbcnt_hi_u32_b32 v206, -1, v206\n"
      "v_lshlrev_b32 v207, 1, v206\n"
      "v_cmp_gt_u32 vcc, s29, v207\n"
      "v_add_u32 v165, 4802, v207\n"
      "s_nop 0\n"
      "v_cndmask_b32 v207, v165, v207, vcc\n"
      "v_add_u32 v207, 110592, v207\n"
      "v_mov_b32 v165, 0\n"
      "s_mov_b64 exec, 0x7f\n"
      "ds_write_b16 v207, v165\n"
      "s_mov_b64 exec, -1\n"
      "v_lshlrev_b32 v207, 4, v206\n"
      "v_add_u32 v207, 110592, v207\n"
      "ds_read_b128 v[164:167], v207 offset:1024\n"
      "ds_read_b128 v[172:175], v207\n"
      "v_min_u32 v206, 111296, v207\n"
      "ds_read_b128 v[196:199], v207 offset:2048\n"
      "ds_read_b128 v[200:203], v207 offset:3072\n"
      "ds_read_b128 v[152:155], v206 offset:4096\n"
      "s_waitcnt lgkmcnt(4)\n"
      "s_waitcnt lgkmcnt(3)\n"
      "v_or3_b32 v172, v172, v173, v174\n"
      "v_or3_b32 v172, v172, v175, v164\n"
      "v_or3_b32 v172, v172, v165, v166\n"
      "s_waitcnt lgkmcnt(2)\n"
      "v_or3_b32 v172, v172, v167, v196\n"
      "v_or3_b32 v172, v172, v197, v198\n"
      "s_waitcnt lgkmcnt(1)\n"
      "s_waitcnt lgkmcnt(0)\n"
      "v_or3_b32 v172, v172, v199, v200\n"
      "v_or3_b32 v172, v172, v201, v202\n"
      "v_or3_b32 v172, v172, v203, v152\n"
      "v_or3_b32 v172, v172, v153, v154\n"
      "v_bitop3_b32 v172, v172, v155, s40 bitop3:0xa8\n"
      "v_cmp_ne_u32 s[48:49], 0, v172\n"
      "s_cmp_lg_u64 s[48:49], 0\n"
      "s_cbranch_scc1 L_gen_29_%=\n"
      "s_cmp_eq_u64 s[50:51], 0\n"
      "s_cbranch_scc1 L_fast_31_%=\n"
      "L_go_147_%=:\n"
      "v_mfma_f32_16x16x32_f16 v[164:167], v[168:171], v[192:195], v[90:93]\n"
      "ds_read_b128 v[172:175], v128 offset:3072\n"
      "v_max3_f32 v206, v143, v188, v189\n"
      "v_mfma_f32_16x16x32_f16 v[164:167], v[34:37], v[38:41], v[164:167]\n"
      "v_max3_f32 v206, v206, v136, v137\n"
      "v_max3_f32 v207, v140, v141, v142\n"
      "v_mfma_f32_16x16x32_f16 v[196:199], v[160:163], v[192:195], v[94:97]\n"
      "v_max3_f32 v207, v207, v190, v191\n"
      "v_max3_f32 v207, v207, v138, v139\n"
      "v_mfma_f32_16x16x32_f16 v[196:199], v[40:43], v[38:41], v[196:199]\n"
      "v_max3_f32 v207, v207, v206, v204\n"
      "v_mov_b32 v206, v207\n"
      "v_mfma_f32_16x16x32_f16 v[200:203], v[220:223], v[192:195], v[72:75]\n"
      "s_nop 0\n"
      "v_permlane16_swap_b32 v207, v206\n"
      "v_mfma_f32_16x16x32_f16 v[200:203], v[228:231], v[160:163], v[200:203]\n"
      "v_max_f32 v207, v207, v206\n"
      "v_mov_b32 v206, v207\n"
      "v_mfma_f32_16x16x32_f16 v[200:203], v[144:147], v[38:41], v[200:203]\n"
      "s_nop 0\n"
      "v_permlane32_swap_b32 v207, v206\n"
      "v_max_f32 v207, v207, v206\n"
      "v_sub_f32 v138, v138, v207\n"
      "v_sub_f32 v190, v190, v207\n"
      "v_sub_f32 v141, v141, v207\n"
      "v_mfma_f32_16x16x32_f16 v[152:155], v[148:151], v[192:195], v[98:101]\n"
      "v_sub_f32 v137, v137, v207\n"
      "v_sub_f32 v142, v142, v207\n"
      "v_mfma_f32_16x16x32_f16 v[152:155], v[46:49], v[38:41], v[152:155]\n"
      "s_waitcnt lgkmcnt(0)\n"
      "v_exp_f32 v142, v142\n"
      "v_sub_f32 v191, v191, v207\n"
      "v_sub_f32 v188, v188, v207\n"
      "v_sub_f32 v189, v189, v207\n"
      "v_exp_f32 v189, v189\n"
      "v_exp_f32 v188, v188\n"
      "v_sub_f32 v136, v136, v207\n"
      "v_sub_f32 v140, v140, v207\n"
      "v_sub_f32 v139, v139, v207\n"
      "v_exp_f32 v140, v140\n"
      "v_exp_f32 v190, v190\n"
      "v_sub_f32 v143, v143, v207\n"
      "v_exp_f32 v143, v143\n"
      "v_bitop3_b32 v172, v172, v173, s4 bitop3:0x7e\n"
      "v_exp_f32 v141, v141\n"
      "v_sub_f32_e64 v204, v204, v207\n"
      "v_cvt_pk_bf16_f32 v140, v140, v141\n"
      "v_bitop3_b32 v174, v174, v175, s4 bitop3:0x7e\n"
      "v_exp_f32 v204, v204\n"
      "v_exp_f32 v137, v137\n"
      "v_or_b32 v172, v172, v174\n"
      "v_cvt_pk_bf16_f32 v141, v142, v143\n"
      "v_exp_f32 v136, v136\n"
      "v_cmp_ne_u32 vcc, 0, v172\n"
      "v_exp_f32 v191, v191\n"
      "v_cvt_pk_bf16_f32 v64, v204, 0\n"
      "v_cvt_pk_bf16_f32 v142, v188, v189\n"
      "v_exp_f32 v138, v138\n"
      "v_exp_f32 v139, v139\n"
      "v_cvt_pk_bf16_f32 v143, v190, v191\n"
      "v_cvt_pk_bf16_f32 v63, v138, v139\n"
      "v_cvt_pk_bf16_f32 v62, v136, v137\n"
      "v_mfma_f32_16x16x32_bf16 v[188:191], v[66:69], v[140:143], 0\n"
      "s_mov_b64 s[48:49], vcc\n"
      "v_max3_f32 v203, v167, v196, v197\n"
      "v_max3_f32 v202, v164, v165, v166\n"
      "v_mfma_f32_16x16x32_bf16 v[188:191], v[66:69], v[62:65], v[188:191]\n"
      "s_cbranch_vccnz L_flg_148_%=\n"
      "L_fb_149_%=:\n"
      "v_mfma_f32_16x16x32_bf16 v[172:175], v[216:219], v[140:143], 0\n"
      "v_max3_f32 v202, v202, v198, v199\n"
      "v_max3_f32 v202, v202, v154, v155\n"
      "v_mfma_f32_16x16x32_bf16 v[172:175], v[50:53], v[62:65], v[172:175]\n"
      "v_max3_f32 v203, v203, v152, v153\n"
      "v_max3_f32 v202, v202, v203, v200\n"
      "v_mov_b32 v203, v202\n"
      "v_mfma_f32_16x16x32_bf16 v[192:195], v[244:247], v[140:143], 0\n"
      "s_nop 0\n"
      "v_permlane16_swap_b32 v202, v203\n"
      "v_mfma_f32_16x16x32_bf16 v[192:195], v[54:57], v[62:65], v[192:195]\n"
      "v_max_f32 v202, v202, v203\n"
      "v_mov_b32 v203, v202\n"
      "v_mfma_f32_16x16x32_bf16 v[136:139], v[240:243], v[140:143], 0\n"
      "s_nop 0\n"
      "v_permlane32_swap_b32 v202, v203\n"
      "v_mfma_f32_16x16x32_bf16 v[136:139], v[58:61], v[62:65], v[136:139]\n"
      "v_max_f32 v202, v202, v203\n"
      "v_sub_f32 v198, v198, v202\n"
      "v_sub_f32 v152, v152, v202\n"
      "v_sub_f32 v153, v153, v202\n"
      "v_sub_f32 v167, v167, v202\n"
      "v_sub_f32 v199, v199, v202\n"
      "v_mfma_f32_16x16x32_f16 v[140:143], v[168:171], v[208:211], v[102:105]\n"
      "v_sub_f32 v200, v200, v202\n"
      "v_sub_f32 v154, v154, v202\n"
      "v_mfma_f32_16x16x32_f16 v[140:143], v[34:37], v[44:47], v[140:143]\n"
      "v_sub_f32 v164, v164, v202\n"
      "v_sub_f32 v165, v165, v202\n"
      "v_exp_f32 v164, v164\n"
      "v_sub_f32 v155, v155, v202\n"
      "v_sub_f32 v166, v166, v202\n"
      "v_sub_f32 v197, v197, v202\n"
      "v_exp_f32 v166, v166\n"
      "v_exp_f32_e64 v165, v165\n"
      "v_mfma_f32_16x16x32_f16 v[168:171], v[160:163], v[208:211], v[106:109]\n"
      "ds_read_b128 v[160:163], v120 offset:18432\n"
      "v_cvt_pk_bf16_f32 v164, v164, v165\n"
      "v_mfma_f32_16x16x32_f16 v[168:171], v[40:43], v[44:47], v[168:171]\n"
      "v_exp_f32 v199, v199\n"
      "v_exp_f32 v167, v167\n"
      "v_sub_f32 v196, v196, v202\n"
      "v_mfma_f32_16x16x32_f16 v[212:215], v[220:223], v[208:211], v[74:77]\n"
      "v_exp_f32 v198, v198\n"
      "v_mfma_f32_16x16x32_f16 v[212:215], v[228:231], v[148:151], v[212:215]\n"
      "v_exp_f32 v197, v197\n"
      "v_mfma_f32_16x16x32_f16 v[212:215], v[144:147], v[44:47], v[212:215]\n"
      "v_exp_f32 v152, v152\n"
      "v_exp_f32 v153, v153\n"
      "v_cvt_pk_bf16_f32 v165, v166, v167\n"
      "v_exp_f32 v196, v196\n"
      "v_exp_f32_e64 v200, v200\n"
      "v_mfma_f32_16x16x32_f16 v[232:235], v[148:151], v[208:211], v[110:113]\n"
      "v_cvt_pk_bf16_f32 v166, v196, v197\n"
      "v_cvt_pk_bf16_f32 v167, v198, v199\n"
      "v_mfma_f32_16x16x32_f16 v[232:235], v[46:49], v[44:47], v[232:235]\n"
      "s_waitcnt lgkmcnt(0)\n"
      "v_exp_f32 v155, v155\n"
      "v_mfma_f32_16x16x32_f16 v[208:211], v[228:231], v[220:223], v[76:79]\n"
      "global_store_dwordx4 v117, v[160:163], s[54:55] sc1\n"
      "v_exp_f32 v154, v154\n"
      "v_cvt_pk_bf16_f32 v64, v200, 0\n"
      "v_cvt_pk_bf16_f32 v62, v152, v153\n"
      "v_cvt_pk_bf16_f32 v63, v154, v155\n"
      "v_rcp_f32 v188, v188\n"
      "v_mfma_f32_16x16x16_f16 v[208:211], v[146:147], v[144:145], v[208:211]\n"
      "v_mul_f32 v174, v188, v174\n"
      "v_mul_f32 v172, v188, v172\n"
      "v_mfma_f32_16x16x32_bf16 v[196:199], v[66:69], v[164:167], 0\n"
      "v_mul_f32_e64 v173, v188, v173\n"
      "v_cvt_pk_bf16_f32 v172, v172, v173\n"
      "v_mfma_f32_16x16x32_bf16 v[196:199], v[66:69], v[62:65], v[196:199]\n"
      "v_mul_f32 v195, v188, v195\n"
      "v_mul_f32 v194, v188, v194\n"
      "v_mul_f32_e64 v175, v188, v175\n"
      "v_mfma_f32_16x16x32_bf16 v[144:147], v[216:219], v[164:167], 0\n"
      "v_mul_f32 v193, v188, v193\n"
      "v_cvt_pk_bf16_f32 v173, v174, v175\n"
      "v_mfma_f32_16x16x32_bf16 v[144:147], v[50:53], v[62:65], v[144:147]\n"
      "v_mul_f32 v192, v188, v192\n"
      "v_cvt_pk_bf16_f32 v174, v192, v193\n"
      "v_mul_f32 v138, v188, v138\n"
      "v_mfma_f32_16x16x32_bf16 v[148:151], v[244:247], v[164:167], 0\n"
      "v_cvt_pk_bf16_f32 v175, v194, v195\n"
      "v_mul_f32 v136, v188, v136\n"
      "v_mfma_f32_16x16x32_bf16 v[148:151], v[54:57], v[62:65], v[148:151]\n"
      "ds_write_b128 v124, v[172:175]\n"
      "v_mfma_f32_16x16x32_bf16 v[152:155], v[240:243], v[164:167], 0\n"
      "ds_read_b128 v[192:195], v121 offset:18432\n"
      "v_mul_f32 v139, v188, v139\n"
      "v_mfma_f32_16x16x32_bf16 v[152:155], v[58:61], v[62:65], v[152:155]\n"
      "v_mul_f32 v137, v188, v137\n"
      "v_cvt_pk_bf16_f32 v136, v136, v137\n"
      "v_cvt_pk_bf16_f32 v137, v138, v139\n"
      "v_max3_f32 v139, v140, v141, v142\n"
      "v_max3_f32 v138, v143, v168, v169\n"
      "v_max3_f32 v138, v138, v232, v233\n"
      "v_max3_f32 v139, v139, v170, v171\n"
      "v_max3_f32 v139, v139, v234, v235\n"
      "v_max3_f32 v139, v139, v138, v212\n"
      "v_mov_b32 v138, v139\n"
      "v_rcp_f32 v196, v196\n"
      "s_nop 0\n"
      "v_permlane16_swap_b32 v139, v138\n"
      "v_max_f32 v139, v139, v138\n"
      "v_mov_b32 v138, v139\n"
      "v_mul_f32 v147, v196, v147\n"
      "v_mul_f32 v144, v196, v144\n"
      "v_permlane32_swap_b32 v139, v138\n"
      "v_max_f32 v139, v139, v138\n"
      "v_sub_f32 v142, v142, v139\n"
      "v_sub_f32 v212, v212, v139\n"
      "v_exp_f32 v212, v212\n"
      "v_sub_f32 v233, v233, v139\n"
      "v_sub_f32 v143, v143, v139\n"
      "v_sub_f32 v170, v170, v139\n"
      "v_sub_f32 v140, v140, v139\n"
      "v_sub_f32 v232, v232, v139\n"
      "v_exp_f32 v142, v142\n"
      "v_exp_f32 v140, v140\n"
      "v_sub_f32 v168, v168, v139\n"
      "v_sub_f32 v235, v235, v139\n"
      "v_sub_f32 v141, v141, v139\n"
      "v_exp_f32 v232, v232\n"
      "v_sub_f32 v171, v171, v139\n"
      "v_exp_f32 v141, v141\n"
      "v_exp_f32 v143, v143\n"
      "v_sub_f32 v169, v169, v139\n"
      "v_cvt_pk_bf16_f32 v140, v140, v141\n"
      "v_cvt_pk_bf16_f32 v141, v142, v143\n"
      "v_exp_f32 v170, v170\n"
      "v_sub_f32 v234, v234, v139\n"
      "v_exp_f32 v171, v171\n"
      "v_exp_f32 v233, v233\n"
      "v_cvt_pk_bf16_f32 v64, v212, 0\n"
      "v_exp_f32 v169, v169\n"
      "v_exp_f32 v168, v168\n"
      "v_mul_f32 v145, v196, v145\n"
      "v_exp_f32 v234, v234\n"
      "v_mul_f32 v146, v196, v146\n"
      "v_cvt_pk_bf16_f32 v142, v168, v169\n"
      "v_exp_f32 v235, v235\n"
      "v_cvt_pk_bf16_f32 v143, v170, v171\n"
      "v_cvt_pk_bf16_f32 v62, v232, v233\n"
      "v_cvt_pk_bf16_f32 v63, v234, v235\n"
      "v_mfma_f32_16x16x32_bf16 v[160:163], v[66:69], v[140:143], 0\n"
      "ds_read_b128 v[232:235], v123 offset:22528\n"
      "v_cvt_pk_bf16_f32 v144, v144, v145\n"
      "v_mfma_f32_16x16x32_bf16 v[160:163], v[66:69], v[62:65], v[160:163]\n"
      "ds_read_b128 v[220:223], v123 offset:19456\n"
      "v_mul_f32 v150, v196, v150\n"
      "v_mfma_f32_16x16x32_bf16 v[164:167], v[216:219], v[140:143], 0\n"
      "s_waitcnt lgkmcnt(2)\n"
      "v_cvt_pk_bf16_f32 v145, v146, v147\n"
      "v_mul_f32_e64 v149, v196, v149\n"
      "v_mfma_f32_16x16x32_bf16 v[164:167], v[50:53], v[62:65], v[164:167]\n"
      "s_mov_b32 exec_hi, 0\n"
      "global_store_dwordx4 v118, v[192:195], s[54:55] sc1\n"
      "s_mov_b32 exec_hi, -1\n"
      "v_mfma_f32_16x16x32_bf16 v[172:175], v[244:247], v[140:143], 0\n"
      "s_add_u32 s28, s27, 2\n"
      "v_mul_f32 v148, v196, v148\n"
      "v_cvt_pk_bf16_f32 v146, v148, v149\n"
      "v_mfma_f32_16x16x32_bf16 v[172:175], v[54:57], v[62:65], v[172:175]\n"
      "s_min_u32 m0, s28, s3\n"
      "v_mul_f32 v153, v196, v153\n"
      "v_mul_f32_e64 v151, v196, v151\n"
      "v_cvt_pk_bf16_f32 v147, v150, v151\n"
      "v_mfma_f32_16x16x32_bf16 v[168:171], v[240:243], v[140:143], 0\n"
      "ds_read_b128 v[140:143], v123 offset:25600\n"
      "v_mul_f32 v152, v196, v152\n"
      "v_mfma_f32_16x16x32_bf16 v[168:171], v[58:61], v[62:65], v[168:171]\n"
      "ds_write_b128 v124, v[144:147] offset:1024\n"
      "s_waitcnt lgkmcnt(2)\n"
      "v_mfma_f32_16x16x32_bf16 v[188:191], a[216:219], v[220:223], 0\n"
      "ds_read_b128 v[144:147], v123 offset:28672\n"
      "v_cvt_pk_bf16_f32 v152, v152, v153\n"
      "v_mfma_f32_16x16x32_bf16 v[188:191], a[228:231], v[232:235], v[188:191]\n"
      "ds_read_b128 v[148:151], v123 offset:34816\n"
      "v_mul_f32 v154, v196, v154\n"
      "v_mfma_f32_16x16x32_bf16 v[192:195], a[220:223], v[220:223], 0\n"
      "s_movrels_b32 s9, s80\n"
      "v_mul_f32_e64 v155, v196, v155\n"
      "v_cvt_pk_bf16_f32 v153, v154, v155\n"
      "v_mfma_f32_16x16x32_bf16 v[192:195], a[232:235], v[232:235], v[192:195]\n"
      "ds_write2st64_b64 v125, v[136:137], v[152:153] offset0:0 offset1:2\n"
      "v_mfma_f32_16x16x32_bf16 v[196:199], a[224:227], v[220:223], 0\n"
      "ds_read_b128 v[136:139], v123 offset:31744\n"
      "v_cndmask_b32 v205, v205, v201, s[42:43]\n"
      "v_mfma_f32_16x16x32_bf16 v[196:199], a[236:239], v[232:235], v[196:199]\n"
      "ds_read_b128 v[152:155], v123 offset:20480\n"
      "v_cndmask_b32 v205, v205, v213, s[44:45]\n"
      "v_cndmask_b32 v205, v205, v209, s[46:47]\n"
      "s_lshr_b32 s8, s9, 8\n"
      "s_and_b32 s9, s9, 0xff\n"
      "v_max_f32_dpp v204, v205, v205 row_ror:8 row_mask:0xf bank_mask:0xf\n"
      "s_mul_i32 s10, s8, 73\n"
      "s_nop 0\n"
      "v_max_f32_dpp v204, v204, v204 row_ror:4 row_mask:0xf bank_mask:0xf\n"
      "s_waitcnt lgkmcnt(4)\n"
      "v_mfma_f32_16x16x32_bf16 v[188:191], a[240:243], v[140:143], v[188:191]\n"
      "ds_read_b128 v[232:235], v123 offset:23552\n"
      "v_max_f32_dpp v204, v204, v204 row_ror:2 row_mask:0xf bank_mask:0xf\n"
      "v_mfma_f32_16x16x32_bf16 v[188:191], a[252:255], v[144:147], v[188:191]\n"
      "s_nop 0\n"
      "v_max_f32_dpp v204, v204, v204 row_ror:1 row_mask:0xf bank_mask:0xf\n"
      "v_mfma_f32_16x16x32_bf16 v[192:195], a[244:247], v[140:143], v[192:195]\n"
      "s_add_u32 s10, s10, s9\n"
      "v_max_f32_dpp v204, v204, v204 row_bcast:15 row_mask:0xa bank_mask:0xf\n"
      "s_mul_i32 s28, s10, 4802\n"
      "v_mfma_f32_16x16x32_bf16 v[192:195], v[0:3], v[144:147], v[192:195]\n"
      "s_add_u32 s56, s18, s28\n"
      "v_max_f32_dpp v204, v204, v204 row_bcast:31 row_mask:0xc bank_mask:0xf\n"
      "v_mfma_f32_16x16x32_bf16 v[196:199], a[248:251], v[140:143], v[196:199]\n"
      "s_waitcnt lgkmcnt(2)\n"
      "v_readlane_b32 s31, v204, 63\n"
      "v_mfma_f32_16x16x32_bf16 v[196:199], v[4:7], v[144:147], v[196:199]\n"
      "ds_read_b128 v[144:147], v123 offset:26624\n"
      "v_subrev_f32 v205, s31, v205\n"
      "v_mfma_f32_16x16x32_bf16 v[188:191], v[8:11], v[136:139], v[188:191]\n"
      "s_addc_u32 s57, s19, 0\n"
      "v_exp_f32_e64 v205, v205\n"
      "v_mfma_f32_16x16x32_bf16 v[188:191], v[20:23], v[148:151], v[188:191]\n"
      "s_and_b32 s56, s56, -16\n"
      "s_sub_u32 s58, s56, 110592\n"
      "s_subb_u32 s59, s57, 0\n"
      "v_mfma_f32_16x16x32_bf16 v[196:199], v[16:19], v[136:139], v[196:199]\n"
      "v_cvt_pk_bf16_f32 v205, v205, v205\n"
      "s_waitcnt lgkmcnt(1)\n"
      "v_mfma_f32_16x16x32_bf16 v[196:199], v[28:31], v[148:151], v[196:199]\n"
      "ds_write_b16 v130, v205\n"
      "v_mfma_f32_16x16x32_bf16 v[192:195], v[12:15], v[136:139], v[192:195]\n"
      "s_sub_u32 s56, s56, 73728\n"
      "ds_read_b128 v[220:223], v131 offset:64\n"
      "v_mfma_f32_16x16x32_bf16 v[192:195], v[24:27], v[148:151], v[192:195]\n"
      "ds_read_b128 v[136:139], v114 offset:18432\n"
      "s_subb_u32 s57, s57, 0\n"
      "v_mfma_f32_16x16x32_bf16 v[200:203], a[216:219], v[152:155], 0\n"
      "ds_read_b128 v[212:215], v123 offset:29696\n"
      "v_mfma_f32_16x16x32_bf16 v[204:207], a[220:223], v[152:155], 0\n"
      "ds_read_b128 v[148:151], v131\n"
      "ds_read_b128 v[140:143], v115 offset:18432\n"
      "s_mul_i32 s28, s8, 1373568\n"
      "s_mul_i32 s29, s9, 2688\n"
      "v_mfma_f32_16x16x32_bf16 v[208:211], a[224:227], v[152:155], 0\n"
      "s_add_u32 s28, s28, s29\n"
      "v_fma_f32 v156, v188, v188, s39\n"
      "v_fmac_f32 v156, v189, v189\n"
      "v_mfma_f32_16x16x32_bf16 v[208:211], a[236:239], v[232:235], v[208:211]\n"
      "v_fmac_f32 v156, v190, v190\n"
      "v_fmac_f32 v156, v191, v191\n"
      "v_fmac_f32 v156, v192, v192\n"
      "v_mfma_f32_16x16x32_bf16 v[200:203], a[228:231], v[232:235], v[200:203]\n"
      "v_fmac_f32 v156, v193, v193\n"
      "v_fmac_f32 v156, v194, v194\n"
      "v_fmac_f32 v156, v195, v195\n"
      "v_fmac_f32 v156, v196, v196\n"
      "v_fmac_f32 v156, v197, v197\n"
      "v_fmac_f32 v156, v198, v198\n"
      "v_fmac_f32 v156, v199, v199\n"
      "v_mfma_f32_16x16x32_bf16 v[204:207], a[232:235], v[232:235], v[204:207]\n"
      "ds_read_b128 v[152:155], v123 offset:32768\n"
      "s_cmp_eq_u32 s8, 72\n"
      "s_waitcnt lgkmcnt(3)\n"
      "v_mfma_f32_16x16x32_bf16 v[200:203], a[240:243], v[144:147], v[200:203]\n"
      "s_cselect_b32 s8, 0, 100270464\n"
      "v_permlane16_swap_b32 v158, v156\n"
      "v_mfma_f32_16x16x32_bf16 v[200:203], a[252:255], v[212:215], v[200:203]\n"
      "s_waitcnt lgkmcnt(1)\n"
      "ds_read_b128 v[232:235], v123 offset:35840\n"
      "v_mfma_f32_16x16x32_bf16 v[204:207], a[244:247], v[144:147], v[204:207]\n"
      "s_cmp_eq_u32 s9, 72\n"
      "s_cselect_b32 s9, 0, 196224\n"
      "v_mfma_f32_16x16x32_bf16 v[204:207], v[0:3], v[212:215], v[204:207]\n"
      "s_add_u32 s10, s8, s9\n"
      "s_add_u32 s28, s28, s10\n"
      "s_add_u32 s52, s16, s28\n"
      "v_mfma_f32_16x16x32_bf16 v[208:211], a[248:251], v[144:147], v[208:211]\n"
      "s_addc_u32 s53, s17, 0\n"
      "s_add_u32 s27, s27, 1\n"
      "s_cmp_ge_u32 s27, s26\n"
      "v_mfma_f32_16x16x32_bf16 v[208:211], v[4:7], v[212:215], v[208:211]\n"
      "s_cselect_b32 s28, 1, 0\n"
      "s_xor_b32 s29, s10, s11\n"
      "v_mfma_f32_16x16x32_bf16 v[212:215], v[148:151], v[66:69], 0\n"
      "s_or_b32 s28, s28, s29\n"
      "v_add_f32 v158, v158, v156\n"
      "v_mfma_f32_16x16x32_bf16 v[212:215], v[220:223], v[66:69], v[212:215]\n"
      "v_mfma_f32_16x16x32_bf16 v[228:231], v[148:151], v[216:219], 0\n"
      "v_mfma_f32_16x16x32_bf16 v[228:231], v[220:223], v[50:53], v[228:231]\n"
      "s_waitcnt lgkmcnt(0)\n"
      "v_mfma_f32_16x16x32_bf16 v[200:203], v[8:11], v[152:155], v[200:203]\n"
      "v_mfma_f32_16x16x32_bf16 v[200:203], v[20:23], v[232:235], v[200:203]\n"
      "v_mfma_f32_16x16x32_bf16 v[204:207], v[12:15], v[152:155], v[204:207]\n"
      "s_nop 1\n"
      "v_mov_b32_dpp v212, v212 quad_perm:[0,0,0,0] row_mask:0xf bank_mask:0xf\n"
      "v_mfma_f32_16x16x32_bf16 v[204:207], v[24:27], v[232:235], v[204:207]\n"
      "v_rcp_f32 v212, v212\n"
      "v_mfma_f32_16x16x32_bf16 v[208:211], v[16:19], v[152:155], v[208:211]\n"
      "s_nop 0\n"
      "v_fma_f32 v156, v200, v200, s39\n"
      "v_fmac_f32 v156, v201, v201\n"
      "v_mfma_f32_16x16x32_bf16 v[208:211], v[28:31], v[232:235], v[208:211]\n"
      "v_fmac_f32 v156, v202, v202\n"
      "v_fmac_f32_e64 v156, v203, v203\n"
      "v_mfma_f32_16x16x32_bf16 v[232:235], v[148:151], v[240:243], 0\n"
      "v_fmac_f32 v156, v204, v204\n"
      "v_fmac_f32 v156, v205, v205\n"
      "v_mfma_f32_16x16x32_bf16 v[232:235], v[220:223], v[58:61], v[232:235]\n"
      "v_fmac_f32 v156, v206, v206\n"
      "v_fmac_f32 v156, v207, v207\n"
      "v_mfma_f32_16x16x32_bf16 v[152:155], v[148:151], v[244:247], 0\n"
      "v_fmac_f32 v156, v208, v208\n"
      "v_mfma_f32_16x16x32_bf16 v[152:155], v[220:223], v[54:57], v[152:155]\n"
      "v_fmac_f32 v156, v209, v209\n"
      "v_fmac_f32_e64 v156, v210, v210\n"
      "v_mfma_f32_16x16x32_bf16 v[144:147], a[0:3], v[136:139], 0\n"
      "v_fmac_f32 v156, v211, v211\n"
      "v_mov_b32 v157, v156\n"
      "v_mfma_f32_16x16x32_bf16 v[144:147], a[36:39], v[140:143], v[144:147]\n"
      "s_nop 0\n"
      "v_permlane16_swap_b32 v156, v157\n"
      "v_mfma_f32_16x16x32_bf16 v[148:151], a[4:7], v[136:139], 0\n"
      "v_cndmask_b32 v228, v228, v152, s[42:43]\n"
      "v_add_f32 v156, v156, v157\n"
      "v_mfma_f32_16x16x32_bf16 v[148:151], a[40:43], v[140:143], v[148:151]\n"
      "s_nop 0\n"
      "v_permlane32_swap_b32 v158, v156\n"
      "v_mfma_f32_16x16x32_bf16 v[152:155], a[8:11], v[136:139], 0\n"
      "v_add_f32 v158, v158, v156\n"
      "v_cndmask_b32 v228, v228, v232, s[44:45]\n"
      "v_mfma_f32_16x16x32_bf16 v[152:155], a[44:47], v[140:143], v[152:155]\n"
      "ds_write_b32 v129, v158\n"
      "v_mfma_f32_16x16x32_bf16 v[156:159], a[12:15], v[136:139], 0\n"
      "v_mul_f32_e64 v228, v212, v228\n"
      "v_cvt_pk_bf16_f32 v228, v228, v228\n"
      "v_mfma_f32_16x16x32_bf16 v[156:159], a[48:51], v[140:143], v[156:159]\n"
      "ds_write_b16 v135, v228\n"
      "s_branch L_join_30_%=\n"
      "L_flg_148_%=:\n"
      "v_cmp_gt_i32 vcc, 0, v172\n"
      "s_cbranch_vccz L_fb_149_%=\n"
      "s_sleep 1\n"
      "ds_read_b128 v[172:175], v128 offset:3072\n"
      "s_waitcnt lgkmcnt(0)\n"
      "v_bitop3_b32 v172, v172, v173, s4 bitop3:0x7e\n"
      "v_bitop3_b32 v174, v174, v175, s4 bitop3:0x7e\n"
      "v_or_b32 v172, v172, v174\n"
      "v_cmp_ne_u32 s[48:49], 0, v172\n"
      "s_branch L_flg_148_%=\n"
      "L_gen_29_%=:\n"
      "s_nop 0\n"
      "s_nop 0\n"
      "v_mbcnt_lo_u32_b32 v206, -1, 0\n"
      "v_mbcnt_hi_u32_b32 v206, -1, v206\n"
      "v_and_b32 v207, 15, v206\n"
      "v_mul_u32_u24 v165, 0x62, v207\n"
      "v_lshrrev_b32 v164, 4, v206\n"
      "v_lshl_add_u32 v165, v164, 3, v165\n"
      "v_add_u32 v165, s30, v165\n"
      "v_lshlrev_b32 v164, 2, v164\n"
      "v_and_b32 v206, -4, v165\n"
      "v_sub_u32 v207, v207, v164\n"
      "v_and_b32 v165, 2, v165\n"
      "v_and_b32 v164, 1, v207\n"
      "v_lshrrev_b32 v207, 1, v207\n"
      "v_lshlrev_b32 v164, 4, v164\n"
      "s_movk_i32 s31, 0x3fb9\n"
      "ds_read2_b32 v[166:167], v206 offset0:0 offset1:1\n"
      "ds_read2_b32 v[174:175], v206 offset0:8 offset1:9\n"
      "ds_read2_b32 v[198:199], v206 offset0:18 offset1:24\n"
      "ds_read2_b32 v[172:173], v206 offset0:16 offset1:17\n"
      "ds_read2_b32 v[196:197], v206 offset0:2 offset1:10\n"
      "v_add_u32 v206, 1568, v206\n"
      "ds_read2_b32 v[152:153], v206 offset0:16 offset1:17\n"
      "ds_read2_b32 v[200:201], v206 offset0:0 offset1:1\n"
      "v_mfma_f32_16x16x32_f16 v[236:239], v[168:171], v[192:195], v[90:93]\n"
      "ds_read2_b32 v[156:157], v206 offset0:8 offset1:9\n"
      "ds_read2_b32 v[202:203], v206 offset0:2 offset1:10\n"
      "ds_read2_b32 v[154:155], v206 offset0:18 offset1:24\n"
      "v_add_u32 v206, 1568, v206\n"
      "v_lshlrev_b32 v164, v164, s31\n"
      "v_cmp_eq_u32 vcc, 0, v207\n"
      "ds_read2_b32 v[214:215], v206 offset0:8 offset1:9\n"
      "ds_read2_b32 v[226:227], v206 offset0:2 offset1:10\n"
      "ds_read2_b32 v[212:213], v206 offset0:0 offset1:1\n"
      "ds_read2_b32 v[224:225], v206 offset0:16 offset1:17\n"
      "ds_read2_b32 v[232:233], v206 offset0:18 offset1:24\n"
      "v_cndmask_b32 v234, 0, v164, vcc\n"
      "s_waitcnt lgkmcnt(12)\n"
      "v_alignbyte_b32 v166, v167, v166, v165\n"
      "v_alignbyte_b32 v174, v175, v174, v165\n"
      "v_mfma_f32_16x16x32_f16 v[236:239], v[34:37], v[38:41], v[236:239]\n"
      "s_waitcnt lgkmcnt(10)\n"
      "v_cmp_eq_u32 vcc, 1, v207\n"
      "v_alignbyte_b32 v175, v197, v175, v165\n"
      "v_alignbyte_b32 v167, v196, v167, v165\n"
      "v_alignbyte_b32 v199, v199, v199, v165\n"
      "v_mfma_f32_16x16x32_f16 v[248:251], v[160:163], v[192:195], v[94:97]\n"
      "s_waitcnt lgkmcnt(8)\n"
      "v_alignbyte_b32 v172, v173, v172, v165\n"
      "v_alignbyte_b32 v173, v198, v173, v165\n"
      "v_mfma_f32_16x16x32_f16 v[248:251], v[40:43], v[38:41], v[248:251]\n"
      "s_waitcnt lgkmcnt(6)\n"
      "v_lshlrev_b32 v199, 16, v199\n"
      "v_cndmask_b32 v235, 0, v164, vcc\n"
      "v_mfma_f32_16x16x32_f16 v[252:255], v[220:223], v[192:195], v[72:75]\n"
      "s_waitcnt lgkmcnt(5)\n"
      "v_fmac_f32_e64 v204, s37, v199\n"
      "v_alignbyte_b32 v200, v201, v200, v165\n"
      "v_mfma_f32_16x16x32_f16 v[252:255], v[228:231], v[160:163], v[252:255]\n"
      "v_alignbyte_b32 v152, v153, v152, v165\n"
      "v_alignbyte_b32 v156, v157, v156, v165\n"
      "v_mfma_f32_16x16x32_f16 v[252:255], v[144:147], v[38:41], v[252:255]\n"
      "s_waitcnt lgkmcnt(3)\n"
      "v_alignbyte_b32 v157, v203, v157, v165\n"
      "s_waitcnt lgkmcnt(2)\n"
      "v_mfma_f32_16x16x32_f16 v[196:199], v[148:151], v[192:195], v[98:101]\n"
      "s_waitcnt lgkmcnt(0)\n"
      "v_alignbyte_b32 v155, v155, v155, v165\n"
      "v_mfma_f32_16x16x32_f16 v[196:199], v[46:49], v[38:41], v[196:199]\n"
      "v_alignbyte_b32 v212, v213, v212, v165\n"
      "v_alignbyte_b32 v233, v233, v233, v165\n"
      "v_mfma_f32_16x16x16_bf16 v[140:143], v[234:235], v[166:167], v[140:143]\n"
      "v_lshlrev_b32 v155, 16, v155\n"
      "v_alignbyte_b32 v213, v226, v213, v165\n"
      "v_mfma_f32_16x16x16_bf16 v[188:191], v[234:235], v[174:175], v[188:191]\n"
      "v_fmac_f32 v252, s37, v155\n"
      "v_alignbyte_b32 v201, v202, v201, v165\n"
      "v_alignbyte_b32 v214, v215, v214, v165\n"
      "v_mfma_f32_16x16x16_bf16 v[136:139], v[234:235], v[172:173], v[136:139]\n"
      "s_nop 0\n"
      "v_max3_f32 v207, v140, v141, v142\n"
      "v_alignbyte_b32 v153, v154, v153, v165\n"
      "v_mfma_f32_16x16x16_bf16 v[236:239], v[234:235], v[200:201], v[236:239]\n"
      "ds_read_b128 v[172:175], v128 offset:3072\n"
      "v_max3_f32 v207, v207, v190, v191\n"
      "v_mfma_f32_16x16x16_bf16 v[196:199], v[234:235], v[152:153], v[196:199]\n"
      "s_nop 0\n"
      "v_max3_f32 v207, v207, v138, v139\n"
      "v_max3_f32 v159, v143, v188, v189\n"
      "v_max3_f32 v159, v159, v136, v137\n"
      "v_max3_f32 v207, v207, v159, v204\n"
      "v_mov_b32 v166, v207\n"
      "v_alignbyte_b32 v215, v227, v215, v165\n"
      "v_alignbyte_b32 v224, v225, v224, v165\n"
      "v_permlane16_swap_b32 v207, v166\n"
      "v_max_f32 v207, v207, v166\n"
      "v_mov_b32 v166, v207\n"
      "v_lshlrev_b32 v233, 16, v233\n"
      "v_alignbyte_b32 v225, v232, v225, v165\n"
      "v_permlane32_swap_b32 v207, v166\n"
      "v_max_f32 v207, v207, v166\n"
      "v_sub_f32 v141, v141, v207\n"
      "v_sub_f32 v204, v204, v207\n"
      "v_sub_f32 v139, v139, v207\n"
      "v_exp_f32 v204, v204\n"
      "v_sub_f32 v190, v190, v207\n"
      "v_sub_f32 v143, v143, v207\n"
      "v_sub_f32 v137, v137, v207\n"
      "v_sub_f32 v191, v191, v207\n"
      "v_exp_f32 v137, v137\n"
      "v_sub_f32 v142, v142, v207\n"
      "v_exp_f32 v143, v143\n"
      "v_mfma_f32_16x16x16_bf16 v[248:251], v[234:235], v[156:157], v[248:251]\n"
      "s_waitcnt lgkmcnt(0)\n"
      "v_exp_f32 v190, v190\n"
      "v_sub_f32 v140, v140, v207\n"
      "v_sub_f32 v189, v189, v207\n"
      "v_exp_f32 v142, v142\n"
      "v_exp_f32 v140, v140\n"
      "v_exp_f32 v141, v141\n"
      "v_sub_f32 v188, v188, v207\n"
      "v_sub_f32 v138, v138, v207\n"
      "v_cvt_pk_bf16_f32 v64, v204, 0\n"
      "v_sub_f32 v136, v136, v207\n"
      "v_max3_f32 v207, v236, v237, v238\n"
      "v_cvt_pk_bf16_f32 v140, v140, v141\n"
      "v_exp_f32 v138, v138\n"
      "v_cvt_pk_bf16_f32 v141, v142, v143\n"
      "v_max3_f32 v204, v239, v248, v249\n"
      "v_max3_f32 v204, v204, v196, v197\n"
      "v_exp_f32 v136, v136\n"
      "v_exp_f32 v191, v191\n"
      "v_max3_f32 v207, v207, v250, v251\n"
      "v_max3_f32 v207, v207, v198, v199\n"
      "v_exp_f32 v139, v139\n"
      "v_exp_f32 v189, v189\n"
      "v_max3_f32 v207, v207, v204, v252\n"
      "v_mov_b32 v204, v207\n"
      "v_bitop3_b32 v174, v174, v175, s4 bitop3:0x7e\n"
      "v_bitop3_b32 v172, v172, v173, s4 bitop3:0x7e\n"
      "v_cvt_pk_bf16_f32 v143, v190, v191\n"
      "v_permlane16_swap_b32 v207, v204\n"
      "v_max_f32 v207, v207, v204\n"
      "v_exp_f32 v188, v188\n"
      "v_mov_b32 v204, v207\n"
      "v_cvt_pk_bf16_f32 v63, v138, v139\n"
      "v_or_b32 v172, v172, v174\n"
      "v_permlane32_swap_b32 v207, v204\n"
      "v_max_f32 v207, v207, v204\n"
      "v_cvt_pk_bf16_f32 v142, v188, v189\n"
      "v_sub_f32 v199, v199, v207\n"
      "v_sub_f32 v252, v252, v207\n"
      "v_sub_f32 v239, v239, v207\n"
      "v_sub_f32 v238, v238, v207\n"
      "v_sub_f32 v236, v236, v207\n"
      "v_sub_f32 v248, v248, v207\n"
      "v_cmp_ne_u32 vcc, 0, v172\n"
      "v_sub_f32 v237, v237, v207\n"
      "v_sub_f32 v250, v250, v207\n"
      "v_mfma_f32_16x16x32_bf16 v[200:203], v[240:243], v[140:143], 0\n"
      "v_exp_f32 v236, v236\n"
      "v_exp_f32 v237, v237\n"
      "v_sub_f32 v249, v249, v207\n"
      "v_exp_f32 v248, v248\n"
      "v_exp_f32 v238, v238\n"
      "v_exp_f32 v252, v252\n"
      "v_sub_f32 v196, v196, v207\n"
      "v_cvt_pk_bf16_f32 v62, v136, v137\n"
      "v_exp_f32 v249, v249\n"
      "v_sub_f32 v251, v251, v207\n"
      "v_mfma_f32_16x16x32_bf16 v[164:167], v[66:69], v[140:143], 0\n"
      "s_mov_b64 s[48:49], vcc\n"
      "v_sub_f32 v197, v197, v207\n"
      "v_cvt_pk_bf16_f32 v236, v236, v237\n"
      "v_mfma_f32_16x16x32_bf16 v[164:167], v[66:69], v[62:65], v[164:167]\n"
      "s_cbranch_vccnz L_flg_160_%=\n"
      "L_fb_161_%=:\n"
      "v_sub_f32 v198, v198, v207\n"
      "v_exp_f32 v239, v239\n"
      "v_mfma_f32_16x16x32_bf16 v[172:175], v[216:219], v[140:143], 0\n"
      "v_exp_f32 v196, v196\n"
      "v_mfma_f32_16x16x32_bf16 v[172:175], v[50:53], v[62:65], v[172:175]\n"
      "v_exp_f32 v199, v199\n"
      "v_exp_f32 v251, v251\n"
      "v_mfma_f32_16x16x32_bf16 v[188:191], v[244:247], v[140:143], 0\n"
      "v_exp_f32_e64 v198, v198\n"
      "v_mfma_f32_16x16x32_bf16 v[188:191], v[54:57], v[62:65], v[188:191]\n"
      "ds_read_b128 v[192:195], v120 offset:18432\n"
      "v_exp_f32 v250, v250\n"
      "v_cvt_pk_bf16_f32 v237, v238, v239\n"
      "v_mfma_f32_16x16x32_bf16 v[200:203], v[58:61], v[62:65], v[200:203]\n"
      "v_exp_f32 v197, v197\n"
      "v_mfma_f32_16x16x32_f16 v[140:143], v[168:171], v[208:211], v[102:105]\n"
      "v_cvt_pk_bf16_f32 v62, v196, v197\n"
      "v_cvt_pk_bf16_f32 v64, v252, 0\n"
      "v_mfma_f32_16x16x32_f16 v[140:143], v[34:37], v[44:47], v[140:143]\n"
      "v_rcp_f32 v164, v164\n"
      "v_mfma_f32_16x16x32_f16 v[168:171], v[160:163], v[208:211], v[106:109]\n"
      "v_mul_f32 v172, v164, v172\n"
      "v_mul_f32 v173, v164, v173\n"
      "v_mfma_f32_16x16x32_f16 v[168:171], v[40:43], v[44:47], v[168:171]\n"
      "v_mul_f32 v202, v164, v202\n"
      "v_mul_f32 v191, v164, v191\n"
      "v_mul_f32 v203, v164, v203\n"
      "v_mfma_f32_16x16x16_bf16 v[140:143], v[234:235], v[212:213], v[140:143]\n"
      "v_cvt_pk_bf16_f32 v172, v172, v173\n"
      "v_mul_f32 v201, v164, v201\n"
      "v_mul_f32 v190, v164, v190\n"
      "v_cvt_pk_bf16_f32 v63, v198, v199\n"
      "v_mfma_f32_16x16x16_bf16 v[168:171], v[234:235], v[214:215], v[168:171]\n"
      "s_waitcnt lgkmcnt(0)\n"
      "v_cvt_pk_bf16_f32 v238, v248, v249\n"
      "v_mul_f32 v189, v164, v189\n"
      "v_mul_f32 v200, v164, v200\n"
      "v_cvt_pk_bf16_f32 v239, v250, v251\n"
      "v_mul_f32 v175, v164, v175\n"
      "v_mfma_f32_16x16x32_f16 v[160:163], v[148:151], v[208:211], v[110:113]\n"
      "v_mul_f32 v174, v164, v174\n"
      "v_mul_f32 v188, v164, v188\n"
      "v_mfma_f32_16x16x32_f16 v[196:199], v[220:223], v[208:211], v[74:77]\n"
      "v_cvt_pk_bf16_f32 v173, v174, v175\n"
      "v_cvt_pk_bf16_f32 v200, v200, v201\n"
      "v_mfma_f32_16x16x32_f16 v[196:199], v[228:231], v[148:151], v[196:199]\n"
      "v_cvt_pk_bf16_f32 v174, v188, v189\n"
      "v_cvt_pk_bf16_f32 v175, v190, v191\n"
      "v_mfma_f32_16x16x32_f16 v[196:199], v[144:147], v[44:47], v[196:199]\n"
      "global_store_dwordx4 v117, v[192:195], s[54:55] sc1\n"
      "v_mfma_f32_16x16x32_f16 v[160:163], v[46:49], v[44:47], v[160:163]\n"
      "ds_write_b128 v124, v[172:175]\n"
      "v_mfma_f32_16x16x32_f16 v[208:211], v[228:231], v[220:223], v[76:79]\n"
      "v_cvt_pk_bf16_f32 v201, v202, v203\n"
      "v_max3_f32 v203, v140, v141, v142\n"
      "v_mfma_f32_16x16x32_bf16 v[192:195], v[66:69], v[236:239], 0\n"
      "v_mfma_f32_16x16x32_bf16 v[192:195], v[66:69], v[62:65], v[192:195]\n"
      "v_fmac_f32_e64 v196, s37, v233\n"
      "v_mfma_f32_16x16x16_bf16 v[160:163], v[234:235], v[224:225], v[160:163]\n"
      "v_max3_f32 v203, v203, v170, v171\n"
      "v_max3_f32 v202, v143, v168, v169\n"
      "v_mfma_f32_16x16x32_bf16 v[188:191], v[216:219], v[236:239], 0\n"
      "ds_read_b128 v[148:151], v121 offset:18432\n"
      "v_mfma_f32_16x16x32_bf16 v[188:191], v[50:53], v[62:65], v[188:191]\n"
      "v_mfma_f32_16x16x16_f16 v[208:211], v[146:147], v[144:145], v[208:211]\n"
      "v_rcp_f32_e64 v192, v192\n"
      "v_mfma_f32_16x16x32_bf16 v[144:147], v[240:243], v[236:239], 0\n"
      "v_max3_f32 v202, v202, v160, v161\n"
      "v_max3_f32 v203, v203, v162, v163\n"
      "v_max3_f32 v203, v203, v202, v196\n"
      "v_mfma_f32_16x16x32_bf16 v[136:139], v[244:247], v[236:239], 0\n"
      "s_nop 0\n"
      "v_mul_f32 v188, v192, v188\n"
      "v_mov_b32 v202, v203\n"
      "v_mfma_f32_16x16x32_bf16 v[144:147], v[58:61], v[62:65], v[144:147]\n"
      "s_nop 0\n"
      "v_permlane16_swap_b32 v203, v202\n"
      "v_max_f32 v203, v203, v202\n"
      "v_mov_b32 v202, v203\n"
      "v_mfma_f32_16x16x32_bf16 v[136:139], v[54:57], v[62:65], v[136:139]\n"
      "s_nop 0\n"
      "v_permlane32_swap_b32 v203, v202\n"
      "v_max_f32 v203, v203, v202\n"
      "v_sub_f32 v171, v171, v203\n"
      "v_sub_f32 v141, v141, v203\n"
      "v_sub_f32 v142, v142, v203\n"
      "v_sub_f32 v160, v160, v203\n"
      "v_sub_f32 v162, v162, v203\n"
      "v_exp_f32 v141, v141\n"
      "v_sub_f32 v143, v143, v203\n"
      "v_mul_f32 v190, v192, v190\n"
      "v_sub_f32 v140, v140, v203\n"
      "v_exp_f32 v140, v140\n"
      "v_exp_f32 v142, v142\n"
      "v_sub_f32 v161, v161, v203\n"
      "v_sub_f32 v170, v170, v203\n"
      "v_sub_f32 v169, v169, v203\n"
      "v_sub_f32 v163, v163, v203\n"
      "v_exp_f32 v160, v160\n"
      "v_sub_f32 v168, v168, v203\n"
      "v_cvt_pk_bf16_f32 v140, v140, v141\n"
      "v_exp_f32 v161, v161\n"
      "v_exp_f32 v162, v162\n"
      "s_waitcnt lgkmcnt(0)\n"
      "v_exp_f32 v170, v170\n"
      "v_mul_f32 v189, v192, v189\n"
      "v_mul_f32 v191, v192, v191\n"
      "v_cvt_pk_bf16_f32 v62, v160, v161\n"
      "v_exp_f32 v143, v143\n"
      "v_mul_f32 v137, v192, v137\n"
      "v_cvt_pk_bf16_f32 v141, v142, v143\n"
      "v_exp_f32 v171, v171\n"
      "v_exp_f32 v169, v169\n"
      "v_exp_f32 v168, v168\n"
      "v_cvt_pk_bf16_f32 v143, v170, v171\n"
      "v_exp_f32_e64 v163, v163\n"
      "v_cvt_pk_bf16_f32 v142, v168, v169\n"
      "v_cvt_pk_bf16_f32 v188, v188, v189\n"
      "v_sub_f32 v196, v196, v203\n"
      "v_exp_f32 v196, v196\n"
      "v_cvt_pk_bf16_f32 v63, v162, v163\n"
      "v_cvt_pk_bf16_f32 v64, v196, 0\n"
      "v_mfma_f32_16x16x32_bf16 v[160:163], v[66:69], v[140:143], 0\n"
      "ds_read_b128 v[152:155], v123 offset:19456\n"
      "v_cvt_pk_bf16_f32 v189, v190, v191\n"
      "v_mfma_f32_16x16x32_bf16 v[160:163], v[66:69], v[62:65], v[160:163]\n"
      "ds_read_b128 v[210:213], v123 offset:22528\n"
      "v_mul_f32 v136, v192, v136\n"
      "v_mfma_f32_16x16x32_bf16 v[164:167], v[216:219], v[140:143], 0\n"
      "v_cvt_pk_bf16_f32 v190, v136, v137\n"
      "v_mul_f32 v138, v192, v138\n"
      "v_mfma_f32_16x16x32_bf16 v[164:167], v[50:53], v[62:65], v[164:167]\n"
      "s_mov_b32 exec_hi, 0\n"
      "global_store_dwordx4 v118, v[148:151], s[54:55] sc1\n"
      "s_mov_b32 exec_hi, -1\n"
      "v_mfma_f32_16x16x32_bf16 v[172:175], v[244:247], v[140:143], 0\n"
      "s_add_u32 s28, s27, 2\n"
      "v_mul_f32 v139, v192, v139\n"
      "v_cvt_pk_bf16_f32 v191, v138, v139\n"
      "v_mfma_f32_16x16x32_bf16 v[172:175], v[54:57], v[62:65], v[172:175]\n"
      "ds_write_b128 v124, v[188:191] offset:1024\n"
      "v_mfma_f32_16x16x32_bf16 v[168:171], v[240:243], v[140:143], 0\n"
      "ds_read_b128 v[136:139], v123 offset:25600\n"
      "v_mul_f32 v146, v192, v146\n"
      "v_mfma_f32_16x16x32_bf16 v[168:171], v[58:61], v[62:65], v[168:171]\n"
      "ds_read_b128 v[140:143], v123 offset:28672\n"
      "v_mul_f32 v144, v192, v144\n"
      "s_waitcnt lgkmcnt(3)\n"
      "v_mul_f32 v145, v192, v145\n"
      "v_mfma_f32_16x16x32_bf16 v[188:191], a[216:219], v[152:155], 0\n"
      "ds_read_b128 v[148:151], v123 offset:31744\n"
      "v_cvt_pk_bf16_f32 v144, v144, v145\n"
      "v_mfma_f32_16x16x32_bf16 v[188:191], a[228:231], v[210:213], v[188:191]\n"
      "s_min_u32 m0, s28, s3\n"
      "v_mul_f32_e64 v147, v192, v147\n"
      "v_cvt_pk_bf16_f32 v145, v146, v147\n"
      "v_mfma_f32_16x16x32_bf16 v[192:195], a[220:223], v[152:155], 0\n"
      "s_movrels_b32 s9, s80\n"
      "v_cndmask_b32 v205, v205, v253, s[42:43]\n"
      "v_cndmask_b32 v205, v205, v197, s[44:45]\n"
      "v_mfma_f32_16x16x32_bf16 v[192:195], a[232:235], v[210:213], v[192:195]\n"
      "ds_write2st64_b64 v125, v[200:201], v[144:145] offset0:0 offset1:2\n"
      "v_mfma_f32_16x16x32_bf16 v[196:199], a[224:227], v[152:155], 0\n"
      "ds_read_b128 v[200:203], v123 offset:34816\n"
      "v_cndmask_b32 v205, v205, v209, s[46:47]\n"
      "v_mfma_f32_16x16x32_bf16 v[196:199], a[236:239], v[210:213], v[196:199]\n"
      "ds_read_b128 v[144:147], v123 offset:20480\n"
      "v_mbcnt_lo_u32_b32 v206, -1, 0\n"
      "v_mbcnt_hi_u32_b32 v206, -1, v206\n"
      "s_waitcnt lgkmcnt(4)\n"
      "v_mfma_f32_16x16x32_bf16 v[188:191], a[240:243], v[136:139], v[188:191]\n"
      "ds_read_b128 v[152:155], v123 offset:23552\n"
      "v_min_u32 v206, 48, v206\n"
      "v_mfma_f32_16x16x32_bf16 v[188:191], a[252:255], v[140:143], v[188:191]\n"
      "s_lshr_b32 s8, s9, 8\n"
      "v_lshlrev_b32 v206, 1, v206\n"
      "v_add_u32 v206, s30, v206\n"
      "v_mfma_f32_16x16x32_bf16 v[192:195], a[244:247], v[136:139], v[192:195]\n"
      "ds_read_u16 v207, v206 offset:4704\n"
      "v_mfma_f32_16x16x32_bf16 v[192:195], v[0:3], v[140:143], v[192:195]\n"
      "s_and_b32 s9, s9, 0xff\n"
      "s_mul_i32 s10, s8, 73\n"
      "s_add_u32 s10, s10, s9\n"
      "v_mfma_f32_16x16x32_bf16 v[196:199], a[248:251], v[136:139], v[196:199]\n"
      "s_mul_i32 s28, s10, 4802\n"
      "ds_read_b128 v[212:215], v123 offset:26624\n"
      "v_mfma_f32_16x16x32_bf16 v[196:199], v[4:7], v[140:143], v[196:199]\n"
      "s_add_u32 s56, s18, s28\n"
      "s_addc_u32 s57, s19, 0\n"
      "s_and_b32 s56, s56, -16\n"
      "s_waitcnt lgkmcnt(4)\n"
      "v_mfma_f32_16x16x32_bf16 v[188:191], v[8:11], v[148:151], v[188:191]\n"
      "s_sub_u32 s58, s56, 110592\n"
      "s_subb_u32 s59, s57, 0\n"
      "s_sub_u32 s56, s56, 73728\n"
      "v_mfma_f32_16x16x32_bf16 v[188:191], v[20:23], v[200:203], v[188:191]\n"
      "s_subb_u32 s57, s57, 0\n"
      "s_mul_i32 s28, s8, 1373568\n"
      "s_mul_i32 s29, s9, 2688\n"
      "v_mfma_f32_16x16x32_bf16 v[192:195], v[12:15], v[148:151], v[192:195]\n"
      "s_nop 0\n"
      "v_mfma_f32_16x16x32_bf16 v[192:195], v[24:27], v[200:203], v[192:195]\n"
      "s_add_u32 s28, s28, s29\n"
      "s_cmp_eq_u32 s8, 72\n"
      "s_cselect_b32 s8, 0, 100270464\n"
      "v_mfma_f32_16x16x32_bf16 v[196:199], v[16:19], v[148:151], v[196:199]\n"
      "s_waitcnt lgkmcnt(1)\n"
      "v_lshlrev_b32 v207, 16, v207\n"
      "v_fmac_f32_e64 v205, s37, v207\n"
      "v_mfma_f32_16x16x32_bf16 v[196:199], v[28:31], v[200:203], v[196:199]\n"
      "s_nop 0\n"
      "v_max_f32_dpp v207, v205, v205 row_ror:8 row_mask:0xf bank_mask:0xf\n"
      "s_cmp_eq_u32 s9, 72\n"
      "v_mfma_f32_16x16x32_bf16 v[200:203], a[216:219], v[144:147], 0\n"
      "s_cselect_b32 s9, 0, 196224\n"
      "v_max_f32_dpp v207, v207, v207 row_ror:4 row_mask:0xf bank_mask:0xf\n"
      "s_add_u32 s10, s8, s9\n"
      "v_mfma_f32_16x16x32_bf16 v[200:203], a[228:231], v[152:155], v[200:203]\n"
      "s_add_u32 s28, s28, s10\n"
      "s_add_u32 s52, s16, s28\n"
      "v_max_f32_dpp v207, v207, v207 row_ror:2 row_mask:0xf bank_mask:0xf\n"
      "v_mfma_f32_16x16x32_bf16 v[208:211], a[224:227], v[144:147], 0\n"
      "ds_read_b128 v[148:151], v123 offset:29696\n"
      "v_max_f32_dpp v207, v207, v207 row_ror:1 row_mask:0xf bank_mask:0xf\n"
      "v_mfma_f32_16x16x32_bf16 v[208:211], a[236:239], v[152:155], v[208:211]\n"
      "ds_read_b128 v[236:239], v123 offset:32768\n"
      "v_max_f32_dpp v207, v207, v207 row_bcast:15 row_mask:0xa bank_mask:0xf\n"
      "v_fma_f32 v206, v188, v188, s39\n"
      "v_fmac_f32 v206, v189, v189\n"
      "v_max_f32_dpp v207, v207, v207 row_bcast:31 row_mask:0xc bank_mask:0xf\n"
      "v_fmac_f32 v206, v190, v190\n"
      "v_readlane_b32 s31, v207, 63\n"
      "v_fmac_f32 v206, v191, v191\n"
      "v_fmac_f32 v206, v192, v192\n"
      "v_subrev_f32 v205, s31, v205\n"
      "v_fmac_f32 v206, v193, v193\n"
      "v_fmac_f32 v206, v194, v194\n"
      "v_fmac_f32 v206, v195, v195\n"
      "v_fmac_f32 v206, v196, v196\n"
      "v_fmac_f32 v206, v197, v197\n"
      "v_exp_f32 v205, v205\n"
      "v_fmac_f32 v206, v198, v198\n"
      "v_cvt_pk_bf16_f32 v205, v205, v205\n"
      "v_fmac_f32 v206, v199, v199\n"
      "s_addc_u32 s53, s17, 0\n"
      "ds_write_b16 v130, v205\n"
      "ds_read_b128 v[220:223], v131 offset:64\n"
      "v_permlane16_swap_b32 v158, v206\n"
      "v_add_f32 v158, v158, v206\n"
      "ds_read_b128 v[228:231], v131\n"
      "v_mfma_f32_16x16x32_bf16 v[204:207], a[220:223], v[144:147], 0\n"
      "s_nop 0\n"
      "v_mfma_f32_16x16x32_bf16 v[204:207], a[232:235], v[152:155], v[204:207]\n"
      "s_waitcnt lgkmcnt(3)\n"
      "ds_read_b128 v[144:147], v123 offset:35840\n"
      "v_mfma_f32_16x16x32_bf16 v[204:207], a[244:247], v[212:215], v[204:207]\n"
      "ds_read_b128 v[136:139], v114 offset:18432\n"
      "v_mfma_f32_16x16x32_bf16 v[200:203], a[240:243], v[212:215], v[200:203]\n"
      "ds_read_b128 v[140:143], v115 offset:18432\n"
      "v_mfma_f32_16x16x32_bf16 v[200:203], a[252:255], v[148:151], v[200:203]\n"
      "v_mfma_f32_16x16x32_bf16 v[204:207], v[0:3], v[148:151], v[204:207]\n"
      "s_add_u32 s27, s27, 1\n"
      "v_mfma_f32_16x16x32_bf16 v[208:211], a[248:251], v[212:215], v[208:211]\n"
      "s_cmp_ge_u32 s27, s26\n"
      "v_mfma_f32_16x16x32_bf16 v[208:211], v[4:7], v[148:151], v[208:211]\n"
      "s_waitcnt lgkmcnt(3)\n"
      "v_mfma_f32_16x16x32_bf16 v[212:215], v[228:231], v[66:69], 0\n"
      "s_cselect_b32 s28, 1, 0\n"
      "v_mfma_f32_16x16x32_bf16 v[212:215], v[220:223], v[66:69], v[212:215]\n"
      "s_waitcnt lgkmcnt(2)\n"
      "v_mfma_f32_16x16x32_bf16 v[200:203], v[8:11], v[236:239], v[200:203]\n"
      "v_mfma_f32_16x16x32_bf16 v[200:203], v[20:23], v[144:147], v[200:203]\n"
      "s_xor_b32 s29, s10, s11\n"
      "s_waitcnt lgkmcnt(0)\n"
      "s_or_b32 s28, s28, s29\n"
      "v_mfma_f32_16x16x32_bf16 v[208:211], v[16:19], v[236:239], v[208:211]\n"
      "s_nop 0\n"
      "v_mov_b32_dpp v212, v212 quad_perm:[0,0,0,0] row_mask:0xf bank_mask:0xf\n"
      "v_mfma_f32_16x16x32_bf16 v[208:211], v[28:31], v[144:147], v[208:211]\n"
      "v_rcp_f32 v212, v212\n"
      "v_mfma_f32_16x16x32_bf16 v[204:207], v[12:15], v[236:239], v[204:207]\n"
      "v_mfma_f32_16x16x32_bf16 v[204:207], v[24:27], v[144:147], v[204:207]\n"
      "v_fma_f32 v156, v200, v200, s39\n"
      "v_fmac_f32_e64 v156, v201, v201\n"
      "v_mfma_f32_16x16x32_bf16 v[236:239], v[228:231], v[216:219], 0\n"
      "v_fmac_f32 v156, v202, v202\n"
      "v_fmac_f32 v156, v203, v203\n"
      "v_mfma_f32_16x16x32_bf16 v[236:239], v[220:223], v[50:53], v[236:239]\n"
      "s_nop 1\n"
      "v_fmac_f32 v156, v204, v204\n"
      "v_mfma_f32_16x16x32_bf16 v[152:155], v[228:231], v[244:247], 0\n"
      "v_fmac_f32 v156, v205, v205\n"
      "v_fmac_f32 v156, v206, v206\n"
      "v_mfma_f32_16x16x32_bf16 v[152:155], v[220:223], v[54:57], v[152:155]\n"
      "v_fmac_f32 v156, v207, v207\n"
      "v_fmac_f32 v156, v208, v208\n"
      "v_mfma_f32_16x16x32_bf16 v[148:151], v[228:231], v[240:243], 0\n"
      "v_fmac_f32 v156, v209, v209\n"
      "v_fmac_f32 v156, v210, v210\n"
      "v_mfma_f32_16x16x32_bf16 v[148:151], v[220:223], v[58:61], v[148:151]\n"
      "v_fmac_f32 v156, v211, v211\n"
      "v_mov_b32 v157, v156\n"
      "v_mfma_f32_16x16x32_bf16 v[144:147], a[0:3], v[136:139], 0\n"
      "v_cndmask_b32 v236, v236, v152, s[42:43]\n"
      "v_mfma_f32_16x16x32_bf16 v[144:147], a[36:39], v[140:143], v[144:147]\n"
      "v_permlane16_swap_b32_e64 v156, v157\n"
      "v_mfma_f32_16x16x32_bf16 v[152:155], a[8:11], v[136:139], 0\n"
      "v_add_f32 v156, v156, v157\n"
      "v_cndmask_b32 v236, v236, v148, s[44:45]\n"
      "v_mfma_f32_16x16x32_bf16 v[152:155], a[44:47], v[140:143], v[152:155]\n"
      "v_permlane32_swap_b32 v158, v156\n"
      "v_mfma_f32_16x16x32_bf16 v[148:151], a[4:7], v[136:139], 0\n"
      "v_mul_f32_e64 v236, v212, v236\n"
      "v_add_f32_e64 v158, v158, v156\n"
      "v_mfma_f32_16x16x32_bf16 v[148:151], a[40:43], v[140:143], v[148:151]\n"
      "ds_write_b32 v129, v158\n"
      "v_mfma_f32_16x16x32_bf16 v[156:159], a[12:15], v[136:139], 0\n"
      "v_cvt_pk_bf16_f32 v236, v236, v236\n"
      "v_mfma_f32_16x16x32_bf16 v[156:159], a[48:51], v[140:143], v[156:159]\n"
      "ds_write_b16 v135, v236\n"
      "s_branch L_join_30_%=\n"
      "L_flg_160_%=:\n"
      "v_cmp_gt_i32 vcc, 0, v172\n"
      "s_cbranch_vccz L_fb_161_%=\n"
      "s_sleep 1\n"
      "ds_read_b128 v[172:175], v128 offset:3072\n"
      "s_waitcnt lgkmcnt(0)\n"
      "v_bitop3_b32 v172, v172, v173, s4 bitop3:0x7e\n"
      "v_bitop3_b32 v174, v174, v175, s4 bitop3:0x7e\n"
      "v_or_b32 v172, v172, v174\n"
      "v_cmp_ne_u32 s[48:49], 0, v172\n"
      "s_branch L_flg_160_%=\n"
      "L_edge_1_%=:\n"
      "s_mov_b32 s11, s10\n"
      "s_sub_u32 s28, s60, s8\n"
      "s_sub_u32 s29, s61, s9\n"
      "s_mov_b32 s60, s8\n"
      "s_mov_b32 s61, s9\n"
      "s_mov_b64 exec, s[64:65]\n"
      "v_add_u32 v116, s28, v116\n"
      "s_mov_b64 exec, s[70:71]\n"
      "v_add_u32 v116, s29, v116\n"
      "s_mov_b64 exec, s[66:67]\n"
      "v_add_u32 v117, s28, v117\n"
      "s_mov_b64 exec, s[72:73]\n"
      "v_add_u32 v117, s29, v117\n"
      "s_mov_b64 exec, s[68:69]\n"
      "v_add_u32 v118, s28, v118\n"
      "s_mov_b64 exec, s[74:75]\n"
      "v_add_u32 v118, s29, v118\n"
      "s_mov_b64 exec, -1\n"
      "s_nop 4\n"
      "s_branch L_eb_2_%=\n"
      "L_flg_3_%=:\n"
      "v_cmp_gt_i32 vcc, 0, v148\n"
      "s_cbranch_vccz L_fb_4_%=\n"
      "s_sleep 1\n"
      "ds_read_b128 v[148:151], v128 offset:2048\n"
      "s_waitcnt lgkmcnt(0)\n"
      "v_bitop3_b32 v148, v148, v149, s4 bitop3:0x7e\n"
      "v_bitop3_b32 v150, v150, v151, s4 bitop3:0x7e\n"
      "v_or_b32 v148, v148, v150\n"
      "v_cmp_ne_u32 s[48:49], 0, v148\n"
      "s_branch L_flg_3_%=\n"
      "L_edge_6_%=:\n"
      "s_mov_b32 s11, s10\n"
      "s_sub_u32 s28, s60, s8\n"
      "s_sub_u32 s29, s61, s9\n"
      "s_mov_b32 s60, s8\n"
      "s_mov_b32 s61, s9\n"
      "s_mov_b64 exec, s[64:65]\n"
      "v_add_u32 v116, s28, v116\n"
      "s_mov_b64 exec, s[70:71]\n"
      "v_add_u32 v116, s29, v116\n"
      "s_mov_b64 exec, s[66:67]\n"
      "v_add_u32 v117, s28, v117\n"
      "s_mov_b64 exec, s[72:73]\n"
      "v_add_u32 v117, s29, v117\n"
      "s_mov_b64 exec, s[68:69]\n"
      "v_add_u32 v118, s28, v118\n"
      "s_mov_b64 exec, s[74:75]\n"
      "v_add_u32_e64 v118, s29, v118\n"
      "s_mov_b64 exec, -1\n"
      "s_nop 4\n"
      "s_branch L_eb_7_%=\n"
      "L_flg_12_%=:\n"
      "v_cmp_gt_i32 vcc, 0, v136\n"
      "s_cbranch_vccz L_fb_13_%=\n"
      "s_sleep 1\n"
      "ds_read_b128 v[136:139], v128 offset:3072\n"
      "s_waitcnt lgkmcnt(0)\n"
      "v_bitop3_b32 v136, v136, v137, s4 bitop3:0x7e\n"
      "v_bitop3_b32 v138, v138, v139, s4 bitop3:0x7e\n"
      "v_or_b32 v136, v136, v138\n"
      "v_cmp_ne_u32 s[48:49], 0, v136\n"
      "s_branch L_flg_12_%=\n"
      "L_edge_15_%=:\n"
      "s_mov_b32 s11, s10\n"
      "s_sub_u32 s28, s60, s8\n"
      "s_sub_u32 s29, s61, s9\n"
      "s_mov_b32 s60, s8\n"
      "s_mov_b32 s61, s9\n"
      "s_mov_b64 exec, s[64:65]\n"
      "v_add_u32 v116, s28, v116\n"
      "s_mov_b64 exec, s[70:71]\n"
      "v_add_u32 v116, s29, v116\n"
      "s_mov_b64 exec, s[66:67]\n"
      "v_add_u32 v117, s28, v117\n"
      "s_mov_b64 exec, s[72:73]\n"
      "v_add_u32 v117, s29, v117\n"
      "s_mov_b64 exec, s[68:69]\n"
      "v_add_u32 v118, s28, v118\n"
      "s_mov_b64 exec, s[74:75]\n"
      "v_add_u32 v118, s29, v118\n"
      "s_mov_b64 exec, -1\n"
      "s_nop 4\n"
      "s_branch L_eb_16_%=\n"
      "L_edge_17_%=:\n"
      "s_mov_b32 s11, s10\n"
      "s_sub_u32 s28, s60, s8\n"
      "s_sub_u32 s29, s61, s9\n"
      "s_mov_b32 s60, s8\n"
      "s_mov_b32 s61, s9\n"
      "s_mov_b64 exec, s[64:65]\n"
      "v_add_u32 v116, s28, v116\n"
      "s_mov_b64 exec, s[70:71]\n"
      "v_add_u32 v116, s29, v116\n"
      "s_mov_b64 exec, s[66:67]\n"
      "v_add_u32 v117, s28, v117\n"
      "s_mov_b64 exec, s[72:73]\n"
      "v_add_u32 v117, s29, v117\n"
      "s_mov_b64 exec, s[68:69]\n"
      "v_add_u32 v118, s28, v118\n"
      "s_mov_b64 exec, s[74:75]\n"
      "v_add_u32 v118, s29, v118\n"
      "s_mov_b64 exec, -1\n"
      "s_nop 4\n"
      "s_branch L_eb_18_%=\n"
      "L_flg_23_%=:\n"
      "v_cmp_gt_i32 vcc, 0, v248\n"
      "s_cbranch_vccz L_fb_24_%=\n"
      "s_sleep 1\n"
      "ds_read_b128 v[248:251], v128 offset:2048\n"
      "s_waitcnt lgkmcnt(0)\n"
      "v_bitop3_b32 v248, v248, v249, s4 bitop3:0x7e\n"
      "v_bitop3_b32 v250, v250, v251, s4 bitop3:0x7e\n"
      "v_or_b32 v248, v248, v250\n"
      "v_cmp_ne_u32 s[48:49], 0, v248\n"
      "s_branch L_flg_23_%=\n"
      "L_edge_26_%=:\n"
      "s_mov_b32 s11, s10\n"
      "s_sub_u32 s28, s60, s8\n"
      "s_sub_u32 s29, s61, s9\n"
      "s_mov_b32 s60, s8\n"
      "s_mov_b32 s61, s9\n"
      "s_mov_b64 exec, s[64:65]\n"
      "v_add_u32 v116, s28, v116\n"
      "s_mov_b64 exec, s[70:71]\n"
      "v_add_u32 v116, s29, v116\n"
      "s_mov_b64 exec, s[66:67]\n"
      "v_add_u32 v117, s28, v117\n"
      "s_mov_b64 exec, s[72:73]\n"
      "v_add_u32 v117, s29, v117\n"
      "s_mov_b64 exec, s[68:69]\n"
      "v_add_u32 v118, s28, v118\n"
      "s_mov_b64 exec, s[74:75]\n"
      "v_add_u32 v118, s29, v118\n"
      "s_mov_b64 exec, -1\n"
      "s_nop 4\n"
      "s_branch L_eb_27_%=\n"
      "L_flg_32_%=:\n"
      "v_cmp_gt_i32 vcc, 0, v232\n"
      "s_cbranch_vccz L_fb_33_%=\n"
      "s_sleep 1\n"
      "ds_read_b128 v[232:235], v128 offset:3072\n"
      "s_waitcnt lgkmcnt(0)\n"
      "v_bitop3_b32 v232, v232, v233, s4 bitop3:0x7e\n"
      "v_bitop3_b32 v234, v234, v235, s4 bitop3:0x7e\n"
      "v_or_b32 v232, v232, v234\n"
      "v_cmp_ne_u32 s[48:49], 0, v232\n"
      "s_branch L_flg_32_%=\n"
      "L_edge_36_%=:\n"
      "s_mov_b32 s11, s10\n"
      "s_sub_u32 s28, s60, s8\n"
      "s_sub_u32 s29, s61, s9\n"
      "s_mov_b32 s60, s8\n"
      "s_mov_b32 s61, s9\n"
      "s_mov_b64 exec, s[64:65]\n"
      "v_add_u32 v116, s28, v116\n"
      "s_mov_b64 exec, s[70:71]\n"
      "v_add_u32 v116, s29, v116\n"
      "s_mov_b64 exec, s[66:67]\n"
      "v_add_u32 v117, s28, v117\n"
      "s_mov_b64 exec, s[72:73]\n"
      "v_add_u32 v117, s29, v117\n"
      "s_mov_b64 exec, s[68:69]\n"
      "v_add_u32 v118, s28, v118\n"
      "s_mov_b64 exec, s[74:75]\n"
      "v_add_u32 v118, s29, v118\n"
      "s_mov_b64 exec, -1\n"
      "s_nop 4\n"
      "s_branch L_eb_37_%=\n"
      "L_edge_38_%=:\n"
      "s_mov_b32 s11, s10\n"
      "s_sub_u32 s28, s60, s8\n"
      "s_sub_u32 s29, s61, s9\n"
      "s_mov_b32 s60, s8\n"
      "s_mov_b32 s61, s9\n"
      "s_mov_b64 exec, s[64:65]\n"
      "v_add_u32 v116, s28, v116\n"
      "s_mov_b64 exec, s[70:71]\n"
      "v_add_u32 v116, s29, v116\n"
      "s_mov_b64 exec, s[66:67]\n"
      "v_add_u32 v117, s28, v117\n"
      "s_mov_b64 exec, s[72:73]\n"
      "v_add_u32 v117, s29, v117\n"
      "s_mov_b64 exec, s[68:69]\n"
      "v_add_u32 v118, s28, v118\n"
      "s_mov_b64 exec, s[74:75]\n"
      "v_add_u32 v118, s29, v118\n"
      "s_mov_b64 exec, -1\n"
      "s_nop 4\n"
      "s_branch L_eb_39_%=\n"
      "L_edge_40_%=:\n"
      "s_mov_b32 s11, s10\n"
      "s_sub_u32 s28, s60, s8\n"
      "s_sub_u32 s29, s61, s9\n"
      "s_mov_b32 s60, s8\n"
      "s_mov_b32 s61, s9\n"
      "s_mov_b64 exec, s[64:65]\n"
      "v_add_u32 v116, s28, v116\n"
      "s_mov_b64 exec, s[70:71]\n"
      "v_add_u32 v116, s29, v116\n"
      "s_mov_b64 exec, s[66:67]\n"
      "v_add_u32 v117, s28, v117\n"
      "s_mov_b64 exec, s[72:73]\n"
      "v_add_u32 v117, s29, v117\n"
      "s_mov_b64 exec, s[68:69]\n"
      "v_add_u32 v118, s28, v118\n"
      "s_mov_b64 exec, s[74:75]\n"
      "v_add_u32 v118, s29, v118\n"
      "s_mov_b64 exec, -1\n"
      "s_nop 4\n"
      "s_branch L_eb_41_%=\n"
      "L_edge_44_%=:\n"
      "s_mov_b32 s11, s10\n"
      "s_sub_u32 s28, s60, s8\n"
      "s_sub_u32 s29, s61, s9\n"
      "s_mov_b32 s60, s8\n"
      "s_mov_b32 s61, s9\n"
      "s_mov_b64 exec, s[64:65]\n"
      "v_add_u32 v116, s28, v116\n"
      "s_mov_b64 exec, s[70:71]\n"
      "v_add_u32 v116, s29, v116\n"
      "s_mov_b64 exec, s[66:67]\n"
      "v_add_u32 v117, s28, v117\n"
      "s_mov_b64 exec, s[72:73]\n"
      "v_add_u32 v117, s29, v117\n"
      "s_mov_b64 exec, s[68:69]\n"
      "v_add_u32 v118, s28, v118\n"
      "s_mov_b64 exec, s[74:75]\n"
      "v_add_u32 v118, s29, v118\n"
      "s_mov_b64 exec, -1\n"
      "s_nop 4\n"
      "s_branch L_eb_45_%=\n"
      "L_edge_46_%=:\n"
      "s_mov_b32 s11, s10\n"
      "s_sub_u32 s28, s60, s8\n"
      "s_sub_u32 s29, s61, s9\n"
      "s_mov_b32 s60, s8\n"
      "s_mov_b32 s61, s9\n"
      "s_mov_b64 exec, s[64:65]\n"
      "v_add_u32 v116, s28, v116\n"
      "s_mov_b64 exec, s[70:71]\n"
      "v_add_u32 v116, s29, v116\n"
      "s_mov_b64 exec, s[66:67]\n"
      "v_add_u32 v117, s28, v117\n"
      "s_mov_b64 exec, s[72:73]\n"
      "v_add_u32 v117, s29, v117\n"
      "s_mov_b64 exec, s[68:69]\n"
      "v_add_u32 v118, s28, v118\n"
      "s_mov_b64 exec, s[74:75]\n"
      "v_add_u32 v118, s29, v118\n"
      "s_mov_b64 exec, -1\n"
      "s_nop 4\n"
      "s_branch L_eb_47_%=\n"
      "L_edge_48_%=:\n"
      "s_mov_b32 s11, s10\n"
      "s_sub_u32 s28, s60, s8\n"
      "s_sub_u32 s29, s61, s9\n"
      "s_mov_b32 s60, s8\n"
      "s_mov_b32 s61, s9\n"
      "s_mov_b64 exec, s[64:65]\n"
      "v_add_u32 v116, s28, v116\n"
      "s_mov_b64 exec, s[70:71]\n"
      "v_add_u32 v116, s29, v116\n"
      "s_mov_b64 exec, s[66:67]\n"
      "v_add_u32 v117, s28, v117\n"
      "s_mov_b64 exec, s[72:73]\n"
      "v_add_u32 v117, s29, v117\n"
      "s_mov_b64 exec, s[68:69]\n"
      "v_add_u32 v118, s28, v118\n"
      "s_mov_b64 exec, s[74:75]\n"
      "v_add_u32 v118, s29, v118\n"
      "s_mov_b64 exec, -1\n"
      "s_nop 4\n"
      "s_branch L_eb_49_%=\n"
      "L_cend_%=:\n"
      :
      : "s"(__builtin_amdgcn_kernarg_segment_ptr()), "s"(wave), "s"(blockIdx.x), "s"(CT_TAB), "s"(lds0)
      : "v0","v1","v2","v3","v4","v5","v6","v7","v8","v9","v10","v11","v12","v13","v14","v15","v16","v17","v18","v19","v20","v21","v22","v23","v24","v25","v26","v27","v28","v29","v30","v31","v32","v33","v34","v35","v36","v37","v38","v39","v40","v41","v42","v43","v44","v45","v46","v47","v48","v49","v50","v51","v52","v53","v54","v55","v56","v57","v58","v59","v60","v61","v62","v63","v64","v65","v66","v67","v68","v69","v70","v71","v72","v73","v74","v75","v76","v77","v78","v79","v80","v81","v82","v83","v84","v85","v86","v87","v88","v89","v90","v91","v92","v93","v94","v95","v96","v97","v98","v99","v100","v101","v102","v103","v104","v105","v106","v107","v108","v109","v110","v111","v112","v113","v114","v115","v116","v117","v118","v119","v120","v121","v122","v123","v124","v125","v126","v127","v128","v129","v130","v131","v132","v133","v134","v135","v136","v137","v138","v139","v140","v141","v142","v143","v144","v145","v146","v147","v148","v149","v150","v151","v152","v153","v154","v155","v156","v157","v158","v159","v160","v161","v162","v163","v164","v165","v166","v167","v168","v169","v170","v171","v172","v173","v174","v175","v176","v177","v178","v179","v180","v181","v182","v183","v184","v185","v186","v187","v188","v189","v190","v191","v192","v193","v194","v195","v196","v197","v198","v199","v200","v201","v202","v203","v204","v205","v206","v207","v208","v209","v210","v211","v212","v213","v214","v215","v216","v217","v218","v219","v220","v221","v222","v223","v224","v225","v226","v227","v228","v229","v230","v231","v232","v233","v234","v235","v236","v237","v238","v239","v240","v241","v242","v243","v244","v245","v246","v247","v248","v249","v250","v251","v252","v253","v254","v255","a0","a1","a2","a3","a4","a5","a6","a7","a8","a9","a10","a11","a12","a13","a14","a15","a16","a17","a18","a19","a20","a21","a22","a23","a24","a25","a26","a27","a28","a29","a30","a31","a32","a33","a34","a35","a36","a37","a38","a39","a40","a41","a42","a43","a44","a45","a46","a47","a48","a49","a50","a51","a52","a53","a54","a55","a56","a57","a58","a59","a60","a61","a62","a63","a64","a65","a66","a67","a68","a69","a70","a71","a72","a73","a74","a75","a76","a77","a78","a79","a80","a81","a82","a83","a84","a85","a86","a87","a88","a89","a90","a91","a92","a93","a94","a95","a96","a97","a98","a99","a100","a101","a102","a103","a104","a105","a106","a107","a108","a109","a110","a111","a112","a113","a114","a115","a116","a117","a118","a119","a120","a121","a122","a123","a124","a125","a126","a127","a128","a129","a130","a131","a132","a133","a134","a135","a136","a137","a138","a139","a140","a141","a142","a143","a144","a145","a146","a147","a148","a149","a150","a151","a152","a153","a154","a155","a156","a157","a158","a159","a160","a161","a162","a163","a164","a165","a166","a167","a168","a169","a170","a171","a172","a173","a174","a175","a176","a177","a178","a179","a180","a181","a182","a183","a184","a185","a186","a187","a188","a189","a190","a191","a192","a193","a194","a195","a196","a197","a198","a199","a200","a201","a202","a203","a204","a205","a206","a207","a208","a209","a210","a211","a212","a213","a214","a215","a216","a217","a218","a219","a220","a221","a222","a223","a224","a225","a226","a227","a228","a229","a230","a231","a232","a233","a234","a235","a236","a237","a238","a239","a240","a241","a242","a243","a244","a245","a246","a247","a248","a249","a250","a251","a252","a253","a254","a255","s2","s3","s4","s5","s6","s7","s8","s9","s10","s11","s12","s13","s16","s17","s18","s19","s20","s21","s22","s23","s24","s25","s26","s27","s28","s29","s30","s31","s34","s35","s36","s37","s38","s39","s40","s41","s42","s43","s44","s45","s46","s47","s48","s49","s50","s51","s52","s53","s54","s55","s60","s61","s62","s63","s64","s65","s66","s67","s68","s69","s70","s71","s72","s73","s74","s75","s76","s77","s78","s79","s80","s81","s82","s83","s84","s85","s86","s87","s88","s89","s90","s91","s92","s93","s94","s95","s96","s97","s98","s99","s100","s101", "vcc", "scc", "memory");
}

}

extern "C" void kernel_launch(void* const* d_in, const int* in_sizes, int n_in, void* d_out, int out_size, void* d_ws,
                              size_t ws_size, hipStream_t stream) {
  const uint16_t* x = (const uint16_t*)d_in[0];
  const uint16_t* wqkv = (const uint16_t*)d_in[1];
  const uint16_t* wo = (const uint16_t*)d_in[2];
  const uint16_t* ls = (const uint16_t*)d_in[3];
  const uint16_t* w1 = (const uint16_t*)d_in[4];
  const uint16_t* w2 = (const uint16_t*)d_in[5];
  const uint16_t* rc = (const uint16_t*)d_in[6];
  const int* ridx = (const int*)d_in[7];
  const uint16_t* mask = (const uint16_t*)d_in[8];
  swin::swin_main<<<256, 256, 0, stream>>>(x, mask, (char*)d_ws, (uint16_t*)d_out, wqkv, wo, ls, w1, w2, rc, ridx);
}
```

```cpp
#include <hip/hip_runtime.h>
#include <hip/hip_bf16.h>
#include <stdint.h>

namespace swin {

constexpr unsigned LDS_BYTES = 163840u;

__device__ __attribute__((aligned(256))) const unsigned CT_TAB[26704] = {0,64,0,0xc0000,144,208,16,0xc0010,288,352,32,0xc0020,432,496,48,0xc0030,576,512,64,0xc0040,720,656,80,0xc0050,864,800,96,0xc0060,1008,944,112,0xc0070,1024,1088,400,0xc0190,1168,1232,384,0xc0180,1312,1376,432,0xc01b0,1456,1520,416,0xc01a0,1600,1536,464,0xc01d0,1744,1680,448,0xc01c0,1888,1824,496,0xc01f0,2032,1968,480,0xc01e0,16,80,800,0xc0320,128,192,816,0xc0330,304,368,768,0xc0300,416,480,784,0xc0310,592,528,864,0xc0360,704,640,880,0xc0370,880,816,832,0xc0340,992,928,848,0xc0350,1040,1104,1200,0xef8b0,1152,1216,1184,0xef8a0,1328,1392,1168,0xef890,1440,1504,1152,0xef880,1616,1552,1264,0xef8f0,1728,1664,1248,0xef8e0,1904,1840,1232,0xef8d0,2016,1952,1216,0xef8c0,32,96,1600,0xefa40,176,240,1616,0xefa50,256,320,1632,0xefa60,400,464,1648,0xefa70,608,544,1536,0xefa00,752,688,1552,0xefa10,832,768,1568,0xefa20,976,912,1584,0xefa30,1056,1120,2000,0xefbd0,1200,1264,1984,0xefbc0,1280,1344,2032,0xefbf0,1424,1488,2016,0xefbe0,1632,1568,1936,0xefb90,1776,1712,1920,0xefb80,1856,1792,1968,0xefbb0,2000,1936,1952,0xefba0,48,112,2400,0xefd60,160,224,2416,0xefd70,272,336,2368,0xefd40,384,448,2384,0xefd50,624,560,2336,0xefd20,736,672,2352,0xefd30,848,784,2304,0xefd00,960,896,2320,0xefd10,1072,1136,0x2fef0,0xefef0,1184,1248,0x2fee0,0xefee0,1296,1360,0x2fed0,0xefed0,1408,1472,0x2fec0,0xefec0,1648,1584,0x2feb0,0xefeb0,1760,1696,0x2fea0,0xefea0,1872,1808,0x2fe90,0xefe90,1984,1920,0x2fe80,0xefe80,0xc0100,73728,77824,90112,0xc0110,73744,77840,90128,0xc0120,73760,77856,90144,0xc0130,73776,77872,90160,0xc0140,73792,77888,90176,0xc0150,73808,77904,90192,0xc0160,73824,77920,90208,0xc0170,73840,77936,90224,0xc0290,73856,77952,90240,0xc0280,73872,77968,90256,0xc02b0,73888,77984,90272,0xc02a0,73904,78000,90288,0xc02d0,73920,78016,90304,0xc02c0,73936,78032,90320,0xc02f0,73952,78048,90336,0xc02e0,73968,78064,90352,0xc0420,73984,78080,90368,0xc0430,74000,78096,90384,0xc0400,74016,78112,90400,0xc0410,74032,78128,90416,0xc0460,74048,78144,90432,0xc0470,74064,78160,90448,0xc0440,74080,78176,90464,0xc0450,74096,78192,90480,0xef9b0,74112,78208,90496,0xef9a0,74128,78224,90512,0xef990,74144,78240,90528,0xef980,74160,78256,90544,0xef9f0,74176,78272,90560,0xef9e0,74192,78288,90576,0xef9d0,74208,78304,90592,0xef9c0,74224,78320,90608,0xc0100,74240,78336,90624,0xc0110,74256,78352,90640,0xc0120,74272,78368,90656,0xc0130,74288,78384,90672,0xc0140,74304,78400,90688,0xc0150,74320,78416,90704,0xc0160,74336,78432,90720,0xc0170,74352,78448,90736,0xc0290,74368,78464,90752,0xc0280,74384,78480,90768,0xc02b0,74400,78496,90784,0xc02a0,74416,78512,90800,0xc02d0,74432,78528,90816,0xc02c0,74448,78544,90832,0xc02f0,74464,78560,90848,0xc02e0,74480,78576,90864,0xc0420,74496,78592,90880,0xc0430,74512,78608,90896,0xc0400,74528,78624,90912,0xc0410,74544,78640,90928,0xc0460,74560,78656,90944,0xc0470,74576,78672,90960,0xc0440,74592,78688,90976,0xc0450,74608,78704,90992,0xef9b0,74624,78720,91008,0xef9a0,74640,78736,91024,0xef990,74656,78752,91040,0xef980,74672,78768,91056,0xef9f0,74688,78784,91072,0xef9e0,74704,78800,91088,0xef9d0,74720,78816,91104,0xef9c0,74736,78832,91120,0x1c000,36864,36864,49152,0x1c004,36880,36880,49168,0x1c008,36896,36896,49184,0x1c00c,36912,36912,49200,0x1c010,36928,36928,49216,0x1c014,36944,36944,49232,0x1c018,36960,36960,49248,0x1c01c,36976,36976,49264,0x1c020,36992,36992,49280,0x1c024,37008,37008,49296,0x1c028,37024,37024,49312,0x1c02c,37040,37040,49328,0x1c030,37056,37056,49344,0x1c034,37072,37072,49360,0x1c038,37088,37088,49376,0x1c03c,37104,37104,49392,0x1c040,37120,37120,49408,0x1c044,37136,37136,49424,0x1c048,37152,37152,49440,0x1c04c,37168,37168,49456,0x1c050,37184,37184,49472,0x1c054,37200,37200,49488,0x1c058,37216,37216,49504,0x1c05c,37232,37232,49520,0x1c060,37248,37248,49536,0x1c064,37264,37264,49552,0x1c068,37280,37280,49568,0x1c06c,37296,37296,49584,0x1c070,37312,37312,49600,0x1c074,37328,37328,49616,0x1c078,37344,37344,49632,0x1c07c,37360,37360,49648,0x1c080,37376,37376,49664,0x1c084,37392,37392,49680,0x1c088,37408,37408,49696,0x1c08c,37424,37424,49712,0x1c090,37440,37440,49728,0x1c094,37456,37456,49744,0x1c098,37472,37472,49760,0x1c09c,37488,37488,49776,0x1c0a0,37504,37504,49792,0x1c0a4,37520,37520,49808,0x1c0a8,37536,37536,49824,0x1c0ac,37552,37552,49840,0x1c0b0,37568,37568,49856,0x1c0b4,37584,37584,49872,0x1c0b8,37600,37600,49888,0x1c0bc,37616,37616,49904,0x1c0c0,37632,37632,49920,0x1c0c4,37648,37648,49936,0x1c0c8,37664,37664,49952,0x1c0cc,37680,37680,49968,0x1c0d0,37696,37696,49984,0x1c0d4,37712,37712,50000,0x1c0d8,37728,37728,50016,0x1c0dc,37744,37744,50032,0x1c0e0,37760,37760,50048,0x1c0e4,37776,37776,50064,0x1c0e8,37792,37792,50080,0x1c0ec,37808,37808,50096,0x1c0f0,37824,37824,50112,0x1c0f4,37840,37840,50128,0x1c0f8,37856,37856,50144,0x1c0fc,37872,37872,50160,73728,73792,0x1d600,0x1d600,73872,73936,0x1d610,0x1d610,74016,74080,0x1d620,0x1d620,74160,74224,0x1d630,0x1d630,74304,74240,0x1d640,0x1d640,74448,74384,0x1d650,0x1d650,74592,74528,0x1d660,0x1d660,74736,74672,0x1d670,0x1d670,74752,74816,0x1d680,0x1d680,74896,74960,0x1d690,0x1d690,75040,75104,0x1d6a0,0x1d6a0,75184,75248,0x1d6b0,0x1d6b0,75328,75264,0x1d6c0,0x1d6c0,75472,75408,0x1d6d0,0x1d6d0,75616,75552,0x1d6e0,0x1d6e0,75760,75696,0x1d6f0,0x1d6f0,73744,73800,0x1d700,0x1d700,73856,73944,0x1d710,0x1d710,74032,74088,0x1d720,0x1d720,74144,74232,0x1d730,0x1d730,74320,74248,0x1d740,0x1d740,74432,74392,0x1d750,0x1d750,74608,74536,0x1d760,0x1d760,74720,74680,0x1d770,0x1d770,74768,74824,0x1d780,0x1d780,74880,74968,0x1d790,0x1d790,75056,75112,0x1d7a0,0x1d7a0,75168,75256,0x1d7b0,0x1d7b0,75344,75272,0x1d7c0,0x1d7c0,75456,75416,0x1d7d0,0x1d7d0,75632,75560,0x1d7e0,0x1d7e0,75744,75704,0x1d7f0,0x1d7f0,73760,73808,0x1d800,0x1d800,73904,73920,0x1d810,0x1d810,73984,74096,0x1d820,0x1d820,74128,74208,0x1d830,0x1d830,74336,74256,0x1d840,0x1d840,74480,74368,0x1d850,0x1d850,74560,74544,0x1d860,0x1d860,74704,74656,0x1d870,0x1d870,74784,74832,0x1d880,0x1d880,74928,74944,0x1d890,0x1d890,75008,75120,0x1d8a0,0x1d8a0,75152,75232,0x1d8b0,0x1d8b0,75360,75280,0x1d8c0,0x1d8c0,75504,75392,0x1d8d0,0x1d8d0,75584,75568,0x1d8e0,0x1d8e0,75728,75680,0x1d8f0,0x1d8f0,73776,73816,0x1d900,0x1d900,73888,73928,0x1d910,0x1d910,74000,74104,0x1d920,0x1d920,74112,74216,0x1d930,0x1d930,74352,74264,0x1d940,0x1d940,74464,74376,0x1d950,0x1d950,74576,74552,0x1d960,0x1d960,74688,74664,0x1d970,0x1d970,74800,74840,0x1d980,0x1d980,74912,74952,0x1d990,0x1d990,75024,75128,0x1d9a0,0x1d9a0,75136,75240,0x1d9b0,0x1d9b0,75376,75288,0x1d9c0,0x1d9c0,75488,75400,0x1d9d0,0x1d9d0,75600,75576,0x1d9e0,0x1d9e0,75712,75688,0x1d9f0,0x1d9f0,0x1e600,0x1e600,0x1e800,0x100000,0x1e602,0x100000,0x100000,0x1e800,0x1e604,0x100000,0x100000,0x100000,0x1e606,0x100000,0x100000,0x100000,0x1e610,0x1e600,0x100000,0x100000,0x1e612,0x100000,0x100000,0x1e800,0x1e614,0x100000,0x100000,0x100000,0x1e616,0x100000,0x100000,0x100000,0x1e620,0x1e600,0x100000,0x100000,0x1e622,0x100000,0x100000,0x1e800,0x1e624,0x100000,0x100000,0x100000,0x1e626,0x100000,0x100000,0x100000,0x1e630,0x1e600,0x100000,0x100000,0x1e632,0x100000,0x100000,0x1e800,0x1e634,0x100000,0x100000,0x100000,0x1e636,0x100000,0x100000,0x100000,0x1e608,0x1e610,0x1e830,0x100000,0x1e60a,0x100000,0x100000,0x1e830,0x1e60c,0x100000,0x100000,0x100000,0x1e60e,0x100000,0x100000,0x100000,0x1e618,0x1e610,0x100000,0x100000,0x1e61a,0x100000,0x100000,0x1e830,0x1e61c,0x100000,0x100000,0x100000,0x1e61e,0x100000,0x100000,0x100000,0x1e628,0x1e610,0x100000,0x100000,0x1e62a,0x100000,0x100000,0x1e830,0x1e62c,0x100000,0x100000,0x100000,0x1e62e,0x100000,0x100000,0x100000,0x1e638,0x1e610,0x100000,0x100000,0x1e63a,0x100000,0x100000,0x1e830,0x1e63c,0x100000,0x100000,0x100000,0x1e63e,0x100000,0x100000,0x100000,0x1e640,0x1e620,0x1e860,0x100000,0x1e642,0x100000,0x100000,0x1e860,0x1e644,0x100000,0x100000,0x100000,0x1e646,0x100000,0x100000,0x100000,0x1e650,0x1e620,0x100000,0x100000,0x1e652,0x100000,0x100000,0x1e860,0x1e654,0x100000,0x100000,0x100000,0x1e656,0x100000,0x100000,0x100000,0x1e660,0x1e620,0x100000,0x100000,0x1e662,0x100000,0x100000,0x1e860,0x1e664,0x100000,0x100000,0x100000,0x1e666,0x100000,0x100000,0x100000,0x1e670,0x1e620,0x100000,0x100000,0x1e672,0x100000,0x100000,0x1e860,0x1e674,0x100000,0x100000,0x100000,0x1e676,0x100000,0x100000,0x100000,0x1e648,0x1e630,0x1e890,0x100000,0x1e64a,0x100000,0x100000,0x1e890,0x1e64c,0x100000,0x100000,0x100000,0x1e64e,0x100000,0x100000,0x100000,0x1e658,0x1e630,0x100000,0x100000,0x1e65a,0x100000,0x100000,0x1e890,0x1e65c,0x100000,0x100000,0x100000,0x1e65e,0x100000,0x100000,0x100000,0x1e668,0x1e630,0x100000,0x100000,0x1e66a,0x100000,0x100000,0x1e890,0x1e66c,0x100000,0x100000,0x100000,0x1e66e,0x100000,0x100000,0x100000,0x1e678,0x1e630,0x100000,0x100000,0x1e67a,0x100000,0x100000,0x1e890,0x1e67c,0x100000,0x100000,0x100000,0x1e67e,0x100000,0x100000,0x100000,0x1eb00,0x24c70,36864,36928,0x1eb02,0x24c72,36880,36936,0x1eb04,0x24c74,36896,36944,0x1eb06,0x24c76,36912,36952,0x1eb08,0x24dd0,37248,37312,0x1eb0a,0x24dd2,37264,37320,0x1eb0c,0x24dd4,37280,37328,0x1eb0e,0x24dd6,37296,37336,0x1eb10,0x24f30,37664,37728,0x1eb12,0x24f32,37680,37736,0x1eb14,0x24f34,37632,37744,0x1eb16,0x24f36,37648,37752,0x1eb18,0x25090,38048,38112,0x1eb1a,0x25092,38064,38120,0x1eb1c,0x25094,38016,38128,0x1eb1e,0x25096,38032,38136,0x1eb00,0x24c78,38464,38400,0x1eb02,0x24c7a,38480,38408,0x1eb04,0x24c7c,38496,38416,0x1eb06,0x24c7e,38512,38424,0x1eb08,0x24dd8,38848,38784,0x1eb0a,0x24dda,38864,38792,0x1eb0c,0x24ddc,38880,38800,0x1eb0e,0x24dde,38896,38808,0x1eb10,0x24f38,39264,39200,0x1eb12,0x24f3a,39280,39208,0x1eb14,0x24f3c,39232,39216,0x1eb16,0x24f3e,39248,39224,0x1eb18,0x25098,39648,39584,0x1eb1a,0x2509a,39664,39592,0x1eb1c,0x2509c,39616,39600,0x1eb1e,0x2509e,39632,39608,0x1eb00,0x26270,39936,40000,0x1eb02,0x26272,39952,40008,0x1eb04,0x26274,39968,40016,0x1eb06,0x26276,39984,40024,0x1eb08,0x263d0,40320,40384,0x1eb0a,0x263d2,40336,40392,0x1eb0c,0x263d4,40352,40400,0x1eb0e,0x263d6,40368,40408,0x1eb10,0x26530,40736,40800,0x1eb12,0x26532,40752,40808,0x1eb14,0x26534,40704,40816,0x1eb16,0x26536,40720,40824,0x1eb18,0x26690,41120,41184,0x1eb1a,0x26692,41136,41192,0x1eb1c,0x26694,41088,41200,0x1eb1e,0x26696,41104,41208,0x1eb00,0x100000,41536,41472,0x1eb02,0x100000,41552,41480,0x1eb04,0x100000,41568,41488,0x1eb06,0x100000,41584,41496,0x1eb08,0x100000,41920,41856,0x1eb0a,0x100000,41936,41864,0x1eb0c,0x100000,41952,41872,0x1eb0e,0x100000,41968,41880,0x1eb10,0x100000,42336,42272,0x1eb12,0x100000,42352,42280,0x1eb14,0x100000,42304,42288,0x1eb16,0x100000,42320,42296,0x1eb18,0x100000,42720,42656,0x1eb1a,0x100000,42736,42664,0x1eb1c,0x100000,42688,42672,0x1eb1e,0x100000,42704,42680,36864,36896,36928,36864,36872,36904,36936,36880,36880,36912,36944,36896,36888,36920,36952,36912,38016,38048,38080,36928,38024,38056,38088,36944,38032,38064,38096,36960,38040,38072,38104,36976,39200,39168,39264,36992,39208,39176,39272,37008,39216,39184,39280,37024,39224,39192,39288,37040,40352,40320,40416,37056,40360,40328,40424,37072,40368,40336,40432,37088,40376,40344,40440,37104,46144,46176,46080,37120,46152,46184,46088,37136,46160,46192,46096,37152,46168,46200,46104,37168,47296,47328,47232,37184,47304,47336,47240,37200,47312,47344,47248,37216,47320,47352,47256,37232,48480,48448,48416,37248,48488,48456,48424,37264,48496,48464,48432,37280,48504,48472,48440,37296,49632,49600,49568,37312,49640,49608,49576,37328,49648,49616,49584,37344,49656,49624,49592,37360,55296,55328,55360,37376,55304,55336,55368,37392,55312,55344,55376,37408,55320,55352,55384,37424,56448,56480,56512,37440,56456,56488,56520,37456,56464,56496,56528,37472,56472,56504,56536,37488,57632,57600,57696,37504,57640,57608,57704,37520,57648,57616,57712,37536,57656,57624,57720,37552,58784,58752,58848,37568,58792,58760,58856,37584,58800,58768,58864,37600,58808,58776,58872,37616,64576,64608,64512,37632,64584,64616,64520,37648,64592,64624,64528,37664,64600,64632,64536,37680,65728,65760,65664,37696,65736,65768,65672,37712,65744,65776,65680,37728,65752,65784,65688,37744,66912,66880,66848,37760,66920,66888,66856,37776,66928,66896,66864,37792,66936,66904,66872,37808,68064,68032,68000,37824,68072,68040,68008,37840,68080,68048,68016,37856,68088,68056,68024,37872,0x1e800,0x1e800,0x20cc0,0,0x1e8c4,0x1e8c4,0x20cc4,16,0x1e988,0x1e988,0x20cc8,32,0x1ea4c,0x1ea4c,0x20ccc,48,0x1eb10,0x1eb10,0x20cd0,64,0x1ebd4,0x1ebd4,0x20cd4,80,0x1ec98,0x1ec98,0x20cd8,96,0x1ed5c,0x1ed5c,0x20cdc,112,0x1ee20,0x1ee20,0x20ce0,384,0x1eee4,0x1eee4,0x20ce4,400,0x1efa8,0x1efa8,0x20ce8,416,0x1f06c,0x1f06c,0x20cec,432,0x1f130,0x1f130,0x20cf0,448,0x1f1f4,0x1f1f4,0x20cf4,464,0x1f2b8,0x1f2b8,0x20cf8,480,0x1f37c,0x1f37c,0x20cfc,496,0x1e810,0x1e800,0x20cc0,768,0x1e8d4,0x1e8c4,0x20cc4,784,0x1e998,0x1e988,0x20cc8,800,0x1ea5c,0x1ea4c,0x20ccc,816,0x1eb20,0x1eb10,0x20cd0,832,0x1ebe4,0x1ebd4,0x20cd4,848,0x1eca8,0x1ec98,0x20cd8,864,0x1ed6c,0x1ed5c,0x20cdc,880,0x1ee30,0x1ee20,0x20ce0,1152,0x1eef4,0x1eee4,0x20ce4,1168,0x1efb8,0x1efa8,0x20ce8,1184,0x1f07c,0x1f06c,0x20cec,1200,0x1f140,0x1f130,0x20cf0,1216,0x1f204,0x1f1f4,0x20cf4,1232,0x1f2c8,0x1f2b8,0x20cf8,1248,0x1f38c,0x1f37c,0x20cfc,1264,0x1e820,0x1e800,0x20cc0,1536,0x1e8e4,0x1e8c4,0x20cc4,1552,0x1e9a8,0x1e988,0x20cc8,1568,0x1ea6c,0x1ea4c,0x20ccc,1584,0x1eb30,0x1eb10,0x20cd0,1600,0x1ebf4,0x1ebd4,0x20cd4,1616,0x1ecb8,0x1ec98,0x20cd8,1632,0x1ed7c,0x1ed5c,0x20cdc,1648,0x1ee40,0x1ee20,0x20ce0,1920,0x1ef04,0x1eee4,0x20ce4,1936,0x1efc8,0x1efa8,0x20ce8,1952,0x1f08c,0x1f06c,0x20cec,1968,0x1f150,0x1f130,0x20cf0,1984,0x1f214,0x1f1f4,0x20cf4,2000,0x1f2d8,0x1f2b8,0x20cf8,2016,0x1f39c,0x1f37c,0x20cfc,2032,0x1e830,0x1e800,0x20cc0,2304,0x1e8f4,0x1e8c4,0x20cc4,2320,0x1e9b8,0x1e988,0x20cc8,2336,0x1ea7c,0x1ea4c,0x20ccc,2352,0x1eb40,0x1eb10,0x20cd0,2368,0x1ec04,0x1ebd4,0x20cd4,2384,0x1ecc8,0x1ec98,0x20cd8,2400,0x1ed8c,0x1ed5c,0x20cdc,2416,0x1ee50,0x1ee20,0x20ce0,2688,0x1ef14,0x1eee4,0x20ce4,2704,0x1efd8,0x1efa8,0x20ce8,2720,0x1f09c,0x1f06c,0x20cec,2736,0x1f160,0x1f130,0x20cf0,2752,0x1f224,0x1f1f4,0x20cf4,2768,0x1f2e8,0x1f2b8,0x20cf8,2784,0x1f3ac,0x1f37c,0x20cfc,2800,36864,0x26d70,0x1e800,0x1eb00,36880,0x26d74,0x1ec90,0x1ef90,36896,0x26d78,0x1f120,0x1f420,36912,0x26d7c,0x1f5b0,0x1f8b0,36928,0x26d80,0x1fa40,0x1fd40,36944,0x26d84,0x1fed0,0x201d0,36960,0x26d88,0x20360,0x20660,36976,0x26d8c,0x207f0,0x20af0,37248,0x26d90,0x20c80,0x20f80,37264,0x26d94,0x21110,0x21410,37280,0x26d98,0x215a0,0x218a0,37296,0x26d9c,0x21a30,0x21d30,37312,0x26da0,0x21ec0,0x221c0,37328,0x26da4,0x22350,0x22650,37344,0x26da8,0x227e0,0x22ae0,37360,0x26dac,0x22c70,0x22f70,37664,0x26db0,0x1e830,0x1eb08,37680,0x26db4,0x1ecc0,0x1ef98,37632,0x26db8,0x1f150,0x1f428,37648,0x26dbc,0x1f5e0,0x1f8b8,37728,0x26dc0,0x1fa70,0x1fd48,37744,0x26dc4,0x1ff00,0x201d8,37696,0x26dc8,0x20390,0x20668,37712,0x26dcc,0x20820,0x20af8,38048,0x26dd0,0x20cb0,0x20f88,38064,0x26dd4,0x21140,0x21418,38016,0x26dd8,0x215d0,0x218a8,38032,0x26ddc,0x21a60,0x21d38,38112,0x26de0,0x21ef0,0x221c8,38128,0x26de4,0x22380,0x22658,38080,0x26de8,0x22810,0x22ae8,38096,0x26dec,0x22ca0,0x22f78,38464,0x26df0,0x1e860,0x1eb10,38480,0x26df4,0x1ecf0,0x1efa0,38496,0x26df8,0x1f180,0x1f430,38512,0x26dfc,0x1f610,0x1f8c0,38400,0x26e00,0x1faa0,0x1fd50,38416,0x26e04,0x1ff30,0x201e0,38432,0x26e08,0x203c0,0x20670,38448,0x26e0c,0x20850,0x20b00,38848,0x26e10,0x20ce0,0x20f90,38864,0x26e14,0x21170,0x21420,38880,0x26e18,0x21600,0x218b0,38896,0x26e1c,0x21a90,0x21d40,38784,0x26e20,0x21f20,0x221d0,38800,0x26e24,0x223b0,0x22660,38816,0x26e28,0x22840,0x22af0,38832,0x26e2c,0x22cd0,0x22f80,39264,0x26e30,0x1e890,0x1eb18,39280,0x26e34,0x1ed20,0x1efa8,39232,0x26e38,0x1f1b0,0x1f438,39248,0x26e3c,0x1f640,0x1f8c8,39200,0x26e40,0x1fad0,0x1fd58,39216,0x26e44,0x1ff60,0x201e8,39168,0x26e48,0x203f0,0x20678,39184,0x26e4c,0x20880,0x20b08,39648,0x26e50,0x20d10,0x20f98,39664,0x26e54,0x211a0,0x21428,39616,0x26e58,0x21630,0x218b8,39632,0x26e5c,0x21ac0,0x21d48,39584,0x26e60,0x21f50,0x221d8,39600,0x26e64,0x223e0,0x22668,39552,0x26e68,0x22870,0x22af8,39568,0x26e6c,0x22d00,0x22f88,0x3f803f80,0x3f803f80,16256,0,0,0x3f803f80,0x3f800000,0,0,0x3f803f80,0,16256,0,0x3f803f80,0,0x3f800000,0x3f803f80,0,0,0,0,0,0,0,0,0,0,0,0,0,0,0,0x3f803f80,0,0,0,0,0,0,0,0,0,0,0,0,0,0,0,0x3f803f80,0,0,0,0,0,0,0,0,0,0,0,0,0,0,0,0x3f803f80,0,0,0,0,0,0,0,0,0,0,0,0,0,0,0,0x3f803f80,0x3f803f80,16256,0,0,0x3f803f80,0x3f800000,0,0,0x3f803f80,0,16256,0,0x3f803f80,0,0x3f800000,0x3f803f80,0,0,0,0,0,0,0,0,0,0,0,0,0,0,0,0x3f803f80,0,0,0,0,0,0,0,0,0,0,0,0,0,0,0,0x3f803f80,0,0,0,0,0,0,0,0,0,0,0,0,0,0,0,0x3f803f80,0,0,0,0,0,0,0,0,0,0,0,0,0,0,0,0x3f803f80,0x3f803f80,16256,0,0,0x3f803f80,0x3f800000,0,0,0x3f803f80,0,16256,0,0x3f803f80,0,0x3f800000,0x3f803f80,0,0,0,0,0,0,0,0,0,0,0,0,0,0,0,0x3f803f80,0,0,0,0,0,0,0,0,0,0,0,0,0,0,0,0x3f803f80,0,0,0,0,0,0,0,0,0,0,0,0,0,0,0,0x3f803f80,0,0,0,0,0,0,0,0,0,0,0,0,0,0,0,0x3f803f80,0x3f803f80,16256,0,0,0x3f803f80,0x3f800000,0,0,0x3f803f80,0,16256,0,0x3f803f80,0,0x3f800000,0,64,0x30000,0xf0000,144,208,0x30010,0xf0010,288,352,0x30020,0xf0020,432,496,0x30030,0xf0030,576,512,0x30040,0xf0040,720,656,0x30050,0xf0050,864,800,0x30060,0xf0060,1008,944,0x30070,0xf0070,1024,1088,0x30190,0xf0190,1168,1232,0x30180,0xf0180,1312,1376,0x301b0,0xf01b0,1456,1520,0x301a0,0xf01a0,1600,1536,0x301d0,0xf01d0,1744,1680,0x301c0,0xf01c0,1888,1824,0x301f0,0xf01f0,2032,1968,0x301e0,0xf01e0,16,80,0x30320,0x11f720,128,192,0x30330,0x11f730,304,368,0x30300,0x11f700,416,480,0x30310,0x11f710,592,528,0x30360,0x11f760,704,640,0x30370,0x11f770,880,816,0x30340,0x11f740,992,928,0x30350,0x11f750,1040,1104,0x304b0,0x11f8b0,1152,1216,0x304a0,0x11f8a0,1328,1392,0x30490,0x11f890,1440,1504,0x30480,0x11f880,1616,1552,0x304f0,0x11f8f0,1728,1664,0x304e0,0x11f8e0,1904,1840,0x304d0,0x11f8d0,2016,1952,0x304c0,0x11f8c0,32,96,0x30640,0x11fa40,176,240,0x30650,0x11fa50,256,320,0x30660,0x11fa60,400,464,0x30670,0x11fa70,608,544,0x30600,0x11fa00,752,688,0x30610,0x11fa10,832,768,0x30620,0x11fa20,976,912,0x30630,0x11fa30,1056,1120,0x307d0,0x11fbd0,1200,1264,0x307c0,0x11fbc0,1280,1344,0x307f0,0x11fbf0,1424,1488,0x307e0,0x11fbe0,1632,1568,0x30790,0x11fb90,1776,1712,0x30780,0x11fb80,1856,1792,0x307b0,0x11fbb0,2000,1936,0x307a0,0x11fba0,48,112,0x5fd60,0x11fd60,160,224,0x5fd70,0x11fd70,272,336,0x5fd40,0x11fd40,384,448,0x5fd50,0x11fd50,624,560,0x5fd20,0x11fd20,736,672,0x5fd30,0x11fd30,848,784,0x5fd00,0x11fd00,960,896,0x5fd10,0x11fd10,1072,1136,0x5fef0,0x11fef0,1184,1248,0x5fee0,0x11fee0,1296,1360,0x5fed0,0x11fed0,1408,1472,0x5fec0,0x11fec0,1648,1584,0x5feb0,0x11feb0,1760,1696,0x5fea0,0x11fea0,1872,1808,0x5fe90,0x11fe90,1984,1920,0x5fe80,0x11fe80,0xefb40,74752,78848,90624,0xefb50,74768,78864,90640,0xefb60,74784,78880,90656,0xefb70,74800,78896,90672,0xefb00,74816,78912,90688,0xefb10,74832,78928,90704,0xefb20,74848,78944,90720,0xefb30,74864,78960,90736,0xefcd0,74880,78976,90752,0xefcc0,74896,78992,90768,0xefcf0,74912,79008,90784,0xefce0,74928,79024,90800,0xefc90,74944,79040,90816,0xefc80,74960,79056,90832,0xefcb0,74976,79072,90848,0xefca0,74992,79088,90864,0xefe60,75008,79104,90880,0xefe70,75024,79120,90896,0xefe40,75040,79136,90912,0xefe50,75056,79152,90928,0xefe20,75072,79168,90944,0xefe30,75088,79184,90960,0xefe00,75104,79200,90976,0xefe10,75120,79216,90992,0xefff0,75136,79232,91008,0xeffe0,75152,79248,91024,0xeffd0,75168,79264,91040,0xeffc0,75184,79280,91056,0xeffb0,75200,79296,91072,0xeffa0,75216,79312,91088,0xeff90,75232,79328,91104,0xeff80,75248,79344,91120,0xefb40,75264,79360,91136,0xefb50,75280,79376,91152,0xefb60,75296,79392,91168,0xefb70,75312,79408,91184,0xefb00,75328,79424,91200,0xefb10,75344,79440,91216,0xefb20,75360,79456,91232,0xefb30,75376,79472,91248,0xefcd0,75392,79488,91264,0xefcc0,75408,79504,91280,0xefcf0,75424,79520,91296,0xefce0,75440,79536,91312,0xefc90,75456,79552,91328,0xefc80,75472,79568,91344,0xefcb0,75488,79584,91360,0xefca0,75504,79600,91376,0xefe60,75520,79616,91392,0xefe70,75536,79632,91408,0xefe40,75552,79648,91424,0xefe50,75568,79664,91440,0xefe20,75584,79680,91456,0xefe30,75600,79696,91472,0xefe00,75616,79712,91488,0xefe10,75632,79728,91504,0xefff0,75648,79744,91520,0xeffe0,75664,79760,91536,0xeffd0,75680,79776,91552,0xeffc0,75696,79792,91568,0xeffb0,75712,79808,91584,0xeffa0,75728,79824,91600,0xeff90,75744,79840,91616,0xeff80,75760,79856,91632,0x1c0a0,36864,39936,49160,0x1c0a4,36880,39952,49176,0x1c0a8,36896,39968,49192,0x1c0ac,36912,39984,49208,0x1c0b0,36928,40000,49224,0x1c0b4,36944,40016,49240,0x1c0b8,36960,40032,49256,0x1c0bc,36976,40048,49272,0x1c0c0,36992,40064,49288,0x1c0c4,37008,40080,49304,0x1c0c8,37024,40096,49320,0x1c0cc,37040,40112,49336,0x1c0d0,37056,40128,49352,0x1c0d4,37072,40144,49368,0x1c0d8,37088,40160,49384,0x1c0dc,37104,40176,49400,0x1c0e0,37120,40192,49416,0x1c0e4,37136,40208,49432,0x1c0e8,37152,40224,49448,0x1c0ec,37168,40240,49464,0x1c0f0,37184,40256,49480,0x1c0f4,37200,40272,49496,0x1c0f8,37216,40288,49512,0x1c0fc,37232,40304,49528,0x1c100,37248,40320,49544,0x1c104,37264,40336,49560,0x1c108,37280,40352,49576,0x1c10c,37296,40368,49592,0x1c110,37312,40384,49608,0x1c114,37328,40400,49624,0x1c118,37344,40416,49640,0x1c11c,37360,40432,49656,0x1c120,37376,40448,49672,0x1c124,37392,40464,49688,0x1c128,37408,40480,49704,0x1c12c,37424,40496,49720,0x1c130,37440,40512,49736,0x1c134,37456,40528,49752,0x1c138,37472,40544,49768,0x1c13c,37488,40560,49784,0x1c140,37504,40576,49800,0x1c144,37520,40592,49816,0x1c148,37536,40608,49832,0x1c14c,37552,40624,49848,0x1c150,37568,40640,49864,0x1c154,37584,40656,49880,0x1c158,37600,40672,49896,0x1c15c,37616,40688,49912,0x1c160,37632,40704,49928,0x1c164,37648,40720,49944,0x1c168,37664,40736,49960,0x1c16c,37680,40752,49976,0x1c170,37696,40768,49992,0x1c174,37712,40784,50008,0x1c178,37728,40800,50024,0x1c17c,37744,40816,50040,0x1c180,37760,40832,50056,0x1c184,37776,40848,50072,0x1c188,37792,40864,50088,0x1c18c,37808,40880,50104,0x1c190,37824,40896,50120,0x1c194,37840,40912,50136,0x1c198,37856,40928,50152,0x1c19c,37872,40944,50168,73824,79904,0x1d600,0x1d604,73968,80048,0x1d610,0x1d614,74048,80128,0x1d620,0x1d624,74192,80272,0x1d630,0x1d634,74272,80480,0x1d640,0x1d644,74416,80624,0x1d650,0x1d654,74496,80704,0x1d660,0x1d664,74640,80848,0x1d670,0x1d674,74848,80928,0x1d680,0x1d684,74992,81072,0x1d690,0x1d694,75072,81152,0x1d6a0,0x1d6a4,75216,81296,0x1d6b0,0x1d6b4,75296,81504,0x1d6c0,0x1d6c4,75440,81648,0x1d6d0,0x1d6d4,75520,81728,0x1d6e0,0x1d6e4,75664,81872,0x1d6f0,0x1d6f4,73840,79912,0x1d700,0x1d704,73952,80056,0x1d710,0x1d714,74064,80136,0x1d720,0x1d724,74176,80280,0x1d730,0x1d734,74288,80488,0x1d740,0x1d744,74400,80632,0x1d750,0x1d754,74512,80712,0x1d760,0x1d764,74624,80856,0x1d770,0x1d774,74864,80936,0x1d780,0x1d784,74976,81080,0x1d790,0x1d794,75088,81160,0x1d7a0,0x1d7a4,75200,81304,0x1d7b0,0x1d7b4,75312,81512,0x1d7c0,0x1d7c4,75424,81656,0x1d7d0,0x1d7d4,75536,81736,0x1d7e0,0x1d7e4,75648,81880,0x1d7f0,0x1d7f4,79872,79920,0x1d800,0x1d804,80016,80032,0x1d810,0x1d814,80160,80144,0x1d820,0x1d824,80304,80256,0x1d830,0x1d834,80448,80496,0x1d840,0x1d844,80592,80608,0x1d850,0x1d854,80736,80720,0x1d860,0x1d864,80880,80832,0x1d870,0x1d874,80896,80944,0x1d880,0x1d884,81040,81056,0x1d890,0x1d894,81184,81168,0x1d8a0,0x1d8a4,81328,81280,0x1d8b0,0x1d8b4,81472,81520,0x1d8c0,0x1d8c4,81616,81632,0x1d8d0,0x1d8d4,81760,81744,0x1d8e0,0x1d8e4,81904,81856,0x1d8f0,0x1d8f4,79888,79928,0x1d900,0x1d904,80000,80040,0x1d910,0x1d914,80176,80152,0x1d920,0x1d924,80288,80264,0x1d930,0x1d934,80464,80504,0x1d940,0x1d944,80576,80616,0x1d950,0x1d954,80752,80728,0x1d960,0x1d964,80864,80840,0x1d970,0x1d974,80912,80952,0x1d980,0x1d984,81024,81064,0x1d990,0x1d994,81200,81176,0x1d9a0,0x1d9a4,81312,81288,0x1d9b0,0x1d9b4,81488,81528,0x1d9c0,0x1d9c4,81600,81640,0x1d9d0,0x1d9d4,81776,81752,0x1d9e0,0x1d9e4,81888,81864,0x1d9f0,0x1d9f4,0x1e680,0x1e680,0x1e8c0,0x100000,0x1e682,0x100000,0x100000,0x1e8c0,0x1e684,0x100000,0x100000,0x100000,0x1e686,0x100000,0x100000,0x100000,0x1e690,0x1e680,0x100000,0x100000,0x1e692,0x100000,0x100000,0x1e8c0,0x1e694,0x100000,0x100000,0x100000,0x1e696,0x100000,0x100000,0x100000,0x1e6a0,0x1e680,0x100000,0x100000,0x1e6a2,0x100000,0x100000,0x1e8c0,0x1e6a4,0x100000,0x100000,0x100000,0x1e6a6,0x100000,0x100000,0x100000,0x1e6b0,0x1e680,0x100000,0x100000,0x1e6b2,0x100000,0x100000,0x1e8c0,0x1e6b4,0x100000,0x100000,0x100000,0x1e6b6,0x100000,0x100000,0x100000,0x1e688,0x1e690,0x1e8f0,0x100000,0x1e68a,0x100000,0x100000,0x1e8f0,0x1e68c,0x100000,0x100000,0x100000,0x1e68e,0x100000,0x100000,0x100000,0x1e698,0x1e690,0x100000,0x100000,0x1e69a,0x100000,0x100000,0x1e8f0,0x1e69c,0x100000,0x100000,0x100000,0x1e69e,0x100000,0x100000,0x100000,0x1e6a8,0x1e690,0x100000,0x100000,0x1e6aa,0x100000,0x100000,0x1e8f0,0x1e6ac,0x100000,0x100000,0x100000,0x1e6ae,0x100000,0x100000,0x100000,0x1e6b8,0x1e690,0x100000,0x100000,0x1e6ba,0x100000,0x100000,0x1e8f0,0x1e6bc,0x100000,0x100000,0x100000,0x1e6be,0x100000,0x100000,0x100000,0x1e6c0,0x1e6a0,0x1e920,0x100000,0x1e6c2,0x100000,0x100000,0x1e920,0x1e6c4,0x100000,0x100000,0x100000,0x1e6c6,0x100000,0x100000,0x100000,0x1e6d0,0x1e6a0,0x100000,0x100000,0x1e6d2,0x100000,0x100000,0x1e920,0x1e6d4,0x100000,0x100000,0x100000,0x1e6d6,0x100000,0x100000,0x100000,0x1e6e0,0x1e6a0,0x100000,0x100000,0x1e6e2,0x100000,0x100000,0x1e920,0x1e6e4,0x100000,0x100000,0x100000,0x1e6e6,0x100000,0x100000,0x100000,0x1e6f0,0x1e6a0,0x100000,0x100000,0x1e6f2,0x100000,0x100000,0x1e920,0x1e6f4,0x100000,0x100000,0x100000,0x1e6f6,0x100000,0x100000,0x100000,0x1e6c8,0x1e6b0,0x1e950,0x100000,0x1e6ca,0x100000,0x100000,0x1e950,0x1e6cc,0x100000,0x100000,0x100000,0x1e6ce,0x100000,0x100000,0x100000,0x1e6d8,0x1e6b0,0x100000,0x100000,0x1e6da,0x100000,0x100000,0x1e950,0x1e6dc,0x100000,0x100000,0x100000,0x1e6de,0x100000,0x100000,0x100000,0x1e6e8,0x1e6b0,0x100000,0x100000,0x1e6ea,0x100000,0x100000,0x1e950,0x1e6ec,0x100000,0x100000,0x100000,0x1e6ee,0x100000,0x100000,0x100000,0x1e6f8,0x1e6b0,0x100000,0x100000,0x1e6fa,0x100000,0x100000,0x1e950,0x1e6fc,0x100000,0x100000,0x100000,0x1e6fe,0x100000,0x100000,0x100000,0x1eb60,0x251f0,36960,37024,0x1eb62,0x251f2,36976,37032,0x1eb64,0x251f4,36992,37040,0x1eb66,0x251f6,37008,37048,0x1eb68,0x25350,37344,37408,0x1eb6a,0x25352,37360,37416,0x1eb6c,0x25354,37376,37424,0x1eb6e,0x25356,37392,37432,0x1eb70,0x254b0,37696,37760,0x1eb72,0x254b2,37712,37768,0x1eb74,0x254b4,37792,37776,0x1eb76,0x254b6,37808,37784,0x1eb78,0x25610,38080,38144,0x1eb7a,0x25612,38096,38152,0x1eb7c,0x25614,38176,38160,0x1eb7e,0x25616,38192,38168,0x1eb60,0x251f8,38432,38624,0x1eb62,0x251fa,38448,38632,0x1eb64,0x251fc,38592,38640,0x1eb66,0x251fe,38608,38648,0x1eb68,0x25358,38816,39008,0x1eb6a,0x2535a,38832,39016,0x1eb6c,0x2535c,38976,39024,0x1eb6e,0x2535e,38992,39032,0x1eb70,0x254b8,39168,39360,0x1eb72,0x254ba,39184,39368,0x1eb74,0x254bc,39392,39376,0x1eb76,0x254be,39408,39384,0x1eb78,0x25618,39552,39744,0x1eb7a,0x2561a,39568,39752,0x1eb7c,0x2561c,39776,39760,0x1eb7e,0x2561e,39792,39768,0x1eb60,0x26278,40032,40096,0x1eb62,0x2627a,40048,40104,0x1eb64,0x2627c,40064,40112,0x1eb66,0x2627e,40080,40120,0x1eb68,0x263d8,40416,40480,0x1eb6a,0x263da,40432,40488,0x1eb6c,0x263dc,40448,40496,0x1eb6e,0x263de,40464,40504,0x1eb70,0x26538,40768,40832,0x1eb72,0x2653a,40784,40840,0x1eb74,0x2653c,40864,40848,0x1eb76,0x2653e,40880,40856,0x1eb78,0x26698,41152,41216,0x1eb7a,0x2669a,41168,41224,0x1eb7c,0x2669c,41248,41232,0x1eb7e,0x2669e,41264,41240,0x1eb60,0x100000,41504,41696,0x1eb62,0x100000,41520,41704,0x1eb64,0x100000,41664,41712,0x1eb66,0x100000,41680,41720,0x1eb68,0x100000,41888,42080,0x1eb6a,0x100000,41904,42088,0x1eb6c,0x100000,42048,42096,0x1eb6e,0x100000,42064,42104,0x1eb70,0x100000,42240,42432,0x1eb72,0x100000,42256,42440,0x1eb74,0x100000,42464,42448,0x1eb76,0x100000,42480,42456,0x1eb78,0x100000,42624,42816,0x1eb7a,0x100000,42640,42824,0x1eb7c,0x100000,42848,42832,0x1eb7e,0x100000,42864,42840,36960,36992,37024,46080,36968,37000,37032,46096,36976,37008,37040,46112,36984,37016,37048,46128,38112,38144,38176,46144,38120,38152,38184,46160,38128,38160,38192,46176,38136,38168,38200,46192,39232,39328,39296,46208,39240,39336,39304,46224,39248,39344,39312,46240,39256,39352,39320,46256,40384,40480,40448,46272,40392,40488,40456,46288,40400,40496,40464,46304,40408,40504,40472,46320,46112,46272,46304,46336,46120,46280,46312,46352,46128,46288,46320,46368,46136,46296,46328,46384,47264,47424,47456,46400,47272,47432,47464,46416,47280,47440,47472,46432,47288,47448,47480,46448,48384,48608,48576,46464,48392,48616,48584,46480,48400,48624,48592,46496,48408,48632,48600,46512,49536,49760,49728,46528,49544,49768,49736,46544,49552,49776,49744,46560,49560,49784,49752,46576,55392,55424,55456,46592,55400,55432,55464,46608,55408,55440,55472,46624,55416,55448,55480,46640,56544,56576,56608,46656,56552,56584,56616,46672,56560,56592,56624,46688,56568,56600,56632,46704,57664,57760,57728,46720,57672,57768,57736,46736,57680,57776,57744,46752,57688,57784,57752,46768,58816,58912,58880,46784,58824,58920,58888,46800,58832,58928,58896,46816,58840,58936,58904,46832,64544,64704,64736,46848,64552,64712,64744,46864,64560,64720,64752,46880,64568,64728,64760,46896,65696,65856,65888,46912,65704,65864,65896,46928,65712,65872,65904,46944,65720,65880,65912,46960,66816,67040,67008,46976,66824,67048,67016,46992,66832,67056,67024,47008,66840,67064,67032,47024,67968,68192,68160,47040,67976,68200,68168,47056,67984,68208,68176,47072,67992,68216,68184,47088,0x1e800,0x1e800,0x20cc0,18432,0x1e8c4,0x1e8c4,0x20cc4,18448,0x1e988,0x1e988,0x20cc8,18464,0x1ea4c,0x1ea4c,0x20ccc,18480,0x1eb10,0x1eb10,0x20cd0,18496,0x1ebd4,0x1ebd4,0x20cd4,18512,0x1ec98,0x1ec98,0x20cd8,18528,0x1ed5c,0x1ed5c,0x20cdc,18544,0x1ee20,0x1ee20,0x20ce0,18816,0x1eee4,0x1eee4,0x20ce4,18832,0x1efa8,0x1efa8,0x20ce8,18848,0x1f06c,0x1f06c,0x20cec,18864,0x1f130,0x1f130,0x20cf0,18880,0x1f1f4,0x1f1f4,0x20cf4,18896,0x1f2b8,0x1f2b8,0x20cf8,18912,0x1f37c,0x1f37c,0x20cfc,18928,0x1e810,0x1e800,0x20cc0,19200,0x1e8d4,0x1e8c4,0x20cc4,19216,0x1e998,0x1e988,0x20cc8,19232,0x1ea5c,0x1ea4c,0x20ccc,19248,0x1eb20,0x1eb10,0x20cd0,19264,0x1ebe4,0x1ebd4,0x20cd4,19280,0x1eca8,0x1ec98,0x20cd8,19296,0x1ed6c,0x1ed5c,0x20cdc,19312,0x1ee30,0x1ee20,0x20ce0,19584,0x1eef4,0x1eee4,0x20ce4,19600,0x1efb8,0x1efa8,0x20ce8,19616,0x1f07c,0x1f06c,0x20cec,19632,0x1f140,0x1f130,0x20cf0,19648,0x1f204,0x1f1f4,0x20cf4,19664,0x1f2c8,0x1f2b8,0x20cf8,19680,0x1f38c,0x1f37c,0x20cfc,19696,0x1e820,0x1e800,0x20cc0,19968,0x1e8e4,0x1e8c4,0x20cc4,19984,0x1e9a8,0x1e988,0x20cc8,20000,0x1ea6c,0x1ea4c,0x20ccc,20016,0x1eb30,0x1eb10,0x20cd0,20032,0x1ebf4,0x1ebd4,0x20cd4,20048,0x1ecb8,0x1ec98,0x20cd8,20064,0x1ed7c,0x1ed5c,0x20cdc,20080,0x1ee40,0x1ee20,0x20ce0,20352,0x1ef04,0x1eee4,0x20ce4,20368,0x1efc8,0x1efa8,0x20ce8,20384,0x1f08c,0x1f06c,0x20cec,20400,0x1f150,0x1f130,0x20cf0,20416,0x1f214,0x1f1f4,0x20cf4,20432,0x1f2d8,0x1f2b8,0x20cf8,20448,0x1f39c,0x1f37c,0x20cfc,20464,0x1e830,0x1e800,0x20cc0,20736,0x1e8f4,0x1e8c4,0x20cc4,20752,0x1e9b8,0x1e988,0x20cc8,20768,0x1ea7c,0x1ea4c,0x20ccc,20784,0x1eb40,0x1eb10,0x20cd0,20800,0x1ec04,0x1ebd4,0x20cd4,20816,0x1ecc8,0x1ec98,0x20cd8,20832,0x1ed8c,0x1ed5c,0x20cdc,20848,0x1ee50,0x1ee20,0x20ce0,21120,0x1ef14,0x1eee4,0x20ce4,21136,0x1efd8,0x1efa8,0x20ce8,21152,0x1f09c,0x1f06c,0x20cec,21168,0x1f160,0x1f130,0x20cf0,21184,0x1f224,0x1f1f4,0x20cf4,21200,0x1f2e8,0x1f2b8,0x20cf8,21216,0x1f3ac,0x1f37c,0x20cfc,21232,55296,0x26e70,0x1e8c0,0x1eb60,55312,0x26e74,0x1ed50,0x1eff0,55328,0x26e78,0x1f1e0,0x1f480,55344,0x26e7c,0x1f670,0x1f910,55360,0x26e80,0x1fb00,0x1fda0,55376,0x26e84,0x1ff90,0x20230,55392,0x26e88,0x20420,0x206c0,55408,0x26e8c,0x208b0,0x20b50,55680,0x26e90,0x20d40,0x20fe0,55696,0x26e94,0x211d0,0x21470,55712,0x26e98,0x21660,0x21900,55728,0x26e9c,0x21af0,0x21d90,55744,0x26ea0,0x21f80,0x22220,55760,0x26ea4,0x22410,0x226b0,55776,0x26ea8,0x228a0,0x22b40,55792,0x26eac,0x22d30,0x22fd0,56096,0x26eb0,0x1e8f0,0x1eb68,56112,0x26eb4,0x1ed80,0x1eff8,56064,0x26eb8,0x1f210,0x1f488,56080,0x26ebc,0x1f6a0,0x1f918,56160,0x26ec0,0x1fb30,0x1fda8,56176,0x26ec4,0x1ffc0,0x20238,56128,0x26ec8,0x20450,0x206c8,56144,0x26ecc,0x208e0,0x20b58,56480,0x26ed0,0x20d70,0x20fe8,56496,0x26ed4,0x21200,0x21478,56448,0x26ed8,0x21690,0x21908,56464,0x26edc,0x21b20,0x21d98,56544,0x26ee0,0x21fb0,0x22228,56560,0x26ee4,0x22440,0x226b8,56512,0x26ee8,0x228d0,0x22b48,56528,0x26eec,0x22d60,0x22fd8,56896,0x26ef0,0x1e920,0x1eb70,56912,0x26ef4,0x1edb0,0x1f000,56928,0x26ef8,0x1f240,0x1f490,56944,0x26efc,0x1f6d0,0x1f920,56832,0x26f00,0x1fb60,0x1fdb0,56848,0x26f04,0x1fff0,0x20240,56864,0x26f08,0x20480,0x206d0,56880,0x26f0c,0x20910,0x20b60,57280,0x26f10,0x20da0,0x20ff0,57296,0x26f14,0x21230,0x21480,57312,0x26f18,0x216c0,0x21910,57328,0x26f1c,0x21b50,0x21da0,57216,0x26f20,0x21fe0,0x22230,57232,0x26f24,0x22470,0x226c0,57248,0x26f28,0x22900,0x22b50,57264,0x26f2c,0x22d90,0x22fe0,57696,0x26f30,0x1e950,0x1eb78,57712,0x26f34,0x1ede0,0x1f008,57664,0x26f38,0x1f270,0x1f498,57680,0x26f3c,0x1f700,0x1f928,57632,0x26f40,0x1fb90,0x1fdb8,57648,0x26f44,0x20020,0x20248,57600,0x26f48,0x204b0,0x206d8,57616,0x26f4c,0x20940,0x20b68,58080,0x26f50,0x20dd0,0x20ff8,58096,0x26f54,0x21260,0x21488,58048,0x26f58,0x216f0,0x21918,58064,0x26f5c,0x21b80,0x21da8,58016,0x26f60,0x22010,0x22238,58032,0x26f64,0x224a0,0x226c8,57984,0x26f68,0x22930,0x22b58,58000,0x26f6c,0x22dc0,0x22fe8,0x3f803f80,0x3f803f80,16256,0,0,0x3f803f80,0x3f800000,0,0,0x3f803f80,0,16256,0,0x3f803f80,0,0x3f800000,0x3f803f80,0,0,0,0,0,0,0,0,0,0,0,0,0,0,0,0x3f803f80,0,0,0,0,0,0,0,0,0,0,0,0,0,0,0,0x3f803f80,0,0,0,0,0,0,0,0,0,0,0,0,0,0,0,0x3f803f80,0,0,0,0,0,0,0,0,0,0,0,0,0,0,0,0x3f803f80,0x3f803f80,16256,0,0,0x3f803f80,0x3f800000,0,0,0x3f803f80,0,16256,0,0x3f803f80,0,0x3f800000,0x3f803f80,0,0,0,0,0,0,0,0,0,0,0,0,0,0,0,0x3f803f80,0,0,0,0,0,0,0,0,0,0,0,0,0,0,0,0x3f803f80,0,0,0,0,0,0,0,0,0,0,0,0,0,0,0,0x3f803f80,0,0,0,0,0,0,0,0,0,0,0,0,0,0,0,0x3f803f80,0x3f803f80,16256,0,0,0x3f803f80,0x3f800000,0,0,0x3f803f80,0,16256,0,0x3f803f80,0,0x3f800000,0x3f803f80,0,0,0,0,0,0,0,0,0,0,0,0,0,0,0,0x3f803f80,0,0,0,0,0,0,0,0,0,0,0,0,0,0,0,0x3f803f80,0,0,0,0,0,0,0,0,0,0,0,0,0,0,0,0x3f803f80,0,0,0,0,0,0,0,0,0,0,0,0,0,0,0,0x3f803f80,0x3f803f80,16256,0,0,0x3f803f80,0x3f800000,0,0,0x3f803f80,0,16256,0,0x3f803f80,0,0x3f800000,0,64,0x60000,0xc0080,144,208,0x60010,0xc0090,288,352,0x60020,0xc00a0,432,496,0x60030,0xc00b0,576,512,0x60040,0xc00c0,720,656,0x60050,0xc00d0,864,800,0x60060,0xc00e0,1008,944,0x60070,0xc00f0,1024,1088,0x60190,0xc0210,1168,1232,0x60180,0xc0200,1312,1376,0x601b0,0xc0230,1456,1520,0x601a0,0xc0220,1600,1536,0x601d0,0xc0250,1744,1680,0x601c0,0xc0240,1888,1824,0x601f0,0xc0270,2032,1968,0x601e0,0xc0260,16,80,0x60320,0xc03a0,128,192,0x60330,0xc03b0,304,368,0x60300,0xc0380,416,480,0x60310,0xc0390,592,528,0x60360,0xc03e0,704,640,0x60370,0xc03f0,880,816,0x60340,0xc03c0,992,928,0x60350,0xc03d0,1040,1104,0x604b0,0xef930,1152,1216,0x604a0,0xef920,1328,1392,0x60490,0xef910,1440,1504,0x60480,0xef900,1616,1552,0x604f0,0xef970,1728,1664,0x604e0,0xef960,1904,1840,0x604d0,0xef950,2016,1952,0x604c0,0xef940,32,96,0x60640,0xefac0,176,240,0x60650,0xefad0,256,320,0x60660,0xefae0,400,464,0x60670,0xefaf0,608,544,0x60600,0xefa80,752,688,0x60610,0xefa90,832,768,0x60620,0xefaa0,976,912,0x60630,0xefab0,1056,1120,0x8fbd0,0xefc50,1200,1264,0x8fbc0,0xefc40,1280,1344,0x8fbf0,0xefc70,1424,1488,0x8fbe0,0xefc60,1632,1568,0x8fb90,0xefc10,1776,1712,0x8fb80,0xefc00,1856,1792,0x8fbb0,0xefc30,2000,1936,0x8fba0,0xefc20,48,112,0x8fd60,0xefde0,160,224,0x8fd70,0xefdf0,272,336,0x8fd40,0xefdc0,384,448,0x8fd50,0xefdd0,624,560,0x8fd20,0xefda0,736,672,0x8fd30,0xefdb0,848,784,0x8fd00,0xefd80,960,896,0x8fd10,0xefd90,1072,1136,0x8fef0,0xeff70,1184,1248,0x8fee0,0xeff60,1296,1360,0x8fed0,0xeff50,1408,1472,0x8fec0,0xeff40,1648,1584,0x8feb0,0xeff30,1760,1696,0x8fea0,0xeff20,1872,1808,0x8fe90,0xeff10,1984,1920,0x8fe80,0xeff00,0xf0100,75776,83968,91136,0xf0110,75792,83984,91152,0xf0120,75808,84000,91168,0xf0130,75824,84016,91184,0xf0140,75840,84032,91200,0xf0150,75856,84048,91216,0xf0160,75872,84064,91232,0xf0170,75888,84080,91248,0xf0290,75904,84096,91264,0xf0280,75920,84112,91280,0xf02b0,75936,84128,91296,0xf02a0,75952,84144,91312,0xf02d0,75968,84160,91328,0xf02c0,75984,84176,91344,0xf02f0,76000,84192,91360,0xf02e0,76016,84208,91376,0x11f820,76032,84224,91392,0x11f830,76048,84240,91408,0x11f800,76064,84256,91424,0x11f810,76080,84272,91440,0x11f860,76096,84288,91456,0x11f870,76112,84304,91472,0x11f840,76128,84320,91488,0x11f850,76144,84336,91504,0x11f9b0,76160,84352,91520,0x11f9a0,76176,84368,91536,0x11f990,76192,84384,91552,0x11f980,76208,84400,91568,0x11f9f0,76224,84416,91584,0x11f9e0,76240,84432,91600,0x11f9d0,76256,84448,91616,0x11f9c0,76272,84464,91632,0xf0100,76288,84480,91648,0xf0110,76304,84496,91664,0xf0120,76320,84512,91680,0xf0130,76336,84528,91696,0xf0140,76352,84544,91712,0xf0150,76368,84560,91728,0xf0160,76384,84576,91744,0xf0170,76400,84592,91760,0xf0290,76416,84608,91776,0xf0280,76432,84624,91792,0xf02b0,76448,84640,91808,0xf02a0,76464,84656,91824,0xf02d0,76480,84672,91840,0xf02c0,76496,84688,91856,0xf02f0,76512,84704,91872,0xf02e0,76528,84720,91888,0x11f820,76544,84736,91904,0x11f830,76560,84752,91920,0x11f800,76576,84768,91936,0x11f810,76592,84784,91952,0x11f860,76608,84800,91968,0x11f870,76624,84816,91984,0x11f840,76640,84832,92000,0x11f850,76656,84848,92016,0x11f9b0,76672,84864,92032,0x11f9a0,76688,84880,92048,0x11f990,76704,84896,92064,0x11f980,76720,84912,92080,0x11f9f0,76736,84928,92096,0x11f9e0,76752,84944,92112,0x11f9d0,76768,84960,92128,0x11f9c0,76784,84976,92144,0x1c140,36864,43008,52224,0x1c144,36880,43024,52240,0x1c148,36896,43040,52256,0x1c14c,36912,43056,52272,0x1c150,36928,43072,52288,0x1c154,36944,43088,52304,0x1c158,36960,43104,52320,0x1c15c,36976,43120,52336,0x1c160,36992,43136,52352,0x1c164,37008,43152,52368,0x1c168,37024,43168,52384,0x1c16c,37040,43184,52400,0x1c170,37056,43200,52416,0x1c174,37072,43216,52432,0x1c178,37088,43232,52448,0x1c17c,37104,43248,52464,0x1c180,37120,43264,52480,0x1c184,37136,43280,52496,0x1c188,37152,43296,52512,0x1c18c,37168,43312,52528,0x1c190,37184,43328,52544,0x1c194,37200,43344,52560,0x1c198,37216,43360,52576,0x1c19c,37232,43376,52592,0x1c1a0,37248,43392,52608,0x1c1a4,37264,43408,52624,0x1c1a8,37280,43424,52640,0x1c1ac,37296,43440,52656,0x1c1b0,37312,43456,52672,0x1c1b4,37328,43472,52688,0x1c1b8,37344,43488,52704,0x1c1bc,37360,43504,52720,0x1c1c0,37376,43520,52736,0x1c1c4,37392,43536,52752,0x1c1c8,37408,43552,52768,0x1c1cc,37424,43568,52784,0x1c1d0,37440,43584,52800,0x1c1d4,37456,43600,52816,0x1c1d8,37472,43616,52832,0x1c1dc,37488,43632,52848,0x1c1e0,37504,43648,52864,0x1c1e4,37520,43664,52880,0x1c1e8,37536,43680,52896,0x1c1ec,37552,43696,52912,0x1c1f0,37568,43712,52928,0x1c1f4,37584,43728,52944,0x1c1f8,37600,43744,52960,0x1c1fc,37616,43760,52976,0x1c200,37632,43776,52992,0x1c204,37648,43792,53008,0x1c208,37664,43808,53024,0x1c20c,37680,43824,53040,0x1c210,37696,43840,53056,0x1c214,37712,43856,53072,0x1c218,37728,43872,53088,0x1c21c,37744,43888,53104,0x1c220,37760,43904,53120,0x1c224,37776,43920,53136,0x1c228,37792,43936,53152,0x1c22c,37808,43952,53168,0x1c230,37824,43968,53184,0x1c234,37840,43984,53200,0x1c238,37856,44000,53216,0x1c23c,37872,44016,53232,79936,86016,0x1d600,0x1d608,80080,86160,0x1d610,0x1d618,80224,86304,0x1d620,0x1d628,80368,86448,0x1d630,0x1d638,80384,86592,0x1d640,0x1d648,80528,86736,0x1d650,0x1d658,80672,86880,0x1d660,0x1d668,80816,87024,0x1d670,0x1d678,80960,87040,0x1d680,0x1d688,81104,87184,0x1d690,0x1d698,81248,87328,0x1d6a0,0x1d6a8,81392,87472,0x1d6b0,0x1d6b8,81408,87616,0x1d6c0,0x1d6c8,81552,87760,0x1d6d0,0x1d6d8,81696,87904,0x1d6e0,0x1d6e8,81840,88048,0x1d6f0,0x1d6f8,79952,86024,0x1d700,0x1d708,80064,86168,0x1d710,0x1d718,80240,86312,0x1d720,0x1d728,80352,86456,0x1d730,0x1d738,80400,86600,0x1d740,0x1d748,80512,86744,0x1d750,0x1d758,80688,86888,0x1d760,0x1d768,80800,87032,0x1d770,0x1d778,80976,87048,0x1d780,0x1d788,81088,87192,0x1d790,0x1d798,81264,87336,0x1d7a0,0x1d7a8,81376,87480,0x1d7b0,0x1d7b8,81424,87624,0x1d7c0,0x1d7c8,81536,87768,0x1d7d0,0x1d7d8,81712,87912,0x1d7e0,0x1d7e8,81824,88056,0x1d7f0,0x1d7f8,79968,86032,0x1d800,0x1d808,80112,86144,0x1d810,0x1d818,80192,86320,0x1d820,0x1d828,80336,86432,0x1d830,0x1d838,80416,86608,0x1d840,0x1d848,80560,86720,0x1d850,0x1d858,80640,86896,0x1d860,0x1d868,80784,87008,0x1d870,0x1d878,80992,87056,0x1d880,0x1d888,81136,87168,0x1d890,0x1d898,81216,87344,0x1d8a0,0x1d8a8,81360,87456,0x1d8b0,0x1d8b8,81440,87632,0x1d8c0,0x1d8c8,81584,87744,0x1d8d0,0x1d8d8,81664,87920,0x1d8e0,0x1d8e8,81808,88032,0x1d8f0,0x1d8f8,79984,86040,0x1d900,0x1d908,80096,86152,0x1d910,0x1d918,80208,86328,0x1d920,0x1d928,80320,86440,0x1d930,0x1d938,80432,86616,0x1d940,0x1d948,80544,86728,0x1d950,0x1d958,80656,86904,0x1d960,0x1d968,80768,87016,0x1d970,0x1d978,81008,87064,0x1d980,0x1d988,81120,87176,0x1d990,0x1d998,81232,87352,0x1d9a0,0x1d9a8,81344,87464,0x1d9b0,0x1d9b8,81456,87640,0x1d9c0,0x1d9c8,81568,87752,0x1d9d0,0x1d9d8,81680,87928,0x1d9e0,0x1d9e8,81792,88040,0x1d9f0,0x1d9f8,0x1e700,0x1e700,0x1e980,0x100000,0x1e702,0x100000,0x100000,0x1e980,0x1e704,0x100000,0x100000,0x100000,0x1e706,0x100000,0x100000,0x100000,0x1e710,0x1e700,0x100000,0x100000,0x1e712,0x100000,0x100000,0x1e980,0x1e714,0x100000,0x100000,0x100000,0x1e716,0x100000,0x100000,0x100000,0x1e720,0x1e700,0x100000,0x100000,0x1e722,0x100000,0x100000,0x1e980,0x1e724,0x100000,0x100000,0x100000,0x1e726,0x100000,0x100000,0x100000,0x1e730,0x1e700,0x100000,0x100000,0x1e732,0x100000,0x100000,0x1e980,0x1e734,0x100000,0x100000,0x100000,0x1e736,0x100000,0x100000,0x100000,0x1e708,0x1e710,0x1e9b0,0x100000,0x1e70a,0x100000,0x100000,0x1e9b0,0x1e70c,0x100000,0x100000,0x100000,0x1e70e,0x100000,0x100000,0x100000,0x1e718,0x1e710,0x100000,0x100000,0x1e71a,0x100000,0x100000,0x1e9b0,0x1e71c,0x100000,0x100000,0x100000,0x1e71e,0x100000,0x100000,0x100000,0x1e728,0x1e710,0x100000,0x100000,0x1e72a,0x100000,0x100000,0x1e9b0,0x1e72c,0x100000,0x100000,0x100000,0x1e72e,0x100000,0x100000,0x100000,0x1e738,0x1e710,0x100000,0x100000,0x1e73a,0x100000,0x100000,0x1e9b0,0x1e73c,0x100000,0x100000,0x100000,0x1e73e,0x100000,0x100000,0x100000,0x1e740,0x1e720,0x1e9e0,0x100000,0x1e742,0x100000,0x100000,0x1e9e0,0x1e744,0x100000,0x100000,0x100000,0x1e746,0x100000,0x100000,0x100000,0x1e750,0x1e720,0x100000,0x100000,0x1e752,0x100000,0x100000,0x1e9e0,0x1e754,0x100000,0x100000,0x100000,0x1e756,0x100000,0x100000,0x100000,0x1e760,0x1e720,0x100000,0x100000,0x1e762,0x100000,0x100000,0x1e9e0,0x1e764,0x100000,0x100000,0x100000,0x1e766,0x100000,0x100000,0x100000,0x1e770,0x1e720,0x100000,0x100000,0x1e772,0x100000,0x100000,0x1e9e0,0x1e774,0x100000,0x100000,0x100000,0x1e776,0x100000,0x100000,0x100000,0x1e748,0x1e730,0x1ea10,0x100000,0x1e74a,0x100000,0x100000,0x1ea10,0x1e74c,0x100000,0x100000,0x100000,0x1e74e,0x100000,0x100000,0x100000,0x1e758,0x1e730,0x100000,0x100000,0x1e75a,0x100000,0x100000,0x1ea10,0x1e75c,0x100000,0x100000,0x100000,0x1e75e,0x100000,0x100000,0x100000,0x1e768,0x1e730,0x100000,0x100000,0x1e76a,0x100000,0x100000,0x1ea10,0x1e76c,0x100000,0x100000,0x100000,0x1e76e,0x100000,0x100000,0x100000,0x1e778,0x1e730,0x100000,0x100000,0x1e77a,0x100000,0x100000,0x1ea10,0x1e77c,0x100000,0x100000,0x100000,0x1e77e,0x100000,0x100000,0x100000,0x1ebc0,0x25770,37056,37120,0x1ebc2,0x25772,37072,37128,0x1ebc4,0x25774,37088,37136,0x1ebc6,0x25776,37104,37144,0x1ebc8,0x258d0,37440,37504,0x1ebca,0x258d2,37456,37512,0x1ebcc,0x258d4,37472,37520,0x1ebce,0x258d6,37488,37528,0x1ebd0,0x25a30,37856,37920,0x1ebd2,0x25a32,37872,37928,0x1ebd4,0x25a34,37824,37936,0x1ebd6,0x25a36,37840,37944,0x1ebd8,0x25b90,38240,38304,0x1ebda,0x25b92,38256,38312,0x1ebdc,0x25b94,38208,38320,0x1ebde,0x25b96,38224,38328,0x1ebc0,0x25778,38528,38720,0x1ebc2,0x2577a,38544,38728,0x1ebc4,0x2577c,38560,38736,0x1ebc6,0x2577e,38576,38744,0x1ebc8,0x258d8,38912,39104,0x1ebca,0x258da,38928,39112,0x1ebcc,0x258dc,38944,39120,0x1ebce,0x258de,38960,39128,0x1ebd0,0x25a38,39328,39520,0x1ebd2,0x25a3a,39344,39528,0x1ebd4,0x25a3c,39296,39536,0x1ebd6,0x25a3e,39312,39544,0x1ebd8,0x25b98,39712,39904,0x1ebda,0x25b9a,39728,39912,0x1ebdc,0x25b9c,39680,39920,0x1ebde,0x25b9e,39696,39928,0x1ebc0,0x267f0,40128,40192,0x1ebc2,0x267f2,40144,40200,0x1ebc4,0x267f4,40160,40208,0x1ebc6,0x267f6,40176,40216,0x1ebc8,0x26950,40512,40576,0x1ebca,0x26952,40528,40584,0x1ebcc,0x26954,40544,40592,0x1ebce,0x26956,40560,40600,0x1ebd0,0x26ab0,40928,40992,0x1ebd2,0x26ab2,40944,41000,0x1ebd4,0x26ab4,40896,41008,0x1ebd6,0x26ab6,40912,41016,0x1ebd8,0x26c10,41312,41376,0x1ebda,0x26c12,41328,41384,0x1ebdc,0x26c14,41280,41392,0x1ebde,0x26c16,41296,41400,0x1ebc0,0x100000,41600,41792,0x1ebc2,0x100000,41616,41800,0x1ebc4,0x100000,41632,41808,0x1ebc6,0x100000,41648,41816,0x1ebc8,0x100000,41984,42176,0x1ebca,0x100000,42000,42184,0x1ebcc,0x100000,42016,42192,0x1ebce,0x100000,42032,42200,0x1ebd0,0x100000,42400,42592,0x1ebd2,0x100000,42416,42600,0x1ebd4,0x100000,42368,42608,0x1ebd6,0x100000,42384,42616,0x1ebd8,0x100000,42784,42976,0x1ebda,0x100000,42800,42984,0x1ebdc,0x100000,42752,42992,0x1ebde,0x100000,42768,43000,37056,37088,37120,55296,37064,37096,37128,55312,37072,37104,37136,55328,37080,37112,37144,55344,38208,38240,38272,55360,38216,38248,38280,55376,38224,38256,38288,55392,38232,38264,38296,55408,39392,39360,39456,55424,39400,39368,39464,55440,39408,39376,39472,55456,39416,39384,39480,55472,40544,40512,40608,55488,40552,40520,40616,55504,40560,40528,40624,55520,40568,40536,40632,55536,46208,46240,46400,55552,46216,46248,46408,55568,46224,46256,46416,55584,46232,46264,46424,55600,47360,47392,47552,55616,47368,47400,47560,55632,47376,47408,47568,55648,47384,47416,47576,55664,48544,48512,48736,55680,48552,48520,48744,55696,48560,48528,48752,55712,48568,48536,48760,55728,49696,49664,49888,55744,49704,49672,49896,55760,49712,49680,49904,55776,49720,49688,49912,55792,55488,55520,55552,55808,55496,55528,55560,55824,55504,55536,55568,55840,55512,55544,55576,55856,56640,56672,56704,55872,56648,56680,56712,55888,56656,56688,56720,55904,56664,56696,56728,55920,57824,57792,57888,55936,57832,57800,57896,55952,57840,57808,57904,55968,57848,57816,57912,55984,58976,58944,59040,56000,58984,58952,59048,56016,58992,58960,59056,56032,59000,58968,59064,56048,64640,64672,64832,56064,64648,64680,64840,56080,64656,64688,64848,56096,64664,64696,64856,56112,65792,65824,65984,56128,65800,65832,65992,56144,65808,65840,66000,56160,65816,65848,66008,56176,66976,66944,67168,56192,66984,66952,67176,56208,66992,66960,67184,56224,67000,66968,67192,56240,68128,68096,68320,56256,68136,68104,68328,56272,68144,68112,68336,56288,68152,68120,68344,56304,0x1e800,0x1e800,0x20cc0,36864,0x1e8c4,0x1e8c4,0x20cc4,36880,0x1e988,0x1e988,0x20cc8,36896,0x1ea4c,0x1ea4c,0x20ccc,36912,0x1eb10,0x1eb10,0x20cd0,36928,0x1ebd4,0x1ebd4,0x20cd4,36944,0x1ec98,0x1ec98,0x20cd8,36960,0x1ed5c,0x1ed5c,0x20cdc,36976,0x1ee20,0x1ee20,0x20ce0,37248,0x1eee4,0x1eee4,0x20ce4,37264,0x1efa8,0x1efa8,0x20ce8,37280,0x1f06c,0x1f06c,0x20cec,37296,0x1f130,0x1f130,0x20cf0,37312,0x1f1f4,0x1f1f4,0x20cf4,37328,0x1f2b8,0x1f2b8,0x20cf8,37344,0x1f37c,0x1f37c,0x20cfc,37360,0x1e810,0x1e800,0x20cc0,37632,0x1e8d4,0x1e8c4,0x20cc4,37648,0x1e998,0x1e988,0x20cc8,37664,0x1ea5c,0x1ea4c,0x20ccc,37680,0x1eb20,0x1eb10,0x20cd0,37696,0x1ebe4,0x1ebd4,0x20cd4,37712,0x1eca8,0x1ec98,0x20cd8,37728,0x1ed6c,0x1ed5c,0x20cdc,37744,0x1ee30,0x1ee20,0x20ce0,38016,0x1eef4,0x1eee4,0x20ce4,38032,0x1efb8,0x1efa8,0x20ce8,38048,0x1f07c,0x1f06c,0x20cec,38064,0x1f140,0x1f130,0x20cf0,38080,0x1f204,0x1f1f4,0x20cf4,38096,0x1f2c8,0x1f2b8,0x20cf8,38112,0x1f38c,0x1f37c,0x20cfc,38128,0x1e820,0x1e800,0x20cc0,38400,0x1e8e4,0x1e8c4,0x20cc4,38416,0x1e9a8,0x1e988,0x20cc8,38432,0x1ea6c,0x1ea4c,0x20ccc,38448,0x1eb30,0x1eb10,0x20cd0,38464,0x1ebf4,0x1ebd4,0x20cd4,38480,0x1ecb8,0x1ec98,0x20cd8,38496,0x1ed7c,0x1ed5c,0x20cdc,38512,0x1ee40,0x1ee20,0x20ce0,38784,0x1ef04,0x1eee4,0x20ce4,38800,0x1efc8,0x1efa8,0x20ce8,38816,0x1f08c,0x1f06c,0x20cec,38832,0x1f150,0x1f130,0x20cf0,38848,0x1f214,0x1f1f4,0x20cf4,38864,0x1f2d8,0x1f2b8,0x20cf8,38880,0x1f39c,0x1f37c,0x20cfc,38896,0x1e830,0x1e800,0x20cc0,39168,0x1e8f4,0x1e8c4,0x20cc4,39184,0x1e9b8,0x1e988,0x20cc8,39200,0x1ea7c,0x1ea4c,0x20ccc,39216,0x1eb40,0x1eb10,0x20cd0,39232,0x1ec04,0x1ebd4,0x20cd4,39248,0x1ecc8,0x1ec98,0x20cd8,39264,0x1ed8c,0x1ed5c,0x20cdc,39280,0x1ee50,0x1ee20,0x20ce0,39552,0x1ef14,0x1eee4,0x20ce4,39568,0x1efd8,0x1efa8,0x20ce8,39584,0x1f09c,0x1f06c,0x20cec,39600,0x1f160,0x1f130,0x20cf0,39616,0x1f224,0x1f1f4,0x20cf4,39632,0x1f2e8,0x1f2b8,0x20cf8,39648,0x1f3ac,0x1f37c,0x20cfc,39664,73728,0x26f70,0x1e980,0x1ebc0,73744,0x26f74,0x1ee10,0x1f050,73760,0x26f78,0x1f2a0,0x1f4e0,73776,0x26f7c,0x1f730,0x1f970,73792,0x26f80,0x1fbc0,0x1fe00,73808,0x26f84,0x20050,0x20290,73824,0x26f88,0x204e0,0x20720,73840,0x26f8c,0x20970,0x20bb0,74112,0x26f90,0x20e00,0x21040,74128,0x26f94,0x21290,0x214d0,74144,0x26f98,0x21720,0x21960,74160,0x26f9c,0x21bb0,0x21df0,74176,0x26fa0,0x22040,0x22280,74192,0x26fa4,0x224d0,0x22710,74208,0x26fa8,0x22960,0x22ba0,74224,0x26fac,0x22df0,0x23030,74528,0x26fb0,0x1e9b0,0x1ebc8,74544,0x26fb4,0x1ee40,0x1f058,74496,0x26fb8,0x1f2d0,0x1f4e8,74512,0x26fbc,0x1f760,0x1f978,74592,0x26fc0,0x1fbf0,0x1fe08,74608,0x26fc4,0x20080,0x20298,74560,0x26fc8,0x20510,0x20728,74576,0x26fcc,0x209a0,0x20bb8,74912,0x26fd0,0x20e30,0x21048,74928,0x26fd4,0x212c0,0x214d8,74880,0x26fd8,0x21750,0x21968,74896,0x26fdc,0x21be0,0x21df8,74976,0x26fe0,0x22070,0x22288,74992,0x26fe4,0x22500,0x22718,74944,0x26fe8,0x22990,0x22ba8,74960,0x26fec,0x22e20,0x23038,75328,0x26ff0,0x1e9e0,0x1ebd0,75344,0x26ff4,0x1ee70,0x1f060,75360,0x26ff8,0x1f300,0x1f4f0,75376,0x26ffc,0x1f790,0x1f980,75264,0x27000,0x1fc20,0x1fe10,75280,0x27004,0x200b0,0x202a0,75296,0x27008,0x20540,0x20730,75312,0x2700c,0x209d0,0x20bc0,75712,0x27010,0x20e60,0x21050,75728,0x27014,0x212f0,0x214e0,75744,0x27018,0x21780,0x21970,75760,0x2701c,0x21c10,0x21e00,75648,0x27020,0x220a0,0x22290,75664,0x27024,0x22530,0x22720,75680,0x27028,0x229c0,0x22bb0,75696,0x2702c,0x22e50,0x23040,76128,0x27030,0x1ea10,0x1ebd8,76144,0x27034,0x1eea0,0x1f068,76096,0x27038,0x1f330,0x1f4f8,76112,0x2703c,0x1f7c0,0x1f988,76064,0x27040,0x1fc50,0x1fe18,76080,0x27044,0x200e0,0x202a8,76032,0x27048,0x20570,0x20738,76048,0x2704c,0x20a00,0x20bc8,76512,0x27050,0x20e90,0x21058,76528,0x27054,0x21320,0x214e8,76480,0x27058,0x217b0,0x21978,76496,0x2705c,0x21c40,0x21e08,76448,0x27060,0x220d0,0x22298,76464,0x27064,0x22560,0x22728,76416,0x27068,0x229f0,0x22bb8,76432,0x2706c,0x22e80,0x23048,0x3f803f80,0x3f803f80,16256,0,0,0x3f803f80,0x3f800000,0,0,0x3f803f80,0,16256,0,0x3f803f80,0,0x3f800000,0x3f803f80,0,0,0,0,0,0,0,0,0,0,0,0,0,0,0,0x3f803f80,0,0,0,0,0,0,0,0,0,0,0,0,0,0,0,0x3f803f80,0,0,0,0,0,0,0,0,0,0,0,0,0,0,0,0x3f803f80,0,0,0,0,0,0,0,0,0,0,0,0,0,0,0,0x3f803f80,0x3f803f80,16256,0,0,0x3f803f80,0x3f800000,0,0,0x3f803f80,0,16256,0,0x3f803f80,0,0x3f800000,0x3f803f80,0,0,0,0,0,0,0,0,0,0,0,0,0,0,0,0x3f803f80,0,0,0,0,0,0,0,0,0,0,0,0,0,0,0,0x3f803f80,0,0,0,0,0,0,0,0,0,0,0,0,0,0,0,0x3f803f80,0,0,0,0,0,0,0,0,0,0,0,0,0,0,0,0x3f803f80,0x3f803f80,16256,0,0,0x3f803f80,0x3f800000,0,0,0x3f803f80,0,16256,0,0x3f803f80,0,0x3f800000,0x3f803f80,0,0,0,0,0,0,0,0,0,0,0,0,0,0,0,0x3f803f80,0,0,0,0,0,0,0,0,0,0,0,0,0,0,0,0x3f803f80,0,0,0,0,0,0,0,0,0,0,0,0,0,0,0,0x3f803f80,0,0,0,0,0,0,0,0,0,0,0,0,0,0,0,0x3f803f80,0x3f803f80,16256,0,0,0x3f803f80,0x3f800000,0,0,0x3f803f80,0,16256,0,0x3f803f80,0,0x3f800000,0,64,0x90000,0xf0080,144,208,0x90010,0xf0090,288,352,0x90020,0xf00a0,432,496,0x90030,0xf00b0,576,512,0x90040,0xf00c0,720,656,0x90050,0xf00d0,864,800,0x90060,0xf00e0,1008,944,0x90070,0xf00f0,1024,1088,0x90190,0xf0210,1168,1232,0x90180,0xf0200,1312,1376,0x901b0,0xf0230,1456,1520,0x901a0,0xf0220,1600,1536,0x901d0,0xf0250,1744,1680,0x901c0,0xf0240,1888,1824,0x901f0,0xf0270,2032,1968,0x901e0,0xf0260,16,80,0x90320,0x11f7a0,128,192,0x90330,0x11f7b0,304,368,0x90300,0x11f780,416,480,0x90310,0x11f790,592,528,0x90360,0x11f7e0,704,640,0x90370,0x11f7f0,880,816,0x90340,0x11f7c0,992,928,0x90350,0x11f7d0,1040,1104,0x904b0,0x11f930,1152,1216,0x904a0,0x11f920,1328,1392,0x90490,0x11f910,1440,1504,0x90480,0x11f900,1616,1552,0x904f0,0x11f970,1728,1664,0x904e0,0x11f960,1904,1840,0x904d0,0x11f950,2016,1952,0x904c0,0x11f940,32,96,0xbfa40,0x11fac0,176,240,0xbfa50,0x11fad0,256,320,0xbfa60,0x11fae0,400,464,0xbfa70,0x11faf0,608,544,0xbfa00,0x11fa80,752,688,0xbfa10,0x11fa90,832,768,0xbfa20,0x11faa0,976,912,0xbfa30,0x11fab0,1056,1120,0xbfbd0,0x11fc50,1200,1264,0xbfbc0,0x11fc40,1280,1344,0xbfbf0,0x11fc70,1424,1488,0xbfbe0,0x11fc60,1632,1568,0xbfb90,0x11fc10,1776,1712,0xbfb80,0x11fc00,1856,1792,0xbfbb0,0x11fc30,2000,1936,0xbfba0,0x11fc20,48,112,0xbfd60,0x11fde0,160,224,0xbfd70,0x11fdf0,272,336,0xbfd40,0x11fdc0,384,448,0xbfd50,0x11fdd0,624,560,0xbfd20,0x11fda0,736,672,0xbfd30,0x11fdb0,848,784,0xbfd00,0x11fd80,960,896,0xbfd10,0x11fd90,1072,1136,0xbfef0,0x11ff70,1184,1248,0xbfee0,0x11ff60,1296,1360,0xbfed0,0x11ff50,1408,1472,0xbfec0,0x11ff40,1648,1584,0xbfeb0,0x11ff30,1760,1696,0xbfea0,0x11ff20,1872,1808,0xbfe90,0x11ff10,1984,1920,0xbfe80,0x11ff00,0x11fb40,76800,84992,91648,0x11fb50,76816,85008,91664,0x11fb60,76832,85024,91680,0x11fb70,76848,85040,91696,0x11fb00,76864,85056,91712,0x11fb10,76880,85072,91728,0x11fb20,76896,85088,91744,0x11fb30,76912,85104,91760,0x11fcd0,76928,85120,91776,0x11fcc0,76944,85136,91792,0x11fcf0,76960,85152,91808,0x11fce0,76976,85168,91824,0x11fc90,76992,85184,91840,0x11fc80,77008,85200,91856,0x11fcb0,77024,85216,91872,0x11fca0,77040,85232,91888,0x11fe60,77056,85248,91904,0x11fe70,77072,85264,91920,0x11fe40,77088,85280,91936,0x11fe50,77104,85296,91952,0x11fe20,77120,85312,91968,0x11fe30,77136,85328,91984,0x11fe00,77152,85344,92000,0x11fe10,77168,85360,92016,0x11fff0,77184,85376,92032,0x11ffe0,77200,85392,92048,0x11ffd0,77216,85408,92064,0x11ffc0,77232,85424,92080,0x11ffb0,77248,85440,92096,0x11ffa0,77264,85456,92112,0x11ff90,77280,85472,92128,0x11ff80,77296,85488,92144,0x11fb40,77312,85504,92160,0x11fb50,77328,85520,92176,0x11fb60,77344,85536,92192,0x11fb70,77360,85552,92208,0x11fb00,77376,85568,92224,0x11fb10,77392,85584,92240,0x11fb20,77408,85600,92256,0x11fb30,77424,85616,92272,0x11fcd0,77440,85632,92288,0x11fcc0,77456,85648,92304,0x11fcf0,77472,85664,92320,0x11fce0,77488,85680,92336,0x11fc90,77504,85696,92352,0x11fc80,77520,85712,92368,0x11fcb0,77536,85728,92384,0x11fca0,77552,85744,92400,0x11fe60,77568,85760,92416,0x11fe70,77584,85776,92432,0x11fe40,77600,85792,92448,0x11fe50,77616,85808,92464,0x11fe20,77632,85824,92480,0x11fe30,77648,85840,92496,0x11fe00,77664,85856,92512,0x11fe10,77680,85872,92528,0x11fff0,77696,85888,92544,0x11ffe0,77712,85904,92560,0x11ffd0,77728,85920,92576,0x11ffc0,77744,85936,92592,0x11ffb0,77760,85952,92608,0x11ffa0,77776,85968,92624,0x11ff90,77792,85984,92640,0x11ff80,77808,86000,92656,0x1c1d0,36864,46080,52232,0x1c1d4,36880,46096,52248,0x1c1d8,36896,46112,52264,0x1c1dc,36912,46128,52280,0x1c1e0,36928,46144,52296,0x1c1e4,36944,46160,52312,0x1c1e8,36960,46176,52328,0x1c1ec,36976,46192,52344,0x1c1f0,36992,46208,52360,0x1c1f4,37008,46224,52376,0x1c1f8,37024,46240,52392,0x1c1fc,37040,46256,52408,0x1c200,37056,46272,52424,0x1c204,37072,46288,52440,0x1c208,37088,46304,52456,0x1c20c,37104,46320,52472,0x1c210,37120,46336,52488,0x1c214,37136,46352,52504,0x1c218,37152,46368,52520,0x1c21c,37168,46384,52536,0x1c220,37184,46400,52552,0x1c224,37200,46416,52568,0x1c228,37216,46432,52584,0x1c22c,37232,46448,52600,0x1c230,37248,46464,52616,0x1c234,37264,46480,52632,0x1c238,37280,46496,52648,0x1c23c,37296,46512,52664,0x1c240,37312,46528,52680,0x1c244,37328,46544,52696,0x1c248,37344,46560,52712,0x1c24c,37360,46576,52728,0x1c250,37376,46592,52744,0x1c254,37392,46608,52760,0x1c258,37408,46624,52776,0x1c25c,37424,46640,52792,0x1c260,37440,46656,52808,0x1c264,37456,46672,52824,0x1c268,37472,46688,52840,0x1c26c,37488,46704,52856,0x1c270,37504,46720,52872,0x1c274,37520,46736,52888,0x1c278,37536,46752,52904,0x1c27c,37552,46768,52920,0x1c280,37568,46784,52936,0x1c284,37584,46800,52952,0x1c288,37600,46816,52968,0x1c28c,37616,46832,52984,0x1c290,37632,46848,53000,0x1c294,37648,46864,53016,0x1c298,37664,46880,53032,0x1c29c,37680,46896,53048,0x1c2a0,37696,46912,53064,0x1c2a4,37712,46928,53080,0x1c2a8,37728,46944,53096,0x1c2ac,37744,46960,53112,0x1c2b0,37760,46976,53128,0x1c2b4,37776,46992,53144,0x1c2b8,37792,47008,53160,0x1c2bc,37808,47024,53176,0x1c2c0,37824,47040,53192,0x1c2c4,37840,47056,53208,0x1c2c8,37856,47072,53224,0x1c2cc,37872,47088,53240,86048,86112,0x1d600,0x1d60c,86192,86256,0x1d610,0x1d61c,86272,86336,0x1d620,0x1d62c,86416,86480,0x1d630,0x1d63c,86624,86560,0x1d640,0x1d64c,86768,86704,0x1d650,0x1d65c,86848,86784,0x1d660,0x1d66c,86992,86928,0x1d670,0x1d67c,87072,87136,0x1d680,0x1d68c,87216,87280,0x1d690,0x1d69c,87296,87360,0x1d6a0,0x1d6ac,87440,87504,0x1d6b0,0x1d6bc,87648,87584,0x1d6c0,0x1d6cc,87792,87728,0x1d6d0,0x1d6dc,87872,87808,0x1d6e0,0x1d6ec,88016,87952,0x1d6f0,0x1d6fc,86064,86120,0x1d700,0x1d70c,86176,86264,0x1d710,0x1d71c,86288,86344,0x1d720,0x1d72c,86400,86488,0x1d730,0x1d73c,86640,86568,0x1d740,0x1d74c,86752,86712,0x1d750,0x1d75c,86864,86792,0x1d760,0x1d76c,86976,86936,0x1d770,0x1d77c,87088,87144,0x1d780,0x1d78c,87200,87288,0x1d790,0x1d79c,87312,87368,0x1d7a0,0x1d7ac,87424,87512,0x1d7b0,0x1d7bc,87664,87592,0x1d7c0,0x1d7cc,87776,87736,0x1d7d0,0x1d7dc,87888,87816,0x1d7e0,0x1d7ec,88000,87960,0x1d7f0,0x1d7fc,86080,86128,0x1d800,0x1d80c,86224,86240,0x1d810,0x1d81c,86368,86352,0x1d820,0x1d82c,86512,86464,0x1d830,0x1d83c,86528,86576,0x1d840,0x1d84c,86672,86688,0x1d850,0x1d85c,86816,86800,0x1d860,0x1d86c,86960,86912,0x1d870,0x1d87c,87104,87152,0x1d880,0x1d88c,87248,87264,0x1d890,0x1d89c,87392,87376,0x1d8a0,0x1d8ac,87536,87488,0x1d8b0,0x1d8bc,87552,87600,0x1d8c0,0x1d8cc,87696,87712,0x1d8d0,0x1d8dc,87840,87824,0x1d8e0,0x1d8ec,87984,87936,0x1d8f0,0x1d8fc,86096,86136,0x1d900,0x1d90c,86208,86248,0x1d910,0x1d91c,86384,86360,0x1d920,0x1d92c,86496,86472,0x1d930,0x1d93c,86544,86584,0x1d940,0x1d94c,86656,86696,0x1d950,0x1d95c,86832,86808,0x1d960,0x1d96c,86944,86920,0x1d970,0x1d97c,87120,87160,0x1d980,0x1d98c,87232,87272,0x1d990,0x1d99c,87408,87384,0x1d9a0,0x1d9ac,87520,87496,0x1d9b0,0x1d9bc,87568,87608,0x1d9c0,0x1d9cc,87680,87720,0x1d9d0,0x1d9dc,87856,87832,0x1d9e0,0x1d9ec,87968,87944,0x1d9f0,0x1d9fc,0x1e780,0x1e780,0x1ea40,0x100000,0x1e782,0x100000,0x100000,0x1ea40,0x1e784,0x100000,0x100000,0x100000,0x1e786,0x100000,0x100000,0x100000,0x1e790,0x1e780,0x100000,0x100000,0x1e792,0x100000,0x100000,0x1ea40,0x1e794,0x100000,0x100000,0x100000,0x1e796,0x100000,0x100000,0x100000,0x1e7a0,0x1e780,0x100000,0x100000,0x1e7a2,0x100000,0x100000,0x1ea40,0x1e7a4,0x100000,0x100000,0x100000,0x1e7a6,0x100000,0x100000,0x100000,0x1e7b0,0x1e780,0x100000,0x100000,0x1e7b2,0x100000,0x100000,0x1ea40,0x1e7b4,0x100000,0x100000,0x100000,0x1e7b6,0x100000,0x100000,0x100000,0x1e788,0x1e790,0x1ea70,0x100000,0x1e78a,0x100000,0x100000,0x1ea70,0x1e78c,0x100000,0x100000,0x100000,0x1e78e,0x100000,0x100000,0x100000,0x1e798,0x1e790,0x100000,0x100000,0x1e79a,0x100000,0x100000,0x1ea70,0x1e79c,0x100000,0x100000,0x100000,0x1e79e,0x100000,0x100000,0x100000,0x1e7a8,0x1e790,0x100000,0x100000,0x1e7aa,0x100000,0x100000,0x1ea70,0x1e7ac,0x100000,0x100000,0x100000,0x1e7ae,0x100000,0x100000,0x100000,0x1e7b8,0x1e790,0x100000,0x100000,0x1e7ba,0x100000,0x100000,0x1ea70,0x1e7bc,0x100000,0x100000,0x100000,0x1e7be,0x100000,0x100000,0x100000,0x1e7c0,0x1e7a0,0x1eaa0,0x100000,0x1e7c2,0x100000,0x100000,0x1eaa0,0x1e7c4,0x100000,0x100000,0x100000,0x1e7c6,0x100000,0x100000,0x100000,0x1e7d0,0x1e7a0,0x100000,0x100000,0x1e7d2,0x100000,0x100000,0x1eaa0,0x1e7d4,0x100000,0x100000,0x100000,0x1e7d6,0x100000,0x100000,0x100000,0x1e7e0,0x1e7a0,0x100000,0x100000,0x1e7e2,0x100000,0x100000,0x1eaa0,0x1e7e4,0x100000,0x100000,0x100000,0x1e7e6,0x100000,0x100000,0x100000,0x1e7f0,0x1e7a0,0x100000,0x100000,0x1e7f2,0x100000,0x100000,0x1eaa0,0x1e7f4,0x100000,0x100000,0x100000,0x1e7f6,0x100000,0x100000,0x100000,0x1e7c8,0x1e7b0,0x1ead0,0x100000,0x1e7ca,0x100000,0x100000,0x1ead0,0x1e7cc,0x100000,0x100000,0x100000,0x1e7ce,0x100000,0x100000,0x100000,0x1e7d8,0x1e7b0,0x100000,0x100000,0x1e7da,0x100000,0x100000,0x1ead0,0x1e7dc,0x100000,0x100000,0x100000,0x1e7de,0x100000,0x100000,0x100000,0x1e7e8,0x1e7b0,0x100000,0x100000,0x1e7ea,0x100000,0x100000,0x1ead0,0x1e7ec,0x100000,0x100000,0x100000,0x1e7ee,0x100000,0x100000,0x100000,0x1e7f8,0x1e7b0,0x100000,0x100000,0x1e7fa,0x100000,0x100000,0x1ead0,0x1e7fc,0x100000,0x100000,0x100000,0x1e7fe,0x100000,0x100000,0x100000,0x1ec20,0x25cf0,37152,37216,0x1ec22,0x25cf2,37168,37224,0x1ec24,0x25cf4,37184,37232,0x1ec26,0x25cf6,37200,37240,0x1ec28,0x25e50,37536,37600,0x1ec2a,0x25e52,37552,37608,0x1ec2c,0x25e54,37568,37616,0x1ec2e,0x25e56,37584,37624,0x1ec30,0x25fb0,37888,37952,0x1ec32,0x25fb2,37904,37960,0x1ec34,0x25fb4,37984,37968,0x1ec36,0x25fb6,38000,37976,0x1ec38,0x26110,38272,38336,0x1ec3a,0x26112,38288,38344,0x1ec3c,0x26114,38368,38352,0x1ec3e,0x26116,38384,38360,0x1ec20,0x25cf8,38752,38688,0x1ec22,0x25cfa,38768,38696,0x1ec24,0x25cfc,38656,38704,0x1ec26,0x25cfe,38672,38712,0x1ec28,0x25e58,39136,39072,0x1ec2a,0x25e5a,39152,39080,0x1ec2c,0x25e5c,39040,39088,0x1ec2e,0x25e5e,39056,39096,0x1ec30,0x25fb8,39488,39424,0x1ec32,0x25fba,39504,39432,0x1ec34,0x25fbc,39456,39440,0x1ec36,0x25fbe,39472,39448,0x1ec38,0x26118,39872,39808,0x1ec3a,0x2611a,39888,39816,0x1ec3c,0x2611c,39840,39824,0x1ec3e,0x2611e,39856,39832,0x1ec20,0x267f8,40224,40288,0x1ec22,0x267fa,40240,40296,0x1ec24,0x267fc,40256,40304,0x1ec26,0x267fe,40272,40312,0x1ec28,0x26958,40608,40672,0x1ec2a,0x2695a,40624,40680,0x1ec2c,0x2695c,40640,40688,0x1ec2e,0x2695e,40656,40696,0x1ec30,0x26ab8,40960,41024,0x1ec32,0x26aba,40976,41032,0x1ec34,0x26abc,41056,41040,0x1ec36,0x26abe,41072,41048,0x1ec38,0x26c18,41344,41408,0x1ec3a,0x26c1a,41360,41416,0x1ec3c,0x26c1c,41440,41424,0x1ec3e,0x26c1e,41456,41432,0x1ec20,0x100000,41824,41760,0x1ec22,0x100000,41840,41768,0x1ec24,0x100000,41728,41776,0x1ec26,0x100000,41744,41784,0x1ec28,0x100000,42208,42144,0x1ec2a,0x100000,42224,42152,0x1ec2c,0x100000,42112,42160,0x1ec2e,0x100000,42128,42168,0x1ec30,0x100000,42560,42496,0x1ec32,0x100000,42576,42504,0x1ec34,0x100000,42528,42512,0x1ec36,0x100000,42544,42520,0x1ec38,0x100000,42944,42880,0x1ec3a,0x100000,42960,42888,0x1ec3c,0x100000,42912,42896,0x1ec3e,0x100000,42928,42904,37152,37184,37216,64512,37160,37192,37224,64528,37168,37200,37232,64544,37176,37208,37240,64560,38304,38336,38368,64576,38312,38344,38376,64592,38320,38352,38384,64608,38328,38360,38392,64624,39424,39520,39488,64640,39432,39528,39496,64656,39440,39536,39504,64672,39448,39544,39512,64688,40576,40672,40640,64704,40584,40680,40648,64720,40592,40688,40656,64736,40600,40696,40664,64752,46432,46336,46368,64768,46440,46344,46376,64784,46448,46352,46384,64800,46456,46360,46392,64816,47584,47488,47520,64832,47592,47496,47528,64848,47600,47504,47536,64864,47608,47512,47544,64880,48704,48672,48640,64896,48712,48680,48648,64912,48720,48688,48656,64928,48728,48696,48664,64944,49856,49824,49792,64960,49864,49832,49800,64976,49872,49840,49808,64992,49880,49848,49816,65008,55584,55616,55648,65024,55592,55624,55656,65040,55600,55632,55664,65056,55608,55640,55672,65072,56736,56768,56800,65088,56744,56776,56808,65104,56752,56784,56816,65120,56760,56792,56824,65136,57856,57952,57920,65152,57864,57960,57928,65168,57872,57968,57936,65184,57880,57976,57944,65200,59008,59104,59072,65216,59016,59112,59080,65232,59024,59120,59088,65248,59032,59128,59096,65264,64864,64768,64800,65280,64872,64776,64808,65296,64880,64784,64816,65312,64888,64792,64824,65328,66016,65920,65952,65344,66024,65928,65960,65360,66032,65936,65968,65376,66040,65944,65976,65392,67136,67104,67072,65408,67144,67112,67080,65424,67152,67120,67088,65440,67160,67128,67096,65456,68288,68256,68224,65472,68296,68264,68232,65488,68304,68272,68240,65504,68312,68280,68248,65520,0x1e800,0x1e800,0x20cc0,55296,0x1e8c4,0x1e8c4,0x20cc4,55312,0x1e988,0x1e988,0x20cc8,55328,0x1ea4c,0x1ea4c,0x20ccc,55344,0x1eb10,0x1eb10,0x20cd0,55360,0x1ebd4,0x1ebd4,0x20cd4,55376,0x1ec98,0x1ec98,0x20cd8,55392,0x1ed5c,0x1ed5c,0x20cdc,55408,0x1ee20,0x1ee20,0x20ce0,55680,0x1eee4,0x1eee4,0x20ce4,55696,0x1efa8,0x1efa8,0x20ce8,55712,0x1f06c,0x1f06c,0x20cec,55728,0x1f130,0x1f130,0x20cf0,55744,0x1f1f4,0x1f1f4,0x20cf4,55760,0x1f2b8,0x1f2b8,0x20cf8,55776,0x1f37c,0x1f37c,0x20cfc,55792,0x1e810,0x1e800,0x20cc0,56064,0x1e8d4,0x1e8c4,0x20cc4,56080,0x1e998,0x1e988,0x20cc8,56096,0x1ea5c,0x1ea4c,0x20ccc,56112,0x1eb20,0x1eb10,0x20cd0,56128,0x1ebe4,0x1ebd4,0x20cd4,56144,0x1eca8,0x1ec98,0x20cd8,56160,0x1ed6c,0x1ed5c,0x20cdc,56176,0x1ee30,0x1ee20,0x20ce0,56448,0x1eef4,0x1eee4,0x20ce4,56464,0x1efb8,0x1efa8,0x20ce8,56480,0x1f07c,0x1f06c,0x20cec,56496,0x1f140,0x1f130,0x20cf0,56512,0x1f204,0x1f1f4,0x20cf4,56528,0x1f2c8,0x1f2b8,0x20cf8,56544,0x1f38c,0x1f37c,0x20cfc,56560,0x1e820,0x1e800,0x20cc0,56832,0x1e8e4,0x1e8c4,0x20cc4,56848,0x1e9a8,0x1e988,0x20cc8,56864,0x1ea6c,0x1ea4c,0x20ccc,56880,0x1eb30,0x1eb10,0x20cd0,56896,0x1ebf4,0x1ebd4,0x20cd4,56912,0x1ecb8,0x1ec98,0x20cd8,56928,0x1ed7c,0x1ed5c,0x20cdc,56944,0x1ee40,0x1ee20,0x20ce0,57216,0x1ef04,0x1eee4,0x20ce4,57232,0x1efc8,0x1efa8,0x20ce8,57248,0x1f08c,0x1f06c,0x20cec,57264,0x1f150,0x1f130,0x20cf0,57280,0x1f214,0x1f1f4,0x20cf4,57296,0x1f2d8,0x1f2b8,0x20cf8,57312,0x1f39c,0x1f37c,0x20cfc,57328,0x1e830,0x1e800,0x20cc0,57600,0x1e8f4,0x1e8c4,0x20cc4,57616,0x1e9b8,0x1e988,0x20cc8,57632,0x1ea7c,0x1ea4c,0x20ccc,57648,0x1eb40,0x1eb10,0x20cd0,57664,0x1ec04,0x1ebd4,0x20cd4,57680,0x1ecc8,0x1ec98,0x20cd8,57696,0x1ed8c,0x1ed5c,0x20cdc,57712,0x1ee50,0x1ee20,0x20ce0,57984,0x1ef14,0x1eee4,0x20ce4,58000,0x1efd8,0x1efa8,0x20ce8,58016,0x1f09c,0x1f06c,0x20cec,58032,0x1f160,0x1f130,0x20cf0,58048,0x1f224,0x1f1f4,0x20cf4,58064,0x1f2e8,0x1f2b8,0x20cf8,58080,0x1f3ac,0x1f37c,0x20cfc,58096,92160,0x27070,0x1ea40,0x1ec20,92176,0x27074,0x1eed0,0x1f0b0,92192,0x27078,0x1f360,0x1f540,92208,0x2707c,0x1f7f0,0x1f9d0,92224,0x27080,0x1fc80,0x1fe60,92240,0x27084,0x20110,0x202f0,92256,0x27088,0x205a0,0x20780,92272,0x2708c,0x20a30,0x20c10,92544,0x27090,0x20ec0,0x210a0,92560,0x27094,0x21350,0x21530,92576,0x27098,0x217e0,0x219c0,92592,0x2709c,0x21c70,0x21e50,92608,0x270a0,0x22100,0x222e0,92624,0x270a4,0x22590,0x22770,92640,0x270a8,0x22a20,0x22c00,92656,0x270ac,0x22eb0,0x23090,92960,0x270b0,0x1ea70,0x1ec28,92976,0x270b4,0x1ef00,0x1f0b8,92928,0x270b8,0x1f390,0x1f548,92944,0x270bc,0x1f820,0x1f9d8,93024,0x270c0,0x1fcb0,0x1fe68,93040,0x270c4,0x20140,0x202f8,92992,0x270c8,0x205d0,0x20788,93008,0x270cc,0x20a60,0x20c18,93344,0x270d0,0x20ef0,0x210a8,93360,0x270d4,0x21380,0x21538,93312,0x270d8,0x21810,0x219c8,93328,0x270dc,0x21ca0,0x21e58,93408,0x270e0,0x22130,0x222e8,93424,0x270e4,0x225c0,0x22778,93376,0x270e8,0x22a50,0x22c08,93392,0x270ec,0x22ee0,0x23098,93760,0x270f0,0x1eaa0,0x1ec30,93776,0x270f4,0x1ef30,0x1f0c0,93792,0x270f8,0x1f3c0,0x1f550,93808,0x270fc,0x1f850,0x1f9e0,93696,0x27100,0x1fce0,0x1fe70,93712,0x27104,0x20170,0x20300,93728,0x27108,0x20600,0x20790,93744,0x2710c,0x20a90,0x20c20,94144,0x27110,0x20f20,0x210b0,94160,0x27114,0x213b0,0x21540,94176,0x27118,0x21840,0x219d0,94192,0x2711c,0x21cd0,0x21e60,94080,0x27120,0x22160,0x222f0,94096,0x27124,0x225f0,0x22780,94112,0x27128,0x22a80,0x22c10,94128,0x2712c,0x22f10,0x230a0,94560,0x27130,0x1ead0,0x1ec38,94576,0x27134,0x1ef60,0x1f0c8,94528,0x27138,0x1f3f0,0x1f558,94544,0x2713c,0x1f880,0x1f9e8,94496,0x27140,0x1fd10,0x1fe78,94512,0x27144,0x201a0,0x20308,94464,0x27148,0x20630,0x20798,94480,0x2714c,0x20ac0,0x20c28,94944,0x27150,0x20f50,0x210b8,94960,0x27154,0x213e0,0x21548,94912,0x27158,0x21870,0x219d8,94928,0x2715c,0x21d00,0x21e68,94880,0x27160,0x22190,0x222f8,94896,0x27164,0x22620,0x22788,94848,0x27168,0x22ab0,0x22c18,94864,0x2716c,0x22f40,0x230a8,0x3f803f80,0x3f803f80,16256,0,0,0x3f803f80,0x3f800000,0,0,0x3f803f80,0,16256,0,0x3f803f80,0,0x3f800000,0x3f803f80,0,0,0,0,0,0,0,0,0,0,0,0,0,0,0,0x3f803f80,0,0,0,0,0,0,0,0,0,0,0,0,0,0,0,0x3f803f80,0,0,0,0,0,0,0,0,0,0,0,0,0,0,0,0x3f803f80,0,0,0,0,0,0,0,0,0,0,0,0,0,0,0,0x3f803f80,0x3f803f80,16256,0,0,0x3f803f80,0x3f800000,0,0,0x3f803f80,0,16256,0,0x3f803f80,0,0x3f800000,0x3f803f80,0,0,0,0,0,0,0,0,0,0,0,0,0,0,0,0x3f803f80,0,0,0,0,0,0,0,0,0,0,0,0,0,0,0,0x3f803f80,0,0,0,0,0,0,0,0,0,0,0,0,0,0,0,0x3f803f80,0,0,0,0,0,0,0,0,0,0,0,0,0,0,0,0x3f803f80,0x3f803f80,16256,0,0,0x3f803f80,0x3f800000,0,0,0x3f803f80,0,16256,0,0x3f803f80,0,0x3f800000,0x3f803f80,0,0,0,0,0,0,0,0,0,0,0,0,0,0,0,0x3f803f80,0,0,0,0,0,0,0,0,0,0,0,0,0,0,0,0x3f803f80,0,0,0,0,0,0,0,0,0,0,0,0,0,0,0,0x3f803f80,0,0,0,0,0,0,0,0,0,0,0,0,0,0,0,0x3f803f80,0x3f803f80,16256,0,0,0x3f803f80,0x3f800000,0,0,0x3f803f80,0,16256,0,0x3f803f80,0,0x3f800000,0xffffffff,0xffffffff,0,0,0,0,0xffffffff,0xff000000,0xff000000,0xffffff,0xff000000,0xff000000,0,4096,16384,0x1c000,0xffffffff,0xffffffff,0,0,0,0,0xffffff,0xffff0000,0xffff0000,65535,0xffffff,0xffffff,1024,5120,16896,0x1c0a0,0xffffffff,0xffffffff,0,0,0,0,65535,0xffffff00,0xff000000,0xffffff,0xffff0000,0xffff0000,2048,10240,17408,0x1c140,0xffffffff,0,0,0,0,0,255,0xffffffff,0xffff0000,65535,65535,65535,3072,11264,17920,0x1c1d0,0,0x3fb8aa3b,0x2a8cbccc,0x38fba882,0x7fff7fff,0x415db3d7,0xffff0000,0,0,65535,0,0xffff0000,0,0,0,0,0,808,1800,2608,3600,4408,5400,6208,7200,8192,9000,9992,10800,11792,12600,18432,14385,18447,16170,18462,18462,18462,18462,18462,20,19,0,0,0,0,0,0,0x1b0000,0x5b8480,0xae0a80,0xee8f00,0x1411500,0x1819980,0x1d41f80,0x214a400,0x2672a00,0x2b9b000,0x2fa3480,0x34cba80,0x38d3f00,0x3dfc500,0x4204980,0x60a80,0x4b2b680,0x6a800,0x5452380,0x74580,0x74580,0x74580,0x74580,0x74580,0x74580,0x74580,0x74580,0x74580,0x74580,0x74580,0x74580,0x74580,1,809,1801,2609,3601,4409,5401,6209,7201,8193,9001,9993,10801,11793,12601,13592,14386,15377,16171,17162,17671,17671,17671,17671,21,20,0,0,0,0,0,0,0x1b0a80,0x5b8f00,0xae1500,0xee9980,0x1411f80,0x181a400,0x1d42a00,0x214ae80,0x2673480,0x2b9ba80,0x2fa3f00,0x34cc500,0x38d4980,0x3dfcf80,0x4205400,0x472cf80,0x4b2c100,0x5053c80,0x5452e00,0x597a980,0x5c17500,0x5c17500,0x5c17500,0x5c17500,0x5c17500,0x5c17500,0x5c17500,0x5c17500,0x5c17500,0x5c17500,0x5c17500,0x5c17500,2,810,1802,2610,3602,4410,5402,6210,7202,8194,9002,9994,10802,11794,12602,13593,14387,15378,16172,17163,17672,18216,18216,18216,22,21,0,0,0,0,0,0,0x1b1500,0x5b9980,0xae1f80,0xeea400,0x1412a00,0x181ae80,0x1d43480,0x214b900,0x2673f00,0x2b9c500,0x2fa4980,0x34ccf80,0x38d5400,0x3dfda00,0x4205e80,0x472da00,0x4b2cb80,0x5054700,0x5453880,0x597b400,0x5c17f80,0x5ecba80,0x5ecba80,0x5ecba80,0x5ecba80,0x5ecba80,0x5ecba80,0x5ecba80,0x5ecba80,0x5ecba80,0x5ecba80,0x5ecba80,3,811,1803,2611,3603,4411,5403,6211,7203,8195,9003,9995,10803,11795,12603,13594,14388,15379,16173,17164,17673,17673,17673,17673,21,20,0,0,0,0,0,0,0x1b1f80,0x5ba400,0xae2a00,0xeeae80,0x1413480,0x181b900,0x1d43f00,0x214c380,0x2674980,0x2b9cf80,0x2fa5400,0x34cda00,0x38d5e80,0x3dfe480,0x4206900,0x472e480,0x4b2d600,0x5055180,0x5454300,0x597be80,0x5c18a00,0x5c18a00,0x5c18a00,0x5c18a00,0x5c18a00,0x5c18a00,0x5c18a00,0x5c18a00,0x5c18a00,0x5c18a00,0x5c18a00,0x5c18a00,4,812,1804,2612,3604,4412,5404,6212,7204,8196,9004,9996,10804,11796,12604,13595,14389,15380,16174,18463,17674,17674,17674,17674,21,20,0,0,0,0,0,0,0x1b2a00,0x5bae80,0xae3480,0xeeb900,0x1413f00,0x181c380,0x1d44980,0x214ce00,0x2675400,0x2b9da00,0x2fa5e80,0x34ce480,0x38d6900,0x3dfef00,0x4207380,0x472ef00,0x4b2e080,0x5055c00,0x5454d80,0x75000,0x5c19480,0x5c19480,0x5c19480,0x5c19480,0x5c19480,0x5c19480,0x5c19480,0x5c19480,0x5c19480,0x5c19480,0x5c19480,0x5c19480,5,813,1805,2613,3605,4413,5405,6213,7205,8197,9005,9997,10805,11797,12605,13596,14390,15381,16175,18464,18464,18464,18464,18464,20,19,0,0,0,0,0,0,0x1b3480,0x5bb900,0xae3f00,0xeec380,0x1414980,0x181ce00,0x1d45400,0x214d880,0x2675e80,0x2b9e480,0x2fa6900,0x34cef00,0x38d7380,0x3dff980,0x4207e00,0x472f980,0x4b2eb00,0x5056680,0x5455800,0x75a80,0x75a80,0x75a80,0x75a80,0x75a80,0x75a80,0x75a80,0x75a80,0x75a80,0x75a80,0x75a80,0x75a80,0x75a80,6,814,1806,2614,3606,4414,5406,6214,7206,8198,9006,9998,10806,11798,12606,13597,14391,15382,16176,18465,17675,17675,17675,17675,21,20,0,0,0,0,0,0,0x1b3f00,0x5bc380,0xae4980,0xeece00,0x1415400,0x181d880,0x1d45e80,0x214e300,0x2676900,0x2b9ef00,0x2fa7380,0x34cf980,0x38d7e00,0x3e00400,0x4208880,0x4730400,0x4b2f580,0x5057100,0x5456280,0x76500,0x5c19f00,0x5c19f00,0x5c19f00,0x5c19f00,0x5c19f00,0x5c19f00,0x5c19f00,0x5c19f00,0x5c19f00,0x5c19f00,0x5c19f00,0x5c19f00,7,815,1807,2615,3607,4415,5407,6215,7207,8199,9007,9999,10807,11799,12607,13598,14392,15383,16177,18466,18466,18466,18466,18466,20,19,0,0,0,0,0,0,0x1b4980,0x5bce00,0xae5400,0xeed880,0x1415e80,0x181e300,0x1d46900,0x214ed80,0x2677380,0x2b9f980,0x2fa7e00,0x34d0400,0x38d8880,0x3e00e80,0x4209300,0x4730e80,0x4b30000,0x5057b80,0x5456d00,0x76f80,0x76f80,0x76f80,0x76f80,0x76f80,0x76f80,0x76f80,0x76f80,0x76f80,0x76f80,0x76f80,0x76f80,0x76f80,8,816,1808,2616,3608,4416,5408,6400,7208,8200,9008,10000,10808,11800,12608,18433,14393,18448,16178,18467,18467,18467,18467,18467,20,19,0,0,0,0,0,0,0x1b5400,0x5bd880,0xae5e80,0xeee300,0x1416900,0x181ed80,0x1d47380,0x226f980,0x2677e00,0x2ba0400,0x2fa8880,0x34d0e80,0x38d9300,0x3e01900,0x4209d80,0x61500,0x4b30a80,0x6b280,0x5457780,0x77a00,0x77a00,0x77a00,0x77a00,0x77a00,0x77a00,0x77a00,0x77a00,0x77a00,0x77a00,0x77a00,0x77a00,0x77a00,9,817,1809,2617,3609,4417,5409,6401,7209,8201,9009,10001,10809,11801,12609,13599,14394,15384,16179,17165,17676,17676,17676,17676,21,20,0,0,0,0,0,0,0x1b5e80,0x5be300,0xae6900,0xeeed80,0x1417380,0x181f800,0x1d47e00,0x2270400,0x2678880,0x2ba0e80,0x2fa9300,0x34d1900,0x38d9d80,0x3e02380,0x420a800,0x4731900,0x4b31500,0x5058600,0x5458200,0x597c900,0x5c1a980,0x5c1a980,0x5c1a980,0x5c1a980,0x5c1a980,0x5c1a980,0x5c1a980,0x5c1a980,0x5c1a980,0x5c1a980,0x5c1a980,0x5c1a980,10,818,1810,2618,3610,4418,5410,6402,7210,8202,9010,10002,10810,11802,12610,13600,14395,15385,16180,17166,17677,18217,18217,18217,22,21,0,0,0,0,0,0,0x1b6900,0x5bed80,0xae7380,0xeef800,0x1417e00,0x1820280,0x1d48880,0x2270e80,0x2679300,0x2ba1900,0x2fa9d80,0x34d2380,0x38da800,0x3e02e00,0x420b280,0x4732380,0x4b31f80,0x5059080,0x5458c80,0x597d380,0x5c1b400,0x5ecc500,0x5ecc500,0x5ecc500,0x5ecc500,0x5ecc500,0x5ecc500,0x5ecc500,0x5ecc500,0x5ecc500,0x5ecc500,0x5ecc500,11,819,1811,2619,3611,4419,5411,6403,7211,8203,9011,10003,10811,11803,12611,13601,14396,15386,16181,17167,17678,17678,17678,17678,21,20,0,0,0,0,0,0,0x1b7380,0x5bf800,0xae7e00,0xef0280,0x1418880,0x1820d00,0x1d49300,0x2271900,0x2679d80,0x2ba2380,0x2faa800,0x34d2e00,0x38db280,0x3e03880,0x420bd00,0x4732e00,0x4b32a00,0x5059b00,0x5459700,0x597de00,0x5c1be80,0x5c1be80,0x5c1be80,0x5c1be80,0x5c1be80,0x5c1be80,0x5c1be80,0x5c1be80,0x5c1be80,0x5c1be80,0x5c1be80,0x5c1be80,12,820,1812,2620,3612,4420,5412,6404,7212,8204,9012,10004,10812,11804,12612,13602,14397,15387,16182,18468,17679,17679,17679,17679,21,20,0,0,0,0,0,0,0x1b7e00,0x5c0280,0xae8880,0xef0d00,0x1419300,0x1821780,0x1d49d80,0x2272380,0x267a800,0x2ba2e00,0x2fab280,0x34d3880,0x38dbd00,0x3e04300,0x420c780,0x4733880,0x4b33480,0x505a580,0x545a180,0x78480,0x5c1c900,0x5c1c900,0x5c1c900,0x5c1c900,0x5c1c900,0x5c1c900,0x5c1c900,0x5c1c900,0x5c1c900,0x5c1c900,0x5c1c900,0x5c1c900,13,821,1813,2621,3613,4421,5413,6405,7213,8205,9013,10005,10813,11805,12613,13603,14398,15388,16183,18469,18469,18469,18469,18469,20,19,0,0,0,0,0,0,0x1b8880,0x5c0d00,0xae9300,0xef1780,0x1419d80,0x1822200,0x1d4a800,0x2272e00,0x267b280,0x2ba3880,0x2fabd00,0x34d4300,0x38dc780,0x3e04d80,0x420d200,0x4734300,0x4b33f00,0x505b000,0x545ac00,0x78f00,0x78f00,0x78f00,0x78f00,0x78f00,0x78f00,0x78f00,0x78f00,0x78f00,0x78f00,0x78f00,0x78f00,0x78f00,14,822,1814,2622,3614,4422,5414,6406,7214,8206,9014,10006,10814,11806,12614,13604,14399,15389,16184,18470,17680,17680,17680,17680,21,20,0,0,0,0,0,0,0x1b9300,0x5c1780,0xae9d80,0xef2200,0x141a800,0x1822c80,0x1d4b280,0x2273880,0x267bd00,0x2ba4300,0x2fac780,0x34d4d80,0x38dd200,0x3e05800,0x420dc80,0x4734d80,0x4b34980,0x505ba80,0x545b680,0x79980,0x5c1d380,0x5c1d380,0x5c1d380,0x5c1d380,0x5c1d380,0x5c1d380,0x5c1d380,0x5c1d380,0x5c1d380,0x5c1d380,0x5c1d380,0x5c1d380,15,823,1815,2623,3615,4423,5415,6407,7215,8207,9015,10007,10815,11807,12615,13605,14400,15390,16185,18471,18471,18471,18471,18471,20,19,0,0,0,0,0,0,0x1b9d80,0x5c2200,0xaea800,0xef2c80,0x141b280,0x1823700,0x1d4bd00,0x2274300,0x267c780,0x2ba4d80,0x2fad200,0x34d5800,0x38ddc80,0x3e06280,0x420e700,0x4735800,0x4b35400,0x505c500,0x545c100,0x7a400,0x7a400,0x7a400,0x7a400,0x7a400,0x7a400,0x7a400,0x7a400,0x7a400,0x7a400,0x7a400,0x7a400,0x7a400,16,824,1816,2624,3616,4608,5416,6408,7216,8208,9016,10008,10816,11808,12800,18434,14401,18449,16186,18472,18472,18472,18472,18472,20,19,0,0,0,0,0,0,0x1ba800,0x5c2c80,0xaeb280,0xef3700,0x141bd00,0x1944300,0x1d4c780,0x2274d80,0x267d200,0x2ba5800,0x2fadc80,0x34d6280,0x38de700,0x3e06d00,0x432f300,0x61f80,0x4b35e80,0x6bd00,0x545cb80,0x7ae80,0x7ae80,0x7ae80,0x7ae80,0x7ae80,0x7ae80,0x7ae80,0x7ae80,0x7ae80,0x7ae80,0x7ae80,0x7ae80,0x7ae80,17,825,1817,2625,3617,4609,5417,6409,7217,8209,9017,10009,10817,11809,12801,13606,14402,15391,16187,17168,17681,17681,17681,17681,21,20,0,0,0,0,0,0,0x1bb280,0x5c3700,0xaebd00,0xef4180,0x141c780,0x1944d80,0x1d4d200,0x2275800,0x267dc80,0x2ba6280,0x2fae700,0x34d6d00,0x38df180,0x3e07780,0x432fd80,0x4736280,0x4b36900,0x505cf80,0x545d600,0x597e880,0x5c1de00,0x5c1de00,0x5c1de00,0x5c1de00,0x5c1de00,0x5c1de00,0x5c1de00,0x5c1de00,0x5c1de00,0x5c1de00,0x5c1de00,0x5c1de00,18,826,1818,2626,3618,4610,5418,6410,7218,8210,9018,10010,10818,11810,12802,13607,14403,15392,16188,17169,17682,18218,18218,18218,22,21,0,0,0,0,0,0,0x1bbd00,0x5c4180,0xaec780,0xef4c00,0x141d200,0x1945800,0x1d4dc80,0x2276280,0x267e700,0x2ba6d00,0x2faf180,0x34d7780,0x38dfc00,0x3e08200,0x4330800,0x4736d00,0x4b37380,0x505da00,0x545e080,0x597f300,0x5c1e880,0x5eccf80,0x5eccf80,0x5eccf80,0x5eccf80,0x5eccf80,0x5eccf80,0x5eccf80,0x5eccf80,0x5eccf80,0x5eccf80,0x5eccf80,19,827,1819,2627,3619,4611,5419,6411,7219,8211,9019,10011,10819,11811,12803,13608,14404,15393,16189,17170,17683,17683,17683,17683,21,20,0,0,0,0,0,0,0x1bc780,0x5c4c00,0xaed200,0xef5680,0x141dc80,0x1946280,0x1d4e700,0x2276d00,0x267f180,0x2ba7780,0x2fafc00,0x34d8200,0x38e0680,0x3e08c80,0x4331280,0x4737780,0x4b37e00,0x505e480,0x545eb00,0x597fd80,0x5c1f300,0x5c1f300,0x5c1f300,0x5c1f300,0x5c1f300,0x5c1f300,0x5c1f300,0x5c1f300,0x5c1f300,0x5c1f300,0x5c1f300,0x5c1f300,20,828,1820,2628,3620,4612,5420,6412,7220,8212,9020,10012,10820,11812,12804,13609,14405,15394,16190,18473,17684,17684,17684,17684,21,20,0,0,0,0,0,0,0x1bd200,0x5c5680,0xaedc80,0xef6100,0x141e700,0x1946d00,0x1d4f180,0x2277780,0x267fc00,0x2ba8200,0x2fb0680,0x34d8c80,0x38e1100,0x3e09700,0x4331d00,0x4738200,0x4b38880,0x505ef00,0x545f580,0x7b900,0x5c1fd80,0x5c1fd80,0x5c1fd80,0x5c1fd80,0x5c1fd80,0x5c1fd80,0x5c1fd80,0x5c1fd80,0x5c1fd80,0x5c1fd80,0x5c1fd80,0x5c1fd80,21,829,1821,2629,3621,4613,5421,6413,7221,8213,9021,10013,10821,11813,12805,13610,14406,15395,16191,18474,18474,18474,18474,18474,20,19,0,0,0,0,0,0,0x1bdc80,0x5c6100,0xaee700,0xef6b80,0x141f180,0x1947780,0x1d4fc00,0x2278200,0x2680680,0x2ba8c80,0x2fb1100,0x34d9700,0x38e1b80,0x3e0a180,0x4332780,0x4738c80,0x4b39300,0x505f980,0x5460000,0x7c380,0x7c380,0x7c380,0x7c380,0x7c380,0x7c380,0x7c380,0x7c380,0x7c380,0x7c380,0x7c380,0x7c380,0x7c380,22,830,1822,2630,3622,4614,5422,6414,7222,8214,9022,10014,10822,11814,12806,13611,14407,15396,16192,18475,17685,17685,17685,17685,21,20,0,0,0,0,0,0,0x1be700,0x5c6b80,0xaef180,0xef7600,0x141fc00,0x1948200,0x1d50680,0x2278c80,0x2681100,0x2ba9700,0x2fb1b80,0x34da180,0x38e2600,0x3e0ac00,0x4333200,0x4739700,0x4b39d80,0x5060400,0x5460a80,0x7ce00,0x5c20800,0x5c20800,0x5c20800,0x5c20800,0x5c20800,0x5c20800,0x5c20800,0x5c20800,0x5c20800,0x5c20800,0x5c20800,0x5c20800,23,831,1823,2631,3623,4615,5423,6415,7223,8215,9023,10015,10823,11815,12807,13612,14592,15397,16193,18476,18476,18476,18476,18476,20,19,0,0,0,0,0,0,0x1bf180,0x5c7600,0xaefc00,0xef8080,0x1420680,0x1948c80,0x1d51100,0x2279700,0x2681b80,0x2baa180,0x2fb2600,0x34dac00,0x38e3080,0x3e0b680,0x4333c80,0x473a180,0x4c5a980,0x5060e80,0x5461500,0x7d880,0x7d880,0x7d880,0x7d880,0x7d880,0x7d880,0x7d880,0x7d880,0x7d880,0x7d880,0x7d880,0x7d880,0x7d880,24,832,1824,2816,3624,4616,5424,6416,7224,8216,9024,10016,11008,11816,12808,18435,14593,18450,16194,18477,18477,18477,18477,18477,20,19,0,0,0,0,0,0,0x1bfc00,0x5c8080,0xaf0680,0x1018c80,0x1421100,0x1949700,0x1d51b80,0x227a180,0x2682600,0x2baac00,0x2fb3080,0x34db680,0x3a03c80,0x3e0c100,0x4334700,0x62a00,0x4c5b400,0x6c780,0x5461f80,0x7e300,0x7e300,0x7e300,0x7e300,0x7e300,0x7e300,0x7e300,0x7e300,0x7e300,0x7e300,0x7e300,0x7e300,0x7e300,25,833,1825,2817,3625,4617,5425,6417,7225,8217,9025,10017,11009,11817,12809,13613,14594,15398,16195,17171,17686,17686,17686,17686,21,20,0,0,0,0,0,0,0x1c0680,0x5c8b00,0xaf1100,0x1019700,0x1421b80,0x194a180,0x1d52600,0x227ac00,0x2683080,0x2bab680,0x2fb3b00,0x34dc100,0x3a04700,0x3e0cb80,0x4335180,0x473ac00,0x4c5be80,0x5061900,0x5462a00,0x5980800,0x5c21280,0x5c21280,0x5c21280,0x5c21280,0x5c21280,0x5c21280,0x5c21280,0x5c21280,0x5c21280,0x5c21280,0x5c21280,0x5c21280,26,834,1826,2818,3626,4618,5426,6418,7226,8218,9026,10018,11010,11818,12810,13614,14595,15399,16196,17172,17687,18219,18219,18219,22,21,0,0,0,0,0,0,0x1c1100,0x5c9580,0xaf1b80,0x101a180,0x1422600,0x194ac00,0x1d53080,0x227b680,0x2683b00,0x2bac100,0x2fb4580,0x34dcb80,0x3a05180,0x3e0d600,0x4335c00,0x473b680,0x4c5c900,0x5062380,0x5463480,0x5981280,0x5c21d00,0x5ecda00,0x5ecda00,0x5ecda00,0x5ecda00,0x5ecda00,0x5ecda00,0x5ecda00,0x5ecda00,0x5ecda00,0x5ecda00,0x5ecda00,27,835,1827,2819,3627,4619,5427,6419,7227,8219,9027,10019,11011,11819,12811,13615,14596,15400,16197,17173,17688,17688,17688,17688,21,20,0,0,0,0,0,0,0x1c1b80,0x5ca000,0xaf2600,0x101ac00,0x1423080,0x194b680,0x1d53b00,0x227c100,0x2684580,0x2bacb80,0x2fb5000,0x34dd600,0x3a05c00,0x3e0e080,0x4336680,0x473c100,0x4c5d380,0x5062e00,0x5463f00,0x5981d00,0x5c22780,0x5c22780,0x5c22780,0x5c22780,0x5c22780,0x5c22780,0x5c22780,0x5c22780,0x5c22780,0x5c22780,0x5c22780,0x5c22780,28,836,1828,2820,3628,4620,5428,6420,7228,8220,9028,10020,11012,11820,12812,13616,14597,15401,16198,18478,17689,17689,17689,17689,21,20,0,0,0,0,0,0,0x1c2600,0x5caa80,0xaf3080,0x101b680,0x1423b00,0x194c100,0x1d54580,0x227cb80,0x2685000,0x2bad600,0x2fb5a80,0x34de080,0x3a06680,0x3e0eb00,0x4337100,0x473cb80,0x4c5de00,0x5063880,0x5464980,0x7ed80,0x5c23200,0x5c23200,0x5c23200,0x5c23200,0x5c23200,0x5c23200,0x5c23200,0x5c23200,0x5c23200,0x5c23200,0x5c23200,0x5c23200,29,837,1829,2821,3629,4621,5429,6421,7229,8221,9029,10021,11013,11821,12813,13617,14598,15402,16199,18479,18479,18479,18479,18479,20,19,0,0,0,0,0,0,0x1c3080,0x5cb500,0xaf3b00,0x101c100,0x1424580,0x194cb80,0x1d55000,0x227d600,0x2685a80,0x2bae080,0x2fb6500,0x34deb00,0x3a07100,0x3e0f580,0x4337b80,0x473d600,0x4c5e880,0x5064300,0x5465400,0x7f800,0x7f800,0x7f800,0x7f800,0x7f800,0x7f800,0x7f800,0x7f800,0x7f800,0x7f800,0x7f800,0x7f800,0x7f800,30,838,1830,2822,3630,4622,5430,6422,7230,8222,9030,10022,11014,11822,12814,13618,14599,15403,16384,18480,17690,17690,17690,17690,21,20,0,0,0,0,0,0,0x1c3b00,0x5cbf80,0xaf4580,0x101cb80,0x1425000,0x194d600,0x1d55a80,0x227e080,0x2686500,0x2baeb00,0x2fb6f80,0x34df580,0x3a07b80,0x3e10000,0x4338600,0x473e080,0x4c5f300,0x5064d80,0x5586000,0x80280,0x5c23c80,0x5c23c80,0x5c23c80,0x5c23c80,0x5c23c80,0x5c23c80,0x5c23c80,0x5c23c80,0x5c23c80,0x5c23c80,0x5c23c80,0x5c23c80,31,839,1831,2823,3631,4623,5431,6423,7231,8223,9031,10023,11015,11823,12815,13619,14600,15404,16385,18481,18481,18481,18481,18481,20,19,0,0,0,0,0,0,0x1c4580,0x5cca00,0xaf5000,0x101d600,0x1425a80,0x194e080,0x1d56500,0x227eb00,0x2686f80,0x2baf580,0x2fb7a00,0x34e0000,0x3a08600,0x3e10a80,0x4339080,0x473eb00,0x4c5fd80,0x5065800,0x5586a80,0x80d00,0x80d00,0x80d00,0x80d00,0x80d00,0x80d00,0x80d00,0x80d00,0x80d00,0x80d00,0x80d00,0x80d00,0x80d00,32,1024,1832,2824,3632,4624,5432,6424,7232,8224,9216,10024,11016,11824,12816,18436,14601,18451,16386,18482,18482,18482,18482,18482,20,19,0,0,0,0,0,0,0x1c5000,0x6ed600,0xaf5a80,0x101e080,0x1426500,0x194eb00,0x1d56f80,0x227f580,0x2687a00,0x2bb0000,0x30d8600,0x34e0a80,0x3a09080,0x3e11500,0x4339b00,0x63480,0x4c60800,0x6d200,0x5587500,0x81780,0x81780,0x81780,0x81780,0x81780,0x81780,0x81780,0x81780,0x81780,0x81780,0x81780,0x81780,0x81780,33,1025,1833,2825,3633,4625,5433,6425,7233,8225,9217,10025,11017,11825,12817,13620,14602,15405,16387,17174,17691,17691,17691,17691,21,20,0,0,0,0,0,0,0x1c5a80,0x6ee080,0xaf6500,0x101eb00,0x1426f80,0x194f580,0x1d57a00,0x2280000,0x2688480,0x2bb0a80,0x30d9080,0x34e1500,0x3a09b00,0x3e11f80,0x433a580,0x473f580,0x4c61280,0x5066280,0x5587f80,0x5982780,0x5c24700,0x5c24700,0x5c24700,0x5c24700,0x5c24700,0x5c24700,0x5c24700,0x5c24700,0x5c24700,0x5c24700,0x5c24700,0x5c24700,34,1026,1834,2826,3634,4626,5434,6426,7234,8226,9218,10026,11018,11826,12818,13621,14603,15406,16388,17175,17692,18220,18220,18220,22,21,0,0,0,0,0,0,0x1c6500,0x6eeb00,0xaf6f80,0x101f580,0x1427a00,0x1950000,0x1d58480,0x2280a80,0x2688f00,0x2bb1500,0x30d9b00,0x34e1f80,0x3a0a580,0x3e12a00,0x433b000,0x4740000,0x4c61d00,0x5066d00,0x5588a00,0x5983200,0x5c25180,0x5ece480,0x5ece480,0x5ece480,0x5ece480,0x5ece480,0x5ece480,0x5ece480,0x5ece480,0x5ece480,0x5ece480,0x5ece480,35,1027,1835,2827,3635,4627,5435,6427,7235,8227,9219,10027,11019,11827,12819,13622,14604,15407,16389,17176,17693,17693,17693,17693,21,20,0,0,0,0,0,0,0x1c6f80,0x6ef580,0xaf7a00,0x1020000,0x1428480,0x1950a80,0x1d58f00,0x2281500,0x2689980,0x2bb1f80,0x30da580,0x34e2a00,0x3a0b000,0x3e13480,0x433ba80,0x4740a80,0x4c62780,0x5067780,0x5589480,0x5983c80,0x5c25c00,0x5c25c00,0x5c25c00,0x5c25c00,0x5c25c00,0x5c25c00,0x5c25c00,0x5c25c00,0x5c25c00,0x5c25c00,0x5c25c00,0x5c25c00,36,1028,1836,2828,3636,4628,5436,6428,7236,8228,9220,10028,11020,11828,12820,13623,14605,15408,16390,17177,17694,17694,17694,17694,21,20,0,0,0,0,0,0,0x1c7a00,0x6f0000,0xaf8480,0x1020a80,0x1428f00,0x1951500,0x1d59980,0x2281f80,0x268a400,0x2bb2a00,0x30db000,0x34e3480,0x3a0ba80,0x3e13f00,0x433c500,0x4741500,0x4c63200,0x5068200,0x5589f00,0x5984700,0x5c26680,0x5c26680,0x5c26680,0x5c26680,0x5c26680,0x5c26680,0x5c26680,0x5c26680,0x5c26680,0x5c26680,0x5c26680,0x5c26680,37,1029,1837,2829,3637,4629,5437,6429,7237,8229,9221,10029,11021,11829,12821,13624,14606,15409,16391,18483,18483,18483,18483,18483,20,19,0,0,0,0,0,0,0x1c8480,0x6f0a80,0xaf8f00,0x1021500,0x1429980,0x1951f80,0x1d5a400,0x2282a00,0x268ae80,0x2bb3480,0x30dba80,0x34e3f00,0x3a0c500,0x3e14980,0x433cf80,0x4741f80,0x4c63c80,0x5068c80,0x558a980,0x82200,0x82200,0x82200,0x82200,0x82200,0x82200,0x82200,0x82200,0x82200,0x82200,0x82200,0x82200,0x82200,38,1030,1838,2830,3638,4630,5438,6430,7238,8230,9222,10030,11022,11830,12822,13625,14607,15410,16392,18484,17695,17695,17695,17695,21,20,0,0,0,0,0,0,0x1c8f00,0x6f1500,0xaf9980,0x1021f80,0x142a400,0x1952a00,0x1d5ae80,0x2283480,0x268b900,0x2bb3f00,0x30dc500,0x34e4980,0x3a0cf80,0x3e15400,0x433da00,0x4742a00,0x4c64700,0x5069700,0x558b400,0x82c80,0x5c27100,0x5c27100,0x5c27100,0x5c27100,0x5c27100,0x5c27100,0x5c27100,0x5c27100,0x5c27100,0x5c27100,0x5c27100,0x5c27100,39,1031,1839,2831,3639,4631,5439,6431,7239,8231,9223,10031,11023,11831,12823,13626,14608,15411,16393,18485,18485,18485,18485,18485,20,19,0,0,0,0,0,0,0x1c9980,0x6f1f80,0xafa400,0x1022a00,0x142ae80,0x1953480,0x1d5b900,0x2283f00,0x268c380,0x2bb4980,0x30dcf80,0x34e5400,0x3a0da00,0x3e15e80,0x433e480,0x4743480,0x4c65180,0x506a180,0x558be80,0x83700,0x83700,0x83700,0x83700,0x83700,0x83700,0x83700,0x83700,0x83700,0x83700,0x83700,0x83700,0x83700,40,1032,1840,2832,3640,4632,5440,6432,7424,8232,9224,10032,11024,11832,12824,18437,14609,18452,16394,18486,18486,18486,18486,18486,20,19,0,0,0,0,0,0,0x1ca400,0x6f2a00,0xafae80,0x1023480,0x142b900,0x1953f00,0x1d5c380,0x2284980,0x27acf80,0x2bb5400,0x30dda00,0x34e5e80,0x3a0e480,0x3e16900,0x433ef00,0x63f00,0x4c65c00,0x6dc80,0x558c900,0x84180,0x84180,0x84180,0x84180,0x84180,0x84180,0x84180,0x84180,0x84180,0x84180,0x84180,0x84180,0x84180,41,1033,1841,2833,3641,4633,5441,6433,7425,8233,9225,10033,11025,11833,12825,13627,14610,15412,16395,17178,17696,17696,17696,17696,21,20,0,0,0,0,0,0,0x1cae80,0x6f3480,0xafb900,0x1023f00,0x142c380,0x1954980,0x1d5ce00,0x2285400,0x27ada00,0x2bb5e80,0x30de480,0x34e6900,0x3a0ef00,0x3e17380,0x433f980,0x4743f00,0x4c66680,0x506ac00,0x558d380,0x5985180,0x5c27b80,0x5c27b80,0x5c27b80,0x5c27b80,0x5c27b80,0x5c27b80,0x5c27b80,0x5c27b80,0x5c27b80,0x5c27b80,0x5c27b80,0x5c27b80,42,1034,1842,2834,3642,4634,5442,6434,7426,8234,9226,10034,11026,11834,12826,13628,14611,15413,16396,17179,17697,18221,18221,18221,22,21,0,0,0,0,0,0,0x1cb900,0x6f3f00,0xafc380,0x1024980,0x142ce00,0x1955400,0x1d5d880,0x2285e80,0x27ae480,0x2bb6900,0x30def00,0x34e7380,0x3a0f980,0x3e17e00,0x4340400,0x4744980,0x4c67100,0x506b680,0x558de00,0x5985c00,0x5c28600,0x5ecef00,0x5ecef00,0x5ecef00,0x5ecef00,0x5ecef00,0x5ecef00,0x5ecef00,0x5ecef00,0x5ecef00,0x5ecef00,0x5ecef00,43,1035,1843,2835,3643,4635,5443,6435,7427,8235,9227,10035,11027,11835,12827,13629,14612,15414,16397,17180,17698,17698,17698,17698,21,20,0,0,0,0,0,0,0x1cc380,0x6f4980,0xafce00,0x1025400,0x142d880,0x1955e80,0x1d5e300,0x2286900,0x27aef00,0x2bb7380,0x30df980,0x34e7e00,0x3a10400,0x3e18880,0x4340e80,0x4745400,0x4c67b80,0x506c100,0x558e880,0x5986680,0x5c29080,0x5c29080,0x5c29080,0x5c29080,0x5c29080,0x5c29080,0x5c29080,0x5c29080,0x5c29080,0x5c29080,0x5c29080,0x5c29080,44,1036,1844,2836,3644,4636,5444,6436,7428,8236,9228,10036,11028,11836,12828,13630,14613,15415,16398,17181,17699,17699,17699,17699,21,20,0,0,0,0,0,0,0x1cce00,0x6f5400,0xafd880,0x1025e80,0x142e300,0x1956900,0x1d5ed80,0x2287380,0x27af980,0x2bb7e00,0x30e0400,0x34e8880,0x3a10e80,0x3e19300,0x4341900,0x4745e80,0x4c68600,0x506cb80,0x558f300,0x5987100,0x5c29b00,0x5c29b00,0x5c29b00,0x5c29b00,0x5c29b00,0x5c29b00,0x5c29b00,0x5c29b00,0x5c29b00,0x5c29b00,0x5c29b00,0x5c29b00,45,1037,1845,2837,3645,4637,5445,6437,7429,8237,9229,10037,11029,11837,12829,13631,14614,15416,16399,18487,18487,18487,18487,18487,20,19,0,0,0,0,0,0,0x1cd880,0x6f5e80,0xafe300,0x1026900,0x142ed80,0x1957380,0x1d5f800,0x2287e00,0x27b0400,0x2bb8880,0x30e0e80,0x34e9300,0x3a11900,0x3e19d80,0x4342380,0x4746900,0x4c69080,0x506d600,0x558fd80,0x84c00,0x84c00,0x84c00,0x84c00,0x84c00,0x84c00,0x84c00,0x84c00,0x84c00,0x84c00,0x84c00,0x84c00,0x84c00,46,1038,1846,2838,3646,4638,5446,6438,7430,8238,9230,10038,11030,11838,12830,13632,14615,15417,16400,18488,17700,17700,17700,17700,21,20,0,0,0,0,0,0,0x1ce300,0x6f6900,0xafed80,0x1027380,0x142f800,0x1957e00,0x1d60280,0x2288880,0x27b0e80,0x2bb9300,0x30e1900,0x34e9d80,0x3a12380,0x3e1a800,0x4342e00,0x4747380,0x4c69b00,0x506e080,0x5590800,0x85680,0x5c2a580,0x5c2a580,0x5c2a580,0x5c2a580,0x5c2a580,0x5c2a580,0x5c2a580,0x5c2a580,0x5c2a580,0x5c2a580,0x5c2a580,0x5c2a580,47,1039,1847,2839,3647,4639,5447,6439,7431,8239,9231,10039,11031,11839,12831,13633,14616,15418,16401,18489,18489,18489,18489,18489,20,19,0,0,0,0,0,0,0x1ced80,0x6f7380,0xaff800,0x1027e00,0x1430280,0x1958880,0x1d60d00,0x2289300,0x27b1900,0x2bb9d80,0x30e2380,0x34ea800,0x3a12e00,0x3e1b280,0x4343880,0x4747e00,0x4c6a580,0x506eb00,0x5591280,0x86100,0x86100,0x86100,0x86100,0x86100,0x86100,0x86100,0x86100,0x86100,0x86100,0x86100,0x86100,0x86100,48,1040,1848,2840,3648,4640,5632,6440,7432,8240,9232,10040,11032,11840,12832,18438,14617,18453,16402,18490,18490,18490,18490,18490,20,19,0,0,0,0,0,0,0x1cf800,0x6f7e00,0xb00280,0x1028880,0x1430d00,0x1959300,0x1e81900,0x2289d80,0x27b2380,0x2bba800,0x30e2e00,0x34eb280,0x3a13880,0x3e1bd00,0x4344300,0x64980,0x4c6b000,0x6e700,0x5591d00,0x86b80,0x86b80,0x86b80,0x86b80,0x86b80,0x86b80,0x86b80,0x86b80,0x86b80,0x86b80,0x86b80,0x86b80,0x86b80,49,1041,1849,2841,3649,4641,5633,6441,7433,8241,9233,10041,11033,11841,12833,13634,14618,15419,16403,17182,17701,17701,17701,17701,21,20,0,0,0,0,0,0,0x1d0280,0x6f8880,0xb00d00,0x1029300,0x1431780,0x1959d80,0x1e82380,0x228a800,0x27b2e00,0x2bbb280,0x30e3880,0x34ebd00,0x3a14300,0x3e1c780,0x4344d80,0x4748880,0x4c6ba80,0x506f580,0x5592780,0x5987b80,0x5c2b000,0x5c2b000,0x5c2b000,0x5c2b000,0x5c2b000,0x5c2b000,0x5c2b000,0x5c2b000,0x5c2b000,0x5c2b000,0x5c2b000,0x5c2b000,50,1042,1850,2842,3650,4642,5634,6442,7434,8242,9234,10042,11034,11842,12834,13635,14619,15420,16404,17183,17702,18222,18222,18222,22,21,0,0,0,0,0,0,0x1d0d00,0x6f9300,0xb01780,0x1029d80,0x1432200,0x195a800,0x1e82e00,0x228b280,0x27b3880,0x2bbbd00,0x30e4300,0x34ec780,0x3a14d80,0x3e1d200,0x4345800,0x4749300,0x4c6c500,0x5070000,0x5593200,0x5988600,0x5c2ba80,0x5ecf980,0x5ecf980,0x5ecf980,0x5ecf980,0x5ecf980,0x5ecf980,0x5ecf980,0x5ecf980,0x5ecf980,0x5ecf980,0x5ecf980,51,1043,1851,2843,3651,4643,5635,6443,7435,8243,9235,10043,11035,11843,12835,13636,14620,15421,16405,17184,17703,17703,17703,17703,21,20,0,0,0,0,0,0,0x1d1780,0x6f9d80,0xb02200,0x102a800,0x1432c80,0x195b280,0x1e83880,0x228bd00,0x27b4300,0x2bbc780,0x30e4d80,0x34ed200,0x3a15800,0x3e1dc80,0x4346280,0x4749d80,0x4c6cf80,0x5070a80,0x5593c80,0x5989080,0x5c2c500,0x5c2c500,0x5c2c500,0x5c2c500,0x5c2c500,0x5c2c500,0x5c2c500,0x5c2c500,0x5c2c500,0x5c2c500,0x5c2c500,0x5c2c500,52,1044,1852,2844,3652,4644,5636,6444,7436,8244,9236,10044,11036,11844,12836,13637,14621,15422,16406,17185,17704,17704,17704,17704,21,20,0,0,0,0,0,0,0x1d2200,0x6fa800,0xb02c80,0x102b280,0x1433700,0x195bd00,0x1e84300,0x228c780,0x27b4d80,0x2bbd200,0x30e5800,0x34edc80,0x3a16280,0x3e1e700,0x4346d00,0x474a800,0x4c6da00,0x5071500,0x5594700,0x5989b00,0x5c2cf80,0x5c2cf80,0x5c2cf80,0x5c2cf80,0x5c2cf80,0x5c2cf80,0x5c2cf80,0x5c2cf80,0x5c2cf80,0x5c2cf80,0x5c2cf80,0x5c2cf80,53,1045,1853,2845,3653,4645,5637,6445,7437,8245,9237,10045,11037,11845,12837,13638,14622,15423,16407,18491,18491,18491,18491,18491,20,19,0,0,0,0,0,0,0x1d2c80,0x6fb280,0xb03700,0x102bd00,0x1434180,0x195c780,0x1e84d80,0x228d200,0x27b5800,0x2bbdc80,0x30e6280,0x34ee700,0x3a16d00,0x3e1f180,0x4347780,0x474b280,0x4c6e480,0x5071f80,0x5595180,0x87600,0x87600,0x87600,0x87600,0x87600,0x87600,0x87600,0x87600,0x87600,0x87600,0x87600,0x87600,0x87600,54,1046,1854,2846,3654,4646,5638,6446,7438,8246,9238,10046,11038,11846,12838,13639,14623,15424,16408,18492,17705,17705,17705,17705,21,20,0,0,0,0,0,0,0x1d3700,0x6fbd00,0xb04180,0x102c780,0x1434c00,0x195d200,0x1e85800,0x228dc80,0x27b6280,0x2bbe700,0x30e6d00,0x34ef180,0x3a17780,0x3e1fc00,0x4348200,0x474bd00,0x4c6ef00,0x5072a00,0x5595c00,0x88080,0x5c2da00,0x5c2da00,0x5c2da00,0x5c2da00,0x5c2da00,0x5c2da00,0x5c2da00,0x5c2da00,0x5c2da00,0x5c2da00,0x5c2da00,0x5c2da00,55,1047,1855,2847,3655,4647,5639,6447,7439,8247,9239,10047,11039,11847,12839,13824,14624,15425,16409,18493,18493,18493,18493,18493,20,19,0,0,0,0,0,0,0x1d4180,0x6fc780,0xb04c00,0x102d200,0x1435680,0x195dc80,0x1e86280,0x228e700,0x27b6d00,0x2bbf180,0x30e7780,0x34efc00,0x3a18200,0x3e20680,0x4348c80,0x486c900,0x4c6f980,0x5073480,0x5596680,0x88b00,0x88b00,0x88b00,0x88b00,0x88b00,0x88b00,0x88b00,0x88b00,0x88b00,0x88b00,0x88b00,0x88b00,0x88b00,56,1048,1856,2848,3840,4648,5640,6448,7440,8248,9240,10048,11040,12032,12840,18439,14625,18454,16410,18494,18494,18494,18494,18494,20,19,0,0,0,0,0,0,0x1d4c00,0x6fd200,0xb05680,0x102dc80,0x1556280,0x195e700,0x1e86d00,0x228f180,0x27b7780,0x2bbfc00,0x30e8200,0x34f0680,0x3a18c80,0x3f41280,0x4349700,0x65400,0x4c70400,0x6f180,0x5597100,0x89580,0x89580,0x89580,0x89580,0x89580,0x89580,0x89580,0x89580,0x89580,0x89580,0x89580,0x89580,0x89580,57,1049,1857,2849,3841,4649,5641,6449,7441,8249,9241,10049,11041,12033,12841,13825,14626,15426,16411,17186,17706,17706,17706,17706,21,20,0,0,0,0,0,0,0x1d5680,0x6fdc80,0xb06100,0x102e700,0x1556d00,0x195f180,0x1e87780,0x228fc00,0x27b8200,0x2bc0680,0x30e8c80,0x34f1100,0x3a19700,0x3f41d00,0x434a180,0x486d380,0x4c70e80,0x5073f00,0x5597b80,0x598a580,0x5c2e480,0x5c2e480,0x5c2e480,0x5c2e480,0x5c2e480,0x5c2e480,0x5c2e480,0x5c2e480,0x5c2e480,0x5c2e480,0x5c2e480,0x5c2e480,58,1050,1858,2850,3842,4650,5642,6450,7442,8250,9242,10050,11042,12034,12842,13826,14627,15427,16412,17187,17707,18223,18223,18223,22,21,0,0,0,0,0,0,0x1d6100,0x6fe700,0xb06b80,0x102f180,0x1557780,0x195fc00,0x1e88200,0x2290680,0x27b8c80,0x2bc1100,0x30e9700,0x34f1b80,0x3a1a180,0x3f42780,0x434ac00,0x486de00,0x4c71900,0x5074980,0x5598600,0x598b000,0x5c2ef00,0x5ed0400,0x5ed0400,0x5ed0400,0x5ed0400,0x5ed0400,0x5ed0400,0x5ed0400,0x5ed0400,0x5ed0400,0x5ed0400,0x5ed0400,59,1051,1859,2851,3843,4651,5643,6451,7443,8251,9243,10051,11043,12035,12843,13827,14628,15428,16413,17188,17708,17708,17708,17708,21,20,0,0,0,0,0,0,0x1d6b80,0x6ff180,0xb07600,0x102fc00,0x1558200,0x1960680,0x1e88c80,0x2291100,0x27b9700,0x2bc1b80,0x30ea180,0x34f2600,0x3a1ac00,0x3f43200,0x434b680,0x486e880,0x4c72380,0x5075400,0x5599080,0x598ba80,0x5c2f980,0x5c2f980,0x5c2f980,0x5c2f980,0x5c2f980,0x5c2f980,0x5c2f980,0x5c2f980,0x5c2f980,0x5c2f980,0x5c2f980,0x5c2f980,60,1052,1860,2852,3844,4652,5644,6452,7444,8252,9244,10052,11044,12036,12844,13828,14629,15429,16414,17189,17709,17709,17709,17709,21,20,0,0,0,0,0,0,0x1d7600,0x6ffc00,0xb08080,0x1030680,0x1558c80,0x1961100,0x1e89700,0x2291b80,0x27ba180,0x2bc2600,0x30eac00,0x34f3080,0x3a1b680,0x3f43c80,0x434c100,0x486f300,0x4c72e00,0x5075e80,0x5599b00,0x598c500,0x5c30400,0x5c30400,0x5c30400,0x5c30400,0x5c30400,0x5c30400,0x5c30400,0x5c30400,0x5c30400,0x5c30400,0x5c30400,0x5c30400,61,1053,1861,2853,3845,4653,5645,6453,7445,8253,9245,10053,11045,12037,12845,13829,14630,15430,16415,18495,18495,18495,18495,18495,20,19,0,0,0,0,0,0,0x1d8080,0x700680,0xb08b00,0x1031100,0x1559700,0x1961b80,0x1e8a180,0x2292600,0x27bac00,0x2bc3080,0x30eb680,0x34f3b00,0x3a1c100,0x3f44700,0x434cb80,0x486fd80,0x4c73880,0x5076900,0x559a580,0x8a000,0x8a000,0x8a000,0x8a000,0x8a000,0x8a000,0x8a000,0x8a000,0x8a000,0x8a000,0x8a000,0x8a000,0x8a000,62,1054,1862,2854,3846,4654,5646,6454,7446,8254,9246,10054,11046,12038,12846,13830,14631,15431,16416,18496,17710,17710,17710,17710,21,20,0,0,0,0,0,0,0x1d8b00,0x701100,0xb09580,0x1031b80,0x155a180,0x1962600,0x1e8ac00,0x2293080,0x27bb680,0x2bc3b00,0x30ec100,0x34f4580,0x3a1cb80,0x3f45180,0x434d600,0x4870800,0x4c74300,0x5077380,0x559b000,0x8aa80,0x5c30e80,0x5c30e80,0x5c30e80,0x5c30e80,0x5c30e80,0x5c30e80,0x5c30e80,0x5c30e80,0x5c30e80,0x5c30e80,0x5c30e80,0x5c30e80,63,1055,1863,2855,3847,4655,5647,6455,7447,8255,9247,10055,11047,12039,12847,13831,14632,15616,16417,18497,18497,18497,18497,18497,20,19,0,0,0,0,0,0,0x1d9580,0x701b80,0xb0a000,0x1032600,0x155ac00,0x1963080,0x1e8b680,0x2293b00,0x27bc100,0x2bc4580,0x30ecb80,0x34f5000,0x3a1d600,0x3f45c00,0x434e080,0x4871280,0x4c74d80,0x5197f80,0x559ba80,0x8b500,0x8b500,0x8b500,0x8b500,0x8b500,0x8b500,0x8b500,0x8b500,0x8b500,0x8b500,0x8b500,0x8b500,0x8b500,64,1056,2048,2856,3848,4656,5648,6456,7448,8256,9248,10240,11048,12040,12848,18440,14633,18455,16418,18498,18498,18498,18498,18498,20,19,0,0,0,0,0,0,0x1da000,0x702600,0xc2ac00,0x1033080,0x155b680,0x1963b00,0x1e8c100,0x2294580,0x27bcb80,0x2bc5000,0x30ed600,0x3615c00,0x3a1e080,0x3f46680,0x434eb00,0x65e80,0x4c75800,0x6fc00,0x559c500,0x8bf80,0x8bf80,0x8bf80,0x8bf80,0x8bf80,0x8bf80,0x8bf80,0x8bf80,0x8bf80,0x8bf80,0x8bf80,0x8bf80,0x8bf80,65,1057,2049,2857,3849,4657,5649,6457,7449,8257,9249,10241,11049,12041,12849,13832,14634,15617,16419,17190,17711,17711,17711,17711,21,20,0,0,0,0,0,0,0x1daa80,0x703080,0xc2b680,0x1033b00,0x155c100,0x1964580,0x1e8cb80,0x2295000,0x27bd600,0x2bc5a80,0x30ee080,0x3616680,0x3a1eb00,0x3f47100,0x434f580,0x4871d00,0x4c76280,0x5198a00,0x559cf80,0x598cf80,0x5c31900,0x5c31900,0x5c31900,0x5c31900,0x5c31900,0x5c31900,0x5c31900,0x5c31900,0x5c31900,0x5c31900,0x5c31900,0x5c31900,66,1058,2050,2858,3850,4658,5650,6458,7450,8258,9250,10242,11050,12042,12850,13833,14635,15618,16420,17191,17712,18224,18224,18224,22,21,0,0,0,0,0,0,0x1db500,0x703b00,0xc2c100,0x1034580,0x155cb80,0x1965000,0x1e8d600,0x2295a80,0x27be080,0x2bc6500,0x30eeb00,0x3617100,0x3a1f580,0x3f47b80,0x4350000,0x4872780,0x4c76d00,0x5199480,0x559da00,0x598da00,0x5c32380,0x5ed0e80,0x5ed0e80,0x5ed0e80,0x5ed0e80,0x5ed0e80,0x5ed0e80,0x5ed0e80,0x5ed0e80,0x5ed0e80,0x5ed0e80,0x5ed0e80,67,1059,2051,2859,3851,4659,5651,6459,7451,8259,9251,10243,11051,12043,12851,13834,14636,15619,16421,17192,17713,17713,17713,17713,21,20,0,0,0,0,0,0,0x1dbf80,0x704580,0xc2cb80,0x1035000,0x155d600,0x1965a80,0x1e8e080,0x2296500,0x27beb00,0x2bc6f80,0x30ef580,0x3617b80,0x3a20000,0x3f48600,0x4350a80,0x4873200,0x4c77780,0x5199f00,0x559e480,0x598e480,0x5c32e00,0x5c32e00,0x5c32e00,0x5c32e00,0x5c32e00,0x5c32e00,0x5c32e00,0x5c32e00,0x5c32e00,0x5c32e00,0x5c32e00,0x5c32e00,68,1060,2052,2860,3852,4660,5652,6460,7452,8260,9252,10244,11052,12044,12852,13835,14637,15620,16422,17193,17714,17714,17714,17714,21,20,0,0,0,0,0,0,0x1dca00,0x705000,0xc2d600,0x1035a80,0x155e080,0x1966500,0x1e8eb00,0x2296f80,0x27bf580,0x2bc7a00,0x30f0000,0x3618600,0x3a20a80,0x3f49080,0x4351500,0x4873c80,0x4c78200,0x519a980,0x559ef00,0x598ef00,0x5c33880,0x5c33880,0x5c33880,0x5c33880,0x5c33880,0x5c33880,0x5c33880,0x5c33880,0x5c33880,0x5c33880,0x5c33880,0x5c33880,69,1061,2053,2861,3853,4661,5653,6461,7453,8261,9253,10245,11053,12045,12853,13836,14638,15621,16423,18499,18499,18499,18499,18499,20,19,0,0,0,0,0,0,0x1dd480,0x705a80,0xc2e080,0x1036500,0x155eb00,0x1966f80,0x1e8f580,0x2297a00,0x27c0000,0x2bc8480,0x30f0a80,0x3619080,0x3a21500,0x3f49b00,0x4351f80,0x4874700,0x4c78c80,0x519b400,0x559f980,0x8ca00,0x8ca00,0x8ca00,0x8ca00,0x8ca00,0x8ca00,0x8ca00,0x8ca00,0x8ca00,0x8ca00,0x8ca00,0x8ca00,0x8ca00,70,1062,2054,2862,3854,4662,5654,6462,7454,8262,9254,10246,11054,12046,12854,13837,14639,15622,16424,18500,17715,17715,17715,17715,21,20,0,0,0,0,0,0,0x1ddf00,0x706500,0xc2eb00,0x1036f80,0x155f580,0x1967a00,0x1e90000,0x2298480,0x27c0a80,0x2bc8f00,0x30f1500,0x3619b00,0x3a21f80,0x3f4a580,0x4352a00,0x4875180,0x4c79700,0x519be80,0x55a0400,0x8d480,0x5c34300,0x5c34300,0x5c34300,0x5c34300,0x5c34300,0x5c34300,0x5c34300,0x5c34300,0x5c34300,0x5c34300,0x5c34300,0x5c34300,71,1063,2055,2863,3855,4663,5655,6463,7455,8263,9255,10247,11055,12047,12855,13838,14640,15623,16425,18501,18501,18501,18501,18501,20,19,0,0,0,0,0,0,0x1de980,0x706f80,0xc2f580,0x1037a00,0x1560000,0x1968480,0x1e90a80,0x2298f00,0x27c1500,0x2bc9980,0x30f1f80,0x361a580,0x3a22a00,0x3f4b000,0x4353480,0x4875c00,0x4c7a180,0x519c900,0x55a0e80,0x8df00,0x8df00,0x8df00,0x8df00,0x8df00,0x8df00,0x8df00,0x8df00,0x8df00,0x8df00,0x8df00,0x8df00,0x8df00,256,1064,2056,2864,3856,4664,5656,6464,7456,8448,9256,10248,11056,12048,12856,18441,14641,18456,16426,18502,18502,18502,18502,18502,20,19,0,0,0,0,0,0,0x2ff580,0x707a00,0xc30000,0x1038480,0x1560a80,0x1968f00,0x1e91500,0x2299980,0x27c1f80,0x2cea580,0x30f2a00,0x361b000,0x3a23480,0x3f4ba80,0x4353f00,0x66900,0x4c7ac00,0x70680,0x55a1900,0x8e980,0x8e980,0x8e980,0x8e980,0x8e980,0x8e980,0x8e980,0x8e980,0x8e980,0x8e980,0x8e980,0x8e980,0x8e980,257,1065,2057,2865,3857,4665,5657,6465,7457,8449,9257,10249,11057,12049,12857,13839,14642,15624,16427,17194,17716,17716,17716,17716,21,20,0,0,0,0,0,0,0x300000,0x708480,0xc30a80,0x1038f00,0x1561500,0x1969980,0x1e91f80,0x229a400,0x27c2a00,0x2ceb000,0x30f3480,0x361ba80,0x3a23f00,0x3f4c500,0x4354980,0x4876680,0x4c7b680,0x519d380,0x55a2380,0x598f980,0x5c34d80,0x5c34d80,0x5c34d80,0x5c34d80,0x5c34d80,0x5c34d80,0x5c34d80,0x5c34d80,0x5c34d80,0x5c34d80,0x5c34d80,0x5c34d80,258,1066,2058,2866,3858,4666,5658,6466,7458,8450,9258,10250,11058,12050,12858,13840,14643,15625,16428,17195,17717,18225,18225,18225,22,21,0,0,0,0,0,0,0x300a80,0x708f00,0xc31500,0x1039980,0x1561f80,0x196a400,0x1e92a00,0x229ae80,0x27c3480,0x2ceba80,0x30f3f00,0x361c500,0x3a24980,0x3f4cf80,0x4355400,0x4877100,0x4c7c100,0x519de00,0x55a2e00,0x5990400,0x5c35800,0x5ed1900,0x5ed1900,0x5ed1900,0x5ed1900,0x5ed1900,0x5ed1900,0x5ed1900,0x5ed1900,0x5ed1900,0x5ed1900,0x5ed1900,259,1067,2059,2867,3859,4667,5659,6467,7459,8451,9259,10251,11059,12051,12859,13841,14644,15626,16429,17196,17718,17718,17718,17718,21,20,0,0,0,0,0,0,0x301500,0x709980,0xc31f80,0x103a400,0x1562a00,0x196ae80,0x1e93480,0x229b900,0x27c3f00,0x2cec500,0x30f4980,0x361cf80,0x3a25400,0x3f4da00,0x4355e80,0x4877b80,0x4c7cb80,0x519e880,0x55a3880,0x5990e80,0x5c36280,0x5c36280,0x5c36280,0x5c36280,0x5c36280,0x5c36280,0x5c36280,0x5c36280,0x5c36280,0x5c36280,0x5c36280,0x5c36280,260,1068,2060,2868,3860,4668,5660,6468,7460,8452,9260,10252,11060,12052,12860,13842,14645,15627,16430,17197,17719,17719,17719,17719,21,20,0,0,0,0,0,0,0x301f80,0x70a400,0xc32a00,0x103ae80,0x1563480,0x196b900,0x1e93f00,0x229c380,0x27c4980,0x2cecf80,0x30f5400,0x361da00,0x3a25e80,0x3f4e480,0x4356900,0x4878600,0x4c7d600,0x519f300,0x55a4300,0x5991900,0x5c36d00,0x5c36d00,0x5c36d00,0x5c36d00,0x5c36d00,0x5c36d00,0x5c36d00,0x5c36d00,0x5c36d00,0x5c36d00,0x5c36d00,0x5c36d00,261,1069,2061,2869,3861,4669,5661,6469,7461,8453,9261,10253,11061,12053,12861,13843,14646,15628,16431,18503,18503,18503,18503,18503,20,19,0,0,0,0,0,0,0x302a00,0x70ae80,0xc33480,0x103b900,0x1563f00,0x196c380,0x1e94980,0x229ce00,0x27c5400,0x2ceda00,0x30f5e80,0x361e480,0x3a26900,0x3f4ef00,0x4357380,0x4879080,0x4c7e080,0x519fd80,0x55a4d80,0x8f400,0x8f400,0x8f400,0x8f400,0x8f400,0x8f400,0x8f400,0x8f400,0x8f400,0x8f400,0x8f400,0x8f400,0x8f400,262,1070,2062,2870,3862,4670,5662,6470,7462,8454,9262,10254,11062,12054,12862,13844,14647,15629,16432,18504,17720,17720,17720,17720,21,20,0,0,0,0,0,0,0x303480,0x70b900,0xc33f00,0x103c380,0x1564980,0x196ce00,0x1e95400,0x229d880,0x27c5e80,0x2cee480,0x30f6900,0x361ef00,0x3a27380,0x3f4f980,0x4357e00,0x4879b00,0x4c7eb00,0x51a0800,0x55a5800,0x60000,0x5c37780,0x5c37780,0x5c37780,0x5c37780,0x5c37780,0x5c37780,0x5c37780,0x5c37780,0x5c37780,0x5c37780,0x5c37780,0x5c37780,263,1071,2063,2871,3863,4671,5663,6471,7463,8455,9263,10255,11063,12055,12863,13845,14648,15630,16433,72,72,72,72,72,20,19,0,0,0,0,0,0,0x303f00,0x70c380,0xc34980,0x103ce00,0x1565400,0x196d880,0x1e95e80,0x229e300,0x27c6900,0x2ceef00,0x30f7380,0x361f980,0x3a27e00,0x3f50400,0x4358880,0x487a580,0x4c7f580,0x51a1280,0x55a6280,0x1af580,0x1af580,0x1af580,0x1af580,0x1af580,0x1af580,0x1af580,0x1af580,0x1af580,0x1af580,0x1af580,0x1af580,0x1af580,264,1072,2064,2872,3864,4672,5664,6656,7464,8456,9264,10256,11064,12056,12864,18442,14649,18457,16434,328,328,328,328,328,20,19,0,0,0,0,0,0,0x304980,0x70ce00,0xc35400,0x103d880,0x1565e80,0x196e300,0x1e96900,0x23bef00,0x27c7380,0x2cef980,0x30f7e00,0x3620400,0x3a28880,0x3f50e80,0x4359300,0x67380,0x4c80000,0x71100,0x55a6d00,0x2feb00,0x2feb00,0x2feb00,0x2feb00,0x2feb00,0x2feb00,0x2feb00,0x2feb00,0x2feb00,0x2feb00,0x2feb00,0x2feb00,0x2feb00,265,1073,2065,2873,3865,4673,5665,6657,7465,8457,9265,10257,11065,12057,12865,13846,14650,15631,16435,17198,17721,17721,17721,17721,21,20,0,0,0,0,0,0,0x305400,0x70d880,0xc35e80,0x103e300,0x1566900,0x196ed80,0x1e97380,0x23bf980,0x27c7e00,0x2cf0400,0x30f8880,0x3620e80,0x3a29300,0x3f51900,0x4359d80,0x487b000,0x4c80a80,0x51a1d00,0x55a7780,0x5992380,0x5c38200,0x5c38200,0x5c38200,0x5c38200,0x5c38200,0x5c38200,0x5c38200,0x5c38200,0x5c38200,0x5c38200,0x5c38200,0x5c38200,266,1074,2066,2874,3866,4674,5666,6658,7466,8458,9266,10258,11066,12058,12866,13847,14651,15632,16436,17199,17722,18226,18226,18226,22,21,0,0,0,0,0,0,0x305e80,0x70e300,0xc36900,0x103ed80,0x1567380,0x196f800,0x1e97e00,0x23c0400,0x27c8880,0x2cf0e80,0x30f9300,0x3621900,0x3a29d80,0x3f52380,0x435a800,0x487ba80,0x4c81500,0x51a2780,0x55a8200,0x5992e00,0x5c38c80,0x5ed2380,0x5ed2380,0x5ed2380,0x5ed2380,0x5ed2380,0x5ed2380,0x5ed2380,0x5ed2380,0x5ed2380,0x5ed2380,0x5ed2380,267,1075,2067,2875,3867,4675,5667,6659,7467,8459,9267,10259,11067,12059,12867,13848,14652,15633,16437,17200,17723,17723,17723,17723,21,20,0,0,0,0,0,0,0x306900,0x70ed80,0xc37380,0x103f800,0x1567e00,0x1970280,0x1e98880,0x23c0e80,0x27c9300,0x2cf1900,0x30f9d80,0x3622380,0x3a2a800,0x3f52e00,0x435b280,0x487c500,0x4c81f80,0x51a3200,0x55a8c80,0x5993880,0x5c39700,0x5c39700,0x5c39700,0x5c39700,0x5c39700,0x5c39700,0x5c39700,0x5c39700,0x5c39700,0x5c39700,0x5c39700,0x5c39700,268,1076,2068,2876,3868,4676,5668,6660,7468,8460,9268,10260,11068,12060,12868,13849,14653,15634,16438,17201,17724,17724,17724,17724,21,20,0,0,0,0,0,0,0x307380,0x70f800,0xc37e00,0x1040280,0x1568880,0x1970d00,0x1e99300,0x23c1900,0x27c9d80,0x2cf2380,0x30fa800,0x3622e00,0x3a2b280,0x3f53880,0x435bd00,0x487cf80,0x4c82a00,0x51a3c80,0x55a9700,0x5994300,0x5c3a180,0x5c3a180,0x5c3a180,0x5c3a180,0x5c3a180,0x5c3a180,0x5c3a180,0x5c3a180,0x5c3a180,0x5c3a180,0x5c3a180,0x5c3a180,269,1077,2069,2877,3869,4677,5669,6661,7469,8461,9269,10261,11069,12061,12869,13850,14654,15635,16439,584,584,584,584,584,20,19,0,0,0,0,0,0,0x307e00,0x710280,0xc38880,0x1040d00,0x1569300,0x1971780,0x1e99d80,0x23c2380,0x27ca800,0x2cf2e00,0x30fb280,0x3623880,0x3a2bd00,0x3f54300,0x435c780,0x487da00,0x4c83480,0x51a4700,0x55aa180,0x44e080,0x44e080,0x44e080,0x44e080,0x44e080,0x44e080,0x44e080,0x44e080,0x44e080,0x44e080,0x44e080,0x44e080,0x44e080,270,1078,2070,2878,3870,4678,5670,6662,7470,8462,9270,10262,11070,12062,12870,13851,14655,15636,16440,840,17725,17725,17725,17725,21,20,0,0,0,0,0,0,0x308880,0x710d00,0xc39300,0x1041780,0x1569d80,0x1972200,0x1e9a800,0x23c2e00,0x27cb280,0x2cf3880,0x30fbd00,0x3624300,0x3a2c780,0x3f54d80,0x435d200,0x487e480,0x4c83f00,0x51a5180,0x55aac00,0x59d600,0x5c3ac00,0x5c3ac00,0x5c3ac00,0x5c3ac00,0x5c3ac00,0x5c3ac00,0x5c3ac00,0x5c3ac00,0x5c3ac00,0x5c3ac00,0x5c3ac00,0x5c3ac00,271,1079,2071,2879,3871,4679,5671,6663,7471,8463,9271,10263,11071,12063,12871,13852,14656,15637,16441,1096,1096,1096,1096,1096,20,19,0,0,0,0,0,0,0x309300,0x711780,0xc39d80,0x1042200,0x156a800,0x1972c80,0x1e9b280,0x23c3880,0x27cbd00,0x2cf4300,0x30fc780,0x3624d80,0x3a2d200,0x3f55800,0x435dc80,0x487ef00,0x4c84980,0x51a5c00,0x55ab680,0x6ecb80,0x6ecb80,0x6ecb80,0x6ecb80,0x6ecb80,0x6ecb80,0x6ecb80,0x6ecb80,0x6ecb80,0x6ecb80,0x6ecb80,0x6ecb80,0x6ecb80,272,1080,2072,2880,3872,4864,5672,6664,7472,8464,9272,10264,11072,12064,13056,18443,14657,18458,16442,1352,1352,1352,1352,1352,20,19,0,0,0,0,0,0,0x309d80,0x712200,0xc3a800,0x1042c80,0x156b280,0x1a93880,0x1e9bd00,0x23c4300,0x27cc780,0x2cf4d80,0x30fd200,0x3625800,0x3a2dc80,0x3f56280,0x447e880,0x67e00,0x4c85400,0x71b80,0x55ac100,0x83c100,0x83c100,0x83c100,0x83c100,0x83c100,0x83c100,0x83c100,0x83c100,0x83c100,0x83c100,0x83c100,0x83c100,0x83c100,273,1081,2073,2881,3873,4865,5673,6665,7473,8465,9273,10265,11073,12065,13057,13853,14658,15638,16443,17202,17726,17726,17726,17726,21,20,0,0,0,0,0,0,0x30a800,0x712c80,0xc3b280,0x1043700,0x156bd00,0x1a94300,0x1e9c780,0x23c4d80,0x27cd200,0x2cf5800,0x30fdc80,0x3626280,0x3a2e700,0x3f56d00,0x447f300,0x487f980,0x4c85e80,0x51a6680,0x55acb80,0x5994d80,0x5c3b680,0x5c3b680,0x5c3b680,0x5c3b680,0x5c3b680,0x5c3b680,0x5c3b680,0x5c3b680,0x5c3b680,0x5c3b680,0x5c3b680,0x5c3b680,274,1082,2074,2882,3874,4866,5674,6666,7474,8466,9274,10266,11074,12066,13058,13854,14659,15639,16444,17203,17727,18227,18227,18227,22,21,0,0,0,0,0,0,0x30b280,0x713700,0xc3bd00,0x1044180,0x156c780,0x1a94d80,0x1e9d200,0x23c5800,0x27cdc80,0x2cf6280,0x30fe700,0x3626d00,0x3a2f180,0x3f57780,0x447fd80,0x4880400,0x4c86900,0x51a7100,0x55ad600,0x5995800,0x5c3c100,0x5ed2e00,0x5ed2e00,0x5ed2e00,0x5ed2e00,0x5ed2e00,0x5ed2e00,0x5ed2e00,0x5ed2e00,0x5ed2e00,0x5ed2e00,0x5ed2e00,275,1083,2075,2883,3875,4867,5675,6667,7475,8467,9275,10267,11075,12067,13059,13855,14660,15640,16445,17204,17728,17728,17728,17728,21,20,0,0,0,0,0,0,0x30bd00,0x714180,0xc3c780,0x1044c00,0x156d200,0x1a95800,0x1e9dc80,0x23c6280,0x27ce700,0x2cf6d00,0x30ff180,0x3627780,0x3a2fc00,0x3f58200,0x4480800,0x4880e80,0x4c87380,0x51a7b80,0x55ae080,0x5996280,0x5c3cb80,0x5c3cb80,0x5c3cb80,0x5c3cb80,0x5c3cb80,0x5c3cb80,0x5c3cb80,0x5c3cb80,0x5c3cb80,0x5c3cb80,0x5c3cb80,0x5c3cb80,276,1084,2076,2884,3876,4868,5676,6668,7476,8468,9276,10268,11076,12068,13060,13856,14661,15641,16446,17205,17729,17729,17729,17729,21,20,0,0,0,0,0,0,0x30c780,0x714c00,0xc3d200,0x1045680,0x156dc80,0x1a96280,0x1e9e700,0x23c6d00,0x27cf180,0x2cf7780,0x30ffc00,0x3628200,0x3a30680,0x3f58c80,0x4481280,0x4881900,0x4c87e00,0x51a8600,0x55aeb00,0x5996d00,0x5c3d600,0x5c3d600,0x5c3d600,0x5c3d600,0x5c3d600,0x5c3d600,0x5c3d600,0x5c3d600,0x5c3d600,0x5c3d600,0x5c3d600,0x5c3d600,277,1085,2077,2885,3877,4869,5677,6669,7477,8469,9277,10269,11077,12069,13061,13857,14662,15642,16447,1608,1608,1608,1608,1608,20,19,0,0,0,0,0,0,0x30d200,0x715680,0xc3dc80,0x1046100,0x156e700,0x1a96d00,0x1e9f180,0x23c7780,0x27cfc00,0x2cf8200,0x3100680,0x3628c80,0x3a31100,0x3f59700,0x4481d00,0x4882380,0x4c88880,0x51a9080,0x55af580,0x98b680,0x98b680,0x98b680,0x98b680,0x98b680,0x98b680,0x98b680,0x98b680,0x98b680,0x98b680,0x98b680,0x98b680,0x98b680,278,1086,2078,2886,3878,4870,5678,6670,7478,8470,9278,10270,11078,12070,13062,13858,14663,15643,16448,1864,17730,17730,17730,17730,21,20,0,0,0,0,0,0,0x30dc80,0x716100,0xc3e700,0x1046b80,0x156f180,0x1a97780,0x1e9fc00,0x23c8200,0x27d0680,0x2cf8c80,0x3101100,0x3629700,0x3a31b80,0x3f5a180,0x4482780,0x4882e00,0x4c89300,0x51a9b00,0x55b0000,0xadac00,0x5c3e080,0x5c3e080,0x5c3e080,0x5c3e080,0x5c3e080,0x5c3e080,0x5c3e080,0x5c3e080,0x5c3e080,0x5c3e080,0x5c3e080,0x5c3e080,279,1087,2079,2887,3879,4871,5679,6671,7479,8471,9279,10271,11079,12071,13063,13859,14848,15644,16449,2120,2120,2120,2120,2120,20,19,0,0,0,0,0,0,0x30e700,0x716b80,0xc3f180,0x1047600,0x156fc00,0x1a98200,0x1ea0680,0x23c8c80,0x27d1100,0x2cf9700,0x3101b80,0x362a180,0x3a32600,0x3f5ac00,0x4483200,0x4883880,0x4da9f00,0x51aa580,0x55b0a80,0xc2a180,0xc2a180,0xc2a180,0xc2a180,0xc2a180,0xc2a180,0xc2a180,0xc2a180,0xc2a180,0xc2a180,0xc2a180,0xc2a180,0xc2a180,280,1088,2080,3072,3880,4872,5680,6672,7480,8472,9280,10272,11264,12072,13064,18444,14849,18459,16450,2376,2376,2376,2376,2376,20,19,0,0,0,0,0,0,0x30f180,0x717600,0xc3fc00,0x1168200,0x1570680,0x1a98c80,0x1ea1100,0x23c9700,0x27d1b80,0x2cfa180,0x3102600,0x362ac00,0x3b53200,0x3f5b680,0x4483c80,0x68880,0x4daa980,0x72600,0x55b1500,0xd79700,0xd79700,0xd79700,0xd79700,0xd79700,0xd79700,0xd79700,0xd79700,0xd79700,0xd79700,0xd79700,0xd79700,0xd79700,281,1089,2081,3073,3881,4873,5681,6673,7481,8473,9281,10273,11265,12073,13065,13860,14850,15645,16451,17206,17731,17731,17731,17731,21,20,0,0,0,0,0,0,0x30fc00,0x718080,0xc40680,0x1168c80,0x1571100,0x1a99700,0x1ea1b80,0x23ca180,0x27d2600,0x2cfac00,0x3103080,0x362b680,0x3b53c80,0x3f5c100,0x4484700,0x4884300,0x4dab400,0x51ab000,0x55b1f80,0x5997780,0x5c3eb00,0x5c3eb00,0x5c3eb00,0x5c3eb00,0x5c3eb00,0x5c3eb00,0x5c3eb00,0x5c3eb00,0x5c3eb00,0x5c3eb00,0x5c3eb00,0x5c3eb00,282,1090,2082,3074,3882,4874,5682,6674,7482,8474,9282,10274,11266,12074,13066,13861,14851,15646,16452,17207,17732,18228,18228,18228,22,21,0,0,0,0,0,0,0x310680,0x718b00,0xc41100,0x1169700,0x1571b80,0x1a9a180,0x1ea2600,0x23cac00,0x27d3080,0x2cfb680,0x3103b00,0x362c100,0x3b54700,0x3f5cb80,0x4485180,0x4884d80,0x4dabe80,0x51aba80,0x55b2a00,0x5998200,0x5c3f580,0x5ed3880,0x5ed3880,0x5ed3880,0x5ed3880,0x5ed3880,0x5ed3880,0x5ed3880,0x5ed3880,0x5ed3880,0x5ed3880,0x5ed3880,283,1091,2083,3075,3883,4875,5683,6675,7483,8475,9283,10275,11267,12075,13067,13862,14852,15647,16453,17208,17733,17733,17733,17733,21,20,0,0,0,0,0,0,0x311100,0x719580,0xc41b80,0x116a180,0x1572600,0x1a9ac00,0x1ea3080,0x23cb680,0x27d3b00,0x2cfc100,0x3104580,0x362cb80,0x3b55180,0x3f5d600,0x4485c00,0x4885800,0x4dac900,0x51ac500,0x55b3480,0x5998c80,0x5c40000,0x5c40000,0x5c40000,0x5c40000,0x5c40000,0x5c40000,0x5c40000,0x5c40000,0x5c40000,0x5c40000,0x5c40000,0x5c40000,284,1092,2084,3076,3884,4876,5684,6676,7484,8476,9284,10276,11268,12076,13068,13863,14853,15648,16454,17209,17734,17734,17734,17734,21,20,0,0,0,0,0,0,0x311b80,0x71a000,0xc42600,0x116ac00,0x1573080,0x1a9b680,0x1ea3b00,0x23cc100,0x27d4580,0x2cfcb80,0x3105000,0x362d600,0x3b55c00,0x3f5e080,0x4486680,0x4886280,0x4dad380,0x51acf80,0x55b3f00,0x5999700,0x5c40a80,0x5c40a80,0x5c40a80,0x5c40a80,0x5c40a80,0x5c40a80,0x5c40a80,0x5c40a80,0x5c40a80,0x5c40a80,0x5c40a80,0x5c40a80,285,1093,2085,3077,3885,4877,5685,6677,7485,8477,9285,10277,11269,12077,13069,13864,14854,15649,16455,2632,2632,2632,2632,2632,20,19,0,0,0,0,0,0,0x312600,0x71aa80,0xc43080,0x116b680,0x1573b00,0x1a9c100,0x1ea4580,0x23ccb80,0x27d5000,0x2cfd600,0x3105a80,0x362e080,0x3b56680,0x3f5eb00,0x4487100,0x4886d00,0x4dade00,0x51ada00,0x55b4980,0xec8c80,0xec8c80,0xec8c80,0xec8c80,0xec8c80,0xec8c80,0xec8c80,0xec8c80,0xec8c80,0xec8c80,0xec8c80,0xec8c80,0xec8c80,286,1094,2086,3078,3886,4878,5686,6678,7486,8478,9286,10278,11270,12078,13070,13865,14855,15650,16640,2888,17735,17735,17735,17735,21,20,0,0,0,0,0,0,0x313080,0x71b500,0xc43b00,0x116c100,0x1574580,0x1a9cb80,0x1ea5000,0x23cd600,0x27d5a80,0x2cfe080,0x3106500,0x362eb00,0x3b57100,0x3f5f580,0x4487b80,0x4887780,0x4dae880,0x51ae480,0x56d5580,0x1018200,0x5c41500,0x5c41500,0x5c41500,0x5c41500,0x5c41500,0x5c41500,0x5c41500,0x5c41500,0x5c41500,0x5c41500,0x5c41500,0x5c41500,287,1095,2087,3079,3887,4879,5687,6679,7487,8479,9287,10279,11271,12079,13071,13866,14856,15651,16641,3144,3144,3144,3144,3144,20,19,0,0,0,0,0,0,0x313b00,0x71bf80,0xc44580,0x116cb80,0x1575000,0x1a9d600,0x1ea5a80,0x23ce080,0x27d6500,0x2cfeb00,0x3106f80,0x362f580,0x3b57b80,0x3f60000,0x4488600,0x4888200,0x4daf300,0x51aef00,0x56d6000,0x1167780,0x1167780,0x1167780,0x1167780,0x1167780,0x1167780,0x1167780,0x1167780,0x1167780,0x1167780,0x1167780,0x1167780,0x1167780,288,1280,2088,3080,3888,4880,5688,6680,7488,8480,9472,10280,11272,12080,13072,18445,14857,18460,16642,3400,3400,3400,3400,3400,20,19,0,0,0,0,0,0,0x314580,0x83cb80,0xc45000,0x116d600,0x1575a80,0x1a9e080,0x1ea6500,0x23ceb00,0x27d6f80,0x2cff580,0x3227b80,0x3630000,0x3b58600,0x3f60a80,0x4489080,0x69300,0x4dafd80,0x73080,0x56d6a80,0x12b6d00,0x12b6d00,0x12b6d00,0x12b6d00,0x12b6d00,0x12b6d00,0x12b6d00,0x12b6d00,0x12b6d00,0x12b6d00,0x12b6d00,0x12b6d00,0x12b6d00,289,1281,2089,3081,3889,4881,5689,6681,7489,8481,9473,10281,11273,12081,13073,13867,14858,15652,16643,17210,17920,17920,17920,17920,21,20,0,0,0,0,0,0,0x315000,0x83d600,0xc45a80,0x116e080,0x1576500,0x1a9eb00,0x1ea6f80,0x23cf580,0x27d7a00,0x2d00000,0x3228600,0x3630a80,0x3b59080,0x3f61500,0x4489b00,0x4888c80,0x4db0800,0x51af980,0x56d7500,0x599a180,0x5d62100,0x5d62100,0x5d62100,0x5d62100,0x5d62100,0x5d62100,0x5d62100,0x5d62100,0x5d62100,0x5d62100,0x5d62100,0x5d62100,290,1282,2090,3082,3890,4882,5690,6682,7490,8482,9474,10282,11274,12082,13074,13868,14859,15653,16644,17211,17921,18229,18229,18229,22,21,0,0,0,0,0,0,0x315a80,0x83e080,0xc46500,0x116eb00,0x1576f80,0x1a9f580,0x1ea7a00,0x23d0000,0x27d8480,0x2d00a80,0x3229080,0x3631500,0x3b59b00,0x3f61f80,0x448a580,0x4889700,0x4db1280,0x51b0400,0x56d7f80,0x599ac00,0x5d62b80,0x5ed4300,0x5ed4300,0x5ed4300,0x5ed4300,0x5ed4300,0x5ed4300,0x5ed4300,0x5ed4300,0x5ed4300,0x5ed4300,0x5ed4300,291,1283,2091,3083,3891,4883,5691,6683,7491,8483,9475,10283,11275,12083,13075,13869,14860,15654,16645,17212,17922,17922,17922,17922,21,20,0,0,0,0,0,0,0x316500,0x83eb00,0xc46f80,0x116f580,0x1577a00,0x1aa0000,0x1ea8480,0x23d0a80,0x27d8f00,0x2d01500,0x3229b00,0x3631f80,0x3b5a580,0x3f62a00,0x448b000,0x488a180,0x4db1d00,0x51b0e80,0x56d8a00,0x599b680,0x5d63600,0x5d63600,0x5d63600,0x5d63600,0x5d63600,0x5d63600,0x5d63600,0x5d63600,0x5d63600,0x5d63600,0x5d63600,0x5d63600,292,1284,2092,3084,3892,4884,5692,6684,7492,8484,9476,10284,11276,12084,13076,13870,14861,15655,16646,17213,17923,17923,17923,17923,21,20,0,0,0,0,0,0,0x316f80,0x83f580,0xc47a00,0x1170000,0x1578480,0x1aa0a80,0x1ea8f00,0x23d1500,0x27d9980,0x2d01f80,0x322a580,0x3632a00,0x3b5b000,0x3f63480,0x448ba80,0x488ac00,0x4db2780,0x51b1900,0x56d9480,0x599c100,0x5d64080,0x5d64080,0x5d64080,0x5d64080,0x5d64080,0x5d64080,0x5d64080,0x5d64080,0x5d64080,0x5d64080,0x5d64080,0x5d64080,293,1285,2093,3085,3893,4885,5693,6685,7493,8485,9477,10285,11277,12085,13077,13871,14862,15656,16647,3656,3656,3656,3656,3656,20,19,0,0,0,0,0,0,0x317a00,0x840000,0xc48480,0x1170a80,0x1578f00,0x1aa1500,0x1ea9980,0x23d1f80,0x27da400,0x2d02a00,0x322b000,0x3633480,0x3b5ba80,0x3f63f00,0x448c500,0x488b680,0x4db3200,0x51b2380,0x56d9f00,0x1406280,0x1406280,0x1406280,0x1406280,0x1406280,0x1406280,0x1406280,0x1406280,0x1406280,0x1406280,0x1406280,0x1406280,0x1406280,294,1286,2094,3086,3894,4886,5694,6686,7494,8486,9478,10286,11278,12086,13078,13872,14863,15657,16648,3912,17924,17924,17924,17924,21,20,0,0,0,0,0,0,0x318480,0x840a80,0xc48f00,0x1171500,0x1579980,0x1aa1f80,0x1eaa400,0x23d2a00,0x27dae80,0x2d03480,0x322ba80,0x3633f00,0x3b5c500,0x3f64980,0x448cf80,0x488c100,0x4db3c80,0x51b2e00,0x56da980,0x1555800,0x5d64b00,0x5d64b00,0x5d64b00,0x5d64b00,0x5d64b00,0x5d64b00,0x5d64b00,0x5d64b00,0x5d64b00,0x5d64b00,0x5d64b00,0x5d64b00,295,1287,2095,3087,3895,4887,5695,6687,7495,8487,9479,10287,11279,12087,13079,13873,14864,15658,16649,4168,4168,4168,4168,4168,20,19,0,0,0,0,0,0,0x318f00,0x841500,0xc49980,0x1171f80,0x157a400,0x1aa2a00,0x1eaae80,0x23d3480,0x27db900,0x2d03f00,0x322c500,0x3634980,0x3b5cf80,0x3f65400,0x448da00,0x488cb80,0x4db4700,0x51b3880,0x56db400,0x16a4d80,0x16a4d80,0x16a4d80,0x16a4d80,0x16a4d80,0x16a4d80,0x16a4d80,0x16a4d80,0x16a4d80,0x16a4d80,0x16a4d80,0x16a4d80,0x16a4d80,296,1288,2096,3088,3896,4888,5696,6688,7680,8488,9480,10288,11280,12088,13080,18446,14865,18461,16650,4424,4424,4424,4424,4424,20,19,0,0,0,0,0,0,0x319980,0x841f80,0xc4a400,0x1172a00,0x157ae80,0x1aa3480,0x1eab900,0x23d3f00,0x28fc500,0x2d04980,0x322cf80,0x3635400,0x3b5da00,0x3f65e80,0x448e480,0x69d80,0x4db5180,0x73b00,0x56dbe80,0x17f4300,0x17f4300,0x17f4300,0x17f4300,0x17f4300,0x17f4300,0x17f4300,0x17f4300,0x17f4300,0x17f4300,0x17f4300,0x17f4300,0x17f4300,297,1289,2097,3089,3897,4889,5697,6689,7681,8489,9481,10289,11281,12089,13081,13874,14866,15659,16651,17214,17925,17925,17925,17925,21,20,0,0,0,0,0,0,0x31a400,0x842a00,0xc4ae80,0x1173480,0x157b900,0x1aa3f00,0x1eac380,0x23d4980,0x28fcf80,0x2d05400,0x322da00,0x3635e80,0x3b5e480,0x3f66900,0x448ef00,0x488d600,0x4db5c00,0x51b4300,0x56dc900,0x599cb80,0x5d65580,0x5d65580,0x5d65580,0x5d65580,0x5d65580,0x5d65580,0x5d65580,0x5d65580,0x5d65580,0x5d65580,0x5d65580,0x5d65580,298,1290,2098,3090,3898,4890,5698,6690,7682,8490,9482,10290,11282,12090,13082,13875,14867,15660,16652,17215,17926,18230,18230,18230,22,21,0,0,0,0,0,0,0x31ae80,0x843480,0xc4b900,0x1173f00,0x157c380,0x1aa4980,0x1eace00,0x23d5400,0x28fda00,0x2d05e80,0x322e480,0x3636900,0x3b5ef00,0x3f67380,0x448f980,0x488e080,0x4db6680,0x51b4d80,0x56dd380,0x599d600,0x5d66000,0x5ed4d80,0x5ed4d80,0x5ed4d80,0x5ed4d80,0x5ed4d80,0x5ed4d80,0x5ed4d80,0x5ed4d80,0x5ed4d80,0x5ed4d80,0x5ed4d80,299,1291,2099,3091,3899,4891,5699,6691,7683,8491,9483,10291,11283,12091,13083,13876,14868,15661,16653,17216,17927,17927,17927,17927,21,20,0,0,0,0,0,0,0x31b900,0x843f00,0xc4c380,0x1174980,0x157ce00,0x1aa5400,0x1ead880,0x23d5e80,0x28fe480,0x2d06900,0x322ef00,0x3637380,0x3b5f980,0x3f67e00,0x4490400,0x488eb00,0x4db7100,0x51b5800,0x56dde00,0x599e080,0x5d66a80,0x5d66a80,0x5d66a80,0x5d66a80,0x5d66a80,0x5d66a80,0x5d66a80,0x5d66a80,0x5d66a80,0x5d66a80,0x5d66a80,0x5d66a80,300,1292,2100,3092,3900,4892,5700,6692,7684,8492,9484,10292,11284,12092,13084,13877,14869,15662,16654,17217,17928,17928,17928,17928,21,20,0,0,0,0,0,0,0x31c380,0x844980,0xc4ce00,0x1175400,0x157d880,0x1aa5e80,0x1eae300,0x23d6900,0x28fef00,0x2d07380,0x322f980,0x3637e00,0x3b60400,0x3f68880,0x4490e80,0x488f580,0x4db7b80,0x51b6280,0x56de880,0x599eb00,0x5d67500,0x5d67500,0x5d67500,0x5d67500,0x5d67500,0x5d67500,0x5d67500,0x5d67500,0x5d67500,0x5d67500,0x5d67500,0x5d67500,301,1293,2101,3093,3901,4893,5701,6693,7685,8493,9485,10293,11285,12093,13085,13878,14870,15663,16655,4680,4680,4680,4680,4680,20,19,0,0,0,0,0,0,0x31ce00,0x845400,0xc4d880,0x1175e80,0x157e300,0x1aa6900,0x1eaed80,0x23d7380,0x28ff980,0x2d07e00,0x3230400,0x3638880,0x3b60e80,0x3f69300,0x4491900,0x4890000,0x4db8600,0x51b6d00,0x56df300,0x1943880,0x1943880,0x1943880,0x1943880,0x1943880,0x1943880,0x1943880,0x1943880,0x1943880,0x1943880,0x1943880,0x1943880,0x1943880,302,1294,2102,3094,3902,4894,5702,6694,7686,8494,9486,10294,11286,12094,13086,13879,14871,15664,16656,4936,17929,17929,17929,17929,21,20,0,0,0,0,0,0,0x31d880,0x845e80,0xc4e300,0x1176900,0x157ed80,0x1aa7380,0x1eaf800,0x23d7e00,0x2900400,0x2d08880,0x3230e80,0x3639300,0x3b61900,0x3f69d80,0x4492380,0x4890a80,0x4db9080,0x51b7780,0x56dfd80,0x1a92e00,0x5d67f80,0x5d67f80,0x5d67f80,0x5d67f80,0x5d67f80,0x5d67f80,0x5d67f80,0x5d67f80,0x5d67f80,0x5d67f80,0x5d67f80,0x5d67f80,303,1295,2103,3095,3903,4895,5703,6695,7687,8495,9487,10295,11287,12095,13087,13880,14872,15665,16657,5192,5192,5192,5192,5192,20,19,0,0,0,0,0,0,0x31e300,0x846900,0xc4ed80,0x1177380,0x157f800,0x1aa7e00,0x1eb0280,0x23d8880,0x2900e80,0x2d09300,0x3231900,0x3639d80,0x3b62380,0x3f6a800,0x4492e00,0x4891500,0x4db9b00,0x51b8200,0x56e0800,0x1be2380,0x1be2380,0x1be2380,0x1be2380,0x1be2380,0x1be2380,0x1be2380,0x1be2380,0x1be2380,0x1be2380,0x1be2380,0x1be2380,0x1be2380,304,1296,2104,3096,3904,4896,5888,6696,7688,8496,9488,10296,11288,12096,13088,13881,14873,15666,16658,17218,17930,17930,17930,17930,21,20,0,0,0,0,0,0,0x31ed80,0x847380,0xc4f800,0x1177e00,0x1580280,0x1aa8880,0x1fd0e80,0x23d9300,0x2901900,0x2d09d80,0x3232380,0x363a800,0x3b62e00,0x3f6b280,0x4493880,0x4891f80,0x4dba580,0x51b8c80,0x56e1280,0x599f580,0x5d68a00,0x5d68a00,0x5d68a00,0x5d68a00,0x5d68a00,0x5d68a00,0x5d68a00,0x5d68a00,0x5d68a00,0x5d68a00,0x5d68a00,0x5d68a00,305,1297,2105,3097,3905,4897,5889,6697,7689,8497,9489,10297,11289,12097,13089,13882,14874,15667,16659,17219,17931,17931,17931,17931,21,20,0,0,0,0,0,0,0x31f800,0x847e00,0xc50280,0x1178880,0x1580d00,0x1aa9300,0x1fd1900,0x23d9d80,0x2902380,0x2d0a800,0x3232e00,0x363b280,0x3b63880,0x3f6bd00,0x4494300,0x4892a00,0x4dbb000,0x51b9700,0x56e1d00,0x59a0000,0x5d69480,0x5d69480,0x5d69480,0x5d69480,0x5d69480,0x5d69480,0x5d69480,0x5d69480,0x5d69480,0x5d69480,0x5d69480,0x5d69480,306,1298,2106,3098,3906,4898,5890,6698,7690,8498,9490,10298,11290,12098,13090,13883,14875,15668,16660,17220,17932,18231,18231,18231,22,21,0,0,0,0,0,0,0x320280,0x848880,0xc50d00,0x1179300,0x1581780,0x1aa9d80,0x1fd2380,0x23da800,0x2902e00,0x2d0b280,0x3233880,0x363bd00,0x3b64300,0x3f6c780,0x4494d80,0x4893480,0x4dbba80,0x51ba180,0x56e2780,0x59a0a80,0x5d69f00,0x5ed5800,0x5ed5800,0x5ed5800,0x5ed5800,0x5ed5800,0x5ed5800,0x5ed5800,0x5ed5800,0x5ed5800,0x5ed5800,0x5ed5800,307,1299,2107,3099,3907,4899,5891,6699,7691,8499,9491,10299,11291,12099,13091,13884,14876,15669,16661,17221,17933,17933,17933,17933,21,20,0,0,0,0,0,0,0x320d00,0x849300,0xc51780,0x1179d80,0x1582200,0x1aaa800,0x1fd2e00,0x23db280,0x2903880,0x2d0bd00,0x3234300,0x363c780,0x3b64d80,0x3f6d200,0x4495800,0x4893f00,0x4dbc500,0x51bac00,0x56e3200,0x59a1500,0x5d6a980,0x5d6a980,0x5d6a980,0x5d6a980,0x5d6a980,0x5d6a980,0x5d6a980,0x5d6a980,0x5d6a980,0x5d6a980,0x5d6a980,0x5d6a980,308,1300,2108,3100,3908,4900,5892,6700,7692,8500,9492,10300,11292,12100,13092,13885,14877,15670,16662,17222,17934,17934,17934,17934,21,20,0,0,0,0,0,0,0x321780,0x849d80,0xc52200,0x117a800,0x1582c80,0x1aab280,0x1fd3880,0x23dbd00,0x2904300,0x2d0c780,0x3234d80,0x363d200,0x3b65800,0x3f6dc80,0x4496280,0x4894980,0x4dbcf80,0x51bb680,0x56e3c80,0x59a1f80,0x5d6b400,0x5d6b400,0x5d6b400,0x5d6b400,0x5d6b400,0x5d6b400,0x5d6b400,0x5d6b400,0x5d6b400,0x5d6b400,0x5d6b400,0x5d6b400,309,1301,2109,3101,3909,4901,5893,6701,7693,8501,9493,10301,11293,12101,13093,13886,14878,15671,16663,5448,5448,5448,5448,5448,20,19,0,0,0,0,0,0,0x322200,0x84a800,0xc52c80,0x117b280,0x1583700,0x1aabd00,0x1fd4300,0x23dc780,0x2904d80,0x2d0d200,0x3235800,0x363dc80,0x3b66280,0x3f6e700,0x4496d00,0x4895400,0x4dbda00,0x51bc100,0x56e4700,0x1d31900,0x1d31900,0x1d31900,0x1d31900,0x1d31900,0x1d31900,0x1d31900,0x1d31900,0x1d31900,0x1d31900,0x1d31900,0x1d31900,0x1d31900,310,1302,2110,3102,3910,4902,5894,6702,7694,8502,9494,10302,11294,12102,13094,13887,14879,15672,16664,5704,17935,17935,17935,17935,21,20,0,0,0,0,0,0,0x322c80,0x84b280,0xc53700,0x117bd00,0x1584180,0x1aac780,0x1fd4d80,0x23dd200,0x2905800,0x2d0dc80,0x3236280,0x363e700,0x3b66d00,0x3f6f180,0x4497780,0x4895e80,0x4dbe480,0x51bcb80,0x56e5180,0x1e80e80,0x5d6be80,0x5d6be80,0x5d6be80,0x5d6be80,0x5d6be80,0x5d6be80,0x5d6be80,0x5d6be80,0x5d6be80,0x5d6be80,0x5d6be80,0x5d6be80,311,1303,2111,3103,3911,4903,5895,6703,7695,8503,9495,10303,11295,12103,13095,13888,14880,15673,16665,5960,5960,5960,5960,5960,20,19,0,0,0,0,0,0,0x323700,0x84bd00,0xc54180,0x117c780,0x1584c00,0x1aad200,0x1fd5800,0x23ddc80,0x2906280,0x2d0e700,0x3236d00,0x363f180,0x3b67780,0x3f6fc00,0x4498200,0x4896900,0x4dbef00,0x51bd600,0x56e5c00,0x1fd0400,0x1fd0400,0x1fd0400,0x1fd0400,0x1fd0400,0x1fd0400,0x1fd0400,0x1fd0400,0x1fd0400,0x1fd0400,0x1fd0400,0x1fd0400,0x1fd0400,312,1304,2112,3104,4096,4904,5896,6704,7696,8504,9496,10304,11296,12288,13096,13889,14881,15674,16666,17223,17936,17936,17936,17936,21,20,0,0,0,0,0,0,0x324180,0x84c780,0xc54c00,0x117d200,0x16a5800,0x1aadc80,0x1fd6280,0x23de700,0x2906d00,0x2d0f180,0x3237780,0x363fc00,0x3b68200,0x4090800,0x4498c80,0x4897380,0x4dbf980,0x51be080,0x56e6680,0x59a2a00,0x5d6c900,0x5d6c900,0x5d6c900,0x5d6c900,0x5d6c900,0x5d6c900,0x5d6c900,0x5d6c900,0x5d6c900,0x5d6c900,0x5d6c900,0x5d6c900,313,1305,2113,3105,4097,4905,5897,6705,7697,8505,9497,10305,11297,12289,13097,13890,14882,15675,16667,17408,17937,17937,17937,17937,21,20,0,0,0,0,0,0,0x324c00,0x84d200,0xc55680,0x117dc80,0x16a6280,0x1aae700,0x1fd6d00,0x23df180,0x2907780,0x2d0fc00,0x3238200,0x3640680,0x3b68c80,0x4091280,0x4499700,0x4897e00,0x4dc0400,0x51beb00,0x56e7100,0x5ac3600,0x5d6d380,0x5d6d380,0x5d6d380,0x5d6d380,0x5d6d380,0x5d6d380,0x5d6d380,0x5d6d380,0x5d6d380,0x5d6d380,0x5d6d380,0x5d6d380,314,1306,2114,3106,4098,4906,5898,6706,7698,8506,9498,10306,11298,12290,13098,13891,14883,15676,16668,17409,17938,18232,18232,18232,22,21,0,0,0,0,0,0,0x325680,0x84dc80,0xc56100,0x117e700,0x16a6d00,0x1aaf180,0x1fd7780,0x23dfc00,0x2908200,0x2d10680,0x3238c80,0x3641100,0x3b69700,0x4091d00,0x449a180,0x4898880,0x4dc0e80,0x51bf580,0x56e7b80,0x5ac4080,0x5d6de00,0x5ed6280,0x5ed6280,0x5ed6280,0x5ed6280,0x5ed6280,0x5ed6280,0x5ed6280,0x5ed6280,0x5ed6280,0x5ed6280,0x5ed6280,315,1307,2115,3107,4099,4907,5899,6707,7699,8507,9499,10307,11299,12291,13099,13892,14884,15677,16669,17410,17939,17939,17939,17939,21,20,0,0,0,0,0,0,0x326100,0x84e700,0xc56b80,0x117f180,0x16a7780,0x1aafc00,0x1fd8200,0x23e0680,0x2908c80,0x2d11100,0x3239700,0x3641b80,0x3b6a180,0x4092780,0x449ac00,0x4899300,0x4dc1900,0x51c0000,0x56e8600,0x5ac4b00,0x5d6e880,0x5d6e880,0x5d6e880,0x5d6e880,0x5d6e880,0x5d6e880,0x5d6e880,0x5d6e880,0x5d6e880,0x5d6e880,0x5d6e880,0x5d6e880,316,1308,2116,3108,4100,4908,5900,6708,7700,8508,9500,10308,11300,12292,13100,13893,14885,15678,16670,17411,17940,17940,17940,17940,21,20,0,0,0,0,0,0,0x326b80,0x84f180,0xc57600,0x117fc00,0x16a8200,0x1ab0680,0x1fd8c80,0x23e1100,0x2909700,0x2d11b80,0x323a180,0x3642600,0x3b6ac00,0x4093200,0x449b680,0x4899d80,0x4dc2380,0x51c0a80,0x56e9080,0x5ac5580,0x5d6f300,0x5d6f300,0x5d6f300,0x5d6f300,0x5d6f300,0x5d6f300,0x5d6f300,0x5d6f300,0x5d6f300,0x5d6f300,0x5d6f300,0x5d6f300,317,1309,2117,3109,4101,4909,5901,6709,7701,8509,9501,10309,11301,12293,13101,13894,14886,15679,16671,6216,6216,6216,6216,6216,20,19,0,0,0,0,0,0,0x327600,0x84fc00,0xc58080,0x1180680,0x16a8c80,0x1ab1100,0x1fd9700,0x23e1b80,0x290a180,0x2d12600,0x323ac00,0x3643080,0x3b6b680,0x4093c80,0x449c100,0x489a800,0x4dc2e00,0x51c1500,0x56e9b00,0x211f980,0x211f980,0x211f980,0x211f980,0x211f980,0x211f980,0x211f980,0x211f980,0x211f980,0x211f980,0x211f980,0x211f980,0x211f980,318,1310,2118,3110,4102,4910,5902,6710,7702,8510,9502,10310,11302,12294,13102,13895,14887,15680,16672,6472,17941,17941,17941,17941,21,20,0,0,0,0,0,0,0x328080,0x850680,0xc58b00,0x1181100,0x16a9700,0x1ab1b80,0x1fda180,0x23e2600,0x290ac00,0x2d13080,0x323b680,0x3643b00,0x3b6c100,0x4094700,0x449cb80,0x489b280,0x4dc3880,0x51c1f80,0x56ea580,0x226ef00,0x5d6fd80,0x5d6fd80,0x5d6fd80,0x5d6fd80,0x5d6fd80,0x5d6fd80,0x5d6fd80,0x5d6fd80,0x5d6fd80,0x5d6fd80,0x5d6fd80,0x5d6fd80,319,1311,2119,3111,4103,4911,5903,6711,7703,8511,9503,10311,11303,12295,13103,14080,14888,15681,16673,6728,6728,6728,6728,6728,20,19,0,0,0,0,0,0,0x328b00,0x851100,0xc59580,0x1181b80,0x16aa180,0x1ab2600,0x1fdac00,0x23e3080,0x290b680,0x2d13b00,0x323c100,0x3644580,0x3b6cb80,0x4095180,0x449d600,0x49bbe80,0x4dc4300,0x51c2a00,0x56eb000,0x23be480,0x23be480,0x23be480,0x23be480,0x23be480,0x23be480,0x23be480,0x23be480,0x23be480,0x23be480,0x23be480,0x23be480,0x23be480,320,1312,2304,3112,4104,4912,5904,6712,7704,8512,9504,10496,11304,12296,13104,14081,14889,15682,16674,17412,17942,17942,17942,17942,21,20,0,0,0,0,0,0,0x329580,0x851b80,0xd7a180,0x1182600,0x16aac00,0x1ab3080,0x1fdb680,0x23e3b00,0x290c100,0x2d14580,0x323cb80,0x3765180,0x3b6d600,0x4095c00,0x449e080,0x49bc900,0x4dc4d80,0x51c3480,0x56eba80,0x5ac6000,0x5d70800,0x5d70800,0x5d70800,0x5d70800,0x5d70800,0x5d70800,0x5d70800,0x5d70800,0x5d70800,0x5d70800,0x5d70800,0x5d70800,321,1313,2305,3113,4105,4913,5905,6713,7705,8513,9505,10497,11305,12297,13105,14082,14890,15683,16675,17413,17943,17943,17943,17943,21,20,0,0,0,0,0,0,0x32a000,0x852600,0xd7ac00,0x1183080,0x16ab680,0x1ab3b00,0x1fdc100,0x23e4580,0x290cb80,0x2d15000,0x323d600,0x3765c00,0x3b6e080,0x4096680,0x449eb00,0x49bd380,0x4dc5800,0x51c3f00,0x56ec500,0x5ac6a80,0x5d71280,0x5d71280,0x5d71280,0x5d71280,0x5d71280,0x5d71280,0x5d71280,0x5d71280,0x5d71280,0x5d71280,0x5d71280,0x5d71280,322,1314,2306,3114,4106,4914,5906,6714,7706,8514,9506,10498,11306,12298,13106,14083,14891,15684,16676,17414,17944,18233,18233,18233,22,21,0,0,0,0,0,0,0x32aa80,0x853080,0xd7b680,0x1183b00,0x16ac100,0x1ab4580,0x1fdcb80,0x23e5000,0x290d600,0x2d15a80,0x323e080,0x3766680,0x3b6eb00,0x4097100,0x449f580,0x49bde00,0x4dc6280,0x51c4980,0x56ecf80,0x5ac7500,0x5d71d00,0x5ed6d00,0x5ed6d00,0x5ed6d00,0x5ed6d00,0x5ed6d00,0x5ed6d00,0x5ed6d00,0x5ed6d00,0x5ed6d00,0x5ed6d00,0x5ed6d00,323,1315,2307,3115,4107,4915,5907,6715,7707,8515,9507,10499,11307,12299,13107,14084,14892,15685,16677,17415,17945,17945,17945,17945,21,20,0,0,0,0,0,0,0x32b500,0x853b00,0xd7c100,0x1184580,0x16acb80,0x1ab5000,0x1fdd600,0x23e5a80,0x290e080,0x2d16500,0x323eb00,0x3767100,0x3b6f580,0x4097b80,0x44a0000,0x49be880,0x4dc6d00,0x51c5400,0x56eda00,0x5ac7f80,0x5d72780,0x5d72780,0x5d72780,0x5d72780,0x5d72780,0x5d72780,0x5d72780,0x5d72780,0x5d72780,0x5d72780,0x5d72780,0x5d72780,324,1316,2308,3116,4108,4916,5908,6716,7708,8516,9508,10500,11308,12300,13108,14085,14893,15686,16678,17416,17946,17946,17946,17946,21,20,0,0,0,0,0,0,0x32bf80,0x854580,0xd7cb80,0x1185000,0x16ad600,0x1ab5a80,0x1fde080,0x23e6500,0x290eb00,0x2d16f80,0x323f580,0x3767b80,0x3b70000,0x4098600,0x44a0a80,0x49bf300,0x4dc7780,0x51c5e80,0x56ee480,0x5ac8a00,0x5d73200,0x5d73200,0x5d73200,0x5d73200,0x5d73200,0x5d73200,0x5d73200,0x5d73200,0x5d73200,0x5d73200,0x5d73200,0x5d73200,325,1317,2309,3117,4109,4917,5909,6717,7709,8517,9509,10501,11309,12301,13109,14086,14894,15687,16679,6984,6984,6984,6984,6984,20,19,0,0,0,0,0,0,0x32ca00,0x855000,0xd7d600,0x1185a80,0x16ae080,0x1ab6500,0x1fdeb00,0x23e6f80,0x290f580,0x2d17a00,0x3240000,0x3768600,0x3b70a80,0x4099080,0x44a1500,0x49bfd80,0x4dc8200,0x51c6900,0x56eef00,0x250da00,0x250da00,0x250da00,0x250da00,0x250da00,0x250da00,0x250da00,0x250da00,0x250da00,0x250da00,0x250da00,0x250da00,0x250da00,326,1318,2310,3118,4110,4918,5910,6718,7710,8518,9510,10502,11310,12302,13110,14087,14895,15872,16680,7240,17947,17947,17947,17947,21,20,0,0,0,0,0,0,0x32d480,0x855a80,0xd7e080,0x1186500,0x16aeb00,0x1ab6f80,0x1fdf580,0x23e7a00,0x2910000,0x2d18480,0x3240a80,0x3769080,0x3b71500,0x4099b00,0x44a1f80,0x49c0800,0x4dc8c80,0x52e7500,0x56ef980,0x265cf80,0x5d73c80,0x5d73c80,0x5d73c80,0x5d73c80,0x5d73c80,0x5d73c80,0x5d73c80,0x5d73c80,0x5d73c80,0x5d73c80,0x5d73c80,0x5d73c80,327,1319,2311,3119,4111,4919,5911,6719,7711,8519,9511,10503,11311,12303,13111,14088,14896,15873,16681,7496,7496,7496,7496,7496,20,19,0,0,0,0,0,0,0x32df00,0x856500,0xd7eb00,0x1186f80,0x16af580,0x1ab7a00,0x1fe0000,0x23e8480,0x2910a80,0x2d18f00,0x3241500,0x3769b00,0x3b71f80,0x409a580,0x44a2a00,0x49c1280,0x4dc9700,0x52e7f80,0x56f0400,0x27ac500,0x27ac500,0x27ac500,0x27ac500,0x27ac500,0x27ac500,0x27ac500,0x27ac500,0x27ac500,0x27ac500,0x27ac500,0x27ac500,0x27ac500,512,1320,2312,3120,4112,4920,5912,6720,7712,8704,9512,10504,11312,12304,13112,14089,14897,15874,16682,17417,17948,17948,17948,17948,21,20,0,0,0,0,0,0,0x44eb00,0x856f80,0xd7f580,0x1187a00,0x16b0000,0x1ab8480,0x1fe0a80,0x23e8f00,0x2911500,0x2e39b00,0x3241f80,0x376a580,0x3b72a00,0x409b000,0x44a3480,0x49c1d00,0x4dca180,0x52e8a00,0x56f0e80,0x5ac9480,0x5d74700,0x5d74700,0x5d74700,0x5d74700,0x5d74700,0x5d74700,0x5d74700,0x5d74700,0x5d74700,0x5d74700,0x5d74700,0x5d74700,513,1321,2313,3121,4113,4921,5913,6721,7713,8705,9513,10505,11313,12305,13113,14090,14898,15875,16683,17418,17949,17949,17949,17949,21,20,0,0,0,0,0,0,0x44f580,0x857a00,0xd80000,0x1188480,0x16b0a80,0x1ab8f00,0x1fe1500,0x23e9980,0x2911f80,0x2e3a580,0x3242a00,0x376b000,0x3b73480,0x409ba80,0x44a3f00,0x49c2780,0x4dcac00,0x52e9480,0x56f1900,0x5ac9f00,0x5d75180,0x5d75180,0x5d75180,0x5d75180,0x5d75180,0x5d75180,0x5d75180,0x5d75180,0x5d75180,0x5d75180,0x5d75180,0x5d75180,514,1322,2314,3122,4114,4922,5914,6722,7714,8706,9514,10506,11314,12306,13114,14091,14899,15876,16684,17419,17950,18234,18234,18234,22,21,0,0,0,0,0,0,0x450000,0x858480,0xd80a80,0x1188f00,0x16b1500,0x1ab9980,0x1fe1f80,0x23ea400,0x2912a00,0x2e3b000,0x3243480,0x376ba80,0x3b73f00,0x409c500,0x44a4980,0x49c3200,0x4dcb680,0x52e9f00,0x56f2380,0x5aca980,0x5d75c00,0x5ed7780,0x5ed7780,0x5ed7780,0x5ed7780,0x5ed7780,0x5ed7780,0x5ed7780,0x5ed7780,0x5ed7780,0x5ed7780,0x5ed7780,515,1323,2315,3123,4115,4923,5915,6723,7715,8707,9515,10507,11315,12307,13115,14092,14900,15877,16685,17420,17951,17951,17951,17951,21,20,0,0,0,0,0,0,0x450a80,0x858f00,0xd81500,0x1189980,0x16b1f80,0x1aba400,0x1fe2a00,0x23eae80,0x2913480,0x2e3ba80,0x3243f00,0x376c500,0x3b74980,0x409cf80,0x44a5400,0x49c3c80,0x4dcc100,0x52ea980,0x56f2e00,0x5acb400,0x5d76680,0x5d76680,0x5d76680,0x5d76680,0x5d76680,0x5d76680,0x5d76680,0x5d76680,0x5d76680,0x5d76680,0x5d76680,0x5d76680,516,1324,2316,3124,4116,4924,5916,6724,7716,8708,9516,10508,11316,12308,13116,14093,14901,15878,16686,17421,17952,17952,17952,17952,21,20,0,0,0,0,0,0,0x451500,0x859980,0xd81f80,0x118a400,0x16b2a00,0x1abae80,0x1fe3480,0x23eb900,0x2913f00,0x2e3c500,0x3244980,0x376cf80,0x3b75400,0x409da00,0x44a5e80,0x49c4700,0x4dccb80,0x52eb400,0x56f3880,0x5acbe80,0x5d77100,0x5d77100,0x5d77100,0x5d77100,0x5d77100,0x5d77100,0x5d77100,0x5d77100,0x5d77100,0x5d77100,0x5d77100,0x5d77100,517,1325,2317,3125,4117,4925,5917,6725,7717,8709,9517,10509,11317,12309,13117,14094,14902,15879,16687,7752,7752,7752,7752,7752,20,19,0,0,0,0,0,0,0x451f80,0x85a400,0xd82a00,0x118ae80,0x16b3480,0x1abb900,0x1fe3f00,0x23ec380,0x2914980,0x2e3cf80,0x3245400,0x376da00,0x3b75e80,0x409e480,0x44a6900,0x49c5180,0x4dcd600,0x52ebe80,0x56f4300,0x28fba80,0x28fba80,0x28fba80,0x28fba80,0x28fba80,0x28fba80,0x28fba80,0x28fba80,0x28fba80,0x28fba80,0x28fba80,0x28fba80,0x28fba80,518,1326,2318,3126,4118,4926,5918,6726,7718,8710,9518,10510,11318,12310,13118,14095,14903,15880,16688,8008,17953,17953,17953,17953,21,20,0,0,0,0,0,0,0x452a00,0x85ae80,0xd83480,0x118b900,0x16b3f00,0x1abc380,0x1fe4980,0x23ece00,0x2915400,0x2e3da00,0x3245e80,0x376e480,0x3b76900,0x409ef00,0x44a7380,0x49c5c00,0x4dce080,0x52ec900,0x56f4d80,0x2a4b000,0x5d77b80,0x5d77b80,0x5d77b80,0x5d77b80,0x5d77b80,0x5d77b80,0x5d77b80,0x5d77b80,0x5d77b80,0x5d77b80,0x5d77b80,0x5d77b80,519,1327,2319,3127,4119,4927,5919,6727,7719,8711,9519,10511,11319,12311,13119,14096,14904,15881,16689,8264,8264,8264,8264,8264,20,19,0,0,0,0,0,0,0x453480,0x85b900,0xd83f00,0x118c380,0x16b4980,0x1abce00,0x1fe5400,0x23ed880,0x2915e80,0x2e3e480,0x3246900,0x376ef00,0x3b77380,0x409f980,0x44a7e00,0x49c6680,0x4dceb00,0x52ed380,0x56f5800,0x2b9a580,0x2b9a580,0x2b9a580,0x2b9a580,0x2b9a580,0x2b9a580,0x2b9a580,0x2b9a580,0x2b9a580,0x2b9a580,0x2b9a580,0x2b9a580,0x2b9a580,520,1328,2320,3128,4120,4928,5920,6912,7720,8712,9520,10512,11320,12312,13120,14097,14905,15882,16690,17422,17954,17954,17954,17954,21,20,0,0,0,0,0,0,0x453f00,0x85c380,0xd84980,0x118ce00,0x16b5400,0x1abd880,0x1fe5e80,0x250e480,0x2916900,0x2e3ef00,0x3247380,0x376f980,0x3b77e00,0x40a0400,0x44a8880,0x49c7100,0x4dcf580,0x52ede00,0x56f6280,0x5acc900,0x5d78600,0x5d78600,0x5d78600,0x5d78600,0x5d78600,0x5d78600,0x5d78600,0x5d78600,0x5d78600,0x5d78600,0x5d78600,0x5d78600,521,1329,2321,3129,4121,4929,5921,6913,7721,8713,9521,10513,11321,12313,13121,14098,14906,15883,16691,17423,17955,17955,17955,17955,21,20,0,0,0,0,0,0,0x454980,0x85ce00,0xd85400,0x118d880,0x16b5e80,0x1abe300,0x1fe6900,0x250ef00,0x2917380,0x2e3f980,0x3247e00,0x3770400,0x3b78880,0x40a0e80,0x44a9300,0x49c7b80,0x4dd0000,0x52ee880,0x56f6d00,0x5acd380,0x5d79080,0x5d79080,0x5d79080,0x5d79080,0x5d79080,0x5d79080,0x5d79080,0x5d79080,0x5d79080,0x5d79080,0x5d79080,0x5d79080,522,1330,2322,3130,4122,4930,5922,6914,7722,8714,9522,10514,11322,12314,13122,14099,14907,15884,16692,17424,17956,18235,18235,18235,22,21,0,0,0,0,0,0,0x455400,0x85d880,0xd85e80,0x118e300,0x16b6900,0x1abed80,0x1fe7380,0x250f980,0x2917e00,0x2e40400,0x3248880,0x3770e80,0x3b79300,0x40a1900,0x44a9d80,0x49c8600,0x4dd0a80,0x52ef300,0x56f7780,0x5acde00,0x5d79b00,0x5ed8200,0x5ed8200,0x5ed8200,0x5ed8200,0x5ed8200,0x5ed8200,0x5ed8200,0x5ed8200,0x5ed8200,0x5ed8200,0x5ed8200,523,1331,2323,3131,4123,4931,5923,6915,7723,8715,9523,10515,11323,12315,13123,14100,14908,15885,16693,17425,17957,17957,17957,17957,21,20,0,0,0,0,0,0,0x455e80,0x85e300,0xd86900,0x118ed80,0x16b7380,0x1abf800,0x1fe7e00,0x2510400,0x2918880,0x2e40e80,0x3249300,0x3771900,0x3b79d80,0x40a2380,0x44aa800,0x49c9080,0x4dd1500,0x52efd80,0x56f8200,0x5ace880,0x5d7a580,0x5d7a580,0x5d7a580,0x5d7a580,0x5d7a580,0x5d7a580,0x5d7a580,0x5d7a580,0x5d7a580,0x5d7a580,0x5d7a580,0x5d7a580,524,1332,2324,3132,4124,4932,5924,6916,7724,8716,9524,10516,11324,12316,13124,14101,14909,15886,16694,17426,17958,17958,17958,17958,21,20,0,0,0,0,0,0,0x456900,0x85ed80,0xd87380,0x118f800,0x16b7e00,0x1ac0280,0x1fe8880,0x2510e80,0x2919300,0x2e41900,0x3249d80,0x3772380,0x3b7a800,0x40a2e00,0x44ab280,0x49c9b00,0x4dd1f80,0x52f0800,0x56f8c80,0x5acf300,0x5d7b000,0x5d7b000,0x5d7b000,0x5d7b000,0x5d7b000,0x5d7b000,0x5d7b000,0x5d7b000,0x5d7b000,0x5d7b000,0x5d7b000,0x5d7b000,525,1333,2325,3133,4125,4933,5925,6917,7725,8717,9525,10517,11325,12317,13125,14102,14910,15887,16695,8520,8520,8520,8520,8520,20,19,0,0,0,0,0,0,0x457380,0x85f800,0xd87e00,0x1190280,0x16b8880,0x1ac0d00,0x1fe9300,0x2511900,0x2919d80,0x2e42380,0x324a800,0x3772e00,0x3b7b280,0x40a3880,0x44abd00,0x49ca580,0x4dd2a00,0x52f1280,0x56f9700,0x2ce9b00,0x2ce9b00,0x2ce9b00,0x2ce9b00,0x2ce9b00,0x2ce9b00,0x2ce9b00,0x2ce9b00,0x2ce9b00,0x2ce9b00,0x2ce9b00,0x2ce9b00,0x2ce9b00,526,1334,2326,3134,4126,4934,5926,6918,7726,8718,9526,10518,11326,12318,13126,14103,14911,15888,16696,8776,17959,17959,17959,17959,21,20,0,0,0,0,0,0,0x457e00,0x860280,0xd88880,0x1190d00,0x16b9300,0x1ac1780,0x1fe9d80,0x2512380,0x291a800,0x2e42e00,0x324b280,0x3773880,0x3b7bd00,0x40a4300,0x44ac780,0x49cb000,0x4dd3480,0x52f1d00,0x56fa180,0x2e39080,0x5d7ba80,0x5d7ba80,0x5d7ba80,0x5d7ba80,0x5d7ba80,0x5d7ba80,0x5d7ba80,0x5d7ba80,0x5d7ba80,0x5d7ba80,0x5d7ba80,0x5d7ba80,527,1335,2327,3135,4127,4935,5927,6919,7727,8719,9527,10519,11327,12319,13127,14104,14912,15889,16697,9032,9032,9032,9032,9032,20,19,0,0,0,0,0,0,0x458880,0x860d00,0xd89300,0x1191780,0x16b9d80,0x1ac2200,0x1fea800,0x2512e00,0x291b280,0x2e43880,0x324bd00,0x3774300,0x3b7c780,0x40a4d80,0x44ad200,0x49cba80,0x4dd3f00,0x52f2780,0x56fac00,0x2f88600,0x2f88600,0x2f88600,0x2f88600,0x2f88600,0x2f88600,0x2f88600,0x2f88600,0x2f88600,0x2f88600,0x2f88600,0x2f88600,0x2f88600,528,1336,2328,3136,4128,5120,5928,6920,7728,8720,9528,10520,11328,12320,13312,14105,14913,15890,16698,17427,17960,17960,17960,17960,21,20,0,0,0,0,0,0,0x459300,0x861780,0xd89d80,0x1192200,0x16ba800,0x1be2e00,0x1feb280,0x2513880,0x291bd00,0x2e44300,0x324c780,0x3774d80,0x3b7d200,0x40a5800,0x45cde00,0x49cc500,0x4dd4980,0x52f3200,0x56fb680,0x5acfd80,0x5d7c500,0x5d7c500,0x5d7c500,0x5d7c500,0x5d7c500,0x5d7c500,0x5d7c500,0x5d7c500,0x5d7c500,0x5d7c500,0x5d7c500,0x5d7c500,529,1337,2329,3137,4129,5121,5929,6921,7729,8721,9529,10521,11329,12321,13313,14106,14914,15891,16699,17428,17961,17961,17961,17961,21,20,0,0,0,0,0,0,0x459d80,0x862200,0xd8a800,0x1192c80,0x16bb280,0x1be3880,0x1febd00,0x2514300,0x291c780,0x2e44d80,0x324d200,0x3775800,0x3b7dc80,0x40a6280,0x45ce880,0x49ccf80,0x4dd5400,0x52f3c80,0x56fc100,0x5ad0800,0x5d7cf80,0x5d7cf80,0x5d7cf80,0x5d7cf80,0x5d7cf80,0x5d7cf80,0x5d7cf80,0x5d7cf80,0x5d7cf80,0x5d7cf80,0x5d7cf80,0x5d7cf80,530,1338,2330,3138,4130,5122,5930,6922,7730,8722,9530,10522,11330,12322,13314,14107,14915,15892,16700,17429,17962,18236,18236,18236,22,21,0,0,0,0,0,0,0x45a800,0x862c80,0xd8b280,0x1193700,0x16bbd00,0x1be4300,0x1fec780,0x2514d80,0x291d200,0x2e45800,0x324dc80,0x3776280,0x3b7e700,0x40a6d00,0x45cf300,0x49cda00,0x4dd5e80,0x52f4700,0x56fcb80,0x5ad1280,0x5d7da00,0x5ed8c80,0x5ed8c80,0x5ed8c80,0x5ed8c80,0x5ed8c80,0x5ed8c80,0x5ed8c80,0x5ed8c80,0x5ed8c80,0x5ed8c80,0x5ed8c80,531,1339,2331,3139,4131,5123,5931,6923,7731,8723,9531,10523,11331,12323,13315,14108,14916,15893,16701,17430,17963,17963,17963,17963,21,20,0,0,0,0,0,0,0x45b280,0x863700,0xd8bd00,0x1194180,0x16bc780,0x1be4d80,0x1fed200,0x2515800,0x291dc80,0x2e46280,0x324e700,0x3776d00,0x3b7f180,0x40a7780,0x45cfd80,0x49ce480,0x4dd6900,0x52f5180,0x56fd600,0x5ad1d00,0x5d7e480,0x5d7e480,0x5d7e480,0x5d7e480,0x5d7e480,0x5d7e480,0x5d7e480,0x5d7e480,0x5d7e480,0x5d7e480,0x5d7e480,0x5d7e480,532,1340,2332,3140,4132,5124,5932,6924,7732,8724,9532,10524,11332,12324,13316,14109,14917,15894,16702,17431,17964,17964,17964,17964,21,20,0,0,0,0,0,0,0x45bd00,0x864180,0xd8c780,0x1194c00,0x16bd200,0x1be5800,0x1fedc80,0x2516280,0x291e700,0x2e46d00,0x324f180,0x3777780,0x3b7fc00,0x40a8200,0x45d0800,0x49cef00,0x4dd7380,0x52f5c00,0x56fe080,0x5ad2780,0x5d7ef00,0x5d7ef00,0x5d7ef00,0x5d7ef00,0x5d7ef00,0x5d7ef00,0x5d7ef00,0x5d7ef00,0x5d7ef00,0x5d7ef00,0x5d7ef00,0x5d7ef00,533,1341,2333,3141,4133,5125,5933,6925,7733,8725,9533,10525,11333,12325,13317,14110,14918,15895,16703,9288,9288,9288,9288,9288,20,19,0,0,0,0,0,0,0x45c780,0x864c00,0xd8d200,0x1195680,0x16bdc80,0x1be6280,0x1fee700,0x2516d00,0x291f180,0x2e47780,0x324fc00,0x3778200,0x3b80680,0x40a8c80,0x45d1280,0x49cf980,0x4dd7e00,0x52f6680,0x56feb00,0x30d7b80,0x30d7b80,0x30d7b80,0x30d7b80,0x30d7b80,0x30d7b80,0x30d7b80,0x30d7b80,0x30d7b80,0x30d7b80,0x30d7b80,0x30d7b80,0x30d7b80,534,1342,2334,3142,4134,5126,5934,6926,7734,8726,9534,10526,11334,12326,13318,14111,14919,15896,16704,9544,17965,17965,17965,17965,21,20,0,0,0,0,0,0,0x45d200,0x865680,0xd8dc80,0x1196100,0x16be700,0x1be6d00,0x1fef180,0x2517780,0x291fc00,0x2e48200,0x3250680,0x3778c80,0x3b81100,0x40a9700,0x45d1d00,0x49d0400,0x4dd8880,0x52f7100,0x56ff580,0x3227100,0x5d7f980,0x5d7f980,0x5d7f980,0x5d7f980,0x5d7f980,0x5d7f980,0x5d7f980,0x5d7f980,0x5d7f980,0x5d7f980,0x5d7f980,0x5d7f980,535,1343,2335,3143,4135,5127,5935,6927,7735,8727,9535,10527,11335,12327,13319,14112,15104,15897,16705,9800,9800,9800,9800,9800,20,19,0,0,0,0,0,0,0x45dc80,0x866100,0xd8e700,0x1196b80,0x16bf180,0x1be7780,0x1fefc00,0x2518200,0x2920680,0x2e48c80,0x3251100,0x3779700,0x3b81b80,0x40aa180,0x45d2780,0x49d0e80,0x4ef9480,0x52f7b80,0x5700000,0x3376680,0x3376680,0x3376680,0x3376680,0x3376680,0x3376680,0x3376680,0x3376680,0x3376680,0x3376680,0x3376680,0x3376680,0x3376680,536,1344,2336,3328,4136,5128,5936,6928,7736,8728,9536,10528,11520,12328,13320,14113,15105,15898,16706,17432,17966,17966,17966,17966,21,20,0,0,0,0,0,0,0x45e700,0x866b80,0xd8f180,0x12b7780,0x16bfc00,0x1be8200,0x1ff0680,0x2518c80,0x2921100,0x2e49700,0x3251b80,0x377a180,0x3ca2780,0x40aac00,0x45d3200,0x49d1900,0x4ef9f00,0x52f8600,0x5700a80,0x5ad3200,0x5d80400,0x5d80400,0x5d80400,0x5d80400,0x5d80400,0x5d80400,0x5d80400,0x5d80400,0x5d80400,0x5d80400,0x5d80400,0x5d80400,537,1345,2337,3329,4137,5129,5937,6929,7737,8729,9537,10529,11521,12329,13321,14114,15106,15899,16707,17433,17967,17967,17967,17967,21,20,0,0,0,0,0,0,0x45f180,0x867600,0xd8fc00,0x12b8200,0x16c0680,0x1be8c80,0x1ff1100,0x2519700,0x2921b80,0x2e4a180,0x3252600,0x377ac00,0x3ca3200,0x40ab680,0x45d3c80,0x49d2380,0x4efa980,0x52f9080,0x5701500,0x5ad3c80,0x5d80e80,0x5d80e80,0x5d80e80,0x5d80e80,0x5d80e80,0x5d80e80,0x5d80e80,0x5d80e80,0x5d80e80,0x5d80e80,0x5d80e80,0x5d80e80,538,1346,2338,3330,4138,5130,5938,6930,7738,8730,9538,10530,11522,12330,13322,14115,15107,15900,16708,17434,17968,18237,18237,18237,22,21,0,0,0,0,0,0,0x45fc00,0x868080,0xd90680,0x12b8c80,0x16c1100,0x1be9700,0x1ff1b80,0x251a180,0x2922600,0x2e4ac00,0x3253080,0x377b680,0x3ca3c80,0x40ac100,0x45d4700,0x49d2e00,0x4efb400,0x52f9b00,0x5701f80,0x5ad4700,0x5d81900,0x5ed9700,0x5ed9700,0x5ed9700,0x5ed9700,0x5ed9700,0x5ed9700,0x5ed9700,0x5ed9700,0x5ed9700,0x5ed9700,0x5ed9700,539,1347,2339,3331,4139,5131,5939,6931,7739,8731,9539,10531,11523,12331,13323,14116,15108,15901,16709,17435,17969,17969,17969,17969,21,20,0,0,0,0,0,0,0x460680,0x868b00,0xd91100,0x12b9700,0x16c1b80,0x1bea180,0x1ff2600,0x251ac00,0x2923080,0x2e4b680,0x3253b00,0x377c100,0x3ca4700,0x40acb80,0x45d5180,0x49d3880,0x4efbe80,0x52fa580,0x5702a00,0x5ad5180,0x5d82380,0x5d82380,0x5d82380,0x5d82380,0x5d82380,0x5d82380,0x5d82380,0x5d82380,0x5d82380,0x5d82380,0x5d82380,0x5d82380,540,1348,2340,3332,4140,5132,5940,6932,7740,8732,9540,10532,11524,12332,13324,14117,15109,15902,16710,17436,17970,17970,17970,17970,21,20,0,0,0,0,0,0,0x461100,0x869580,0xd91b80,0x12ba180,0x16c2600,0x1beac00,0x1ff3080,0x251b680,0x2923b00,0x2e4c100,0x3254580,0x377cb80,0x3ca5180,0x40ad600,0x45d5c00,0x49d4300,0x4efc900,0x52fb000,0x5703480,0x5ad5c00,0x5d82e00,0x5d82e00,0x5d82e00,0x5d82e00,0x5d82e00,0x5d82e00,0x5d82e00,0x5d82e00,0x5d82e00,0x5d82e00,0x5d82e00,0x5d82e00,541,1349,2341,3333,4141,5133,5941,6933,7741,8733,9541,10533,11525,12333,13325,14118,15110,15903,16711,10056,10056,10056,10056,10056,20,19,0,0,0,0,0,0,0x461b80,0x86a000,0xd92600,0x12bac00,0x16c3080,0x1beb680,0x1ff3b00,0x251c100,0x2924580,0x2e4cb80,0x3255000,0x377d600,0x3ca5c00,0x40ae080,0x45d6680,0x49d4d80,0x4efd380,0x52fba80,0x5703f00,0x34c5c00,0x34c5c00,0x34c5c00,0x34c5c00,0x34c5c00,0x34c5c00,0x34c5c00,0x34c5c00,0x34c5c00,0x34c5c00,0x34c5c00,0x34c5c00,0x34c5c00,542,1350,2342,3334,4142,5134,5942,6934,7742,8734,9542,10534,11526,12334,13326,14119,15111,15904,16896,10312,17971,17971,17971,17971,21,20,0,0,0,0,0,0,0x462600,0x86aa80,0xd93080,0x12bb680,0x16c3b00,0x1bec100,0x1ff4580,0x251cb80,0x2925000,0x2e4d600,0x3255a80,0x377e080,0x3ca6680,0x40aeb00,0x45d7100,0x49d5800,0x4efde00,0x52fc500,0x5824b00,0x3615180,0x5d83880,0x5d83880,0x5d83880,0x5d83880,0x5d83880,0x5d83880,0x5d83880,0x5d83880,0x5d83880,0x5d83880,0x5d83880,0x5d83880,543,1351,2343,3335,4143,5135,5943,6935,7743,8735,9543,10535,11527,12335,13327,14120,15112,15905,16897,10568,10568,10568,10568,10568,20,19,0,0,0,0,0,0,0x463080,0x86b500,0xd93b00,0x12bc100,0x16c4580,0x1becb80,0x1ff5000,0x251d600,0x2925a80,0x2e4e080,0x3256500,0x377eb00,0x3ca7100,0x40af580,0x45d7b80,0x49d6280,0x4efe880,0x52fcf80,0x5825580,0x3764700,0x3764700,0x3764700,0x3764700,0x3764700,0x3764700,0x3764700,0x3764700,0x3764700,0x3764700,0x3764700,0x3764700,0x3764700,544,1536,2344,3336,4144,5136,5944,6936,7744,8736,9728,10536,11528,12336,13328,14121,15113,15906,16898,17437,17972,17972,17972,17972,21,20,0,0,0,0,0,0,0x463b00,0x98c100,0xd94580,0x12bcb80,0x16c5000,0x1bed600,0x1ff5a80,0x251e080,0x2926500,0x2e4eb00,0x3377100,0x377f580,0x3ca7b80,0x40b0000,0x45d8600,0x49d6d00,0x4eff300,0x52fda00,0x5826000,0x5ad6680,0x5d84300,0x5d84300,0x5d84300,0x5d84300,0x5d84300,0x5d84300,0x5d84300,0x5d84300,0x5d84300,0x5d84300,0x5d84300,0x5d84300,545,1537,2345,3337,4145,5137,5945,6937,7745,8737,9729,10537,11529,12337,13329,14122,15114,15907,16899,17438,17973,17973,17973,17973,21,20,0,0,0,0,0,0,0x464580,0x98cb80,0xd95000,0x12bd600,0x16c5a80,0x1bee080,0x1ff6500,0x251eb00,0x2926f80,0x2e4f580,0x3377b80,0x3780000,0x3ca8600,0x40b0a80,0x45d9080,0x49d7780,0x4effd80,0x52fe480,0x5826a80,0x5ad7100,0x5d84d80,0x5d84d80,0x5d84d80,0x5d84d80,0x5d84d80,0x5d84d80,0x5d84d80,0x5d84d80,0x5d84d80,0x5d84d80,0x5d84d80,0x5d84d80,546,1538,2346,3338,4146,5138,5946,6938,7746,8738,9730,10538,11530,12338,13330,14123,15115,15908,16900,17439,17974,18238,18238,18238,22,21,0,0,0,0,0,0,0x465000,0x98d600,0xd95a80,0x12be080,0x16c6500,0x1beeb00,0x1ff6f80,0x251f580,0x2927a00,0x2e50000,0x3378600,0x3780a80,0x3ca9080,0x40b1500,0x45d9b00,0x49d8200,0x4f00800,0x52fef00,0x5827500,0x5ad7b80,0x5d85800,0x5eda180,0x5eda180,0x5eda180,0x5eda180,0x5eda180,0x5eda180,0x5eda180,0x5eda180,0x5eda180,0x5eda180,0x5eda180,547,1539,2347,3339,4147,5139,5947,6939,7747,8739,9731,10539,11531,12339,13331,14124,15116,15909,16901,17440,17975,17975,17975,17975,21,20,0,0,0,0,0,0,0x465a80,0x98e080,0xd96500,0x12beb00,0x16c6f80,0x1bef580,0x1ff7a00,0x2520000,0x2928480,0x2e50a80,0x3379080,0x3781500,0x3ca9b00,0x40b1f80,0x45da580,0x49d8c80,0x4f01280,0x52ff980,0x5827f80,0x5ad8600,0x5d86280,0x5d86280,0x5d86280,0x5d86280,0x5d86280,0x5d86280,0x5d86280,0x5d86280,0x5d86280,0x5d86280,0x5d86280,0x5d86280,548,1540,2348,3340,4148,5140,5948,6940,7748,8740,9732,10540,11532,12340,13332,14125,15117,15910,16902,17441,17976,17976,17976,17976,21,20,0,0,0,0,0,0,0x466500,0x98eb00,0xd96f80,0x12bf580,0x16c7a00,0x1bf0000,0x1ff8480,0x2520a80,0x2928f00,0x2e51500,0x3379b00,0x3781f80,0x3caa580,0x40b2a00,0x45db000,0x49d9700,0x4f01d00,0x5300400,0x5828a00,0x5ad9080,0x5d86d00,0x5d86d00,0x5d86d00,0x5d86d00,0x5d86d00,0x5d86d00,0x5d86d00,0x5d86d00,0x5d86d00,0x5d86d00,0x5d86d00,0x5d86d00,549,1541,2349,3341,4149,5141,5949,6941,7749,8741,9733,10541,11533,12341,13333,14126,15118,15911,16903,10824,10824,10824,10824,10824,20,19,0,0,0,0,0,0,0x466f80,0x98f580,0xd97a00,0x12c0000,0x16c8480,0x1bf0a80,0x1ff8f00,0x2521500,0x2929980,0x2e51f80,0x337a580,0x3782a00,0x3cab000,0x40b3480,0x45dba80,0x49da180,0x4f02780,0x5300e80,0x5829480,0x38b3c80,0x38b3c80,0x38b3c80,0x38b3c80,0x38b3c80,0x38b3c80,0x38b3c80,0x38b3c80,0x38b3c80,0x38b3c80,0x38b3c80,0x38b3c80,0x38b3c80,550,1542,2350,3342,4150,5142,5950,6942,7750,8742,9734,10542,11534,12342,13334,14127,15119,15912,16904,11080,17977,17977,17977,17977,21,20,0,0,0,0,0,0,0x467a00,0x990000,0xd98480,0x12c0a80,0x16c8f00,0x1bf1500,0x1ff9980,0x2521f80,0x292a400,0x2e52a00,0x337b000,0x3783480,0x3caba80,0x40b3f00,0x45dc500,0x49dac00,0x4f03200,0x5301900,0x5829f00,0x3a03200,0x5d87780,0x5d87780,0x5d87780,0x5d87780,0x5d87780,0x5d87780,0x5d87780,0x5d87780,0x5d87780,0x5d87780,0x5d87780,0x5d87780,551,1543,2351,3343,4151,5143,5951,6943,7751,8743,9735,10543,11535,12343,13335,14128,15120,15913,16905,11336,11336,11336,11336,11336,20,19,0,0,0,0,0,0,0x468480,0x990a80,0xd98f00,0x12c1500,0x16c9980,0x1bf1f80,0x1ffa400,0x2522a00,0x292ae80,0x2e53480,0x337ba80,0x3783f00,0x3cac500,0x40b4980,0x45dcf80,0x49db680,0x4f03c80,0x5302380,0x582a980,0x3b52780,0x3b52780,0x3b52780,0x3b52780,0x3b52780,0x3b52780,0x3b52780,0x3b52780,0x3b52780,0x3b52780,0x3b52780,0x3b52780,0x3b52780,552,1544,2352,3344,4152,5144,5952,6944,7936,8744,9736,10544,11536,12344,13336,14129,15121,15914,16906,17442,17978,17978,17978,17978,21,20,0,0,0,0,0,0,0x468f00,0x991500,0xd99980,0x12c1f80,0x16ca400,0x1bf2a00,0x1ffae80,0x2523480,0x2a4ba80,0x2e53f00,0x337c500,0x3784980,0x3cacf80,0x40b5400,0x45dda00,0x49dc100,0x4f04700,0x5302e00,0x582b400,0x5ad9b00,0x5d88200,0x5d88200,0x5d88200,0x5d88200,0x5d88200,0x5d88200,0x5d88200,0x5d88200,0x5d88200,0x5d88200,0x5d88200,0x5d88200,553,1545,2353,3345,4153,5145,5953,6945,7937,8745,9737,10545,11537,12345,13337,14130,15122,15915,16907,17443,17979,17979,17979,17979,21,20,0,0,0,0,0,0,0x469980,0x991f80,0xd9a400,0x12c2a00,0x16cae80,0x1bf3480,0x1ffb900,0x2523f00,0x2a4c500,0x2e54980,0x337cf80,0x3785400,0x3cada00,0x40b5e80,0x45de480,0x49dcb80,0x4f05180,0x5303880,0x582be80,0x5ada580,0x5d88c80,0x5d88c80,0x5d88c80,0x5d88c80,0x5d88c80,0x5d88c80,0x5d88c80,0x5d88c80,0x5d88c80,0x5d88c80,0x5d88c80,0x5d88c80,554,1546,2354,3346,4154,5146,5954,6946,7938,8746,9738,10546,11538,12346,13338,14131,15123,15916,16908,17444,17980,18239,18239,18239,22,21,0,0,0,0,0,0,0x46a400,0x992a00,0xd9ae80,0x12c3480,0x16cb900,0x1bf3f00,0x1ffc380,0x2524980,0x2a4cf80,0x2e55400,0x337da00,0x3785e80,0x3cae480,0x40b6900,0x45def00,0x49dd600,0x4f05c00,0x5304300,0x582c900,0x5adb000,0x5d89700,0x5edac00,0x5edac00,0x5edac00,0x5edac00,0x5edac00,0x5edac00,0x5edac00,0x5edac00,0x5edac00,0x5edac00,0x5edac00,555,1547,2355,3347,4155,5147,5955,6947,7939,8747,9739,10547,11539,12347,13339,14132,15124,15917,16909,17445,17981,17981,17981,17981,21,20,0,0,0,0,0,0,0x46ae80,0x993480,0xd9b900,0x12c3f00,0x16cc380,0x1bf4980,0x1ffce00,0x2525400,0x2a4da00,0x2e55e80,0x337e480,0x3786900,0x3caef00,0x40b7380,0x45df980,0x49de080,0x4f06680,0x5304d80,0x582d380,0x5adba80,0x5d8a180,0x5d8a180,0x5d8a180,0x5d8a180,0x5d8a180,0x5d8a180,0x5d8a180,0x5d8a180,0x5d8a180,0x5d8a180,0x5d8a180,0x5d8a180,556,1548,2356,3348,4156,5148,5956,6948,7940,8748,9740,10548,11540,12348,13340,14133,15125,15918,16910,17446,17982,17982,17982,17982,21,20,0,0,0,0,0,0,0x46b900,0x993f00,0xd9c380,0x12c4980,0x16cce00,0x1bf5400,0x1ffd880,0x2525e80,0x2a4e480,0x2e56900,0x337ef00,0x3787380,0x3caf980,0x40b7e00,0x45e0400,0x49deb00,0x4f07100,0x5305800,0x582de00,0x5adc500,0x5d8ac00,0x5d8ac00,0x5d8ac00,0x5d8ac00,0x5d8ac00,0x5d8ac00,0x5d8ac00,0x5d8ac00,0x5d8ac00,0x5d8ac00,0x5d8ac00,0x5d8ac00,557,1549,2357,3349,4157,5149,5957,6949,7941,8749,9741,10549,11541,12349,13341,14134,15126,15919,16911,11592,11592,11592,11592,11592,20,19,0,0,0,0,0,0,0x46c380,0x994980,0xd9ce00,0x12c5400,0x16cd880,0x1bf5e80,0x1ffe300,0x2526900,0x2a4ef00,0x2e57380,0x337f980,0x3787e00,0x3cb0400,0x40b8880,0x45e0e80,0x49df580,0x4f07b80,0x5306280,0x582e880,0x3ca1d00,0x3ca1d00,0x3ca1d00,0x3ca1d00,0x3ca1d00,0x3ca1d00,0x3ca1d00,0x3ca1d00,0x3ca1d00,0x3ca1d00,0x3ca1d00,0x3ca1d00,0x3ca1d00,558,1550,2358,3350,4158,5150,5958,6950,7942,8750,9742,10550,11542,12350,13342,14135,15127,15920,16912,11848,17983,17983,17983,17983,21,20,0,0,0,0,0,0,0x46ce00,0x995400,0xd9d880,0x12c5e80,0x16ce300,0x1bf6900,0x1ffed80,0x2527380,0x2a4f980,0x2e57e00,0x3380400,0x3788880,0x3cb0e80,0x40b9300,0x45e1900,0x49e0000,0x4f08600,0x5306d00,0x582f300,0x3df1280,0x5d8b680,0x5d8b680,0x5d8b680,0x5d8b680,0x5d8b680,0x5d8b680,0x5d8b680,0x5d8b680,0x5d8b680,0x5d8b680,0x5d8b680,0x5d8b680,559,1551,2359,3351,4159,5151,5959,6951,7943,8751,9743,10551,11543,12351,13343,14136,15128,15921,16913,12104,12104,12104,12104,12104,20,19,0,0,0,0,0,0,0x46d880,0x995e80,0xd9e300,0x12c6900,0x16ced80,0x1bf7380,0x1fff800,0x2527e00,0x2a50400,0x2e58880,0x3380e80,0x3789300,0x3cb1900,0x40b9d80,0x45e2380,0x49e0a80,0x4f09080,0x5307780,0x582fd80,0x3f40800,0x3f40800,0x3f40800,0x3f40800,0x3f40800,0x3f40800,0x3f40800,0x3f40800,0x3f40800,0x3f40800,0x3f40800,0x3f40800,0x3f40800,560,1552,2360,3352,4160,5152,6144,6952,7944,8752,9744,10552,11544,12352,13344,14137,15129,15922,16914,17447,17984,17984,17984,17984,21,20,0,0,0,0,0,0,0x46e300,0x996900,0xd9ed80,0x12c7380,0x16cf800,0x1bf7e00,0x2120400,0x2528880,0x2a50e80,0x2e59300,0x3381900,0x3789d80,0x3cb2380,0x40ba800,0x45e2e00,0x49e1500,0x4f09b00,0x5308200,0x5830800,0x5adcf80,0x5d8c100,0x5d8c100,0x5d8c100,0x5d8c100,0x5d8c100,0x5d8c100,0x5d8c100,0x5d8c100,0x5d8c100,0x5d8c100,0x5d8c100,0x5d8c100,561,1553,2361,3353,4161,5153,6145,6953,7945,8753,9745,10553,11545,12353,13345,14138,15130,15923,16915,17448,17985,17985,17985,17985,21,20,0,0,0,0,0,0,0x46ed80,0x997380,0xd9f800,0x12c7e00,0x16d0280,0x1bf8880,0x2120e80,0x2529300,0x2a51900,0x2e59d80,0x3382380,0x378a800,0x3cb2e00,0x40bb280,0x45e3880,0x49e1f80,0x4f0a580,0x5308c80,0x5831280,0x5adda00,0x5d8cb80,0x5d8cb80,0x5d8cb80,0x5d8cb80,0x5d8cb80,0x5d8cb80,0x5d8cb80,0x5d8cb80,0x5d8cb80,0x5d8cb80,0x5d8cb80,0x5d8cb80,562,1554,2362,3354,4162,5154,6146,6954,7946,8754,9746,10554,11546,12354,13346,14139,15131,15924,16916,17449,17986,18240,18240,18240,22,21,0,0,0,0,0,0,0x46f800,0x997e00,0xda0280,0x12c8880,0x16d0d00,0x1bf9300,0x2121900,0x2529d80,0x2a52380,0x2e5a800,0x3382e00,0x378b280,0x3cb3880,0x40bbd00,0x45e4300,0x49e2a00,0x4f0b000,0x5309700,0x5831d00,0x5ade480,0x5d8d600,0x5edb680,0x5edb680,0x5edb680,0x5edb680,0x5edb680,0x5edb680,0x5edb680,0x5edb680,0x5edb680,0x5edb680,0x5edb680,563,1555,2363,3355,4163,5155,6147,6955,7947,8755,9747,10555,11547,12355,13347,14140,15132,15925,16917,17450,17987,17987,17987,17987,21,20,0,0,0,0,0,0,0x470280,0x998880,0xda0d00,0x12c9300,0x16d1780,0x1bf9d80,0x2122380,0x252a800,0x2a52e00,0x2e5b280,0x3383880,0x378bd00,0x3cb4300,0x40bc780,0x45e4d80,0x49e3480,0x4f0ba80,0x530a180,0x5832780,0x5adef00,0x5d8e080,0x5d8e080,0x5d8e080,0x5d8e080,0x5d8e080,0x5d8e080,0x5d8e080,0x5d8e080,0x5d8e080,0x5d8e080,0x5d8e080,0x5d8e080,564,1556,2364,3356,4164,5156,6148,6956,7948,8756,9748,10556,11548,12356,13348,14141,15133,15926,16918,17451,17988,17988,17988,17988,21,20,0,0,0,0,0,0,0x470d00,0x999300,0xda1780,0x12c9d80,0x16d2200,0x1bfa800,0x2122e00,0x252b280,0x2a53880,0x2e5bd00,0x3384300,0x378c780,0x3cb4d80,0x40bd200,0x45e5800,0x49e3f00,0x4f0c500,0x530ac00,0x5833200,0x5adf980,0x5d8eb00,0x5d8eb00,0x5d8eb00,0x5d8eb00,0x5d8eb00,0x5d8eb00,0x5d8eb00,0x5d8eb00,0x5d8eb00,0x5d8eb00,0x5d8eb00,0x5d8eb00,565,1557,2365,3357,4165,5157,6149,6957,7949,8757,9749,10557,11549,12357,13349,14142,15134,15927,16919,12360,12360,12360,12360,12360,20,19,0,0,0,0,0,0,0x471780,0x999d80,0xda2200,0x12ca800,0x16d2c80,0x1bfb280,0x2123880,0x252bd00,0x2a54300,0x2e5c780,0x3384d80,0x378d200,0x3cb5800,0x40bdc80,0x45e6280,0x49e4980,0x4f0cf80,0x530b680,0x5833c80,0x408fd80,0x408fd80,0x408fd80,0x408fd80,0x408fd80,0x408fd80,0x408fd80,0x408fd80,0x408fd80,0x408fd80,0x408fd80,0x408fd80,0x408fd80,566,1558,2366,3358,4166,5158,6150,6958,7950,8758,9750,10558,11550,12358,13350,14143,15135,15928,16920,12616,17989,17989,17989,17989,21,20,0,0,0,0,0,0,0x472200,0x99a800,0xda2c80,0x12cb280,0x16d3700,0x1bfbd00,0x2124300,0x252c780,0x2a54d80,0x2e5d200,0x3385800,0x378dc80,0x3cb6280,0x40be700,0x45e6d00,0x49e5400,0x4f0da00,0x530c100,0x5834700,0x41df300,0x5d8f580,0x5d8f580,0x5d8f580,0x5d8f580,0x5d8f580,0x5d8f580,0x5d8f580,0x5d8f580,0x5d8f580,0x5d8f580,0x5d8f580,0x5d8f580,567,1559,2367,3359,4167,5159,6151,6959,7951,8759,9751,10559,11551,12359,13351,14144,15136,15929,16921,12872,12872,12872,12872,12872,20,19,0,0,0,0,0,0,0x472c80,0x99b280,0xda3700,0x12cbd00,0x16d4180,0x1bfc780,0x2124d80,0x252d200,0x2a55800,0x2e5dc80,0x3386280,0x378e700,0x3cb6d00,0x40bf180,0x45e7780,0x49e5e80,0x4f0e480,0x530cb80,0x5835180,0x432e880,0x432e880,0x432e880,0x432e880,0x432e880,0x432e880,0x432e880,0x432e880,0x432e880,0x432e880,0x432e880,0x432e880,0x432e880,568,1560,2368,3360,4352,5160,6152,6960,7952,8760,9752,10560,11552,12544,13352,14145,15137,15930,16922,17452,17990,17990,17990,17990,21,20,0,0,0,0,0,0,0x473700,0x99bd00,0xda4180,0x12cc780,0x17f4d80,0x1bfd200,0x2125800,0x252dc80,0x2a56280,0x2e5e700,0x3386d00,0x378f180,0x3cb7780,0x41dfd80,0x45e8200,0x49e6900,0x4f0ef00,0x530d600,0x5835c00,0x5ae0400,0x5d90000,0x5d90000,0x5d90000,0x5d90000,0x5d90000,0x5d90000,0x5d90000,0x5d90000,0x5d90000,0x5d90000,0x5d90000,0x5d90000,569,1561,2369,3361,4353,5161,6153,6961,7953,8761,9753,10561,11553,12545,13353,14146,15138,15931,16923,17453,17991,17991,17991,17991,21,20,0,0,0,0,0,0,0x474180,0x99c780,0xda4c00,0x12cd200,0x17f5800,0x1bfdc80,0x2126280,0x252e700,0x2a56d00,0x2e5f180,0x3387780,0x378fc00,0x3cb8200,0x41e0800,0x45e8c80,0x49e7380,0x4f0f980,0x530e080,0x5836680,0x5ae0e80,0x5d90a80,0x5d90a80,0x5d90a80,0x5d90a80,0x5d90a80,0x5d90a80,0x5d90a80,0x5d90a80,0x5d90a80,0x5d90a80,0x5d90a80,0x5d90a80,570,1562,2370,3362,4354,5162,6154,6962,7954,8762,9754,10562,11554,12546,13354,14147,15139,15932,16924,17454,18176,18241,18241,18241,22,21,0,0,0,0,0,0,0x474c00,0x99d200,0xda5680,0x12cdc80,0x17f6280,0x1bfe700,0x2126d00,0x252f180,0x2a57780,0x2e5fc00,0x3388200,0x3790680,0x3cb8c80,0x41e1280,0x45e9700,0x49e7e00,0x4f10400,0x530eb00,0x5837100,0x5ae1900,0x5eb1680,0x5edc100,0x5edc100,0x5edc100,0x5edc100,0x5edc100,0x5edc100,0x5edc100,0x5edc100,0x5edc100,0x5edc100,0x5edc100,571,1563,2371,3363,4355,5163,6155,6963,7955,8763,9755,10563,11555,12547,13355,14148,15140,15933,16925,17455,18177,18177,18177,18177,21,20,0,0,0,0,0,0,0x475680,0x99dc80,0xda6100,0x12ce700,0x17f6d00,0x1bff180,0x2127780,0x252fc00,0x2a58200,0x2e60680,0x3388c80,0x3791100,0x3cb9700,0x41e1d00,0x45ea180,0x49e8880,0x4f10e80,0x530f580,0x5837b80,0x5ae2380,0x5eb2100,0x5eb2100,0x5eb2100,0x5eb2100,0x5eb2100,0x5eb2100,0x5eb2100,0x5eb2100,0x5eb2100,0x5eb2100,0x5eb2100,0x5eb2100,572,1564,2372,3364,4356,5164,6156,6964,7956,8764,9756,10564,11556,12548,13356,14149,15141,15934,16926,17456,18178,18178,18178,18178,21,20,0,0,0,0,0,0,0x476100,0x99e700,0xda6b80,0x12cf180,0x17f7780,0x1bffc00,0x2128200,0x2530680,0x2a58c80,0x2e61100,0x3389700,0x3791b80,0x3cba180,0x41e2780,0x45eac00,0x49e9300,0x4f11900,0x5310000,0x5838600,0x5ae2e00,0x5eb2b80,0x5eb2b80,0x5eb2b80,0x5eb2b80,0x5eb2b80,0x5eb2b80,0x5eb2b80,0x5eb2b80,0x5eb2b80,0x5eb2b80,0x5eb2b80,0x5eb2b80,573,1565,2373,3365,4357,5165,6157,6965,7957,8765,9757,10565,11557,12549,13357,14150,15142,15935,16927,13128,13128,13128,13128,13128,20,19,0,0,0,0,0,0,0x476b80,0x99f180,0xda7600,0x12cfc00,0x17f8200,0x1c00680,0x2128c80,0x2531100,0x2a59700,0x2e61b80,0x338a180,0x3792600,0x3cbac00,0x41e3200,0x45eb680,0x49e9d80,0x4f12380,0x5310a80,0x5839080,0x447de00,0x447de00,0x447de00,0x447de00,0x447de00,0x447de00,0x447de00,0x447de00,0x447de00,0x447de00,0x447de00,0x447de00,0x447de00,574,1566,2374,3366,4358,5166,6158,6966,7958,8766,9758,10566,11558,12550,13358,14151,15143,15936,16928,13384,18179,18179,18179,18179,21,20,0,0,0,0,0,0,0x477600,0x99fc00,0xda8080,0x12d0680,0x17f8c80,0x1c01100,0x2129700,0x2531b80,0x2a5a180,0x2e62600,0x338ac00,0x3793080,0x3cbb680,0x41e3c80,0x45ec100,0x49ea800,0x4f12e00,0x5311500,0x5839b00,0x45cd380,0x5eb3600,0x5eb3600,0x5eb3600,0x5eb3600,0x5eb3600,0x5eb3600,0x5eb3600,0x5eb3600,0x5eb3600,0x5eb3600,0x5eb3600,0x5eb3600,575,1567,2375,3367,4359,5167,6159,6967,7959,8767,9759,10567,11559,12551,13359,14336,15144,15937,16929,13640,13640,13640,13640,13640,20,19,0,0,0,0,0,0,0x478080,0x9a0680,0xda8b00,0x12d1100,0x17f9700,0x1c01b80,0x212a180,0x2532600,0x2a5ac00,0x2e63080,0x338b680,0x3793b00,0x3cbc100,0x41e4700,0x45ecb80,0x4b0b400,0x4f13880,0x5311f80,0x583a580,0x471c900,0x471c900,0x471c900,0x471c900,0x471c900,0x471c900,0x471c900,0x471c900,0x471c900,0x471c900,0x471c900,0x471c900,0x471c900,576,1568,2560,3368,4360,5168,6160,6968,7960,8768,9760,10752,11560,12552,13360,14337,15145,15938,16930,17457,18180,18180,18180,18180,21,20,0,0,0,0,0,0,0x478b00,0x9a1100,0xec9700,0x12d1b80,0x17fa180,0x1c02600,0x212ac00,0x2533080,0x2a5b680,0x2e63b00,0x338c100,0x38b4700,0x3cbcb80,0x41e5180,0x45ed600,0x4b0be80,0x4f14300,0x5312a00,0x583b000,0x5ae3880,0x5eb4080,0x5eb4080,0x5eb4080,0x5eb4080,0x5eb4080,0x5eb4080,0x5eb4080,0x5eb4080,0x5eb4080,0x5eb4080,0x5eb4080,0x5eb4080,577,1569,2561,3369,4361,5169,6161,6969,7961,8769,9761,10753,11561,12553,13361,14338,15146,15939,16931,17458,18181,18181,18181,18181,21,20,0,0,0,0,0,0,0x479580,0x9a1b80,0xeca180,0x12d2600,0x17fac00,0x1c03080,0x212b680,0x2533b00,0x2a5c100,0x2e64580,0x338cb80,0x38b5180,0x3cbd600,0x41e5c00,0x45ee080,0x4b0c900,0x4f14d80,0x5313480,0x583ba80,0x5ae4300,0x5eb4b00,0x5eb4b00,0x5eb4b00,0x5eb4b00,0x5eb4b00,0x5eb4b00,0x5eb4b00,0x5eb4b00,0x5eb4b00,0x5eb4b00,0x5eb4b00,0x5eb4b00,578,1570,2562,3370,4362,5170,6162,6970,7962,8770,9762,10754,11562,12554,13362,14339,15147,15940,16932,17459,18182,18242,18242,18242,22,21,0,0,0,0,0,0,0x47a000,0x9a2600,0xecac00,0x12d3080,0x17fb680,0x1c03b00,0x212c100,0x2534580,0x2a5cb80,0x2e65000,0x338d600,0x38b5c00,0x3cbe080,0x41e6680,0x45eeb00,0x4b0d380,0x4f15800,0x5313f00,0x583c500,0x5ae4d80,0x5eb5580,0x5edcb80,0x5edcb80,0x5edcb80,0x5edcb80,0x5edcb80,0x5edcb80,0x5edcb80,0x5edcb80,0x5edcb80,0x5edcb80,0x5edcb80,579,1571,2563,3371,4363,5171,6163,6971,7963,8771,9763,10755,11563,12555,13363,14340,15148,15941,16933,17460,18183,18183,18183,18183,21,20,0,0,0,0,0,0,0x47aa80,0x9a3080,0xecb680,0x12d3b00,0x17fc100,0x1c04580,0x212cb80,0x2535000,0x2a5d600,0x2e65a80,0x338e080,0x38b6680,0x3cbeb00,0x41e7100,0x45ef580,0x4b0de00,0x4f16280,0x5314980,0x583cf80,0x5ae5800,0x5eb6000,0x5eb6000,0x5eb6000,0x5eb6000,0x5eb6000,0x5eb6000,0x5eb6000,0x5eb6000,0x5eb6000,0x5eb6000,0x5eb6000,0x5eb6000,580,1572,2564,3372,4364,5172,6164,6972,7964,8772,9764,10756,11564,12556,13364,14341,15149,15942,16934,17461,18184,18184,18184,18184,21,20,0,0,0,0,0,0,0x47b500,0x9a3b00,0xecc100,0x12d4580,0x17fcb80,0x1c05000,0x212d600,0x2535a80,0x2a5e080,0x2e66500,0x338eb00,0x38b7100,0x3cbf580,0x41e7b80,0x45f0000,0x4b0e880,0x4f16d00,0x5315400,0x583da00,0x5ae6280,0x5eb6a80,0x5eb6a80,0x5eb6a80,0x5eb6a80,0x5eb6a80,0x5eb6a80,0x5eb6a80,0x5eb6a80,0x5eb6a80,0x5eb6a80,0x5eb6a80,0x5eb6a80,581,1573,2565,3373,4365,5173,6165,6973,7965,8773,9765,10757,11565,12557,13365,14342,15150,15943,16935,13896,13896,13896,13896,13896,20,19,0,0,0,0,0,0,0x47bf80,0x9a4580,0xeccb80,0x12d5000,0x17fd600,0x1c05a80,0x212e080,0x2536500,0x2a5eb00,0x2e66f80,0x338f580,0x38b7b80,0x3cc0000,0x41e8600,0x45f0a80,0x4b0f300,0x4f17780,0x5315e80,0x583e480,0x486be80,0x486be80,0x486be80,0x486be80,0x486be80,0x486be80,0x486be80,0x486be80,0x486be80,0x486be80,0x486be80,0x486be80,0x486be80,582,1574,2566,3374,4366,5174,6166,6974,7966,8774,9766,10758,11566,12558,13366,14343,15151,16128,16936,14152,18185,18185,18185,18185,21,20,0,0,0,0,0,0,0x47ca00,0x9a5000,0xecd600,0x12d5a80,0x17fe080,0x1c06500,0x212eb00,0x2536f80,0x2a5f580,0x2e67a00,0x3390000,0x38b8600,0x3cc0a80,0x41e9080,0x45f1500,0x4b0fd80,0x4f18200,0x5436a80,0x583ef00,0x49bb400,0x5eb7500,0x5eb7500,0x5eb7500,0x5eb7500,0x5eb7500,0x5eb7500,0x5eb7500,0x5eb7500,0x5eb7500,0x5eb7500,0x5eb7500,0x5eb7500,583,1575,2567,3375,4367,5175,6167,6975,7967,8775,9767,10759,11567,12559,13367,14344,15152,16129,16937,14408,14408,14408,14408,14408,20,19,0,0,0,0,0,0,0x47d480,0x9a5a80,0xece080,0x12d6500,0x17feb00,0x1c06f80,0x212f580,0x2537a00,0x2a60000,0x2e68480,0x3390a80,0x38b9080,0x3cc1500,0x41e9b00,0x45f1f80,0x4b10800,0x4f18c80,0x5437500,0x583f980,0x4b0a980,0x4b0a980,0x4b0a980,0x4b0a980,0x4b0a980,0x4b0a980,0x4b0a980,0x4b0a980,0x4b0a980,0x4b0a980,0x4b0a980,0x4b0a980,0x4b0a980,768,1576,2568,3376,4368,5176,6168,6976,7968,8960,9768,10760,11568,12560,13368,14345,15153,16130,16938,17462,18186,18186,18186,18186,21,20,0,0,0,0,0,0,0x59e080,0x9a6500,0xeceb00,0x12d6f80,0x17ff580,0x1c07a00,0x2130000,0x2538480,0x2a60a80,0x2f89080,0x3391500,0x38b9b00,0x3cc1f80,0x41ea580,0x45f2a00,0x4b11280,0x4f19700,0x5437f80,0x5840400,0x5ae6d00,0x5eb7f80,0x5eb7f80,0x5eb7f80,0x5eb7f80,0x5eb7f80,0x5eb7f80,0x5eb7f80,0x5eb7f80,0x5eb7f80,0x5eb7f80,0x5eb7f80,0x5eb7f80,769,1577,2569,3377,4369,5177,6169,6977,7969,8961,9769,10761,11569,12561,13369,14346,15154,16131,16939,17463,18187,18187,18187,18187,21,20,0,0,0,0,0,0,0x59eb00,0x9a6f80,0xecf580,0x12d7a00,0x1800000,0x1c08480,0x2130a80,0x2538f00,0x2a61500,0x2f89b00,0x3391f80,0x38ba580,0x3cc2a00,0x41eb000,0x45f3480,0x4b11d00,0x4f1a180,0x5438a00,0x5840e80,0x5ae7780,0x5eb8a00,0x5eb8a00,0x5eb8a00,0x5eb8a00,0x5eb8a00,0x5eb8a00,0x5eb8a00,0x5eb8a00,0x5eb8a00,0x5eb8a00,0x5eb8a00,0x5eb8a00,770,1578,2570,3378,4370,5178,6170,6978,7970,8962,9770,10762,11570,12562,13370,14347,15155,16132,16940,17464,18188,18243,18243,18243,22,21,0,0,0,0,0,0,0x59f580,0x9a7a00,0xed0000,0x12d8480,0x1800a80,0x1c08f00,0x2131500,0x2539980,0x2a61f80,0x2f8a580,0x3392a00,0x38bb000,0x3cc3480,0x41eba80,0x45f3f00,0x4b12780,0x4f1ac00,0x5439480,0x5841900,0x5ae8200,0x5eb9480,0x5edd600,0x5edd600,0x5edd600,0x5edd600,0x5edd600,0x5edd600,0x5edd600,0x5edd600,0x5edd600,0x5edd600,0x5edd600,771,1579,2571,3379,4371,5179,6171,6979,7971,8963,9771,10763,11571,12563,13371,14348,15156,16133,16941,17465,18189,18189,18189,18189,21,20,0,0,0,0,0,0,0x5a0000,0x9a8480,0xed0a80,0x12d8f00,0x1801500,0x1c09980,0x2131f80,0x253a400,0x2a62a00,0x2f8b000,0x3393480,0x38bba80,0x3cc3f00,0x41ec500,0x45f4980,0x4b13200,0x4f1b680,0x5439f00,0x5842380,0x5ae8c80,0x5eb9f00,0x5eb9f00,0x5eb9f00,0x5eb9f00,0x5eb9f00,0x5eb9f00,0x5eb9f00,0x5eb9f00,0x5eb9f00,0x5eb9f00,0x5eb9f00,0x5eb9f00,772,1580,2572,3380,4372,5180,6172,6980,7972,8964,9772,10764,11572,12564,13372,14349,15157,16134,16942,17466,18190,18190,18190,18190,21,20,0,0,0,0,0,0,0x5a0a80,0x9a8f00,0xed1500,0x12d9980,0x1801f80,0x1c0a400,0x2132a00,0x253ae80,0x2a63480,0x2f8ba80,0x3393f00,0x38bc500,0x3cc4980,0x41ecf80,0x45f5400,0x4b13c80,0x4f1c100,0x543a980,0x5842e00,0x5ae9700,0x5eba980,0x5eba980,0x5eba980,0x5eba980,0x5eba980,0x5eba980,0x5eba980,0x5eba980,0x5eba980,0x5eba980,0x5eba980,0x5eba980,773,1581,2573,3381,4373,5181,6173,6981,7973,8965,9773,10765,11573,12565,13373,14350,15158,16135,16943,14664,14664,14664,14664,14664,20,19,0,0,0,0,0,0,0x5a1500,0x9a9980,0xed1f80,0x12da400,0x1802a00,0x1c0ae80,0x2133480,0x253b900,0x2a63f00,0x2f8c500,0x3394980,0x38bcf80,0x3cc5400,0x41eda00,0x45f5e80,0x4b14700,0x4f1cb80,0x543b400,0x5843880,0x4c59f00,0x4c59f00,0x4c59f00,0x4c59f00,0x4c59f00,0x4c59f00,0x4c59f00,0x4c59f00,0x4c59f00,0x4c59f00,0x4c59f00,0x4c59f00,0x4c59f00,774,1582,2574,3382,4374,5182,6174,6982,7974,8966,9774,10766,11574,12566,13374,14351,15159,16136,16944,14920,18191,18191,18191,18191,21,20,0,0,0,0,0,0,0x5a1f80,0x9aa400,0xed2a00,0x12dae80,0x1803480,0x1c0b900,0x2133f00,0x253c380,0x2a64980,0x2f8cf80,0x3395400,0x38bda00,0x3cc5e80,0x41ee480,0x45f6900,0x4b15180,0x4f1d600,0x543be80,0x5844300,0x4da9480,0x5ebb400,0x5ebb400,0x5ebb400,0x5ebb400,0x5ebb400,0x5ebb400,0x5ebb400,0x5ebb400,0x5ebb400,0x5ebb400,0x5ebb400,0x5ebb400,775,1583,2575,3383,4375,5183,6175,6983,7975,8967,9775,10767,11575,12567,13375,14352,15160,16137,16945,15176,15176,15176,15176,15176,20,19,0,0,0,0,0,0,0x5a2a00,0x9aae80,0xed3480,0x12db900,0x1803f00,0x1c0c380,0x2134980,0x253ce00,0x2a65400,0x2f8da00,0x3395e80,0x38be480,0x3cc6900,0x41eef00,0x45f7380,0x4b15c00,0x4f1e080,0x543c900,0x5844d80,0x4ef8a00,0x4ef8a00,0x4ef8a00,0x4ef8a00,0x4ef8a00,0x4ef8a00,0x4ef8a00,0x4ef8a00,0x4ef8a00,0x4ef8a00,0x4ef8a00,0x4ef8a00,0x4ef8a00,776,1584,2576,3384,4376,5184,6176,7168,7976,8968,9776,10768,11576,12568,13376,14353,15161,16138,16946,17467,18192,18192,18192,18192,21,20,0,0,0,0,0,0,0x5a3480,0x9ab900,0xed3f00,0x12dc380,0x1804980,0x1c0ce00,0x2135400,0x265da00,0x2a65e80,0x2f8e480,0x3396900,0x38bef00,0x3cc7380,0x41ef980,0x45f7e00,0x4b16680,0x4f1eb00,0x543d380,0x5845800,0x5aea180,0x5ebbe80,0x5ebbe80,0x5ebbe80,0x5ebbe80,0x5ebbe80,0x5ebbe80,0x5ebbe80,0x5ebbe80,0x5ebbe80,0x5ebbe80,0x5ebbe80,0x5ebbe80,777,1585,2577,3385,4377,5185,6177,7169,7977,8969,9777,10769,11577,12569,13377,14354,15162,16139,16947,17468,18193,18193,18193,18193,21,20,0,0,0,0,0,0,0x5a3f00,0x9ac380,0xed4980,0x12dce00,0x1805400,0x1c0d880,0x2135e80,0x265e480,0x2a66900,0x2f8ef00,0x3397380,0x38bf980,0x3cc7e00,0x41f0400,0x45f8880,0x4b17100,0x4f1f580,0x543de00,0x5846280,0x5aeac00,0x5ebc900,0x5ebc900,0x5ebc900,0x5ebc900,0x5ebc900,0x5ebc900,0x5ebc900,0x5ebc900,0x5ebc900,0x5ebc900,0x5ebc900,0x5ebc900,778,1586,2578,3386,4378,5186,6178,7170,7978,8970,9778,10770,11578,12570,13378,14355,15163,16140,16948,17469,18194,18244,18244,18244,22,21,0,0,0,0,0,0,0x5a4980,0x9ace00,0xed5400,0x12dd880,0x1805e80,0x1c0e300,0x2136900,0x265ef00,0x2a67380,0x2f8f980,0x3397e00,0x38c0400,0x3cc8880,0x41f0e80,0x45f9300,0x4b17b80,0x4f20000,0x543e880,0x5846d00,0x5aeb680,0x5ebd380,0x5ede080,0x5ede080,0x5ede080,0x5ede080,0x5ede080,0x5ede080,0x5ede080,0x5ede080,0x5ede080,0x5ede080,0x5ede080,779,1587,2579,3387,4379,5187,6179,7171,7979,8971,9779,10771,11579,12571,13379,14356,15164,16141,16949,17470,18195,18195,18195,18195,21,20,0,0,0,0,0,0,0x5a5400,0x9ad880,0xed5e80,0x12de300,0x1806900,0x1c0ed80,0x2137380,0x265f980,0x2a67e00,0x2f90400,0x3398880,0x38c0e80,0x3cc9300,0x41f1900,0x45f9d80,0x4b18600,0x4f20a80,0x543f300,0x5847780,0x5aec100,0x5ebde00,0x5ebde00,0x5ebde00,0x5ebde00,0x5ebde00,0x5ebde00,0x5ebde00,0x5ebde00,0x5ebde00,0x5ebde00,0x5ebde00,0x5ebde00,780,1588,2580,3388,4380,5188,6180,7172,7980,8972,9780,10772,11580,12572,13380,14357,15165,16142,16950,17471,18196,18196,18196,18196,21,20,0,0,0,0,0,0,0x5a5e80,0x9ae300,0xed6900,0x12ded80,0x1807380,0x1c0f800,0x2137e00,0x2660400,0x2a68880,0x2f90e80,0x3399300,0x38c1900,0x3cc9d80,0x41f2380,0x45fa800,0x4b19080,0x4f21500,0x543fd80,0x5848200,0x5aecb80,0x5ebe880,0x5ebe880,0x5ebe880,0x5ebe880,0x5ebe880,0x5ebe880,0x5ebe880,0x5ebe880,0x5ebe880,0x5ebe880,0x5ebe880,0x5ebe880,781,1589,2581,3389,4381,5189,6181,7173,7981,8973,9781,10773,11581,12573,13381,14358,15166,16143,16951,15432,15432,15432,15432,15432,20,19,0,0,0,0,0,0,0x5a6900,0x9aed80,0xed7380,0x12df800,0x1807e00,0x1c10280,0x2138880,0x2660e80,0x2a69300,0x2f91900,0x3399d80,0x38c2380,0x3cca800,0x41f2e00,0x45fb280,0x4b19b00,0x4f21f80,0x5440800,0x5848c80,0x5047f80,0x5047f80,0x5047f80,0x5047f80,0x5047f80,0x5047f80,0x5047f80,0x5047f80,0x5047f80,0x5047f80,0x5047f80,0x5047f80,0x5047f80,782,1590,2582,3390,4382,5190,6182,7174,7982,8974,9782,10774,11582,12574,13382,14359,15167,16144,16952,15688,18197,18197,18197,18197,21,20,0,0,0,0,0,0,0x5a7380,0x9af800,0xed7e00,0x12e0280,0x1808880,0x1c10d00,0x2139300,0x2661900,0x2a69d80,0x2f92380,0x339a800,0x38c2e00,0x3ccb280,0x41f3880,0x45fbd00,0x4b1a580,0x4f22a00,0x5441280,0x5849700,0x5197500,0x5ebf300,0x5ebf300,0x5ebf300,0x5ebf300,0x5ebf300,0x5ebf300,0x5ebf300,0x5ebf300,0x5ebf300,0x5ebf300,0x5ebf300,0x5ebf300,783,1591,2583,3391,4383,5191,6183,7175,7983,8975,9783,10775,11583,12575,13383,14360,15168,16145,16953,15944,15944,15944,15944,15944,20,19,0,0,0,0,0,0,0x5a7e00,0x9b0280,0xed8880,0x12e0d00,0x1809300,0x1c11780,0x2139d80,0x2662380,0x2a6a800,0x2f92e00,0x339b280,0x38c3880,0x3ccbd00,0x41f4300,0x45fc780,0x4b1b000,0x4f23480,0x5441d00,0x584a180,0x52e6a80,0x52e6a80,0x52e6a80,0x52e6a80,0x52e6a80,0x52e6a80,0x52e6a80,0x52e6a80,0x52e6a80,0x52e6a80,0x52e6a80,0x52e6a80,0x52e6a80,784,1592,2584,3392,4384,5376,6184,7176,7984,8976,9784,10776,11584,12576,13568,14361,15169,16146,16954,17472,18198,18198,18198,18198,21,20,0,0,0,0,0,0,0x5a8880,0x9b0d00,0xed9300,0x12e1780,0x1809d80,0x1d32380,0x213a800,0x2662e00,0x2a6b280,0x2f93880,0x339bd00,0x38c4300,0x3ccc780,0x41f4d80,0x471d380,0x4b1ba80,0x4f23f00,0x5442780,0x584ac00,0x5aed600,0x5ebfd80,0x5ebfd80,0x5ebfd80,0x5ebfd80,0x5ebfd80,0x5ebfd80,0x5ebfd80,0x5ebfd80,0x5ebfd80,0x5ebfd80,0x5ebfd80,0x5ebfd80,785,1593,2585,3393,4385,5377,6185,7177,7985,8977,9785,10777,11585,12577,13569,14362,15170,16147,16955,17473,18199,18199,18199,18199,21,20,0,0,0,0,0,0,0x5a9300,0x9b1780,0xed9d80,0x12e2200,0x180a800,0x1d32e00,0x213b280,0x2663880,0x2a6bd00,0x2f94300,0x339c780,0x38c4d80,0x3ccd200,0x41f5800,0x471de00,0x4b1c500,0x4f24980,0x5443200,0x584b680,0x5aee080,0x5ec0800,0x5ec0800,0x5ec0800,0x5ec0800,0x5ec0800,0x5ec0800,0x5ec0800,0x5ec0800,0x5ec0800,0x5ec0800,0x5ec0800,0x5ec0800,786,1594,2586,3394,4386,5378,6186,7178,7986,8978,9786,10778,11586,12578,13570,14363,15171,16148,16956,17474,18200,18245,18245,18245,22,21,0,0,0,0,0,0,0x5a9d80,0x9b2200,0xeda800,0x12e2c80,0x180b280,0x1d33880,0x213bd00,0x2664300,0x2a6c780,0x2f94d80,0x339d200,0x38c5800,0x3ccdc80,0x41f6280,0x471e880,0x4b1cf80,0x4f25400,0x5443c80,0x584c100,0x5aeeb00,0x5ec1280,0x5edeb00,0x5edeb00,0x5edeb00,0x5edeb00,0x5edeb00,0x5edeb00,0x5edeb00,0x5edeb00,0x5edeb00,0x5edeb00,0x5edeb00,787,1595,2587,3395,4387,5379,6187,7179,7987,8979,9787,10779,11587,12579,13571,14364,15172,16149,16957,17475,18201,18201,18201,18201,21,20,0,0,0,0,0,0,0x5aa800,0x9b2c80,0xedb280,0x12e3700,0x180bd00,0x1d34300,0x213c780,0x2664d80,0x2a6d200,0x2f95800,0x339dc80,0x38c6280,0x3cce700,0x41f6d00,0x471f300,0x4b1da00,0x4f25e80,0x5444700,0x584cb80,0x5aef580,0x5ec1d00,0x5ec1d00,0x5ec1d00,0x5ec1d00,0x5ec1d00,0x5ec1d00,0x5ec1d00,0x5ec1d00,0x5ec1d00,0x5ec1d00,0x5ec1d00,0x5ec1d00,788,1596,2588,3396,4388,5380,6188,7180,7988,8980,9788,10780,11588,12580,13572,14365,15173,16150,16958,17476,18202,18202,18202,18202,21,20,0,0,0,0,0,0,0x5ab280,0x9b3700,0xedbd00,0x12e4180,0x180c780,0x1d34d80,0x213d200,0x2665800,0x2a6dc80,0x2f96280,0x339e700,0x38c6d00,0x3ccf180,0x41f7780,0x471fd80,0x4b1e480,0x4f26900,0x5445180,0x584d600,0x5af0000,0x5ec2780,0x5ec2780,0x5ec2780,0x5ec2780,0x5ec2780,0x5ec2780,0x5ec2780,0x5ec2780,0x5ec2780,0x5ec2780,0x5ec2780,0x5ec2780,789,1597,2589,3397,4389,5381,6189,7181,7989,8981,9789,10781,11589,12581,13573,14366,15174,16151,16959,16200,16200,16200,16200,16200,20,19,0,0,0,0,0,0,0x5abd00,0x9b4180,0xedc780,0x12e4c00,0x180d200,0x1d35800,0x213dc80,0x2666280,0x2a6e700,0x2f96d00,0x339f180,0x38c7780,0x3ccfc00,0x41f8200,0x4720800,0x4b1ef00,0x4f27380,0x5445c00,0x584e080,0x5436000,0x5436000,0x5436000,0x5436000,0x5436000,0x5436000,0x5436000,0x5436000,0x5436000,0x5436000,0x5436000,0x5436000,0x5436000,790,1598,2590,3398,4390,5382,6190,7182,7990,8982,9790,10782,11590,12582,13574,14367,15175,16152,16960,16456,18203,18203,18203,18203,21,20,0,0,0,0,0,0,0x5ac780,0x9b4c00,0xedd200,0x12e5680,0x180dc80,0x1d36280,0x213e700,0x2666d00,0x2a6f180,0x2f97780,0x339fc00,0x38c8200,0x3cd0680,0x41f8c80,0x4721280,0x4b1f980,0x4f27e00,0x5446680,0x584eb00,0x5585580,0x5ec3200,0x5ec3200,0x5ec3200,0x5ec3200,0x5ec3200,0x5ec3200,0x5ec3200,0x5ec3200,0x5ec3200,0x5ec3200,0x5ec3200,0x5ec3200,791,1599,2591,3399,4391,5383,6191,7183,7991,8983,9791,10783,11591,12583,13575,14368,15360,16153,16961,16712,16712,16712,16712,16712,20,19,0,0,0,0,0,0,0x5ad200,0x9b5680,0xeddc80,0x12e6100,0x180e700,0x1d36d00,0x213f180,0x2667780,0x2a6fc00,0x2f98200,0x33a0680,0x38c8c80,0x3cd1100,0x41f9700,0x4721d00,0x4b20400,0x5048a00,0x5447100,0x584f580,0x56d4b00,0x56d4b00,0x56d4b00,0x56d4b00,0x56d4b00,0x56d4b00,0x56d4b00,0x56d4b00,0x56d4b00,0x56d4b00,0x56d4b00,0x56d4b00,0x56d4b00,792,1600,2592,3584,4392,5384,6192,7184,7992,8984,9792,10784,11776,12584,13576,14369,15361,16154,16962,17477,18204,18204,18204,18204,21,20,0,0,0,0,0,0,0x5adc80,0x9b6100,0xede700,0x1406d00,0x180f180,0x1d37780,0x213fc00,0x2668200,0x2a70680,0x2f98c80,0x33a1100,0x38c9700,0x3df1d00,0x41fa180,0x4722780,0x4b20e80,0x5049480,0x5447b80,0x5850000,0x5af0a80,0x5ec3c80,0x5ec3c80,0x5ec3c80,0x5ec3c80,0x5ec3c80,0x5ec3c80,0x5ec3c80,0x5ec3c80,0x5ec3c80,0x5ec3c80,0x5ec3c80,0x5ec3c80,793,1601,2593,3585,4393,5385,6193,7185,7993,8985,9793,10785,11777,12585,13577,14370,15362,16155,16963,17478,18205,18205,18205,18205,21,20,0,0,0,0,0,0,0x5ae700,0x9b6b80,0xedf180,0x1407780,0x180fc00,0x1d38200,0x2140680,0x2668c80,0x2a71100,0x2f99700,0x33a1b80,0x38ca180,0x3df2780,0x41fac00,0x4723200,0x4b21900,0x5049f00,0x5448600,0x5850a80,0x5af1500,0x5ec4700,0x5ec4700,0x5ec4700,0x5ec4700,0x5ec4700,0x5ec4700,0x5ec4700,0x5ec4700,0x5ec4700,0x5ec4700,0x5ec4700,0x5ec4700,794,1602,2594,3586,4394,5386,6194,7186,7994,8986,9794,10786,11778,12586,13578,14371,15363,16156,16964,17479,18206,18246,18246,18246,22,21,0,0,0,0,0,0,0x5af180,0x9b7600,0xedfc00,0x1408200,0x1810680,0x1d38c80,0x2141100,0x2669700,0x2a71b80,0x2f9a180,0x33a2600,0x38cac00,0x3df3200,0x41fb680,0x4723c80,0x4b22380,0x504a980,0x5449080,0x5851500,0x5af1f80,0x5ec5180,0x5edf580,0x5edf580,0x5edf580,0x5edf580,0x5edf580,0x5edf580,0x5edf580,0x5edf580,0x5edf580,0x5edf580,0x5edf580,795,1603,2595,3587,4395,5387,6195,7187,7995,8987,9795,10787,11779,12587,13579,14372,15364,16157,16965,17664,18207,18207,18207,18207,21,20,0,0,0,0,0,0,0x5afc00,0x9b8080,0xee0680,0x1408c80,0x1811100,0x1d39700,0x2141b80,0x266a180,0x2a72600,0x2f9ac00,0x33a3080,0x38cb680,0x3df3c80,0x41fc100,0x4724700,0x4b22e00,0x504b400,0x5449b00,0x5851f80,0x5c12b80,0x5ec5c00,0x5ec5c00,0x5ec5c00,0x5ec5c00,0x5ec5c00,0x5ec5c00,0x5ec5c00,0x5ec5c00,0x5ec5c00,0x5ec5c00,0x5ec5c00,0x5ec5c00,796,1604,2596,3588,4396,5388,6196,7188,7996,8988,9796,10788,11780,12588,13580,14373,15365,16158,16966,17665,18208,18208,18208,18208,21,20,0,0,0,0,0,0,0x5b0680,0x9b8b00,0xee1100,0x1409700,0x1811b80,0x1d3a180,0x2142600,0x266ac00,0x2a73080,0x2f9b680,0x33a3b00,0x38cc100,0x3df4700,0x41fcb80,0x4725180,0x4b23880,0x504be80,0x544a580,0x5852a00,0x5c13600,0x5ec6680,0x5ec6680,0x5ec6680,0x5ec6680,0x5ec6680,0x5ec6680,0x5ec6680,0x5ec6680,0x5ec6680,0x5ec6680,0x5ec6680,0x5ec6680,797,1605,2597,3589,4397,5389,6197,7189,7997,8989,9797,10789,11781,12589,13581,14374,15366,16159,16967,16968,16968,16968,16968,16968,20,19,0,0,0,0,0,0,0x5b1100,0x9b9580,0xee1b80,0x140a180,0x1812600,0x1d3ac00,0x2143080,0x266b680,0x2a73b00,0x2f9c100,0x33a4580,0x38ccb80,0x3df5180,0x41fd600,0x4725c00,0x4b24300,0x504c900,0x544b000,0x5853480,0x5824080,0x5824080,0x5824080,0x5824080,0x5824080,0x5824080,0x5824080,0x5824080,0x5824080,0x5824080,0x5824080,0x5824080,0x5824080,798,1606,2598,3590,4398,5390,6198,7190,7998,8990,9798,10790,11782,12590,13582,14375,15367,16160,17152,17224,18209,18209,18209,18209,21,20,0,0,0,0,0,0,0x5b1b80,0x9ba000,0xee2600,0x140ac00,0x1813080,0x1d3b680,0x2143b00,0x266c100,0x2a74580,0x2f9cb80,0x33a5000,0x38cd600,0x3df5c00,0x41fe080,0x4726680,0x4b24d80,0x504d380,0x544ba80,0x5974080,0x5973600,0x5ec7100,0x5ec7100,0x5ec7100,0x5ec7100,0x5ec7100,0x5ec7100,0x5ec7100,0x5ec7100,0x5ec7100,0x5ec7100,0x5ec7100,0x5ec7100,799,1607,2599,3591,4399,5391,6199,7191,7999,8991,9799,10791,11783,12591,13583,14376,15368,16161,17153,17480,17480,17480,17480,17480,20,19,0,0,0,0,0,0,0x5b2600,0x9baa80,0xee3080,0x140b680,0x1813b00,0x1d3c100,0x2144580,0x266cb80,0x2a75000,0x2f9d600,0x33a5a80,0x38ce080,0x3df6680,0x41feb00,0x4727100,0x4b25800,0x504de00,0x544c500,0x5974b00,0x5ac2b80,0x5ac2b80,0x5ac2b80,0x5ac2b80,0x5ac2b80,0x5ac2b80,0x5ac2b80,0x5ac2b80,0x5ac2b80,0x5ac2b80,0x5ac2b80,0x5ac2b80,0x5ac2b80,800,1792,2600,3592,4400,5392,6200,7192,8000,8992,9984,10792,11784,12592,13584,14377,15369,16162,17154,17666,18210,18210,18210,18210,21,20,0,0,0,0,0,0,0x5b3080,0xadb680,0xee3b00,0x140c100,0x1814580,0x1d3cb80,0x2145000,0x266d600,0x2a75a80,0x2f9e080,0x34c6680,0x38ceb00,0x3df7100,0x41ff580,0x4727b80,0x4b26280,0x504e880,0x544cf80,0x5975580,0x5c14080,0x5ec7b80,0x5ec7b80,0x5ec7b80,0x5ec7b80,0x5ec7b80,0x5ec7b80,0x5ec7b80,0x5ec7b80,0x5ec7b80,0x5ec7b80,0x5ec7b80,0x5ec7b80,801,1793,2601,3593,4401,5393,6201,7193,8001,8993,9985,10793,11785,12593,13585,14378,15370,16163,17155,17667,18211,18211,18211,18211,21,20,0,0,0,0,0,0,0x5b3b00,0xadc100,0xee4580,0x140cb80,0x1815000,0x1d3d600,0x2145a80,0x266e080,0x2a76500,0x2f9eb00,0x34c7100,0x38cf580,0x3df7b80,0x4200000,0x4728600,0x4b26d00,0x504f300,0x544da00,0x5976000,0x5c14b00,0x5ec8600,0x5ec8600,0x5ec8600,0x5ec8600,0x5ec8600,0x5ec8600,0x5ec8600,0x5ec8600,0x5ec8600,0x5ec8600,0x5ec8600,0x5ec8600,802,1794,2602,3594,4402,5394,6202,7194,8002,8994,9986,10794,11786,12594,13586,14379,15371,16164,17156,17668,18212,18247,18247,18247,22,21,0,0,0,0,0,0,0x5b4580,0xadcb80,0xee5000,0x140d600,0x1815a80,0x1d3e080,0x2146500,0x266eb00,0x2a76f80,0x2f9f580,0x34c7b80,0x38d0000,0x3df8600,0x4200a80,0x4729080,0x4b27780,0x504fd80,0x544e480,0x5976a80,0x5c15580,0x5ec9080,0x5ee0000,0x5ee0000,0x5ee0000,0x5ee0000,0x5ee0000,0x5ee0000,0x5ee0000,0x5ee0000,0x5ee0000,0x5ee0000,0x5ee0000,803,1795,2603,3595,4403,5395,6203,7195,8003,8995,9987,10795,11787,12595,13587,14380,15372,16165,17157,17669,18213,18213,18213,18213,21,20,0,0,0,0,0,0,0x5b5000,0xadd600,0xee5a80,0x140e080,0x1816500,0x1d3eb00,0x2146f80,0x266f580,0x2a77a00,0x2fa0000,0x34c8600,0x38d0a80,0x3df9080,0x4201500,0x4729b00,0x4b28200,0x5050800,0x544ef00,0x5977500,0x5c16000,0x5ec9b00,0x5ec9b00,0x5ec9b00,0x5ec9b00,0x5ec9b00,0x5ec9b00,0x5ec9b00,0x5ec9b00,0x5ec9b00,0x5ec9b00,0x5ec9b00,0x5ec9b00,804,1796,2604,3596,4404,5396,6204,7196,8004,8996,9988,10796,11788,12596,13588,14381,15373,16166,17158,17670,18214,18214,18214,18214,21,20,0,0,0,0,0,0,0x5b5a80,0xade080,0xee6500,0x140eb00,0x1816f80,0x1d3f580,0x2147a00,0x2670000,0x2a78480,0x2fa0a80,0x34c9080,0x38d1500,0x3df9b00,0x4201f80,0x472a580,0x4b28c80,0x5051280,0x544f980,0x5977f80,0x5c16a80,0x5eca580,0x5eca580,0x5eca580,0x5eca580,0x5eca580,0x5eca580,0x5eca580,0x5eca580,0x5eca580,0x5eca580,0x5eca580,0x5eca580,805,1797,2605,3597,4405,5397,6205,7197,8005,8997,9989,10797,11789,12597,13589,14382,15374,16167,17159,17736,17736,17736,17736,17736,20,19,0,0,0,0,0,0,0x5b6500,0xadeb00,0xee6f80,0x140f580,0x1817a00,0x1d40000,0x2148480,0x2670a80,0x2a78f00,0x2fa1500,0x34c9b00,0x38d1f80,0x3dfa580,0x4202a00,0x472b000,0x4b29700,0x5051d00,0x5450400,0x5978a00,0x5c12100,0x5c12100,0x5c12100,0x5c12100,0x5c12100,0x5c12100,0x5c12100,0x5c12100,0x5c12100,0x5c12100,0x5c12100,0x5c12100,0x5c12100,806,1798,2606,3598,4406,5398,6206,7198,8006,8998,9990,10798,11790,12598,13590,14383,15375,16168,17160,17992,18215,18215,18215,18215,21,20,0,0,0,0,0,0,0x5b6f80,0xadf580,0xee7a00,0x1410000,0x1818480,0x1d40a80,0x2148f00,0x2671500,0x2a79980,0x2fa1f80,0x34ca580,0x38d2a00,0x3dfb000,0x4203480,0x472ba80,0x4b2a180,0x5052780,0x5450e80,0x5979480,0x5d61680,0x5ecb000,0x5ecb000,0x5ecb000,0x5ecb000,0x5ecb000,0x5ecb000,0x5ecb000,0x5ecb000,0x5ecb000,0x5ecb000,0x5ecb000,0x5ecb000,807,1799,2607,3599,4407,5399,6207,7199,8007,8999,9991,10799,11791,12599,13591,14384,15376,16169,17161,18248,18248,18248,18248,18248,20,19,0,0,0,0,0,0,0x5b7a00,0xae0000,0xee8480,0x1410a80,0x1818f00,0x1d41500,0x2149980,0x2671f80,0x2a7a400,0x2fa2a00,0x34cb000,0x38d3480,0x3dfba80,0x4203f00,0x472c500,0x4b2ac00,0x5053200,0x5451900,0x5979f00,0x5eb0c00,0x5eb0c00,0x5eb0c00,0x5eb0c00,0x5eb0c00,0x5eb0c00,0x5eb0c00,0x5eb0c00,0x5eb0c00,0x5eb0c00,0x5eb0c00,0x5eb0c00,0x5eb0c00};

__global__ __launch_bounds__(256, 1) void swin_main(const uint16_t* x, const uint16_t* mask, char* ws, uint16_t* out,
                                                    const uint16_t* wqkv, const uint16_t* wo, const uint16_t* ls,
                                                    const uint16_t* w1, const uint16_t* w2, const uint16_t* rc,
                                                    const int* ridx) {
  __shared__ __attribute__((aligned(16))) char smem[LDS_BYTES];
  const int wave = __builtin_amdgcn_readfirstlane(threadIdx.x >> 6);
  const unsigned lds0 = (unsigned)(size_t)smem;
  asm volatile(
      ".p2align 5\n"
      "s_load_dwordx8 s[16:23], %0, 0\n"
      "s_load_dwordx4 s[4:7], %0, 32\n"
      "s_load_dwordx8 s[80:87], %0, 48\n"
      "s_load_dwordx2 s[90:91], %0, 80\n"
      "s_mov_b32 s24, %1\n"
      "s_mov_b32 s25, %2\n"
      "s_mov_b64 s[60:61], %3\n"
      "s_getpc_b64 s[12:13]\n"
      "L_ipf0_%=:\n"
      "v_mbcnt_lo_u32_b32 v225, -1, 0\n"
      "v_mbcnt_hi_u32_b32 v225, -1, v225\n"
      "s_lshr_b32 s28, s25, 3\n"
      "s_lshl_b32 s29, s28, 2\n"
      "s_add_u32 s29, s29, s24\n"
      "s_mul_i32 s29, s29, 14\n"
      "v_min_u32_e64 v218, 13, v225\n"
      "v_add_lshl_u32 v218, v218, s29, 7\n"
      "v_min_u32 v218, 221056, v218\n"
      "v_lshlrev_b32_e64 v164, 4, v225\n"
      "s_lshl_b32 s29, s24, 6\n"
      "s_add_u32 s54, s60, s29\n"
      "s_addc_u32 s55, s61, 0\n"
      "s_and_b32 s29, s29, 64\n"
      "v_xor_b32_e64 v226, s29, v164\n"
      "s_mul_i32 s30, s24, 9216\n"
      "v_add_u32_e64 v226, s30, v226\n"
      "s_add_u32 s2, s30, 36864\n"
      "v_xor_b32 v216, 32, v226\n"
      "s_mov_b64 s[34:35], 0xffff\n"
      "v_cndmask_b32 v147, v216, v226, s[34:35]\n"
      "s_lshl_b64 s[34:35], s[34:35], 48\n"
      "v_cndmask_b32 v224, v226, v216, s[34:35]\n"
      "s_lshl_b64 s[34:35], -1, 32\n"
      "v_cndmask_b32 v219, v216, v226, s[34:35]\n"
      "s_waitcnt lgkmcnt(0)\n"
      "global_load_dword v31, v218, s[4:5]\n"
      "s_add_u32 m0, s2, 0\n"
      "s_nop 0\n"
      "global_load_lds_dwordx4 v226, s[4:5]\n"
      "global_load_lds_dwordx4 v226, s[4:5] offset:1024\n"
      "global_load_lds_dwordx4 v147, s[4:5] offset:2048\n"
      "global_load_lds_dwordx4 v216, s[4:5] offset:3072\n"
      "s_add_u32 s4, s4, 4096\n"
      "s_addc_u32 s5, s5, 0\n"
      "s_add_u32 m0, s2, 4096\n"
      "s_nop 0\n"
      "global_load_lds_dwordx4 v219, s[4:5]\n"
      "global_load_lds_dwordx4 v226, s[4:5] offset:1024\n"
      "global_load_lds_dwordx4 v224, s[4:5] offset:2048\n"
      "global_load_lds_dwordx4 v216, s[4:5] offset:3072\n"
      "s_add_u32 s4, s4, 4096\n"
      "s_addc_u32 s5, s5, 0\n"
      "s_add_u32 m0, s2, 8192\n"
      "s_nop 0\n"
      "global_load_lds_dwordx4 v216, s[4:5]\n"
      "s_add_u32 s4, s4, 28672\n"
      "s_addc_u32 s5, s5, 0\n"
      "s_load_dwordx16 s[36:51], s[82:83], 0\n"
      "s_load_dwordx2 s[8:9], s[80:81], 0\n"
      "s_load_dwordx16 s[64:79], s[84:85], 0\n"
      "s_lshl_b32 s29, s24, 8\n"
      "v_lshl_add_u32 v217, v225, 2, s29\n"
      "v_min_u32 v217, 672, v217\n"
      "v_add_u32 v227, 150640, v217\n"
      "s_lshl_b32 s29, s28, 2\n"
      "s_add_u32 s29, s29, s24\n"
      "s_mov_b64 s[34:35], s[16:17]\n"
      "s_mov_b32 s30, s29\n"
      "s_sub_u32 s31, s29, 48\n"
      "s_cmp_ge_u32 s29, 48\n"
      "s_cselect_b64 s[34:35], s[22:23], s[34:35]\n"
      "s_cselect_b32 s30, s31, s30\n"
      "s_sub_u32 s31, s29, 96\n"
      "s_cmp_ge_u32 s29, 96\n"
      "s_cselect_b64 s[34:35], s[18:19], s[34:35]\n"
      "s_cselect_b32 s30, s31, s30\n"
      "s_sub_u32 s31, s29, 54\n"
      "s_cmp_ge_u32 s29, 109\n"
      "s_cselect_b64 s[34:35], s[20:21], s[34:35]\n"
      "s_cselect_b32 s30, s31, s30\n"
      "s_lshl_b32 s30, s30, 21\n"
      "s_load_dword s100, s[34:35], s30\n"
      "s_and_b32 s29, s29, 7\n"
      "s_cmp_eq_u32 s29, 0\n"
      "s_cselect_b64 s[34:35], s[6:7], s[34:35]\n"
      "s_cmp_eq_u32 s29, 1\n"
      "s_cselect_b64 s[34:35], s[90:91], s[34:35]\n"
      "s_cmp_eq_u32 s29, 2\n"
      "s_cselect_b64 s[34:35], s[86:87], s[34:35]\n"
      "s_cmp_eq_u32 s29, 3\n"
      "s_cselect_b64 s[34:35], s[60:61], s[34:35]\n"
      "s_cmp_eq_u32 s29, 4\n"
      "s_cselect_b64 s[34:35], s[12:13], s[34:35]\n"
      "s_load_dword s100, s[34:35], 0\n"
      "s_add_u32 m0, s2, 36864\n"
      "s_nop 0\n"
      "global_load_lds_dwordx4 v226, s[4:5]\n"
      "global_load_lds_dwordx4 v226, s[4:5] offset:1024\n"
      "global_load_lds_dwordx4 v147, s[4:5] offset:2048\n"
      "global_load_lds_dwordx4 v216, s[4:5] offset:3072\n"
      "s_add_u32 s4, s4, 4096\n"
      "s_addc_u32 s5, s5, 0\n"
      "s_add_u32 m0, s2, 40960\n"
      "s_nop 0\n"
      "global_load_lds_dwordx4 v219, s[4:5]\n"
      "global_load_lds_dwordx4 v226, s[4:5] offset:1024\n"
      "global_load_lds_dwordx4 v224, s[4:5] offset:2048\n"
      "global_load_lds_dwordx4 v216, s[4:5] offset:3072\n"
      "s_add_u32 s4, s4, 4096\n"
      "s_addc_u32 s5, s5, 0\n"
      "s_add_u32 m0, s2, 45056\n"
      "s_nop 0\n"
      "global_load_lds_dwordx4 v216, s[4:5]\n"
      "s_add_u32 s4, s4, 32768\n"
      "s_addc_u32 s5, s5, 0\n"
      "v_add_u32 v146, 4096, v216\n"
      "v_add_u32 v144, s2, v164\n"
      "v_lshrrev_b32 v145, 2, v225\n"
      "global_load_dwordx4 a[72:75], v226, s[4:5] offset:-4096\n"
      "v_and_b32 v165, 3, v145\n"
      "v_lshrrev_b32_e64 v166, 4, v225\n"
      "global_load_dwordx4 a[76:79], v226, s[4:5] offset:-3072\n"
      "v_lshl_add_u32 v165, v166, 3, v165\n"
      "v_mul_u32_u24 v165, 1152, v165\n"
      "global_load_dwordx4 a[80:83], v147, s[4:5] offset:-2048\n"
      "v_and_b32_e64 v166, 1, v225\n"
      "v_lshl_add_u32 v165, v166, 3, v165\n"
      "global_load_dwordx4 a[84:87], v216, s[4:5] offset:-1024\n"
      "v_add_u32 v165, 36864, v165\n"
      "v_and_b32_e64 v145, 6, v145\n"
      "global_load_dwordx4 a[88:91], v219, s[4:5]\n"
      "v_bfe_u32 v166, v225, 1, 1\n"
      "s_mul_i32 s29, s24, 6\n"
      "global_load_dwordx4 a[92:95], v226, s[4:5] offset:1024\n"
      "v_add_u32 v166, s29, v166\n"
      "v_xor_b32_e64 v232, v166, v145\n"
      "global_load_dwordx4 a[96:99], v224, s[4:5] offset:2048\n"
      "v_lshl_add_u32 v232, v232, 4, v165\n"
      "v_add_u32_e64 v166, 2, v166\n"
      "v_xor_b32_e64 v235, v166, v145\n"
      "global_load_dwordx4 a[100:103], v216, s[4:5] offset:3072\n"
      "v_lshl_add_u32 v235, v235, 4, v165\n"
      "v_add_u32_e64 v166, 2, v166\n"
      "global_load_dwordx4 a[104:107], v146, s[4:5]\n"
      "v_xor_b32_e64 v167, v166, v145\n"
      "v_lshl_add_u32 v167, v167, 4, v165\n"
      "s_movk_i32 s29, 18\n"
      "s_mov_b32 s30, 73600\n"
      "s_cmp_eq_u32 s24, 1\n"
      "v_add_u32 v226, 36864, v226\n"
      "v_add_u32 v216, 36864, v216\n"
      "v_add_u32 v147, 36864, v147\n"
      "v_add_u32 v219, 36864, v219\n"
      "v_add_u32 v224, 36864, v224\n"
      "v_add_u32 v146, 36864, v146\n"
      "global_load_dwordx4 a[108:111], v226, s[4:5] offset:-4096\n"
      "s_cselect_b64 s[12:13], s[6:7], s[12:13]\n"
      "s_cmp_eq_u32 s24, 2\n"
      "s_cselect_b32 s29, 16, s29\n"
      "s_cselect_b32 s30, L_cend_%=-L_ipf0_%=-128, s30\n"
      "s_cmp_eq_u32 s24, 3\n"
      "global_load_dwordx4 a[112:115], v226, s[4:5] offset:-3072\n"
      "s_cselect_b64 s[12:13], s[60:61], s[12:13]\n"
      "s_cselect_b32 s29, 10, s29\n"
      "s_cselect_b32 s30, 40832, s30\n"
      "global_load_dwordx4 a[116:119], v147, s[4:5] offset:-2048\n"
      "s_cmp_eq_u32 s24, 0\n"
      "s_cselect_b64 s[12:13], s[90:91], s[12:13]\n"
      "global_load_dwordx4 a[120:123], v216, s[4:5] offset:-1024\n"
      "s_cselect_b32 s29, 3, s29\n"
      "s_cselect_b32 s30, 9476, s30\n"
      "global_load_dwordx4 a[124:127], v219, s[4:5]\n"
      "s_mul_i32 s28, s28, s29\n"
      "s_sub_u32 s29, s29, 1\n"
      "global_load_dwordx4 a[128:131], v226, s[4:5] offset:1024\n"
      "v_min_u32 v218, s29, v225\n"
      "v_add_lshl_u32 v218, v218, s28, 7\n"
      "global_load_dwordx4 a[132:135], v224, s[4:5] offset:2048\n"
      "v_min_u32_e64 v218, s30, v218\n"
      "s_mul_i32 s28, s24, 10240\n"
      "global_load_dwordx4 a[136:139], v216, s[4:5] offset:3072\n"
      "s_add_u32 s28, s28, 4096\n"
      "v_add_u32 v165, s28, v164\n"
      "global_load_dwordx4 a[140:143], v146, s[4:5]\n"
      "v_add_u32 v145, 8192, v165\n"
      "global_load_dword v217, v217, s[86:87]\n"
      "s_mov_b64 s[56:57], s[90:91]\n"
      "s_load_dwordx16 s[84:99], s[84:85], 64\n"
      "s_lshl_b32 s29, s25, 8\n"
      "s_add_u32 s52, s60, s29\n"
      "s_addc_u32 s53, s61, 0\n"
      "s_lshl_b32 s29, s24, 5\n"
      "v_lshrrev_b32_e64 v166, 3, v225\n"
      "v_lshl_add_u32 v234, v166, 2, s29\n"
      "v_add_u32 v234, 41408, v234\n"
      "global_load_dword v234, v234, s[52:53]\n"
      "global_load_dword v30, v218, s[12:13]\n"
      "v_xor_b32 v166, v166, v225\n"
      "v_and_b32 v166, 7, v166\n"
      "s_waitcnt vmcnt(21)\n"
      "s_barrier\n"
      "ds_read_b64_tr_b16 a[0:1], v232\n"
      "ds_read_b64_tr_b16 a[2:3], v232 offset:4608\n"
      "ds_read_b64_tr_b16 a[4:5], v235\n"
      "ds_read_b64_tr_b16 a[6:7], v235 offset:4608\n"
      "ds_read_b64_tr_b16 a[8:9], v167\n"
      "ds_read_b64_tr_b16 a[10:11], v167 offset:4608\n"
      "ds_read_b64_tr_b16 a[12:13], v232 offset:384\n"
      "ds_read_b64_tr_b16 a[14:15], v232 offset:4992\n"
      "ds_read_b64_tr_b16 a[16:17], v235 offset:384\n"
      "v_add_u32 v226, 36864, v226\n"
      "v_add_u32 v216, 36864, v216\n"
      "v_add_u32 v147, 36864, v147\n"
      "v_add_u32 v219, 36864, v219\n"
      "v_add_u32 v224, 36864, v224\n"
      "v_add_u32 v146, 36864, v146\n"
      "global_load_dwordx4 a[144:147], v226, s[4:5] offset:-4096\n"
      "ds_read_b64_tr_b16 a[18:19], v235 offset:4992\n"
      "ds_read_b64_tr_b16 a[20:21], v167 offset:384\n"
      "ds_read_b64_tr_b16 a[22:23], v167 offset:4992\n"
      "ds_read_b64_tr_b16 a[24:25], v232 offset:768\n"
      "ds_read_b64_tr_b16 a[26:27], v232 offset:5376\n"
      "ds_read_b64_tr_b16 a[28:29], v235 offset:768\n"
      "ds_read_b64_tr_b16 a[30:31], v235 offset:5376\n"
      "ds_read_b64_tr_b16 a[32:33], v167 offset:768\n"
      "ds_read_b64_tr_b16 a[34:35], v167 offset:5376\n"
      "ds_read_b64_tr_b16 a[36:37], v232 offset:36864\n"
      "ds_read_b64_tr_b16 a[38:39], v232 offset:41472\n"
      "global_load_dwordx4 a[148:151], v226, s[4:5] offset:-3072\n"
      "ds_read_b64_tr_b16 a[40:41], v235 offset:36864\n"
      "ds_read_b64_tr_b16 a[42:43], v235 offset:41472\n"
      "ds_read_b64_tr_b16 a[44:45], v167 offset:36864\n"
      "ds_read_b64_tr_b16 a[46:47], v167 offset:41472\n"
      "global_load_dwordx4 a[152:155], v147, s[4:5] offset:-2048\n"
      "ds_read_b64_tr_b16 a[48:49], v232 offset:37248\n"
      "ds_read_b64_tr_b16 a[50:51], v232 offset:41856\n"
      "ds_read_b64_tr_b16 a[52:53], v235 offset:37248\n"
      "ds_read_b64_tr_b16 a[54:55], v235 offset:41856\n"
      "global_load_dwordx4 a[156:159], v216, s[4:5] offset:-1024\n"
      "ds_read_b64_tr_b16 a[56:57], v167 offset:37248\n"
      "ds_read_b64_tr_b16 a[58:59], v167 offset:41856\n"
      "ds_read_b64_tr_b16 a[60:61], v232 offset:37632\n"
      "ds_read_b64_tr_b16 a[62:63], v232 offset:42240\n"
      "global_load_dwordx4 a[160:163], v219, s[4:5]\n"
      "ds_read_b64_tr_b16 a[64:65], v235 offset:37632\n"
      "ds_read_b64_tr_b16 a[66:67], v235 offset:42240\n"
      "ds_read_b64_tr_b16 a[68:69], v167 offset:37632\n"
      "ds_read_b64_tr_b16 a[70:71], v167 offset:42240\n"
      "s_waitcnt lgkmcnt(0)\n"
      "s_pack_ll_b32_b16 s28, s36, s44\n"
      "s_waitcnt vmcnt(7)\n"
      "v_dot2_f32_bf16 v218, s28, v217, 0\n"
      "s_pack_hh_b32_b16 s29, s36, s44\n"
      "v_dot2_f32_bf16 v225, s29, v217, 0\n"
      "s_pack_ll_b32_b16 s30, s37, s45\n"
      "v_dot2_f32_bf16 v161, s30, v217, 0\n"
      "s_pack_hh_b32_b16 s31, s37, s45\n"
      "v_dot2_f32_bf16 v209, s31, v217, 0\n"
      "s_pack_ll_b32_b16 s28, s38, s46\n"
      "v_dot2_f32_bf16 v163, s28, v217, 0\n"
      "s_pack_hh_b32_b16 s29, s38, s46\n"
      "v_dot2_f32_bf16 v162, s29, v217, 0\n"
      "s_pack_ll_b32_b16 s30, s39, s47\n"
      "v_dot2_f32_bf16 v233, s30, v217, 0\n"
      "s_pack_hh_b32_b16 s31, s39, s47\n"
      "v_dot2_f32_bf16 v211, s31, v217, 0\n"
      "s_pack_ll_b32_b16 s28, s40, s48\n"
      "v_dot2_f32_bf16 v160, s28, v217, 0\n"
      "s_waitcnt vmcnt(6)\n"
      "v_lshl_add_u32 v234, v166, 4, v234\n"
      "s_lshl_b32 s10, s24, 10\n"
      "s_add_u32 m0, s10, 18432\n"
      "s_nop 0\n"
      "global_load_lds_dwordx4 v234, s[16:17]\n"
      "s_add_u32 m0, s10, 24448\n"
      "s_nop 0\n"
      "global_load_lds_dwordx4 v234, s[16:17] offset:128\n"
      "s_add_u32 m0, s10, 30464\n"
      "s_nop 0\n"
      "global_load_lds_dwordx4 v234, s[16:17] offset:256\n"
      "v_add_u32_e64 v166, s10, v164\n"
      "s_add_u32 m0, s10, 124928\n"
      "s_nop 0\n"
      "global_load_lds_dwordx4 v166, s[56:57]\n"
      "v_add_u32 v166, 4096, v166\n"
      "s_add_u32 m0, s10, 129024\n"
      "s_nop 0\n"
      "global_load_lds_dwordx4 v166, s[56:57]\n"
      "v_add_u32 v166, 4096, v166\n"
      "v_min_u32 v166, 9600, v166\n"
      "s_add_u32 m0, s10, 133120\n"
      "s_nop 0\n"
      "global_load_lds_dwordx4 v166, s[56:57]\n"
      "s_pack_hh_b32_b16 s29, s40, s48\n"
      "v_dot2_f32_bf16 v166, s29, v217, 0\n"
      "s_pack_ll_b32_b16 s30, s41, s49\n"
      "v_dot2_f32_bf16 v164, s30, v217, 0\n"
      "s_pack_hh_b32_b16 s31, s41, s49\n"
      "v_dot2_f32_bf16 v182, s31, v217, 0\n"
      "s_pack_ll_b32_b16 s28, s42, s50\n"
      "v_dot2_f32_bf16 v247, s28, v217, 0\n"
      "s_pack_hh_b32_b16 s29, s42, s50\n"
      "v_dot2_f32_bf16 v210, s29, v217, 0\n"
      "s_pack_ll_b32_b16 s30, s43, s51\n"
      "v_dot2_f32_bf16 v208, s30, v217, 0\n"
      "s_pack_hh_b32_b16 s31, s43, s51\n"
      "v_dot2_f32_bf16 v244, s31, v217, 0\n"
      "s_load_dwordx8 s[36:43], s[60:61], 41216\n"
      "s_load_dwordx4 s[44:47], s[60:61], 41248\n"
      "s_mov_b64 s[48:49], s[60:61]\n"
      "v_max_f32 v218, 0, v218\n"
      "v_max_f32 v225, 0, v225\n"
      "v_cvt_pk_bf16_f32 v218, v218, v225\n"
      "v_max_f32 v161, 0, v161\n"
      "v_max_f32 v209, 0, v209\n"
      "global_load_dwordx4 v[114:117], v165, s[60:61] offset:-4096\n"
      "v_cvt_pk_bf16_f32 v161, v161, v209\n"
      "v_max_f32 v163, 0, v163\n"
      "v_max_f32_e64 v162, 0, v162\n"
      "v_cvt_pk_bf16_f32 v163, v163, v162\n"
      "v_max_f32 v233, 0, v233\n"
      "global_load_dwordx4 a[164:167], v226, s[4:5] offset:1024\n"
      "v_max_f32 v211, 0, v211\n"
      "v_cvt_pk_bf16_f32 v233, v233, v211\n"
      "v_max_f32 v160, 0, v160\n"
      "v_max_f32 v166, 0, v166\n"
      "global_load_dwordx4 a[168:171], v224, s[4:5] offset:2048\n"
      "v_cvt_pk_bf16_f32 v160, v160, v166\n"
      "v_max_f32 v164, 0, v164\n"
      "v_max_f32 v182, 0, v182\n"
      "v_cvt_pk_bf16_f32 v164, v164, v182\n"
      "global_load_dwordx4 a[172:175], v216, s[4:5] offset:3072\n"
      "v_max_f32 v247, 0, v247\n"
      "v_max_f32 v210, 0, v210\n"
      "v_cvt_pk_bf16_f32 v247, v247, v210\n"
      "v_max_f32 v208, 0, v208\n"
      "global_load_dwordx4 a[176:179], v146, s[4:5]\n"
      "v_max_f32 v244, 0, v244\n"
      "v_cvt_pk_bf16_f32 v208, v208, v244\n"
      "s_waitcnt lgkmcnt(0)\n"
      "s_barrier\n"
      "s_pack_ll_b32_b16 s28, s64, s66\n"
      "v_dot2_f32_bf16 v176, s28, v218, 0\n"
      "s_pack_hh_b32_b16 s29, s64, s66\n"
      "v_dot2_f32_bf16 v177, s29, v218, 0\n"
      "s_pack_ll_b32_b16 s30, s65, s67\n"
      "v_dot2_f32_bf16 v178, s30, v218, 0\n"
      "s_pack_hh_b32_b16 s31, s65, s67\n"
      "v_dot2_f32_bf16 v179, s31, v218, 0\n"
      "s_pack_ll_b32_b16 s28, s68, s70\n"
      "v_dot2_f32_bf16 v176, s28, v161, v176\n"
      "s_pack_hh_b32_b16 s29, s68, s70\n"
      "v_dot2_f32_bf16 v177, s29, v161, v177\n"
      "s_pack_ll_b32_b16 s30, s69, s71\n"
      "v_dot2_f32_bf16 v178, s30, v161, v178\n"
      "s_pack_hh_b32_b16 s31, s69, s71\n"
      "v_dot2_f32_bf16 v179, s31, v161, v179\n"
      "v_add_u32 v226, 36864, v226\n"
      "v_add_u32 v216, 36864, v216\n"
      "v_add_u32 v147, 36864, v147\n"
      "v_add_u32 v219, 36864, v219\n"
      "v_add_u32 v224, 36864, v224\n"
      "v_add_u32 v146, 36864, v146\n"
      "global_load_dwordx4 a[180:183], v226, s[4:5] offset:-4096\n"
      "s_pack_ll_b32_b16 s28, s72, s74\n"
      "v_dot2_f32_bf16 v176, s28, v163, v176\n"
      "s_pack_hh_b32_b16 s29, s72, s74\n"
      "v_dot2_f32_bf16 v177, s29, v163, v177\n"
      "s_pack_ll_b32_b16 s30, s73, s75\n"
      "v_dot2_f32_bf16 v178, s30, v163, v178\n"
      "s_pack_hh_b32_b16 s31, s73, s75\n"
      "v_dot2_f32_bf16 v179, s31, v163, v179\n"
      "s_pack_ll_b32_b16 s28, s76, s78\n"
      "v_dot2_f32_bf16 v176, s28, v233, v176\n"
      "s_pack_hh_b32_b16 s29, s76, s78\n"
      "v_dot2_f32_bf16 v177, s29, v233, v177\n"
      "s_pack_ll_b32_b16 s30, s77, s79\n"
      "v_dot2_f32_bf16 v178, s30, v233, v178\n"
      "s_pack_hh_b32_b16 s31, s77, s79\n"
      "v_dot2_f32_bf16 v179, s31, v233, v179\n"
      "s_load_dwordx16 s[64:79], s[54:55], 40960\n"
      "s_lshl_b32 s28, s24, 4\n"
      "global_load_dwordx4 a[184:187], v226, s[4:5] offset:-3072\n"
      "s_lshr_b64 s[8:9], s[8:9], s28\n"
      "s_lshl_b32 s8, s8, 16\n"
      "v_mov_b32 v225, s8\n"
      "v_max_f32 v225, 0xc2c80000, v225\n"
      "v_min_f32 v225, 0x40930000, v225\n"
      "v_mul_f32 v225, 0x3fb8aa3b, v225\n"
      "v_exp_f32 v225, v225\n"
      "s_nop 0\n"
      "v_cvt_pk_bf16_f32 v225, v225, v225\n"
      "global_load_dwordx4 a[188:191], v147, s[4:5] offset:-2048\n"
      "v_lshlrev_b32_e64 v225, 16, v225\n"
      "s_pack_ll_b32_b16 s28, s84, s86\n"
      "v_dot2_f32_bf16 v176, s28, v160, v176\n"
      "s_pack_hh_b32_b16 s29, s84, s86\n"
      "v_dot2_f32_bf16 v177, s29, v160, v177\n"
      "s_pack_ll_b32_b16 s30, s85, s87\n"
      "v_dot2_f32_bf16 v178, s30, v160, v178\n"
      "global_load_dwordx4 a[192:195], v216, s[4:5] offset:-1024\n"
      "s_pack_hh_b32_b16 s31, s85, s87\n"
      "v_dot2_f32_bf16 v179, s31, v160, v179\n"
      "s_pack_ll_b32_b16 s28, s88, s90\n"
      "v_dot2_f32_bf16 v176, s28, v164, v176\n"
      "s_pack_hh_b32_b16 s29, s88, s90\n"
      "v_dot2_f32_bf16 v177, s29, v164, v177\n"
      "s_pack_ll_b32_b16 s30, s89, s91\n"
      "v_dot2_f32_bf16 v178, s30, v164, v178\n"
      "global_load_dwordx4 a[196:199], v219, s[4:5]\n"
      "s_pack_hh_b32_b16 s31, s89, s91\n"
      "v_dot2_f32_bf16 v179, s31, v164, v179\n"
      "s_pack_ll_b32_b16 s28, s92, s94\n"
      "v_dot2_f32_bf16 v176, s28, v247, v176\n"
      "s_pack_hh_b32_b16 s29, s92, s94\n"
      "v_dot2_f32_bf16 v177, s29, v247, v177\n"
      "s_pack_ll_b32_b16 s30, s93, s95\n"
      "v_dot2_f32_bf16 v178, s30, v247, v178\n"
      "global_load_dwordx4 a[200:203], v226, s[4:5] offset:1024\n"
      "s_pack_hh_b32_b16 s31, s93, s95\n"
      "v_dot2_f32_bf16 v179, s31, v247, v179\n"
      "s_pack_ll_b32_b16 s28, s96, s98\n"
      "v_dot2_f32_bf16 v176, s28, v208, v176\n"
      "s_pack_hh_b32_b16 s29, s96, s98\n"
      "v_dot2_f32_bf16 v177, s29, v208, v177\n"
      "s_pack_ll_b32_b16 s30, s97, s99\n"
      "global_load_dwordx4 a[204:207], v224, s[4:5] offset:2048\n"
      "v_dot2_f32_bf16 v178, s30, v208, v178\n"
      "s_pack_hh_b32_b16 s31, s97, s99\n"
      "v_dot2_f32_bf16 v179, s31, v208, v179\n"
      "s_load_dwordx16 s[80:95], s[52:53], 41280\n"
      "s_load_dwordx4 s[96:99], s[52:53], 41344\n"
      "s_load_dwordx2 s[100:101], s[52:53], 41360\n"
      "s_load_dword s26, s[52:53], 41376\n"
      "s_load_dword s3, s[52:53], 41380\n"
      "global_load_dwordx4 a[208:211], v216, s[4:5] offset:3072\n"
      "ds_write_b128 v144, a[72:75]\n"
      "ds_write_b128 v144, a[76:79] offset:1024\n"
      "ds_write_b128 v144, a[80:83] offset:2048\n"
      "ds_write_b128 v144, a[84:87] offset:3072\n"
      "ds_write_b128 v144, a[88:91] offset:4096\n"
      "ds_write_b128 v144, a[92:95] offset:5120\n"
      "ds_write_b128 v144, a[96:99] offset:6144\n"
      "global_load_dwordx4 a[212:215], v146, s[4:5]\n"
      "ds_write_b128 v144, a[100:103] offset:7168\n"
      "ds_write_b128 v144, a[104:107] offset:8192\n"
      "ds_write_b128 v144, a[108:111] offset:36864\n"
      "ds_write_b128 v144, a[112:115] offset:37888\n"
      "ds_write_b128 v144, a[116:119] offset:38912\n"
      "ds_write_b128 v144, a[120:123] offset:39936\n"
      "ds_write_b128 v144, a[124:127] offset:40960\n"
      "ds_write_b128 v144, a[128:131] offset:41984\n"
      "global_load_dwordx4 v[208:211], v165, s[60:61] offset:3072\n"
      "ds_write_b128 v144, a[132:135] offset:43008\n"
      "ds_write_b128 v144, a[136:139] offset:44032\n"
      "ds_write_b128 v144, a[140:143] offset:45056\n"
      "s_waitcnt vmcnt(15)\n"
      "s_waitcnt lgkmcnt(0)\n"
      "s_barrier\n"
      "L_sched_%=:\n"
      "s_waitcnt vmcnt(14)\n"
      "ds_read_b128 v[140:143], v115 offset:20480\n"
      "ds_read_b128 v[248:251], v114 offset:20480\n"
      "ds_read_b128 v[244:247], v114 offset:18432\n"
      "ds_read_b128 v[228:231], v115 offset:18432\n"
      "v_cmp_nge_f32 vcc, 0x42480000, v225\n"
      "v_cvt_pk_bf16_f32 v177, v176, v177\n"
      "v_lshlrev_b32 v176, 16, v177\n"
      "s_waitcnt vmcnt(0)\n"
      "v_add_u32 v147, 3072, v211\n"
      "v_and_b32 v177, 0xffff0000, v177\n"
      "v_mul_f32 v177, 0xbfb8aa3b, v177\n"
      "v_mul_f32 v176, 0xbfb8aa3b, v176\n"
      "v_cvt_pk_bf16_f32 v179, v178, v179\n"
      "v_lshlrev_b32_e64 v178, 16, v179\n"
      "ds_read_b64_tr_b16 a[84:85], v232 offset:384\n"
      "ds_read_b64_tr_b16 a[74:75], v232 offset:4608\n"
      "v_and_b32 v179, 0xffff0000, v179\n"
      "global_load_dwordx4 a[216:219], v147, s[6:7] offset:-3072\n"
      "ds_read_b64_tr_b16 a[80:81], v167\n"
      "ds_read_b64_tr_b16 a[72:73], v232\n"
      "ds_read_b64_tr_b16 a[88:89], v235 offset:384\n"
      "v_exp_f32_e64 v176, v176\n"
      "v_mul_f32 v225, 0x3fb8aa3b, v225\n"
      "v_mul_f32 v178, 0xbfb8aa3b, v178\n"
      "v_exp_f32_e64 v177, v177\n"
      "s_waitcnt lgkmcnt(5)\n"
      "v_mfma_f32_16x16x32_bf16 v[204:207], a[0:3], v[244:247], 0\n"
      "s_mov_b64 s[50:51], vcc\n"
      "ds_read_b64_tr_b16 a[92:93], v167 offset:384\n"
      "v_mfma_f32_16x16x32_bf16 v[204:207], a[36:39], v[228:231], v[204:207]\n"
      "ds_read_b64_tr_b16 a[112:113], v235 offset:36864\n"
      "ds_read_b64_tr_b16 a[76:77], v235\n"
      "v_mfma_f32_16x16x32_bf16 v[160:163], a[4:7], v[244:247], 0\n"
      "ds_read_b64_tr_b16 a[98:99], v232 offset:5376\n"
      "v_add_f32_e64 v177, 1.0, v177\n"
      "v_mfma_f32_16x16x32_bf16 v[160:163], a[40:43], v[228:231], v[160:163]\n"
      "ds_read_b64_tr_b16 a[82:83], v167 offset:4608\n"
      "v_mul_f32 v179, 0xbfb8aa3b, v179\n"
      "v_mfma_f32_16x16x32_bf16 v[180:183], a[8:11], v[244:247], 0\n"
      "ds_read_b64_tr_b16 a[86:87], v232 offset:4992\n"
      "v_add_f32_e64 v176, 1.0, v176\n"
      "v_mfma_f32_16x16x32_bf16 v[180:183], a[44:47], v[228:231], v[180:183]\n"
      "ds_read_b64_tr_b16 a[96:97], v232 offset:768\n"
      "ds_read_b64_tr_b16 a[94:95], v167 offset:4992\n"
      "v_mfma_f32_16x16x32_bf16 v[184:187], a[12:15], v[244:247], 0\n"
      "global_load_dwordx4 a[220:223], v147, s[6:7] offset:-2944\n"
      "v_exp_f32_e64 v178, v178\n"
      "v_mfma_f32_16x16x32_bf16 v[184:187], a[48:51], v[228:231], v[184:187]\n"
      "ds_read_b64_tr_b16 a[104:105], v167 offset:768\n"
      "v_exp_f32 v179, v179\n"
      "v_mfma_f32_16x16x32_bf16 v[216:219], a[16:19], v[244:247], 0\n"
      "ds_read_b64_tr_b16 a[106:107], v167 offset:5376\n"
      "v_add_f32 v178, 1.0, v178\n"
      "v_mfma_f32_16x16x32_bf16 v[216:219], a[52:55], v[228:231], v[216:219]\n"
      "ds_read_b64_tr_b16 a[90:91], v235 offset:4992\n"
      "ds_read_b64_tr_b16 a[108:109], v232 offset:36864\n"
      "v_mfma_f32_16x16x32_bf16 v[192:195], a[20:23], v[244:247], 0\n"
      "ds_read_b64_tr_b16 a[78:79], v235 offset:4608\n"
      "v_rcp_f32 v176, v176\n"
      "v_mfma_f32_16x16x32_bf16 v[192:195], a[56:59], v[228:231], v[192:195]\n"
      "ds_read_b64_tr_b16 a[100:101], v235 offset:768\n"
      "v_rcp_f32 v178, v178\n"
      "v_mfma_f32_16x16x32_bf16 v[152:155], a[24:27], v[244:247], 0\n"
      "ds_read_b64_tr_b16 a[102:103], v235 offset:5376\n"
      "v_add_f32_e64 v179, 1.0, v179\n"
      "v_mfma_f32_16x16x32_bf16 v[152:155], a[60:63], v[228:231], v[152:155]\n"
      "ds_read_b64_tr_b16 a[110:111], v232 offset:41472\n"
      "v_rcp_f32_e64 v177, v177\n"
      "v_mfma_f32_16x16x32_bf16 v[168:171], a[28:31], v[244:247], 0\n"
      "ds_read_b64_tr_b16 a[118:119], v167 offset:41472\n"
      "v_cvt_pk_bf16_f32 v177, v176, v177\n"
      "v_mfma_f32_16x16x32_bf16 v[168:171], a[64:67], v[228:231], v[168:171]\n"
      "global_load_dwordx4 a[224:227], v147, s[6:7] offset:-2816\n"
      "v_mfma_f32_16x16x32_bf16 v[240:243], a[32:35], v[244:247], 0\n"
      "ds_read_b64_tr_b16 a[122:123], v232 offset:41856\n"
      "v_lshlrev_b32_e64 v176, 16, v177\n"
      "v_mfma_f32_16x16x32_bf16 v[240:243], a[68:71], v[228:231], v[240:243]\n"
      "ds_read_b64_tr_b16 a[120:121], v232 offset:37248\n"
      "v_and_b32 v177, 0xffff0000, v177\n"
      "v_mfma_f32_16x16x32_bf16 v[228:231], a[0:3], v[248:251], 0\n"
      "ds_read_b64_tr_b16 a[124:125], v235 offset:37248\n"
      "v_add_f32_e64 v176, -0.5, v176\n"
      "v_mfma_f32_16x16x32_bf16 v[228:231], a[36:39], v[140:143], v[228:231]\n"
      "ds_read_b64_tr_b16 a[114:115], v235 offset:41472\n"
      "ds_read_b64_tr_b16 a[116:117], v167 offset:36864\n"
      "v_mfma_f32_16x16x32_bf16 v[172:175], a[4:7], v[248:251], 0\n"
      "ds_read_b64_tr_b16 a[126:127], v235 offset:41856\n"
      "v_add_f32_e64 v177, -0.5, v177\n"
      "v_mfma_f32_16x16x32_bf16 v[172:175], a[40:43], v[140:143], v[172:175]\n"
      "ds_read_b64_tr_b16 a[130:131], v167 offset:41856\n"
      "v_mul_f32 v176, 0x41b8aa3b, v176\n"
      "v_mfma_f32_16x16x32_bf16 v[200:203], a[8:11], v[248:251], 0\n"
      "ds_read_b64_tr_b16 a[132:133], v232 offset:37632\n"
      "v_mul_f32 v177, 0x41b8aa3b, v177\n"
      "v_mfma_f32_16x16x32_bf16 v[200:203], a[44:47], v[140:143], v[200:203]\n"
      "global_load_dwordx4 a[228:231], v147, s[6:7]\n"
      "v_mfma_f32_16x16x32_bf16 v[196:199], a[12:15], v[248:251], 0\n"
      "ds_write_b32 v227, v177 offset:768\n"
      "v_rcp_f32_e64 v179, v179\n"
      "v_mfma_f32_16x16x32_bf16 v[196:199], a[48:51], v[140:143], v[196:199]\n"
      "ds_read_b64_tr_b16 a[138:139], v235 offset:42240\n"
      "ds_read_b64_tr_b16 a[134:135], v232 offset:42240\n"
      "v_mfma_f32_16x16x32_bf16 v[136:139], a[16:19], v[248:251], 0\n"
      "ds_read_b64_tr_b16 a[136:137], v235 offset:37632\n"
      "v_cvt_pk_bf16_f32 v179, v178, v179\n"
      "v_mfma_f32_16x16x32_bf16 v[136:139], a[52:55], v[140:143], v[136:139]\n"
      "ds_read_b64_tr_b16 a[142:143], v167 offset:42240\n"
      "v_lshlrev_b32_e64 v178, 16, v179\n"
      "v_add_f32_e64 v178, -0.5, v178\n"
      "v_mfma_f32_16x16x32_bf16 v[236:239], a[20:23], v[248:251], 0\n"
      "ds_read_b64_tr_b16 a[140:141], v167 offset:37632\n"
      "v_and_b32 v179, 0xffff0000, v179\n"
      "v_mfma_f32_16x16x32_bf16 v[236:239], a[56:59], v[140:143], v[236:239]\n"
      "ds_write_b32 v227, v176\n"
      "global_load_dwordx4 a[232:235], v147, s[6:7] offset:128\n"
      "v_mfma_f32_16x16x32_bf16 v[212:215], a[24:27], v[248:251], 0\n"
      "global_load_dwordx4 a[236:239], v147, s[6:7] offset:256\n"
      "v_add_u32 v147, 9216, v211\n"
      "v_mfma_f32_16x16x32_bf16 v[212:215], a[60:63], v[140:143], v[212:215]\n"
      "global_load_dwordx4 a[240:243], v147, s[6:7] offset:-3072\n"
      "v_mfma_f32_16x16x32_bf16 v[156:159], a[28:31], v[248:251], 0\n"
      "ds_read_b64_tr_b16 a[128:129], v167 offset:37248\n"
      "v_add_f32_e64 v179, -0.5, v179\n"
      "v_mfma_f32_16x16x32_bf16 v[156:159], a[64:67], v[140:143], v[156:159]\n"
      "global_load_dwordx4 a[244:247], v147, s[6:7] offset:-2944\n"
      "v_mul_f32 v179, 0x41b8aa3b, v179\n"
      "v_mfma_f32_16x16x32_bf16 v[148:151], a[32:35], v[248:251], 0\n"
      "ds_write_b32 v227, v179 offset:2304\n"
      "global_load_dwordx4 a[248:251], v147, s[6:7] offset:-2816\n"
      "v_mul_f32 v178, 0x41b8aa3b, v178\n"
      "v_mfma_f32_16x16x32_bf16 v[148:151], a[68:71], v[140:143], v[148:151]\n"
      "ds_write_b32 v227, v178 offset:1536\n"
      "s_waitcnt lgkmcnt(0)\n"
      "s_barrier\n"
      "ds_read_b128 v[176:179], v114 offset:24576\n"
      "v_readfirstlane_b32 s36, v225\n"
      "s_mov_b32 s11, 100466688\n"
      "ds_read_b128 v[224:227], v115 offset:24576\n"
      "s_sub_u32 s16, s16, 99876864\n"
      "v_mov_b32 v49, 0\n"
      "v_add_u32 v146, 3136, v208\n"
      "s_subb_u32 s17, s17, 0\n"
      "v_mov_b32 v37, 0\n"
      "v_mov_b32 v233, 134528\n"
      "v_mov_b32 v65, 0\n"
      "v_mov_b32 v36, 0\n"
      "s_sub_u32 s22, s22, 99876864\n"
      "s_subb_u32 s23, s23, 0\n"
      "s_add_u32 s12, s20, 16777216\n"
      "v_mov_b32_e64 v42, 0\n"
      "s_addc_u32 s13, s21, 0\n"
      "ds_read2_b32 v[96:97], v146 offset0:18 offset1:19\n"
      "ds_read2_b32 v[92:93], v146 offset0:2 offset1:3\n"
      "ds_read_b128 v[244:247], v115 offset:26624\n"
      "ds_read2_b32 v[78:79], v208 offset0:0 offset1:1\n"
      "global_load_dwordx4 v[118:121], v165, s[48:49] offset:-3072\n"
      "ds_read2_b32 v[80:81], v208 offset0:2 offset1:3\n"
      "ds_read_b128 v[140:143], v114 offset:26624\n"
      "ds_read2_b32 v[84:85], v208 offset0:18 offset1:19\n"
      "s_waitcnt lgkmcnt(7)\n"
      "s_mul_i32 s2, s24, 768\n"
      "v_mfma_f32_16x16x32_bf16 v[204:207], a[72:75], v[176:179], v[204:207]\n"
      "ds_read2_b32 v[94:95], v146 offset0:16 offset1:17\n"
      "v_mov_b32 v53, 0\n"
      "v_mfma_f32_16x16x32_bf16 v[204:207], a[108:111], v[224:227], v[204:207]\n"
      "ds_read2_b32 v[86:87], v208 offset0:32 offset1:33\n"
      "v_add_u32 v166, 6272, v208\n"
      "s_add_u32 s2, s2, 150640\n"
      "v_mfma_f32_16x16x32_bf16 v[160:163], a[76:79], v[176:179], v[160:163]\n"
      "ds_read2_b32 v[90:91], v146 offset0:0 offset1:1\n"
      "v_mov_b32_e64 v61, 0\n"
      "v_mfma_f32_16x16x32_bf16 v[160:163], a[112:115], v[224:227], v[160:163]\n"
      "ds_read2_b32 v[100:101], v146 offset0:34 offset1:35\n"
      "v_mov_b32_e64 v57, 0\n"
      "v_mfma_f32_16x16x32_bf16 v[180:183], a[80:83], v[176:179], v[180:183]\n"
      "global_load_dwordx4 v[122:125], v165, s[48:49] offset:-2048\n"
      "v_mfma_f32_16x16x32_bf16 v[180:183], a[116:119], v[224:227], v[180:183]\n"
      "ds_read2_b32 v[98:99], v146 offset0:32 offset1:33\n"
      "ds_read2_b32 v[102:103], v166 offset0:0 offset1:1\n"
      "v_mfma_f32_16x16x32_bf16 v[184:187], a[84:87], v[176:179], v[184:187]\n"
      "ds_read2_b32 v[104:105], v166 offset0:2 offset1:3\n"
      "v_mov_b32_e64 v43, 0\n"
      "v_mfma_f32_16x16x32_bf16 v[184:187], a[120:123], v[224:227], v[184:187]\n"
      "ds_read_b32 v73, v210 offset:64\n"
      "ds_read_b32 v72, v209 offset:3328\n"
      "v_mfma_f32_16x16x32_bf16 v[216:219], a[88:91], v[176:179], v[216:219]\n"
      "ds_read2_b32 v[108:109], v166 offset0:18 offset1:19\n"
      "v_mov_b32 v48, 0\n"
      "v_mfma_f32_16x16x32_bf16 v[216:219], a[124:127], v[224:227], v[216:219]\n"
      "ds_read2_b32 v[106:107], v166 offset0:16 offset1:17\n"
      "ds_read2_b32 v[88:89], v208 offset0:34 offset1:35\n"
      "v_mfma_f32_16x16x32_bf16 v[192:195], a[92:95], v[176:179], v[192:195]\n"
      "ds_read_b32 v70, v209 offset:192\n"
      "ds_read2_b32 v[112:113], v166 offset0:34 offset1:35\n"
      "v_mfma_f32_16x16x32_bf16 v[192:195], a[128:131], v[224:227], v[192:195]\n"
      "s_waitcnt lgkmcnt(10)\n"
      "ds_read2_b32 v[110:111], v166 offset0:32 offset1:33\n"
      "v_mfma_f32_16x16x32_bf16 v[152:155], a[96:99], v[176:179], v[152:155]\n"
      "global_load_dwordx4 v[250:253], v145, s[48:49] offset:-3072\n"
      "v_mfma_f32_16x16x32_bf16 v[152:155], a[132:135], v[224:227], v[152:155]\n"
      "ds_read_b32 v74, v209 offset:6464\n"
      "ds_read2_b32 v[82:83], v208 offset0:16 offset1:17\n"
      "v_mfma_f32_16x16x32_bf16 v[168:171], a[100:103], v[176:179], v[168:171]\n"
      "ds_write_b128 v144, a[176:179] offset:8192\n"
      "v_mfma_f32_16x16x32_bf16 v[168:171], a[136:139], v[224:227], v[168:171]\n"
      "ds_write_b128 v144, a[160:163] offset:4096\n"
      "v_mfma_f32_16x16x32_bf16 v[240:243], a[104:107], v[176:179], v[240:243]\n"
      "ds_read_b32 v71, v210\n"
      "v_mfma_f32_16x16x32_bf16 v[240:243], a[140:143], v[224:227], v[240:243]\n"
      "ds_write_b128 v144, a[192:195] offset:39936\n"
      "v_mfma_f32_16x16x32_bf16 v[228:231], a[72:75], v[140:143], v[228:231]\n"
      "ds_read_b32 v75, v210 offset:128\n"
      "v_mfma_f32_16x16x32_bf16 v[228:231], a[108:111], v[244:247], v[228:231]\n"
      "ds_write_b128 v144, a[144:147]\n"
      "v_mfma_f32_16x16x32_bf16 v[172:175], a[76:79], v[140:143], v[172:175]\n"
      "ds_read_b32 v77, v233\n"
      "v_mfma_f32_16x16x32_bf16 v[172:175], a[112:115], v[244:247], v[172:175]\n"
      "ds_write_b128 v144, a[148:151] offset:1024\n"
      "v_mfma_f32_16x16x32_bf16 v[200:203], a[80:83], v[140:143], v[200:203]\n"
      "global_load_dwordx4 v[188:191], v145, s[48:49] offset:-4096\n"
      "v_mfma_f32_16x16x32_bf16 v[200:203], a[116:119], v[244:247], v[200:203]\n"
      "ds_write_b128 v144, a[152:155] offset:2048\n"
      "v_mfma_f32_16x16x32_bf16 v[196:199], a[84:87], v[140:143], v[196:199]\n"
      "ds_write_b128 v144, a[188:191] offset:38912\n"
      "v_mfma_f32_16x16x32_bf16 v[136:139], a[88:91], v[140:143], v[136:139]\n"
      "ds_write_b128 v144, a[156:159] offset:3072\n"
      "v_mfma_f32_16x16x32_bf16 v[236:239], a[92:95], v[140:143], v[236:239]\n"
      "ds_write_b128 v144, a[204:207] offset:43008\n"
      "v_mfma_f32_16x16x32_bf16 v[236:239], a[128:131], v[244:247], v[236:239]\n"
      "ds_write_b128 v144, a[196:199] offset:40960\n"
      "v_mfma_f32_16x16x32_bf16 v[148:151], a[104:107], v[140:143], v[148:151]\n"
      "ds_write_b128 v144, a[164:167] offset:5120\n"
      "v_mfma_f32_16x16x32_bf16 v[212:215], a[96:99], v[140:143], v[212:215]\n"
      "ds_write_b128 v144, a[208:211] offset:44032\n"
      "v_mfma_f32_16x16x32_bf16 v[212:215], a[132:135], v[244:247], v[212:215]\n"
      "ds_write_b128 v144, a[172:175] offset:7168\n"
      "v_mfma_f32_16x16x32_bf16 v[156:159], a[100:103], v[140:143], v[156:159]\n"
      "ds_write_b128 v144, a[168:171] offset:6144\n"
      "v_mfma_f32_16x16x32_bf16 v[136:139], a[124:127], v[244:247], v[136:139]\n"
      "ds_write_b128 v144, a[200:203] offset:41984\n"
      "v_mfma_f32_16x16x32_bf16 v[196:199], a[120:123], v[244:247], v[196:199]\n"
      "ds_write_b128 v144, a[184:187] offset:37888\n"
      "v_mfma_f32_16x16x32_bf16 v[156:159], a[136:139], v[244:247], v[156:159]\n"
      "ds_write_b128 v144, a[212:215] offset:45056\n"
      "v_mfma_f32_16x16x32_bf16 v[148:151], a[140:143], v[244:247], v[148:151]\n"
      "ds_write_b128 v144, a[180:183] offset:36864\n"
      "s_waitcnt lgkmcnt(0)\n"
      "s_barrier\n"
      "s_mov_b32 s60, 100270464\n"
      "s_waitcnt vmcnt(1)\n"
      "v_mov_b32 v68, v250\n"
      "v_lshl_add_u32 v78, v78, 2, s2\n"
      "v_lshl_add_u32 v87, v87, 2, s2\n"
      "s_mov_b32 s61, 196224\n"
      "ds_read_b64_tr_b16 a[202:203], v167 offset:41856\n"
      "ds_read_b64_tr_b16 a[146:147], v232 offset:4608\n"
      "s_lshr_b32 s8, s80, 8\n"
      "v_lshl_add_u32 v80, v80, 2, s2\n"
      "v_lshl_add_u32 v82, v82, 2, s2\n"
      "v_lshl_add_u32 v84, v84, 2, s2\n"
      "s_and_b32 s9, s80, 0xff\n"
      "ds_read_b64_tr_b16 a[148:149], v235\n"
      "ds_read_b64_tr_b16 a[178:179], v167 offset:5376\n"
      "v_mov_b32 v67, v250\n"
      "s_mul_i32 s10, s8, 73\n"
      "ds_read_b32 v82, v82\n"
      "v_mov_b32 v224, v251\n"
      "s_add_u32 s10, s10, s9\n"
      "ds_read_b64_tr_b16 a[144:145], v232\n"
      "s_mul_i32 s28, s10, 4802\n"
      "s_add_u32 s56, s18, s28\n"
      "ds_read_b64_tr_b16 a[182:183], v232 offset:41472\n"
      "v_mov_b32 v66, v250\n"
      "v_mov_b32_e64 v225, v251\n"
      "v_mov_b32_e64 v69, v250\n"
      "s_addc_u32 s57, s19, 0\n"
      "v_lshl_add_u32 v79, v79, 2, s2\n"
      "s_and_b32 s56, s56, -16\n"
      "ds_read_b64_tr_b16 a[186:187], v235 offset:41472\n"
      "ds_read_b64_tr_b16 a[156:157], v232 offset:384\n"
      "ds_read_b128 v[140:143], v114 offset:30720\n"
      "s_sub_u32 s58, s56, 110592\n"
      "ds_read_b128 v[176:179], v114 offset:32768\n"
      "ds_read_b64_tr_b16 a[180:181], v232 offset:36864\n"
      "s_subb_u32 s59, s57, 0\n"
      "v_mov_b32 v226, v251\n"
      "v_lshl_add_u32 v88, v88, 2, s2\n"
      "v_lshl_add_u32 v86, v86, 2, s2\n"
      "s_sub_u32 s56, s56, 73728\n"
      "ds_read_b64_tr_b16 a[150:151], v235 offset:4608\n"
      "v_mov_b32 v250, 0\n"
      "s_subb_u32 s57, s57, 0\n"
      "ds_read_b64_tr_b16 a[170:171], v232 offset:5376\n"
      "v_lshl_add_u32 v83, v83, 2, s2\n"
      "s_mul_i32 s28, s8, 1373568\n"
      "ds_read_b64_tr_b16 a[188:189], v167 offset:36864\n"
      "ds_read_b64_tr_b16 a[174:175], v235 offset:5376\n"
      "v_lshl_add_u32 v85, v85, 2, s2\n"
      "v_lshl_add_u32 v81, v81, 2, s2\n"
      "s_mul_i32 s29, s9, 2688\n"
      "ds_read_b64_tr_b16 a[190:191], v167 offset:41472\n"
      "s_add_u32 s28, s28, s29\n"
      "ds_read_b32 v79, v79\n"
      "v_mov_b32 v227, v251\n"
      "s_cmp_eq_u32 s8, 72\n"
      "v_add_u32 v234, 99876864, v234\n"
      "s_cselect_b32 s8, 0, 100270464\n"
      "ds_read_b32 v84, v84\n"
      "ds_read_b64_tr_b16 a[166:167], v167 offset:4992\n"
      "ds_read_b64_tr_b16 a[160:161], v235 offset:384\n"
      "ds_read_b64_tr_b16 a[214:215], v167 offset:42240\n"
      "s_cmp_eq_u32 s9, 72\n"
      "ds_read_b64_tr_b16 a[176:177], v167 offset:768\n"
      "ds_read_b64_tr_b16 a[154:155], v167 offset:4608\n"
      "s_cselect_b32 s9, 0, 196224\n"
      "ds_read_b64_tr_b16 a[204:205], v232 offset:37632\n"
      "ds_read_b64_tr_b16 a[192:193], v232 offset:37248\n"
      "s_add_u32 s10, s8, s9\n"
      "ds_read_b64_tr_b16 a[184:185], v235 offset:36864\n"
      "ds_read_b64_tr_b16 a[164:165], v167 offset:384\n"
      "ds_read_b128 v[220:223], v115 offset:30720\n"
      "s_add_u32 s28, s28, s10\n"
      "ds_read_b64_tr_b16 a[168:169], v232 offset:768\n"
      "ds_read_b64_tr_b16 a[194:195], v232 offset:41856\n"
      "ds_read_b64_tr_b16 a[208:209], v235 offset:37632\n"
      "ds_read_b64_tr_b16 a[200:201], v167 offset:37248\n"
      "ds_read_b64_tr_b16 a[152:153], v167\n"
      "ds_read_b64_tr_b16 a[210:211], v235 offset:42240\n"
      "ds_read_b64_tr_b16 a[162:163], v235 offset:4992\n"
      "s_add_u32 s52, s16, s28\n"
      "ds_read_b64_tr_b16 a[198:199], v235 offset:41856\n"
      "v_lshl_add_u32 v90, v90, 2, s2\n"
      "v_lshl_add_u32 v89, v89, 2, s2\n"
      "s_addc_u32 s53, s17, 0\n"
      "ds_read_b64_tr_b16 a[172:173], v235 offset:768\n"
      "ds_read_b64_tr_b16 a[158:159], v232 offset:4992\n"
      "ds_read_b64_tr_b16 a[196:197], v235 offset:37248\n"
      "s_cmp_lg_u32 s10, s11\n"
      "s_cbranch_scc1 L_edge_1_%=\n"
      "L_eb_2_%=:\n"
      "s_add_u32 m0, s76, 0\n"
      "v_lshl_add_u32 v95, v95, 2, s2\n"
      "s_waitcnt lgkmcnt(11)\n"
      "global_load_lds_dwordx4 v116, s[52:53]\n"
      "s_add_u32 m0, s76, 6016\n"
      "v_mfma_f32_16x16x32_bf16 v[204:207], a[144:147], v[140:143], v[204:207]\n"
      "ds_read_b32 v89, v89\n"
      "v_mov_b32 v251, 0\n"
      "v_mfma_f32_16x16x32_bf16 v[204:207], a[180:183], v[220:223], v[204:207]\n"
      "global_load_lds_dwordx4 v116, s[52:53] offset:128\n"
      "ds_read_b32 v78, v78\n"
      "ds_read_b32 v85, v85\n"
      "ds_read_b64_tr_b16 a[212:213], v167 offset:37632\n"
      "v_mfma_f32_16x16x32_bf16 v[160:163], a[148:151], v[140:143], v[160:163]\n"
      "s_waitcnt lgkmcnt(6)\n"
      "v_lshl_add_u32 v92, v92, 2, s2\n"
      "v_lshl_add_u32 v93, v93, 2, s2\n"
      "s_waitcnt lgkmcnt(4)\n"
      "v_mfma_f32_16x16x32_bf16 v[160:163], a[184:187], v[220:223], v[160:163]\n"
      "s_waitcnt vmcnt(2)\n"
      "ds_write_b32 v189, v234\n"
      "ds_read_b32 v80, v80\n"
      "ds_read_b32 v86, v86\n"
      "v_mfma_f32_16x16x32_bf16 v[180:183], a[152:155], v[140:143], v[180:183]\n"
      "s_add_u32 m0, s76, 12032\n"
      "v_lshl_add_u32 v96, v96, 2, s2\n"
      "v_lshl_add_u32 v100, v100, 2, s2\n"
      "v_mfma_f32_16x16x32_bf16 v[180:183], a[188:191], v[220:223], v[180:183]\n"
      "global_load_lds_dwordx4 v116, s[52:53] offset:256\n"
      "v_mov_b32 v255, 0\n"
      "v_mfma_f32_16x16x32_bf16 v[216:219], a[160:163], v[140:143], v[216:219]\n"
      "s_add_u32 m0, s77, 0\n"
      "ds_read_b128 v[244:247], v115 offset:32768\n"
      "global_load_lds_dwordx4 v117, s[52:53]\n"
      "s_add_u32 m0, s78, 0\n"
      "v_lshl_add_u32 v97, v97, 2, s2\n"
      "s_mov_b32 exec_hi, 0\n"
      "global_load_lds_dwordx4 v118, s[52:53]\n"
      "s_mov_b32 exec_hi, -1\n"
      "ds_read_b64_tr_b16 a[206:207], v232 offset:42240\n"
      "ds_read_b32 v83, v83\n"
      "ds_read_b32 v81, v81\n"
      "s_add_u32 m0, s76, 110592\n"
      "ds_read_b32 v92, v92\n"
      "v_mfma_f32_16x16x32_bf16 v[184:187], a[156:159], v[140:143], v[184:187]\n"
      "v_lshl_add_u32 v91, v91, 2, s2\n"
      "v_lshl_add_u32 v101, v101, 2, s2\n"
      "v_mfma_f32_16x16x32_bf16 v[184:187], a[192:195], v[220:223], v[184:187]\n"
      "ds_read_b32 v91, v91\n"
      "ds_read_b32 v87, v87\n"
      "global_load_lds_dwordx4 v119, s[56:57]\n"
      "s_add_u32 m0, s79, 0\n"
      "v_mfma_f32_16x16x32_bf16 v[216:219], a[196:199], v[220:223], v[216:219]\n"
      "global_load_lds_dword v122, s[58:59]\n"
      "v_lshl_add_u32 v70, v70, 2, s2\n"
      "v_mfma_f32_16x16x32_bf16 v[192:195], a[164:167], v[140:143], v[192:195]\n"
      "ds_read_b32 v88, v88\n"
      "v_mov_b32 v254, 0\n"
      "v_mfma_f32_16x16x32_bf16 v[192:195], a[200:203], v[220:223], v[192:195]\n"
      "ds_read_b32 v90, v90\n"
      "s_waitcnt lgkmcnt(7)\n"
      "v_lshl_add_u32 v106, v106, 2, s2\n"
      "ds_read_b32 v95, v95\n"
      "v_lshl_add_u32 v110, v110, 2, s2\n"
      "v_lshl_add_u32 v94, v94, 2, s2\n"
      "v_mfma_f32_16x16x32_bf16 v[152:155], a[168:171], v[140:143], v[152:155]\n"
      "ds_read_b32 v93, v93\n"
      "v_lshl_add_u32 v103, v103, 2, s2\n"
      "v_mfma_f32_16x16x32_bf16 v[152:155], a[204:207], v[220:223], v[152:155]\n"
      "global_load_dwordx4 v[126:129], v165, s[48:49] offset:-1024\n"
      "v_mfma_f32_16x16x32_bf16 v[168:171], a[172:175], v[140:143], v[168:171]\n"
      "global_load_dwordx4 v[130:133], v165, s[48:49]\n"
      "global_load_dwordx4 v[232:235], v165, s[48:49] offset:1024\n"
      "v_mfma_f32_16x16x32_bf16 v[168:171], a[208:211], v[220:223], v[168:171]\n"
      "global_load_dwordx4 a[252:255], v147, s[6:7]\n"
      "global_load_dwordx4 v[0:3], v147, s[6:7] offset:128\n"
      "v_lshl_add_u32 v99, v99, 2, s2\n"
      "v_lshl_add_u32 v98, v98, 2, s2\n"
      "v_lshl_add_u32 v112, v112, 2, s2\n"
      "v_lshl_add_u32 v104, v104, 2, s2\n"
      "v_lshl_add_u32 v77, v77, 2, s2\n"
      "v_lshl_add_u32 v111, v111, 2, s2\n"
      "v_lshl_add_u32 v113, v113, 2, s2\n"
      "v_mfma_f32_16x16x32_bf16 v[240:243], a[176:179], v[140:143], v[240:243]\n"
      "ds_read_b32 v94, v94\n"
      "v_lshl_add_u32 v73, v73, 2, s2\n"
      "v_mfma_f32_16x16x32_bf16 v[240:243], a[212:215], v[220:223], v[240:243]\n"
      "ds_read_b32 v100, v100\n"
      "ds_read_b32 v96, v96\n"
      "v_lshl_add_u32 v107, v107, 2, s2\n"
      "v_lshl_add_u32 v75, v75, 2, s2\n"
      "v_mfma_f32_16x16x32_bf16 v[228:231], a[144:147], v[176:179], v[228:231]\n"
      "global_load_dwordx4 v[4:7], v147, s[6:7] offset:256\n"
      "v_lshl_add_u32 v108, v108, 2, s2\n"
      "v_lshl_add_u32 v72, v72, 2, s2\n"
      "v_lshl_add_u32 v105, v105, 2, s2\n"
      "v_lshl_add_u32 v102, v102, 2, s2\n"
      "v_mfma_f32_16x16x32_bf16 v[228:231], a[180:183], v[244:247], v[228:231]\n"
      "ds_read_b32 v97, v97\n"
      "v_fma_f32 v249, v204, v204, s38\n"
      "v_lshl_add_u32 v71, v71, 2, s2\n"
      "v_mfma_f32_16x16x32_bf16 v[172:175], a[148:151], v[176:179], v[172:175]\n"
      "ds_read_b32 v98, v98\n"
      "v_fma_f32 v248, v184, v184, s38\n"
      "v_mfma_f32_16x16x32_bf16 v[172:175], a[184:187], v[244:247], v[172:175]\n"
      "ds_read_b32 v99, v99\n"
      "v_fmac_f32 v248, v185, v185\n"
      "v_add_u32 v147, 15360, v211\n"
      "v_fmac_f32 v248, v186, v186\n"
      "v_fmac_f32 v248, v187, v187\n"
      "v_fmac_f32 v248, v216, v216\n"
      "v_fmac_f32 v249, v205, v205\n"
      "v_fmac_f32 v249, v206, v206\n"
      "v_fmac_f32 v249, v207, v207\n"
      "v_fmac_f32 v249, v160, v160\n"
      "v_mfma_f32_16x16x32_bf16 v[200:203], a[152:155], v[176:179], v[200:203]\n"
      "ds_read_b32 v104, v104\n"
      "global_load_dwordx4 v[8:11], v147, s[6:7] offset:-3072\n"
      "v_fmac_f32 v248, v217, v217\n"
      "v_fmac_f32 v249, v161, v161\n"
      "v_lshl_add_u32 v109, v109, 2, s2\n"
      "v_lshl_add_u32 v74, v74, 2, s2\n"
      "v_mfma_f32_16x16x32_bf16 v[200:203], a[188:191], v[244:247], v[200:203]\n"
      "ds_read_b32 v110, v110\n"
      "v_fmac_f32 v249, v162, v162\n"
      "v_fmac_f32 v249, v163, v163\n"
      "v_fmac_f32 v248, v218, v218\n"
      "v_fmac_f32 v248, v219, v219\n"
      "v_fmac_f32 v248, v192, v192\n"
      "v_fmac_f32 v249, v180, v180\n"
      "v_fmac_f32 v248, v193, v193\n"
      "v_fmac_f32 v248, v194, v194\n"
      "v_mfma_f32_16x16x32_bf16 v[196:199], a[156:159], v[176:179], v[196:199]\n"
      "ds_read_b32 v72, v72\n"
      "v_fmac_f32 v249, v181, v181\n"
      "v_fmac_f32 v249, v182, v182\n"
      "v_fmac_f32 v248, v195, v195\n"
      "v_fmac_f32 v249, v183, v183\n"
      "v_mfma_f32_16x16x32_bf16 v[196:199], a[192:195], v[244:247], v[196:199]\n"
      "ds_read_b32 v109, v109\n"
      "global_load_dwordx4 v[12:15], v147, s[6:7] offset:-2944\n"
      "ds_read_b32 v74, v74\n"
      "v_permlane16_swap_b32 v249, v248\n"
      "v_add_f32 v249, v249, v248\n"
      "v_mov_b32_e64 v248, v249\n"
      "v_mfma_f32_16x16x32_bf16 v[136:139], a[160:163], v[176:179], v[136:139]\n"
      "ds_read_b32 v103, v103\n"
      "v_permlane32_swap_b32 v249, v248\n"
      "v_add_f32 v249, v249, v248\n"
      "v_rsq_f32 v249, v249\n"
      "s_nop 0\n"
      "v_mov_b32_e64 v248, v249\n"
      "v_mfma_f32_16x16x32_bf16 v[136:139], a[196:199], v[244:247], v[136:139]\n"
      "ds_read_b32 v106, v106\n"
      "ds_read_b32 v108, v108\n"
      "ds_read_b32 v101, v101\n"
      "ds_read_b32 v113, v113\n"
      "ds_read_b32 v105, v105\n"
      "global_load_dwordx4 v[16:19], v147, s[6:7] offset:-2816\n"
      "ds_read_b32 v112, v112\n"
      "ds_read_b32 v107, v107\n"
      "ds_read_b32 v71, v71\n"
      "v_permlane16_swap_b32 v249, v248\n"
      "v_mul_f32 v249, s36, v249\n"
      "v_mul_f32 v216, v248, v216\n"
      "v_mul_f32 v160, v249, v160\n"
      "v_mul_f32 v163, v249, v163\n"
      "v_mul_f32 v206, v249, v206\n"
      "v_mul_f32 v194, v248, v194\n"
      "v_mul_f32 v184, v248, v184\n"
      "v_mul_f32 v205, v249, v205\n"
      "v_mul_f32 v218, v248, v218\n"
      "v_mul_f32 v185, v248, v185\n"
      "v_mfma_f32_16x16x32_bf16 v[236:239], a[164:167], v[176:179], v[236:239]\n"
      "ds_read_b32 v102, v102\n"
      "v_mul_f32 v204, v249, v204\n"
      "v_mfma_f32_16x16x32_bf16 v[236:239], a[200:203], v[244:247], v[236:239]\n"
      "ds_read_b32 v70, v70\n"
      "global_load_dwordx4 v[20:23], v147, s[6:7]\n"
      "v_mul_f32 v162, v249, v162\n"
      "s_waitcnt vmcnt(7)\n"
      "v_mov_b32 v135, v233\n"
      "v_cvt_pk_f16_f32 v184, v184, v185\n"
      "v_mfma_f32_16x16x32_bf16 v[212:215], a[168:171], v[176:179], v[212:215]\n"
      "ds_read_b32 v75, v75\n"
      "v_mul_f32 v207, v249, v207\n"
      "v_mfma_f32_16x16x32_bf16 v[212:215], a[204:207], v[244:247], v[212:215]\n"
      "ds_read_b32 v77, v77\n"
      "ds_read_b32 v111, v111\n"
      "v_mul_f32 v195, v248, v195\n"
      "v_mul_f32 v187, v248, v187\n"
      "v_mul_f32 v217, v248, v217\n"
      "v_mul_f32 v182, v249, v182\n"
      "v_add_u32 v189, 36864, v235\n"
      "v_cvt_pk_f16_f32 v204, v204, v205\n"
      "v_mul_f32 v161, v249, v161\n"
      "v_mul_f32 v181, v249, v181\n"
      "v_mul_f32_e64 v186, v248, v186\n"
      "v_cvt_pk_f16_f32 v205, v206, v207\n"
      "v_mul_f32 v180, v249, v180\n"
      "v_mul_f32 v193, v248, v193\n"
      "v_cvt_pk_f16_f32 v185, v186, v187\n"
      "v_cvt_pk_f16_f32 v207, v162, v163\n"
      "v_mul_f32_e64 v219, v248, v219\n"
      "v_mfma_f32_16x16x32_bf16 v[156:159], a[172:175], v[176:179], v[156:159]\n"
      "ds_read_b32 v73, v73\n"
      "global_load_dwordx4 v[24:27], v147, s[6:7] offset:128\n"
      "global_load_dwordx4 v[28:31], v147, s[6:7] offset:256\n"
      "v_cvt_pk_f16_f32 v206, v160, v161\n"
      "v_mul_f32_e64 v192, v248, v192\n"
      "v_cvt_pk_f16_f32 v32, v180, v181\n"
      "ds_read_b32 v248, v122\n"
      "ds_read_b128 v[160:163], v119 offset:36864\n"
      "v_cvt_pk_f16_f32 v34, v192, v193\n"
      "v_cvt_pk_f16_f32 v187, v218, v219\n"
      "s_waitcnt lgkmcnt(3)\n"
      "v_mul_f32 v183, v249, v183\n"
      "v_cvt_pk_f16_f32 v35, v194, v195\n"
      "v_cvt_pk_f16_f32 v186, v216, v217\n"
      "v_mov_b32_e64 v233, -1\n"
      "v_mfma_f32_16x16x32_bf16 v[148:151], a[176:179], v[176:179], v[148:151]\n"
      "ds_write_b32 v129, v233\n"
      "v_mfma_f32_16x16x32_bf16 v[156:159], a[208:211], v[244:247], v[156:159]\n"
      "s_mov_b32 exec_lo, 0xffff0000\n"
      "s_mov_b32 exec_hi, 0xffffffff\n"
      "v_mov_b32 v70, 0xf149f2ca\n"
      "v_mov_b32 v72, 0xf149f2ca\n"
      "v_mov_b32 v74, 0xf149f2ca\n"
      "s_mov_b64 exec, -1\n"
      "v_cvt_pk_f16_f32 v33, v182, v183\n"
      "v_add_u32 v249, 36864, v234\n"
      "s_mov_b32 exec_lo, 0x0\n"
      "s_mov_b32 exec_hi, 0xfffe0000\n"
      "v_mov_b32 v77, 0xf149f2ca\n"
      "s_mov_b64 exec, -1\n"
      "ds_write_b32 v129, v233 offset:3072\n"
      "s_mov_b32 s4, 0\n"
      "s_mov_b32 s5, 0\n"
      "ds_write_b32 v129, v233 offset:1024\n"
      "s_waitcnt lgkmcnt(3)\n"
      "v_or3_b32 v160, v160, v161, v162\n"
      "v_and_b32 v160, s40, v160\n"
      "v_bitop3_b32 v248, v248, v163, s40 bitop3:0xa8\n"
      "v_or3_b32 v160, v160, v248, s4\n"
      "v_mfma_f32_16x16x32_bf16 v[148:151], a[212:215], v[244:247], v[148:151]\n"
      "ds_write_b32 v129, v160 offset:2048\n"
      "v_mov_b32_e64 v134, v232\n"
      "s_waitcnt lgkmcnt(0)\n"
      "s_barrier\n"
      "v_cvt_pk_bf16_f32 v152, v152, v153\n"
      "v_cvt_pk_bf16_f32 v153, v154, v155\n"
      "v_fma_f32 v155, v228, v228, s38\n"
      "v_fma_f32 v154, v196, v196, s38\n"
      "v_fmac_f32 v155, v229, v229\n"
      "v_fmac_f32 v154, v197, v197\n"
      "v_fmac_f32 v154, v198, v198\n"
      "v_fmac_f32 v155, v230, v230\n"
      "v_fmac_f32 v154, v199, v199\n"
      "v_fmac_f32 v155, v231, v231\n"
      "v_fmac_f32 v154, v136, v136\n"
      "v_cvt_f16_f32 v232, s36\n"
      "v_fmac_f32 v154, v137, v137\n"
      "v_fmac_f32 v154, v138, v138\n"
      "v_fmac_f32 v155, v172, v172\n"
      "v_fmac_f32 v154, v139, v139\n"
      "v_readfirstlane_b32 s7, v232\n"
      "v_fmac_f32 v155, v173, v173\n"
      "v_fmac_f32 v154, v236, v236\n"
      "v_fmac_f32 v154, v237, v237\n"
      "v_fmac_f32 v155, v174, v174\n"
      "v_fmac_f32 v155, v175, v175\n"
      "v_fmac_f32 v155, v200, v200\n"
      "v_fmac_f32 v155, v201, v201\n"
      "v_fmac_f32 v154, v238, v238\n"
      "v_fmac_f32 v154, v239, v239\n"
      "v_fmac_f32 v155, v202, v202\n"
      "v_fmac_f32 v155, v203, v203\n"
      "s_mov_b32 s27, 0\n"
      "v_cvt_pk_bf16_f32 v240, v240, v241\n"
      "v_permlane16_swap_b32 v155, v154\n"
      "v_add_f32 v155, v155, v154\n"
      "v_mov_b32 v154, v155\n"
      "v_cvt_pk_bf16_f32 v241, v242, v243\n"
      "s_add_u32 s28, s27, 1\n"
      "v_permlane32_swap_b32 v155, v154\n"
      "v_cvt_pk_bf16_f32 v168, v168, v169\n"
      "v_add_f32 v155, v155, v154\n"
      "v_rsq_f32 v155, v155\n"
      "s_min_u32 m0, s28, s3\n"
      "v_mov_b32 v154, v155\n"
      "v_cvt_pk_bf16_f32 v169, v170, v171\n"
      "s_movrels_b32 s9, s80\n"
      "v_permlane16_swap_b32 v155, v154\n"
      "v_mul_f32 v136, v154, v136\n"
      "v_mul_f32 v196, v154, v196\n"
      "v_mul_f32 v155, s36, v155\n"
      "v_mul_f32 v197, v154, v197\n"
      "v_mul_f32 v137, v154, v137\n"
      "v_mul_f32 v229, v155, v229\n"
      "v_mul_f32 v228, v155, v228\n"
      "v_cvt_pk_f16_f32 v228, v228, v229\n"
      "v_mul_f32 v139, v154, v139\n"
      "v_mul_f32 v198, v154, v198\n"
      "v_mul_f32 v172, v155, v172\n"
      "v_cvt_pk_f16_f32 v196, v196, v197\n"
      "v_mul_f32 v199, v154, v199\n"
      "v_mul_f32 v237, v154, v237\n"
      "v_mul_f32_e64 v239, v154, v239\n"
      "v_cvt_pk_f16_f32 v197, v198, v199\n"
      "v_mul_f32 v138, v154, v138\n"
      "v_cvt_pk_f16_f32 v198, v136, v137\n"
      "v_cvt_pk_f16_f32 v199, v138, v139\n"
      "s_lshr_b32 s8, s9, 8\n"
      "ds_read_b128 v[136:139], v115\n"
      "ds_read_b128 v[176:179], v114\n"
      "ds_read_b128 v[192:195], v128 offset:2048\n"
      "s_and_b32 s9, s9, 0xff\n"
      "v_mul_f32 v238, v154, v238\n"
      "v_mul_f32 v236, v154, v236\n"
      "v_mul_f32 v231, v155, v231\n"
      "s_mul_i32 s10, s8, 73\n"
      "ds_write_b128 v190, v[204:207]\n"
      "ds_write_b128 v190, v[32:35] offset:16\n"
      "s_add_u32 s10, s10, s9\n"
      "s_mul_i32 s28, s10, 4802\n"
      "ds_write_b128 v190, v[184:187] offset:32\n"
      "v_cvt_pk_f16_f32 v40, v236, v237\n"
      "v_mul_f32 v230, v155, v230\n"
      "v_cvt_pk_f16_f32 v41, v238, v239\n"
      "v_cvt_pk_f16_f32 v229, v230, v231\n"
      "s_waitcnt lgkmcnt(4)\n"
      "v_mfma_f32_16x16x32_bf16 v[244:247], a[0:3], v[176:179], 0\n"
      "ds_read_b128 v[236:239], v114 offset:6144\n"
      "v_mul_f32 v173, v155, v173\n"
      "v_mfma_f32_16x16x32_bf16 v[244:247], a[36:39], v[136:139], v[244:247]\n"
      "ds_write_b64 v191, v[152:153]\n"
      "v_mfma_f32_16x16x32_bf16 v[204:207], a[4:7], v[176:179], 0\n"
      "s_add_u32 s56, s18, s28\n"
      "ds_read_b128 v[220:223], v115 offset:6144\n"
      "v_mfma_f32_16x16x32_bf16 v[204:207], a[40:43], v[136:139], v[204:207]\n"
      "ds_write_b64 v191, v[168:169] offset:32\n"
      "v_mfma_f32_16x16x32_bf16 v[160:163], a[8:11], v[176:179], 0\n"
      "s_addc_u32 s57, s19, 0\n"
      "s_and_b32 s56, s56, -16\n"
      "s_sub_u32 s58, s56, 110592\n"
      "v_mfma_f32_16x16x32_bf16 v[160:163], a[44:47], v[136:139], v[160:163]\n"
      "ds_write_b64 v191, v[240:241] offset:64\n"
      "v_mfma_f32_16x16x32_bf16 v[240:243], a[12:15], v[176:179], 0\n"
      "s_subb_u32 s59, s57, 0\n"
      "v_mul_f32 v175, v155, v175\n"
      "v_cvt_pk_f16_f32 v230, v172, v173\n"
      "v_mfma_f32_16x16x32_bf16 v[240:243], a[48:51], v[136:139], v[240:243]\n"
      "s_sub_u32 s56, s56, 73728\n"
      "v_mul_f32 v174, v155, v174\n"
      "s_subb_u32 s57, s57, 0\n"
      "v_mfma_f32_16x16x32_bf16 v[208:211], a[16:19], v[176:179], 0\n"
      "s_mul_i32 s28, s8, 1373568\n"
      "v_mul_f32_e64 v200, v155, v200\n"
      "v_mul_f32_e64 v201, v155, v201\n"
      "v_mfma_f32_16x16x32_bf16 v[208:211], a[52:55], v[136:139], v[208:211]\n"
      "v_cvt_pk_f16_f32 v231, v174, v175\n"
      "v_mul_f32_e64 v203, v155, v203\n"
      "v_mfma_f32_16x16x32_bf16 v[180:183], a[20:23], v[176:179], 0\n"
      "v_cvt_pk_bf16_f32 v212, v212, v213\n"
      "s_mul_i32 s29, s9, 2688\n"
      "v_mfma_f32_16x16x32_bf16 v[180:183], a[56:59], v[136:139], v[180:183]\n"
      "ds_read_b128 v[164:167], v114 offset:12288\n"
      "v_cvt_pk_f16_f32 v38, v200, v201\n"
      "v_mfma_f32_16x16x32_bf16 v[168:171], v[176:179], a[24:27], 0\n"
      "s_add_u32 s28, s28, s29\n"
      "v_cvt_pk_bf16_f32 v213, v214, v215\n"
      "v_mul_f32_e64 v202, v155, v202\n"
      "v_mfma_f32_16x16x32_bf16 v[168:171], v[136:139], a[60:63], v[168:171]\n"
      "s_waitcnt lgkmcnt(2)\n"
      "v_cvt_pk_bf16_f32 v156, v156, v157\n"
      "v_cvt_pk_f16_f32 v39, v202, v203\n"
      "v_mfma_f32_16x16x32_bf16 v[184:187], v[176:179], a[28:31], 0\n"
      "ds_read_b128 v[200:203], v115 offset:12288\n"
      "s_cmp_eq_u32 s8, 72\n"
      "v_mfma_f32_16x16x32_bf16 v[184:187], v[136:139], a[64:67], v[184:187]\n"
      "s_cselect_b32 s8, 0, 100270464\n"
      "v_cvt_pk_bf16_f32 v157, v158, v159\n"
      "v_cvt_pk_bf16_f32 v148, v148, v149\n"
      "v_mfma_f32_16x16x32_bf16 v[152:155], v[176:179], a[32:35], 0\n"
      "s_cmp_eq_u32 s9, 72\n"
      "s_cselect_b32 s9, 0, 196224\n"
      "s_add_u32 s10, s8, s9\n"
      "v_mfma_f32_16x16x32_bf16 v[152:155], v[136:139], a[68:71], v[152:155]\n"
      "s_add_u32 s28, s28, s10\n"
      "s_add_u32 s52, s16, s28\n"
      "s_addc_u32 s53, s17, 0\n"
      "v_mfma_f32_16x16x32_bf16 v[244:247], a[72:75], v[236:239], v[244:247]\n"
      "s_cmp_lg_u32 s10, s11\n"
      "v_cvt_pk_bf16_f32 v149, v150, v151\n"
      "v_bitop3_b32 v194, v194, v195, s4 bitop3:0x7e\n"
      "v_mfma_f32_16x16x32_bf16 v[244:247], a[108:111], v[220:223], v[244:247]\n"
      "s_cbranch_scc1 L_edge_6_%=\n"
      "L_eb_7_%=:\n"
      "v_mfma_f32_16x16x32_bf16 v[204:207], a[76:79], v[236:239], v[204:207]\n"
      "v_bitop3_b32 v192, v192, v193, s4 bitop3:0x7e\n"
      "v_mfma_f32_16x16x32_bf16 v[204:207], a[112:115], v[220:223], v[204:207]\n"
      "s_add_u32 m0, s76, 18432\n"
      "v_or_b32 v192, v192, v194\n"
      "v_cmp_ne_u32 vcc, 0, v192\n"
      "v_mfma_f32_16x16x32_bf16 v[160:163], a[80:83], v[236:239], v[160:163]\n"
      "v_mfma_f32_16x16x32_bf16 v[160:163], a[116:119], v[220:223], v[160:163]\n"
      "global_load_lds_dwordx4 v116, s[52:53]\n"
      "v_mfma_f32_16x16x32_bf16 v[180:183], a[92:95], v[236:239], v[180:183]\n"
      "s_mov_b32 exec_lo, 0x003f003f\n"
      "s_mov_b32 exec_hi, 0x003f003f\n"
      "ds_write_b128 v190, v[228:231] offset:18688\n"
      "ds_write_b128 v190, v[38:41] offset:18704\n"
      "ds_write_b128 v190, v[196:199] offset:18720\n"
      "ds_write_b64 v191, v[212:213] offset:18688\n"
      "ds_write_b64 v191, v[156:157] offset:18720\n"
      "ds_write_b64 v191, v[148:149] offset:18752\n"
      "s_mov_b64 exec, -1\n"
      "v_mfma_f32_16x16x32_bf16 v[240:243], a[84:87], v[236:239], v[240:243]\n"
      "s_mov_b64 s[48:49], vcc\n"
      "s_add_u32 m0, s76, 24448\n"
      "s_waitcnt lgkmcnt(6)\n"
      "v_mfma_f32_16x16x32_bf16 v[240:243], a[120:123], v[220:223], v[240:243]\n"
      "global_load_lds_dwordx4 v116, s[52:53] offset:128\n"
      "v_mfma_f32_16x16x32_bf16 v[208:211], a[88:91], v[236:239], v[208:211]\n"
      "s_nop 0\n"
      "v_mfma_f32_16x16x32_bf16 v[208:211], a[124:127], v[220:223], v[208:211]\n"
      "s_cbranch_vccnz L_flg_3_%=\n"
      "L_fb_4_%=:\n"
      "v_mfma_f32_16x16x32_bf16 v[180:183], a[128:131], v[220:223], v[180:183]\n"
      "v_mfma_f32_16x16x32_bf16 v[148:151], v[224:227], a[216:219], 0\n"
      "s_nop 0\n"
      "v_mfma_f32_16x16x32_bf16 v[148:151], v[224:227], a[220:223], v[148:151]\n"
      "v_mfma_f32_16x16x32_bf16 v[148:151], v[224:227], a[224:227], v[148:151]\n"
      "v_mfma_f32_16x16x32_bf16 v[168:171], v[236:239], a[96:99], v[168:171]\n"
      "s_add_u32 m0, s76, 30464\n"
      "ds_read_b128 v[144:147], v114 offset:2048\n"
      "v_mfma_f32_16x16x32_bf16 v[168:171], v[220:223], a[132:135], v[168:171]\n"
      "global_load_lds_dwordx4 v116, s[52:53] offset:256\n"
      "v_mfma_f32_16x16x32_bf16 v[184:187], v[236:239], a[100:103], v[184:187]\n"
      "s_nop 1\n"
      "v_add_f32_dpp v148, v148, v148 quad_perm:[1,0,3,2] row_mask:0xf bank_mask:0xf\n"
      "v_mfma_f32_16x16x32_bf16 v[184:187], v[220:223], a[136:139], v[184:187]\n"
      "v_mfma_f32_16x16x32_bf16 v[152:155], v[236:239], a[104:107], v[152:155]\n"
      "s_nop 0\n"
      "v_add_f32_dpp v148, v148, v148 quad_perm:[2,3,0,1] row_mask:0xf bank_mask:0xf\n"
      "v_mfma_f32_16x16x32_bf16 v[152:155], v[220:223], a[140:143], v[152:155]\n"
      "ds_read_b128 v[176:179], v115 offset:2048\n"
      "v_mfma_f32_16x16x32_bf16 v[244:247], a[144:147], v[164:167], v[244:247]\n"
      "v_add_f32_dpp v148, v148, v148 row_half_mirror row_mask:0xf bank_mask:0xf\n"
      "v_mul_f32 v150, 0xbbaaaaab, v148\n"
      "v_mfma_f32_16x16x32_bf16 v[244:247], a[180:183], v[200:203], v[244:247]\n"
      "v_mul_f32 v151, 0xbbaaaaab, v148\n"
      "v_mul_f32 v149, 0xbbaaaaab, v148\n"
      "v_mfma_f32_16x16x32_bf16 v[204:207], a[148:151], v[164:167], v[204:207]\n"
      "v_mfma_f32_16x16x32_bf16 v[204:207], a[184:187], v[200:203], v[204:207]\n"
      "v_mfma_f32_16x16x32_bf16 v[160:163], a[152:155], v[164:167], v[160:163]\n"
      "s_nop 0\n"
      "v_mul_f32 v148, 0xbbaaaaab, v148\n"
      "v_mfma_f32_16x16x32_bf16 v[160:163], a[188:191], v[200:203], v[160:163]\n"
      "s_nop 0\n"
      "v_cvt_pk_f16_f32 v244, v244, v245\n"
      "v_mfma_f32_16x16x32_bf16 v[236:239], v[252:255], a[216:219], v[148:151]\n"
      "v_mfma_f32_16x16x32_bf16 v[156:159], v[250:253], a[216:219], v[148:151]\n"
      "v_mfma_f32_16x16x32_bf16 v[192:195], v[252:255], a[220:223], v[148:151]\n"
      "v_pk_mul_f16 v248, v244, v244\n"
      "s_add_u32 m0, s77, 18432\n"
      "v_mfma_f32_16x16x32_bf16 v[216:219], v[252:255], a[224:227], v[148:151]\n"
      "global_load_lds_dwordx4 v117, s[52:53]\n"
      "v_mfma_f32_16x16x32_bf16 v[196:199], v[250:253], a[220:223], v[148:151]\n"
      "s_nop 0\n"
      "v_cvt_pk_bf16_f32 v236, v236, v237\n"
      "v_cvt_pk_bf16_f32 v237, v238, v239\n"
      "v_mfma_f32_16x16x32_bf16 v[228:231], v[250:253], a[224:227], v[148:151]\n"
      "v_cvt_pk_bf16_f32 v239, v158, v159\n"
      "v_cvt_pk_bf16_f32 v192, v192, v193\n"
      "v_mfma_f32_16x16x32_bf16 v[136:139], v[224:227], a[228:231], 0\n"
      "s_waitcnt lgkmcnt(0)\n"
      "v_mfma_f32_16x16x32_bf16 v[136:139], v[224:227], a[232:235], v[136:139]\n"
      "v_cvt_pk_bf16_f32 v238, v156, v157\n"
      "v_mfma_f32_16x16x32_bf16 v[136:139], v[224:227], a[236:239], v[136:139]\n"
      "ds_write_b128 v188, v[236:239]\n"
      "v_mfma_f32_16x16x32_bf16 v[240:243], a[156:159], v[164:167], v[240:243]\n"
      "v_cvt_pk_bf16_f32 v216, v216, v217\n"
      "v_cvt_pk_bf16_f32 v193, v194, v195\n"
      "v_mfma_f32_16x16x32_bf16 v[240:243], a[192:195], v[200:203], v[240:243]\n"
      "v_cvt_pk_bf16_f32 v194, v196, v197\n"
      "v_cvt_pk_bf16_f32 v217, v218, v219\n"
      "v_mfma_f32_16x16x32_bf16 v[208:211], a[160:163], v[164:167], v[208:211]\n"
      "v_cvt_pk_bf16_f32 v219, v230, v231\n"
      "v_cvt_pk_bf16_f32 v195, v198, v199\n"
      "v_mfma_f32_16x16x32_bf16 v[208:211], a[196:199], v[200:203], v[208:211]\n"
      "ds_write_b128 v188, v[192:195] offset:128\n"
      "v_mfma_f32_16x16x32_bf16 v[180:183], a[164:167], v[164:167], v[180:183]\n"
      "v_add_f32_dpp v136, v136, v136 quad_perm:[1,0,3,2] row_mask:0xf bank_mask:0xf\n"
      "v_cvt_pk_bf16_f32 v218, v228, v229\n"
      "v_mfma_f32_16x16x32_bf16 v[180:183], a[200:203], v[200:203], v[180:183]\n"
      "ds_write_b128 v188, v[216:219] offset:256\n"
      "v_mfma_f32_16x16x32_bf16 v[168:171], v[164:167], a[168:171], v[168:171]\n"
      "s_nop 0\n"
      "v_cvt_pk_f16_f32 v240, v240, v241\n"
      "v_add_f32_dpp v136, v136, v136 quad_perm:[2,3,0,1] row_mask:0xf bank_mask:0xf\n"
      "v_mfma_f32_16x16x32_bf16 v[168:171], v[200:203], a[204:207], v[168:171]\n"
      "s_nop 0\n"
      "v_add_f32_dpp v136, v136, v136 row_half_mirror row_mask:0xf bank_mask:0xf\n"
      "v_mul_f32 v137, 0xbbaaaaab, v136\n"
      "v_mfma_f32_16x16x32_bf16 v[184:187], v[164:167], a[172:175], v[184:187]\n"
      "v_mul_f32 v139, 0xbbaaaaab, v136\n"
      "v_mul_f32 v138, 0xbbaaaaab, v136\n"
      "v_mfma_f32_16x16x32_bf16 v[184:187], v[200:203], a[208:211], v[184:187]\n"
      "v_pk_mul_f16 v190, v240, v240\n"
      "v_mul_f32 v136, 0xbbaaaaab, v136\n"
      "v_mfma_f32_16x16x32_bf16 v[152:155], v[164:167], a[176:179], v[152:155]\n"
      "v_cvt_pk_f16_f32 v245, v246, v247\n"
      "v_pk_fma_f16 v248, v245, v245, v248\n"
      "v_mfma_f32_16x16x32_bf16 v[152:155], v[200:203], a[212:215], v[152:155]\n"
      "v_cvt_pk_f16_f32 v241, v242, v243\n"
      "v_cvt_pk_f16_f32 v246, v204, v205\n"
      "v_mfma_f32_16x16x32_bf16 v[164:167], v[250:253], a[228:231], v[136:139]\n"
      "v_pk_fma_f16 v190, v241, v241, v190\n"
      "v_pk_fma_f16 v248, v246, v246, v248\n"
      "v_mfma_f32_16x16x32_bf16 v[196:199], v[252:255], a[228:231], v[136:139]\n"
      "v_cvt_pk_f16_f32 v242, v208, v209\n"
      "v_pk_fma_f16 v190, v242, v242, v190\n"
      "v_mfma_f32_16x16x32_bf16 v[148:151], v[252:255], a[232:235], v[136:139]\n"
      "v_cvt_pk_f16_f32 v247, v206, v207\n"
      "v_cvt_pk_f16_f32 v243, v210, v211\n"
      "v_mfma_f32_16x16x32_bf16 v[200:203], v[250:253], a[232:235], v[136:139]\n"
      "v_pk_fma_f16 v248, v247, v247, v248\n"
      "v_pk_fma_f16 v190, v243, v243, v190\n"
      "v_mfma_f32_16x16x32_bf16 v[236:239], v[252:255], a[236:239], v[136:139]\n"
      "v_cvt_pk_bf16_f32 v196, v196, v197\n"
      "v_cvt_pk_bf16_f32 v197, v198, v199\n"
      "v_mfma_f32_16x16x32_bf16 v[192:195], v[250:253], a[236:239], v[136:139]\n"
      "v_cvt_pk_bf16_f32 v199, v166, v167\n"
      "v_cvt_pk_bf16_f32 v198, v164, v165\n"
      "v_mfma_f32_16x16x32_bf16 v[204:207], a[0:3], v[144:147], 0\n"
      "ds_read_b128 v[212:215], v114 offset:8192\n"
      "v_cvt_pk_bf16_f32 v148, v148, v149\n"
      "v_mfma_f32_16x16x32_bf16 v[204:207], a[36:39], v[176:179], v[204:207]\n"
      "ds_write_b128 v188, v[196:199] offset:3072\n"
      "v_mfma_f32_16x16x32_bf16 v[228:231], a[4:7], v[144:147], 0\n"
      "ds_read_b128 v[216:219], v115 offset:8192\n"
      "v_cvt_pk_bf16_f32 v149, v150, v151\n"
      "v_mfma_f32_16x16x32_bf16 v[228:231], a[40:43], v[176:179], v[228:231]\n"
      "v_cvt_pk_bf16_f32 v150, v200, v201\n"
      "v_cvt_pk_bf16_f32 v151, v202, v203\n"
      "v_mfma_f32_16x16x32_bf16 v[220:223], a[8:11], v[144:147], 0\n"
      "v_cvt_pk_bf16_f32 v236, v236, v237\n"
      "v_cvt_pk_bf16_f32 v237, v238, v239\n"
      "v_mfma_f32_16x16x32_bf16 v[220:223], a[44:47], v[176:179], v[220:223]\n"
      "ds_write_b128 v188, v[148:151] offset:3200\n"
      "v_mfma_f32_16x16x32_bf16 v[164:167], a[12:15], v[144:147], 0\n"
      "v_cvt_pk_bf16_f32 v238, v192, v193\n"
      "v_cvt_pk_bf16_f32 v239, v194, v195\n"
      "v_mfma_f32_16x16x32_bf16 v[164:167], a[48:51], v[176:179], v[164:167]\n"
      "ds_write_b128 v188, v[236:239] offset:3328\n"
      "v_mfma_f32_16x16x32_bf16 v[196:199], a[16:19], v[144:147], 0\n"
      "v_cvt_pk_f16_f32 v32, v160, v161\n"
      "v_pk_fma_f16 v248, v32, v32, v248\n"
      "v_mfma_f32_16x16x32_bf16 v[196:199], a[52:55], v[176:179], v[196:199]\n"
      "v_cvt_pk_f16_f32 v34, v180, v181\n"
      "v_pk_fma_f16 v190, v34, v34, v190\n"
      "v_mfma_f32_16x16x32_bf16 v[156:159], a[20:23], v[144:147], 0\n"
      "v_cvt_pk_f16_f32 v33, v162, v163\n"
      "v_pk_fma_f16 v248, v33, v33, v248\n"
      "v_mfma_f32_16x16x32_bf16 v[156:159], a[56:59], v[176:179], v[156:159]\n"
      "v_cvt_pk_f16_f32 v35, v182, v183\n"
      "v_pk_fma_f16 v190, v35, v35, v190\n"
      "v_mfma_f32_16x16x32_bf16 v[192:195], v[144:147], a[24:27], 0\n"
      "s_nop 0\n"
      "v_permlane16_swap_b32 v248, v190\n"
      "v_mfma_f32_16x16x32_bf16 v[192:195], v[176:179], a[60:63], v[192:195]\n"
      "ds_read_b128 v[136:139], v114 offset:14336\n"
      "s_waitcnt lgkmcnt(2)\n"
      "v_mfma_f32_16x16x32_bf16 v[172:175], v[144:147], a[28:31], 0\n"
      "v_pk_add_f16 v248, v248, v190\n"
      "v_mov_b32 v190, v248\n"
      "v_mfma_f32_16x16x32_bf16 v[172:175], v[176:179], a[64:67], v[172:175]\n"
      "s_nop 0\n"
      "v_permlane32_swap_b32 v248, v190\n"
      "v_mfma_f32_16x16x32_bf16 v[208:211], v[144:147], a[32:35], 0\n"
      "v_pk_add_f16 v248, v248, v190\n"
      "v_add_f16_sdwa v190, v248, v248 dst_sel:DWORD dst_unused:UNUSED_PAD src0_sel:WORD_0 src1_sel:WORD_1\n"
      "v_mfma_f32_16x16x32_bf16 v[208:211], v[176:179], a[68:71], v[208:211]\n"
      "ds_read_b128 v[160:163], v115 offset:14336\n"
      "v_rsq_f16 v248, v190\n"
      "v_mfma_f32_16x16x32_bf16 v[204:207], a[72:75], v[212:215], v[204:207]\n"
      "v_cmp_nle_f16 vcc, 0x1c00, v190\n"
      "v_mfma_f32_16x16x32_bf16 v[204:207], a[108:111], v[216:219], v[204:207]\n"
      "s_mov_b64 s[34:35], vcc\n"
      "v_cmp_nge_f16 vcc, 0x7b00, v190\n"
      "v_mfma_f32_16x16x32_bf16 v[236:239], v[224:227], a[240:243], 0\n"
      "v_mov_b32 v190, v248\n"
      "v_mfma_f32_16x16x32_bf16 v[236:239], v[224:227], a[244:247], v[236:239]\n"
      "s_or_b64 s[34:35], s[34:35], vcc\n"
      "s_add_u32 m0, s78, 18432\n"
      "v_mfma_f32_16x16x32_bf16 v[228:231], a[76:79], v[212:215], v[228:231]\n"
      "v_permlane16_swap_b32 v248, v190\n"
      "v_mfma_f32_16x16x32_bf16 v[228:231], a[112:115], v[216:219], v[228:231]\n"
      "s_mov_b32 exec_hi, 0\n"
      "global_load_lds_dwordx4 v118, s[52:53]\n"
      "s_mov_b32 exec_hi, -1\n"
      "v_mfma_f32_16x16x32_bf16 v[236:239], v[224:227], a[248:251], v[236:239]\n"
      "v_pk_mul_f16 v243, v243, v190 op_sel_hi:[1,0]\n"
      "v_mfma_f32_16x16x32_bf16 v[220:223], a[80:83], v[212:215], v[220:223]\n"
      "v_mul_f16 v248, s7, v248\n"
      "v_mfma_f32_16x16x32_bf16 v[220:223], a[116:119], v[216:219], v[220:223]\n"
      "v_pk_mul_f16 v247, v247, v248 op_sel_hi:[1,0]\n"
      "v_mfma_f32_16x16x32_bf16 v[164:167], a[84:87], v[212:215], v[164:167]\n"
      "v_pk_mul_f16 v33, v33, v248 op_sel_hi:[1,0]\n"
      "v_pk_mul_f16 v34, v34, v190 op_sel_hi:[1,0]\n"
      "v_mfma_f32_16x16x32_bf16 v[164:167], a[120:123], v[216:219], v[164:167]\n"
      "v_add_f32_dpp v236, v236, v236 quad_perm:[1,0,3,2] row_mask:0xf bank_mask:0xf\n"
      "v_mfma_f32_16x16x32_bf16 v[196:199], a[88:91], v[212:215], v[196:199]\n"
      "s_nop 0\n"
      "v_add_f32_dpp v236, v236, v236 quad_perm:[2,3,0,1] row_mask:0xf bank_mask:0xf\n"
      "v_mfma_f32_16x16x32_bf16 v[196:199], a[124:127], v[216:219], v[196:199]\n"
      "s_nop 0\n"
      "v_add_f32_dpp v236, v236, v236 row_half_mirror row_mask:0xf bank_mask:0xf\n"
      "v_mul_f32 v237, 0xbbaaaaab, v236\n"
      "v_mfma_f32_16x16x32_bf16 v[156:159], a[92:95], v[212:215], v[156:159]\n"
      "s_add_u32 m0, s76, 115456\n"
      "v_pk_mul_f16 v32, v32, v248 op_sel_hi:[1,0]\n"
      "v_mul_f32 v239, 0xbbaaaaab, v236\n"
      "v_mfma_f32_16x16x32_bf16 v[156:159], a[128:131], v[216:219], v[156:159]\n"
      "global_load_lds_dwordx4 v119, s[56:57]\n"
      "v_mfma_f32_16x16x32_bf16 v[192:195], v[212:215], a[96:99], v[192:195]\n"
      "v_mul_f32 v238, 0xbbaaaaab, v236\n"
      "v_mul_f32 v236, 0xbbaaaaab, v236\n"
      "v_mfma_f32_16x16x32_bf16 v[192:195], v[216:219], a[132:135], v[192:195]\n"
      "v_pk_mul_f16 v241, v241, v190 op_sel_hi:[1,0]\n"
      "v_mfma_f32_16x16x32_bf16 v[148:151], v[252:255], a[240:243], v[236:239]\n"
      "v_pk_mul_f16 v35, v35, v190 op_sel_hi:[1,0]\n"
      "v_pk_mul_f16 v245, v245, v248 op_sel_hi:[1,0]\n"
      "v_mfma_f32_16x16x32_bf16 v[178:181], v[250:253], a[240:243], v[236:239]\n"
      "v_pk_mul_f16 v244, v244, v248 op_sel_hi:[1,0]\n"
      "v_mfma_f32_16x16x32_bf16 v[144:147], v[252:255], a[244:247], v[236:239]\n"
      "v_mfma_f32_16x16x32_bf16 v[172:175], v[212:215], a[100:103], v[172:175]\n"
      "s_add_u32 m0, s79, 4864\n"
      "s_nop 0\n"
      "v_cvt_pk_bf16_f32 v148, v148, v149\n"
      "v_mfma_f32_16x16x32_bf16 v[172:175], v[216:219], a[136:139], v[172:175]\n"
      "global_load_lds_dword v122, s[58:59]\n"
      "v_mfma_f32_16x16x32_bf16 v[208:211], v[212:215], a[104:107], v[208:211]\n"
      "v_cvt_pk_bf16_f32 v149, v150, v151\n"
      "v_cvt_pk_bf16_f32 v144, v144, v145\n"
      "v_mfma_f32_16x16x32_bf16 v[208:211], v[216:219], a[140:143], v[208:211]\n"
      "s_waitcnt lgkmcnt(0)\n"
      "v_cvt_pk_bf16_f32 v150, v178, v179\n"
      "v_mfma_f32_16x16x32_bf16 v[212:215], v[250:253], a[244:247], v[236:239]\n"
      "v_pk_mul_f16 v242, v242, v190 op_sel_hi:[1,0]\n"
      "v_pk_mul_f16 v246, v246, v248 op_sel_hi:[1,0]\n"
      "v_mfma_f32_16x16x32_bf16 v[216:219], v[252:255], a[248:251], v[236:239]\n"
      "v_pk_mul_f16 v240, v240, v190 op_sel_hi:[1,0]\n"
      "v_mfma_f32_16x16x32_bf16 v[204:207], a[144:147], v[136:139], v[204:207]\n"
      "v_cvt_pk_bf16_f32 v151, v180, v181\n"
      "v_cvt_pk_bf16_f32 v145, v146, v147\n"
      "v_mfma_f32_16x16x32_bf16 v[204:207], a[180:183], v[160:163], v[204:207]\n"
      "ds_write_b128 v188, v[148:151] offset:6144\n"
      "v_mfma_f32_16x16x32_bf16 v[176:179], v[250:253], a[248:251], v[236:239]\n"
      "ds_read_b128 v[148:151], v114 offset:4096\n"
      "v_cvt_pk_bf16_f32 v168, v168, v169\n"
      "v_mfma_f32_16x16x32_bf16 v[228:231], a[148:151], v[136:139], v[228:231]\n"
      "v_cvt_pk_bf16_f32 v147, v214, v215\n"
      "v_cvt_pk_bf16_f32 v146, v212, v213\n"
      "v_mfma_f32_16x16x32_bf16 v[228:231], a[184:187], v[160:163], v[228:231]\n"
      "ds_write_b128 v188, v[144:147] offset:6272\n"
      "s_waitcnt vmcnt(15)\n"
      "v_mfma_f32_16x16x32_bf16 v[144:147], v[224:227], a[252:255], 0\n"
      "ds_read_b128 v[236:239], v115 offset:4096\n"
      "v_cvt_pk_bf16_f32 v216, v216, v217\n"
      "s_waitcnt vmcnt(14)\n"
      "v_mfma_f32_16x16x32_bf16 v[144:147], v[224:227], v[0:3], v[144:147]\n"
      "v_cvt_pk_bf16_f32 v217, v218, v219\n"
      "v_cvt_pk_bf16_f32 v184, v184, v185\n"
      "s_waitcnt vmcnt(13)\n"
      "v_mfma_f32_16x16x32_bf16 v[144:147], v[224:227], v[4:7], v[144:147]\n"
      "s_nop 0\n"
      "v_cvt_pk_bf16_f32 v169, v170, v171\n"
      "v_cvt_pk_bf16_f32 v218, v176, v177\n"
      "v_mfma_f32_16x16x32_bf16 v[220:223], a[152:155], v[136:139], v[220:223]\n"
      "v_cvt_pk_bf16_f32 v219, v178, v179\n"
      "v_mfma_f32_16x16x32_bf16 v[220:223], a[188:191], v[160:163], v[220:223]\n"
      "ds_write_b128 v188, v[216:219] offset:6400\n"
      "v_mfma_f32_16x16x32_bf16 v[164:167], a[156:159], v[136:139], v[164:167]\n"
      "s_nop 0\n"
      "v_add_f32_dpp v144, v144, v144 quad_perm:[1,0,3,2] row_mask:0xf bank_mask:0xf\n"
      "v_cvt_pk_bf16_f32 v152, v152, v153\n"
      "v_mfma_f32_16x16x32_bf16 v[164:167], a[192:195], v[160:163], v[164:167]\n"
      "s_nop 0\n"
      "v_cvt_pk_f16_f32 v204, v204, v205\n"
      "v_add_f32_dpp v144, v144, v144 quad_perm:[2,3,0,1] row_mask:0xf bank_mask:0xf\n"
      "v_mfma_f32_16x16x32_bf16 v[196:199], a[160:163], v[136:139], v[196:199]\n"
      "v_cvt_pk_bf16_f32 v185, v186, v187\n"
      "v_add_f32_dpp v144, v144, v144 row_half_mirror row_mask:0xf bank_mask:0xf\n"
      "v_mfma_f32_16x16x32_bf16 v[196:199], a[196:199], v[160:163], v[196:199]\n"
      "v_mul_f32 v145, 0xbbaaaaab, v144\n"
      "v_mul_f32 v147, 0xbbaaaaab, v144\n"
      "v_mfma_f32_16x16x32_bf16 v[156:159], a[164:167], v[136:139], v[156:159]\n"
      "s_waitcnt lgkmcnt(1)\n"
      "v_mul_f32 v146, 0xbbaaaaab, v144\n"
      "v_mul_f32 v144, 0xbbaaaaab, v144\n"
      "v_mfma_f32_16x16x32_bf16 v[156:159], a[200:203], v[160:163], v[156:159]\n"
      "s_nop 0\n"
      "v_cvt_pk_f16_f32 v205, v206, v207\n"
      "v_cvt_pk_bf16_f32 v153, v154, v155\n"
      "v_mfma_f32_16x16x32_bf16 v[200:203], v[252:255], a[252:255], v[144:147]\n"
      "v_pk_mul_f16 v248, v204, v204\n"
      "v_mfma_f32_16x16x32_bf16 v[212:215], v[250:253], a[252:255], v[144:147]\n"
      "v_cvt_pk_f16_f32 v164, v164, v165\n"
      "v_pk_mul_f16 v233, v164, v164\n"
      "v_mfma_f32_16x16x32_bf16 v[192:195], v[136:139], a[168:171], v[192:195]\n"
      "v_pk_fma_f16 v248, v205, v205, v248\n"
      "v_cvt_pk_f16_f32 v165, v166, v167\n"
      "v_mfma_f32_16x16x32_bf16 v[192:195], v[160:163], a[204:207], v[192:195]\n"
      "v_cvt_pk_bf16_f32 v200, v200, v201\n"
      "v_cvt_pk_bf16_f32 v201, v202, v203\n"
      "v_mfma_f32_16x16x32_bf16 v[172:175], v[136:139], a[172:175], v[172:175]\n"
      "v_cvt_pk_bf16_f32 v203, v214, v215\n"
      "v_cvt_pk_bf16_f32 v202, v212, v213\n"
      "v_mfma_f32_16x16x32_bf16 v[172:175], v[160:163], a[208:211], v[172:175]\n"
      "ds_write_b128 v188, v[200:203] offset:9216\n"
      "v_mfma_f32_16x16x32_bf16 v[216:219], v[252:255], v[0:3], v[144:147]\n"
      "v_cvt_pk_bf16_f32 v171, v194, v195\n"
      "v_cvt_pk_bf16_f32 v170, v192, v193\n"
      "v_mfma_f32_16x16x32_bf16 v[200:203], v[250:253], v[0:3], v[144:147]\n"
      "v_cvt_pk_f16_f32 v206, v228, v229\n"
      "v_pk_fma_f16 v233, v165, v165, v233\n"
      "v_mfma_f32_16x16x32_bf16 v[192:195], v[252:255], v[4:7], v[144:147]\n"
      "v_cvt_pk_bf16_f32 v186, v172, v173\n"
      "v_cvt_pk_bf16_f32 v187, v174, v175\n"
      "v_mfma_f32_16x16x32_bf16 v[176:179], v[250:253], v[4:7], v[144:147]\n"
      "v_cvt_pk_bf16_f32 v216, v216, v217\n"
      "v_cvt_pk_bf16_f32 v217, v218, v219\n"
      "v_mfma_f32_16x16x32_bf16 v[208:211], v[136:139], a[176:179], v[208:211]\n"
      "ds_read_b128 v[180:183], v114 offset:10240\n"
      "v_cvt_pk_bf16_f32 v218, v200, v201\n"
      "v_mfma_f32_16x16x32_bf16 v[208:211], v[160:163], a[212:215], v[208:211]\n"
      "v_cvt_pk_bf16_f32 v192, v192, v193\n"
      "v_cvt_pk_bf16_f32 v219, v202, v203\n"
      "v_mfma_f32_16x16x32_bf16 v[172:175], a[0:3], v[148:151], 0\n"
      "ds_read_b128 v[212:215], v115 offset:10240\n"
      "v_cvt_pk_bf16_f32 v193, v194, v195\n"
      "v_mfma_f32_16x16x32_bf16 v[172:175], a[36:39], v[236:239], v[172:175]\n"
      "ds_write_b128 v188, v[216:219] offset:9344\n"
      "v_mfma_f32_16x16x32_bf16 v[160:163], a[4:7], v[148:151], 0\n"
      "v_cvt_pk_bf16_f32 v195, v178, v179\n"
      "v_cvt_pk_bf16_f32 v194, v176, v177\n"
      "v_mfma_f32_16x16x32_bf16 v[160:163], a[40:43], v[236:239], v[160:163]\n"
      "ds_write_b128 v188, v[192:195] offset:9472\n"
      "v_mfma_f32_16x16x32_bf16 v[140:143], a[8:11], v[148:151], 0\n"
      "v_cvt_pk_f16_f32 v166, v196, v197\n"
      "v_cvt_pk_bf16_f32 v155, v210, v211\n"
      "v_mfma_f32_16x16x32_bf16 v[140:143], a[44:47], v[236:239], v[140:143]\n"
      "v_cvt_pk_f16_f32 v41, v158, v159\n"
      "v_pk_fma_f16 v248, v206, v206, v248\n"
      "v_mfma_f32_16x16x32_bf16 v[200:203], a[12:15], v[148:151], 0\n"
      "v_pk_fma_f16 v233, v166, v166, v233\n"
      "v_mfma_f32_16x16x32_bf16 v[200:203], a[48:51], v[236:239], v[200:203]\n"
      "v_cvt_pk_f16_f32 v38, v220, v221\n"
      "v_cvt_pk_f16_f32 v167, v198, v199\n"
      "v_mfma_f32_16x16x32_bf16 v[144:147], a[16:19], v[148:151], 0\n"
      "v_pk_fma_f16 v233, v167, v167, v233\n"
      "v_cvt_pk_bf16_f32 v154, v208, v209\n"
      "v_mfma_f32_16x16x32_bf16 v[144:147], a[52:55], v[236:239], v[144:147]\n"
      "v_cvt_pk_f16_f32 v207, v230, v231\n"
      "v_cvt_pk_f16_f32 v40, v156, v157\n"
      "v_mfma_f32_16x16x32_bf16 v[136:139], a[20:23], v[148:151], 0\n"
      "s_waitcnt lgkmcnt(1)\n"
      "v_pk_fma_f16 v248, v207, v207, v248\n"
      "v_pk_fma_f16 v233, v40, v40, v233\n"
      "v_mfma_f32_16x16x32_bf16 v[136:139], a[56:59], v[236:239], v[136:139]\n"
      "s_nop 0\n"
      "v_pk_fma_f16 v248, v38, v38, v248\n"
      "v_cvt_pk_f16_f32 v39, v222, v223\n"
      "v_mfma_f32_16x16x32_bf16 v[220:223], v[148:151], a[24:27], 0\n"
      "v_pk_fma_f16 v233, v41, v41, v233\n"
      "v_pk_fma_f16 v248, v39, v39, v248\n"
      "v_mfma_f32_16x16x32_bf16 v[220:223], v[236:239], a[60:63], v[220:223]\n"
      "s_nop 0\n"
      "v_permlane16_swap_b32 v248, v233\n"
      "v_mfma_f32_16x16x32_bf16 v[208:211], v[148:151], a[28:31], 0\n"
      "v_pk_add_f16 v248, v248, v233\n"
      "v_mov_b32 v233, v248\n"
      "v_mfma_f32_16x16x32_bf16 v[208:211], v[236:239], a[64:67], v[208:211]\n"
      "s_nop 0\n"
      "v_permlane32_swap_b32_e64 v248, v233\n"
      "v_mfma_f32_16x16x32_bf16 v[176:179], v[148:151], a[32:35], 0\n"
      "v_pk_add_f16 v248, v248, v233\n"
      "v_add_f16_sdwa v233, v248, v248 dst_sel:DWORD dst_unused:UNUSED_PAD src0_sel:WORD_0 src1_sel:WORD_1\n"
      "v_mfma_f32_16x16x32_bf16 v[176:179], v[236:239], a[68:71], v[176:179]\n"
      "v_rsq_f16 v248, v233\n"
      "v_mfma_f32_16x16x32_bf16 v[172:175], a[72:75], v[180:183], v[172:175]\n"
      "v_cmp_nle_f16 vcc, 0x1c00, v233\n"
      "v_mfma_f32_16x16x32_bf16 v[172:175], a[108:111], v[212:215], v[172:175]\n"
      "s_or_b64 s[34:35], s[34:35], vcc\n"
      "v_cmp_nge_f16 vcc, 0x7b00, v233\n"
      "v_mov_b32 v233, v248\n"
      "v_mfma_f32_16x16x32_bf16 v[160:163], a[76:79], v[180:183], v[160:163]\n"
      "s_nop 0\n"
      "v_permlane16_swap_b32_e64 v248, v233\n"
      "v_mfma_f32_16x16x32_bf16 v[160:163], a[112:115], v[212:215], v[160:163]\n"
      "s_waitcnt vmcnt(12)\n"
      "v_mfma_f32_16x16x32_bf16 v[216:219], v[224:227], v[8:11], 0\n"
      "v_mul_f16 v248, s7, v248\n"
      "v_pk_mul_f16 v40, v40, v233 op_sel_hi:[1,0]\n"
      "s_waitcnt vmcnt(11)\n"
      "v_mfma_f32_16x16x32_bf16 v[216:219], v[224:227], v[12:15], v[216:219]\n"
      "v_pk_mul_f16 v164, v164, v233 op_sel_hi:[1,0]\n"
      "s_waitcnt vmcnt(10)\n"
      "v_mfma_f32_16x16x32_bf16 v[216:219], v[224:227], v[16:19], v[216:219]\n"
      "v_mfma_f32_16x16x32_bf16 v[140:143], a[80:83], v[180:183], v[140:143]\n"
      "v_pk_mul_f16 v207, v207, v248 op_sel_hi:[1,0]\n"
      "v_pk_mul_f16 v167, v167, v233 op_sel_hi:[1,0]\n"
      "v_mfma_f32_16x16x32_bf16 v[140:143], a[116:119], v[212:215], v[140:143]\n"
      "s_nop 0\n"
      "v_pk_mul_f16 v204, v204, v248 op_sel_hi:[1,0]\n"
      "v_mfma_f32_16x16x32_bf16 v[200:203], a[84:87], v[180:183], v[200:203]\n"
      "s_nop 1\n"
      "v_add_f32_dpp v216, v216, v216 quad_perm:[1,0,3,2] row_mask:0xf bank_mask:0xf\n"
      "v_mfma_f32_16x16x32_bf16 v[200:203], a[120:123], v[212:215], v[200:203]\n"
      "v_pk_mul_f16 v165, v165, v233 op_sel_hi:[1,0]\n"
      "v_add_f32_dpp v216, v216, v216 quad_perm:[2,3,0,1] row_mask:0xf bank_mask:0xf\n"
      "v_mfma_f32_16x16x32_bf16 v[144:147], a[88:91], v[180:183], v[144:147]\n"
      "s_nop 0\n"
      "v_add_f32_dpp v216, v216, v216 row_half_mirror row_mask:0xf bank_mask:0xf\n"
      "v_mul_f32 v218, 0xbbaaaaab, v216\n"
      "v_mfma_f32_16x16x32_bf16 v[144:147], a[124:127], v[212:215], v[144:147]\n"
      "s_nop 0\n"
      "v_pk_mul_f16 v206, v206, v248 op_sel_hi:[1,0]\n"
      "v_mul_f32 v219, 0xbbaaaaab, v216\n"
      "v_mfma_f32_16x16x32_bf16 v[136:139], a[92:95], v[180:183], v[136:139]\n"
      "v_mul_f32 v217, 0xbbaaaaab, v216\n"
      "v_mul_f32 v216, 0xbbaaaaab, v216\n"
      "v_mfma_f32_16x16x32_bf16 v[136:139], a[128:131], v[212:215], v[136:139]\n"
      "v_pk_mul_f16 v166, v166, v233 op_sel_hi:[1,0]\n"
      "v_pk_mul_f16 v41, v41, v233 op_sel_hi:[1,0]\n"
      "v_mfma_f32_16x16x32_bf16 v[230:233], v[252:255], v[8:11], v[216:219]\n"
      "ds_read_b128 v[148:151], v114 offset:16384\n"
      "v_pk_mul_f16 v205, v205, v248 op_sel_hi:[1,0]\n"
      "v_mfma_f32_16x16x32_bf16 v[208:211], v[180:183], a[100:103], v[208:211]\n"
      "v_mfma_f32_16x16x32_bf16 v[208:211], v[212:215], a[136:139], v[208:211]\n"
      "v_mfma_f32_16x16x32_bf16 v[220:223], v[180:183], a[96:99], v[220:223]\n"
      "s_nop 0\n"
      "v_pk_mul_f16 v38, v38, v248 op_sel_hi:[1,0]\n"
      "s_or_b64 s[34:35], s[34:35], vcc\n"
      "v_mfma_f32_16x16x32_bf16 v[220:223], v[212:215], a[132:135], v[220:223]\n"
      "ds_read_b128 v[192:195], v115 offset:16384\n"
      "v_mfma_f32_16x16x32_bf16 v[156:159], v[250:253], v[8:11], v[216:219]\n"
      "v_mfma_f32_16x16x32_bf16 v[176:179], v[180:183], a[104:107], v[176:179]\n"
      "v_cvt_pk_bf16_f32 v230, v230, v231\n"
      "v_cvt_pk_bf16_f32 v231, v232, v233\n"
      "v_mfma_f32_16x16x32_bf16 v[176:179], v[212:215], a[140:143], v[176:179]\n"
      "ds_read_b128 v[196:199], v132 offset:32\n"
      "v_mfma_f32_16x16x32_bf16 v[212:215], v[252:255], v[12:15], v[216:219]\n"
      "s_nop 1\n"
      "v_cvt_pk_bf16_f32 v232, v156, v157\n"
      "v_cvt_pk_bf16_f32 v233, v158, v159\n"
      "v_mfma_f32_16x16x32_bf16 v[180:183], v[250:253], v[12:15], v[216:219]\n"
      "ds_write_b128 v188, v[230:233] offset:12288\n"
      "v_mfma_f32_16x16x32_bf16 v[236:239], v[252:255], v[16:19], v[216:219]\n"
      "s_nop 0\n"
      "v_cvt_pk_bf16_f32 v212, v212, v213\n"
      "v_cvt_pk_bf16_f32 v213, v214, v215\n"
      "s_waitcnt vmcnt(9)\n"
      "v_mfma_f32_16x16x32_bf16 v[228:231], v[224:227], v[20:23], 0\n"
      "s_nop 0\n"
      "v_cvt_pk_bf16_f32 v215, v182, v183\n"
      "s_waitcnt vmcnt(8)\n"
      "v_mfma_f32_16x16x32_bf16 v[228:231], v[224:227], v[24:27], v[228:231]\n"
      "s_nop 0\n"
      "v_cvt_pk_bf16_f32 v214, v180, v181\n"
      "s_waitcnt lgkmcnt(2)\n"
      "s_waitcnt vmcnt(7)\n"
      "v_mfma_f32_16x16x32_bf16 v[228:231], v[224:227], v[28:31], v[228:231]\n"
      "ds_write_b128 v188, v[212:215] offset:12416\n"
      "v_mfma_f32_16x16x32_bf16 v[212:215], v[250:253], v[16:19], v[216:219]\n"
      "ds_read_u16 v52, v134\n"
      "v_cvt_pk_bf16_f32 v236, v236, v237\n"
      "v_mfma_f32_16x16x32_bf16 v[172:175], a[144:147], v[148:151], v[172:175]\n"
      "ds_read_b64 v[224:225], v132 offset:24\n"
      "v_mfma_f32_16x16x32_bf16 v[172:175], a[180:183], v[192:195], v[172:175]\n"
      "v_cvt_pk_bf16_f32 v237, v238, v239\n"
      "v_pk_mul_f16 v39, v39, v248 op_sel_hi:[1,0]\n"
      "v_mfma_f32_16x16x32_bf16 v[160:163], a[148:151], v[148:151], v[160:163]\n"
      "v_mfma_f32_16x16x32_bf16 v[160:163], a[184:187], v[192:195], v[160:163]\n"
      "v_mfma_f32_16x16x32_bf16 v[140:143], a[152:155], v[148:151], v[140:143]\n"
      "s_nop 0\n"
      "v_add_f32_dpp v228, v228, v228 quad_perm:[1,0,3,2] row_mask:0xf bank_mask:0xf\n"
      "v_mfma_f32_16x16x32_bf16 v[140:143], a[188:191], v[192:195], v[140:143]\n"
      "s_nop 0\n"
      "v_add_f32_dpp v228, v228, v228 quad_perm:[2,3,0,1] row_mask:0xf bank_mask:0xf\n"
      "v_cvt_pk_bf16_f32 v239, v214, v215\n"
      "v_mfma_f32_16x16x32_bf16 v[200:203], a[156:159], v[148:151], v[200:203]\n"
      "v_mfma_f32_16x16x32_bf16 v[200:203], a[192:195], v[192:195], v[200:203]\n"
      "v_add_f32_dpp v228, v228, v228 row_half_mirror row_mask:0xf bank_mask:0xf\n"
      "v_mul_f32 v230, 0xbbaaaaab, v228\n"
      "v_mfma_f32_16x16x32_bf16 v[144:147], a[160:163], v[148:151], v[144:147]\n"
      "v_mul_f32 v231, 0xbbaaaaab, v228\n"
      "v_mfma_f32_16x16x32_bf16 v[144:147], a[196:199], v[192:195], v[144:147]\n"
      "ds_read_b128 v[180:183], v133\n"
      "v_mul_f32 v229, 0xbbaaaaab, v228\n"
      "v_mfma_f32_16x16x32_bf16 v[136:139], a[164:167], v[148:151], v[136:139]\n"
      "v_mul_f32 v228, 0xbbaaaaab, v228\n"
      "v_cvt_pk_bf16_f32 v238, v212, v213\n"
      "v_mfma_f32_16x16x32_bf16 v[136:139], a[200:203], v[192:195], v[136:139]\n"
      "ds_write_b128 v188, v[236:239] offset:12544\n"
      "v_mfma_f32_16x16x32_bf16 v[212:215], v[252:255], v[20:23], v[228:231]\n"
      "ds_read_u16 v60, v134 offset:64\n"
      "v_mfma_f32_16x16x32_bf16 v[220:223], v[148:151], a[168:171], v[220:223]\n"
      "ds_read_b64 v[226:227], v133 offset:16\n"
      "v_mfma_f32_16x16x32_bf16 v[216:219], v[250:253], v[20:23], v[228:231]\n"
      "ds_read_u16 v56, v134 offset:32\n"
      "v_mfma_f32_16x16x32_bf16 v[156:159], v[252:255], v[24:27], v[228:231]\n"
      "v_mfma_f32_16x16x32_bf16 v[176:179], v[148:151], a[176:179], v[176:179]\n"
      "s_nop 0\n"
      "v_cvt_pk_bf16_f32 v212, v212, v213\n"
      "v_cvt_pk_bf16_f32 v213, v214, v215\n"
      "v_mfma_f32_16x16x32_bf16 v[176:179], v[192:195], a[212:215], v[176:179]\n"
      "s_nop 0\n"
      "v_cvt_pk_bf16_f32 v215, v218, v219\n"
      "v_cvt_pk_bf16_f32 v214, v216, v217\n"
      "v_mfma_f32_16x16x32_bf16 v[216:219], v[250:253], v[24:27], v[228:231]\n"
      "v_cvt_pk_bf16_f32 v156, v156, v157\n"
      "v_cvt_pk_bf16_f32 v157, v158, v159\n"
      "v_mfma_f32_16x16x32_bf16 v[220:223], v[192:195], a[204:207], v[220:223]\n"
      "ds_write_b128 v188, v[212:215] offset:15360\n"
      "v_mfma_f32_16x16x32_bf16 v[236:239], v[250:253], v[28:31], v[228:231]\n"
      "s_waitcnt vmcnt(0)\n"
      "ds_read_b128 v[212:215], v119 offset:41728\n"
      "v_mfma_f32_16x16x32_bf16 v[208:211], v[148:151], a[172:175], v[208:211]\n"
      "ds_read_b32 v233, v122 offset:4864\n"
      "v_mfma_f32_16x16x32_bf16 v[208:211], v[192:195], a[208:211], v[208:211]\n"
      "v_cvt_pk_bf16_f32 v158, v216, v217\n"
      "v_mfma_f32_16x16x32_bf16 v[192:195], v[252:255], v[28:31], v[228:231]\n"
      "s_nop 7\n"
      "v_cvt_pk_bf16_f32 v192, v192, v193\n"
      "v_cvt_pk_bf16_f32 v193, v194, v195\n"
      "v_cvt_pk_bf16_f32 v194, v236, v237\n"
      "v_cvt_pk_bf16_f32 v159, v218, v219\n"
      "v_cvt_pk_bf16_f32 v195, v238, v239\n"
      "ds_write_b128 v188, v[192:195] offset:15616\n"
      "ds_write_b128 v188, v[156:159] offset:15488\n"
      "s_waitcnt lgkmcnt(2)\n"
      "v_or3_b32 v212, v212, v213, v214\n"
      "v_and_b32 v212, s40, v212\n"
      "v_bitop3_b32 v233, v233, v215, s40 bitop3:0xa8\n"
      "v_or3_b32 v212, v212, v233, s4\n"
      "ds_write_b32 v129, v212 offset:3072\n"
      "s_waitcnt lgkmcnt(0)\n"
      "s_barrier\n"
      "v_mfma_f32_16x16x32_f16 v[148:151], v[240:243], v[244:247], v[78:81]\n"
      "ds_read_b64_tr_b16 a[220:221], v234 offset:8\n"
      "v_cvt_pk_f16_f32 v172, v172, v173\n"
      "v_mfma_f32_16x16x32_f16 v[148:151], v[34:37], v[32:35], v[148:151]\n"
      "ds_read_b64_tr_b16 a[216:217], v234\n"
      "v_cvt_pk_f16_f32 v44, v140, v141\n"
      "v_mfma_f32_16x16x32_f16 v[216:219], v[164:167], v[244:247], v[82:85]\n"
      "ds_read_b64_tr_b16 a[218:219], v234 offset:6144\n"
      "v_cvt_pk_f16_f32 v200, v200, v201\n"
      "v_mfma_f32_16x16x32_f16 v[216:219], v[40:43], v[32:35], v[216:219]\n"
      "ds_read_b64_tr_b16 a[224:225], v235\n"
      "v_pk_mul_f16 v212, v172, v172\n"
      "v_cvt_pk_f16_f32 v201, v202, v203\n"
      "v_mfma_f32_16x16x32_f16 v[156:159], v[196:199], v[244:247], v[70:73]\n"
      "ds_read_b64_tr_b16 a[226:227], v235 offset:6144\n"
      "v_cvt_pk_f16_f32 v173, v174, v175\n"
      "v_mfma_f32_16x16x32_f16 v[156:159], v[180:183], v[240:243], v[156:159]\n"
      "ds_read_b64_tr_b16 a[222:223], v234 offset:6152\n"
      "v_pk_fma_f16 v212, v173, v173, v212\n"
      "v_mfma_f32_16x16x32_f16 v[156:159], v[224:227], v[32:35], v[156:159]\n"
      "ds_read_b64_tr_b16 a[228:229], v234 offset:18432\n"
      "v_cvt_pk_f16_f32 v174, v160, v161\n"
      "v_pk_mul_f16 v233, v200, v200\n"
      "v_cvt_pk_f16_f32 v175, v162, v163\n"
      "v_cvt_pk_f16_f32 v202, v144, v145\n"
      "v_pk_fma_f16 v233, v201, v201, v233\n"
      "v_cvt_pk_f16_f32 v47, v138, v139\n"
      "v_pk_fma_f16 v212, v174, v174, v212\n"
      "v_cvt_pk_f16_f32 v203, v146, v147\n"
      "v_pk_fma_f16 v233, v202, v202, v233\n"
      "v_pk_fma_f16 v233, v203, v203, v233\n"
      "v_cvt_pk_f16_f32 v46, v136, v137\n"
      "v_pk_fma_f16 v212, v175, v175, v212\n"
      "v_cvt_pk_f16_f32 v45, v142, v143\n"
      "v_pk_fma_f16 v212, v44, v44, v212\n"
      "v_pk_fma_f16 v233, v46, v46, v233\n"
      "v_pk_fma_f16 v233, v47, v47, v233\n"
      "v_pk_fma_f16 v212, v45, v45, v212\n"
      "v_cvt_pk_bf16_f32 v50, v220, v221\n"
      "v_cvt_pk_bf16_f32 v55, v210, v211\n"
      "v_permlane16_swap_b32 v212, v233\n"
      "v_pk_add_f16 v212, v212, v233\n"
      "v_mov_b32 v233, v212\n"
      "v_cvt_pk_bf16_f32 v59, v178, v179\n"
      "v_cvt_pk_bf16_f32 v54, v208, v209\n"
      "v_permlane32_swap_b32_e64 v212, v233\n"
      "v_pk_add_f16 v212, v212, v233\n"
      "v_add_f16_sdwa v233, v212, v212 dst_sel:DWORD dst_unused:UNUSED_PAD src0_sel:WORD_0 src1_sel:WORD_1\n"
      "v_cmp_nle_f16 vcc, 0x1c00, v233\n"
      "v_rsq_f16 v212, v233\n"
      "s_or_b64 s[34:35], s[34:35], vcc\n"
      "ds_read_b64_tr_b16 a[252:253], v249 offset:18432\n"
      "ds_read_b64_tr_b16 a[242:243], v249 offset:6144\n"
      "ds_read_b64_tr_b16 a[236:237], v235 offset:18432\n"
      "v_cmp_nge_f16 vcc, 0x7b00, v233\n"
      "v_mov_b32 v233, v212\n"
      "v_cvt_pk_bf16_f32 v58, v176, v177\n"
      "v_cvt_pk_bf16_f32 v51, v222, v223\n"
      "v_permlane16_swap_b32 v212, v233\n"
      "v_mul_f16 v212, s7, v212\n"
      "v_pk_mul_f16 v200, v200, v233 op_sel_hi:[1,0]\n"
      "v_pk_mul_f16 v172, v172, v212 op_sel_hi:[1,0]\n"
      "v_pk_mul_f16 v46, v46, v233 op_sel_hi:[1,0]\n"
      "v_pk_mul_f16 v203, v203, v233 op_sel_hi:[1,0]\n"
      "v_pk_mul_f16 v201, v201, v233 op_sel_hi:[1,0]\n"
      "v_pk_mul_f16 v202, v202, v233 op_sel_hi:[1,0]\n"
      "v_pk_mul_f16 v47, v47, v233 op_sel_hi:[1,0]\n"
      "s_or_b64 s[34:35], s[34:35], vcc\n"
      "ds_read_b64_tr_b16 v[18:19], v235 offset:30720\n"
      "ds_read_b64_tr_b16 v[12:13], v234 offset:12296\n"
      "ds_read_b64_tr_b16 v[8:9], v234 offset:12288\n"
      "ds_read_b64_tr_b16 a[248:249], v189\n"
      "ds_read_b64_tr_b16 v[6:7], v189 offset:24576\n"
      "ds_read_b64_tr_b16 a[230:231], v234 offset:24576\n"
      "ds_read_b64_tr_b16 a[244:245], v249 offset:8\n"
      "ds_read_b64_tr_b16 a[250:251], v189 offset:6144\n"
      "ds_read_b64_tr_b16 v[20:21], v249 offset:12288\n"
      "ds_read_b64_tr_b16 v[24:25], v249 offset:12296\n"
      "ds_read_b64_tr_b16 a[238:239], v235 offset:24576\n"
      "v_mfma_f32_16x16x32_f16 v[220:223], v[200:203], v[244:247], v[86:89]\n"
      "ds_read_b64_tr_b16 a[232:233], v234 offset:18440\n"
      "v_pk_mul_f16 v174, v174, v212 op_sel_hi:[1,0]\n"
      "v_mfma_f32_16x16x32_f16 v[220:223], v[46:49], v[32:35], v[220:223]\n"
      "ds_read_b64_tr_b16 v[28:29], v189 offset:12288\n"
      "ds_read_b64_tr_b16 a[234:235], v234 offset:24584\n"
      "ds_read_b64_tr_b16 a[240:241], v249\n"
      "ds_read_b64_tr_b16 v[26:27], v249 offset:30728\n"
      "ds_read_b64_tr_b16 v[16:17], v235 offset:12288\n"
      "ds_read_b64_tr_b16 v[30:31], v189 offset:30720\n"
      "ds_read_b64_tr_b16 a[254:255], v249 offset:24576\n"
      "ds_read_b64_tr_b16 v[14:15], v234 offset:30728\n"
      "ds_read_b64_tr_b16 v[22:23], v249 offset:30720\n"
      "ds_read_b64_tr_b16 v[4:5], v189 offset:18432\n"
      "ds_read_b64_tr_b16 v[10:11], v234 offset:30720\n"
      "ds_read_b64_tr_b16 v[2:3], v249 offset:24584\n"
      "ds_read_b64_tr_b16 a[246:247], v249 offset:6152\n"
      "ds_read_b64_tr_b16 v[0:1], v249 offset:18440\n"
      "v_pk_mul_f16 v45, v45, v212 op_sel_hi:[1,0]\n"
      "v_pk_mul_f16 v173, v173, v212 op_sel_hi:[1,0]\n"
      "v_pk_mul_f16 v175, v175, v212 op_sel_hi:[1,0]\n"
      "v_pk_mul_f16 v44, v44, v212 op_sel_hi:[1,0]\n"
      "s_waitcnt lgkmcnt(0)\n"
      "s_barrier\n"
      "s_or_b64 vcc, s[48:49], s[50:51]\n"
      "s_or_b64 vcc, vcc, s[34:35]\n"
      "s_cbranch_scc1 L_gen_8_%=\n"
      "L_fast_11_%=:\n"
      "v_exp_f32 v217, v217\n"
      "v_mfma_f32_16x16x32_f16 v[144:147], v[240:243], v[204:207], v[90:93]\n"
      "v_exp_f32 v156, v156\n"
      "v_mfma_f32_16x16x32_f16 v[144:147], v[34:37], v[38:41], v[144:147]\n"
      "ds_read_b128 v[228:231], v128 offset:3072\n"
      "v_exp_f32 v149, v149\n"
      "v_cvt_pk_bf16_f32 v64, v156, 0\n"
      "v_mfma_f32_16x16x32_f16 v[236:239], v[164:167], v[204:207], v[94:97]\n"
      "v_exp_f32 v148, v148\n"
      "v_mfma_f32_16x16x32_f16 v[236:239], v[40:43], v[38:41], v[236:239]\n"
      "v_exp_f32 v150, v150\n"
      "v_mfma_f32_16x16x32_f16 v[176:179], v[196:199], v[204:207], v[72:75]\n"
      "v_exp_f32 v151, v151\n"
      "v_mfma_f32_16x16x32_f16 v[176:179], v[180:183], v[164:167], v[176:179]\n"
      "v_exp_f32 v219, v219\n"
      "v_mfma_f32_16x16x32_f16 v[176:179], v[224:227], v[38:41], v[176:179]\n"
      "v_exp_f32 v145, v145\n"
      "v_exp_f32 v146, v146\n"
      "v_mfma_f32_16x16x32_f16 v[232:235], v[200:203], v[204:207], v[98:101]\n"
      "v_cvt_pk_bf16_f32 v148, v148, v149\n"
      "v_cvt_pk_bf16_f32 v149, v150, v151\n"
      "v_mfma_f32_16x16x32_f16 v[232:235], v[46:49], v[38:41], v[232:235]\n"
      "v_exp_f32 v216, v216\n"
      "v_mfma_f32_16x16x32_f16 v[244:247], v[240:243], v[172:175], v[102:105]\n"
      "s_waitcnt lgkmcnt(0)\n"
      "v_bitop3_b32 v230, v230, v231, s4 bitop3:0x7e\n"
      "v_bitop3_b32 v228, v228, v229, s4 bitop3:0x7e\n"
      "v_mfma_f32_16x16x32_f16 v[244:247], v[34:37], v[44:47], v[244:247]\n"
      "v_exp_f32_e64 v218, v218\n"
      "v_mfma_f32_16x16x32_f16 v[252:255], v[164:167], v[172:175], v[106:109]\n"
      "v_cvt_pk_bf16_f32 v150, v216, v217\n"
      "v_or_b32 v228, v228, v230\n"
      "v_mfma_f32_16x16x32_f16 v[252:255], v[40:43], v[44:47], v[252:255]\n"
      "v_exp_f32 v220, v220\n"
      "v_mfma_f32_16x16x32_f16 v[164:167], v[196:199], v[172:175], v[74:77]\n"
      "v_exp_f32 v223, v223\n"
      "v_mfma_f32_16x16x32_f16 v[164:167], v[180:183], v[200:203], v[164:167]\n"
      "v_cmp_ne_u32 vcc, 0, v228\n"
      "v_cvt_pk_bf16_f32 v151, v218, v219\n"
      "v_mfma_f32_16x16x32_f16 v[164:167], v[224:227], v[44:47], v[164:167]\n"
      "v_cndmask_b32 v157, v157, v177, s[42:43]\n"
      "v_exp_f32 v235, v235\n"
      "v_mfma_f32_16x16x32_f16 v[192:195], v[180:183], v[196:199], v[76:79]\n"
      "v_exp_f32 v222, v222\n"
      "v_exp_f32_e64 v234, v234\n"
      "v_cvt_pk_bf16_f32 v63, v222, v223\n"
      "v_exp_f32 v233, v233\n"
      "v_mfma_f32_16x16x32_f16 v[160:163], v[200:203], v[172:175], v[110:113]\n"
      "s_mov_b64 s[48:49], vcc\n"
      "v_exp_f32_e64 v221, v221\n"
      "v_mfma_f32_16x16x32_f16 v[160:163], v[46:49], v[44:47], v[160:163]\n"
      "v_cvt_pk_bf16_f32 v62, v220, v221\n"
      "v_cndmask_b32 v157, v157, v165, s[44:45]\n"
      "v_mfma_f32_16x16x16_f16 v[192:195], v[226:227], v[224:225], v[192:195]\n"
      "v_exp_f32 v176, v176\n"
      "v_mfma_f32_16x16x32_bf16 v[216:219], v[66:69], v[148:151], 0\n"
      "v_exp_f32 v144, v144\n"
      "v_mfma_f32_16x16x32_bf16 v[216:219], v[66:69], v[62:65], v[216:219]\n"
      "v_cvt_pk_bf16_f32 v144, v144, v145\n"
      "v_exp_f32 v239, v239\n"
      "v_mfma_f32_16x16x32_bf16 v[180:183], v[168:171], v[148:151], 0\n"
      "v_exp_f32_e64 v147, v147\n"
      "v_mfma_f32_16x16x32_bf16 v[180:183], v[50:53], v[62:65], v[180:183]\n"
      "s_add_u32 s28, s27, 2\n"
      "v_cvt_pk_bf16_f32 v145, v146, v147\n"
      "v_exp_f32 v232, v232\n"
      "v_mfma_f32_16x16x32_bf16 v[224:227], v[184:187], v[148:151], 0\n"
      "v_exp_f32 v236, v236\n"
      "v_mfma_f32_16x16x32_bf16 v[224:227], v[54:57], v[62:65], v[224:227]\n"
      "v_exp_f32 v238, v238\n"
      "v_mfma_f32_16x16x32_bf16 v[196:199], v[152:155], v[148:151], 0\n"
      "s_min_u32 m0, s28, s3\n"
      "v_exp_f32 v237, v237\n"
      "v_mfma_f32_16x16x32_bf16 v[196:199], v[58:61], v[62:65], v[196:199]\n"
      "s_cbranch_vccnz L_flg_12_%=\n"
      "L_fb_13_%=:\n"
      "v_cvt_pk_bf16_f32 v63, v234, v235\n"
      "v_cvt_pk_bf16_f32 v147, v238, v239\n"
      "v_cvt_pk_bf16_f32 v146, v236, v237\n"
      "v_cvt_pk_bf16_f32 v62, v232, v233\n"
      "v_cvt_pk_bf16_f32 v64, v176, 0\n"
      "v_rcp_f32 v216, v216\n"
      "v_mfma_f32_16x16x32_bf16 v[236:239], v[66:69], v[144:147], 0\n"
      "ds_read_b128 v[248:251], v114 offset:18432\n"
      "v_mul_f32 v180, v216, v180\n"
      "v_mfma_f32_16x16x32_bf16 v[236:239], v[66:69], v[62:65], v[236:239]\n"
      "ds_read_b128 v[220:223], v115 offset:18432\n"
      "v_mul_f32 v183, v216, v183\n"
      "v_mul_f32 v181, v216, v181\n"
      "v_mul_f32_e64 v198, v216, v198\n"
      "v_mfma_f32_16x16x32_bf16 v[148:151], v[168:171], v[144:147], 0\n"
      "s_movrels_b32 s9, s80\n"
      "v_cvt_pk_bf16_f32 v180, v180, v181\n"
      "v_mul_f32 v182, v216, v182\n"
      "v_mfma_f32_16x16x32_bf16 v[148:151], v[50:53], v[62:65], v[148:151]\n"
      "s_lshr_b32 s8, s9, 8\n"
      "v_mul_f32 v197, v216, v197\n"
      "v_mul_f32_e64 v196, v216, v196\n"
      "v_mfma_f32_16x16x32_bf16 v[140:143], v[184:187], v[144:147], 0\n"
      "v_mul_f32_e64 v224, v216, v224\n"
      "v_mul_f32_e64 v226, v216, v226\n"
      "v_mfma_f32_16x16x32_bf16 v[140:143], v[54:57], v[62:65], v[140:143]\n"
      "s_and_b32 s9, s9, 0xff\n"
      "v_mul_f32_e64 v225, v216, v225\n"
      "v_cvt_pk_bf16_f32 v181, v182, v183\n"
      "v_cvt_pk_bf16_f32 v182, v224, v225\n"
      "v_mfma_f32_16x16x32_bf16 v[204:207], v[152:155], v[144:147], 0\n"
      "s_mul_i32 s10, s8, 73\n"
      "v_mul_f32 v227, v216, v227\n"
      "v_cvt_pk_bf16_f32 v183, v226, v227\n"
      "v_mfma_f32_16x16x32_bf16 v[204:207], v[58:61], v[62:65], v[204:207]\n"
      "ds_write_b128 v124, v[180:183]\n"
      "s_waitcnt lgkmcnt(1)\n"
      "v_mfma_f32_16x16x32_bf16 v[188:191], a[0:3], v[248:251], 0\n"
      "s_add_u32 s10, s10, s9\n"
      "v_mul_f32 v199, v216, v199\n"
      "v_cvt_pk_bf16_f32 v196, v196, v197\n"
      "v_mfma_f32_16x16x32_bf16 v[188:191], a[36:39], v[220:223], v[188:191]\n"
      "s_mul_i32 s28, s10, 4802\n"
      "v_cndmask_b32 v157, v157, v193, s[46:47]\n"
      "v_cvt_pk_bf16_f32 v197, v198, v199\n"
      "v_mfma_f32_16x16x32_bf16 v[172:175], a[4:7], v[248:251], 0\n"
      "s_add_u32 s56, s18, s28\n"
      "v_exp_f32 v157, v157\n"
      "v_mfma_f32_16x16x32_bf16 v[172:175], a[40:43], v[220:223], v[172:175]\n"
      "s_addc_u32 s57, s19, 0\n"
      "v_mov_b32 v198, 0\n"
      "v_cvt_pk_bf16_f32 v157, v157, v157\n"
      "v_mfma_f32_16x16x32_bf16 v[176:179], a[8:11], v[248:251], 0\n"
      "s_and_b32 s56, s56, -16\n"
      "v_exp_f32 v244, v244\n"
      "v_mfma_f32_16x16x32_bf16 v[176:179], a[44:47], v[220:223], v[176:179]\n"
      "ds_write_b16 v130, v157\n"
      "v_mfma_f32_16x16x32_bf16 v[228:231], a[12:15], v[248:251], 0\n"
      "s_sub_u32 s58, s56, 110592\n"
      "ds_read_b128 v[144:147], v131\n"
      "v_mfma_f32_16x16x32_bf16 v[228:231], a[48:51], v[220:223], v[228:231]\n"
      "ds_read_b128 v[224:227], v131 offset:64\n"
      "v_exp_f32 v255, v255\n"
      "v_exp_f32 v252, v252\n"
      "v_mfma_f32_16x16x32_bf16 v[156:159], a[16:19], v[248:251], 0\n"
      "s_subb_u32 s59, s57, 0\n"
      "v_exp_f32 v245, v245\n"
      "v_mfma_f32_16x16x32_bf16 v[156:159], a[52:55], v[220:223], v[156:159]\n"
      "s_sub_u32 s56, s56, 73728\n"
      "v_exp_f32 v254, v254\n"
      "v_cvt_pk_bf16_f32 v244, v244, v245\n"
      "v_exp_f32 v162, v162\n"
      "v_mfma_f32_16x16x32_bf16 v[216:219], a[20:23], v[248:251], 0\n"
      "s_subb_u32 s57, s57, 0\n"
      "v_exp_f32 v247, v247\n"
      "v_mfma_f32_16x16x32_bf16 v[216:219], a[56:59], v[220:223], v[216:219]\n"
      "s_mul_i32 s28, s8, 1373568\n"
      "v_exp_f32 v253, v253\n"
      "v_exp_f32 v246, v246\n"
      "v_exp_f32 v160, v160\n"
      "v_cvt_pk_bf16_f32 v245, v246, v247\n"
      "v_exp_f32 v161, v161\n"
      "v_cvt_pk_bf16_f32 v247, v254, v255\n"
      "s_waitcnt lgkmcnt(0)\n"
      "v_mfma_f32_16x16x32_bf16 v[240:243], v[144:147], v[66:69], 0\n"
      "s_mul_i32 s29, s9, 2688\n"
      "v_exp_f32 v163, v163\n"
      "v_mfma_f32_16x16x32_bf16 v[240:243], v[224:227], v[66:69], v[240:243]\n"
      "s_add_u32 s28, s28, s29\n"
      "v_exp_f32 v164, v164\n"
      "v_mfma_f32_16x16x32_bf16 v[192:195], v[144:147], v[168:171], 0\n"
      "s_cmp_eq_u32 s8, 72\n"
      "v_cvt_pk_bf16_f32 v62, v160, v161\n"
      "v_cvt_pk_bf16_f32 v63, v162, v163\n"
      "v_mfma_f32_16x16x32_bf16 v[192:195], v[224:227], v[50:53], v[192:195]\n"
      "s_cselect_b32 s8, 0, 100270464\n"
      "v_cvt_pk_bf16_f32 v64, v164, 0\n"
      "v_cvt_pk_bf16_f32 v246, v252, v253\n"
      "v_mfma_f32_16x16x32_bf16 v[180:183], v[144:147], v[184:187], 0\n"
      "ds_read_b128 v[200:203], v114 offset:24576\n"
      "s_cmp_eq_u32 s9, 72\n"
      "v_mfma_f32_16x16x32_bf16 v[180:183], v[224:227], v[54:57], v[180:183]\n"
      "s_cselect_b32 s9, 0, 196224\n"
      "v_rcp_f32_e64 v236, v236\n"
      "v_mov_b32_dpp v240, v240 quad_perm:[0,0,0,0] row_mask:0xf bank_mask:0xf\n"
      "v_mfma_f32_16x16x32_bf16 v[232:235], v[66:69], v[244:247], 0\n"
      "ds_read_b128 v[212:215], v115 offset:24576\n"
      "v_mul_f32 v148, v236, v148\n"
      "v_mfma_f32_16x16x32_bf16 v[232:235], v[66:69], v[62:65], v[232:235]\n"
      "s_add_u32 s10, s8, s9\n"
      "v_mul_f32 v149, v236, v149\n"
      "v_cvt_pk_bf16_f32 v148, v148, v149\n"
      "v_mfma_f32_16x16x32_bf16 v[252:255], v[168:171], v[244:247], 0\n"
      "s_add_u32 s28, s28, s10\n"
      "v_mul_f32 v150, v236, v150\n"
      "v_mul_f32 v151, v236, v151\n"
      "v_mfma_f32_16x16x32_bf16 v[252:255], v[50:53], v[62:65], v[252:255]\n"
      "s_add_u32 s52, s16, s28\n"
      "v_cvt_pk_bf16_f32 v149, v150, v151\n"
      "v_mul_f32 v141, v236, v141\n"
      "v_mfma_f32_16x16x32_bf16 v[136:139], v[184:187], v[244:247], 0\n"
      "s_addc_u32 s53, s17, 0\n"
      "v_mul_f32 v140, v236, v140\n"
      "v_cvt_pk_bf16_f32 v150, v140, v141\n"
      "v_mfma_f32_16x16x32_bf16 v[136:139], v[54:57], v[62:65], v[136:139]\n"
      "s_cmp_lg_u32 s10, s11\n"
      "v_mul_f32 v142, v236, v142\n"
      "v_mul_f32 v143, v236, v143\n"
      "v_mfma_f32_16x16x32_bf16 v[208:211], v[152:155], v[244:247], 0\n"
      "s_cbranch_scc1 L_edge_15_%=\n"
      "L_eb_16_%=:\n"
      "v_mfma_f32_16x16x32_bf16 v[208:211], v[58:61], v[62:65], v[208:211]\n"
      "v_mul_f32_e64 v204, v236, v204\n"
      "v_cvt_pk_bf16_f32 v151, v142, v143\n"
      "v_mfma_f32_16x16x32_bf16 v[184:187], v[144:147], v[152:155], 0\n"
      "s_waitcnt lgkmcnt(0)\n"
      "v_mul_f32 v207, v236, v207\n"
      "v_mul_f32_e64 v205, v236, v205\n"
      "v_mfma_f32_16x16x32_bf16 v[184:187], v[224:227], v[58:61], v[184:187]\n"
      "ds_write_b128 v124, v[148:151] offset:1024\n"
      "v_mul_f32_e64 v206, v236, v206\n"
      "v_cvt_pk_bf16_f32 v204, v204, v205\n"
      "v_cvt_pk_bf16_f32 v205, v206, v207\n"
      "v_rcp_f32_e64 v240, v240\n"
      "v_cndmask_b32 v192, v192, v180, s[42:43]\n"
      "ds_write2st64_b64 v125, v[196:197], v[204:205] offset0:0 offset1:2\n"
      "ds_write_b32 v129, v198\n"
      "v_cndmask_b32 v192, v192, v184, s[44:45]\n"
      "v_mul_f32_e64 v192, v240, v192\n"
      "v_cvt_pk_bf16_f32 v192, v192, v192\n"
      "ds_write_b16 v135, v192\n"
      "L_join_10_%=:\n"
      "s_mov_b32 s27, 1\n"
      ".p2align 6\n"
      "L_loop_%=:\n"
      "v_mfma_f32_16x16x32_bf16 v[152:155], v[248:251], a[24:27], 0\n"
      "v_rcp_f32_e64 v232, v232\n"
      "v_mfma_f32_16x16x32_bf16 v[152:155], v[220:223], a[60:63], v[152:155]\n"
      "s_waitcnt lgkmcnt(0)\n"
      "s_barrier\n"
      "v_mfma_f32_16x16x32_bf16 v[234:237], v[248:251], a[28:31], 0\n"
      "s_add_u32 m0, s76, 0\n"
      "v_mul_f32 v252, v232, v252\n"
      "v_mul_f32 v254, v232, v254\n"
      "v_mfma_f32_16x16x32_bf16 v[234:237], v[220:223], a[64:67], v[234:237]\n"
      "v_mul_f32 v253, v232, v253\n"
      "v_cvt_pk_bf16_f32 v252, v252, v253\n"
      "v_mfma_f32_16x16x32_bf16 v[184:187], v[248:251], a[32:35], 0\n"
      "ds_read_b128 v[248:251], v128\n"
      "v_mul_f32_e64 v255, v232, v255\n"
      "v_mfma_f32_16x16x32_bf16 v[184:187], v[220:223], a[68:71], v[184:187]\n"
      "global_load_lds_dwordx4 v116, s[52:53]\n"
      "v_mfma_f32_16x16x32_bf16 v[188:191], a[72:75], v[200:203], v[188:191]\n"
      "ds_read_b128 v[220:223], v114 offset:30720\n"
      "v_cvt_pk_bf16_f32 v253, v254, v255\n"
      "v_mfma_f32_16x16x32_bf16 v[188:191], a[108:111], v[212:215], v[188:191]\n"
      "v_mul_f32 v137, v232, v137\n"
      "v_mul_f32 v136, v232, v136\n"
      "v_mfma_f32_16x16x32_bf16 v[172:175], a[76:79], v[200:203], v[172:175]\n"
      "v_cvt_pk_bf16_f32 v254, v136, v137\n"
      "v_mul_f32 v138, v232, v138\n"
      "v_mfma_f32_16x16x32_bf16 v[172:175], a[112:115], v[212:215], v[172:175]\n"
      "v_mul_f32 v139, v232, v139\n"
      "v_cvt_pk_bf16_f32 v255, v138, v139\n"
      "v_mfma_f32_16x16x32_bf16 v[176:179], a[80:83], v[200:203], v[176:179]\n"
      "ds_read_b128 v[136:139], v115 offset:30720\n"
      "v_mul_f32_e64 v208, v232, v208\n"
      "v_mfma_f32_16x16x32_bf16 v[176:179], a[116:119], v[212:215], v[176:179]\n"
      "ds_write_b128 v124, v[252:255] offset:2048\n"
      "v_mfma_f32_16x16x32_bf16 v[228:231], a[84:87], v[200:203], v[228:231]\n"
      "v_mul_f32 v211, v232, v211\n"
      "v_mul_f32 v209, v232, v209\n"
      "v_mfma_f32_16x16x32_bf16 v[228:231], a[120:123], v[212:215], v[228:231]\n"
      "s_waitcnt lgkmcnt(3)\n"
      "v_mul_f32 v210, v232, v210\n"
      "v_cvt_pk_bf16_f32 v208, v208, v209\n"
      "v_mfma_f32_16x16x32_bf16 v[156:159], a[88:91], v[200:203], v[156:159]\n"
      "s_add_u32 m0, s76, 6016\n"
      "v_add_f32 v248, |v248|, |v249|\n"
      "v_cvt_pk_bf16_f32 v209, v210, v211\n"
      "v_mfma_f32_16x16x32_bf16 v[156:159], a[124:127], v[212:215], v[156:159]\n"
      "ds_write_b64 v125, v[208:209] offset:2048\n"
      "v_mfma_f32_16x16x32_bf16 v[216:219], a[92:95], v[200:203], v[216:219]\n"
      "global_load_lds_dwordx4 v116, s[52:53] offset:128\n"
      "v_mfma_f32_16x16x32_bf16 v[216:219], a[128:131], v[212:215], v[216:219]\n"
      "v_add_f32 v250, |v250|, |v251|\n"
      "v_add_f32 v248, v248, v250\n"
      "v_mfma_f32_16x16x32_bf16 v[152:155], v[200:203], a[96:99], v[152:155]\n"
      "s_waitcnt lgkmcnt(2)\n"
      "v_rsq_f32 v248, v248\n"
      "v_mfma_f32_16x16x32_bf16 v[152:155], v[212:215], a[132:135], v[152:155]\n"
      "v_mul_f32 v248, s41, v248\n"
      "v_mfma_f32_16x16x32_bf16 v[234:237], v[200:203], a[100:103], v[234:237]\n"
      "ds_read_b128 v[208:211], v114 offset:20480\n"
      "v_mov_b32 v232, v248\n"
      "v_mfma_f32_16x16x32_bf16 v[234:237], v[212:215], a[136:139], v[234:237]\n"
      "s_nop 0\n"
      "v_permlane32_swap_b32 v248, v232\n"
      "v_mfma_f32_16x16x32_bf16 v[184:187], v[200:203], a[104:107], v[184:187]\n"
      "ds_read_b128 v[252:255], v115 offset:20480\n"
      "v_mov_b32 v233, v248\n"
      "v_mfma_f32_16x16x32_bf16 v[184:187], v[212:215], a[140:143], v[184:187]\n"
      "s_add_u32 m0, s76, 12032\n"
      "v_permlane16_swap_b32 v248, v233\n"
      "v_mfma_f32_16x16x32_bf16 v[188:191], a[144:147], v[220:223], v[188:191]\n"
      "v_mul_f32 v169, v248, v169\n"
      "v_mul_f32_e64 v168, v248, v168\n"
      "v_mfma_f32_16x16x32_bf16 v[188:191], a[180:183], v[136:139], v[188:191]\n"
      "v_mul_f32 v170, v248, v170\n"
      "v_mul_f32 v171, v248, v171\n"
      "v_mfma_f32_16x16x32_bf16 v[172:175], a[148:151], v[220:223], v[172:175]\n"
      "v_mul_f32_e64 v199, v248, v199\n"
      "v_cvt_pk_bf16_f32 v168, v168, v169\n"
      "v_mfma_f32_16x16x32_bf16 v[172:175], a[184:187], v[136:139], v[172:175]\n"
      "global_load_lds_dwordx4 v116, s[52:53] offset:256\n"
      "v_mfma_f32_16x16x32_bf16 v[176:179], a[152:155], v[220:223], v[176:179]\n"
      "v_cvt_pk_bf16_f32 v169, v170, v171\n"
      "v_mfma_f32_16x16x32_bf16 v[176:179], a[188:191], v[136:139], v[176:179]\n"
      "v_mul_f32 v197, v248, v197\n"
      "v_mul_f32 v198, v248, v198\n"
      "v_mfma_f32_16x16x32_bf16 v[228:231], a[156:159], v[220:223], v[228:231]\n"
      "s_add_u32 m0, s77, 0\n"
      "v_mul_f32 v196, v248, v196\n"
      "v_cvt_pk_bf16_f32 v170, v196, v197\n"
      "v_mfma_f32_16x16x32_bf16 v[228:231], a[192:195], v[136:139], v[228:231]\n"
      "v_mul_f32 v148, v248, v148\n"
      "v_mul_f32 v150, v248, v150\n"
      "v_mfma_f32_16x16x32_bf16 v[156:159], a[160:163], v[220:223], v[156:159]\n"
      "s_waitcnt lgkmcnt(0)\n"
      "v_mul_f32 v149, v248, v149\n"
      "v_cvt_pk_bf16_f32 v171, v198, v199\n"
      "v_mfma_f32_16x16x32_bf16 v[156:159], a[196:199], v[136:139], v[156:159]\n"
      "ds_write_b128 v126, v[168:171] offset:18432\n"
      "v_mfma_f32_16x16x32_bf16 v[216:219], a[164:167], v[220:223], v[216:219]\n"
      "v_mul_f32_e64 v151, v248, v151\n"
      "v_cvt_pk_bf16_f32 v148, v148, v149\n"
      "v_mfma_f32_16x16x32_bf16 v[216:219], a[200:203], v[136:139], v[216:219]\n"
      "v_mul_f32 v244, v233, v244\n"
      "v_cvt_pk_bf16_f32 v149, v150, v151\n"
      "v_mfma_f32_16x16x32_bf16 v[152:155], v[220:223], a[168:171], v[152:155]\n"
      "v_mul_f32 v245, v233, v245\n"
      "v_mul_f32 v246, v233, v246\n"
      "v_mfma_f32_16x16x32_bf16 v[152:155], v[136:139], a[204:207], v[152:155]\n"
      "global_load_lds_dwordx4 v117, s[52:53]\n"
      "v_mfma_f32_16x16x32_bf16 v[234:237], v[220:223], a[172:175], v[234:237]\n"
      "v_mul_f32 v144, v233, v144\n"
      "v_cvt_pk_bf16_f32 v244, v244, v245\n"
      "v_mfma_f32_16x16x32_bf16 v[234:237], v[136:139], a[208:211], v[234:237]\n"
      "s_add_u32 m0, s78, 0\n"
      "v_mul_f32 v247, v233, v247\n"
      "v_cvt_pk_bf16_f32 v245, v246, v247\n"
      "v_mfma_f32_16x16x32_bf16 v[184:187], v[220:223], a[176:179], v[184:187]\n"
      "ds_read_b128 v[168:171], v114 offset:26624\n"
      "v_mul_f32_e64 v145, v233, v145\n"
      "v_mfma_f32_16x16x32_bf16 v[184:187], v[136:139], a[212:215], v[184:187]\n"
      "ds_read_b128 v[196:199], v115 offset:26624\n"
      "v_cvt_pk_bf16_f32 v246, v144, v145\n"
      "v_mfma_f32_16x16x32_bf16 v[220:223], a[0:3], v[208:211], 0\n"
      "v_mul_f32 v146, v233, v146\n"
      "v_mul_f32 v147, v233, v147\n"
      "v_mfma_f32_16x16x32_bf16 v[220:223], a[36:39], v[252:255], v[220:223]\n"
      "v_cvt_pk_bf16_f32 v247, v146, v147\n"
      "v_mul_f32 v204, v233, v204\n"
      "v_mfma_f32_16x16x32_bf16 v[144:147], a[4:7], v[208:211], 0\n"
      "v_mul_f32 v205, v233, v205\n"
      "v_cvt_pk_bf16_f32 v204, v204, v205\n"
      "v_mfma_f32_16x16x32_bf16 v[144:147], a[40:43], v[252:255], v[144:147]\n"
      "ds_write_b128 v126, v[244:247] offset:20480\n"
      "v_mfma_f32_16x16x32_bf16 v[244:247], a[8:11], v[208:211], 0\n"
      "v_mul_f32 v206, v233, v206\n"
      "v_mul_f32 v207, v233, v207\n"
      "v_mfma_f32_16x16x32_bf16 v[244:247], a[44:47], v[252:255], v[244:247]\n"
      "s_mov_b32 exec_hi, 0\n"
      "global_load_lds_dwordx4 v118, s[52:53]\n"
      "s_mov_b32 exec_hi, -1\n"
      "v_mfma_f32_16x16x32_bf16 v[248:251], a[12:15], v[208:211], 0\n"
      "v_cvt_pk_bf16_f32 v205, v206, v207\n"
      "v_mul_f32_e64 v160, v232, v160\n"
      "v_mfma_f32_16x16x32_bf16 v[248:251], a[48:51], v[252:255], v[248:251]\n"
      "ds_write2st64_b64 v127, v[148:149], v[204:205] offset0:36 offset1:40\n"
      "v_mfma_f32_16x16x32_bf16 v[148:151], a[16:19], v[208:211], 0\n"
      "s_waitcnt lgkmcnt(2)\n"
      "v_mul_f32 v161, v232, v161\n"
      "v_cvt_pk_bf16_f32 v160, v160, v161\n"
      "v_mfma_f32_16x16x32_bf16 v[148:151], a[52:55], v[252:255], v[148:151]\n"
      "v_mul_f32 v163, v232, v163\n"
      "v_mul_f32 v162, v232, v162\n"
      "v_mfma_f32_16x16x32_bf16 v[204:207], a[20:23], v[208:211], 0\n"
      "v_cvt_pk_bf16_f32 v161, v162, v163\n"
      "v_mul_f32 v140, v232, v140\n"
      "v_mfma_f32_16x16x32_bf16 v[204:207], a[56:59], v[252:255], v[204:207]\n"
      "v_mul_f32 v141, v232, v141\n"
      "v_cvt_pk_bf16_f32 v162, v140, v141\n"
      "v_mfma_f32_16x16x32_bf16 v[136:139], v[208:211], a[24:27], 0\n"
      "ds_read_b128 v[200:203], v114 offset:32768\n"
      "v_mul_f32_e64 v142, v232, v142\n"
      "v_mfma_f32_16x16x32_bf16 v[136:139], v[252:255], a[60:63], v[136:139]\n"
      "s_add_u32 m0, s76, 110592\n"
      "v_mul_f32_e64 v143, v232, v143\n"
      "v_cvt_pk_bf16_f32 v163, v142, v143\n"
      "v_mfma_f32_16x16x32_bf16 v[212:215], v[208:211], a[28:31], 0\n"
      "global_load_lds_dwordx4 v119, s[56:57]\n"
      "v_mfma_f32_16x16x32_bf16 v[212:215], v[252:255], a[64:67], v[212:215]\n"
      "ds_write_b128 v126, v[160:163] offset:22528\n"
      "v_mfma_f32_16x16x32_bf16 v[160:163], v[208:211], a[32:35], 0\n"
      "ds_read_b128 v[208:211], v115 offset:32768\n"
      "v_mul_f32_e64 v224, v232, v224\n"
      "v_mfma_f32_16x16x32_bf16 v[160:163], v[252:255], a[68:71], v[160:163]\n"
      "v_mul_f32_e64 v225, v232, v225\n"
      "v_cvt_pk_bf16_f32 v224, v224, v225\n"
      "v_mfma_f32_16x16x32_bf16 v[220:223], a[72:75], v[168:171], v[220:223]\n"
      "s_add_u32 m0, s79, 0\n"
      "v_mul_f32 v227, v232, v227\n"
      "v_mul_f32_e64 v226, v232, v226\n"
      "v_mfma_f32_16x16x32_bf16 v[144:147], a[76:79], v[168:171], v[144:147]\n"
      "v_cvt_pk_bf16_f32 v225, v226, v227\n"
      "v_cvt_pk_f16_f32 v188, v188, v189\n"
      "v_mfma_f32_16x16x32_bf16 v[220:223], a[108:111], v[196:199], v[220:223]\n"
      "global_load_lds_dword v122, s[58:59]\n"
      "v_mfma_f32_16x16x32_bf16 v[144:147], a[112:115], v[196:199], v[144:147]\n"
      "ds_write_b64 v127, v[224:225] offset:22528\n"
      "v_mfma_f32_16x16x32_bf16 v[244:247], a[80:83], v[168:171], v[244:247]\n"
      "v_pk_mul_f16 v232, v188, v188\n"
      "v_cvt_pk_f16_f32 v228, v228, v229\n"
      "v_mfma_f32_16x16x32_bf16 v[244:247], a[116:119], v[196:199], v[244:247]\n"
      "v_cvt_pk_f16_f32 v189, v190, v191\n"
      "v_pk_mul_f16 v233, v228, v228\n"
      "v_mfma_f32_16x16x32_bf16 v[248:251], a[84:87], v[168:171], v[248:251]\n"
      "v_cvt_pk_f16_f32 v229, v230, v231\n"
      "v_pk_fma_f16 v232, v189, v189, v232\n"
      "v_mfma_f32_16x16x32_bf16 v[248:251], a[120:123], v[196:199], v[248:251]\n"
      "ds_read_b128 v[140:143], v114 offset:22528\n"
      "v_cvt_pk_f16_f32 v190, v172, v173\n"
      "v_mfma_f32_16x16x32_bf16 v[148:151], a[88:91], v[168:171], v[148:151]\n"
      "v_pk_fma_f16 v233, v229, v229, v233\n"
      "v_pk_fma_f16 v232, v190, v190, v232\n"
      "v_mfma_f32_16x16x32_bf16 v[148:151], a[124:127], v[196:199], v[148:151]\n"
      "ds_read_b128 v[224:227], v115 offset:22528\n"
      "v_cvt_pk_f16_f32 v230, v156, v157\n"
      "v_mfma_f32_16x16x32_bf16 v[204:207], a[92:95], v[168:171], v[204:207]\n"
      "v_cvt_pk_f16_f32 v191, v174, v175\n"
      "v_pk_fma_f16 v233, v230, v230, v233\n"
      "v_mfma_f32_16x16x32_bf16 v[204:207], a[128:131], v[196:199], v[204:207]\n"
      "s_waitcnt lgkmcnt(3)\n"
      "v_cvt_pk_f16_f32 v231, v158, v159\n"
      "v_pk_fma_f16 v232, v191, v191, v232\n"
      "v_mfma_f32_16x16x32_bf16 v[136:139], v[168:171], a[96:99], v[136:139]\n"
      "s_nop 0\n"
      "v_pk_fma_f16 v233, v231, v231, v233\n"
      "v_cvt_pk_f16_f32 v32, v176, v177\n"
      "v_mfma_f32_16x16x32_bf16 v[136:139], v[196:199], a[132:135], v[136:139]\n"
      "v_cvt_pk_f16_f32 v34, v216, v217\n"
      "v_pk_fma_f16 v232, v32, v32, v232\n"
      "v_mfma_f32_16x16x32_bf16 v[212:215], v[168:171], a[100:103], v[212:215]\n"
      "v_cvt_pk_f16_f32 v33, v178, v179\n"
      "v_pk_fma_f16 v233, v34, v34, v233\n"
      "v_mfma_f32_16x16x32_bf16 v[212:215], v[196:199], a[136:139], v[212:215]\n"
      "ds_read_b128 v[252:255], v119\n"
      "v_pk_fma_f16 v232, v33, v33, v232\n"
      "v_mfma_f32_16x16x32_bf16 v[160:163], v[168:171], a[104:107], v[160:163]\n"
      "v_cvt_pk_f16_f32 v35, v218, v219\n"
      "v_pk_fma_f16 v233, v35, v35, v233\n"
      "v_mfma_f32_16x16x32_bf16 v[160:163], v[196:199], a[140:143], v[160:163]\n"
      "v_cvt_pk_bf16_f32 v152, v152, v153\n"
      "v_cvt_pk_bf16_f32 v153, v154, v155\n"
      "v_mfma_f32_16x16x32_bf16 v[220:223], a[144:147], v[200:203], v[220:223]\n"
      "v_permlane16_swap_b32_e64 v232, v233\n"
      "v_mfma_f32_16x16x32_bf16 v[220:223], a[180:183], v[208:211], v[220:223]\n"
      "v_pk_add_f16 v232, v232, v233\n"
      "v_mov_b32 v233, v232\n"
      "v_mfma_f32_16x16x32_bf16 v[144:147], a[148:151], v[200:203], v[144:147]\n"
      "s_nop 0\n"
      "v_permlane32_swap_b32_e64 v232, v233\n"
      "v_mfma_f32_16x16x32_bf16 v[144:147], a[184:187], v[208:211], v[144:147]\n"
      "v_pk_add_f16 v232, v232, v233\n"
      "v_add_f16_sdwa v233, v232, v232 dst_sel:DWORD dst_unused:UNUSED_PAD src0_sel:WORD_0 src1_sel:WORD_1\n"
      "v_mfma_f32_16x16x32_bf16 v[244:247], a[152:155], v[200:203], v[244:247]\n"
      "ds_read_b128 v[168:171], v114 offset:28672\n"
      "v_cmp_nle_f16 vcc, 0x1c00, v233\n"
      "v_mfma_f32_16x16x32_bf16 v[244:247], a[188:191], v[208:211], v[244:247]\n"
      "s_mov_b64 s[34:35], vcc\n"
      "v_rsq_f16 v232, v233\n"
      "v_mfma_f32_16x16x32_bf16 v[248:251], a[156:159], v[200:203], v[248:251]\n"
      "s_waitcnt lgkmcnt(1)\n"
      "v_cmp_nge_f16 vcc, 0x7b00, v233\n"
      "v_mov_b32 v233, v232\n"
      "v_mfma_f32_16x16x32_bf16 v[248:251], a[192:195], v[208:211], v[248:251]\n"
      "ds_read_b128 v[196:199], v115 offset:28672\n"
      "v_cvt_pk_bf16_f32 v234, v234, v235\n"
      "v_mfma_f32_16x16x32_bf16 v[148:151], a[160:163], v[200:203], v[148:151]\n"
      "s_or_b64 s[34:35], s[34:35], vcc\n"
      "v_permlane16_swap_b32 v232, v233\n"
      "v_mfma_f32_16x16x32_bf16 v[148:151], a[196:199], v[208:211], v[148:151]\n"
      "s_sub_u32 s28, s27, 3\n"
      "v_pk_mul_f16 v229, v229, v233 op_sel_hi:[1,0]\n"
      "v_pk_mul_f16 v228, v228, v233 op_sel_hi:[1,0]\n"
      "v_mfma_f32_16x16x32_bf16 v[204:207], a[164:167], v[200:203], v[204:207]\n"
      "s_cselect_b64 s[54:55], s[12:13], s[22:23]\n"
      "v_pk_mul_f16 v34, v34, v233 op_sel_hi:[1,0]\n"
      "v_pk_mul_f16 v230, v230, v233 op_sel_hi:[1,0]\n"
      "v_mfma_f32_16x16x32_bf16 v[204:207], a[200:203], v[208:211], v[204:207]\n"
      "s_max_i32 m0, s28, 0\n"
      "v_pk_mul_f16 v231, v231, v233 op_sel_hi:[1,0]\n"
      "v_pk_mul_f16 v35, v35, v233 op_sel_hi:[1,0]\n"
      "v_mfma_f32_16x16x32_bf16 v[136:139], v[200:203], a[168:171], v[136:139]\n"
      "s_movrels_b32 s9, s80\n"
      "v_mul_f16 v232, s7, v232\n"
      "v_pk_mul_f16 v188, v188, v232 op_sel_hi:[1,0]\n"
      "v_mfma_f32_16x16x32_bf16 v[136:139], v[208:211], a[204:207], v[136:139]\n"
      "ds_read_b128 v[172:175], v119 offset:6144\n"
      "v_pk_mul_f16 v190, v190, v232 op_sel_hi:[1,0]\n"
      "v_mfma_f32_16x16x32_bf16 v[212:215], v[200:203], a[172:175], v[212:215]\n"
      "s_lshr_b32 s8, s9, 8\n"
      "v_pk_mul_f16 v32, v32, v232 op_sel_hi:[1,0]\n"
      "v_pk_mul_f16 v189, v189, v232 op_sel_hi:[1,0]\n"
      "v_mfma_f32_16x16x32_bf16 v[212:215], v[208:211], a[208:211], v[212:215]\n"
      "s_and_b32 s9, s9, 0xff\n"
      "v_pk_mul_f16 v33, v33, v232 op_sel_hi:[1,0]\n"
      "v_pk_mul_f16 v191, v191, v232 op_sel_hi:[1,0]\n"
      "v_mfma_f32_16x16x32_bf16 v[160:163], v[200:203], a[176:179], v[160:163]\n"
      "s_mul_i32 s28, s8, 1373568\n"
      "v_cvt_pk_bf16_f32 v154, v136, v137\n"
      "v_cvt_pk_bf16_f32 v155, v138, v139\n"
      "v_mfma_f32_16x16x32_bf16 v[160:163], v[208:211], a[212:215], v[160:163]\n"
      "s_mul_i32 s29, s9, 2688\n"
      "v_cvt_pk_bf16_f32 v235, v236, v237\n"
      "v_cvt_pk_bf16_f32 v236, v212, v213\n"
      "v_mfma_f32_16x16x32_bf16 v[136:139], a[0:3], v[140:143], 0\n"
      "s_add_u32 s28, s28, s29\n"
      "v_cvt_pk_bf16_f32 v184, v184, v185\n"
      "v_cvt_pk_bf16_f32 v237, v214, v215\n"
      "v_mfma_f32_16x16x32_bf16 v[136:139], a[36:39], v[224:227], v[136:139]\n"
      "s_waitcnt lgkmcnt(1)\n"
      "v_cvt_pk_bf16_f32 v185, v186, v187\n"
      "v_cvt_pk_bf16_f32 v186, v160, v161\n"
      "v_mfma_f32_16x16x32_bf16 v[200:203], a[4:7], v[140:143], 0\n"
      "s_cmp_eq_u32 s8, 72\n"
      "v_cvt_pk_bf16_f32 v187, v162, v163\n"
      "v_cvt_pk_f16_f32 v220, v220, v221\n"
      "v_mfma_f32_16x16x32_bf16 v[200:203], a[40:43], v[224:227], v[200:203]\n"
      "s_cselect_b32 s8, 0, 100270464\n"
      "v_cvt_pk_f16_f32 v248, v248, v249\n"
      "v_pk_mul_f16 v232, v220, v220\n"
      "v_mfma_f32_16x16x32_bf16 v[212:215], a[8:11], v[140:143], 0\n"
      "s_cmp_eq_u32 s9, 72\n"
      "v_pk_mul_f16 v233, v248, v248\n"
      "v_cvt_pk_f16_f32 v221, v222, v223\n"
      "v_mfma_f32_16x16x32_bf16 v[212:215], a[44:47], v[224:227], v[212:215]\n"
      "s_cselect_b32 s9, 0, 196224\n"
      "v_pk_fma_f16 v232, v221, v221, v232\n"
      "v_cvt_pk_f16_f32 v249, v250, v251\n"
      "v_mfma_f32_16x16x32_bf16 v[160:163], a[12:15], v[140:143], 0\n"
      "s_add_u32 s10, s8, s9\n"
      "v_pk_fma_f16 v233, v249, v249, v233\n"
      "v_cvt_pk_f16_f32 v222, v144, v145\n"
      "v_mfma_f32_16x16x32_bf16 v[160:163], a[48:51], v[224:227], v[160:163]\n"
      "s_add_u32 s28, s28, s10\n"
      "v_cvt_pk_f16_f32 v250, v148, v149\n"
      "v_pk_fma_f16 v232, v222, v222, v232\n"
      "v_mfma_f32_16x16x32_bf16 v[156:159], a[16:19], v[140:143], 0\n"
      "ds_read_b128 v[208:211], v114 offset:34816\n"
      "v_pk_fma_f16 v233, v250, v250, v233\n"
      "v_mfma_f32_16x16x32_bf16 v[156:159], a[52:55], v[224:227], v[156:159]\n"
      "s_add_u32 s54, s54, s28\n"
      "v_cvt_pk_f16_f32 v223, v146, v147\n"
      "v_pk_fma_f16 v232, v223, v223, v232\n"
      "v_mfma_f32_16x16x32_bf16 v[144:147], a[20:23], v[140:143], 0\n"
      "s_addc_u32 s55, s55, 0\n"
      "v_cvt_pk_f16_f32 v251, v150, v151\n"
      "v_pk_fma_f16 v233, v251, v251, v233\n"
      "v_mfma_f32_16x16x32_bf16 v[144:147], a[56:59], v[224:227], v[144:147]\n"
      "ds_read_b128 v[176:179], v115 offset:34816\n"
      "v_cvt_pk_f16_f32 v38, v244, v245\n"
      "v_mfma_f32_16x16x32_bf16 v[148:151], v[140:143], a[24:27], 0\n"
      "s_cmp_lg_u32 s10, s11\n"
      "v_pk_fma_f16 v232, v38, v38, v232\n"
      "v_cvt_pk_f16_f32 v40, v204, v205\n"
      "v_mfma_f32_16x16x32_bf16 v[148:151], v[224:227], a[60:63], v[148:151]\n"
      "s_cbranch_scc1 L_edge_17_%=\n"
      "L_eb_18_%=:\n"
      "v_mfma_f32_16x16x32_bf16 v[216:219], v[140:143], a[28:31], 0\n"
      "global_store_dwordx4 v116, v[252:255], s[54:55] sc1\n"
      "v_mfma_f32_16x16x32_bf16 v[216:219], v[224:227], a[64:67], v[216:219]\n"
      "ds_read_b128 v[242:245], v119 offset:12288\n"
      "v_pk_fma_f16 v233, v40, v40, v233\n"
      "v_mfma_f32_16x16x32_bf16 v[252:255], v[140:143], a[32:35], 0\n"
      "s_mov_b32 s5, s4\n"
      "v_cvt_pk_f16_f32 v39, v246, v247\n"
      "v_pk_fma_f16 v232, v39, v39, v232\n"
      "v_mfma_f32_16x16x32_bf16 v[252:255], v[224:227], a[68:71], v[252:255]\n"
      "s_nop 0\n"
      "v_cvt_pk_f16_f32 v41, v206, v207\n"
      "v_pk_fma_f16 v233, v41, v41, v233\n"
      "v_mfma_f32_16x16x32_bf16 v[136:139], a[72:75], v[168:171], v[136:139]\n"
      "s_add_u32 s4, s27, 1\n"
      "v_permlane16_swap_b32_e64 v232, v233\n"
      "v_mfma_f32_16x16x32_bf16 v[136:139], a[108:111], v[196:199], v[136:139]\n"
      "s_lshl_b32 s4, s4, 30\n"
      "v_pk_add_f16 v232, v232, v233\n"
      "v_mov_b32 v233, v232\n"
      "v_mfma_f32_16x16x32_bf16 v[200:203], a[76:79], v[168:171], v[200:203]\n"
      "s_waitcnt lgkmcnt(1)\n"
      "v_permlane32_swap_b32_e64 v232, v233\n"
      "v_mfma_f32_16x16x32_bf16 v[200:203], a[112:115], v[196:199], v[200:203]\n"
      "v_pk_add_f16 v232, v232, v233\n"
      "v_add_f16_sdwa v233, v232, v232 dst_sel:DWORD dst_unused:UNUSED_PAD src0_sel:WORD_0 src1_sel:WORD_1\n"
      "v_mfma_f32_16x16x32_bf16 v[212:215], a[80:83], v[168:171], v[212:215]\n"
      "s_and_b32 s4, s4, 0x80000000\n"
      "v_rsq_f16 v232, v233\n"
      "v_mfma_f32_16x16x32_bf16 v[212:215], a[116:119], v[196:199], v[212:215]\n"
      "ds_read_b128 v[204:207], v132 offset:1200\n"
      "v_mfma_f32_16x16x32_bf16 v[160:163], a[84:87], v[168:171], v[160:163]\n"
      "global_store_dwordx4 v116, v[172:175], s[54:55] offset:128 sc1\n"
      "v_mfma_f32_16x16x32_bf16 v[160:163], a[120:123], v[196:199], v[160:163]\n"
      "ds_read_b128 v[140:143], v133 offset:1168\n"
      "v_cmp_nle_f16 vcc, 0x1c00, v233\n"
      "v_mfma_f32_16x16x32_bf16 v[156:159], a[88:91], v[168:171], v[156:159]\n"
      "s_or_b64 s[34:35], s[34:35], vcc\n"
      "ds_read_b64 v[224:225], v132 offset:1192\n"
      "v_mfma_f32_16x16x32_bf16 v[156:159], a[124:127], v[196:199], v[156:159]\n"
      "ds_read_b64 v[226:227], v133 offset:1184\n"
      "v_cmp_nge_f16 vcc, 0x7b00, v233\n"
      "v_mfma_f32_16x16x32_bf16 v[144:147], a[92:95], v[168:171], v[144:147]\n"
      "ds_read_u16 v52, v134 offset:1168\n"
      "v_mov_b32 v233, v232\n"
      "v_mfma_f32_16x16x32_bf16 v[144:147], a[128:131], v[196:199], v[144:147]\n"
      "ds_read_u16 v60, v134 offset:1232\n"
      "v_permlane16_swap_b32 v232, v233\n"
      "v_mfma_f32_16x16x32_bf16 v[148:151], v[168:171], a[96:99], v[148:151]\n"
      "ds_read_u16 v56, v134 offset:1200\n"
      "v_pk_mul_f16 v248, v248, v233 op_sel_hi:[1,0]\n"
      "v_mfma_f32_16x16x32_bf16 v[148:151], v[196:199], a[132:135], v[148:151]\n"
      "ds_read_b128 v[172:175], v123\n"
      "v_pk_mul_f16 v250, v250, v233 op_sel_hi:[1,0]\n"
      "v_mfma_f32_16x16x32_bf16 v[216:219], v[168:171], a[100:103], v[216:219]\n"
      "v_pk_mul_f16 v251, v251, v233 op_sel_hi:[1,0]\n"
      "v_pk_mul_f16 v249, v249, v233 op_sel_hi:[1,0]\n"
      "v_mfma_f32_16x16x32_bf16 v[216:219], v[196:199], a[136:139], v[216:219]\n"
      "v_pk_mul_f16 v40, v40, v233 op_sel_hi:[1,0]\n"
      "v_pk_mul_f16 v41, v41, v233 op_sel_hi:[1,0]\n"
      "v_mfma_f32_16x16x32_bf16 v[252:255], v[168:171], a[104:107], v[252:255]\n"
      "s_waitcnt lgkmcnt(6)\n"
      "global_store_dwordx4 v116, v[242:245], s[54:55] offset:256 sc1\n"
      "v_mfma_f32_16x16x32_bf16 v[252:255], v[196:199], a[140:143], v[252:255]\n"
      "ds_read_b128 v[196:199], v123 offset:3072\n"
      "v_mul_f16 v232, s7, v232\n"
      "v_mfma_f32_16x16x32_bf16 v[136:139], a[144:147], v[208:211], v[136:139]\n"
      "ds_read_b128 v[168:171], v123 offset:9216\n"
      "v_pk_mul_f16 v220, v220, v232 op_sel_hi:[1,0]\n"
      "v_mfma_f32_16x16x32_bf16 v[136:139], a[180:183], v[176:179], v[136:139]\n"
      "ds_read_b128 v[244:247], v123 offset:6144\n"
      "v_pk_mul_f16 v221, v221, v232 op_sel_hi:[1,0]\n"
      "v_mfma_f32_16x16x32_bf16 v[200:203], a[148:151], v[208:211], v[200:203]\n"
      "v_pk_mul_f16 v222, v222, v232 op_sel_hi:[1,0]\n"
      "v_pk_mul_f16 v223, v223, v232 op_sel_hi:[1,0]\n"
      "v_mfma_f32_16x16x32_bf16 v[200:203], a[184:187], v[176:179], v[200:203]\n"
      "v_pk_mul_f16 v38, v38, v232 op_sel_hi:[1,0]\n"
      "v_pk_mul_f16 v39, v39, v232 op_sel_hi:[1,0]\n"
      "v_mfma_f32_16x16x32_bf16 v[212:215], a[152:155], v[208:211], v[212:215]\n"
      "v_cvt_pk_f16_f32 v136, v136, v137\n"
      "v_pk_mul_f16 v233, v136, v136\n"
      "v_mfma_f32_16x16x32_bf16 v[212:215], a[188:191], v[176:179], v[212:215]\n"
      "s_nop 0\n"
      "v_cvt_pk_f16_f32 v137, v138, v139\n"
      "v_pk_fma_f16 v233, v137, v137, v233\n"
      "v_mfma_f32_16x16x32_bf16 v[160:163], a[156:159], v[208:211], v[160:163]\n"
      "s_or_b64 s[34:35], s[34:35], vcc\n"
      "v_cvt_pk_f16_f32 v138, v200, v201\n"
      "v_pk_fma_f16 v233, v138, v138, v233\n"
      "v_mfma_f32_16x16x32_bf16 v[160:163], a[192:195], v[176:179], v[160:163]\n"
      "v_cvt_pk_f16_f32 v139, v202, v203\n"
      "v_pk_fma_f16 v233, v139, v139, v233\n"
      "v_mfma_f32_16x16x32_bf16 v[156:159], a[160:163], v[208:211], v[156:159]\n"
      "s_waitcnt lgkmcnt(2)\n"
      "v_cvt_pk_f16_f32 v44, v212, v213\n"
      "v_pk_fma_f16 v233, v44, v44, v233\n"
      "v_mfma_f32_16x16x32_bf16 v[156:159], a[196:199], v[176:179], v[156:159]\n"
      "s_nop 0\n"
      "v_cvt_pk_f16_f32 v160, v160, v161\n"
      "v_pk_mul_f16 v232, v160, v160\n"
      "v_mfma_f32_16x16x32_bf16 v[144:147], a[164:167], v[208:211], v[144:147]\n"
      "v_cvt_pk_f16_f32 v161, v162, v163\n"
      "v_pk_fma_f16 v232, v161, v161, v232\n"
      "v_mfma_f32_16x16x32_bf16 v[144:147], a[200:203], v[176:179], v[144:147]\n"
      "s_nop 0\n"
      "v_cvt_pk_f16_f32 v162, v156, v157\n"
      "v_pk_fma_f16 v232, v162, v162, v232\n"
      "v_mfma_f32_16x16x32_bf16 v[148:151], v[208:211], a[168:171], v[148:151]\n"
      "ds_read_b128 v[200:203], v123 offset:12288\n"
      "v_cvt_pk_f16_f32 v163, v158, v159\n"
      "v_mfma_f32_16x16x32_bf16 v[148:151], v[176:179], a[204:207], v[148:151]\n"
      "s_waitcnt lgkmcnt(1)\n"
      "v_pk_fma_f16 v232, v163, v163, v232\n"
      "v_cvt_pk_f16_f32 v46, v144, v145\n"
      "v_mfma_f32_16x16x32_bf16 v[216:219], v[208:211], a[172:175], v[216:219]\n"
      "ds_read_b128 v[156:159], v123 offset:15360\n"
      "v_pk_fma_f16 v232, v46, v46, v232\n"
      "v_mfma_f32_16x16x32_bf16 v[216:219], v[176:179], a[208:211], v[216:219]\n"
      "v_cvt_pk_f16_f32 v45, v214, v215\n"
      "v_pk_fma_f16 v233, v45, v45, v233\n"
      "v_mfma_f32_16x16x32_bf16 v[252:255], v[208:211], a[176:179], v[252:255]\n"
      "v_cvt_pk_f16_f32 v47, v146, v147\n"
      "v_pk_fma_f16 v232, v47, v47, v232\n"
      "v_mfma_f32_16x16x32_bf16 v[252:255], v[176:179], a[212:215], v[252:255]\n"
      "s_nop 0\n"
      "v_permlane16_swap_b32 v233, v232\n"
      "v_mfma_f32_16x16x32_bf16 v[144:147], a[216:219], v[172:175], 0\n"
      "v_pk_add_f16 v233, v233, v232\n"
      "v_mov_b32 v232, v233\n"
      "v_mfma_f32_16x16x32_bf16 v[144:147], a[228:231], v[196:199], v[144:147]\n"
      "s_nop 0\n"
      "v_permlane32_swap_b32 v233, v232\n"
      "v_mfma_f32_16x16x32_bf16 v[208:211], a[220:223], v[172:175], 0\n"
      "v_pk_add_f16 v233, v233, v232\n"
      "v_add_f16_sdwa v232, v233, v233 dst_sel:DWORD dst_unused:UNUSED_PAD src0_sel:WORD_0 src1_sel:WORD_1\n"
      "v_mfma_f32_16x16x32_bf16 v[208:211], a[232:235], v[196:199], v[208:211]\n"
      "v_rsq_f16 v233, v232\n"
      "v_mfma_f32_16x16x32_bf16 v[176:179], a[224:227], v[172:175], 0\n"
      "v_cmp_nle_f16 vcc, 0x1c00, v232\n"
      "v_mfma_f32_16x16x32_bf16 v[176:179], a[236:239], v[196:199], v[176:179]\n"
      "s_or_b64 s[34:35], s[34:35], vcc\n"
      "v_cmp_nge_f16 vcc, 0x7b00, v232\n"
      "v_mov_b32_e64 v232, v233\n"
      "v_mfma_f32_16x16x32_bf16 v[144:147], a[240:243], v[244:247], v[144:147]\n"
      "s_or_b64 s[34:35], s[34:35], vcc\n"
      "v_permlane16_swap_b32 v233, v232\n"
      "v_mfma_f32_16x16x32_bf16 v[144:147], a[252:255], v[168:171], v[144:147]\n"
      "s_or_b64 vcc, s[48:49], s[50:51]\n"
      "v_pk_mul_f16 v160, v160, v232 op_sel_hi:[1,0]\n"
      "v_pk_mul_f16 v161, v161, v232 op_sel_hi:[1,0]\n"
      "v_mfma_f32_16x16x32_bf16 v[208:211], a[244:247], v[244:247], v[208:211]\n"
      "v_pk_mul_f16 v162, v162, v232 op_sel_hi:[1,0]\n"
      "v_pk_mul_f16 v46, v46, v232 op_sel_hi:[1,0]\n"
      "v_mfma_f32_16x16x32_bf16 v[208:211], v[0:3], v[168:171], v[208:211]\n"
      "v_add_u32 v132, 2336, v132\n"
      "v_mfma_f32_16x16x32_bf16 v[176:179], a[248:251], v[244:247], v[176:179]\n"
      "s_waitcnt lgkmcnt(0)\n"
      "v_pk_mul_f16 v163, v163, v232 op_sel_hi:[1,0]\n"
      "v_pk_mul_f16 v47, v47, v232 op_sel_hi:[1,0]\n"
      "v_mfma_f32_16x16x32_bf16 v[176:179], v[4:7], v[168:171], v[176:179]\n"
      "v_add_u32 v133, 2336, v133\n"
      "v_mul_f16 v233, s7, v233\n"
      "v_mfma_f32_16x16x32_bf16 v[144:147], v[8:11], v[200:203], v[144:147]\n"
      "s_or_b64 vcc, vcc, s[34:35]\n"
      "v_pk_mul_f16 v136, v136, v233 op_sel_hi:[1,0]\n"
      "v_add_u32 v134, 2336, v134\n"
      "v_mfma_f32_16x16x32_bf16 v[144:147], v[20:23], v[156:159], v[144:147]\n"
      "s_waitcnt vmcnt(3)\n"
      "v_pk_mul_f16 v137, v137, v233 op_sel_hi:[1,0]\n"
      "v_pk_mul_f16 v45, v45, v233 op_sel_hi:[1,0]\n"
      "v_mfma_f32_16x16x32_bf16 v[208:211], v[12:15], v[200:203], v[208:211]\n"
      "ds_read_b32 v232, v122\n"
      "v_pk_mul_f16 v139, v139, v233 op_sel_hi:[1,0]\n"
      "v_mfma_f32_16x16x32_bf16 v[208:211], v[24:27], v[156:159], v[208:211]\n"
      "ds_read_b128 v[168:171], v119 offset:36864\n"
      "v_pk_mul_f16 v44, v44, v233 op_sel_hi:[1,0]\n"
      "v_mfma_f32_16x16x32_bf16 v[176:179], v[16:19], v[200:203], v[176:179]\n"
      "v_cvt_pk_bf16_f32 v51, v150, v151\n"
      "v_pk_mul_f16 v138, v138, v233 op_sel_hi:[1,0]\n"
      "v_mfma_f32_16x16x32_bf16 v[176:179], v[28:31], v[156:159], v[176:179]\n"
      "v_cvt_pk_bf16_f32 v55, v218, v219\n"
      "v_cvt_pk_bf16_f32 v50, v148, v149\n"
      "v_mfma_f32_16x16x32_f16 v[148:151], v[228:231], v[188:191], v[78:81]\n"
      "v_fma_f32 v233, v144, v144, s39\n"
      "v_fmac_f32 v233, v145, v145\n"
      "v_mfma_f32_16x16x32_f16 v[148:151], v[34:37], v[32:35], v[148:151]\n"
      "v_cvt_pk_bf16_f32 v54, v216, v217\n"
      "v_fmac_f32 v233, v146, v146\n"
      "v_mfma_f32_16x16x32_f16 v[196:199], v[248:251], v[188:191], v[82:85]\n"
      "v_fmac_f32 v233, v147, v147\n"
      "v_cvt_pk_bf16_f32 v59, v254, v255\n"
      "v_mfma_f32_16x16x32_f16 v[196:199], v[40:43], v[32:35], v[196:199]\n"
      "v_fmac_f32 v233, v208, v208\n"
      "v_fmac_f32_e64 v233, v209, v209\n"
      "v_mfma_f32_16x16x32_f16 v[244:247], v[204:207], v[188:191], v[70:73]\n"
      "v_fmac_f32 v233, v210, v210\n"
      "v_cvt_pk_bf16_f32 v58, v252, v253\n"
      "v_mfma_f32_16x16x32_f16 v[244:247], v[140:143], v[228:231], v[244:247]\n"
      "v_fmac_f32 v233, v211, v211\n"
      "v_fmac_f32 v233, v176, v176\n"
      "v_mfma_f32_16x16x32_f16 v[244:247], v[224:227], v[32:35], v[244:247]\n"
      "s_waitcnt lgkmcnt(0)\n"
      "v_fmac_f32_e64 v233, v177, v177\n"
      "v_or3_b32 v168, v168, v169, v170\n"
      "v_and_b32_e64 v168, s40, v168\n"
      "v_fmac_f32_e64 v233, v178, v178\n"
      "v_bitop3_b32 v232, v232, v171, s40 bitop3:0xa8\n"
      "v_mfma_f32_16x16x32_f16 v[200:203], v[160:163], v[188:191], v[86:89]\n"
      "v_fmac_f32 v233, v179, v179\n"
      "v_or3_b32 v168, v168, v232, s4\n"
      "v_mfma_f32_16x16x32_f16 v[200:203], v[46:49], v[32:35], v[200:203]\n"
      "ds_write_b32 v129, v168 offset:2048\n"
      "s_cbranch_scc1 L_gen_19_%=\n"
      "L_fast_22_%=:\n"
      "v_mfma_f32_16x16x32_f16 v[168:171], v[228:231], v[220:223], v[90:93]\n"
      "ds_read_b128 v[172:175], v120\n"
      "v_exp_f32_e64 v196, v196\n"
      "v_mfma_f32_16x16x32_f16 v[168:171], v[34:37], v[38:41], v[168:171]\n"
      "ds_read_b128 v[212:215], v123 offset:4096\n"
      "v_exp_f32 v148, v148\n"
      "v_mfma_f32_16x16x32_f16 v[188:191], v[248:251], v[220:223], v[94:97]\n"
      "ds_read_b128 v[216:219], v123 offset:1024\n"
      "v_exp_f32 v197, v197\n"
      "v_mfma_f32_16x16x32_f16 v[188:191], v[40:43], v[38:41], v[188:191]\n"
      "ds_read_b128 v[156:159], v128 offset:2048\n"
      "v_exp_f32 v198, v198\n"
      "v_exp_f32 v199, v199\n"
      "v_mfma_f32_16x16x32_f16 v[192:195], v[204:207], v[220:223], v[72:75]\n"
      "v_exp_f32 v149, v149\n"
      "v_mfma_f32_16x16x32_f16 v[192:195], v[140:143], v[248:251], v[192:195]\n"
      "v_exp_f32 v151, v151\n"
      "v_mfma_f32_16x16x32_f16 v[192:195], v[224:227], v[38:41], v[192:195]\n"
      "ds_read_b128 v[252:255], v123 offset:7168\n"
      "v_exp_f32 v150, v150\n"
      "v_cvt_pk_bf16_f32 v148, v148, v149\n"
      "v_cvt_pk_bf16_f32 v149, v150, v151\n"
      "v_exp_f32 v168, v168\n"
      "v_cvt_pk_bf16_f32 v151, v198, v199\n"
      "v_exp_f32 v200, v200\n"
      "v_cvt_pk_bf16_f32 v150, v196, v197\n"
      "v_mfma_f32_16x16x32_f16 v[240:243], v[160:163], v[220:223], v[98:101]\n"
      "ds_read_b128 v[220:223], v123 offset:10240\n"
      "s_waitcnt lgkmcnt(3)\n"
      "v_mfma_f32_16x16x32_f16 v[240:243], v[46:49], v[38:41], v[240:243]\n"
      "global_store_dwordx4 v117, v[172:175], s[54:55] sc1\n"
      "v_exp_f32_e64 v201, v201\n"
      "v_mfma_f32_16x16x32_bf16 v[196:199], a[216:219], v[216:219], 0\n"
      "v_exp_f32 v202, v202\n"
      "v_mfma_f32_16x16x32_bf16 v[196:199], a[228:231], v[212:215], v[196:199]\n"
      "s_waitcnt lgkmcnt(2)\n"
      "v_bitop3_b32 v158, v158, v159, s4 bitop3:0x7e\n"
      "v_bitop3_b32 v156, v156, v157, s4 bitop3:0x7e\n"
      "v_mfma_f32_16x16x32_bf16 v[172:175], a[220:223], v[216:219], 0\n"
      "v_exp_f32 v203, v203\n"
      "v_mfma_f32_16x16x32_bf16 v[172:175], a[232:235], v[212:215], v[172:175]\n"
      "v_exp_f32 v244, v244\n"
      "v_mfma_f32_16x16x32_bf16 v[180:183], a[224:227], v[216:219], 0\n"
      "v_cvt_pk_bf16_f32 v62, v200, v201\n"
      "v_cvt_pk_bf16_f32 v63, v202, v203\n"
      "v_mfma_f32_16x16x32_bf16 v[180:183], a[236:239], v[212:215], v[180:183]\n"
      "ds_read_b128 v[200:203], v123 offset:13312\n"
      "s_waitcnt lgkmcnt(1)\n"
      "v_mfma_f32_16x16x32_bf16 v[196:199], a[240:243], v[252:255], v[196:199]\n"
      "v_or_b32_e64 v156, v156, v158\n"
      "v_cmp_ne_u32 vcc, 0, v156\n"
      "v_mfma_f32_16x16x32_bf16 v[196:199], a[252:255], v[220:223], v[196:199]\n"
      "ds_read_b128 v[212:215], v123 offset:16384\n"
      "v_exp_f32 v171, v171\n"
      "v_mfma_f32_16x16x32_bf16 v[172:175], a[244:247], v[252:255], v[172:175]\n"
      "s_mov_b64 s[48:49], vcc\n"
      "v_exp_f32_e64 v169, v169\n"
      "v_mfma_f32_16x16x32_bf16 v[172:175], v[0:3], v[220:223], v[172:175]\n"
      "ds_read_b128 v[216:219], v121\n"
      "v_exp_f32 v188, v188\n"
      "v_exp_f32 v170, v170\n"
      "v_cvt_pk_bf16_f32 v168, v168, v169\n"
      "v_cvt_pk_bf16_f32 v169, v170, v171\n"
      "v_mfma_f32_16x16x32_bf16 v[180:183], a[248:251], v[252:255], v[180:183]\n"
      "v_exp_f32 v190, v190\n"
      "v_mfma_f32_16x16x32_bf16 v[180:183], v[4:7], v[220:223], v[180:183]\n"
      "s_cbranch_vccnz L_flg_23_%=\n"
      "L_fb_24_%=:\n"
      "v_exp_f32 v189, v189\n"
      "v_exp_f32 v240, v240\n"
      "v_exp_f32 v191, v191\n"
      "v_cvt_pk_bf16_f32 v170, v188, v189\n"
      "s_waitcnt lgkmcnt(1)\n"
      "v_mfma_f32_16x16x32_bf16 v[196:199], v[8:11], v[200:203], v[196:199]\n"
      "ds_read_b128 v[156:159], v123 offset:5120\n"
      "v_cvt_pk_bf16_f32 v171, v190, v191\n"
      "v_mfma_f32_16x16x32_bf16 v[196:199], v[20:23], v[212:215], v[196:199]\n"
      "ds_read_b128 v[220:223], v123 offset:2048\n"
      "v_cvt_pk_bf16_f32 v64, v244, 0\n"
      "v_mfma_f32_16x16x32_bf16 v[172:175], v[12:15], v[200:203], v[172:175]\n"
      "s_waitcnt lgkmcnt(2)\n"
      "v_exp_f32 v241, v241\n"
      "v_mfma_f32_16x16x32_bf16 v[172:175], v[24:27], v[212:215], v[172:175]\n"
      "s_mov_b32 exec_hi, 0\n"
      "global_store_dwordx4 v118, v[216:219], s[54:55] sc1\n"
      "s_mov_b32 exec_hi, -1\n"
      "v_mfma_f32_16x16x32_bf16 v[180:183], v[16:19], v[200:203], v[180:183]\n"
      "v_exp_f32_e64 v242, v242\n"
      "v_mfma_f32_16x16x32_bf16 v[180:183], v[28:31], v[212:215], v[180:183]\n"
      "v_exp_f32 v192, v192\n"
      "v_mfma_f32_16x16x32_bf16 v[200:203], v[66:69], v[148:151], 0\n"
      "v_exp_f32 v243, v243\n"
      "v_mfma_f32_16x16x32_bf16 v[200:203], v[66:69], v[62:65], v[200:203]\n"
      "v_fma_f32 v232, v196, v196, s39\n"
      "v_fmac_f32 v232, v197, v197\n"
      "v_mfma_f32_16x16x32_bf16 v[252:255], v[152:155], v[148:151], 0\n"
      "v_fmac_f32 v232, v198, v198\n"
      "v_fmac_f32_e64 v232, v199, v199\n"
      "v_mfma_f32_16x16x32_bf16 v[252:255], v[50:53], v[62:65], v[252:255]\n"
      "v_fmac_f32 v232, v172, v172\n"
      "v_fmac_f32 v232, v173, v173\n"
      "v_mfma_f32_16x16x32_bf16 v[188:191], v[234:237], v[148:151], 0\n"
      "v_fmac_f32 v232, v174, v174\n"
      "v_fmac_f32 v232, v175, v175\n"
      "v_mfma_f32_16x16x32_bf16 v[188:191], v[54:57], v[62:65], v[188:191]\n"
      "v_fmac_f32 v232, v180, v180\n"
      "v_fmac_f32 v232, v181, v181\n"
      "v_mfma_f32_16x16x32_bf16 v[216:219], v[184:187], v[148:151], 0\n"
      "v_fmac_f32 v232, v182, v182\n"
      "v_fmac_f32 v232, v183, v183\n"
      "v_mfma_f32_16x16x32_bf16 v[216:219], v[58:61], v[62:65], v[216:219]\n"
      "v_cvt_pk_bf16_f32 v64, v192, 0\n"
      "v_cvt_pk_bf16_f32 v62, v240, v241\n"
      "v_mfma_f32_16x16x32_f16 v[148:151], v[228:231], v[136:139], v[102:105]\n"
      "s_add_u32 s28, s27, 2\n"
      "v_cvt_pk_bf16_f32 v63, v242, v243\n"
      "v_cndmask_b32 v245, v245, v193, s[42:43]\n"
      "v_mfma_f32_16x16x32_f16 v[148:151], v[34:37], v[44:47], v[148:151]\n"
      "v_rcp_f32 v200, v200\n"
      "v_mfma_f32_16x16x32_f16 v[228:231], v[248:251], v[136:139], v[106:109]\n"
      "v_mul_f32 v253, v200, v253\n"
      "v_mul_f32 v252, v200, v252\n"
      "v_mfma_f32_16x16x32_f16 v[228:231], v[40:43], v[44:47], v[228:231]\n"
      "s_waitcnt lgkmcnt(0)\n"
      "v_mul_f32 v255, v200, v255\n"
      "v_cvt_pk_bf16_f32 v252, v252, v253\n"
      "v_mfma_f32_16x16x32_f16 v[212:215], v[204:207], v[136:139], v[74:77]\n"
      "ds_read_b128 v[248:251], v123 offset:8192\n"
      "v_mul_f32 v254, v200, v254\n"
      "v_mfma_f32_16x16x32_f16 v[212:215], v[140:143], v[160:163], v[212:215]\n"
      "v_cvt_pk_bf16_f32 v253, v254, v255\n"
      "v_mul_f32 v188, v200, v188\n"
      "v_mfma_f32_16x16x32_f16 v[212:215], v[224:227], v[44:47], v[212:215]\n"
      "v_mul_f32 v190, v200, v190\n"
      "v_mul_f32 v191, v200, v191\n"
      "v_mfma_f32_16x16x32_f16 v[192:195], v[160:163], v[136:139], v[110:113]\n"
      "ds_read_b128 v[160:163], v123 offset:11264\n"
      "v_mul_f32 v189, v200, v189\n"
      "v_mfma_f32_16x16x32_f16 v[192:195], v[46:49], v[44:47], v[192:195]\n"
      "s_min_u32 m0, s28, s3\n"
      "v_cvt_pk_bf16_f32 v255, v190, v191\n"
      "v_cvt_pk_bf16_f32 v254, v188, v189\n"
      "v_mfma_f32_16x16x32_f16 v[136:139], v[140:143], v[204:207], v[76:79]\n"
      "ds_write_b128 v124, v[252:255] offset:18432\n"
      "v_mfma_f32_16x16x32_bf16 v[204:207], v[66:69], v[168:171], 0\n"
      "ds_read_b128 v[252:255], v123 offset:14336\n"
      "v_mul_f32 v216, v200, v216\n"
      "v_mfma_f32_16x16x32_bf16 v[204:207], v[66:69], v[62:65], v[204:207]\n"
      "v_mul_f32 v218, v200, v218\n"
      "v_mul_f32 v217, v200, v217\n"
      "v_exp_f32_e64 v228, v228\n"
      "v_mfma_f32_16x16x16_f16 v[136:139], v[226:227], v[224:225], v[136:139]\n"
      "s_movrels_b32 s9, s80\n"
      "v_mul_f32_e64 v219, v200, v219\n"
      "v_cvt_pk_bf16_f32 v216, v216, v217\n"
      "v_exp_f32_e64 v231, v231\n"
      "v_mfma_f32_16x16x32_bf16 v[224:227], v[152:155], v[168:171], 0\n"
      "v_cndmask_b32 v245, v245, v213, s[44:45]\n"
      "v_cvt_pk_bf16_f32 v217, v218, v219\n"
      "v_mfma_f32_16x16x32_bf16 v[224:227], v[50:53], v[62:65], v[224:227]\n"
      "s_lshr_b32 s8, s9, 8\n"
      "v_cndmask_b32 v245, v245, v137, s[46:47]\n"
      "v_exp_f32 v245, v245\n"
      "v_mfma_f32_16x16x32_bf16 v[188:191], v[234:237], v[168:171], 0\n"
      "s_waitcnt lgkmcnt(2)\n"
      "v_exp_f32 v150, v150\n"
      "v_cvt_pk_bf16_f32 v245, v245, v245\n"
      "v_mfma_f32_16x16x32_bf16 v[188:191], v[54:57], v[62:65], v[188:191]\n"
      "ds_write_b16 v130, v245\n"
      "v_mfma_f32_16x16x32_bf16 v[244:247], v[184:187], v[168:171], 0\n"
      "ds_read_b128 v[140:143], v131\n"
      "s_and_b32 s9, s9, 0xff\n"
      "v_mfma_f32_16x16x32_bf16 v[244:247], v[58:61], v[62:65], v[244:247]\n"
      "ds_read_b128 v[168:171], v131 offset:64\n"
      "v_exp_f32_e64 v193, v193\n"
      "v_mfma_f32_16x16x32_bf16 v[200:203], a[216:219], v[220:223], 0\n"
      "s_mul_i32 s10, s8, 73\n"
      "v_exp_f32 v148, v148\n"
      "v_mfma_f32_16x16x32_bf16 v[200:203], a[228:231], v[156:159], v[200:203]\n"
      "s_add_u32 s10, s10, s9\n"
      "v_exp_f32_e64 v192, v192\n"
      "v_mfma_f32_16x16x32_bf16 v[136:139], a[220:223], v[220:223], 0\n"
      "s_mul_i32 s28, s10, 4802\n"
      "v_exp_f32 v151, v151\n"
      "v_mfma_f32_16x16x32_bf16 v[136:139], a[232:235], v[156:159], v[136:139]\n"
      "s_add_u32 s56, s18, s28\n"
      "v_exp_f32 v149, v149\n"
      "v_mfma_f32_16x16x32_bf16 v[200:203], a[240:243], v[248:251], v[200:203]\n"
      "s_addc_u32 s57, s19, 0\n"
      "v_cvt_pk_bf16_f32 v148, v148, v149\n"
      "v_cvt_pk_bf16_f32 v149, v150, v151\n"
      "v_mfma_f32_16x16x32_bf16 v[200:203], a[252:255], v[160:163], v[200:203]\n"
      "s_and_b32 s56, s56, -16\n"
      "v_exp_f32 v229, v229\n"
      "v_mfma_f32_16x16x32_bf16 v[136:139], a[244:247], v[248:251], v[136:139]\n"
      "s_sub_u32 s58, s56, 110592\n"
      "v_exp_f32 v230, v230\n"
      "v_mfma_f32_16x16x32_bf16 v[136:139], v[0:3], v[160:163], v[136:139]\n"
      "s_waitcnt lgkmcnt(0)\n"
      "v_cvt_pk_bf16_f32 v151, v230, v231\n"
      "v_cvt_pk_bf16_f32 v150, v228, v229\n"
      "v_mfma_f32_16x16x32_bf16 v[228:231], a[224:227], v[220:223], 0\n"
      "ds_read_b128 v[240:243], v123 offset:17408\n"
      "v_cvt_pk_bf16_f32 v62, v192, v193\n"
      "v_mfma_f32_16x16x32_bf16 v[228:231], a[236:239], v[156:159], v[228:231]\n"
      "ds_read_b128 v[156:159], v115\n"
      "v_exp_f32 v194, v194\n"
      "v_mfma_f32_16x16x32_bf16 v[228:231], a[248:251], v[248:251], v[228:231]\n"
      "ds_read_b128 v[220:223], v114\n"
      "s_subb_u32 s59, s57, 0\n"
      "v_mfma_f32_16x16x32_bf16 v[228:231], v[4:7], v[160:163], v[228:231]\n"
      "s_sub_u32 s56, s56, 73728\n"
      "v_exp_f32 v195, v195\n"
      "v_mfma_f32_16x16x32_bf16 v[160:163], v[140:143], v[66:69], 0\n"
      "s_subb_u32 s57, s57, 0\n"
      "v_exp_f32_e64 v212, v212\n"
      "v_mfma_f32_16x16x32_bf16 v[160:163], v[168:171], v[66:69], v[160:163]\n"
      "s_mul_i32 s28, s8, 1373568\n"
      "v_cvt_pk_bf16_f32 v63, v194, v195\n"
      "v_cvt_pk_bf16_f32 v64, v212, 0\n"
      "v_mfma_f32_16x16x32_bf16 v[248:251], v[140:143], v[152:155], 0\n"
      "s_mul_i32 s29, s9, 2688\n"
      "v_rcp_f32_e64 v204, v204\n"
      "v_mfma_f32_16x16x32_bf16 v[248:251], v[168:171], v[50:53], v[248:251]\n"
      "s_add_u32 s28, s28, s29\n"
      "v_mul_f32 v225, v204, v225\n"
      "v_mul_f32 v224, v204, v224\n"
      "v_mfma_f32_16x16x32_bf16 v[212:215], v[66:69], v[148:151], 0\n"
      "s_waitcnt lgkmcnt(2)\n"
      "v_cvt_pk_bf16_f32 v224, v224, v225\n"
      "v_mul_f32 v227, v204, v227\n"
      "v_mfma_f32_16x16x32_bf16 v[212:215], v[66:69], v[62:65], v[212:215]\n"
      "s_cmp_eq_u32 s8, 72\n"
      "v_mul_f32 v226, v204, v226\n"
      "v_cvt_pk_bf16_f32 v225, v226, v227\n"
      "v_mfma_f32_16x16x32_bf16 v[200:203], v[8:11], v[252:255], v[200:203]\n"
      "s_cselect_b32 s8, 0, 100270464\n"
      "v_mul_f32 v190, v204, v190\n"
      "v_mul_f32 v189, v204, v189\n"
      "v_mfma_f32_16x16x32_bf16 v[200:203], v[20:23], v[240:243], v[200:203]\n"
      "v_mul_f32 v188, v204, v188\n"
      "s_waitcnt lgkmcnt(0)\n"
      "v_mfma_f32_16x16x32_bf16 v[136:139], v[12:15], v[252:255], v[136:139]\n"
      "s_cmp_eq_u32 s9, 72\n"
      "v_mul_f32_e64 v191, v204, v191\n"
      "v_cvt_pk_bf16_f32 v226, v188, v189\n"
      "v_mfma_f32_16x16x32_bf16 v[136:139], v[24:27], v[240:243], v[136:139]\n"
      "s_cselect_b32 s9, 0, 196224\n"
      "v_cvt_pk_bf16_f32 v227, v190, v191\n"
      "v_mul_f32 v245, v204, v245\n"
      "v_mfma_f32_16x16x32_bf16 v[228:231], v[16:19], v[252:255], v[228:231]\n"
      "s_add_u32 s10, s8, s9\n"
      "v_mul_f32_e64 v244, v204, v244\n"
      "v_cvt_pk_bf16_f32 v244, v244, v245\n"
      "v_mfma_f32_16x16x32_bf16 v[228:231], v[28:31], v[240:243], v[228:231]\n"
      "ds_write_b128 v124, v[224:227] offset:19456\n"
      "v_mfma_f32_16x16x32_bf16 v[252:255], v[152:155], v[148:151], 0\n"
      "ds_read_b128 v[224:227], v114 offset:6144\n"
      "v_mul_f32_e64 v246, v204, v246\n"
      "v_mfma_f32_16x16x32_bf16 v[252:255], v[50:53], v[62:65], v[252:255]\n"
      "ds_read_b128 v[188:191], v115 offset:6144\n"
      "v_mul_f32 v247, v204, v247\n"
      "v_mfma_f32_16x16x32_bf16 v[204:207], v[234:237], v[148:151], 0\n"
      "s_add_u32 s28, s28, s10\n"
      "v_cvt_pk_bf16_f32 v245, v246, v247\n"
      "v_fma_f32 v246, v200, v200, s39\n"
      "v_mfma_f32_16x16x32_bf16 v[204:207], v[54:57], v[62:65], v[204:207]\n"
      "ds_write2st64_b64 v125, v[216:217], v[244:245] offset0:36 offset1:38\n"
      "v_mfma_f32_16x16x32_bf16 v[216:219], v[184:187], v[148:151], 0\n"
      "s_add_u32 s52, s16, s28\n"
      "v_fmac_f32 v246, v201, v201\n"
      "v_fmac_f32 v246, v202, v202\n"
      "v_mfma_f32_16x16x32_bf16 v[216:219], v[58:61], v[62:65], v[216:219]\n"
      "s_addc_u32 s53, s17, 0\n"
      "v_fmac_f32 v246, v203, v203\n"
      "v_fmac_f32 v246, v136, v136\n"
      "v_mfma_f32_16x16x32_bf16 v[148:151], v[140:143], v[234:237], 0\n"
      "s_add_u32 s27, s27, 1\n"
      "v_fmac_f32 v246, v137, v137\n"
      "v_fmac_f32 v246, v138, v138\n"
      "v_mfma_f32_16x16x32_bf16 v[148:151], v[168:171], v[54:57], v[148:151]\n"
      "s_cmp_ge_u32 s27, s26\n"
      "v_fmac_f32 v246, v139, v139\n"
      "v_fmac_f32 v246, v228, v228\n"
      "v_mfma_f32_16x16x32_bf16 v[242:245], v[140:143], v[184:187], 0\n"
      "s_cselect_b32 s28, 1, 0\n"
      "v_fmac_f32 v246, v229, v229\n"
      "v_fmac_f32 v246, v230, v230\n"
      "v_mfma_f32_16x16x32_bf16 v[242:245], v[168:171], v[58:61], v[242:245]\n"
      "s_waitcnt lgkmcnt(1)\n"
      "v_fmac_f32 v246, v231, v231\n"
      "v_mov_b32 v247, v246\n"
      "v_mfma_f32_16x16x32_bf16 v[140:143], a[0:3], v[220:223], 0\n"
      "v_permlane16_swap_b32_e64 v233, v232\n"
      "v_mfma_f32_16x16x32_bf16 v[140:143], a[36:39], v[156:159], v[140:143]\n"
      "v_mov_b32_dpp v160, v160 quad_perm:[0,0,0,0] row_mask:0xf bank_mask:0xf\n"
      "v_add_f32 v233, v233, v232\n"
      "v_mfma_f32_16x16x32_bf16 v[162:165], a[4:7], v[220:223], 0\n"
      "v_permlane16_swap_b32 v246, v247\n"
      "v_mfma_f32_16x16x32_bf16 v[162:165], a[40:43], v[156:159], v[162:165]\n"
      "v_cndmask_b32 v248, v248, v148, s[42:43]\n"
      "v_add_f32 v246, v246, v247\n"
      "v_mfma_f32_16x16x32_bf16 v[148:151], a[8:11], v[220:223], 0\n"
      "s_xor_b32 s29, s10, s11\n"
      "v_permlane32_swap_b32_e64 v233, v246\n"
      "v_mfma_f32_16x16x32_bf16 v[148:151], a[44:47], v[156:159], v[148:151]\n"
      "v_cndmask_b32 v248, v248, v242, s[44:45]\n"
      "v_add_f32 v233, v233, v246\n"
      "v_mfma_f32_16x16x32_bf16 v[234:237], a[12:15], v[220:223], 0\n"
      "s_or_b32 s28, s28, s29\n"
      "v_rcp_f32 v160, v160\n"
      "v_mfma_f32_16x16x32_bf16 v[234:237], a[48:51], v[156:159], v[234:237]\n"
      "ds_write_b32 v129, v233 offset:1024\n"
      "v_mfma_f32_16x16x32_bf16 v[168:171], a[16:19], v[220:223], 0\n"
      "v_mul_f32 v248, v160, v248\n"
      "v_cvt_pk_bf16_f32 v248, v248, v248\n"
      "v_mfma_f32_16x16x32_bf16 v[168:171], a[52:55], v[156:159], v[168:171]\n"
      "ds_write_b16 v135, v248 offset:16\n"
      "v_mfma_f32_16x16x32_bf16 v[244:247], a[20:23], v[220:223], 0\n"
      "v_mfma_f32_16x16x32_bf16 v[244:247], a[56:59], v[156:159], v[244:247]\n"
      "L_join_21_%=:\n"
      "s_cbranch_scc1 L_tail1_%=\n"
      "L_mid_%=:\n"
      "v_mfma_f32_16x16x32_bf16 v[192:195], v[220:223], a[24:27], 0\n"
      "v_rcp_f32 v212, v212\n"
      "v_mfma_f32_16x16x32_bf16 v[192:195], v[156:159], a[60:63], v[192:195]\n"
      "s_waitcnt lgkmcnt(0)\n"
      "s_barrier\n"
      "v_mfma_f32_16x16x32_bf16 v[152:155], v[220:223], a[28:31], 0\n"
      "s_add_u32 m0, s76, 18432\n"
      "v_mul_f32 v252, v212, v252\n"
      "v_mul_f32 v253, v212, v253\n"
      "v_mfma_f32_16x16x32_bf16 v[152:155], v[156:159], a[64:67], v[152:155]\n"
      "v_mul_f32 v254, v212, v254\n"
      "v_cvt_pk_bf16_f32 v252, v252, v253\n"
      "v_mfma_f32_16x16x32_bf16 v[240:243], v[220:223], a[32:35], 0\n"
      "ds_read_b128 v[220:223], v128 offset:1024\n"
      "v_mul_f32 v255, v212, v255\n"
      "v_mfma_f32_16x16x32_bf16 v[240:243], v[156:159], a[68:71], v[240:243]\n"
      "global_load_lds_dwordx4 v116, s[52:53]\n"
      "v_mfma_f32_16x16x32_bf16 v[140:143], a[72:75], v[224:227], v[140:143]\n"
      "ds_read_b128 v[248:251], v114 offset:12288\n"
      "v_mul_f32 v204, v212, v204\n"
      "v_mfma_f32_16x16x32_bf16 v[140:143], a[108:111], v[188:191], v[140:143]\n"
      "s_add_u32 m0, s76, 24448\n"
      "v_mul_f32 v205, v212, v205\n"
      "v_cvt_pk_bf16_f32 v253, v254, v255\n"
      "v_mfma_f32_16x16x32_bf16 v[162:165], a[76:79], v[224:227], v[162:165]\n"
      "v_mul_f32_e64 v206, v212, v206\n"
      "v_cvt_pk_bf16_f32 v254, v204, v205\n"
      "v_mfma_f32_16x16x32_bf16 v[162:165], a[112:115], v[188:191], v[162:165]\n"
      "v_mul_f32_e64 v207, v212, v207\n"
      "v_cvt_pk_bf16_f32 v255, v206, v207\n"
      "v_mfma_f32_16x16x32_bf16 v[148:151], a[80:83], v[224:227], v[148:151]\n"
      "ds_read_b128 v[204:207], v115 offset:12288\n"
      "v_mul_f32_e64 v216, v212, v216\n"
      "v_mfma_f32_16x16x32_bf16 v[148:151], a[116:119], v[188:191], v[148:151]\n"
      "ds_write_b128 v124, v[252:255] offset:20480\n"
      "v_mfma_f32_16x16x32_bf16 v[234:237], a[84:87], v[224:227], v[234:237]\n"
      "v_mul_f32 v217, v212, v217\n"
      "v_cvt_pk_bf16_f32 v216, v216, v217\n"
      "v_mfma_f32_16x16x32_bf16 v[234:237], a[120:123], v[188:191], v[234:237]\n"
      "s_waitcnt lgkmcnt(3)\n"
      "v_mul_f32 v219, v212, v219\n"
      "v_mul_f32 v218, v212, v218\n"
      "v_mfma_f32_16x16x32_bf16 v[168:171], a[88:91], v[224:227], v[168:171]\n"
      "v_add_f32 v220, |v220|, |v221|\n"
      "v_cvt_pk_bf16_f32 v217, v218, v219\n"
      "v_mfma_f32_16x16x32_bf16 v[168:171], a[124:127], v[188:191], v[168:171]\n"
      "ds_write_b64 v125, v[216:217] offset:20480\n"
      "v_mfma_f32_16x16x32_bf16 v[244:247], a[92:95], v[224:227], v[244:247]\n"
      "global_load_lds_dwordx4 v116, s[52:53] offset:128\n"
      "v_mfma_f32_16x16x32_bf16 v[244:247], a[128:131], v[188:191], v[244:247]\n"
      "s_waitcnt lgkmcnt(2)\n"
      "v_add_f32 v222, |v222|, |v223|\n"
      "v_add_f32 v220, v220, v222\n"
      "v_mfma_f32_16x16x32_bf16 v[192:195], v[224:227], a[96:99], v[192:195]\n"
      "v_rsq_f32 v220, v220\n"
      "v_mfma_f32_16x16x32_bf16 v[192:195], v[188:191], a[132:135], v[192:195]\n"
      "v_mul_f32 v220, s41, v220\n"
      "v_mfma_f32_16x16x32_bf16 v[152:155], v[224:227], a[100:103], v[152:155]\n"
      "ds_read_b128 v[252:255], v114 offset:2048\n"
      "v_mov_b32 v233, v220\n"
      "v_mfma_f32_16x16x32_bf16 v[152:155], v[188:191], a[136:139], v[152:155]\n"
      "s_nop 0\n"
      "v_permlane32_swap_b32_e64 v220, v233\n"
      "v_mfma_f32_16x16x32_bf16 v[240:243], v[224:227], a[104:107], v[240:243]\n"
      "ds_read_b128 v[224:227], v115 offset:2048\n"
      "v_mov_b32 v160, v220\n"
      "v_mfma_f32_16x16x32_bf16 v[240:243], v[188:191], a[140:143], v[240:243]\n"
      "s_add_u32 m0, s76, 30464\n"
      "v_permlane16_swap_b32 v220, v160\n"
      "v_mfma_f32_16x16x32_bf16 v[140:143], a[144:147], v[248:251], v[140:143]\n"
      "v_mul_f32 v145, v220, v145\n"
      "v_mul_f32 v144, v220, v144\n"
      "v_mfma_f32_16x16x32_bf16 v[140:143], a[180:183], v[204:207], v[140:143]\n"
      "v_cvt_pk_bf16_f32 v144, v144, v145\n"
      "v_mul_f32 v146, v220, v146\n"
      "v_mfma_f32_16x16x32_bf16 v[162:165], a[148:151], v[248:251], v[162:165]\n"
      "v_mul_f32 v147, v220, v147\n"
      "v_cvt_pk_bf16_f32 v145, v146, v147\n"
      "v_mfma_f32_16x16x32_bf16 v[162:165], a[184:187], v[204:207], v[162:165]\n"
      "global_load_lds_dwordx4 v116, s[52:53] offset:256\n"
      "v_mfma_f32_16x16x32_bf16 v[148:151], a[152:155], v[248:251], v[148:151]\n"
      "v_mul_f32 v209, v220, v209\n"
      "v_mul_f32 v208, v220, v208\n"
      "v_mfma_f32_16x16x32_bf16 v[148:151], a[188:191], v[204:207], v[148:151]\n"
      "v_cvt_pk_bf16_f32 v146, v208, v209\n"
      "v_mul_f32 v210, v220, v210\n"
      "v_mfma_f32_16x16x32_bf16 v[234:237], a[156:159], v[248:251], v[234:237]\n"
      "s_waitcnt lgkmcnt(0)\n"
      "v_mul_f32 v211, v220, v211\n"
      "v_mul_f32 v177, v220, v177\n"
      "v_mfma_f32_16x16x32_bf16 v[234:237], a[192:195], v[204:207], v[234:237]\n"
      "v_mul_f32_e64 v176, v220, v176\n"
      "v_cvt_pk_bf16_f32 v147, v210, v211\n"
      "v_mfma_f32_16x16x32_bf16 v[168:171], a[160:163], v[248:251], v[168:171]\n"
      "s_add_u32 m0, s77, 18432\n"
      "v_mul_f32 v178, v220, v178\n"
      "v_mul_f32 v179, v220, v179\n"
      "v_mfma_f32_16x16x32_bf16 v[168:171], a[196:199], v[204:207], v[168:171]\n"
      "ds_write_b128 v126, v[144:147]\n"
      "v_mfma_f32_16x16x32_bf16 v[244:247], a[164:167], v[248:251], v[244:247]\n"
      "v_mul_f32 v198, v160, v198\n"
      "v_cvt_pk_bf16_f32 v176, v176, v177\n"
      "v_mfma_f32_16x16x32_bf16 v[244:247], a[200:203], v[204:207], v[244:247]\n"
      "v_mul_f32 v197, v160, v197\n"
      "v_mul_f32 v196, v160, v196\n"
      "v_mfma_f32_16x16x32_bf16 v[192:195], v[248:251], a[168:171], v[192:195]\n"
      "v_mul_f32 v199, v160, v199\n"
      "v_cvt_pk_bf16_f32 v196, v196, v197\n"
      "v_mfma_f32_16x16x32_bf16 v[192:195], v[204:207], a[204:207], v[192:195]\n"
      "global_load_lds_dwordx4 v117, s[52:53]\n"
      "v_mfma_f32_16x16x32_bf16 v[152:155], v[248:251], a[172:175], v[152:155]\n"
      "v_mul_f32_e64 v173, v160, v173\n"
      "v_cvt_pk_bf16_f32 v177, v178, v179\n"
      "v_mfma_f32_16x16x32_bf16 v[152:155], v[204:207], a[208:211], v[152:155]\n"
      "ds_read_b128 v[144:147], v114 offset:8192\n"
      "v_mfma_f32_16x16x32_bf16 v[240:243], v[248:251], a[176:179], v[240:243]\n"
      "v_cvt_pk_bf16_f32 v197, v198, v199\n"
      "v_mul_f32_e64 v172, v160, v172\n"
      "v_mfma_f32_16x16x32_bf16 v[240:243], v[204:207], a[212:215], v[240:243]\n"
      "ds_read_b128 v[220:223], v115 offset:8192\n"
      "v_cvt_pk_bf16_f32 v198, v172, v173\n"
      "v_mfma_f32_16x16x32_bf16 v[204:207], a[4:7], v[252:255], 0\n"
      "v_mul_f32 v175, v160, v175\n"
      "v_mul_f32 v174, v160, v174\n"
      "v_mfma_f32_16x16x32_bf16 v[204:207], a[40:43], v[224:227], v[204:207]\n"
      "v_mul_f32_e64 v180, v160, v180\n"
      "v_cvt_pk_bf16_f32 v199, v174, v175\n"
      "v_mfma_f32_16x16x32_bf16 v[248:251], a[0:3], v[252:255], 0\n"
      "s_add_u32 m0, s78, 18432\n"
      "v_mul_f32_e64 v181, v160, v181\n"
      "v_cvt_pk_bf16_f32 v180, v180, v181\n"
      "v_mfma_f32_16x16x32_bf16 v[248:251], a[36:39], v[224:227], v[248:251]\n"
      "ds_write_b128 v126, v[196:199] offset:2048\n"
      "v_mfma_f32_16x16x32_bf16 v[196:199], a[8:11], v[252:255], 0\n"
      "v_mul_f32 v183, v160, v183\n"
      "v_mul_f32 v182, v160, v182\n"
      "v_mfma_f32_16x16x32_bf16 v[196:199], a[44:47], v[224:227], v[196:199]\n"
      "s_mov_b32 exec_hi, 0\n"
      "global_load_lds_dwordx4 v118, s[52:53]\n"
      "s_mov_b32 exec_hi, -1\n"
      "v_mfma_f32_16x16x32_bf16 v[208:211], a[12:15], v[252:255], 0\n"
      "s_add_u32 m0, s76, 115456\n"
      "v_mul_f32_e64 v201, v233, v201\n"
      "v_cvt_pk_bf16_f32 v181, v182, v183\n"
      "v_mfma_f32_16x16x32_bf16 v[208:211], a[48:51], v[224:227], v[208:211]\n"
      "ds_write2st64_b64 v127, v[176:177], v[180:181] offset0:0 offset1:4\n"
      "v_mfma_f32_16x16x32_bf16 v[212:215], a[16:19], v[252:255], 0\n"
      "v_mul_f32_e64 v200, v233, v200\n"
      "v_cvt_pk_bf16_f32 v200, v200, v201\n"
      "v_mfma_f32_16x16x32_bf16 v[212:215], a[52:55], v[224:227], v[212:215]\n"
      "v_mul_f32 v202, v233, v202\n"
      "v_mul_f32 v203, v233, v203\n"
      "v_mfma_f32_16x16x32_bf16 v[188:191], a[20:23], v[252:255], 0\n"
      "s_waitcnt lgkmcnt(2)\n"
      "v_mul_f32 v137, v233, v137\n"
      "v_cvt_pk_bf16_f32 v201, v202, v203\n"
      "v_mfma_f32_16x16x32_bf16 v[188:191], a[56:59], v[224:227], v[188:191]\n"
      "v_mul_f32_e64 v136, v233, v136\n"
      "v_cvt_pk_bf16_f32 v202, v136, v137\n"
      "v_mfma_f32_16x16x32_bf16 v[176:179], v[252:255], a[24:27], 0\n"
      "ds_read_b128 v[216:219], v114 offset:14336\n"
      "v_mul_f32_e64 v138, v233, v138\n"
      "v_mfma_f32_16x16x32_bf16 v[176:179], v[224:227], a[60:63], v[176:179]\n"
      "v_mul_f32_e64 v139, v233, v139\n"
      "v_cvt_pk_bf16_f32 v203, v138, v139\n"
      "v_mfma_f32_16x16x32_bf16 v[172:175], v[252:255], a[28:31], 0\n"
      "global_load_lds_dwordx4 v119, s[56:57]\n"
      "v_mfma_f32_16x16x32_bf16 v[172:175], v[224:227], a[64:67], v[172:175]\n"
      "ds_write_b128 v126, v[200:203] offset:4096\n"
      "v_mfma_f32_16x16x32_bf16 v[136:139], v[252:255], a[32:35], 0\n"
      "ds_read_b128 v[252:255], v115 offset:14336\n"
      "v_mul_f32 v228, v233, v228\n"
      "v_mfma_f32_16x16x32_bf16 v[136:139], v[224:227], a[68:71], v[136:139]\n"
      "s_add_u32 m0, s79, 4864\n"
      "v_mul_f32 v229, v233, v229\n"
      "v_cvt_pk_f16_f32 v140, v140, v141\n"
      "v_mfma_f32_16x16x32_bf16 v[204:207], a[76:79], v[144:147], v[204:207]\n"
      "v_mul_f32 v231, v233, v231\n"
      "v_mul_f32 v230, v233, v230\n"
      "v_mfma_f32_16x16x32_bf16 v[248:251], a[72:75], v[144:147], v[248:251]\n"
      "v_cvt_pk_bf16_f32 v228, v228, v229\n"
      "v_cvt_pk_bf16_f32 v229, v230, v231\n"
      "v_mfma_f32_16x16x32_bf16 v[248:251], a[108:111], v[220:223], v[248:251]\n"
      "global_load_lds_dword v122, s[58:59]\n"
      "v_mfma_f32_16x16x32_bf16 v[204:207], a[112:115], v[220:223], v[204:207]\n"
      "ds_write_b64 v127, v[228:229] offset:4096\n"
      "v_mfma_f32_16x16x32_bf16 v[196:199], a[80:83], v[144:147], v[196:199]\n"
      "v_pk_mul_f16 v160, v140, v140\n"
      "v_cvt_pk_f16_f32 v234, v234, v235\n"
      "v_mfma_f32_16x16x32_bf16 v[196:199], a[116:119], v[220:223], v[196:199]\n"
      "v_cvt_pk_f16_f32 v141, v142, v143\n"
      "v_pk_mul_f16 v161, v234, v234\n"
      "v_mfma_f32_16x16x32_bf16 v[208:211], a[84:87], v[144:147], v[208:211]\n"
      "v_cvt_pk_f16_f32 v235, v236, v237\n"
      "v_pk_fma_f16 v160, v141, v141, v160\n"
      "v_mfma_f32_16x16x32_bf16 v[208:211], a[120:123], v[220:223], v[208:211]\n"
      "ds_read_b128 v[200:203], v114 offset:4096\n"
      "v_pk_fma_f16 v161, v235, v235, v161\n"
      "v_mfma_f32_16x16x32_bf16 v[212:215], a[88:91], v[144:147], v[212:215]\n"
      "ds_read_b128 v[224:227], v115 offset:4096\n"
      "v_cvt_pk_f16_f32 v142, v162, v163\n"
      "v_mfma_f32_16x16x32_bf16 v[212:215], a[124:127], v[220:223], v[212:215]\n"
      "v_pk_fma_f16 v160, v142, v142, v160\n"
      "v_cvt_pk_f16_f32 v236, v168, v169\n"
      "v_mfma_f32_16x16x32_bf16 v[188:191], a[92:95], v[144:147], v[188:191]\n"
      "s_waitcnt lgkmcnt(3)\n"
      "v_cvt_pk_f16_f32 v143, v164, v165\n"
      "v_pk_fma_f16 v161, v236, v236, v161\n"
      "v_mfma_f32_16x16x32_bf16 v[188:191], a[128:131], v[220:223], v[188:191]\n"
      "v_cvt_pk_f16_f32 v237, v170, v171\n"
      "v_pk_fma_f16 v160, v143, v143, v160\n"
      "v_mfma_f32_16x16x32_bf16 v[176:179], v[144:147], a[96:99], v[176:179]\n"
      "s_nop 0\n"
      "v_cvt_pk_f16_f32 v32, v148, v149\n"
      "v_pk_fma_f16 v161, v237, v237, v161\n"
      "v_mfma_f32_16x16x32_bf16 v[176:179], v[220:223], a[132:135], v[176:179]\n"
      "v_cvt_pk_bf16_f32 v192, v192, v193\n"
      "v_pk_fma_f16 v160, v32, v32, v160\n"
      "v_mfma_f32_16x16x32_bf16 v[172:175], v[144:147], a[100:103], v[172:175]\n"
      "v_cvt_pk_f16_f32 v33, v150, v151\n"
      "v_pk_fma_f16 v160, v33, v33, v160\n"
      "v_mfma_f32_16x16x32_bf16 v[172:175], v[220:223], a[136:139], v[172:175]\n"
      "v_cvt_pk_f16_f32 v34, v244, v245\n"
      "v_cvt_pk_f16_f32 v35, v246, v247\n"
      "v_mfma_f32_16x16x32_bf16 v[136:139], v[144:147], a[104:107], v[136:139]\n"
      "ds_read_b128 v[168:171], v119 offset:18432\n"
      "v_mfma_f32_16x16x32_bf16 v[136:139], v[220:223], a[140:143], v[136:139]\n"
      "v_pk_fma_f16 v161, v34, v34, v161\n"
      "v_pk_fma_f16 v161, v35, v35, v161\n"
      "v_mfma_f32_16x16x32_bf16 v[248:251], a[144:147], v[216:219], v[248:251]\n"
      "s_nop 0\n"
      "v_permlane16_swap_b32 v160, v161\n"
      "v_mfma_f32_16x16x32_bf16 v[248:251], a[180:183], v[252:255], v[248:251]\n"
      "v_pk_add_f16 v160, v160, v161\n"
      "v_mov_b32 v161, v160\n"
      "v_mfma_f32_16x16x32_bf16 v[204:207], a[148:151], v[216:219], v[204:207]\n"
      "s_nop 0\n"
      "v_permlane32_swap_b32_e64 v160, v161\n"
      "v_mfma_f32_16x16x32_bf16 v[204:207], a[184:187], v[252:255], v[204:207]\n"
      "s_waitcnt lgkmcnt(1)\n"
      "v_pk_add_f16 v160, v160, v161\n"
      "v_add_f16_sdwa v161, v160, v160 dst_sel:DWORD dst_unused:UNUSED_PAD src0_sel:WORD_0 src1_sel:WORD_1\n"
      "v_mfma_f32_16x16x32_bf16 v[196:199], a[152:155], v[216:219], v[196:199]\n"
      "v_rsq_f16 v160, v161\n"
      "v_mfma_f32_16x16x32_bf16 v[196:199], a[188:191], v[252:255], v[196:199]\n"
      "ds_read_b128 v[148:151], v114 offset:10240\n"
      "v_cmp_nle_f16 vcc, 0x1c00, v161\n"
      "v_mfma_f32_16x16x32_bf16 v[208:211], a[156:159], v[216:219], v[208:211]\n"
      "s_mov_b64 s[34:35], vcc\n"
      "v_cmp_nge_f16 vcc, 0x7b00, v161\n"
      "v_mov_b32_e64 v161, v160\n"
      "v_mfma_f32_16x16x32_bf16 v[208:211], a[192:195], v[252:255], v[208:211]\n"
      "ds_read_b128 v[144:147], v115 offset:10240\n"
      "v_cvt_pk_bf16_f32 v193, v194, v195\n"
      "v_mfma_f32_16x16x32_bf16 v[212:215], a[160:163], v[216:219], v[212:215]\n"
      "s_or_b64 s[34:35], s[34:35], vcc\n"
      "v_permlane16_swap_b32_e64 v160, v161\n"
      "v_mfma_f32_16x16x32_bf16 v[212:215], a[196:199], v[252:255], v[212:215]\n"
      "s_sub_u32 s28, s27, 3\n"
      "v_pk_mul_f16 v234, v234, v161 op_sel_hi:[1,0]\n"
      "v_pk_mul_f16 v235, v235, v161 op_sel_hi:[1,0]\n"
      "v_mfma_f32_16x16x32_bf16 v[188:191], a[164:167], v[216:219], v[188:191]\n"
      "s_cselect_b64 s[54:55], s[12:13], s[22:23]\n"
      "v_pk_mul_f16 v237, v237, v161 op_sel_hi:[1,0]\n"
      "v_pk_mul_f16 v236, v236, v161 op_sel_hi:[1,0]\n"
      "v_mfma_f32_16x16x32_bf16 v[188:191], a[200:203], v[252:255], v[188:191]\n"
      "s_max_i32 m0, s28, 0\n"
      "v_pk_mul_f16 v35, v35, v161 op_sel_hi:[1,0]\n"
      "v_pk_mul_f16 v34, v34, v161 op_sel_hi:[1,0]\n"
      "v_mfma_f32_16x16x32_bf16 v[176:179], v[216:219], a[168:171], v[176:179]\n"
      "s_movrels_b32 s9, s80\n"
      "v_mul_f16 v160, s7, v160\n"
      "v_pk_mul_f16 v140, v140, v160 op_sel_hi:[1,0]\n"
      "v_mfma_f32_16x16x32_bf16 v[176:179], v[252:255], a[204:207], v[176:179]\n"
      "ds_read_b128 v[244:247], v119 offset:24576\n"
      "v_pk_mul_f16 v141, v141, v160 op_sel_hi:[1,0]\n"
      "v_mfma_f32_16x16x32_bf16 v[172:175], v[216:219], a[172:175], v[172:175]\n"
      "s_lshr_b32 s8, s9, 8\n"
      "v_pk_mul_f16 v143, v143, v160 op_sel_hi:[1,0]\n"
      "v_pk_mul_f16 v142, v142, v160 op_sel_hi:[1,0]\n"
      "v_mfma_f32_16x16x32_bf16 v[172:175], v[252:255], a[208:211], v[172:175]\n"
      "s_and_b32 s9, s9, 0xff\n"
      "v_pk_mul_f16 v32, v32, v160 op_sel_hi:[1,0]\n"
      "v_pk_mul_f16 v33, v33, v160 op_sel_hi:[1,0]\n"
      "v_mfma_f32_16x16x32_bf16 v[136:139], v[216:219], a[176:179], v[136:139]\n"
      "s_mul_i32 s28, s8, 1373568\n"
      "v_cvt_pk_bf16_f32 v194, v176, v177\n"
      "v_cvt_pk_bf16_f32 v152, v152, v153\n"
      "v_mfma_f32_16x16x32_bf16 v[136:139], v[252:255], a[212:215], v[136:139]\n"
      "s_waitcnt lgkmcnt(1)\n"
      "v_cvt_pk_bf16_f32 v195, v178, v179\n"
      "v_cvt_pk_bf16_f32 v153, v154, v155\n"
      "v_mfma_f32_16x16x32_bf16 v[160:163], a[0:3], v[200:203], 0\n"
      "s_mul_i32 s29, s9, 2688\n"
      "v_cvt_pk_bf16_f32 v154, v172, v173\n"
      "v_cvt_pk_bf16_f32 v155, v174, v175\n"
      "v_mfma_f32_16x16x32_bf16 v[160:163], a[36:39], v[224:227], v[160:163]\n"
      "s_add_u32 s28, s28, s29\n"
      "v_cvt_pk_bf16_f32 v240, v240, v241\n"
      "v_cvt_pk_bf16_f32 v241, v242, v243\n"
      "v_mfma_f32_16x16x32_bf16 v[220:223], a[4:7], v[200:203], 0\n"
      "s_cmp_eq_u32 s8, 72\n"
      "v_cvt_pk_f16_f32 v248, v248, v249\n"
      "v_cvt_pk_bf16_f32 v242, v136, v137\n"
      "v_mfma_f32_16x16x32_bf16 v[220:223], a[40:43], v[224:227], v[220:223]\n"
      "s_cselect_b32 s8, 0, 100270464\n"
      "v_cvt_pk_bf16_f32 v243, v138, v139\n"
      "v_pk_mul_f16 v232, v248, v248\n"
      "v_mfma_f32_16x16x32_bf16 v[252:255], a[8:11], v[200:203], 0\n"
      "s_cmp_eq_u32 s9, 72\n"
      "v_cvt_pk_f16_f32 v208, v208, v209\n"
      "v_pk_mul_f16 v233, v208, v208\n"
      "v_mfma_f32_16x16x32_bf16 v[252:255], a[44:47], v[224:227], v[252:255]\n"
      "s_cselect_b32 s9, 0, 196224\n"
      "v_cvt_pk_f16_f32 v249, v250, v251\n"
      "v_pk_fma_f16 v232, v249, v249, v232\n"
      "v_mfma_f32_16x16x32_bf16 v[136:139], a[12:15], v[200:203], 0\n"
      "s_add_u32 s10, s8, s9\n"
      "v_cvt_pk_f16_f32 v209, v210, v211\n"
      "v_pk_fma_f16 v233, v209, v209, v233\n"
      "v_mfma_f32_16x16x32_bf16 v[136:139], a[48:51], v[224:227], v[136:139]\n"
      "s_add_u32 s28, s28, s10\n"
      "v_cvt_pk_f16_f32 v210, v212, v213\n"
      "v_cvt_pk_f16_f32 v250, v204, v205\n"
      "v_mfma_f32_16x16x32_bf16 v[176:179], a[16:19], v[200:203], 0\n"
      "ds_read_b128 v[172:175], v114 offset:16384\n"
      "v_pk_fma_f16 v232, v250, v250, v232\n"
      "v_mfma_f32_16x16x32_bf16 v[176:179], a[52:55], v[224:227], v[176:179]\n"
      "s_add_u32 s54, s54, s28\n"
      "v_cvt_pk_f16_f32 v211, v214, v215\n"
      "v_pk_fma_f16 v233, v210, v210, v233\n"
      "v_mfma_f32_16x16x32_bf16 v[228:231], a[20:23], v[200:203], 0\n"
      "s_addc_u32 s55, s55, 0\n"
      "v_cvt_pk_f16_f32 v251, v206, v207\n"
      "v_pk_fma_f16 v232, v251, v251, v232\n"
      "v_mfma_f32_16x16x32_bf16 v[228:231], a[56:59], v[224:227], v[228:231]\n"
      "ds_read_b128 v[212:215], v115 offset:16384\n"
      "v_pk_fma_f16 v233, v211, v211, v233\n"
      "v_mfma_f32_16x16x32_bf16 v[216:219], v[200:203], a[24:27], 0\n"
      "s_cmp_lg_u32 s10, s11\n"
      "v_cvt_pk_f16_f32 v38, v196, v197\n"
      "v_pk_fma_f16 v232, v38, v38, v232\n"
      "v_mfma_f32_16x16x32_bf16 v[216:219], v[224:227], a[60:63], v[216:219]\n"
      "s_cbranch_scc1 L_edge_26_%=\n"
      "L_eb_27_%=:\n"
      "v_mfma_f32_16x16x32_bf16 v[204:207], v[200:203], a[28:31], 0\n"
      "s_mov_b32 s5, s4\n"
      "v_cvt_pk_f16_f32 v40, v188, v189\n"
      "v_pk_fma_f16 v233, v40, v40, v233\n"
      "v_mfma_f32_16x16x32_bf16 v[204:207], v[224:227], a[64:67], v[204:207]\n"
      "v_cvt_pk_f16_f32 v39, v198, v199\n"
      "v_pk_fma_f16 v232, v39, v39, v232\n"
      "v_mfma_f32_16x16x32_bf16 v[156:159], v[200:203], a[32:35], 0\n"
      "s_add_u32 s4, s27, 1\n"
      "v_cvt_pk_f16_f32 v41, v190, v191\n"
      "v_pk_fma_f16 v233, v41, v41, v233\n"
      "v_mfma_f32_16x16x32_bf16 v[156:159], v[224:227], a[68:71], v[156:159]\n"
      "s_lshl_b32 s4, s4, 30\n"
      "v_permlane16_swap_b32 v232, v233\n"
      "v_mfma_f32_16x16x32_bf16 v[160:163], a[72:75], v[148:151], v[160:163]\n"
      "s_and_b32 s4, s4, 0x80000000\n"
      "v_pk_add_f16 v232, v232, v233\n"
      "v_mov_b32_e64 v233, v232\n"
      "v_mfma_f32_16x16x32_bf16 v[160:163], a[108:111], v[144:147], v[160:163]\n"
      "s_nop 0\n"
      "v_permlane32_swap_b32 v232, v233\n"
      "v_mfma_f32_16x16x32_bf16 v[220:223], a[76:79], v[148:151], v[220:223]\n"
      "global_store_dwordx4 v116, v[168:171], s[54:55] sc1\n"
      "ds_read_b128 v[196:199], v119 offset:30720\n"
      "v_mfma_f32_16x16x32_bf16 v[220:223], a[112:115], v[144:147], v[220:223]\n"
      "ds_read_b128 v[200:203], v133\n"
      "v_pk_add_f16 v232, v232, v233\n"
      "v_mfma_f32_16x16x32_bf16 v[252:255], a[80:83], v[148:151], v[252:255]\n"
      "s_waitcnt lgkmcnt(2)\n"
      "v_add_f16_sdwa v233, v232, v232 dst_sel:DWORD dst_unused:UNUSED_PAD src0_sel:WORD_0 src1_sel:WORD_1\n"
      "v_cmp_nle_f16 vcc, 0x1c00, v233\n"
      "v_mfma_f32_16x16x32_bf16 v[252:255], a[116:119], v[144:147], v[252:255]\n"
      "s_or_b64 s[34:35], s[34:35], vcc\n"
      "v_rsq_f16 v232, v233\n"
      "v_mfma_f32_16x16x32_bf16 v[136:139], a[84:87], v[148:151], v[136:139]\n"
      "ds_read_b64 v[190:191], v133 offset:16\n"
      "v_mfma_f32_16x16x32_bf16 v[136:139], a[120:123], v[144:147], v[136:139]\n"
      "ds_read_b128 v[224:227], v132 offset:32\n"
      "v_cmp_nge_f16 vcc, 0x7b00, v233\n"
      "v_mov_b32 v233, v232\n"
      "v_mfma_f32_16x16x32_bf16 v[176:179], a[88:91], v[148:151], v[176:179]\n"
      "global_store_dwordx4 v116, v[244:247], s[54:55] offset:128 sc1\n"
      "v_mfma_f32_16x16x32_bf16 v[176:179], a[124:127], v[144:147], v[176:179]\n"
      "ds_read_b64 v[188:189], v132 offset:24\n"
      "v_add_u32_e64 v135, 32, v135\n"
      "v_mfma_f32_16x16x32_bf16 v[228:231], a[92:95], v[148:151], v[228:231]\n"
      "ds_read_u16 v52, v134\n"
      "s_or_b64 s[34:35], s[34:35], vcc\n"
      "v_mfma_f32_16x16x32_bf16 v[228:231], a[128:131], v[144:147], v[228:231]\n"
      "v_permlane16_swap_b32 v232, v233\n"
      "v_mfma_f32_16x16x32_bf16 v[216:219], v[148:151], a[96:99], v[216:219]\n"
      "ds_read_u16 v56, v134 offset:32\n"
      "v_pk_mul_f16 v209, v209, v233 op_sel_hi:[1,0]\n"
      "v_mfma_f32_16x16x32_bf16 v[216:219], v[144:147], a[132:135], v[216:219]\n"
      "ds_read_u16 v60, v134 offset:64\n"
      "v_pk_mul_f16 v210, v210, v233 op_sel_hi:[1,0]\n"
      "v_mfma_f32_16x16x32_bf16 v[204:207], v[148:151], a[100:103], v[204:207]\n"
      "ds_read_b128 v[180:183], v123 offset:21504\n"
      "v_pk_mul_f16 v211, v211, v233 op_sel_hi:[1,0]\n"
      "v_mfma_f32_16x16x32_bf16 v[204:207], v[144:147], a[136:139], v[204:207]\n"
      "s_waitcnt lgkmcnt(5)\n"
      "v_pk_mul_f16 v208, v208, v233 op_sel_hi:[1,0]\n"
      "v_pk_mul_f16 v40, v40, v233 op_sel_hi:[1,0]\n"
      "v_mfma_f32_16x16x32_bf16 v[156:159], v[148:151], a[104:107], v[156:159]\n"
      "global_store_dwordx4 v116, v[196:199], s[54:55] offset:256 sc1\n"
      "v_mfma_f32_16x16x32_bf16 v[156:159], v[144:147], a[140:143], v[156:159]\n"
      "ds_read_b128 v[244:247], v123 offset:18432\n"
      "v_pk_mul_f16 v41, v41, v233 op_sel_hi:[1,0]\n"
      "v_mfma_f32_16x16x32_bf16 v[160:163], a[144:147], v[172:175], v[160:163]\n"
      "ds_read_b128 v[144:147], v123 offset:24576\n"
      "v_mul_f16 v232, s7, v232\n"
      "v_mfma_f32_16x16x32_bf16 v[160:163], a[180:183], v[212:215], v[160:163]\n"
      "ds_read_b128 v[164:167], v123 offset:27648\n"
      "v_pk_mul_f16 v248, v248, v232 op_sel_hi:[1,0]\n"
      "v_mfma_f32_16x16x32_bf16 v[220:223], a[148:151], v[172:175], v[220:223]\n"
      "v_pk_mul_f16 v249, v249, v232 op_sel_hi:[1,0]\n"
      "v_pk_mul_f16 v251, v251, v232 op_sel_hi:[1,0]\n"
      "v_mfma_f32_16x16x32_bf16 v[220:223], a[184:187], v[212:215], v[220:223]\n"
      "v_pk_mul_f16 v250, v250, v232 op_sel_hi:[1,0]\n"
      "v_pk_mul_f16 v39, v39, v232 op_sel_hi:[1,0]\n"
      "v_mfma_f32_16x16x32_bf16 v[252:255], a[152:155], v[172:175], v[252:255]\n"
      "v_pk_mul_f16 v38, v38, v232 op_sel_hi:[1,0]\n"
      "v_cvt_pk_f16_f32 v160, v160, v161\n"
      "v_mfma_f32_16x16x32_bf16 v[252:255], a[188:191], v[212:215], v[252:255]\n"
      "v_cvt_pk_f16_f32 v161, v162, v163\n"
      "v_pk_mul_f16 v232, v160, v160\n"
      "v_mfma_f32_16x16x32_bf16 v[136:139], a[156:159], v[172:175], v[136:139]\n"
      "v_pk_fma_f16 v232, v161, v161, v232\n"
      "v_cvt_pk_f16_f32 v162, v220, v221\n"
      "v_mfma_f32_16x16x32_bf16 v[136:139], a[192:195], v[212:215], v[136:139]\n"
      "v_pk_fma_f16 v232, v162, v162, v232\n"
      "v_cvt_pk_f16_f32 v163, v222, v223\n"
      "v_mfma_f32_16x16x32_bf16 v[176:179], a[160:163], v[172:175], v[176:179]\n"
      "s_waitcnt lgkmcnt(2)\n"
      "v_cvt_pk_f16_f32 v44, v252, v253\n"
      "v_pk_fma_f16 v232, v163, v163, v232\n"
      "v_mfma_f32_16x16x32_bf16 v[176:179], a[196:199], v[212:215], v[176:179]\n"
      "s_nop 0\n"
      "v_cvt_pk_f16_f32 v136, v136, v137\n"
      "v_pk_mul_f16 v233, v136, v136\n"
      "v_mfma_f32_16x16x32_bf16 v[228:231], a[164:167], v[172:175], v[228:231]\n"
      "v_cvt_pk_f16_f32 v137, v138, v139\n"
      "v_pk_fma_f16 v233, v137, v137, v233\n"
      "v_mfma_f32_16x16x32_bf16 v[228:231], a[200:203], v[212:215], v[228:231]\n"
      "s_waitcnt lgkmcnt(0)\n"
      "v_cvt_pk_f16_f32 v138, v176, v177\n"
      "v_pk_fma_f16 v233, v138, v138, v233\n"
      "v_mfma_f32_16x16x32_bf16 v[216:219], v[172:175], a[168:171], v[216:219]\n"
      "s_nop 0\n"
      "v_cvt_pk_f16_f32 v139, v178, v179\n"
      "v_pk_fma_f16 v233, v139, v139, v233\n"
      "v_mfma_f32_16x16x32_bf16 v[216:219], v[212:215], a[204:207], v[216:219]\n"
      "ds_read_b128 v[220:223], v123 offset:30720\n"
      "v_cvt_pk_f16_f32 v45, v254, v255\n"
      "v_mfma_f32_16x16x32_bf16 v[204:207], v[172:175], a[172:175], v[204:207]\n"
      "v_pk_fma_f16 v232, v44, v44, v232\n"
      "v_mfma_f32_16x16x32_bf16 v[204:207], v[212:215], a[208:211], v[204:207]\n"
      "v_cvt_pk_f16_f32 v46, v228, v229\n"
      "v_pk_fma_f16 v233, v46, v46, v233\n"
      "v_mfma_f32_16x16x32_bf16 v[156:159], v[172:175], a[176:179], v[156:159]\n"
      "v_cvt_pk_f16_f32 v47, v230, v231\n"
      "v_pk_fma_f16 v233, v47, v47, v233\n"
      "v_mfma_f32_16x16x32_bf16 v[156:159], v[212:215], a[212:215], v[156:159]\n"
      "ds_read_b128 v[252:255], v123 offset:33792\n"
      "v_pk_fma_f16 v232, v45, v45, v232\n"
      "v_mfma_f32_16x16x32_bf16 v[168:171], a[216:219], v[244:247], 0\n"
      "s_nop 0\n"
      "v_permlane16_swap_b32 v232, v233\n"
      "v_mfma_f32_16x16x32_bf16 v[168:171], a[228:231], v[180:183], v[168:171]\n"
      "v_pk_add_f16 v232, v232, v233\n"
      "v_mov_b32 v233, v232\n"
      "v_mfma_f32_16x16x32_bf16 v[196:199], a[220:223], v[244:247], 0\n"
      "s_nop 0\n"
      "v_permlane32_swap_b32_e64 v232, v233\n"
      "v_mfma_f32_16x16x32_bf16 v[196:199], a[232:235], v[180:183], v[196:199]\n"
      "v_pk_add_f16 v232, v232, v233\n"
      "v_add_f16_sdwa v233, v232, v232 dst_sel:DWORD dst_unused:UNUSED_PAD src0_sel:WORD_0 src1_sel:WORD_1\n"
      "v_mfma_f32_16x16x32_bf16 v[148:151], a[224:227], v[244:247], 0\n"
      "s_nop 0\n"
      "v_rsq_f16 v232, v233\n"
      "v_mfma_f32_16x16x32_bf16 v[148:151], a[236:239], v[180:183], v[148:151]\n"
      "v_cvt_pk_bf16_f32 v50, v216, v217\n"
      "v_cmp_nle_f16 vcc, 0x1c00, v233\n"
      "v_mfma_f32_16x16x32_bf16 v[168:171], a[240:243], v[144:147], v[168:171]\n"
      "s_or_b64 s[34:35], s[34:35], vcc\n"
      "v_cmp_nge_f16 vcc, 0x7b00, v233\n"
      "v_mov_b32 v233, v232\n"
      "v_mfma_f32_16x16x32_bf16 v[168:171], a[252:255], v[164:167], v[168:171]\n"
      "s_nop 0\n"
      "v_permlane16_swap_b32 v232, v233\n"
      "v_mfma_f32_16x16x32_bf16 v[196:199], a[244:247], v[144:147], v[196:199]\n"
      "v_pk_mul_f16 v137, v137, v233 op_sel_hi:[1,0]\n"
      "v_pk_mul_f16 v136, v136, v233 op_sel_hi:[1,0]\n"
      "v_mfma_f32_16x16x32_bf16 v[196:199], v[0:3], v[164:167], v[196:199]\n"
      "s_or_b64 s[34:35], s[34:35], vcc\n"
      "v_pk_mul_f16 v139, v139, v233 op_sel_hi:[1,0]\n"
      "v_pk_mul_f16 v138, v138, v233 op_sel_hi:[1,0]\n"
      "v_mfma_f32_16x16x32_bf16 v[148:151], a[248:251], v[144:147], v[148:151]\n"
      "s_or_b64 vcc, s[48:49], s[50:51]\n"
      "v_pk_mul_f16 v47, v47, v233 op_sel_hi:[1,0]\n"
      "v_pk_mul_f16 v46, v46, v233 op_sel_hi:[1,0]\n"
      "v_mfma_f32_16x16x32_bf16 v[148:151], v[4:7], v[164:167], v[148:151]\n"
      "s_waitcnt lgkmcnt(0)\n"
      "v_mul_f16 v232, s7, v232\n"
      "v_pk_mul_f16 v160, v160, v232 op_sel_hi:[1,0]\n"
      "v_mfma_f32_16x16x32_bf16 v[168:171], v[8:11], v[220:223], v[168:171]\n"
      "v_pk_mul_f16 v161, v161, v232 op_sel_hi:[1,0]\n"
      "v_pk_mul_f16 v162, v162, v232 op_sel_hi:[1,0]\n"
      "v_mfma_f32_16x16x32_bf16 v[168:171], v[20:23], v[252:255], v[168:171]\n"
      "s_or_b64 vcc, vcc, s[34:35]\n"
      "v_pk_mul_f16 v44, v44, v232 op_sel_hi:[1,0]\n"
      "v_pk_mul_f16 v163, v163, v232 op_sel_hi:[1,0]\n"
      "v_mfma_f32_16x16x32_bf16 v[196:199], v[12:15], v[220:223], v[196:199]\n"
      "s_waitcnt vmcnt(3)\n"
      "v_pk_mul_f16 v45, v45, v232 op_sel_hi:[1,0]\n"
      "v_cvt_pk_bf16_f32 v51, v218, v219\n"
      "v_mfma_f32_16x16x32_bf16 v[196:199], v[24:27], v[252:255], v[196:199]\n"
      "ds_read_b32 v232, v122 offset:4864\n"
      "v_cvt_pk_bf16_f32 v54, v204, v205\n"
      "v_mfma_f32_16x16x32_bf16 v[148:151], v[16:19], v[220:223], v[148:151]\n"
      "ds_read_b128 v[144:147], v119 offset:41728\n"
      "v_cvt_pk_bf16_f32 v55, v206, v207\n"
      "v_mfma_f32_16x16x32_bf16 v[148:151], v[28:31], v[252:255], v[148:151]\n"
      "v_cvt_pk_bf16_f32 v59, v158, v159\n"
      "v_fma_f32 v159, v168, v168, s39\n"
      "v_mfma_f32_16x16x32_f16 v[220:223], v[234:237], v[140:143], v[78:81]\n"
      "v_cvt_pk_bf16_f32 v58, v156, v157\n"
      "v_fmac_f32 v159, v169, v169\n"
      "v_mfma_f32_16x16x32_f16 v[220:223], v[34:37], v[32:35], v[220:223]\n"
      "v_fmac_f32 v159, v170, v170\n"
      "v_fmac_f32 v159, v171, v171\n"
      "v_mfma_f32_16x16x32_f16 v[204:207], v[208:211], v[140:143], v[82:85]\n"
      "v_fmac_f32 v159, v196, v196\n"
      "v_fmac_f32 v159, v197, v197\n"
      "v_mfma_f32_16x16x32_f16 v[204:207], v[40:43], v[32:35], v[204:207]\n"
      "v_fmac_f32 v159, v198, v198\n"
      "v_fmac_f32 v159, v199, v199\n"
      "v_mfma_f32_16x16x32_f16 v[212:215], v[224:227], v[140:143], v[70:73]\n"
      "v_fmac_f32 v159, v148, v148\n"
      "v_fmac_f32 v159, v149, v149\n"
      "v_mfma_f32_16x16x32_f16 v[212:215], v[200:203], v[234:237], v[212:215]\n"
      "v_fmac_f32 v159, v150, v150\n"
      "v_fmac_f32 v159, v151, v151\n"
      "v_mfma_f32_16x16x32_f16 v[212:215], v[188:191], v[32:35], v[212:215]\n"
      "s_waitcnt lgkmcnt(0)\n"
      "v_bitop3_b32 v232, v232, v147, s40 bitop3:0xa8\n"
      "v_or3_b32 v144, v144, v145, v146\n"
      "v_mfma_f32_16x16x32_f16 v[252:255], v[136:139], v[140:143], v[86:89]\n"
      "v_and_b32_e64 v144, s40, v144\n"
      "v_or3_b32 v144, v144, v232, s4\n"
      "v_mfma_f32_16x16x32_f16 v[252:255], v[46:49], v[32:35], v[252:255]\n"
      "ds_write_b32 v129, v144 offset:3072\n"
      "s_cbranch_scc1 L_gen_28_%=\n"
      "L_fast_31_%=:\n"
      "v_mfma_f32_16x16x32_f16 v[228:231], v[234:237], v[248:251], v[90:93]\n"
      "ds_read_b128 v[140:143], v123 offset:19456\n"
      "v_exp_f32 v220, v220\n"
      "v_mfma_f32_16x16x32_f16 v[228:231], v[34:37], v[38:41], v[228:231]\n"
      "ds_read_b128 v[144:147], v120 offset:18432\n"
      "v_exp_f32_e64 v207, v207\n"
      "v_mfma_f32_16x16x32_f16 v[176:179], v[208:211], v[248:251], v[94:97]\n"
      "ds_read_b128 v[172:175], v123 offset:22528\n"
      "v_exp_f32 v222, v222\n"
      "v_mfma_f32_16x16x32_f16 v[176:179], v[40:43], v[38:41], v[176:179]\n"
      "ds_read_b128 v[164:167], v128 offset:3072\n"
      "v_exp_f32 v204, v204\n"
      "v_mfma_f32_16x16x32_f16 v[180:183], v[224:227], v[248:251], v[72:75]\n"
      "v_exp_f32 v221, v221\n"
      "v_mfma_f32_16x16x32_f16 v[180:183], v[200:203], v[208:211], v[180:183]\n"
      "v_exp_f32 v223, v223\n"
      "v_mfma_f32_16x16x32_f16 v[180:183], v[188:191], v[38:41], v[180:183]\n"
      "ds_read_b128 v[216:219], v123 offset:25600\n"
      "v_exp_f32 v252, v252\n"
      "v_exp_f32 v205, v205\n"
      "v_cvt_pk_bf16_f32 v220, v220, v221\n"
      "v_exp_f32 v206, v206\n"
      "v_cvt_pk_bf16_f32 v221, v222, v223\n"
      "v_cvt_pk_bf16_f32 v223, v206, v207\n"
      "s_nop 0\n"
      "v_mfma_f32_16x16x32_f16 v[182:185], v[136:139], v[248:251], v[98:101]\n"
      "ds_read_b128 v[248:251], v123 offset:28672\n"
      "s_waitcnt lgkmcnt(3)\n"
      "v_mfma_f32_16x16x32_f16 v[182:185], v[46:49], v[38:41], v[182:185]\n"
      "global_store_dwordx4 v117, v[144:147], s[54:55] sc1\n"
      "v_cvt_pk_bf16_f32 v222, v204, v205\n"
      "v_exp_f32 v228, v228\n"
      "v_mfma_f32_16x16x32_bf16 v[244:247], a[216:219], v[140:143], 0\n"
      "v_exp_f32 v254, v254\n"
      "v_mfma_f32_16x16x32_bf16 v[244:247], a[228:231], v[172:175], v[244:247]\n"
      "v_exp_f32 v253, v253\n"
      "v_mfma_f32_16x16x32_bf16 v[144:147], a[220:223], v[140:143], 0\n"
      "s_waitcnt lgkmcnt(2)\n"
      "v_exp_f32 v255, v255\n"
      "v_mfma_f32_16x16x32_bf16 v[144:147], a[232:235], v[172:175], v[144:147]\n"
      "v_bitop3_b32 v164, v164, v165, s4 bitop3:0x7e\n"
      "v_cvt_pk_bf16_f32 v62, v252, v253\n"
      "v_bitop3_b32 v166, v166, v167, s4 bitop3:0x7e\n"
      "v_mfma_f32_16x16x32_bf16 v[204:207], a[224:227], v[140:143], 0\n"
      "ds_read_b128 v[140:143], v123 offset:31744\n"
      "v_cvt_pk_bf16_f32 v63, v254, v255\n"
      "v_mfma_f32_16x16x32_bf16 v[204:207], a[236:239], v[172:175], v[204:207]\n"
      "s_waitcnt lgkmcnt(1)\n"
      "v_exp_f32 v212, v212\n"
      "v_mfma_f32_16x16x32_bf16 v[244:247], a[240:243], v[216:219], v[244:247]\n"
      "v_or_b32 v164, v164, v166\n"
      "v_cmp_ne_u32 vcc, 0, v164\n"
      "v_mfma_f32_16x16x32_bf16 v[244:247], a[252:255], v[248:251], v[244:247]\n"
      "ds_read_b128 v[252:255], v123 offset:34816\n"
      "v_exp_f32 v230, v230\n"
      "v_mfma_f32_16x16x32_bf16 v[144:147], a[244:247], v[216:219], v[144:147]\n"
      "s_mov_b64 s[48:49], vcc\n"
      "v_exp_f32 v229, v229\n"
      "v_mfma_f32_16x16x32_bf16 v[144:147], v[0:3], v[248:251], v[144:147]\n"
      "ds_read_b128 v[172:175], v121 offset:18432\n"
      "v_exp_f32 v179, v179\n"
      "v_exp_f32 v231, v231\n"
      "v_exp_f32 v177, v177\n"
      "v_exp_f32 v182, v182\n"
      "v_mfma_f32_16x16x32_bf16 v[204:207], a[248:251], v[216:219], v[204:207]\n"
      "v_exp_f32_e64 v178, v178\n"
      "v_mfma_f32_16x16x32_bf16 v[204:207], v[4:7], v[248:251], v[204:207]\n"
      "s_cbranch_vccnz L_flg_32_%=\n"
      "L_fb_33_%=:\n"
      "v_exp_f32 v176, v176\n"
      "v_cvt_pk_bf16_f32 v228, v228, v229\n"
      "v_cvt_pk_bf16_f32 v229, v230, v231\n"
      "v_cvt_pk_bf16_f32 v230, v176, v177\n"
      "s_waitcnt lgkmcnt(1)\n"
      "v_mfma_f32_16x16x32_bf16 v[244:247], v[8:11], v[140:143], v[244:247]\n"
      "ds_read_b128 v[216:219], v123 offset:23552\n"
      "v_cvt_pk_bf16_f32 v231, v178, v179\n"
      "v_mfma_f32_16x16x32_bf16 v[244:247], v[20:23], v[252:255], v[244:247]\n"
      "ds_read_b128 v[248:251], v123 offset:20480\n"
      "v_cvt_pk_bf16_f32 v64, v212, 0\n"
      "v_mfma_f32_16x16x32_bf16 v[144:147], v[12:15], v[140:143], v[144:147]\n"
      "s_waitcnt lgkmcnt(2)\n"
      "v_exp_f32 v183, v183\n"
      "v_mfma_f32_16x16x32_bf16 v[144:147], v[24:27], v[252:255], v[144:147]\n"
      "s_mov_b32 exec_hi, 0\n"
      "global_store_dwordx4 v118, v[172:175], s[54:55] sc1\n"
      "s_mov_b32 exec_hi, -1\n"
      "v_mfma_f32_16x16x32_bf16 v[204:207], v[16:19], v[140:143], v[204:207]\n"
      "v_exp_f32 v184, v184\n"
      "v_mfma_f32_16x16x32_bf16 v[204:207], v[28:31], v[252:255], v[204:207]\n"
      "v_exp_f32 v180, v180\n"
      "v_mfma_f32_16x16x32_bf16 v[140:143], v[66:69], v[220:223], 0\n"
      "v_exp_f32_e64 v185, v185\n"
      "v_mfma_f32_16x16x32_bf16 v[140:143], v[66:69], v[62:65], v[140:143]\n"
      "v_fma_f32 v158, v244, v244, s39\n"
      "v_fmac_f32 v158, v245, v245\n"
      "v_mfma_f32_16x16x32_bf16 v[252:255], v[192:195], v[220:223], 0\n"
      "v_fmac_f32 v158, v246, v246\n"
      "v_fmac_f32 v158, v247, v247\n"
      "v_mfma_f32_16x16x32_bf16 v[252:255], v[50:53], v[62:65], v[252:255]\n"
      "v_fmac_f32 v158, v144, v144\n"
      "v_fmac_f32 v158, v145, v145\n"
      "v_mfma_f32_16x16x32_bf16 v[172:175], v[152:155], v[220:223], 0\n"
      "v_fmac_f32 v158, v146, v146\n"
      "v_fmac_f32 v158, v147, v147\n"
      "v_mfma_f32_16x16x32_bf16 v[172:175], v[54:57], v[62:65], v[172:175]\n"
      "v_fmac_f32 v158, v204, v204\n"
      "v_fmac_f32 v158, v205, v205\n"
      "v_mfma_f32_16x16x32_bf16 v[176:179], v[240:243], v[220:223], 0\n"
      "s_waitcnt lgkmcnt(0)\n"
      "v_fmac_f32 v158, v206, v206\n"
      "v_fmac_f32 v158, v207, v207\n"
      "v_mfma_f32_16x16x32_bf16 v[176:179], v[58:61], v[62:65], v[176:179]\n"
      "v_cvt_pk_bf16_f32 v64, v180, 0\n"
      "v_cvt_pk_bf16_f32 v62, v182, v183\n"
      "v_mfma_f32_16x16x32_f16 v[164:167], v[234:237], v[160:163], v[102:105]\n"
      "v_cvt_pk_bf16_f32 v63, v184, v185\n"
      "v_cndmask_b32 v213, v213, v181, s[42:43]\n"
      "v_mfma_f32_16x16x32_f16 v[164:167], v[34:37], v[44:47], v[164:167]\n"
      "v_rcp_f32 v140, v140\n"
      "v_mfma_f32_16x16x32_f16 v[220:223], v[208:211], v[160:163], v[106:109]\n"
      "v_mul_f32 v253, v140, v253\n"
      "v_mul_f32_e64 v252, v140, v252\n"
      "v_mfma_f32_16x16x32_f16 v[220:223], v[40:43], v[44:47], v[220:223]\n"
      "v_cvt_pk_bf16_f32 v252, v252, v253\n"
      "v_mul_f32 v255, v140, v255\n"
      "v_mfma_f32_16x16x32_f16 v[232:235], v[224:227], v[160:163], v[74:77]\n"
      "ds_read_b128 v[208:211], v123 offset:26624\n"
      "v_mul_f32 v254, v140, v254\n"
      "v_mfma_f32_16x16x32_f16 v[232:235], v[200:203], v[136:139], v[232:235]\n"
      "v_cvt_pk_bf16_f32 v253, v254, v255\n"
      "v_mul_f32 v172, v140, v172\n"
      "v_mfma_f32_16x16x32_f16 v[232:235], v[188:191], v[44:47], v[232:235]\n"
      "s_add_u32 s28, s27, 2\n"
      "v_mul_f32 v175, v140, v175\n"
      "v_mul_f32 v174, v140, v174\n"
      "v_mfma_f32_16x16x32_f16 v[236:239], v[136:139], v[160:163], v[110:113]\n"
      "ds_read_b128 v[180:183], v123 offset:29696\n"
      "v_mul_f32_e64 v173, v140, v173\n"
      "v_mfma_f32_16x16x32_f16 v[236:239], v[46:49], v[44:47], v[236:239]\n"
      "v_mul_f32_e64 v178, v140, v178\n"
      "v_cvt_pk_bf16_f32 v255, v174, v175\n"
      "v_cvt_pk_bf16_f32 v254, v172, v173\n"
      "v_mfma_f32_16x16x32_f16 v[160:163], v[200:203], v[224:227], v[76:79]\n"
      "ds_write_b128 v124, v[252:255]\n"
      "v_mul_f32_e64 v177, v140, v177\n"
      "v_mfma_f32_16x16x32_bf16 v[136:139], v[66:69], v[228:231], 0\n"
      "ds_read_b128 v[252:255], v123 offset:32768\n"
      "v_mul_f32 v176, v140, v176\n"
      "v_mfma_f32_16x16x32_bf16 v[136:139], v[66:69], v[62:65], v[136:139]\n"
      "v_exp_f32 v220, v220\n"
      "v_mfma_f32_16x16x16_f16 v[160:163], v[190:191], v[188:189], v[160:163]\n"
      "v_mul_f32 v179, v140, v179\n"
      "v_cvt_pk_bf16_f32 v176, v176, v177\n"
      "v_mfma_f32_16x16x32_bf16 v[200:203], v[192:195], v[228:231], 0\n"
      "s_min_u32 m0, s28, s3\n"
      "v_cndmask_b32 v213, v213, v233, s[44:45]\n"
      "v_cvt_pk_bf16_f32 v177, v178, v179\n"
      "v_mfma_f32_16x16x32_bf16 v[200:203], v[50:53], v[62:65], v[200:203]\n"
      "s_movrels_b32 s9, s80\n"
      "v_exp_f32 v237, v237\n"
      "v_cndmask_b32 v213, v213, v161, s[46:47]\n"
      "v_exp_f32_e64 v213, v213\n"
      "v_mfma_f32_16x16x32_bf16 v[172:175], v[152:155], v[228:231], 0\n"
      "s_lshr_b32 s8, s9, 8\n"
      "s_waitcnt lgkmcnt(2)\n"
      "v_cvt_pk_bf16_f32 v213, v213, v213\n"
      "v_mfma_f32_16x16x32_bf16 v[172:175], v[54:57], v[62:65], v[172:175]\n"
      "ds_write_b16 v130, v213\n"
      "v_mfma_f32_16x16x32_bf16 v[184:187], v[240:243], v[228:231], 0\n"
      "ds_read_b128 v[228:231], v131\n"
      "s_and_b32 s9, s9, 0xff\n"
      "v_mfma_f32_16x16x32_bf16 v[184:187], v[58:61], v[62:65], v[184:187]\n"
      "ds_read_b128 v[188:191], v131 offset:64\n"
      "v_exp_f32_e64 v236, v236\n"
      "v_mfma_f32_16x16x32_bf16 v[160:163], a[216:219], v[248:251], 0\n"
      "s_mul_i32 s10, s8, 73\n"
      "v_exp_f32 v164, v164\n"
      "v_mfma_f32_16x16x32_bf16 v[160:163], a[228:231], v[216:219], v[160:163]\n"
      "s_add_u32 s10, s10, s9\n"
      "v_exp_f32 v166, v166\n"
      "v_mfma_f32_16x16x32_bf16 v[140:143], a[220:223], v[248:251], 0\n"
      "s_mul_i32 s28, s10, 4802\n"
      "v_exp_f32 v167, v167\n"
      "v_mfma_f32_16x16x32_bf16 v[140:143], a[232:235], v[216:219], v[140:143]\n"
      "s_add_u32 s56, s18, s28\n"
      "v_exp_f32 v223, v223\n"
      "v_exp_f32 v165, v165\n"
      "v_mfma_f32_16x16x32_bf16 v[160:163], a[240:243], v[208:211], v[160:163]\n"
      "s_addc_u32 s57, s19, 0\n"
      "v_cvt_pk_bf16_f32 v164, v164, v165\n"
      "v_cvt_pk_bf16_f32 v165, v166, v167\n"
      "v_mfma_f32_16x16x32_bf16 v[160:163], a[252:255], v[180:183], v[160:163]\n"
      "s_and_b32 s56, s56, -16\n"
      "v_exp_f32 v221, v221\n"
      "v_mfma_f32_16x16x32_bf16 v[140:143], a[244:247], v[208:211], v[140:143]\n"
      "s_sub_u32 s58, s56, 110592\n"
      "v_exp_f32 v222, v222\n"
      "v_mfma_f32_16x16x32_bf16 v[140:143], v[0:3], v[180:183], v[140:143]\n"
      "s_waitcnt lgkmcnt(0)\n"
      "v_cvt_pk_bf16_f32 v166, v220, v221\n"
      "v_cvt_pk_bf16_f32 v167, v222, v223\n"
      "v_mfma_f32_16x16x32_bf16 v[224:227], a[224:227], v[248:251], 0\n"
      "ds_read_b128 v[212:215], v123 offset:35840\n"
      "v_cvt_pk_bf16_f32 v62, v236, v237\n"
      "v_mfma_f32_16x16x32_bf16 v[224:227], a[236:239], v[216:219], v[224:227]\n"
      "ds_read_b128 v[220:223], v115 offset:18432\n"
      "v_exp_f32 v238, v238\n"
      "v_mfma_f32_16x16x32_bf16 v[224:227], a[248:251], v[208:211], v[224:227]\n"
      "s_subb_u32 s59, s57, 0\n"
      "ds_read_b128 v[248:251], v114 offset:18432\n"
      "v_mfma_f32_16x16x32_bf16 v[224:227], v[4:7], v[180:183], v[224:227]\n"
      "s_sub_u32 s56, s56, 73728\n"
      "v_exp_f32_e64 v239, v239\n"
      "v_mfma_f32_16x16x32_bf16 v[216:219], v[228:231], v[66:69], 0\n"
      "s_subb_u32 s57, s57, 0\n"
      "v_exp_f32 v232, v232\n"
      "v_mfma_f32_16x16x32_bf16 v[216:219], v[188:191], v[66:69], v[216:219]\n"
      "s_mul_i32 s28, s8, 1373568\n"
      "v_cvt_pk_bf16_f32 v64, v232, 0\n"
      "v_cvt_pk_bf16_f32 v63, v238, v239\n"
      "v_mfma_f32_16x16x32_bf16 v[180:183], v[228:231], v[192:195], 0\n"
      "s_mul_i32 s29, s9, 2688\n"
      "v_rcp_f32 v136, v136\n"
      "v_mfma_f32_16x16x32_bf16 v[180:183], v[188:191], v[50:53], v[180:183]\n"
      "s_add_u32 s28, s28, s29\n"
      "v_mul_f32 v200, v136, v200\n"
      "v_mul_f32 v201, v136, v201\n"
      "v_mfma_f32_16x16x32_bf16 v[232:235], v[66:69], v[164:167], 0\n"
      "s_waitcnt lgkmcnt(2)\n"
      "v_cvt_pk_bf16_f32 v200, v200, v201\n"
      "v_mul_f32 v203, v136, v203\n"
      "v_mfma_f32_16x16x32_bf16 v[232:235], v[66:69], v[62:65], v[232:235]\n"
      "v_mul_f32_e64 v202, v136, v202\n"
      "v_cvt_pk_bf16_f32 v201, v202, v203\n"
      "v_mfma_f32_16x16x32_bf16 v[160:163], v[8:11], v[252:255], v[160:163]\n"
      "s_cmp_eq_u32 s8, 72\n"
      "s_cselect_b32 s8, 0, 100270464\n"
      "v_mul_f32 v174, v136, v174\n"
      "v_mfma_f32_16x16x32_bf16 v[160:163], v[20:23], v[212:215], v[160:163]\n"
      "s_waitcnt lgkmcnt(0)\n"
      "v_mul_f32 v173, v136, v173\n"
      "v_mul_f32 v172, v136, v172\n"
      "v_mfma_f32_16x16x32_bf16 v[140:143], v[12:15], v[252:255], v[140:143]\n"
      "s_cmp_eq_u32 s9, 72\n"
      "v_mul_f32_e64 v175, v136, v175\n"
      "v_cvt_pk_bf16_f32 v202, v172, v173\n"
      "v_mfma_f32_16x16x32_bf16 v[140:143], v[24:27], v[212:215], v[140:143]\n"
      "s_cselect_b32 s9, 0, 196224\n"
      "v_cvt_pk_bf16_f32 v203, v174, v175\n"
      "v_mul_f32 v185, v136, v185\n"
      "v_mfma_f32_16x16x32_bf16 v[224:227], v[16:19], v[252:255], v[224:227]\n"
      "s_add_u32 s10, s8, s9\n"
      "v_mul_f32_e64 v184, v136, v184\n"
      "v_cvt_pk_bf16_f32 v184, v184, v185\n"
      "v_mfma_f32_16x16x32_bf16 v[224:227], v[28:31], v[212:215], v[224:227]\n"
      "ds_write_b128 v124, v[200:203] offset:1024\n"
      "v_mfma_f32_16x16x32_bf16 v[252:255], v[192:195], v[164:167], 0\n"
      "ds_read_b128 v[200:203], v114 offset:24576\n"
      "v_mul_f32_e64 v186, v136, v186\n"
      "v_mfma_f32_16x16x32_bf16 v[252:255], v[50:53], v[62:65], v[252:255]\n"
      "ds_read_b128 v[212:215], v115 offset:24576\n"
      "v_mul_f32 v187, v136, v187\n"
      "v_mfma_f32_16x16x32_bf16 v[136:139], v[152:155], v[164:167], 0\n"
      "s_add_u32 s28, s28, s10\n"
      "v_cvt_pk_bf16_f32 v185, v186, v187\n"
      "v_fma_f32 v157, v160, v160, s39\n"
      "v_mfma_f32_16x16x32_bf16 v[136:139], v[54:57], v[62:65], v[136:139]\n"
      "ds_write2st64_b64 v125, v[176:177], v[184:185] offset0:0 offset1:2\n"
      "v_mfma_f32_16x16x32_bf16 v[208:211], v[240:243], v[164:167], 0\n"
      "s_add_u32 s52, s16, s28\n"
      "v_fmac_f32 v157, v161, v161\n"
      "v_fmac_f32 v157, v162, v162\n"
      "v_mfma_f32_16x16x32_bf16 v[208:211], v[58:61], v[62:65], v[208:211]\n"
      "s_addc_u32 s53, s17, 0\n"
      "v_fmac_f32 v157, v163, v163\n"
      "v_fmac_f32 v157, v140, v140\n"
      "v_mfma_f32_16x16x32_bf16 v[176:179], v[228:231], v[152:155], 0\n"
      "s_add_u32 s27, s27, 1\n"
      "v_fmac_f32 v157, v141, v141\n"
      "v_fmac_f32 v157, v142, v142\n"
      "v_mfma_f32_16x16x32_bf16 v[176:179], v[188:191], v[54:57], v[176:179]\n"
      "s_cmp_ge_u32 s27, s26\n"
      "v_fmac_f32 v157, v143, v143\n"
      "v_fmac_f32 v157, v224, v224\n"
      "v_mfma_f32_16x16x32_bf16 v[182:185], v[228:231], v[240:243], 0\n"
      "v_fmac_f32 v157, v225, v225\n"
      "v_fmac_f32 v157, v226, v226\n"
      "v_mfma_f32_16x16x32_bf16 v[182:185], v[188:191], v[58:61], v[182:185]\n"
      "s_waitcnt lgkmcnt(1)\n"
      "v_fmac_f32 v157, v227, v227\n"
      "v_mov_b32 v156, v157\n"
      "v_mfma_f32_16x16x32_bf16 v[188:191], a[0:3], v[248:251], 0\n"
      "s_cselect_b32 s28, 1, 0\n"
      "v_permlane16_swap_b32_e64 v159, v158\n"
      "v_mfma_f32_16x16x32_bf16 v[188:191], a[36:39], v[220:223], v[188:191]\n"
      "s_xor_b32 s29, s10, s11\n"
      "v_mov_b32_dpp v216, v216 quad_perm:[0,0,0,0] row_mask:0xf bank_mask:0xf\n"
      "v_add_f32 v159, v159, v158\n"
      "v_mfma_f32_16x16x32_bf16 v[172:175], a[4:7], v[248:251], 0\n"
      "v_permlane16_swap_b32 v157, v156\n"
      "v_mfma_f32_16x16x32_bf16 v[172:175], a[40:43], v[220:223], v[172:175]\n"
      "v_add_f32 v157, v157, v156\n"
      "v_cndmask_b32 v180, v180, v176, s[42:43]\n"
      "v_mfma_f32_16x16x32_bf16 v[176:179], a[8:11], v[248:251], 0\n"
      "v_permlane32_swap_b32 v159, v157\n"
      "v_mfma_f32_16x16x32_bf16 v[176:179], a[44:47], v[220:223], v[176:179]\n"
      "v_add_f32 v159, v159, v157\n"
      "v_cndmask_b32 v180, v180, v182, s[44:45]\n"
      "v_mfma_f32_16x16x32_bf16 v[228:231], a[12:15], v[248:251], 0\n"
      "v_rcp_f32 v216, v216\n"
      "v_mfma_f32_16x16x32_bf16 v[228:231], a[48:51], v[220:223], v[228:231]\n"
      "ds_write_b32 v129, v159\n"
      "v_mfma_f32_16x16x32_bf16 v[156:159], a[16:19], v[248:251], 0\n"
      "v_mul_f32 v180, v216, v180\n"
      "v_cvt_pk_bf16_f32 v180, v180, v180\n"
      "v_mfma_f32_16x16x32_bf16 v[156:159], a[52:55], v[220:223], v[156:159]\n"
      "ds_write_b16 v135, v180\n"
      "v_mfma_f32_16x16x32_bf16 v[216:219], a[20:23], v[248:251], 0\n"
      "s_or_b32 s28, s28, s29\n"
      "v_mfma_f32_16x16x32_bf16 v[216:219], a[56:59], v[220:223], v[216:219]\n"
      "L_join_30_%=:\n"
      "s_cbranch_scc0 L_loop_%=\n"
      "L_tail0_%=:\n"
      "s_cmp_eq_u32 s10, s11\n"
      "s_cbranch_scc1 L_sk_35_%=\n"
      "s_mov_b32 s11, s10\n"
      "s_sub_u32 s28, s60, s8\n"
      "s_sub_u32 s29, s61, s9\n"
      "s_mov_b32 s60, s8\n"
      "s_mov_b32 s61, s9\n"
      "s_mov_b64 exec, s[64:65]\n"
      "v_add_u32 v116, s28, v116\n"
      "s_mov_b64 exec, s[70:71]\n"
      "v_add_u32 v116, s29, v116\n"
      "s_mov_b64 exec, s[66:67]\n"
      "v_add_u32 v117, s28, v117\n"
      "s_mov_b64 exec, s[72:73]\n"
      "v_add_u32 v117, s29, v117\n"
      "s_mov_b64 exec, s[68:69]\n"
      "v_add_u32 v118, s28, v118\n"
      "s_mov_b64 exec, s[74:75]\n"
      "v_add_u32 v118, s29, v118\n"
      "s_mov_b64 exec, -1\n"
      "s_nop 4\n"
      "L_sk_35_%=:\n"
      "s_cmp_lt_u32 s27, s26\n"
      "s_cbranch_scc1 L_loop_%=\n"
      "s_nop 7\n"
      "v_rcp_f32_e64 v232, v232\n"
      "v_mbcnt_lo_u32_b32 v173, -1, 0\n"
      "v_pk_mul_f32 v[138:139], v[138:139], v[232:233] op_sel_hi:[1,0]\n"
      "v_pk_mul_f32 v[208:209], v[208:209], v[232:233] op_sel_hi:[1,0]\n"
      "v_cvt_pk_bf16_f32 v208, v208, v209\n"
      "v_pk_mul_f32 v[252:253], v[252:253], v[232:233] op_sel_hi:[1,0]\n"
      "v_pk_mul_f32 v[136:137], v[136:137], v[232:233] op_sel_hi:[1,0]\n"
      "v_cvt_pk_bf16_f32 v252, v252, v253\n"
      "v_mbcnt_hi_u32_b32 v173, -1, v173\n"
      "v_pk_mul_f32 v[254:255], v[254:255], v[232:233] op_sel_hi:[1,0]\n"
      "v_lshrrev_b32 v174, 4, v173\n"
      "v_and_b32 v175, 15, v173\n"
      "v_cvt_pk_bf16_f32 v253, v254, v255\n"
      "v_pk_mul_f32 v[210:211], v[210:211], v[232:233] op_sel_hi:[1,0]\n"
      "v_mul_u32_u24 v174, 352, v174\n"
      "v_cvt_pk_bf16_f32 v254, v136, v137\n"
      "v_cvt_pk_bf16_f32 v255, v138, v139\n"
      "v_cvt_pk_bf16_f32 v209, v210, v211\n"
      "ds_write_b64 v125, v[208:209] offset:2048\n"
      "ds_write_b128 v124, v[252:255] offset:2048\n"
      "v_lshl_add_u32 v174, v175, 4, v174\n"
      "v_add_u32 v174, 150640, v174\n"
      "s_waitcnt lgkmcnt(0)\n"
      "s_barrier\n"
      "ds_read_b128 v[208:211], v128\n"
      "ds_read_b128 v[176:179], v123\n"
      "ds_read_b128 v[228:231], v123 offset:3072\n"
      "s_sub_u32 s28, s27, 3\n"
      "s_cselect_b64 s[54:55], s[12:13], s[22:23]\n"
      "s_max_i32 m0, s28, 0\n"
      "ds_read_b128 v[252:255], v121\n"
      "ds_read_b128 v[136:139], v119 offset:12288\n"
      "s_movrels_b32 s9, s80\n"
      "ds_read_b128 v[212:215], v120\n"
      "s_lshr_b32 s8, s9, 8\n"
      "s_and_b32 s9, s9, 0xff\n"
      "s_mul_i32 s28, s8, 1373568\n"
      "s_mul_i32 s29, s9, 2688\n"
      "s_add_u32 s28, s28, s29\n"
      "s_cmp_eq_u32 s8, 72\n"
      "ds_read_b128 v[248:251], v123 offset:6144\n"
      "ds_read_b128 v[200:203], v119 offset:6144\n"
      "s_cselect_b32 s8, 0, 100270464\n"
      "s_cmp_eq_u32 s9, 72\n"
      "s_cselect_b32 s9, 0, 196224\n"
      "ds_read_b128 v[216:219], v119\n"
      "s_add_u32 s10, s8, s9\n"
      "s_add_u32 s28, s28, s10\n"
      "s_waitcnt lgkmcnt(8)\n"
      "v_add_f32 v208, |v208|, |v209|\n"
      "v_add_f32 v210, |v210|, |v211|\n"
      "s_add_u32 s54, s54, s28\n"
      "s_waitcnt lgkmcnt(6)\n"
      "v_mfma_f32_16x16x32_bf16 v[220:223], a[216:219], v[176:179], 0\n"
      "ds_read_b128 v[180:183], v123 offset:9216\n"
      "v_add_f32 v208, v208, v210\n"
      "v_mfma_f32_16x16x32_bf16 v[220:223], a[228:231], v[228:231], v[220:223]\n"
      "ds_read_b128 v[240:243], v123 offset:1024\n"
      "v_rsq_f32 v208, v208\n"
      "v_mfma_f32_16x16x32_bf16 v[188:191], a[220:223], v[176:179], 0\n"
      "ds_read_b128 v[184:187], v123 offset:15360\n"
      "v_mul_f32 v208, s41, v208\n"
      "v_mfma_f32_16x16x32_bf16 v[188:191], a[232:235], v[228:231], v[188:191]\n"
      "ds_read_b128 v[156:159], v123 offset:12288\n"
      "v_mov_b32 v172, v208\n"
      "v_mfma_f32_16x16x32_bf16 v[192:195], a[224:227], v[176:179], 0\n"
      "s_addc_u32 s55, s55, 0\n"
      "v_permlane32_swap_b32 v208, v172\n"
      "v_mov_b32 v175, v208\n"
      "s_cmp_lg_u32 s10, s11\n"
      "v_mfma_f32_16x16x32_bf16 v[192:195], a[236:239], v[228:231], v[192:195]\n"
      "ds_read_b128 v[176:179], v123 offset:4096\n"
      "ds_read_b128 v[228:231], v123 offset:7168\n"
      "v_permlane16_swap_b32 v208, v175\n"
      "v_mul_f32 v171, v208, v171\n"
      "v_mul_f32 v169, v208, v169\n"
      "v_mul_f32_e64 v168, v208, v168\n"
      "s_waitcnt lgkmcnt(5)\n"
      "v_mfma_f32_16x16x32_bf16 v[220:223], a[240:243], v[248:251], v[220:223]\n"
      "s_cbranch_scc1 L_edge_36_%=\n"
      "L_eb_37_%=:\n"
      "v_mfma_f32_16x16x32_bf16 v[220:223], a[252:255], v[180:183], v[220:223]\n"
      "global_store_dwordx4 v116, v[216:219], s[54:55] sc1\n"
      "v_mfma_f32_16x16x32_bf16 v[188:191], a[244:247], v[248:251], v[188:191]\n"
      "ds_read_b128 v[164:167], v123 offset:10240\n"
      "v_mul_f32_e64 v170, v208, v170\n"
      "v_mfma_f32_16x16x32_bf16 v[188:191], v[0:3], v[180:183], v[188:191]\n"
      "ds_read_b128 v[216:219], v123 offset:13312\n"
      "v_cvt_pk_bf16_f32 v168, v168, v169\n"
      "v_mfma_f32_16x16x32_bf16 v[192:195], a[248:251], v[248:251], v[192:195]\n"
      "s_waitcnt lgkmcnt(4)\n"
      "v_mul_f32 v197, v208, v197\n"
      "v_mul_f32_e64 v196, v208, v196\n"
      "v_mfma_f32_16x16x32_bf16 v[192:195], v[4:7], v[180:183], v[192:195]\n"
      "ds_read_b128 v[180:183], v123 offset:16384\n"
      "v_cvt_pk_bf16_f32 v169, v170, v171\n"
      "v_mfma_f32_16x16x32_bf16 v[220:223], v[8:11], v[156:159], v[220:223]\n"
      "s_waitcnt lgkmcnt(3)\n"
      "v_mul_f32 v198, v208, v198\n"
      "v_mul_f32 v199, v208, v199\n"
      "v_mfma_f32_16x16x32_bf16 v[220:223], v[20:23], v[184:187], v[220:223]\n"
      "v_mul_f32 v149, v208, v149\n"
      "v_mul_f32_e64 v148, v208, v148\n"
      "v_mfma_f32_16x16x32_bf16 v[188:191], v[12:15], v[156:159], v[188:191]\n"
      "v_cvt_pk_bf16_f32 v170, v196, v197\n"
      "v_cvt_pk_bf16_f32 v171, v198, v199\n"
      "v_mfma_f32_16x16x32_bf16 v[188:191], v[24:27], v[184:187], v[188:191]\n"
      "ds_write_b128 v126, v[168:171] offset:18432\n"
      "v_mfma_f32_16x16x32_bf16 v[192:195], v[16:19], v[156:159], v[192:195]\n"
      "ds_read_b128 v[196:199], v123 offset:2048\n"
      "v_mfma_f32_16x16x32_bf16 v[192:195], v[28:31], v[184:187], v[192:195]\n"
      "global_store_dwordx4 v116, v[200:203], s[54:55] offset:128 sc1\n"
      "v_mfma_f32_16x16x32_bf16 v[232:235], a[216:219], v[240:243], 0\n"
      "ds_read_b128 v[156:159], v123 offset:5120\n"
      "v_cvt_pk_bf16_f32 v148, v148, v149\n"
      "v_mfma_f32_16x16x32_bf16 v[232:235], a[228:231], v[176:179], v[232:235]\n"
      "s_waitcnt lgkmcnt(3)\n"
      "v_mul_f32 v151, v208, v151\n"
      "v_mul_f32 v150, v208, v150\n"
      "v_mfma_f32_16x16x32_bf16 v[208:211], a[220:223], v[240:243], 0\n"
      "s_lshl_b32 s29, s24, 8\n"
      "v_cvt_pk_bf16_f32 v149, v150, v151\n"
      "v_mul_f32_e64 v244, v175, v244\n"
      "v_mfma_f32_16x16x32_bf16 v[208:211], a[232:235], v[176:179], v[208:211]\n"
      "ds_write_b64 v127, v[148:149] offset:18432\n"
      "v_mfma_f32_16x16x32_bf16 v[200:203], a[224:227], v[240:243], 0\n"
      "ds_read_b128 v[240:243], v123 offset:8192\n"
      "v_mul_f32_e64 v245, v175, v245\n"
      "v_mfma_f32_16x16x32_bf16 v[200:203], a[236:239], v[176:179], v[200:203]\n"
      "ds_read_b128 v[176:179], v123 offset:11264\n"
      "v_cvt_pk_bf16_f32 v244, v244, v245\n"
      "v_mfma_f32_16x16x32_bf16 v[232:235], a[240:243], v[228:231], v[232:235]\n"
      "v_mul_f32 v247, v175, v247\n"
      "v_mul_f32 v246, v175, v246\n"
      "v_mfma_f32_16x16x32_bf16 v[232:235], a[252:255], v[164:167], v[232:235]\n"
      "v_cvt_pk_bf16_f32 v245, v246, v247\n"
      "v_mul_f32 v144, v175, v144\n"
      "v_mfma_f32_16x16x32_bf16 v[208:211], a[244:247], v[228:231], v[208:211]\n"
      "v_mul_f32 v145, v175, v145\n"
      "v_cvt_pk_bf16_f32 v246, v144, v145\n"
      "v_mfma_f32_16x16x32_bf16 v[208:211], v[0:3], v[164:167], v[208:211]\n"
      "v_mul_f32 v147, v175, v147\n"
      "v_mul_f32 v146, v175, v146\n"
      "v_mfma_f32_16x16x32_bf16 v[200:203], a[248:251], v[228:231], v[200:203]\n"
      "s_add_u32 s29, s29, 159088\n"
      "v_mul_f32_e64 v204, v175, v204\n"
      "v_cvt_pk_bf16_f32 v247, v146, v147\n"
      "v_mfma_f32_16x16x32_bf16 v[200:203], v[4:7], v[164:167], v[200:203]\n"
      "ds_write_b128 v126, v[244:247] offset:20480\n"
      "v_mfma_f32_16x16x32_bf16 v[232:235], v[8:11], v[216:219], v[232:235]\n"
      "ds_read_b128 v[144:147], v123 offset:14336\n"
      "v_mul_f32_e64 v205, v175, v205\n"
      "v_mfma_f32_16x16x32_bf16 v[232:235], v[20:23], v[180:183], v[232:235]\n"
      "ds_read_b128 v[228:231], v123 offset:17408\n"
      "v_cvt_pk_bf16_f32 v204, v204, v205\n"
      "v_mfma_f32_16x16x32_bf16 v[208:211], v[12:15], v[216:219], v[208:211]\n"
      "s_waitcnt lgkmcnt(3)\n"
      "v_mul_f32 v207, v175, v207\n"
      "v_mul_f32_e64 v206, v175, v206\n"
      "v_mfma_f32_16x16x32_bf16 v[208:211], v[24:27], v[180:183], v[208:211]\n"
      "v_cvt_pk_bf16_f32 v205, v206, v207\n"
      "v_fma_f32 v175, v220, v220, s39\n"
      "v_mfma_f32_16x16x32_bf16 v[200:203], v[16:19], v[216:219], v[200:203]\n"
      "global_store_dwordx4 v116, v[136:139], s[54:55] offset:256 sc1\n"
      "v_mfma_f32_16x16x32_bf16 v[200:203], v[28:31], v[180:183], v[200:203]\n"
      "ds_write_b64 v127, v[204:205] offset:20480\n"
      "v_mfma_f32_16x16x32_bf16 v[204:207], a[216:219], v[196:199], 0\n"
      "ds_read_b128 v[136:139], v174\n"
      "v_fmac_f32_e64 v175, v221, v221\n"
      "v_mfma_f32_16x16x32_bf16 v[204:207], a[228:231], v[156:159], v[204:207]\n"
      "ds_read_b128 v[216:219], v174 offset:1408\n"
      "v_fmac_f32 v175, v222, v222\n"
      "v_mfma_f32_16x16x32_bf16 v[180:183], a[220:223], v[196:199], 0\n"
      "v_fmac_f32 v175, v223, v223\n"
      "v_fmac_f32_e64 v175, v188, v188\n"
      "v_mfma_f32_16x16x32_bf16 v[180:183], a[232:235], v[156:159], v[180:183]\n"
      "s_waitcnt lgkmcnt(3)\n"
      "v_fmac_f32 v175, v189, v189\n"
      "v_fmac_f32 v175, v190, v190\n"
      "v_mfma_f32_16x16x32_bf16 v[148:151], a[224:227], v[196:199], 0\n"
      "v_fmac_f32 v175, v191, v191\n"
      "v_fmac_f32 v175, v192, v192\n"
      "v_mfma_f32_16x16x32_bf16 v[148:151], a[236:239], v[156:159], v[148:151]\n"
      "v_fmac_f32 v175, v193, v193\n"
      "v_fmac_f32 v175, v194, v194\n"
      "v_mfma_f32_16x16x32_bf16 v[204:207], a[240:243], v[240:243], v[204:207]\n"
      "v_mul_f32 v161, v172, v161\n"
      "v_fmac_f32 v175, v195, v195\n"
      "v_mfma_f32_16x16x32_bf16 v[204:207], a[252:255], v[176:179], v[204:207]\n"
      "ds_read_b128 v[184:187], v174 offset:2816\n"
      "v_mul_f32 v160, v172, v160\n"
      "v_mfma_f32_16x16x32_bf16 v[180:183], a[244:247], v[240:243], v[180:183]\n"
      "ds_read_b128 v[196:199], v174 offset:4224\n"
      "v_mul_f32 v162, v172, v162\n"
      "v_mfma_f32_16x16x32_bf16 v[180:183], v[0:3], v[176:179], v[180:183]\n"
      "v_mul_f32 v163, v172, v163\n"
      "v_cvt_pk_bf16_f32 v160, v160, v161\n"
      "v_mfma_f32_16x16x32_bf16 v[148:151], a[248:251], v[240:243], v[148:151]\n"
      "global_store_dwordx4 v117, v[212:215], s[54:55] sc1\n"
      "v_mfma_f32_16x16x32_bf16 v[148:151], v[4:7], v[176:179], v[148:151]\n"
      "v_mul_f32_e64 v140, v172, v140\n"
      "v_cvt_pk_bf16_f32 v161, v162, v163\n"
      "v_mfma_f32_16x16x32_bf16 v[204:207], v[8:11], v[144:147], v[204:207]\n"
      "v_mul_f32_e64 v141, v172, v141\n"
      "v_cvt_pk_bf16_f32 v162, v140, v141\n"
      "v_mfma_f32_16x16x32_bf16 v[204:207], v[20:23], v[228:231], v[204:207]\n"
      "ds_read_b128 v[176:179], v174 offset:5632\n"
      "v_mul_f32 v143, v172, v143\n"
      "v_mfma_f32_16x16x32_bf16 v[180:183], v[12:15], v[144:147], v[180:183]\n"
      "s_waitcnt lgkmcnt(3)\n"
      "v_mul_f32_e64 v142, v172, v142\n"
      "v_cvt_pk_bf16_f32 v163, v142, v143\n"
      "v_mfma_f32_16x16x32_bf16 v[180:183], v[24:27], v[228:231], v[180:183]\n"
      "ds_write_b128 v126, v[160:163] offset:22528\n"
      "v_mfma_f32_16x16x32_bf16 v[148:151], v[16:19], v[144:147], v[148:151]\n"
      "ds_read_b128 v[144:147], v174 offset:7040\n"
      "v_mul_f32 v224, v172, v224\n"
      "v_mfma_f32_16x16x32_bf16 v[148:151], v[28:31], v[228:231], v[148:151]\n"
      "s_waitcnt lgkmcnt(3)\n"
      "v_mul_f32 v225, v172, v225\n"
      "v_cvt_pk_bf16_f32 v224, v224, v225\n"
      "v_mfma_f32_16x16x32_bf16 v[212:215], a[216:219], v[136:139], 0\n"
      "v_mul_f32 v227, v172, v227\n"
      "v_mul_f32_e64 v226, v172, v226\n"
      "v_mfma_f32_16x16x32_bf16 v[212:215], a[228:231], v[216:219], v[212:215]\n"
      "ds_read_b128 v[240:243], v174 offset:256\n"
      "v_cvt_pk_bf16_f32 v225, v226, v227\n"
      "v_mfma_f32_16x16x32_bf16 v[228:231], a[220:223], v[136:139], 0\n"
      "ds_read_b128 v[160:163], v174 offset:1664\n"
      "v_fma_f32 v172, v232, v232, s39\n"
      "v_mfma_f32_16x16x32_bf16 v[228:231], a[232:235], v[216:219], v[228:231]\n"
      "ds_write_b64 v127, v[224:225] offset:22528\n"
      "v_mfma_f32_16x16x32_bf16 v[140:143], a[224:227], v[136:139], 0\n"
      "v_fmac_f32 v172, v233, v233\n"
      "v_fmac_f32 v172, v234, v234\n"
      "v_mfma_f32_16x16x32_bf16 v[140:143], a[236:239], v[216:219], v[140:143]\n"
      "ds_read_b128 v[216:219], v174 offset:3072\n"
      "v_fmac_f32 v172, v235, v235\n"
      "v_mfma_f32_16x16x32_bf16 v[212:215], a[240:243], v[184:187], v[212:215]\n"
      "v_fmac_f32 v172, v208, v208\n"
      "v_fmac_f32_e64 v172, v209, v209\n"
      "v_mfma_f32_16x16x32_bf16 v[212:215], a[252:255], v[196:199], v[212:215]\n"
      "ds_read_b128 v[136:139], v174 offset:4480\n"
      "v_fmac_f32 v172, v210, v210\n"
      "v_mfma_f32_16x16x32_bf16 v[228:231], a[244:247], v[184:187], v[228:231]\n"
      "v_fmac_f32 v172, v211, v211\n"
      "v_fmac_f32_e64 v172, v200, v200\n"
      "v_mfma_f32_16x16x32_bf16 v[228:231], v[0:3], v[196:199], v[228:231]\n"
      "v_fmac_f32 v172, v201, v201\n"
      "v_fmac_f32 v172, v202, v202\n"
      "v_mfma_f32_16x16x32_bf16 v[140:143], a[248:251], v[184:187], v[140:143]\n"
      "s_waitcnt lgkmcnt(3)\n"
      "v_fmac_f32 v172, v203, v203\n"
      "v_lshl_add_u32 v249, v173, 2, s29\n"
      "v_mfma_f32_16x16x32_bf16 v[140:143], v[4:7], v[196:199], v[140:143]\n"
      "ds_read_b32 v249, v249\n"
      "v_lshrrev_b32_e64 v199, 3, v173\n"
      "v_mfma_f32_16x16x32_bf16 v[212:215], v[8:11], v[176:179], v[212:215]\n"
      "ds_read_b128 v[184:187], v174 offset:5888\n"
      "v_fma_f32 v173, v204, v204, s39\n"
      "v_mfma_f32_16x16x32_bf16 v[212:215], v[20:23], v[144:147], v[212:215]\n"
      "ds_read_b128 v[224:227], v174 offset:7296\n"
      "v_fmac_f32 v173, v205, v205\n"
      "v_mfma_f32_16x16x32_bf16 v[228:231], v[12:15], v[176:179], v[228:231]\n"
      "s_lshl_b32 s29, s24, 3\n"
      "v_add_u32_e64 v199, s29, v199\n"
      "v_cmp_gt_u32 s[48:49], s26, v199\n"
      "v_mfma_f32_16x16x32_bf16 v[228:231], v[24:27], v[144:147], v[228:231]\n"
      "v_fmac_f32 v173, v206, v206\n"
      "v_fmac_f32 v173, v207, v207\n"
      "v_mfma_f32_16x16x32_bf16 v[140:143], v[16:19], v[176:179], v[140:143]\n"
      "v_fmac_f32 v173, v180, v180\n"
      "v_fmac_f32 v173, v181, v181\n"
      "v_mfma_f32_16x16x32_bf16 v[140:143], v[28:31], v[144:147], v[140:143]\n"
      "s_mov_b32 exec_hi, 0\n"
      "global_store_dwordx4 v118, v[252:255], s[54:55] sc1\n"
      "s_mov_b32 exec_hi, -1\n"
      "v_mfma_f32_16x16x32_bf16 v[176:179], a[216:219], v[240:243], 0\n"
      "s_waitcnt lgkmcnt(3)\n"
      "v_fmac_f32 v173, v182, v182\n"
      "v_fmac_f32 v173, v183, v183\n"
      "v_mfma_f32_16x16x32_bf16 v[176:179], a[228:231], v[160:163], v[176:179]\n"
      "v_fmac_f32 v173, v148, v148\n"
      "v_fmac_f32 v173, v149, v149\n"
      "v_mfma_f32_16x16x32_bf16 v[196:199], a[220:223], v[240:243], 0\n"
      "v_fmac_f32 v173, v150, v150\n"
      "v_fmac_f32 v173, v151, v151\n"
      "v_mfma_f32_16x16x32_bf16 v[196:199], a[232:235], v[160:163], v[196:199]\n"
      "v_permlane16_swap_b32 v175, v172\n"
      "v_mfma_f32_16x16x32_bf16 v[252:255], a[224:227], v[240:243], 0\n"
      "v_mov_b32 v174, v173\n"
      "v_mfma_f32_16x16x32_bf16 v[252:255], a[236:239], v[160:163], v[252:255]\n"
      "v_mfma_f32_16x16x32_bf16 v[252:255], a[248:251], v[216:219], v[252:255]\n"
      "s_waitcnt lgkmcnt(0)\n"
      "v_mfma_f32_16x16x32_bf16 v[176:179], a[240:243], v[216:219], v[176:179]\n"
      "v_permlane16_swap_b32 v173, v174\n"
      "v_mfma_f32_16x16x32_bf16 v[176:179], a[252:255], v[136:139], v[176:179]\n"
      "v_add_f32 v173, v173, v174\n"
      "v_add_f32_e64 v175, v175, v172\n"
      "v_mfma_f32_16x16x32_bf16 v[196:199], a[244:247], v[216:219], v[196:199]\n"
      "s_nop 0\n"
      "v_permlane32_swap_b32 v175, v173\n"
      "v_mfma_f32_16x16x32_bf16 v[196:199], v[0:3], v[136:139], v[196:199]\n"
      "v_fma_f32 v174, v212, v212, s39\n"
      "v_fmac_f32 v174, v213, v213\n"
      "v_mfma_f32_16x16x32_bf16 v[196:199], v[12:15], v[184:187], v[196:199]\n"
      "v_add_f32 v175, v175, v173\n"
      "v_mfma_f32_16x16x32_bf16 v[196:199], v[24:27], v[224:227], v[196:199]\n"
      "ds_write_b32 v129, v175 offset:1024\n"
      "v_mfma_f32_16x16x32_bf16 v[252:255], v[4:7], v[136:139], v[252:255]\n"
      "v_fmac_f32 v174, v214, v214\n"
      "v_fmac_f32 v174, v215, v215\n"
      "v_mfma_f32_16x16x32_bf16 v[252:255], v[16:19], v[184:187], v[252:255]\n"
      "v_fmac_f32 v174, v228, v228\n"
      "v_fmac_f32 v174, v229, v229\n"
      "v_mfma_f32_16x16x32_bf16 v[176:179], v[8:11], v[184:187], v[176:179]\n"
      "v_fmac_f32 v174, v230, v230\n"
      "v_fmac_f32 v174, v231, v231\n"
      "v_mfma_f32_16x16x32_bf16 v[176:179], v[20:23], v[224:227], v[176:179]\n"
      "v_fmac_f32 v174, v140, v140\n"
      "v_fmac_f32 v174, v141, v141\n"
      "v_mfma_f32_16x16x32_bf16 v[252:255], v[28:31], v[224:227], v[252:255]\n"
      "v_fmac_f32 v174, v142, v142\n"
      "v_fmac_f32_e64 v174, v143, v143\n"
      "s_nop 2\n"
      "v_pk_mul_f32 v[172:173], v[176:177], v[176:177]\n"
      "v_pk_fma_f32 v[172:173], v[178:179], v[178:179], v[172:173]\n"
      "v_pk_fma_f32 v[172:173], v[196:197], v[196:197], v[172:173]\n"
      "v_pk_fma_f32 v[172:173], v[198:199], v[198:199], v[172:173]\n"
      "v_pk_fma_f32 v[172:173], v[252:253], v[252:253], v[172:173]\n"
      "v_pk_fma_f32 v[172:173], v[254:255], v[254:255], v[172:173]\n"
      "v_add_f32 v172, s39, v172\n"
      "v_add_f32 v175, v172, v173\n"
      "v_mov_b32 v172, v175\n"
      "v_mov_b32 v173, v172\n"
      "v_permlane16_swap_b32 v174, v175\n"
      "v_add_f32 v174, v174, v175\n"
      "v_permlane16_swap_b32 v172, v173\n"
      "v_add_f32 v172, v172, v173\n"
      "s_nop 1\n"
      "v_permlane32_swap_b32 v174, v172\n"
      "v_add_f32 v174, v174, v172\n"
      "ds_write_b32 v129, v174 offset:2048\n"
      "s_waitcnt lgkmcnt(0)\n"
      "s_barrier\n"
      "ds_read_b128 v[136:139], v128 offset:2048\n"
      "ds_read_b128 v[172:175], v128 offset:1024\n"
      "s_add_u32 s27, s27, 1\n"
      "s_sub_u32 s28, s27, 3\n"
      "s_cselect_b64 s[54:55], s[12:13], s[22:23]\n"
      "ds_read_b128 v[236:239], v120 offset:18432\n"
      "s_max_i32 m0, s28, 0\n"
      "ds_read_b128 v[224:227], v119 offset:24576\n"
      "ds_read_b128 v[156:159], v121 offset:18432\n"
      "s_movrels_b32 s9, s80\n"
      "s_lshr_b32 s8, s9, 8\n"
      "s_and_b32 s9, s9, 0xff\n"
      "s_mul_i32 s28, s8, 1373568\n"
      "s_mul_i32 s29, s9, 2688\n"
      "s_add_u32 s28, s28, s29\n"
      "s_cmp_eq_u32 s8, 72\n"
      "s_cselect_b32 s8, 0, 100270464\n"
      "s_cmp_eq_u32 s9, 72\n"
      "s_cselect_b32 s9, 0, 196224\n"
      "ds_read_b128 v[216:219], v119 offset:18432\n"
      "s_add_u32 s10, s8, s9\n"
      "s_add_u32 s28, s28, s10\n"
      "s_add_u32 s54, s54, s28\n"
      "s_addc_u32 s55, s55, 0\n"
      "s_cmp_lg_u32 s10, s11\n"
      "s_cbranch_scc1 L_edge_38_%=\n"
      "L_eb_39_%=:\n"
      "ds_read_b128 v[144:147], v119 offset:30720\n"
      "s_waitcnt lgkmcnt(5)\n"
      "v_add_f32 v172, |v172|, |v173|\n"
      "v_add_f32 v174, |v174|, |v175|\n"
      "v_add_f32 v172, v172, v174\n"
      "v_rsq_f32 v172, v172\n"
      "s_waitcnt lgkmcnt(3)\n"
      "v_mul_f32 v172, s41, v172\n"
      "v_mov_b32 v173, v172\n"
      "v_add_f32 v138, |v138|, |v139|\n"
      "v_add_f32 v136, |v136|, |v137|\n"
      "v_add_f32 v136, v136, v138\n"
      "v_permlane32_swap_b32 v172, v173\n"
      "v_subrev_u32 v138, 73728, v126\n"
      "v_mov_b32_e64 v137, v172\n"
      "v_pk_mul_f32 v[206:207], v[206:207], v[172:173] op_sel:[0,1]\n"
      "v_pk_mul_f32 v[148:149], v[148:149], v[172:173] op_sel:[0,1]\n"
      "v_rsq_f32 v136, v136\n"
      "v_permlane16_swap_b32 v172, v137\n"
      "v_pk_mul_f32 v[204:205], v[204:205], v[172:173] op_sel:[0,1]\n"
      "v_pk_mul_f32 v[220:221], v[220:221], v[172:173] op_sel_hi:[1,0]\n"
      "v_pk_mul_f32 v[234:235], v[234:235], v[136:137] op_sel:[0,1]\n"
      "v_pk_mul_f32 v[232:233], v[232:233], v[136:137] op_sel:[0,1]\n"
      "v_pk_mul_f32 v[222:223], v[222:223], v[172:173] op_sel_hi:[1,0]\n"
      "v_pk_mul_f32 v[208:209], v[208:209], v[136:137] op_sel:[0,1]\n"
      "v_pk_mul_f32 v[202:203], v[202:203], v[136:137] op_sel:[0,1]\n"
      "v_pk_mul_f32 v[210:211], v[210:211], v[136:137] op_sel:[0,1]\n"
      "v_mul_f32_e64 v136, s41, v136\n"
      "v_pk_mul_f32 v[188:189], v[188:189], v[172:173] op_sel_hi:[1,0]\n"
      "v_pk_mul_f32 v[200:201], v[200:201], v[136:137] op_sel:[0,1]\n"
      "v_mov_b32_e64 v137, v136\n"
      "v_pk_mul_f32 v[192:193], v[192:193], v[172:173] op_sel_hi:[1,0]\n"
      "v_pk_mul_f32 v[182:183], v[182:183], v[172:173] op_sel:[0,1]\n"
      "v_permlane32_swap_b32 v136, v137\n"
      "v_pk_mul_f32 v[150:151], v[150:151], v[172:173] op_sel:[0,1]\n"
      "v_cvt_pk_bf16_f32 v204, v204, v205\n"
      "s_waitcnt lgkmcnt(0)\n"
      "v_mov_b32_e64 v137, v136\n"
      "v_cvt_pk_bf16_f32 v220, v220, v221\n"
      "v_cvt_pk_bf16_f32 v221, v222, v223\n"
      "v_cvt_pk_bf16_f32 v192, v192, v193\n"
      "v_cvt_pk_bf16_f32 v222, v188, v189\n"
      "v_pk_mul_f32 v[190:191], v[190:191], v[172:173] op_sel_hi:[1,0]\n"
      "v_permlane16_swap_b32_e64 v136, v137\n"
      "v_cvt_pk_bf16_f32 v232, v232, v233\n"
      "v_cvt_pk_bf16_f32 v205, v206, v207\n"
      "v_pk_mul_f32 v[198:199], v[198:199], v[136:137] op_sel:[0,1]\n"
      "v_pk_mul_f32 v[140:141], v[140:141], v[136:137] op_sel_hi:[1,0]\n"
      "v_pk_mul_f32 v[214:215], v[214:215], v[136:137] op_sel_hi:[1,0]\n"
      "v_pk_mul_f32 v[230:231], v[230:231], v[136:137] op_sel_hi:[1,0]\n"
      "v_cvt_pk_bf16_f32 v223, v190, v191\n"
      "v_pk_mul_f32 v[252:253], v[252:253], v[136:137] op_sel:[0,1]\n"
      "global_store_dwordx4 v116, v[216:219], s[54:55] sc1\n"
      "ds_write_b128 v126, v[220:223]\n"
      "v_pk_mul_f32 v[180:181], v[180:181], v[172:173] op_sel:[0,1]\n"
      "v_pk_mul_f32 v[176:177], v[176:177], v[136:137] op_sel:[0,1]\n"
      "v_cvt_pk_bf16_f32 v233, v234, v235\n"
      "v_pk_mul_f32 v[194:195], v[194:195], v[172:173] op_sel_hi:[1,0]\n"
      "v_subrev_u32 v139, 73728, v127\n"
      "v_cvt_pk_bf16_f32 v252, v252, v253\n"
      "v_pk_mul_f32 v[212:213], v[212:213], v[136:137] op_sel_hi:[1,0]\n"
      "v_cvt_pk_bf16_f32 v200, v200, v201\n"
      "v_cvt_pk_bf16_f32 v234, v208, v209\n"
      "v_pk_mul_f32 v[228:229], v[228:229], v[136:137] op_sel_hi:[1,0]\n"
      "v_pk_mul_f32 v[254:255], v[254:255], v[136:137] op_sel:[0,1]\n"
      "v_pk_mul_f32 v[142:143], v[142:143], v[136:137] op_sel_hi:[1,0]\n"
      "v_cvt_pk_bf16_f32 v148, v148, v149\n"
      "v_cvt_pk_bf16_f32 v201, v202, v203\n"
      "v_pk_mul_f32 v[178:179], v[178:179], v[136:137] op_sel:[0,1]\n"
      "v_cvt_pk_bf16_f32 v207, v182, v183\n"
      "v_cvt_pk_bf16_f32 v176, v176, v177\n"
      "v_cvt_pk_bf16_f32 v140, v140, v141\n"
      "v_cvt_pk_bf16_f32 v212, v212, v213\n"
      "v_cvt_pk_bf16_f32 v213, v214, v215\n"
      "v_cvt_pk_bf16_f32 v193, v194, v195\n"
      "v_cvt_pk_bf16_f32 v215, v230, v231\n"
      "v_pk_mul_f32 v[196:197], v[196:197], v[136:137] op_sel:[0,1]\n"
      "v_cvt_pk_bf16_f32 v177, v178, v179\n"
      "v_cvt_pk_bf16_f32 v214, v228, v229\n"
      "v_cvt_pk_bf16_f32 v178, v196, v197\n"
      "v_cvt_pk_bf16_f32 v206, v180, v181\n"
      "v_cvt_pk_bf16_f32 v149, v150, v151\n"
      "v_cvt_pk_bf16_f32 v235, v210, v211\n"
      "v_cvt_pk_bf16_f32 v179, v198, v199\n"
      "v_cvt_pk_bf16_f32 v253, v254, v255\n"
      "v_cvt_pk_bf16_f32 v141, v142, v143\n"
      "global_store_dwordx4 v116, v[224:227], s[54:55] offset:128 sc1\n"
      "ds_write_b128 v126, v[232:235] offset:2048\n"
      "ds_write_b64 v139, v[252:253] offset:2048\n"
      "ds_write_b128 v126, v[204:207] offset:4096\n"
      "ds_write_b64 v139, v[140:141]\n"
      "ds_write_b64 v127, v[148:149] offset:4096\n"
      "global_store_dwordx4 v116, v[144:147], s[54:55] offset:256 sc1\n"
      "global_store_dwordx4 v117, v[236:239], s[54:55] sc1\n"
      "ds_write_b128 v138, v[212:215]\n"
      "ds_write_b64 v127, v[200:201] offset:2048\n"
      "ds_write_b64 v127, v[192:193]\n"
      "ds_write_b128 v138, v[176:179] offset:2048\n"
      "s_mov_b32 exec_hi, 0\n"
      "global_store_dwordx4 v118, v[156:159], s[54:55] sc1\n"
      "s_mov_b32 exec_hi, -1\n"
      "s_waitcnt lgkmcnt(0)\n"
      "s_barrier\n"
      "ds_read_b128 v[176:179], v119 offset:6144\n"
      "ds_read_b128 v[252:255], v121\n"
      "ds_read_b128 v[228:231], v119 offset:12288\n"
      "ds_read_b128 v[204:207], v119\n"
      "v_subrev_u32 v199, 73728, v119\n"
      "s_add_u32 s27, s27, 1\n"
      "ds_read_b128 v[180:183], v199 offset:6144\n"
      "s_sub_u32 s28, s27, 3\n"
      "s_cselect_b64 s[54:55], s[12:13], s[22:23]\n"
      "s_max_i32 m0, s28, 0\n"
      "ds_read_b128 v[144:147], v199 offset:12288\n"
      "s_movrels_b32 s9, s80\n"
      "s_lshr_b32 s8, s9, 8\n"
      "s_and_b32 s9, s9, 0xff\n"
      "s_mul_i32 s28, s8, 1373568\n"
      "s_mul_i32 s29, s9, 2688\n"
      "s_add_u32 s28, s28, s29\n"
      "s_cmp_eq_u32 s8, 72\n"
      "s_cselect_b32 s8, 0, 100270464\n"
      "ds_read_b128 v[136:139], v120\n"
      "ds_read_b128 v[216:219], v199\n"
      "s_cmp_eq_u32 s9, 72\n"
      "s_cselect_b32 s9, 0, 196224\n"
      "s_add_u32 s10, s8, s9\n"
      "s_add_u32 s28, s28, s10\n"
      "s_add_u32 s54, s54, s28\n"
      "s_addc_u32 s55, s55, 0\n"
      "s_cmp_lg_u32 s10, s11\n"
      "s_cbranch_scc1 L_edge_40_%=\n"
      "L_eb_41_%=:\n"
      "s_waitcnt lgkmcnt(4)\n"
      "global_store_dwordx4 v116, v[204:207], s[54:55] sc1\n"
      "global_store_dwordx4 v116, v[176:179], s[54:55] offset:128 sc1\n"
      "global_store_dwordx4 v116, v[228:231], s[54:55] offset:256 sc1\n"
      "s_waitcnt lgkmcnt(0)\n"
      "global_store_dwordx4 v117, v[136:139], s[54:55] sc1\n"
      "s_mov_b32 exec_hi, 0\n"
      "global_store_dwordx4 v118, v[252:255], s[54:55] sc1\n"
      "s_mov_b32 exec_hi, -1\n"
      "s_nop 0\n"
      "s_mov_b64 exec, s[48:49]\n"
      "s_cbranch_execz L_np_42_%=\n"
      "global_store_dwordx4 v249, v[216:219], s[22:23] sc1\n"
      "global_store_dwordx4 v249, v[180:183], s[22:23] offset:128 sc1\n"
      "global_store_dwordx4 v249, v[144:147], s[22:23] offset:256 sc1\n"
      "L_np_42_%=:\n"
      "s_mov_b64 exec, -1\n"
      "s_endpgm\n"
      "L_tail1_%=:\n"
      "s_cmp_eq_u32 s10, s11\n"
      "s_cbranch_scc1 L_sk_43_%=\n"
      "s_mov_b32 s11, s10\n"
      "s_sub_u32 s28, s60, s8\n"
      "s_sub_u32 s29, s61, s9\n"
      "s_mov_b32 s60, s8\n"
      "s_mov_b32 s61, s9\n"
      "s_mov_b64 exec, s[64:65]\n"
      "v_add_u32 v116, s28, v116\n"
      "s_mov_b64 exec, s[70:71]\n"
      "v_add_u32 v116, s29, v116\n"
      "s_mov_b64 exec, s[66:67]\n"
      "v_add_u32 v117, s28, v117\n"
      "s_mov_b64 exec, s[72:73]\n"
      "v_add_u32 v117, s29, v117\n"
      "s_mov_b64 exec, s[68:69]\n"
      "v_add_u32 v118, s28, v118\n"
      "s_mov_b64 exec, s[74:75]\n"
      "v_add_u32 v118, s29, v118\n"
      "s_mov_b64 exec, -1\n"
      "s_nop 4\n"
      "L_sk_43_%=:\n"
      "s_cmp_lt_u32 s27, s26\n"
      "s_cbranch_scc1 L_mid_%=\n"
      "s_nop 7\n"
      "v_rcp_f32_e64 v212, v212\n"
      "v_mbcnt_lo_u32_b32 v214, -1, 0\n"
      "v_pk_mul_f32 v[206:207], v[206:207], v[212:213] op_sel_hi:[1,0]\n"
      "v_pk_mul_f32 v[216:217], v[216:217], v[212:213] op_sel_hi:[1,0]\n"
      "v_cvt_pk_bf16_f32 v216, v216, v217\n"
      "v_pk_mul_f32 v[252:253], v[252:253], v[212:213] op_sel_hi:[1,0]\n"
      "v_pk_mul_f32 v[204:205], v[204:205], v[212:213] op_sel_hi:[1,0]\n"
      "v_cvt_pk_bf16_f32 v252, v252, v253\n"
      "v_mbcnt_hi_u32_b32 v214, -1, v214\n"
      "v_pk_mul_f32 v[254:255], v[254:255], v[212:213] op_sel_hi:[1,0]\n"
      "v_lshrrev_b32 v213, 4, v214\n"
      "v_and_b32 v239, 15, v214\n"
      "v_cvt_pk_bf16_f32 v253, v254, v255\n"
      "v_pk_mul_f32 v[218:219], v[218:219], v[212:213] op_sel_hi:[1,0]\n"
      "v_mul_u32_u24 v213, 352, v213\n"
      "v_cvt_pk_bf16_f32 v254, v204, v205\n"
      "v_cvt_pk_bf16_f32 v255, v206, v207\n"
      "v_cvt_pk_bf16_f32 v217, v218, v219\n"
      "ds_write_b64 v125, v[216:217] offset:20480\n"
      "ds_write_b128 v124, v[252:255] offset:20480\n"
      "v_lshl_add_u32 v213, v239, 4, v213\n"
      "v_add_u32 v213, 150640, v213\n"
      "s_waitcnt lgkmcnt(0)\n"
      "s_barrier\n"
      "ds_read_b128 v[156:159], v128 offset:1024\n"
      "ds_read_b128 v[148:151], v123 offset:21504\n"
      "ds_read_b128 v[216:219], v123 offset:18432\n"
      "s_sub_u32 s28, s27, 3\n"
      "s_cselect_b64 s[54:55], s[12:13], s[22:23]\n"
      "ds_read_b128 v[140:143], v119 offset:24576\n"
      "s_max_i32 m0, s28, 0\n"
      "ds_read_b128 v[224:227], v121 offset:18432\n"
      "ds_read_b128 v[248:251], v123 offset:24576\n"
      "s_movrels_b32 s9, s80\n"
      "s_lshr_b32 s8, s9, 8\n"
      "s_and_b32 s9, s9, 0xff\n"
      "s_mul_i32 s28, s8, 1373568\n"
      "s_mul_i32 s29, s9, 2688\n"
      "s_add_u32 s28, s28, s29\n"
      "s_cmp_eq_u32 s8, 72\n"
      "ds_read_b128 v[244:247], v119 offset:30720\n"
      "s_cselect_b32 s8, 0, 100270464\n"
      "ds_read_b128 v[192:195], v119 offset:18432\n"
      "s_cmp_eq_u32 s9, 72\n"
      "s_cselect_b32 s9, 0, 196224\n"
      "s_add_u32 s10, s8, s9\n"
      "s_add_u32 s28, s28, s10\n"
      "ds_read_b128 v[188:191], v120 offset:18432\n"
      "s_waitcnt lgkmcnt(8)\n"
      "v_add_f32 v156, |v156|, |v157|\n"
      "v_add_f32 v158, |v158|, |v159|\n"
      "s_add_u32 s54, s54, s28\n"
      "s_waitcnt lgkmcnt(6)\n"
      "v_mfma_f32_16x16x32_bf16 v[232:235], a[216:219], v[216:219], 0\n"
      "ds_read_b128 v[168:171], v123 offset:27648\n"
      "v_add_f32 v156, v156, v158\n"
      "v_mfma_f32_16x16x32_bf16 v[232:235], a[228:231], v[148:151], v[232:235]\n"
      "ds_read_b128 v[160:163], v123 offset:19456\n"
      "v_rsq_f32 v156, v156\n"
      "v_mfma_f32_16x16x32_bf16 v[236:239], a[220:223], v[216:219], 0\n"
      "ds_read_b128 v[164:167], v123 offset:33792\n"
      "v_mul_f32 v156, s41, v156\n"
      "v_mfma_f32_16x16x32_bf16 v[236:239], a[232:235], v[148:151], v[236:239]\n"
      "ds_read_b128 v[184:187], v123 offset:30720\n"
      "ds_read_b128 v[152:155], v123 offset:22528\n"
      "v_mov_b32_e64 v159, v156\n"
      "v_mfma_f32_16x16x32_bf16 v[204:207], a[224:227], v[216:219], 0\n"
      "ds_read_b128 v[252:255], v123 offset:25600\n"
      "v_permlane32_swap_b32 v156, v159\n"
      "s_addc_u32 s55, s55, 0\n"
      "v_mov_b32 v215, v156\n"
      "v_mfma_f32_16x16x32_bf16 v[204:207], a[236:239], v[148:151], v[204:207]\n"
      "s_cmp_lg_u32 s10, s11\n"
      "v_permlane16_swap_b32 v156, v215\n"
      "v_mul_f32 v145, v156, v145\n"
      "v_mul_f32 v146, v156, v146\n"
      "v_mul_f32 v144, v156, v144\n"
      "s_waitcnt lgkmcnt(5)\n"
      "v_mfma_f32_16x16x32_bf16 v[232:235], a[240:243], v[248:251], v[232:235]\n"
      "s_cbranch_scc1 L_edge_44_%=\n"
      "L_eb_45_%=:\n"
      "v_mfma_f32_16x16x32_bf16 v[232:235], a[252:255], v[168:171], v[232:235]\n"
      "global_store_dwordx4 v116, v[192:195], s[54:55] sc1\n"
      "v_mfma_f32_16x16x32_bf16 v[236:239], a[244:247], v[248:251], v[236:239]\n"
      "ds_read_b128 v[148:151], v123 offset:28672\n"
      "v_cvt_pk_bf16_f32 v144, v144, v145\n"
      "v_mfma_f32_16x16x32_bf16 v[236:239], v[0:3], v[168:171], v[236:239]\n"
      "s_waitcnt lgkmcnt(2)\n"
      "v_mul_f32 v147, v156, v147\n"
      "v_mul_f32_e64 v208, v156, v208\n"
      "v_mfma_f32_16x16x32_bf16 v[204:207], a[248:251], v[248:251], v[204:207]\n"
      "ds_read_b128 v[248:251], v123 offset:31744\n"
      "v_mul_f32_e64 v209, v156, v209\n"
      "v_mfma_f32_16x16x32_bf16 v[204:207], v[4:7], v[168:171], v[204:207]\n"
      "ds_read_b128 v[168:171], v123 offset:34816\n"
      "v_cvt_pk_bf16_f32 v145, v146, v147\n"
      "v_mfma_f32_16x16x32_bf16 v[232:235], v[8:11], v[184:187], v[232:235]\n"
      "v_mul_f32 v176, v156, v176\n"
      "v_mul_f32 v210, v156, v210\n"
      "v_mfma_f32_16x16x32_bf16 v[232:235], v[20:23], v[164:167], v[232:235]\n"
      "v_mul_f32_e64 v211, v156, v211\n"
      "v_mfma_f32_16x16x32_bf16 v[236:239], v[12:15], v[184:187], v[236:239]\n"
      "v_cvt_pk_bf16_f32 v146, v208, v209\n"
      "v_cvt_pk_bf16_f32 v147, v210, v211\n"
      "v_mfma_f32_16x16x32_bf16 v[236:239], v[24:27], v[164:167], v[236:239]\n"
      "ds_write_b128 v126, v[144:147]\n"
      "v_mfma_f32_16x16x32_bf16 v[204:207], v[16:19], v[184:187], v[204:207]\n"
      "ds_read_b128 v[144:147], v123 offset:20480\n"
      "v_mul_f32_e64 v177, v156, v177\n"
      "v_mfma_f32_16x16x32_bf16 v[204:207], v[28:31], v[164:167], v[204:207]\n"
      "global_store_dwordx4 v116, v[140:143], s[54:55] offset:128 sc1\n"
      "v_mfma_f32_16x16x32_bf16 v[192:195], a[216:219], v[160:163], 0\n"
      "ds_read_b128 v[216:219], v123 offset:23552\n"
      "v_cvt_pk_bf16_f32 v176, v176, v177\n"
      "v_mfma_f32_16x16x32_bf16 v[192:195], a[228:231], v[152:155], v[192:195]\n"
      "s_waitcnt lgkmcnt(3)\n"
      "v_mul_f32 v178, v156, v178\n"
      "v_mul_f32 v179, v156, v179\n"
      "v_mfma_f32_16x16x32_bf16 v[208:211], a[220:223], v[160:163], 0\n"
      "s_lshl_b32 s29, s24, 8\n"
      "v_cvt_pk_bf16_f32 v177, v178, v179\n"
      "v_mul_f32_e64 v196, v215, v196\n"
      "v_mfma_f32_16x16x32_bf16 v[208:211], a[232:235], v[152:155], v[208:211]\n"
      "ds_write_b64 v127, v[176:177]\n"
      "v_mfma_f32_16x16x32_bf16 v[240:243], a[224:227], v[160:163], 0\n"
      "ds_read_b128 v[176:179], v123 offset:26624\n"
      "v_mul_f32_e64 v197, v215, v197\n"
      "v_mfma_f32_16x16x32_bf16 v[240:243], a[236:239], v[152:155], v[240:243]\n"
      "ds_read_b128 v[160:163], v123 offset:29696\n"
      "v_cvt_pk_bf16_f32 v196, v196, v197\n"
      "v_mfma_f32_16x16x32_bf16 v[192:195], a[240:243], v[252:255], v[192:195]\n"
      "v_mul_f32 v198, v215, v198\n"
      "v_mul_f32 v199, v215, v199\n"
      "v_mfma_f32_16x16x32_bf16 v[192:195], a[252:255], v[148:151], v[192:195]\n"
      "v_mul_f32 v172, v215, v172\n"
      "v_cvt_pk_bf16_f32 v197, v198, v199\n"
      "v_mfma_f32_16x16x32_bf16 v[208:211], a[244:247], v[252:255], v[208:211]\n"
      "v_mul_f32 v173, v215, v173\n"
      "v_cvt_pk_bf16_f32 v198, v172, v173\n"
      "v_mfma_f32_16x16x32_bf16 v[208:211], v[0:3], v[148:151], v[208:211]\n"
      "s_add_u32 s29, s29, 159088\n"
      "v_mul_f32 v175, v215, v175\n"
      "v_mul_f32 v174, v215, v174\n"
      "v_mfma_f32_16x16x32_bf16 v[240:243], a[248:251], v[252:255], v[240:243]\n"
      "v_cvt_pk_bf16_f32 v199, v174, v175\n"
      "v_mul_f32 v180, v215, v180\n"
      "v_mfma_f32_16x16x32_bf16 v[240:243], v[4:7], v[148:151], v[240:243]\n"
      "ds_write_b128 v126, v[196:199] offset:2048\n"
      "v_mfma_f32_16x16x32_bf16 v[192:195], v[8:11], v[248:251], v[192:195]\n"
      "ds_read_b128 v[148:151], v123 offset:32768\n"
      "v_mul_f32 v181, v215, v181\n"
      "v_mfma_f32_16x16x32_bf16 v[192:195], v[20:23], v[168:171], v[192:195]\n"
      "ds_read_b128 v[172:175], v123 offset:35840\n"
      "v_cvt_pk_bf16_f32 v180, v180, v181\n"
      "v_mfma_f32_16x16x32_bf16 v[208:211], v[12:15], v[248:251], v[208:211]\n"
      "s_waitcnt lgkmcnt(3)\n"
      "v_mul_f32 v183, v215, v183\n"
      "v_mul_f32_e64 v182, v215, v182\n"
      "v_mfma_f32_16x16x32_bf16 v[208:211], v[24:27], v[168:171], v[208:211]\n"
      "v_fma_f32 v215, v232, v232, s39\n"
      "v_cvt_pk_bf16_f32 v181, v182, v183\n"
      "v_mfma_f32_16x16x32_bf16 v[240:243], v[16:19], v[248:251], v[240:243]\n"
      "global_store_dwordx4 v116, v[244:247], s[54:55] offset:256 sc1\n"
      "v_mfma_f32_16x16x32_bf16 v[240:243], v[28:31], v[168:171], v[240:243]\n"
      "ds_write_b64 v127, v[180:181] offset:2048\n"
      "v_mfma_f32_16x16x32_bf16 v[196:199], a[216:219], v[144:147], 0\n"
      "ds_read_b128 v[168:171], v213\n"
      "v_fmac_f32 v215, v233, v233\n"
      "v_mfma_f32_16x16x32_bf16 v[196:199], a[228:231], v[216:219], v[196:199]\n"
      "ds_read_b128 v[248:251], v213 offset:1408\n"
      "v_fmac_f32 v215, v234, v234\n"
      "v_mfma_f32_16x16x32_bf16 v[152:155], a[220:223], v[144:147], 0\n"
      "v_fmac_f32 v215, v235, v235\n"
      "v_fmac_f32 v215, v236, v236\n"
      "v_mfma_f32_16x16x32_bf16 v[152:155], a[232:235], v[216:219], v[152:155]\n"
      "v_fmac_f32 v215, v237, v237\n"
      "v_fmac_f32 v215, v238, v238\n"
      "v_mfma_f32_16x16x32_bf16 v[140:143], a[224:227], v[144:147], 0\n"
      "v_fmac_f32 v215, v239, v239\n"
      "v_fmac_f32 v215, v204, v204\n"
      "v_mfma_f32_16x16x32_bf16 v[140:143], a[236:239], v[216:219], v[140:143]\n"
      "s_waitcnt lgkmcnt(3)\n"
      "v_fmac_f32 v215, v205, v205\n"
      "v_fmac_f32 v215, v206, v206\n"
      "v_mfma_f32_16x16x32_bf16 v[196:199], a[240:243], v[176:179], v[196:199]\n"
      "v_fmac_f32 v215, v207, v207\n"
      "v_mul_f32_e64 v201, v159, v201\n"
      "v_mfma_f32_16x16x32_bf16 v[196:199], a[252:255], v[160:163], v[196:199]\n"
      "ds_read_b128 v[164:167], v213 offset:2816\n"
      "v_mul_f32 v200, v159, v200\n"
      "v_mfma_f32_16x16x32_bf16 v[152:155], a[244:247], v[176:179], v[152:155]\n"
      "ds_read_b128 v[252:255], v213 offset:4224\n"
      "v_mul_f32 v202, v159, v202\n"
      "v_mfma_f32_16x16x32_bf16 v[152:155], v[0:3], v[160:163], v[152:155]\n"
      "v_mul_f32_e64 v203, v159, v203\n"
      "v_cvt_pk_bf16_f32 v200, v200, v201\n"
      "v_mfma_f32_16x16x32_bf16 v[140:143], a[248:251], v[176:179], v[140:143]\n"
      "global_store_dwordx4 v117, v[188:191], s[54:55] sc1\n"
      "v_mfma_f32_16x16x32_bf16 v[140:143], v[4:7], v[160:163], v[140:143]\n"
      "s_waitcnt lgkmcnt(2)\n"
      "v_cvt_pk_bf16_f32 v201, v202, v203\n"
      "v_mul_f32 v136, v159, v136\n"
      "v_mfma_f32_16x16x32_bf16 v[196:199], v[8:11], v[148:151], v[196:199]\n"
      "v_mul_f32_e64 v137, v159, v137\n"
      "v_cvt_pk_bf16_f32 v202, v136, v137\n"
      "v_mfma_f32_16x16x32_bf16 v[196:199], v[20:23], v[172:175], v[196:199]\n"
      "ds_read_b128 v[176:179], v213 offset:5632\n"
      "v_mul_f32 v139, v159, v139\n"
      "v_mfma_f32_16x16x32_bf16 v[152:155], v[12:15], v[148:151], v[152:155]\n"
      "v_mul_f32 v138, v159, v138\n"
      "v_cvt_pk_bf16_f32 v203, v138, v139\n"
      "v_mfma_f32_16x16x32_bf16 v[152:155], v[24:27], v[172:175], v[152:155]\n"
      "ds_write_b128 v126, v[200:203] offset:4096\n"
      "v_mfma_f32_16x16x32_bf16 v[140:143], v[16:19], v[148:151], v[140:143]\n"
      "ds_read_b128 v[136:139], v213 offset:7040\n"
      "v_mul_f32 v228, v159, v228\n"
      "v_mfma_f32_16x16x32_bf16 v[140:143], v[28:31], v[172:175], v[140:143]\n"
      "v_mul_f32 v229, v159, v229\n"
      "v_cvt_pk_bf16_f32 v228, v228, v229\n"
      "v_mfma_f32_16x16x32_bf16 v[160:163], a[216:219], v[168:171], 0\n"
      "s_waitcnt lgkmcnt(3)\n"
      "v_mul_f32 v230, v159, v230\n"
      "v_mul_f32_e64 v231, v159, v231\n"
      "v_mfma_f32_16x16x32_bf16 v[160:163], a[228:231], v[248:251], v[160:163]\n"
      "ds_read_b128 v[148:151], v213 offset:256\n"
      "v_cvt_pk_bf16_f32 v229, v230, v231\n"
      "v_mfma_f32_16x16x32_bf16 v[180:183], a[220:223], v[168:171], 0\n"
      "ds_read_b128 v[216:219], v213 offset:1664\n"
      "v_fma_f32 v212, v192, v192, s39\n"
      "v_mfma_f32_16x16x32_bf16 v[180:183], a[232:235], v[248:251], v[180:183]\n"
      "ds_write_b64 v127, v[228:229] offset:4096\n"
      "v_mfma_f32_16x16x32_bf16 v[244:247], a[224:227], v[168:171], 0\n"
      "v_fmac_f32 v212, v193, v193\n"
      "v_fmac_f32 v212, v194, v194\n"
      "v_mfma_f32_16x16x32_bf16 v[244:247], a[236:239], v[248:251], v[244:247]\n"
      "ds_read_b128 v[172:175], v213 offset:3072\n"
      "v_fmac_f32 v212, v195, v195\n"
      "v_mfma_f32_16x16x32_bf16 v[160:163], a[240:243], v[164:167], v[160:163]\n"
      "v_fmac_f32 v212, v208, v208\n"
      "v_fmac_f32 v212, v209, v209\n"
      "v_mfma_f32_16x16x32_bf16 v[160:163], a[252:255], v[252:255], v[160:163]\n"
      "ds_read_b128 v[188:191], v213 offset:4480\n"
      "v_fmac_f32 v212, v210, v210\n"
      "v_mfma_f32_16x16x32_bf16 v[180:183], a[244:247], v[164:167], v[180:183]\n"
      "v_fmac_f32 v212, v211, v211\n"
      "v_fmac_f32 v212, v240, v240\n"
      "v_mfma_f32_16x16x32_bf16 v[180:183], v[0:3], v[252:255], v[180:183]\n"
      "v_fmac_f32 v212, v241, v241\n"
      "v_fmac_f32 v212, v242, v242\n"
      "v_mfma_f32_16x16x32_bf16 v[244:247], a[248:251], v[164:167], v[244:247]\n"
      "s_waitcnt lgkmcnt(3)\n"
      "v_fmac_f32 v212, v243, v243\n"
      "v_lshl_add_u32 v167, v214, 2, s29\n"
      "v_mfma_f32_16x16x32_bf16 v[244:247], v[4:7], v[252:255], v[244:247]\n"
      "ds_read_b32 v167, v167\n"
      "v_lshrrev_b32_e64 v166, 3, v214\n"
      "v_mfma_f32_16x16x32_bf16 v[160:163], v[8:11], v[176:179], v[160:163]\n"
      "ds_read_b128 v[184:187], v213 offset:5888\n"
      "v_fma_f32 v214, v196, v196, s39\n"
      "v_mfma_f32_16x16x32_bf16 v[160:163], v[20:23], v[136:139], v[160:163]\n"
      "ds_read_b128 v[248:251], v213 offset:7296\n"
      "v_fmac_f32 v214, v197, v197\n"
      "v_mfma_f32_16x16x32_bf16 v[180:183], v[12:15], v[176:179], v[180:183]\n"
      "s_lshl_b32 s29, s24, 3\n"
      "v_add_u32 v166, s29, v166\n"
      "v_cmp_gt_u32 s[48:49], s26, v166\n"
      "v_mfma_f32_16x16x32_bf16 v[180:183], v[24:27], v[136:139], v[180:183]\n"
      "v_fmac_f32 v214, v198, v198\n"
      "v_fmac_f32_e64 v214, v199, v199\n"
      "v_mfma_f32_16x16x32_bf16 v[244:247], v[16:19], v[176:179], v[244:247]\n"
      "v_fmac_f32 v214, v152, v152\n"
      "v_fmac_f32_e64 v214, v153, v153\n"
      "v_mfma_f32_16x16x32_bf16 v[244:247], v[28:31], v[136:139], v[244:247]\n"
      "s_mov_b32 exec_hi, 0\n"
      "global_store_dwordx4 v118, v[224:227], s[54:55] sc1\n"
      "s_mov_b32 exec_hi, -1\n"
      "v_mfma_f32_16x16x32_bf16 v[252:255], a[216:219], v[148:151], 0\n"
      "v_fmac_f32 v214, v154, v154\n"
      "v_fmac_f32_e64 v214, v155, v155\n"
      "v_mfma_f32_16x16x32_bf16 v[252:255], a[228:231], v[216:219], v[252:255]\n"
      "s_waitcnt lgkmcnt(2)\n"
      "v_fmac_f32 v214, v140, v140\n"
      "v_fmac_f32 v214, v141, v141\n"
      "v_mfma_f32_16x16x32_bf16 v[200:203], a[220:223], v[148:151], 0\n"
      "v_fmac_f32 v214, v142, v142\n"
      "v_mfma_f32_16x16x32_bf16 v[200:203], a[232:235], v[216:219], v[200:203]\n"
      "v_permlane16_swap_b32 v215, v212\n"
      "v_mfma_f32_16x16x32_bf16 v[176:179], a[224:227], v[148:151], 0\n"
      "v_fmac_f32 v214, v143, v143\n"
      "v_mfma_f32_16x16x32_bf16 v[176:179], a[236:239], v[216:219], v[176:179]\n"
      "v_mov_b32 v213, v214\n"
      "v_mfma_f32_16x16x32_bf16 v[176:179], a[248:251], v[172:175], v[176:179]\n"
      "s_nop 0\n"
      "v_permlane16_swap_b32 v214, v213\n"
      "v_mfma_f32_16x16x32_bf16 v[252:255], a[240:243], v[172:175], v[252:255]\n"
      "v_add_f32 v215, v215, v212\n"
      "v_add_f32 v214, v214, v213\n"
      "v_mfma_f32_16x16x32_bf16 v[252:255], a[252:255], v[188:191], v[252:255]\n"
      "s_nop 0\n"
      "v_permlane32_swap_b32 v215, v214\n"
      "v_mfma_f32_16x16x32_bf16 v[200:203], a[244:247], v[172:175], v[200:203]\n"
      "s_waitcnt lgkmcnt(0)\n"
      "v_fma_f32 v213, v160, v160, s39\n"
      "v_add_f32_e64 v215, v215, v214\n"
      "v_mfma_f32_16x16x32_bf16 v[200:203], v[0:3], v[188:191], v[200:203]\n"
      "ds_write_b32 v129, v215\n"
      "v_mfma_f32_16x16x32_bf16 v[200:203], v[12:15], v[184:187], v[200:203]\n"
      "v_fmac_f32 v213, v161, v161\n"
      "v_fmac_f32 v213, v162, v162\n"
      "v_mfma_f32_16x16x32_bf16 v[176:179], v[4:7], v[188:191], v[176:179]\n"
      "v_fmac_f32 v213, v163, v163\n"
      "v_fmac_f32 v213, v180, v180\n"
      "v_mfma_f32_16x16x32_bf16 v[176:179], v[16:19], v[184:187], v[176:179]\n"
      "v_fmac_f32 v213, v181, v181\n"
      "v_fmac_f32 v213, v182, v182\n"
      "v_mfma_f32_16x16x32_bf16 v[176:179], v[28:31], v[248:251], v[176:179]\n"
      "v_mfma_f32_16x16x32_bf16 v[252:255], v[8:11], v[184:187], v[252:255]\n"
      "v_fmac_f32 v213, v183, v183\n"
      "v_mfma_f32_16x16x32_bf16 v[252:255], v[20:23], v[248:251], v[252:255]\n"
      "v_fmac_f32 v213, v244, v244\n"
      "v_fmac_f32 v213, v245, v245\n"
      "v_mfma_f32_16x16x32_bf16 v[200:203], v[24:27], v[248:251], v[200:203]\n"
      "v_fmac_f32 v213, v246, v246\n"
      "v_fmac_f32_e64 v213, v247, v247\n"
      "s_nop 2\n"
      "v_pk_mul_f32 v[214:215], v[252:253], v[252:253]\n"
      "v_pk_fma_f32 v[214:215], v[254:255], v[254:255], v[214:215]\n"
      "s_nop 0\n"
      "v_pk_fma_f32 v[214:215], v[200:201], v[200:201], v[214:215]\n"
      "v_pk_fma_f32 v[214:215], v[202:203], v[202:203], v[214:215]\n"
      "v_pk_fma_f32 v[214:215], v[176:177], v[176:177], v[214:215]\n"
      "v_pk_fma_f32 v[214:215], v[178:179], v[178:179], v[214:215]\n"
      "v_add_f32 v214, s39, v214\n"
      "v_add_f32 v164, v214, v215\n"
      "v_mov_b32 v215, v164\n"
      "v_mov_b32 v212, v215\n"
      "v_permlane16_swap_b32 v213, v164\n"
      "v_add_f32 v213, v213, v164\n"
      "v_permlane16_swap_b32 v215, v212\n"
      "v_add_f32 v215, v215, v212\n"
      "s_nop 1\n"
      "v_permlane32_swap_b32_e64 v213, v215\n"
      "v_add_f32_e64 v213, v213, v215\n"
      "ds_write_b32 v129, v213 offset:2048\n"
      "s_waitcnt lgkmcnt(0)\n"
      "s_barrier\n"
      "ds_read_b128 v[248:251], v128 offset:2048\n"
      "ds_read_b128 v[212:215], v128\n"
      "s_add_u32 s27, s27, 1\n"
      "s_sub_u32 s28, s27, 3\n"
      "s_cselect_b64 s[54:55], s[12:13], s[22:23]\n"
      "ds_read_b128 v[172:175], v120\n"
      "s_max_i32 m0, s28, 0\n"
      "ds_read_b128 v[228:231], v119 offset:6144\n"
      "ds_read_b128 v[216:219], v121\n"
      "s_movrels_b32 s9, s80\n"
      "s_lshr_b32 s8, s9, 8\n"
      "s_and_b32 s9, s9, 0xff\n"
      "s_mul_i32 s28, s8, 1373568\n"
      "s_mul_i32 s29, s9, 2688\n"
      "s_add_u32 s28, s28, s29\n"
      "s_cmp_eq_u32 s8, 72\n"
      "s_cselect_b32 s8, 0, 100270464\n"
      "s_cmp_eq_u32 s9, 72\n"
      "s_cselect_b32 s9, 0, 196224\n"
      "ds_read_b128 v[224:227], v119\n"
      "s_add_u32 s10, s8, s9\n"
      "s_add_u32 s28, s28, s10\n"
      "s_add_u32 s54, s54, s28\n"
      "s_addc_u32 s55, s55, 0\n"
      "s_cmp_lg_u32 s10, s11\n"
      "s_cbranch_scc1 L_edge_46_%=\n"
      "L_eb_47_%=:\n"
      "ds_read_b128 v[184:187], v119 offset:12288\n"
      "s_waitcnt lgkmcnt(5)\n"
      "v_add_f32 v212, |v212|, |v213|\n"
      "v_add_f32 v214, |v214|, |v215|\n"
      "v_add_f32 v212, v212, v214\n"
      "v_rsq_f32 v212, v212\n"
      "s_waitcnt lgkmcnt(3)\n"
      "v_mul_f32 v212, s41, v212\n"
      "v_mov_b32 v213, v212\n"
      "v_add_f32 v250, |v250|, |v251|\n"
      "v_add_f32 v248, |v248|, |v249|\n"
      "v_add_f32 v248, v248, v250\n"
      "v_permlane32_swap_b32 v212, v213\n"
      "v_subrev_u32 v250, 73728, v126\n"
      "v_mov_b32_e64 v251, v212\n"
      "v_pk_mul_f32 v[198:199], v[198:199], v[212:213] op_sel:[0,1]\n"
      "v_pk_mul_f32 v[140:141], v[140:141], v[212:213] op_sel:[0,1]\n"
      "v_rsq_f32 v248, v248\n"
      "v_permlane16_swap_b32 v212, v251\n"
      "v_pk_mul_f32 v[196:197], v[196:197], v[212:213] op_sel:[0,1]\n"
      "v_pk_mul_f32 v[232:233], v[232:233], v[212:213] op_sel_hi:[1,0]\n"
      "v_pk_mul_f32 v[194:195], v[194:195], v[250:251] op_sel:[0,1]\n"
      "v_pk_mul_f32 v[192:193], v[192:193], v[250:251] op_sel:[0,1]\n"
      "v_pk_mul_f32 v[234:235], v[234:235], v[212:213] op_sel_hi:[1,0]\n"
      "v_pk_mul_f32 v[208:209], v[208:209], v[250:251] op_sel:[0,1]\n"
      "v_pk_mul_f32 v[242:243], v[242:243], v[250:251] op_sel:[0,1]\n"
      "v_pk_mul_f32 v[210:211], v[210:211], v[250:251] op_sel:[0,1]\n"
      "v_mul_f32_e64 v248, s41, v248\n"
      "v_pk_mul_f32 v[236:237], v[236:237], v[212:213] op_sel_hi:[1,0]\n"
      "v_pk_mul_f32 v[240:241], v[240:241], v[250:251] op_sel:[0,1]\n"
      "v_mov_b32_e64 v251, v248\n"
      "v_pk_mul_f32 v[204:205], v[204:205], v[212:213] op_sel_hi:[1,0]\n"
      "v_pk_mul_f32 v[154:155], v[154:155], v[212:213] op_sel:[0,1]\n"
      "v_permlane32_swap_b32 v248, v251\n"
      "v_pk_mul_f32 v[142:143], v[142:143], v[212:213] op_sel:[0,1]\n"
      "v_cvt_pk_bf16_f32 v196, v196, v197\n"
      "s_waitcnt lgkmcnt(0)\n"
      "v_mov_b32_e64 v251, v248\n"
      "v_cvt_pk_bf16_f32 v232, v232, v233\n"
      "v_cvt_pk_bf16_f32 v233, v234, v235\n"
      "v_cvt_pk_bf16_f32 v204, v204, v205\n"
      "v_cvt_pk_bf16_f32 v234, v236, v237\n"
      "v_pk_mul_f32 v[238:239], v[238:239], v[212:213] op_sel_hi:[1,0]\n"
      "v_permlane16_swap_b32_e64 v248, v251\n"
      "v_cvt_pk_bf16_f32 v192, v192, v193\n"
      "v_cvt_pk_bf16_f32 v197, v198, v199\n"
      "v_pk_mul_f32 v[202:203], v[202:203], v[250:251] op_sel:[0,1]\n"
      "v_pk_mul_f32 v[244:245], v[244:245], v[248:249] op_sel_hi:[1,0]\n"
      "v_pk_mul_f32 v[162:163], v[162:163], v[248:249] op_sel_hi:[1,0]\n"
      "v_pk_mul_f32 v[182:183], v[182:183], v[248:249] op_sel_hi:[1,0]\n"
      "v_cvt_pk_bf16_f32 v235, v238, v239\n"
      "v_pk_mul_f32 v[176:177], v[176:177], v[250:251] op_sel:[0,1]\n"
      "global_store_dwordx4 v116, v[224:227], s[54:55] sc1\n"
      "ds_write_b128 v126, v[232:235] offset:18432\n"
      "v_pk_mul_f32 v[152:153], v[152:153], v[212:213] op_sel:[0,1]\n"
      "v_pk_mul_f32 v[252:253], v[252:253], v[250:251] op_sel:[0,1]\n"
      "v_cvt_pk_bf16_f32 v193, v194, v195\n"
      "v_pk_mul_f32 v[206:207], v[206:207], v[212:213] op_sel_hi:[1,0]\n"
      "v_subrev_u32 v249, 73728, v127\n"
      "v_cvt_pk_bf16_f32 v176, v176, v177\n"
      "v_pk_mul_f32 v[160:161], v[160:161], v[248:249] op_sel_hi:[1,0]\n"
      "v_cvt_pk_bf16_f32 v240, v240, v241\n"
      "v_cvt_pk_bf16_f32 v194, v208, v209\n"
      "v_pk_mul_f32 v[180:181], v[180:181], v[248:249] op_sel_hi:[1,0]\n"
      "v_pk_mul_f32 v[178:179], v[178:179], v[250:251] op_sel:[0,1]\n"
      "v_pk_mul_f32 v[246:247], v[246:247], v[248:249] op_sel_hi:[1,0]\n"
      "v_cvt_pk_bf16_f32 v140, v140, v141\n"
      "v_cvt_pk_bf16_f32 v241, v242, v243\n"
      "v_pk_mul_f32 v[254:255], v[254:255], v[250:251] op_sel:[0,1]\n"
      "v_cvt_pk_bf16_f32 v199, v154, v155\n"
      "v_cvt_pk_bf16_f32 v252, v252, v253\n"
      "v_cvt_pk_bf16_f32 v244, v244, v245\n"
      "v_cvt_pk_bf16_f32 v160, v160, v161\n"
      "v_cvt_pk_bf16_f32 v161, v162, v163\n"
      "v_cvt_pk_bf16_f32 v205, v206, v207\n"
      "v_cvt_pk_bf16_f32 v163, v182, v183\n"
      "v_pk_mul_f32 v[200:201], v[200:201], v[250:251] op_sel:[0,1]\n"
      "v_cvt_pk_bf16_f32 v253, v254, v255\n"
      "v_cvt_pk_bf16_f32 v162, v180, v181\n"
      "v_cvt_pk_bf16_f32 v254, v200, v201\n"
      "v_cvt_pk_bf16_f32 v198, v152, v153\n"
      "v_cvt_pk_bf16_f32 v141, v142, v143\n"
      "v_cvt_pk_bf16_f32 v195, v210, v211\n"
      "v_cvt_pk_bf16_f32 v255, v202, v203\n"
      "v_cvt_pk_bf16_f32 v177, v178, v179\n"
      "v_cvt_pk_bf16_f32 v245, v246, v247\n"
      "global_store_dwordx4 v116, v[228:231], s[54:55] offset:128 sc1\n"
      "ds_write_b128 v126, v[192:195] offset:20480\n"
      "ds_write_b64 v249, v[176:177] offset:20480\n"
      "ds_write_b128 v126, v[196:199] offset:22528\n"
      "ds_write_b64 v249, v[244:245] offset:18432\n"
      "ds_write_b64 v127, v[140:141] offset:22528\n"
      "global_store_dwordx4 v116, v[184:187], s[54:55] offset:256 sc1\n"
      "global_store_dwordx4 v117, v[172:175], s[54:55] sc1\n"
      "ds_write_b128 v250, v[160:163] offset:18432\n"
      "ds_write_b64 v127, v[240:241] offset:20480\n"
      "ds_write_b64 v127, v[204:205] offset:18432\n"
      "ds_write_b128 v250, v[252:255] offset:20480\n"
      "s_mov_b32 exec_hi, 0\n"
      "global_store_dwordx4 v118, v[216:219], s[54:55] sc1\n"
      "s_mov_b32 exec_hi, -1\n"
      "s_waitcnt lgkmcnt(0)\n"
      "s_barrier\n"
      "ds_read_b128 v[248:251], v119 offset:24576\n"
      "ds_read_b128 v[204:207], v121 offset:18432\n"
      "ds_read_b128 v[232:235], v119 offset:30720\n"
      "ds_read_b128 v[148:151], v119 offset:18432\n"
      "v_subrev_u32 v164, 73728, v119\n"
      "s_add_u32 s27, s27, 1\n"
      "ds_read_b128 v[224:227], v164 offset:24576\n"
      "s_sub_u32 s28, s27, 3\n"
      "s_cselect_b64 s[54:55], s[12:13], s[22:23]\n"
      "s_max_i32 m0, s28, 0\n"
      "ds_read_b128 v[176:179], v164 offset:30720\n"
      "s_movrels_b32 s9, s80\n"
      "s_lshr_b32 s8, s9, 8\n"
      "s_and_b32 s9, s9, 0xff\n"
      "s_mul_i32 s28, s8, 1373568\n"
      "s_mul_i32 s29, s9, 2688\n"
      "s_add_u32 s28, s28, s29\n"
      "s_cmp_eq_u32 s8, 72\n"
      "s_cselect_b32 s8, 0, 100270464\n"
      "ds_read_b128 v[208:211], v120 offset:18432\n"
      "ds_read_b128 v[236:239], v164 offset:18432\n"
      "s_cmp_eq_u32 s9, 72\n"
      "s_cselect_b32 s9, 0, 196224\n"
      "s_add_u32 s10, s8, s9\n"
      "s_add_u32 s28, s28, s10\n"
      "s_add_u32 s54, s54, s28\n"
      "s_addc_u32 s55, s55, 0\n"
      "s_cmp_lg_u32 s10, s11\n"
      "s_cbranch_scc1 L_edge_48_%=\n"
      "L_eb_49_%=:\n"
      "s_waitcnt lgkmcnt(4)\n"
      "global_store_dwordx4 v116, v[148:151], s[54:55] sc1\n"
      "global_store_dwordx4 v116, v[248:251], s[54:55] offset:128 sc1\n"
      "global_store_dwordx4 v116, v[232:235], s[54:55] offset:256 sc1\n"
      "s_waitcnt lgkmcnt(0)\n"
      "global_store_dwordx4 v117, v[208:211], s[54:55] sc1\n"
      "s_mov_b32 exec_hi, 0\n"
      "global_store_dwordx4 v118, v[204:207], s[54:55] sc1\n"
      "s_mov_b32 exec_hi, -1\n"
      "s_nop 0\n"
      "s_mov_b64 exec, s[48:49]\n"
      "s_cbranch_execz L_np_50_%=\n"
      "global_store_dwordx4 v167, v[236:239], s[22:23] sc1\n"
      "global_store_dwordx4 v167, v[224:227], s[22:23] offset:128 sc1\n"
      "global_store_dwordx4 v167, v[176:179], s[22:23] offset:256 sc1\n"
      "L_np_50_%=:\n"
      "s_mov_b64 exec, -1\n"
      "s_endpgm\n"
      "L_gen_8_%=:\n"
      "s_waitcnt lgkmcnt(0)\n"
      "s_nop 5\n"
      "s_cmp_eq_u64 s[34:35], 0\n"
      "s_cbranch_scc1 L_ok_65_%=\n"
      "ds_read_b128 v[248:251], v114\n"
      "ds_read_b128 v[232:235], v115\n"
      "ds_read_b128 v[212:215], v114 offset:12288\n"
      "ds_read_b128 v[252:255], v114 offset:6144\n"
      "ds_read_b128 v[136:139], v115 offset:6144\n"
      "s_waitcnt lgkmcnt(3)\n"
      "v_mfma_f32_16x16x32_bf16 v[188:191], a[0:3], v[248:251], 0\n"
      "s_nop 0\n"
      "v_mfma_f32_16x16x32_bf16 v[188:191], a[36:39], v[232:235], v[188:191]\n"
      "ds_read_b128 v[208:211], v115 offset:12288\n"
      "v_mfma_f32_16x16x32_bf16 v[148:151], a[4:7], v[248:251], 0\n"
      "v_mfma_f32_16x16x32_bf16 v[148:151], a[40:43], v[232:235], v[148:151]\n"
      "v_mfma_f32_16x16x32_bf16 v[216:219], a[8:11], v[248:251], 0\n"
      "v_mfma_f32_16x16x32_bf16 v[216:219], a[44:47], v[232:235], v[216:219]\n"
      "v_mfma_f32_16x16x32_bf16 v[240:243], a[12:15], v[248:251], 0\n"
      "s_waitcnt lgkmcnt(1)\n"
      "v_mfma_f32_16x16x32_bf16 v[240:243], a[48:51], v[232:235], v[240:243]\n"
      "s_nop 0\n"
      "v_mfma_f32_16x16x32_bf16 v[220:223], a[16:19], v[248:251], 0\n"
      "v_mfma_f32_16x16x32_bf16 v[220:223], a[52:55], v[232:235], v[220:223]\n"
      "v_mfma_f32_16x16x32_bf16 v[228:231], a[20:23], v[248:251], 0\n"
      "v_mfma_f32_16x16x32_bf16 v[228:231], a[56:59], v[232:235], v[228:231]\n"
      "v_mfma_f32_16x16x32_bf16 v[188:191], a[72:75], v[252:255], v[188:191]\n"
      "v_mfma_f32_16x16x32_bf16 v[188:191], a[108:111], v[136:139], v[188:191]\n"
      "v_mfma_f32_16x16x32_bf16 v[216:219], a[80:83], v[252:255], v[216:219]\n"
      "v_mfma_f32_16x16x32_bf16 v[216:219], a[116:119], v[136:139], v[216:219]\n"
      "v_mfma_f32_16x16x32_bf16 v[148:151], a[76:79], v[252:255], v[148:151]\n"
      "s_waitcnt lgkmcnt(0)\n"
      "v_mfma_f32_16x16x32_bf16 v[148:151], a[112:115], v[136:139], v[148:151]\n"
      "v_mfma_f32_16x16x32_bf16 v[220:223], a[88:91], v[252:255], v[220:223]\n"
      "s_nop 0\n"
      "v_mfma_f32_16x16x32_bf16 v[220:223], a[124:127], v[136:139], v[220:223]\n"
      "v_mfma_f32_16x16x32_bf16 v[240:243], a[84:87], v[252:255], v[240:243]\n"
      "v_mfma_f32_16x16x32_bf16 v[240:243], a[120:123], v[136:139], v[240:243]\n"
      "v_mfma_f32_16x16x32_bf16 v[188:191], a[144:147], v[212:215], v[188:191]\n"
      "v_mfma_f32_16x16x32_bf16 v[188:191], a[180:183], v[208:211], v[188:191]\n"
      "v_mfma_f32_16x16x32_bf16 v[228:231], a[92:95], v[252:255], v[228:231]\n"
      "v_mfma_f32_16x16x32_bf16 v[228:231], a[128:131], v[136:139], v[228:231]\n"
      "ds_read_b128 v[252:255], v115 offset:2048\n"
      "v_mfma_f32_16x16x32_bf16 v[216:219], a[152:155], v[212:215], v[216:219]\n"
      "v_mfma_f32_16x16x32_bf16 v[148:151], a[148:151], v[212:215], v[148:151]\n"
      "v_mfma_f32_16x16x32_bf16 v[148:151], a[184:187], v[208:211], v[148:151]\n"
      "v_mfma_f32_16x16x32_bf16 v[216:219], a[188:191], v[208:211], v[216:219]\n"
      "s_nop 0\n"
      "v_fma_f32 v158, v188, v188, s38\n"
      "v_mfma_f32_16x16x32_bf16 v[240:243], a[156:159], v[212:215], v[240:243]\n"
      "v_fmac_f32 v158, v189, v189\n"
      "v_fmac_f32_e64 v158, v190, v190\n"
      "v_mfma_f32_16x16x32_bf16 v[240:243], a[192:195], v[208:211], v[240:243]\n"
      "v_fmac_f32 v158, v191, v191\n"
      "v_fmac_f32 v158, v148, v148\n"
      "v_mfma_f32_16x16x32_bf16 v[220:223], a[160:163], v[212:215], v[220:223]\n"
      "v_fmac_f32 v158, v149, v149\n"
      "v_fmac_f32 v158, v150, v150\n"
      "v_mfma_f32_16x16x32_bf16 v[220:223], a[196:199], v[208:211], v[220:223]\n"
      "v_fmac_f32 v158, v151, v151\n"
      "v_fmac_f32 v158, v216, v216\n"
      "v_mfma_f32_16x16x32_bf16 v[228:231], a[164:167], v[212:215], v[228:231]\n"
      "ds_read_b128 v[232:235], v114 offset:2048\n"
      "v_fma_f32 v159, v240, v240, s38\n"
      "v_mfma_f32_16x16x32_bf16 v[228:231], a[200:203], v[208:211], v[228:231]\n"
      "v_fmac_f32 v159, v241, v241\n"
      "v_fmac_f32 v159, v242, v242\n"
      "v_fmac_f32 v159, v243, v243\n"
      "v_fmac_f32 v159, v220, v220\n"
      "v_fmac_f32 v158, v217, v217\n"
      "v_fmac_f32 v159, v221, v221\n"
      "v_fmac_f32 v159, v222, v222\n"
      "v_fmac_f32 v159, v223, v223\n"
      "v_fmac_f32 v159, v228, v228\n"
      "v_fmac_f32 v159, v229, v229\n"
      "v_fmac_f32 v158, v218, v218\n"
      "v_fmac_f32 v158, v219, v219\n"
      "v_fmac_f32 v159, v230, v230\n"
      "v_fmac_f32 v159, v231, v231\n"
      "s_nop 1\n"
      "v_permlane16_swap_b32 v158, v159\n"
      "v_add_f32 v158, v158, v159\n"
      "v_mov_b32 v159, v158\n"
      "s_nop 1\n"
      "v_permlane32_swap_b32 v158, v159\n"
      "v_add_f32 v158, v158, v159\n"
      "v_rsq_f32 v158, v158\n"
      "s_waitcnt lgkmcnt(0)\n"
      "v_mov_b32 v159, v158\n"
      "v_mfma_f32_16x16x32_bf16 v[204:207], a[0:3], v[232:235], 0\n"
      "ds_read_b128 v[248:251], v114 offset:8192\n"
      "v_permlane16_swap_b32 v158, v159\n"
      "v_mfma_f32_16x16x32_bf16 v[204:207], a[36:39], v[252:255], v[204:207]\n"
      "ds_read_b128 v[212:215], v115 offset:8192\n"
      "v_mul_f32 v241, v159, v241\n"
      "v_mul_f32_e64 v240, v159, v240\n"
      "v_mfma_f32_16x16x32_bf16 v[164:167], a[12:15], v[232:235], 0\n"
      "ds_read_b128 v[136:139], v114 offset:14336\n"
      "v_cvt_pk_f16_f32 v240, v240, v241\n"
      "v_mfma_f32_16x16x32_bf16 v[164:167], a[48:51], v[252:255], v[164:167]\n"
      "ds_read_b128 v[208:211], v115 offset:14336\n"
      "v_mul_f32 v242, v159, v242\n"
      "v_mfma_f32_16x16x32_bf16 v[176:179], a[20:23], v[232:235], 0\n"
      "v_mul_f32 v220, v159, v220\n"
      "v_mul_f32_e64 v243, v159, v243\n"
      "v_mfma_f32_16x16x32_bf16 v[176:179], a[56:59], v[252:255], v[176:179]\n"
      "v_cvt_pk_f16_f32 v241, v242, v243\n"
      "v_mul_f32 v221, v159, v221\n"
      "v_mul_f32 v222, v159, v222\n"
      "v_mul_f32 v223, v159, v223\n"
      "v_mul_f32 v231, v159, v231\n"
      "v_cvt_pk_f16_f32 v243, v222, v223\n"
      "v_cvt_pk_f16_f32 v242, v220, v221\n"
      "v_mfma_f32_16x16x32_bf16 v[220:223], a[16:19], v[232:235], 0\n"
      "v_mul_f32 v228, v159, v228\n"
      "v_mul_f32 v229, v159, v229\n"
      "v_mfma_f32_16x16x32_bf16 v[220:223], a[52:55], v[252:255], v[220:223]\n"
      "v_cvt_pk_f16_f32 v34, v228, v229\n"
      "v_mul_f32 v230, v159, v230\n"
      "s_waitcnt lgkmcnt(2)\n"
      "v_mfma_f32_16x16x32_bf16 v[204:207], a[72:75], v[248:251], v[204:207]\n"
      "v_mul_f32 v158, s36, v158\n"
      "v_mul_f32 v188, v158, v188\n"
      "v_mfma_f32_16x16x32_bf16 v[204:207], a[108:111], v[212:215], v[204:207]\n"
      "v_mul_f32 v189, v158, v189\n"
      "v_cvt_pk_f16_f32 v35, v230, v231\n"
      "v_mul_f32 v191, v158, v191\n"
      "v_mfma_f32_16x16x32_bf16 v[164:167], a[84:87], v[248:251], v[164:167]\n"
      "s_waitcnt lgkmcnt(0)\n"
      "v_cvt_pk_f16_f32 v188, v188, v189\n"
      "v_mul_f32 v190, v158, v190\n"
      "v_mfma_f32_16x16x32_bf16 v[164:167], a[120:123], v[212:215], v[164:167]\n"
      "v_mul_f32 v216, v158, v216\n"
      "v_mul_f32 v148, v158, v148\n"
      "v_mfma_f32_16x16x32_bf16 v[220:223], a[88:91], v[248:251], v[220:223]\n"
      "v_cvt_pk_f16_f32 v189, v190, v191\n"
      "v_mul_f32_e64 v149, v158, v149\n"
      "v_mfma_f32_16x16x32_bf16 v[220:223], a[124:127], v[212:215], v[220:223]\n"
      "v_mul_f32 v151, v158, v151\n"
      "v_cvt_pk_f16_f32 v190, v148, v149\n"
      "v_mfma_f32_16x16x32_bf16 v[176:179], a[92:95], v[248:251], v[176:179]\n"
      "v_mul_f32 v150, v158, v150\n"
      "v_mfma_f32_16x16x32_bf16 v[176:179], a[128:131], v[212:215], v[176:179]\n"
      "v_cvt_pk_f16_f32 v191, v150, v151\n"
      "v_mul_f32_e64 v217, v158, v217\n"
      "v_mfma_f32_16x16x32_bf16 v[148:151], a[4:7], v[232:235], 0\n"
      "v_mfma_f32_16x16x32_bf16 v[148:151], a[40:43], v[252:255], v[148:151]\n"
      "v_mul_f32_e64 v218, v158, v218\n"
      "v_mul_f32_e64 v219, v158, v219\n"
      "v_mfma_f32_16x16x32_bf16 v[148:151], a[76:79], v[248:251], v[148:151]\n"
      "v_mfma_f32_16x16x32_bf16 v[148:151], a[112:115], v[212:215], v[148:151]\n"
      "v_cvt_pk_f16_f32 v33, v218, v219\n"
      "v_cvt_pk_f16_f32 v32, v216, v217\n"
      "v_mfma_f32_16x16x32_bf16 v[216:219], a[8:11], v[232:235], 0\n"
      "v_mfma_f32_16x16x32_bf16 v[216:219], a[44:47], v[252:255], v[216:219]\n"
      "v_mfma_f32_16x16x32_bf16 v[216:219], a[80:83], v[248:251], v[216:219]\n"
      "v_mfma_f32_16x16x32_bf16 v[216:219], a[116:119], v[212:215], v[216:219]\n"
      "ds_read_b128 v[248:251], v115 offset:4096\n"
      "v_mfma_f32_16x16x32_bf16 v[204:207], a[144:147], v[136:139], v[204:207]\n"
      "v_mfma_f32_16x16x32_bf16 v[204:207], a[180:183], v[208:211], v[204:207]\n"
      "v_mfma_f32_16x16x32_bf16 v[148:151], a[148:151], v[136:139], v[148:151]\n"
      "v_mfma_f32_16x16x32_bf16 v[148:151], a[184:187], v[208:211], v[148:151]\n"
      "v_mfma_f32_16x16x32_bf16 v[216:219], a[152:155], v[136:139], v[216:219]\n"
      "v_mfma_f32_16x16x32_bf16 v[216:219], a[188:191], v[208:211], v[216:219]\n"
      "v_mfma_f32_16x16x32_bf16 v[164:167], a[156:159], v[136:139], v[164:167]\n"
      "v_mfma_f32_16x16x32_bf16 v[164:167], a[192:195], v[208:211], v[164:167]\n"
      "s_nop 1\n"
      "v_fma_f32 v158, v204, v204, s38\n"
      "v_mfma_f32_16x16x32_bf16 v[220:223], a[160:163], v[136:139], v[220:223]\n"
      "v_fmac_f32 v158, v205, v205\n"
      "v_fmac_f32_e64 v158, v206, v206\n"
      "v_mfma_f32_16x16x32_bf16 v[220:223], a[196:199], v[208:211], v[220:223]\n"
      "v_fmac_f32 v158, v207, v207\n"
      "v_fma_f32 v159, v164, v164, s38\n"
      "v_fmac_f32 v159, v165, v165\n"
      "v_mfma_f32_16x16x32_bf16 v[176:179], a[164:167], v[136:139], v[176:179]\n"
      "ds_read_b128 v[232:235], v114 offset:4096\n"
      "v_fmac_f32 v159, v166, v166\n"
      "v_mfma_f32_16x16x32_bf16 v[176:179], a[200:203], v[208:211], v[176:179]\n"
      "v_fmac_f32 v159, v167, v167\n"
      "v_fmac_f32 v158, v148, v148\n"
      "v_fmac_f32 v158, v149, v149\n"
      "v_fmac_f32 v159, v220, v220\n"
      "v_fmac_f32 v158, v150, v150\n"
      "v_fmac_f32 v158, v151, v151\n"
      "v_fmac_f32 v159, v221, v221\n"
      "v_fmac_f32 v159, v222, v222\n"
      "v_fmac_f32 v159, v223, v223\n"
      "v_fmac_f32 v158, v216, v216\n"
      "v_fmac_f32 v158, v217, v217\n"
      "v_fmac_f32 v158, v218, v218\n"
      "v_fmac_f32 v159, v176, v176\n"
      "v_fmac_f32 v159, v177, v177\n"
      "v_fmac_f32 v159, v178, v178\n"
      "v_fmac_f32 v159, v179, v179\n"
      "v_fmac_f32 v158, v219, v219\n"
      "s_nop 1\n"
      "v_permlane16_swap_b32 v158, v159\n"
      "v_add_f32 v158, v158, v159\n"
      "v_mov_b32 v159, v158\n"
      "s_nop 1\n"
      "v_permlane32_swap_b32 v158, v159\n"
      "v_add_f32 v158, v158, v159\n"
      "v_rsq_f32_e64 v158, v158\n"
      "s_waitcnt lgkmcnt(0)\n"
      "v_mfma_f32_16x16x32_bf16 v[172:175], a[0:3], v[232:235], 0\n"
      "ds_read_b128 v[252:255], v114 offset:10240\n"
      "v_mov_b32_e64 v159, v158\n"
      "v_mfma_f32_16x16x32_bf16 v[172:175], a[36:39], v[248:251], v[172:175]\n"
      "ds_read_b128 v[136:139], v115 offset:10240\n"
      "v_permlane16_swap_b32 v158, v159\n"
      "v_mfma_f32_16x16x32_bf16 v[200:203], a[12:15], v[232:235], 0\n"
      "ds_read_b128 v[208:211], v114 offset:16384\n"
      "v_mul_f32 v164, v159, v164\n"
      "v_mfma_f32_16x16x32_bf16 v[200:203], a[48:51], v[248:251], v[200:203]\n"
      "ds_read_b128 v[212:215], v115 offset:16384\n"
      "v_mul_f32 v222, v159, v222\n"
      "v_mfma_f32_16x16x32_bf16 v[228:231], a[20:23], v[232:235], 0\n"
      "v_mul_f32 v167, v159, v167\n"
      "v_mul_f32 v166, v159, v166\n"
      "v_mfma_f32_16x16x32_bf16 v[228:231], a[56:59], v[248:251], v[228:231]\n"
      "v_mul_f32 v165, v159, v165\n"
      "v_cvt_pk_f16_f32 v164, v164, v165\n"
      "v_mul_f32 v220, v159, v220\n"
      "v_mul_f32 v221, v159, v221\n"
      "v_mul_f32_e64 v223, v159, v223\n"
      "v_cvt_pk_f16_f32 v165, v166, v167\n"
      "v_cvt_pk_f16_f32 v167, v222, v223\n"
      "v_cvt_pk_f16_f32 v166, v220, v221\n"
      "v_mfma_f32_16x16x32_bf16 v[220:223], a[16:19], v[232:235], 0\n"
      "v_mul_f32 v176, v159, v176\n"
      "v_mul_f32 v177, v159, v177\n"
      "v_mfma_f32_16x16x32_bf16 v[220:223], a[52:55], v[248:251], v[220:223]\n"
      "v_mul_f32 v179, v159, v179\n"
      "s_waitcnt lgkmcnt(2)\n"
      "v_mfma_f32_16x16x32_bf16 v[172:175], a[72:75], v[252:255], v[172:175]\n"
      "v_cvt_pk_f16_f32 v40, v176, v177\n"
      "v_mul_f32 v178, v159, v178\n"
      "v_mfma_f32_16x16x32_bf16 v[172:175], a[108:111], v[136:139], v[172:175]\n"
      "v_mul_f32 v158, s36, v158\n"
      "v_cvt_pk_f16_f32 v41, v178, v179\n"
      "v_mfma_f32_16x16x32_bf16 v[220:223], a[88:91], v[252:255], v[220:223]\n"
      "v_mul_f32 v204, v158, v204\n"
      "v_mul_f32 v205, v158, v205\n"
      "v_mfma_f32_16x16x32_bf16 v[220:223], a[124:127], v[136:139], v[220:223]\n"
      "v_mul_f32 v206, v158, v206\n"
      "v_cvt_pk_f16_f32 v204, v204, v205\n"
      "v_mfma_f32_16x16x32_bf16 v[200:203], a[84:87], v[252:255], v[200:203]\n"
      "v_mul_f32 v149, v158, v149\n"
      "v_mul_f32_e64 v207, v158, v207\n"
      "v_mfma_f32_16x16x32_bf16 v[200:203], a[120:123], v[136:139], v[200:203]\n"
      "v_cvt_pk_f16_f32 v205, v206, v207\n"
      "v_mul_f32 v148, v158, v148\n"
      "v_mfma_f32_16x16x32_bf16 v[228:231], a[92:95], v[252:255], v[228:231]\n"
      "s_waitcnt lgkmcnt(0)\n"
      "v_mul_f32 v150, v158, v150\n"
      "v_cvt_pk_f16_f32 v206, v148, v149\n"
      "v_mfma_f32_16x16x32_bf16 v[228:231], a[128:131], v[136:139], v[228:231]\n"
      "v_mul_f32 v151, v158, v151\n"
      "v_cvt_pk_f16_f32 v207, v150, v151\n"
      "v_mfma_f32_16x16x32_bf16 v[148:151], a[4:7], v[232:235], 0\n"
      "v_mul_f32 v216, v158, v216\n"
      "v_mul_f32 v217, v158, v217\n"
      "v_mfma_f32_16x16x32_bf16 v[148:151], a[40:43], v[248:251], v[148:151]\n"
      "v_mul_f32_e64 v218, v158, v218\n"
      "v_mfma_f32_16x16x32_bf16 v[148:151], a[76:79], v[252:255], v[148:151]\n"
      "v_cvt_pk_f16_f32 v38, v216, v217\n"
      "v_mfma_f32_16x16x32_bf16 v[148:151], a[112:115], v[136:139], v[148:151]\n"
      "v_mul_f32_e64 v219, v158, v219\n"
      "v_cvt_pk_f16_f32 v39, v218, v219\n"
      "v_mfma_f32_16x16x32_bf16 v[216:219], a[8:11], v[232:235], 0\n"
      "v_mfma_f32_16x16x32_bf16 v[216:219], a[44:47], v[248:251], v[216:219]\n"
      "v_mfma_f32_16x16x32_bf16 v[216:219], a[80:83], v[252:255], v[216:219]\n"
      "v_mfma_f32_16x16x32_bf16 v[216:219], a[116:119], v[136:139], v[216:219]\n"
      "v_mfma_f32_16x16x32_bf16 v[172:175], a[144:147], v[208:211], v[172:175]\n"
      "v_mfma_f32_16x16x32_bf16 v[172:175], a[180:183], v[212:215], v[172:175]\n"
      "v_mfma_f32_16x16x32_bf16 v[148:151], a[148:151], v[208:211], v[148:151]\n"
      "v_mfma_f32_16x16x32_bf16 v[148:151], a[184:187], v[212:215], v[148:151]\n"
      "v_mfma_f32_16x16x32_bf16 v[216:219], a[152:155], v[208:211], v[216:219]\n"
      "v_mfma_f32_16x16x32_bf16 v[216:219], a[188:191], v[212:215], v[216:219]\n"
      "v_mfma_f32_16x16x32_bf16 v[200:203], a[156:159], v[208:211], v[200:203]\n"
      "v_mfma_f32_16x16x32_bf16 v[200:203], a[192:195], v[212:215], v[200:203]\n"
      "s_nop 1\n"
      "v_fma_f32 v158, v172, v172, s38\n"
      "v_mfma_f32_16x16x32_bf16 v[220:223], a[160:163], v[208:211], v[220:223]\n"
      "v_fmac_f32 v158, v173, v173\n"
      "v_fmac_f32 v158, v174, v174\n"
      "v_mfma_f32_16x16x32_bf16 v[220:223], a[196:199], v[212:215], v[220:223]\n"
      "s_nop 0\n"
      "v_fma_f32 v159, v200, v200, s38\n"
      "v_fmac_f32_e64 v159, v201, v201\n"
      "v_mfma_f32_16x16x32_bf16 v[228:231], a[164:167], v[208:211], v[228:231]\n"
      "v_fmac_f32 v159, v202, v202\n"
      "v_fmac_f32 v158, v175, v175\n"
      "v_mfma_f32_16x16x32_bf16 v[228:231], a[200:203], v[212:215], v[228:231]\n"
      "v_fmac_f32 v158, v148, v148\n"
      "v_fmac_f32 v159, v203, v203\n"
      "v_mfma_f32_16x16x32_f16 v[232:235], v[196:199], v[188:191], v[70:73]\n"
      "v_fmac_f32 v158, v149, v149\n"
      "v_fmac_f32 v158, v150, v150\n"
      "v_mfma_f32_16x16x32_f16 v[232:235], v[180:183], v[240:243], v[232:235]\n"
      "v_fmac_f32 v159, v220, v220\n"
      "v_fmac_f32 v159, v221, v221\n"
      "v_mfma_f32_16x16x32_f16 v[232:235], v[224:227], v[32:35], v[232:235]\n"
      "v_fmac_f32 v158, v151, v151\n"
      "v_fmac_f32 v159, v222, v222\n"
      "v_fmac_f32 v159, v223, v223\n"
      "v_fmac_f32 v158, v216, v216\n"
      "v_fmac_f32 v159, v228, v228\n"
      "v_fmac_f32 v159, v229, v229\n"
      "v_fmac_f32 v158, v217, v217\n"
      "v_fmac_f32 v158, v218, v218\n"
      "v_fmac_f32 v159, v230, v230\n"
      "v_fmac_f32 v159, v231, v231\n"
      "v_fmac_f32 v158, v219, v219\n"
      "v_mov_b32 v157, v233\n"
      "v_mov_b32 v156, v232\n"
      "v_permlane16_swap_b32 v158, v159\n"
      "v_add_f32 v158, v158, v159\n"
      "v_mov_b32 v159, v158\n"
      "s_nop 1\n"
      "v_permlane32_swap_b32 v158, v159\n"
      "v_add_f32 v158, v158, v159\n"
      "v_rsq_f32 v158, v158\n"
      "s_nop 0\n"
      "v_mov_b32 v159, v158\n"
      "s_nop 1\n"
      "v_permlane16_swap_b32 v158, v159\n"
      "v_mul_f32 v222, v159, v222\n"
      "v_mul_f32 v203, v159, v203\n"
      "v_mul_f32 v228, v159, v228\n"
      "v_mul_f32 v230, v159, v230\n"
      "v_mul_f32 v202, v159, v202\n"
      "v_mul_f32 v200, v159, v200\n"
      "v_mul_f32 v201, v159, v201\n"
      "v_mul_f32 v220, v159, v220\n"
      "v_cvt_pk_f16_f32 v200, v200, v201\n"
      "v_mul_f32 v158, s36, v158\n"
      "v_cvt_pk_f16_f32 v201, v202, v203\n"
      "v_mul_f32 v221, v159, v221\n"
      "v_mul_f32 v223, v159, v223\n"
      "v_mul_f32 v151, v158, v151\n"
      "v_mul_f32 v150, v158, v150\n"
      "v_mul_f32 v149, v158, v149\n"
      "v_mul_f32 v231, v159, v231\n"
      "v_mul_f32 v229, v159, v229\n"
      "v_mul_f32_e64 v172, v158, v172\n"
      "v_cvt_pk_f16_f32 v46, v228, v229\n"
      "v_cvt_pk_f16_f32 v203, v222, v223\n"
      "v_cvt_pk_f16_f32 v202, v220, v221\n"
      "v_mul_f32 v216, v158, v216\n"
      "v_mul_f32 v175, v158, v175\n"
      "v_cvt_pk_f16_f32 v47, v230, v231\n"
      "v_mul_f32 v218, v158, v218\n"
      "v_mul_f32 v148, v158, v148\n"
      "v_mul_f32 v174, v158, v174\n"
      "v_mfma_f32_16x16x32_f16 v[220:223], v[200:203], v[188:191], v[86:89]\n"
      "v_mul_f32 v173, v158, v173\n"
      "v_cvt_pk_f16_f32 v172, v172, v173\n"
      "v_mfma_f32_16x16x32_f16 v[220:223], v[46:49], v[32:35], v[220:223]\n"
      "v_cvt_pk_f16_f32 v173, v174, v175\n"
      "v_cvt_pk_f16_f32 v174, v148, v149\n"
      "v_cvt_pk_f16_f32 v175, v150, v151\n"
      "v_mfma_f32_16x16x32_f16 v[148:151], v[240:243], v[188:191], v[78:81]\n"
      "v_mul_f32_e64 v217, v158, v217\n"
      "v_cvt_pk_f16_f32 v44, v216, v217\n"
      "v_mfma_f32_16x16x32_f16 v[148:151], v[34:37], v[32:35], v[148:151]\n"
      "v_mul_f32_e64 v219, v158, v219\n"
      "v_cvt_pk_f16_f32 v45, v218, v219\n"
      "v_mfma_f32_16x16x32_f16 v[216:219], v[164:167], v[188:191], v[82:85]\n"
      "v_mfma_f32_16x16x32_f16 v[216:219], v[40:43], v[32:35], v[216:219]\n"
      "s_nop 7\n"
      "s_or_b64 vcc, s[48:49], s[50:51]\n"
      "s_cbranch_scc0 L_fast_11_%=\n"
      "L_ok_65_%=:\n"
      "s_cmp_eq_u64 s[48:49], 0\n"
      "s_cbranch_scc1 L_go_66_%=\n"
      "s_mov_b32 m0, s27\n"
      "s_nop 0\n"
      "s_movrels_b32 s9, s80\n"
      "s_lshr_b32 s8, s9, 8\n"
      "s_and_b32 s9, s9, 0xff\n"
      "s_mul_i32 s10, s8, 73\n"
      "s_add_u32 s10, s10, s9\n"
      "s_mul_i32 s30, s10, 4802\n"
      "s_add_u32 s30, s30, s18\n"
      "s_and_b32 s29, s30, 15\n"
      "s_add_u32 s30, s29, 110592\n"
      "s_lshl_b64 s[34:35], s[48:49], 4\n"
      "s_lshr_b64 s[34:35], s[34:35], 5\n"
      "s_cbranch_scc1 L_gen_9_%=\n"
      "v_mbcnt_lo_u32_b32 v158, -1, 0\n"
      "v_mbcnt_hi_u32_b32 v158, -1, v158\n"
      "v_lshlrev_b32 v159, 1, v158\n"
      "v_add_u32 v233, 4802, v159\n"
      "v_cmp_gt_u32 vcc, s29, v159\n"
      "s_nop 1\n"
      "v_cndmask_b32 v159, v233, v159, vcc\n"
      "v_mov_b32 v233, 0\n"
      "v_add_u32 v159, 110592, v159\n"
      "s_mov_b64 exec, 0x7f\n"
      "ds_write_b16 v159, v233\n"
      "s_mov_b64 exec, -1\n"
      "v_lshlrev_b32 v159, 4, v158\n"
      "v_add_u32 v159, 110592, v159\n"
      "ds_read_b128 v[232:235], v159\n"
      "ds_read_b128 v[248:251], v159 offset:1024\n"
      "v_min_u32 v158, 111296, v159\n"
      "ds_read_b128 v[252:255], v159 offset:2048\n"
      "ds_read_b128 v[188:191], v158 offset:4096\n"
      "ds_read_b128 v[212:215], v159 offset:3072\n"
      "s_waitcnt lgkmcnt(4)\n"
      "v_or3_b32 v232, v232, v233, v234\n"
      "s_waitcnt lgkmcnt(3)\n"
      "v_or3_b32 v232, v232, v235, v248\n"
      "v_or3_b32 v232, v232, v249, v250\n"
      "s_waitcnt lgkmcnt(2)\n"
      "v_or3_b32 v232, v232, v251, v252\n"
      "v_or3_b32 v232, v232, v253, v254\n"
      "s_waitcnt lgkmcnt(0)\n"
      "v_or3_b32 v232, v232, v255, v212\n"
      "v_or3_b32 v232, v232, v213, v214\n"
      "v_or3_b32 v232, v232, v215, v188\n"
      "v_or3_b32 v232, v232, v189, v190\n"
      "v_bitop3_b32 v232, v232, v191, s40 bitop3:0xa8\n"
      "v_cmp_ne_u32 s[48:49], 0, v232\n"
      "s_cmp_lg_u64 s[48:49], 0\n"
      "s_cbranch_scc1 L_gen_9_%=\n"
      "s_cmp_eq_u64 s[50:51], 0\n"
      "s_cbranch_scc1 L_fast_11_%=\n"
      "L_go_66_%=:\n"
      "v_mfma_f32_16x16x32_f16 v[232:235], v[240:243], v[204:207], v[90:93]\n"
      "ds_read_b128 v[248:251], v128 offset:3072\n"
      "v_max3_f32 v159, v151, v216, v217\n"
      "v_mfma_f32_16x16x32_f16 v[232:235], v[34:37], v[38:41], v[232:235]\n"
      "v_max3_f32 v158, v148, v149, v150\n"
      "v_max3_f32 v158, v158, v218, v219\n"
      "v_mfma_f32_16x16x32_f16 v[252:255], v[164:167], v[204:207], v[94:97]\n"
      "v_max3_f32 v159, v159, v220, v221\n"
      "v_max3_f32 v158, v158, v222, v223\n"
      "v_mfma_f32_16x16x32_f16 v[252:255], v[40:43], v[38:41], v[252:255]\n"
      "v_max3_f32 v158, v158, v159, v156\n"
      "v_mov_b32_e64 v159, v158\n"
      "v_mfma_f32_16x16x32_f16 v[212:215], v[196:199], v[204:207], v[72:75]\n"
      "s_nop 0\n"
      "v_permlane16_swap_b32 v158, v159\n"
      "v_mfma_f32_16x16x32_f16 v[212:215], v[180:183], v[164:167], v[212:215]\n"
      "v_max_f32 v158, v158, v159\n"
      "v_mov_b32 v159, v158\n"
      "v_mfma_f32_16x16x32_f16 v[212:215], v[224:227], v[38:41], v[212:215]\n"
      "s_nop 0\n"
      "v_permlane32_swap_b32 v158, v159\n"
      "v_mfma_f32_16x16x32_f16 v[188:191], v[200:203], v[204:207], v[98:101]\n"
      "v_max_f32 v158, v158, v159\n"
      "v_sub_f32 v148, v148, v158\n"
      "v_mfma_f32_16x16x32_f16 v[188:191], v[46:49], v[38:41], v[188:191]\n"
      "v_sub_f32 v216, v216, v158\n"
      "v_sub_f32 v149, v149, v158\n"
      "v_mfma_f32_16x16x32_f16 v[228:231], v[240:243], v[172:175], v[102:105]\n"
      "v_sub_f32 v151, v151, v158\n"
      "v_sub_f32 v217, v217, v158\n"
      "v_mfma_f32_16x16x32_f16 v[228:231], v[34:37], v[44:47], v[228:231]\n"
      "v_exp_f32 v149, v149\n"
      "v_sub_f32 v218, v218, v158\n"
      "v_exp_f32 v216, v216\n"
      "v_exp_f32 v151, v151\n"
      "v_mfma_f32_16x16x32_f16 v[136:139], v[164:167], v[172:175], v[106:109]\n"
      "v_sub_f32 v219, v219, v158\n"
      "v_sub_f32 v223, v223, v158\n"
      "v_mfma_f32_16x16x32_f16 v[136:139], v[40:43], v[44:47], v[136:139]\n"
      "v_sub_f32 v150, v150, v158\n"
      "v_sub_f32 v221, v221, v158\n"
      "v_sub_f32_e64 v220, v220, v158\n"
      "v_mfma_f32_16x16x32_f16 v[208:211], v[196:199], v[172:175], v[74:77]\n"
      "v_sub_f32 v222, v222, v158\n"
      "v_sub_f32 v156, v156, v158\n"
      "v_mfma_f32_16x16x32_f16 v[208:211], v[180:183], v[200:203], v[208:211]\n"
      "v_exp_f32_e64 v148, v148\n"
      "v_mfma_f32_16x16x32_f16 v[208:211], v[224:227], v[44:47], v[208:211]\n"
      "v_exp_f32 v217, v217\n"
      "v_mfma_f32_16x16x32_f16 v[176:179], v[200:203], v[172:175], v[110:113]\n"
      "s_waitcnt lgkmcnt(0)\n"
      "v_exp_f32_e64 v150, v150\n"
      "v_mfma_f32_16x16x32_f16 v[176:179], v[46:49], v[44:47], v[176:179]\n"
      "v_cvt_pk_bf16_f32 v148, v148, v149\n"
      "v_exp_f32_e64 v222, v222\n"
      "v_mfma_f32_16x16x32_f16 v[200:203], v[180:183], v[196:199], v[76:79]\n"
      "v_cvt_pk_bf16_f32 v149, v150, v151\n"
      "v_bitop3_b32 v250, v250, v251, s4 bitop3:0x7e\n"
      "v_exp_f32 v223, v223\n"
      "v_exp_f32 v220, v220\n"
      "v_exp_f32 v219, v219\n"
      "v_exp_f32 v218, v218\n"
      "v_mfma_f32_16x16x16_f16 v[200:203], v[226:227], v[224:225], v[200:203]\n"
      "v_exp_f32_e64 v156, v156\n"
      "v_bitop3_b32 v248, v248, v249, s4 bitop3:0x7e\n"
      "v_cvt_pk_bf16_f32 v150, v216, v217\n"
      "v_cvt_pk_bf16_f32 v63, v222, v223\n"
      "v_or_b32 v248, v248, v250\n"
      "v_exp_f32 v221, v221\n"
      "v_cvt_pk_bf16_f32 v64, v156, 0\n"
      "v_cvt_pk_bf16_f32 v151, v218, v219\n"
      "v_cmp_ne_u32 vcc, 0, v248\n"
      "v_cvt_pk_bf16_f32 v62, v220, v221\n"
      "v_mfma_f32_16x16x32_bf16 v[220:223], v[66:69], v[148:151], 0\n"
      "s_mov_b64 s[48:49], vcc\n"
      "v_max3_f32 v210, v232, v233, v234\n"
      "v_max3_f32 v210, v210, v254, v255\n"
      "v_mfma_f32_16x16x32_bf16 v[220:223], v[66:69], v[62:65], v[220:223]\n"
      "s_cbranch_vccnz L_flg_67_%=\n"
      "L_fb_68_%=:\n"
      "v_mfma_f32_16x16x32_bf16 v[248:251], v[168:171], v[148:151], 0\n"
      "s_nop 0\n"
      "v_max3_f32 v211, v235, v252, v253\n"
      "v_max3_f32 v211, v211, v188, v189\n"
      "v_mfma_f32_16x16x32_bf16 v[248:251], v[50:53], v[62:65], v[248:251]\n"
      "v_max3_f32 v210, v210, v190, v191\n"
      "v_max3_f32 v210, v210, v211, v212\n"
      "v_mfma_f32_16x16x32_bf16 v[172:175], v[184:187], v[148:151], 0\n"
      "v_mov_b32_e64 v211, v210\n"
      "v_max3_f32 v214, v231, v136, v137\n"
      "v_mfma_f32_16x16x32_bf16 v[172:175], v[54:57], v[62:65], v[172:175]\n"
      "v_permlane16_swap_b32 v210, v211\n"
      "v_mfma_f32_16x16x32_bf16 v[216:219], v[152:155], v[148:151], 0\n"
      "v_max_f32 v210, v210, v211\n"
      "v_mov_b32_e64 v211, v210\n"
      "v_mfma_f32_16x16x32_bf16 v[216:219], v[58:61], v[62:65], v[216:219]\n"
      "s_nop 0\n"
      "v_permlane32_swap_b32 v210, v211\n"
      "v_max_f32 v210, v210, v211\n"
      "v_sub_f32 v212, v212, v210\n"
      "v_sub_f32 v190, v190, v210\n"
      "v_sub_f32 v189, v189, v210\n"
      "v_rcp_f32 v220, v220\n"
      "v_sub_f32 v234, v234, v210\n"
      "v_sub_f32 v188, v188, v210\n"
      "v_sub_f32 v232, v232, v210\n"
      "v_sub_f32 v253, v253, v210\n"
      "v_sub_f32 v191, v191, v210\n"
      "v_sub_f32 v235, v235, v210\n"
      "v_sub_f32 v254, v254, v210\n"
      "v_sub_f32 v255, v255, v210\n"
      "v_sub_f32 v252, v252, v210\n"
      "v_exp_f32 v212, v212\n"
      "v_exp_f32 v253, v253\n"
      "v_mul_f32 v249, v220, v249\n"
      "v_exp_f32 v232, v232\n"
      "v_exp_f32 v234, v234\n"
      "v_exp_f32 v254, v254\n"
      "v_exp_f32 v255, v255\n"
      "v_exp_f32 v189, v189\n"
      "v_mul_f32 v248, v220, v248\n"
      "v_sub_f32 v233, v233, v210\n"
      "v_exp_f32 v252, v252\n"
      "v_exp_f32 v188, v188\n"
      "v_exp_f32 v233, v233\n"
      "v_exp_f32 v235, v235\n"
      "v_cvt_pk_bf16_f32 v232, v232, v233\n"
      "v_cvt_pk_bf16_f32 v233, v234, v235\n"
      "v_cvt_pk_bf16_f32 v235, v254, v255\n"
      "v_exp_f32 v191, v191\n"
      "v_exp_f32 v190, v190\n"
      "v_cvt_pk_bf16_f32 v64, v212, 0\n"
      "v_cvt_pk_bf16_f32 v234, v252, v253\n"
      "v_cvt_pk_bf16_f32 v62, v188, v189\n"
      "v_cvt_pk_bf16_f32 v63, v190, v191\n"
      "v_mfma_f32_16x16x32_bf16 v[158:161], v[66:69], v[232:235], 0\n"
      "s_add_u32 s28, s27, 2\n"
      "v_mul_f32 v250, v220, v250\n"
      "v_cvt_pk_bf16_f32 v248, v248, v249\n"
      "v_mfma_f32_16x16x32_bf16 v[158:161], v[66:69], v[62:65], v[158:161]\n"
      "s_min_u32 m0, s28, s3\n"
      "v_mul_f32 v251, v220, v251\n"
      "v_cvt_pk_bf16_f32 v249, v250, v251\n"
      "v_mfma_f32_16x16x32_bf16 v[148:151], v[168:171], v[232:235], 0\n"
      "s_movrels_b32 s9, s80\n"
      "v_mul_f32 v173, v220, v173\n"
      "v_mul_f32 v172, v220, v172\n"
      "v_mfma_f32_16x16x32_bf16 v[148:151], v[50:53], v[62:65], v[148:151]\n"
      "s_lshr_b32 s8, s9, 8\n"
      "v_mul_f32_e64 v175, v220, v175\n"
      "v_cvt_pk_bf16_f32 v250, v172, v173\n"
      "v_mfma_f32_16x16x32_bf16 v[190:193], v[184:187], v[232:235], 0\n"
      "s_and_b32 s9, s9, 0xff\n"
      "v_mul_f32_e64 v174, v220, v174\n"
      "v_cvt_pk_bf16_f32 v251, v174, v175\n"
      "v_mfma_f32_16x16x32_bf16 v[190:193], v[54:57], v[62:65], v[190:193]\n"
      "ds_write_b128 v124, v[248:251]\n"
      "v_mul_f32_e64 v219, v220, v219\n"
      "v_mfma_f32_16x16x32_bf16 v[224:227], v[152:155], v[232:235], 0\n"
      "s_mul_i32 s10, s8, 73\n"
      "v_mul_f32 v218, v220, v218\n"
      "v_mul_f32_e64 v216, v220, v216\n"
      "v_mfma_f32_16x16x32_bf16 v[224:227], v[58:61], v[62:65], v[224:227]\n"
      "s_add_u32 s10, s10, s9\n"
      "v_max3_f32 v214, v214, v176, v177\n"
      "v_max3_f32 v212, v228, v229, v230\n"
      "v_mul_f32_e64 v217, v220, v217\n"
      "v_cvt_pk_bf16_f32 v216, v216, v217\n"
      "v_max3_f32 v212, v212, v138, v139\n"
      "v_max3_f32 v212, v212, v178, v179\n"
      "v_max3_f32 v212, v212, v214, v208\n"
      "v_mov_b32_e64 v214, v212\n"
      "v_cvt_pk_bf16_f32 v217, v218, v219\n"
      "v_cndmask_b32 v157, v157, v213, s[42:43]\n"
      "v_permlane16_swap_b32 v212, v214\n"
      "v_cndmask_b32 v157, v157, v209, s[44:45]\n"
      "v_max_f32 v212, v212, v214\n"
      "v_mov_b32 v188, v212\n"
      "v_cndmask_b32 v157, v157, v201, s[46:47]\n"
      "v_rcp_f32 v158, v158\n"
      "v_permlane32_swap_b32 v212, v188\n"
      "v_max_f32 v212, v212, v188\n"
      "v_sub_f32 v137, v137, v212\n"
      "v_sub_f32 v178, v178, v212\n"
      "v_sub_f32 v179, v179, v212\n"
      "v_sub_f32 v230, v230, v212\n"
      "v_sub_f32 v136, v136, v212\n"
      "v_sub_f32 v138, v138, v212\n"
      "v_sub_f32 v176, v176, v212\n"
      "v_sub_f32 v231, v231, v212\n"
      "v_sub_f32 v208, v208, v212\n"
      "v_exp_f32 v136, v136\n"
      "v_sub_f32 v229, v229, v212\n"
      "v_exp_f32 v176, v176\n"
      "v_exp_f32 v230, v230\n"
      "v_sub_f32 v139, v139, v212\n"
      "v_exp_f32 v137, v137\n"
      "v_exp_f32 v229, v229\n"
      "v_exp_f32 v138, v138\n"
      "v_sub_f32 v228, v228, v212\n"
      "v_exp_f32 v139, v139\n"
      "v_sub_f32 v177, v177, v212\n"
      "v_exp_f32 v228, v228\n"
      "v_exp_f32 v231, v231\n"
      "v_exp_f32 v178, v178\n"
      "v_exp_f32 v208, v208\n"
      "v_cvt_pk_bf16_f32 v228, v228, v229\n"
      "v_cvt_pk_bf16_f32 v229, v230, v231\n"
      "v_cvt_pk_bf16_f32 v231, v138, v139\n"
      "v_cvt_pk_bf16_f32 v230, v136, v137\n"
      "v_exp_f32 v179, v179\n"
      "v_exp_f32 v177, v177\n"
      "v_cvt_pk_bf16_f32 v63, v178, v179\n"
      "v_cvt_pk_bf16_f32 v62, v176, v177\n"
      "v_max_f32_dpp v188, v157, v157 row_ror:8 row_mask:0xf bank_mask:0xf\n"
      "v_mul_f32_e64 v148, v158, v148\n"
      "v_cvt_pk_bf16_f32 v64, v208, 0\n"
      "v_mfma_f32_16x16x32_bf16 v[232:235], v[66:69], v[228:231], 0\n"
      "ds_read_b128 v[220:223], v115 offset:18432\n"
      "v_max_f32_dpp v188, v188, v188 row_ror:4 row_mask:0xf bank_mask:0xf\n"
      "v_mfma_f32_16x16x32_bf16 v[232:235], v[66:69], v[62:65], v[232:235]\n"
      "ds_read_b128 v[248:251], v114 offset:18432\n"
      "v_max_f32_dpp v188, v188, v188 row_ror:2 row_mask:0xf bank_mask:0xf\n"
      "v_mfma_f32_16x16x32_bf16 v[252:255], v[168:171], v[228:231], 0\n"
      "s_mul_i32 s28, s10, 4802\n"
      "v_max_f32_dpp v188, v188, v188 row_ror:1 row_mask:0xf bank_mask:0xf\n"
      "v_mul_f32 v149, v158, v149\n"
      "v_mfma_f32_16x16x32_bf16 v[252:255], v[50:53], v[62:65], v[252:255]\n"
      "ds_read_b128 v[200:203], v114 offset:24576\n"
      "v_max_f32_dpp v188, v188, v188 row_bcast:15 row_mask:0xa bank_mask:0xf\n"
      "v_mfma_f32_16x16x32_bf16 v[136:139], v[184:187], v[228:231], 0\n"
      "s_add_u32 s56, s18, s28\n"
      "v_cvt_pk_bf16_f32 v148, v148, v149\n"
      "v_max_f32_dpp v188, v188, v188 row_bcast:31 row_mask:0xc bank_mask:0xf\n"
      "v_mfma_f32_16x16x32_bf16 v[136:139], v[54:57], v[62:65], v[136:139]\n"
      "ds_read_b128 v[212:215], v115 offset:24576\n"
      "v_readlane_b32 s31, v188, 63\n"
      "v_mul_f32 v190, v158, v190\n"
      "v_mfma_f32_16x16x32_bf16 v[208:211], v[152:155], v[228:231], 0\n"
      "s_addc_u32 s57, s19, 0\n"
      "v_subrev_f32 v157, s31, v157\n"
      "v_mul_f32 v150, v158, v150\n"
      "v_mfma_f32_16x16x32_bf16 v[208:211], v[58:61], v[62:65], v[208:211]\n"
      "s_and_b32 s56, s56, -16\n"
      "v_exp_f32 v157, v157\n"
      "v_mul_f32 v151, v158, v151\n"
      "v_mul_f32 v191, v158, v191\n"
      "v_cvt_pk_bf16_f32 v149, v150, v151\n"
      "v_cvt_pk_bf16_f32 v157, v157, v157\n"
      "v_cvt_pk_bf16_f32 v150, v190, v191\n"
      "s_waitcnt lgkmcnt(2)\n"
      "v_mfma_f32_16x16x32_bf16 v[188:191], a[0:3], v[248:251], 0\n"
      "s_sub_u32 s58, s56, 110592\n"
      "v_mul_f32 v193, v158, v193\n"
      "v_mul_f32_e64 v192, v158, v192\n"
      "v_mfma_f32_16x16x32_bf16 v[188:191], a[36:39], v[220:223], v[188:191]\n"
      "ds_write_b16 v130, v157\n"
      "v_mfma_f32_16x16x32_bf16 v[172:175], a[4:7], v[248:251], 0\n"
      "ds_read_b128 v[180:183], v131 offset:64\n"
      "v_cvt_pk_bf16_f32 v151, v192, v193\n"
      "v_mfma_f32_16x16x32_bf16 v[172:175], a[40:43], v[220:223], v[172:175]\n"
      "ds_read_b128 v[192:195], v131\n"
      "v_mul_f32 v225, v158, v225\n"
      "v_mfma_f32_16x16x32_bf16 v[228:231], a[12:15], v[248:251], 0\n"
      "s_subb_u32 s59, s57, 0\n"
      "v_mul_f32_e64 v224, v158, v224\n"
      "v_cvt_pk_bf16_f32 v224, v224, v225\n"
      "v_mfma_f32_16x16x32_bf16 v[228:231], a[48:51], v[220:223], v[228:231]\n"
      "ds_write_b128 v124, v[148:151] offset:1024\n"
      "v_mfma_f32_16x16x32_bf16 v[176:179], a[8:11], v[248:251], 0\n"
      "s_sub_u32 s56, s56, 73728\n"
      "v_mul_f32 v226, v158, v226\n"
      "v_mul_f32_e64 v227, v158, v227\n"
      "v_mfma_f32_16x16x32_bf16 v[176:179], a[44:47], v[220:223], v[176:179]\n"
      "s_subb_u32 s57, s57, 0\n"
      "v_cvt_pk_bf16_f32 v225, v226, v227\n"
      "v_mov_b32_e64 v218, 0\n"
      "v_mfma_f32_16x16x32_bf16 v[156:159], a[16:19], v[248:251], 0\n"
      "s_mul_i32 s28, s8, 1373568\n"
      "s_mul_i32 s29, s9, 2688\n"
      "s_add_u32 s28, s28, s29\n"
      "v_mfma_f32_16x16x32_bf16 v[156:159], a[52:55], v[220:223], v[156:159]\n"
      "ds_write2st64_b64 v125, v[216:217], v[224:225] offset0:0 offset1:2\n"
      "s_waitcnt lgkmcnt(2)\n"
      "v_mfma_f32_16x16x32_bf16 v[224:227], v[192:195], v[66:69], 0\n"
      "s_cmp_eq_u32 s8, 72\n"
      "s_cselect_b32 s8, 0, 100270464\n"
      "s_cmp_eq_u32 s9, 72\n"
      "v_mfma_f32_16x16x32_bf16 v[224:227], v[180:183], v[66:69], v[224:227]\n"
      "ds_write_b32 v129, v218\n"
      "v_mfma_f32_16x16x32_bf16 v[148:151], v[192:195], v[168:171], 0\n"
      "s_cselect_b32 s9, 0, 196224\n"
      "s_add_u32 s10, s8, s9\n"
      "s_add_u32 s28, s28, s10\n"
      "v_mfma_f32_16x16x32_bf16 v[148:151], v[180:183], v[50:53], v[148:151]\n"
      "v_mfma_f32_16x16x32_bf16 v[168:171], v[192:195], v[184:187], 0\n"
      "s_add_u32 s52, s16, s28\n"
      "v_mov_b32_dpp v224, v224 quad_perm:[0,0,0,0] row_mask:0xf bank_mask:0xf\n"
      "s_addc_u32 s53, s17, 0\n"
      "v_mfma_f32_16x16x32_bf16 v[168:171], v[180:183], v[54:57], v[168:171]\n"
      "s_cmp_lg_u32 s10, s11\n"
      "v_mfma_f32_16x16x32_bf16 v[184:187], v[192:195], v[152:155], 0\n"
      "s_nop 0\n"
      "v_rcp_f32_e64 v224, v224\n"
      "v_mfma_f32_16x16x32_bf16 v[184:187], v[180:183], v[58:61], v[184:187]\n"
      "s_cbranch_scc1 L_edge_70_%=\n"
      "L_eb_71_%=:\n"
      "v_mfma_f32_16x16x32_bf16 v[216:219], a[20:23], v[248:251], 0\n"
      "s_nop 1\n"
      "v_cndmask_b32 v148, v148, v168, s[42:43]\n"
      "v_mfma_f32_16x16x32_bf16 v[216:219], a[56:59], v[220:223], v[216:219]\n"
      "s_nop 1\n"
      "v_cndmask_b32 v148, v148, v184, s[44:45]\n"
      "v_mul_f32 v148, v224, v148\n"
      "v_cvt_pk_bf16_f32 v148, v148, v148\n"
      "ds_write_b16 v135, v148\n"
      "s_branch L_join_10_%=\n"
      "L_flg_67_%=:\n"
      "v_cmp_gt_i32 vcc, 0, v248\n"
      "s_cbranch_vccz L_fb_68_%=\n"
      "s_sleep 1\n"
      "ds_read_b128 v[248:251], v128 offset:3072\n"
      "s_waitcnt lgkmcnt(0)\n"
      "v_bitop3_b32 v248, v248, v249, s4 bitop3:0x7e\n"
      "v_bitop3_b32 v250, v250, v251, s4 bitop3:0x7e\n"
      "v_or_b32 v248, v248, v250\n"
      "v_cmp_ne_u32 s[48:49], 0, v248\n"
      "s_branch L_flg_67_%=\n"
      "L_edge_70_%=:\n"
      "s_mov_b32 s11, s10\n"
      "s_sub_u32 s28, s60, s8\n"
      "s_sub_u32 s29, s61, s9\n"
      "s_mov_b32 s60, s8\n"
      "s_mov_b32 s61, s9\n"
      "s_mov_b64 exec, s[64:65]\n"
      "v_add_u32 v116, s28, v116\n"
      "s_mov_b64 exec, s[70:71]\n"
      "v_add_u32 v116, s29, v116\n"
      "s_mov_b64 exec, s[66:67]\n"
      "v_add_u32 v117, s28, v117\n"
      "s_mov_b64 exec, s[72:73]\n"
      "v_add_u32 v117, s29, v117\n"
      "s_mov_b64 exec, s[68:69]\n"
      "v_add_u32 v118, s28, v118\n"
      "s_mov_b64 exec, s[74:75]\n"
      "v_add_u32_e64 v118, s29, v118\n"
      "s_mov_b64 exec, -1\n"
      "s_nop 4\n"
      "s_branch L_eb_71_%=\n"
      "L_gen_9_%=:\n"
      "s_nop 0\n"
      "s_nop 0\n"
      "v_mfma_f32_16x16x32_f16 v[232:235], v[240:243], v[204:207], v[90:93]\n"
      "v_mbcnt_lo_u32_b32 v158, -1, 0\n"
      "v_mbcnt_hi_u32_b32 v158, -1, v158\n"
      "v_mfma_f32_16x16x32_f16 v[232:235], v[34:37], v[38:41], v[232:235]\n"
      "s_movk_i32 s31, 0x3fb9\n"
      "v_and_b32 v159, 15, v158\n"
      "v_lshrrev_b32 v248, 4, v158\n"
      "v_mul_u32_u24 v249, 0x62, v159\n"
      "v_lshl_add_u32 v249, v248, 3, v249\n"
      "v_add_u32 v249, s30, v249\n"
      "v_lshlrev_b32 v248, 2, v248\n"
      "v_and_b32 v158, -4, v249\n"
      "v_sub_u32 v159, v159, v248\n"
      "v_and_b32 v248, 1, v159\n"
      "v_and_b32 v249, 2, v249\n"
      "v_lshrrev_b32 v159, 1, v159\n"
      "v_lshlrev_b32 v248, 4, v248\n"
      "v_cmp_eq_u32 vcc, 0, v159\n"
      "v_mfma_f32_16x16x32_f16 v[252:255], v[164:167], v[204:207], v[94:97]\n"
      "ds_read2_b32 v[136:137], v158 offset0:0 offset1:1\n"
      "v_lshlrev_b32 v248, v248, s31\n"
      "v_cndmask_b32 v250, 0, v248, vcc\n"
      "v_cmp_eq_u32 vcc, 1, v159\n"
      "ds_read2_b32 v[208:209], v158 offset0:16 offset1:17\n"
      "ds_read2_b32 v[210:211], v158 offset0:18 offset1:24\n"
      "ds_read2_b32 v[188:189], v158 offset0:2 offset1:10\n"
      "ds_read2_b32 v[138:139], v158 offset0:8 offset1:9\n"
      "v_add_u32 v158, 1568, v158\n"
      "ds_read2_b32 v[190:191], v158 offset0:8 offset1:9\n"
      "ds_read2_b32 v[176:177], v158 offset0:0 offset1:1\n"
      "ds_read2_b32 v[178:179], v158 offset0:2 offset1:10\n"
      "ds_read2_b32 v[228:229], v158 offset0:18 offset1:24\n"
      "ds_read2_b32 v[230:231], v158 offset0:16 offset1:17\n"
      "v_add_u32 v158, 1568, v158\n"
      "v_mfma_f32_16x16x32_f16 v[160:163], v[196:199], v[204:207], v[72:75]\n"
      "ds_read2_b32 v[142:143], v158 offset0:2 offset1:10\n"
      "ds_read2_b32 v[212:213], v158 offset0:8 offset1:9\n"
      "ds_read2_b32 v[144:145], v158 offset0:18 offset1:24\n"
      "ds_read2_b32 v[140:141], v158 offset0:0 offset1:1\n"
      "v_mfma_f32_16x16x32_f16 v[160:163], v[180:183], v[164:167], v[160:163]\n"
      "ds_read2_b32 v[214:215], v158 offset0:16 offset1:17\n"
      "v_cndmask_b32 v251, 0, v248, vcc\n"
      "s_waitcnt lgkmcnt(13)\n"
      "v_mfma_f32_16x16x32_f16 v[252:255], v[40:43], v[38:41], v[252:255]\n"
      "s_nop 0\n"
      "v_alignbyte_b32 v208, v209, v208, v249\n"
      "s_waitcnt lgkmcnt(10)\n"
      "v_alignbyte_b32 v209, v210, v209, v249\n"
      "v_alignbyte_b32 v138, v139, v138, v249\n"
      "v_alignbyte_b32 v211, v211, v211, v249\n"
      "v_lshlrev_b32 v211, 16, v211\n"
      "v_mfma_f32_16x16x16_bf16 v[220:223], v[250:251], v[208:209], v[220:223]\n"
      "s_waitcnt lgkmcnt(8)\n"
      "v_alignbyte_b32 v176, v177, v176, v249\n"
      "v_alignbyte_b32 v136, v137, v136, v249\n"
      "v_alignbyte_b32 v190, v191, v190, v249\n"
      "v_alignbyte_b32 v137, v188, v137, v249\n"
      "v_alignbyte_b32 v139, v189, v139, v249\n"
      "v_mfma_f32_16x16x32_f16 v[160:163], v[224:227], v[38:41], v[160:163]\n"
      "s_waitcnt lgkmcnt(5)\n"
      "v_alignbyte_b32 v191, v179, v191, v249\n"
      "v_fmac_f32 v156, s37, v211\n"
      "v_mfma_f32_16x16x32_f16 v[208:211], v[200:203], v[204:207], v[98:101]\n"
      "ds_read_b128 v[204:207], v128 offset:3072\n"
      "v_alignbyte_b32 v230, v231, v230, v249\n"
      "v_mfma_f32_16x16x32_f16 v[208:211], v[46:49], v[38:41], v[208:211]\n"
      "v_alignbyte_b32 v229, v229, v229, v249\n"
      "s_waitcnt lgkmcnt(2)\n"
      "v_mfma_f32_16x16x16_bf16 v[148:151], v[250:251], v[136:137], v[148:151]\n"
      "s_nop 0\n"
      "v_alignbyte_b32 v140, v141, v140, v249\n"
      "v_alignbyte_b32 v231, v228, v231, v249\n"
      "v_mfma_f32_16x16x16_bf16 v[216:219], v[250:251], v[138:139], v[216:219]\n"
      "s_waitcnt lgkmcnt(1)\n"
      "v_lshlrev_b32 v229, 16, v229\n"
      "v_alignbyte_b32 v177, v178, v177, v249\n"
      "v_mfma_f32_16x16x32_f16 v[192:195], v[240:243], v[172:175], v[102:105]\n"
      "v_alignbyte_b32 v212, v213, v212, v249\n"
      "v_alignbyte_b32 v213, v143, v213, v249\n"
      "v_mfma_f32_16x16x32_f16 v[192:195], v[34:37], v[44:47], v[192:195]\n"
      "v_alignbyte_b32 v214, v215, v214, v249\n"
      "v_max3_f32 v248, v148, v149, v150\n"
      "v_mfma_f32_16x16x32_f16 v[136:139], v[164:167], v[172:175], v[106:109]\n"
      "v_alignbyte_b32 v145, v145, v145, v249\n"
      "v_alignbyte_b32 v215, v144, v215, v249\n"
      "v_mfma_f32_16x16x32_f16 v[136:139], v[40:43], v[44:47], v[136:139]\n"
      "v_alignbyte_b32 v141, v142, v141, v249\n"
      "v_lshlrev_b32_e64 v145, 16, v145\n"
      "v_mfma_f32_16x16x32_f16 v[164:167], v[196:199], v[172:175], v[74:77]\n"
      "v_max3_f32 v248, v248, v218, v219\n"
      "v_max3_f32 v249, v151, v216, v217\n"
      "v_mfma_f32_16x16x32_f16 v[164:167], v[180:183], v[200:203], v[164:167]\n"
      "v_max3_f32 v248, v248, v222, v223\n"
      "v_max3_f32 v249, v249, v220, v221\n"
      "v_mfma_f32_16x16x32_f16 v[164:167], v[224:227], v[44:47], v[164:167]\n"
      "v_max3_f32 v248, v248, v249, v156\n"
      "v_mov_b32_e64 v249, v248\n"
      "v_mfma_f32_16x16x32_f16 v[240:243], v[200:203], v[172:175], v[110:113]\n"
      "s_nop 0\n"
      "v_permlane16_swap_b32 v248, v249\n"
      "v_mfma_f32_16x16x32_f16 v[240:243], v[46:49], v[44:47], v[240:243]\n"
      "v_max_f32 v248, v248, v249\n"
      "v_mov_b32 v249, v248\n"
      "v_mfma_f32_16x16x32_f16 v[172:175], v[180:183], v[196:199], v[76:79]\n"
      "s_waitcnt lgkmcnt(0)\n"
      "v_permlane32_swap_b32 v248, v249\n"
      "v_max_f32 v248, v248, v249\n"
      "v_sub_f32 v149, v149, v248\n"
      "v_sub_f32 v148, v148, v248\n"
      "v_fmac_f32 v160, s37, v229\n"
      "v_sub_f32 v218, v218, v248\n"
      "v_sub_f32 v150, v150, v248\n"
      "v_sub_f32 v222, v222, v248\n"
      "v_exp_f32 v222, v222\n"
      "v_mfma_f32_16x16x16_bf16 v[232:235], v[250:251], v[176:177], v[232:235]\n"
      "v_exp_f32 v150, v150\n"
      "v_sub_f32 v216, v216, v248\n"
      "v_mfma_f32_16x16x16_f16 v[172:175], v[226:227], v[224:225], v[172:175]\n"
      "v_exp_f32 v218, v218\n"
      "v_sub_f32 v151, v151, v248\n"
      "v_exp_f32 v151, v151\n"
      "v_sub_f32 v221, v221, v248\n"
      "v_bitop3_b32 v206, v206, v207, s4 bitop3:0x7e\n"
      "v_sub_f32 v217, v217, v248\n"
      "v_sub_f32 v223, v223, v248\n"
      "v_exp_f32 v221, v221\n"
      "v_exp_f32 v223, v223\n"
      "v_sub_f32 v220, v220, v248\n"
      "v_sub_f32 v156, v156, v248\n"
      "v_mfma_f32_16x16x16_bf16 v[192:195], v[250:251], v[140:141], v[192:195]\n"
      "v_exp_f32 v156, v156\n"
      "v_exp_f32 v149, v149\n"
      "v_sub_f32 v219, v219, v248\n"
      "v_mfma_f32_16x16x16_bf16 v[252:255], v[250:251], v[190:191], v[252:255]\n"
      "v_exp_f32 v217, v217\n"
      "v_exp_f32 v220, v220\n"
      "v_exp_f32 v148, v148\n"
      "v_mfma_f32_16x16x16_bf16 v[136:139], v[250:251], v[212:213], v[136:139]\n"
      "v_exp_f32_e64 v216, v216\n"
      "v_mfma_f32_16x16x16_bf16 v[240:243], v[250:251], v[214:215], v[240:243]\n"
      "v_exp_f32_e64 v219, v219\n"
      "v_cvt_pk_bf16_f32 v148, v148, v149\n"
      "v_cvt_pk_bf16_f32 v149, v150, v151\n"
      "v_cvt_pk_bf16_f32 v151, v218, v219\n"
      "v_bitop3_b32 v204, v204, v205, s4 bitop3:0x7e\n"
      "v_mfma_f32_16x16x16_bf16 v[208:211], v[250:251], v[230:231], v[208:211]\n"
      "v_cvt_pk_bf16_f32 v150, v216, v217\n"
      "v_cvt_pk_bf16_f32 v64, v156, 0\n"
      "v_cvt_pk_bf16_f32 v63, v222, v223\n"
      "v_cvt_pk_bf16_f32 v62, v220, v221\n"
      "v_mfma_f32_16x16x32_bf16 v[220:223], v[66:69], v[148:151], 0\n"
      "v_or_b32 v204, v204, v206\n"
      "v_cmp_ne_u32 vcc, 0, v204\n"
      "v_mfma_f32_16x16x32_bf16 v[220:223], v[66:69], v[62:65], v[220:223]\n"
      "s_mov_b64 s[48:49], vcc\n"
      "v_max3_f32 v156, v232, v233, v234\n"
      "v_max3_f32 v159, v235, v252, v253\n"
      "v_mfma_f32_16x16x32_bf16 v[248:251], v[168:171], v[148:151], 0\n"
      "s_cbranch_vccnz L_flg_89_%=\n"
      "L_fb_90_%=:\n"
      "v_max3_f32 v156, v156, v254, v255\n"
      "v_max3_f32 v159, v159, v208, v209\n"
      "v_mfma_f32_16x16x32_bf16 v[188:191], v[184:187], v[148:151], 0\n"
      "v_max3_f32 v156, v156, v210, v211\n"
      "v_max3_f32 v156, v156, v159, v160\n"
      "v_mfma_f32_16x16x32_bf16 v[188:191], v[54:57], v[62:65], v[188:191]\n"
      "v_max3_f32 v221, v195, v136, v137\n"
      "v_mov_b32 v159, v156\n"
      "v_rcp_f32 v220, v220\n"
      "v_fmac_f32 v164, s37, v145\n"
      "v_mfma_f32_16x16x32_bf16 v[216:219], v[152:155], v[148:151], 0\n"
      "v_permlane16_swap_b32 v156, v159\n"
      "v_mfma_f32_16x16x32_bf16 v[216:219], v[58:61], v[62:65], v[216:219]\n"
      "v_max_f32 v156, v156, v159\n"
      "v_mov_b32 v159, v156\n"
      "v_mfma_f32_16x16x32_bf16 v[248:251], v[50:53], v[62:65], v[248:251]\n"
      "s_nop 0\n"
      "v_permlane32_swap_b32 v156, v159\n"
      "v_max_f32 v156, v156, v159\n"
      "v_sub_f32 v233, v233, v156\n"
      "v_sub_f32 v235, v235, v156\n"
      "v_exp_f32 v233, v233\n"
      "v_sub_f32 v232, v232, v156\n"
      "v_exp_f32 v232, v232\n"
      "v_sub_f32 v234, v234, v156\n"
      "v_cvt_pk_bf16_f32 v232, v232, v233\n"
      "v_sub_f32 v160, v160, v156\n"
      "v_sub_f32 v252, v252, v156\n"
      "v_exp_f32 v235, v235\n"
      "v_sub_f32 v254, v254, v156\n"
      "v_sub_f32 v210, v210, v156\n"
      "v_exp_f32 v254, v254\n"
      "v_sub_f32 v253, v253, v156\n"
      "v_exp_f32 v234, v234\n"
      "v_sub_f32 v255, v255, v156\n"
      "v_sub_f32 v208, v208, v156\n"
      "v_exp_f32 v210, v210\n"
      "v_exp_f32 v255, v255\n"
      "v_cvt_pk_bf16_f32 v233, v234, v235\n"
      "v_mul_f32 v249, v220, v249\n"
      "v_cvt_pk_bf16_f32 v235, v254, v255\n"
      "v_sub_f32 v209, v209, v156\n"
      "v_exp_f32 v253, v253\n"
      "v_exp_f32 v252, v252\n"
      "v_sub_f32 v211, v211, v156\n"
      "v_exp_f32 v208, v208\n"
      "v_exp_f32 v209, v209\n"
      "v_exp_f32 v211, v211\n"
      "v_exp_f32 v160, v160\n"
      "v_cvt_pk_bf16_f32 v62, v208, v209\n"
      "v_cvt_pk_bf16_f32 v234, v252, v253\n"
      "v_mul_f32 v248, v220, v248\n"
      "v_cvt_pk_bf16_f32 v64, v160, 0\n"
      "v_cvt_pk_bf16_f32 v63, v210, v211\n"
      "v_mul_f32 v251, v220, v251\n"
      "v_mfma_f32_16x16x32_bf16 v[228:231], v[66:69], v[232:235], 0\n"
      "v_mul_f32 v250, v220, v250\n"
      "v_cvt_pk_bf16_f32 v248, v248, v249\n"
      "v_mfma_f32_16x16x32_bf16 v[228:231], v[66:69], v[62:65], v[228:231]\n"
      "v_mul_f32 v218, v220, v218\n"
      "v_cvt_pk_bf16_f32 v249, v250, v251\n"
      "v_mfma_f32_16x16x32_bf16 v[176:179], v[168:171], v[232:235], 0\n"
      "v_mul_f32 v188, v220, v188\n"
      "v_mul_f32 v190, v220, v190\n"
      "v_mfma_f32_16x16x32_bf16 v[176:179], v[50:53], v[62:65], v[176:179]\n"
      "v_mul_f32 v189, v220, v189\n"
      "v_cvt_pk_bf16_f32 v250, v188, v189\n"
      "v_max3_f32 v221, v221, v240, v241\n"
      "v_mfma_f32_16x16x32_bf16 v[200:203], v[184:187], v[232:235], 0\n"
      "s_add_u32 s28, s27, 2\n"
      "v_mul_f32_e64 v191, v220, v191\n"
      "v_cvt_pk_bf16_f32 v251, v190, v191\n"
      "v_mfma_f32_16x16x32_bf16 v[200:203], v[54:57], v[62:65], v[200:203]\n"
      "ds_write_b128 v124, v[248:251]\n"
      "v_mfma_f32_16x16x32_bf16 v[212:215], v[152:155], v[232:235], 0\n"
      "v_mul_f32 v216, v220, v216\n"
      "v_mul_f32 v217, v220, v217\n"
      "v_mfma_f32_16x16x32_bf16 v[212:215], v[58:61], v[62:65], v[212:215]\n"
      "s_min_u32 m0, s28, s3\n"
      "v_mul_f32 v219, v220, v219\n"
      "v_cvt_pk_bf16_f32 v216, v216, v217\n"
      "v_cvt_pk_bf16_f32 v217, v218, v219\n"
      "v_max3_f32 v218, v192, v193, v194\n"
      "v_max3_f32 v218, v218, v138, v139\n"
      "v_max3_f32 v218, v218, v242, v243\n"
      "v_max3_f32 v218, v218, v221, v164\n"
      "v_mov_b32_e64 v219, v218\n"
      "v_mbcnt_lo_u32_b32 v158, -1, 0\n"
      "v_cndmask_b32 v157, v157, v161, s[42:43]\n"
      "v_permlane16_swap_b32 v218, v219\n"
      "v_max_f32 v218, v218, v219\n"
      "v_mov_b32_e64 v219, v218\n"
      "v_mbcnt_hi_u32_b32 v158, -1, v158\n"
      "v_cndmask_b32 v157, v157, v165, s[44:45]\n"
      "v_permlane32_swap_b32 v218, v219\n"
      "v_max_f32 v218, v218, v219\n"
      "v_sub_f32 v138, v138, v218\n"
      "v_sub_f32 v139, v139, v218\n"
      "v_sub_f32 v194, v194, v218\n"
      "v_sub_f32 v192, v192, v218\n"
      "v_sub_f32 v164, v164, v218\n"
      "v_exp_f32 v138, v138\n"
      "v_sub_f32 v241, v241, v218\n"
      "v_sub_f32 v193, v193, v218\n"
      "v_sub_f32 v243, v243, v218\n"
      "v_exp_f32 v241, v241\n"
      "v_exp_f32 v192, v192\n"
      "v_sub_f32 v136, v136, v218\n"
      "v_sub_f32 v240, v240, v218\n"
      "v_exp_f32 v243, v243\n"
      "v_exp_f32 v240, v240\n"
      "v_sub_f32 v137, v137, v218\n"
      "v_sub_f32 v195, v195, v218\n"
      "v_exp_f32 v195, v195\n"
      "v_exp_f32 v164, v164\n"
      "v_exp_f32 v194, v194\n"
      "v_exp_f32 v193, v193\n"
      "v_cvt_pk_bf16_f32 v62, v240, v241\n"
      "v_sub_f32 v242, v242, v218\n"
      "v_exp_f32 v137, v137\n"
      "v_exp_f32 v139, v139\n"
      "v_cvt_pk_bf16_f32 v192, v192, v193\n"
      "v_cvt_pk_bf16_f32 v193, v194, v195\n"
      "v_exp_f32 v136, v136\n"
      "v_min_u32 v158, 48, v158\n"
      "v_exp_f32_e64 v242, v242\n"
      "v_cvt_pk_bf16_f32 v194, v136, v137\n"
      "v_cvt_pk_bf16_f32 v63, v242, v243\n"
      "v_cvt_pk_bf16_f32 v195, v138, v139\n"
      "v_cvt_pk_bf16_f32 v64, v164, 0\n"
      "v_cndmask_b32 v157, v157, v173, s[46:47]\n"
      "v_mfma_f32_16x16x32_bf16 v[232:235], v[66:69], v[192:195], 0\n"
      "ds_read_b128 v[248:251], v114 offset:18432\n"
      "v_lshlrev_b32_e64 v158, 1, v158\n"
      "v_mfma_f32_16x16x32_bf16 v[232:235], v[66:69], v[62:65], v[232:235]\n"
      "ds_read_b128 v[220:223], v115 offset:18432\n"
      "v_add_u32 v158, s30, v158\n"
      "v_mfma_f32_16x16x32_bf16 v[252:255], v[168:171], v[192:195], 0\n"
      "ds_read_u16 v218, v158 offset:4704\n"
      "v_mov_b32 v240, 0\n"
      "v_mfma_f32_16x16x32_bf16 v[252:255], v[50:53], v[62:65], v[252:255]\n"
      "s_movrels_b32 s9, s80\n"
      "v_rcp_f32 v228, v228\n"
      "v_mfma_f32_16x16x32_bf16 v[136:139], v[184:187], v[192:195], 0\n"
      "s_lshr_b32 s8, s9, 8\n"
      "v_mul_f32 v177, v228, v177\n"
      "v_mul_f32 v176, v228, v176\n"
      "v_mfma_f32_16x16x32_bf16 v[136:139], v[54:57], v[62:65], v[136:139]\n"
      "s_and_b32 s9, s9, 0xff\n"
      "v_mul_f32 v178, v228, v178\n"
      "v_mul_f32_e64 v212, v228, v212\n"
      "v_cvt_pk_bf16_f32 v176, v176, v177\n"
      "v_mul_f32 v213, v228, v213\n"
      "v_mfma_f32_16x16x32_bf16 v[208:211], v[152:155], v[192:195], 0\n"
      "s_mul_i32 s10, s8, 73\n"
      "v_mul_f32 v179, v228, v179\n"
      "v_mul_f32 v200, v228, v200\n"
      "v_mfma_f32_16x16x32_bf16 v[208:211], v[58:61], v[62:65], v[208:211]\n"
      "s_add_u32 s10, s10, s9\n"
      "v_mul_f32 v202, v228, v202\n"
      "v_cvt_pk_bf16_f32 v177, v178, v179\n"
      "v_mul_f32 v201, v228, v201\n"
      "v_cvt_pk_bf16_f32 v178, v200, v201\n"
      "v_mul_f32 v203, v228, v203\n"
      "v_cvt_pk_bf16_f32 v179, v202, v203\n"
      "s_waitcnt lgkmcnt(1)\n"
      "v_mfma_f32_16x16x32_bf16 v[188:191], a[0:3], v[248:251], 0\n"
      "s_mul_i32 s28, s10, 4802\n"
      "v_cvt_pk_bf16_f32 v212, v212, v213\n"
      "v_mul_f32_e64 v214, v228, v214\n"
      "v_mfma_f32_16x16x32_bf16 v[188:191], a[36:39], v[220:223], v[188:191]\n"
      "ds_write_b128 v124, v[176:179] offset:1024\n"
      "v_mfma_f32_16x16x32_bf16 v[172:175], a[4:7], v[248:251], 0\n"
      "s_waitcnt lgkmcnt(1)\n"
      "v_lshlrev_b32 v218, 16, v218\n"
      "v_fmac_f32_e64 v157, s37, v218\n"
      "v_mfma_f32_16x16x32_bf16 v[172:175], a[40:43], v[220:223], v[172:175]\n"
      "s_add_u32 s56, s18, s28\n"
      "v_mul_f32_e64 v215, v228, v215\n"
      "v_max_f32_dpp v158, v157, v157 row_ror:8 row_mask:0xf bank_mask:0xf\n"
      "v_mfma_f32_16x16x32_bf16 v[176:179], a[8:11], v[248:251], 0\n"
      "s_addc_u32 s57, s19, 0\n"
      "v_max_f32_dpp v158, v158, v158 row_ror:4 row_mask:0xf bank_mask:0xf\n"
      "v_cvt_pk_bf16_f32 v213, v214, v215\n"
      "v_mfma_f32_16x16x32_bf16 v[176:179], a[44:47], v[220:223], v[176:179]\n"
      "s_and_b32 s56, s56, -16\n"
      "s_sub_u32 s58, s56, 110592\n"
      "v_max_f32_dpp v158, v158, v158 row_ror:2 row_mask:0xf bank_mask:0xf\n"
      "s_subb_u32 s59, s57, 0\n"
      "ds_write2st64_b64 v125, v[216:217], v[212:213] offset0:0 offset1:2\n"
      "v_max_f32_dpp v158, v158, v158 row_ror:1 row_mask:0xf bank_mask:0xf\n"
      "v_mfma_f32_16x16x32_bf16 v[216:219], a[20:23], v[248:251], 0\n"
      "ds_read_b128 v[200:203], v114 offset:24576\n"
      "v_max_f32_dpp v158, v158, v158 row_bcast:15 row_mask:0xa bank_mask:0xf\n"
      "v_mfma_f32_16x16x32_bf16 v[216:219], a[56:59], v[220:223], v[216:219]\n"
      "s_nop 0\n"
      "v_max_f32_dpp v158, v158, v158 row_bcast:31 row_mask:0xc bank_mask:0xf\n"
      "s_nop 0\n"
      "v_readlane_b32 s31, v158, 63\n"
      "v_mfma_f32_16x16x32_bf16 v[228:231], a[12:15], v[248:251], 0\n"
      "s_sub_u32 s56, s56, 73728\n"
      "v_subrev_f32 v157, s31, v157\n"
      "v_exp_f32_e64 v157, v157\n"
      "s_subb_u32 s57, s57, 0\n"
      "v_cvt_pk_bf16_f32 v157, v157, v157\n"
      "ds_write_b16 v130, v157\n"
      "ds_read_b128 v[192:195], v131 offset:64\n"
      "ds_read_b128 v[164:167], v131\n"
      "s_mul_i32 s28, s8, 1373568\n"
      "s_mul_i32 s29, s9, 2688\n"
      "ds_read_b128 v[212:215], v115 offset:24576\n"
      "v_mfma_f32_16x16x32_bf16 v[156:159], a[16:19], v[248:251], 0\n"
      "v_mfma_f32_16x16x32_bf16 v[156:159], a[52:55], v[220:223], v[156:159]\n"
      "ds_write_b32 v129, v240\n"
      "s_add_u32 s28, s28, s29\n"
      "s_cmp_eq_u32 s8, 72\n"
      "s_cselect_b32 s8, 0, 100270464\n"
      "s_cmp_eq_u32 s9, 72\n"
      "s_cselect_b32 s9, 0, 196224\n"
      "s_add_u32 s10, s8, s9\n"
      "s_add_u32 s28, s28, s10\n"
      "s_add_u32 s52, s16, s28\n"
      "s_addc_u32 s53, s17, 0\n"
      "s_cmp_lg_u32 s10, s11\n"
      "v_mfma_f32_16x16x32_bf16 v[228:231], a[48:51], v[220:223], v[228:231]\n"
      "s_cbranch_scc1 L_edge_92_%=\n"
      "L_eb_93_%=:\n"
      "s_waitcnt lgkmcnt(2)\n"
      "v_mfma_f32_16x16x32_bf16 v[240:243], v[164:167], v[66:69], 0\n"
      "v_mfma_f32_16x16x32_bf16 v[240:243], v[192:195], v[66:69], v[240:243]\n"
      "s_waitcnt lgkmcnt(1)\n"
      "v_mfma_f32_16x16x32_bf16 v[148:151], v[164:167], v[168:171], 0\n"
      "v_mfma_f32_16x16x32_bf16 v[148:151], v[192:195], v[50:53], v[148:151]\n"
      "v_mfma_f32_16x16x32_bf16 v[168:171], v[164:167], v[184:187], 0\n"
      "v_mfma_f32_16x16x32_bf16 v[168:171], v[192:195], v[54:57], v[168:171]\n"
      "v_mfma_f32_16x16x32_bf16 v[184:187], v[164:167], v[152:155], 0\n"
      "s_nop 1\n"
      "v_mov_b32_dpp v240, v240 quad_perm:[0,0,0,0] row_mask:0xf bank_mask:0xf\n"
      "v_mfma_f32_16x16x32_bf16 v[184:187], v[192:195], v[58:61], v[184:187]\n"
      "s_nop 2\n"
      "v_cndmask_b32 v148, v148, v168, s[42:43]\n"
      "v_rcp_f32_e64 v240, v240\n"
      "s_nop 2\n"
      "v_cndmask_b32 v148, v148, v184, s[44:45]\n"
      "v_mul_f32 v148, v240, v148\n"
      "v_cvt_pk_bf16_f32 v148, v148, v148\n"
      "ds_write_b16 v135, v148\n"
      "s_branch L_join_10_%=\n"
      "L_flg_89_%=:\n"
      "v_cmp_gt_i32 vcc, 0, v204\n"
      "s_cbranch_vccz L_fb_90_%=\n"
      "s_sleep 1\n"
      "ds_read_b128 v[204:207], v128 offset:3072\n"
      "s_waitcnt lgkmcnt(0)\n"
      "v_bitop3_b32 v204, v204, v205, s4 bitop3:0x7e\n"
      "v_bitop3_b32 v206, v206, v207, s4 bitop3:0x7e\n"
      "v_or_b32 v204, v204, v206\n"
      "v_cmp_ne_u32 s[48:49], 0, v204\n"
      "s_branch L_flg_89_%=\n"
      "L_edge_92_%=:\n"
      "s_mov_b32 s11, s10\n"
      "s_sub_u32 s28, s60, s8\n"
      "s_sub_u32 s29, s61, s9\n"
      "s_mov_b32 s60, s8\n"
      "s_mov_b32 s61, s9\n"
      "s_mov_b64 exec, s[64:65]\n"
      "v_add_u32 v116, s28, v116\n"
      "s_mov_b64 exec, s[70:71]\n"
      "v_add_u32 v116, s29, v116\n"
      "s_mov_b64 exec, s[66:67]\n"
      "v_add_u32 v117, s28, v117\n"
      "s_mov_b64 exec, s[72:73]\n"
      "v_add_u32 v117, s29, v117\n"
      "s_mov_b64 exec, s[68:69]\n"
      "v_add_u32 v118, s28, v118\n"
      "s_mov_b64 exec, s[74:75]\n"
      "v_add_u32 v118, s29, v118\n"
      "s_mov_b64 exec, -1\n"
      "s_nop 4\n"
      "s_branch L_eb_93_%=\n"
      "L_gen_19_%=:\n"
      "s_waitcnt lgkmcnt(0)\n"
      "s_nop 5\n"
      "s_cmp_eq_u64 s[34:35], 0\n"
      "s_cbranch_scc1 L_ok_114_%=\n"
      "ds_read_b128 v[252:255], v114 offset:18432\n"
      "ds_read_b128 v[212:215], v115 offset:18432\n"
      "ds_read_b128 v[180:183], v114 offset:30720\n"
      "ds_read_b128 v[216:219], v114 offset:24576\n"
      "ds_read_b128 v[172:175], v115 offset:24576\n"
      "s_waitcnt lgkmcnt(3)\n"
      "v_mfma_f32_16x16x32_bf16 v[156:159], a[0:3], v[252:255], 0\n"
      "v_mfma_f32_16x16x32_bf16 v[156:159], a[36:39], v[212:215], v[156:159]\n"
      "v_mfma_f32_16x16x32_bf16 v[148:151], a[4:7], v[252:255], 0\n"
      "s_nop 0\n"
      "v_mfma_f32_16x16x32_bf16 v[148:151], a[40:43], v[212:215], v[148:151]\n"
      "v_mfma_f32_16x16x32_bf16 v[196:199], a[8:11], v[252:255], 0\n"
      "s_waitcnt lgkmcnt(0)\n"
      "v_mfma_f32_16x16x32_bf16 v[228:231], a[12:15], v[252:255], 0\n"
      "s_nop 0\n"
      "v_mfma_f32_16x16x32_bf16 v[228:231], a[48:51], v[212:215], v[228:231]\n"
      "v_mfma_f32_16x16x32_bf16 v[200:203], a[16:19], v[252:255], 0\n"
      "v_mfma_f32_16x16x32_bf16 v[200:203], a[52:55], v[212:215], v[200:203]\n"
      "v_mfma_f32_16x16x32_bf16 v[200:203], a[88:91], v[216:219], v[200:203]\n"
      "v_mfma_f32_16x16x32_bf16 v[228:231], a[84:87], v[216:219], v[228:231]\n"
      "v_mfma_f32_16x16x32_bf16 v[188:191], a[20:23], v[252:255], 0\n"
      "ds_read_b128 v[252:255], v115 offset:30720\n"
      "v_mfma_f32_16x16x32_bf16 v[188:191], a[56:59], v[212:215], v[188:191]\n"
      "v_mfma_f32_16x16x32_bf16 v[156:159], a[72:75], v[216:219], v[156:159]\n"
      "v_mfma_f32_16x16x32_bf16 v[156:159], a[108:111], v[172:175], v[156:159]\n"
      "v_mfma_f32_16x16x32_bf16 v[196:199], a[44:47], v[212:215], v[196:199]\n"
      "v_mfma_f32_16x16x32_bf16 v[196:199], a[80:83], v[216:219], v[196:199]\n"
      "v_mfma_f32_16x16x32_bf16 v[196:199], a[116:119], v[172:175], v[196:199]\n"
      "v_mfma_f32_16x16x32_bf16 v[148:151], a[76:79], v[216:219], v[148:151]\n"
      "s_nop 0\n"
      "v_mfma_f32_16x16x32_bf16 v[148:151], a[112:115], v[172:175], v[148:151]\n"
      "s_waitcnt lgkmcnt(0)\n"
      "v_mfma_f32_16x16x32_bf16 v[156:159], a[144:147], v[180:183], v[156:159]\n"
      "ds_read_b128 v[212:215], v114 offset:20480\n"
      "v_mfma_f32_16x16x32_bf16 v[156:159], a[180:183], v[252:255], v[156:159]\n"
      "v_mfma_f32_16x16x32_bf16 v[188:191], a[92:95], v[216:219], v[188:191]\n"
      "v_mfma_f32_16x16x32_bf16 v[188:191], a[128:131], v[172:175], v[188:191]\n"
      "v_mfma_f32_16x16x32_bf16 v[196:199], a[152:155], v[180:183], v[196:199]\n"
      "v_mfma_f32_16x16x32_bf16 v[196:199], a[188:191], v[252:255], v[196:199]\n"
      "v_mfma_f32_16x16x32_bf16 v[148:151], a[148:151], v[180:183], v[148:151]\n"
      "v_mfma_f32_16x16x32_bf16 v[148:151], a[184:187], v[252:255], v[148:151]\n"
      "s_nop 1\n"
      "v_fma_f32 v246, v156, v156, s38\n"
      "v_mfma_f32_16x16x32_bf16 v[200:203], a[124:127], v[172:175], v[200:203]\n"
      "s_nop 0\n"
      "v_mfma_f32_16x16x32_bf16 v[228:231], a[120:123], v[172:175], v[228:231]\n"
      "v_mfma_f32_16x16x32_bf16 v[228:231], a[156:159], v[180:183], v[228:231]\n"
      "v_fmac_f32 v246, v157, v157\n"
      "v_mfma_f32_16x16x32_bf16 v[228:231], a[192:195], v[252:255], v[228:231]\n"
      "v_fmac_f32 v246, v158, v158\n"
      "v_mfma_f32_16x16x32_bf16 v[200:203], a[160:163], v[180:183], v[200:203]\n"
      "v_fmac_f32 v246, v159, v159\n"
      "v_fmac_f32 v246, v148, v148\n"
      "v_mfma_f32_16x16x32_bf16 v[200:203], a[196:199], v[252:255], v[200:203]\n"
      "v_fmac_f32 v246, v149, v149\n"
      "v_fmac_f32 v246, v150, v150\n"
      "v_mfma_f32_16x16x32_bf16 v[188:191], a[164:167], v[180:183], v[188:191]\n"
      "v_fma_f32 v247, v228, v228, s38\n"
      "v_fmac_f32_e64 v246, v151, v151\n"
      "v_mfma_f32_16x16x32_bf16 v[188:191], a[200:203], v[252:255], v[188:191]\n"
      "ds_read_b128 v[252:255], v115 offset:20480\n"
      "v_fmac_f32 v247, v229, v229\n"
      "v_fmac_f32 v247, v230, v230\n"
      "v_fmac_f32 v247, v231, v231\n"
      "v_fmac_f32 v247, v200, v200\n"
      "v_fmac_f32 v247, v201, v201\n"
      "v_fmac_f32 v247, v202, v202\n"
      "v_fmac_f32 v246, v196, v196\n"
      "v_fmac_f32 v247, v203, v203\n"
      "v_fmac_f32 v247, v188, v188\n"
      "v_fmac_f32 v247, v189, v189\n"
      "v_fmac_f32 v247, v190, v190\n"
      "v_fmac_f32 v246, v197, v197\n"
      "v_fmac_f32 v246, v198, v198\n"
      "v_fmac_f32 v247, v191, v191\n"
      "v_fmac_f32 v246, v199, v199\n"
      "s_nop 1\n"
      "v_permlane16_swap_b32 v246, v247\n"
      "v_add_f32 v246, v246, v247\n"
      "v_mov_b32 v247, v246\n"
      "s_nop 1\n"
      "v_permlane32_swap_b32 v246, v247\n"
      "v_add_f32 v246, v246, v247\n"
      "v_rsq_f32 v246, v246\n"
      "s_nop 0\n"
      "v_mov_b32 v247, v246\n"
      "s_waitcnt lgkmcnt(0)\n"
      "v_mfma_f32_16x16x32_bf16 v[220:223], a[0:3], v[212:215], 0\n"
      "ds_read_b128 v[216:219], v114 offset:26624\n"
      "v_permlane16_swap_b32 v246, v247\n"
      "v_mfma_f32_16x16x32_bf16 v[220:223], a[36:39], v[252:255], v[220:223]\n"
      "ds_read_b128 v[172:175], v115 offset:26624\n"
      "v_mul_f32 v230, v247, v230\n"
      "v_mul_f32 v229, v247, v229\n"
      "v_mul_f32 v231, v247, v231\n"
      "v_mul_f32_e64 v228, v247, v228\n"
      "v_mfma_f32_16x16x32_bf16 v[248:251], a[12:15], v[212:215], 0\n"
      "ds_read_b128 v[180:183], v114 offset:32768\n"
      "v_cvt_pk_f16_f32 v228, v228, v229\n"
      "v_mfma_f32_16x16x32_bf16 v[248:251], a[48:51], v[252:255], v[248:251]\n"
      "v_mul_f32 v201, v247, v201\n"
      "v_cvt_pk_f16_f32 v229, v230, v231\n"
      "v_mul_f32 v202, v247, v202\n"
      "v_mul_f32_e64 v203, v247, v203\n"
      "v_cvt_pk_f16_f32 v231, v202, v203\n"
      "v_mul_f32 v200, v247, v200\n"
      "v_cvt_pk_f16_f32 v230, v200, v201\n"
      "v_mfma_f32_16x16x32_bf16 v[200:203], a[16:19], v[212:215], 0\n"
      "v_mul_f32 v189, v247, v189\n"
      "v_mul_f32 v188, v247, v188\n"
      "v_mfma_f32_16x16x32_bf16 v[200:203], a[52:55], v[252:255], v[200:203]\n"
      "v_cvt_pk_f16_f32 v34, v188, v189\n"
      "v_mul_f32 v191, v247, v191\n"
      "v_mfma_f32_16x16x32_bf16 v[168:171], a[20:23], v[212:215], 0\n"
      "ds_read_b128 v[164:167], v115 offset:32768\n"
      "v_mul_f32 v190, v247, v190\n"
      "v_mfma_f32_16x16x32_bf16 v[168:171], a[56:59], v[252:255], v[168:171]\n"
      "v_cvt_pk_f16_f32 v35, v190, v191\n"
      "s_waitcnt lgkmcnt(2)\n"
      "v_mfma_f32_16x16x32_bf16 v[220:223], a[72:75], v[216:219], v[220:223]\n"
      "v_mul_f32 v246, s36, v246\n"
      "v_mul_f32 v156, v246, v156\n"
      "v_mfma_f32_16x16x32_bf16 v[220:223], a[108:111], v[172:175], v[220:223]\n"
      "v_mul_f32_e64 v157, v246, v157\n"
      "v_cvt_pk_f16_f32 v156, v156, v157\n"
      "v_mfma_f32_16x16x32_bf16 v[248:251], a[84:87], v[216:219], v[248:251]\n"
      "v_mul_f32 v158, v246, v158\n"
      "v_mul_f32 v159, v246, v159\n"
      "v_mfma_f32_16x16x32_bf16 v[248:251], a[120:123], v[172:175], v[248:251]\n"
      "v_cvt_pk_f16_f32 v157, v158, v159\n"
      "v_mul_f32 v148, v246, v148\n"
      "v_mfma_f32_16x16x32_bf16 v[200:203], a[88:91], v[216:219], v[200:203]\n"
      "v_mul_f32 v149, v246, v149\n"
      "v_cvt_pk_f16_f32 v158, v148, v149\n"
      "v_mfma_f32_16x16x32_bf16 v[200:203], a[124:127], v[172:175], v[200:203]\n"
      "v_mul_f32 v151, v246, v151\n"
      "v_mul_f32 v150, v246, v150\n"
      "v_mfma_f32_16x16x32_bf16 v[168:171], a[92:95], v[216:219], v[168:171]\n"
      "s_waitcnt lgkmcnt(0)\n"
      "v_mul_f32 v196, v246, v196\n"
      "v_mfma_f32_16x16x32_bf16 v[168:171], a[128:131], v[172:175], v[168:171]\n"
      "v_cvt_pk_f16_f32 v159, v150, v151\n"
      "v_mul_f32 v197, v246, v197\n"
      "v_mfma_f32_16x16x32_bf16 v[148:151], a[4:7], v[212:215], 0\n"
      "v_mul_f32 v198, v246, v198\n"
      "v_mul_f32_e64 v199, v246, v199\n"
      "v_mfma_f32_16x16x32_bf16 v[148:151], a[40:43], v[252:255], v[148:151]\n"
      "v_cvt_pk_f16_f32 v32, v196, v197\n"
      "v_cvt_pk_f16_f32 v33, v198, v199\n"
      "v_mfma_f32_16x16x32_bf16 v[148:151], a[76:79], v[216:219], v[148:151]\n"
      "v_mfma_f32_16x16x32_bf16 v[148:151], a[112:115], v[172:175], v[148:151]\n"
      "v_mfma_f32_16x16x32_bf16 v[196:199], a[8:11], v[212:215], 0\n"
      "v_mfma_f32_16x16x32_bf16 v[196:199], a[44:47], v[252:255], v[196:199]\n"
      "v_mfma_f32_16x16x32_bf16 v[196:199], a[80:83], v[216:219], v[196:199]\n"
      "ds_read_b128 v[252:255], v115 offset:22528\n"
      "v_mfma_f32_16x16x32_bf16 v[196:199], a[116:119], v[172:175], v[196:199]\n"
      "v_mfma_f32_16x16x32_bf16 v[220:223], a[144:147], v[180:183], v[220:223]\n"
      "v_mfma_f32_16x16x32_bf16 v[220:223], a[180:183], v[164:167], v[220:223]\n"
      "v_mfma_f32_16x16x32_bf16 v[148:151], a[148:151], v[180:183], v[148:151]\n"
      "v_mfma_f32_16x16x32_bf16 v[148:151], a[184:187], v[164:167], v[148:151]\n"
      "v_mfma_f32_16x16x32_bf16 v[196:199], a[152:155], v[180:183], v[196:199]\n"
      "v_mfma_f32_16x16x32_bf16 v[196:199], a[188:191], v[164:167], v[196:199]\n"
      "v_mfma_f32_16x16x32_bf16 v[248:251], a[156:159], v[180:183], v[248:251]\n"
      "s_nop 0\n"
      "v_mfma_f32_16x16x32_bf16 v[248:251], a[192:195], v[164:167], v[248:251]\n"
      "s_nop 1\n"
      "v_fma_f32 v246, v220, v220, s38\n"
      "v_mfma_f32_16x16x32_bf16 v[200:203], a[160:163], v[180:183], v[200:203]\n"
      "v_fmac_f32 v246, v221, v221\n"
      "v_fmac_f32 v246, v222, v222\n"
      "v_mfma_f32_16x16x32_bf16 v[200:203], a[196:199], v[164:167], v[200:203]\n"
      "v_fmac_f32 v246, v223, v223\n"
      "v_fma_f32 v247, v248, v248, s38\n"
      "v_fmac_f32 v246, v148, v148\n"
      "v_fmac_f32_e64 v247, v249, v249\n"
      "v_mfma_f32_16x16x32_bf16 v[168:171], a[164:167], v[180:183], v[168:171]\n"
      "ds_read_b128 v[212:215], v114 offset:22528\n"
      "v_fmac_f32 v247, v250, v250\n"
      "v_mfma_f32_16x16x32_bf16 v[168:171], a[200:203], v[164:167], v[168:171]\n"
      "v_fmac_f32 v247, v251, v251\n"
      "v_fmac_f32 v247, v200, v200\n"
      "v_fmac_f32 v247, v201, v201\n"
      "v_fmac_f32 v247, v202, v202\n"
      "v_fmac_f32 v246, v149, v149\n"
      "v_fmac_f32 v246, v150, v150\n"
      "v_fmac_f32 v246, v151, v151\n"
      "v_fmac_f32 v246, v196, v196\n"
      "v_fmac_f32 v246, v197, v197\n"
      "v_fmac_f32 v247, v203, v203\n"
      "v_fmac_f32 v247, v168, v168\n"
      "v_fmac_f32 v246, v198, v198\n"
      "v_fmac_f32 v247, v169, v169\n"
      "v_fmac_f32 v247, v170, v170\n"
      "v_fmac_f32 v246, v199, v199\n"
      "v_fmac_f32 v247, v171, v171\n"
      "s_nop 1\n"
      "v_permlane16_swap_b32 v246, v247\n"
      "v_add_f32 v246, v246, v247\n"
      "v_mov_b32 v247, v246\n"
      "s_nop 1\n"
      "v_permlane32_swap_b32 v246, v247\n"
      "v_add_f32 v246, v246, v247\n"
      "v_rsq_f32 v246, v246\n"
      "s_waitcnt lgkmcnt(0)\n"
      "v_mfma_f32_16x16x32_bf16 v[136:139], a[0:3], v[212:215], 0\n"
      "ds_read_b128 v[216:219], v114 offset:28672\n"
      "v_mov_b32 v247, v246\n"
      "v_mfma_f32_16x16x32_bf16 v[136:139], a[36:39], v[252:255], v[136:139]\n"
      "ds_read_b128 v[172:175], v115 offset:28672\n"
      "v_permlane16_swap_b32 v246, v247\n"
      "v_mfma_f32_16x16x32_bf16 v[160:163], a[12:15], v[212:215], 0\n"
      "ds_read_b128 v[180:183], v114 offset:34816\n"
      "v_mul_f32 v248, v247, v248\n"
      "v_mfma_f32_16x16x32_bf16 v[160:163], a[48:51], v[252:255], v[160:163]\n"
      "v_mul_f32 v251, v247, v251\n"
      "v_mul_f32 v249, v247, v249\n"
      "v_mul_f32 v201, v247, v201\n"
      "v_mul_f32 v250, v247, v250\n"
      "v_cvt_pk_f16_f32 v248, v248, v249\n"
      "v_mul_f32 v202, v247, v202\n"
      "v_cvt_pk_f16_f32 v249, v250, v251\n"
      "v_mul_f32 v203, v247, v203\n"
      "v_mul_f32 v200, v247, v200\n"
      "v_cvt_pk_f16_f32 v250, v200, v201\n"
      "v_cvt_pk_f16_f32 v251, v202, v203\n"
      "v_mfma_f32_16x16x32_bf16 v[200:203], a[16:19], v[212:215], 0\n"
      "v_mul_f32 v168, v247, v168\n"
      "v_mul_f32 v169, v247, v169\n"
      "v_mfma_f32_16x16x32_bf16 v[200:203], a[52:55], v[252:255], v[200:203]\n"
      "v_mul_f32_e64 v171, v247, v171\n"
      "v_cvt_pk_f16_f32 v40, v168, v169\n"
      "v_mfma_f32_16x16x32_bf16 v[188:191], a[20:23], v[212:215], 0\n"
      "ds_read_b128 v[164:167], v115 offset:34816\n"
      "v_mul_f32 v170, v247, v170\n"
      "v_mfma_f32_16x16x32_bf16 v[188:191], a[56:59], v[252:255], v[188:191]\n"
      "s_waitcnt lgkmcnt(2)\n"
      "v_mul_f32 v246, s36, v246\n"
      "v_cvt_pk_f16_f32 v41, v170, v171\n"
      "v_mfma_f32_16x16x32_bf16 v[136:139], a[72:75], v[216:219], v[136:139]\n"
      "v_mul_f32 v220, v246, v220\n"
      "v_mul_f32_e64 v221, v246, v221\n"
      "v_mfma_f32_16x16x32_bf16 v[136:139], a[108:111], v[172:175], v[136:139]\n"
      "v_mul_f32 v222, v246, v222\n"
      "v_cvt_pk_f16_f32 v220, v220, v221\n"
      "v_mfma_f32_16x16x32_bf16 v[200:203], a[88:91], v[216:219], v[200:203]\n"
      "v_mul_f32 v223, v246, v223\n"
      "v_mul_f32 v149, v246, v149\n"
      "v_mfma_f32_16x16x32_bf16 v[200:203], a[124:127], v[172:175], v[200:203]\n"
      "v_cvt_pk_f16_f32 v221, v222, v223\n"
      "v_mul_f32 v148, v246, v148\n"
      "v_mfma_f32_16x16x32_bf16 v[160:163], a[84:87], v[216:219], v[160:163]\n"
      "v_cvt_pk_f16_f32 v222, v148, v149\n"
      "v_mul_f32 v150, v246, v150\n"
      "v_mfma_f32_16x16x32_bf16 v[160:163], a[120:123], v[172:175], v[160:163]\n"
      "v_mul_f32 v151, v246, v151\n"
      "v_cvt_pk_f16_f32 v223, v150, v151\n"
      "v_mfma_f32_16x16x32_bf16 v[148:151], a[4:7], v[212:215], 0\n"
      "v_mul_f32 v197, v246, v197\n"
      "s_waitcnt lgkmcnt(0)\n"
      "v_mfma_f32_16x16x32_bf16 v[148:151], a[40:43], v[252:255], v[148:151]\n"
      "v_mul_f32_e64 v199, v246, v199\n"
      "v_mul_f32_e64 v198, v246, v198\n"
      "v_mfma_f32_16x16x32_bf16 v[148:151], a[76:79], v[216:219], v[148:151]\n"
      "v_mul_f32_e64 v196, v246, v196\n"
      "v_mfma_f32_16x16x32_bf16 v[148:151], a[112:115], v[172:175], v[148:151]\n"
      "v_cvt_pk_f16_f32 v39, v198, v199\n"
      "v_cvt_pk_f16_f32 v38, v196, v197\n"
      "v_mfma_f32_16x16x32_bf16 v[196:199], a[8:11], v[212:215], 0\n"
      "v_mfma_f32_16x16x32_bf16 v[196:199], a[44:47], v[252:255], v[196:199]\n"
      "v_mfma_f32_16x16x32_bf16 v[196:199], a[80:83], v[216:219], v[196:199]\n"
      "v_mfma_f32_16x16x32_bf16 v[196:199], a[116:119], v[172:175], v[196:199]\n"
      "v_mfma_f32_16x16x32_bf16 v[188:191], a[92:95], v[216:219], v[188:191]\n"
      "v_mfma_f32_16x16x32_bf16 v[188:191], a[128:131], v[172:175], v[188:191]\n"
      "v_mfma_f32_16x16x32_bf16 v[136:139], a[144:147], v[180:183], v[136:139]\n"
      "v_mfma_f32_16x16x32_bf16 v[136:139], a[180:183], v[164:167], v[136:139]\n"
      "v_mfma_f32_16x16x32_bf16 v[148:151], a[148:151], v[180:183], v[148:151]\n"
      "v_mfma_f32_16x16x32_bf16 v[148:151], a[184:187], v[164:167], v[148:151]\n"
      "v_mfma_f32_16x16x32_bf16 v[196:199], a[152:155], v[180:183], v[196:199]\n"
      "v_mfma_f32_16x16x32_bf16 v[196:199], a[188:191], v[164:167], v[196:199]\n"
      "v_mfma_f32_16x16x32_bf16 v[160:163], a[156:159], v[180:183], v[160:163]\n"
      "v_mfma_f32_16x16x32_bf16 v[160:163], a[192:195], v[164:167], v[160:163]\n"
      "s_nop 1\n"
      "v_fma_f32 v246, v136, v136, s38\n"
      "v_mfma_f32_16x16x32_bf16 v[200:203], a[160:163], v[180:183], v[200:203]\n"
      "v_fmac_f32 v246, v137, v137\n"
      "v_fmac_f32_e64 v246, v138, v138\n"
      "v_mfma_f32_16x16x32_bf16 v[200:203], a[196:199], v[164:167], v[200:203]\n"
      "s_nop 0\n"
      "v_fma_f32 v247, v160, v160, s38\n"
      "v_fmac_f32 v247, v161, v161\n"
      "v_mfma_f32_16x16x32_bf16 v[188:191], a[164:167], v[180:183], v[188:191]\n"
      "v_fmac_f32 v247, v162, v162\n"
      "v_fmac_f32 v246, v139, v139\n"
      "v_mfma_f32_16x16x32_bf16 v[188:191], a[200:203], v[164:167], v[188:191]\n"
      "v_fmac_f32 v246, v148, v148\n"
      "v_fmac_f32 v247, v163, v163\n"
      "v_mfma_f32_16x16x32_f16 v[212:215], v[204:207], v[156:159], v[70:73]\n"
      "v_fmac_f32 v246, v149, v149\n"
      "v_fmac_f32 v246, v150, v150\n"
      "v_mfma_f32_16x16x32_f16 v[212:215], v[140:143], v[228:231], v[212:215]\n"
      "v_fmac_f32 v247, v200, v200\n"
      "v_fmac_f32 v247, v201, v201\n"
      "v_mfma_f32_16x16x32_f16 v[212:215], v[224:227], v[32:35], v[212:215]\n"
      "v_fmac_f32 v246, v151, v151\n"
      "v_fmac_f32 v247, v202, v202\n"
      "v_fmac_f32 v246, v196, v196\n"
      "v_fmac_f32 v246, v197, v197\n"
      "v_fmac_f32 v247, v203, v203\n"
      "v_fmac_f32 v247, v188, v188\n"
      "v_fmac_f32 v247, v189, v189\n"
      "v_fmac_f32 v247, v190, v190\n"
      "v_fmac_f32 v246, v198, v198\n"
      "v_fmac_f32 v246, v199, v199\n"
      "v_fmac_f32 v247, v191, v191\n"
      "v_mov_b32 v245, v213\n"
      "v_mov_b32 v244, v212\n"
      "v_permlane16_swap_b32 v246, v247\n"
      "v_add_f32 v246, v246, v247\n"
      "v_mov_b32 v247, v246\n"
      "s_nop 1\n"
      "v_permlane32_swap_b32 v246, v247\n"
      "v_add_f32 v246, v246, v247\n"
      "v_rsq_f32 v246, v246\n"
      "s_nop 0\n"
      "v_mov_b32 v247, v246\n"
      "s_nop 1\n"
      "v_permlane16_swap_b32 v246, v247\n"
      "v_mul_f32 v160, v247, v160\n"
      "v_mul_f32 v163, v247, v163\n"
      "v_mul_f32 v161, v247, v161\n"
      "v_mul_f32 v201, v247, v201\n"
      "v_mul_f32 v162, v247, v162\n"
      "v_mul_f32 v189, v247, v189\n"
      "v_mul_f32 v188, v247, v188\n"
      "v_mul_f32 v200, v247, v200\n"
      "v_mul_f32 v246, s36, v246\n"
      "v_cvt_pk_f16_f32 v160, v160, v161\n"
      "v_cvt_pk_f16_f32 v161, v162, v163\n"
      "v_cvt_pk_f16_f32 v46, v188, v189\n"
      "v_mul_f32 v148, v246, v148\n"
      "v_mul_f32 v202, v247, v202\n"
      "v_mul_f32 v190, v247, v190\n"
      "v_mul_f32 v136, v246, v136\n"
      "v_mul_f32 v151, v246, v151\n"
      "v_mul_f32 v191, v247, v191\n"
      "v_mul_f32 v203, v247, v203\n"
      "v_cvt_pk_f16_f32 v47, v190, v191\n"
      "v_cvt_pk_f16_f32 v163, v202, v203\n"
      "v_mul_f32 v196, v246, v196\n"
      "v_mul_f32 v138, v246, v138\n"
      "v_cvt_pk_f16_f32 v162, v200, v201\n"
      "v_mul_f32 v150, v246, v150\n"
      "v_mul_f32 v149, v246, v149\n"
      "v_mul_f32 v139, v246, v139\n"
      "v_mfma_f32_16x16x32_f16 v[200:203], v[160:163], v[156:159], v[86:89]\n"
      "v_mul_f32 v137, v246, v137\n"
      "v_cvt_pk_f16_f32 v136, v136, v137\n"
      "v_mfma_f32_16x16x32_f16 v[200:203], v[46:49], v[32:35], v[200:203]\n"
      "v_cvt_pk_f16_f32 v137, v138, v139\n"
      "v_cvt_pk_f16_f32 v138, v148, v149\n"
      "v_mul_f32_e64 v199, v246, v199\n"
      "v_cvt_pk_f16_f32 v139, v150, v151\n"
      "v_mfma_f32_16x16x32_f16 v[148:151], v[228:231], v[156:159], v[78:81]\n"
      "v_mul_f32_e64 v197, v246, v197\n"
      "v_cvt_pk_f16_f32 v44, v196, v197\n"
      "v_mfma_f32_16x16x32_f16 v[148:151], v[34:37], v[32:35], v[148:151]\n"
      "v_mul_f32 v198, v246, v198\n"
      "v_cvt_pk_f16_f32 v45, v198, v199\n"
      "v_mfma_f32_16x16x32_f16 v[196:199], v[248:251], v[156:159], v[82:85]\n"
      "v_mfma_f32_16x16x32_f16 v[196:199], v[40:43], v[32:35], v[196:199]\n"
      "s_nop 7\n"
      "s_or_b64 vcc, s[48:49], s[50:51]\n"
      "s_cbranch_scc0 L_fast_22_%=\n"
      "L_ok_114_%=:\n"
      "s_cmp_eq_u64 s[48:49], 0\n"
      "s_cbranch_scc1 L_go_115_%=\n"
      "s_mov_b32 m0, s27\n"
      "s_nop 0\n"
      "s_movrels_b32 s9, s80\n"
      "s_lshr_b32 s8, s9, 8\n"
      "s_and_b32 s9, s9, 0xff\n"
      "s_mul_i32 s10, s8, 73\n"
      "s_add_u32 s10, s10, s9\n"
      "s_mul_i32 s30, s10, 4802\n"
      "s_add_u32 s30, s30, s18\n"
      "s_and_b32 s29, s30, 15\n"
      "s_add_u32 s30, s29, 115456\n"
      "s_lshl_b64 s[34:35], s[48:49], 4\n"
      "s_lshr_b64 s[34:35], s[34:35], 5\n"
      "s_cbranch_scc1 L_gen_20_%=\n"
      "v_mbcnt_lo_u32_b32 v246, -1, 0\n"
      "v_mbcnt_hi_u32_b32 v246, -1, v246\n"
      "v_lshlrev_b32 v247, 1, v246\n"
      "v_add_u32 v213, 4802, v247\n"
      "v_cmp_gt_u32 vcc, s29, v247\n"
      "s_nop 1\n"
      "v_cndmask_b32 v247, v213, v247, vcc\n"
      "v_mov_b32 v213, 0\n"
      "v_add_u32 v247, 115456, v247\n"
      "s_mov_b64 exec, 0x7f\n"
      "ds_write_b16 v247, v213\n"
      "s_mov_b64 exec, -1\n"
      "v_lshlrev_b32_e64 v247, 4, v246\n"
      "v_add_u32 v247, 115456, v247\n"
      "ds_read_b128 v[212:215], v247\n"
      "ds_read_b128 v[252:255], v247 offset:1024\n"
      "v_min_u32 v246, 116160, v247\n"
      "ds_read_b128 v[216:219], v247 offset:2048\n"
      "ds_read_b128 v[172:175], v246 offset:4096\n"
      "ds_read_b128 v[180:183], v247 offset:3072\n"
      "s_waitcnt lgkmcnt(4)\n"
      "v_or3_b32 v212, v212, v213, v214\n"
      "s_waitcnt lgkmcnt(3)\n"
      "v_or3_b32 v212, v212, v215, v252\n"
      "v_or3_b32 v212, v212, v253, v254\n"
      "s_waitcnt lgkmcnt(2)\n"
      "v_or3_b32 v212, v212, v255, v216\n"
      "v_or3_b32 v212, v212, v217, v218\n"
      "s_waitcnt lgkmcnt(0)\n"
      "v_or3_b32 v212, v212, v219, v180\n"
      "v_or3_b32 v212, v212, v181, v182\n"
      "v_or3_b32 v212, v212, v183, v172\n"
      "v_or3_b32 v212, v212, v173, v174\n"
      "v_bitop3_b32 v212, v212, v175, s40 bitop3:0xa8\n"
      "v_cmp_ne_u32 s[48:49], 0, v212\n"
      "s_cmp_lg_u64 s[48:49], 0\n"
      "s_cbranch_scc1 L_gen_20_%=\n"
      "s_cmp_eq_u64 s[50:51], 0\n"
      "s_cbranch_scc1 L_fast_22_%=\n"
      "L_go_115_%=:\n"
      "v_mfma_f32_16x16x32_f16 v[212:215], v[228:231], v[220:223], v[90:93]\n"
      "ds_read_b128 v[252:255], v128 offset:2048\n"
      "v_max3_f32 v247, v148, v149, v150\n"
      "v_mfma_f32_16x16x32_f16 v[212:215], v[34:37], v[38:41], v[212:215]\n"
      "v_max3_f32 v246, v151, v196, v197\n"
      "v_max3_f32 v246, v246, v200, v201\n"
      "v_mfma_f32_16x16x32_f16 v[216:219], v[248:251], v[220:223], v[94:97]\n"
      "v_max3_f32 v247, v247, v198, v199\n"
      "v_max3_f32 v247, v247, v202, v203\n"
      "v_mfma_f32_16x16x32_f16 v[216:219], v[40:43], v[38:41], v[216:219]\n"
      "v_max3_f32 v247, v247, v246, v244\n"
      "v_mov_b32 v246, v247\n"
      "v_mfma_f32_16x16x32_f16 v[172:175], v[204:207], v[220:223], v[72:75]\n"
      "s_nop 0\n"
      "v_permlane16_swap_b32 v247, v246\n"
      "v_mfma_f32_16x16x32_f16 v[172:175], v[140:143], v[248:251], v[172:175]\n"
      "v_max_f32 v247, v247, v246\n"
      "v_mov_b32 v246, v247\n"
      "v_mfma_f32_16x16x32_f16 v[172:175], v[224:227], v[38:41], v[172:175]\n"
      "s_nop 0\n"
      "v_permlane32_swap_b32 v247, v246\n"
      "v_max_f32 v247, v247, v246\n"
      "v_mfma_f32_16x16x32_f16 v[180:183], v[160:163], v[220:223], v[98:101]\n"
      "v_sub_f32 v149, v149, v247\n"
      "v_sub_f32 v197, v197, v247\n"
      "v_mfma_f32_16x16x32_f16 v[180:183], v[46:49], v[38:41], v[180:183]\n"
      "s_waitcnt lgkmcnt(0)\n"
      "v_exp_f32 v149, v149\n"
      "v_sub_f32 v201, v201, v247\n"
      "v_sub_f32 v151, v151, v247\n"
      "v_sub_f32 v150, v150, v247\n"
      "v_sub_f32 v196, v196, v247\n"
      "v_bitop3_b32 v254, v254, v255, s4 bitop3:0x7e\n"
      "v_sub_f32 v202, v202, v247\n"
      "v_exp_f32 v197, v197\n"
      "v_sub_f32 v148, v148, v247\n"
      "v_sub_f32 v199, v199, v247\n"
      "v_exp_f32 v148, v148\n"
      "v_exp_f32 v150, v150\n"
      "v_exp_f32_e64 v201, v201\n"
      "v_cvt_pk_bf16_f32 v148, v148, v149\n"
      "v_exp_f32 v196, v196\n"
      "v_sub_f32 v244, v244, v247\n"
      "v_sub_f32 v198, v198, v247\n"
      "v_exp_f32 v151, v151\n"
      "v_exp_f32 v198, v198\n"
      "v_exp_f32 v244, v244\n"
      "v_bitop3_b32 v252, v252, v253, s4 bitop3:0x7e\n"
      "v_cvt_pk_bf16_f32 v149, v150, v151\n"
      "v_sub_f32 v200, v200, v247\n"
      "v_exp_f32 v202, v202\n"
      "v_cvt_pk_bf16_f32 v150, v196, v197\n"
      "v_exp_f32 v200, v200\n"
      "v_sub_f32 v203, v203, v247\n"
      "v_exp_f32 v203, v203\n"
      "v_exp_f32 v199, v199\n"
      "v_cvt_pk_bf16_f32 v63, v202, v203\n"
      "v_cvt_pk_bf16_f32 v151, v198, v199\n"
      "v_cvt_pk_bf16_f32 v64, v244, 0\n"
      "v_cvt_pk_bf16_f32 v62, v200, v201\n"
      "v_mfma_f32_16x16x32_bf16 v[196:199], v[66:69], v[148:151], 0\n"
      "v_or_b32 v252, v252, v254\n"
      "v_cmp_ne_u32 vcc, 0, v252\n"
      "v_mfma_f32_16x16x32_bf16 v[196:199], v[66:69], v[62:65], v[196:199]\n"
      "s_mov_b64 s[48:49], vcc\n"
      "v_max3_f32 v174, v215, v216, v217\n"
      "v_max3_f32 v175, v212, v213, v214\n"
      "v_mfma_f32_16x16x32_bf16 v[200:203], v[152:155], v[148:151], 0\n"
      "s_cbranch_vccnz L_flg_116_%=\n"
      "L_fb_117_%=:\n"
      "v_mfma_f32_16x16x32_bf16 v[200:203], v[50:53], v[62:65], v[200:203]\n"
      "v_max3_f32 v174, v174, v180, v181\n"
      "v_max3_f32 v175, v175, v218, v219\n"
      "v_mfma_f32_16x16x32_bf16 v[252:255], v[234:237], v[148:151], 0\n"
      "v_max3_f32 v175, v175, v182, v183\n"
      "v_max3_f32 v175, v175, v174, v172\n"
      "v_mfma_f32_16x16x32_bf16 v[252:255], v[54:57], v[62:65], v[252:255]\n"
      "v_mov_b32 v174, v175\n"
      "v_rcp_f32 v196, v196\n"
      "v_mfma_f32_16x16x32_bf16 v[220:223], v[184:187], v[148:151], 0\n"
      "v_permlane16_swap_b32 v175, v174\n"
      "v_mfma_f32_16x16x32_bf16 v[220:223], v[58:61], v[62:65], v[220:223]\n"
      "v_max_f32 v175, v175, v174\n"
      "v_mov_b32 v174, v175\n"
      "v_mul_f32 v200, v196, v200\n"
      "v_mul_f32 v201, v196, v201\n"
      "v_permlane32_swap_b32 v175, v174\n"
      "v_max_f32 v175, v175, v174\n"
      "v_sub_f32 v214, v214, v175\n"
      "v_sub_f32 v216, v216, v175\n"
      "v_sub_f32 v218, v218, v175\n"
      "v_mfma_f32_16x16x32_f16 v[148:151], v[228:231], v[136:139], v[102:105]\n"
      "v_sub_f32 v183, v183, v175\n"
      "v_sub_f32 v182, v182, v175\n"
      "v_mfma_f32_16x16x32_f16 v[148:151], v[34:37], v[44:47], v[148:151]\n"
      "v_sub_f32 v212, v212, v175\n"
      "v_sub_f32 v215, v215, v175\n"
      "v_exp_f32 v215, v215\n"
      "v_sub_f32 v217, v217, v175\n"
      "v_sub_f32 v213, v213, v175\n"
      "v_exp_f32 v213, v213\n"
      "v_exp_f32 v214, v214\n"
      "v_exp_f32 v212, v212\n"
      "v_exp_f32_e64 v218, v218\n"
      "v_mfma_f32_16x16x32_f16 v[228:231], v[248:251], v[136:139], v[106:109]\n"
      "ds_read_b128 v[156:159], v120\n"
      "v_cvt_pk_bf16_f32 v212, v212, v213\n"
      "v_mfma_f32_16x16x32_f16 v[228:231], v[40:43], v[44:47], v[228:231]\n"
      "v_exp_f32_e64 v216, v216\n"
      "v_mfma_f32_16x16x32_f16 v[188:191], v[204:207], v[136:139], v[74:77]\n"
      "v_cvt_pk_bf16_f32 v213, v214, v215\n"
      "v_cvt_pk_bf16_f32 v200, v200, v201\n"
      "v_mfma_f32_16x16x32_f16 v[188:191], v[140:143], v[160:163], v[188:191]\n"
      "v_exp_f32 v217, v217\n"
      "v_mfma_f32_16x16x32_f16 v[188:191], v[224:227], v[44:47], v[188:191]\n"
      "v_sub_f32 v172, v172, v175\n"
      "v_sub_f32 v219, v219, v175\n"
      "v_sub_f32 v181, v181, v175\n"
      "v_sub_f32 v180, v180, v175\n"
      "v_cvt_pk_bf16_f32 v214, v216, v217\n"
      "v_exp_f32 v219, v219\n"
      "v_mfma_f32_16x16x32_f16 v[168:171], v[160:163], v[136:139], v[110:113]\n"
      "v_exp_f32_e64 v180, v180\n"
      "v_mfma_f32_16x16x32_f16 v[168:171], v[46:49], v[44:47], v[168:171]\n"
      "v_exp_f32 v182, v182\n"
      "v_mfma_f32_16x16x32_f16 v[136:139], v[140:143], v[204:207], v[76:79]\n"
      "s_waitcnt lgkmcnt(0)\n"
      "v_cvt_pk_bf16_f32 v215, v218, v219\n"
      "v_exp_f32 v181, v181\n"
      "v_mul_f32 v202, v196, v202\n"
      "v_exp_f32 v183, v183\n"
      "v_exp_f32 v172, v172\n"
      "v_cvt_pk_bf16_f32 v62, v180, v181\n"
      "v_mfma_f32_16x16x16_f16 v[136:139], v[226:227], v[224:225], v[136:139]\n"
      "v_mul_f32 v253, v196, v253\n"
      "v_cvt_pk_bf16_f32 v63, v182, v183\n"
      "v_cvt_pk_bf16_f32 v64, v172, 0\n"
      "v_mul_f32 v203, v196, v203\n"
      "v_mfma_f32_16x16x32_bf16 v[216:219], v[66:69], v[212:215], 0\n"
      "v_cvt_pk_bf16_f32 v201, v202, v203\n"
      "v_mul_f32_e64 v252, v196, v252\n"
      "v_mfma_f32_16x16x32_bf16 v[216:219], v[66:69], v[62:65], v[216:219]\n"
      "global_store_dwordx4 v117, v[156:159], s[54:55] sc1\n"
      "v_mfma_f32_16x16x32_bf16 v[180:183], v[152:155], v[212:215], 0\n"
      "v_mul_f32 v254, v196, v254\n"
      "v_mul_f32 v255, v196, v255\n"
      "v_mfma_f32_16x16x32_bf16 v[180:183], v[50:53], v[62:65], v[180:183]\n"
      "v_cvt_pk_bf16_f32 v203, v254, v255\n"
      "v_cvt_pk_bf16_f32 v202, v252, v253\n"
      "v_mul_f32 v220, v196, v220\n"
      "v_mfma_f32_16x16x32_bf16 v[252:255], v[234:237], v[212:215], 0\n"
      "v_mul_f32 v221, v196, v221\n"
      "v_cvt_pk_bf16_f32 v220, v220, v221\n"
      "v_mfma_f32_16x16x32_bf16 v[252:255], v[54:57], v[62:65], v[252:255]\n"
      "ds_write_b128 v124, v[200:203] offset:18432\n"
      "v_mfma_f32_16x16x32_bf16 v[200:203], v[184:187], v[212:215], 0\n"
      "v_mul_f32 v223, v196, v223\n"
      "v_mul_f32 v222, v196, v222\n"
      "v_mfma_f32_16x16x32_bf16 v[200:203], v[58:61], v[62:65], v[200:203]\n"
      "ds_read_b128 v[224:227], v121\n"
      "v_cvt_pk_bf16_f32 v221, v222, v223\n"
      "v_max3_f32 v222, v148, v149, v150\n"
      "v_max3_f32 v223, v151, v228, v229\n"
      "v_max3_f32 v222, v222, v230, v231\n"
      "v_max3_f32 v222, v222, v170, v171\n"
      "v_max3_f32 v223, v223, v168, v169\n"
      "v_max3_f32 v222, v222, v223, v188\n"
      "v_mov_b32 v223, v222\n"
      "v_rcp_f32 v216, v216\n"
      "s_nop 0\n"
      "v_permlane16_swap_b32 v222, v223\n"
      "v_max_f32 v222, v222, v223\n"
      "v_mov_b32 v223, v222\n"
      "v_mul_f32 v180, v216, v180\n"
      "v_mul_f32 v181, v216, v181\n"
      "v_permlane32_swap_b32 v222, v223\n"
      "v_max_f32 v222, v222, v223\n"
      "v_sub_f32 v170, v170, v222\n"
      "v_sub_f32 v149, v149, v222\n"
      "v_sub_f32 v229, v229, v222\n"
      "v_sub_f32 v169, v169, v222\n"
      "v_sub_f32 v168, v168, v222\n"
      "v_sub_f32 v188, v188, v222\n"
      "v_sub_f32 v231, v231, v222\n"
      "v_sub_f32 v148, v148, v222\n"
      "v_sub_f32 v150, v150, v222\n"
      "v_exp_f32 v229, v229\n"
      "v_sub_f32 v171, v171, v222\n"
      "v_exp_f32 v149, v149\n"
      "v_exp_f32 v231, v231\n"
      "v_exp_f32 v170, v170\n"
      "v_sub_f32 v228, v228, v222\n"
      "v_sub_f32 v230, v230, v222\n"
      "v_sub_f32 v151, v151, v222\n"
      "v_exp_f32 v188, v188\n"
      "v_exp_f32 v168, v168\n"
      "v_exp_f32 v169, v169\n"
      "v_exp_f32 v148, v148\n"
      "v_exp_f32 v171, v171\n"
      "v_exp_f32 v228, v228\n"
      "v_cvt_pk_bf16_f32 v148, v148, v149\n"
      "v_exp_f32 v150, v150\n"
      "v_exp_f32_e64 v151, v151\n"
      "v_cvt_pk_bf16_f32 v62, v168, v169\n"
      "v_exp_f32_e64 v230, v230\n"
      "v_cvt_pk_bf16_f32 v149, v150, v151\n"
      "v_cvt_pk_bf16_f32 v150, v228, v229\n"
      "v_cvt_pk_bf16_f32 v151, v230, v231\n"
      "v_mul_f32_e64 v182, v216, v182\n"
      "v_cvt_pk_bf16_f32 v64, v188, 0\n"
      "v_cvt_pk_bf16_f32 v63, v170, v171\n"
      "v_mfma_f32_16x16x32_bf16 v[212:215], v[66:69], v[148:151], 0\n"
      "ds_read_b128 v[228:231], v123 offset:4096\n"
      "v_cvt_pk_bf16_f32 v180, v180, v181\n"
      "v_mfma_f32_16x16x32_bf16 v[212:215], v[66:69], v[62:65], v[212:215]\n"
      "v_mul_f32 v183, v216, v183\n"
      "v_cvt_pk_bf16_f32 v181, v182, v183\n"
      "v_mul_f32 v252, v216, v252\n"
      "v_mfma_f32_16x16x32_bf16 v[204:207], v[234:237], v[148:151], 0\n"
      "v_mul_f32_e64 v253, v216, v253\n"
      "v_cvt_pk_bf16_f32 v182, v252, v253\n"
      "s_waitcnt lgkmcnt(1)\n"
      "v_mfma_f32_16x16x32_bf16 v[204:207], v[54:57], v[62:65], v[204:207]\n"
      "ds_read_b128 v[156:159], v123 offset:1024\n"
      "v_mul_f32 v203, v216, v203\n"
      "v_mul_f32 v254, v216, v254\n"
      "v_mul_f32 v202, v216, v202\n"
      "v_mul_f32 v200, v216, v200\n"
      "v_mul_f32 v201, v216, v201\n"
      "v_cvt_pk_bf16_f32 v200, v200, v201\n"
      "v_mul_f32_e64 v255, v216, v255\n"
      "v_cvt_pk_bf16_f32 v183, v254, v255\n"
      "v_mfma_f32_16x16x32_bf16 v[252:255], v[152:155], v[148:151], 0\n"
      "v_cndmask_b32 v245, v245, v173, s[42:43]\n"
      "v_cvt_pk_bf16_f32 v201, v202, v203\n"
      "v_mfma_f32_16x16x32_bf16 v[252:255], v[50:53], v[62:65], v[252:255]\n"
      "ds_write_b128 v124, v[180:183] offset:19456\n"
      "v_mfma_f32_16x16x32_bf16 v[216:219], v[184:187], v[148:151], 0\n"
      "ds_read_b128 v[148:151], v123 offset:7168\n"
      "v_cndmask_b32 v245, v245, v189, s[44:45]\n"
      "v_mfma_f32_16x16x32_bf16 v[216:219], v[58:61], v[62:65], v[216:219]\n"
      "ds_write2st64_b64 v125, v[220:221], v[200:201] offset0:36 offset1:38\n"
      "v_cndmask_b32 v245, v245, v137, s[46:47]\n"
      "s_waitcnt lgkmcnt(3)\n"
      "v_mfma_f32_16x16x32_bf16 v[196:199], a[216:219], v[156:159], 0\n"
      "ds_read_b128 v[200:203], v123 offset:10240\n"
      "v_max_f32_dpp v244, v245, v245 row_ror:8 row_mask:0xf bank_mask:0xf\n"
      "v_mfma_f32_16x16x32_bf16 v[196:199], a[228:231], v[228:231], v[196:199]\n"
      "s_mov_b32 exec_hi, 0\n"
      "global_store_dwordx4 v118, v[224:227], s[54:55] sc1\n"
      "s_mov_b32 exec_hi, -1\n"
      "v_mfma_f32_16x16x32_bf16 v[172:175], a[220:223], v[156:159], 0\n"
      "s_add_u32 s28, s27, 2\n"
      "s_min_u32 m0, s28, s3\n"
      "v_max_f32_dpp v244, v244, v244 row_ror:4 row_mask:0xf bank_mask:0xf\n"
      "v_mfma_f32_16x16x32_bf16 v[172:175], a[232:235], v[228:231], v[172:175]\n"
      "s_nop 0\n"
      "v_max_f32_dpp v244, v244, v244 row_ror:2 row_mask:0xf bank_mask:0xf\n"
      "s_movrels_b32 s9, s80\n"
      "v_mfma_f32_16x16x32_bf16 v[180:183], a[224:227], v[156:159], 0\n"
      "ds_read_b128 v[220:223], v123 offset:13312\n"
      "v_max_f32_dpp v244, v244, v244 row_ror:1 row_mask:0xf bank_mask:0xf\n"
      "v_mfma_f32_16x16x32_bf16 v[180:183], a[236:239], v[228:231], v[180:183]\n"
      "s_lshr_b32 s8, s9, 8\n"
      "v_max_f32_dpp v244, v244, v244 row_bcast:15 row_mask:0xa bank_mask:0xf\n"
      "ds_read_b128 v[228:231], v123 offset:16384\n"
      "s_nop 0\n"
      "v_max_f32_dpp v244, v244, v244 row_bcast:31 row_mask:0xc bank_mask:0xf\n"
      "s_nop 0\n"
      "v_readlane_b32 s31, v244, 63\n"
      "s_and_b32 s9, s9, 0xff\n"
      "s_waitcnt lgkmcnt(2)\n"
      "v_subrev_f32 v245, s31, v245\n"
      "v_mfma_f32_16x16x32_bf16 v[196:199], a[240:243], v[148:151], v[196:199]\n"
      "s_mul_i32 s10, s8, 73\n"
      "v_exp_f32 v245, v245\n"
      "v_mfma_f32_16x16x32_bf16 v[196:199], a[252:255], v[200:203], v[196:199]\n"
      "s_add_u32 s10, s10, s9\n"
      "v_cvt_pk_bf16_f32 v245, v245, v245\n"
      "v_mfma_f32_16x16x32_bf16 v[172:175], a[244:247], v[148:151], v[172:175]\n"
      "s_mul_i32 s28, s10, 4802\n"
      "s_add_u32 s56, s18, s28\n"
      "s_addc_u32 s57, s19, 0\n"
      "v_mfma_f32_16x16x32_bf16 v[172:175], v[0:3], v[200:203], v[172:175]\n"
      "ds_write_b16 v130, v245\n"
      "v_mfma_f32_16x16x32_bf16 v[180:183], a[248:251], v[148:151], v[180:183]\n"
      "ds_read_b128 v[224:227], v123 offset:2048\n"
      "s_and_b32 s56, s56, -16\n"
      "v_mfma_f32_16x16x32_bf16 v[180:183], v[4:7], v[200:203], v[180:183]\n"
      "ds_read_b128 v[156:159], v123 offset:5120\n"
      "s_waitcnt lgkmcnt(3)\n"
      "v_mfma_f32_16x16x32_bf16 v[196:199], v[8:11], v[220:223], v[196:199]\n"
      "s_sub_u32 s58, s56, 110592\n"
      "s_subb_u32 s59, s57, 0\n"
      "s_sub_u32 s56, s56, 73728\n"
      "v_mfma_f32_16x16x32_bf16 v[196:199], v[20:23], v[228:231], v[196:199]\n"
      "s_subb_u32 s57, s57, 0\n"
      "s_mul_i32 s28, s8, 1373568\n"
      "v_mfma_f32_16x16x32_bf16 v[172:175], v[12:15], v[220:223], v[172:175]\n"
      "s_mul_i32 s29, s9, 2688\n"
      "s_add_u32 s28, s28, s29\n"
      "s_cmp_eq_u32 s8, 72\n"
      "s_cselect_b32 s8, 0, 100270464\n"
      "s_cmp_eq_u32 s9, 72\n"
      "v_mfma_f32_16x16x32_bf16 v[180:183], v[16:19], v[220:223], v[180:183]\n"
      "s_cselect_b32 s9, 0, 196224\n"
      "ds_read_b128 v[148:151], v131\n"
      "v_mfma_f32_16x16x32_bf16 v[180:183], v[28:31], v[228:231], v[180:183]\n"
      "s_add_u32 s10, s8, s9\n"
      "s_add_u32 s28, s28, s10\n"
      "s_add_u32 s52, s16, s28\n"
      "s_addc_u32 s53, s17, 0\n"
      "ds_read_b128 v[168:171], v131 offset:64\n"
      "v_mfma_f32_16x16x32_bf16 v[172:175], v[24:27], v[228:231], v[172:175]\n"
      "s_add_u32 s27, s27, 1\n"
      "s_cmp_ge_u32 s27, s26\n"
      "s_cselect_b32 s28, 1, 0\n"
      "s_waitcnt lgkmcnt(2)\n"
      "v_mfma_f32_16x16x32_bf16 v[200:203], a[216:219], v[224:227], 0\n"
      "ds_read_b128 v[188:191], v123 offset:8192\n"
      "v_mfma_f32_16x16x32_bf16 v[200:203], a[228:231], v[156:159], v[200:203]\n"
      "s_xor_b32 s29, s10, s11\n"
      "ds_read_b128 v[140:143], v123 offset:11264\n"
      "v_mfma_f32_16x16x32_bf16 v[136:139], a[220:223], v[224:227], 0\n"
      "ds_read_b128 v[220:223], v114\n"
      "v_mfma_f32_16x16x32_bf16 v[228:231], a[224:227], v[224:227], 0\n"
      "s_or_b32 s28, s28, s29\n"
      "v_fma_f32 v224, v196, v196, s39\n"
      "v_fmac_f32 v224, v197, v197\n"
      "v_mfma_f32_16x16x32_bf16 v[228:231], a[236:239], v[156:159], v[228:231]\n"
      "ds_read_b128 v[244:247], v123 offset:17408\n"
      "v_fmac_f32 v224, v198, v198\n"
      "v_fmac_f32 v224, v199, v199\n"
      "v_mfma_f32_16x16x32_bf16 v[136:139], a[232:235], v[156:159], v[136:139]\n"
      "ds_read_b128 v[156:159], v123 offset:14336\n"
      "v_fmac_f32 v224, v172, v172\n"
      "v_fmac_f32 v224, v173, v173\n"
      "v_fmac_f32 v224, v174, v174\n"
      "v_fmac_f32 v224, v175, v175\n"
      "v_fmac_f32 v224, v180, v180\n"
      "v_fmac_f32 v224, v181, v181\n"
      "v_fmac_f32 v224, v182, v182\n"
      "v_fmac_f32_e64 v224, v183, v183\n"
      "s_nop 1\n"
      "v_permlane16_swap_b32_e64 v233, v224\n"
      "s_waitcnt lgkmcnt(4)\n"
      "v_mfma_f32_16x16x32_bf16 v[248:251], v[148:151], v[152:155], 0\n"
      "s_waitcnt lgkmcnt(3)\n"
      "v_mfma_f32_16x16x32_bf16 v[200:203], a[240:243], v[188:191], v[200:203]\n"
      "v_mfma_f32_16x16x32_bf16 v[200:203], a[252:255], v[140:143], v[200:203]\n"
      "v_mfma_f32_16x16x32_bf16 v[136:139], a[244:247], v[188:191], v[136:139]\n"
      "v_mfma_f32_16x16x32_bf16 v[136:139], v[0:3], v[140:143], v[136:139]\n"
      "v_add_f32 v233, v233, v224\n"
      "s_waitcnt lgkmcnt(0)\n"
      "v_mfma_f32_16x16x32_bf16 v[228:231], a[248:251], v[188:191], v[228:231]\n"
      "v_mfma_f32_16x16x32_bf16 v[228:231], v[4:7], v[140:143], v[228:231]\n"
      "v_mfma_f32_16x16x32_bf16 v[200:203], v[8:11], v[156:159], v[200:203]\n"
      "ds_read_b128 v[188:191], v115 offset:6144\n"
      "v_mfma_f32_16x16x32_bf16 v[200:203], v[20:23], v[244:247], v[200:203]\n"
      "v_mfma_f32_16x16x32_bf16 v[136:139], v[12:15], v[156:159], v[136:139]\n"
      "v_mfma_f32_16x16x32_bf16 v[136:139], v[24:27], v[244:247], v[136:139]\n"
      "ds_read_b128 v[224:227], v114 offset:6144\n"
      "v_mfma_f32_16x16x32_bf16 v[228:231], v[16:19], v[156:159], v[228:231]\n"
      "ds_read_b128 v[156:159], v115\n"
      "v_mfma_f32_16x16x32_bf16 v[228:231], v[28:31], v[244:247], v[228:231]\n"
      "v_mfma_f32_16x16x32_bf16 v[244:247], v[148:151], v[66:69], 0\n"
      "s_nop 0\n"
      "v_fma_f32 v162, v200, v200, s39\n"
      "v_mfma_f32_16x16x32_bf16 v[244:247], v[168:171], v[66:69], v[244:247]\n"
      "v_fmac_f32 v162, v201, v201\n"
      "v_mfma_f32_16x16x32_bf16 v[248:251], v[168:171], v[50:53], v[248:251]\n"
      "v_fmac_f32 v162, v202, v202\n"
      "v_fmac_f32 v162, v203, v203\n"
      "v_mfma_f32_16x16x32_bf16 v[140:143], v[148:151], v[234:237], 0\n"
      "v_fmac_f32 v162, v136, v136\n"
      "v_fmac_f32 v162, v137, v137\n"
      "v_mfma_f32_16x16x32_bf16 v[140:143], v[168:171], v[54:57], v[140:143]\n"
      "v_fmac_f32 v162, v138, v138\n"
      "v_fmac_f32 v162, v139, v139\n"
      "v_fmac_f32 v162, v228, v228\n"
      "v_mov_b32_dpp v244, v244 quad_perm:[0,0,0,0] row_mask:0xf bank_mask:0xf\n"
      "v_fmac_f32 v162, v229, v229\n"
      "v_fmac_f32 v162, v230, v230\n"
      "v_fmac_f32 v162, v231, v231\n"
      "v_mfma_f32_16x16x32_bf16 v[234:237], v[148:151], v[184:187], 0\n"
      "v_cndmask_b32 v248, v248, v140, s[42:43]\n"
      "v_mov_b32 v245, v162\n"
      "v_mfma_f32_16x16x32_bf16 v[234:237], v[168:171], v[58:61], v[234:237]\n"
      "s_waitcnt lgkmcnt(0)\n"
      "v_permlane16_swap_b32 v162, v245\n"
      "v_add_f32 v162, v162, v245\n"
      "v_mfma_f32_16x16x32_bf16 v[140:143], a[0:3], v[220:223], 0\n"
      "s_nop 0\n"
      "v_permlane32_swap_b32 v233, v162\n"
      "v_mfma_f32_16x16x32_bf16 v[140:143], a[36:39], v[156:159], v[140:143]\n"
      "v_add_f32_e64 v233, v233, v162\n"
      "v_rcp_f32_e64 v244, v244\n"
      "v_mfma_f32_16x16x32_bf16 v[162:165], a[4:7], v[220:223], 0\n"
      "v_cndmask_b32 v248, v248, v234, s[44:45]\n"
      "v_mul_f32_e64 v248, v244, v248\n"
      "v_mfma_f32_16x16x32_bf16 v[162:165], a[40:43], v[156:159], v[162:165]\n"
      "ds_write_b32 v129, v233 offset:1024\n"
      "v_mfma_f32_16x16x32_bf16 v[148:151], a[8:11], v[220:223], 0\n"
      "v_mfma_f32_16x16x32_bf16 v[148:151], a[44:47], v[156:159], v[148:151]\n"
      "v_mfma_f32_16x16x32_bf16 v[234:237], a[12:15], v[220:223], 0\n"
      "v_cvt_pk_bf16_f32 v248, v248, v248\n"
      "v_mfma_f32_16x16x32_bf16 v[234:237], a[48:51], v[156:159], v[234:237]\n"
      "ds_write_b16 v135, v248 offset:16\n"
      "v_mfma_f32_16x16x32_bf16 v[168:171], a[16:19], v[220:223], 0\n"
      "v_mfma_f32_16x16x32_bf16 v[168:171], a[52:55], v[156:159], v[168:171]\n"
      "v_mfma_f32_16x16x32_bf16 v[244:247], a[20:23], v[220:223], 0\n"
      "v_mfma_f32_16x16x32_bf16 v[244:247], a[56:59], v[156:159], v[244:247]\n"
      "s_branch L_join_21_%=\n"
      "L_flg_116_%=:\n"
      "v_cmp_gt_i32 vcc, 0, v252\n"
      "s_cbranch_vccz L_fb_117_%=\n"
      "s_sleep 1\n"
      "ds_read_b128 v[252:255], v128 offset:2048\n"
      "s_waitcnt lgkmcnt(0)\n"
      "v_bitop3_b32 v252, v252, v253, s4 bitop3:0x7e\n"
      "v_bitop3_b32 v254, v254, v255, s4 bitop3:0x7e\n"
      "v_or_b32 v252, v252, v254\n"
      "v_cmp_ne_u32 s[48:49], 0, v252\n"
      "s_branch L_flg_116_%=\n"
      "L_gen_20_%=:\n"
      "s_nop 0\n"
      "s_nop 0\n"
      "v_mbcnt_lo_u32_b32 v232, -1, 0\n"
      "v_mbcnt_hi_u32_b32 v232, -1, v232\n"
      "v_and_b32 v169, 15, v232\n"
      "v_mul_u32_u24 v238, 0x62, v169\n"
      "v_lshrrev_b32 v168, 4, v232\n"
      "v_lshl_add_u32 v238, v168, 3, v238\n"
      "v_add_u32 v238, s30, v238\n"
      "v_lshlrev_b32 v168, 2, v168\n"
      "v_sub_u32 v169, v169, v168\n"
      "v_and_b32 v232, -4, v238\n"
      "v_and_b32 v168, 1, v169\n"
      "v_lshrrev_b32 v169, 1, v169\n"
      "s_movk_i32 s31, 0x3fb9\n"
      "ds_read2_b32 v[246:247], v232 offset0:8 offset1:9\n"
      "ds_read2_b32 v[252:253], v232 offset0:2 offset1:10\n"
      "ds_read2_b32 v[212:213], v232 offset0:16 offset1:17\n"
      "v_lshlrev_b32_e64 v168, 4, v168\n"
      "v_cmp_eq_u32 vcc, 0, v169\n"
      "v_mfma_f32_16x16x32_f16 v[192:195], v[248:251], v[220:223], v[94:97]\n"
      "ds_read2_b32 v[254:255], v232 offset0:18 offset1:24\n"
      "ds_read2_b32 v[214:215], v232 offset0:0 offset1:1\n"
      "v_add_u32 v232, 1568, v232\n"
      "v_lshlrev_b32 v168, v168, s31\n"
      "v_cndmask_b32 v164, 0, v168, vcc\n"
      "ds_read2_b32 v[216:217], v232 offset0:0 offset1:1\n"
      "ds_read2_b32 v[172:173], v232 offset0:16 offset1:17\n"
      "ds_read2_b32 v[174:175], v232 offset0:2 offset1:10\n"
      "ds_read2_b32 v[218:219], v232 offset0:8 offset1:9\n"
      "v_cmp_eq_u32 vcc, 1, v169\n"
      "ds_read2_b32 v[180:181], v232 offset0:18 offset1:24\n"
      "v_and_b32_e64 v238, 2, v238\n"
      "v_add_u32 v232, 1568, v232\n"
      "v_mfma_f32_16x16x32_f16 v[240:243], v[204:207], v[220:223], v[72:75]\n"
      "ds_read2_b32 v[156:157], v232 offset0:8 offset1:9\n"
      "ds_read2_b32 v[190:191], v232 offset0:18 offset1:24\n"
      "ds_read2_b32 v[182:183], v232 offset0:0 offset1:1\n"
      "s_waitcnt lgkmcnt(10)\n"
      "v_cndmask_b32 v165, 0, v168, vcc\n"
      "v_alignbyte_b32 v246, v247, v246, v238\n"
      "v_mfma_f32_16x16x32_f16 v[168:171], v[228:231], v[220:223], v[90:93]\n"
      "ds_read2_b32 v[188:189], v232 offset0:2 offset1:10\n"
      "s_waitcnt lgkmcnt(9)\n"
      "v_mfma_f32_16x16x32_f16 v[168:171], v[34:37], v[38:41], v[168:171]\n"
      "ds_read2_b32 v[158:159], v232 offset0:16 offset1:17\n"
      "v_alignbyte_b32 v255, v255, v255, v238\n"
      "v_alignbyte_b32 v214, v215, v214, v238\n"
      "v_alignbyte_b32 v212, v213, v212, v238\n"
      "v_alignbyte_b32 v213, v254, v213, v238\n"
      "v_alignbyte_b32 v215, v252, v215, v238\n"
      "v_mfma_f32_16x16x32_f16 v[192:195], v[40:43], v[38:41], v[192:195]\n"
      "s_waitcnt lgkmcnt(8)\n"
      "v_lshlrev_b32_e64 v255, 16, v255\n"
      "v_alignbyte_b32 v216, v217, v216, v238\n"
      "v_mfma_f32_16x16x32_f16 v[240:243], v[140:143], v[248:251], v[240:243]\n"
      "s_waitcnt lgkmcnt(6)\n"
      "v_alignbyte_b32 v218, v219, v218, v238\n"
      "v_fmac_f32 v244, s37, v255\n"
      "v_mfma_f32_16x16x32_f16 v[240:243], v[224:227], v[38:41], v[240:243]\n"
      "v_alignbyte_b32 v219, v175, v219, v238\n"
      "s_waitcnt lgkmcnt(5)\n"
      "v_mfma_f32_16x16x16_bf16 v[148:151], v[164:165], v[214:215], v[148:151]\n"
      "v_alignbyte_b32 v247, v253, v247, v238\n"
      "v_alignbyte_b32 v172, v173, v172, v238\n"
      "v_mfma_f32_16x16x32_f16 v[252:255], v[160:163], v[220:223], v[98:101]\n"
      "v_alignbyte_b32 v173, v180, v173, v238\n"
      "v_mfma_f32_16x16x16_bf16 v[196:199], v[164:165], v[246:247], v[196:199]\n"
      "v_alignbyte_b32 v217, v174, v217, v238\n"
      "v_alignbyte_b32 v181, v181, v181, v238\n"
      "s_waitcnt lgkmcnt(1)\n"
      "v_mfma_f32_16x16x16_bf16 v[200:203], v[164:165], v[212:213], v[200:203]\n"
      "ds_read_b128 v[212:215], v128 offset:2048\n"
      "v_max3_f32 v180, v148, v149, v150\n"
      "v_mfma_f32_16x16x32_f16 v[252:255], v[46:49], v[38:41], v[252:255]\n"
      "s_nop 0\n"
      "v_max3_f32 v180, v180, v198, v199\n"
      "v_lshlrev_b32 v181, 16, v181\n"
      "v_mfma_f32_16x16x16_bf16 v[192:195], v[164:165], v[218:219], v[192:195]\n"
      "s_nop 0\n"
      "v_max3_f32 v180, v180, v202, v203\n"
      "v_max3_f32 v175, v151, v196, v197\n"
      "v_max3_f32 v175, v175, v200, v201\n"
      "v_max3_f32 v180, v180, v175, v244\n"
      "v_mov_b32 v218, v180\n"
      "v_mfma_f32_16x16x16_bf16 v[168:171], v[164:165], v[216:217], v[168:171]\n"
      "s_nop 0\n"
      "v_permlane16_swap_b32 v180, v218\n"
      "v_max_f32 v180, v180, v218\n"
      "v_mov_b32_e64 v246, v180\n"
      "v_mfma_f32_16x16x16_bf16 v[252:255], v[164:165], v[172:173], v[252:255]\n"
      "s_nop 0\n"
      "v_permlane32_swap_b32 v180, v246\n"
      "v_max_f32 v180, v180, v246\n"
      "v_sub_f32 v196, v196, v180\n"
      "v_sub_f32 v148, v148, v180\n"
      "v_sub_f32 v151, v151, v180\n"
      "v_alignbyte_b32 v156, v157, v156, v238\n"
      "v_sub_f32 v150, v150, v180\n"
      "v_sub_f32 v201, v201, v180\n"
      "v_exp_f32 v148, v148\n"
      "v_exp_f32 v151, v151\n"
      "s_waitcnt lgkmcnt(0)\n"
      "v_sub_f32 v149, v149, v180\n"
      "v_sub_f32 v200, v200, v180\n"
      "v_exp_f32 v150, v150\n"
      "v_fmac_f32 v240, s37, v181\n"
      "v_sub_f32 v197, v197, v180\n"
      "v_sub_f32 v198, v198, v180\n"
      "v_exp_f32 v198, v198\n"
      "v_exp_f32 v149, v149\n"
      "v_exp_f32 v200, v200\n"
      "v_sub_f32 v199, v199, v180\n"
      "v_exp_f32 v197, v197\n"
      "v_cvt_pk_bf16_f32 v148, v148, v149\n"
      "v_sub_f32 v203, v203, v180\n"
      "v_alignbyte_b32 v182, v183, v182, v238\n"
      "v_sub_f32 v244, v244, v180\n"
      "v_exp_f32 v199, v199\n"
      "v_exp_f32 v244, v244\n"
      "v_alignbyte_b32 v158, v159, v158, v238\n"
      "v_exp_f32 v196, v196\n"
      "v_exp_f32 v201, v201\n"
      "v_bitop3_b32 v212, v212, v213, s4 bitop3:0x7e\n"
      "v_sub_f32_e64 v202, v202, v180\n"
      "v_alignbyte_b32 v159, v190, v159, v238\n"
      "v_max3_f32 v180, v171, v192, v193\n"
      "v_max3_f32 v181, v168, v169, v170\n"
      "v_max3_f32 v181, v181, v194, v195\n"
      "v_cvt_pk_bf16_f32 v149, v150, v151\n"
      "v_max3_f32 v180, v180, v252, v253\n"
      "v_exp_f32_e64 v203, v203\n"
      "v_cvt_pk_bf16_f32 v150, v196, v197\n"
      "v_cvt_pk_bf16_f32 v64, v244, 0\n"
      "v_alignbyte_b32 v183, v188, v183, v238\n"
      "v_cvt_pk_bf16_f32 v151, v198, v199\n"
      "v_max3_f32 v181, v181, v254, v255\n"
      "v_max3_f32 v181, v181, v180, v240\n"
      "v_bitop3_b32 v214, v214, v215, s4 bitop3:0x7e\n"
      "v_mov_b32 v180, v181\n"
      "v_or_b32 v212, v212, v214\n"
      "v_cmp_ne_u32 vcc, 0, v212\n"
      "v_permlane16_swap_b32 v181, v180\n"
      "v_alignbyte_b32 v157, v189, v157, v238\n"
      "v_max_f32 v181, v181, v180\n"
      "v_mov_b32 v180, v181\n"
      "v_alignbyte_b32 v191, v191, v191, v238\n"
      "s_mov_b64 s[48:49], vcc\n"
      "v_permlane32_swap_b32 v181, v180\n"
      "v_exp_f32 v202, v202\n"
      "v_max_f32 v181, v181, v180\n"
      "v_sub_f32 v171, v171, v181\n"
      "s_cbranch_vccnz L_flg_122_%=\n"
      "L_fb_123_%=:\n"
      "v_sub_f32 v168, v168, v181\n"
      "v_sub_f32 v169, v169, v181\n"
      "v_exp_f32 v169, v169\n"
      "v_exp_f32 v168, v168\n"
      "v_sub_f32 v192, v192, v181\n"
      "v_lshlrev_b32 v191, 16, v191\n"
      "v_sub_f32 v193, v193, v181\n"
      "v_cvt_pk_bf16_f32 v63, v202, v203\n"
      "v_exp_f32 v171, v171\n"
      "v_sub_f32 v252, v252, v181\n"
      "v_sub_f32 v194, v194, v181\n"
      "v_sub_f32 v240, v240, v181\n"
      "v_exp_f32 v193, v193\n"
      "v_cvt_pk_bf16_f32 v62, v200, v201\n"
      "v_mfma_f32_16x16x32_bf16 v[212:215], v[66:69], v[148:151], 0\n"
      "v_cvt_pk_bf16_f32 v168, v168, v169\n"
      "v_sub_f32 v254, v254, v181\n"
      "v_mfma_f32_16x16x32_bf16 v[212:215], v[66:69], v[62:65], v[212:215]\n"
      "v_sub_f32 v170, v170, v181\n"
      "v_sub_f32 v195, v195, v181\n"
      "v_exp_f32 v170, v170\n"
      "v_mfma_f32_16x16x32_bf16 v[216:219], v[152:155], v[148:151], 0\n"
      "v_exp_f32 v192, v192\n"
      "v_mfma_f32_16x16x32_bf16 v[216:219], v[50:53], v[62:65], v[216:219]\n"
      "v_cvt_pk_bf16_f32 v169, v170, v171\n"
      "v_sub_f32 v253, v253, v181\n"
      "v_mfma_f32_16x16x32_bf16 v[196:199], v[234:237], v[148:151], 0\n"
      "v_exp_f32_e64 v194, v194\n"
      "v_mfma_f32_16x16x32_bf16 v[196:199], v[54:57], v[62:65], v[196:199]\n"
      "ds_read_b128 v[172:175], v120\n"
      "v_cvt_pk_bf16_f32 v170, v192, v193\n"
      "v_mfma_f32_16x16x32_bf16 v[200:203], v[184:187], v[148:151], 0\n"
      "v_exp_f32_e64 v195, v195\n"
      "v_mfma_f32_16x16x32_bf16 v[200:203], v[58:61], v[62:65], v[200:203]\n"
      "v_cvt_pk_bf16_f32 v171, v194, v195\n"
      "v_exp_f32 v252, v252\n"
      "v_mfma_f32_16x16x32_f16 v[220:223], v[228:231], v[136:139], v[102:105]\n"
      "v_exp_f32 v253, v253\n"
      "v_mfma_f32_16x16x32_f16 v[220:223], v[34:37], v[44:47], v[220:223]\n"
      "v_sub_f32 v255, v255, v181\n"
      "v_exp_f32 v240, v240\n"
      "v_mfma_f32_16x16x32_f16 v[228:231], v[248:251], v[136:139], v[106:109]\n"
      "v_exp_f32_e64 v254, v254\n"
      "v_mfma_f32_16x16x32_f16 v[228:231], v[40:43], v[44:47], v[228:231]\n"
      "v_cvt_pk_bf16_f32 v64, v240, 0\n"
      "v_cvt_pk_bf16_f32 v62, v252, v253\n"
      "v_mfma_f32_16x16x16_bf16 v[220:223], v[164:165], v[182:183], v[220:223]\n"
      "v_exp_f32 v255, v255\n"
      "v_rcp_f32 v212, v212\n"
      "v_cvt_pk_bf16_f32 v63, v254, v255\n"
      "v_mul_f32 v219, v212, v219\n"
      "v_mfma_f32_16x16x16_bf16 v[228:231], v[164:165], v[156:157], v[228:231]\n"
      "s_waitcnt lgkmcnt(0)\n"
      "v_mul_f32 v218, v212, v218\n"
      "v_mul_f32 v217, v212, v217\n"
      "v_mul_f32 v216, v212, v216\n"
      "v_mul_f32 v196, v212, v196\n"
      "v_mul_f32 v197, v212, v197\n"
      "v_cvt_pk_bf16_f32 v216, v216, v217\n"
      "v_mul_f32 v198, v212, v198\n"
      "v_mfma_f32_16x16x32_f16 v[180:183], v[204:207], v[136:139], v[74:77]\n"
      "v_cvt_pk_bf16_f32 v217, v218, v219\n"
      "v_cvt_pk_bf16_f32 v218, v196, v197\n"
      "v_mfma_f32_16x16x32_f16 v[180:183], v[140:143], v[160:163], v[180:183]\n"
      "v_mul_f32_e64 v199, v212, v199\n"
      "v_cvt_pk_bf16_f32 v219, v198, v199\n"
      "v_mfma_f32_16x16x32_f16 v[180:183], v[224:227], v[44:47], v[180:183]\n"
      "ds_write_b128 v124, v[216:219] offset:18432\n"
      "v_mfma_f32_16x16x32_f16 v[196:199], v[140:143], v[204:207], v[76:79]\n"
      "v_mul_f32 v202, v212, v202\n"
      "v_mfma_f32_16x16x32_f16 v[252:255], v[160:163], v[136:139], v[110:113]\n"
      "v_mul_f32 v200, v212, v200\n"
      "v_mfma_f32_16x16x32_f16 v[252:255], v[46:49], v[44:47], v[252:255]\n"
      "global_store_dwordx4 v117, v[172:175], s[54:55] sc1\n"
      "s_nop 0\n"
      "v_max3_f32 v182, v220, v221, v222\n"
      "v_mfma_f32_16x16x32_bf16 v[172:175], v[66:69], v[168:171], 0\n"
      "v_mul_f32 v201, v212, v201\n"
      "v_mfma_f32_16x16x32_bf16 v[172:175], v[66:69], v[62:65], v[172:175]\n"
      "v_mul_f32 v203, v212, v203\n"
      "v_cvt_pk_bf16_f32 v200, v200, v201\n"
      "v_mfma_f32_16x16x16_bf16 v[252:255], v[164:165], v[158:159], v[252:255]\n"
      "v_cvt_pk_bf16_f32 v201, v202, v203\n"
      "v_fmac_f32 v180, s37, v191\n"
      "v_max3_f32 v182, v182, v230, v231\n"
      "v_mfma_f32_16x16x16_f16 v[196:199], v[226:227], v[224:225], v[196:199]\n"
      "ds_read_b128 v[224:227], v121\n"
      "v_max3_f32 v183, v223, v228, v229\n"
      "v_mfma_f32_16x16x32_bf16 v[156:159], v[184:187], v[168:171], 0\n"
      "s_nop 0\n"
      "v_max3_f32 v182, v182, v254, v255\n"
      "v_max3_f32 v183, v183, v252, v253\n"
      "v_max3_f32 v182, v182, v183, v180\n"
      "v_mov_b32 v183, v182\n"
      "v_rcp_f32 v172, v172\n"
      "v_mfma_f32_16x16x32_bf16 v[156:159], v[58:61], v[62:65], v[156:159]\n"
      "v_permlane16_swap_b32 v182, v183\n"
      "v_max_f32 v182, v182, v183\n"
      "v_mov_b32 v183, v182\n"
      "v_mfma_f32_16x16x32_bf16 v[136:139], v[234:237], v[168:171], 0\n"
      "s_nop 0\n"
      "v_permlane32_swap_b32 v182, v183\n"
      "v_max_f32 v182, v182, v183\n"
      "v_sub_f32 v221, v221, v182\n"
      "v_sub_f32 v252, v252, v182\n"
      "v_sub_f32 v231, v231, v182\n"
      "v_sub_f32 v229, v229, v182\n"
      "v_sub_f32 v230, v230, v182\n"
      "v_sub_f32 v222, v222, v182\n"
      "v_mfma_f32_16x16x32_bf16 v[216:219], v[152:155], v[168:171], 0\n"
      "v_exp_f32 v222, v222\n"
      "v_mfma_f32_16x16x32_bf16 v[216:219], v[50:53], v[62:65], v[216:219]\n"
      "v_sub_f32 v254, v254, v182\n"
      "v_exp_f32 v221, v221\n"
      "v_sub_f32 v223, v223, v182\n"
      "v_mfma_f32_16x16x32_bf16 v[136:139], v[54:57], v[62:65], v[136:139]\n"
      "v_sub_f32 v253, v253, v182\n"
      "v_sub_f32 v228, v228, v182\n"
      "v_sub_f32 v220, v220, v182\n"
      "v_exp_f32 v223, v223\n"
      "v_exp_f32 v220, v220\n"
      "v_exp_f32 v228, v228\n"
      "v_cvt_pk_bf16_f32 v220, v220, v221\n"
      "v_cvt_pk_bf16_f32 v221, v222, v223\n"
      "v_sub_f32 v180, v180, v182\n"
      "v_sub_f32 v255, v255, v182\n"
      "v_mul_f32 v219, v172, v219\n"
      "v_exp_f32 v231, v231\n"
      "v_mul_f32 v136, v172, v136\n"
      "v_exp_f32 v230, v230\n"
      "v_mul_f32 v218, v172, v218\n"
      "v_mul_f32 v216, v172, v216\n"
      "v_exp_f32 v180, v180\n"
      "v_mul_f32 v217, v172, v217\n"
      "v_exp_f32 v252, v252\n"
      "v_exp_f32 v253, v253\n"
      "v_cvt_pk_bf16_f32 v64, v180, 0\n"
      "v_exp_f32 v229, v229\n"
      "v_exp_f32 v255, v255\n"
      "v_mul_f32 v137, v172, v137\n"
      "v_cvt_pk_bf16_f32 v222, v228, v229\n"
      "v_cvt_pk_bf16_f32 v62, v252, v253\n"
      "v_cvt_pk_bf16_f32 v223, v230, v231\n"
      "v_exp_f32_e64 v254, v254\n"
      "v_mul_f32 v159, v172, v159\n"
      "v_cvt_pk_bf16_f32 v63, v254, v255\n"
      "v_mfma_f32_16x16x32_bf16 v[212:215], v[66:69], v[220:223], 0\n"
      "s_waitcnt lgkmcnt(0)\n"
      "ds_read_b128 v[228:231], v123 offset:4096\n"
      "v_cvt_pk_bf16_f32 v216, v216, v217\n"
      "v_mfma_f32_16x16x32_bf16 v[212:215], v[66:69], v[62:65], v[212:215]\n"
      "v_mul_f32_e64 v138, v172, v138\n"
      "v_cvt_pk_bf16_f32 v217, v218, v219\n"
      "v_mfma_f32_16x16x32_bf16 v[252:255], v[152:155], v[220:223], 0\n"
      "v_cndmask_b32 v245, v245, v241, s[42:43]\n"
      "v_mul_f32_e64 v139, v172, v139\n"
      "v_mfma_f32_16x16x32_bf16 v[252:255], v[50:53], v[62:65], v[252:255]\n"
      "s_mov_b32 exec_hi, 0\n"
      "global_store_dwordx4 v118, v[224:227], s[54:55] sc1\n"
      "s_mov_b32 exec_hi, -1\n"
      "v_cvt_pk_bf16_f32 v218, v136, v137\n"
      "v_mfma_f32_16x16x32_bf16 v[204:207], v[234:237], v[220:223], 0\n"
      "ds_read_b128 v[140:143], v123 offset:1024\n"
      "v_cvt_pk_bf16_f32 v219, v138, v139\n"
      "v_mfma_f32_16x16x32_bf16 v[204:207], v[54:57], v[62:65], v[204:207]\n"
      "ds_write_b128 v124, v[216:219] offset:19456\n"
      "v_mfma_f32_16x16x32_bf16 v[216:219], v[184:187], v[220:223], 0\n"
      "ds_read_b128 v[136:139], v123 offset:7168\n"
      "v_mul_f32 v156, v172, v156\n"
      "v_mfma_f32_16x16x32_bf16 v[216:219], v[58:61], v[62:65], v[216:219]\n"
      "s_add_u32 s28, s27, 2\n"
      "v_mbcnt_lo_u32_b32 v232, -1, 0\n"
      "v_cndmask_b32 v245, v245, v181, s[44:45]\n"
      "v_mul_f32 v157, v172, v157\n"
      "v_cndmask_b32 v245, v245, v197, s[46:47]\n"
      "v_mul_f32 v158, v172, v158\n"
      "v_mbcnt_hi_u32_b32 v232, -1, v232\n"
      "v_cvt_pk_bf16_f32 v156, v156, v157\n"
      "v_cvt_pk_bf16_f32 v157, v158, v159\n"
      "v_min_u32 v232, 48, v232\n"
      "v_lshlrev_b32 v232, 1, v232\n"
      "s_min_u32 m0, s28, s3\n"
      "ds_write2st64_b64 v125, v[200:201], v[156:157] offset0:36 offset1:38\n"
      "s_waitcnt lgkmcnt(3)\n"
      "v_mfma_f32_16x16x32_bf16 v[196:199], a[216:219], v[140:143], 0\n"
      "ds_read_b128 v[200:203], v123 offset:10240\n"
      "s_movrels_b32 s9, s80\n"
      "v_mfma_f32_16x16x32_bf16 v[196:199], a[228:231], v[228:231], v[196:199]\n"
      "s_lshr_b32 s8, s9, 8\n"
      "s_and_b32 s9, s9, 0xff\n"
      "s_mul_i32 s10, s8, 73\n"
      "s_add_u32 s10, s10, s9\n"
      "v_mfma_f32_16x16x32_bf16 v[172:175], a[220:223], v[140:143], 0\n"
      "s_mul_i32 s28, s10, 4802\n"
      "v_add_u32 v232, s30, v232\n"
      "s_add_u32 s56, s18, s28\n"
      "v_mfma_f32_16x16x32_bf16 v[172:175], a[232:235], v[228:231], v[172:175]\n"
      "s_addc_u32 s57, s19, 0\n"
      "ds_read_b128 v[220:223], v123 offset:16384\n"
      "v_mfma_f32_16x16x32_bf16 v[180:183], a[224:227], v[140:143], 0\n"
      "ds_read_u16 v224, v232 offset:4704\n"
      "s_and_b32 s56, s56, -16\n"
      "v_mfma_f32_16x16x32_bf16 v[180:183], a[236:239], v[228:231], v[180:183]\n"
      "ds_read_b128 v[228:231], v123 offset:13312\n"
      "s_sub_u32 s58, s56, 110592\n"
      "s_subb_u32 s59, s57, 0\n"
      "s_sub_u32 s56, s56, 73728\n"
      "s_subb_u32 s57, s57, 0\n"
      "s_mul_i32 s28, s8, 1373568\n"
      "s_mul_i32 s29, s9, 2688\n"
      "s_add_u32 s28, s28, s29\n"
      "s_cmp_eq_u32 s8, 72\n"
      "s_cselect_b32 s8, 0, 100270464\n"
      "s_waitcnt lgkmcnt(3)\n"
      "v_mfma_f32_16x16x32_bf16 v[196:199], a[240:243], v[136:139], v[196:199]\n"
      "s_cmp_eq_u32 s9, 72\n"
      "s_cselect_b32 s9, 0, 196224\n"
      "v_mfma_f32_16x16x32_bf16 v[196:199], a[252:255], v[200:203], v[196:199]\n"
      "s_add_u32 s10, s8, s9\n"
      "s_add_u32 s28, s28, s10\n"
      "s_add_u32 s52, s16, s28\n"
      "v_mfma_f32_16x16x32_bf16 v[172:175], a[244:247], v[136:139], v[172:175]\n"
      "s_addc_u32 s53, s17, 0\n"
      "s_add_u32 s27, s27, 1\n"
      "s_cmp_ge_u32 s27, s26\n"
      "v_mfma_f32_16x16x32_bf16 v[172:175], v[0:3], v[200:203], v[172:175]\n"
      "s_cselect_b32 s28, 1, 0\n"
      "ds_read_b128 v[156:159], v123 offset:2048\n"
      "v_mfma_f32_16x16x32_bf16 v[180:183], a[248:251], v[136:139], v[180:183]\n"
      "v_mfma_f32_16x16x32_bf16 v[180:183], v[4:7], v[200:203], v[180:183]\n"
      "s_waitcnt lgkmcnt(1)\n"
      "v_lshlrev_b32 v224, 16, v224\n"
      "v_fmac_f32 v245, s37, v224\n"
      "v_mfma_f32_16x16x32_bf16 v[196:199], v[8:11], v[228:231], v[196:199]\n"
      "ds_read_b128 v[224:227], v123 offset:5120\n"
      "v_max_f32_dpp v200, v245, v245 row_ror:8 row_mask:0xf bank_mask:0xf\n"
      "v_mfma_f32_16x16x32_bf16 v[196:199], v[20:23], v[220:223], v[196:199]\n"
      "ds_read_b128 v[188:191], v123 offset:11264\n"
      "v_max_f32_dpp v200, v200, v200 row_ror:4 row_mask:0xf bank_mask:0xf\n"
      "v_mfma_f32_16x16x32_bf16 v[172:175], v[12:15], v[228:231], v[172:175]\n"
      "s_nop 0\n"
      "v_max_f32_dpp v200, v200, v200 row_ror:2 row_mask:0xf bank_mask:0xf\n"
      "s_xor_b32 s29, s10, s11\n"
      "s_nop 0\n"
      "v_max_f32_dpp v200, v200, v200 row_ror:1 row_mask:0xf bank_mask:0xf\n"
      "v_mfma_f32_16x16x32_bf16 v[180:183], v[16:19], v[228:231], v[180:183]\n"
      "s_nop 0\n"
      "v_max_f32_dpp v200, v200, v200 row_bcast:15 row_mask:0xa bank_mask:0xf\n"
      "v_mfma_f32_16x16x32_bf16 v[180:183], v[28:31], v[220:223], v[180:183]\n"
      "s_nop 0\n"
      "v_max_f32_dpp v200, v200, v200 row_bcast:31 row_mask:0xc bank_mask:0xf\n"
      "s_or_b32 s28, s28, s29\n"
      "v_readlane_b32 s31, v200, 63\n"
      "v_mfma_f32_16x16x32_bf16 v[172:175], v[24:27], v[220:223], v[172:175]\n"
      "s_nop 0\n"
      "v_subrev_f32 v245, s31, v245\n"
      "v_exp_f32 v245, v245\n"
      "s_nop 0\n"
      "v_cvt_pk_bf16_f32 v245, v245, v245\n"
      "ds_write_b16 v130, v245\n"
      "s_waitcnt lgkmcnt(2)\n"
      "v_mfma_f32_16x16x32_bf16 v[200:203], a[216:219], v[156:159], 0\n"
      "ds_read_b128 v[220:223], v123 offset:8192\n"
      "ds_read_b128 v[168:171], v131 offset:64\n"
      "v_mfma_f32_16x16x32_bf16 v[228:231], a[224:227], v[156:159], 0\n"
      "ds_read_b128 v[140:143], v131\n"
      "v_fma_f32 v245, v196, v196, s39\n"
      "v_mfma_f32_16x16x32_bf16 v[228:231], a[236:239], v[224:227], v[228:231]\n"
      "v_fmac_f32 v245, v197, v197\n"
      "v_fmac_f32 v245, v198, v198\n"
      "v_fmac_f32_e64 v245, v199, v199\n"
      "v_fmac_f32_e64 v245, v172, v172\n"
      "v_mfma_f32_16x16x32_bf16 v[200:203], a[228:231], v[224:227], v[200:203]\n"
      "v_fmac_f32_e64 v245, v173, v173\n"
      "v_fmac_f32_e64 v245, v174, v174\n"
      "v_fmac_f32_e64 v245, v175, v175\n"
      "v_mfma_f32_16x16x32_bf16 v[136:139], a[220:223], v[156:159], 0\n"
      "ds_read_b128 v[156:159], v123 offset:14336\n"
      "v_fmac_f32_e64 v245, v180, v180\n"
      "v_fmac_f32 v245, v181, v181\n"
      "v_mfma_f32_16x16x32_bf16 v[136:139], a[232:235], v[224:227], v[136:139]\n"
      "ds_read_b128 v[224:227], v123 offset:17408\n"
      "s_waitcnt lgkmcnt(4)\n"
      "v_mfma_f32_16x16x32_bf16 v[136:139], a[244:247], v[220:223], v[136:139]\n"
      "v_mfma_f32_16x16x32_bf16 v[136:139], v[0:3], v[188:191], v[136:139]\n"
      "v_fmac_f32 v245, v182, v182\n"
      "v_fmac_f32_e64 v245, v183, v183\n"
      "v_mfma_f32_16x16x32_bf16 v[200:203], a[240:243], v[220:223], v[200:203]\n"
      "s_waitcnt lgkmcnt(2)\n"
      "v_mfma_f32_16x16x32_bf16 v[200:203], a[252:255], v[188:191], v[200:203]\n"
      "v_permlane16_swap_b32 v233, v245\n"
      "v_mfma_f32_16x16x32_bf16 v[228:231], a[248:251], v[220:223], v[228:231]\n"
      "v_mfma_f32_16x16x32_bf16 v[228:231], v[4:7], v[188:191], v[228:231]\n"
      "s_waitcnt lgkmcnt(0)\n"
      "v_mfma_f32_16x16x32_bf16 v[200:203], v[8:11], v[156:159], v[200:203]\n"
      "v_mfma_f32_16x16x32_bf16 v[200:203], v[20:23], v[224:227], v[200:203]\n"
      "v_mfma_f32_16x16x32_bf16 v[136:139], v[12:15], v[156:159], v[136:139]\n"
      "ds_read_b128 v[220:223], v114\n"
      "v_add_f32_e64 v233, v233, v245\n"
      "v_mfma_f32_16x16x32_bf16 v[136:139], v[24:27], v[224:227], v[136:139]\n"
      "v_mfma_f32_16x16x32_bf16 v[228:231], v[16:19], v[156:159], v[228:231]\n"
      "ds_read_b128 v[156:159], v115\n"
      "v_mfma_f32_16x16x32_bf16 v[228:231], v[28:31], v[224:227], v[228:231]\n"
      "v_mfma_f32_16x16x32_bf16 v[148:151], v[140:143], v[66:69], 0\n"
      "v_fma_f32 v232, v200, v200, s39\n"
      "v_mfma_f32_16x16x32_bf16 v[148:151], v[168:171], v[66:69], v[148:151]\n"
      "v_fmac_f32 v232, v201, v201\n"
      "v_fmac_f32 v232, v202, v202\n"
      "v_mfma_f32_16x16x32_bf16 v[244:247], v[140:143], v[152:155], 0\n"
      "v_fmac_f32 v232, v203, v203\n"
      "v_fmac_f32 v232, v136, v136\n"
      "v_mfma_f32_16x16x32_bf16 v[244:247], v[168:171], v[50:53], v[244:247]\n"
      "v_fmac_f32 v232, v137, v137\n"
      "v_mfma_f32_16x16x32_bf16 v[162:165], v[140:143], v[234:237], 0\n"
      "ds_read_b128 v[224:227], v114 offset:6144\n"
      "v_fmac_f32 v232, v138, v138\n"
      "v_mfma_f32_16x16x32_bf16 v[162:165], v[168:171], v[54:57], v[162:165]\n"
      "v_fmac_f32 v232, v139, v139\n"
      "v_fmac_f32 v232, v228, v228\n"
      "v_mfma_f32_16x16x32_bf16 v[234:237], v[140:143], v[184:187], 0\n"
      "ds_read_b128 v[188:191], v115 offset:6144\n"
      "v_fmac_f32_e64 v232, v229, v229\n"
      "v_mfma_f32_16x16x32_bf16 v[234:237], v[168:171], v[58:61], v[234:237]\n"
      "v_fmac_f32 v232, v230, v230\n"
      "v_fmac_f32 v232, v231, v231\n"
      "v_mov_b32 v149, v232\n"
      "v_cndmask_b32 v244, v244, v162, s[42:43]\n"
      "s_waitcnt lgkmcnt(2)\n"
      "v_mov_b32_dpp v148, v148 quad_perm:[0,0,0,0] row_mask:0xf bank_mask:0xf\n"
      "v_permlane16_swap_b32 v232, v149\n"
      "v_add_f32 v232, v232, v149\n"
      "v_mfma_f32_16x16x32_bf16 v[140:143], a[0:3], v[220:223], 0\n"
      "s_nop 0\n"
      "v_permlane32_swap_b32 v233, v232\n"
      "v_mfma_f32_16x16x32_bf16 v[140:143], a[36:39], v[156:159], v[140:143]\n"
      "v_rcp_f32 v148, v148\n"
      "v_mfma_f32_16x16x32_bf16 v[162:165], a[4:7], v[220:223], 0\n"
      "v_cndmask_b32 v244, v244, v234, s[44:45]\n"
      "v_mul_f32 v244, v148, v244\n"
      "v_mfma_f32_16x16x32_bf16 v[162:165], a[40:43], v[156:159], v[162:165]\n"
      "v_add_f32 v233, v233, v232\n"
      "v_mfma_f32_16x16x32_bf16 v[148:151], a[8:11], v[220:223], 0\n"
      "v_mfma_f32_16x16x32_bf16 v[148:151], a[44:47], v[156:159], v[148:151]\n"
      "s_waitcnt lgkmcnt(0)\n"
      "v_mfma_f32_16x16x32_bf16 v[234:237], a[12:15], v[220:223], 0\n"
      "v_cvt_pk_bf16_f32 v244, v244, v244\n"
      "v_mfma_f32_16x16x32_bf16 v[234:237], a[48:51], v[156:159], v[234:237]\n"
      "ds_write_b16 v135, v244 offset:16\n"
      "v_mfma_f32_16x16x32_bf16 v[168:171], a[16:19], v[220:223], 0\n"
      "v_mfma_f32_16x16x32_bf16 v[168:171], a[52:55], v[156:159], v[168:171]\n"
      "ds_write_b32 v129, v233 offset:1024\n"
      "v_mfma_f32_16x16x32_bf16 v[244:247], a[20:23], v[220:223], 0\n"
      "v_mfma_f32_16x16x32_bf16 v[244:247], a[56:59], v[156:159], v[244:247]\n"
      "s_branch L_join_21_%=\n"
      "L_flg_122_%=:\n"
      "v_cmp_gt_i32 vcc, 0, v212\n"
      "s_cbranch_vccz L_fb_123_%=\n"
      "s_sleep 1\n"
      "ds_read_b128 v[212:215], v128 offset:2048\n"
      "s_waitcnt lgkmcnt(0)\n"
      "v_bitop3_b32 v212, v212, v213, s4 bitop3:0x7e\n"
      "v_bitop3_b32 v214, v214, v215, s4 bitop3:0x7e\n"
      "v_or_b32 v212, v212, v214\n"
      "v_cmp_ne_u32 s[48:49], 0, v212\n"
      "s_branch L_flg_122_%=\n"
      "L_gen_28_%=:\n"
      "s_waitcnt lgkmcnt(0)\n"
      "s_nop 5\n"
      "s_cmp_eq_u64 s[34:35], 0\n"
      "s_cbranch_scc1 L_ok_146_%=\n"
      "ds_read_b128 v[144:147], v114\n"
      "ds_read_b128 v[244:247], v115\n"
      "ds_read_b128 v[176:179], v114 offset:12288\n"
      "ds_read_b128 v[140:143], v114 offset:6144\n"
      "ds_read_b128 v[172:175], v115 offset:6144\n"
      "s_waitcnt lgkmcnt(3)\n"
      "v_mfma_f32_16x16x32_bf16 v[228:231], a[0:3], v[144:147], 0\n"
      "v_mfma_f32_16x16x32_bf16 v[228:231], a[36:39], v[244:247], v[228:231]\n"
      "v_mfma_f32_16x16x32_bf16 v[220:223], a[4:7], v[144:147], 0\n"
      "v_mfma_f32_16x16x32_bf16 v[220:223], a[40:43], v[244:247], v[220:223]\n"
      "v_mfma_f32_16x16x32_bf16 v[204:207], a[8:11], v[144:147], 0\n"
      "s_waitcnt lgkmcnt(0)\n"
      "v_mfma_f32_16x16x32_bf16 v[234:237], a[12:15], v[144:147], 0\n"
      "v_mfma_f32_16x16x32_bf16 v[234:237], a[48:51], v[244:247], v[234:237]\n"
      "s_nop 0\n"
      "v_mfma_f32_16x16x32_bf16 v[252:255], a[16:19], v[144:147], 0\n"
      "v_mfma_f32_16x16x32_bf16 v[252:255], a[52:55], v[244:247], v[252:255]\n"
      "v_mfma_f32_16x16x32_bf16 v[252:255], a[88:91], v[140:143], v[252:255]\n"
      "v_mfma_f32_16x16x32_bf16 v[234:237], a[84:87], v[140:143], v[234:237]\n"
      "v_mfma_f32_16x16x32_bf16 v[216:219], a[20:23], v[144:147], 0\n"
      "ds_read_b128 v[144:147], v115 offset:12288\n"
      "v_mfma_f32_16x16x32_bf16 v[216:219], a[56:59], v[244:247], v[216:219]\n"
      "v_mfma_f32_16x16x32_bf16 v[228:231], a[72:75], v[140:143], v[228:231]\n"
      "v_mfma_f32_16x16x32_bf16 v[228:231], a[108:111], v[172:175], v[228:231]\n"
      "v_mfma_f32_16x16x32_bf16 v[204:207], a[44:47], v[244:247], v[204:207]\n"
      "v_mfma_f32_16x16x32_bf16 v[204:207], a[80:83], v[140:143], v[204:207]\n"
      "v_mfma_f32_16x16x32_bf16 v[204:207], a[116:119], v[172:175], v[204:207]\n"
      "v_mfma_f32_16x16x32_bf16 v[220:223], a[76:79], v[140:143], v[220:223]\n"
      "s_nop 0\n"
      "v_mfma_f32_16x16x32_bf16 v[220:223], a[112:115], v[172:175], v[220:223]\n"
      "s_waitcnt lgkmcnt(0)\n"
      "v_mfma_f32_16x16x32_bf16 v[228:231], a[144:147], v[176:179], v[228:231]\n"
      "ds_read_b128 v[244:247], v114 offset:2048\n"
      "v_mfma_f32_16x16x32_bf16 v[228:231], a[180:183], v[144:147], v[228:231]\n"
      "v_mfma_f32_16x16x32_bf16 v[216:219], a[92:95], v[140:143], v[216:219]\n"
      "v_mfma_f32_16x16x32_bf16 v[216:219], a[128:131], v[172:175], v[216:219]\n"
      "v_mfma_f32_16x16x32_bf16 v[204:207], a[152:155], v[176:179], v[204:207]\n"
      "v_mfma_f32_16x16x32_bf16 v[204:207], a[188:191], v[144:147], v[204:207]\n"
      "v_mfma_f32_16x16x32_bf16 v[220:223], a[148:151], v[176:179], v[220:223]\n"
      "v_mfma_f32_16x16x32_bf16 v[220:223], a[184:187], v[144:147], v[220:223]\n"
      "s_nop 1\n"
      "v_fma_f32 v232, v228, v228, s38\n"
      "v_mfma_f32_16x16x32_bf16 v[252:255], a[124:127], v[172:175], v[252:255]\n"
      "v_mfma_f32_16x16x32_bf16 v[234:237], a[120:123], v[172:175], v[234:237]\n"
      "s_nop 0\n"
      "v_mfma_f32_16x16x32_bf16 v[234:237], a[156:159], v[176:179], v[234:237]\n"
      "v_fmac_f32 v232, v229, v229\n"
      "v_mfma_f32_16x16x32_bf16 v[234:237], a[192:195], v[144:147], v[234:237]\n"
      "v_fmac_f32 v232, v230, v230\n"
      "v_mfma_f32_16x16x32_bf16 v[252:255], a[160:163], v[176:179], v[252:255]\n"
      "v_fmac_f32 v232, v231, v231\n"
      "v_fmac_f32 v232, v220, v220\n"
      "v_mfma_f32_16x16x32_bf16 v[252:255], a[196:199], v[144:147], v[252:255]\n"
      "v_fmac_f32 v232, v221, v221\n"
      "v_fmac_f32 v232, v222, v222\n"
      "v_mfma_f32_16x16x32_bf16 v[216:219], a[164:167], v[176:179], v[216:219]\n"
      "v_fma_f32 v233, v234, v234, s38\n"
      "v_fmac_f32 v232, v223, v223\n"
      "v_mfma_f32_16x16x32_bf16 v[216:219], a[200:203], v[144:147], v[216:219]\n"
      "ds_read_b128 v[144:147], v115 offset:2048\n"
      "v_fmac_f32 v233, v235, v235\n"
      "v_fmac_f32 v233, v236, v236\n"
      "v_fmac_f32 v233, v237, v237\n"
      "v_fmac_f32 v233, v252, v252\n"
      "v_fmac_f32 v233, v253, v253\n"
      "v_fmac_f32 v233, v254, v254\n"
      "v_fmac_f32 v232, v204, v204\n"
      "v_fmac_f32 v233, v255, v255\n"
      "v_fmac_f32 v233, v216, v216\n"
      "v_fmac_f32 v233, v217, v217\n"
      "v_fmac_f32 v233, v218, v218\n"
      "v_fmac_f32 v232, v205, v205\n"
      "v_fmac_f32 v232, v206, v206\n"
      "v_fmac_f32 v233, v219, v219\n"
      "v_fmac_f32 v232, v207, v207\n"
      "s_nop 1\n"
      "v_permlane16_swap_b32 v232, v233\n"
      "v_add_f32 v232, v232, v233\n"
      "v_mov_b32 v233, v232\n"
      "s_nop 1\n"
      "v_permlane32_swap_b32 v232, v233\n"
      "v_add_f32 v232, v232, v233\n"
      "v_rsq_f32 v232, v232\n"
      "s_nop 0\n"
      "v_mov_b32 v233, v232\n"
      "s_waitcnt lgkmcnt(0)\n"
      "v_mfma_f32_16x16x32_bf16 v[248:251], a[0:3], v[244:247], 0\n"
      "ds_read_b128 v[140:143], v114 offset:8192\n"
      "v_permlane16_swap_b32 v232, v233\n"
      "v_mfma_f32_16x16x32_bf16 v[248:251], a[36:39], v[144:147], v[248:251]\n"
      "ds_read_b128 v[176:179], v115 offset:8192\n"
      "v_mul_f32 v236, v233, v236\n"
      "v_mul_f32 v235, v233, v235\n"
      "v_mul_f32 v237, v233, v237\n"
      "v_mul_f32 v234, v233, v234\n"
      "v_mfma_f32_16x16x32_bf16 v[208:211], a[12:15], v[244:247], 0\n"
      "ds_read_b128 v[172:175], v114 offset:14336\n"
      "v_cvt_pk_f16_f32 v234, v234, v235\n"
      "v_mfma_f32_16x16x32_bf16 v[208:211], a[48:51], v[144:147], v[208:211]\n"
      "v_mul_f32 v253, v233, v253\n"
      "v_cvt_pk_f16_f32 v235, v236, v237\n"
      "v_mul_f32 v254, v233, v254\n"
      "v_mul_f32 v255, v233, v255\n"
      "v_cvt_pk_f16_f32 v237, v254, v255\n"
      "v_mul_f32 v252, v233, v252\n"
      "v_cvt_pk_f16_f32 v236, v252, v253\n"
      "v_mfma_f32_16x16x32_bf16 v[252:255], a[16:19], v[244:247], 0\n"
      "v_mul_f32 v217, v233, v217\n"
      "v_mul_f32 v216, v233, v216\n"
      "v_mfma_f32_16x16x32_bf16 v[252:255], a[52:55], v[144:147], v[252:255]\n"
      "v_cvt_pk_f16_f32 v34, v216, v217\n"
      "v_mul_f32 v219, v233, v219\n"
      "v_mfma_f32_16x16x32_bf16 v[214:217], a[20:23], v[244:247], 0\n"
      "ds_read_b128 v[164:167], v115 offset:14336\n"
      "v_mul_f32 v218, v233, v218\n"
      "v_mfma_f32_16x16x32_bf16 v[214:217], a[56:59], v[144:147], v[214:217]\n"
      "v_cvt_pk_f16_f32 v35, v218, v219\n"
      "s_waitcnt lgkmcnt(2)\n"
      "v_mfma_f32_16x16x32_bf16 v[248:251], a[72:75], v[140:143], v[248:251]\n"
      "v_mul_f32 v232, s36, v232\n"
      "v_mul_f32_e64 v228, v232, v228\n"
      "v_mfma_f32_16x16x32_bf16 v[248:251], a[108:111], v[176:179], v[248:251]\n"
      "v_mul_f32 v229, v232, v229\n"
      "v_cvt_pk_f16_f32 v228, v228, v229\n"
      "v_mfma_f32_16x16x32_bf16 v[208:211], a[84:87], v[140:143], v[208:211]\n"
      "v_mul_f32 v230, v232, v230\n"
      "v_mul_f32 v231, v232, v231\n"
      "v_mfma_f32_16x16x32_bf16 v[208:211], a[120:123], v[176:179], v[208:211]\n"
      "v_cvt_pk_f16_f32 v229, v230, v231\n"
      "v_mul_f32 v220, v232, v220\n"
      "v_mfma_f32_16x16x32_bf16 v[252:255], a[88:91], v[140:143], v[252:255]\n"
      "v_mul_f32 v221, v232, v221\n"
      "v_cvt_pk_f16_f32 v230, v220, v221\n"
      "v_mfma_f32_16x16x32_bf16 v[252:255], a[124:127], v[176:179], v[252:255]\n"
      "v_mul_f32 v223, v232, v223\n"
      "v_mul_f32_e64 v222, v232, v222\n"
      "v_mfma_f32_16x16x32_bf16 v[214:217], a[92:95], v[140:143], v[214:217]\n"
      "s_waitcnt lgkmcnt(0)\n"
      "v_mul_f32 v204, v232, v204\n"
      "v_mfma_f32_16x16x32_bf16 v[214:217], a[128:131], v[176:179], v[214:217]\n"
      "v_cvt_pk_f16_f32 v231, v222, v223\n"
      "v_mul_f32 v205, v232, v205\n"
      "v_mfma_f32_16x16x32_bf16 v[220:223], a[4:7], v[244:247], 0\n"
      "v_mul_f32 v206, v232, v206\n"
      "v_mul_f32_e64 v207, v232, v207\n"
      "v_mfma_f32_16x16x32_bf16 v[220:223], a[40:43], v[144:147], v[220:223]\n"
      "v_cvt_pk_f16_f32 v32, v204, v205\n"
      "v_cvt_pk_f16_f32 v33, v206, v207\n"
      "v_mfma_f32_16x16x32_bf16 v[220:223], a[76:79], v[140:143], v[220:223]\n"
      "v_mfma_f32_16x16x32_bf16 v[220:223], a[112:115], v[176:179], v[220:223]\n"
      "v_mfma_f32_16x16x32_bf16 v[204:207], a[8:11], v[244:247], 0\n"
      "v_mfma_f32_16x16x32_bf16 v[204:207], a[44:47], v[144:147], v[204:207]\n"
      "v_mfma_f32_16x16x32_bf16 v[204:207], a[80:83], v[140:143], v[204:207]\n"
      "ds_read_b128 v[144:147], v115 offset:4096\n"
      "v_mfma_f32_16x16x32_bf16 v[204:207], a[116:119], v[176:179], v[204:207]\n"
      "v_mfma_f32_16x16x32_bf16 v[248:251], a[144:147], v[172:175], v[248:251]\n"
      "v_mfma_f32_16x16x32_bf16 v[248:251], a[180:183], v[164:167], v[248:251]\n"
      "v_mfma_f32_16x16x32_bf16 v[220:223], a[148:151], v[172:175], v[220:223]\n"
      "v_mfma_f32_16x16x32_bf16 v[220:223], a[184:187], v[164:167], v[220:223]\n"
      "v_mfma_f32_16x16x32_bf16 v[204:207], a[152:155], v[172:175], v[204:207]\n"
      "v_mfma_f32_16x16x32_bf16 v[204:207], a[188:191], v[164:167], v[204:207]\n"
      "v_mfma_f32_16x16x32_bf16 v[208:211], a[156:159], v[172:175], v[208:211]\n"
      "v_mfma_f32_16x16x32_bf16 v[208:211], a[192:195], v[164:167], v[208:211]\n"
      "s_nop 1\n"
      "v_fma_f32 v232, v248, v248, s38\n"
      "v_mfma_f32_16x16x32_bf16 v[252:255], a[160:163], v[172:175], v[252:255]\n"
      "v_fmac_f32 v232, v249, v249\n"
      "v_fmac_f32 v232, v250, v250\n"
      "v_mfma_f32_16x16x32_bf16 v[252:255], a[196:199], v[164:167], v[252:255]\n"
      "v_fmac_f32 v232, v251, v251\n"
      "v_fma_f32 v233, v208, v208, s38\n"
      "v_fmac_f32 v232, v220, v220\n"
      "v_fmac_f32 v233, v209, v209\n"
      "v_mfma_f32_16x16x32_bf16 v[214:217], a[164:167], v[172:175], v[214:217]\n"
      "ds_read_b128 v[244:247], v114 offset:4096\n"
      "v_fmac_f32 v233, v210, v210\n"
      "v_mfma_f32_16x16x32_bf16 v[214:217], a[200:203], v[164:167], v[214:217]\n"
      "v_fmac_f32 v233, v211, v211\n"
      "v_fmac_f32 v233, v252, v252\n"
      "v_fmac_f32 v233, v253, v253\n"
      "v_fmac_f32 v233, v254, v254\n"
      "v_fmac_f32 v232, v221, v221\n"
      "v_fmac_f32 v232, v222, v222\n"
      "v_fmac_f32 v232, v223, v223\n"
      "v_fmac_f32 v232, v204, v204\n"
      "v_fmac_f32 v232, v205, v205\n"
      "v_fmac_f32 v233, v255, v255\n"
      "v_fmac_f32 v233, v214, v214\n"
      "v_fmac_f32 v232, v206, v206\n"
      "v_fmac_f32 v233, v215, v215\n"
      "v_fmac_f32 v233, v216, v216\n"
      "v_fmac_f32 v232, v207, v207\n"
      "v_fmac_f32 v233, v217, v217\n"
      "s_nop 1\n"
      "v_permlane16_swap_b32 v232, v233\n"
      "v_add_f32 v232, v232, v233\n"
      "v_mov_b32 v233, v232\n"
      "s_nop 1\n"
      "v_permlane32_swap_b32 v232, v233\n"
      "v_add_f32 v232, v232, v233\n"
      "v_rsq_f32 v232, v232\n"
      "s_waitcnt lgkmcnt(0)\n"
      "v_mfma_f32_16x16x32_bf16 v[160:163], a[0:3], v[244:247], 0\n"
      "ds_read_b128 v[140:143], v114 offset:10240\n"
      "v_mov_b32_e64 v233, v232\n"
      "v_mfma_f32_16x16x32_bf16 v[160:163], a[36:39], v[144:147], v[160:163]\n"
      "ds_read_b128 v[172:175], v115 offset:10240\n"
      "v_permlane16_swap_b32 v232, v233\n"
      "v_mfma_f32_16x16x32_bf16 v[136:139], a[12:15], v[244:247], 0\n"
      "ds_read_b128 v[176:179], v114 offset:16384\n"
      "v_mul_f32 v208, v233, v208\n"
      "v_mfma_f32_16x16x32_bf16 v[136:139], a[48:51], v[144:147], v[136:139]\n"
      "v_mul_f32 v211, v233, v211\n"
      "v_mul_f32 v209, v233, v209\n"
      "v_mul_f32 v253, v233, v253\n"
      "v_mul_f32 v210, v233, v210\n"
      "v_cvt_pk_f16_f32 v208, v208, v209\n"
      "v_mul_f32 v254, v233, v254\n"
      "v_cvt_pk_f16_f32 v209, v210, v211\n"
      "v_mul_f32 v255, v233, v255\n"
      "v_mul_f32_e64 v252, v233, v252\n"
      "v_cvt_pk_f16_f32 v210, v252, v253\n"
      "v_cvt_pk_f16_f32 v211, v254, v255\n"
      "v_mfma_f32_16x16x32_bf16 v[252:255], a[16:19], v[244:247], 0\n"
      "v_mul_f32 v214, v233, v214\n"
      "v_mul_f32 v215, v233, v215\n"
      "v_mfma_f32_16x16x32_bf16 v[252:255], a[52:55], v[144:147], v[252:255]\n"
      "v_mul_f32 v217, v233, v217\n"
      "v_cvt_pk_f16_f32 v40, v214, v215\n"
      "v_mfma_f32_16x16x32_bf16 v[164:167], a[20:23], v[244:247], 0\n"
      "ds_read_b128 v[180:183], v115 offset:16384\n"
      "v_mul_f32 v216, v233, v216\n"
      "v_mfma_f32_16x16x32_bf16 v[164:167], a[56:59], v[144:147], v[164:167]\n"
      "s_waitcnt lgkmcnt(2)\n"
      "v_mul_f32 v232, s36, v232\n"
      "v_cvt_pk_f16_f32 v41, v216, v217\n"
      "v_mfma_f32_16x16x32_bf16 v[160:163], a[72:75], v[140:143], v[160:163]\n"
      "v_mul_f32 v248, v232, v248\n"
      "v_mul_f32 v249, v232, v249\n"
      "v_mfma_f32_16x16x32_bf16 v[160:163], a[108:111], v[172:175], v[160:163]\n"
      "v_mul_f32_e64 v250, v232, v250\n"
      "v_cvt_pk_f16_f32 v248, v248, v249\n"
      "v_mfma_f32_16x16x32_bf16 v[252:255], a[88:91], v[140:143], v[252:255]\n"
      "v_mul_f32 v251, v232, v251\n"
      "v_mul_f32 v221, v232, v221\n"
      "v_mfma_f32_16x16x32_bf16 v[252:255], a[124:127], v[172:175], v[252:255]\n"
      "v_cvt_pk_f16_f32 v249, v250, v251\n"
      "v_mul_f32 v220, v232, v220\n"
      "v_mfma_f32_16x16x32_bf16 v[136:139], a[84:87], v[140:143], v[136:139]\n"
      "v_cvt_pk_f16_f32 v250, v220, v221\n"
      "v_mul_f32 v222, v232, v222\n"
      "v_mfma_f32_16x16x32_bf16 v[136:139], a[120:123], v[172:175], v[136:139]\n"
      "v_mul_f32 v223, v232, v223\n"
      "v_cvt_pk_f16_f32 v251, v222, v223\n"
      "v_mfma_f32_16x16x32_bf16 v[220:223], a[4:7], v[244:247], 0\n"
      "v_mul_f32_e64 v205, v232, v205\n"
      "s_waitcnt lgkmcnt(0)\n"
      "v_mfma_f32_16x16x32_bf16 v[220:223], a[40:43], v[144:147], v[220:223]\n"
      "v_mul_f32 v207, v232, v207\n"
      "v_mul_f32 v206, v232, v206\n"
      "v_mfma_f32_16x16x32_bf16 v[220:223], a[76:79], v[140:143], v[220:223]\n"
      "v_mul_f32_e64 v204, v232, v204\n"
      "v_mfma_f32_16x16x32_bf16 v[220:223], a[112:115], v[172:175], v[220:223]\n"
      "v_cvt_pk_f16_f32 v39, v206, v207\n"
      "v_cvt_pk_f16_f32 v38, v204, v205\n"
      "v_mfma_f32_16x16x32_bf16 v[204:207], a[8:11], v[244:247], 0\n"
      "v_mfma_f32_16x16x32_bf16 v[204:207], a[44:47], v[144:147], v[204:207]\n"
      "v_mfma_f32_16x16x32_bf16 v[204:207], a[80:83], v[140:143], v[204:207]\n"
      "v_mfma_f32_16x16x32_bf16 v[204:207], a[116:119], v[172:175], v[204:207]\n"
      "v_mfma_f32_16x16x32_bf16 v[164:167], a[92:95], v[140:143], v[164:167]\n"
      "v_mfma_f32_16x16x32_bf16 v[164:167], a[128:131], v[172:175], v[164:167]\n"
      "v_mfma_f32_16x16x32_bf16 v[160:163], a[144:147], v[176:179], v[160:163]\n"
      "v_mfma_f32_16x16x32_bf16 v[160:163], a[180:183], v[180:183], v[160:163]\n"
      "v_mfma_f32_16x16x32_bf16 v[220:223], a[148:151], v[176:179], v[220:223]\n"
      "v_mfma_f32_16x16x32_bf16 v[220:223], a[184:187], v[180:183], v[220:223]\n"
      "v_mfma_f32_16x16x32_bf16 v[204:207], a[152:155], v[176:179], v[204:207]\n"
      "v_mfma_f32_16x16x32_bf16 v[204:207], a[188:191], v[180:183], v[204:207]\n"
      "v_mfma_f32_16x16x32_bf16 v[136:139], a[156:159], v[176:179], v[136:139]\n"
      "v_mfma_f32_16x16x32_bf16 v[136:139], a[192:195], v[180:183], v[136:139]\n"
      "s_nop 1\n"
      "v_fma_f32 v232, v160, v160, s38\n"
      "v_mfma_f32_16x16x32_bf16 v[252:255], a[160:163], v[176:179], v[252:255]\n"
      "v_fmac_f32 v232, v161, v161\n"
      "v_fmac_f32 v232, v162, v162\n"
      "v_mfma_f32_16x16x32_bf16 v[252:255], a[196:199], v[180:183], v[252:255]\n"
      "s_nop 0\n"
      "v_fma_f32 v233, v136, v136, s38\n"
      "v_fmac_f32_e64 v233, v137, v137\n"
      "v_mfma_f32_16x16x32_bf16 v[164:167], a[164:167], v[176:179], v[164:167]\n"
      "v_fmac_f32 v233, v138, v138\n"
      "v_fmac_f32 v232, v163, v163\n"
      "v_mfma_f32_16x16x32_bf16 v[164:167], a[200:203], v[180:183], v[164:167]\n"
      "v_fmac_f32 v232, v220, v220\n"
      "v_fmac_f32 v233, v139, v139\n"
      "v_mfma_f32_16x16x32_f16 v[244:247], v[224:227], v[228:231], v[70:73]\n"
      "v_fmac_f32 v232, v221, v221\n"
      "v_fmac_f32 v232, v222, v222\n"
      "v_mfma_f32_16x16x32_f16 v[244:247], v[200:203], v[234:237], v[244:247]\n"
      "v_fmac_f32 v233, v252, v252\n"
      "v_fmac_f32 v233, v253, v253\n"
      "v_mfma_f32_16x16x32_f16 v[244:247], v[188:191], v[32:35], v[244:247]\n"
      "v_fmac_f32 v232, v223, v223\n"
      "v_fmac_f32 v233, v254, v254\n"
      "v_fmac_f32 v232, v204, v204\n"
      "v_fmac_f32 v232, v205, v205\n"
      "v_fmac_f32 v233, v255, v255\n"
      "v_fmac_f32 v233, v164, v164\n"
      "v_fmac_f32 v233, v165, v165\n"
      "v_fmac_f32 v233, v166, v166\n"
      "v_fmac_f32 v232, v206, v206\n"
      "v_fmac_f32 v232, v207, v207\n"
      "v_fmac_f32 v233, v167, v167\n"
      "v_mov_b32 v213, v245\n"
      "v_mov_b32 v212, v244\n"
      "v_permlane16_swap_b32 v232, v233\n"
      "v_add_f32 v232, v232, v233\n"
      "v_mov_b32 v233, v232\n"
      "s_nop 1\n"
      "v_permlane32_swap_b32 v232, v233\n"
      "v_add_f32 v232, v232, v233\n"
      "v_rsq_f32 v232, v232\n"
      "s_nop 0\n"
      "v_mov_b32 v233, v232\n"
      "s_nop 1\n"
      "v_permlane16_swap_b32 v232, v233\n"
      "v_mul_f32 v136, v233, v136\n"
      "v_mul_f32 v139, v233, v139\n"
      "v_mul_f32 v137, v233, v137\n"
      "v_mul_f32 v253, v233, v253\n"
      "v_mul_f32 v138, v233, v138\n"
      "v_mul_f32 v165, v233, v165\n"
      "v_mul_f32 v164, v233, v164\n"
      "v_mul_f32 v252, v233, v252\n"
      "v_mul_f32_e64 v232, s36, v232\n"
      "v_cvt_pk_f16_f32 v136, v136, v137\n"
      "v_cvt_pk_f16_f32 v137, v138, v139\n"
      "v_cvt_pk_f16_f32 v46, v164, v165\n"
      "v_mul_f32 v220, v232, v220\n"
      "v_mul_f32 v254, v233, v254\n"
      "v_mul_f32 v166, v233, v166\n"
      "v_mul_f32 v160, v232, v160\n"
      "v_mul_f32 v223, v232, v223\n"
      "v_mul_f32 v167, v233, v167\n"
      "v_mul_f32 v255, v233, v255\n"
      "v_cvt_pk_f16_f32 v47, v166, v167\n"
      "v_cvt_pk_f16_f32 v139, v254, v255\n"
      "v_mul_f32 v204, v232, v204\n"
      "v_mul_f32_e64 v162, v232, v162\n"
      "v_cvt_pk_f16_f32 v138, v252, v253\n"
      "v_mul_f32 v222, v232, v222\n"
      "v_mul_f32 v221, v232, v221\n"
      "v_mul_f32 v163, v232, v163\n"
      "v_mfma_f32_16x16x32_f16 v[252:255], v[136:139], v[228:231], v[86:89]\n"
      "v_mul_f32 v161, v232, v161\n"
      "v_cvt_pk_f16_f32 v160, v160, v161\n"
      "v_mfma_f32_16x16x32_f16 v[252:255], v[46:49], v[32:35], v[252:255]\n"
      "v_cvt_pk_f16_f32 v161, v162, v163\n"
      "v_cvt_pk_f16_f32 v162, v220, v221\n"
      "v_mul_f32 v207, v232, v207\n"
      "v_cvt_pk_f16_f32 v163, v222, v223\n"
      "v_mfma_f32_16x16x32_f16 v[220:223], v[234:237], v[228:231], v[78:81]\n"
      "v_mul_f32 v205, v232, v205\n"
      "v_cvt_pk_f16_f32 v44, v204, v205\n"
      "v_mfma_f32_16x16x32_f16 v[220:223], v[34:37], v[32:35], v[220:223]\n"
      "v_mul_f32_e64 v206, v232, v206\n"
      "v_cvt_pk_f16_f32 v45, v206, v207\n"
      "v_mfma_f32_16x16x32_f16 v[204:207], v[208:211], v[228:231], v[82:85]\n"
      "v_mfma_f32_16x16x32_f16 v[204:207], v[40:43], v[32:35], v[204:207]\n"
      "s_nop 7\n"
      "s_or_b64 vcc, s[48:49], s[50:51]\n"
      "s_cbranch_scc0 L_fast_31_%=\n"
      "L_ok_146_%=:\n"
      "s_cmp_eq_u64 s[48:49], 0\n"
      "s_cbranch_scc1 L_go_147_%=\n"
      "s_mov_b32 m0, s27\n"
      "s_nop 0\n"
      "s_movrels_b32 s9, s80\n"
      "s_lshr_b32 s8, s9, 8\n"
      "s_and_b32 s9, s9, 0xff\n"
      "s_mul_i32 s10, s8, 73\n"
      "s_add_u32 s10, s10, s9\n"
      "s_mul_i32 s30, s10, 4802\n"
      "s_add_u32 s30, s30, s18\n"
      "s_and_b32 s29, s30, 15\n"
      "s_add_u32 s30, s29, 110592\n"
      "s_lshl_b64 s[34:35], s[48:49], 4\n"
      "s_lshr_b64 s[34:35], s[34:35], 5\n"
      "s_cbranch_scc1 L_gen_29_%=\n"
      "v_mbcnt_lo_u32_b32 v232, -1, 0\n"
      "v_mbcnt_hi_u32_b32 v232, -1, v232\n"
      "v_lshlrev_b32 v233, 1, v232\n"
      "v_add_u32 v214, 4802, v233\n"
      "v_cmp_gt_u32 vcc, s29, v233\n"
      "s_nop 1\n"
      "v_cndmask_b32 v233, v214, v233, vcc\n"
      "v_mov_b32 v214, 0\n"
      "v_add_u32 v233, 110592, v233\n"
      "s_mov_b64 exec, 0x7f\n"
      "ds_write_b16 v233, v214\n"
      "s_mov_b64 exec, -1\n"
      "v_lshlrev_b32 v233, 4, v232\n"
      "v_add_u32 v233, 110592, v233\n"
      "ds_read_b128 v[244:247], v233\n"
      "ds_read_b128 v[144:147], v233 offset:1024\n"
      "v_min_u32 v232, 111296, v233\n"
      "ds_read_b128 v[140:143], v233 offset:2048\n"
      "ds_read_b128 v[228:231], v232 offset:4096\n"
      "ds_read_b128 v[172:175], v233 offset:3072\n"
      "s_waitcnt lgkmcnt(4)\n"
      "v_or3_b32 v244, v244, v245, v246\n"
      "s_waitcnt lgkmcnt(3)\n"
      "v_or3_b32 v244, v244, v247, v144\n"
      "v_or3_b32 v244, v244, v145, v146\n"
      "s_waitcnt lgkmcnt(2)\n"
      "v_or3_b32 v244, v244, v147, v140\n"
      "v_or3_b32 v244, v244, v141, v142\n"
      "s_waitcnt lgkmcnt(0)\n"
      "v_or3_b32 v244, v244, v143, v172\n"
      "v_or3_b32 v244, v244, v173, v174\n"
      "v_or3_b32 v244, v244, v175, v228\n"
      "v_or3_b32 v244, v244, v229, v230\n"
      "v_bitop3_b32 v244, v244, v231, s40 bitop3:0xa8\n"
      "v_cmp_ne_u32 s[48:49], 0, v244\n"
      "s_cmp_lg_u64 s[48:49], 0\n"
      "s_cbranch_scc1 L_gen_29_%=\n"
      "s_cmp_eq_u64 s[50:51], 0\n"
      "s_cbranch_scc1 L_fast_31_%=\n"
      "L_go_147_%=:\n"
      "v_mfma_f32_16x16x32_f16 v[244:247], v[234:237], v[248:251], v[90:93]\n"
      "ds_read_b128 v[144:147], v128 offset:3072\n"
      "v_max3_f32 v233, v220, v221, v222\n"
      "v_mfma_f32_16x16x32_f16 v[244:247], v[34:37], v[38:41], v[244:247]\n"
      "v_max3_f32 v232, v223, v204, v205\n"
      "v_max3_f32 v232, v232, v252, v253\n"
      "v_mfma_f32_16x16x32_f16 v[140:143], v[208:211], v[248:251], v[94:97]\n"
      "v_max3_f32 v233, v233, v206, v207\n"
      "v_max3_f32 v233, v233, v254, v255\n"
      "v_mfma_f32_16x16x32_f16 v[140:143], v[40:43], v[38:41], v[140:143]\n"
      "v_max3_f32 v233, v233, v232, v212\n"
      "v_mov_b32_e64 v232, v233\n"
      "v_mfma_f32_16x16x32_f16 v[172:175], v[224:227], v[248:251], v[72:75]\n"
      "s_nop 0\n"
      "v_permlane16_swap_b32 v233, v232\n"
      "v_mfma_f32_16x16x32_f16 v[172:175], v[200:203], v[208:211], v[172:175]\n"
      "v_max_f32 v233, v233, v232\n"
      "v_mov_b32 v232, v233\n"
      "v_mfma_f32_16x16x32_f16 v[172:175], v[188:191], v[38:41], v[172:175]\n"
      "s_nop 0\n"
      "v_permlane32_swap_b32 v233, v232\n"
      "v_max_f32 v233, v233, v232\n"
      "v_sub_f32 v204, v204, v233\n"
      "v_sub_f32_e64 v205, v205, v233\n"
      "v_mfma_f32_16x16x32_f16 v[228:231], v[136:139], v[248:251], v[98:101]\n"
      "v_sub_f32 v221, v221, v233\n"
      "v_sub_f32 v222, v222, v233\n"
      "v_mfma_f32_16x16x32_f16 v[228:231], v[46:49], v[38:41], v[228:231]\n"
      "s_waitcnt lgkmcnt(0)\n"
      "v_sub_f32 v223, v223, v233\n"
      "v_sub_f32 v206, v206, v233\n"
      "v_sub_f32 v220, v220, v233\n"
      "v_exp_f32 v220, v220\n"
      "v_exp_f32 v223, v223\n"
      "v_exp_f32 v205, v205\n"
      "v_sub_f32 v207, v207, v233\n"
      "v_sub_f32 v252, v252, v233\n"
      "v_sub_f32 v253, v253, v233\n"
      "v_exp_f32 v221, v221\n"
      "v_exp_f32 v252, v252\n"
      "v_cvt_pk_bf16_f32 v220, v220, v221\n"
      "v_sub_f32 v255, v255, v233\n"
      "v_bitop3_b32 v144, v144, v145, s4 bitop3:0x7e\n"
      "v_exp_f32 v255, v255\n"
      "v_exp_f32 v253, v253\n"
      "v_sub_f32 v254, v254, v233\n"
      "v_cvt_pk_bf16_f32 v62, v252, v253\n"
      "v_exp_f32 v222, v222\n"
      "v_exp_f32 v206, v206\n"
      "v_sub_f32_e64 v212, v212, v233\n"
      "v_bitop3_b32 v146, v146, v147, s4 bitop3:0x7e\n"
      "v_exp_f32 v207, v207\n"
      "v_cvt_pk_bf16_f32 v221, v222, v223\n"
      "v_exp_f32 v204, v204\n"
      "v_exp_f32_e64 v212, v212\n"
      "v_cvt_pk_bf16_f32 v222, v204, v205\n"
      "v_exp_f32_e64 v254, v254\n"
      "v_cvt_pk_bf16_f32 v223, v206, v207\n"
      "v_cvt_pk_bf16_f32 v64, v212, 0\n"
      "v_cvt_pk_bf16_f32 v63, v254, v255\n"
      "v_mfma_f32_16x16x32_bf16 v[252:255], v[66:69], v[220:223], 0\n"
      "v_or_b32 v144, v144, v146\n"
      "v_cmp_ne_u32 vcc, 0, v144\n"
      "v_mfma_f32_16x16x32_bf16 v[252:255], v[66:69], v[62:65], v[252:255]\n"
      "s_mov_b64 s[48:49], vcc\n"
      "v_max3_f32 v233, v247, v140, v141\n"
      "v_max3_f32 v232, v244, v245, v246\n"
      "v_mfma_f32_16x16x32_bf16 v[204:207], v[192:195], v[220:223], 0\n"
      "s_cbranch_vccnz L_flg_148_%=\n"
      "L_fb_149_%=:\n"
      "v_mfma_f32_16x16x32_bf16 v[204:207], v[50:53], v[62:65], v[204:207]\n"
      "v_max3_f32 v233, v233, v228, v229\n"
      "v_max3_f32 v232, v232, v142, v143\n"
      "v_mfma_f32_16x16x32_bf16 v[144:147], v[152:155], v[220:223], 0\n"
      "v_max3_f32 v232, v232, v230, v231\n"
      "v_max3_f32 v232, v232, v233, v172\n"
      "v_mfma_f32_16x16x32_bf16 v[144:147], v[54:57], v[62:65], v[144:147]\n"
      "v_mov_b32 v233, v232\n"
      "v_rcp_f32 v252, v252\n"
      "v_mfma_f32_16x16x32_bf16 v[248:251], v[240:243], v[220:223], 0\n"
      "v_permlane16_swap_b32 v232, v233\n"
      "v_mfma_f32_16x16x32_bf16 v[248:251], v[58:61], v[62:65], v[248:251]\n"
      "v_max_f32 v232, v232, v233\n"
      "v_mov_b32 v233, v232\n"
      "v_mul_f32 v205, v252, v205\n"
      "v_mul_f32 v204, v252, v204\n"
      "v_permlane32_swap_b32 v232, v233\n"
      "v_max_f32 v232, v232, v233\n"
      "v_sub_f32 v245, v245, v232\n"
      "v_sub_f32 v246, v246, v232\n"
      "v_sub_f32 v244, v244, v232\n"
      "v_sub_f32_e64 v229, v229, v232\n"
      "v_mfma_f32_16x16x32_f16 v[220:223], v[234:237], v[160:163], v[102:105]\n"
      "v_sub_f32 v231, v231, v232\n"
      "v_sub_f32 v230, v230, v232\n"
      "v_mfma_f32_16x16x32_f16 v[220:223], v[34:37], v[44:47], v[220:223]\n"
      "v_sub_f32 v247, v247, v232\n"
      "v_exp_f32 v246, v246\n"
      "v_sub_f32 v228, v228, v232\n"
      "v_sub_f32 v143, v143, v232\n"
      "v_exp_f32 v245, v245\n"
      "v_exp_f32 v244, v244\n"
      "v_sub_f32 v142, v142, v232\n"
      "v_sub_f32 v172, v172, v232\n"
      "v_sub_f32 v140, v140, v232\n"
      "v_exp_f32 v172, v172\n"
      "v_sub_f32_e64 v141, v141, v232\n"
      "v_mfma_f32_16x16x32_f16 v[232:235], v[208:211], v[160:163], v[106:109]\n"
      "ds_read_b128 v[208:211], v120 offset:18432\n"
      "v_cvt_pk_bf16_f32 v244, v244, v245\n"
      "v_mfma_f32_16x16x32_f16 v[232:235], v[40:43], v[44:47], v[232:235]\n"
      "v_exp_f32 v247, v247\n"
      "v_exp_f32 v140, v140\n"
      "v_exp_f32_e64 v229, v229\n"
      "v_mfma_f32_16x16x32_f16 v[176:179], v[224:227], v[160:163], v[74:77]\n"
      "v_cvt_pk_bf16_f32 v204, v204, v205\n"
      "v_cvt_pk_bf16_f32 v245, v246, v247\n"
      "v_mfma_f32_16x16x32_f16 v[176:179], v[200:203], v[136:139], v[176:179]\n"
      "v_exp_f32_e64 v141, v141\n"
      "v_mfma_f32_16x16x32_f16 v[176:179], v[188:191], v[44:47], v[176:179]\n"
      "v_exp_f32 v142, v142\n"
      "v_mfma_f32_16x16x32_f16 v[216:219], v[136:139], v[160:163], v[110:113]\n"
      "v_exp_f32 v228, v228\n"
      "v_mfma_f32_16x16x32_f16 v[216:219], v[46:49], v[44:47], v[216:219]\n"
      "v_cvt_pk_bf16_f32 v246, v140, v141\n"
      "v_exp_f32 v231, v231\n"
      "v_cvt_pk_bf16_f32 v64, v172, 0\n"
      "v_mfma_f32_16x16x32_f16 v[160:163], v[200:203], v[224:227], v[76:79]\n"
      "s_waitcnt lgkmcnt(0)\n"
      "v_exp_f32 v143, v143\n"
      "v_exp_f32 v230, v230\n"
      "v_mul_f32 v207, v252, v207\n"
      "v_cvt_pk_bf16_f32 v63, v230, v231\n"
      "v_cvt_pk_bf16_f32 v247, v142, v143\n"
      "v_mul_f32 v206, v252, v206\n"
      "v_mfma_f32_16x16x16_f16 v[160:163], v[190:191], v[188:189], v[160:163]\n"
      "global_store_dwordx4 v117, v[208:211], s[54:55] sc1\n"
      "v_cvt_pk_bf16_f32 v62, v228, v229\n"
      "v_mfma_f32_16x16x32_bf16 v[140:143], v[66:69], v[244:247], 0\n"
      "v_cvt_pk_bf16_f32 v205, v206, v207\n"
      "v_mul_f32 v144, v252, v144\n"
      "v_mfma_f32_16x16x32_bf16 v[140:143], v[66:69], v[62:65], v[140:143]\n"
      "v_mul_f32 v145, v252, v145\n"
      "v_cvt_pk_bf16_f32 v206, v144, v145\n"
      "v_mul_f32 v250, v252, v250\n"
      "v_mfma_f32_16x16x32_bf16 v[224:227], v[192:195], v[244:247], 0\n"
      "v_mul_f32 v147, v252, v147\n"
      "v_mul_f32 v146, v252, v146\n"
      "v_mfma_f32_16x16x32_bf16 v[224:227], v[50:53], v[62:65], v[224:227]\n"
      "v_mul_f32 v251, v252, v251\n"
      "v_mul_f32 v248, v252, v248\n"
      "v_cvt_pk_bf16_f32 v207, v146, v147\n"
      "v_mfma_f32_16x16x32_bf16 v[208:211], v[152:155], v[244:247], 0\n"
      "v_mul_f32 v249, v252, v249\n"
      "v_cvt_pk_bf16_f32 v248, v248, v249\n"
      "v_mfma_f32_16x16x32_bf16 v[208:211], v[54:57], v[62:65], v[208:211]\n"
      "ds_write_b128 v124, v[204:207]\n"
      "v_cvt_pk_bf16_f32 v249, v250, v251\n"
      "v_max3_f32 v250, v220, v221, v222\n"
      "v_max3_f32 v250, v250, v234, v235\n"
      "v_max3_f32 v251, v223, v232, v233\n"
      "v_max3_f32 v250, v250, v218, v219\n"
      "v_max3_f32 v251, v251, v216, v217\n"
      "v_mfma_f32_16x16x32_bf16 v[144:147], v[240:243], v[244:247], 0\n"
      "v_max3_f32 v250, v250, v251, v176\n"
      "v_mov_b32 v251, v250\n"
      "v_mfma_f32_16x16x32_bf16 v[144:147], v[58:61], v[62:65], v[144:147]\n"
      "s_nop 0\n"
      "v_permlane16_swap_b32 v250, v251\n"
      "v_max_f32 v250, v250, v251\n"
      "v_rcp_f32 v140, v140\n"
      "v_mov_b32 v251, v250\n"
      "v_mul_f32 v225, v140, v225\n"
      "v_mul_f32 v224, v140, v224\n"
      "v_permlane32_swap_b32 v250, v251\n"
      "v_max_f32 v250, v250, v251\n"
      "v_sub_f32 v220, v220, v250\n"
      "v_sub_f32 v216, v216, v250\n"
      "v_sub_f32 v234, v234, v250\n"
      "v_sub_f32 v222, v222, v250\n"
      "v_sub_f32 v233, v233, v250\n"
      "v_sub_f32 v217, v217, v250\n"
      "v_sub_f32 v232, v232, v250\n"
      "v_sub_f32 v235, v235, v250\n"
      "v_sub_f32 v219, v219, v250\n"
      "v_exp_f32 v235, v235\n"
      "v_sub_f32 v223, v223, v250\n"
      "v_exp_f32 v220, v220\n"
      "v_exp_f32 v232, v232\n"
      "v_exp_f32 v234, v234\n"
      "v_sub_f32 v221, v221, v250\n"
      "v_sub_f32 v176, v176, v250\n"
      "v_exp_f32 v223, v223\n"
      "v_sub_f32 v218, v218, v250\n"
      "v_exp_f32 v221, v221\n"
      "v_exp_f32 v219, v219\n"
      "v_exp_f32 v222, v222\n"
      "v_exp_f32 v216, v216\n"
      "v_cvt_pk_bf16_f32 v220, v220, v221\n"
      "v_exp_f32 v217, v217\n"
      "v_cvt_pk_bf16_f32 v221, v222, v223\n"
      "v_exp_f32 v233, v233\n"
      "v_cvt_pk_bf16_f32 v223, v234, v235\n"
      "v_mul_f32 v211, v140, v211\n"
      "v_mul_f32 v208, v140, v208\n"
      "v_exp_f32 v176, v176\n"
      "v_mul_f32 v226, v140, v226\n"
      "v_exp_f32_e64 v218, v218\n"
      "v_cvt_pk_bf16_f32 v62, v216, v217\n"
      "v_cvt_pk_bf16_f32 v222, v232, v233\n"
      "v_cvt_pk_bf16_f32 v63, v218, v219\n"
      "v_cvt_pk_bf16_f32 v64, v176, 0\n"
      "v_mfma_f32_16x16x32_bf16 v[232:235], v[66:69], v[220:223], 0\n"
      "ds_read_b128 v[200:203], v121 offset:18432\n"
      "v_mul_f32_e64 v145, v140, v145\n"
      "v_mfma_f32_16x16x32_bf16 v[232:235], v[66:69], v[62:65], v[232:235]\n"
      "ds_read_b128 v[216:219], v123 offset:19456\n"
      "v_mul_f32_e64 v227, v140, v227\n"
      "v_cvt_pk_bf16_f32 v224, v224, v225\n"
      "v_mfma_f32_16x16x32_bf16 v[252:255], v[192:195], v[220:223], 0\n"
      "ds_read_b128 v[188:191], v123 offset:22528\n"
      "v_cvt_pk_bf16_f32 v225, v226, v227\n"
      "v_mul_f32_e64 v209, v140, v209\n"
      "v_cvt_pk_bf16_f32 v226, v208, v209\n"
      "v_mfma_f32_16x16x32_bf16 v[136:139], v[152:155], v[220:223], 0\n"
      "v_mul_f32_e64 v210, v140, v210\n"
      "v_cvt_pk_bf16_f32 v227, v210, v211\n"
      "v_mfma_f32_16x16x32_bf16 v[136:139], v[54:57], v[62:65], v[136:139]\n"
      "v_mul_f32_e64 v144, v140, v144\n"
      "v_cvt_pk_bf16_f32 v144, v144, v145\n"
      "v_cndmask_b32 v213, v213, v173, s[42:43]\n"
      "v_mfma_f32_16x16x32_bf16 v[208:211], v[240:243], v[220:223], 0\n"
      "ds_read_b128 v[220:223], v123 offset:25600\n"
      "v_cndmask_b32 v213, v213, v177, s[44:45]\n"
      "v_mfma_f32_16x16x32_bf16 v[208:211], v[58:61], v[62:65], v[208:211]\n"
      "ds_write_b128 v124, v[224:227] offset:1024\n"
      "v_cndmask_b32 v213, v213, v161, s[46:47]\n"
      "v_mfma_f32_16x16x32_bf16 v[252:255], v[50:53], v[62:65], v[252:255]\n"
      "s_nop 0\n"
      "v_max_f32_dpp v250, v213, v213 row_ror:8 row_mask:0xf bank_mask:0xf\n"
      "s_nop 1\n"
      "v_max_f32_dpp v250, v250, v250 row_ror:4 row_mask:0xf bank_mask:0xf\n"
      "v_mul_f32_e64 v147, v140, v147\n"
      "v_mul_f32 v146, v140, v146\n"
      "v_max_f32_dpp v250, v250, v250 row_ror:2 row_mask:0xf bank_mask:0xf\n"
      "s_waitcnt lgkmcnt(2)\n"
      "v_cvt_pk_bf16_f32 v145, v146, v147\n"
      "v_max_f32_dpp v250, v250, v250 row_ror:1 row_mask:0xf bank_mask:0xf\n"
      "v_mfma_f32_16x16x32_bf16 v[244:247], a[216:219], v[216:219], 0\n"
      "ds_read_b128 v[224:227], v123 offset:28672\n"
      "v_max_f32_dpp v250, v250, v250 row_bcast:15 row_mask:0xa bank_mask:0xf\n"
      "v_mfma_f32_16x16x32_bf16 v[244:247], a[228:231], v[188:191], v[244:247]\n"
      "ds_write2st64_b64 v125, v[248:249], v[144:145] offset0:0 offset1:2\n"
      "v_mfma_f32_16x16x32_bf16 v[144:147], a[220:223], v[216:219], 0\n"
      "ds_read_b128 v[160:163], v123 offset:31744\n"
      "v_max_f32_dpp v250, v250, v250 row_bcast:31 row_mask:0xc bank_mask:0xf\n"
      "v_mfma_f32_16x16x32_bf16 v[144:147], a[232:235], v[188:191], v[144:147]\n"
      "s_mov_b32 exec_hi, 0\n"
      "global_store_dwordx4 v118, v[200:203], s[54:55] sc1\n"
      "s_mov_b32 exec_hi, -1\n"
      "v_mfma_f32_16x16x32_bf16 v[204:207], a[224:227], v[216:219], 0\n"
      "s_nop 0\n"
      "v_readlane_b32 s31, v250, 63\n"
      "v_mfma_f32_16x16x32_bf16 v[204:207], a[236:239], v[188:191], v[204:207]\n"
      "ds_read_b128 v[248:251], v123 offset:34816\n"
      "v_subrev_f32 v213, s31, v213\n"
      "v_exp_f32 v213, v213\n"
      "s_add_u32 s28, s27, 2\n"
      "v_cvt_pk_bf16_f32 v213, v213, v213\n"
      "s_waitcnt lgkmcnt(3)\n"
      "v_mfma_f32_16x16x32_bf16 v[244:247], a[240:243], v[220:223], v[244:247]\n"
      "ds_read_b128 v[200:203], v123 offset:20480\n"
      "s_min_u32 m0, s28, s3\n"
      "v_mfma_f32_16x16x32_bf16 v[244:247], a[252:255], v[224:227], v[244:247]\n"
      "ds_write_b16 v130, v213\n"
      "v_mfma_f32_16x16x32_bf16 v[144:147], a[244:247], v[220:223], v[144:147]\n"
      "s_movrels_b32 s9, s80\n"
      "s_lshr_b32 s8, s9, 8\n"
      "s_and_b32 s9, s9, 0xff\n"
      "v_mfma_f32_16x16x32_bf16 v[144:147], v[0:3], v[224:227], v[144:147]\n"
      "s_mul_i32 s10, s8, 73\n"
      "s_add_u32 s10, s10, s9\n"
      "v_mfma_f32_16x16x32_bf16 v[204:207], a[248:251], v[220:223], v[204:207]\n"
      "ds_read_b128 v[188:191], v123 offset:23552\n"
      "s_mul_i32 s28, s10, 4802\n"
      "v_mfma_f32_16x16x32_bf16 v[204:207], v[4:7], v[224:227], v[204:207]\n"
      "ds_read_b128 v[216:219], v131\n"
      "s_waitcnt lgkmcnt(4)\n"
      "v_mfma_f32_16x16x32_bf16 v[244:247], v[8:11], v[160:163], v[244:247]\n"
      "s_add_u32 s56, s18, s28\n"
      "s_addc_u32 s57, s19, 0\n"
      "s_and_b32 s56, s56, -16\n"
      "v_mfma_f32_16x16x32_bf16 v[244:247], v[20:23], v[248:251], v[244:247]\n"
      "s_sub_u32 s58, s56, 110592\n"
      "s_subb_u32 s59, s57, 0\n"
      "s_sub_u32 s56, s56, 73728\n"
      "v_mfma_f32_16x16x32_bf16 v[144:147], v[12:15], v[160:163], v[144:147]\n"
      "s_subb_u32 s57, s57, 0\n"
      "s_mul_i32 s28, s8, 1373568\n"
      "s_mul_i32 s29, s9, 2688\n"
      "v_mfma_f32_16x16x32_bf16 v[144:147], v[24:27], v[248:251], v[144:147]\n"
      "ds_read_b128 v[212:215], v123 offset:26624\n"
      "s_add_u32 s28, s28, s29\n"
      "s_cmp_eq_u32 s8, 72\n"
      "v_mfma_f32_16x16x32_bf16 v[204:207], v[16:19], v[160:163], v[204:207]\n"
      "s_cselect_b32 s8, 0, 100270464\n"
      "ds_read_b128 v[176:179], v131 offset:64\n"
      "v_mfma_f32_16x16x32_bf16 v[204:207], v[28:31], v[248:251], v[204:207]\n"
      "s_cmp_eq_u32 s9, 72\n"
      "s_cselect_b32 s9, 0, 196224\n"
      "s_waitcnt lgkmcnt(3)\n"
      "v_mfma_f32_16x16x32_bf16 v[160:163], a[216:219], v[200:203], 0\n"
      "ds_read_b128 v[248:251], v114 offset:18432\n"
      "s_add_u32 s10, s8, s9\n"
      "v_mfma_f32_16x16x32_bf16 v[160:163], a[228:231], v[188:191], v[160:163]\n"
      "ds_read_b128 v[172:175], v123 offset:29696\n"
      "s_add_u32 s28, s28, s10\n"
      "v_mfma_f32_16x16x32_bf16 v[140:143], a[220:223], v[200:203], 0\n"
      "s_add_u32 s52, s16, s28\n"
      "s_addc_u32 s53, s17, 0\n"
      "v_fma_f32 v156, v244, v244, s39\n"
      "v_mfma_f32_16x16x32_bf16 v[224:227], a[224:227], v[200:203], 0\n"
      "v_fmac_f32_e64 v156, v245, v245\n"
      "s_add_u32 s27, s27, 1\n"
      "v_mfma_f32_16x16x32_bf16 v[224:227], a[236:239], v[188:191], v[224:227]\n"
      "ds_read_b128 v[220:223], v115 offset:18432\n"
      "v_fmac_f32 v156, v246, v246\n"
      "v_fmac_f32 v156, v247, v247\n"
      "v_fmac_f32 v156, v144, v144\n"
      "v_fmac_f32 v156, v145, v145\n"
      "v_fmac_f32 v156, v146, v146\n"
      "v_mfma_f32_16x16x32_bf16 v[140:143], a[232:235], v[188:191], v[140:143]\n"
      "ds_read_b128 v[236:239], v123 offset:35840\n"
      "ds_read_b128 v[228:231], v123 offset:32768\n"
      "v_fmac_f32 v156, v147, v147\n"
      "v_fmac_f32 v156, v204, v204\n"
      "v_fmac_f32 v156, v205, v205\n"
      "v_fmac_f32 v156, v206, v206\n"
      "v_fmac_f32 v156, v207, v207\n"
      "s_nop 1\n"
      "v_permlane16_swap_b32 v159, v156\n"
      "s_cmp_ge_u32 s27, s26\n"
      "s_waitcnt lgkmcnt(3)\n"
      "v_mfma_f32_16x16x32_bf16 v[160:163], a[240:243], v[212:215], v[160:163]\n"
      "v_mfma_f32_16x16x32_bf16 v[160:163], a[252:255], v[172:175], v[160:163]\n"
      "v_add_f32_e64 v159, v159, v156\n"
      "v_mfma_f32_16x16x32_bf16 v[140:143], a[244:247], v[212:215], v[140:143]\n"
      "s_cselect_b32 s28, 1, 0\n"
      "s_waitcnt lgkmcnt(2)\n"
      "v_mfma_f32_16x16x32_bf16 v[140:143], v[0:3], v[172:175], v[140:143]\n"
      "ds_read_b128 v[200:203], v114 offset:24576\n"
      "v_mfma_f32_16x16x32_bf16 v[224:227], a[248:251], v[212:215], v[224:227]\n"
      "v_mfma_f32_16x16x32_bf16 v[224:227], v[4:7], v[172:175], v[224:227]\n"
      "ds_read_b128 v[212:215], v115 offset:24576\n"
      "v_mfma_f32_16x16x32_bf16 v[188:191], a[0:3], v[248:251], 0\n"
      "v_mfma_f32_16x16x32_bf16 v[188:191], a[36:39], v[220:223], v[188:191]\n"
      "s_waitcnt lgkmcnt(2)\n"
      "v_mfma_f32_16x16x32_bf16 v[160:163], v[8:11], v[228:231], v[160:163]\n"
      "s_xor_b32 s29, s10, s11\n"
      "v_mfma_f32_16x16x32_bf16 v[160:163], v[20:23], v[236:239], v[160:163]\n"
      "v_mfma_f32_16x16x32_bf16 v[140:143], v[12:15], v[228:231], v[140:143]\n"
      "v_mfma_f32_16x16x32_bf16 v[140:143], v[24:27], v[236:239], v[140:143]\n"
      "v_mfma_f32_16x16x32_bf16 v[224:227], v[16:19], v[228:231], v[224:227]\n"
      "v_mfma_f32_16x16x32_bf16 v[224:227], v[28:31], v[236:239], v[224:227]\n"
      "s_waitcnt lgkmcnt(0)\n"
      "s_or_b32 s28, s28, s29\n"
      "v_mfma_f32_16x16x32_bf16 v[236:239], v[216:219], v[66:69], 0\n"
      "s_nop 0\n"
      "v_fma_f32 v156, v160, v160, s39\n"
      "v_mfma_f32_16x16x32_bf16 v[236:239], v[176:179], v[66:69], v[236:239]\n"
      "v_fmac_f32 v156, v161, v161\n"
      "v_fmac_f32 v156, v162, v162\n"
      "v_mfma_f32_16x16x32_bf16 v[164:167], v[216:219], v[192:195], 0\n"
      "v_fmac_f32 v156, v163, v163\n"
      "v_fmac_f32 v156, v140, v140\n"
      "v_mfma_f32_16x16x32_bf16 v[164:167], v[176:179], v[50:53], v[164:167]\n"
      "v_fmac_f32 v156, v141, v141\n"
      "v_fmac_f32 v156, v142, v142\n"
      "v_mfma_f32_16x16x32_bf16 v[228:231], v[216:219], v[152:155], 0\n"
      "v_fmac_f32 v156, v143, v143\n"
      "v_fmac_f32 v156, v224, v224\n"
      "v_mfma_f32_16x16x32_bf16 v[228:231], v[176:179], v[54:57], v[228:231]\n"
      "v_fmac_f32 v156, v225, v225\n"
      "v_fmac_f32 v156, v226, v226\n"
      "v_mfma_f32_16x16x32_bf16 v[152:155], v[216:219], v[240:243], 0\n"
      "v_fmac_f32 v156, v227, v227\n"
      "v_mov_b32 v157, v156\n"
      "v_mfma_f32_16x16x32_bf16 v[152:155], v[176:179], v[58:61], v[152:155]\n"
      "s_nop 0\n"
      "v_permlane16_swap_b32 v156, v157\n"
      "v_mfma_f32_16x16x32_bf16 v[172:175], a[4:7], v[248:251], 0\n"
      "v_add_f32 v156, v156, v157\n"
      "v_mov_b32_dpp v236, v236 quad_perm:[0,0,0,0] row_mask:0xf bank_mask:0xf\n"
      "v_mfma_f32_16x16x32_bf16 v[172:175], a[40:43], v[220:223], v[172:175]\n"
      "v_permlane32_swap_b32_e64 v159, v156\n"
      "v_mfma_f32_16x16x32_bf16 v[176:179], a[8:11], v[248:251], 0\n"
      "v_cndmask_b32 v164, v164, v228, s[42:43]\n"
      "v_add_f32_e64 v159, v159, v156\n"
      "v_mfma_f32_16x16x32_bf16 v[176:179], a[44:47], v[220:223], v[176:179]\n"
      "ds_write_b32 v129, v159\n"
      "v_mfma_f32_16x16x32_bf16 v[228:231], a[12:15], v[248:251], 0\n"
      "v_cndmask_b32 v164, v164, v152, s[44:45]\n"
      "v_mfma_f32_16x16x32_bf16 v[228:231], a[48:51], v[220:223], v[228:231]\n"
      "v_rcp_f32_e64 v236, v236\n"
      "v_mfma_f32_16x16x32_bf16 v[156:159], a[16:19], v[248:251], 0\n"
      "v_mul_f32_e64 v164, v236, v164\n"
      "v_cvt_pk_bf16_f32 v164, v164, v164\n"
      "v_mfma_f32_16x16x32_bf16 v[156:159], a[52:55], v[220:223], v[156:159]\n"
      "ds_write_b16 v135, v164\n"
      "v_mfma_f32_16x16x32_bf16 v[216:219], a[20:23], v[248:251], 0\n"
      "v_mfma_f32_16x16x32_bf16 v[216:219], a[56:59], v[220:223], v[216:219]\n"
      "s_branch L_join_30_%=\n"
      "L_flg_148_%=:\n"
      "v_cmp_gt_i32 vcc, 0, v144\n"
      "s_cbranch_vccz L_fb_149_%=\n"
      "s_sleep 1\n"
      "ds_read_b128 v[144:147], v128 offset:3072\n"
      "s_waitcnt lgkmcnt(0)\n"
      "v_bitop3_b32 v144, v144, v145, s4 bitop3:0x7e\n"
      "v_bitop3_b32 v146, v146, v147, s4 bitop3:0x7e\n"
      "v_or_b32 v144, v144, v146\n"
      "v_cmp_ne_u32 s[48:49], 0, v144\n"
      "s_branch L_flg_148_%=\n"
      "L_gen_29_%=:\n"
      "s_nop 0\n"
      "s_nop 0\n"
      "v_mbcnt_lo_u32_b32 v158, -1, 0\n"
      "v_mbcnt_hi_u32_b32 v158, -1, v158\n"
      "v_and_b32 v165, 15, v158\n"
      "v_mul_u32_u24 v218, 0x62, v165\n"
      "v_lshrrev_b32 v164, 4, v158\n"
      "v_lshl_add_u32 v218, v164, 3, v218\n"
      "v_add_u32 v218, s30, v218\n"
      "v_and_b32 v158, -4, v218\n"
      "v_lshlrev_b32 v164, 2, v164\n"
      "v_sub_u32 v165, v165, v164\n"
      "v_and_b32 v218, 2, v218\n"
      "v_and_b32 v164, 1, v165\n"
      "v_lshrrev_b32_e64 v165, 1, v165\n"
      "s_movk_i32 s31, 0x3fb9\n"
      "ds_read2_b32 v[244:245], v158 offset0:0 offset1:1\n"
      "ds_read2_b32 v[232:233], v158 offset0:8 offset1:9\n"
      "ds_read2_b32 v[246:247], v158 offset0:2 offset1:10\n"
      "ds_read2_b32 v[214:215], v158 offset0:16 offset1:17\n"
      "ds_read2_b32 v[144:145], v158 offset0:18 offset1:24\n"
      "v_add_u32 v158, 1568, v158\n"
      "v_lshlrev_b32_e64 v164, 4, v164\n"
      "v_mfma_f32_16x16x32_f16 v[184:187], v[224:227], v[248:251], v[72:75]\n"
      "ds_read2_b32 v[142:143], v158 offset0:2 offset1:10\n"
      "v_cmp_eq_u32 vcc, 0, v165\n"
      "v_mfma_f32_16x16x32_f16 v[184:187], v[200:203], v[208:211], v[184:187]\n"
      "ds_read2_b32 v[140:141], v158 offset0:16 offset1:17\n"
      "ds_read2_b32 v[146:147], v158 offset0:0 offset1:1\n"
      "ds_read2_b32 v[172:173], v158 offset0:18 offset1:24\n"
      "ds_read2_b32 v[156:157], v158 offset0:8 offset1:9\n"
      "v_lshlrev_b32 v164, v164, s31\n"
      "v_add_u32 v158, 1568, v158\n"
      "v_cndmask_b32 v216, 0, v164, vcc\n"
      "v_mfma_f32_16x16x32_f16 v[180:183], v[208:211], v[248:251], v[94:97]\n"
      "ds_read2_b32 v[174:175], v158 offset0:0 offset1:1\n"
      "ds_read2_b32 v[228:229], v158 offset0:2 offset1:10\n"
      "ds_read2_b32 v[178:179], v158 offset0:16 offset1:17\n"
      "v_cmp_eq_u32 vcc, 1, v165\n"
      "s_waitcnt lgkmcnt(11)\n"
      "v_alignbyte_b32 v232, v233, v232, v218\n"
      "v_cndmask_b32 v217, 0, v164, vcc\n"
      "v_alignbyte_b32 v244, v245, v244, v218\n"
      "v_mfma_f32_16x16x32_f16 v[164:167], v[234:237], v[248:251], v[90:93]\n"
      "ds_read2_b32 v[176:177], v158 offset0:8 offset1:9\n"
      "s_waitcnt lgkmcnt(9)\n"
      "v_mfma_f32_16x16x32_f16 v[164:167], v[34:37], v[38:41], v[164:167]\n"
      "ds_read2_b32 v[230:231], v158 offset0:18 offset1:24\n"
      "v_alignbyte_b32 v214, v215, v214, v218\n"
      "v_alignbyte_b32 v233, v247, v233, v218\n"
      "v_alignbyte_b32 v215, v144, v215, v218\n"
      "v_mfma_f32_16x16x32_f16 v[184:187], v[188:191], v[38:41], v[184:187]\n"
      "v_alignbyte_b32 v145, v145, v145, v218\n"
      "v_lshlrev_b32 v145, 16, v145\n"
      "v_mfma_f32_16x16x16_bf16 v[204:207], v[216:217], v[232:233], v[204:207]\n"
      "s_waitcnt lgkmcnt(6)\n"
      "v_alignbyte_b32 v146, v147, v146, v218\n"
      "v_alignbyte_b32 v147, v142, v147, v218\n"
      "v_mfma_f32_16x16x32_f16 v[180:183], v[40:43], v[38:41], v[180:183]\n"
      "s_waitcnt lgkmcnt(5)\n"
      "v_fmac_f32 v212, s37, v145\n"
      "v_alignbyte_b32 v156, v157, v156, v218\n"
      "v_alignbyte_b32 v140, v141, v140, v218\n"
      "v_alignbyte_b32 v141, v172, v141, v218\n"
      "v_alignbyte_b32 v245, v246, v245, v218\n"
      "v_alignbyte_b32 v157, v143, v157, v218\n"
      "v_mfma_f32_16x16x32_f16 v[142:145], v[136:139], v[248:251], v[98:101]\n"
      "s_waitcnt lgkmcnt(2)\n"
      "v_alignbyte_b32 v173, v173, v173, v218\n"
      "v_mfma_f32_16x16x32_f16 v[142:145], v[46:49], v[38:41], v[142:145]\n"
      "s_nop 0\n"
      "v_alignbyte_b32 v174, v175, v174, v218\n"
      "v_lshlrev_b32_e64 v173, 16, v173\n"
      "v_alignbyte_b32 v175, v228, v175, v218\n"
      "v_mfma_f32_16x16x16_bf16 v[220:223], v[216:217], v[244:245], v[220:223]\n"
      "ds_read_b128 v[244:247], v128 offset:3072\n"
      "v_fmac_f32 v184, s37, v173\n"
      "v_mfma_f32_16x16x16_bf16 v[252:255], v[216:217], v[214:215], v[252:255]\n"
      "s_waitcnt lgkmcnt(1)\n"
      "v_alignbyte_b32 v176, v177, v176, v218\n"
      "v_mfma_f32_16x16x16_bf16 v[180:183], v[216:217], v[156:157], v[180:183]\n"
      "s_nop 1\n"
      "v_max3_f32 v232, v220, v221, v222\n"
      "v_mfma_f32_16x16x16_bf16 v[164:167], v[216:217], v[146:147], v[164:167]\n"
      "s_nop 0\n"
      "v_alignbyte_b32 v178, v179, v178, v218\n"
      "v_max3_f32 v232, v232, v206, v207\n"
      "v_max3_f32 v232, v232, v254, v255\n"
      "v_mfma_f32_16x16x16_bf16 v[142:145], v[216:217], v[140:141], v[142:145]\n"
      "v_max3_f32 v233, v223, v204, v205\n"
      "v_max3_f32 v233, v233, v252, v253\n"
      "v_max3_f32 v232, v232, v233, v212\n"
      "v_mov_b32_e64 v233, v232\n"
      "v_alignbyte_b32 v231, v231, v231, v218\n"
      "v_alignbyte_b32 v179, v230, v179, v218\n"
      "v_permlane16_swap_b32 v232, v233\n"
      "v_max_f32 v232, v232, v233\n"
      "v_mov_b32_e64 v233, v232\n"
      "v_max3_f32 v146, v167, v180, v181\n"
      "v_lshlrev_b32 v231, 16, v231\n"
      "v_permlane32_swap_b32 v232, v233\n"
      "v_max_f32 v232, v232, v233\n"
      "v_sub_f32 v221, v221, v232\n"
      "v_sub_f32 v205, v205, v232\n"
      "v_sub_f32 v255, v255, v232\n"
      "v_sub_f32 v254, v254, v232\n"
      "v_sub_f32 v253, v253, v232\n"
      "v_exp_f32 v221, v221\n"
      "v_sub_f32 v207, v207, v232\n"
      "v_sub_f32 v222, v222, v232\n"
      "v_alignbyte_b32 v177, v229, v177, v218\n"
      "v_exp_f32 v207, v207\n"
      "v_exp_f32 v253, v253\n"
      "v_sub_f32 v220, v220, v232\n"
      "v_sub_f32 v212, v212, v232\n"
      "v_sub_f32 v204, v204, v232\n"
      "v_max3_f32 v147, v164, v165, v166\n"
      "v_exp_f32 v220, v220\n"
      "v_sub_f32 v206, v206, v232\n"
      "v_sub_f32 v223, v223, v232\n"
      "v_exp_f32 v222, v222\n"
      "v_max3_f32 v146, v146, v142, v143\n"
      "v_exp_f32_e64 v223, v223\n"
      "v_max3_f32 v147, v147, v182, v183\n"
      "v_cvt_pk_bf16_f32 v220, v220, v221\n"
      "v_max3_f32 v147, v147, v144, v145\n"
      "v_max3_f32 v147, v147, v146, v184\n"
      "s_waitcnt lgkmcnt(0)\n"
      "v_exp_f32 v254, v254\n"
      "v_mov_b32 v146, v147\n"
      "v_exp_f32 v212, v212\n"
      "v_cvt_pk_bf16_f32 v221, v222, v223\n"
      "v_exp_f32 v255, v255\n"
      "v_bitop3_b32 v246, v246, v247, s4 bitop3:0x7e\n"
      "v_exp_f32 v206, v206\n"
      "v_exp_f32 v205, v205\n"
      "v_bitop3_b32 v244, v244, v245, s4 bitop3:0x7e\n"
      "v_permlane16_swap_b32 v147, v146\n"
      "v_cvt_pk_bf16_f32 v63, v254, v255\n"
      "v_exp_f32 v204, v204\n"
      "v_sub_f32 v252, v252, v232\n"
      "v_or_b32 v244, v244, v246\n"
      "v_cvt_pk_bf16_f32 v223, v206, v207\n"
      "v_max_f32 v147, v147, v146\n"
      "v_mov_b32 v146, v147\n"
      "v_exp_f32 v252, v252\n"
      "v_cmp_ne_u32 vcc, 0, v244\n"
      "v_permlane32_swap_b32 v147, v146\n"
      "v_max_f32 v147, v147, v146\n"
      "v_sub_f32 v145, v145, v147\n"
      "v_sub_f32 v181, v181, v147\n"
      "v_sub_f32 v184, v184, v147\n"
      "v_sub_f32 v183, v183, v147\n"
      "v_cvt_pk_bf16_f32 v62, v252, v253\n"
      "v_sub_f32 v180, v180, v147\n"
      "v_sub_f32 v182, v182, v147\n"
      "v_exp_f32 v180, v180\n"
      "v_sub_f32 v166, v166, v147\n"
      "v_sub_f32 v165, v165, v147\n"
      "v_sub_f32 v167, v167, v147\n"
      "v_exp_f32 v182, v182\n"
      "v_sub_f32 v142, v142, v147\n"
      "v_sub_f32 v164, v164, v147\n"
      "v_exp_f32 v164, v164\n"
      "v_cvt_pk_bf16_f32 v222, v204, v205\n"
      "v_exp_f32 v166, v166\n"
      "v_exp_f32 v181, v181\n"
      "v_exp_f32 v183, v183\n"
      "v_cvt_pk_bf16_f32 v64, v212, 0\n"
      "v_sub_f32 v143, v143, v147\n"
      "v_exp_f32 v165, v165\n"
      "v_exp_f32 v167, v167\n"
      "s_mov_b64 s[48:49], vcc\n"
      "v_cvt_pk_bf16_f32 v164, v164, v165\n"
      "v_cvt_pk_bf16_f32 v165, v166, v167\n"
      "v_exp_f32 v184, v184\n"
      "v_cvt_pk_bf16_f32 v166, v180, v181\n"
      "v_mfma_f32_16x16x32_bf16 v[252:255], v[66:69], v[220:223], 0\n"
      "s_cbranch_vccnz L_flg_160_%=\n"
      "L_fb_161_%=:\n"
      "v_mfma_f32_16x16x32_bf16 v[252:255], v[66:69], v[62:65], v[252:255]\n"
      "v_cvt_pk_bf16_f32 v167, v182, v183\n"
      "v_sub_f32 v144, v144, v147\n"
      "v_mfma_f32_16x16x32_bf16 v[244:247], v[192:195], v[220:223], 0\n"
      "v_exp_f32 v143, v143\n"
      "v_mfma_f32_16x16x32_bf16 v[244:247], v[50:53], v[62:65], v[244:247]\n"
      "v_exp_f32 v142, v142\n"
      "v_mfma_f32_16x16x32_bf16 v[204:207], v[152:155], v[220:223], 0\n"
      "v_exp_f32 v144, v144\n"
      "v_mfma_f32_16x16x32_bf16 v[204:207], v[54:57], v[62:65], v[204:207]\n"
      "ds_read_b128 v[248:251], v120 offset:18432\n"
      "v_exp_f32 v145, v145\n"
      "v_mfma_f32_16x16x32_bf16 v[180:183], v[240:243], v[220:223], 0\n"
      "v_rcp_f32 v252, v252\n"
      "v_mfma_f32_16x16x32_bf16 v[180:183], v[58:61], v[62:65], v[180:183]\n"
      "v_cvt_pk_bf16_f32 v62, v142, v143\n"
      "v_cvt_pk_bf16_f32 v63, v144, v145\n"
      "v_mfma_f32_16x16x32_f16 v[144:147], v[234:237], v[160:163], v[102:105]\n"
      "v_cvt_pk_bf16_f32 v64, v184, 0\n"
      "v_mul_f32_e64 v244, v252, v244\n"
      "v_mfma_f32_16x16x32_f16 v[144:147], v[34:37], v[44:47], v[144:147]\n"
      "v_mul_f32 v204, v252, v204\n"
      "v_mul_f32 v245, v252, v245\n"
      "v_mfma_f32_16x16x32_f16 v[232:235], v[208:211], v[160:163], v[106:109]\n"
      "v_mul_f32 v246, v252, v246\n"
      "v_mul_f32 v247, v252, v247\n"
      "v_mfma_f32_16x16x32_f16 v[232:235], v[40:43], v[44:47], v[232:235]\n"
      "v_mul_f32 v206, v252, v206\n"
      "v_mul_f32 v181, v252, v181\n"
      "v_mul_f32 v207, v252, v207\n"
      "v_mul_f32 v182, v252, v182\n"
      "v_mfma_f32_16x16x16_bf16 v[144:147], v[216:217], v[174:175], v[144:147]\n"
      "v_cvt_pk_bf16_f32 v244, v244, v245\n"
      "v_cvt_pk_bf16_f32 v245, v246, v247\n"
      "v_mfma_f32_16x16x16_bf16 v[232:235], v[216:217], v[176:177], v[232:235]\n"
      "v_cvt_pk_bf16_f32 v247, v206, v207\n"
      "v_mul_f32 v183, v252, v183\n"
      "v_mul_f32 v180, v252, v180\n"
      "v_mul_f32 v205, v252, v205\n"
      "v_mfma_f32_16x16x32_f16 v[252:255], v[224:227], v[160:163], v[74:77]\n"
      "v_cvt_pk_bf16_f32 v246, v204, v205\n"
      "s_waitcnt lgkmcnt(0)\n"
      "v_mfma_f32_16x16x32_f16 v[252:255], v[200:203], v[136:139], v[252:255]\n"
      "v_mfma_f32_16x16x32_f16 v[252:255], v[188:191], v[44:47], v[252:255]\n"
      "ds_write_b128 v124, v[244:247]\n"
      "v_mfma_f32_16x16x32_f16 v[208:211], v[136:139], v[160:163], v[110:113]\n"
      "v_cvt_pk_bf16_f32 v180, v180, v181\n"
      "v_mfma_f32_16x16x32_f16 v[208:211], v[46:49], v[44:47], v[208:211]\n"
      "v_mfma_f32_16x16x32_f16 v[136:139], v[200:203], v[224:227], v[76:79]\n"
      "global_store_dwordx4 v117, v[248:251], s[54:55] sc1\n"
      "v_mfma_f32_16x16x32_bf16 v[244:247], v[66:69], v[164:167], 0\n"
      "v_mfma_f32_16x16x32_bf16 v[244:247], v[66:69], v[62:65], v[244:247]\n"
      "v_fmac_f32 v252, s37, v231\n"
      "s_nop 0\n"
      "v_mfma_f32_16x16x16_bf16 v[208:211], v[216:217], v[178:179], v[208:211]\n"
      "v_mfma_f32_16x16x32_bf16 v[224:227], v[240:243], v[164:167], 0\n"
      "v_cvt_pk_bf16_f32 v181, v182, v183\n"
      "v_max3_f32 v254, v144, v145, v146\n"
      "v_mfma_f32_16x16x32_bf16 v[204:207], v[192:195], v[164:167], 0\n"
      "v_mfma_f32_16x16x32_bf16 v[204:207], v[50:53], v[62:65], v[204:207]\n"
      "v_max3_f32 v254, v254, v234, v235\n"
      "v_mfma_f32_16x16x32_bf16 v[160:163], v[152:155], v[164:167], 0\n"
      "v_max3_f32 v255, v147, v232, v233\n"
      "v_max3_f32 v254, v254, v210, v211\n"
      "v_mfma_f32_16x16x32_bf16 v[160:163], v[54:57], v[62:65], v[160:163]\n"
      "v_rcp_f32 v244, v244\n"
      "v_mfma_f32_16x16x32_bf16 v[224:227], v[58:61], v[62:65], v[224:227]\n"
      "s_nop 0\n"
      "v_mul_f32 v207, v244, v207\n"
      "v_mul_f32 v206, v244, v206\n"
      "v_max3_f32 v255, v255, v208, v209\n"
      "v_mul_f32 v205, v244, v205\n"
      "v_mul_f32 v204, v244, v204\n"
      "v_max3_f32 v254, v254, v255, v252\n"
      "v_mfma_f32_16x16x16_f16 v[136:139], v[190:191], v[188:189], v[136:139]\n"
      "v_cvt_pk_bf16_f32 v204, v204, v205\n"
      "v_cvt_pk_bf16_f32 v205, v206, v207\n"
      "v_mov_b32 v255, v254\n"
      "v_mul_f32 v160, v244, v160\n"
      "v_mul_f32 v224, v244, v224\n"
      "v_mul_f32 v226, v244, v226\n"
      "v_permlane16_swap_b32 v254, v255\n"
      "v_mul_f32 v163, v244, v163\n"
      "v_mul_f32 v162, v244, v162\n"
      "v_max_f32 v254, v254, v255\n"
      "v_mul_f32 v161, v244, v161\n"
      "v_mov_b32 v255, v254\n"
      "v_mul_f32 v225, v244, v225\n"
      "v_cvt_pk_bf16_f32 v207, v162, v163\n"
      "v_permlane32_swap_b32 v254, v255\n"
      "v_cvt_pk_bf16_f32 v206, v160, v161\n"
      "v_max_f32 v254, v254, v255\n"
      "v_sub_f32 v235, v235, v254\n"
      "ds_read_b128 v[160:163], v121 offset:18432\n"
      "v_sub_f32 v252, v252, v254\n"
      "v_sub_f32 v232, v232, v254\n"
      "v_sub_f32 v144, v144, v254\n"
      "v_exp_f32 v235, v235\n"
      "v_sub_f32 v209, v209, v254\n"
      "v_sub_f32 v146, v146, v254\n"
      "v_exp_f32 v146, v146\n"
      "v_sub_f32 v233, v233, v254\n"
      "v_sub_f32 v145, v145, v254\n"
      "v_cvt_pk_bf16_f32 v224, v224, v225\n"
      "v_exp_f32 v144, v144\n"
      "v_exp_f32 v145, v145\n"
      "v_sub_f32 v147, v147, v254\n"
      "v_sub_f32 v211, v211, v254\n"
      "v_mul_f32 v227, v244, v227\n"
      "v_exp_f32_e64 v147, v147\n"
      "v_cvt_pk_bf16_f32 v144, v144, v145\n"
      "v_mbcnt_lo_u32_b32 v158, -1, 0\n"
      "v_cvt_pk_bf16_f32 v145, v146, v147\n"
      "v_exp_f32 v252, v252\n"
      "v_exp_f32 v233, v233\n"
      "v_sub_f32 v208, v208, v254\n"
      "v_exp_f32 v211, v211\n"
      "v_exp_f32 v209, v209\n"
      "v_sub_f32 v234, v234, v254\n"
      "v_exp_f32 v232, v232\n"
      "v_sub_f32 v210, v210, v254\n"
      "v_cvt_pk_bf16_f32 v146, v232, v233\n"
      "v_exp_f32_e64 v234, v234\n"
      "v_exp_f32_e64 v210, v210\n"
      "v_cndmask_b32 v213, v213, v185, s[42:43]\n"
      "v_cvt_pk_bf16_f32 v225, v226, v227\n"
      "v_cvt_pk_bf16_f32 v63, v210, v211\n"
      "v_cvt_pk_bf16_f32 v147, v234, v235\n"
      "v_exp_f32 v208, v208\n"
      "v_cvt_pk_bf16_f32 v64, v252, 0\n"
      "s_waitcnt lgkmcnt(0)\n"
      "v_cvt_pk_bf16_f32 v62, v208, v209\n"
      "v_mfma_f32_16x16x32_bf16 v[232:235], v[66:69], v[144:147], 0\n"
      "ds_read_b128 v[248:251], v123 offset:22528\n"
      "v_cndmask_b32 v213, v213, v253, s[44:45]\n"
      "v_mfma_f32_16x16x32_bf16 v[232:235], v[66:69], v[62:65], v[232:235]\n"
      "ds_read_b128 v[140:143], v123 offset:19456\n"
      "v_cndmask_b32 v213, v213, v137, s[46:47]\n"
      "v_mfma_f32_16x16x32_bf16 v[252:255], v[192:195], v[144:147], 0\n"
      "ds_read_b128 v[220:223], v123 offset:25600\n"
      "v_mbcnt_hi_u32_b32 v158, -1, v158\n"
      "v_mfma_f32_16x16x32_bf16 v[252:255], v[50:53], v[62:65], v[252:255]\n"
      "ds_write_b128 v124, v[204:207] offset:1024\n"
      "v_mfma_f32_16x16x32_bf16 v[136:139], v[152:155], v[144:147], 0\n"
      "ds_read_b128 v[200:203], v123 offset:28672\n"
      "v_min_u32 v158, 48, v158\n"
      "v_mfma_f32_16x16x32_bf16 v[136:139], v[54:57], v[62:65], v[136:139]\n"
      "ds_write2st64_b64 v125, v[180:181], v[224:225] offset0:0 offset1:2\n"
      "v_mfma_f32_16x16x32_bf16 v[208:211], v[240:243], v[144:147], 0\n"
      "v_lshlrev_b32 v158, 1, v158\n"
      "v_add_u32 v158, s30, v158\n"
      "v_mfma_f32_16x16x32_bf16 v[208:211], v[58:61], v[62:65], v[208:211]\n"
      "s_mov_b32 exec_hi, 0\n"
      "global_store_dwordx4 v118, v[160:163], s[54:55] sc1\n"
      "s_mov_b32 exec_hi, -1\n"
      "s_waitcnt lgkmcnt(4)\n"
      "v_mfma_f32_16x16x32_bf16 v[244:247], a[216:219], v[140:143], 0\n"
      "ds_read_u16 v156, v158 offset:4704\n"
      "s_add_u32 s28, s27, 2\n"
      "v_mfma_f32_16x16x32_bf16 v[244:247], a[228:231], v[248:251], v[244:247]\n"
      "ds_read_b128 v[160:163], v123 offset:31744\n"
      "v_mfma_f32_16x16x32_bf16 v[144:147], a[220:223], v[140:143], 0\n"
      "s_min_u32 m0, s28, s3\n"
      "s_nop 0\n"
      "s_movrels_b32 s9, s80\n"
      "v_mfma_f32_16x16x32_bf16 v[204:207], a[224:227], v[140:143], 0\n"
      "ds_read_b128 v[140:143], v123 offset:34816\n"
      "s_lshr_b32 s8, s9, 8\n"
      "v_mfma_f32_16x16x32_bf16 v[144:147], a[232:235], v[248:251], v[144:147]\n"
      "s_and_b32 s9, s9, 0xff\n"
      "s_waitcnt lgkmcnt(4)\n"
      "v_mfma_f32_16x16x32_bf16 v[204:207], a[236:239], v[248:251], v[204:207]\n"
      "ds_read_b128 v[248:251], v123 offset:20480\n"
      "v_mfma_f32_16x16x32_bf16 v[244:247], a[240:243], v[220:223], v[244:247]\n"
      "s_mul_i32 s10, s8, 73\n"
      "s_add_u32 s10, s10, s9\n"
      "v_mfma_f32_16x16x32_bf16 v[244:247], a[252:255], v[200:203], v[244:247]\n"
      "s_mul_i32 s28, s10, 4802\n"
      "s_add_u32 s56, s18, s28\n"
      "s_addc_u32 s57, s19, 0\n"
      "v_mfma_f32_16x16x32_bf16 v[144:147], a[244:247], v[220:223], v[144:147]\n"
      "s_and_b32 s56, s56, -16\n"
      "s_waitcnt lgkmcnt(3)\n"
      "v_lshlrev_b32 v156, 16, v156\n"
      "v_mfma_f32_16x16x32_bf16 v[144:147], v[0:3], v[200:203], v[144:147]\n"
      "ds_read_b128 v[188:191], v123 offset:26624\n"
      "v_fmac_f32_e64 v213, s37, v156\n"
      "v_mfma_f32_16x16x32_bf16 v[204:207], a[248:251], v[220:223], v[204:207]\n"
      "ds_read_b128 v[220:223], v123 offset:23552\n"
      "v_max_f32_dpp v156, v213, v213 row_ror:8 row_mask:0xf bank_mask:0xf\n"
      "v_mfma_f32_16x16x32_bf16 v[204:207], v[4:7], v[200:203], v[204:207]\n"
      "s_nop 0\n"
      "v_max_f32_dpp v156, v156, v156 row_ror:4 row_mask:0xf bank_mask:0xf\n"
      "s_waitcnt lgkmcnt(3)\n"
      "s_sub_u32 s58, s56, 110592\n"
      "v_max_f32_dpp v156, v156, v156 row_ror:2 row_mask:0xf bank_mask:0xf\n"
      "v_mfma_f32_16x16x32_bf16 v[244:247], v[8:11], v[160:163], v[244:247]\n"
      "s_subb_u32 s59, s57, 0\n"
      "s_sub_u32 s56, s56, 73728\n"
      "v_max_f32_dpp v156, v156, v156 row_ror:1 row_mask:0xf bank_mask:0xf\n"
      "v_mfma_f32_16x16x32_bf16 v[244:247], v[20:23], v[140:143], v[244:247]\n"
      "s_subb_u32 s57, s57, 0\n"
      "s_mul_i32 s28, s8, 1373568\n"
      "v_max_f32_dpp v156, v156, v156 row_bcast:15 row_mask:0xa bank_mask:0xf\n"
      "v_mfma_f32_16x16x32_bf16 v[144:147], v[12:15], v[160:163], v[144:147]\n"
      "s_nop 0\n"
      "v_max_f32_dpp v156, v156, v156 row_bcast:31 row_mask:0xc bank_mask:0xf\n"
      "s_mul_i32 s29, s9, 2688\n"
      "v_mfma_f32_16x16x32_bf16 v[144:147], v[24:27], v[140:143], v[144:147]\n"
      "v_readlane_b32 s31, v156, 63\n"
      "s_add_u32 s28, s28, s29\n"
      "v_mfma_f32_16x16x32_bf16 v[204:207], v[16:19], v[160:163], v[204:207]\n"
      "s_cmp_eq_u32 s8, 72\n"
      "s_cselect_b32 s8, 0, 100270464\n"
      "v_subrev_f32 v213, s31, v213\n"
      "v_mfma_f32_16x16x32_bf16 v[204:207], v[28:31], v[140:143], v[204:207]\n"
      "v_exp_f32_e64 v213, v213\n"
      "s_waitcnt lgkmcnt(0)\n"
      "v_mfma_f32_16x16x32_bf16 v[160:163], a[216:219], v[248:251], 0\n"
      "s_cmp_eq_u32 s9, 72\n"
      "s_cselect_b32 s9, 0, 196224\n"
      "v_cvt_pk_bf16_f32 v213, v213, v213\n"
      "v_mfma_f32_16x16x32_bf16 v[160:163], a[228:231], v[220:223], v[160:163]\n"
      "ds_write_b16 v130, v213\n"
      "v_mfma_f32_16x16x32_bf16 v[224:227], a[224:227], v[248:251], 0\n"
      "ds_read_b128 v[212:215], v123 offset:29696\n"
      "v_fma_f32 v156, v244, v244, s39\n"
      "v_mfma_f32_16x16x32_bf16 v[140:143], a[220:223], v[248:251], 0\n"
      "ds_read_b128 v[172:175], v131 offset:64\n"
      "v_fmac_f32_e64 v156, v245, v245\n"
      "v_mfma_f32_16x16x32_bf16 v[140:143], a[232:235], v[220:223], v[140:143]\n"
      "ds_read_b128 v[176:179], v131\n"
      "v_fmac_f32 v156, v246, v246\n"
      "v_mfma_f32_16x16x32_bf16 v[224:227], a[236:239], v[220:223], v[224:227]\n"
      "s_add_u32 s10, s8, s9\n"
      "v_fmac_f32 v156, v247, v247\n"
      "ds_read_b128 v[228:231], v123 offset:32768\n"
      "v_fmac_f32 v156, v144, v144\n"
      "s_add_u32 s28, s28, s10\n"
      "v_fmac_f32 v156, v145, v145\n"
      "v_fmac_f32 v156, v146, v146\n"
      "s_add_u32 s52, s16, s28\n"
      "v_fmac_f32 v156, v147, v147\n"
      "v_fmac_f32 v156, v204, v204\n"
      "v_fmac_f32 v156, v205, v205\n"
      "v_fmac_f32 v156, v206, v206\n"
      "s_addc_u32 s53, s17, 0\n"
      "s_add_u32 s27, s27, 1\n"
      "ds_read_b128 v[220:223], v115 offset:18432\n"
      "ds_read_b128 v[248:251], v114 offset:18432\n"
      "v_fmac_f32 v156, v207, v207\n"
      "s_waitcnt lgkmcnt(5)\n"
      "v_mfma_f32_16x16x32_bf16 v[160:163], a[240:243], v[188:191], v[160:163]\n"
      "ds_read_b128 v[216:219], v123 offset:35840\n"
      "s_cmp_ge_u32 s27, s26\n"
      "v_mfma_f32_16x16x32_bf16 v[160:163], a[252:255], v[212:215], v[160:163]\n"
      "v_mfma_f32_16x16x32_bf16 v[140:143], a[244:247], v[188:191], v[140:143]\n"
      "ds_read_b128 v[200:203], v114 offset:24576\n"
      "s_cselect_b32 s28, 1, 0\n"
      "v_mfma_f32_16x16x32_bf16 v[140:143], v[0:3], v[212:215], v[140:143]\n"
      "s_waitcnt lgkmcnt(5)\n"
      "v_mfma_f32_16x16x32_bf16 v[224:227], a[248:251], v[188:191], v[224:227]\n"
      "s_nop 0\n"
      "v_mfma_f32_16x16x32_bf16 v[224:227], v[4:7], v[212:215], v[224:227]\n"
      "ds_read_b128 v[212:215], v115 offset:24576\n"
      "s_waitcnt lgkmcnt(3)\n"
      "v_mfma_f32_16x16x32_bf16 v[188:191], a[0:3], v[248:251], 0\n"
      "v_mfma_f32_16x16x32_bf16 v[188:191], a[36:39], v[220:223], v[188:191]\n"
      "s_waitcnt lgkmcnt(2)\n"
      "v_mfma_f32_16x16x32_bf16 v[160:163], v[8:11], v[228:231], v[160:163]\n"
      "v_mfma_f32_16x16x32_bf16 v[160:163], v[20:23], v[216:219], v[160:163]\n"
      "s_xor_b32 s29, s10, s11\n"
      "v_permlane16_swap_b32 v159, v156\n"
      "v_mfma_f32_16x16x32_bf16 v[140:143], v[12:15], v[228:231], v[140:143]\n"
      "v_mfma_f32_16x16x32_bf16 v[140:143], v[24:27], v[216:219], v[140:143]\n"
      "s_waitcnt lgkmcnt(0)\n"
      "v_mfma_f32_16x16x32_bf16 v[224:227], v[16:19], v[228:231], v[224:227]\n"
      "v_add_f32 v159, v159, v156\n"
      "v_mfma_f32_16x16x32_bf16 v[224:227], v[28:31], v[216:219], v[224:227]\n"
      "v_mfma_f32_16x16x32_bf16 v[216:219], v[176:179], v[66:69], 0\n"
      "s_or_b32 s28, s28, s29\n"
      "v_fma_f32 v158, v160, v160, s39\n"
      "v_fmac_f32 v158, v161, v161\n"
      "v_mfma_f32_16x16x32_bf16 v[216:219], v[172:175], v[66:69], v[216:219]\n"
      "v_fmac_f32 v158, v162, v162\n"
      "v_fmac_f32 v158, v163, v163\n"
      "v_mfma_f32_16x16x32_bf16 v[180:183], v[176:179], v[192:195], 0\n"
      "v_fmac_f32 v158, v140, v140\n"
      "v_mfma_f32_16x16x32_bf16 v[180:183], v[172:175], v[50:53], v[180:183]\n"
      "v_fmac_f32 v158, v141, v141\n"
      "v_fmac_f32 v158, v142, v142\n"
      "v_mfma_f32_16x16x32_bf16 v[228:231], v[176:179], v[152:155], 0\n"
      "v_fmac_f32 v158, v143, v143\n"
      "v_fmac_f32 v158, v224, v224\n"
      "v_mfma_f32_16x16x32_bf16 v[228:231], v[172:175], v[54:57], v[228:231]\n"
      "v_fmac_f32 v158, v225, v225\n"
      "v_fmac_f32 v158, v226, v226\n"
      "v_mfma_f32_16x16x32_bf16 v[154:157], v[176:179], v[240:243], 0\n"
      "v_fmac_f32 v158, v227, v227\n"
      "v_mov_b32 v217, v158\n"
      "v_mfma_f32_16x16x32_bf16 v[154:157], v[172:175], v[58:61], v[154:157]\n"
      "s_nop 0\n"
      "v_permlane16_swap_b32 v158, v217\n"
      "v_mfma_f32_16x16x32_bf16 v[172:175], a[4:7], v[248:251], 0\n"
      "v_add_f32 v158, v158, v217\n"
      "v_cndmask_b32 v180, v180, v228, s[42:43]\n"
      "v_mfma_f32_16x16x32_bf16 v[172:175], a[40:43], v[220:223], v[172:175]\n"
      "v_permlane32_swap_b32 v159, v158\n"
      "v_mfma_f32_16x16x32_bf16 v[176:179], a[8:11], v[248:251], 0\n"
      "v_cndmask_b32 v180, v180, v154, s[44:45]\n"
      "v_add_f32 v159, v159, v158\n"
      "v_mfma_f32_16x16x32_bf16 v[176:179], a[44:47], v[220:223], v[176:179]\n"
      "ds_write_b32 v129, v159\n"
      "v_mfma_f32_16x16x32_bf16 v[228:231], a[12:15], v[248:251], 0\n"
      "v_mov_b32_dpp v216, v216 quad_perm:[0,0,0,0] row_mask:0xf bank_mask:0xf\n"
      "v_mfma_f32_16x16x32_bf16 v[228:231], a[48:51], v[220:223], v[228:231]\n"
      "v_rcp_f32 v216, v216\n"
      "v_mfma_f32_16x16x32_bf16 v[156:159], a[16:19], v[248:251], 0\n"
      "v_mul_f32 v180, v216, v180\n"
      "v_cvt_pk_bf16_f32 v180, v180, v180\n"
      "v_mfma_f32_16x16x32_bf16 v[156:159], a[52:55], v[220:223], v[156:159]\n"
      "ds_write_b16 v135, v180\n"
      "v_mfma_f32_16x16x32_bf16 v[216:219], a[20:23], v[248:251], 0\n"
      "s_nop 0\n"
      "v_mfma_f32_16x16x32_bf16 v[216:219], a[56:59], v[220:223], v[216:219]\n"
      "s_branch L_join_30_%=\n"
      "L_flg_160_%=:\n"
      "v_cmp_gt_i32 vcc, 0, v244\n"
      "s_cbranch_vccz L_fb_161_%=\n"
      "s_sleep 1\n"
      "ds_read_b128 v[244:247], v128 offset:3072\n"
      "s_waitcnt lgkmcnt(0)\n"
      "v_bitop3_b32 v244, v244, v245, s4 bitop3:0x7e\n"
      "v_bitop3_b32 v246, v246, v247, s4 bitop3:0x7e\n"
      "v_or_b32 v244, v244, v246\n"
      "v_cmp_ne_u32 s[48:49], 0, v244\n"
      "s_branch L_flg_160_%=\n"
      "L_edge_1_%=:\n"
      "s_mov_b32 s11, s10\n"
      "s_sub_u32 s28, s60, s8\n"
      "s_sub_u32 s29, s61, s9\n"
      "s_mov_b32 s60, s8\n"
      "s_mov_b32 s61, s9\n"
      "s_mov_b64 exec, s[64:65]\n"
      "v_add_u32 v116, s28, v116\n"
      "s_mov_b64 exec, s[70:71]\n"
      "v_add_u32 v116, s29, v116\n"
      "s_mov_b64 exec, s[66:67]\n"
      "v_add_u32 v117, s28, v117\n"
      "s_mov_b64 exec, s[72:73]\n"
      "v_add_u32 v117, s29, v117\n"
      "s_mov_b64 exec, s[68:69]\n"
      "v_add_u32 v118, s28, v118\n"
      "s_mov_b64 exec, s[74:75]\n"
      "v_add_u32 v118, s29, v118\n"
      "s_mov_b64 exec, -1\n"
      "s_nop 4\n"
      "s_branch L_eb_2_%=\n"
      "L_flg_3_%=:\n"
      "v_cmp_gt_i32 vcc, 0, v192\n"
      "s_cbranch_vccz L_fb_4_%=\n"
      "s_sleep 1\n"
      "ds_read_b128 v[192:195], v128 offset:2048\n"
      "s_waitcnt lgkmcnt(0)\n"
      "v_bitop3_b32 v192, v192, v193, s4 bitop3:0x7e\n"
      "v_bitop3_b32 v194, v194, v195, s4 bitop3:0x7e\n"
      "v_or_b32 v192, v192, v194\n"
      "v_cmp_ne_u32 s[48:49], 0, v192\n"
      "s_branch L_flg_3_%=\n"
      "L_edge_6_%=:\n"
      "s_mov_b32 s11, s10\n"
      "s_sub_u32 s28, s60, s8\n"
      "s_sub_u32 s29, s61, s9\n"
      "s_mov_b32 s60, s8\n"
      "s_mov_b32 s61, s9\n"
      "s_mov_b64 exec, s[64:65]\n"
      "v_add_u32 v116, s28, v116\n"
      "s_mov_b64 exec, s[70:71]\n"
      "v_add_u32 v116, s29, v116\n"
      "s_mov_b64 exec, s[66:67]\n"
      "v_add_u32 v117, s28, v117\n"
      "s_mov_b64 exec, s[72:73]\n"
      "v_add_u32 v117, s29, v117\n"
      "s_mov_b64 exec, s[68:69]\n"
      "v_add_u32 v118, s28, v118\n"
      "s_mov_b64 exec, s[74:75]\n"
      "v_add_u32_e64 v118, s29, v118\n"
      "s_mov_b64 exec, -1\n"
      "s_nop 4\n"
      "s_branch L_eb_7_%=\n"
      "L_flg_12_%=:\n"
      "v_cmp_gt_i32 vcc, 0, v228\n"
      "s_cbranch_vccz L_fb_13_%=\n"
      "s_sleep 1\n"
      "ds_read_b128 v[228:231], v128 offset:3072\n"
      "s_waitcnt lgkmcnt(0)\n"
      "v_bitop3_b32 v228, v228, v229, s4 bitop3:0x7e\n"
      "v_bitop3_b32 v230, v230, v231, s4 bitop3:0x7e\n"
      "v_or_b32 v228, v228, v230\n"
      "v_cmp_ne_u32 s[48:49], 0, v228\n"
      "s_branch L_flg_12_%=\n"
      "L_edge_15_%=:\n"
      "s_mov_b32 s11, s10\n"
      "s_sub_u32 s28, s60, s8\n"
      "s_sub_u32 s29, s61, s9\n"
      "s_mov_b32 s60, s8\n"
      "s_mov_b32 s61, s9\n"
      "s_mov_b64 exec, s[64:65]\n"
      "v_add_u32 v116, s28, v116\n"
      "s_mov_b64 exec, s[70:71]\n"
      "v_add_u32 v116, s29, v116\n"
      "s_mov_b64 exec, s[66:67]\n"
      "v_add_u32 v117, s28, v117\n"
      "s_mov_b64 exec, s[72:73]\n"
      "v_add_u32 v117, s29, v117\n"
      "s_mov_b64 exec, s[68:69]\n"
      "v_add_u32 v118, s28, v118\n"
      "s_mov_b64 exec, s[74:75]\n"
      "v_add_u32 v118, s29, v118\n"
      "s_mov_b64 exec, -1\n"
      "s_nop 4\n"
      "s_branch L_eb_16_%=\n"
      "L_edge_17_%=:\n"
      "s_mov_b32 s11, s10\n"
      "s_sub_u32 s28, s60, s8\n"
      "s_sub_u32 s29, s61, s9\n"
      "s_mov_b32 s60, s8\n"
      "s_mov_b32 s61, s9\n"
      "s_mov_b64 exec, s[64:65]\n"
      "v_add_u32 v116, s28, v116\n"
      "s_mov_b64 exec, s[70:71]\n"
      "v_add_u32 v116, s29, v116\n"
      "s_mov_b64 exec, s[66:67]\n"
      "v_add_u32 v117, s28, v117\n"
      "s_mov_b64 exec, s[72:73]\n"
      "v_add_u32 v117, s29, v117\n"
      "s_mov_b64 exec, s[68:69]\n"
      "v_add_u32 v118, s28, v118\n"
      "s_mov_b64 exec, s[74:75]\n"
      "v_add_u32 v118, s29, v118\n"
      "s_mov_b64 exec, -1\n"
      "s_nop 4\n"
      "s_branch L_eb_18_%=\n"
      "L_flg_23_%=:\n"
      "v_cmp_gt_i32 vcc, 0, v156\n"
      "s_cbranch_vccz L_fb_24_%=\n"
      "s_sleep 1\n"
      "ds_read_b128 v[156:159], v128 offset:2048\n"
      "s_waitcnt lgkmcnt(0)\n"
      "v_bitop3_b32 v156, v156, v157, s4 bitop3:0x7e\n"
      "v_bitop3_b32 v158, v158, v159, s4 bitop3:0x7e\n"
      "v_or_b32 v156, v156, v158\n"
      "v_cmp_ne_u32 s[48:49], 0, v156\n"
      "s_branch L_flg_23_%=\n"
      "L_edge_26_%=:\n"
      "s_mov_b32 s11, s10\n"
      "s_sub_u32 s28, s60, s8\n"
      "s_sub_u32 s29, s61, s9\n"
      "s_mov_b32 s60, s8\n"
      "s_mov_b32 s61, s9\n"
      "s_mov_b64 exec, s[64:65]\n"
      "v_add_u32 v116, s28, v116\n"
      "s_mov_b64 exec, s[70:71]\n"
      "v_add_u32 v116, s29, v116\n"
      "s_mov_b64 exec, s[66:67]\n"
      "v_add_u32 v117, s28, v117\n"
      "s_mov_b64 exec, s[72:73]\n"
      "v_add_u32 v117, s29, v117\n"
      "s_mov_b64 exec, s[68:69]\n"
      "v_add_u32 v118, s28, v118\n"
      "s_mov_b64 exec, s[74:75]\n"
      "v_add_u32 v118, s29, v118\n"
      "s_mov_b64 exec, -1\n"
      "s_nop 4\n"
      "s_branch L_eb_27_%=\n"
      "L_flg_32_%=:\n"
      "v_cmp_gt_i32 vcc, 0, v164\n"
      "s_cbranch_vccz L_fb_33_%=\n"
      "s_sleep 1\n"
      "ds_read_b128 v[164:167], v128 offset:3072\n"
      "s_waitcnt lgkmcnt(0)\n"
      "v_bitop3_b32 v164, v164, v165, s4 bitop3:0x7e\n"
      "v_bitop3_b32 v166, v166, v167, s4 bitop3:0x7e\n"
      "v_or_b32 v164, v164, v166\n"
      "v_cmp_ne_u32 s[48:49], 0, v164\n"
      "s_branch L_flg_32_%=\n"
      "L_edge_36_%=:\n"
      "s_mov_b32 s11, s10\n"
      "s_sub_u32 s28, s60, s8\n"
      "s_sub_u32 s29, s61, s9\n"
      "s_mov_b32 s60, s8\n"
      "s_mov_b32 s61, s9\n"
      "s_mov_b64 exec, s[64:65]\n"
      "v_add_u32 v116, s28, v116\n"
      "s_mov_b64 exec, s[70:71]\n"
      "v_add_u32 v116, s29, v116\n"
      "s_mov_b64 exec, s[66:67]\n"
      "v_add_u32 v117, s28, v117\n"
      "s_mov_b64 exec, s[72:73]\n"
      "v_add_u32 v117, s29, v117\n"
      "s_mov_b64 exec, s[68:69]\n"
      "v_add_u32 v118, s28, v118\n"
      "s_mov_b64 exec, s[74:75]\n"
      "v_add_u32 v118, s29, v118\n"
      "s_mov_b64 exec, -1\n"
      "s_nop 4\n"
      "s_branch L_eb_37_%=\n"
      "L_edge_38_%=:\n"
      "s_mov_b32 s11, s10\n"
      "s_sub_u32 s28, s60, s8\n"
      "s_sub_u32 s29, s61, s9\n"
      "s_mov_b32 s60, s8\n"
      "s_mov_b32 s61, s9\n"
      "s_mov_b64 exec, s[64:65]\n"
      "v_add_u32 v116, s28, v116\n"
      "s_mov_b64 exec, s[70:71]\n"
      "v_add_u32 v116, s29, v116\n"
      "s_mov_b64 exec, s[66:67]\n"
      "v_add_u32 v117, s28, v117\n"
      "s_mov_b64 exec, s[72:73]\n"
      "v_add_u32 v117, s29, v117\n"
      "s_mov_b64 exec, s[68:69]\n"
      "v_add_u32 v118, s28, v118\n"
      "s_mov_b64 exec, s[74:75]\n"
      "v_add_u32 v118, s29, v118\n"
      "s_mov_b64 exec, -1\n"
      "s_nop 4\n"
      "s_branch L_eb_39_%=\n"
      "L_edge_40_%=:\n"
      "s_mov_b32 s11, s10\n"
      "s_sub_u32 s28, s60, s8\n"
      "s_sub_u32 s29, s61, s9\n"
      "s_mov_b32 s60, s8\n"
      "s_mov_b32 s61, s9\n"
      "s_mov_b64 exec, s[64:65]\n"
      "v_add_u32 v116, s28, v116\n"
      "s_mov_b64 exec, s[70:71]\n"
      "v_add_u32 v116, s29, v116\n"
      "s_mov_b64 exec, s[66:67]\n"
      "v_add_u32 v117, s28, v117\n"
      "s_mov_b64 exec, s[72:73]\n"
      "v_add_u32 v117, s29, v117\n"
      "s_mov_b64 exec, s[68:69]\n"
      "v_add_u32 v118, s28, v118\n"
      "s_mov_b64 exec, s[74:75]\n"
      "v_add_u32 v118, s29, v118\n"
      "s_mov_b64 exec, -1\n"
      "s_nop 4\n"
      "s_branch L_eb_41_%=\n"
      "L_edge_44_%=:\n"
      "s_mov_b32 s11, s10\n"
      "s_sub_u32 s28, s60, s8\n"
      "s_sub_u32 s29, s61, s9\n"
      "s_mov_b32 s60, s8\n"
      "s_mov_b32 s61, s9\n"
      "s_mov_b64 exec, s[64:65]\n"
      "v_add_u32 v116, s28, v116\n"
      "s_mov_b64 exec, s[70:71]\n"
      "v_add_u32 v116, s29, v116\n"
      "s_mov_b64 exec, s[66:67]\n"
      "v_add_u32 v117, s28, v117\n"
      "s_mov_b64 exec, s[72:73]\n"
      "v_add_u32 v117, s29, v117\n"
      "s_mov_b64 exec, s[68:69]\n"
      "v_add_u32 v118, s28, v118\n"
      "s_mov_b64 exec, s[74:75]\n"
      "v_add_u32 v118, s29, v118\n"
      "s_mov_b64 exec, -1\n"
      "s_nop 4\n"
      "s_branch L_eb_45_%=\n"
      "L_edge_46_%=:\n"
      "s_mov_b32 s11, s10\n"
      "s_sub_u32 s28, s60, s8\n"
      "s_sub_u32 s29, s61, s9\n"
      "s_mov_b32 s60, s8\n"
      "s_mov_b32 s61, s9\n"
      "s_mov_b64 exec, s[64:65]\n"
      "v_add_u32 v116, s28, v116\n"
      "s_mov_b64 exec, s[70:71]\n"
      "v_add_u32 v116, s29, v116\n"
      "s_mov_b64 exec, s[66:67]\n"
      "v_add_u32 v117, s28, v117\n"
      "s_mov_b64 exec, s[72:73]\n"
      "v_add_u32 v117, s29, v117\n"
      "s_mov_b64 exec, s[68:69]\n"
      "v_add_u32 v118, s28, v118\n"
      "s_mov_b64 exec, s[74:75]\n"
      "v_add_u32 v118, s29, v118\n"
      "s_mov_b64 exec, -1\n"
      "s_nop 4\n"
      "s_branch L_eb_47_%=\n"
      "L_edge_48_%=:\n"
      "s_mov_b32 s11, s10\n"
      "s_sub_u32 s28, s60, s8\n"
      "s_sub_u32 s29, s61, s9\n"
      "s_mov_b32 s60, s8\n"
      "s_mov_b32 s61, s9\n"
      "s_mov_b64 exec, s[64:65]\n"
      "v_add_u32 v116, s28, v116\n"
      "s_mov_b64 exec, s[70:71]\n"
      "v_add_u32 v116, s29, v116\n"
      "s_mov_b64 exec, s[66:67]\n"
      "v_add_u32 v117, s28, v117\n"
      "s_mov_b64 exec, s[72:73]\n"
      "v_add_u32 v117, s29, v117\n"
      "s_mov_b64 exec, s[68:69]\n"
      "v_add_u32 v118, s28, v118\n"
      "s_mov_b64 exec, s[74:75]\n"
      "v_add_u32 v118, s29, v118\n"
      "s_mov_b64 exec, -1\n"
      "s_nop 4\n"
      "s_branch L_eb_49_%=\n"
      "L_cend_%=:\n"
      :
      : "s"(__builtin_amdgcn_kernarg_segment_ptr()), "s"(wave), "s"(blockIdx.x), "s"(CT_TAB), "s"(lds0)
      : "v0","v1","v2","v3","v4","v5","v6","v7","v8","v9","v10","v11","v12","v13","v14","v15","v16","v17","v18","v19","v20","v21","v22","v23","v24","v25","v26","v27","v28","v29","v30","v31","v32","v33","v34","v35","v36","v37","v38","v39","v40","v41","v42","v43","v44","v45","v46","v47","v48","v49","v50","v51","v52","v53","v54","v55","v56","v57","v58","v59","v60","v61","v62","v63","v64","v65","v66","v67","v68","v69","v70","v71","v72","v73","v74","v75","v76","v77","v78","v79","v80","v81","v82","v83","v84","v85","v86","v87","v88","v89","v90","v91","v92","v93","v94","v95","v96","v97","v98","v99","v100","v101","v102","v103","v104","v105","v106","v107","v108","v109","v110","v111","v112","v113","v114","v115","v116","v117","v118","v119","v120","v121","v122","v123","v124","v125","v126","v127","v128","v129","v130","v131","v132","v133","v134","v135","v136","v137","v138","v139","v140","v141","v142","v143","v144","v145","v146","v147","v148","v149","v150","v151","v152","v153","v154","v155","v156","v157","v158","v159","v160","v161","v162","v163","v164","v165","v166","v167","v168","v169","v170","v171","v172","v173","v174","v175","v176","v177","v178","v179","v180","v181","v182","v183","v184","v185","v186","v187","v188","v189","v190","v191","v192","v193","v194","v195","v196","v197","v198","v199","v200","v201","v202","v203","v204","v205","v206","v207","v208","v209","v210","v211","v212","v213","v214","v215","v216","v217","v218","v219","v220","v221","v222","v223","v224","v225","v226","v227","v228","v229","v230","v231","v232","v233","v234","v235","v236","v237","v238","v239","v240","v241","v242","v243","v244","v245","v246","v247","v248","v249","v250","v251","v252","v253","v254","v255","a0","a1","a2","a3","a4","a5","a6","a7","a8","a9","a10","a11","a12","a13","a14","a15","a16","a17","a18","a19","a20","a21","a22","a23","a24","a25","a26","a27","a28","a29","a30","a31","a32","a33","a34","a35","a36","a37","a38","a39","a40","a41","a42","a43","a44","a45","a46","a47","a48","a49","a50","a51","a52","a53","a54","a55","a56","a57","a58","a59","a60","a61","a62","a63","a64","a65","a66","a67","a68","a69","a70","a71","a72","a73","a74","a75","a76","a77","a78","a79","a80","a81","a82","a83","a84","a85","a86","a87","a88","a89","a90","a91","a92","a93","a94","a95","a96","a97","a98","a99","a100","a101","a102","a103","a104","a105","a106","a107","a108","a109","a110","a111","a112","a113","a114","a115","a116","a117","a118","a119","a120","a121","a122","a123","a124","a125","a126","a127","a128","a129","a130","a131","a132","a133","a134","a135","a136","a137","a138","a139","a140","a141","a142","a143","a144","a145","a146","a147","a148","a149","a150","a151","a152","a153","a154","a155","a156","a157","a158","a159","a160","a161","a162","a163","a164","a165","a166","a167","a168","a169","a170","a171","a172","a173","a174","a175","a176","a177","a178","a179","a180","a181","a182","a183","a184","a185","a186","a187","a188","a189","a190","a191","a192","a193","a194","a195","a196","a197","a198","a199","a200","a201","a202","a203","a204","a205","a206","a207","a208","a209","a210","a211","a212","a213","a214","a215","a216","a217","a218","a219","a220","a221","a222","a223","a224","a225","a226","a227","a228","a229","a230","a231","a232","a233","a234","a235","a236","a237","a238","a239","a240","a241","a242","a243","a244","a245","a246","a247","a248","a249","a250","a251","a252","a253","a254","a255","s2","s3","s4","s5","s6","s7","s8","s9","s10","s11","s12","s13","s16","s17","s18","s19","s20","s21","s22","s23","s24","s25","s26","s27","s28","s29","s30","s31","s34","s35","s36","s37","s38","s39","s40","s41","s42","s43","s44","s45","s46","s47","s48","s49","s50","s51","s52","s53","s54","s55","s60","s61","s62","s63","s64","s65","s66","s67","s68","s69","s70","s71","s72","s73","s74","s75","s76","s77","s78","s79","s80","s81","s82","s83","s84","s85","s86","s87","s88","s89","s90","s91","s92","s93","s94","s95","s96","s97","s98","s99","s100","s101", "vcc", "scc", "memory");
}

}

extern "C" void kernel_launch(void* const* d_in, const int* in_sizes, int n_in, void* d_out, int out_size, void* d_ws,
                              size_t ws_size, hipStream_t stream) {
  const uint16_t* x = (const uint16_t*)d_in[0];
  const uint16_t* wqkv = (const uint16_t*)d_in[1];
  const uint16_t* wo = (const uint16_t*)d_in[2];
  const uint16_t* ls = (const uint16_t*)d_in[3];
  const uint16_t* w1 = (const uint16_t*)d_in[4];
  const uint16_t* w2 = (const uint16_t*)d_in[5];
  const uint16_t* rc = (const uint16_t*)d_in[6];
  const int* ridx = (const int*)d_in[7];
  const uint16_t* mask = (const uint16_t*)d_in[8];
  swin::swin_main<<<256, 256, 0, stream>>>(x, mask, (char*)d_ws, (uint16_t*)d_out, wqkv, wo, ls, w1, w2, rc, ridx);
}
```

```cpp
#include <hip/hip_runtime.h>
#include <hip/hip_bf16.h>
#include <stdint.h>

namespace swin {

constexpr unsigned LDS_BYTES = 163840u;

__device__ __attribute__((aligned(256))) const unsigned CT_TAB[26704] = {0,64,0,0xc0000,144,208,16,0xc0010,288,352,32,0xc0020,432,496,48,0xc0030,576,512,64,0xc0040,720,656,80,0xc0050,864,800,96,0xc0060,1008,944,112,0xc0070,1024,1088,400,0xc0190,1168,1232,384,0xc0180,1312,1376,432,0xc01b0,1456,1520,416,0xc01a0,1600,1536,464,0xc01d0,1744,1680,448,0xc01c0,1888,1824,496,0xc01f0,2032,1968,480,0xc01e0,16,80,800,0xc0320,128,192,816,0xc0330,304,368,768,0xc0300,416,480,784,0xc0310,592,528,864,0xc0360,704,640,880,0xc0370,880,816,832,0xc0340,992,928,848,0xc0350,1040,1104,1200,0xef8b0,1152,1216,1184,0xef8a0,1328,1392,1168,0xef890,1440,1504,1152,0xef880,1616,1552,1264,0xef8f0,1728,1664,1248,0xef8e0,1904,1840,1232,0xef8d0,2016,1952,1216,0xef8c0,32,96,1600,0xefa40,176,240,1616,0xefa50,256,320,1632,0xefa60,400,464,1648,0xefa70,608,544,1536,0xefa00,752,688,1552,0xefa10,832,768,1568,0xefa20,976,912,1584,0xefa30,1056,1120,2000,0xefbd0,1200,1264,1984,0xefbc0,1280,1344,2032,0xefbf0,1424,1488,2016,0xefbe0,1632,1568,1936,0xefb90,1776,1712,1920,0xefb80,1856,1792,1968,0xefbb0,2000,1936,1952,0xefba0,48,112,2400,0xefd60,160,224,2416,0xefd70,272,336,2368,0xefd40,384,448,2384,0xefd50,624,560,2336,0xefd20,736,672,2352,0xefd30,848,784,2304,0xefd00,960,896,2320,0xefd10,1072,1136,0x2fef0,0xefef0,1184,1248,0x2fee0,0xefee0,1296,1360,0x2fed0,0xefed0,1408,1472,0x2fec0,0xefec0,1648,1584,0x2feb0,0xefeb0,1760,1696,0x2fea0,0xefea0,1872,1808,0x2fe90,0xefe90,1984,1920,0x2fe80,0xefe80,0xc0100,73728,77824,90112,0xc0110,73744,77840,90128,0xc0120,73760,77856,90144,0xc0130,73776,77872,90160,0xc0140,73792,77888,90176,0xc0150,73808,77904,90192,0xc0160,73824,77920,90208,0xc0170,73840,77936,90224,0xc0290,73856,77952,90240,0xc0280,73872,77968,90256,0xc02b0,73888,77984,90272,0xc02a0,73904,78000,90288,0xc02d0,73920,78016,90304,0xc02c0,73936,78032,90320,0xc02f0,73952,78048,90336,0xc02e0,73968,78064,90352,0xc0420,73984,78080,90368,0xc0430,74000,78096,90384,0xc0400,74016,78112,90400,0xc0410,74032,78128,90416,0xc0460,74048,78144,90432,0xc0470,74064,78160,90448,0xc0440,74080,78176,90464,0xc0450,74096,78192,90480,0xef9b0,74112,78208,90496,0xef9a0,74128,78224,90512,0xef990,74144,78240,90528,0xef980,74160,78256,90544,0xef9f0,74176,78272,90560,0xef9e0,74192,78288,90576,0xef9d0,74208,78304,90592,0xef9c0,74224,78320,90608,0xc0100,74240,78336,90624,0xc0110,74256,78352,90640,0xc0120,74272,78368,90656,0xc0130,74288,78384,90672,0xc0140,74304,78400,90688,0xc0150,74320,78416,90704,0xc0160,74336,78432,90720,0xc0170,74352,78448,90736,0xc0290,74368,78464,90752,0xc0280,74384,78480,90768,0xc02b0,74400,78496,90784,0xc02a0,74416,78512,90800,0xc02d0,74432,78528,90816,0xc02c0,74448,78544,90832,0xc02f0,74464,78560,90848,0xc02e0,74480,78576,90864,0xc0420,74496,78592,90880,0xc0430,74512,78608,90896,0xc0400,74528,78624,90912,0xc0410,74544,78640,90928,0xc0460,74560,78656,90944,0xc0470,74576,78672,90960,0xc0440,74592,78688,90976,0xc0450,74608,78704,90992,0xef9b0,74624,78720,91008,0xef9a0,74640,78736,91024,0xef990,74656,78752,91040,0xef980,74672,78768,91056,0xef9f0,74688,78784,91072,0xef9e0,74704,78800,91088,0xef9d0,74720,78816,91104,0xef9c0,74736,78832,91120,0x1c000,36864,36864,49152,0x1c004,36880,36880,49168,0x1c008,36896,36896,49184,0x1c00c,36912,36912,49200,0x1c010,36928,36928,49216,0x1c014,36944,36944,49232,0x1c018,36960,36960,49248,0x1c01c,36976,36976,49264,0x1c020,36992,36992,49280,0x1c024,37008,37008,49296,0x1c028,37024,37024,49312,0x1c02c,37040,37040,49328,0x1c030,37056,37056,49344,0x1c034,37072,37072,49360,0x1c038,37088,37088,49376,0x1c03c,37104,37104,49392,0x1c040,37120,37120,49408,0x1c044,37136,37136,49424,0x1c048,37152,37152,49440,0x1c04c,37168,37168,49456,0x1c050,37184,37184,49472,0x1c054,37200,37200,49488,0x1c058,37216,37216,49504,0x1c05c,37232,37232,49520,0x1c060,37248,37248,49536,0x1c064,37264,37264,49552,0x1c068,37280,37280,49568,0x1c06c,37296,37296,49584,0x1c070,37312,37312,49600,0x1c074,37328,37328,49616,0x1c078,37344,37344,49632,0x1c07c,37360,37360,49648,0x1c080,37376,37376,49664,0x1c084,37392,37392,49680,0x1c088,37408,37408,49696,0x1c08c,37424,37424,49712,0x1c090,37440,37440,49728,0x1c094,37456,37456,49744,0x1c098,37472,37472,49760,0x1c09c,37488,37488,49776,0x1c0a0,37504,37504,49792,0x1c0a4,37520,37520,49808,0x1c0a8,37536,37536,49824,0x1c0ac,37552,37552,49840,0x1c0b0,37568,37568,49856,0x1c0b4,37584,37584,49872,0x1c0b8,37600,37600,49888,0x1c0bc,37616,37616,49904,0x1c0c0,37632,37632,49920,0x1c0c4,37648,37648,49936,0x1c0c8,37664,37664,49952,0x1c0cc,37680,37680,49968,0x1c0d0,37696,37696,49984,0x1c0d4,37712,37712,50000,0x1c0d8,37728,37728,50016,0x1c0dc,37744,37744,50032,0x1c0e0,37760,37760,50048,0x1c0e4,37776,37776,50064,0x1c0e8,37792,37792,50080,0x1c0ec,37808,37808,50096,0x1c0f0,37824,37824,50112,0x1c0f4,37840,37840,50128,0x1c0f8,37856,37856,50144,0x1c0fc,37872,37872,50160,73728,73792,0x1d600,0x1d600,73872,73936,0x1d610,0x1d610,74016,74080,0x1d620,0x1d620,74160,74224,0x1d630,0x1d630,74304,74240,0x1d640,0x1d640,74448,74384,0x1d650,0x1d650,74592,74528,0x1d660,0x1d660,74736,74672,0x1d670,0x1d670,74752,74816,0x1d680,0x1d680,74896,74960,0x1d690,0x1d690,75040,75104,0x1d6a0,0x1d6a0,75184,75248,0x1d6b0,0x1d6b0,75328,75264,0x1d6c0,0x1d6c0,75472,75408,0x1d6d0,0x1d6d0,75616,75552,0x1d6e0,0x1d6e0,75760,75696,0x1d6f0,0x1d6f0,73744,73800,0x1d700,0x1d700,73856,73944,0x1d710,0x1d710,74032,74088,0x1d720,0x1d720,74144,74232,0x1d730,0x1d730,74320,74248,0x1d740,0x1d740,74432,74392,0x1d750,0x1d750,74608,74536,0x1d760,0x1d760,74720,74680,0x1d770,0x1d770,74768,74824,0x1d780,0x1d780,74880,74968,0x1d790,0x1d790,75056,75112,0x1d7a0,0x1d7a0,75168,75256,0x1d7b0,0x1d7b0,75344,75272,0x1d7c0,0x1d7c0,75456,75416,0x1d7d0,0x1d7d0,75632,75560,0x1d7e0,0x1d7e0,75744,75704,0x1d7f0,0x1d7f0,73760,73808,0x1d800,0x1d800,73904,73920,0x1d810,0x1d810,73984,74096,0x1d820,0x1d820,74128,74208,0x1d830,0x1d830,74336,74256,0x1d840,0x1d840,74480,74368,0x1d850,0x1d850,74560,74544,0x1d860,0x1d860,74704,74656,0x1d870,0x1d870,74784,74832,0x1d880,0x1d880,74928,74944,0x1d890,0x1d890,75008,75120,0x1d8a0,0x1d8a0,75152,75232,0x1d8b0,0x1d8b0,75360,75280,0x1d8c0,0x1d8c0,75504,75392,0x1d8d0,0x1d8d0,75584,75568,0x1d8e0,0x1d8e0,75728,75680,0x1d8f0,0x1d8f0,73776,73816,0x1d900,0x1d900,73888,73928,0x1d910,0x1d910,74000,74104,0x1d920,0x1d920,74112,74216,0x1d930,0x1d930,74352,74264,0x1d940,0x1d940,74464,74376,0x1d950,0x1d950,74576,74552,0x1d960,0x1d960,74688,74664,0x1d970,0x1d970,74800,74840,0x1d980,0x1d980,74912,74952,0x1d990,0x1d990,75024,75128,0x1d9a0,0x1d9a0,75136,75240,0x1d9b0,0x1d9b0,75376,75288,0x1d9c0,0x1d9c0,75488,75400,0x1d9d0,0x1d9d0,75600,75576,0x1d9e0,0x1d9e0,75712,75688,0x1d9f0,0x1d9f0,0x1e600,0x1e600,0x1e800,0x100000,0x1e602,0x100000,0x100000,0x1e800,0x1e604,0x100000,0x100000,0x100000,0x1e606,0x100000,0x100000,0x100000,0x1e610,0x1e600,0x100000,0x100000,0x1e612,0x100000,0x100000,0x1e800,0x1e614,0x100000,0x100000,0x100000,0x1e616,0x100000,0x100000,0x100000,0x1e620,0x1e600,0x100000,0x100000,0x1e622,0x100000,0x100000,0x1e800,0x1e624,0x100000,0x100000,0x100000,0x1e626,0x100000,0x100000,0x100000,0x1e630,0x1e600,0x100000,0x100000,0x1e632,0x100000,0x100000,0x1e800,0x1e634,0x100000,0x100000,0x100000,0x1e636,0x100000,0x100000,0x100000,0x1e608,0x1e610,0x1e830,0x100000,0x1e60a,0x100000,0x100000,0x1e830,0x1e60c,0x100000,0x100000,0x100000,0x1e60e,0x100000,0x100000,0x100000,0x1e618,0x1e610,0x100000,0x100000,0x1e61a,0x100000,0x100000,0x1e830,0x1e61c,0x100000,0x100000,0x100000,0x1e61e,0x100000,0x100000,0x100000,0x1e628,0x1e610,0x100000,0x100000,0x1e62a,0x100000,0x100000,0x1e830,0x1e62c,0x100000,0x100000,0x100000,0x1e62e,0x100000,0x100000,0x100000,0x1e638,0x1e610,0x100000,0x100000,0x1e63a,0x100000,0x100000,0x1e830,0x1e63c,0x100000,0x100000,0x100000,0x1e63e,0x100000,0x100000,0x100000,0x1e640,0x1e620,0x1e860,0x100000,0x1e642,0x100000,0x100000,0x1e860,0x1e644,0x100000,0x100000,0x100000,0x1e646,0x100000,0x100000,0x100000,0x1e650,0x1e620,0x100000,0x100000,0x1e652,0x100000,0x100000,0x1e860,0x1e654,0x100000,0x100000,0x100000,0x1e656,0x100000,0x100000,0x100000,0x1e660,0x1e620,0x100000,0x100000,0x1e662,0x100000,0x100000,0x1e860,0x1e664,0x100000,0x100000,0x100000,0x1e666,0x100000,0x100000,0x100000,0x1e670,0x1e620,0x100000,0x100000,0x1e672,0x100000,0x100000,0x1e860,0x1e674,0x100000,0x100000,0x100000,0x1e676,0x100000,0x100000,0x100000,0x1e648,0x1e630,0x1e890,0x100000,0x1e64a,0x100000,0x100000,0x1e890,0x1e64c,0x100000,0x100000,0x100000,0x1e64e,0x100000,0x100000,0x100000,0x1e658,0x1e630,0x100000,0x100000,0x1e65a,0x100000,0x100000,0x1e890,0x1e65c,0x100000,0x100000,0x100000,0x1e65e,0x100000,0x100000,0x100000,0x1e668,0x1e630,0x100000,0x100000,0x1e66a,0x100000,0x100000,0x1e890,0x1e66c,0x100000,0x100000,0x100000,0x1e66e,0x100000,0x100000,0x100000,0x1e678,0x1e630,0x100000,0x100000,0x1e67a,0x100000,0x100000,0x1e890,0x1e67c,0x100000,0x100000,0x100000,0x1e67e,0x100000,0x100000,0x100000,0x1eb00,0x24c70,36864,36928,0x1eb02,0x24c72,36880,36936,0x1eb04,0x24c74,36896,36944,0x1eb06,0x24c76,36912,36952,0x1eb08,0x24dd0,37248,37312,0x1eb0a,0x24dd2,37264,37320,0x1eb0c,0x24dd4,37280,37328,0x1eb0e,0x24dd6,37296,37336,0x1eb10,0x24f30,37664,37728,0x1eb12,0x24f32,37680,37736,0x1eb14,0x24f34,37632,37744,0x1eb16,0x24f36,37648,37752,0x1eb18,0x25090,38048,38112,0x1eb1a,0x25092,38064,38120,0x1eb1c,0x25094,38016,38128,0x1eb1e,0x25096,38032,38136,0x1eb00,0x24c78,38464,38400,0x1eb02,0x24c7a,38480,38408,0x1eb04,0x24c7c,38496,38416,0x1eb06,0x24c7e,38512,38424,0x1eb08,0x24dd8,38848,38784,0x1eb0a,0x24dda,38864,38792,0x1eb0c,0x24ddc,38880,38800,0x1eb0e,0x24dde,38896,38808,0x1eb10,0x24f38,39264,39200,0x1eb12,0x24f3a,39280,39208,0x1eb14,0x24f3c,39232,39216,0x1eb16,0x24f3e,39248,39224,0x1eb18,0x25098,39648,39584,0x1eb1a,0x2509a,39664,39592,0x1eb1c,0x2509c,39616,39600,0x1eb1e,0x2509e,39632,39608,0x1eb00,0x26270,39936,40000,0x1eb02,0x26272,39952,40008,0x1eb04,0x26274,39968,40016,0x1eb06,0x26276,39984,40024,0x1eb08,0x263d0,40320,40384,0x1eb0a,0x263d2,40336,40392,0x1eb0c,0x263d4,40352,40400,0x1eb0e,0x263d6,40368,40408,0x1eb10,0x26530,40736,40800,0x1eb12,0x26532,40752,40808,0x1eb14,0x26534,40704,40816,0x1eb16,0x26536,40720,40824,0x1eb18,0x26690,41120,41184,0x1eb1a,0x26692,41136,41192,0x1eb1c,0x26694,41088,41200,0x1eb1e,0x26696,41104,41208,0x1eb00,0x100000,41536,41472,0x1eb02,0x100000,41552,41480,0x1eb04,0x100000,41568,41488,0x1eb06,0x100000,41584,41496,0x1eb08,0x100000,41920,41856,0x1eb0a,0x100000,41936,41864,0x1eb0c,0x100000,41952,41872,0x1eb0e,0x100000,41968,41880,0x1eb10,0x100000,42336,42272,0x1eb12,0x100000,42352,42280,0x1eb14,0x100000,42304,42288,0x1eb16,0x100000,42320,42296,0x1eb18,0x100000,42720,42656,0x1eb1a,0x100000,42736,42664,0x1eb1c,0x100000,42688,42672,0x1eb1e,0x100000,42704,42680,36864,36896,36928,36864,36872,36904,36936,36880,36880,36912,36944,36896,36888,36920,36952,36912,38016,38048,38080,36928,38024,38056,38088,36944,38032,38064,38096,36960,38040,38072,38104,36976,39200,39168,39264,36992,39208,39176,39272,37008,39216,39184,39280,37024,39224,39192,39288,37040,40352,40320,40416,37056,40360,40328,40424,37072,40368,40336,40432,37088,40376,40344,40440,37104,46144,46176,46080,37120,46152,46184,46088,37136,46160,46192,46096,37152,46168,46200,46104,37168,47296,47328,47232,37184,47304,47336,47240,37200,47312,47344,47248,37216,47320,47352,47256,37232,48480,48448,48416,37248,48488,48456,48424,37264,48496,48464,48432,37280,48504,48472,48440,37296,49632,49600,49568,37312,49640,49608,49576,37328,49648,49616,49584,37344,49656,49624,49592,37360,55296,55328,55360,37376,55304,55336,55368,37392,55312,55344,55376,37408,55320,55352,55384,37424,56448,56480,56512,37440,56456,56488,56520,37456,56464,56496,56528,37472,56472,56504,56536,37488,57632,57600,57696,37504,57640,57608,57704,37520,57648,57616,57712,37536,57656,57624,57720,37552,58784,58752,58848,37568,58792,58760,58856,37584,58800,58768,58864,37600,58808,58776,58872,37616,64576,64608,64512,37632,64584,64616,64520,37648,64592,64624,64528,37664,64600,64632,64536,37680,65728,65760,65664,37696,65736,65768,65672,37712,65744,65776,65680,37728,65752,65784,65688,37744,66912,66880,66848,37760,66920,66888,66856,37776,66928,66896,66864,37792,66936,66904,66872,37808,68064,68032,68000,37824,68072,68040,68008,37840,68080,68048,68016,37856,68088,68056,68024,37872,0x1e800,0x1e800,0x20cc0,0,0x1e8c4,0x1e8c4,0x20cc4,16,0x1e988,0x1e988,0x20cc8,32,0x1ea4c,0x1ea4c,0x20ccc,48,0x1eb10,0x1eb10,0x20cd0,64,0x1ebd4,0x1ebd4,0x20cd4,80,0x1ec98,0x1ec98,0x20cd8,96,0x1ed5c,0x1ed5c,0x20cdc,112,0x1ee20,0x1ee20,0x20ce0,384,0x1eee4,0x1eee4,0x20ce4,400,0x1efa8,0x1efa8,0x20ce8,416,0x1f06c,0x1f06c,0x20cec,432,0x1f130,0x1f130,0x20cf0,448,0x1f1f4,0x1f1f4,0x20cf4,464,0x1f2b8,0x1f2b8,0x20cf8,480,0x1f37c,0x1f37c,0x20cfc,496,0x1e810,0x1e800,0x20cc0,768,0x1e8d4,0x1e8c4,0x20cc4,784,0x1e998,0x1e988,0x20cc8,800,0x1ea5c,0x1ea4c,0x20ccc,816,0x1eb20,0x1eb10,0x20cd0,832,0x1ebe4,0x1ebd4,0x20cd4,848,0x1eca8,0x1ec98,0x20cd8,864,0x1ed6c,0x1ed5c,0x20cdc,880,0x1ee30,0x1ee20,0x20ce0,1152,0x1eef4,0x1eee4,0x20ce4,1168,0x1efb8,0x1efa8,0x20ce8,1184,0x1f07c,0x1f06c,0x20cec,1200,0x1f140,0x1f130,0x20cf0,1216,0x1f204,0x1f1f4,0x20cf4,1232,0x1f2c8,0x1f2b8,0x20cf8,1248,0x1f38c,0x1f37c,0x20cfc,1264,0x1e820,0x1e800,0x20cc0,1536,0x1e8e4,0x1e8c4,0x20cc4,1552,0x1e9a8,0x1e988,0x20cc8,1568,0x1ea6c,0x1ea4c,0x20ccc,1584,0x1eb30,0x1eb10,0x20cd0,1600,0x1ebf4,0x1ebd4,0x20cd4,1616,0x1ecb8,0x1ec98,0x20cd8,1632,0x1ed7c,0x1ed5c,0x20cdc,1648,0x1ee40,0x1ee20,0x20ce0,1920,0x1ef04,0x1eee4,0x20ce4,1936,0x1efc8,0x1efa8,0x20ce8,1952,0x1f08c,0x1f06c,0x20cec,1968,0x1f150,0x1f130,0x20cf0,1984,0x1f214,0x1f1f4,0x20cf4,2000,0x1f2d8,0x1f2b8,0x20cf8,2016,0x1f39c,0x1f37c,0x20cfc,2032,0x1e830,0x1e800,0x20cc0,2304,0x1e8f4,0x1e8c4,0x20cc4,2320,0x1e9b8,0x1e988,0x20cc8,2336,0x1ea7c,0x1ea4c,0x20ccc,2352,0x1eb40,0x1eb10,0x20cd0,2368,0x1ec04,0x1ebd4,0x20cd4,2384,0x1ecc8,0x1ec98,0x20cd8,2400,0x1ed8c,0x1ed5c,0x20cdc,2416,0x1ee50,0x1ee20,0x20ce0,2688,0x1ef14,0x1eee4,0x20ce4,2704,0x1efd8,0x1efa8,0x20ce8,2720,0x1f09c,0x1f06c,0x20cec,2736,0x1f160,0x1f130,0x20cf0,2752,0x1f224,0x1f1f4,0x20cf4,2768,0x1f2e8,0x1f2b8,0x20cf8,2784,0x1f3ac,0x1f37c,0x20cfc,2800,36864,0x26d70,0x1e800,0x1eb00,36880,0x26d74,0x1ec90,0x1ef90,36896,0x26d78,0x1f120,0x1f420,36912,0x26d7c,0x1f5b0,0x1f8b0,36928,0x26d80,0x1fa40,0x1fd40,36944,0x26d84,0x1fed0,0x201d0,36960,0x26d88,0x20360,0x20660,36976,0x26d8c,0x207f0,0x20af0,37248,0x26d90,0x20c80,0x20f80,37264,0x26d94,0x21110,0x21410,37280,0x26d98,0x215a0,0x218a0,37296,0x26d9c,0x21a30,0x21d30,37312,0x26da0,0x21ec0,0x221c0,37328,0x26da4,0x22350,0x22650,37344,0x26da8,0x227e0,0x22ae0,37360,0x26dac,0x22c70,0x22f70,37664,0x26db0,0x1e830,0x1eb08,37680,0x26db4,0x1ecc0,0x1ef98,37632,0x26db8,0x1f150,0x1f428,37648,0x26dbc,0x1f5e0,0x1f8b8,37728,0x26dc0,0x1fa70,0x1fd48,37744,0x26dc4,0x1ff00,0x201d8,37696,0x26dc8,0x20390,0x20668,37712,0x26dcc,0x20820,0x20af8,38048,0x26dd0,0x20cb0,0x20f88,38064,0x26dd4,0x21140,0x21418,38016,0x26dd8,0x215d0,0x218a8,38032,0x26ddc,0x21a60,0x21d38,38112,0x26de0,0x21ef0,0x221c8,38128,0x26de4,0x22380,0x22658,38080,0x26de8,0x22810,0x22ae8,38096,0x26dec,0x22ca0,0x22f78,38464,0x26df0,0x1e860,0x1eb10,38480,0x26df4,0x1ecf0,0x1efa0,38496,0x26df8,0x1f180,0x1f430,38512,0x26dfc,0x1f610,0x1f8c0,38400,0x26e00,0x1faa0,0x1fd50,38416,0x26e04,0x1ff30,0x201e0,38432,0x26e08,0x203c0,0x20670,38448,0x26e0c,0x20850,0x20b00,38848,0x26e10,0x20ce0,0x20f90,38864,0x26e14,0x21170,0x21420,38880,0x26e18,0x21600,0x218b0,38896,0x26e1c,0x21a90,0x21d40,38784,0x26e20,0x21f20,0x221d0,38800,0x26e24,0x223b0,0x22660,38816,0x26e28,0x22840,0x22af0,38832,0x26e2c,0x22cd0,0x22f80,39264,0x26e30,0x1e890,0x1eb18,39280,0x26e34,0x1ed20,0x1efa8,39232,0x26e38,0x1f1b0,0x1f438,39248,0x26e3c,0x1f640,0x1f8c8,39200,0x26e40,0x1fad0,0x1fd58,39216,0x26e44,0x1ff60,0x201e8,39168,0x26e48,0x203f0,0x20678,39184,0x26e4c,0x20880,0x20b08,39648,0x26e50,0x20d10,0x20f98,39664,0x26e54,0x211a0,0x21428,39616,0x26e58,0x21630,0x218b8,39632,0x26e5c,0x21ac0,0x21d48,39584,0x26e60,0x21f50,0x221d8,39600,0x26e64,0x223e0,0x22668,39552,0x26e68,0x22870,0x22af8,39568,0x26e6c,0x22d00,0x22f88,0x3f803f80,0x3f803f80,16256,0,0,0x3f803f80,0x3f800000,0,0,0x3f803f80,0,16256,0,0x3f803f80,0,0x3f800000,0x3f803f80,0,0,0,0,0,0,0,0,0,0,0,0,0,0,0,0x3f803f80,0,0,0,0,0,0,0,0,0,0,0,0,0,0,0,0x3f803f80,0,0,0,0,0,0,0,0,0,0,0,0,0,0,0,0x3f803f80,0,0,0,0,0,0,0,0,0,0,0,0,0,0,0,0x3f803f80,0x3f803f80,16256,0,0,0x3f803f80,0x3f800000,0,0,0x3f803f80,0,16256,0,0x3f803f80,0,0x3f800000,0x3f803f80,0,0,0,0,0,0,0,0,0,0,0,0,0,0,0,0x3f803f80,0,0,0,0,0,0,0,0,0,0,0,0,0,0,0,0x3f803f80,0,0,0,0,0,0,0,0,0,0,0,0,0,0,0,0x3f803f80,0,0,0,0,0,0,0,0,0,0,0,0,0,0,0,0x3f803f80,0x3f803f80,16256,0,0,0x3f803f80,0x3f800000,0,0,0x3f803f80,0,16256,0,0x3f803f80,0,0x3f800000,0x3f803f80,0,0,0,0,0,0,0,0,0,0,0,0,0,0,0,0x3f803f80,0,0,0,0,0,0,0,0,0,0,0,0,0,0,0,0x3f803f80,0,0,0,0,0,0,0,0,0,0,0,0,0,0,0,0x3f803f80,0,0,0,0,0,0,0,0,0,0,0,0,0,0,0,0x3f803f80,0x3f803f80,16256,0,0,0x3f803f80,0x3f800000,0,0,0x3f803f80,0,16256,0,0x3f803f80,0,0x3f800000,0,64,0x30000,0xf0000,144,208,0x30010,0xf0010,288,352,0x30020,0xf0020,432,496,0x30030,0xf0030,576,512,0x30040,0xf0040,720,656,0x30050,0xf0050,864,800,0x30060,0xf0060,1008,944,0x30070,0xf0070,1024,1088,0x30190,0xf0190,1168,1232,0x30180,0xf0180,1312,1376,0x301b0,0xf01b0,1456,1520,0x301a0,0xf01a0,1600,1536,0x301d0,0xf01d0,1744,1680,0x301c0,0xf01c0,1888,1824,0x301f0,0xf01f0,2032,1968,0x301e0,0xf01e0,16,80,0x30320,0x11f720,128,192,0x30330,0x11f730,304,368,0x30300,0x11f700,416,480,0x30310,0x11f710,592,528,0x30360,0x11f760,704,640,0x30370,0x11f770,880,816,0x30340,0x11f740,992,928,0x30350,0x11f750,1040,1104,0x304b0,0x11f8b0,1152,1216,0x304a0,0x11f8a0,1328,1392,0x30490,0x11f890,1440,1504,0x30480,0x11f880,1616,1552,0x304f0,0x11f8f0,1728,1664,0x304e0,0x11f8e0,1904,1840,0x304d0,0x11f8d0,2016,1952,0x304c0,0x11f8c0,32,96,0x30640,0x11fa40,176,240,0x30650,0x11fa50,256,320,0x30660,0x11fa60,400,464,0x30670,0x11fa70,608,544,0x30600,0x11fa00,752,688,0x30610,0x11fa10,832,768,0x30620,0x11fa20,976,912,0x30630,0x11fa30,1056,1120,0x307d0,0x11fbd0,1200,1264,0x307c0,0x11fbc0,1280,1344,0x307f0,0x11fbf0,1424,1488,0x307e0,0x11fbe0,1632,1568,0x30790,0x11fb90,1776,1712,0x30780,0x11fb80,1856,1792,0x307b0,0x11fbb0,2000,1936,0x307a0,0x11fba0,48,112,0x5fd60,0x11fd60,160,224,0x5fd70,0x11fd70,272,336,0x5fd40,0x11fd40,384,448,0x5fd50,0x11fd50,624,560,0x5fd20,0x11fd20,736,672,0x5fd30,0x11fd30,848,784,0x5fd00,0x11fd00,960,896,0x5fd10,0x11fd10,1072,1136,0x5fef0,0x11fef0,1184,1248,0x5fee0,0x11fee0,1296,1360,0x5fed0,0x11fed0,1408,1472,0x5fec0,0x11fec0,1648,1584,0x5feb0,0x11feb0,1760,1696,0x5fea0,0x11fea0,1872,1808,0x5fe90,0x11fe90,1984,1920,0x5fe80,0x11fe80,0xefb40,74752,78848,90624,0xefb50,74768,78864,90640,0xefb60,74784,78880,90656,0xefb70,74800,78896,90672,0xefb00,74816,78912,90688,0xefb10,74832,78928,90704,0xefb20,74848,78944,90720,0xefb30,74864,78960,90736,0xefcd0,74880,78976,90752,0xefcc0,74896,78992,90768,0xefcf0,74912,79008,90784,0xefce0,74928,79024,90800,0xefc90,74944,79040,90816,0xefc80,74960,79056,90832,0xefcb0,74976,79072,90848,0xefca0,74992,79088,90864,0xefe60,75008,79104,90880,0xefe70,75024,79120,90896,0xefe40,75040,79136,90912,0xefe50,75056,79152,90928,0xefe20,75072,79168,90944,0xefe30,75088,79184,90960,0xefe00,75104,79200,90976,0xefe10,75120,79216,90992,0xefff0,75136,79232,91008,0xeffe0,75152,79248,91024,0xeffd0,75168,79264,91040,0xeffc0,75184,79280,91056,0xeffb0,75200,79296,91072,0xeffa0,75216,79312,91088,0xeff90,75232,79328,91104,0xeff80,75248,79344,91120,0xefb40,75264,79360,91136,0xefb50,75280,79376,91152,0xefb60,75296,79392,91168,0xefb70,75312,79408,91184,0xefb00,75328,79424,91200,0xefb10,75344,79440,91216,0xefb20,75360,79456,91232,0xefb30,75376,79472,91248,0xefcd0,75392,79488,91264,0xefcc0,75408,79504,91280,0xefcf0,75424,79520,91296,0xefce0,75440,79536,91312,0xefc90,75456,79552,91328,0xefc80,75472,79568,91344,0xefcb0,75488,79584,91360,0xefca0,75504,79600,91376,0xefe60,75520,79616,91392,0xefe70,75536,79632,91408,0xefe40,75552,79648,91424,0xefe50,75568,79664,91440,0xefe20,75584,79680,91456,0xefe30,75600,79696,91472,0xefe00,75616,79712,91488,0xefe10,75632,79728,91504,0xefff0,75648,79744,91520,0xeffe0,75664,79760,91536,0xeffd0,75680,79776,91552,0xeffc0,75696,79792,91568,0xeffb0,75712,79808,91584,0xeffa0,75728,79824,91600,0xeff90,75744,79840,91616,0xeff80,75760,79856,91632,0x1c0a0,36864,39936,49160,0x1c0a4,36880,39952,49176,0x1c0a8,36896,39968,49192,0x1c0ac,36912,39984,49208,0x1c0b0,36928,40000,49224,0x1c0b4,36944,40016,49240,0x1c0b8,36960,40032,49256,0x1c0bc,36976,40048,49272,0x1c0c0,36992,40064,49288,0x1c0c4,37008,40080,49304,0x1c0c8,37024,40096,49320,0x1c0cc,37040,40112,49336,0x1c0d0,37056,40128,49352,0x1c0d4,37072,40144,49368,0x1c0d8,37088,40160,49384,0x1c0dc,37104,40176,49400,0x1c0e0,37120,40192,49416,0x1c0e4,37136,40208,49432,0x1c0e8,37152,40224,49448,0x1c0ec,37168,40240,49464,0x1c0f0,37184,40256,49480,0x1c0f4,37200,40272,49496,0x1c0f8,37216,40288,49512,0x1c0fc,37232,40304,49528,0x1c100,37248,40320,49544,0x1c104,37264,40336,49560,0x1c108,37280,40352,49576,0x1c10c,37296,40368,49592,0x1c110,37312,40384,49608,0x1c114,37328,40400,49624,0x1c118,37344,40416,49640,0x1c11c,37360,40432,49656,0x1c120,37376,40448,49672,0x1c124,37392,40464,49688,0x1c128,37408,40480,49704,0x1c12c,37424,40496,49720,0x1c130,37440,40512,49736,0x1c134,37456,40528,49752,0x1c138,37472,40544,49768,0x1c13c,37488,40560,49784,0x1c140,37504,40576,49800,0x1c144,37520,40592,49816,0x1c148,37536,40608,49832,0x1c14c,37552,40624,49848,0x1c150,37568,40640,49864,0x1c154,37584,40656,49880,0x1c158,37600,40672,49896,0x1c15c,37616,40688,49912,0x1c160,37632,40704,49928,0x1c164,37648,40720,49944,0x1c168,37664,40736,49960,0x1c16c,37680,40752,49976,0x1c170,37696,40768,49992,0x1c174,37712,40784,50008,0x1c178,37728,40800,50024,0x1c17c,37744,40816,50040,0x1c180,37760,40832,50056,0x1c184,37776,40848,50072,0x1c188,37792,40864,50088,0x1c18c,37808,40880,50104,0x1c190,37824,40896,50120,0x1c194,37840,40912,50136,0x1c198,37856,40928,50152,0x1c19c,37872,40944,50168,73824,79904,0x1d600,0x1d604,73968,80048,0x1d610,0x1d614,74048,80128,0x1d620,0x1d624,74192,80272,0x1d630,0x1d634,74272,80480,0x1d640,0x1d644,74416,80624,0x1d650,0x1d654,74496,80704,0x1d660,0x1d664,74640,80848,0x1d670,0x1d674,74848,80928,0x1d680,0x1d684,74992,81072,0x1d690,0x1d694,75072,81152,0x1d6a0,0x1d6a4,75216,81296,0x1d6b0,0x1d6b4,75296,81504,0x1d6c0,0x1d6c4,75440,81648,0x1d6d0,0x1d6d4,75520,81728,0x1d6e0,0x1d6e4,75664,81872,0x1d6f0,0x1d6f4,73840,79912,0x1d700,0x1d704,73952,80056,0x1d710,0x1d714,74064,80136,0x1d720,0x1d724,74176,80280,0x1d730,0x1d734,74288,80488,0x1d740,0x1d744,74400,80632,0x1d750,0x1d754,74512,80712,0x1d760,0x1d764,74624,80856,0x1d770,0x1d774,74864,80936,0x1d780,0x1d784,74976,81080,0x1d790,0x1d794,75088,81160,0x1d7a0,0x1d7a4,75200,81304,0x1d7b0,0x1d7b4,75312,81512,0x1d7c0,0x1d7c4,75424,81656,0x1d7d0,0x1d7d4,75536,81736,0x1d7e0,0x1d7e4,75648,81880,0x1d7f0,0x1d7f4,79872,79920,0x1d800,0x1d804,80016,80032,0x1d810,0x1d814,80160,80144,0x1d820,0x1d824,80304,80256,0x1d830,0x1d834,80448,80496,0x1d840,0x1d844,80592,80608,0x1d850,0x1d854,80736,80720,0x1d860,0x1d864,80880,80832,0x1d870,0x1d874,80896,80944,0x1d880,0x1d884,81040,81056,0x1d890,0x1d894,81184,81168,0x1d8a0,0x1d8a4,81328,81280,0x1d8b0,0x1d8b4,81472,81520,0x1d8c0,0x1d8c4,81616,81632,0x1d8d0,0x1d8d4,81760,81744,0x1d8e0,0x1d8e4,81904,81856,0x1d8f0,0x1d8f4,79888,79928,0x1d900,0x1d904,80000,80040,0x1d910,0x1d914,80176,80152,0x1d920,0x1d924,80288,80264,0x1d930,0x1d934,80464,80504,0x1d940,0x1d944,80576,80616,0x1d950,0x1d954,80752,80728,0x1d960,0x1d964,80864,80840,0x1d970,0x1d974,80912,80952,0x1d980,0x1d984,81024,81064,0x1d990,0x1d994,81200,81176,0x1d9a0,0x1d9a4,81312,81288,0x1d9b0,0x1d9b4,81488,81528,0x1d9c0,0x1d9c4,81600,81640,0x1d9d0,0x1d9d4,81776,81752,0x1d9e0,0x1d9e4,81888,81864,0x1d9f0,0x1d9f4,0x1e680,0x1e680,0x1e8c0,0x100000,0x1e682,0x100000,0x100000,0x1e8c0,0x1e684,0x100000,0x100000,0x100000,0x1e686,0x100000,0x100000,0x100000,0x1e690,0x1e680,0x100000,0x100000,0x1e692,0x100000,0x100000,0x1e8c0,0x1e694,0x100000,0x100000,0x100000,0x1e696,0x100000,0x100000,0x100000,0x1e6a0,0x1e680,0x100000,0x100000,0x1e6a2,0x100000,0x100000,0x1e8c0,0x1e6a4,0x100000,0x100000,0x100000,0x1e6a6,0x100000,0x100000,0x100000,0x1e6b0,0x1e680,0x100000,0x100000,0x1e6b2,0x100000,0x100000,0x1e8c0,0x1e6b4,0x100000,0x100000,0x100000,0x1e6b6,0x100000,0x100000,0x100000,0x1e688,0x1e690,0x1e8f0,0x100000,0x1e68a,0x100000,0x100000,0x1e8f0,0x1e68c,0x100000,0x100000,0x100000,0x1e68e,0x100000,0x100000,0x100000,0x1e698,0x1e690,0x100000,0x100000,0x1e69a,0x100000,0x100000,0x1e8f0,0x1e69c,0x100000,0x100000,0x100000,0x1e69e,0x100000,0x100000,0x100000,0x1e6a8,0x1e690,0x100000,0x100000,0x1e6aa,0x100000,0x100000,0x1e8f0,0x1e6ac,0x100000,0x100000,0x100000,0x1e6ae,0x100000,0x100000,0x100000,0x1e6b8,0x1e690,0x100000,0x100000,0x1e6ba,0x100000,0x100000,0x1e8f0,0x1e6bc,0x100000,0x100000,0x100000,0x1e6be,0x100000,0x100000,0x100000,0x1e6c0,0x1e6a0,0x1e920,0x100000,0x1e6c2,0x100000,0x100000,0x1e920,0x1e6c4,0x100000,0x100000,0x100000,0x1e6c6,0x100000,0x100000,0x100000,0x1e6d0,0x1e6a0,0x100000,0x100000,0x1e6d2,0x100000,0x100000,0x1e920,0x1e6d4,0x100000,0x100000,0x100000,0x1e6d6,0x100000,0x100000,0x100000,0x1e6e0,0x1e6a0,0x100000,0x100000,0x1e6e2,0x100000,0x100000,0x1e920,0x1e6e4,0x100000,0x100000,0x100000,0x1e6e6,0x100000,0x100000,0x100000,0x1e6f0,0x1e6a0,0x100000,0x100000,0x1e6f2,0x100000,0x100000,0x1e920,0x1e6f4,0x100000,0x100000,0x100000,0x1e6f6,0x100000,0x100000,0x100000,0x1e6c8,0x1e6b0,0x1e950,0x100000,0x1e6ca,0x100000,0x100000,0x1e950,0x1e6cc,0x100000,0x100000,0x100000,0x1e6ce,0x100000,0x100000,0x100000,0x1e6d8,0x1e6b0,0x100000,0x100000,0x1e6da,0x100000,0x100000,0x1e950,0x1e6dc,0x100000,0x100000,0x100000,0x1e6de,0x100000,0x100000,0x100000,0x1e6e8,0x1e6b0,0x100000,0x100000,0x1e6ea,0x100000,0x100000,0x1e950,0x1e6ec,0x100000,0x100000,0x100000,0x1e6ee,0x100000,0x100000,0x100000,0x1e6f8,0x1e6b0,0x100000,0x100000,0x1e6fa,0x100000,0x100000,0x1e950,0x1e6fc,0x100000,0x100000,0x100000,0x1e6fe,0x100000,0x100000,0x100000,0x1eb60,0x251f0,36960,37024,0x1eb62,0x251f2,36976,37032,0x1eb64,0x251f4,36992,37040,0x1eb66,0x251f6,37008,37048,0x1eb68,0x25350,37344,37408,0x1eb6a,0x25352,37360,37416,0x1eb6c,0x25354,37376,37424,0x1eb6e,0x25356,37392,37432,0x1eb70,0x254b0,37696,37760,0x1eb72,0x254b2,37712,37768,0x1eb74,0x254b4,37792,37776,0x1eb76,0x254b6,37808,37784,0x1eb78,0x25610,38080,38144,0x1eb7a,0x25612,38096,38152,0x1eb7c,0x25614,38176,38160,0x1eb7e,0x25616,38192,38168,0x1eb60,0x251f8,38432,38624,0x1eb62,0x251fa,38448,38632,0x1eb64,0x251fc,38592,38640,0x1eb66,0x251fe,38608,38648,0x1eb68,0x25358,38816,39008,0x1eb6a,0x2535a,38832,39016,0x1eb6c,0x2535c,38976,39024,0x1eb6e,0x2535e,38992,39032,0x1eb70,0x254b8,39168,39360,0x1eb72,0x254ba,39184,39368,0x1eb74,0x254bc,39392,39376,0x1eb76,0x254be,39408,39384,0x1eb78,0x25618,39552,39744,0x1eb7a,0x2561a,39568,39752,0x1eb7c,0x2561c,39776,39760,0x1eb7e,0x2561e,39792,39768,0x1eb60,0x26278,40032,40096,0x1eb62,0x2627a,40048,40104,0x1eb64,0x2627c,40064,40112,0x1eb66,0x2627e,40080,40120,0x1eb68,0x263d8,40416,40480,0x1eb6a,0x263da,40432,40488,0x1eb6c,0x263dc,40448,40496,0x1eb6e,0x263de,40464,40504,0x1eb70,0x26538,40768,40832,0x1eb72,0x2653a,40784,40840,0x1eb74,0x2653c,40864,40848,0x1eb76,0x2653e,40880,40856,0x1eb78,0x26698,41152,41216,0x1eb7a,0x2669a,41168,41224,0x1eb7c,0x2669c,41248,41232,0x1eb7e,0x2669e,41264,41240,0x1eb60,0x100000,41504,41696,0x1eb62,0x100000,41520,41704,0x1eb64,0x100000,41664,41712,0x1eb66,0x100000,41680,41720,0x1eb68,0x100000,41888,42080,0x1eb6a,0x100000,41904,42088,0x1eb6c,0x100000,42048,42096,0x1eb6e,0x100000,42064,42104,0x1eb70,0x100000,42240,42432,0x1eb72,0x100000,42256,42440,0x1eb74,0x100000,42464,42448,0x1eb76,0x100000,42480,42456,0x1eb78,0x100000,42624,42816,0x1eb7a,0x100000,42640,42824,0x1eb7c,0x100000,42848,42832,0x1eb7e,0x100000,42864,42840,36960,36992,37024,46080,36968,37000,37032,46096,36976,37008,37040,46112,36984,37016,37048,46128,38112,38144,38176,46144,38120,38152,38184,46160,38128,38160,38192,46176,38136,38168,38200,46192,39232,39328,39296,46208,39240,39336,39304,46224,39248,39344,39312,46240,39256,39352,39320,46256,40384,40480,40448,46272,40392,40488,40456,46288,40400,40496,40464,46304,40408,40504,40472,46320,46112,46272,46304,46336,46120,46280,46312,46352,46128,46288,46320,46368,46136,46296,46328,46384,47264,47424,47456,46400,47272,47432,47464,46416,47280,47440,47472,46432,47288,47448,47480,46448,48384,48608,48576,46464,48392,48616,48584,46480,48400,48624,48592,46496,48408,48632,48600,46512,49536,49760,49728,46528,49544,49768,49736,46544,49552,49776,49744,46560,49560,49784,49752,46576,55392,55424,55456,46592,55400,55432,55464,46608,55408,55440,55472,46624,55416,55448,55480,46640,56544,56576,56608,46656,56552,56584,56616,46672,56560,56592,56624,46688,56568,56600,56632,46704,57664,57760,57728,46720,57672,57768,57736,46736,57680,57776,57744,46752,57688,57784,57752,46768,58816,58912,58880,46784,58824,58920,58888,46800,58832,58928,58896,46816,58840,58936,58904,46832,64544,64704,64736,46848,64552,64712,64744,46864,64560,64720,64752,46880,64568,64728,64760,46896,65696,65856,65888,46912,65704,65864,65896,46928,65712,65872,65904,46944,65720,65880,65912,46960,66816,67040,67008,46976,66824,67048,67016,46992,66832,67056,67024,47008,66840,67064,67032,47024,67968,68192,68160,47040,67976,68200,68168,47056,67984,68208,68176,47072,67992,68216,68184,47088,0x1e800,0x1e800,0x20cc0,18432,0x1e8c4,0x1e8c4,0x20cc4,18448,0x1e988,0x1e988,0x20cc8,18464,0x1ea4c,0x1ea4c,0x20ccc,18480,0x1eb10,0x1eb10,0x20cd0,18496,0x1ebd4,0x1ebd4,0x20cd4,18512,0x1ec98,0x1ec98,0x20cd8,18528,0x1ed5c,0x1ed5c,0x20cdc,18544,0x1ee20,0x1ee20,0x20ce0,18816,0x1eee4,0x1eee4,0x20ce4,18832,0x1efa8,0x1efa8,0x20ce8,18848,0x1f06c,0x1f06c,0x20cec,18864,0x1f130,0x1f130,0x20cf0,18880,0x1f1f4,0x1f1f4,0x20cf4,18896,0x1f2b8,0x1f2b8,0x20cf8,18912,0x1f37c,0x1f37c,0x20cfc,18928,0x1e810,0x1e800,0x20cc0,19200,0x1e8d4,0x1e8c4,0x20cc4,19216,0x1e998,0x1e988,0x20cc8,19232,0x1ea5c,0x1ea4c,0x20ccc,19248,0x1eb20,0x1eb10,0x20cd0,19264,0x1ebe4,0x1ebd4,0x20cd4,19280,0x1eca8,0x1ec98,0x20cd8,19296,0x1ed6c,0x1ed5c,0x20cdc,19312,0x1ee30,0x1ee20,0x20ce0,19584,0x1eef4,0x1eee4,0x20ce4,19600,0x1efb8,0x1efa8,0x20ce8,19616,0x1f07c,0x1f06c,0x20cec,19632,0x1f140,0x1f130,0x20cf0,19648,0x1f204,0x1f1f4,0x20cf4,19664,0x1f2c8,0x1f2b8,0x20cf8,19680,0x1f38c,0x1f37c,0x20cfc,19696,0x1e820,0x1e800,0x20cc0,19968,0x1e8e4,0x1e8c4,0x20cc4,19984,0x1e9a8,0x1e988,0x20cc8,20000,0x1ea6c,0x1ea4c,0x20ccc,20016,0x1eb30,0x1eb10,0x20cd0,20032,0x1ebf4,0x1ebd4,0x20cd4,20048,0x1ecb8,0x1ec98,0x20cd8,20064,0x1ed7c,0x1ed5c,0x20cdc,20080,0x1ee40,0x1ee20,0x20ce0,20352,0x1ef04,0x1eee4,0x20ce4,20368,0x1efc8,0x1efa8,0x20ce8,20384,0x1f08c,0x1f06c,0x20cec,20400,0x1f150,0x1f130,0x20cf0,20416,0x1f214,0x1f1f4,0x20cf4,20432,0x1f2d8,0x1f2b8,0x20cf8,20448,0x1f39c,0x1f37c,0x20cfc,20464,0x1e830,0x1e800,0x20cc0,20736,0x1e8f4,0x1e8c4,0x20cc4,20752,0x1e9b8,0x1e988,0x20cc8,20768,0x1ea7c,0x1ea4c,0x20ccc,20784,0x1eb40,0x1eb10,0x20cd0,20800,0x1ec04,0x1ebd4,0x20cd4,20816,0x1ecc8,0x1ec98,0x20cd8,20832,0x1ed8c,0x1ed5c,0x20cdc,20848,0x1ee50,0x1ee20,0x20ce0,21120,0x1ef14,0x1eee4,0x20ce4,21136,0x1efd8,0x1efa8,0x20ce8,21152,0x1f09c,0x1f06c,0x20cec,21168,0x1f160,0x1f130,0x20cf0,21184,0x1f224,0x1f1f4,0x20cf4,21200,0x1f2e8,0x1f2b8,0x20cf8,21216,0x1f3ac,0x1f37c,0x20cfc,21232,55296,0x26e70,0x1e8c0,0x1eb60,55312,0x26e74,0x1ed50,0x1eff0,55328,0x26e78,0x1f1e0,0x1f480,55344,0x26e7c,0x1f670,0x1f910,55360,0x26e80,0x1fb00,0x1fda0,55376,0x26e84,0x1ff90,0x20230,55392,0x26e88,0x20420,0x206c0,55408,0x26e8c,0x208b0,0x20b50,55680,0x26e90,0x20d40,0x20fe0,55696,0x26e94,0x211d0,0x21470,55712,0x26e98,0x21660,0x21900,55728,0x26e9c,0x21af0,0x21d90,55744,0x26ea0,0x21f80,0x22220,55760,0x26ea4,0x22410,0x226b0,55776,0x26ea8,0x228a0,0x22b40,55792,0x26eac,0x22d30,0x22fd0,56096,0x26eb0,0x1e8f0,0x1eb68,56112,0x26eb4,0x1ed80,0x1eff8,56064,0x26eb8,0x1f210,0x1f488,56080,0x26ebc,0x1f6a0,0x1f918,56160,0x26ec0,0x1fb30,0x1fda8,56176,0x26ec4,0x1ffc0,0x20238,56128,0x26ec8,0x20450,0x206c8,56144,0x26ecc,0x208e0,0x20b58,56480,0x26ed0,0x20d70,0x20fe8,56496,0x26ed4,0x21200,0x21478,56448,0x26ed8,0x21690,0x21908,56464,0x26edc,0x21b20,0x21d98,56544,0x26ee0,0x21fb0,0x22228,56560,0x26ee4,0x22440,0x226b8,56512,0x26ee8,0x228d0,0x22b48,56528,0x26eec,0x22d60,0x22fd8,56896,0x26ef0,0x1e920,0x1eb70,56912,0x26ef4,0x1edb0,0x1f000,56928,0x26ef8,0x1f240,0x1f490,56944,0x26efc,0x1f6d0,0x1f920,56832,0x26f00,0x1fb60,0x1fdb0,56848,0x26f04,0x1fff0,0x20240,56864,0x26f08,0x20480,0x206d0,56880,0x26f0c,0x20910,0x20b60,57280,0x26f10,0x20da0,0x20ff0,57296,0x26f14,0x21230,0x21480,57312,0x26f18,0x216c0,0x21910,57328,0x26f1c,0x21b50,0x21da0,57216,0x26f20,0x21fe0,0x22230,57232,0x26f24,0x22470,0x226c0,57248,0x26f28,0x22900,0x22b50,57264,0x26f2c,0x22d90,0x22fe0,57696,0x26f30,0x1e950,0x1eb78,57712,0x26f34,0x1ede0,0x1f008,57664,0x26f38,0x1f270,0x1f498,57680,0x26f3c,0x1f700,0x1f928,57632,0x26f40,0x1fb90,0x1fdb8,57648,0x26f44,0x20020,0x20248,57600,0x26f48,0x204b0,0x206d8,57616,0x26f4c,0x20940,0x20b68,58080,0x26f50,0x20dd0,0x20ff8,58096,0x26f54,0x21260,0x21488,58048,0x26f58,0x216f0,0x21918,58064,0x26f5c,0x21b80,0x21da8,58016,0x26f60,0x22010,0x22238,58032,0x26f64,0x224a0,0x226c8,57984,0x26f68,0x22930,0x22b58,58000,0x26f6c,0x22dc0,0x22fe8,0x3f803f80,0x3f803f80,16256,0,0,0x3f803f80,0x3f800000,0,0,0x3f803f80,0,16256,0,0x3f803f80,0,0x3f800000,0x3f803f80,0,0,0,0,0,0,0,0,0,0,0,0,0,0,0,0x3f803f80,0,0,0,0,0,0,0,0,0,0,0,0,0,0,0,0x3f803f80,0,0,0,0,0,0,0,0,0,0,0,0,0,0,0,0x3f803f80,0,0,0,0,0,0,0,0,0,0,0,0,0,0,0,0x3f803f80,0x3f803f80,16256,0,0,0x3f803f80,0x3f800000,0,0,0x3f803f80,0,16256,0,0x3f803f80,0,0x3f800000,0x3f803f80,0,0,0,0,0,0,0,0,0,0,0,0,0,0,0,0x3f803f80,0,0,0,0,0,0,0,0,0,0,0,0,0,0,0,0x3f803f80,0,0,0,0,0,0,0,0,0,0,0,0,0,0,0,0x3f803f80,0,0,0,0,0,0,0,0,0,0,0,0,0,0,0,0x3f803f80,0x3f803f80,16256,0,0,0x3f803f80,0x3f800000,0,0,0x3f803f80,0,16256,0,0x3f803f80,0,0x3f800000,0x3f803f80,0,0,0,0,0,0,0,0,0,0,0,0,0,0,0,0x3f803f80,0,0,0,0,0,0,0,0,0,0,0,0,0,0,0,0x3f803f80,0,0,0,0,0,0,0,0,0,0,0,0,0,0,0,0x3f803f80,0,0,0,0,0,0,0,0,0,0,0,0,0,0,0,0x3f803f80,0x3f803f80,16256,0,0,0x3f803f80,0x3f800000,0,0,0x3f803f80,0,16256,0,0x3f803f80,0,0x3f800000,0,64,0x60000,0xc0080,144,208,0x60010,0xc0090,288,352,0x60020,0xc00a0,432,496,0x60030,0xc00b0,576,512,0x60040,0xc00c0,720,656,0x60050,0xc00d0,864,800,0x60060,0xc00e0,1008,944,0x60070,0xc00f0,1024,1088,0x60190,0xc0210,1168,1232,0x60180,0xc0200,1312,1376,0x601b0,0xc0230,1456,1520,0x601a0,0xc0220,1600,1536,0x601d0,0xc0250,1744,1680,0x601c0,0xc0240,1888,1824,0x601f0,0xc0270,2032,1968,0x601e0,0xc0260,16,80,0x60320,0xc03a0,128,192,0x60330,0xc03b0,304,368,0x60300,0xc0380,416,480,0x60310,0xc0390,592,528,0x60360,0xc03e0,704,640,0x60370,0xc03f0,880,816,0x60340,0xc03c0,992,928,0x60350,0xc03d0,1040,1104,0x604b0,0xef930,1152,1216,0x604a0,0xef920,1328,1392,0x60490,0xef910,1440,1504,0x60480,0xef900,1616,1552,0x604f0,0xef970,1728,1664,0x604e0,0xef960,1904,1840,0x604d0,0xef950,2016,1952,0x604c0,0xef940,32,96,0x60640,0xefac0,176,240,0x60650,0xefad0,256,320,0x60660,0xefae0,400,464,0x60670,0xefaf0,608,544,0x60600,0xefa80,752,688,0x60610,0xefa90,832,768,0x60620,0xefaa0,976,912,0x60630,0xefab0,1056,1120,0x8fbd0,0xefc50,1200,1264,0x8fbc0,0xefc40,1280,1344,0x8fbf0,0xefc70,1424,1488,0x8fbe0,0xefc60,1632,1568,0x8fb90,0xefc10,1776,1712,0x8fb80,0xefc00,1856,1792,0x8fbb0,0xefc30,2000,1936,0x8fba0,0xefc20,48,112,0x8fd60,0xefde0,160,224,0x8fd70,0xefdf0,272,336,0x8fd40,0xefdc0,384,448,0x8fd50,0xefdd0,624,560,0x8fd20,0xefda0,736,672,0x8fd30,0xefdb0,848,784,0x8fd00,0xefd80,960,896,0x8fd10,0xefd90,1072,1136,0x8fef0,0xeff70,1184,1248,0x8fee0,0xeff60,1296,1360,0x8fed0,0xeff50,1408,1472,0x8fec0,0xeff40,1648,1584,0x8feb0,0xeff30,1760,1696,0x8fea0,0xeff20,1872,1808,0x8fe90,0xeff10,1984,1920,0x8fe80,0xeff00,0xf0100,75776,83968,91136,0xf0110,75792,83984,91152,0xf0120,75808,84000,91168,0xf0130,75824,84016,91184,0xf0140,75840,84032,91200,0xf0150,75856,84048,91216,0xf0160,75872,84064,91232,0xf0170,75888,84080,91248,0xf0290,75904,84096,91264,0xf0280,75920,84112,91280,0xf02b0,75936,84128,91296,0xf02a0,75952,84144,91312,0xf02d0,75968,84160,91328,0xf02c0,75984,84176,91344,0xf02f0,76000,84192,91360,0xf02e0,76016,84208,91376,0x11f820,76032,84224,91392,0x11f830,76048,84240,91408,0x11f800,76064,84256,91424,0x11f810,76080,84272,91440,0x11f860,76096,84288,91456,0x11f870,76112,84304,91472,0x11f840,76128,84320,91488,0x11f850,76144,84336,91504,0x11f9b0,76160,84352,91520,0x11f9a0,76176,84368,91536,0x11f990,76192,84384,91552,0x11f980,76208,84400,91568,0x11f9f0,76224,84416,91584,0x11f9e0,76240,84432,91600,0x11f9d0,76256,84448,91616,0x11f9c0,76272,84464,91632,0xf0100,76288,84480,91648,0xf0110,76304,84496,91664,0xf0120,76320,84512,91680,0xf0130,76336,84528,91696,0xf0140,76352,84544,91712,0xf0150,76368,84560,91728,0xf0160,76384,84576,91744,0xf0170,76400,84592,91760,0xf0290,76416,84608,91776,0xf0280,76432,84624,91792,0xf02b0,76448,84640,91808,0xf02a0,76464,84656,91824,0xf02d0,76480,84672,91840,0xf02c0,76496,84688,91856,0xf02f0,76512,84704,91872,0xf02e0,76528,84720,91888,0x11f820,76544,84736,91904,0x11f830,76560,84752,91920,0x11f800,76576,84768,91936,0x11f810,76592,84784,91952,0x11f860,76608,84800,91968,0x11f870,76624,84816,91984,0x11f840,76640,84832,92000,0x11f850,76656,84848,92016,0x11f9b0,76672,84864,92032,0x11f9a0,76688,84880,92048,0x11f990,76704,84896,92064,0x11f980,76720,84912,92080,0x11f9f0,76736,84928,92096,0x11f9e0,76752,84944,92112,0x11f9d0,76768,84960,92128,0x11f9c0,76784,84976,92144,0x1c140,36864,43008,52224,0x1c144,36880,43024,52240,0x1c148,36896,43040,52256,0x1c14c,36912,43056,52272,0x1c150,36928,43072,52288,0x1c154,36944,43088,52304,0x1c158,36960,43104,52320,0x1c15c,36976,43120,52336,0x1c160,36992,43136,52352,0x1c164,37008,43152,52368,0x1c168,37024,43168,52384,0x1c16c,37040,43184,52400,0x1c170,37056,43200,52416,0x1c174,37072,43216,52432,0x1c178,37088,43232,52448,0x1c17c,37104,43248,52464,0x1c180,37120,43264,52480,0x1c184,37136,43280,52496,0x1c188,37152,43296,52512,0x1c18c,37168,43312,52528,0x1c190,37184,43328,52544,0x1c194,37200,43344,52560,0x1c198,37216,43360,52576,0x1c19c,37232,43376,52592,0x1c1a0,37248,43392,52608,0x1c1a4,37264,43408,52624,0x1c1a8,37280,43424,52640,0x1c1ac,37296,43440,52656,0x1c1b0,37312,43456,52672,0x1c1b4,37328,43472,52688,0x1c1b8,37344,43488,52704,0x1c1bc,37360,43504,52720,0x1c1c0,37376,43520,52736,0x1c1c4,37392,43536,52752,0x1c1c8,37408,43552,52768,0x1c1cc,37424,43568,52784,0x1c1d0,37440,43584,52800,0x1c1d4,37456,43600,52816,0x1c1d8,37472,43616,52832,0x1c1dc,37488,43632,52848,0x1c1e0,37504,43648,52864,0x1c1e4,37520,43664,52880,0x1c1e8,37536,43680,52896,0x1c1ec,37552,43696,52912,0x1c1f0,37568,43712,52928,0x1c1f4,37584,43728,52944,0x1c1f8,37600,43744,52960,0x1c1fc,37616,43760,52976,0x1c200,37632,43776,52992,0x1c204,37648,43792,53008,0x1c208,37664,43808,53024,0x1c20c,37680,43824,53040,0x1c210,37696,43840,53056,0x1c214,37712,43856,53072,0x1c218,37728,43872,53088,0x1c21c,37744,43888,53104,0x1c220,37760,43904,53120,0x1c224,37776,43920,53136,0x1c228,37792,43936,53152,0x1c22c,37808,43952,53168,0x1c230,37824,43968,53184,0x1c234,37840,43984,53200,0x1c238,37856,44000,53216,0x1c23c,37872,44016,53232,79936,86016,0x1d600,0x1d608,80080,86160,0x1d610,0x1d618,80224,86304,0x1d620,0x1d628,80368,86448,0x1d630,0x1d638,80384,86592,0x1d640,0x1d648,80528,86736,0x1d650,0x1d658,80672,86880,0x1d660,0x1d668,80816,87024,0x1d670,0x1d678,80960,87040,0x1d680,0x1d688,81104,87184,0x1d690,0x1d698,81248,87328,0x1d6a0,0x1d6a8,81392,87472,0x1d6b0,0x1d6b8,81408,87616,0x1d6c0,0x1d6c8,81552,87760,0x1d6d0,0x1d6d8,81696,87904,0x1d6e0,0x1d6e8,81840,88048,0x1d6f0,0x1d6f8,79952,86024,0x1d700,0x1d708,80064,86168,0x1d710,0x1d718,80240,86312,0x1d720,0x1d728,80352,86456,0x1d730,0x1d738,80400,86600,0x1d740,0x1d748,80512,86744,0x1d750,0x1d758,80688,86888,0x1d760,0x1d768,80800,87032,0x1d770,0x1d778,80976,87048,0x1d780,0x1d788,81088,87192,0x1d790,0x1d798,81264,87336,0x1d7a0,0x1d7a8,81376,87480,0x1d7b0,0x1d7b8,81424,87624,0x1d7c0,0x1d7c8,81536,87768,0x1d7d0,0x1d7d8,81712,87912,0x1d7e0,0x1d7e8,81824,88056,0x1d7f0,0x1d7f8,79968,86032,0x1d800,0x1d808,80112,86144,0x1d810,0x1d818,80192,86320,0x1d820,0x1d828,80336,86432,0x1d830,0x1d838,80416,86608,0x1d840,0x1d848,80560,86720,0x1d850,0x1d858,80640,86896,0x1d860,0x1d868,80784,87008,0x1d870,0x1d878,80992,87056,0x1d880,0x1d888,81136,87168,0x1d890,0x1d898,81216,87344,0x1d8a0,0x1d8a8,81360,87456,0x1d8b0,0x1d8b8,81440,87632,0x1d8c0,0x1d8c8,81584,87744,0x1d8d0,0x1d8d8,81664,87920,0x1d8e0,0x1d8e8,81808,88032,0x1d8f0,0x1d8f8,79984,86040,0x1d900,0x1d908,80096,86152,0x1d910,0x1d918,80208,86328,0x1d920,0x1d928,80320,86440,0x1d930,0x1d938,80432,86616,0x1d940,0x1d948,80544,86728,0x1d950,0x1d958,80656,86904,0x1d960,0x1d968,80768,87016,0x1d970,0x1d978,81008,87064,0x1d980,0x1d988,81120,87176,0x1d990,0x1d998,81232,87352,0x1d9a0,0x1d9a8,81344,87464,0x1d9b0,0x1d9b8,81456,87640,0x1d9c0,0x1d9c8,81568,87752,0x1d9d0,0x1d9d8,81680,87928,0x1d9e0,0x1d9e8,81792,88040,0x1d9f0,0x1d9f8,0x1e700,0x1e700,0x1e980,0x100000,0x1e702,0x100000,0x100000,0x1e980,0x1e704,0x100000,0x100000,0x100000,0x1e706,0x100000,0x100000,0x100000,0x1e710,0x1e700,0x100000,0x100000,0x1e712,0x100000,0x100000,0x1e980,0x1e714,0x100000,0x100000,0x100000,0x1e716,0x100000,0x100000,0x100000,0x1e720,0x1e700,0x100000,0x100000,0x1e722,0x100000,0x100000,0x1e980,0x1e724,0x100000,0x100000,0x100000,0x1e726,0x100000,0x100000,0x100000,0x1e730,0x1e700,0x100000,0x100000,0x1e732,0x100000,0x100000,0x1e980,0x1e734,0x100000,0x100000,0x100000,0x1e736,0x100000,0x100000,0x100000,0x1e708,0x1e710,0x1e9b0,0x100000,0x1e70a,0x100000,0x100000,0x1e9b0,0x1e70c,0x100000,0x100000,0x100000,0x1e70e,0x100000,0x100000,0x100000,0x1e718,0x1e710,0x100000,0x100000,0x1e71a,0x100000,0x100000,0x1e9b0,0x1e71c,0x100000,0x100000,0x100000,0x1e71e,0x100000,0x100000,0x100000,0x1e728,0x1e710,0x100000,0x100000,0x1e72a,0x100000,0x100000,0x1e9b0,0x1e72c,0x100000,0x100000,0x100000,0x1e72e,0x100000,0x100000,0x100000,0x1e738,0x1e710,0x100000,0x100000,0x1e73a,0x100000,0x100000,0x1e9b0,0x1e73c,0x100000,0x100000,0x100000,0x1e73e,0x100000,0x100000,0x100000,0x1e740,0x1e720,0x1e9e0,0x100000,0x1e742,0x100000,0x100000,0x1e9e0,0x1e744,0x100000,0x100000,0x100000,0x1e746,0x100000,0x100000,0x100000,0x1e750,0x1e720,0x100000,0x100000,0x1e752,0x100000,0x100000,0x1e9e0,0x1e754,0x100000,0x100000,0x100000,0x1e756,0x100000,0x100000,0x100000,0x1e760,0x1e720,0x100000,0x100000,0x1e762,0x100000,0x100000,0x1e9e0,0x1e764,0x100000,0x100000,0x100000,0x1e766,0x100000,0x100000,0x100000,0x1e770,0x1e720,0x100000,0x100000,0x1e772,0x100000,0x100000,0x1e9e0,0x1e774,0x100000,0x100000,0x100000,0x1e776,0x100000,0x100000,0x100000,0x1e748,0x1e730,0x1ea10,0x100000,0x1e74a,0x100000,0x100000,0x1ea10,0x1e74c,0x100000,0x100000,0x100000,0x1e74e,0x100000,0x100000,0x100000,0x1e758,0x1e730,0x100000,0x100000,0x1e75a,0x100000,0x100000,0x1ea10,0x1e75c,0x100000,0x100000,0x100000,0x1e75e,0x100000,0x100000,0x100000,0x1e768,0x1e730,0x100000,0x100000,0x1e76a,0x100000,0x100000,0x1ea10,0x1e76c,0x100000,0x100000,0x100000,0x1e76e,0x100000,0x100000,0x100000,0x1e778,0x1e730,0x100000,0x100000,0x1e77a,0x100000,0x100000,0x1ea10,0x1e77c,0x100000,0x100000,0x100000,0x1e77e,0x100000,0x100000,0x100000,0x1ebc0,0x25770,37056,37120,0x1ebc2,0x25772,37072,37128,0x1ebc4,0x25774,37088,37136,0x1ebc6,0x25776,37104,37144,0x1ebc8,0x258d0,37440,37504,0x1ebca,0x258d2,37456,37512,0x1ebcc,0x258d4,37472,37520,0x1ebce,0x258d6,37488,37528,0x1ebd0,0x25a30,37856,37920,0x1ebd2,0x25a32,37872,37928,0x1ebd4,0x25a34,37824,37936,0x1ebd6,0x25a36,37840,37944,0x1ebd8,0x25b90,38240,38304,0x1ebda,0x25b92,38256,38312,0x1ebdc,0x25b94,38208,38320,0x1ebde,0x25b96,38224,38328,0x1ebc0,0x25778,38528,38720,0x1ebc2,0x2577a,38544,38728,0x1ebc4,0x2577c,38560,38736,0x1ebc6,0x2577e,38576,38744,0x1ebc8,0x258d8,38912,39104,0x1ebca,0x258da,38928,39112,0x1ebcc,0x258dc,38944,39120,0x1ebce,0x258de,38960,39128,0x1ebd0,0x25a38,39328,39520,0x1ebd2,0x25a3a,39344,39528,0x1ebd4,0x25a3c,39296,39536,0x1ebd6,0x25a3e,39312,39544,0x1ebd8,0x25b98,39712,39904,0x1ebda,0x25b9a,39728,39912,0x1ebdc,0x25b9c,39680,39920,0x1ebde,0x25b9e,39696,39928,0x1ebc0,0x267f0,40128,40192,0x1ebc2,0x267f2,40144,40200,0x1ebc4,0x267f4,40160,40208,0x1ebc6,0x267f6,40176,40216,0x1ebc8,0x26950,40512,40576,0x1ebca,0x26952,40528,40584,0x1ebcc,0x26954,40544,40592,0x1ebce,0x26956,40560,40600,0x1ebd0,0x26ab0,40928,40992,0x1ebd2,0x26ab2,40944,41000,0x1ebd4,0x26ab4,40896,41008,0x1ebd6,0x26ab6,40912,41016,0x1ebd8,0x26c10,41312,41376,0x1ebda,0x26c12,41328,41384,0x1ebdc,0x26c14,41280,41392,0x1ebde,0x26c16,41296,41400,0x1ebc0,0x100000,41600,41792,0x1ebc2,0x100000,41616,41800,0x1ebc4,0x100000,41632,41808,0x1ebc6,0x100000,41648,41816,0x1ebc8,0x100000,41984,42176,0x1ebca,0x100000,42000,42184,0x1ebcc,0x100000,42016,42192,0x1ebce,0x100000,42032,42200,0x1ebd0,0x100000,42400,42592,0x1ebd2,0x100000,42416,42600,0x1ebd4,0x100000,42368,42608,0x1ebd6,0x100000,42384,42616,0x1ebd8,0x100000,42784,42976,0x1ebda,0x100000,42800,42984,0x1ebdc,0x100000,42752,42992,0x1ebde,0x100000,42768,43000,37056,37088,37120,55296,37064,37096,37128,55312,37072,37104,37136,55328,37080,37112,37144,55344,38208,38240,38272,55360,38216,38248,38280,55376,38224,38256,38288,55392,38232,38264,38296,55408,39392,39360,39456,55424,39400,39368,39464,55440,39408,39376,39472,55456,39416,39384,39480,55472,40544,40512,40608,55488,40552,40520,40616,55504,40560,40528,40624,55520,40568,40536,40632,55536,46208,46240,46400,55552,46216,46248,46408,55568,46224,46256,46416,55584,46232,46264,46424,55600,47360,47392,47552,55616,47368,47400,47560,55632,47376,47408,47568,55648,47384,47416,47576,55664,48544,48512,48736,55680,48552,48520,48744,55696,48560,48528,48752,55712,48568,48536,48760,55728,49696,49664,49888,55744,49704,49672,49896,55760,49712,49680,49904,55776,49720,49688,49912,55792,55488,55520,55552,55808,55496,55528,55560,55824,55504,55536,55568,55840,55512,55544,55576,55856,56640,56672,56704,55872,56648,56680,56712,55888,56656,56688,56720,55904,56664,56696,56728,55920,57824,57792,57888,55936,57832,57800,57896,55952,57840,57808,57904,55968,57848,57816,57912,55984,58976,58944,59040,56000,58984,58952,59048,56016,58992,58960,59056,56032,59000,58968,59064,56048,64640,64672,64832,56064,64648,64680,64840,56080,64656,64688,64848,56096,64664,64696,64856,56112,65792,65824,65984,56128,65800,65832,65992,56144,65808,65840,66000,56160,65816,65848,66008,56176,66976,66944,67168,56192,66984,66952,67176,56208,66992,66960,67184,56224,67000,66968,67192,56240,68128,68096,68320,56256,68136,68104,68328,56272,68144,68112,68336,56288,68152,68120,68344,56304,0x1e800,0x1e800,0x20cc0,36864,0x1e8c4,0x1e8c4,0x20cc4,36880,0x1e988,0x1e988,0x20cc8,36896,0x1ea4c,0x1ea4c,0x20ccc,36912,0x1eb10,0x1eb10,0x20cd0,36928,0x1ebd4,0x1ebd4,0x20cd4,36944,0x1ec98,0x1ec98,0x20cd8,36960,0x1ed5c,0x1ed5c,0x20cdc,36976,0x1ee20,0x1ee20,0x20ce0,37248,0x1eee4,0x1eee4,0x20ce4,37264,0x1efa8,0x1efa8,0x20ce8,37280,0x1f06c,0x1f06c,0x20cec,37296,0x1f130,0x1f130,0x20cf0,37312,0x1f1f4,0x1f1f4,0x20cf4,37328,0x1f2b8,0x1f2b8,0x20cf8,37344,0x1f37c,0x1f37c,0x20cfc,37360,0x1e810,0x1e800,0x20cc0,37632,0x1e8d4,0x1e8c4,0x20cc4,37648,0x1e998,0x1e988,0x20cc8,37664,0x1ea5c,0x1ea4c,0x20ccc,37680,0x1eb20,0x1eb10,0x20cd0,37696,0x1ebe4,0x1ebd4,0x20cd4,37712,0x1eca8,0x1ec98,0x20cd8,37728,0x1ed6c,0x1ed5c,0x20cdc,37744,0x1ee30,0x1ee20,0x20ce0,38016,0x1eef4,0x1eee4,0x20ce4,38032,0x1efb8,0x1efa8,0x20ce8,38048,0x1f07c,0x1f06c,0x20cec,38064,0x1f140,0x1f130,0x20cf0,38080,0x1f204,0x1f1f4,0x20cf4,38096,0x1f2c8,0x1f2b8,0x20cf8,38112,0x1f38c,0x1f37c,0x20cfc,38128,0x1e820,0x1e800,0x20cc0,38400,0x1e8e4,0x1e8c4,0x20cc4,38416,0x1e9a8,0x1e988,0x20cc8,38432,0x1ea6c,0x1ea4c,0x20ccc,38448,0x1eb30,0x1eb10,0x20cd0,38464,0x1ebf4,0x1ebd4,0x20cd4,38480,0x1ecb8,0x1ec98,0x20cd8,38496,0x1ed7c,0x1ed5c,0x20cdc,38512,0x1ee40,0x1ee20,0x20ce0,38784,0x1ef04,0x1eee4,0x20ce4,38800,0x1efc8,0x1efa8,0x20ce8,38816,0x1f08c,0x1f06c,0x20cec,38832,0x1f150,0x1f130,0x20cf0,38848,0x1f214,0x1f1f4,0x20cf4,38864,0x1f2d8,0x1f2b8,0x20cf8,38880,0x1f39c,0x1f37c,0x20cfc,38896,0x1e830,0x1e800,0x20cc0,39168,0x1e8f4,0x1e8c4,0x20cc4,39184,0x1e9b8,0x1e988,0x20cc8,39200,0x1ea7c,0x1ea4c,0x20ccc,39216,0x1eb40,0x1eb10,0x20cd0,39232,0x1ec04,0x1ebd4,0x20cd4,39248,0x1ecc8,0x1ec98,0x20cd8,39264,0x1ed8c,0x1ed5c,0x20cdc,39280,0x1ee50,0x1ee20,0x20ce0,39552,0x1ef14,0x1eee4,0x20ce4,39568,0x1efd8,0x1efa8,0x20ce8,39584,0x1f09c,0x1f06c,0x20cec,39600,0x1f160,0x1f130,0x20cf0,39616,0x1f224,0x1f1f4,0x20cf4,39632,0x1f2e8,0x1f2b8,0x20cf8,39648,0x1f3ac,0x1f37c,0x20cfc,39664,73728,0x26f70,0x1e980,0x1ebc0,73744,0x26f74,0x1ee10,0x1f050,73760,0x26f78,0x1f2a0,0x1f4e0,73776,0x26f7c,0x1f730,0x1f970,73792,0x26f80,0x1fbc0,0x1fe00,73808,0x26f84,0x20050,0x20290,73824,0x26f88,0x204e0,0x20720,73840,0x26f8c,0x20970,0x20bb0,74112,0x26f90,0x20e00,0x21040,74128,0x26f94,0x21290,0x214d0,74144,0x26f98,0x21720,0x21960,74160,0x26f9c,0x21bb0,0x21df0,74176,0x26fa0,0x22040,0x22280,74192,0x26fa4,0x224d0,0x22710,74208,0x26fa8,0x22960,0x22ba0,74224,0x26fac,0x22df0,0x23030,74528,0x26fb0,0x1e9b0,0x1ebc8,74544,0x26fb4,0x1ee40,0x1f058,74496,0x26fb8,0x1f2d0,0x1f4e8,74512,0x26fbc,0x1f760,0x1f978,74592,0x26fc0,0x1fbf0,0x1fe08,74608,0x26fc4,0x20080,0x20298,74560,0x26fc8,0x20510,0x20728,74576,0x26fcc,0x209a0,0x20bb8,74912,0x26fd0,0x20e30,0x21048,74928,0x26fd4,0x212c0,0x214d8,74880,0x26fd8,0x21750,0x21968,74896,0x26fdc,0x21be0,0x21df8,74976,0x26fe0,0x22070,0x22288,74992,0x26fe4,0x22500,0x22718,74944,0x26fe8,0x22990,0x22ba8,74960,0x26fec,0x22e20,0x23038,75328,0x26ff0,0x1e9e0,0x1ebd0,75344,0x26ff4,0x1ee70,0x1f060,75360,0x26ff8,0x1f300,0x1f4f0,75376,0x26ffc,0x1f790,0x1f980,75264,0x27000,0x1fc20,0x1fe10,75280,0x27004,0x200b0,0x202a0,75296,0x27008,0x20540,0x20730,75312,0x2700c,0x209d0,0x20bc0,75712,0x27010,0x20e60,0x21050,75728,0x27014,0x212f0,0x214e0,75744,0x27018,0x21780,0x21970,75760,0x2701c,0x21c10,0x21e00,75648,0x27020,0x220a0,0x22290,75664,0x27024,0x22530,0x22720,75680,0x27028,0x229c0,0x22bb0,75696,0x2702c,0x22e50,0x23040,76128,0x27030,0x1ea10,0x1ebd8,76144,0x27034,0x1eea0,0x1f068,76096,0x27038,0x1f330,0x1f4f8,76112,0x2703c,0x1f7c0,0x1f988,76064,0x27040,0x1fc50,0x1fe18,76080,0x27044,0x200e0,0x202a8,76032,0x27048,0x20570,0x20738,76048,0x2704c,0x20a00,0x20bc8,76512,0x27050,0x20e90,0x21058,76528,0x27054,0x21320,0x214e8,76480,0x27058,0x217b0,0x21978,76496,0x2705c,0x21c40,0x21e08,76448,0x27060,0x220d0,0x22298,76464,0x27064,0x22560,0x22728,76416,0x27068,0x229f0,0x22bb8,76432,0x2706c,0x22e80,0x23048,0x3f803f80,0x3f803f80,16256,0,0,0x3f803f80,0x3f800000,0,0,0x3f803f80,0,16256,0,0x3f803f80,0,0x3f800000,0x3f803f80,0,0,0,0,0,0,0,0,0,0,0,0,0,0,0,0x3f803f80,0,0,0,0,0,0,0,0,0,0,0,0,0,0,0,0x3f803f80,0,0,0,0,0,0,0,0,0,0,0,0,0,0,0,0x3f803f80,0,0,0,0,0,0,0,0,0,0,0,0,0,0,0,0x3f803f80,0x3f803f80,16256,0,0,0x3f803f80,0x3f800000,0,0,0x3f803f80,0,16256,0,0x3f803f80,0,0x3f800000,0x3f803f80,0,0,0,0,0,0,0,0,0,0,0,0,0,0,0,0x3f803f80,0,0,0,0,0,0,0,0,0,0,0,0,0,0,0,0x3f803f80,0,0,0,0,0,0,0,0,0,0,0,0,0,0,0,0x3f803f80,0,0,0,0,0,0,0,0,0,0,0,0,0,0,0,0x3f803f80,0x3f803f80,16256,0,0,0x3f803f80,0x3f800000,0,0,0x3f803f80,0,16256,0,0x3f803f80,0,0x3f800000,0x3f803f80,0,0,0,0,0,0,0,0,0,0,0,0,0,0,0,0x3f803f80,0,0,0,0,0,0,0,0,0,0,0,0,0,0,0,0x3f803f80,0,0,0,0,0,0,0,0,0,0,0,0,0,0,0,0x3f803f80,0,0,0,0,0,0,0,0,0,0,0,0,0,0,0,0x3f803f80,0x3f803f80,16256,0,0,0x3f803f80,0x3f800000,0,0,0x3f803f80,0,16256,0,0x3f803f80,0,0x3f800000,0,64,0x90000,0xf0080,144,208,0x90010,0xf0090,288,352,0x90020,0xf00a0,432,496,0x90030,0xf00b0,576,512,0x90040,0xf00c0,720,656,0x90050,0xf00d0,864,800,0x90060,0xf00e0,1008,944,0x90070,0xf00f0,1024,1088,0x90190,0xf0210,1168,1232,0x90180,0xf0200,1312,1376,0x901b0,0xf0230,1456,1520,0x901a0,0xf0220,1600,1536,0x901d0,0xf0250,1744,1680,0x901c0,0xf0240,1888,1824,0x901f0,0xf0270,2032,1968,0x901e0,0xf0260,16,80,0x90320,0x11f7a0,128,192,0x90330,0x11f7b0,304,368,0x90300,0x11f780,416,480,0x90310,0x11f790,592,528,0x90360,0x11f7e0,704,640,0x90370,0x11f7f0,880,816,0x90340,0x11f7c0,992,928,0x90350,0x11f7d0,1040,1104,0x904b0,0x11f930,1152,1216,0x904a0,0x11f920,1328,1392,0x90490,0x11f910,1440,1504,0x90480,0x11f900,1616,1552,0x904f0,0x11f970,1728,1664,0x904e0,0x11f960,1904,1840,0x904d0,0x11f950,2016,1952,0x904c0,0x11f940,32,96,0xbfa40,0x11fac0,176,240,0xbfa50,0x11fad0,256,320,0xbfa60,0x11fae0,400,464,0xbfa70,0x11faf0,608,544,0xbfa00,0x11fa80,752,688,0xbfa10,0x11fa90,832,768,0xbfa20,0x11faa0,976,912,0xbfa30,0x11fab0,1056,1120,0xbfbd0,0x11fc50,1200,1264,0xbfbc0,0x11fc40,1280,1344,0xbfbf0,0x11fc70,1424,1488,0xbfbe0,0x11fc60,1632,1568,0xbfb90,0x11fc10,1776,1712,0xbfb80,0x11fc00,1856,1792,0xbfbb0,0x11fc30,2000,1936,0xbfba0,0x11fc20,48,112,0xbfd60,0x11fde0,160,224,0xbfd70,0x11fdf0,272,336,0xbfd40,0x11fdc0,384,448,0xbfd50,0x11fdd0,624,560,0xbfd20,0x11fda0,736,672,0xbfd30,0x11fdb0,848,784,0xbfd00,0x11fd80,960,896,0xbfd10,0x11fd90,1072,1136,0xbfef0,0x11ff70,1184,1248,0xbfee0,0x11ff60,1296,1360,0xbfed0,0x11ff50,1408,1472,0xbfec0,0x11ff40,1648,1584,0xbfeb0,0x11ff30,1760,1696,0xbfea0,0x11ff20,1872,1808,0xbfe90,0x11ff10,1984,1920,0xbfe80,0x11ff00,0x11fb40,76800,84992,91648,0x11fb50,76816,85008,91664,0x11fb60,76832,85024,91680,0x11fb70,76848,85040,91696,0x11fb00,76864,85056,91712,0x11fb10,76880,85072,91728,0x11fb20,76896,85088,91744,0x11fb30,76912,85104,91760,0x11fcd0,76928,85120,91776,0x11fcc0,76944,85136,91792,0x11fcf0,76960,85152,91808,0x11fce0,76976,85168,91824,0x11fc90,76992,85184,91840,0x11fc80,77008,85200,91856,0x11fcb0,77024,85216,91872,0x11fca0,77040,85232,91888,0x11fe60,77056,85248,91904,0x11fe70,77072,85264,91920,0x11fe40,77088,85280,91936,0x11fe50,77104,85296,91952,0x11fe20,77120,85312,91968,0x11fe30,77136,85328,91984,0x11fe00,77152,85344,92000,0x11fe10,77168,85360,92016,0x11fff0,77184,85376,92032,0x11ffe0,77200,85392,92048,0x11ffd0,77216,85408,92064,0x11ffc0,77232,85424,92080,0x11ffb0,77248,85440,92096,0x11ffa0,77264,85456,92112,0x11ff90,77280,85472,92128,0x11ff80,77296,85488,92144,0x11fb40,77312,85504,92160,0x11fb50,77328,85520,92176,0x11fb60,77344,85536,92192,0x11fb70,77360,85552,92208,0x11fb00,77376,85568,92224,0x11fb10,77392,85584,92240,0x11fb20,77408,85600,92256,0x11fb30,77424,85616,92272,0x11fcd0,77440,85632,92288,0x11fcc0,77456,85648,92304,0x11fcf0,77472,85664,92320,0x11fce0,77488,85680,92336,0x11fc90,77504,85696,92352,0x11fc80,77520,85712,92368,0x11fcb0,77536,85728,92384,0x11fca0,77552,85744,92400,0x11fe60,77568,85760,92416,0x11fe70,77584,85776,92432,0x11fe40,77600,85792,92448,0x11fe50,77616,85808,92464,0x11fe20,77632,85824,92480,0x11fe30,77648,85840,92496,0x11fe00,77664,85856,92512,0x11fe10,77680,85872,92528,0x11fff0,77696,85888,92544,0x11ffe0,77712,85904,92560,0x11ffd0,77728,85920,92576,0x11ffc0,77744,85936,92592,0x11ffb0,77760,85952,92608,0x11ffa0,77776,85968,92624,0x11ff90,77792,85984,92640,0x11ff80,77808,86000,92656,0x1c1d0,36864,46080,52232,0x1c1d4,36880,46096,52248,0x1c1d8,36896,46112,52264,0x1c1dc,36912,46128,52280,0x1c1e0,36928,46144,52296,0x1c1e4,36944,46160,52312,0x1c1e8,36960,46176,52328,0x1c1ec,36976,46192,52344,0x1c1f0,36992,46208,52360,0x1c1f4,37008,46224,52376,0x1c1f8,37024,46240,52392,0x1c1fc,37040,46256,52408,0x1c200,37056,46272,52424,0x1c204,37072,46288,52440,0x1c208,37088,46304,52456,0x1c20c,37104,46320,52472,0x1c210,37120,46336,52488,0x1c214,37136,46352,52504,0x1c218,37152,46368,52520,0x1c21c,37168,46384,52536,0x1c220,37184,46400,52552,0x1c224,37200,46416,52568,0x1c228,37216,46432,52584,0x1c22c,37232,46448,52600,0x1c230,37248,46464,52616,0x1c234,37264,46480,52632,0x1c238,37280,46496,52648,0x1c23c,37296,46512,52664,0x1c240,37312,46528,52680,0x1c244,37328,46544,52696,0x1c248,37344,46560,52712,0x1c24c,37360,46576,52728,0x1c250,37376,46592,52744,0x1c254,37392,46608,52760,0x1c258,37408,46624,52776,0x1c25c,37424,46640,52792,0x1c260,37440,46656,52808,0x1c264,37456,46672,52824,0x1c268,37472,46688,52840,0x1c26c,37488,46704,52856,0x1c270,37504,46720,52872,0x1c274,37520,46736,52888,0x1c278,37536,46752,52904,0x1c27c,37552,46768,52920,0x1c280,37568,46784,52936,0x1c284,37584,46800,52952,0x1c288,37600,46816,52968,0x1c28c,37616,46832,52984,0x1c290,37632,46848,53000,0x1c294,37648,46864,53016,0x1c298,37664,46880,53032,0x1c29c,37680,46896,53048,0x1c2a0,37696,46912,53064,0x1c2a4,37712,46928,53080,0x1c2a8,37728,46944,53096,0x1c2ac,37744,46960,53112,0x1c2b0,37760,46976,53128,0x1c2b4,37776,46992,53144,0x1c2b8,37792,47008,53160,0x1c2bc,37808,47024,53176,0x1c2c0,37824,47040,53192,0x1c2c4,37840,47056,53208,0x1c2c8,37856,47072,53224,0x1c2cc,37872,47088,53240,86048,86112,0x1d600,0x1d60c,86192,86256,0x1d610,0x1d61c,86272,86336,0x1d620,0x1d62c,86416,86480,0x1d630,0x1d63c,86624,86560,0x1d640,0x1d64c,86768,86704,0x1d650,0x1d65c,86848,86784,0x1d660,0x1d66c,86992,86928,0x1d670,0x1d67c,87072,87136,0x1d680,0x1d68c,87216,87280,0x1d690,0x1d69c,87296,87360,0x1d6a0,0x1d6ac,87440,87504,0x1d6b0,0x1d6bc,87648,87584,0x1d6c0,0x1d6cc,87792,87728,0x1d6d0,0x1d6dc,87872,87808,0x1d6e0,0x1d6ec,88016,87952,0x1d6f0,0x1d6fc,86064,86120,0x1d700,0x1d70c,86176,86264,0x1d710,0x1d71c,86288,86344,0x1d720,0x1d72c,86400,86488,0x1d730,0x1d73c,86640,86568,0x1d740,0x1d74c,86752,86712,0x1d750,0x1d75c,86864,86792,0x1d760,0x1d76c,86976,86936,0x1d770,0x1d77c,87088,87144,0x1d780,0x1d78c,87200,87288,0x1d790,0x1d79c,87312,87368,0x1d7a0,0x1d7ac,87424,87512,0x1d7b0,0x1d7bc,87664,87592,0x1d7c0,0x1d7cc,87776,87736,0x1d7d0,0x1d7dc,87888,87816,0x1d7e0,0x1d7ec,88000,87960,0x1d7f0,0x1d7fc,86080,86128,0x1d800,0x1d80c,86224,86240,0x1d810,0x1d81c,86368,86352,0x1d820,0x1d82c,86512,86464,0x1d830,0x1d83c,86528,86576,0x1d840,0x1d84c,86672,86688,0x1d850,0x1d85c,86816,86800,0x1d860,0x1d86c,86960,86912,0x1d870,0x1d87c,87104,87152,0x1d880,0x1d88c,87248,87264,0x1d890,0x1d89c,87392,87376,0x1d8a0,0x1d8ac,87536,87488,0x1d8b0,0x1d8bc,87552,87600,0x1d8c0,0x1d8cc,87696,87712,0x1d8d0,0x1d8dc,87840,87824,0x1d8e0,0x1d8ec,87984,87936,0x1d8f0,0x1d8fc,86096,86136,0x1d900,0x1d90c,86208,86248,0x1d910,0x1d91c,86384,86360,0x1d920,0x1d92c,86496,86472,0x1d930,0x1d93c,86544,86584,0x1d940,0x1d94c,86656,86696,0x1d950,0x1d95c,86832,86808,0x1d960,0x1d96c,86944,86920,0x1d970,0x1d97c,87120,87160,0x1d980,0x1d98c,87232,87272,0x1d990,0x1d99c,87408,87384,0x1d9a0,0x1d9ac,87520,87496,0x1d9b0,0x1d9bc,87568,87608,0x1d9c0,0x1d9cc,87680,87720,0x1d9d0,0x1d9dc,87856,87832,0x1d9e0,0x1d9ec,87968,87944,0x1d9f0,0x1d9fc,0x1e780,0x1e780,0x1ea40,0x100000,0x1e782,0x100000,0x100000,0x1ea40,0x1e784,0x100000,0x100000,0x100000,0x1e786,0x100000,0x100000,0x100000,0x1e790,0x1e780,0x100000,0x100000,0x1e792,0x100000,0x100000,0x1ea40,0x1e794,0x100000,0x100000,0x100000,0x1e796,0x100000,0x100000,0x100000,0x1e7a0,0x1e780,0x100000,0x100000,0x1e7a2,0x100000,0x100000,0x1ea40,0x1e7a4,0x100000,0x100000,0x100000,0x1e7a6,0x100000,0x100000,0x100000,0x1e7b0,0x1e780,0x100000,0x100000,0x1e7b2,0x100000,0x100000,0x1ea40,0x1e7b4,0x100000,0x100000,0x100000,0x1e7b6,0x100000,0x100000,0x100000,0x1e788,0x1e790,0x1ea70,0x100000,0x1e78a,0x100000,0x100000,0x1ea70,0x1e78c,0x100000,0x100000,0x100000,0x1e78e,0x100000,0x100000,0x100000,0x1e798,0x1e790,0x100000,0x100000,0x1e79a,0x100000,0x100000,0x1ea70,0x1e79c,0x100000,0x100000,0x100000,0x1e79e,0x100000,0x100000,0x100000,0x1e7a8,0x1e790,0x100000,0x100000,0x1e7aa,0x100000,0x100000,0x1ea70,0x1e7ac,0x100000,0x100000,0x100000,0x1e7ae,0x100000,0x100000,0x100000,0x1e7b8,0x1e790,0x100000,0x100000,0x1e7ba,0x100000,0x100000,0x1ea70,0x1e7bc,0x100000,0x100000,0x100000,0x1e7be,0x100000,0x100000,0x100000,0x1e7c0,0x1e7a0,0x1eaa0,0x100000,0x1e7c2,0x100000,0x100000,0x1eaa0,0x1e7c4,0x100000,0x100000,0x100000,0x1e7c6,0x100000,0x100000,0x100000,0x1e7d0,0x1e7a0,0x100000,0x100000,0x1e7d2,0x100000,0x100000,0x1eaa0,0x1e7d4,0x100000,0x100000,0x100000,0x1e7d6,0x100000,0x100000,0x100000,0x1e7e0,0x1e7a0,0x100000,0x100000,0x1e7e2,0x100000,0x100000,0x1eaa0,0x1e7e4,0x100000,0x100000,0x100000,0x1e7e6,0x100000,0x100000,0x100000,0x1e7f0,0x1e7a0,0x100000,0x100000,0x1e7f2,0x100000,0x100000,0x1eaa0,0x1e7f4,0x100000,0x100000,0x100000,0x1e7f6,0x100000,0x100000,0x100000,0x1e7c8,0x1e7b0,0x1ead0,0x100000,0x1e7ca,0x100000,0x100000,0x1ead0,0x1e7cc,0x100000,0x100000,0x100000,0x1e7ce,0x100000,0x100000,0x100000,0x1e7d8,0x1e7b0,0x100000,0x100000,0x1e7da,0x100000,0x100000,0x1ead0,0x1e7dc,0x100000,0x100000,0x100000,0x1e7de,0x100000,0x100000,0x100000,0x1e7e8,0x1e7b0,0x100000,0x100000,0x1e7ea,0x100000,0x100000,0x1ead0,0x1e7ec,0x100000,0x100000,0x100000,0x1e7ee,0x100000,0x100000,0x100000,0x1e7f8,0x1e7b0,0x100000,0x100000,0x1e7fa,0x100000,0x100000,0x1ead0,0x1e7fc,0x100000,0x100000,0x100000,0x1e7fe,0x100000,0x100000,0x100000,0x1ec20,0x25cf0,37152,37216,0x1ec22,0x25cf2,37168,37224,0x1ec24,0x25cf4,37184,37232,0x1ec26,0x25cf6,37200,37240,0x1ec28,0x25e50,37536,37600,0x1ec2a,0x25e52,37552,37608,0x1ec2c,0x25e54,37568,37616,0x1ec2e,0x25e56,37584,37624,0x1ec30,0x25fb0,37888,37952,0x1ec32,0x25fb2,37904,37960,0x1ec34,0x25fb4,37984,37968,0x1ec36,0x25fb6,38000,37976,0x1ec38,0x26110,38272,38336,0x1ec3a,0x26112,38288,38344,0x1ec3c,0x26114,38368,38352,0x1ec3e,0x26116,38384,38360,0x1ec20,0x25cf8,38752,38688,0x1ec22,0x25cfa,38768,38696,0x1ec24,0x25cfc,38656,38704,0x1ec26,0x25cfe,38672,38712,0x1ec28,0x25e58,39136,39072,0x1ec2a,0x25e5a,39152,39080,0x1ec2c,0x25e5c,39040,39088,0x1ec2e,0x25e5e,39056,39096,0x1ec30,0x25fb8,39488,39424,0x1ec32,0x25fba,39504,39432,0x1ec34,0x25fbc,39456,39440,0x1ec36,0x25fbe,39472,39448,0x1ec38,0x26118,39872,39808,0x1ec3a,0x2611a,39888,39816,0x1ec3c,0x2611c,39840,39824,0x1ec3e,0x2611e,39856,39832,0x1ec20,0x267f8,40224,40288,0x1ec22,0x267fa,40240,40296,0x1ec24,0x267fc,40256,40304,0x1ec26,0x267fe,40272,40312,0x1ec28,0x26958,40608,40672,0x1ec2a,0x2695a,40624,40680,0x1ec2c,0x2695c,40640,40688,0x1ec2e,0x2695e,40656,40696,0x1ec30,0x26ab8,40960,41024,0x1ec32,0x26aba,40976,41032,0x1ec34,0x26abc,41056,41040,0x1ec36,0x26abe,41072,41048,0x1ec38,0x26c18,41344,41408,0x1ec3a,0x26c1a,41360,41416,0x1ec3c,0x26c1c,41440,41424,0x1ec3e,0x26c1e,41456,41432,0x1ec20,0x100000,41824,41760,0x1ec22,0x100000,41840,41768,0x1ec24,0x100000,41728,41776,0x1ec26,0x100000,41744,41784,0x1ec28,0x100000,42208,42144,0x1ec2a,0x100000,42224,42152,0x1ec2c,0x100000,42112,42160,0x1ec2e,0x100000,42128,42168,0x1ec30,0x100000,42560,42496,0x1ec32,0x100000,42576,42504,0x1ec34,0x100000,42528,42512,0x1ec36,0x100000,42544,42520,0x1ec38,0x100000,42944,42880,0x1ec3a,0x100000,42960,42888,0x1ec3c,0x100000,42912,42896,0x1ec3e,0x100000,42928,42904,37152,37184,37216,64512,37160,37192,37224,64528,37168,37200,37232,64544,37176,37208,37240,64560,38304,38336,38368,64576,38312,38344,38376,64592,38320,38352,38384,64608,38328,38360,38392,64624,39424,39520,39488,64640,39432,39528,39496,64656,39440,39536,39504,64672,39448,39544,39512,64688,40576,40672,40640,64704,40584,40680,40648,64720,40592,40688,40656,64736,40600,40696,40664,64752,46432,46336,46368,64768,46440,46344,46376,64784,46448,46352,46384,64800,46456,46360,46392,64816,47584,47488,47520,64832,47592,47496,47528,64848,47600,47504,47536,64864,47608,47512,47544,64880,48704,48672,48640,64896,48712,48680,48648,64912,48720,48688,48656,64928,48728,48696,48664,64944,49856,49824,49792,64960,49864,49832,49800,64976,49872,49840,49808,64992,49880,49848,49816,65008,55584,55616,55648,65024,55592,55624,55656,65040,55600,55632,55664,65056,55608,55640,55672,65072,56736,56768,56800,65088,56744,56776,56808,65104,56752,56784,56816,65120,56760,56792,56824,65136,57856,57952,57920,65152,57864,57960,57928,65168,57872,57968,57936,65184,57880,57976,57944,65200,59008,59104,59072,65216,59016,59112,59080,65232,59024,59120,59088,65248,59032,59128,59096,65264,64864,64768,64800,65280,64872,64776,64808,65296,64880,64784,64816,65312,64888,64792,64824,65328,66016,65920,65952,65344,66024,65928,65960,65360,66032,65936,65968,65376,66040,65944,65976,65392,67136,67104,67072,65408,67144,67112,67080,65424,67152,67120,67088,65440,67160,67128,67096,65456,68288,68256,68224,65472,68296,68264,68232,65488,68304,68272,68240,65504,68312,68280,68248,65520,0x1e800,0x1e800,0x20cc0,55296,0x1e8c4,0x1e8c4,0x20cc4,55312,0x1e988,0x1e988,0x20cc8,55328,0x1ea4c,0x1ea4c,0x20ccc,55344,0x1eb10,0x1eb10,0x20cd0,55360,0x1ebd4,0x1ebd4,0x20cd4,55376,0x1ec98,0x1ec98,0x20cd8,55392,0x1ed5c,0x1ed5c,0x20cdc,55408,0x1ee20,0x1ee20,0x20ce0,55680,0x1eee4,0x1eee4,0x20ce4,55696,0x1efa8,0x1efa8,0x20ce8,55712,0x1f06c,0x1f06c,0x20cec,55728,0x1f130,0x1f130,0x20cf0,55744,0x1f1f4,0x1f1f4,0x20cf4,55760,0x1f2b8,0x1f2b8,0x20cf8,55776,0x1f37c,0x1f37c,0x20cfc,55792,0x1e810,0x1e800,0x20cc0,56064,0x1e8d4,0x1e8c4,0x20cc4,56080,0x1e998,0x1e988,0x20cc8,56096,0x1ea5c,0x1ea4c,0x20ccc,56112,0x1eb20,0x1eb10,0x20cd0,56128,0x1ebe4,0x1ebd4,0x20cd4,56144,0x1eca8,0x1ec98,0x20cd8,56160,0x1ed6c,0x1ed5c,0x20cdc,56176,0x1ee30,0x1ee20,0x20ce0,56448,0x1eef4,0x1eee4,0x20ce4,56464,0x1efb8,0x1efa8,0x20ce8,56480,0x1f07c,0x1f06c,0x20cec,56496,0x1f140,0x1f130,0x20cf0,56512,0x1f204,0x1f1f4,0x20cf4,56528,0x1f2c8,0x1f2b8,0x20cf8,56544,0x1f38c,0x1f37c,0x20cfc,56560,0x1e820,0x1e800,0x20cc0,56832,0x1e8e4,0x1e8c4,0x20cc4,56848,0x1e9a8,0x1e988,0x20cc8,56864,0x1ea6c,0x1ea4c,0x20ccc,56880,0x1eb30,0x1eb10,0x20cd0,56896,0x1ebf4,0x1ebd4,0x20cd4,56912,0x1ecb8,0x1ec98,0x20cd8,56928,0x1ed7c,0x1ed5c,0x20cdc,56944,0x1ee40,0x1ee20,0x20ce0,57216,0x1ef04,0x1eee4,0x20ce4,57232,0x1efc8,0x1efa8,0x20ce8,57248,0x1f08c,0x1f06c,0x20cec,57264,0x1f150,0x1f130,0x20cf0,57280,0x1f214,0x1f1f4,0x20cf4,57296,0x1f2d8,0x1f2b8,0x20cf8,57312,0x1f39c,0x1f37c,0x20cfc,57328,0x1e830,0x1e800,0x20cc0,57600,0x1e8f4,0x1e8c4,0x20cc4,57616,0x1e9b8,0x1e988,0x20cc8,57632,0x1ea7c,0x1ea4c,0x20ccc,57648,0x1eb40,0x1eb10,0x20cd0,57664,0x1ec04,0x1ebd4,0x20cd4,57680,0x1ecc8,0x1ec98,0x20cd8,57696,0x1ed8c,0x1ed5c,0x20cdc,57712,0x1ee50,0x1ee20,0x20ce0,57984,0x1ef14,0x1eee4,0x20ce4,58000,0x1efd8,0x1efa8,0x20ce8,58016,0x1f09c,0x1f06c,0x20cec,58032,0x1f160,0x1f130,0x20cf0,58048,0x1f224,0x1f1f4,0x20cf4,58064,0x1f2e8,0x1f2b8,0x20cf8,58080,0x1f3ac,0x1f37c,0x20cfc,58096,92160,0x27070,0x1ea40,0x1ec20,92176,0x27074,0x1eed0,0x1f0b0,92192,0x27078,0x1f360,0x1f540,92208,0x2707c,0x1f7f0,0x1f9d0,92224,0x27080,0x1fc80,0x1fe60,92240,0x27084,0x20110,0x202f0,92256,0x27088,0x205a0,0x20780,92272,0x2708c,0x20a30,0x20c10,92544,0x27090,0x20ec0,0x210a0,92560,0x27094,0x21350,0x21530,92576,0x27098,0x217e0,0x219c0,92592,0x2709c,0x21c70,0x21e50,92608,0x270a0,0x22100,0x222e0,92624,0x270a4,0x22590,0x22770,92640,0x270a8,0x22a20,0x22c00,92656,0x270ac,0x22eb0,0x23090,92960,0x270b0,0x1ea70,0x1ec28,92976,0x270b4,0x1ef00,0x1f0b8,92928,0x270b8,0x1f390,0x1f548,92944,0x270bc,0x1f820,0x1f9d8,93024,0x270c0,0x1fcb0,0x1fe68,93040,0x270c4,0x20140,0x202f8,92992,0x270c8,0x205d0,0x20788,93008,0x270cc,0x20a60,0x20c18,93344,0x270d0,0x20ef0,0x210a8,93360,0x270d4,0x21380,0x21538,93312,0x270d8,0x21810,0x219c8,93328,0x270dc,0x21ca0,0x21e58,93408,0x270e0,0x22130,0x222e8,93424,0x270e4,0x225c0,0x22778,93376,0x270e8,0x22a50,0x22c08,93392,0x270ec,0x22ee0,0x23098,93760,0x270f0,0x1eaa0,0x1ec30,93776,0x270f4,0x1ef30,0x1f0c0,93792,0x270f8,0x1f3c0,0x1f550,93808,0x270fc,0x1f850,0x1f9e0,93696,0x27100,0x1fce0,0x1fe70,93712,0x27104,0x20170,0x20300,93728,0x27108,0x20600,0x20790,93744,0x2710c,0x20a90,0x20c20,94144,0x27110,0x20f20,0x210b0,94160,0x27114,0x213b0,0x21540,94176,0x27118,0x21840,0x219d0,94192,0x2711c,0x21cd0,0x21e60,94080,0x27120,0x22160,0x222f0,94096,0x27124,0x225f0,0x22780,94112,0x27128,0x22a80,0x22c10,94128,0x2712c,0x22f10,0x230a0,94560,0x27130,0x1ead0,0x1ec38,94576,0x27134,0x1ef60,0x1f0c8,94528,0x27138,0x1f3f0,0x1f558,94544,0x2713c,0x1f880,0x1f9e8,94496,0x27140,0x1fd10,0x1fe78,94512,0x27144,0x201a0,0x20308,94464,0x27148,0x20630,0x20798,94480,0x2714c,0x20ac0,0x20c28,94944,0x27150,0x20f50,0x210b8,94960,0x27154,0x213e0,0x21548,94912,0x27158,0x21870,0x219d8,94928,0x2715c,0x21d00,0x21e68,94880,0x27160,0x22190,0x222f8,94896,0x27164,0x22620,0x22788,94848,0x27168,0x22ab0,0x22c18,94864,0x2716c,0x22f40,0x230a8,0x3f803f80,0x3f803f80,16256,0,0,0x3f803f80,0x3f800000,0,0,0x3f803f80,0,16256,0,0x3f803f80,0,0x3f800000,0x3f803f80,0,0,0,0,0,0,0,0,0,0,0,0,0,0,0,0x3f803f80,0,0,0,0,0,0,0,0,0,0,0,0,0,0,0,0x3f803f80,0,0,0,0,0,0,0,0,0,0,0,0,0,0,0,0x3f803f80,0,0,0,0,0,0,0,0,0,0,0,0,0,0,0,0x3f803f80,0x3f803f80,16256,0,0,0x3f803f80,0x3f800000,0,0,0x3f803f80,0,16256,0,0x3f803f80,0,0x3f800000,0x3f803f80,0,0,0,0,0,0,0,0,0,0,0,0,0,0,0,0x3f803f80,0,0,0,0,0,0,0,0,0,0,0,0,0,0,0,0x3f803f80,0,0,0,0,0,0,0,0,0,0,0,0,0,0,0,0x3f803f80,0,0,0,0,0,0,0,0,0,0,0,0,0,0,0,0x3f803f80,0x3f803f80,16256,0,0,0x3f803f80,0x3f800000,0,0,0x3f803f80,0,16256,0,0x3f803f80,0,0x3f800000,0x3f803f80,0,0,0,0,0,0,0,0,0,0,0,0,0,0,0,0x3f803f80,0,0,0,0,0,0,0,0,0,0,0,0,0,0,0,0x3f803f80,0,0,0,0,0,0,0,0,0,0,0,0,0,0,0,0x3f803f80,0,0,0,0,0,0,0,0,0,0,0,0,0,0,0,0x3f803f80,0x3f803f80,16256,0,0,0x3f803f80,0x3f800000,0,0,0x3f803f80,0,16256,0,0x3f803f80,0,0x3f800000,0xffffffff,0xffffffff,0,0,0,0,0xffffffff,0xff000000,0xff000000,0xffffff,0xff000000,0xff000000,0,4096,16384,0x1c000,0xffffffff,0xffffffff,0,0,0,0,0xffffff,0xffff0000,0xffff0000,65535,0xffffff,0xffffff,1024,5120,16896,0x1c0a0,0xffffffff,0xffffffff,0,0,0,0,65535,0xffffff00,0xff000000,0xffffff,0xffff0000,0xffff0000,2048,10240,17408,0x1c140,0xffffffff,0,0,0,0,0,255,0xffffffff,0xffff0000,65535,65535,65535,3072,11264,17920,0x1c1d0,0,0x3fb8aa3b,0x2a8cbccc,0x38fba882,0x7fff7fff,0x415db3d7,0xffff0000,0,0,65535,0,0xffff0000,0,0,0,0,0,808,1800,2608,3600,4408,5400,6208,7200,8192,9000,9992,10800,11792,12600,18432,14385,18447,16170,18462,18462,18462,18462,18462,20,19,0,0,0,0,0,0,0x1b0000,0x5b8480,0xae0a80,0xee8f00,0x1411500,0x1819980,0x1d41f80,0x214a400,0x2672a00,0x2b9b000,0x2fa3480,0x34cba80,0x38d3f00,0x3dfc500,0x4204980,0x60a80,0x4b2b680,0x6a800,0x5452380,0x74580,0x74580,0x74580,0x74580,0x74580,0x74580,0x74580,0x74580,0x74580,0x74580,0x74580,0x74580,0x74580,1,809,1801,2609,3601,4409,5401,6209,7201,8193,9001,9993,10801,11793,12601,13592,14386,15377,16171,17162,17671,17671,17671,17671,21,20,0,0,0,0,0,0,0x1b0a80,0x5b8f00,0xae1500,0xee9980,0x1411f80,0x181a400,0x1d42a00,0x214ae80,0x2673480,0x2b9ba80,0x2fa3f00,0x34cc500,0x38d4980,0x3dfcf80,0x4205400,0x472cf80,0x4b2c100,0x5053c80,0x5452e00,0x597a980,0x5c17500,0x5c17500,0x5c17500,0x5c17500,0x5c17500,0x5c17500,0x5c17500,0x5c17500,0x5c17500,0x5c17500,0x5c17500,0x5c17500,2,810,1802,2610,3602,4410,5402,6210,7202,8194,9002,9994,10802,11794,12602,13593,14387,15378,16172,17163,17672,18216,18216,18216,22,21,0,0,0,0,0,0,0x1b1500,0x5b9980,0xae1f80,0xeea400,0x1412a00,0x181ae80,0x1d43480,0x214b900,0x2673f00,0x2b9c500,0x2fa4980,0x34ccf80,0x38d5400,0x3dfda00,0x4205e80,0x472da00,0x4b2cb80,0x5054700,0x5453880,0x597b400,0x5c17f80,0x5ecba80,0x5ecba80,0x5ecba80,0x5ecba80,0x5ecba80,0x5ecba80,0x5ecba80,0x5ecba80,0x5ecba80,0x5ecba80,0x5ecba80,3,811,1803,2611,3603,4411,5403,6211,7203,8195,9003,9995,10803,11795,12603,13594,14388,15379,16173,17164,17673,17673,17673,17673,21,20,0,0,0,0,0,0,0x1b1f80,0x5ba400,0xae2a00,0xeeae80,0x1413480,0x181b900,0x1d43f00,0x214c380,0x2674980,0x2b9cf80,0x2fa5400,0x34cda00,0x38d5e80,0x3dfe480,0x4206900,0x472e480,0x4b2d600,0x5055180,0x5454300,0x597be80,0x5c18a00,0x5c18a00,0x5c18a00,0x5c18a00,0x5c18a00,0x5c18a00,0x5c18a00,0x5c18a00,0x5c18a00,0x5c18a00,0x5c18a00,0x5c18a00,4,812,1804,2612,3604,4412,5404,6212,7204,8196,9004,9996,10804,11796,12604,13595,14389,15380,16174,18463,17674,17674,17674,17674,21,20,0,0,0,0,0,0,0x1b2a00,0x5bae80,0xae3480,0xeeb900,0x1413f00,0x181c380,0x1d44980,0x214ce00,0x2675400,0x2b9da00,0x2fa5e80,0x34ce480,0x38d6900,0x3dfef00,0x4207380,0x472ef00,0x4b2e080,0x5055c00,0x5454d80,0x75000,0x5c19480,0x5c19480,0x5c19480,0x5c19480,0x5c19480,0x5c19480,0x5c19480,0x5c19480,0x5c19480,0x5c19480,0x5c19480,0x5c19480,5,813,1805,2613,3605,4413,5405,6213,7205,8197,9005,9997,10805,11797,12605,13596,14390,15381,16175,18464,18464,18464,18464,18464,20,19,0,0,0,0,0,0,0x1b3480,0x5bb900,0xae3f00,0xeec380,0x1414980,0x181ce00,0x1d45400,0x214d880,0x2675e80,0x2b9e480,0x2fa6900,0x34cef00,0x38d7380,0x3dff980,0x4207e00,0x472f980,0x4b2eb00,0x5056680,0x5455800,0x75a80,0x75a80,0x75a80,0x75a80,0x75a80,0x75a80,0x75a80,0x75a80,0x75a80,0x75a80,0x75a80,0x75a80,0x75a80,6,814,1806,2614,3606,4414,5406,6214,7206,8198,9006,9998,10806,11798,12606,13597,14391,15382,16176,18465,17675,17675,17675,17675,21,20,0,0,0,0,0,0,0x1b3f00,0x5bc380,0xae4980,0xeece00,0x1415400,0x181d880,0x1d45e80,0x214e300,0x2676900,0x2b9ef00,0x2fa7380,0x34cf980,0x38d7e00,0x3e00400,0x4208880,0x4730400,0x4b2f580,0x5057100,0x5456280,0x76500,0x5c19f00,0x5c19f00,0x5c19f00,0x5c19f00,0x5c19f00,0x5c19f00,0x5c19f00,0x5c19f00,0x5c19f00,0x5c19f00,0x5c19f00,0x5c19f00,7,815,1807,2615,3607,4415,5407,6215,7207,8199,9007,9999,10807,11799,12607,13598,14392,15383,16177,18466,18466,18466,18466,18466,20,19,0,0,0,0,0,0,0x1b4980,0x5bce00,0xae5400,0xeed880,0x1415e80,0x181e300,0x1d46900,0x214ed80,0x2677380,0x2b9f980,0x2fa7e00,0x34d0400,0x38d8880,0x3e00e80,0x4209300,0x4730e80,0x4b30000,0x5057b80,0x5456d00,0x76f80,0x76f80,0x76f80,0x76f80,0x76f80,0x76f80,0x76f80,0x76f80,0x76f80,0x76f80,0x76f80,0x76f80,0x76f80,8,816,1808,2616,3608,4416,5408,6400,7208,8200,9008,10000,10808,11800,12608,18433,14393,18448,16178,18467,18467,18467,18467,18467,20,19,0,0,0,0,0,0,0x1b5400,0x5bd880,0xae5e80,0xeee300,0x1416900,0x181ed80,0x1d47380,0x226f980,0x2677e00,0x2ba0400,0x2fa8880,0x34d0e80,0x38d9300,0x3e01900,0x4209d80,0x61500,0x4b30a80,0x6b280,0x5457780,0x77a00,0x77a00,0x77a00,0x77a00,0x77a00,0x77a00,0x77a00,0x77a00,0x77a00,0x77a00,0x77a00,0x77a00,0x77a00,9,817,1809,2617,3609,4417,5409,6401,7209,8201,9009,10001,10809,11801,12609,13599,14394,15384,16179,17165,17676,17676,17676,17676,21,20,0,0,0,0,0,0,0x1b5e80,0x5be300,0xae6900,0xeeed80,0x1417380,0x181f800,0x1d47e00,0x2270400,0x2678880,0x2ba0e80,0x2fa9300,0x34d1900,0x38d9d80,0x3e02380,0x420a800,0x4731900,0x4b31500,0x5058600,0x5458200,0x597c900,0x5c1a980,0x5c1a980,0x5c1a980,0x5c1a980,0x5c1a980,0x5c1a980,0x5c1a980,0x5c1a980,0x5c1a980,0x5c1a980,0x5c1a980,0x5c1a980,10,818,1810,2618,3610,4418,5410,6402,7210,8202,9010,10002,10810,11802,12610,13600,14395,15385,16180,17166,17677,18217,18217,18217,22,21,0,0,0,0,0,0,0x1b6900,0x5bed80,0xae7380,0xeef800,0x1417e00,0x1820280,0x1d48880,0x2270e80,0x2679300,0x2ba1900,0x2fa9d80,0x34d2380,0x38da800,0x3e02e00,0x420b280,0x4732380,0x4b31f80,0x5059080,0x5458c80,0x597d380,0x5c1b400,0x5ecc500,0x5ecc500,0x5ecc500,0x5ecc500,0x5ecc500,0x5ecc500,0x5ecc500,0x5ecc500,0x5ecc500,0x5ecc500,0x5ecc500,11,819,1811,2619,3611,4419,5411,6403,7211,8203,9011,10003,10811,11803,12611,13601,14396,15386,16181,17167,17678,17678,17678,17678,21,20,0,0,0,0,0,0,0x1b7380,0x5bf800,0xae7e00,0xef0280,0x1418880,0x1820d00,0x1d49300,0x2271900,0x2679d80,0x2ba2380,0x2faa800,0x34d2e00,0x38db280,0x3e03880,0x420bd00,0x4732e00,0x4b32a00,0x5059b00,0x5459700,0x597de00,0x5c1be80,0x5c1be80,0x5c1be80,0x5c1be80,0x5c1be80,0x5c1be80,0x5c1be80,0x5c1be80,0x5c1be80,0x5c1be80,0x5c1be80,0x5c1be80,12,820,1812,2620,3612,4420,5412,6404,7212,8204,9012,10004,10812,11804,12612,13602,14397,15387,16182,18468,17679,17679,17679,17679,21,20,0,0,0,0,0,0,0x1b7e00,0x5c0280,0xae8880,0xef0d00,0x1419300,0x1821780,0x1d49d80,0x2272380,0x267a800,0x2ba2e00,0x2fab280,0x34d3880,0x38dbd00,0x3e04300,0x420c780,0x4733880,0x4b33480,0x505a580,0x545a180,0x78480,0x5c1c900,0x5c1c900,0x5c1c900,0x5c1c900,0x5c1c900,0x5c1c900,0x5c1c900,0x5c1c900,0x5c1c900,0x5c1c900,0x5c1c900,0x5c1c900,13,821,1813,2621,3613,4421,5413,6405,7213,8205,9013,10005,10813,11805,12613,13603,14398,15388,16183,18469,18469,18469,18469,18469,20,19,0,0,0,0,0,0,0x1b8880,0x5c0d00,0xae9300,0xef1780,0x1419d80,0x1822200,0x1d4a800,0x2272e00,0x267b280,0x2ba3880,0x2fabd00,0x34d4300,0x38dc780,0x3e04d80,0x420d200,0x4734300,0x4b33f00,0x505b000,0x545ac00,0x78f00,0x78f00,0x78f00,0x78f00,0x78f00,0x78f00,0x78f00,0x78f00,0x78f00,0x78f00,0x78f00,0x78f00,0x78f00,14,822,1814,2622,3614,4422,5414,6406,7214,8206,9014,10006,10814,11806,12614,13604,14399,15389,16184,18470,17680,17680,17680,17680,21,20,0,0,0,0,0,0,0x1b9300,0x5c1780,0xae9d80,0xef2200,0x141a800,0x1822c80,0x1d4b280,0x2273880,0x267bd00,0x2ba4300,0x2fac780,0x34d4d80,0x38dd200,0x3e05800,0x420dc80,0x4734d80,0x4b34980,0x505ba80,0x545b680,0x79980,0x5c1d380,0x5c1d380,0x5c1d380,0x5c1d380,0x5c1d380,0x5c1d380,0x5c1d380,0x5c1d380,0x5c1d380,0x5c1d380,0x5c1d380,0x5c1d380,15,823,1815,2623,3615,4423,5415,6407,7215,8207,9015,10007,10815,11807,12615,13605,14400,15390,16185,18471,18471,18471,18471,18471,20,19,0,0,0,0,0,0,0x1b9d80,0x5c2200,0xaea800,0xef2c80,0x141b280,0x1823700,0x1d4bd00,0x2274300,0x267c780,0x2ba4d80,0x2fad200,0x34d5800,0x38ddc80,0x3e06280,0x420e700,0x4735800,0x4b35400,0x505c500,0x545c100,0x7a400,0x7a400,0x7a400,0x7a400,0x7a400,0x7a400,0x7a400,0x7a400,0x7a400,0x7a400,0x7a400,0x7a400,0x7a400,16,824,1816,2624,3616,4608,5416,6408,7216,8208,9016,10008,10816,11808,12800,18434,14401,18449,16186,18472,18472,18472,18472,18472,20,19,0,0,0,0,0,0,0x1ba800,0x5c2c80,0xaeb280,0xef3700,0x141bd00,0x1944300,0x1d4c780,0x2274d80,0x267d200,0x2ba5800,0x2fadc80,0x34d6280,0x38de700,0x3e06d00,0x432f300,0x61f80,0x4b35e80,0x6bd00,0x545cb80,0x7ae80,0x7ae80,0x7ae80,0x7ae80,0x7ae80,0x7ae80,0x7ae80,0x7ae80,0x7ae80,0x7ae80,0x7ae80,0x7ae80,0x7ae80,17,825,1817,2625,3617,4609,5417,6409,7217,8209,9017,10009,10817,11809,12801,13606,14402,15391,16187,17168,17681,17681,17681,17681,21,20,0,0,0,0,0,0,0x1bb280,0x5c3700,0xaebd00,0xef4180,0x141c780,0x1944d80,0x1d4d200,0x2275800,0x267dc80,0x2ba6280,0x2fae700,0x34d6d00,0x38df180,0x3e07780,0x432fd80,0x4736280,0x4b36900,0x505cf80,0x545d600,0x597e880,0x5c1de00,0x5c1de00,0x5c1de00,0x5c1de00,0x5c1de00,0x5c1de00,0x5c1de00,0x5c1de00,0x5c1de00,0x5c1de00,0x5c1de00,0x5c1de00,18,826,1818,2626,3618,4610,5418,6410,7218,8210,9018,10010,10818,11810,12802,13607,14403,15392,16188,17169,17682,18218,18218,18218,22,21,0,0,0,0,0,0,0x1bbd00,0x5c4180,0xaec780,0xef4c00,0x141d200,0x1945800,0x1d4dc80,0x2276280,0x267e700,0x2ba6d00,0x2faf180,0x34d7780,0x38dfc00,0x3e08200,0x4330800,0x4736d00,0x4b37380,0x505da00,0x545e080,0x597f300,0x5c1e880,0x5eccf80,0x5eccf80,0x5eccf80,0x5eccf80,0x5eccf80,0x5eccf80,0x5eccf80,0x5eccf80,0x5eccf80,0x5eccf80,0x5eccf80,19,827,1819,2627,3619,4611,5419,6411,7219,8211,9019,10011,10819,11811,12803,13608,14404,15393,16189,17170,17683,17683,17683,17683,21,20,0,0,0,0,0,0,0x1bc780,0x5c4c00,0xaed200,0xef5680,0x141dc80,0x1946280,0x1d4e700,0x2276d00,0x267f180,0x2ba7780,0x2fafc00,0x34d8200,0x38e0680,0x3e08c80,0x4331280,0x4737780,0x4b37e00,0x505e480,0x545eb00,0x597fd80,0x5c1f300,0x5c1f300,0x5c1f300,0x5c1f300,0x5c1f300,0x5c1f300,0x5c1f300,0x5c1f300,0x5c1f300,0x5c1f300,0x5c1f300,0x5c1f300,20,828,1820,2628,3620,4612,5420,6412,7220,8212,9020,10012,10820,11812,12804,13609,14405,15394,16190,18473,17684,17684,17684,17684,21,20,0,0,0,0,0,0,0x1bd200,0x5c5680,0xaedc80,0xef6100,0x141e700,0x1946d00,0x1d4f180,0x2277780,0x267fc00,0x2ba8200,0x2fb0680,0x34d8c80,0x38e1100,0x3e09700,0x4331d00,0x4738200,0x4b38880,0x505ef00,0x545f580,0x7b900,0x5c1fd80,0x5c1fd80,0x5c1fd80,0x5c1fd80,0x5c1fd80,0x5c1fd80,0x5c1fd80,0x5c1fd80,0x5c1fd80,0x5c1fd80,0x5c1fd80,0x5c1fd80,21,829,1821,2629,3621,4613,5421,6413,7221,8213,9021,10013,10821,11813,12805,13610,14406,15395,16191,18474,18474,18474,18474,18474,20,19,0,0,0,0,0,0,0x1bdc80,0x5c6100,0xaee700,0xef6b80,0x141f180,0x1947780,0x1d4fc00,0x2278200,0x2680680,0x2ba8c80,0x2fb1100,0x34d9700,0x38e1b80,0x3e0a180,0x4332780,0x4738c80,0x4b39300,0x505f980,0x5460000,0x7c380,0x7c380,0x7c380,0x7c380,0x7c380,0x7c380,0x7c380,0x7c380,0x7c380,0x7c380,0x7c380,0x7c380,0x7c380,22,830,1822,2630,3622,4614,5422,6414,7222,8214,9022,10014,10822,11814,12806,13611,14407,15396,16192,18475,17685,17685,17685,17685,21,20,0,0,0,0,0,0,0x1be700,0x5c6b80,0xaef180,0xef7600,0x141fc00,0x1948200,0x1d50680,0x2278c80,0x2681100,0x2ba9700,0x2fb1b80,0x34da180,0x38e2600,0x3e0ac00,0x4333200,0x4739700,0x4b39d80,0x5060400,0x5460a80,0x7ce00,0x5c20800,0x5c20800,0x5c20800,0x5c20800,0x5c20800,0x5c20800,0x5c20800,0x5c20800,0x5c20800,0x5c20800,0x5c20800,0x5c20800,23,831,1823,2631,3623,4615,5423,6415,7223,8215,9023,10015,10823,11815,12807,13612,14592,15397,16193,18476,18476,18476,18476,18476,20,19,0,0,0,0,0,0,0x1bf180,0x5c7600,0xaefc00,0xef8080,0x1420680,0x1948c80,0x1d51100,0x2279700,0x2681b80,0x2baa180,0x2fb2600,0x34dac00,0x38e3080,0x3e0b680,0x4333c80,0x473a180,0x4c5a980,0x5060e80,0x5461500,0x7d880,0x7d880,0x7d880,0x7d880,0x7d880,0x7d880,0x7d880,0x7d880,0x7d880,0x7d880,0x7d880,0x7d880,0x7d880,24,832,1824,2816,3624,4616,5424,6416,7224,8216,9024,10016,11008,11816,12808,18435,14593,18450,16194,18477,18477,18477,18477,18477,20,19,0,0,0,0,0,0,0x1bfc00,0x5c8080,0xaf0680,0x1018c80,0x1421100,0x1949700,0x1d51b80,0x227a180,0x2682600,0x2baac00,0x2fb3080,0x34db680,0x3a03c80,0x3e0c100,0x4334700,0x62a00,0x4c5b400,0x6c780,0x5461f80,0x7e300,0x7e300,0x7e300,0x7e300,0x7e300,0x7e300,0x7e300,0x7e300,0x7e300,0x7e300,0x7e300,0x7e300,0x7e300,25,833,1825,2817,3625,4617,5425,6417,7225,8217,9025,10017,11009,11817,12809,13613,14594,15398,16195,17171,17686,17686,17686,17686,21,20,0,0,0,0,0,0,0x1c0680,0x5c8b00,0xaf1100,0x1019700,0x1421b80,0x194a180,0x1d52600,0x227ac00,0x2683080,0x2bab680,0x2fb3b00,0x34dc100,0x3a04700,0x3e0cb80,0x4335180,0x473ac00,0x4c5be80,0x5061900,0x5462a00,0x5980800,0x5c21280,0x5c21280,0x5c21280,0x5c21280,0x5c21280,0x5c21280,0x5c21280,0x5c21280,0x5c21280,0x5c21280,0x5c21280,0x5c21280,26,834,1826,2818,3626,4618,5426,6418,7226,8218,9026,10018,11010,11818,12810,13614,14595,15399,16196,17172,17687,18219,18219,18219,22,21,0,0,0,0,0,0,0x1c1100,0x5c9580,0xaf1b80,0x101a180,0x1422600,0x194ac00,0x1d53080,0x227b680,0x2683b00,0x2bac100,0x2fb4580,0x34dcb80,0x3a05180,0x3e0d600,0x4335c00,0x473b680,0x4c5c900,0x5062380,0x5463480,0x5981280,0x5c21d00,0x5ecda00,0x5ecda00,0x5ecda00,0x5ecda00,0x5ecda00,0x5ecda00,0x5ecda00,0x5ecda00,0x5ecda00,0x5ecda00,0x5ecda00,27,835,1827,2819,3627,4619,5427,6419,7227,8219,9027,10019,11011,11819,12811,13615,14596,15400,16197,17173,17688,17688,17688,17688,21,20,0,0,0,0,0,0,0x1c1b80,0x5ca000,0xaf2600,0x101ac00,0x1423080,0x194b680,0x1d53b00,0x227c100,0x2684580,0x2bacb80,0x2fb5000,0x34dd600,0x3a05c00,0x3e0e080,0x4336680,0x473c100,0x4c5d380,0x5062e00,0x5463f00,0x5981d00,0x5c22780,0x5c22780,0x5c22780,0x5c22780,0x5c22780,0x5c22780,0x5c22780,0x5c22780,0x5c22780,0x5c22780,0x5c22780,0x5c22780,28,836,1828,2820,3628,4620,5428,6420,7228,8220,9028,10020,11012,11820,12812,13616,14597,15401,16198,18478,17689,17689,17689,17689,21,20,0,0,0,0,0,0,0x1c2600,0x5caa80,0xaf3080,0x101b680,0x1423b00,0x194c100,0x1d54580,0x227cb80,0x2685000,0x2bad600,0x2fb5a80,0x34de080,0x3a06680,0x3e0eb00,0x4337100,0x473cb80,0x4c5de00,0x5063880,0x5464980,0x7ed80,0x5c23200,0x5c23200,0x5c23200,0x5c23200,0x5c23200,0x5c23200,0x5c23200,0x5c23200,0x5c23200,0x5c23200,0x5c23200,0x5c23200,29,837,1829,2821,3629,4621,5429,6421,7229,8221,9029,10021,11013,11821,12813,13617,14598,15402,16199,18479,18479,18479,18479,18479,20,19,0,0,0,0,0,0,0x1c3080,0x5cb500,0xaf3b00,0x101c100,0x1424580,0x194cb80,0x1d55000,0x227d600,0x2685a80,0x2bae080,0x2fb6500,0x34deb00,0x3a07100,0x3e0f580,0x4337b80,0x473d600,0x4c5e880,0x5064300,0x5465400,0x7f800,0x7f800,0x7f800,0x7f800,0x7f800,0x7f800,0x7f800,0x7f800,0x7f800,0x7f800,0x7f800,0x7f800,0x7f800,30,838,1830,2822,3630,4622,5430,6422,7230,8222,9030,10022,11014,11822,12814,13618,14599,15403,16384,18480,17690,17690,17690,17690,21,20,0,0,0,0,0,0,0x1c3b00,0x5cbf80,0xaf4580,0x101cb80,0x1425000,0x194d600,0x1d55a80,0x227e080,0x2686500,0x2baeb00,0x2fb6f80,0x34df580,0x3a07b80,0x3e10000,0x4338600,0x473e080,0x4c5f300,0x5064d80,0x5586000,0x80280,0x5c23c80,0x5c23c80,0x5c23c80,0x5c23c80,0x5c23c80,0x5c23c80,0x5c23c80,0x5c23c80,0x5c23c80,0x5c23c80,0x5c23c80,0x5c23c80,31,839,1831,2823,3631,4623,5431,6423,7231,8223,9031,10023,11015,11823,12815,13619,14600,15404,16385,18481,18481,18481,18481,18481,20,19,0,0,0,0,0,0,0x1c4580,0x5cca00,0xaf5000,0x101d600,0x1425a80,0x194e080,0x1d56500,0x227eb00,0x2686f80,0x2baf580,0x2fb7a00,0x34e0000,0x3a08600,0x3e10a80,0x4339080,0x473eb00,0x4c5fd80,0x5065800,0x5586a80,0x80d00,0x80d00,0x80d00,0x80d00,0x80d00,0x80d00,0x80d00,0x80d00,0x80d00,0x80d00,0x80d00,0x80d00,0x80d00,32,1024,1832,2824,3632,4624,5432,6424,7232,8224,9216,10024,11016,11824,12816,18436,14601,18451,16386,18482,18482,18482,18482,18482,20,19,0,0,0,0,0,0,0x1c5000,0x6ed600,0xaf5a80,0x101e080,0x1426500,0x194eb00,0x1d56f80,0x227f580,0x2687a00,0x2bb0000,0x30d8600,0x34e0a80,0x3a09080,0x3e11500,0x4339b00,0x63480,0x4c60800,0x6d200,0x5587500,0x81780,0x81780,0x81780,0x81780,0x81780,0x81780,0x81780,0x81780,0x81780,0x81780,0x81780,0x81780,0x81780,33,1025,1833,2825,3633,4625,5433,6425,7233,8225,9217,10025,11017,11825,12817,13620,14602,15405,16387,17174,17691,17691,17691,17691,21,20,0,0,0,0,0,0,0x1c5a80,0x6ee080,0xaf6500,0x101eb00,0x1426f80,0x194f580,0x1d57a00,0x2280000,0x2688480,0x2bb0a80,0x30d9080,0x34e1500,0x3a09b00,0x3e11f80,0x433a580,0x473f580,0x4c61280,0x5066280,0x5587f80,0x5982780,0x5c24700,0x5c24700,0x5c24700,0x5c24700,0x5c24700,0x5c24700,0x5c24700,0x5c24700,0x5c24700,0x5c24700,0x5c24700,0x5c24700,34,1026,1834,2826,3634,4626,5434,6426,7234,8226,9218,10026,11018,11826,12818,13621,14603,15406,16388,17175,17692,18220,18220,18220,22,21,0,0,0,0,0,0,0x1c6500,0x6eeb00,0xaf6f80,0x101f580,0x1427a00,0x1950000,0x1d58480,0x2280a80,0x2688f00,0x2bb1500,0x30d9b00,0x34e1f80,0x3a0a580,0x3e12a00,0x433b000,0x4740000,0x4c61d00,0x5066d00,0x5588a00,0x5983200,0x5c25180,0x5ece480,0x5ece480,0x5ece480,0x5ece480,0x5ece480,0x5ece480,0x5ece480,0x5ece480,0x5ece480,0x5ece480,0x5ece480,35,1027,1835,2827,3635,4627,5435,6427,7235,8227,9219,10027,11019,11827,12819,13622,14604,15407,16389,17176,17693,17693,17693,17693,21,20,0,0,0,0,0,0,0x1c6f80,0x6ef580,0xaf7a00,0x1020000,0x1428480,0x1950a80,0x1d58f00,0x2281500,0x2689980,0x2bb1f80,0x30da580,0x34e2a00,0x3a0b000,0x3e13480,0x433ba80,0x4740a80,0x4c62780,0x5067780,0x5589480,0x5983c80,0x5c25c00,0x5c25c00,0x5c25c00,0x5c25c00,0x5c25c00,0x5c25c00,0x5c25c00,0x5c25c00,0x5c25c00,0x5c25c00,0x5c25c00,0x5c25c00,36,1028,1836,2828,3636,4628,5436,6428,7236,8228,9220,10028,11020,11828,12820,13623,14605,15408,16390,17177,17694,17694,17694,17694,21,20,0,0,0,0,0,0,0x1c7a00,0x6f0000,0xaf8480,0x1020a80,0x1428f00,0x1951500,0x1d59980,0x2281f80,0x268a400,0x2bb2a00,0x30db000,0x34e3480,0x3a0ba80,0x3e13f00,0x433c500,0x4741500,0x4c63200,0x5068200,0x5589f00,0x5984700,0x5c26680,0x5c26680,0x5c26680,0x5c26680,0x5c26680,0x5c26680,0x5c26680,0x5c26680,0x5c26680,0x5c26680,0x5c26680,0x5c26680,37,1029,1837,2829,3637,4629,5437,6429,7237,8229,9221,10029,11021,11829,12821,13624,14606,15409,16391,18483,18483,18483,18483,18483,20,19,0,0,0,0,0,0,0x1c8480,0x6f0a80,0xaf8f00,0x1021500,0x1429980,0x1951f80,0x1d5a400,0x2282a00,0x268ae80,0x2bb3480,0x30dba80,0x34e3f00,0x3a0c500,0x3e14980,0x433cf80,0x4741f80,0x4c63c80,0x5068c80,0x558a980,0x82200,0x82200,0x82200,0x82200,0x82200,0x82200,0x82200,0x82200,0x82200,0x82200,0x82200,0x82200,0x82200,38,1030,1838,2830,3638,4630,5438,6430,7238,8230,9222,10030,11022,11830,12822,13625,14607,15410,16392,18484,17695,17695,17695,17695,21,20,0,0,0,0,0,0,0x1c8f00,0x6f1500,0xaf9980,0x1021f80,0x142a400,0x1952a00,0x1d5ae80,0x2283480,0x268b900,0x2bb3f00,0x30dc500,0x34e4980,0x3a0cf80,0x3e15400,0x433da00,0x4742a00,0x4c64700,0x5069700,0x558b400,0x82c80,0x5c27100,0x5c27100,0x5c27100,0x5c27100,0x5c27100,0x5c27100,0x5c27100,0x5c27100,0x5c27100,0x5c27100,0x5c27100,0x5c27100,39,1031,1839,2831,3639,4631,5439,6431,7239,8231,9223,10031,11023,11831,12823,13626,14608,15411,16393,18485,18485,18485,18485,18485,20,19,0,0,0,0,0,0,0x1c9980,0x6f1f80,0xafa400,0x1022a00,0x142ae80,0x1953480,0x1d5b900,0x2283f00,0x268c380,0x2bb4980,0x30dcf80,0x34e5400,0x3a0da00,0x3e15e80,0x433e480,0x4743480,0x4c65180,0x506a180,0x558be80,0x83700,0x83700,0x83700,0x83700,0x83700,0x83700,0x83700,0x83700,0x83700,0x83700,0x83700,0x83700,0x83700,40,1032,1840,2832,3640,4632,5440,6432,7424,8232,9224,10032,11024,11832,12824,18437,14609,18452,16394,18486,18486,18486,18486,18486,20,19,0,0,0,0,0,0,0x1ca400,0x6f2a00,0xafae80,0x1023480,0x142b900,0x1953f00,0x1d5c380,0x2284980,0x27acf80,0x2bb5400,0x30dda00,0x34e5e80,0x3a0e480,0x3e16900,0x433ef00,0x63f00,0x4c65c00,0x6dc80,0x558c900,0x84180,0x84180,0x84180,0x84180,0x84180,0x84180,0x84180,0x84180,0x84180,0x84180,0x84180,0x84180,0x84180,41,1033,1841,2833,3641,4633,5441,6433,7425,8233,9225,10033,11025,11833,12825,13627,14610,15412,16395,17178,17696,17696,17696,17696,21,20,0,0,0,0,0,0,0x1cae80,0x6f3480,0xafb900,0x1023f00,0x142c380,0x1954980,0x1d5ce00,0x2285400,0x27ada00,0x2bb5e80,0x30de480,0x34e6900,0x3a0ef00,0x3e17380,0x433f980,0x4743f00,0x4c66680,0x506ac00,0x558d380,0x5985180,0x5c27b80,0x5c27b80,0x5c27b80,0x5c27b80,0x5c27b80,0x5c27b80,0x5c27b80,0x5c27b80,0x5c27b80,0x5c27b80,0x5c27b80,0x5c27b80,42,1034,1842,2834,3642,4634,5442,6434,7426,8234,9226,10034,11026,11834,12826,13628,14611,15413,16396,17179,17697,18221,18221,18221,22,21,0,0,0,0,0,0,0x1cb900,0x6f3f00,0xafc380,0x1024980,0x142ce00,0x1955400,0x1d5d880,0x2285e80,0x27ae480,0x2bb6900,0x30def00,0x34e7380,0x3a0f980,0x3e17e00,0x4340400,0x4744980,0x4c67100,0x506b680,0x558de00,0x5985c00,0x5c28600,0x5ecef00,0x5ecef00,0x5ecef00,0x5ecef00,0x5ecef00,0x5ecef00,0x5ecef00,0x5ecef00,0x5ecef00,0x5ecef00,0x5ecef00,43,1035,1843,2835,3643,4635,5443,6435,7427,8235,9227,10035,11027,11835,12827,13629,14612,15414,16397,17180,17698,17698,17698,17698,21,20,0,0,0,0,0,0,0x1cc380,0x6f4980,0xafce00,0x1025400,0x142d880,0x1955e80,0x1d5e300,0x2286900,0x27aef00,0x2bb7380,0x30df980,0x34e7e00,0x3a10400,0x3e18880,0x4340e80,0x4745400,0x4c67b80,0x506c100,0x558e880,0x5986680,0x5c29080,0x5c29080,0x5c29080,0x5c29080,0x5c29080,0x5c29080,0x5c29080,0x5c29080,0x5c29080,0x5c29080,0x5c29080,0x5c29080,44,1036,1844,2836,3644,4636,5444,6436,7428,8236,9228,10036,11028,11836,12828,13630,14613,15415,16398,17181,17699,17699,17699,17699,21,20,0,0,0,0,0,0,0x1cce00,0x6f5400,0xafd880,0x1025e80,0x142e300,0x1956900,0x1d5ed80,0x2287380,0x27af980,0x2bb7e00,0x30e0400,0x34e8880,0x3a10e80,0x3e19300,0x4341900,0x4745e80,0x4c68600,0x506cb80,0x558f300,0x5987100,0x5c29b00,0x5c29b00,0x5c29b00,0x5c29b00,0x5c29b00,0x5c29b00,0x5c29b00,0x5c29b00,0x5c29b00,0x5c29b00,0x5c29b00,0x5c29b00,45,1037,1845,2837,3645,4637,5445,6437,7429,8237,9229,10037,11029,11837,12829,13631,14614,15416,16399,18487,18487,18487,18487,18487,20,19,0,0,0,0,0,0,0x1cd880,0x6f5e80,0xafe300,0x1026900,0x142ed80,0x1957380,0x1d5f800,0x2287e00,0x27b0400,0x2bb8880,0x30e0e80,0x34e9300,0x3a11900,0x3e19d80,0x4342380,0x4746900,0x4c69080,0x506d600,0x558fd80,0x84c00,0x84c00,0x84c00,0x84c00,0x84c00,0x84c00,0x84c00,0x84c00,0x84c00,0x84c00,0x84c00,0x84c00,0x84c00,46,1038,1846,2838,3646,4638,5446,6438,7430,8238,9230,10038,11030,11838,12830,13632,14615,15417,16400,18488,17700,17700,17700,17700,21,20,0,0,0,0,0,0,0x1ce300,0x6f6900,0xafed80,0x1027380,0x142f800,0x1957e00,0x1d60280,0x2288880,0x27b0e80,0x2bb9300,0x30e1900,0x34e9d80,0x3a12380,0x3e1a800,0x4342e00,0x4747380,0x4c69b00,0x506e080,0x5590800,0x85680,0x5c2a580,0x5c2a580,0x5c2a580,0x5c2a580,0x5c2a580,0x5c2a580,0x5c2a580,0x5c2a580,0x5c2a580,0x5c2a580,0x5c2a580,0x5c2a580,47,1039,1847,2839,3647,4639,5447,6439,7431,8239,9231,10039,11031,11839,12831,13633,14616,15418,16401,18489,18489,18489,18489,18489,20,19,0,0,0,0,0,0,0x1ced80,0x6f7380,0xaff800,0x1027e00,0x1430280,0x1958880,0x1d60d00,0x2289300,0x27b1900,0x2bb9d80,0x30e2380,0x34ea800,0x3a12e00,0x3e1b280,0x4343880,0x4747e00,0x4c6a580,0x506eb00,0x5591280,0x86100,0x86100,0x86100,0x86100,0x86100,0x86100,0x86100,0x86100,0x86100,0x86100,0x86100,0x86100,0x86100,48,1040,1848,2840,3648,4640,5632,6440,7432,8240,9232,10040,11032,11840,12832,18438,14617,18453,16402,18490,18490,18490,18490,18490,20,19,0,0,0,0,0,0,0x1cf800,0x6f7e00,0xb00280,0x1028880,0x1430d00,0x1959300,0x1e81900,0x2289d80,0x27b2380,0x2bba800,0x30e2e00,0x34eb280,0x3a13880,0x3e1bd00,0x4344300,0x64980,0x4c6b000,0x6e700,0x5591d00,0x86b80,0x86b80,0x86b80,0x86b80,0x86b80,0x86b80,0x86b80,0x86b80,0x86b80,0x86b80,0x86b80,0x86b80,0x86b80,49,1041,1849,2841,3649,4641,5633,6441,7433,8241,9233,10041,11033,11841,12833,13634,14618,15419,16403,17182,17701,17701,17701,17701,21,20,0,0,0,0,0,0,0x1d0280,0x6f8880,0xb00d00,0x1029300,0x1431780,0x1959d80,0x1e82380,0x228a800,0x27b2e00,0x2bbb280,0x30e3880,0x34ebd00,0x3a14300,0x3e1c780,0x4344d80,0x4748880,0x4c6ba80,0x506f580,0x5592780,0x5987b80,0x5c2b000,0x5c2b000,0x5c2b000,0x5c2b000,0x5c2b000,0x5c2b000,0x5c2b000,0x5c2b000,0x5c2b000,0x5c2b000,0x5c2b000,0x5c2b000,50,1042,1850,2842,3650,4642,5634,6442,7434,8242,9234,10042,11034,11842,12834,13635,14619,15420,16404,17183,17702,18222,18222,18222,22,21,0,0,0,0,0,0,0x1d0d00,0x6f9300,0xb01780,0x1029d80,0x1432200,0x195a800,0x1e82e00,0x228b280,0x27b3880,0x2bbbd00,0x30e4300,0x34ec780,0x3a14d80,0x3e1d200,0x4345800,0x4749300,0x4c6c500,0x5070000,0x5593200,0x5988600,0x5c2ba80,0x5ecf980,0x5ecf980,0x5ecf980,0x5ecf980,0x5ecf980,0x5ecf980,0x5ecf980,0x5ecf980,0x5ecf980,0x5ecf980,0x5ecf980,51,1043,1851,2843,3651,4643,5635,6443,7435,8243,9235,10043,11035,11843,12835,13636,14620,15421,16405,17184,17703,17703,17703,17703,21,20,0,0,0,0,0,0,0x1d1780,0x6f9d80,0xb02200,0x102a800,0x1432c80,0x195b280,0x1e83880,0x228bd00,0x27b4300,0x2bbc780,0x30e4d80,0x34ed200,0x3a15800,0x3e1dc80,0x4346280,0x4749d80,0x4c6cf80,0x5070a80,0x5593c80,0x5989080,0x5c2c500,0x5c2c500,0x5c2c500,0x5c2c500,0x5c2c500,0x5c2c500,0x5c2c500,0x5c2c500,0x5c2c500,0x5c2c500,0x5c2c500,0x5c2c500,52,1044,1852,2844,3652,4644,5636,6444,7436,8244,9236,10044,11036,11844,12836,13637,14621,15422,16406,17185,17704,17704,17704,17704,21,20,0,0,0,0,0,0,0x1d2200,0x6fa800,0xb02c80,0x102b280,0x1433700,0x195bd00,0x1e84300,0x228c780,0x27b4d80,0x2bbd200,0x30e5800,0x34edc80,0x3a16280,0x3e1e700,0x4346d00,0x474a800,0x4c6da00,0x5071500,0x5594700,0x5989b00,0x5c2cf80,0x5c2cf80,0x5c2cf80,0x5c2cf80,0x5c2cf80,0x5c2cf80,0x5c2cf80,0x5c2cf80,0x5c2cf80,0x5c2cf80,0x5c2cf80,0x5c2cf80,53,1045,1853,2845,3653,4645,5637,6445,7437,8245,9237,10045,11037,11845,12837,13638,14622,15423,16407,18491,18491,18491,18491,18491,20,19,0,0,0,0,0,0,0x1d2c80,0x6fb280,0xb03700,0x102bd00,0x1434180,0x195c780,0x1e84d80,0x228d200,0x27b5800,0x2bbdc80,0x30e6280,0x34ee700,0x3a16d00,0x3e1f180,0x4347780,0x474b280,0x4c6e480,0x5071f80,0x5595180,0x87600,0x87600,0x87600,0x87600,0x87600,0x87600,0x87600,0x87600,0x87600,0x87600,0x87600,0x87600,0x87600,54,1046,1854,2846,3654,4646,5638,6446,7438,8246,9238,10046,11038,11846,12838,13639,14623,15424,16408,18492,17705,17705,17705,17705,21,20,0,0,0,0,0,0,0x1d3700,0x6fbd00,0xb04180,0x102c780,0x1434c00,0x195d200,0x1e85800,0x228dc80,0x27b6280,0x2bbe700,0x30e6d00,0x34ef180,0x3a17780,0x3e1fc00,0x4348200,0x474bd00,0x4c6ef00,0x5072a00,0x5595c00,0x88080,0x5c2da00,0x5c2da00,0x5c2da00,0x5c2da00,0x5c2da00,0x5c2da00,0x5c2da00,0x5c2da00,0x5c2da00,0x5c2da00,0x5c2da00,0x5c2da00,55,1047,1855,2847,3655,4647,5639,6447,7439,8247,9239,10047,11039,11847,12839,13824,14624,15425,16409,18493,18493,18493,18493,18493,20,19,0,0,0,0,0,0,0x1d4180,0x6fc780,0xb04c00,0x102d200,0x1435680,0x195dc80,0x1e86280,0x228e700,0x27b6d00,0x2bbf180,0x30e7780,0x34efc00,0x3a18200,0x3e20680,0x4348c80,0x486c900,0x4c6f980,0x5073480,0x5596680,0x88b00,0x88b00,0x88b00,0x88b00,0x88b00,0x88b00,0x88b00,0x88b00,0x88b00,0x88b00,0x88b00,0x88b00,0x88b00,56,1048,1856,2848,3840,4648,5640,6448,7440,8248,9240,10048,11040,12032,12840,18439,14625,18454,16410,18494,18494,18494,18494,18494,20,19,0,0,0,0,0,0,0x1d4c00,0x6fd200,0xb05680,0x102dc80,0x1556280,0x195e700,0x1e86d00,0x228f180,0x27b7780,0x2bbfc00,0x30e8200,0x34f0680,0x3a18c80,0x3f41280,0x4349700,0x65400,0x4c70400,0x6f180,0x5597100,0x89580,0x89580,0x89580,0x89580,0x89580,0x89580,0x89580,0x89580,0x89580,0x89580,0x89580,0x89580,0x89580,57,1049,1857,2849,3841,4649,5641,6449,7441,8249,9241,10049,11041,12033,12841,13825,14626,15426,16411,17186,17706,17706,17706,17706,21,20,0,0,0,0,0,0,0x1d5680,0x6fdc80,0xb06100,0x102e700,0x1556d00,0x195f180,0x1e87780,0x228fc00,0x27b8200,0x2bc0680,0x30e8c80,0x34f1100,0x3a19700,0x3f41d00,0x434a180,0x486d380,0x4c70e80,0x5073f00,0x5597b80,0x598a580,0x5c2e480,0x5c2e480,0x5c2e480,0x5c2e480,0x5c2e480,0x5c2e480,0x5c2e480,0x5c2e480,0x5c2e480,0x5c2e480,0x5c2e480,0x5c2e480,58,1050,1858,2850,3842,4650,5642,6450,7442,8250,9242,10050,11042,12034,12842,13826,14627,15427,16412,17187,17707,18223,18223,18223,22,21,0,0,0,0,0,0,0x1d6100,0x6fe700,0xb06b80,0x102f180,0x1557780,0x195fc00,0x1e88200,0x2290680,0x27b8c80,0x2bc1100,0x30e9700,0x34f1b80,0x3a1a180,0x3f42780,0x434ac00,0x486de00,0x4c71900,0x5074980,0x5598600,0x598b000,0x5c2ef00,0x5ed0400,0x5ed0400,0x5ed0400,0x5ed0400,0x5ed0400,0x5ed0400,0x5ed0400,0x5ed0400,0x5ed0400,0x5ed0400,0x5ed0400,59,1051,1859,2851,3843,4651,5643,6451,7443,8251,9243,10051,11043,12035,12843,13827,14628,15428,16413,17188,17708,17708,17708,17708,21,20,0,0,0,0,0,0,0x1d6b80,0x6ff180,0xb07600,0x102fc00,0x1558200,0x1960680,0x1e88c80,0x2291100,0x27b9700,0x2bc1b80,0x30ea180,0x34f2600,0x3a1ac00,0x3f43200,0x434b680,0x486e880,0x4c72380,0x5075400,0x5599080,0x598ba80,0x5c2f980,0x5c2f980,0x5c2f980,0x5c2f980,0x5c2f980,0x5c2f980,0x5c2f980,0x5c2f980,0x5c2f980,0x5c2f980,0x5c2f980,0x5c2f980,60,1052,1860,2852,3844,4652,5644,6452,7444,8252,9244,10052,11044,12036,12844,13828,14629,15429,16414,17189,17709,17709,17709,17709,21,20,0,0,0,0,0,0,0x1d7600,0x6ffc00,0xb08080,0x1030680,0x1558c80,0x1961100,0x1e89700,0x2291b80,0x27ba180,0x2bc2600,0x30eac00,0x34f3080,0x3a1b680,0x3f43c80,0x434c100,0x486f300,0x4c72e00,0x5075e80,0x5599b00,0x598c500,0x5c30400,0x5c30400,0x5c30400,0x5c30400,0x5c30400,0x5c30400,0x5c30400,0x5c30400,0x5c30400,0x5c30400,0x5c30400,0x5c30400,61,1053,1861,2853,3845,4653,5645,6453,7445,8253,9245,10053,11045,12037,12845,13829,14630,15430,16415,18495,18495,18495,18495,18495,20,19,0,0,0,0,0,0,0x1d8080,0x700680,0xb08b00,0x1031100,0x1559700,0x1961b80,0x1e8a180,0x2292600,0x27bac00,0x2bc3080,0x30eb680,0x34f3b00,0x3a1c100,0x3f44700,0x434cb80,0x486fd80,0x4c73880,0x5076900,0x559a580,0x8a000,0x8a000,0x8a000,0x8a000,0x8a000,0x8a000,0x8a000,0x8a000,0x8a000,0x8a000,0x8a000,0x8a000,0x8a000,62,1054,1862,2854,3846,4654,5646,6454,7446,8254,9246,10054,11046,12038,12846,13830,14631,15431,16416,18496,17710,17710,17710,17710,21,20,0,0,0,0,0,0,0x1d8b00,0x701100,0xb09580,0x1031b80,0x155a180,0x1962600,0x1e8ac00,0x2293080,0x27bb680,0x2bc3b00,0x30ec100,0x34f4580,0x3a1cb80,0x3f45180,0x434d600,0x4870800,0x4c74300,0x5077380,0x559b000,0x8aa80,0x5c30e80,0x5c30e80,0x5c30e80,0x5c30e80,0x5c30e80,0x5c30e80,0x5c30e80,0x5c30e80,0x5c30e80,0x5c30e80,0x5c30e80,0x5c30e80,63,1055,1863,2855,3847,4655,5647,6455,7447,8255,9247,10055,11047,12039,12847,13831,14632,15616,16417,18497,18497,18497,18497,18497,20,19,0,0,0,0,0,0,0x1d9580,0x701b80,0xb0a000,0x1032600,0x155ac00,0x1963080,0x1e8b680,0x2293b00,0x27bc100,0x2bc4580,0x30ecb80,0x34f5000,0x3a1d600,0x3f45c00,0x434e080,0x4871280,0x4c74d80,0x5197f80,0x559ba80,0x8b500,0x8b500,0x8b500,0x8b500,0x8b500,0x8b500,0x8b500,0x8b500,0x8b500,0x8b500,0x8b500,0x8b500,0x8b500,64,1056,2048,2856,3848,4656,5648,6456,7448,8256,9248,10240,11048,12040,12848,18440,14633,18455,16418,18498,18498,18498,18498,18498,20,19,0,0,0,0,0,0,0x1da000,0x702600,0xc2ac00,0x1033080,0x155b680,0x1963b00,0x1e8c100,0x2294580,0x27bcb80,0x2bc5000,0x30ed600,0x3615c00,0x3a1e080,0x3f46680,0x434eb00,0x65e80,0x4c75800,0x6fc00,0x559c500,0x8bf80,0x8bf80,0x8bf80,0x8bf80,0x8bf80,0x8bf80,0x8bf80,0x8bf80,0x8bf80,0x8bf80,0x8bf80,0x8bf80,0x8bf80,65,1057,2049,2857,3849,4657,5649,6457,7449,8257,9249,10241,11049,12041,12849,13832,14634,15617,16419,17190,17711,17711,17711,17711,21,20,0,0,0,0,0,0,0x1daa80,0x703080,0xc2b680,0x1033b00,0x155c100,0x1964580,0x1e8cb80,0x2295000,0x27bd600,0x2bc5a80,0x30ee080,0x3616680,0x3a1eb00,0x3f47100,0x434f580,0x4871d00,0x4c76280,0x5198a00,0x559cf80,0x598cf80,0x5c31900,0x5c31900,0x5c31900,0x5c31900,0x5c31900,0x5c31900,0x5c31900,0x5c31900,0x5c31900,0x5c31900,0x5c31900,0x5c31900,66,1058,2050,2858,3850,4658,5650,6458,7450,8258,9250,10242,11050,12042,12850,13833,14635,15618,16420,17191,17712,18224,18224,18224,22,21,0,0,0,0,0,0,0x1db500,0x703b00,0xc2c100,0x1034580,0x155cb80,0x1965000,0x1e8d600,0x2295a80,0x27be080,0x2bc6500,0x30eeb00,0x3617100,0x3a1f580,0x3f47b80,0x4350000,0x4872780,0x4c76d00,0x5199480,0x559da00,0x598da00,0x5c32380,0x5ed0e80,0x5ed0e80,0x5ed0e80,0x5ed0e80,0x5ed0e80,0x5ed0e80,0x5ed0e80,0x5ed0e80,0x5ed0e80,0x5ed0e80,0x5ed0e80,67,1059,2051,2859,3851,4659,5651,6459,7451,8259,9251,10243,11051,12043,12851,13834,14636,15619,16421,17192,17713,17713,17713,17713,21,20,0,0,0,0,0,0,0x1dbf80,0x704580,0xc2cb80,0x1035000,0x155d600,0x1965a80,0x1e8e080,0x2296500,0x27beb00,0x2bc6f80,0x30ef580,0x3617b80,0x3a20000,0x3f48600,0x4350a80,0x4873200,0x4c77780,0x5199f00,0x559e480,0x598e480,0x5c32e00,0x5c32e00,0x5c32e00,0x5c32e00,0x5c32e00,0x5c32e00,0x5c32e00,0x5c32e00,0x5c32e00,0x5c32e00,0x5c32e00,0x5c32e00,68,1060,2052,2860,3852,4660,5652,6460,7452,8260,9252,10244,11052,12044,12852,13835,14637,15620,16422,17193,17714,17714,17714,17714,21,20,0,0,0,0,0,0,0x1dca00,0x705000,0xc2d600,0x1035a80,0x155e080,0x1966500,0x1e8eb00,0x2296f80,0x27bf580,0x2bc7a00,0x30f0000,0x3618600,0x3a20a80,0x3f49080,0x4351500,0x4873c80,0x4c78200,0x519a980,0x559ef00,0x598ef00,0x5c33880,0x5c33880,0x5c33880,0x5c33880,0x5c33880,0x5c33880,0x5c33880,0x5c33880,0x5c33880,0x5c33880,0x5c33880,0x5c33880,69,1061,2053,2861,3853,4661,5653,6461,7453,8261,9253,10245,11053,12045,12853,13836,14638,15621,16423,18499,18499,18499,18499,18499,20,19,0,0,0,0,0,0,0x1dd480,0x705a80,0xc2e080,0x1036500,0x155eb00,0x1966f80,0x1e8f580,0x2297a00,0x27c0000,0x2bc8480,0x30f0a80,0x3619080,0x3a21500,0x3f49b00,0x4351f80,0x4874700,0x4c78c80,0x519b400,0x559f980,0x8ca00,0x8ca00,0x8ca00,0x8ca00,0x8ca00,0x8ca00,0x8ca00,0x8ca00,0x8ca00,0x8ca00,0x8ca00,0x8ca00,0x8ca00,70,1062,2054,2862,3854,4662,5654,6462,7454,8262,9254,10246,11054,12046,12854,13837,14639,15622,16424,18500,17715,17715,17715,17715,21,20,0,0,0,0,0,0,0x1ddf00,0x706500,0xc2eb00,0x1036f80,0x155f580,0x1967a00,0x1e90000,0x2298480,0x27c0a80,0x2bc8f00,0x30f1500,0x3619b00,0x3a21f80,0x3f4a580,0x4352a00,0x4875180,0x4c79700,0x519be80,0x55a0400,0x8d480,0x5c34300,0x5c34300,0x5c34300,0x5c34300,0x5c34300,0x5c34300,0x5c34300,0x5c34300,0x5c34300,0x5c34300,0x5c34300,0x5c34300,71,1063,2055,2863,3855,4663,5655,6463,7455,8263,9255,10247,11055,12047,12855,13838,14640,15623,16425,18501,18501,18501,18501,18501,20,19,0,0,0,0,0,0,0x1de980,0x706f80,0xc2f580,0x1037a00,0x1560000,0x1968480,0x1e90a80,0x2298f00,0x27c1500,0x2bc9980,0x30f1f80,0x361a580,0x3a22a00,0x3f4b000,0x4353480,0x4875c00,0x4c7a180,0x519c900,0x55a0e80,0x8df00,0x8df00,0x8df00,0x8df00,0x8df00,0x8df00,0x8df00,0x8df00,0x8df00,0x8df00,0x8df00,0x8df00,0x8df00,256,1064,2056,2864,3856,4664,5656,6464,7456,8448,9256,10248,11056,12048,12856,18441,14641,18456,16426,18502,18502,18502,18502,18502,20,19,0,0,0,0,0,0,0x2ff580,0x707a00,0xc30000,0x1038480,0x1560a80,0x1968f00,0x1e91500,0x2299980,0x27c1f80,0x2cea580,0x30f2a00,0x361b000,0x3a23480,0x3f4ba80,0x4353f00,0x66900,0x4c7ac00,0x70680,0x55a1900,0x8e980,0x8e980,0x8e980,0x8e980,0x8e980,0x8e980,0x8e980,0x8e980,0x8e980,0x8e980,0x8e980,0x8e980,0x8e980,257,1065,2057,2865,3857,4665,5657,6465,7457,8449,9257,10249,11057,12049,12857,13839,14642,15624,16427,17194,17716,17716,17716,17716,21,20,0,0,0,0,0,0,0x300000,0x708480,0xc30a80,0x1038f00,0x1561500,0x1969980,0x1e91f80,0x229a400,0x27c2a00,0x2ceb000,0x30f3480,0x361ba80,0x3a23f00,0x3f4c500,0x4354980,0x4876680,0x4c7b680,0x519d380,0x55a2380,0x598f980,0x5c34d80,0x5c34d80,0x5c34d80,0x5c34d80,0x5c34d80,0x5c34d80,0x5c34d80,0x5c34d80,0x5c34d80,0x5c34d80,0x5c34d80,0x5c34d80,258,1066,2058,2866,3858,4666,5658,6466,7458,8450,9258,10250,11058,12050,12858,13840,14643,15625,16428,17195,17717,18225,18225,18225,22,21,0,0,0,0,0,0,0x300a80,0x708f00,0xc31500,0x1039980,0x1561f80,0x196a400,0x1e92a00,0x229ae80,0x27c3480,0x2ceba80,0x30f3f00,0x361c500,0x3a24980,0x3f4cf80,0x4355400,0x4877100,0x4c7c100,0x519de00,0x55a2e00,0x5990400,0x5c35800,0x5ed1900,0x5ed1900,0x5ed1900,0x5ed1900,0x5ed1900,0x5ed1900,0x5ed1900,0x5ed1900,0x5ed1900,0x5ed1900,0x5ed1900,259,1067,2059,2867,3859,4667,5659,6467,7459,8451,9259,10251,11059,12051,12859,13841,14644,15626,16429,17196,17718,17718,17718,17718,21,20,0,0,0,0,0,0,0x301500,0x709980,0xc31f80,0x103a400,0x1562a00,0x196ae80,0x1e93480,0x229b900,0x27c3f00,0x2cec500,0x30f4980,0x361cf80,0x3a25400,0x3f4da00,0x4355e80,0x4877b80,0x4c7cb80,0x519e880,0x55a3880,0x5990e80,0x5c36280,0x5c36280,0x5c36280,0x5c36280,0x5c36280,0x5c36280,0x5c36280,0x5c36280,0x5c36280,0x5c36280,0x5c36280,0x5c36280,260,1068,2060,2868,3860,4668,5660,6468,7460,8452,9260,10252,11060,12052,12860,13842,14645,15627,16430,17197,17719,17719,17719,17719,21,20,0,0,0,0,0,0,0x301f80,0x70a400,0xc32a00,0x103ae80,0x1563480,0x196b900,0x1e93f00,0x229c380,0x27c4980,0x2cecf80,0x30f5400,0x361da00,0x3a25e80,0x3f4e480,0x4356900,0x4878600,0x4c7d600,0x519f300,0x55a4300,0x5991900,0x5c36d00,0x5c36d00,0x5c36d00,0x5c36d00,0x5c36d00,0x5c36d00,0x5c36d00,0x5c36d00,0x5c36d00,0x5c36d00,0x5c36d00,0x5c36d00,261,1069,2061,2869,3861,4669,5661,6469,7461,8453,9261,10253,11061,12053,12861,13843,14646,15628,16431,18503,18503,18503,18503,18503,20,19,0,0,0,0,0,0,0x302a00,0x70ae80,0xc33480,0x103b900,0x1563f00,0x196c380,0x1e94980,0x229ce00,0x27c5400,0x2ceda00,0x30f5e80,0x361e480,0x3a26900,0x3f4ef00,0x4357380,0x4879080,0x4c7e080,0x519fd80,0x55a4d80,0x8f400,0x8f400,0x8f400,0x8f400,0x8f400,0x8f400,0x8f400,0x8f400,0x8f400,0x8f400,0x8f400,0x8f400,0x8f400,262,1070,2062,2870,3862,4670,5662,6470,7462,8454,9262,10254,11062,12054,12862,13844,14647,15629,16432,18504,17720,17720,17720,17720,21,20,0,0,0,0,0,0,0x303480,0x70b900,0xc33f00,0x103c380,0x1564980,0x196ce00,0x1e95400,0x229d880,0x27c5e80,0x2cee480,0x30f6900,0x361ef00,0x3a27380,0x3f4f980,0x4357e00,0x4879b00,0x4c7eb00,0x51a0800,0x55a5800,0x60000,0x5c37780,0x5c37780,0x5c37780,0x5c37780,0x5c37780,0x5c37780,0x5c37780,0x5c37780,0x5c37780,0x5c37780,0x5c37780,0x5c37780,263,1071,2063,2871,3863,4671,5663,6471,7463,8455,9263,10255,11063,12055,12863,13845,14648,15630,16433,72,72,72,72,72,20,19,0,0,0,0,0,0,0x303f00,0x70c380,0xc34980,0x103ce00,0x1565400,0x196d880,0x1e95e80,0x229e300,0x27c6900,0x2ceef00,0x30f7380,0x361f980,0x3a27e00,0x3f50400,0x4358880,0x487a580,0x4c7f580,0x51a1280,0x55a6280,0x1af580,0x1af580,0x1af580,0x1af580,0x1af580,0x1af580,0x1af580,0x1af580,0x1af580,0x1af580,0x1af580,0x1af580,0x1af580,264,1072,2064,2872,3864,4672,5664,6656,7464,8456,9264,10256,11064,12056,12864,18442,14649,18457,16434,328,328,328,328,328,20,19,0,0,0,0,0,0,0x304980,0x70ce00,0xc35400,0x103d880,0x1565e80,0x196e300,0x1e96900,0x23bef00,0x27c7380,0x2cef980,0x30f7e00,0x3620400,0x3a28880,0x3f50e80,0x4359300,0x67380,0x4c80000,0x71100,0x55a6d00,0x2feb00,0x2feb00,0x2feb00,0x2feb00,0x2feb00,0x2feb00,0x2feb00,0x2feb00,0x2feb00,0x2feb00,0x2feb00,0x2feb00,0x2feb00,265,1073,2065,2873,3865,4673,5665,6657,7465,8457,9265,10257,11065,12057,12865,13846,14650,15631,16435,17198,17721,17721,17721,17721,21,20,0,0,0,0,0,0,0x305400,0x70d880,0xc35e80,0x103e300,0x1566900,0x196ed80,0x1e97380,0x23bf980,0x27c7e00,0x2cf0400,0x30f8880,0x3620e80,0x3a29300,0x3f51900,0x4359d80,0x487b000,0x4c80a80,0x51a1d00,0x55a7780,0x5992380,0x5c38200,0x5c38200,0x5c38200,0x5c38200,0x5c38200,0x5c38200,0x5c38200,0x5c38200,0x5c38200,0x5c38200,0x5c38200,0x5c38200,266,1074,2066,2874,3866,4674,5666,6658,7466,8458,9266,10258,11066,12058,12866,13847,14651,15632,16436,17199,17722,18226,18226,18226,22,21,0,0,0,0,0,0,0x305e80,0x70e300,0xc36900,0x103ed80,0x1567380,0x196f800,0x1e97e00,0x23c0400,0x27c8880,0x2cf0e80,0x30f9300,0x3621900,0x3a29d80,0x3f52380,0x435a800,0x487ba80,0x4c81500,0x51a2780,0x55a8200,0x5992e00,0x5c38c80,0x5ed2380,0x5ed2380,0x5ed2380,0x5ed2380,0x5ed2380,0x5ed2380,0x5ed2380,0x5ed2380,0x5ed2380,0x5ed2380,0x5ed2380,267,1075,2067,2875,3867,4675,5667,6659,7467,8459,9267,10259,11067,12059,12867,13848,14652,15633,16437,17200,17723,17723,17723,17723,21,20,0,0,0,0,0,0,0x306900,0x70ed80,0xc37380,0x103f800,0x1567e00,0x1970280,0x1e98880,0x23c0e80,0x27c9300,0x2cf1900,0x30f9d80,0x3622380,0x3a2a800,0x3f52e00,0x435b280,0x487c500,0x4c81f80,0x51a3200,0x55a8c80,0x5993880,0x5c39700,0x5c39700,0x5c39700,0x5c39700,0x5c39700,0x5c39700,0x5c39700,0x5c39700,0x5c39700,0x5c39700,0x5c39700,0x5c39700,268,1076,2068,2876,3868,4676,5668,6660,7468,8460,9268,10260,11068,12060,12868,13849,14653,15634,16438,17201,17724,17724,17724,17724,21,20,0,0,0,0,0,0,0x307380,0x70f800,0xc37e00,0x1040280,0x1568880,0x1970d00,0x1e99300,0x23c1900,0x27c9d80,0x2cf2380,0x30fa800,0x3622e00,0x3a2b280,0x3f53880,0x435bd00,0x487cf80,0x4c82a00,0x51a3c80,0x55a9700,0x5994300,0x5c3a180,0x5c3a180,0x5c3a180,0x5c3a180,0x5c3a180,0x5c3a180,0x5c3a180,0x5c3a180,0x5c3a180,0x5c3a180,0x5c3a180,0x5c3a180,269,1077,2069,2877,3869,4677,5669,6661,7469,8461,9269,10261,11069,12061,12869,13850,14654,15635,16439,584,584,584,584,584,20,19,0,0,0,0,0,0,0x307e00,0x710280,0xc38880,0x1040d00,0x1569300,0x1971780,0x1e99d80,0x23c2380,0x27ca800,0x2cf2e00,0x30fb280,0x3623880,0x3a2bd00,0x3f54300,0x435c780,0x487da00,0x4c83480,0x51a4700,0x55aa180,0x44e080,0x44e080,0x44e080,0x44e080,0x44e080,0x44e080,0x44e080,0x44e080,0x44e080,0x44e080,0x44e080,0x44e080,0x44e080,270,1078,2070,2878,3870,4678,5670,6662,7470,8462,9270,10262,11070,12062,12870,13851,14655,15636,16440,840,17725,17725,17725,17725,21,20,0,0,0,0,0,0,0x308880,0x710d00,0xc39300,0x1041780,0x1569d80,0x1972200,0x1e9a800,0x23c2e00,0x27cb280,0x2cf3880,0x30fbd00,0x3624300,0x3a2c780,0x3f54d80,0x435d200,0x487e480,0x4c83f00,0x51a5180,0x55aac00,0x59d600,0x5c3ac00,0x5c3ac00,0x5c3ac00,0x5c3ac00,0x5c3ac00,0x5c3ac00,0x5c3ac00,0x5c3ac00,0x5c3ac00,0x5c3ac00,0x5c3ac00,0x5c3ac00,271,1079,2071,2879,3871,4679,5671,6663,7471,8463,9271,10263,11071,12063,12871,13852,14656,15637,16441,1096,1096,1096,1096,1096,20,19,0,0,0,0,0,0,0x309300,0x711780,0xc39d80,0x1042200,0x156a800,0x1972c80,0x1e9b280,0x23c3880,0x27cbd00,0x2cf4300,0x30fc780,0x3624d80,0x3a2d200,0x3f55800,0x435dc80,0x487ef00,0x4c84980,0x51a5c00,0x55ab680,0x6ecb80,0x6ecb80,0x6ecb80,0x6ecb80,0x6ecb80,0x6ecb80,0x6ecb80,0x6ecb80,0x6ecb80,0x6ecb80,0x6ecb80,0x6ecb80,0x6ecb80,272,1080,2072,2880,3872,4864,5672,6664,7472,8464,9272,10264,11072,12064,13056,18443,14657,18458,16442,1352,1352,1352,1352,1352,20,19,0,0,0,0,0,0,0x309d80,0x712200,0xc3a800,0x1042c80,0x156b280,0x1a93880,0x1e9bd00,0x23c4300,0x27cc780,0x2cf4d80,0x30fd200,0x3625800,0x3a2dc80,0x3f56280,0x447e880,0x67e00,0x4c85400,0x71b80,0x55ac100,0x83c100,0x83c100,0x83c100,0x83c100,0x83c100,0x83c100,0x83c100,0x83c100,0x83c100,0x83c100,0x83c100,0x83c100,0x83c100,273,1081,2073,2881,3873,4865,5673,6665,7473,8465,9273,10265,11073,12065,13057,13853,14658,15638,16443,17202,17726,17726,17726,17726,21,20,0,0,0,0,0,0,0x30a800,0x712c80,0xc3b280,0x1043700,0x156bd00,0x1a94300,0x1e9c780,0x23c4d80,0x27cd200,0x2cf5800,0x30fdc80,0x3626280,0x3a2e700,0x3f56d00,0x447f300,0x487f980,0x4c85e80,0x51a6680,0x55acb80,0x5994d80,0x5c3b680,0x5c3b680,0x5c3b680,0x5c3b680,0x5c3b680,0x5c3b680,0x5c3b680,0x5c3b680,0x5c3b680,0x5c3b680,0x5c3b680,0x5c3b680,274,1082,2074,2882,3874,4866,5674,6666,7474,8466,9274,10266,11074,12066,13058,13854,14659,15639,16444,17203,17727,18227,18227,18227,22,21,0,0,0,0,0,0,0x30b280,0x713700,0xc3bd00,0x1044180,0x156c780,0x1a94d80,0x1e9d200,0x23c5800,0x27cdc80,0x2cf6280,0x30fe700,0x3626d00,0x3a2f180,0x3f57780,0x447fd80,0x4880400,0x4c86900,0x51a7100,0x55ad600,0x5995800,0x5c3c100,0x5ed2e00,0x5ed2e00,0x5ed2e00,0x5ed2e00,0x5ed2e00,0x5ed2e00,0x5ed2e00,0x5ed2e00,0x5ed2e00,0x5ed2e00,0x5ed2e00,275,1083,2075,2883,3875,4867,5675,6667,7475,8467,9275,10267,11075,12067,13059,13855,14660,15640,16445,17204,17728,17728,17728,17728,21,20,0,0,0,0,0,0,0x30bd00,0x714180,0xc3c780,0x1044c00,0x156d200,0x1a95800,0x1e9dc80,0x23c6280,0x27ce700,0x2cf6d00,0x30ff180,0x3627780,0x3a2fc00,0x3f58200,0x4480800,0x4880e80,0x4c87380,0x51a7b80,0x55ae080,0x5996280,0x5c3cb80,0x5c3cb80,0x5c3cb80,0x5c3cb80,0x5c3cb80,0x5c3cb80,0x5c3cb80,0x5c3cb80,0x5c3cb80,0x5c3cb80,0x5c3cb80,0x5c3cb80,276,1084,2076,2884,3876,4868,5676,6668,7476,8468,9276,10268,11076,12068,13060,13856,14661,15641,16446,17205,17729,17729,17729,17729,21,20,0,0,0,0,0,0,0x30c780,0x714c00,0xc3d200,0x1045680,0x156dc80,0x1a96280,0x1e9e700,0x23c6d00,0x27cf180,0x2cf7780,0x30ffc00,0x3628200,0x3a30680,0x3f58c80,0x4481280,0x4881900,0x4c87e00,0x51a8600,0x55aeb00,0x5996d00,0x5c3d600,0x5c3d600,0x5c3d600,0x5c3d600,0x5c3d600,0x5c3d600,0x5c3d600,0x5c3d600,0x5c3d600,0x5c3d600,0x5c3d600,0x5c3d600,277,1085,2077,2885,3877,4869,5677,6669,7477,8469,9277,10269,11077,12069,13061,13857,14662,15642,16447,1608,1608,1608,1608,1608,20,19,0,0,0,0,0,0,0x30d200,0x715680,0xc3dc80,0x1046100,0x156e700,0x1a96d00,0x1e9f180,0x23c7780,0x27cfc00,0x2cf8200,0x3100680,0x3628c80,0x3a31100,0x3f59700,0x4481d00,0x4882380,0x4c88880,0x51a9080,0x55af580,0x98b680,0x98b680,0x98b680,0x98b680,0x98b680,0x98b680,0x98b680,0x98b680,0x98b680,0x98b680,0x98b680,0x98b680,0x98b680,278,1086,2078,2886,3878,4870,5678,6670,7478,8470,9278,10270,11078,12070,13062,13858,14663,15643,16448,1864,17730,17730,17730,17730,21,20,0,0,0,0,0,0,0x30dc80,0x716100,0xc3e700,0x1046b80,0x156f180,0x1a97780,0x1e9fc00,0x23c8200,0x27d0680,0x2cf8c80,0x3101100,0x3629700,0x3a31b80,0x3f5a180,0x4482780,0x4882e00,0x4c89300,0x51a9b00,0x55b0000,0xadac00,0x5c3e080,0x5c3e080,0x5c3e080,0x5c3e080,0x5c3e080,0x5c3e080,0x5c3e080,0x5c3e080,0x5c3e080,0x5c3e080,0x5c3e080,0x5c3e080,279,1087,2079,2887,3879,4871,5679,6671,7479,8471,9279,10271,11079,12071,13063,13859,14848,15644,16449,2120,2120,2120,2120,2120,20,19,0,0,0,0,0,0,0x30e700,0x716b80,0xc3f180,0x1047600,0x156fc00,0x1a98200,0x1ea0680,0x23c8c80,0x27d1100,0x2cf9700,0x3101b80,0x362a180,0x3a32600,0x3f5ac00,0x4483200,0x4883880,0x4da9f00,0x51aa580,0x55b0a80,0xc2a180,0xc2a180,0xc2a180,0xc2a180,0xc2a180,0xc2a180,0xc2a180,0xc2a180,0xc2a180,0xc2a180,0xc2a180,0xc2a180,0xc2a180,280,1088,2080,3072,3880,4872,5680,6672,7480,8472,9280,10272,11264,12072,13064,18444,14849,18459,16450,2376,2376,2376,2376,2376,20,19,0,0,0,0,0,0,0x30f180,0x717600,0xc3fc00,0x1168200,0x1570680,0x1a98c80,0x1ea1100,0x23c9700,0x27d1b80,0x2cfa180,0x3102600,0x362ac00,0x3b53200,0x3f5b680,0x4483c80,0x68880,0x4daa980,0x72600,0x55b1500,0xd79700,0xd79700,0xd79700,0xd79700,0xd79700,0xd79700,0xd79700,0xd79700,0xd79700,0xd79700,0xd79700,0xd79700,0xd79700,281,1089,2081,3073,3881,4873,5681,6673,7481,8473,9281,10273,11265,12073,13065,13860,14850,15645,16451,17206,17731,17731,17731,17731,21,20,0,0,0,0,0,0,0x30fc00,0x718080,0xc40680,0x1168c80,0x1571100,0x1a99700,0x1ea1b80,0x23ca180,0x27d2600,0x2cfac00,0x3103080,0x362b680,0x3b53c80,0x3f5c100,0x4484700,0x4884300,0x4dab400,0x51ab000,0x55b1f80,0x5997780,0x5c3eb00,0x5c3eb00,0x5c3eb00,0x5c3eb00,0x5c3eb00,0x5c3eb00,0x5c3eb00,0x5c3eb00,0x5c3eb00,0x5c3eb00,0x5c3eb00,0x5c3eb00,282,1090,2082,3074,3882,4874,5682,6674,7482,8474,9282,10274,11266,12074,13066,13861,14851,15646,16452,17207,17732,18228,18228,18228,22,21,0,0,0,0,0,0,0x310680,0x718b00,0xc41100,0x1169700,0x1571b80,0x1a9a180,0x1ea2600,0x23cac00,0x27d3080,0x2cfb680,0x3103b00,0x362c100,0x3b54700,0x3f5cb80,0x4485180,0x4884d80,0x4dabe80,0x51aba80,0x55b2a00,0x5998200,0x5c3f580,0x5ed3880,0x5ed3880,0x5ed3880,0x5ed3880,0x5ed3880,0x5ed3880,0x5ed3880,0x5ed3880,0x5ed3880,0x5ed3880,0x5ed3880,283,1091,2083,3075,3883,4875,5683,6675,7483,8475,9283,10275,11267,12075,13067,13862,14852,15647,16453,17208,17733,17733,17733,17733,21,20,0,0,0,0,0,0,0x311100,0x719580,0xc41b80,0x116a180,0x1572600,0x1a9ac00,0x1ea3080,0x23cb680,0x27d3b00,0x2cfc100,0x3104580,0x362cb80,0x3b55180,0x3f5d600,0x4485c00,0x4885800,0x4dac900,0x51ac500,0x55b3480,0x5998c80,0x5c40000,0x5c40000,0x5c40000,0x5c40000,0x5c40000,0x5c40000,0x5c40000,0x5c40000,0x5c40000,0x5c40000,0x5c40000,0x5c40000,284,1092,2084,3076,3884,4876,5684,6676,7484,8476,9284,10276,11268,12076,13068,13863,14853,15648,16454,17209,17734,17734,17734,17734,21,20,0,0,0,0,0,0,0x311b80,0x71a000,0xc42600,0x116ac00,0x1573080,0x1a9b680,0x1ea3b00,0x23cc100,0x27d4580,0x2cfcb80,0x3105000,0x362d600,0x3b55c00,0x3f5e080,0x4486680,0x4886280,0x4dad380,0x51acf80,0x55b3f00,0x5999700,0x5c40a80,0x5c40a80,0x5c40a80,0x5c40a80,0x5c40a80,0x5c40a80,0x5c40a80,0x5c40a80,0x5c40a80,0x5c40a80,0x5c40a80,0x5c40a80,285,1093,2085,3077,3885,4877,5685,6677,7485,8477,9285,10277,11269,12077,13069,13864,14854,15649,16455,2632,2632,2632,2632,2632,20,19,0,0,0,0,0,0,0x312600,0x71aa80,0xc43080,0x116b680,0x1573b00,0x1a9c100,0x1ea4580,0x23ccb80,0x27d5000,0x2cfd600,0x3105a80,0x362e080,0x3b56680,0x3f5eb00,0x4487100,0x4886d00,0x4dade00,0x51ada00,0x55b4980,0xec8c80,0xec8c80,0xec8c80,0xec8c80,0xec8c80,0xec8c80,0xec8c80,0xec8c80,0xec8c80,0xec8c80,0xec8c80,0xec8c80,0xec8c80,286,1094,2086,3078,3886,4878,5686,6678,7486,8478,9286,10278,11270,12078,13070,13865,14855,15650,16640,2888,17735,17735,17735,17735,21,20,0,0,0,0,0,0,0x313080,0x71b500,0xc43b00,0x116c100,0x1574580,0x1a9cb80,0x1ea5000,0x23cd600,0x27d5a80,0x2cfe080,0x3106500,0x362eb00,0x3b57100,0x3f5f580,0x4487b80,0x4887780,0x4dae880,0x51ae480,0x56d5580,0x1018200,0x5c41500,0x5c41500,0x5c41500,0x5c41500,0x5c41500,0x5c41500,0x5c41500,0x5c41500,0x5c41500,0x5c41500,0x5c41500,0x5c41500,287,1095,2087,3079,3887,4879,5687,6679,7487,8479,9287,10279,11271,12079,13071,13866,14856,15651,16641,3144,3144,3144,3144,3144,20,19,0,0,0,0,0,0,0x313b00,0x71bf80,0xc44580,0x116cb80,0x1575000,0x1a9d600,0x1ea5a80,0x23ce080,0x27d6500,0x2cfeb00,0x3106f80,0x362f580,0x3b57b80,0x3f60000,0x4488600,0x4888200,0x4daf300,0x51aef00,0x56d6000,0x1167780,0x1167780,0x1167780,0x1167780,0x1167780,0x1167780,0x1167780,0x1167780,0x1167780,0x1167780,0x1167780,0x1167780,0x1167780,288,1280,2088,3080,3888,4880,5688,6680,7488,8480,9472,10280,11272,12080,13072,18445,14857,18460,16642,3400,3400,3400,3400,3400,20,19,0,0,0,0,0,0,0x314580,0x83cb80,0xc45000,0x116d600,0x1575a80,0x1a9e080,0x1ea6500,0x23ceb00,0x27d6f80,0x2cff580,0x3227b80,0x3630000,0x3b58600,0x3f60a80,0x4489080,0x69300,0x4dafd80,0x73080,0x56d6a80,0x12b6d00,0x12b6d00,0x12b6d00,0x12b6d00,0x12b6d00,0x12b6d00,0x12b6d00,0x12b6d00,0x12b6d00,0x12b6d00,0x12b6d00,0x12b6d00,0x12b6d00,289,1281,2089,3081,3889,4881,5689,6681,7489,8481,9473,10281,11273,12081,13073,13867,14858,15652,16643,17210,17920,17920,17920,17920,21,20,0,0,0,0,0,0,0x315000,0x83d600,0xc45a80,0x116e080,0x1576500,0x1a9eb00,0x1ea6f80,0x23cf580,0x27d7a00,0x2d00000,0x3228600,0x3630a80,0x3b59080,0x3f61500,0x4489b00,0x4888c80,0x4db0800,0x51af980,0x56d7500,0x599a180,0x5d62100,0x5d62100,0x5d62100,0x5d62100,0x5d62100,0x5d62100,0x5d62100,0x5d62100,0x5d62100,0x5d62100,0x5d62100,0x5d62100,290,1282,2090,3082,3890,4882,5690,6682,7490,8482,9474,10282,11274,12082,13074,13868,14859,15653,16644,17211,17921,18229,18229,18229,22,21,0,0,0,0,0,0,0x315a80,0x83e080,0xc46500,0x116eb00,0x1576f80,0x1a9f580,0x1ea7a00,0x23d0000,0x27d8480,0x2d00a80,0x3229080,0x3631500,0x3b59b00,0x3f61f80,0x448a580,0x4889700,0x4db1280,0x51b0400,0x56d7f80,0x599ac00,0x5d62b80,0x5ed4300,0x5ed4300,0x5ed4300,0x5ed4300,0x5ed4300,0x5ed4300,0x5ed4300,0x5ed4300,0x5ed4300,0x5ed4300,0x5ed4300,291,1283,2091,3083,3891,4883,5691,6683,7491,8483,9475,10283,11275,12083,13075,13869,14860,15654,16645,17212,17922,17922,17922,17922,21,20,0,0,0,0,0,0,0x316500,0x83eb00,0xc46f80,0x116f580,0x1577a00,0x1aa0000,0x1ea8480,0x23d0a80,0x27d8f00,0x2d01500,0x3229b00,0x3631f80,0x3b5a580,0x3f62a00,0x448b000,0x488a180,0x4db1d00,0x51b0e80,0x56d8a00,0x599b680,0x5d63600,0x5d63600,0x5d63600,0x5d63600,0x5d63600,0x5d63600,0x5d63600,0x5d63600,0x5d63600,0x5d63600,0x5d63600,0x5d63600,292,1284,2092,3084,3892,4884,5692,6684,7492,8484,9476,10284,11276,12084,13076,13870,14861,15655,16646,17213,17923,17923,17923,17923,21,20,0,0,0,0,0,0,0x316f80,0x83f580,0xc47a00,0x1170000,0x1578480,0x1aa0a80,0x1ea8f00,0x23d1500,0x27d9980,0x2d01f80,0x322a580,0x3632a00,0x3b5b000,0x3f63480,0x448ba80,0x488ac00,0x4db2780,0x51b1900,0x56d9480,0x599c100,0x5d64080,0x5d64080,0x5d64080,0x5d64080,0x5d64080,0x5d64080,0x5d64080,0x5d64080,0x5d64080,0x5d64080,0x5d64080,0x5d64080,293,1285,2093,3085,3893,4885,5693,6685,7493,8485,9477,10285,11277,12085,13077,13871,14862,15656,16647,3656,3656,3656,3656,3656,20,19,0,0,0,0,0,0,0x317a00,0x840000,0xc48480,0x1170a80,0x1578f00,0x1aa1500,0x1ea9980,0x23d1f80,0x27da400,0x2d02a00,0x322b000,0x3633480,0x3b5ba80,0x3f63f00,0x448c500,0x488b680,0x4db3200,0x51b2380,0x56d9f00,0x1406280,0x1406280,0x1406280,0x1406280,0x1406280,0x1406280,0x1406280,0x1406280,0x1406280,0x1406280,0x1406280,0x1406280,0x1406280,294,1286,2094,3086,3894,4886,5694,6686,7494,8486,9478,10286,11278,12086,13078,13872,14863,15657,16648,3912,17924,17924,17924,17924,21,20,0,0,0,0,0,0,0x318480,0x840a80,0xc48f00,0x1171500,0x1579980,0x1aa1f80,0x1eaa400,0x23d2a00,0x27dae80,0x2d03480,0x322ba80,0x3633f00,0x3b5c500,0x3f64980,0x448cf80,0x488c100,0x4db3c80,0x51b2e00,0x56da980,0x1555800,0x5d64b00,0x5d64b00,0x5d64b00,0x5d64b00,0x5d64b00,0x5d64b00,0x5d64b00,0x5d64b00,0x5d64b00,0x5d64b00,0x5d64b00,0x5d64b00,295,1287,2095,3087,3895,4887,5695,6687,7495,8487,9479,10287,11279,12087,13079,13873,14864,15658,16649,4168,4168,4168,4168,4168,20,19,0,0,0,0,0,0,0x318f00,0x841500,0xc49980,0x1171f80,0x157a400,0x1aa2a00,0x1eaae80,0x23d3480,0x27db900,0x2d03f00,0x322c500,0x3634980,0x3b5cf80,0x3f65400,0x448da00,0x488cb80,0x4db4700,0x51b3880,0x56db400,0x16a4d80,0x16a4d80,0x16a4d80,0x16a4d80,0x16a4d80,0x16a4d80,0x16a4d80,0x16a4d80,0x16a4d80,0x16a4d80,0x16a4d80,0x16a4d80,0x16a4d80,296,1288,2096,3088,3896,4888,5696,6688,7680,8488,9480,10288,11280,12088,13080,18446,14865,18461,16650,4424,4424,4424,4424,4424,20,19,0,0,0,0,0,0,0x319980,0x841f80,0xc4a400,0x1172a00,0x157ae80,0x1aa3480,0x1eab900,0x23d3f00,0x28fc500,0x2d04980,0x322cf80,0x3635400,0x3b5da00,0x3f65e80,0x448e480,0x69d80,0x4db5180,0x73b00,0x56dbe80,0x17f4300,0x17f4300,0x17f4300,0x17f4300,0x17f4300,0x17f4300,0x17f4300,0x17f4300,0x17f4300,0x17f4300,0x17f4300,0x17f4300,0x17f4300,297,1289,2097,3089,3897,4889,5697,6689,7681,8489,9481,10289,11281,12089,13081,13874,14866,15659,16651,17214,17925,17925,17925,17925,21,20,0,0,0,0,0,0,0x31a400,0x842a00,0xc4ae80,0x1173480,0x157b900,0x1aa3f00,0x1eac380,0x23d4980,0x28fcf80,0x2d05400,0x322da00,0x3635e80,0x3b5e480,0x3f66900,0x448ef00,0x488d600,0x4db5c00,0x51b4300,0x56dc900,0x599cb80,0x5d65580,0x5d65580,0x5d65580,0x5d65580,0x5d65580,0x5d65580,0x5d65580,0x5d65580,0x5d65580,0x5d65580,0x5d65580,0x5d65580,298,1290,2098,3090,3898,4890,5698,6690,7682,8490,9482,10290,11282,12090,13082,13875,14867,15660,16652,17215,17926,18230,18230,18230,22,21,0,0,0,0,0,0,0x31ae80,0x843480,0xc4b900,0x1173f00,0x157c380,0x1aa4980,0x1eace00,0x23d5400,0x28fda00,0x2d05e80,0x322e480,0x3636900,0x3b5ef00,0x3f67380,0x448f980,0x488e080,0x4db6680,0x51b4d80,0x56dd380,0x599d600,0x5d66000,0x5ed4d80,0x5ed4d80,0x5ed4d80,0x5ed4d80,0x5ed4d80,0x5ed4d80,0x5ed4d80,0x5ed4d80,0x5ed4d80,0x5ed4d80,0x5ed4d80,299,1291,2099,3091,3899,4891,5699,6691,7683,8491,9483,10291,11283,12091,13083,13876,14868,15661,16653,17216,17927,17927,17927,17927,21,20,0,0,0,0,0,0,0x31b900,0x843f00,0xc4c380,0x1174980,0x157ce00,0x1aa5400,0x1ead880,0x23d5e80,0x28fe480,0x2d06900,0x322ef00,0x3637380,0x3b5f980,0x3f67e00,0x4490400,0x488eb00,0x4db7100,0x51b5800,0x56dde00,0x599e080,0x5d66a80,0x5d66a80,0x5d66a80,0x5d66a80,0x5d66a80,0x5d66a80,0x5d66a80,0x5d66a80,0x5d66a80,0x5d66a80,0x5d66a80,0x5d66a80,300,1292,2100,3092,3900,4892,5700,6692,7684,8492,9484,10292,11284,12092,13084,13877,14869,15662,16654,17217,17928,17928,17928,17928,21,20,0,0,0,0,0,0,0x31c380,0x844980,0xc4ce00,0x1175400,0x157d880,0x1aa5e80,0x1eae300,0x23d6900,0x28fef00,0x2d07380,0x322f980,0x3637e00,0x3b60400,0x3f68880,0x4490e80,0x488f580,0x4db7b80,0x51b6280,0x56de880,0x599eb00,0x5d67500,0x5d67500,0x5d67500,0x5d67500,0x5d67500,0x5d67500,0x5d67500,0x5d67500,0x5d67500,0x5d67500,0x5d67500,0x5d67500,301,1293,2101,3093,3901,4893,5701,6693,7685,8493,9485,10293,11285,12093,13085,13878,14870,15663,16655,4680,4680,4680,4680,4680,20,19,0,0,0,0,0,0,0x31ce00,0x845400,0xc4d880,0x1175e80,0x157e300,0x1aa6900,0x1eaed80,0x23d7380,0x28ff980,0x2d07e00,0x3230400,0x3638880,0x3b60e80,0x3f69300,0x4491900,0x4890000,0x4db8600,0x51b6d00,0x56df300,0x1943880,0x1943880,0x1943880,0x1943880,0x1943880,0x1943880,0x1943880,0x1943880,0x1943880,0x1943880,0x1943880,0x1943880,0x1943880,302,1294,2102,3094,3902,4894,5702,6694,7686,8494,9486,10294,11286,12094,13086,13879,14871,15664,16656,4936,17929,17929,17929,17929,21,20,0,0,0,0,0,0,0x31d880,0x845e80,0xc4e300,0x1176900,0x157ed80,0x1aa7380,0x1eaf800,0x23d7e00,0x2900400,0x2d08880,0x3230e80,0x3639300,0x3b61900,0x3f69d80,0x4492380,0x4890a80,0x4db9080,0x51b7780,0x56dfd80,0x1a92e00,0x5d67f80,0x5d67f80,0x5d67f80,0x5d67f80,0x5d67f80,0x5d67f80,0x5d67f80,0x5d67f80,0x5d67f80,0x5d67f80,0x5d67f80,0x5d67f80,303,1295,2103,3095,3903,4895,5703,6695,7687,8495,9487,10295,11287,12095,13087,13880,14872,15665,16657,5192,5192,5192,5192,5192,20,19,0,0,0,0,0,0,0x31e300,0x846900,0xc4ed80,0x1177380,0x157f800,0x1aa7e00,0x1eb0280,0x23d8880,0x2900e80,0x2d09300,0x3231900,0x3639d80,0x3b62380,0x3f6a800,0x4492e00,0x4891500,0x4db9b00,0x51b8200,0x56e0800,0x1be2380,0x1be2380,0x1be2380,0x1be2380,0x1be2380,0x1be2380,0x1be2380,0x1be2380,0x1be2380,0x1be2380,0x1be2380,0x1be2380,0x1be2380,304,1296,2104,3096,3904,4896,5888,6696,7688,8496,9488,10296,11288,12096,13088,13881,14873,15666,16658,17218,17930,17930,17930,17930,21,20,0,0,0,0,0,0,0x31ed80,0x847380,0xc4f800,0x1177e00,0x1580280,0x1aa8880,0x1fd0e80,0x23d9300,0x2901900,0x2d09d80,0x3232380,0x363a800,0x3b62e00,0x3f6b280,0x4493880,0x4891f80,0x4dba580,0x51b8c80,0x56e1280,0x599f580,0x5d68a00,0x5d68a00,0x5d68a00,0x5d68a00,0x5d68a00,0x5d68a00,0x5d68a00,0x5d68a00,0x5d68a00,0x5d68a00,0x5d68a00,0x5d68a00,305,1297,2105,3097,3905,4897,5889,6697,7689,8497,9489,10297,11289,12097,13089,13882,14874,15667,16659,17219,17931,17931,17931,17931,21,20,0,0,0,0,0,0,0x31f800,0x847e00,0xc50280,0x1178880,0x1580d00,0x1aa9300,0x1fd1900,0x23d9d80,0x2902380,0x2d0a800,0x3232e00,0x363b280,0x3b63880,0x3f6bd00,0x4494300,0x4892a00,0x4dbb000,0x51b9700,0x56e1d00,0x59a0000,0x5d69480,0x5d69480,0x5d69480,0x5d69480,0x5d69480,0x5d69480,0x5d69480,0x5d69480,0x5d69480,0x5d69480,0x5d69480,0x5d69480,306,1298,2106,3098,3906,4898,5890,6698,7690,8498,9490,10298,11290,12098,13090,13883,14875,15668,16660,17220,17932,18231,18231,18231,22,21,0,0,0,0,0,0,0x320280,0x848880,0xc50d00,0x1179300,0x1581780,0x1aa9d80,0x1fd2380,0x23da800,0x2902e00,0x2d0b280,0x3233880,0x363bd00,0x3b64300,0x3f6c780,0x4494d80,0x4893480,0x4dbba80,0x51ba180,0x56e2780,0x59a0a80,0x5d69f00,0x5ed5800,0x5ed5800,0x5ed5800,0x5ed5800,0x5ed5800,0x5ed5800,0x5ed5800,0x5ed5800,0x5ed5800,0x5ed5800,0x5ed5800,307,1299,2107,3099,3907,4899,5891,6699,7691,8499,9491,10299,11291,12099,13091,13884,14876,15669,16661,17221,17933,17933,17933,17933,21,20,0,0,0,0,0,0,0x320d00,0x849300,0xc51780,0x1179d80,0x1582200,0x1aaa800,0x1fd2e00,0x23db280,0x2903880,0x2d0bd00,0x3234300,0x363c780,0x3b64d80,0x3f6d200,0x4495800,0x4893f00,0x4dbc500,0x51bac00,0x56e3200,0x59a1500,0x5d6a980,0x5d6a980,0x5d6a980,0x5d6a980,0x5d6a980,0x5d6a980,0x5d6a980,0x5d6a980,0x5d6a980,0x5d6a980,0x5d6a980,0x5d6a980,308,1300,2108,3100,3908,4900,5892,6700,7692,8500,9492,10300,11292,12100,13092,13885,14877,15670,16662,17222,17934,17934,17934,17934,21,20,0,0,0,0,0,0,0x321780,0x849d80,0xc52200,0x117a800,0x1582c80,0x1aab280,0x1fd3880,0x23dbd00,0x2904300,0x2d0c780,0x3234d80,0x363d200,0x3b65800,0x3f6dc80,0x4496280,0x4894980,0x4dbcf80,0x51bb680,0x56e3c80,0x59a1f80,0x5d6b400,0x5d6b400,0x5d6b400,0x5d6b400,0x5d6b400,0x5d6b400,0x5d6b400,0x5d6b400,0x5d6b400,0x5d6b400,0x5d6b400,0x5d6b400,309,1301,2109,3101,3909,4901,5893,6701,7693,8501,9493,10301,11293,12101,13093,13886,14878,15671,16663,5448,5448,5448,5448,5448,20,19,0,0,0,0,0,0,0x322200,0x84a800,0xc52c80,0x117b280,0x1583700,0x1aabd00,0x1fd4300,0x23dc780,0x2904d80,0x2d0d200,0x3235800,0x363dc80,0x3b66280,0x3f6e700,0x4496d00,0x4895400,0x4dbda00,0x51bc100,0x56e4700,0x1d31900,0x1d31900,0x1d31900,0x1d31900,0x1d31900,0x1d31900,0x1d31900,0x1d31900,0x1d31900,0x1d31900,0x1d31900,0x1d31900,0x1d31900,310,1302,2110,3102,3910,4902,5894,6702,7694,8502,9494,10302,11294,12102,13094,13887,14879,15672,16664,5704,17935,17935,17935,17935,21,20,0,0,0,0,0,0,0x322c80,0x84b280,0xc53700,0x117bd00,0x1584180,0x1aac780,0x1fd4d80,0x23dd200,0x2905800,0x2d0dc80,0x3236280,0x363e700,0x3b66d00,0x3f6f180,0x4497780,0x4895e80,0x4dbe480,0x51bcb80,0x56e5180,0x1e80e80,0x5d6be80,0x5d6be80,0x5d6be80,0x5d6be80,0x5d6be80,0x5d6be80,0x5d6be80,0x5d6be80,0x5d6be80,0x5d6be80,0x5d6be80,0x5d6be80,311,1303,2111,3103,3911,4903,5895,6703,7695,8503,9495,10303,11295,12103,13095,13888,14880,15673,16665,5960,5960,5960,5960,5960,20,19,0,0,0,0,0,0,0x323700,0x84bd00,0xc54180,0x117c780,0x1584c00,0x1aad200,0x1fd5800,0x23ddc80,0x2906280,0x2d0e700,0x3236d00,0x363f180,0x3b67780,0x3f6fc00,0x4498200,0x4896900,0x4dbef00,0x51bd600,0x56e5c00,0x1fd0400,0x1fd0400,0x1fd0400,0x1fd0400,0x1fd0400,0x1fd0400,0x1fd0400,0x1fd0400,0x1fd0400,0x1fd0400,0x1fd0400,0x1fd0400,0x1fd0400,312,1304,2112,3104,4096,4904,5896,6704,7696,8504,9496,10304,11296,12288,13096,13889,14881,15674,16666,17223,17936,17936,17936,17936,21,20,0,0,0,0,0,0,0x324180,0x84c780,0xc54c00,0x117d200,0x16a5800,0x1aadc80,0x1fd6280,0x23de700,0x2906d00,0x2d0f180,0x3237780,0x363fc00,0x3b68200,0x4090800,0x4498c80,0x4897380,0x4dbf980,0x51be080,0x56e6680,0x59a2a00,0x5d6c900,0x5d6c900,0x5d6c900,0x5d6c900,0x5d6c900,0x5d6c900,0x5d6c900,0x5d6c900,0x5d6c900,0x5d6c900,0x5d6c900,0x5d6c900,313,1305,2113,3105,4097,4905,5897,6705,7697,8505,9497,10305,11297,12289,13097,13890,14882,15675,16667,17408,17937,17937,17937,17937,21,20,0,0,0,0,0,0,0x324c00,0x84d200,0xc55680,0x117dc80,0x16a6280,0x1aae700,0x1fd6d00,0x23df180,0x2907780,0x2d0fc00,0x3238200,0x3640680,0x3b68c80,0x4091280,0x4499700,0x4897e00,0x4dc0400,0x51beb00,0x56e7100,0x5ac3600,0x5d6d380,0x5d6d380,0x5d6d380,0x5d6d380,0x5d6d380,0x5d6d380,0x5d6d380,0x5d6d380,0x5d6d380,0x5d6d380,0x5d6d380,0x5d6d380,314,1306,2114,3106,4098,4906,5898,6706,7698,8506,9498,10306,11298,12290,13098,13891,14883,15676,16668,17409,17938,18232,18232,18232,22,21,0,0,0,0,0,0,0x325680,0x84dc80,0xc56100,0x117e700,0x16a6d00,0x1aaf180,0x1fd7780,0x23dfc00,0x2908200,0x2d10680,0x3238c80,0x3641100,0x3b69700,0x4091d00,0x449a180,0x4898880,0x4dc0e80,0x51bf580,0x56e7b80,0x5ac4080,0x5d6de00,0x5ed6280,0x5ed6280,0x5ed6280,0x5ed6280,0x5ed6280,0x5ed6280,0x5ed6280,0x5ed6280,0x5ed6280,0x5ed6280,0x5ed6280,315,1307,2115,3107,4099,4907,5899,6707,7699,8507,9499,10307,11299,12291,13099,13892,14884,15677,16669,17410,17939,17939,17939,17939,21,20,0,0,0,0,0,0,0x326100,0x84e700,0xc56b80,0x117f180,0x16a7780,0x1aafc00,0x1fd8200,0x23e0680,0x2908c80,0x2d11100,0x3239700,0x3641b80,0x3b6a180,0x4092780,0x449ac00,0x4899300,0x4dc1900,0x51c0000,0x56e8600,0x5ac4b00,0x5d6e880,0x5d6e880,0x5d6e880,0x5d6e880,0x5d6e880,0x5d6e880,0x5d6e880,0x5d6e880,0x5d6e880,0x5d6e880,0x5d6e880,0x5d6e880,316,1308,2116,3108,4100,4908,5900,6708,7700,8508,9500,10308,11300,12292,13100,13893,14885,15678,16670,17411,17940,17940,17940,17940,21,20,0,0,0,0,0,0,0x326b80,0x84f180,0xc57600,0x117fc00,0x16a8200,0x1ab0680,0x1fd8c80,0x23e1100,0x2909700,0x2d11b80,0x323a180,0x3642600,0x3b6ac00,0x4093200,0x449b680,0x4899d80,0x4dc2380,0x51c0a80,0x56e9080,0x5ac5580,0x5d6f300,0x5d6f300,0x5d6f300,0x5d6f300,0x5d6f300,0x5d6f300,0x5d6f300,0x5d6f300,0x5d6f300,0x5d6f300,0x5d6f300,0x5d6f300,317,1309,2117,3109,4101,4909,5901,6709,7701,8509,9501,10309,11301,12293,13101,13894,14886,15679,16671,6216,6216,6216,6216,6216,20,19,0,0,0,0,0,0,0x327600,0x84fc00,0xc58080,0x1180680,0x16a8c80,0x1ab1100,0x1fd9700,0x23e1b80,0x290a180,0x2d12600,0x323ac00,0x3643080,0x3b6b680,0x4093c80,0x449c100,0x489a800,0x4dc2e00,0x51c1500,0x56e9b00,0x211f980,0x211f980,0x211f980,0x211f980,0x211f980,0x211f980,0x211f980,0x211f980,0x211f980,0x211f980,0x211f980,0x211f980,0x211f980,318,1310,2118,3110,4102,4910,5902,6710,7702,8510,9502,10310,11302,12294,13102,13895,14887,15680,16672,6472,17941,17941,17941,17941,21,20,0,0,0,0,0,0,0x328080,0x850680,0xc58b00,0x1181100,0x16a9700,0x1ab1b80,0x1fda180,0x23e2600,0x290ac00,0x2d13080,0x323b680,0x3643b00,0x3b6c100,0x4094700,0x449cb80,0x489b280,0x4dc3880,0x51c1f80,0x56ea580,0x226ef00,0x5d6fd80,0x5d6fd80,0x5d6fd80,0x5d6fd80,0x5d6fd80,0x5d6fd80,0x5d6fd80,0x5d6fd80,0x5d6fd80,0x5d6fd80,0x5d6fd80,0x5d6fd80,319,1311,2119,3111,4103,4911,5903,6711,7703,8511,9503,10311,11303,12295,13103,14080,14888,15681,16673,6728,6728,6728,6728,6728,20,19,0,0,0,0,0,0,0x328b00,0x851100,0xc59580,0x1181b80,0x16aa180,0x1ab2600,0x1fdac00,0x23e3080,0x290b680,0x2d13b00,0x323c100,0x3644580,0x3b6cb80,0x4095180,0x449d600,0x49bbe80,0x4dc4300,0x51c2a00,0x56eb000,0x23be480,0x23be480,0x23be480,0x23be480,0x23be480,0x23be480,0x23be480,0x23be480,0x23be480,0x23be480,0x23be480,0x23be480,0x23be480,320,1312,2304,3112,4104,4912,5904,6712,7704,8512,9504,10496,11304,12296,13104,14081,14889,15682,16674,17412,17942,17942,17942,17942,21,20,0,0,0,0,0,0,0x329580,0x851b80,0xd7a180,0x1182600,0x16aac00,0x1ab3080,0x1fdb680,0x23e3b00,0x290c100,0x2d14580,0x323cb80,0x3765180,0x3b6d600,0x4095c00,0x449e080,0x49bc900,0x4dc4d80,0x51c3480,0x56eba80,0x5ac6000,0x5d70800,0x5d70800,0x5d70800,0x5d70800,0x5d70800,0x5d70800,0x5d70800,0x5d70800,0x5d70800,0x5d70800,0x5d70800,0x5d70800,321,1313,2305,3113,4105,4913,5905,6713,7705,8513,9505,10497,11305,12297,13105,14082,14890,15683,16675,17413,17943,17943,17943,17943,21,20,0,0,0,0,0,0,0x32a000,0x852600,0xd7ac00,0x1183080,0x16ab680,0x1ab3b00,0x1fdc100,0x23e4580,0x290cb80,0x2d15000,0x323d600,0x3765c00,0x3b6e080,0x4096680,0x449eb00,0x49bd380,0x4dc5800,0x51c3f00,0x56ec500,0x5ac6a80,0x5d71280,0x5d71280,0x5d71280,0x5d71280,0x5d71280,0x5d71280,0x5d71280,0x5d71280,0x5d71280,0x5d71280,0x5d71280,0x5d71280,322,1314,2306,3114,4106,4914,5906,6714,7706,8514,9506,10498,11306,12298,13106,14083,14891,15684,16676,17414,17944,18233,18233,18233,22,21,0,0,0,0,0,0,0x32aa80,0x853080,0xd7b680,0x1183b00,0x16ac100,0x1ab4580,0x1fdcb80,0x23e5000,0x290d600,0x2d15a80,0x323e080,0x3766680,0x3b6eb00,0x4097100,0x449f580,0x49bde00,0x4dc6280,0x51c4980,0x56ecf80,0x5ac7500,0x5d71d00,0x5ed6d00,0x5ed6d00,0x5ed6d00,0x5ed6d00,0x5ed6d00,0x5ed6d00,0x5ed6d00,0x5ed6d00,0x5ed6d00,0x5ed6d00,0x5ed6d00,323,1315,2307,3115,4107,4915,5907,6715,7707,8515,9507,10499,11307,12299,13107,14084,14892,15685,16677,17415,17945,17945,17945,17945,21,20,0,0,0,0,0,0,0x32b500,0x853b00,0xd7c100,0x1184580,0x16acb80,0x1ab5000,0x1fdd600,0x23e5a80,0x290e080,0x2d16500,0x323eb00,0x3767100,0x3b6f580,0x4097b80,0x44a0000,0x49be880,0x4dc6d00,0x51c5400,0x56eda00,0x5ac7f80,0x5d72780,0x5d72780,0x5d72780,0x5d72780,0x5d72780,0x5d72780,0x5d72780,0x5d72780,0x5d72780,0x5d72780,0x5d72780,0x5d72780,324,1316,2308,3116,4108,4916,5908,6716,7708,8516,9508,10500,11308,12300,13108,14085,14893,15686,16678,17416,17946,17946,17946,17946,21,20,0,0,0,0,0,0,0x32bf80,0x854580,0xd7cb80,0x1185000,0x16ad600,0x1ab5a80,0x1fde080,0x23e6500,0x290eb00,0x2d16f80,0x323f580,0x3767b80,0x3b70000,0x4098600,0x44a0a80,0x49bf300,0x4dc7780,0x51c5e80,0x56ee480,0x5ac8a00,0x5d73200,0x5d73200,0x5d73200,0x5d73200,0x5d73200,0x5d73200,0x5d73200,0x5d73200,0x5d73200,0x5d73200,0x5d73200,0x5d73200,325,1317,2309,3117,4109,4917,5909,6717,7709,8517,9509,10501,11309,12301,13109,14086,14894,15687,16679,6984,6984,6984,6984,6984,20,19,0,0,0,0,0,0,0x32ca00,0x855000,0xd7d600,0x1185a80,0x16ae080,0x1ab6500,0x1fdeb00,0x23e6f80,0x290f580,0x2d17a00,0x3240000,0x3768600,0x3b70a80,0x4099080,0x44a1500,0x49bfd80,0x4dc8200,0x51c6900,0x56eef00,0x250da00,0x250da00,0x250da00,0x250da00,0x250da00,0x250da00,0x250da00,0x250da00,0x250da00,0x250da00,0x250da00,0x250da00,0x250da00,326,1318,2310,3118,4110,4918,5910,6718,7710,8518,9510,10502,11310,12302,13110,14087,14895,15872,16680,7240,17947,17947,17947,17947,21,20,0,0,0,0,0,0,0x32d480,0x855a80,0xd7e080,0x1186500,0x16aeb00,0x1ab6f80,0x1fdf580,0x23e7a00,0x2910000,0x2d18480,0x3240a80,0x3769080,0x3b71500,0x4099b00,0x44a1f80,0x49c0800,0x4dc8c80,0x52e7500,0x56ef980,0x265cf80,0x5d73c80,0x5d73c80,0x5d73c80,0x5d73c80,0x5d73c80,0x5d73c80,0x5d73c80,0x5d73c80,0x5d73c80,0x5d73c80,0x5d73c80,0x5d73c80,327,1319,2311,3119,4111,4919,5911,6719,7711,8519,9511,10503,11311,12303,13111,14088,14896,15873,16681,7496,7496,7496,7496,7496,20,19,0,0,0,0,0,0,0x32df00,0x856500,0xd7eb00,0x1186f80,0x16af580,0x1ab7a00,0x1fe0000,0x23e8480,0x2910a80,0x2d18f00,0x3241500,0x3769b00,0x3b71f80,0x409a580,0x44a2a00,0x49c1280,0x4dc9700,0x52e7f80,0x56f0400,0x27ac500,0x27ac500,0x27ac500,0x27ac500,0x27ac500,0x27ac500,0x27ac500,0x27ac500,0x27ac500,0x27ac500,0x27ac500,0x27ac500,0x27ac500,512,1320,2312,3120,4112,4920,5912,6720,7712,8704,9512,10504,11312,12304,13112,14089,14897,15874,16682,17417,17948,17948,17948,17948,21,20,0,0,0,0,0,0,0x44eb00,0x856f80,0xd7f580,0x1187a00,0x16b0000,0x1ab8480,0x1fe0a80,0x23e8f00,0x2911500,0x2e39b00,0x3241f80,0x376a580,0x3b72a00,0x409b000,0x44a3480,0x49c1d00,0x4dca180,0x52e8a00,0x56f0e80,0x5ac9480,0x5d74700,0x5d74700,0x5d74700,0x5d74700,0x5d74700,0x5d74700,0x5d74700,0x5d74700,0x5d74700,0x5d74700,0x5d74700,0x5d74700,513,1321,2313,3121,4113,4921,5913,6721,7713,8705,9513,10505,11313,12305,13113,14090,14898,15875,16683,17418,17949,17949,17949,17949,21,20,0,0,0,0,0,0,0x44f580,0x857a00,0xd80000,0x1188480,0x16b0a80,0x1ab8f00,0x1fe1500,0x23e9980,0x2911f80,0x2e3a580,0x3242a00,0x376b000,0x3b73480,0x409ba80,0x44a3f00,0x49c2780,0x4dcac00,0x52e9480,0x56f1900,0x5ac9f00,0x5d75180,0x5d75180,0x5d75180,0x5d75180,0x5d75180,0x5d75180,0x5d75180,0x5d75180,0x5d75180,0x5d75180,0x5d75180,0x5d75180,514,1322,2314,3122,4114,4922,5914,6722,7714,8706,9514,10506,11314,12306,13114,14091,14899,15876,16684,17419,17950,18234,18234,18234,22,21,0,0,0,0,0,0,0x450000,0x858480,0xd80a80,0x1188f00,0x16b1500,0x1ab9980,0x1fe1f80,0x23ea400,0x2912a00,0x2e3b000,0x3243480,0x376ba80,0x3b73f00,0x409c500,0x44a4980,0x49c3200,0x4dcb680,0x52e9f00,0x56f2380,0x5aca980,0x5d75c00,0x5ed7780,0x5ed7780,0x5ed7780,0x5ed7780,0x5ed7780,0x5ed7780,0x5ed7780,0x5ed7780,0x5ed7780,0x5ed7780,0x5ed7780,515,1323,2315,3123,4115,4923,5915,6723,7715,8707,9515,10507,11315,12307,13115,14092,14900,15877,16685,17420,17951,17951,17951,17951,21,20,0,0,0,0,0,0,0x450a80,0x858f00,0xd81500,0x1189980,0x16b1f80,0x1aba400,0x1fe2a00,0x23eae80,0x2913480,0x2e3ba80,0x3243f00,0x376c500,0x3b74980,0x409cf80,0x44a5400,0x49c3c80,0x4dcc100,0x52ea980,0x56f2e00,0x5acb400,0x5d76680,0x5d76680,0x5d76680,0x5d76680,0x5d76680,0x5d76680,0x5d76680,0x5d76680,0x5d76680,0x5d76680,0x5d76680,0x5d76680,516,1324,2316,3124,4116,4924,5916,6724,7716,8708,9516,10508,11316,12308,13116,14093,14901,15878,16686,17421,17952,17952,17952,17952,21,20,0,0,0,0,0,0,0x451500,0x859980,0xd81f80,0x118a400,0x16b2a00,0x1abae80,0x1fe3480,0x23eb900,0x2913f00,0x2e3c500,0x3244980,0x376cf80,0x3b75400,0x409da00,0x44a5e80,0x49c4700,0x4dccb80,0x52eb400,0x56f3880,0x5acbe80,0x5d77100,0x5d77100,0x5d77100,0x5d77100,0x5d77100,0x5d77100,0x5d77100,0x5d77100,0x5d77100,0x5d77100,0x5d77100,0x5d77100,517,1325,2317,3125,4117,4925,5917,6725,7717,8709,9517,10509,11317,12309,13117,14094,14902,15879,16687,7752,7752,7752,7752,7752,20,19,0,0,0,0,0,0,0x451f80,0x85a400,0xd82a00,0x118ae80,0x16b3480,0x1abb900,0x1fe3f00,0x23ec380,0x2914980,0x2e3cf80,0x3245400,0x376da00,0x3b75e80,0x409e480,0x44a6900,0x49c5180,0x4dcd600,0x52ebe80,0x56f4300,0x28fba80,0x28fba80,0x28fba80,0x28fba80,0x28fba80,0x28fba80,0x28fba80,0x28fba80,0x28fba80,0x28fba80,0x28fba80,0x28fba80,0x28fba80,518,1326,2318,3126,4118,4926,5918,6726,7718,8710,9518,10510,11318,12310,13118,14095,14903,15880,16688,8008,17953,17953,17953,17953,21,20,0,0,0,0,0,0,0x452a00,0x85ae80,0xd83480,0x118b900,0x16b3f00,0x1abc380,0x1fe4980,0x23ece00,0x2915400,0x2e3da00,0x3245e80,0x376e480,0x3b76900,0x409ef00,0x44a7380,0x49c5c00,0x4dce080,0x52ec900,0x56f4d80,0x2a4b000,0x5d77b80,0x5d77b80,0x5d77b80,0x5d77b80,0x5d77b80,0x5d77b80,0x5d77b80,0x5d77b80,0x5d77b80,0x5d77b80,0x5d77b80,0x5d77b80,519,1327,2319,3127,4119,4927,5919,6727,7719,8711,9519,10511,11319,12311,13119,14096,14904,15881,16689,8264,8264,8264,8264,8264,20,19,0,0,0,0,0,0,0x453480,0x85b900,0xd83f00,0x118c380,0x16b4980,0x1abce00,0x1fe5400,0x23ed880,0x2915e80,0x2e3e480,0x3246900,0x376ef00,0x3b77380,0x409f980,0x44a7e00,0x49c6680,0x4dceb00,0x52ed380,0x56f5800,0x2b9a580,0x2b9a580,0x2b9a580,0x2b9a580,0x2b9a580,0x2b9a580,0x2b9a580,0x2b9a580,0x2b9a580,0x2b9a580,0x2b9a580,0x2b9a580,0x2b9a580,520,1328,2320,3128,4120,4928,5920,6912,7720,8712,9520,10512,11320,12312,13120,14097,14905,15882,16690,17422,17954,17954,17954,17954,21,20,0,0,0,0,0,0,0x453f00,0x85c380,0xd84980,0x118ce00,0x16b5400,0x1abd880,0x1fe5e80,0x250e480,0x2916900,0x2e3ef00,0x3247380,0x376f980,0x3b77e00,0x40a0400,0x44a8880,0x49c7100,0x4dcf580,0x52ede00,0x56f6280,0x5acc900,0x5d78600,0x5d78600,0x5d78600,0x5d78600,0x5d78600,0x5d78600,0x5d78600,0x5d78600,0x5d78600,0x5d78600,0x5d78600,0x5d78600,521,1329,2321,3129,4121,4929,5921,6913,7721,8713,9521,10513,11321,12313,13121,14098,14906,15883,16691,17423,17955,17955,17955,17955,21,20,0,0,0,0,0,0,0x454980,0x85ce00,0xd85400,0x118d880,0x16b5e80,0x1abe300,0x1fe6900,0x250ef00,0x2917380,0x2e3f980,0x3247e00,0x3770400,0x3b78880,0x40a0e80,0x44a9300,0x49c7b80,0x4dd0000,0x52ee880,0x56f6d00,0x5acd380,0x5d79080,0x5d79080,0x5d79080,0x5d79080,0x5d79080,0x5d79080,0x5d79080,0x5d79080,0x5d79080,0x5d79080,0x5d79080,0x5d79080,522,1330,2322,3130,4122,4930,5922,6914,7722,8714,9522,10514,11322,12314,13122,14099,14907,15884,16692,17424,17956,18235,18235,18235,22,21,0,0,0,0,0,0,0x455400,0x85d880,0xd85e80,0x118e300,0x16b6900,0x1abed80,0x1fe7380,0x250f980,0x2917e00,0x2e40400,0x3248880,0x3770e80,0x3b79300,0x40a1900,0x44a9d80,0x49c8600,0x4dd0a80,0x52ef300,0x56f7780,0x5acde00,0x5d79b00,0x5ed8200,0x5ed8200,0x5ed8200,0x5ed8200,0x5ed8200,0x5ed8200,0x5ed8200,0x5ed8200,0x5ed8200,0x5ed8200,0x5ed8200,523,1331,2323,3131,4123,4931,5923,6915,7723,8715,9523,10515,11323,12315,13123,14100,14908,15885,16693,17425,17957,17957,17957,17957,21,20,0,0,0,0,0,0,0x455e80,0x85e300,0xd86900,0x118ed80,0x16b7380,0x1abf800,0x1fe7e00,0x2510400,0x2918880,0x2e40e80,0x3249300,0x3771900,0x3b79d80,0x40a2380,0x44aa800,0x49c9080,0x4dd1500,0x52efd80,0x56f8200,0x5ace880,0x5d7a580,0x5d7a580,0x5d7a580,0x5d7a580,0x5d7a580,0x5d7a580,0x5d7a580,0x5d7a580,0x5d7a580,0x5d7a580,0x5d7a580,0x5d7a580,524,1332,2324,3132,4124,4932,5924,6916,7724,8716,9524,10516,11324,12316,13124,14101,14909,15886,16694,17426,17958,17958,17958,17958,21,20,0,0,0,0,0,0,0x456900,0x85ed80,0xd87380,0x118f800,0x16b7e00,0x1ac0280,0x1fe8880,0x2510e80,0x2919300,0x2e41900,0x3249d80,0x3772380,0x3b7a800,0x40a2e00,0x44ab280,0x49c9b00,0x4dd1f80,0x52f0800,0x56f8c80,0x5acf300,0x5d7b000,0x5d7b000,0x5d7b000,0x5d7b000,0x5d7b000,0x5d7b000,0x5d7b000,0x5d7b000,0x5d7b000,0x5d7b000,0x5d7b000,0x5d7b000,525,1333,2325,3133,4125,4933,5925,6917,7725,8717,9525,10517,11325,12317,13125,14102,14910,15887,16695,8520,8520,8520,8520,8520,20,19,0,0,0,0,0,0,0x457380,0x85f800,0xd87e00,0x1190280,0x16b8880,0x1ac0d00,0x1fe9300,0x2511900,0x2919d80,0x2e42380,0x324a800,0x3772e00,0x3b7b280,0x40a3880,0x44abd00,0x49ca580,0x4dd2a00,0x52f1280,0x56f9700,0x2ce9b00,0x2ce9b00,0x2ce9b00,0x2ce9b00,0x2ce9b00,0x2ce9b00,0x2ce9b00,0x2ce9b00,0x2ce9b00,0x2ce9b00,0x2ce9b00,0x2ce9b00,0x2ce9b00,526,1334,2326,3134,4126,4934,5926,6918,7726,8718,9526,10518,11326,12318,13126,14103,14911,15888,16696,8776,17959,17959,17959,17959,21,20,0,0,0,0,0,0,0x457e00,0x860280,0xd88880,0x1190d00,0x16b9300,0x1ac1780,0x1fe9d80,0x2512380,0x291a800,0x2e42e00,0x324b280,0x3773880,0x3b7bd00,0x40a4300,0x44ac780,0x49cb000,0x4dd3480,0x52f1d00,0x56fa180,0x2e39080,0x5d7ba80,0x5d7ba80,0x5d7ba80,0x5d7ba80,0x5d7ba80,0x5d7ba80,0x5d7ba80,0x5d7ba80,0x5d7ba80,0x5d7ba80,0x5d7ba80,0x5d7ba80,527,1335,2327,3135,4127,4935,5927,6919,7727,8719,9527,10519,11327,12319,13127,14104,14912,15889,16697,9032,9032,9032,9032,9032,20,19,0,0,0,0,0,0,0x458880,0x860d00,0xd89300,0x1191780,0x16b9d80,0x1ac2200,0x1fea800,0x2512e00,0x291b280,0x2e43880,0x324bd00,0x3774300,0x3b7c780,0x40a4d80,0x44ad200,0x49cba80,0x4dd3f00,0x52f2780,0x56fac00,0x2f88600,0x2f88600,0x2f88600,0x2f88600,0x2f88600,0x2f88600,0x2f88600,0x2f88600,0x2f88600,0x2f88600,0x2f88600,0x2f88600,0x2f88600,528,1336,2328,3136,4128,5120,5928,6920,7728,8720,9528,10520,11328,12320,13312,14105,14913,15890,16698,17427,17960,17960,17960,17960,21,20,0,0,0,0,0,0,0x459300,0x861780,0xd89d80,0x1192200,0x16ba800,0x1be2e00,0x1feb280,0x2513880,0x291bd00,0x2e44300,0x324c780,0x3774d80,0x3b7d200,0x40a5800,0x45cde00,0x49cc500,0x4dd4980,0x52f3200,0x56fb680,0x5acfd80,0x5d7c500,0x5d7c500,0x5d7c500,0x5d7c500,0x5d7c500,0x5d7c500,0x5d7c500,0x5d7c500,0x5d7c500,0x5d7c500,0x5d7c500,0x5d7c500,529,1337,2329,3137,4129,5121,5929,6921,7729,8721,9529,10521,11329,12321,13313,14106,14914,15891,16699,17428,17961,17961,17961,17961,21,20,0,0,0,0,0,0,0x459d80,0x862200,0xd8a800,0x1192c80,0x16bb280,0x1be3880,0x1febd00,0x2514300,0x291c780,0x2e44d80,0x324d200,0x3775800,0x3b7dc80,0x40a6280,0x45ce880,0x49ccf80,0x4dd5400,0x52f3c80,0x56fc100,0x5ad0800,0x5d7cf80,0x5d7cf80,0x5d7cf80,0x5d7cf80,0x5d7cf80,0x5d7cf80,0x5d7cf80,0x5d7cf80,0x5d7cf80,0x5d7cf80,0x5d7cf80,0x5d7cf80,530,1338,2330,3138,4130,5122,5930,6922,7730,8722,9530,10522,11330,12322,13314,14107,14915,15892,16700,17429,17962,18236,18236,18236,22,21,0,0,0,0,0,0,0x45a800,0x862c80,0xd8b280,0x1193700,0x16bbd00,0x1be4300,0x1fec780,0x2514d80,0x291d200,0x2e45800,0x324dc80,0x3776280,0x3b7e700,0x40a6d00,0x45cf300,0x49cda00,0x4dd5e80,0x52f4700,0x56fcb80,0x5ad1280,0x5d7da00,0x5ed8c80,0x5ed8c80,0x5ed8c80,0x5ed8c80,0x5ed8c80,0x5ed8c80,0x5ed8c80,0x5ed8c80,0x5ed8c80,0x5ed8c80,0x5ed8c80,531,1339,2331,3139,4131,5123,5931,6923,7731,8723,9531,10523,11331,12323,13315,14108,14916,15893,16701,17430,17963,17963,17963,17963,21,20,0,0,0,0,0,0,0x45b280,0x863700,0xd8bd00,0x1194180,0x16bc780,0x1be4d80,0x1fed200,0x2515800,0x291dc80,0x2e46280,0x324e700,0x3776d00,0x3b7f180,0x40a7780,0x45cfd80,0x49ce480,0x4dd6900,0x52f5180,0x56fd600,0x5ad1d00,0x5d7e480,0x5d7e480,0x5d7e480,0x5d7e480,0x5d7e480,0x5d7e480,0x5d7e480,0x5d7e480,0x5d7e480,0x5d7e480,0x5d7e480,0x5d7e480,532,1340,2332,3140,4132,5124,5932,6924,7732,8724,9532,10524,11332,12324,13316,14109,14917,15894,16702,17431,17964,17964,17964,17964,21,20,0,0,0,0,0,0,0x45bd00,0x864180,0xd8c780,0x1194c00,0x16bd200,0x1be5800,0x1fedc80,0x2516280,0x291e700,0x2e46d00,0x324f180,0x3777780,0x3b7fc00,0x40a8200,0x45d0800,0x49cef00,0x4dd7380,0x52f5c00,0x56fe080,0x5ad2780,0x5d7ef00,0x5d7ef00,0x5d7ef00,0x5d7ef00,0x5d7ef00,0x5d7ef00,0x5d7ef00,0x5d7ef00,0x5d7ef00,0x5d7ef00,0x5d7ef00,0x5d7ef00,533,1341,2333,3141,4133,5125,5933,6925,7733,8725,9533,10525,11333,12325,13317,14110,14918,15895,16703,9288,9288,9288,9288,9288,20,19,0,0,0,0,0,0,0x45c780,0x864c00,0xd8d200,0x1195680,0x16bdc80,0x1be6280,0x1fee700,0x2516d00,0x291f180,0x2e47780,0x324fc00,0x3778200,0x3b80680,0x40a8c80,0x45d1280,0x49cf980,0x4dd7e00,0x52f6680,0x56feb00,0x30d7b80,0x30d7b80,0x30d7b80,0x30d7b80,0x30d7b80,0x30d7b80,0x30d7b80,0x30d7b80,0x30d7b80,0x30d7b80,0x30d7b80,0x30d7b80,0x30d7b80,534,1342,2334,3142,4134,5126,5934,6926,7734,8726,9534,10526,11334,12326,13318,14111,14919,15896,16704,9544,17965,17965,17965,17965,21,20,0,0,0,0,0,0,0x45d200,0x865680,0xd8dc80,0x1196100,0x16be700,0x1be6d00,0x1fef180,0x2517780,0x291fc00,0x2e48200,0x3250680,0x3778c80,0x3b81100,0x40a9700,0x45d1d00,0x49d0400,0x4dd8880,0x52f7100,0x56ff580,0x3227100,0x5d7f980,0x5d7f980,0x5d7f980,0x5d7f980,0x5d7f980,0x5d7f980,0x5d7f980,0x5d7f980,0x5d7f980,0x5d7f980,0x5d7f980,0x5d7f980,535,1343,2335,3143,4135,5127,5935,6927,7735,8727,9535,10527,11335,12327,13319,14112,15104,15897,16705,9800,9800,9800,9800,9800,20,19,0,0,0,0,0,0,0x45dc80,0x866100,0xd8e700,0x1196b80,0x16bf180,0x1be7780,0x1fefc00,0x2518200,0x2920680,0x2e48c80,0x3251100,0x3779700,0x3b81b80,0x40aa180,0x45d2780,0x49d0e80,0x4ef9480,0x52f7b80,0x5700000,0x3376680,0x3376680,0x3376680,0x3376680,0x3376680,0x3376680,0x3376680,0x3376680,0x3376680,0x3376680,0x3376680,0x3376680,0x3376680,536,1344,2336,3328,4136,5128,5936,6928,7736,8728,9536,10528,11520,12328,13320,14113,15105,15898,16706,17432,17966,17966,17966,17966,21,20,0,0,0,0,0,0,0x45e700,0x866b80,0xd8f180,0x12b7780,0x16bfc00,0x1be8200,0x1ff0680,0x2518c80,0x2921100,0x2e49700,0x3251b80,0x377a180,0x3ca2780,0x40aac00,0x45d3200,0x49d1900,0x4ef9f00,0x52f8600,0x5700a80,0x5ad3200,0x5d80400,0x5d80400,0x5d80400,0x5d80400,0x5d80400,0x5d80400,0x5d80400,0x5d80400,0x5d80400,0x5d80400,0x5d80400,0x5d80400,537,1345,2337,3329,4137,5129,5937,6929,7737,8729,9537,10529,11521,12329,13321,14114,15106,15899,16707,17433,17967,17967,17967,17967,21,20,0,0,0,0,0,0,0x45f180,0x867600,0xd8fc00,0x12b8200,0x16c0680,0x1be8c80,0x1ff1100,0x2519700,0x2921b80,0x2e4a180,0x3252600,0x377ac00,0x3ca3200,0x40ab680,0x45d3c80,0x49d2380,0x4efa980,0x52f9080,0x5701500,0x5ad3c80,0x5d80e80,0x5d80e80,0x5d80e80,0x5d80e80,0x5d80e80,0x5d80e80,0x5d80e80,0x5d80e80,0x5d80e80,0x5d80e80,0x5d80e80,0x5d80e80,538,1346,2338,3330,4138,5130,5938,6930,7738,8730,9538,10530,11522,12330,13322,14115,15107,15900,16708,17434,17968,18237,18237,18237,22,21,0,0,0,0,0,0,0x45fc00,0x868080,0xd90680,0x12b8c80,0x16c1100,0x1be9700,0x1ff1b80,0x251a180,0x2922600,0x2e4ac00,0x3253080,0x377b680,0x3ca3c80,0x40ac100,0x45d4700,0x49d2e00,0x4efb400,0x52f9b00,0x5701f80,0x5ad4700,0x5d81900,0x5ed9700,0x5ed9700,0x5ed9700,0x5ed9700,0x5ed9700,0x5ed9700,0x5ed9700,0x5ed9700,0x5ed9700,0x5ed9700,0x5ed9700,539,1347,2339,3331,4139,5131,5939,6931,7739,8731,9539,10531,11523,12331,13323,14116,15108,15901,16709,17435,17969,17969,17969,17969,21,20,0,0,0,0,0,0,0x460680,0x868b00,0xd91100,0x12b9700,0x16c1b80,0x1bea180,0x1ff2600,0x251ac00,0x2923080,0x2e4b680,0x3253b00,0x377c100,0x3ca4700,0x40acb80,0x45d5180,0x49d3880,0x4efbe80,0x52fa580,0x5702a00,0x5ad5180,0x5d82380,0x5d82380,0x5d82380,0x5d82380,0x5d82380,0x5d82380,0x5d82380,0x5d82380,0x5d82380,0x5d82380,0x5d82380,0x5d82380,540,1348,2340,3332,4140,5132,5940,6932,7740,8732,9540,10532,11524,12332,13324,14117,15109,15902,16710,17436,17970,17970,17970,17970,21,20,0,0,0,0,0,0,0x461100,0x869580,0xd91b80,0x12ba180,0x16c2600,0x1beac00,0x1ff3080,0x251b680,0x2923b00,0x2e4c100,0x3254580,0x377cb80,0x3ca5180,0x40ad600,0x45d5c00,0x49d4300,0x4efc900,0x52fb000,0x5703480,0x5ad5c00,0x5d82e00,0x5d82e00,0x5d82e00,0x5d82e00,0x5d82e00,0x5d82e00,0x5d82e00,0x5d82e00,0x5d82e00,0x5d82e00,0x5d82e00,0x5d82e00,541,1349,2341,3333,4141,5133,5941,6933,7741,8733,9541,10533,11525,12333,13325,14118,15110,15903,16711,10056,10056,10056,10056,10056,20,19,0,0,0,0,0,0,0x461b80,0x86a000,0xd92600,0x12bac00,0x16c3080,0x1beb680,0x1ff3b00,0x251c100,0x2924580,0x2e4cb80,0x3255000,0x377d600,0x3ca5c00,0x40ae080,0x45d6680,0x49d4d80,0x4efd380,0x52fba80,0x5703f00,0x34c5c00,0x34c5c00,0x34c5c00,0x34c5c00,0x34c5c00,0x34c5c00,0x34c5c00,0x34c5c00,0x34c5c00,0x34c5c00,0x34c5c00,0x34c5c00,0x34c5c00,542,1350,2342,3334,4142,5134,5942,6934,7742,8734,9542,10534,11526,12334,13326,14119,15111,15904,16896,10312,17971,17971,17971,17971,21,20,0,0,0,0,0,0,0x462600,0x86aa80,0xd93080,0x12bb680,0x16c3b00,0x1bec100,0x1ff4580,0x251cb80,0x2925000,0x2e4d600,0x3255a80,0x377e080,0x3ca6680,0x40aeb00,0x45d7100,0x49d5800,0x4efde00,0x52fc500,0x5824b00,0x3615180,0x5d83880,0x5d83880,0x5d83880,0x5d83880,0x5d83880,0x5d83880,0x5d83880,0x5d83880,0x5d83880,0x5d83880,0x5d83880,0x5d83880,543,1351,2343,3335,4143,5135,5943,6935,7743,8735,9543,10535,11527,12335,13327,14120,15112,15905,16897,10568,10568,10568,10568,10568,20,19,0,0,0,0,0,0,0x463080,0x86b500,0xd93b00,0x12bc100,0x16c4580,0x1becb80,0x1ff5000,0x251d600,0x2925a80,0x2e4e080,0x3256500,0x377eb00,0x3ca7100,0x40af580,0x45d7b80,0x49d6280,0x4efe880,0x52fcf80,0x5825580,0x3764700,0x3764700,0x3764700,0x3764700,0x3764700,0x3764700,0x3764700,0x3764700,0x3764700,0x3764700,0x3764700,0x3764700,0x3764700,544,1536,2344,3336,4144,5136,5944,6936,7744,8736,9728,10536,11528,12336,13328,14121,15113,15906,16898,17437,17972,17972,17972,17972,21,20,0,0,0,0,0,0,0x463b00,0x98c100,0xd94580,0x12bcb80,0x16c5000,0x1bed600,0x1ff5a80,0x251e080,0x2926500,0x2e4eb00,0x3377100,0x377f580,0x3ca7b80,0x40b0000,0x45d8600,0x49d6d00,0x4eff300,0x52fda00,0x5826000,0x5ad6680,0x5d84300,0x5d84300,0x5d84300,0x5d84300,0x5d84300,0x5d84300,0x5d84300,0x5d84300,0x5d84300,0x5d84300,0x5d84300,0x5d84300,545,1537,2345,3337,4145,5137,5945,6937,7745,8737,9729,10537,11529,12337,13329,14122,15114,15907,16899,17438,17973,17973,17973,17973,21,20,0,0,0,0,0,0,0x464580,0x98cb80,0xd95000,0x12bd600,0x16c5a80,0x1bee080,0x1ff6500,0x251eb00,0x2926f80,0x2e4f580,0x3377b80,0x3780000,0x3ca8600,0x40b0a80,0x45d9080,0x49d7780,0x4effd80,0x52fe480,0x5826a80,0x5ad7100,0x5d84d80,0x5d84d80,0x5d84d80,0x5d84d80,0x5d84d80,0x5d84d80,0x5d84d80,0x5d84d80,0x5d84d80,0x5d84d80,0x5d84d80,0x5d84d80,546,1538,2346,3338,4146,5138,5946,6938,7746,8738,9730,10538,11530,12338,13330,14123,15115,15908,16900,17439,17974,18238,18238,18238,22,21,0,0,0,0,0,0,0x465000,0x98d600,0xd95a80,0x12be080,0x16c6500,0x1beeb00,0x1ff6f80,0x251f580,0x2927a00,0x2e50000,0x3378600,0x3780a80,0x3ca9080,0x40b1500,0x45d9b00,0x49d8200,0x4f00800,0x52fef00,0x5827500,0x5ad7b80,0x5d85800,0x5eda180,0x5eda180,0x5eda180,0x5eda180,0x5eda180,0x5eda180,0x5eda180,0x5eda180,0x5eda180,0x5eda180,0x5eda180,547,1539,2347,3339,4147,5139,5947,6939,7747,8739,9731,10539,11531,12339,13331,14124,15116,15909,16901,17440,17975,17975,17975,17975,21,20,0,0,0,0,0,0,0x465a80,0x98e080,0xd96500,0x12beb00,0x16c6f80,0x1bef580,0x1ff7a00,0x2520000,0x2928480,0x2e50a80,0x3379080,0x3781500,0x3ca9b00,0x40b1f80,0x45da580,0x49d8c80,0x4f01280,0x52ff980,0x5827f80,0x5ad8600,0x5d86280,0x5d86280,0x5d86280,0x5d86280,0x5d86280,0x5d86280,0x5d86280,0x5d86280,0x5d86280,0x5d86280,0x5d86280,0x5d86280,548,1540,2348,3340,4148,5140,5948,6940,7748,8740,9732,10540,11532,12340,13332,14125,15117,15910,16902,17441,17976,17976,17976,17976,21,20,0,0,0,0,0,0,0x466500,0x98eb00,0xd96f80,0x12bf580,0x16c7a00,0x1bf0000,0x1ff8480,0x2520a80,0x2928f00,0x2e51500,0x3379b00,0x3781f80,0x3caa580,0x40b2a00,0x45db000,0x49d9700,0x4f01d00,0x5300400,0x5828a00,0x5ad9080,0x5d86d00,0x5d86d00,0x5d86d00,0x5d86d00,0x5d86d00,0x5d86d00,0x5d86d00,0x5d86d00,0x5d86d00,0x5d86d00,0x5d86d00,0x5d86d00,549,1541,2349,3341,4149,5141,5949,6941,7749,8741,9733,10541,11533,12341,13333,14126,15118,15911,16903,10824,10824,10824,10824,10824,20,19,0,0,0,0,0,0,0x466f80,0x98f580,0xd97a00,0x12c0000,0x16c8480,0x1bf0a80,0x1ff8f00,0x2521500,0x2929980,0x2e51f80,0x337a580,0x3782a00,0x3cab000,0x40b3480,0x45dba80,0x49da180,0x4f02780,0x5300e80,0x5829480,0x38b3c80,0x38b3c80,0x38b3c80,0x38b3c80,0x38b3c80,0x38b3c80,0x38b3c80,0x38b3c80,0x38b3c80,0x38b3c80,0x38b3c80,0x38b3c80,0x38b3c80,550,1542,2350,3342,4150,5142,5950,6942,7750,8742,9734,10542,11534,12342,13334,14127,15119,15912,16904,11080,17977,17977,17977,17977,21,20,0,0,0,0,0,0,0x467a00,0x990000,0xd98480,0x12c0a80,0x16c8f00,0x1bf1500,0x1ff9980,0x2521f80,0x292a400,0x2e52a00,0x337b000,0x3783480,0x3caba80,0x40b3f00,0x45dc500,0x49dac00,0x4f03200,0x5301900,0x5829f00,0x3a03200,0x5d87780,0x5d87780,0x5d87780,0x5d87780,0x5d87780,0x5d87780,0x5d87780,0x5d87780,0x5d87780,0x5d87780,0x5d87780,0x5d87780,551,1543,2351,3343,4151,5143,5951,6943,7751,8743,9735,10543,11535,12343,13335,14128,15120,15913,16905,11336,11336,11336,11336,11336,20,19,0,0,0,0,0,0,0x468480,0x990a80,0xd98f00,0x12c1500,0x16c9980,0x1bf1f80,0x1ffa400,0x2522a00,0x292ae80,0x2e53480,0x337ba80,0x3783f00,0x3cac500,0x40b4980,0x45dcf80,0x49db680,0x4f03c80,0x5302380,0x582a980,0x3b52780,0x3b52780,0x3b52780,0x3b52780,0x3b52780,0x3b52780,0x3b52780,0x3b52780,0x3b52780,0x3b52780,0x3b52780,0x3b52780,0x3b52780,552,1544,2352,3344,4152,5144,5952,6944,7936,8744,9736,10544,11536,12344,13336,14129,15121,15914,16906,17442,17978,17978,17978,17978,21,20,0,0,0,0,0,0,0x468f00,0x991500,0xd99980,0x12c1f80,0x16ca400,0x1bf2a00,0x1ffae80,0x2523480,0x2a4ba80,0x2e53f00,0x337c500,0x3784980,0x3cacf80,0x40b5400,0x45dda00,0x49dc100,0x4f04700,0x5302e00,0x582b400,0x5ad9b00,0x5d88200,0x5d88200,0x5d88200,0x5d88200,0x5d88200,0x5d88200,0x5d88200,0x5d88200,0x5d88200,0x5d88200,0x5d88200,0x5d88200,553,1545,2353,3345,4153,5145,5953,6945,7937,8745,9737,10545,11537,12345,13337,14130,15122,15915,16907,17443,17979,17979,17979,17979,21,20,0,0,0,0,0,0,0x469980,0x991f80,0xd9a400,0x12c2a00,0x16cae80,0x1bf3480,0x1ffb900,0x2523f00,0x2a4c500,0x2e54980,0x337cf80,0x3785400,0x3cada00,0x40b5e80,0x45de480,0x49dcb80,0x4f05180,0x5303880,0x582be80,0x5ada580,0x5d88c80,0x5d88c80,0x5d88c80,0x5d88c80,0x5d88c80,0x5d88c80,0x5d88c80,0x5d88c80,0x5d88c80,0x5d88c80,0x5d88c80,0x5d88c80,554,1546,2354,3346,4154,5146,5954,6946,7938,8746,9738,10546,11538,12346,13338,14131,15123,15916,16908,17444,17980,18239,18239,18239,22,21,0,0,0,0,0,0,0x46a400,0x992a00,0xd9ae80,0x12c3480,0x16cb900,0x1bf3f00,0x1ffc380,0x2524980,0x2a4cf80,0x2e55400,0x337da00,0x3785e80,0x3cae480,0x40b6900,0x45def00,0x49dd600,0x4f05c00,0x5304300,0x582c900,0x5adb000,0x5d89700,0x5edac00,0x5edac00,0x5edac00,0x5edac00,0x5edac00,0x5edac00,0x5edac00,0x5edac00,0x5edac00,0x5edac00,0x5edac00,555,1547,2355,3347,4155,5147,5955,6947,7939,8747,9739,10547,11539,12347,13339,14132,15124,15917,16909,17445,17981,17981,17981,17981,21,20,0,0,0,0,0,0,0x46ae80,0x993480,0xd9b900,0x12c3f00,0x16cc380,0x1bf4980,0x1ffce00,0x2525400,0x2a4da00,0x2e55e80,0x337e480,0x3786900,0x3caef00,0x40b7380,0x45df980,0x49de080,0x4f06680,0x5304d80,0x582d380,0x5adba80,0x5d8a180,0x5d8a180,0x5d8a180,0x5d8a180,0x5d8a180,0x5d8a180,0x5d8a180,0x5d8a180,0x5d8a180,0x5d8a180,0x5d8a180,0x5d8a180,556,1548,2356,3348,4156,5148,5956,6948,7940,8748,9740,10548,11540,12348,13340,14133,15125,15918,16910,17446,17982,17982,17982,17982,21,20,0,0,0,0,0,0,0x46b900,0x993f00,0xd9c380,0x12c4980,0x16cce00,0x1bf5400,0x1ffd880,0x2525e80,0x2a4e480,0x2e56900,0x337ef00,0x3787380,0x3caf980,0x40b7e00,0x45e0400,0x49deb00,0x4f07100,0x5305800,0x582de00,0x5adc500,0x5d8ac00,0x5d8ac00,0x5d8ac00,0x5d8ac00,0x5d8ac00,0x5d8ac00,0x5d8ac00,0x5d8ac00,0x5d8ac00,0x5d8ac00,0x5d8ac00,0x5d8ac00,557,1549,2357,3349,4157,5149,5957,6949,7941,8749,9741,10549,11541,12349,13341,14134,15126,15919,16911,11592,11592,11592,11592,11592,20,19,0,0,0,0,0,0,0x46c380,0x994980,0xd9ce00,0x12c5400,0x16cd880,0x1bf5e80,0x1ffe300,0x2526900,0x2a4ef00,0x2e57380,0x337f980,0x3787e00,0x3cb0400,0x40b8880,0x45e0e80,0x49df580,0x4f07b80,0x5306280,0x582e880,0x3ca1d00,0x3ca1d00,0x3ca1d00,0x3ca1d00,0x3ca1d00,0x3ca1d00,0x3ca1d00,0x3ca1d00,0x3ca1d00,0x3ca1d00,0x3ca1d00,0x3ca1d00,0x3ca1d00,558,1550,2358,3350,4158,5150,5958,6950,7942,8750,9742,10550,11542,12350,13342,14135,15127,15920,16912,11848,17983,17983,17983,17983,21,20,0,0,0,0,0,0,0x46ce00,0x995400,0xd9d880,0x12c5e80,0x16ce300,0x1bf6900,0x1ffed80,0x2527380,0x2a4f980,0x2e57e00,0x3380400,0x3788880,0x3cb0e80,0x40b9300,0x45e1900,0x49e0000,0x4f08600,0x5306d00,0x582f300,0x3df1280,0x5d8b680,0x5d8b680,0x5d8b680,0x5d8b680,0x5d8b680,0x5d8b680,0x5d8b680,0x5d8b680,0x5d8b680,0x5d8b680,0x5d8b680,0x5d8b680,559,1551,2359,3351,4159,5151,5959,6951,7943,8751,9743,10551,11543,12351,13343,14136,15128,15921,16913,12104,12104,12104,12104,12104,20,19,0,0,0,0,0,0,0x46d880,0x995e80,0xd9e300,0x12c6900,0x16ced80,0x1bf7380,0x1fff800,0x2527e00,0x2a50400,0x2e58880,0x3380e80,0x3789300,0x3cb1900,0x40b9d80,0x45e2380,0x49e0a80,0x4f09080,0x5307780,0x582fd80,0x3f40800,0x3f40800,0x3f40800,0x3f40800,0x3f40800,0x3f40800,0x3f40800,0x3f40800,0x3f40800,0x3f40800,0x3f40800,0x3f40800,0x3f40800,560,1552,2360,3352,4160,5152,6144,6952,7944,8752,9744,10552,11544,12352,13344,14137,15129,15922,16914,17447,17984,17984,17984,17984,21,20,0,0,0,0,0,0,0x46e300,0x996900,0xd9ed80,0x12c7380,0x16cf800,0x1bf7e00,0x2120400,0x2528880,0x2a50e80,0x2e59300,0x3381900,0x3789d80,0x3cb2380,0x40ba800,0x45e2e00,0x49e1500,0x4f09b00,0x5308200,0x5830800,0x5adcf80,0x5d8c100,0x5d8c100,0x5d8c100,0x5d8c100,0x5d8c100,0x5d8c100,0x5d8c100,0x5d8c100,0x5d8c100,0x5d8c100,0x5d8c100,0x5d8c100,561,1553,2361,3353,4161,5153,6145,6953,7945,8753,9745,10553,11545,12353,13345,14138,15130,15923,16915,17448,17985,17985,17985,17985,21,20,0,0,0,0,0,0,0x46ed80,0x997380,0xd9f800,0x12c7e00,0x16d0280,0x1bf8880,0x2120e80,0x2529300,0x2a51900,0x2e59d80,0x3382380,0x378a800,0x3cb2e00,0x40bb280,0x45e3880,0x49e1f80,0x4f0a580,0x5308c80,0x5831280,0x5adda00,0x5d8cb80,0x5d8cb80,0x5d8cb80,0x5d8cb80,0x5d8cb80,0x5d8cb80,0x5d8cb80,0x5d8cb80,0x5d8cb80,0x5d8cb80,0x5d8cb80,0x5d8cb80,562,1554,2362,3354,4162,5154,6146,6954,7946,8754,9746,10554,11546,12354,13346,14139,15131,15924,16916,17449,17986,18240,18240,18240,22,21,0,0,0,0,0,0,0x46f800,0x997e00,0xda0280,0x12c8880,0x16d0d00,0x1bf9300,0x2121900,0x2529d80,0x2a52380,0x2e5a800,0x3382e00,0x378b280,0x3cb3880,0x40bbd00,0x45e4300,0x49e2a00,0x4f0b000,0x5309700,0x5831d00,0x5ade480,0x5d8d600,0x5edb680,0x5edb680,0x5edb680,0x5edb680,0x5edb680,0x5edb680,0x5edb680,0x5edb680,0x5edb680,0x5edb680,0x5edb680,563,1555,2363,3355,4163,5155,6147,6955,7947,8755,9747,10555,11547,12355,13347,14140,15132,15925,16917,17450,17987,17987,17987,17987,21,20,0,0,0,0,0,0,0x470280,0x998880,0xda0d00,0x12c9300,0x16d1780,0x1bf9d80,0x2122380,0x252a800,0x2a52e00,0x2e5b280,0x3383880,0x378bd00,0x3cb4300,0x40bc780,0x45e4d80,0x49e3480,0x4f0ba80,0x530a180,0x5832780,0x5adef00,0x5d8e080,0x5d8e080,0x5d8e080,0x5d8e080,0x5d8e080,0x5d8e080,0x5d8e080,0x5d8e080,0x5d8e080,0x5d8e080,0x5d8e080,0x5d8e080,564,1556,2364,3356,4164,5156,6148,6956,7948,8756,9748,10556,11548,12356,13348,14141,15133,15926,16918,17451,17988,17988,17988,17988,21,20,0,0,0,0,0,0,0x470d00,0x999300,0xda1780,0x12c9d80,0x16d2200,0x1bfa800,0x2122e00,0x252b280,0x2a53880,0x2e5bd00,0x3384300,0x378c780,0x3cb4d80,0x40bd200,0x45e5800,0x49e3f00,0x4f0c500,0x530ac00,0x5833200,0x5adf980,0x5d8eb00,0x5d8eb00,0x5d8eb00,0x5d8eb00,0x5d8eb00,0x5d8eb00,0x5d8eb00,0x5d8eb00,0x5d8eb00,0x5d8eb00,0x5d8eb00,0x5d8eb00,565,1557,2365,3357,4165,5157,6149,6957,7949,8757,9749,10557,11549,12357,13349,14142,15134,15927,16919,12360,12360,12360,12360,12360,20,19,0,0,0,0,0,0,0x471780,0x999d80,0xda2200,0x12ca800,0x16d2c80,0x1bfb280,0x2123880,0x252bd00,0x2a54300,0x2e5c780,0x3384d80,0x378d200,0x3cb5800,0x40bdc80,0x45e6280,0x49e4980,0x4f0cf80,0x530b680,0x5833c80,0x408fd80,0x408fd80,0x408fd80,0x408fd80,0x408fd80,0x408fd80,0x408fd80,0x408fd80,0x408fd80,0x408fd80,0x408fd80,0x408fd80,0x408fd80,566,1558,2366,3358,4166,5158,6150,6958,7950,8758,9750,10558,11550,12358,13350,14143,15135,15928,16920,12616,17989,17989,17989,17989,21,20,0,0,0,0,0,0,0x472200,0x99a800,0xda2c80,0x12cb280,0x16d3700,0x1bfbd00,0x2124300,0x252c780,0x2a54d80,0x2e5d200,0x3385800,0x378dc80,0x3cb6280,0x40be700,0x45e6d00,0x49e5400,0x4f0da00,0x530c100,0x5834700,0x41df300,0x5d8f580,0x5d8f580,0x5d8f580,0x5d8f580,0x5d8f580,0x5d8f580,0x5d8f580,0x5d8f580,0x5d8f580,0x5d8f580,0x5d8f580,0x5d8f580,567,1559,2367,3359,4167,5159,6151,6959,7951,8759,9751,10559,11551,12359,13351,14144,15136,15929,16921,12872,12872,12872,12872,12872,20,19,0,0,0,0,0,0,0x472c80,0x99b280,0xda3700,0x12cbd00,0x16d4180,0x1bfc780,0x2124d80,0x252d200,0x2a55800,0x2e5dc80,0x3386280,0x378e700,0x3cb6d00,0x40bf180,0x45e7780,0x49e5e80,0x4f0e480,0x530cb80,0x5835180,0x432e880,0x432e880,0x432e880,0x432e880,0x432e880,0x432e880,0x432e880,0x432e880,0x432e880,0x432e880,0x432e880,0x432e880,0x432e880,568,1560,2368,3360,4352,5160,6152,6960,7952,8760,9752,10560,11552,12544,13352,14145,15137,15930,16922,17452,17990,17990,17990,17990,21,20,0,0,0,0,0,0,0x473700,0x99bd00,0xda4180,0x12cc780,0x17f4d80,0x1bfd200,0x2125800,0x252dc80,0x2a56280,0x2e5e700,0x3386d00,0x378f180,0x3cb7780,0x41dfd80,0x45e8200,0x49e6900,0x4f0ef00,0x530d600,0x5835c00,0x5ae0400,0x5d90000,0x5d90000,0x5d90000,0x5d90000,0x5d90000,0x5d90000,0x5d90000,0x5d90000,0x5d90000,0x5d90000,0x5d90000,0x5d90000,569,1561,2369,3361,4353,5161,6153,6961,7953,8761,9753,10561,11553,12545,13353,14146,15138,15931,16923,17453,17991,17991,17991,17991,21,20,0,0,0,0,0,0,0x474180,0x99c780,0xda4c00,0x12cd200,0x17f5800,0x1bfdc80,0x2126280,0x252e700,0x2a56d00,0x2e5f180,0x3387780,0x378fc00,0x3cb8200,0x41e0800,0x45e8c80,0x49e7380,0x4f0f980,0x530e080,0x5836680,0x5ae0e80,0x5d90a80,0x5d90a80,0x5d90a80,0x5d90a80,0x5d90a80,0x5d90a80,0x5d90a80,0x5d90a80,0x5d90a80,0x5d90a80,0x5d90a80,0x5d90a80,570,1562,2370,3362,4354,5162,6154,6962,7954,8762,9754,10562,11554,12546,13354,14147,15139,15932,16924,17454,18176,18241,18241,18241,22,21,0,0,0,0,0,0,0x474c00,0x99d200,0xda5680,0x12cdc80,0x17f6280,0x1bfe700,0x2126d00,0x252f180,0x2a57780,0x2e5fc00,0x3388200,0x3790680,0x3cb8c80,0x41e1280,0x45e9700,0x49e7e00,0x4f10400,0x530eb00,0x5837100,0x5ae1900,0x5eb1680,0x5edc100,0x5edc100,0x5edc100,0x5edc100,0x5edc100,0x5edc100,0x5edc100,0x5edc100,0x5edc100,0x5edc100,0x5edc100,571,1563,2371,3363,4355,5163,6155,6963,7955,8763,9755,10563,11555,12547,13355,14148,15140,15933,16925,17455,18177,18177,18177,18177,21,20,0,0,0,0,0,0,0x475680,0x99dc80,0xda6100,0x12ce700,0x17f6d00,0x1bff180,0x2127780,0x252fc00,0x2a58200,0x2e60680,0x3388c80,0x3791100,0x3cb9700,0x41e1d00,0x45ea180,0x49e8880,0x4f10e80,0x530f580,0x5837b80,0x5ae2380,0x5eb2100,0x5eb2100,0x5eb2100,0x5eb2100,0x5eb2100,0x5eb2100,0x5eb2100,0x5eb2100,0x5eb2100,0x5eb2100,0x5eb2100,0x5eb2100,572,1564,2372,3364,4356,5164,6156,6964,7956,8764,9756,10564,11556,12548,13356,14149,15141,15934,16926,17456,18178,18178,18178,18178,21,20,0,0,0,0,0,0,0x476100,0x99e700,0xda6b80,0x12cf180,0x17f7780,0x1bffc00,0x2128200,0x2530680,0x2a58c80,0x2e61100,0x3389700,0x3791b80,0x3cba180,0x41e2780,0x45eac00,0x49e9300,0x4f11900,0x5310000,0x5838600,0x5ae2e00,0x5eb2b80,0x5eb2b80,0x5eb2b80,0x5eb2b80,0x5eb2b80,0x5eb2b80,0x5eb2b80,0x5eb2b80,0x5eb2b80,0x5eb2b80,0x5eb2b80,0x5eb2b80,573,1565,2373,3365,4357,5165,6157,6965,7957,8765,9757,10565,11557,12549,13357,14150,15142,15935,16927,13128,13128,13128,13128,13128,20,19,0,0,0,0,0,0,0x476b80,0x99f180,0xda7600,0x12cfc00,0x17f8200,0x1c00680,0x2128c80,0x2531100,0x2a59700,0x2e61b80,0x338a180,0x3792600,0x3cbac00,0x41e3200,0x45eb680,0x49e9d80,0x4f12380,0x5310a80,0x5839080,0x447de00,0x447de00,0x447de00,0x447de00,0x447de00,0x447de00,0x447de00,0x447de00,0x447de00,0x447de00,0x447de00,0x447de00,0x447de00,574,1566,2374,3366,4358,5166,6158,6966,7958,8766,9758,10566,11558,12550,13358,14151,15143,15936,16928,13384,18179,18179,18179,18179,21,20,0,0,0,0,0,0,0x477600,0x99fc00,0xda8080,0x12d0680,0x17f8c80,0x1c01100,0x2129700,0x2531b80,0x2a5a180,0x2e62600,0x338ac00,0x3793080,0x3cbb680,0x41e3c80,0x45ec100,0x49ea800,0x4f12e00,0x5311500,0x5839b00,0x45cd380,0x5eb3600,0x5eb3600,0x5eb3600,0x5eb3600,0x5eb3600,0x5eb3600,0x5eb3600,0x5eb3600,0x5eb3600,0x5eb3600,0x5eb3600,0x5eb3600,575,1567,2375,3367,4359,5167,6159,6967,7959,8767,9759,10567,11559,12551,13359,14336,15144,15937,16929,13640,13640,13640,13640,13640,20,19,0,0,0,0,0,0,0x478080,0x9a0680,0xda8b00,0x12d1100,0x17f9700,0x1c01b80,0x212a180,0x2532600,0x2a5ac00,0x2e63080,0x338b680,0x3793b00,0x3cbc100,0x41e4700,0x45ecb80,0x4b0b400,0x4f13880,0x5311f80,0x583a580,0x471c900,0x471c900,0x471c900,0x471c900,0x471c900,0x471c900,0x471c900,0x471c900,0x471c900,0x471c900,0x471c900,0x471c900,0x471c900,576,1568,2560,3368,4360,5168,6160,6968,7960,8768,9760,10752,11560,12552,13360,14337,15145,15938,16930,17457,18180,18180,18180,18180,21,20,0,0,0,0,0,0,0x478b00,0x9a1100,0xec9700,0x12d1b80,0x17fa180,0x1c02600,0x212ac00,0x2533080,0x2a5b680,0x2e63b00,0x338c100,0x38b4700,0x3cbcb80,0x41e5180,0x45ed600,0x4b0be80,0x4f14300,0x5312a00,0x583b000,0x5ae3880,0x5eb4080,0x5eb4080,0x5eb4080,0x5eb4080,0x5eb4080,0x5eb4080,0x5eb4080,0x5eb4080,0x5eb4080,0x5eb4080,0x5eb4080,0x5eb4080,577,1569,2561,3369,4361,5169,6161,6969,7961,8769,9761,10753,11561,12553,13361,14338,15146,15939,16931,17458,18181,18181,18181,18181,21,20,0,0,0,0,0,0,0x479580,0x9a1b80,0xeca180,0x12d2600,0x17fac00,0x1c03080,0x212b680,0x2533b00,0x2a5c100,0x2e64580,0x338cb80,0x38b5180,0x3cbd600,0x41e5c00,0x45ee080,0x4b0c900,0x4f14d80,0x5313480,0x583ba80,0x5ae4300,0x5eb4b00,0x5eb4b00,0x5eb4b00,0x5eb4b00,0x5eb4b00,0x5eb4b00,0x5eb4b00,0x5eb4b00,0x5eb4b00,0x5eb4b00,0x5eb4b00,0x5eb4b00,578,1570,2562,3370,4362,5170,6162,6970,7962,8770,9762,10754,11562,12554,13362,14339,15147,15940,16932,17459,18182,18242,18242,18242,22,21,0,0,0,0,0,0,0x47a000,0x9a2600,0xecac00,0x12d3080,0x17fb680,0x1c03b00,0x212c100,0x2534580,0x2a5cb80,0x2e65000,0x338d600,0x38b5c00,0x3cbe080,0x41e6680,0x45eeb00,0x4b0d380,0x4f15800,0x5313f00,0x583c500,0x5ae4d80,0x5eb5580,0x5edcb80,0x5edcb80,0x5edcb80,0x5edcb80,0x5edcb80,0x5edcb80,0x5edcb80,0x5edcb80,0x5edcb80,0x5edcb80,0x5edcb80,579,1571,2563,3371,4363,5171,6163,6971,7963,8771,9763,10755,11563,12555,13363,14340,15148,15941,16933,17460,18183,18183,18183,18183,21,20,0,0,0,0,0,0,0x47aa80,0x9a3080,0xecb680,0x12d3b00,0x17fc100,0x1c04580,0x212cb80,0x2535000,0x2a5d600,0x2e65a80,0x338e080,0x38b6680,0x3cbeb00,0x41e7100,0x45ef580,0x4b0de00,0x4f16280,0x5314980,0x583cf80,0x5ae5800,0x5eb6000,0x5eb6000,0x5eb6000,0x5eb6000,0x5eb6000,0x5eb6000,0x5eb6000,0x5eb6000,0x5eb6000,0x5eb6000,0x5eb6000,0x5eb6000,580,1572,2564,3372,4364,5172,6164,6972,7964,8772,9764,10756,11564,12556,13364,14341,15149,15942,16934,17461,18184,18184,18184,18184,21,20,0,0,0,0,0,0,0x47b500,0x9a3b00,0xecc100,0x12d4580,0x17fcb80,0x1c05000,0x212d600,0x2535a80,0x2a5e080,0x2e66500,0x338eb00,0x38b7100,0x3cbf580,0x41e7b80,0x45f0000,0x4b0e880,0x4f16d00,0x5315400,0x583da00,0x5ae6280,0x5eb6a80,0x5eb6a80,0x5eb6a80,0x5eb6a80,0x5eb6a80,0x5eb6a80,0x5eb6a80,0x5eb6a80,0x5eb6a80,0x5eb6a80,0x5eb6a80,0x5eb6a80,581,1573,2565,3373,4365,5173,6165,6973,7965,8773,9765,10757,11565,12557,13365,14342,15150,15943,16935,13896,13896,13896,13896,13896,20,19,0,0,0,0,0,0,0x47bf80,0x9a4580,0xeccb80,0x12d5000,0x17fd600,0x1c05a80,0x212e080,0x2536500,0x2a5eb00,0x2e66f80,0x338f580,0x38b7b80,0x3cc0000,0x41e8600,0x45f0a80,0x4b0f300,0x4f17780,0x5315e80,0x583e480,0x486be80,0x486be80,0x486be80,0x486be80,0x486be80,0x486be80,0x486be80,0x486be80,0x486be80,0x486be80,0x486be80,0x486be80,0x486be80,582,1574,2566,3374,4366,5174,6166,6974,7966,8774,9766,10758,11566,12558,13366,14343,15151,16128,16936,14152,18185,18185,18185,18185,21,20,0,0,0,0,0,0,0x47ca00,0x9a5000,0xecd600,0x12d5a80,0x17fe080,0x1c06500,0x212eb00,0x2536f80,0x2a5f580,0x2e67a00,0x3390000,0x38b8600,0x3cc0a80,0x41e9080,0x45f1500,0x4b0fd80,0x4f18200,0x5436a80,0x583ef00,0x49bb400,0x5eb7500,0x5eb7500,0x5eb7500,0x5eb7500,0x5eb7500,0x5eb7500,0x5eb7500,0x5eb7500,0x5eb7500,0x5eb7500,0x5eb7500,0x5eb7500,583,1575,2567,3375,4367,5175,6167,6975,7967,8775,9767,10759,11567,12559,13367,14344,15152,16129,16937,14408,14408,14408,14408,14408,20,19,0,0,0,0,0,0,0x47d480,0x9a5a80,0xece080,0x12d6500,0x17feb00,0x1c06f80,0x212f580,0x2537a00,0x2a60000,0x2e68480,0x3390a80,0x38b9080,0x3cc1500,0x41e9b00,0x45f1f80,0x4b10800,0x4f18c80,0x5437500,0x583f980,0x4b0a980,0x4b0a980,0x4b0a980,0x4b0a980,0x4b0a980,0x4b0a980,0x4b0a980,0x4b0a980,0x4b0a980,0x4b0a980,0x4b0a980,0x4b0a980,0x4b0a980,768,1576,2568,3376,4368,5176,6168,6976,7968,8960,9768,10760,11568,12560,13368,14345,15153,16130,16938,17462,18186,18186,18186,18186,21,20,0,0,0,0,0,0,0x59e080,0x9a6500,0xeceb00,0x12d6f80,0x17ff580,0x1c07a00,0x2130000,0x2538480,0x2a60a80,0x2f89080,0x3391500,0x38b9b00,0x3cc1f80,0x41ea580,0x45f2a00,0x4b11280,0x4f19700,0x5437f80,0x5840400,0x5ae6d00,0x5eb7f80,0x5eb7f80,0x5eb7f80,0x5eb7f80,0x5eb7f80,0x5eb7f80,0x5eb7f80,0x5eb7f80,0x5eb7f80,0x5eb7f80,0x5eb7f80,0x5eb7f80,769,1577,2569,3377,4369,5177,6169,6977,7969,8961,9769,10761,11569,12561,13369,14346,15154,16131,16939,17463,18187,18187,18187,18187,21,20,0,0,0,0,0,0,0x59eb00,0x9a6f80,0xecf580,0x12d7a00,0x1800000,0x1c08480,0x2130a80,0x2538f00,0x2a61500,0x2f89b00,0x3391f80,0x38ba580,0x3cc2a00,0x41eb000,0x45f3480,0x4b11d00,0x4f1a180,0x5438a00,0x5840e80,0x5ae7780,0x5eb8a00,0x5eb8a00,0x5eb8a00,0x5eb8a00,0x5eb8a00,0x5eb8a00,0x5eb8a00,0x5eb8a00,0x5eb8a00,0x5eb8a00,0x5eb8a00,0x5eb8a00,770,1578,2570,3378,4370,5178,6170,6978,7970,8962,9770,10762,11570,12562,13370,14347,15155,16132,16940,17464,18188,18243,18243,18243,22,21,0,0,0,0,0,0,0x59f580,0x9a7a00,0xed0000,0x12d8480,0x1800a80,0x1c08f00,0x2131500,0x2539980,0x2a61f80,0x2f8a580,0x3392a00,0x38bb000,0x3cc3480,0x41eba80,0x45f3f00,0x4b12780,0x4f1ac00,0x5439480,0x5841900,0x5ae8200,0x5eb9480,0x5edd600,0x5edd600,0x5edd600,0x5edd600,0x5edd600,0x5edd600,0x5edd600,0x5edd600,0x5edd600,0x5edd600,0x5edd600,771,1579,2571,3379,4371,5179,6171,6979,7971,8963,9771,10763,11571,12563,13371,14348,15156,16133,16941,17465,18189,18189,18189,18189,21,20,0,0,0,0,0,0,0x5a0000,0x9a8480,0xed0a80,0x12d8f00,0x1801500,0x1c09980,0x2131f80,0x253a400,0x2a62a00,0x2f8b000,0x3393480,0x38bba80,0x3cc3f00,0x41ec500,0x45f4980,0x4b13200,0x4f1b680,0x5439f00,0x5842380,0x5ae8c80,0x5eb9f00,0x5eb9f00,0x5eb9f00,0x5eb9f00,0x5eb9f00,0x5eb9f00,0x5eb9f00,0x5eb9f00,0x5eb9f00,0x5eb9f00,0x5eb9f00,0x5eb9f00,772,1580,2572,3380,4372,5180,6172,6980,7972,8964,9772,10764,11572,12564,13372,14349,15157,16134,16942,17466,18190,18190,18190,18190,21,20,0,0,0,0,0,0,0x5a0a80,0x9a8f00,0xed1500,0x12d9980,0x1801f80,0x1c0a400,0x2132a00,0x253ae80,0x2a63480,0x2f8ba80,0x3393f00,0x38bc500,0x3cc4980,0x41ecf80,0x45f5400,0x4b13c80,0x4f1c100,0x543a980,0x5842e00,0x5ae9700,0x5eba980,0x5eba980,0x5eba980,0x5eba980,0x5eba980,0x5eba980,0x5eba980,0x5eba980,0x5eba980,0x5eba980,0x5eba980,0x5eba980,773,1581,2573,3381,4373,5181,6173,6981,7973,8965,9773,10765,11573,12565,13373,14350,15158,16135,16943,14664,14664,14664,14664,14664,20,19,0,0,0,0,0,0,0x5a1500,0x9a9980,0xed1f80,0x12da400,0x1802a00,0x1c0ae80,0x2133480,0x253b900,0x2a63f00,0x2f8c500,0x3394980,0x38bcf80,0x3cc5400,0x41eda00,0x45f5e80,0x4b14700,0x4f1cb80,0x543b400,0x5843880,0x4c59f00,0x4c59f00,0x4c59f00,0x4c59f00,0x4c59f00,0x4c59f00,0x4c59f00,0x4c59f00,0x4c59f00,0x4c59f00,0x4c59f00,0x4c59f00,0x4c59f00,774,1582,2574,3382,4374,5182,6174,6982,7974,8966,9774,10766,11574,12566,13374,14351,15159,16136,16944,14920,18191,18191,18191,18191,21,20,0,0,0,0,0,0,0x5a1f80,0x9aa400,0xed2a00,0x12dae80,0x1803480,0x1c0b900,0x2133f00,0x253c380,0x2a64980,0x2f8cf80,0x3395400,0x38bda00,0x3cc5e80,0x41ee480,0x45f6900,0x4b15180,0x4f1d600,0x543be80,0x5844300,0x4da9480,0x5ebb400,0x5ebb400,0x5ebb400,0x5ebb400,0x5ebb400,0x5ebb400,0x5ebb400,0x5ebb400,0x5ebb400,0x5ebb400,0x5ebb400,0x5ebb400,775,1583,2575,3383,4375,5183,6175,6983,7975,8967,9775,10767,11575,12567,13375,14352,15160,16137,16945,15176,15176,15176,15176,15176,20,19,0,0,0,0,0,0,0x5a2a00,0x9aae80,0xed3480,0x12db900,0x1803f00,0x1c0c380,0x2134980,0x253ce00,0x2a65400,0x2f8da00,0x3395e80,0x38be480,0x3cc6900,0x41eef00,0x45f7380,0x4b15c00,0x4f1e080,0x543c900,0x5844d80,0x4ef8a00,0x4ef8a00,0x4ef8a00,0x4ef8a00,0x4ef8a00,0x4ef8a00,0x4ef8a00,0x4ef8a00,0x4ef8a00,0x4ef8a00,0x4ef8a00,0x4ef8a00,0x4ef8a00,776,1584,2576,3384,4376,5184,6176,7168,7976,8968,9776,10768,11576,12568,13376,14353,15161,16138,16946,17467,18192,18192,18192,18192,21,20,0,0,0,0,0,0,0x5a3480,0x9ab900,0xed3f00,0x12dc380,0x1804980,0x1c0ce00,0x2135400,0x265da00,0x2a65e80,0x2f8e480,0x3396900,0x38bef00,0x3cc7380,0x41ef980,0x45f7e00,0x4b16680,0x4f1eb00,0x543d380,0x5845800,0x5aea180,0x5ebbe80,0x5ebbe80,0x5ebbe80,0x5ebbe80,0x5ebbe80,0x5ebbe80,0x5ebbe80,0x5ebbe80,0x5ebbe80,0x5ebbe80,0x5ebbe80,0x5ebbe80,777,1585,2577,3385,4377,5185,6177,7169,7977,8969,9777,10769,11577,12569,13377,14354,15162,16139,16947,17468,18193,18193,18193,18193,21,20,0,0,0,0,0,0,0x5a3f00,0x9ac380,0xed4980,0x12dce00,0x1805400,0x1c0d880,0x2135e80,0x265e480,0x2a66900,0x2f8ef00,0x3397380,0x38bf980,0x3cc7e00,0x41f0400,0x45f8880,0x4b17100,0x4f1f580,0x543de00,0x5846280,0x5aeac00,0x5ebc900,0x5ebc900,0x5ebc900,0x5ebc900,0x5ebc900,0x5ebc900,0x5ebc900,0x5ebc900,0x5ebc900,0x5ebc900,0x5ebc900,0x5ebc900,778,1586,2578,3386,4378,5186,6178,7170,7978,8970,9778,10770,11578,12570,13378,14355,15163,16140,16948,17469,18194,18244,18244,18244,22,21,0,0,0,0,0,0,0x5a4980,0x9ace00,0xed5400,0x12dd880,0x1805e80,0x1c0e300,0x2136900,0x265ef00,0x2a67380,0x2f8f980,0x3397e00,0x38c0400,0x3cc8880,0x41f0e80,0x45f9300,0x4b17b80,0x4f20000,0x543e880,0x5846d00,0x5aeb680,0x5ebd380,0x5ede080,0x5ede080,0x5ede080,0x5ede080,0x5ede080,0x5ede080,0x5ede080,0x5ede080,0x5ede080,0x5ede080,0x5ede080,779,1587,2579,3387,4379,5187,6179,7171,7979,8971,9779,10771,11579,12571,13379,14356,15164,16141,16949,17470,18195,18195,18195,18195,21,20,0,0,0,0,0,0,0x5a5400,0x9ad880,0xed5e80,0x12de300,0x1806900,0x1c0ed80,0x2137380,0x265f980,0x2a67e00,0x2f90400,0x3398880,0x38c0e80,0x3cc9300,0x41f1900,0x45f9d80,0x4b18600,0x4f20a80,0x543f300,0x5847780,0x5aec100,0x5ebde00,0x5ebde00,0x5ebde00,0x5ebde00,0x5ebde00,0x5ebde00,0x5ebde00,0x5ebde00,0x5ebde00,0x5ebde00,0x5ebde00,0x5ebde00,780,1588,2580,3388,4380,5188,6180,7172,7980,8972,9780,10772,11580,12572,13380,14357,15165,16142,16950,17471,18196,18196,18196,18196,21,20,0,0,0,0,0,0,0x5a5e80,0x9ae300,0xed6900,0x12ded80,0x1807380,0x1c0f800,0x2137e00,0x2660400,0x2a68880,0x2f90e80,0x3399300,0x38c1900,0x3cc9d80,0x41f2380,0x45fa800,0x4b19080,0x4f21500,0x543fd80,0x5848200,0x5aecb80,0x5ebe880,0x5ebe880,0x5ebe880,0x5ebe880,0x5ebe880,0x5ebe880,0x5ebe880,0x5ebe880,0x5ebe880,0x5ebe880,0x5ebe880,0x5ebe880,781,1589,2581,3389,4381,5189,6181,7173,7981,8973,9781,10773,11581,12573,13381,14358,15166,16143,16951,15432,15432,15432,15432,15432,20,19,0,0,0,0,0,0,0x5a6900,0x9aed80,0xed7380,0x12df800,0x1807e00,0x1c10280,0x2138880,0x2660e80,0x2a69300,0x2f91900,0x3399d80,0x38c2380,0x3cca800,0x41f2e00,0x45fb280,0x4b19b00,0x4f21f80,0x5440800,0x5848c80,0x5047f80,0x5047f80,0x5047f80,0x5047f80,0x5047f80,0x5047f80,0x5047f80,0x5047f80,0x5047f80,0x5047f80,0x5047f80,0x5047f80,0x5047f80,782,1590,2582,3390,4382,5190,6182,7174,7982,8974,9782,10774,11582,12574,13382,14359,15167,16144,16952,15688,18197,18197,18197,18197,21,20,0,0,0,0,0,0,0x5a7380,0x9af800,0xed7e00,0x12e0280,0x1808880,0x1c10d00,0x2139300,0x2661900,0x2a69d80,0x2f92380,0x339a800,0x38c2e00,0x3ccb280,0x41f3880,0x45fbd00,0x4b1a580,0x4f22a00,0x5441280,0x5849700,0x5197500,0x5ebf300,0x5ebf300,0x5ebf300,0x5ebf300,0x5ebf300,0x5ebf300,0x5ebf300,0x5ebf300,0x5ebf300,0x5ebf300,0x5ebf300,0x5ebf300,783,1591,2583,3391,4383,5191,6183,7175,7983,8975,9783,10775,11583,12575,13383,14360,15168,16145,16953,15944,15944,15944,15944,15944,20,19,0,0,0,0,0,0,0x5a7e00,0x9b0280,0xed8880,0x12e0d00,0x1809300,0x1c11780,0x2139d80,0x2662380,0x2a6a800,0x2f92e00,0x339b280,0x38c3880,0x3ccbd00,0x41f4300,0x45fc780,0x4b1b000,0x4f23480,0x5441d00,0x584a180,0x52e6a80,0x52e6a80,0x52e6a80,0x52e6a80,0x52e6a80,0x52e6a80,0x52e6a80,0x52e6a80,0x52e6a80,0x52e6a80,0x52e6a80,0x52e6a80,0x52e6a80,784,1592,2584,3392,4384,5376,6184,7176,7984,8976,9784,10776,11584,12576,13568,14361,15169,16146,16954,17472,18198,18198,18198,18198,21,20,0,0,0,0,0,0,0x5a8880,0x9b0d00,0xed9300,0x12e1780,0x1809d80,0x1d32380,0x213a800,0x2662e00,0x2a6b280,0x2f93880,0x339bd00,0x38c4300,0x3ccc780,0x41f4d80,0x471d380,0x4b1ba80,0x4f23f00,0x5442780,0x584ac00,0x5aed600,0x5ebfd80,0x5ebfd80,0x5ebfd80,0x5ebfd80,0x5ebfd80,0x5ebfd80,0x5ebfd80,0x5ebfd80,0x5ebfd80,0x5ebfd80,0x5ebfd80,0x5ebfd80,785,1593,2585,3393,4385,5377,6185,7177,7985,8977,9785,10777,11585,12577,13569,14362,15170,16147,16955,17473,18199,18199,18199,18199,21,20,0,0,0,0,0,0,0x5a9300,0x9b1780,0xed9d80,0x12e2200,0x180a800,0x1d32e00,0x213b280,0x2663880,0x2a6bd00,0x2f94300,0x339c780,0x38c4d80,0x3ccd200,0x41f5800,0x471de00,0x4b1c500,0x4f24980,0x5443200,0x584b680,0x5aee080,0x5ec0800,0x5ec0800,0x5ec0800,0x5ec0800,0x5ec0800,0x5ec0800,0x5ec0800,0x5ec0800,0x5ec0800,0x5ec0800,0x5ec0800,0x5ec0800,786,1594,2586,3394,4386,5378,6186,7178,7986,8978,9786,10778,11586,12578,13570,14363,15171,16148,16956,17474,18200,18245,18245,18245,22,21,0,0,0,0,0,0,0x5a9d80,0x9b2200,0xeda800,0x12e2c80,0x180b280,0x1d33880,0x213bd00,0x2664300,0x2a6c780,0x2f94d80,0x339d200,0x38c5800,0x3ccdc80,0x41f6280,0x471e880,0x4b1cf80,0x4f25400,0x5443c80,0x584c100,0x5aeeb00,0x5ec1280,0x5edeb00,0x5edeb00,0x5edeb00,0x5edeb00,0x5edeb00,0x5edeb00,0x5edeb00,0x5edeb00,0x5edeb00,0x5edeb00,0x5edeb00,787,1595,2587,3395,4387,5379,6187,7179,7987,8979,9787,10779,11587,12579,13571,14364,15172,16149,16957,17475,18201,18201,18201,18201,21,20,0,0,0,0,0,0,0x5aa800,0x9b2c80,0xedb280,0x12e3700,0x180bd00,0x1d34300,0x213c780,0x2664d80,0x2a6d200,0x2f95800,0x339dc80,0x38c6280,0x3cce700,0x41f6d00,0x471f300,0x4b1da00,0x4f25e80,0x5444700,0x584cb80,0x5aef580,0x5ec1d00,0x5ec1d00,0x5ec1d00,0x5ec1d00,0x5ec1d00,0x5ec1d00,0x5ec1d00,0x5ec1d00,0x5ec1d00,0x5ec1d00,0x5ec1d00,0x5ec1d00,788,1596,2588,3396,4388,5380,6188,7180,7988,8980,9788,10780,11588,12580,13572,14365,15173,16150,16958,17476,18202,18202,18202,18202,21,20,0,0,0,0,0,0,0x5ab280,0x9b3700,0xedbd00,0x12e4180,0x180c780,0x1d34d80,0x213d200,0x2665800,0x2a6dc80,0x2f96280,0x339e700,0x38c6d00,0x3ccf180,0x41f7780,0x471fd80,0x4b1e480,0x4f26900,0x5445180,0x584d600,0x5af0000,0x5ec2780,0x5ec2780,0x5ec2780,0x5ec2780,0x5ec2780,0x5ec2780,0x5ec2780,0x5ec2780,0x5ec2780,0x5ec2780,0x5ec2780,0x5ec2780,789,1597,2589,3397,4389,5381,6189,7181,7989,8981,9789,10781,11589,12581,13573,14366,15174,16151,16959,16200,16200,16200,16200,16200,20,19,0,0,0,0,0,0,0x5abd00,0x9b4180,0xedc780,0x12e4c00,0x180d200,0x1d35800,0x213dc80,0x2666280,0x2a6e700,0x2f96d00,0x339f180,0x38c7780,0x3ccfc00,0x41f8200,0x4720800,0x4b1ef00,0x4f27380,0x5445c00,0x584e080,0x5436000,0x5436000,0x5436000,0x5436000,0x5436000,0x5436000,0x5436000,0x5436000,0x5436000,0x5436000,0x5436000,0x5436000,0x5436000,790,1598,2590,3398,4390,5382,6190,7182,7990,8982,9790,10782,11590,12582,13574,14367,15175,16152,16960,16456,18203,18203,18203,18203,21,20,0,0,0,0,0,0,0x5ac780,0x9b4c00,0xedd200,0x12e5680,0x180dc80,0x1d36280,0x213e700,0x2666d00,0x2a6f180,0x2f97780,0x339fc00,0x38c8200,0x3cd0680,0x41f8c80,0x4721280,0x4b1f980,0x4f27e00,0x5446680,0x584eb00,0x5585580,0x5ec3200,0x5ec3200,0x5ec3200,0x5ec3200,0x5ec3200,0x5ec3200,0x5ec3200,0x5ec3200,0x5ec3200,0x5ec3200,0x5ec3200,0x5ec3200,791,1599,2591,3399,4391,5383,6191,7183,7991,8983,9791,10783,11591,12583,13575,14368,15360,16153,16961,16712,16712,16712,16712,16712,20,19,0,0,0,0,0,0,0x5ad200,0x9b5680,0xeddc80,0x12e6100,0x180e700,0x1d36d00,0x213f180,0x2667780,0x2a6fc00,0x2f98200,0x33a0680,0x38c8c80,0x3cd1100,0x41f9700,0x4721d00,0x4b20400,0x5048a00,0x5447100,0x584f580,0x56d4b00,0x56d4b00,0x56d4b00,0x56d4b00,0x56d4b00,0x56d4b00,0x56d4b00,0x56d4b00,0x56d4b00,0x56d4b00,0x56d4b00,0x56d4b00,0x56d4b00,792,1600,2592,3584,4392,5384,6192,7184,7992,8984,9792,10784,11776,12584,13576,14369,15361,16154,16962,17477,18204,18204,18204,18204,21,20,0,0,0,0,0,0,0x5adc80,0x9b6100,0xede700,0x1406d00,0x180f180,0x1d37780,0x213fc00,0x2668200,0x2a70680,0x2f98c80,0x33a1100,0x38c9700,0x3df1d00,0x41fa180,0x4722780,0x4b20e80,0x5049480,0x5447b80,0x5850000,0x5af0a80,0x5ec3c80,0x5ec3c80,0x5ec3c80,0x5ec3c80,0x5ec3c80,0x5ec3c80,0x5ec3c80,0x5ec3c80,0x5ec3c80,0x5ec3c80,0x5ec3c80,0x5ec3c80,793,1601,2593,3585,4393,5385,6193,7185,7993,8985,9793,10785,11777,12585,13577,14370,15362,16155,16963,17478,18205,18205,18205,18205,21,20,0,0,0,0,0,0,0x5ae700,0x9b6b80,0xedf180,0x1407780,0x180fc00,0x1d38200,0x2140680,0x2668c80,0x2a71100,0x2f99700,0x33a1b80,0x38ca180,0x3df2780,0x41fac00,0x4723200,0x4b21900,0x5049f00,0x5448600,0x5850a80,0x5af1500,0x5ec4700,0x5ec4700,0x5ec4700,0x5ec4700,0x5ec4700,0x5ec4700,0x5ec4700,0x5ec4700,0x5ec4700,0x5ec4700,0x5ec4700,0x5ec4700,794,1602,2594,3586,4394,5386,6194,7186,7994,8986,9794,10786,11778,12586,13578,14371,15363,16156,16964,17479,18206,18246,18246,18246,22,21,0,0,0,0,0,0,0x5af180,0x9b7600,0xedfc00,0x1408200,0x1810680,0x1d38c80,0x2141100,0x2669700,0x2a71b80,0x2f9a180,0x33a2600,0x38cac00,0x3df3200,0x41fb680,0x4723c80,0x4b22380,0x504a980,0x5449080,0x5851500,0x5af1f80,0x5ec5180,0x5edf580,0x5edf580,0x5edf580,0x5edf580,0x5edf580,0x5edf580,0x5edf580,0x5edf580,0x5edf580,0x5edf580,0x5edf580,795,1603,2595,3587,4395,5387,6195,7187,7995,8987,9795,10787,11779,12587,13579,14372,15364,16157,16965,17664,18207,18207,18207,18207,21,20,0,0,0,0,0,0,0x5afc00,0x9b8080,0xee0680,0x1408c80,0x1811100,0x1d39700,0x2141b80,0x266a180,0x2a72600,0x2f9ac00,0x33a3080,0x38cb680,0x3df3c80,0x41fc100,0x4724700,0x4b22e00,0x504b400,0x5449b00,0x5851f80,0x5c12b80,0x5ec5c00,0x5ec5c00,0x5ec5c00,0x5ec5c00,0x5ec5c00,0x5ec5c00,0x5ec5c00,0x5ec5c00,0x5ec5c00,0x5ec5c00,0x5ec5c00,0x5ec5c00,796,1604,2596,3588,4396,5388,6196,7188,7996,8988,9796,10788,11780,12588,13580,14373,15365,16158,16966,17665,18208,18208,18208,18208,21,20,0,0,0,0,0,0,0x5b0680,0x9b8b00,0xee1100,0x1409700,0x1811b80,0x1d3a180,0x2142600,0x266ac00,0x2a73080,0x2f9b680,0x33a3b00,0x38cc100,0x3df4700,0x41fcb80,0x4725180,0x4b23880,0x504be80,0x544a580,0x5852a00,0x5c13600,0x5ec6680,0x5ec6680,0x5ec6680,0x5ec6680,0x5ec6680,0x5ec6680,0x5ec6680,0x5ec6680,0x5ec6680,0x5ec6680,0x5ec6680,0x5ec6680,797,1605,2597,3589,4397,5389,6197,7189,7997,8989,9797,10789,11781,12589,13581,14374,15366,16159,16967,16968,16968,16968,16968,16968,20,19,0,0,0,0,0,0,0x5b1100,0x9b9580,0xee1b80,0x140a180,0x1812600,0x1d3ac00,0x2143080,0x266b680,0x2a73b00,0x2f9c100,0x33a4580,0x38ccb80,0x3df5180,0x41fd600,0x4725c00,0x4b24300,0x504c900,0x544b000,0x5853480,0x5824080,0x5824080,0x5824080,0x5824080,0x5824080,0x5824080,0x5824080,0x5824080,0x5824080,0x5824080,0x5824080,0x5824080,0x5824080,798,1606,2598,3590,4398,5390,6198,7190,7998,8990,9798,10790,11782,12590,13582,14375,15367,16160,17152,17224,18209,18209,18209,18209,21,20,0,0,0,0,0,0,0x5b1b80,0x9ba000,0xee2600,0x140ac00,0x1813080,0x1d3b680,0x2143b00,0x266c100,0x2a74580,0x2f9cb80,0x33a5000,0x38cd600,0x3df5c00,0x41fe080,0x4726680,0x4b24d80,0x504d380,0x544ba80,0x5974080,0x5973600,0x5ec7100,0x5ec7100,0x5ec7100,0x5ec7100,0x5ec7100,0x5ec7100,0x5ec7100,0x5ec7100,0x5ec7100,0x5ec7100,0x5ec7100,0x5ec7100,799,1607,2599,3591,4399,5391,6199,7191,7999,8991,9799,10791,11783,12591,13583,14376,15368,16161,17153,17480,17480,17480,17480,17480,20,19,0,0,0,0,0,0,0x5b2600,0x9baa80,0xee3080,0x140b680,0x1813b00,0x1d3c100,0x2144580,0x266cb80,0x2a75000,0x2f9d600,0x33a5a80,0x38ce080,0x3df6680,0x41feb00,0x4727100,0x4b25800,0x504de00,0x544c500,0x5974b00,0x5ac2b80,0x5ac2b80,0x5ac2b80,0x5ac2b80,0x5ac2b80,0x5ac2b80,0x5ac2b80,0x5ac2b80,0x5ac2b80,0x5ac2b80,0x5ac2b80,0x5ac2b80,0x5ac2b80,800,1792,2600,3592,4400,5392,6200,7192,8000,8992,9984,10792,11784,12592,13584,14377,15369,16162,17154,17666,18210,18210,18210,18210,21,20,0,0,0,0,0,0,0x5b3080,0xadb680,0xee3b00,0x140c100,0x1814580,0x1d3cb80,0x2145000,0x266d600,0x2a75a80,0x2f9e080,0x34c6680,0x38ceb00,0x3df7100,0x41ff580,0x4727b80,0x4b26280,0x504e880,0x544cf80,0x5975580,0x5c14080,0x5ec7b80,0x5ec7b80,0x5ec7b80,0x5ec7b80,0x5ec7b80,0x5ec7b80,0x5ec7b80,0x5ec7b80,0x5ec7b80,0x5ec7b80,0x5ec7b80,0x5ec7b80,801,1793,2601,3593,4401,5393,6201,7193,8001,8993,9985,10793,11785,12593,13585,14378,15370,16163,17155,17667,18211,18211,18211,18211,21,20,0,0,0,0,0,0,0x5b3b00,0xadc100,0xee4580,0x140cb80,0x1815000,0x1d3d600,0x2145a80,0x266e080,0x2a76500,0x2f9eb00,0x34c7100,0x38cf580,0x3df7b80,0x4200000,0x4728600,0x4b26d00,0x504f300,0x544da00,0x5976000,0x5c14b00,0x5ec8600,0x5ec8600,0x5ec8600,0x5ec8600,0x5ec8600,0x5ec8600,0x5ec8600,0x5ec8600,0x5ec8600,0x5ec8600,0x5ec8600,0x5ec8600,802,1794,2602,3594,4402,5394,6202,7194,8002,8994,9986,10794,11786,12594,13586,14379,15371,16164,17156,17668,18212,18247,18247,18247,22,21,0,0,0,0,0,0,0x5b4580,0xadcb80,0xee5000,0x140d600,0x1815a80,0x1d3e080,0x2146500,0x266eb00,0x2a76f80,0x2f9f580,0x34c7b80,0x38d0000,0x3df8600,0x4200a80,0x4729080,0x4b27780,0x504fd80,0x544e480,0x5976a80,0x5c15580,0x5ec9080,0x5ee0000,0x5ee0000,0x5ee0000,0x5ee0000,0x5ee0000,0x5ee0000,0x5ee0000,0x5ee0000,0x5ee0000,0x5ee0000,0x5ee0000,803,1795,2603,3595,4403,5395,6203,7195,8003,8995,9987,10795,11787,12595,13587,14380,15372,16165,17157,17669,18213,18213,18213,18213,21,20,0,0,0,0,0,0,0x5b5000,0xadd600,0xee5a80,0x140e080,0x1816500,0x1d3eb00,0x2146f80,0x266f580,0x2a77a00,0x2fa0000,0x34c8600,0x38d0a80,0x3df9080,0x4201500,0x4729b00,0x4b28200,0x5050800,0x544ef00,0x5977500,0x5c16000,0x5ec9b00,0x5ec9b00,0x5ec9b00,0x5ec9b00,0x5ec9b00,0x5ec9b00,0x5ec9b00,0x5ec9b00,0x5ec9b00,0x5ec9b00,0x5ec9b00,0x5ec9b00,804,1796,2604,3596,4404,5396,6204,7196,8004,8996,9988,10796,11788,12596,13588,14381,15373,16166,17158,17670,18214,18214,18214,18214,21,20,0,0,0,0,0,0,0x5b5a80,0xade080,0xee6500,0x140eb00,0x1816f80,0x1d3f580,0x2147a00,0x2670000,0x2a78480,0x2fa0a80,0x34c9080,0x38d1500,0x3df9b00,0x4201f80,0x472a580,0x4b28c80,0x5051280,0x544f980,0x5977f80,0x5c16a80,0x5eca580,0x5eca580,0x5eca580,0x5eca580,0x5eca580,0x5eca580,0x5eca580,0x5eca580,0x5eca580,0x5eca580,0x5eca580,0x5eca580,805,1797,2605,3597,4405,5397,6205,7197,8005,8997,9989,10797,11789,12597,13589,14382,15374,16167,17159,17736,17736,17736,17736,17736,20,19,0,0,0,0,0,0,0x5b6500,0xadeb00,0xee6f80,0x140f580,0x1817a00,0x1d40000,0x2148480,0x2670a80,0x2a78f00,0x2fa1500,0x34c9b00,0x38d1f80,0x3dfa580,0x4202a00,0x472b000,0x4b29700,0x5051d00,0x5450400,0x5978a00,0x5c12100,0x5c12100,0x5c12100,0x5c12100,0x5c12100,0x5c12100,0x5c12100,0x5c12100,0x5c12100,0x5c12100,0x5c12100,0x5c12100,0x5c12100,806,1798,2606,3598,4406,5398,6206,7198,8006,8998,9990,10798,11790,12598,13590,14383,15375,16168,17160,17992,18215,18215,18215,18215,21,20,0,0,0,0,0,0,0x5b6f80,0xadf580,0xee7a00,0x1410000,0x1818480,0x1d40a80,0x2148f00,0x2671500,0x2a79980,0x2fa1f80,0x34ca580,0x38d2a00,0x3dfb000,0x4203480,0x472ba80,0x4b2a180,0x5052780,0x5450e80,0x5979480,0x5d61680,0x5ecb000,0x5ecb000,0x5ecb000,0x5ecb000,0x5ecb000,0x5ecb000,0x5ecb000,0x5ecb000,0x5ecb000,0x5ecb000,0x5ecb000,0x5ecb000,807,1799,2607,3599,4407,5399,6207,7199,8007,8999,9991,10799,11791,12599,13591,14384,15376,16169,17161,18248,18248,18248,18248,18248,20,19,0,0,0,0,0,0,0x5b7a00,0xae0000,0xee8480,0x1410a80,0x1818f00,0x1d41500,0x2149980,0x2671f80,0x2a7a400,0x2fa2a00,0x34cb000,0x38d3480,0x3dfba80,0x4203f00,0x472c500,0x4b2ac00,0x5053200,0x5451900,0x5979f00,0x5eb0c00,0x5eb0c00,0x5eb0c00,0x5eb0c00,0x5eb0c00,0x5eb0c00,0x5eb0c00,0x5eb0c00,0x5eb0c00,0x5eb0c00,0x5eb0c00,0x5eb0c00,0x5eb0c00};

__global__ __launch_bounds__(256, 1) void swin_main(const uint16_t* x, const uint16_t* mask, char* ws, uint16_t* out,
                                                    const uint16_t* wqkv, const uint16_t* wo, const uint16_t* ls,
                                                    const uint16_t* w1, const uint16_t* w2, const uint16_t* rc,
                                                    const int* ridx) {
  __shared__ __attribute__((aligned(16))) char smem[LDS_BYTES];
  const int wave = __builtin_amdgcn_readfirstlane(threadIdx.x >> 6);
  const unsigned lds0 = (unsigned)(size_t)smem;
  asm volatile(
      ".p2align 5\n"
      "s_load_dwordx8 s[16:23], %0, 0\n"
      "s_load_dwordx4 s[4:7], %0, 32\n"
      "s_load_dwordx8 s[80:87], %0, 48\n"
      "s_load_dwordx2 s[90:91], %0, 80\n"
      "s_mov_b32 s24, %1\n"
      "s_mov_b32 s25, %2\n"
      "s_mov_b64 s[60:61], %3\n"
      "s_getpc_b64 s[12:13]\n"
      "L_ipf0_%=:\n"
      "v_mbcnt_lo_u32_b32 v233, -1, 0\n"
      "v_mbcnt_hi_u32_b32 v233, -1, v233\n"
      "s_lshr_b32 s28, s25, 3\n"
      "s_lshl_b32 s29, s28, 2\n"
      "s_add_u32 s29, s29, s24\n"
      "s_mul_i32 s29, s29, 14\n"
      "v_min_u32_e64 v147, 13, v233\n"
      "v_add_lshl_u32 v147, v147, s29, 7\n"
      "v_min_u32 v147, 221056, v147\n"
      "v_lshlrev_b32 v235, 4, v233\n"
      "s_lshl_b32 s29, s24, 6\n"
      "s_add_u32 s54, s60, s29\n"
      "s_addc_u32 s55, s61, 0\n"
      "s_and_b32 s29, s29, 64\n"
      "v_xor_b32 v234, s29, v235\n"
      "s_mul_i32 s30, s24, 9216\n"
      "v_add_u32 v234, s30, v234\n"
      "s_add_u32 s2, s30, 0\n"
      "v_xor_b32_e64 v144, 32, v234\n"
      "s_mov_b64 s[34:35], 0xffff\n"
      "v_cndmask_b32 v232, v144, v234, s[34:35]\n"
      "s_lshl_b64 s[34:35], s[34:35], 48\n"
      "v_cndmask_b32 v145, v234, v144, s[34:35]\n"
      "s_lshl_b64 s[34:35], -1, 32\n"
      "v_cndmask_b32 v146, v144, v234, s[34:35]\n"
      "s_waitcnt lgkmcnt(0)\n"
      "global_load_dword v31, v147, s[4:5]\n"
      "s_lshl_b32 s29, s25, 8\n"
      "s_lshl_b32 s30, s24, 5\n"
      "s_add_u32 s29, s29, s30\n"
      "s_add_u32 s29, s29, 41408\n"
      "s_load_dwordx8 s[92:99], s[60:61], s29\n"
      "s_add_u32 m0, s2, 0\n"
      "s_nop 0\n"
      "global_load_lds_dwordx4 v234, s[4:5]\n"
      "global_load_lds_dwordx4 v234, s[4:5] offset:1024\n"
      "global_load_lds_dwordx4 v232, s[4:5] offset:2048\n"
      "global_load_lds_dwordx4 v144, s[4:5] offset:3072\n"
      "s_add_u32 s4, s4, 4096\n"
      "s_addc_u32 s5, s5, 0\n"
      "s_add_u32 m0, s2, 4096\n"
      "s_nop 0\n"
      "global_load_lds_dwordx4 v146, s[4:5]\n"
      "global_load_lds_dwordx4 v234, s[4:5] offset:1024\n"
      "global_load_lds_dwordx4 v145, s[4:5] offset:2048\n"
      "global_load_lds_dwordx4 v144, s[4:5] offset:3072\n"
      "s_add_u32 s4, s4, 4096\n"
      "s_addc_u32 s5, s5, 0\n"
      "s_add_u32 m0, s2, 8192\n"
      "s_nop 0\n"
      "global_load_lds_dwordx4 v144, s[4:5]\n"
      "s_add_u32 s4, s4, 28672\n"
      "s_addc_u32 s5, s5, 0\n"
      "s_load_dwordx16 s[36:51], s[82:83], 0\n"
      "s_load_dwordx2 s[8:9], s[80:81], 0\n"
      "s_load_dwordx16 s[64:79], s[84:85], 0\n"
      "s_lshl_b32 s29, s24, 8\n"
      "v_lshl_add_u32 v214, v233, 2, s29\n"
      "v_min_u32 v214, 672, v214\n"
      "v_add_u32 v211, 150640, v214\n"
      "s_lshl_b32 s29, s28, 2\n"
      "s_add_u32 s29, s29, s24\n"
      "s_mov_b64 s[34:35], s[16:17]\n"
      "s_mov_b32 s30, s29\n"
      "s_sub_u32 s31, s29, 48\n"
      "s_cmp_ge_u32 s29, 48\n"
      "s_cselect_b64 s[34:35], s[22:23], s[34:35]\n"
      "s_cselect_b32 s30, s31, s30\n"
      "s_sub_u32 s31, s29, 96\n"
      "s_cmp_ge_u32 s29, 96\n"
      "s_cselect_b64 s[34:35], s[18:19], s[34:35]\n"
      "s_cselect_b32 s30, s31, s30\n"
      "s_sub_u32 s31, s29, 54\n"
      "s_cmp_ge_u32 s29, 109\n"
      "s_cselect_b64 s[34:35], s[20:21], s[34:35]\n"
      "s_cselect_b32 s30, s31, s30\n"
      "s_lshl_b32 s30, s30, 21\n"
      "s_load_dword s100, s[34:35], s30\n"
      "s_and_b32 s29, s29, 7\n"
      "s_cmp_eq_u32 s29, 0\n"
      "s_cselect_b64 s[34:35], s[6:7], s[34:35]\n"
      "s_cmp_eq_u32 s29, 1\n"
      "s_cselect_b64 s[34:35], s[90:91], s[34:35]\n"
      "s_cmp_eq_u32 s29, 2\n"
      "s_cselect_b64 s[34:35], s[86:87], s[34:35]\n"
      "s_cmp_eq_u32 s29, 3\n"
      "s_cselect_b64 s[34:35], s[60:61], s[34:35]\n"
      "s_cmp_eq_u32 s29, 4\n"
      "s_cselect_b64 s[34:35], s[12:13], s[34:35]\n"
      "s_load_dword s100, s[34:35], 0\n"
      "s_add_u32 m0, s2, 36864\n"
      "s_nop 0\n"
      "global_load_lds_dwordx4 v234, s[4:5]\n"
      "global_load_lds_dwordx4 v234, s[4:5] offset:1024\n"
      "global_load_lds_dwordx4 v232, s[4:5] offset:2048\n"
      "global_load_lds_dwordx4 v144, s[4:5] offset:3072\n"
      "s_add_u32 s4, s4, 4096\n"
      "s_addc_u32 s5, s5, 0\n"
      "s_add_u32 m0, s2, 40960\n"
      "s_nop 0\n"
      "global_load_lds_dwordx4 v146, s[4:5]\n"
      "global_load_lds_dwordx4 v234, s[4:5] offset:1024\n"
      "global_load_lds_dwordx4 v145, s[4:5] offset:2048\n"
      "global_load_lds_dwordx4 v144, s[4:5] offset:3072\n"
      "s_add_u32 s4, s4, 4096\n"
      "s_addc_u32 s5, s5, 0\n"
      "s_add_u32 m0, s2, 45056\n"
      "s_nop 0\n"
      "global_load_lds_dwordx4 v144, s[4:5]\n"
      "s_add_u32 s4, s4, 28672\n"
      "s_addc_u32 s5, s5, 0\n"
      "s_add_u32 m0, s2, 73728\n"
      "s_nop 0\n"
      "global_load_lds_dwordx4 v234, s[4:5]\n"
      "global_load_lds_dwordx4 v234, s[4:5] offset:1024\n"
      "global_load_lds_dwordx4 v232, s[4:5] offset:2048\n"
      "global_load_lds_dwordx4 v144, s[4:5] offset:3072\n"
      "s_add_u32 s4, s4, 4096\n"
      "s_addc_u32 s5, s5, 0\n"
      "s_add_u32 m0, s2, 77824\n"
      "s_nop 0\n"
      "global_load_lds_dwordx4 v146, s[4:5]\n"
      "global_load_lds_dwordx4 v234, s[4:5] offset:1024\n"
      "global_load_lds_dwordx4 v145, s[4:5] offset:2048\n"
      "global_load_lds_dwordx4 v144, s[4:5] offset:3072\n"
      "s_add_u32 s4, s4, 4096\n"
      "s_addc_u32 s5, s5, 0\n"
      "s_add_u32 m0, s2, 81920\n"
      "s_nop 0\n"
      "global_load_lds_dwordx4 v144, s[4:5]\n"
      "s_add_u32 s4, s4, 32768\n"
      "s_addc_u32 s5, s5, 0\n"
      "s_waitcnt lgkmcnt(0)\n"
      "s_load_dword s100, s[16:17], s92\n"
      "s_load_dword s100, s[16:17], s93\n"
      "s_load_dword s100, s[16:17], s94\n"
      "s_load_dword s100, s[16:17], s95\n"
      "s_load_dword s100, s[16:17], s96\n"
      "s_load_dword s100, s[16:17], s97\n"
      "s_load_dword s100, s[16:17], s98\n"
      "s_load_dword s100, s[16:17], s99\n"
      "v_add_u32 v212, 4096, v144\n"
      "v_add_u32 v208, s2, v235\n"
      "v_lshrrev_b32 v209, 2, v233\n"
      "global_load_dwordx4 a[108:111], v234, s[4:5] offset:-4096\n"
      "v_and_b32 v213, 3, v209\n"
      "v_lshrrev_b32 v210, 4, v233\n"
      "global_load_dwordx4 a[112:115], v234, s[4:5] offset:-3072\n"
      "v_lshl_add_u32 v213, v210, 3, v213\n"
      "v_mul_u32_u24 v213, 1152, v213\n"
      "global_load_dwordx4 a[116:119], v232, s[4:5] offset:-2048\n"
      "v_and_b32_e64 v210, 1, v233\n"
      "v_lshl_add_u32 v213, v210, 3, v213\n"
      "global_load_dwordx4 a[120:123], v144, s[4:5] offset:-1024\n"
      "v_and_b32 v209, 6, v209\n"
      "v_bfe_u32 v210, v233, 1, 1\n"
      "s_mul_i32 s29, s24, 6\n"
      "global_load_dwordx4 a[124:127], v146, s[4:5]\n"
      "v_add_u32 v210, s29, v210\n"
      "v_xor_b32 v215, v210, v209\n"
      "global_load_dwordx4 a[128:131], v234, s[4:5] offset:1024\n"
      "v_lshl_add_u32 v215, v215, 4, v213\n"
      "v_add_u32 v210, 2, v210\n"
      "global_load_dwordx4 a[132:135], v145, s[4:5] offset:2048\n"
      "v_xor_b32 v179, v210, v209\n"
      "v_lshl_add_u32 v179, v179, 4, v213\n"
      "v_add_u32_e64 v210, 2, v210\n"
      "global_load_dwordx4 a[136:139], v144, s[4:5] offset:3072\n"
      "v_xor_b32 v178, v210, v209\n"
      "v_lshl_add_u32 v178, v178, 4, v213\n"
      "global_load_dwordx4 a[140:143], v212, s[4:5]\n"
      "v_add_u32 v209, 73728, v215\n"
      "v_add_u32 v210, 73728, v179\n"
      "v_add_u32 v213, 73728, v178\n"
      "v_add_u32 v177, 73728, v208\n"
      "s_movk_i32 s29, 18\n"
      "s_mov_b32 s30, 73600\n"
      "v_add_u32 v234, 36864, v234\n"
      "v_add_u32 v144, 36864, v144\n"
      "v_add_u32 v232, 36864, v232\n"
      "v_add_u32 v146, 36864, v146\n"
      "v_add_u32 v145, 36864, v145\n"
      "v_add_u32 v212, 36864, v212\n"
      "global_load_dwordx4 a[144:147], v234, s[4:5] offset:-4096\n"
      "s_cmp_eq_u32 s24, 1\n"
      "s_cselect_b64 s[12:13], s[6:7], s[12:13]\n"
      "s_cmp_eq_u32 s24, 2\n"
      "s_cselect_b32 s29, 16, s29\n"
      "s_cselect_b32 s30, L_cend_%=-L_ipf0_%=-128, s30\n"
      "global_load_dwordx4 a[148:151], v234, s[4:5] offset:-3072\n"
      "s_cmp_eq_u32 s24, 3\n"
      "s_cselect_b64 s[12:13], s[60:61], s[12:13]\n"
      "s_cselect_b32 s29, 10, s29\n"
      "global_load_dwordx4 a[152:155], v232, s[4:5] offset:-2048\n"
      "s_cselect_b32 s30, 40832, s30\n"
      "s_cmp_eq_u32 s24, 0\n"
      "global_load_dwordx4 a[156:159], v144, s[4:5] offset:-1024\n"
      "s_cselect_b64 s[12:13], s[90:91], s[12:13]\n"
      "s_cselect_b32 s29, 3, s29\n"
      "global_load_dwordx4 a[160:163], v146, s[4:5]\n"
      "s_cselect_b32 s30, 9476, s30\n"
      "s_mul_i32 s28, s28, s29\n"
      "global_load_dwordx4 a[164:167], v234, s[4:5] offset:1024\n"
      "s_sub_u32 s29, s29, 1\n"
      "v_min_u32_e64 v147, s29, v233\n"
      "v_add_lshl_u32 v147, v147, s28, 7\n"
      "global_load_dwordx4 a[168:171], v145, s[4:5] offset:2048\n"
      "v_min_u32_e64 v147, s30, v147\n"
      "s_mul_i32 s28, s24, 10240\n"
      "global_load_dwordx4 a[172:175], v144, s[4:5] offset:3072\n"
      "s_add_u32 s28, s28, 4096\n"
      "v_add_u32_e64 v216, s28, v235\n"
      "global_load_dwordx4 a[176:179], v212, s[4:5]\n"
      "v_add_u32 v176, 8192, v216\n"
      "global_load_dword v214, v214, s[86:87]\n"
      "s_mov_b64 s[56:57], s[90:91]\n"
      "s_load_dwordx16 s[84:99], s[84:85], 64\n"
      "s_lshl_b32 s29, s25, 8\n"
      "s_add_u32 s52, s60, s29\n"
      "s_addc_u32 s53, s61, 0\n"
      "s_lshl_b32 s29, s24, 5\n"
      "v_lshrrev_b32 v163, 3, v233\n"
      "v_lshl_add_u32 v218, v163, 2, s29\n"
      "v_add_u32 v218, 41408, v218\n"
      "global_load_dword v218, v218, s[52:53]\n"
      "global_load_dword v30, v147, s[12:13]\n"
      "v_xor_b32_e64 v163, v163, v233\n"
      "v_and_b32_e64 v163, 7, v163\n"
      "s_waitcnt vmcnt(21)\n"
      "s_barrier\n"
      "ds_read_b64_tr_b16 a[0:1], v215\n"
      "ds_read_b64_tr_b16 a[2:3], v215 offset:4608\n"
      "ds_read_b64_tr_b16 a[4:5], v179\n"
      "ds_read_b64_tr_b16 a[6:7], v179 offset:4608\n"
      "ds_read_b64_tr_b16 a[8:9], v178\n"
      "ds_read_b64_tr_b16 a[10:11], v178 offset:4608\n"
      "ds_read_b64_tr_b16 a[12:13], v215 offset:384\n"
      "ds_read_b64_tr_b16 a[14:15], v215 offset:4992\n"
      "ds_read_b64_tr_b16 a[16:17], v179 offset:384\n"
      "ds_read_b64_tr_b16 a[18:19], v179 offset:4992\n"
      "ds_read_b64_tr_b16 a[20:21], v178 offset:384\n"
      "ds_read_b64_tr_b16 a[22:23], v178 offset:4992\n"
      "ds_read_b64_tr_b16 a[24:25], v215 offset:768\n"
      "ds_read_b64_tr_b16 a[26:27], v215 offset:5376\n"
      "v_add_u32 v234, 36864, v234\n"
      "v_add_u32 v144, 36864, v144\n"
      "v_add_u32 v232, 36864, v232\n"
      "v_add_u32 v146, 36864, v146\n"
      "v_add_u32 v145, 36864, v145\n"
      "v_add_u32 v212, 36864, v212\n"
      "global_load_dwordx4 a[180:183], v234, s[4:5] offset:-4096\n"
      "ds_read_b64_tr_b16 a[28:29], v179 offset:768\n"
      "ds_read_b64_tr_b16 a[30:31], v179 offset:5376\n"
      "ds_read_b64_tr_b16 a[32:33], v178 offset:768\n"
      "ds_read_b64_tr_b16 a[34:35], v178 offset:5376\n"
      "ds_read_b64_tr_b16 a[36:37], v215 offset:36864\n"
      "ds_read_b64_tr_b16 a[38:39], v215 offset:41472\n"
      "ds_read_b64_tr_b16 a[40:41], v179 offset:36864\n"
      "ds_read_b64_tr_b16 a[42:43], v179 offset:41472\n"
      "ds_read_b64_tr_b16 a[44:45], v178 offset:36864\n"
      "ds_read_b64_tr_b16 a[46:47], v178 offset:41472\n"
      "ds_read_b64_tr_b16 a[48:49], v215 offset:37248\n"
      "ds_read_b64_tr_b16 a[50:51], v215 offset:41856\n"
      "ds_read_b64_tr_b16 a[52:53], v179 offset:37248\n"
      "ds_read_b64_tr_b16 a[54:55], v179 offset:41856\n"
      "ds_read_b64_tr_b16 a[56:57], v178 offset:37248\n"
      "ds_read_b64_tr_b16 a[58:59], v178 offset:41856\n"
      "global_load_dwordx4 a[184:187], v234, s[4:5] offset:-3072\n"
      "ds_read_b64_tr_b16 a[60:61], v215 offset:37632\n"
      "ds_read_b64_tr_b16 a[62:63], v215 offset:42240\n"
      "ds_read_b64_tr_b16 a[64:65], v179 offset:37632\n"
      "ds_read_b64_tr_b16 a[66:67], v179 offset:42240\n"
      "ds_read_b64_tr_b16 a[68:69], v178 offset:37632\n"
      "ds_read_b64_tr_b16 a[70:71], v178 offset:42240\n"
      "ds_read_b64_tr_b16 a[72:73], v209\n"
      "global_load_dwordx4 a[188:191], v232, s[4:5] offset:-2048\n"
      "ds_read_b64_tr_b16 a[74:75], v209 offset:4608\n"
      "ds_read_b64_tr_b16 a[76:77], v210\n"
      "ds_read_b64_tr_b16 a[78:79], v210 offset:4608\n"
      "ds_read_b64_tr_b16 a[80:81], v213\n"
      "ds_read_b64_tr_b16 a[82:83], v213 offset:4608\n"
      "ds_read_b64_tr_b16 a[84:85], v209 offset:384\n"
      "ds_read_b64_tr_b16 a[86:87], v209 offset:4992\n"
      "global_load_dwordx4 a[192:195], v144, s[4:5] offset:-1024\n"
      "ds_read_b64_tr_b16 a[88:89], v210 offset:384\n"
      "ds_read_b64_tr_b16 a[90:91], v210 offset:4992\n"
      "ds_read_b64_tr_b16 a[92:93], v213 offset:384\n"
      "ds_read_b64_tr_b16 a[94:95], v213 offset:4992\n"
      "ds_read_b64_tr_b16 a[96:97], v209 offset:768\n"
      "ds_read_b64_tr_b16 a[98:99], v209 offset:5376\n"
      "global_load_dwordx4 a[196:199], v146, s[4:5]\n"
      "ds_read_b64_tr_b16 a[100:101], v210 offset:768\n"
      "ds_read_b64_tr_b16 a[102:103], v210 offset:5376\n"
      "ds_read_b64_tr_b16 a[104:105], v213 offset:768\n"
      "ds_read_b64_tr_b16 a[106:107], v213 offset:5376\n"
      "s_waitcnt lgkmcnt(0)\n"
      "s_pack_ll_b32_b16 s28, s36, s44\n"
      "s_waitcnt vmcnt(7)\n"
      "v_dot2_f32_bf16 v233, s28, v214, 0\n"
      "global_load_dwordx4 a[200:203], v234, s[4:5] offset:1024\n"
      "s_pack_hh_b32_b16 s29, s36, s44\n"
      "v_dot2_f32_bf16 v232, s29, v214, 0\n"
      "s_pack_ll_b32_b16 s30, s37, s45\n"
      "v_dot2_f32_bf16 v147, s30, v214, 0\n"
      "s_pack_hh_b32_b16 s31, s37, s45\n"
      "v_dot2_f32_bf16 v219, s31, v214, 0\n"
      "s_pack_ll_b32_b16 s28, s38, s46\n"
      "global_load_dwordx4 a[204:207], v145, s[4:5] offset:2048\n"
      "v_dot2_f32_bf16 v234, s28, v214, 0\n"
      "s_pack_hh_b32_b16 s29, s38, s46\n"
      "v_dot2_f32_bf16 v145, s29, v214, 0\n"
      "s_pack_ll_b32_b16 s30, s39, s47\n"
      "v_dot2_f32_bf16 v146, s30, v214, 0\n"
      "s_pack_hh_b32_b16 s31, s39, s47\n"
      "global_load_dwordx4 a[208:211], v144, s[4:5] offset:3072\n"
      "v_dot2_f32_bf16 v217, s31, v214, 0\n"
      "s_pack_ll_b32_b16 s28, s40, s48\n"
      "v_dot2_f32_bf16 v161, s28, v214, 0\n"
      "s_pack_hh_b32_b16 s29, s40, s48\n"
      "v_dot2_f32_bf16 v160, s29, v214, 0\n"
      "s_pack_ll_b32_b16 s30, s41, s49\n"
      "v_dot2_f32_bf16 v162, s30, v214, 0\n"
      "global_load_dwordx4 a[212:215], v212, s[4:5]\n"
      "s_pack_hh_b32_b16 s31, s41, s49\n"
      "v_dot2_f32_bf16 v199, s31, v214, 0\n"
      "s_pack_ll_b32_b16 s28, s42, s50\n"
      "v_dot2_f32_bf16 v212, s28, v214, 0\n"
      "s_pack_hh_b32_b16 s29, s42, s50\n"
      "v_dot2_f32_bf16 v196, s29, v214, 0\n"
      "s_pack_ll_b32_b16 s30, s43, s51\n"
      "global_load_dwordx4 v[114:117], v216, s[60:61] offset:-4096\n"
      "v_dot2_f32_bf16 v144, s30, v214, 0\n"
      "s_pack_hh_b32_b16 s31, s43, s51\n"
      "v_dot2_f32_bf16 v198, s31, v214, 0\n"
      "s_load_dwordx8 s[36:43], s[60:61], 41216\n"
      "s_load_dwordx4 s[44:47], s[60:61], 41248\n"
      "s_mov_b64 s[48:49], s[60:61]\n"
      "global_load_dwordx4 v[168:171], v216, s[60:61] offset:3072\n"
      "v_max_f32 v233, 0, v233\n"
      "v_max_f32_e64 v232, 0, v232\n"
      "v_cvt_pk_bf16_f32 v233, v233, v232\n"
      "v_max_f32 v147, 0, v147\n"
      "v_max_f32 v219, 0, v219\n"
      "v_cvt_pk_bf16_f32 v147, v147, v219\n"
      "v_max_f32 v234, 0, v234\n"
      "v_max_f32 v145, 0, v145\n"
      "v_cvt_pk_bf16_f32 v234, v234, v145\n"
      "v_max_f32 v146, 0, v146\n"
      "v_max_f32 v217, 0, v217\n"
      "v_cvt_pk_bf16_f32 v146, v146, v217\n"
      "v_max_f32 v161, 0, v161\n"
      "v_max_f32 v160, 0, v160\n"
      "v_cvt_pk_bf16_f32 v161, v161, v160\n"
      "v_max_f32 v162, 0, v162\n"
      "v_max_f32 v199, 0, v199\n"
      "v_cvt_pk_bf16_f32 v162, v162, v199\n"
      "v_max_f32 v212, 0, v212\n"
      "v_max_f32 v196, 0, v196\n"
      "v_cvt_pk_bf16_f32 v212, v212, v196\n"
      "v_max_f32_e64 v144, 0, v144\n"
      "v_max_f32_e64 v198, 0, v198\n"
      "v_cvt_pk_bf16_f32 v144, v144, v198\n"
      "s_pack_ll_b32_b16 s28, s64, s66\n"
      "v_dot2_f32_bf16 v196, s28, v233, 0\n"
      "s_pack_hh_b32_b16 s29, s64, s66\n"
      "v_dot2_f32_bf16 v197, s29, v233, 0\n"
      "s_pack_ll_b32_b16 s30, s65, s67\n"
      "v_dot2_f32_bf16 v198, s30, v233, 0\n"
      "s_pack_hh_b32_b16 s31, s65, s67\n"
      "v_dot2_f32_bf16 v199, s31, v233, 0\n"
      "s_pack_ll_b32_b16 s28, s68, s70\n"
      "v_dot2_f32_bf16 v196, s28, v147, v196\n"
      "s_pack_hh_b32_b16 s29, s68, s70\n"
      "v_dot2_f32_bf16 v197, s29, v147, v197\n"
      "s_waitcnt vmcnt(12)\n"
      "v_lshl_add_u32 v218, v163, 4, v218\n"
      "s_lshl_b32 s10, s24, 10\n"
      "s_add_u32 m0, s10, 137216\n"
      "s_nop 0\n"
      "global_load_lds_dwordx4 v218, s[16:17]\n"
      "s_add_u32 m0, s10, 141184\n"
      "s_nop 0\n"
      "global_load_lds_dwordx4 v218, s[16:17] offset:128\n"
      "s_add_u32 m0, s10, 145152\n"
      "s_nop 0\n"
      "global_load_lds_dwordx4 v218, s[16:17] offset:256\n"
      "v_add_u32 v145, s10, v235\n"
      "s_add_u32 m0, s10, 124928\n"
      "s_nop 0\n"
      "global_load_lds_dwordx4 v145, s[56:57]\n"
      "v_add_u32 v145, 4096, v145\n"
      "s_add_u32 m0, s10, 129024\n"
      "s_nop 0\n"
      "global_load_lds_dwordx4 v145, s[56:57]\n"
      "v_add_u32 v145, 4096, v145\n"
      "v_min_u32 v145, 9600, v145\n"
      "s_add_u32 m0, s10, 133120\n"
      "s_nop 0\n"
      "global_load_lds_dwordx4 v145, s[56:57]\n"
      "s_pack_ll_b32_b16 s30, s69, s71\n"
      "v_dot2_f32_bf16 v198, s30, v147, v198\n"
      "s_pack_hh_b32_b16 s31, s69, s71\n"
      "v_dot2_f32_bf16 v199, s31, v147, v199\n"
      "s_pack_ll_b32_b16 s28, s72, s74\n"
      "v_dot2_f32_bf16 v196, s28, v234, v196\n"
      "s_pack_hh_b32_b16 s29, s72, s74\n"
      "v_dot2_f32_bf16 v197, s29, v234, v197\n"
      "s_pack_ll_b32_b16 s30, s73, s75\n"
      "v_dot2_f32_bf16 v198, s30, v234, v198\n"
      "s_pack_hh_b32_b16 s31, s73, s75\n"
      "v_dot2_f32_bf16 v199, s31, v234, v199\n"
      "s_pack_ll_b32_b16 s28, s76, s78\n"
      "v_dot2_f32_bf16 v196, s28, v146, v196\n"
      "s_pack_hh_b32_b16 s29, s76, s78\n"
      "v_dot2_f32_bf16 v197, s29, v146, v197\n"
      "s_pack_ll_b32_b16 s30, s77, s79\n"
      "v_dot2_f32_bf16 v198, s30, v146, v198\n"
      "s_pack_hh_b32_b16 s31, s77, s79\n"
      "v_dot2_f32_bf16 v199, s31, v146, v199\n"
      "s_load_dwordx16 s[64:79], s[54:55], 40960\n"
      "s_lshl_b32 s28, s24, 4\n"
      "s_lshr_b64 s[8:9], s[8:9], s28\n"
      "s_lshl_b32 s8, s8, 16\n"
      "v_mov_b32_e64 v214, s8\n"
      "v_max_f32 v214, 0xc2c80000, v214\n"
      "v_min_f32 v214, 0x40930000, v214\n"
      "v_mul_f32 v214, 0x3fb8aa3b, v214\n"
      "v_exp_f32 v214, v214\n"
      "s_nop 0\n"
      "v_cvt_pk_bf16_f32 v214, v214, v214\n"
      "v_lshlrev_b32_e64 v214, 16, v214\n"
      "s_waitcnt lgkmcnt(0)\n"
      "s_barrier\n"
      "s_pack_ll_b32_b16 s28, s84, s86\n"
      "v_dot2_f32_bf16 v196, s28, v161, v196\n"
      "ds_write_b128 v208, a[108:111]\n"
      "s_pack_hh_b32_b16 s29, s84, s86\n"
      "v_dot2_f32_bf16 v197, s29, v161, v197\n"
      "s_pack_ll_b32_b16 s30, s85, s87\n"
      "ds_write_b128 v208, a[112:115] offset:1024\n"
      "v_dot2_f32_bf16 v198, s30, v161, v198\n"
      "s_pack_hh_b32_b16 s31, s85, s87\n"
      "v_dot2_f32_bf16 v199, s31, v161, v199\n"
      "ds_write_b128 v208, a[116:119] offset:2048\n"
      "s_pack_ll_b32_b16 s28, s88, s90\n"
      "v_dot2_f32_bf16 v196, s28, v162, v196\n"
      "s_pack_hh_b32_b16 s29, s88, s90\n"
      "ds_write_b128 v208, a[120:123] offset:3072\n"
      "v_dot2_f32_bf16 v197, s29, v162, v197\n"
      "s_pack_ll_b32_b16 s30, s89, s91\n"
      "v_dot2_f32_bf16 v198, s30, v162, v198\n"
      "ds_write_b128 v208, a[124:127] offset:4096\n"
      "s_pack_hh_b32_b16 s31, s89, s91\n"
      "v_dot2_f32_bf16 v199, s31, v162, v199\n"
      "s_pack_ll_b32_b16 s28, s92, s94\n"
      "ds_write_b128 v208, a[128:131] offset:5120\n"
      "v_dot2_f32_bf16 v196, s28, v212, v196\n"
      "s_pack_hh_b32_b16 s29, s92, s94\n"
      "v_dot2_f32_bf16 v197, s29, v212, v197\n"
      "ds_write_b128 v208, a[132:135] offset:6144\n"
      "s_pack_ll_b32_b16 s30, s93, s95\n"
      "v_dot2_f32_bf16 v198, s30, v212, v198\n"
      "s_pack_hh_b32_b16 s31, s93, s95\n"
      "ds_write_b128 v208, a[136:139] offset:7168\n"
      "v_dot2_f32_bf16 v199, s31, v212, v199\n"
      "s_pack_ll_b32_b16 s28, s96, s98\n"
      "v_dot2_f32_bf16 v196, s28, v144, v196\n"
      "ds_write_b128 v208, a[140:143] offset:8192\n"
      "s_pack_hh_b32_b16 s29, s96, s98\n"
      "v_dot2_f32_bf16 v197, s29, v144, v197\n"
      "s_pack_ll_b32_b16 s30, s97, s99\n"
      "ds_write_b128 v208, a[144:147] offset:36864\n"
      "v_dot2_f32_bf16 v198, s30, v144, v198\n"
      "s_pack_hh_b32_b16 s31, s97, s99\n"
      "v_dot2_f32_bf16 v199, s31, v144, v199\n"
      "ds_write_b128 v208, a[148:151] offset:37888\n"
      "global_load_dwordx4 v[118:121], v216, s[48:49] offset:-3072\n"
      "s_load_dwordx16 s[80:95], s[52:53], 41280\n"
      "s_load_dwordx4 s[96:99], s[52:53], 41344\n"
      "s_load_dwordx2 s[100:101], s[52:53], 41360\n"
      "ds_write_b128 v208, a[152:155] offset:38912\n"
      "global_load_dwordx4 v[122:125], v216, s[48:49] offset:-2048\n"
      "s_load_dword s26, s[52:53], 41376\n"
      "s_load_dword s3, s[52:53], 41380\n"
      "v_cmp_nge_f32 vcc, 0x42480000, v214\n"
      "ds_write_b128 v208, a[156:159] offset:39936\n"
      "v_mul_f32 v214, 0x3fb8aa3b, v214\n"
      "s_mov_b64 s[50:51], vcc\n"
      "s_waitcnt vmcnt(8)\n"
      "v_add_u32 v212, 3072, v171\n"
      "global_load_dwordx4 a[216:219], v212, s[6:7] offset:-3072\n"
      "ds_write_b128 v208, a[160:163] offset:40960\n"
      "v_cvt_pk_bf16_f32 v197, v196, v197\n"
      "v_lshlrev_b32 v196, 16, v197\n"
      "v_and_b32 v197, 0xffff0000, v197\n"
      "ds_write_b128 v208, a[164:167] offset:41984\n"
      "global_load_dwordx4 a[220:223], v212, s[6:7] offset:-2944\n"
      "v_cvt_pk_bf16_f32 v199, v198, v199\n"
      "v_lshlrev_b32 v198, 16, v199\n"
      "v_and_b32 v199, 0xffff0000, v199\n"
      "ds_write_b128 v208, a[168:171] offset:43008\n"
      "v_mul_f32 v196, 0xbfb8aa3b, v196\n"
      "global_load_dwordx4 a[224:227], v212, s[6:7] offset:-2816\n"
      "v_mul_f32 v197, 0xbfb8aa3b, v197\n"
      "v_mul_f32 v198, 0xbfb8aa3b, v198\n"
      "ds_write_b128 v208, a[172:175] offset:44032\n"
      "v_mul_f32 v199, 0xbfb8aa3b, v199\n"
      "global_load_dwordx4 a[228:231], v212, s[6:7]\n"
      "v_exp_f32 v196, v196\n"
      "v_exp_f32 v197, v197\n"
      "ds_write_b128 v208, a[176:179] offset:45056\n"
      "v_exp_f32 v198, v198\n"
      "v_exp_f32 v199, v199\n"
      "global_load_dwordx4 a[232:235], v212, s[6:7] offset:128\n"
      "v_add_f32 v196, 1.0, v196\n"
      "ds_write_b128 v177, a[180:183]\n"
      "v_add_f32 v197, 1.0, v197\n"
      "v_add_f32 v198, 1.0, v198\n"
      "global_load_dwordx4 a[236:239], v212, s[6:7] offset:256\n"
      "v_add_f32 v199, 1.0, v199\n"
      "ds_write_b128 v177, a[184:187] offset:1024\n"
      "v_rcp_f32 v196, v196\n"
      "v_rcp_f32 v197, v197\n"
      "v_rcp_f32_e64 v198, v198\n"
      "ds_write_b128 v177, a[188:191] offset:2048\n"
      "v_add_u32 v212, 9216, v171\n"
      "global_load_dwordx4 a[240:243], v212, s[6:7] offset:-3072\n"
      "v_rcp_f32 v199, v199\n"
      "v_cvt_pk_bf16_f32 v197, v196, v197\n"
      "v_lshlrev_b32 v196, 16, v197\n"
      "ds_write_b128 v177, a[192:195] offset:3072\n"
      "v_and_b32 v197, 0xffff0000, v197\n"
      "global_load_dwordx4 a[244:247], v212, s[6:7] offset:-2944\n"
      "v_cvt_pk_bf16_f32 v199, v198, v199\n"
      "v_lshlrev_b32 v198, 16, v199\n"
      "ds_write_b128 v177, a[196:199] offset:4096\n"
      "v_and_b32 v199, 0xffff0000, v199\n"
      "v_add_f32 v196, -0.5, v196\n"
      "global_load_dwordx4 a[248:251], v212, s[6:7] offset:-2816\n"
      "v_mul_f32 v196, 0x41b8aa3b, v196\n"
      "ds_write_b128 v177, a[200:203] offset:5120\n"
      "ds_write_b32 v211, v196\n"
      "v_add_f32_e64 v197, -0.5, v197\n"
      "global_load_dwordx4 a[252:255], v212, s[6:7]\n"
      "v_mul_f32 v197, 0x41b8aa3b, v197\n"
      "ds_write_b128 v177, a[204:207] offset:6144\n"
      "ds_write_b32 v211, v197 offset:768\n"
      "v_add_f32_e64 v198, -0.5, v198\n"
      "v_mul_f32 v198, 0x41b8aa3b, v198\n"
      "global_load_dwordx4 v[0:3], v212, s[6:7] offset:128\n"
      "ds_write_b128 v177, a[208:211] offset:7168\n"
      "ds_write_b32 v211, v198 offset:1536\n"
      "v_add_f32_e64 v199, -0.5, v199\n"
      "v_mul_f32 v199, 0x41b8aa3b, v199\n"
      "global_load_dwordx4 v[4:7], v212, s[6:7] offset:256\n"
      "ds_write_b128 v177, a[212:215] offset:8192\n"
      "ds_write_b32 v211, v199 offset:2304\n"
      "s_waitcnt vmcnt(14)\n"
      "s_waitcnt lgkmcnt(0)\n"
      "s_barrier\n"
      "v_readfirstlane_b32 s36, v214\n"
      "L_sched_%=:\n"
      "v_add_u32 v214, 137216, v114\n"
      "v_add_u32 v177, 137216, v115\n"
      "s_mov_b32 s11, 100466688\n"
      "ds_read_b128 v[232:235], v214\n"
      "ds_read_b128 v[144:147], v177\n"
      "ds_read_b64_tr_b16 a[108:109], v215\n"
      "ds_read_b64_tr_b16 a[124:125], v179 offset:384\n"
      "ds_read_b64_tr_b16 a[134:135], v215 offset:5376\n"
      "ds_read_b64_tr_b16 a[116:117], v178\n"
      "ds_read_b64_tr_b16 a[120:121], v215 offset:384\n"
      "ds_read_b64_tr_b16 a[138:139], v179 offset:5376\n"
      "ds_read_b64_tr_b16 a[130:131], v178 offset:4992\n"
      "ds_read_b64_tr_b16 a[122:123], v215 offset:4992\n"
      "ds_read_b64_tr_b16 a[126:127], v179 offset:4992\n"
      "ds_read_b64_tr_b16 a[128:129], v178 offset:384\n"
      "ds_read_b64_tr_b16 a[114:115], v179 offset:4608\n"
      "ds_read_b64_tr_b16 a[118:119], v178 offset:4608\n"
      "ds_read_b64_tr_b16 a[132:133], v215 offset:768\n"
      "ds_read_b64_tr_b16 a[110:111], v215 offset:4608\n"
      "ds_read_b64_tr_b16 a[112:113], v179\n"
      "s_waitcnt lgkmcnt(15)\n"
      "v_mfma_f32_16x16x32_bf16 v[196:199], a[0:3], v[232:235], 0\n"
      "ds_read_b64_tr_b16 a[136:137], v179 offset:768\n"
      "v_mfma_f32_16x16x32_bf16 v[196:199], a[36:39], v[144:147], v[196:199]\n"
      "ds_read_b128 v[160:163], v177 offset:2048\n"
      "v_mfma_f32_16x16x32_bf16 v[180:183], a[4:7], v[232:235], 0\n"
      "s_sub_u32 s16, s16, 99876864\n"
      "ds_read_b64_tr_b16 a[140:141], v178 offset:768\n"
      "v_mfma_f32_16x16x32_bf16 v[180:183], a[40:43], v[144:147], v[180:183]\n"
      "ds_read_b64_tr_b16 a[144:145], v215 offset:36864\n"
      "s_subb_u32 s17, s17, 0\n"
      "v_mfma_f32_16x16x32_bf16 v[228:231], a[8:11], v[232:235], 0\n"
      "global_load_dwordx4 v[204:207], v176, s[48:49] offset:-3072\n"
      "v_mfma_f32_16x16x32_bf16 v[228:231], a[44:47], v[144:147], v[228:231]\n"
      "ds_read_b64_tr_b16 a[142:143], v178 offset:5376\n"
      "ds_read_b128 v[252:255], v214 offset:2048\n"
      "v_mfma_f32_16x16x32_bf16 v[184:187], a[12:15], v[232:235], 0\n"
      "ds_read_b64_tr_b16 a[146:147], v215 offset:41472\n"
      "v_mov_b32 v37, 0\n"
      "v_mfma_f32_16x16x32_bf16 v[184:187], a[48:51], v[144:147], v[184:187]\n"
      "ds_read_b64_tr_b16 a[148:149], v179 offset:36864\n"
      "v_mov_b32 v36, 0\n"
      "v_mfma_f32_16x16x32_bf16 v[140:143], a[16:19], v[232:235], 0\n"
      "ds_read_b64_tr_b16 a[150:151], v179 offset:41472\n"
      "v_mov_b32 v42, 0\n"
      "v_mfma_f32_16x16x32_bf16 v[140:143], a[52:55], v[144:147], v[140:143]\n"
      "v_mov_b32 v61, 0\n"
      "v_mov_b32 v57, 0\n"
      "v_mfma_f32_16x16x32_bf16 v[164:167], a[20:23], v[232:235], 0\n"
      "ds_read_b64_tr_b16 a[152:153], v178 offset:36864\n"
      "v_mov_b32 v49, 0\n"
      "v_mfma_f32_16x16x32_bf16 v[164:167], a[56:59], v[144:147], v[164:167]\n"
      "ds_read_b64_tr_b16 a[154:155], v178 offset:41472\n"
      "v_mov_b32 v53, 0\n"
      "v_mfma_f32_16x16x32_bf16 v[240:243], a[24:27], v[232:235], 0\n"
      "ds_read_b64_tr_b16 a[156:157], v215 offset:37248\n"
      "v_mov_b32 v43, 0\n"
      "v_mfma_f32_16x16x32_bf16 v[240:243], a[60:63], v[144:147], v[240:243]\n"
      "ds_read_b64_tr_b16 a[160:161], v179 offset:37248\n"
      "v_mov_b32 v65, 0\n"
      "v_mfma_f32_16x16x32_bf16 v[152:155], a[28:31], v[232:235], 0\n"
      "ds_read_b64_tr_b16 a[158:159], v215 offset:41856\n"
      "v_mov_b32 v48, 0\n"
      "v_mfma_f32_16x16x32_bf16 v[152:155], a[64:67], v[144:147], v[152:155]\n"
      "ds_read_b64_tr_b16 a[162:163], v179 offset:41856\n"
      "v_mfma_f32_16x16x32_bf16 v[224:227], a[32:35], v[232:235], 0\n"
      "ds_read_b64_tr_b16 a[164:165], v178 offset:37248\n"
      "s_waitcnt lgkmcnt(7)\n"
      "v_mfma_f32_16x16x32_bf16 v[224:227], a[68:71], v[144:147], v[224:227]\n"
      "ds_read_b64_tr_b16 a[172:173], v179 offset:37632\n"
      "ds_read_b64_tr_b16 a[166:167], v178 offset:41856\n"
      "v_mfma_f32_16x16x32_bf16 v[232:235], a[0:3], v[252:255], 0\n"
      "ds_read_b64_tr_b16 a[168:169], v215 offset:37632\n"
      "s_sub_u32 s22, s22, 99876864\n"
      "v_mfma_f32_16x16x32_bf16 v[232:235], a[36:39], v[160:163], v[232:235]\n"
      "ds_read_b64_tr_b16 a[198:199], v210 offset:4992\n"
      "ds_read_b64_tr_b16 a[184:185], v210\n"
      "v_mfma_f32_16x16x32_bf16 v[144:147], a[4:7], v[252:255], 0\n"
      "s_subb_u32 s23, s23, 0\n"
      "ds_read_b64_tr_b16 a[170:171], v215 offset:42240\n"
      "v_mfma_f32_16x16x32_bf16 v[144:147], a[40:43], v[160:163], v[144:147]\n"
      "ds_read_b64_tr_b16 a[210:211], v210 offset:5376\n"
      "ds_read_b64_tr_b16 a[176:177], v178 offset:37632\n"
      "v_mfma_f32_16x16x32_bf16 v[148:151], a[8:11], v[252:255], 0\n"
      "ds_read_b64_tr_b16 a[174:175], v179 offset:42240\n"
      "v_mfma_f32_16x16x32_bf16 v[148:151], a[44:47], v[160:163], v[148:151]\n"
      "ds_read_b64_tr_b16 a[180:181], v209\n"
      "ds_read_b64_tr_b16 a[188:189], v213\n"
      "v_mfma_f32_16x16x32_bf16 v[236:239], a[12:15], v[252:255], 0\n"
      "ds_read_b64_tr_b16 a[178:179], v178 offset:42240\n"
      "v_mfma_f32_16x16x32_bf16 v[236:239], a[48:51], v[160:163], v[236:239]\n"
      "ds_read_b64_tr_b16 a[212:213], v213 offset:768\n"
      "ds_read_b64_tr_b16 a[182:183], v209 offset:4608\n"
      "v_mfma_f32_16x16x32_bf16 v[200:203], a[16:19], v[252:255], 0\n"
      "ds_read_b64_tr_b16 a[190:191], v213 offset:4608\n"
      "v_mfma_f32_16x16x32_bf16 v[200:203], a[52:55], v[160:163], v[200:203]\n"
      "ds_read_b64_tr_b16 a[186:187], v210 offset:4608\n"
      "v_mfma_f32_16x16x32_bf16 v[136:139], a[20:23], v[252:255], 0\n"
      "s_add_u32 s12, s20, 16777216\n"
      "ds_read_b64_tr_b16 a[196:197], v210 offset:384\n"
      "v_mfma_f32_16x16x32_bf16 v[136:139], a[56:59], v[160:163], v[136:139]\n"
      "ds_read_b64_tr_b16 a[200:201], v213 offset:384\n"
      "ds_read_b64_tr_b16 a[206:207], v209 offset:5376\n"
      "v_mfma_f32_16x16x32_bf16 v[188:191], a[24:27], v[252:255], 0\n"
      "s_addc_u32 s13, s21, 0\n"
      "ds_read_b64_tr_b16 a[192:193], v209 offset:384\n"
      "v_mfma_f32_16x16x32_bf16 v[188:191], a[60:63], v[160:163], v[188:191]\n"
      "ds_read_b64_tr_b16 a[202:203], v213 offset:4992\n"
      "v_mfma_f32_16x16x32_bf16 v[172:175], a[28:31], v[252:255], 0\n"
      "ds_read_b64_tr_b16 a[208:209], v210 offset:768\n"
      "v_mfma_f32_16x16x32_bf16 v[172:175], a[64:67], v[160:163], v[172:175]\n"
      "ds_read_b64_tr_b16 a[204:205], v209 offset:768\n"
      "ds_read_b64_tr_b16 a[194:195], v209 offset:4992\n"
      "v_mfma_f32_16x16x32_bf16 v[156:159], a[32:35], v[252:255], 0\n"
      "ds_read_b64_tr_b16 a[214:215], v213 offset:5376\n"
      "v_mfma_f32_16x16x32_bf16 v[156:159], a[68:71], v[160:163], v[156:159]\n"
      "global_load_dwordx4 v[160:163], v176, s[48:49] offset:-4096\n"
      "s_waitcnt lgkmcnt(0)\n"
      "s_barrier\n"
      "ds_read_b128 v[208:211], v214 offset:4096\n"
      "ds_read_b128 v[252:255], v177 offset:4096\n"
      "s_waitcnt vmcnt(1)\n"
      "v_mov_b32 v66, v204\n"
      "s_mov_b32 s60, 100270464\n"
      "s_mov_b32 s61, 196224\n"
      "s_lshr_b32 s8, s80, 8\n"
      "ds_read_b128 v[192:195], v214 offset:6144\n"
      "s_and_b32 s9, s80, 0xff\n"
      "s_mul_i32 s10, s8, 73\n"
      "v_mov_b32_e64 v68, v204\n"
      "s_add_u32 s10, s10, s9\n"
      "s_mul_i32 s28, s10, 4802\n"
      "s_add_u32 s56, s18, s28\n"
      "s_addc_u32 s57, s19, 0\n"
      "s_and_b32 s56, s56, -16\n"
      "s_sub_u32 s58, s56, 110592\n"
      "ds_read2_b32 v[80:81], v168 offset0:2 offset1:3\n"
      "s_subb_u32 s59, s57, 0\n"
      "s_sub_u32 s56, s56, 73728\n"
      "v_add_u32 v218, 99876864, v218\n"
      "v_mov_b32 v67, v204\n"
      "s_subb_u32 s57, s57, 0\n"
      "s_mul_i32 s28, s8, 1373568\n"
      "s_mul_i32 s29, s9, 2688\n"
      "s_add_u32 s28, s28, s29\n"
      "s_cmp_eq_u32 s8, 72\n"
      "s_cselect_b32 s8, 0, 100270464\n"
      "s_cmp_eq_u32 s9, 72\n"
      "s_cselect_b32 s9, 0, 196224\n"
      "ds_read2_b32 v[78:79], v168 offset0:0 offset1:1\n"
      "s_waitcnt lgkmcnt(3)\n"
      "v_mfma_f32_16x16x32_bf16 v[196:199], a[72:75], v[208:211], v[196:199]\n"
      "ds_read2_b32 v[84:85], v168 offset0:18 offset1:19\n"
      "v_mov_b32 v69, v204\n"
      "v_mfma_f32_16x16x32_bf16 v[196:199], a[108:111], v[252:255], v[196:199]\n"
      "s_add_u32 s10, s8, s9\n"
      "ds_read2_b32 v[82:83], v168 offset0:16 offset1:17\n"
      "v_mfma_f32_16x16x32_bf16 v[180:183], a[76:79], v[208:211], v[180:183]\n"
      "ds_read2_b32 v[88:89], v168 offset0:34 offset1:35\n"
      "s_add_u32 s28, s28, s10\n"
      "v_mfma_f32_16x16x32_bf16 v[180:183], a[112:115], v[252:255], v[180:183]\n"
      "ds_read2_b32 v[86:87], v168 offset0:32 offset1:33\n"
      "s_add_u32 s52, s16, s28\n"
      "v_mfma_f32_16x16x32_bf16 v[228:231], a[80:83], v[208:211], v[228:231]\n"
      "ds_read_b32 v70, v169 offset:192\n"
      "v_mfma_f32_16x16x32_bf16 v[228:231], a[116:119], v[252:255], v[228:231]\n"
      "s_addc_u32 s53, s17, 0\n"
      "ds_read_b32 v74, v169 offset:6464\n"
      "v_mfma_f32_16x16x32_bf16 v[184:187], a[84:87], v[208:211], v[184:187]\n"
      "ds_read_b32 v72, v169 offset:3328\n"
      "v_mov_b32 v204, 0\n"
      "v_mfma_f32_16x16x32_bf16 v[184:187], a[120:123], v[252:255], v[184:187]\n"
      "ds_read_b32 v73, v170 offset:64\n"
      "s_cmp_lg_u32 s10, s11\n"
      "v_mfma_f32_16x16x32_bf16 v[140:143], a[88:91], v[208:211], v[140:143]\n"
      "ds_read_b32 v71, v170\n"
      "v_mfma_f32_16x16x32_bf16 v[140:143], a[124:127], v[252:255], v[140:143]\n"
      "ds_read_b32 v75, v170 offset:128\n"
      "v_mfma_f32_16x16x32_bf16 v[164:167], a[92:95], v[208:211], v[164:167]\n"
      "s_cbranch_scc1 L_edge_1_%=\n"
      "L_eb_2_%=:\n"
      "v_mfma_f32_16x16x32_bf16 v[164:167], a[128:131], v[252:255], v[164:167]\n"
      "s_waitcnt vmcnt(0)\n"
      "ds_write_b32 v161, v218\n"
      "v_mfma_f32_16x16x32_bf16 v[240:243], a[96:99], v[208:211], v[240:243]\n"
      "ds_read_b128 v[218:221], v177 offset:6144\n"
      "v_mov_b32 v161, 134528\n"
      "v_mfma_f32_16x16x32_bf16 v[240:243], a[132:135], v[252:255], v[240:243]\n"
      "ds_read_b32 v77, v161\n"
      "v_add_u32 v161, 3136, v168\n"
      "v_mfma_f32_16x16x32_bf16 v[152:155], a[100:103], v[208:211], v[152:155]\n"
      "ds_read2_b32 v[90:91], v161 offset0:0 offset1:1\n"
      "s_add_u32 m0, s76, 0\n"
      "v_mfma_f32_16x16x32_bf16 v[152:155], a[136:139], v[252:255], v[152:155]\n"
      "global_load_lds_dwordx4 v116, s[52:53]\n"
      "v_mfma_f32_16x16x32_bf16 v[224:227], a[104:107], v[208:211], v[224:227]\n"
      "ds_read2_b32 v[94:95], v161 offset0:16 offset1:17\n"
      "v_add_u32 v217, 6272, v168\n"
      "v_mfma_f32_16x16x32_bf16 v[224:227], a[140:143], v[252:255], v[224:227]\n"
      "s_add_u32 m0, s76, 6016\n"
      "ds_read2_b32 v[96:97], v161 offset0:18 offset1:19\n"
      "global_load_lds_dwordx4 v116, s[52:53] offset:128\n"
      "v_mov_b32_e64 v255, v205\n"
      "s_waitcnt lgkmcnt(4)\n"
      "v_mfma_f32_16x16x32_bf16 v[232:235], a[72:75], v[192:195], v[232:235]\n"
      "s_add_u32 m0, s76, 12032\n"
      "v_mov_b32 v253, v205\n"
      "v_mov_b32_e64 v252, v205\n"
      "v_mfma_f32_16x16x32_bf16 v[232:235], a[108:111], v[218:221], v[232:235]\n"
      "global_load_lds_dwordx4 v116, s[52:53] offset:256\n"
      "v_mfma_f32_16x16x32_bf16 v[144:147], a[76:79], v[192:195], v[144:147]\n"
      "s_add_u32 m0, s77, 0\n"
      "ds_read2_b32 v[100:101], v161 offset0:34 offset1:35\n"
      "v_mfma_f32_16x16x32_bf16 v[144:147], a[112:115], v[218:221], v[144:147]\n"
      "global_load_lds_dwordx4 v117, s[52:53]\n"
      "v_mfma_f32_16x16x32_bf16 v[148:151], a[80:83], v[192:195], v[148:151]\n"
      "ds_read2_b32 v[92:93], v161 offset0:2 offset1:3\n"
      "v_mfma_f32_16x16x32_bf16 v[148:151], a[116:119], v[218:221], v[148:151]\n"
      "ds_read2_b32 v[102:103], v217 offset0:0 offset1:1\n"
      "v_mov_b32_e64 v209, 0\n"
      "v_mfma_f32_16x16x32_bf16 v[236:239], a[84:87], v[192:195], v[236:239]\n"
      "ds_read2_b32 v[106:107], v217 offset0:16 offset1:17\n"
      "v_mfma_f32_16x16x32_bf16 v[236:239], a[120:123], v[218:221], v[236:239]\n"
      "ds_read2_b32 v[98:99], v161 offset0:32 offset1:33\n"
      "v_mov_b32_e64 v208, 0\n"
      "v_mfma_f32_16x16x32_bf16 v[200:203], a[88:91], v[192:195], v[200:203]\n"
      "ds_read2_b32 v[104:105], v217 offset0:2 offset1:3\n"
      "v_mov_b32_e64 v254, v205\n"
      "v_mfma_f32_16x16x32_bf16 v[200:203], a[124:127], v[218:221], v[200:203]\n"
      "ds_read2_b32 v[110:111], v217 offset0:32 offset1:33\n"
      "v_mfma_f32_16x16x32_bf16 v[136:139], a[92:95], v[192:195], v[136:139]\n"
      "ds_read2_b32 v[108:109], v217 offset0:18 offset1:19\n"
      "s_add_u32 m0, s78, 0\n"
      "v_mfma_f32_16x16x32_bf16 v[136:139], a[128:131], v[218:221], v[136:139]\n"
      "s_mov_b32 exec_hi, 0\n"
      "global_load_lds_dwordx4 v118, s[52:53]\n"
      "s_mov_b32 exec_hi, -1\n"
      "v_mfma_f32_16x16x32_bf16 v[188:191], a[96:99], v[192:195], v[188:191]\n"
      "s_add_u32 m0, s76, 110592\n"
      "s_waitcnt lgkmcnt(5)\n"
      "v_mfma_f32_16x16x32_bf16 v[188:191], a[132:135], v[218:221], v[188:191]\n"
      "global_load_lds_dwordx4 v119, s[56:57]\n"
      "v_mfma_f32_16x16x32_bf16 v[172:175], a[100:103], v[192:195], v[172:175]\n"
      "s_add_u32 m0, s79, 0\n"
      "ds_read2_b32 v[112:113], v217 offset0:34 offset1:35\n"
      "v_mfma_f32_16x16x32_bf16 v[172:175], a[136:139], v[218:221], v[172:175]\n"
      "s_waitcnt lgkmcnt(3)\n"
      "s_mul_i32 s2, s24, 768\n"
      "v_mov_b32_e64 v205, 0\n"
      "v_mfma_f32_16x16x32_bf16 v[156:159], a[104:107], v[192:195], v[156:159]\n"
      "s_add_u32 s2, s2, 150640\n"
      "ds_read_b128 v[192:195], v177 offset:8192\n"
      "v_mfma_f32_16x16x32_bf16 v[156:159], a[140:143], v[218:221], v[156:159]\n"
      "ds_read_b128 v[218:221], v214 offset:8192\n"
      "v_lshl_add_u32 v78, v78, 2, s2\n"
      "v_lshl_add_u32 v84, v84, 2, s2\n"
      "v_lshl_add_u32 v89, v89, 2, s2\n"
      "global_load_lds_dword v122, s[58:59]\n"
      "v_lshl_add_u32 v83, v83, 2, s2\n"
      "v_add_u32 v212, 15360, v171\n"
      "v_lshl_add_u32 v85, v85, 2, s2\n"
      "v_lshl_add_u32 v86, v86, 2, s2\n"
      "v_lshl_add_u32 v93, v93, 2, s2\n"
      "v_lshl_add_u32 v92, v92, 2, s2\n"
      "v_lshl_add_u32 v82, v82, 2, s2\n"
      "v_lshl_add_u32 v79, v79, 2, s2\n"
      "v_lshl_add_u32 v81, v81, 2, s2\n"
      "ds_read_b32 v78, v78\n"
      "v_lshl_add_u32 v101, v101, 2, s2\n"
      "v_lshl_add_u32 v97, v97, 2, s2\n"
      "ds_read_b32 v79, v79\n"
      "v_lshl_add_u32 v80, v80, 2, s2\n"
      "v_lshl_add_u32 v87, v87, 2, s2\n"
      "v_lshl_add_u32 v98, v98, 2, s2\n"
      "s_waitcnt lgkmcnt(2)\n"
      "v_mfma_f32_16x16x32_bf16 v[196:199], a[144:147], v[218:221], v[196:199]\n"
      "ds_read_b32 v80, v80\n"
      "v_lshl_add_u32 v95, v95, 2, s2\n"
      "v_mfma_f32_16x16x32_bf16 v[196:199], a[180:183], v[192:195], v[196:199]\n"
      "global_load_dwordx4 v[126:129], v216, s[48:49] offset:-1024\n"
      "v_mfma_f32_16x16x32_bf16 v[228:231], a[152:155], v[218:221], v[228:231]\n"
      "ds_read_b32 v81, v81\n"
      "v_lshl_add_u32 v94, v94, 2, s2\n"
      "v_mfma_f32_16x16x32_bf16 v[180:183], a[148:151], v[218:221], v[180:183]\n"
      "s_nop 0\n"
      "v_lshl_add_u32 v100, v100, 2, s2\n"
      "v_lshl_add_u32 v88, v88, 2, s2\n"
      "v_mfma_f32_16x16x32_bf16 v[180:183], a[184:187], v[192:195], v[180:183]\n"
      "ds_read_b32 v86, v86\n"
      "v_lshl_add_u32 v99, v99, 2, s2\n"
      "v_mfma_f32_16x16x32_bf16 v[228:231], a[188:191], v[192:195], v[228:231]\n"
      "ds_read_b32 v82, v82\n"
      "v_lshl_add_u32 v91, v91, 2, s2\n"
      "v_mfma_f32_16x16x32_bf16 v[184:187], a[156:159], v[218:221], v[184:187]\n"
      "ds_read_b32 v84, v84\n"
      "v_lshl_add_u32 v90, v90, 2, s2\n"
      "v_mfma_f32_16x16x32_bf16 v[184:187], a[192:195], v[192:195], v[184:187]\n"
      "ds_read_b32 v83, v83\n"
      "v_mfma_f32_16x16x32_bf16 v[140:143], a[160:163], v[218:221], v[140:143]\n"
      "global_load_dwordx4 v[130:133], v216, s[48:49]\n"
      "v_mfma_f32_16x16x32_bf16 v[140:143], a[196:199], v[192:195], v[140:143]\n"
      "ds_read_b32 v93, v93\n"
      "ds_read_b32 v90, v90\n"
      "v_mfma_f32_16x16x32_bf16 v[164:167], a[164:167], v[218:221], v[164:167]\n"
      "ds_read_b32 v88, v88\n"
      "v_lshl_add_u32 v102, v102, 2, s2\n"
      "v_mfma_f32_16x16x32_bf16 v[164:167], a[200:203], v[192:195], v[164:167]\n"
      "ds_read_b32 v89, v89\n"
      "ds_read_b32 v87, v87\n"
      "v_lshl_add_u32 v96, v96, 2, s2\n"
      "v_mfma_f32_16x16x32_bf16 v[240:243], a[168:171], v[218:221], v[240:243]\n"
      "v_lshl_add_u32 v105, v105, 2, s2\n"
      "v_lshl_add_u32 v103, v103, 2, s2\n"
      "v_mfma_f32_16x16x32_bf16 v[240:243], a[204:207], v[192:195], v[240:243]\n"
      "ds_read_b32 v85, v85\n"
      "v_lshl_add_u32 v104, v104, 2, s2\n"
      "v_mfma_f32_16x16x32_bf16 v[152:155], a[172:175], v[218:221], v[152:155]\n"
      "global_load_dwordx4 v[168:171], v216, s[48:49] offset:1024\n"
      "v_lshl_add_u32 v109, v109, 2, s2\n"
      "v_lshl_add_u32 v75, v75, 2, s2\n"
      "v_mfma_f32_16x16x32_bf16 v[152:155], a[208:211], v[192:195], v[152:155]\n"
      "ds_read_b32 v91, v91\n"
      "ds_read_b32 v92, v92\n"
      "v_lshl_add_u32 v111, v111, 2, s2\n"
      "v_fma_f32 v211, v184, v184, s38\n"
      "v_lshl_add_u32 v108, v108, 2, s2\n"
      "v_lshl_add_u32 v107, v107, 2, s2\n"
      "v_lshl_add_u32 v110, v110, 2, s2\n"
      "v_mfma_f32_16x16x32_bf16 v[224:227], a[176:179], v[218:221], v[224:227]\n"
      "ds_read_b32 v94, v94\n"
      "v_lshl_add_u32 v72, v72, 2, s2\n"
      "v_mfma_f32_16x16x32_bf16 v[224:227], a[212:215], v[192:195], v[224:227]\n"
      "ds_read_b32 v95, v95\n"
      "v_lshl_add_u32 v70, v70, 2, s2\n"
      "v_lshl_add_u32 v113, v113, 2, s2\n"
      "v_lshl_add_u32 v73, v73, 2, s2\n"
      "v_lshl_add_u32 v71, v71, 2, s2\n"
      "global_load_dwordx4 v[8:11], v212, s[6:7] offset:-3072\n"
      "ds_read_b128 v[216:219], v177 offset:10240\n"
      "ds_read_b32 v98, v98\n"
      "ds_read_b128 v[192:195], v214 offset:10240\n"
      "ds_read_b32 v100, v100\n"
      "v_lshl_add_u32 v112, v112, 2, s2\n"
      "v_fmac_f32 v211, v185, v185\n"
      "v_fmac_f32 v211, v186, v186\n"
      "v_fma_f32 v161, v196, v196, s38\n"
      "v_fmac_f32 v211, v187, v187\n"
      "v_fmac_f32 v161, v197, v197\n"
      "v_fmac_f32_e64 v161, v198, v198\n"
      "ds_read_b32 v99, v99\n"
      "ds_read_b32 v101, v101\n"
      "ds_read_b32 v97, v97\n"
      "ds_read_b32 v96, v96\n"
      "global_load_dwordx4 v[12:15], v212, s[6:7] offset:-2944\n"
      "ds_read_b32 v102, v102\n"
      "v_lshl_add_u32 v74, v74, 2, s2\n"
      "v_fmac_f32 v161, v199, v199\n"
      "v_fmac_f32 v161, v180, v180\n"
      "v_lshl_add_u32 v106, v106, 2, s2\n"
      "v_fmac_f32 v211, v140, v140\n"
      "v_fmac_f32 v161, v181, v181\n"
      "v_lshl_add_u32 v77, v77, 2, s2\n"
      "s_waitcnt lgkmcnt(6)\n"
      "v_mfma_f32_16x16x32_bf16 v[232:235], a[144:147], v[192:195], v[232:235]\n"
      "ds_read_b32 v103, v103\n"
      "v_fmac_f32 v161, v182, v182\n"
      "v_mfma_f32_16x16x32_bf16 v[232:235], a[180:183], v[216:219], v[232:235]\n"
      "ds_read_b32 v104, v104\n"
      "v_fmac_f32 v211, v141, v141\n"
      "v_fmac_f32 v211, v142, v142\n"
      "v_mfma_f32_16x16x32_bf16 v[144:147], a[148:151], v[192:195], v[144:147]\n"
      "ds_read_b32 v105, v105\n"
      "v_fmac_f32_e64 v211, v143, v143\n"
      "v_mfma_f32_16x16x32_bf16 v[144:147], a[184:187], v[216:219], v[144:147]\n"
      "global_load_dwordx4 v[16:19], v212, s[6:7] offset:-2816\n"
      "v_mfma_f32_16x16x32_bf16 v[148:151], a[152:155], v[192:195], v[148:151]\n"
      "ds_read_b32 v106, v106\n"
      "v_fmac_f32 v161, v183, v183\n"
      "v_mfma_f32_16x16x32_bf16 v[148:151], a[188:191], v[216:219], v[148:151]\n"
      "ds_read_b32 v107, v107\n"
      "v_fmac_f32 v161, v228, v228\n"
      "v_fmac_f32 v161, v229, v229\n"
      "v_mfma_f32_16x16x32_bf16 v[236:239], a[156:159], v[192:195], v[236:239]\n"
      "ds_read_b32 v108, v108\n"
      "v_fmac_f32 v211, v164, v164\n"
      "v_mfma_f32_16x16x32_bf16 v[236:239], a[192:195], v[216:219], v[236:239]\n"
      "ds_read_b32 v109, v109\n"
      "v_fmac_f32 v161, v230, v230\n"
      "v_fmac_f32 v161, v231, v231\n"
      "v_fmac_f32 v211, v165, v165\n"
      "v_fmac_f32 v211, v166, v166\n"
      "v_fmac_f32 v211, v167, v167\n"
      "ds_read_b32 v110, v110\n"
      "ds_read_b32 v111, v111\n"
      "v_permlane16_swap_b32 v161, v211\n"
      "v_add_f32 v161, v161, v211\n"
      "v_mov_b32 v211, v161\n"
      "v_mfma_f32_16x16x32_bf16 v[200:203], a[160:163], v[192:195], v[200:203]\n"
      "global_load_dwordx4 v[20:23], v212, s[6:7]\n"
      "v_permlane32_swap_b32 v161, v211\n"
      "v_add_f32 v161, v161, v211\n"
      "v_rsq_f32_e64 v161, v161\n"
      "v_mfma_f32_16x16x32_bf16 v[156:159], a[176:179], v[192:195], v[156:159]\n"
      "ds_read_b32 v112, v112\n"
      "v_mov_b32_e64 v211, v161\n"
      "v_mfma_f32_16x16x32_bf16 v[200:203], a[196:199], v[216:219], v[200:203]\n"
      "ds_read_b32 v74, v74\n"
      "ds_read_b32 v77, v77\n"
      "ds_read_b32 v72, v72\n"
      "ds_read_b32 v113, v113\n"
      "ds_read_b32 v70, v70\n"
      "v_permlane16_swap_b32 v161, v211\n"
      "v_mul_f32 v185, v211, v185\n"
      "v_mul_f32 v143, v211, v143\n"
      "v_mul_f32 v161, s36, v161\n"
      "v_mfma_f32_16x16x32_bf16 v[188:191], a[168:171], v[192:195], v[188:191]\n"
      "global_load_dwordx4 v[24:27], v212, s[6:7] offset:128\n"
      "v_mul_f32 v140, v211, v140\n"
      "v_mul_f32 v184, v211, v184\n"
      "v_mul_f32 v142, v211, v142\n"
      "v_mul_f32 v164, v211, v164\n"
      "v_mul_f32 v231, v161, v231\n"
      "v_mul_f32 v180, v161, v180\n"
      "v_mul_f32 v165, v211, v165\n"
      "v_mul_f32 v167, v211, v167\n"
      "v_mul_f32 v186, v211, v186\n"
      "v_mul_f32 v198, v161, v198\n"
      "v_mul_f32 v228, v161, v228\n"
      "v_mul_f32 v199, v161, v199\n"
      "v_mul_f32 v181, v161, v181\n"
      "v_mul_f32 v187, v211, v187\n"
      "v_cvt_pk_f16_f32 v184, v184, v185\n"
      "v_mul_f32 v197, v161, v197\n"
      "v_cvt_pk_f16_f32 v185, v186, v187\n"
      "v_mul_f32 v141, v211, v141\n"
      "v_mfma_f32_16x16x32_bf16 v[136:139], a[164:167], v[192:195], v[136:139]\n"
      "ds_read_b32 v71, v71\n"
      "v_cvt_pk_f16_f32 v187, v142, v143\n"
      "v_mfma_f32_16x16x32_bf16 v[136:139], a[200:203], v[216:219], v[136:139]\n"
      "ds_read_b32 v75, v75\n"
      "v_mul_f32_e64 v230, v161, v230\n"
      "v_mul_f32 v196, v161, v196\n"
      "v_mfma_f32_16x16x32_bf16 v[172:175], a[172:175], v[192:195], v[172:175]\n"
      "s_waitcnt lgkmcnt(2)\n"
      "v_cvt_pk_f16_f32 v196, v196, v197\n"
      "v_cvt_pk_f16_f32 v197, v198, v199\n"
      "v_mfma_f32_16x16x32_bf16 v[172:175], a[208:211], v[216:219], v[172:175]\n"
      "ds_read_b32 v73, v73\n"
      "v_mul_f32 v182, v161, v182\n"
      "v_mul_f32 v229, v161, v229\n"
      "v_mul_f32_e64 v183, v161, v183\n"
      "v_cvt_pk_f16_f32 v186, v140, v141\n"
      "s_waitcnt vmcnt(5)\n"
      "v_mov_b32 v134, v168\n"
      "v_mfma_f32_16x16x32_bf16 v[188:191], a[204:207], v[216:219], v[188:191]\n"
      "global_load_dwordx4 v[28:31], v212, s[6:7] offset:256\n"
      "v_mfma_f32_16x16x32_bf16 v[156:159], a[212:215], v[216:219], v[156:159]\n"
      "ds_read_b32 v161, v122\n"
      "ds_read_b128 v[216:219], v119 offset:36864\n"
      "s_mov_b32 exec_lo, 0xffff0000\n"
      "s_mov_b32 exec_hi, 0xffffffff\n"
      "v_mov_b32 v70, 0xf149f2ca\n"
      "v_mov_b32 v72, 0xf149f2ca\n"
      "v_mov_b32 v74, 0xf149f2ca\n"
      "s_mov_b64 exec, -1\n"
      "s_mov_b32 exec_lo, 0x0\n"
      "s_mov_b32 exec_hi, 0xfffe0000\n"
      "v_mov_b32 v77, 0xf149f2ca\n"
      "s_mov_b64 exec, -1\n"
      "v_mov_b32_e64 v210, -1\n"
      "v_cvt_pk_f16_f32 v34, v164, v165\n"
      "v_mul_f32 v166, v211, v166\n"
      "v_cvt_pk_f16_f32 v32, v228, v229\n"
      "s_mov_b32 s4, 0\n"
      "ds_write_b32 v129, v210\n"
      "ds_write_b32 v129, v210 offset:1024\n"
      "v_add_u32 v168, 36864, v171\n"
      "v_cvt_pk_f16_f32 v33, v230, v231\n"
      "v_mov_b32_e64 v135, v169\n"
      "v_cvt_pk_f16_f32 v198, v180, v181\n"
      "v_add_u32 v211, 36864, v170\n"
      "s_mov_b32 s5, 0\n"
      "ds_write_b32 v129, v210 offset:3072\n"
      "s_waitcnt lgkmcnt(3)\n"
      "v_or3_b32 v216, v216, v217, v218\n"
      "v_bitop3_b32 v161, v161, v219, s40 bitop3:0xa8\n"
      "v_and_b32_e64 v216, s40, v216\n"
      "v_cvt_pk_f16_f32 v199, v182, v183\n"
      "v_or3_b32 v216, v216, v161, s4\n"
      "ds_write_b32 v129, v216 offset:2048\n"
      "v_cvt_pk_f16_f32 v35, v166, v167\n"
      "s_waitcnt lgkmcnt(0)\n"
      "s_barrier\n"
      "v_fma_f32 v161, v236, v236, s38\n"
      "v_cvt_f16_f32 v210, s36\n"
      "v_fmac_f32 v161, v237, v237\n"
      "v_readfirstlane_b32 s7, v210\n"
      "v_fma_f32 v210, v232, v232, s38\n"
      "v_fmac_f32 v210, v233, v233\n"
      "v_cvt_pk_bf16_f32 v224, v224, v225\n"
      "v_fmac_f32 v210, v234, v234\n"
      "v_fmac_f32 v161, v238, v238\n"
      "v_fmac_f32 v161, v239, v239\n"
      "v_fmac_f32 v161, v200, v200\n"
      "v_fmac_f32 v210, v235, v235\n"
      "v_fmac_f32 v161, v201, v201\n"
      "v_fmac_f32 v210, v144, v144\n"
      "v_fmac_f32 v210, v145, v145\n"
      "v_fmac_f32 v161, v202, v202\n"
      "v_fmac_f32 v161, v203, v203\n"
      "v_fmac_f32 v210, v146, v146\n"
      "v_fmac_f32 v210, v147, v147\n"
      "s_mov_b32 s27, 0\n"
      "v_fmac_f32 v210, v148, v148\n"
      "v_fmac_f32 v210, v149, v149\n"
      "v_fmac_f32 v161, v136, v136\n"
      "v_fmac_f32 v210, v150, v150\n"
      "v_fmac_f32 v161, v137, v137\n"
      "v_fmac_f32 v161, v138, v138\n"
      "v_fmac_f32 v161, v139, v139\n"
      "v_fmac_f32_e64 v210, v151, v151\n"
      "v_cvt_pk_bf16_f32 v240, v240, v241\n"
      "s_add_u32 s28, s27, 1\n"
      "v_permlane16_swap_b32 v210, v161\n"
      "v_add_f32 v210, v210, v161\n"
      "v_mov_b32 v161, v210\n"
      "v_cvt_pk_bf16_f32 v152, v152, v153\n"
      "v_cvt_pk_bf16_f32 v225, v226, v227\n"
      "v_permlane32_swap_b32 v210, v161\n"
      "v_add_f32 v210, v210, v161\n"
      "v_rsq_f32 v210, v210\n"
      "v_cvt_pk_bf16_f32 v153, v154, v155\n"
      "v_mov_b32 v161, v210\n"
      "v_cvt_pk_bf16_f32 v241, v242, v243\n"
      "s_min_u32 m0, s28, s3\n"
      "v_permlane16_swap_b32 v210, v161\n"
      "v_mul_f32 v210, s36, v210\n"
      "v_mul_f32 v203, v161, v203\n"
      "v_mul_f32 v238, v161, v238\n"
      "v_mul_f32 v236, v161, v236\n"
      "v_mul_f32 v202, v161, v202\n"
      "v_mul_f32 v136, v161, v136\n"
      "s_movrels_b32 s9, s80\n"
      "ds_read_b128 v[216:219], v114\n"
      "v_mul_f32 v139, v161, v139\n"
      "v_mul_f32 v232, v210, v232\n"
      "v_mul_f32 v201, v161, v201\n"
      "v_mul_f32 v237, v161, v237\n"
      "v_mul_f32 v200, v161, v200\n"
      "v_mul_f32 v144, v210, v144\n"
      "s_lshr_b32 s8, s9, 8\n"
      "ds_read_b128 v[212:215], v115\n"
      "ds_read_b128 v[180:183], v128 offset:2048\n"
      "v_cvt_pk_f16_f32 v236, v236, v237\n"
      "s_and_b32 s9, s9, 0xff\n"
      "s_mul_i32 s10, s8, 73\n"
      "v_mul_f32 v137, v161, v137\n"
      "v_mul_f32 v233, v210, v233\n"
      "v_mul_f32 v235, v210, v235\n"
      "v_mul_f32 v138, v161, v138\n"
      "v_cvt_pk_f16_f32 v232, v232, v233\n"
      "v_cvt_pk_f16_f32 v41, v138, v139\n"
      "s_add_u32 s10, s10, s9\n"
      "ds_write_b128 v162, v[196:199]\n"
      "ds_write_b128 v162, v[32:35] offset:16\n"
      "ds_write_b128 v162, v[184:187] offset:32\n"
      "v_mul_f32 v239, v161, v239\n"
      "v_cvt_pk_f16_f32 v40, v136, v137\n"
      "v_cvt_pk_f16_f32 v237, v238, v239\n"
      "v_cvt_pk_f16_f32 v238, v200, v201\n"
      "s_waitcnt lgkmcnt(3)\n"
      "s_mul_i32 s28, s10, 4802\n"
      "v_mul_f32 v234, v210, v234\n"
      "v_mfma_f32_16x16x32_bf16 v[176:179], a[0:3], v[216:219], 0\n"
      "ds_read_b128 v[196:199], v114 offset:6144\n"
      "v_mul_f32 v145, v210, v145\n"
      "v_mfma_f32_16x16x32_bf16 v[176:179], a[36:39], v[212:215], v[176:179]\n"
      "ds_write_b64 v163, v[240:241]\n"
      "v_mfma_f32_16x16x32_bf16 v[136:139], a[4:7], v[216:219], 0\n"
      "ds_read_b128 v[184:187], v115 offset:6144\n"
      "s_add_u32 s56, s18, s28\n"
      "v_mfma_f32_16x16x32_bf16 v[136:139], a[40:43], v[212:215], v[136:139]\n"
      "ds_write_b64 v163, v[152:153] offset:32\n"
      "v_cvt_pk_f16_f32 v239, v202, v203\n"
      "v_mfma_f32_16x16x32_bf16 v[164:167], a[8:11], v[216:219], 0\n"
      "s_addc_u32 s57, s19, 0\n"
      "s_and_b32 s56, s56, -16\n"
      "s_sub_u32 s58, s56, 110592\n"
      "v_mfma_f32_16x16x32_bf16 v[164:167], a[44:47], v[212:215], v[164:167]\n"
      "ds_write_b64 v163, v[224:225] offset:64\n"
      "v_cvt_pk_f16_f32 v233, v234, v235\n"
      "v_mfma_f32_16x16x32_bf16 v[152:155], a[12:15], v[216:219], 0\n"
      "s_subb_u32 s59, s57, 0\n"
      "v_cvt_pk_f16_f32 v234, v144, v145\n"
      "v_mul_f32 v147, v210, v147\n"
      "v_mfma_f32_16x16x32_bf16 v[152:155], a[48:51], v[212:215], v[152:155]\n"
      "s_sub_u32 s56, s56, 73728\n"
      "v_mul_f32 v146, v210, v146\n"
      "v_cvt_pk_f16_f32 v235, v146, v147\n"
      "v_mfma_f32_16x16x32_bf16 v[144:147], a[16:19], v[216:219], 0\n"
      "s_subb_u32 s57, s57, 0\n"
      "v_mul_f32 v149, v210, v149\n"
      "v_mul_f32 v148, v210, v148\n"
      "v_mfma_f32_16x16x32_bf16 v[144:147], a[52:55], v[212:215], v[144:147]\n"
      "s_waitcnt lgkmcnt(2)\n"
      "v_cvt_pk_f16_f32 v38, v148, v149\n"
      "s_mul_i32 s28, s8, 1373568\n"
      "v_mfma_f32_16x16x32_bf16 v[240:243], a[20:23], v[216:219], 0\n"
      "s_mul_i32 s29, s9, 2688\n"
      "v_mul_f32 v151, v210, v151\n"
      "v_mul_f32 v150, v210, v150\n"
      "v_mfma_f32_16x16x32_bf16 v[240:243], a[56:59], v[212:215], v[240:243]\n"
      "ds_read_b128 v[224:227], v114 offset:12288\n"
      "v_cvt_pk_bf16_f32 v188, v188, v189\n"
      "v_mfma_f32_16x16x32_bf16 v[228:231], v[216:219], a[24:27], 0\n"
      "s_add_u32 s28, s28, s29\n"
      "v_cvt_pk_f16_f32 v39, v150, v151\n"
      "v_mfma_f32_16x16x32_bf16 v[228:231], v[212:215], a[60:63], v[228:231]\n"
      "ds_read_b128 v[140:143], v115 offset:12288\n"
      "v_cvt_pk_bf16_f32 v172, v172, v173\n"
      "v_mfma_f32_16x16x32_bf16 v[148:151], v[216:219], a[28:31], 0\n"
      "s_cmp_eq_u32 s8, 72\n"
      "v_cvt_pk_bf16_f32 v189, v190, v191\n"
      "s_cselect_b32 s8, 0, 100270464\n"
      "v_mfma_f32_16x16x32_bf16 v[148:151], v[212:215], a[64:67], v[148:151]\n"
      "v_cvt_pk_bf16_f32 v156, v156, v157\n"
      "v_cvt_pk_bf16_f32 v173, v174, v175\n"
      "v_mfma_f32_16x16x32_bf16 v[190:193], v[216:219], a[32:35], 0\n"
      "s_cmp_eq_u32 s9, 72\n"
      "s_cselect_b32 s9, 0, 196224\n"
      "s_add_u32 s10, s8, s9\n"
      "v_mfma_f32_16x16x32_bf16 v[190:193], v[212:215], a[68:71], v[190:193]\n"
      "s_add_u32 s28, s28, s10\n"
      "v_cvt_pk_bf16_f32 v157, v158, v159\n"
      "s_add_u32 s52, s16, s28\n"
      "v_mfma_f32_16x16x32_bf16 v[176:179], a[72:75], v[196:199], v[176:179]\n"
      "s_addc_u32 s53, s17, 0\n"
      "v_bitop3_b32 v180, v180, v181, s4 bitop3:0x7e\n"
      "s_cmp_lg_u32 s10, s11\n"
      "v_mfma_f32_16x16x32_bf16 v[176:179], a[108:111], v[184:187], v[176:179]\n"
      "s_cbranch_scc1 L_edge_6_%=\n"
      "L_eb_7_%=:\n"
      "v_mfma_f32_16x16x32_bf16 v[136:139], a[76:79], v[196:199], v[136:139]\n"
      "s_add_u32 m0, s76, 18432\n"
      "v_bitop3_b32 v182, v182, v183, s4 bitop3:0x7e\n"
      "v_or_b32_e64 v180, v180, v182\n"
      "v_mfma_f32_16x16x32_bf16 v[136:139], a[112:115], v[184:187], v[136:139]\n"
      "v_mfma_f32_16x16x32_bf16 v[144:147], a[88:91], v[196:199], v[144:147]\n"
      "global_load_lds_dwordx4 v116, s[52:53]\n"
      "v_mfma_f32_16x16x32_bf16 v[164:167], a[80:83], v[196:199], v[164:167]\n"
      "s_waitcnt lgkmcnt(0)\n"
      "v_cmp_ne_u32 vcc, 0, v180\n"
      "v_mfma_f32_16x16x32_bf16 v[164:167], a[116:119], v[184:187], v[164:167]\n"
      "s_mov_b32 exec_lo, 0x003f003f\n"
      "s_mov_b32 exec_hi, 0x003f003f\n"
      "ds_write_b128 v162, v[232:235] offset:18688\n"
      "ds_write_b128 v162, v[38:41] offset:18704\n"
      "ds_write_b128 v162, v[236:239] offset:18720\n"
      "ds_write_b64 v163, v[188:189] offset:18688\n"
      "ds_write_b64 v163, v[172:173] offset:18720\n"
      "ds_write_b64 v163, v[156:157] offset:18752\n"
      "s_mov_b64 exec, -1\n"
      "v_mfma_f32_16x16x32_bf16 v[152:155], a[84:87], v[196:199], v[152:155]\n"
      "s_mov_b64 s[48:49], vcc\n"
      "s_add_u32 m0, s76, 24448\n"
      "v_mfma_f32_16x16x32_bf16 v[152:155], a[120:123], v[184:187], v[152:155]\n"
      "global_load_lds_dwordx4 v116, s[52:53] offset:128\n"
      "v_mfma_f32_16x16x32_bf16 v[240:243], a[92:95], v[196:199], v[240:243]\n"
      "s_nop 0\n"
      "v_mfma_f32_16x16x32_bf16 v[240:243], a[128:131], v[184:187], v[240:243]\n"
      "s_cbranch_vccnz L_flg_3_%=\n"
      "L_fb_4_%=:\n"
      "v_mfma_f32_16x16x32_bf16 v[232:235], v[252:255], a[216:219], 0\n"
      "v_mfma_f32_16x16x32_bf16 v[232:235], v[252:255], a[220:223], v[232:235]\n"
      "v_mfma_f32_16x16x32_bf16 v[232:235], v[252:255], a[224:227], v[232:235]\n"
      "v_mfma_f32_16x16x32_bf16 v[228:231], v[196:199], a[96:99], v[228:231]\n"
      "s_add_u32 m0, s76, 30464\n"
      "v_mfma_f32_16x16x32_bf16 v[228:231], v[184:187], a[132:135], v[228:231]\n"
      "global_load_lds_dwordx4 v116, s[52:53] offset:256\n"
      "v_mfma_f32_16x16x32_bf16 v[148:151], v[196:199], a[100:103], v[148:151]\n"
      "v_mfma_f32_16x16x32_bf16 v[148:151], v[184:187], a[136:139], v[148:151]\n"
      "s_nop 1\n"
      "v_add_f32_dpp v232, v232, v232 quad_perm:[1,0,3,2] row_mask:0xf bank_mask:0xf\n"
      "v_mfma_f32_16x16x32_bf16 v[190:193], v[196:199], a[104:107], v[190:193]\n"
      "ds_read_b128 v[180:183], v115 offset:2048\n"
      "v_mfma_f32_16x16x32_bf16 v[190:193], v[184:187], a[140:143], v[190:193]\n"
      "v_add_f32_dpp v232, v232, v232 quad_perm:[2,3,0,1] row_mask:0xf bank_mask:0xf\n"
      "v_mfma_f32_16x16x32_bf16 v[176:179], a[144:147], v[224:227], v[176:179]\n"
      "s_nop 0\n"
      "v_add_f32_dpp v232, v232, v232 row_half_mirror row_mask:0xf bank_mask:0xf\n"
      "v_mfma_f32_16x16x32_bf16 v[176:179], a[180:183], v[140:143], v[176:179]\n"
      "v_mul_f32 v234, 0xbbaaaaab, v232\n"
      "v_mul_f32 v233, 0xbbaaaaab, v232\n"
      "v_mfma_f32_16x16x32_bf16 v[136:139], a[148:151], v[224:227], v[136:139]\n"
      "s_add_u32 m0, s77, 18432\n"
      "v_mul_f32 v235, 0xbbaaaaab, v232\n"
      "v_mfma_f32_16x16x32_bf16 v[136:139], a[184:187], v[140:143], v[136:139]\n"
      "ds_read_b128 v[216:219], v114 offset:2048\n"
      "v_mul_f32 v232, 0xbbaaaaab, v232\n"
      "v_mfma_f32_16x16x32_bf16 v[164:167], a[152:155], v[224:227], v[164:167]\n"
      "v_mfma_f32_16x16x32_bf16 v[164:167], a[188:191], v[140:143], v[164:167]\n"
      "v_mfma_f32_16x16x32_bf16 v[246:249], v[204:207], a[220:223], v[232:235]\n"
      "v_mfma_f32_16x16x32_bf16 v[144:147], a[124:127], v[184:187], v[144:147]\n"
      "v_mfma_f32_16x16x32_bf16 v[196:199], v[206:209], a[216:219], v[232:235]\n"
      "v_mfma_f32_16x16x32_bf16 v[212:215], v[204:207], a[216:219], v[232:235]\n"
      "global_load_lds_dwordx4 v117, s[52:53]\n"
      "v_mfma_f32_16x16x32_bf16 v[172:175], v[204:207], a[224:227], v[232:235]\n"
      "v_cvt_pk_f16_f32 v176, v176, v177\n"
      "v_pk_mul_f16 v210, v176, v176\n"
      "v_mfma_f32_16x16x32_bf16 v[156:159], v[206:209], a[220:223], v[232:235]\n"
      "s_waitcnt lgkmcnt(0)\n"
      "s_nop 0\n"
      "v_cvt_pk_bf16_f32 v196, v196, v197\n"
      "v_mfma_f32_16x16x32_bf16 v[184:187], v[206:209], a[224:227], v[232:235]\n"
      "s_nop 0\n"
      "v_cvt_pk_bf16_f32 v197, v198, v199\n"
      "v_cvt_pk_bf16_f32 v199, v214, v215\n"
      "v_mfma_f32_16x16x32_bf16 v[236:239], v[252:255], a[228:231], 0\n"
      "s_nop 0\n"
      "v_cvt_pk_bf16_f32 v156, v156, v157\n"
      "v_cvt_pk_bf16_f32 v198, v212, v213\n"
      "v_mfma_f32_16x16x32_bf16 v[236:239], v[252:255], a[232:235], v[236:239]\n"
      "ds_write_b128 v160, v[196:199]\n"
      "v_mfma_f32_16x16x32_bf16 v[152:155], a[156:159], v[224:227], v[152:155]\n"
      "v_cvt_pk_bf16_f32 v157, v158, v159\n"
      "v_cvt_pk_bf16_f32 v184, v184, v185\n"
      "v_mfma_f32_16x16x32_bf16 v[152:155], a[192:195], v[140:143], v[152:155]\n"
      "v_cvt_pk_bf16_f32 v185, v186, v187\n"
      "v_mfma_f32_16x16x32_bf16 v[236:239], v[252:255], a[236:239], v[236:239]\n"
      "v_cvt_pk_bf16_f32 v159, v248, v249\n"
      "v_cvt_pk_bf16_f32 v187, v174, v175\n"
      "v_mfma_f32_16x16x32_bf16 v[144:147], a[160:163], v[224:227], v[144:147]\n"
      "v_cvt_pk_bf16_f32 v158, v246, v247\n"
      "v_mfma_f32_16x16x32_bf16 v[144:147], a[196:199], v[140:143], v[144:147]\n"
      "ds_write_b128 v160, v[156:159] offset:128\n"
      "v_mfma_f32_16x16x32_bf16 v[240:243], a[164:167], v[224:227], v[240:243]\n"
      "v_cvt_pk_bf16_f32 v186, v172, v173\n"
      "v_add_f32_dpp v236, v236, v236 quad_perm:[1,0,3,2] row_mask:0xf bank_mask:0xf\n"
      "v_mfma_f32_16x16x32_bf16 v[240:243], a[200:203], v[140:143], v[240:243]\n"
      "ds_write_b128 v160, v[184:187] offset:256\n"
      "v_mfma_f32_16x16x32_bf16 v[228:231], v[224:227], a[168:171], v[228:231]\n"
      "v_add_f32_dpp v236, v236, v236 quad_perm:[2,3,0,1] row_mask:0xf bank_mask:0xf\n"
      "v_cvt_pk_f16_f32 v152, v152, v153\n"
      "v_mfma_f32_16x16x32_bf16 v[228:231], v[140:143], a[204:207], v[228:231]\n"
      "v_add_f32_dpp v236, v236, v236 row_half_mirror row_mask:0xf bank_mask:0xf\n"
      "v_mul_f32 v237, 0xbbaaaaab, v236\n"
      "v_mfma_f32_16x16x32_bf16 v[148:151], v[224:227], a[172:175], v[148:151]\n"
      "v_mul_f32 v238, 0xbbaaaaab, v236\n"
      "v_mul_f32 v239, 0xbbaaaaab, v236\n"
      "v_mfma_f32_16x16x32_bf16 v[148:151], v[140:143], a[208:211], v[148:151]\n"
      "v_pk_mul_f16 v169, v152, v152\n"
      "v_mul_f32 v236, 0xbbaaaaab, v236\n"
      "v_mfma_f32_16x16x32_bf16 v[190:193], v[224:227], a[176:179], v[190:193]\n"
      "v_cvt_pk_f16_f32 v177, v178, v179\n"
      "v_pk_fma_f16 v210, v177, v177, v210\n"
      "v_mfma_f32_16x16x32_bf16 v[190:193], v[140:143], a[212:215], v[190:193]\n"
      "v_cvt_pk_f16_f32 v153, v154, v155\n"
      "v_cvt_pk_f16_f32 v178, v136, v137\n"
      "v_mfma_f32_16x16x32_bf16 v[196:199], v[204:207], a[228:231], v[236:239]\n"
      "v_pk_fma_f16 v210, v178, v178, v210\n"
      "v_pk_fma_f16 v169, v153, v153, v169\n"
      "v_mfma_f32_16x16x32_bf16 v[232:235], v[206:209], a[228:231], v[236:239]\n"
      "v_cvt_pk_f16_f32 v154, v144, v145\n"
      "v_pk_fma_f16 v169, v154, v154, v169\n"
      "v_mfma_f32_16x16x32_bf16 v[212:215], v[206:209], a[232:235], v[236:239]\n"
      "v_cvt_pk_f16_f32 v179, v138, v139\n"
      "v_cvt_pk_f16_f32 v155, v146, v147\n"
      "v_mfma_f32_16x16x32_bf16 v[184:187], v[204:207], a[232:235], v[236:239]\n"
      "v_pk_fma_f16 v169, v155, v155, v169\n"
      "v_pk_fma_f16 v210, v179, v179, v210\n"
      "v_mfma_f32_16x16x32_bf16 v[144:147], v[206:209], a[236:239], v[236:239]\n"
      "v_cvt_pk_bf16_f32 v232, v232, v233\n"
      "v_cvt_pk_bf16_f32 v233, v234, v235\n"
      "v_mfma_f32_16x16x32_bf16 v[140:143], v[204:207], a[236:239], v[236:239]\n"
      "v_cvt_pk_bf16_f32 v234, v196, v197\n"
      "v_cvt_pk_bf16_f32 v235, v198, v199\n"
      "v_mfma_f32_16x16x32_bf16 v[136:139], a[0:3], v[216:219], 0\n"
      "ds_read_b128 v[224:227], v114 offset:8192\n"
      "v_cvt_pk_bf16_f32 v212, v212, v213\n"
      "v_mfma_f32_16x16x32_bf16 v[136:139], a[36:39], v[180:183], v[136:139]\n"
      "ds_write_b128 v160, v[232:235] offset:3072\n"
      "v_mfma_f32_16x16x32_bf16 v[196:199], a[4:7], v[216:219], 0\n"
      "ds_read_b128 v[232:235], v115 offset:8192\n"
      "v_cvt_pk_bf16_f32 v213, v214, v215\n"
      "v_mfma_f32_16x16x32_bf16 v[196:199], a[40:43], v[180:183], v[196:199]\n"
      "v_cvt_pk_bf16_f32 v215, v186, v187\n"
      "v_cvt_pk_bf16_f32 v214, v184, v185\n"
      "v_mfma_f32_16x16x32_bf16 v[184:187], a[8:11], v[216:219], 0\n"
      "v_cvt_pk_bf16_f32 v144, v144, v145\n"
      "v_cvt_pk_bf16_f32 v145, v146, v147\n"
      "v_mfma_f32_16x16x32_bf16 v[184:187], a[44:47], v[180:183], v[184:187]\n"
      "ds_write_b128 v160, v[212:215] offset:3200\n"
      "v_mfma_f32_16x16x32_bf16 v[212:215], a[12:15], v[216:219], 0\n"
      "v_cvt_pk_bf16_f32 v146, v140, v141\n"
      "v_cvt_pk_bf16_f32 v147, v142, v143\n"
      "v_mfma_f32_16x16x32_bf16 v[212:215], a[48:51], v[180:183], v[212:215]\n"
      "ds_write_b128 v160, v[144:147] offset:3328\n"
      "v_mfma_f32_16x16x32_bf16 v[140:143], a[16:19], v[216:219], 0\n"
      "v_cvt_pk_f16_f32 v32, v164, v165\n"
      "v_pk_fma_f16 v210, v32, v32, v210\n"
      "v_mfma_f32_16x16x32_bf16 v[140:143], a[52:55], v[180:183], v[140:143]\n"
      "v_cvt_pk_f16_f32 v34, v240, v241\n"
      "v_pk_fma_f16 v169, v34, v34, v169\n"
      "v_mfma_f32_16x16x32_bf16 v[236:239], a[20:23], v[216:219], 0\n"
      "v_cvt_pk_f16_f32 v33, v166, v167\n"
      "v_pk_fma_f16 v210, v33, v33, v210\n"
      "v_mfma_f32_16x16x32_bf16 v[236:239], a[56:59], v[180:183], v[236:239]\n"
      "v_cvt_pk_f16_f32 v35, v242, v243\n"
      "v_pk_fma_f16 v169, v35, v35, v169\n"
      "v_mfma_f32_16x16x32_bf16 v[144:147], v[216:219], a[24:27], 0\n"
      "s_waitcnt lgkmcnt(2)\n"
      "v_permlane16_swap_b32 v210, v169\n"
      "v_mfma_f32_16x16x32_bf16 v[144:147], v[180:183], a[60:63], v[144:147]\n"
      "v_pk_add_f16 v210, v210, v169\n"
      "v_mfma_f32_16x16x32_bf16 v[164:167], v[216:219], a[28:31], 0\n"
      "ds_read_b128 v[240:243], v114 offset:14336\n"
      "v_mov_b32 v169, v210\n"
      "v_mfma_f32_16x16x32_bf16 v[164:167], v[180:183], a[64:67], v[164:167]\n"
      "s_nop 0\n"
      "v_permlane32_swap_b32_e64 v210, v169\n"
      "v_mfma_f32_16x16x32_bf16 v[172:175], v[216:219], a[32:35], 0\n"
      "v_pk_add_f16 v210, v210, v169\n"
      "v_add_f16_sdwa v169, v210, v210 dst_sel:DWORD dst_unused:UNUSED_PAD src0_sel:WORD_0 src1_sel:WORD_1\n"
      "v_mfma_f32_16x16x32_bf16 v[172:175], v[180:183], a[68:71], v[172:175]\n"
      "s_nop 0\n"
      "v_rsq_f16 v210, v169\n"
      "v_mfma_f32_16x16x32_bf16 v[136:139], a[72:75], v[224:227], v[136:139]\n"
      "ds_read_b128 v[248:251], v115 offset:14336\n"
      "v_cmp_nle_f16 vcc, 0x1c00, v169\n"
      "v_mfma_f32_16x16x32_bf16 v[136:139], a[108:111], v[232:235], v[136:139]\n"
      "s_mov_b64 s[34:35], vcc\n"
      "v_cmp_nge_f16 vcc, 0x7b00, v169\n"
      "v_mov_b32_e64 v169, v210\n"
      "v_mfma_f32_16x16x32_bf16 v[216:219], v[252:255], a[240:243], 0\n"
      "s_or_b64 s[34:35], s[34:35], vcc\n"
      "v_permlane16_swap_b32_e64 v210, v169\n"
      "v_mfma_f32_16x16x32_bf16 v[196:199], a[76:79], v[224:227], v[196:199]\n"
      "s_add_u32 m0, s78, 18432\n"
      "v_mfma_f32_16x16x32_bf16 v[196:199], a[112:115], v[232:235], v[196:199]\n"
      "v_pk_mul_f16 v35, v35, v169 op_sel_hi:[1,0]\n"
      "v_mfma_f32_16x16x32_bf16 v[216:219], v[252:255], a[244:247], v[216:219]\n"
      "s_nop 0\n"
      "v_mul_f16 v210, s7, v210\n"
      "v_pk_mul_f16 v34, v34, v169 op_sel_hi:[1,0]\n"
      "v_mfma_f32_16x16x32_bf16 v[216:219], v[252:255], a[248:251], v[216:219]\n"
      "s_mov_b32 exec_hi, 0\n"
      "global_load_lds_dwordx4 v118, s[52:53]\n"
      "s_mov_b32 exec_hi, -1\n"
      "v_mfma_f32_16x16x32_bf16 v[184:187], a[80:83], v[224:227], v[184:187]\n"
      "v_mfma_f32_16x16x32_bf16 v[184:187], a[116:119], v[232:235], v[184:187]\n"
      "v_pk_mul_f16 v153, v153, v169 op_sel_hi:[1,0]\n"
      "v_cvt_pk_bf16_f32 v148, v148, v149\n"
      "v_mfma_f32_16x16x32_bf16 v[212:215], a[84:87], v[224:227], v[212:215]\n"
      "v_add_f32_dpp v216, v216, v216 quad_perm:[1,0,3,2] row_mask:0xf bank_mask:0xf\n"
      "v_mfma_f32_16x16x32_bf16 v[212:215], a[120:123], v[232:235], v[212:215]\n"
      "v_pk_mul_f16 v152, v152, v169 op_sel_hi:[1,0]\n"
      "v_mfma_f32_16x16x32_bf16 v[140:143], a[88:91], v[224:227], v[140:143]\n"
      "s_nop 0\n"
      "v_pk_mul_f16 v176, v176, v210 op_sel_hi:[1,0]\n"
      "v_add_f32_dpp v216, v216, v216 quad_perm:[2,3,0,1] row_mask:0xf bank_mask:0xf\n"
      "v_mfma_f32_16x16x32_bf16 v[140:143], a[124:127], v[232:235], v[140:143]\n"
      "s_nop 0\n"
      "v_add_f32_dpp v216, v216, v216 row_half_mirror row_mask:0xf bank_mask:0xf\n"
      "v_mul_f32 v219, 0xbbaaaaab, v216\n"
      "v_mfma_f32_16x16x32_bf16 v[236:239], a[92:95], v[224:227], v[236:239]\n"
      "v_mul_f32 v217, 0xbbaaaaab, v216\n"
      "v_mfma_f32_16x16x32_bf16 v[236:239], a[128:131], v[232:235], v[236:239]\n"
      "v_mul_f32 v218, 0xbbaaaaab, v216\n"
      "v_mfma_f32_16x16x32_bf16 v[144:147], v[224:227], a[96:99], v[144:147]\n"
      "s_add_u32 m0, s76, 115456\n"
      "v_mul_f32 v216, 0xbbaaaaab, v216\n"
      "v_mfma_f32_16x16x32_bf16 v[144:147], v[232:235], a[132:135], v[144:147]\n"
      "global_load_lds_dwordx4 v119, s[56:57]\n"
      "v_mfma_f32_16x16x32_bf16 v[156:159], v[206:209], a[240:243], v[216:219]\n"
      "v_mfma_f32_16x16x32_bf16 v[180:183], v[204:207], a[240:243], v[216:219]\n"
      "s_add_u32 m0, s79, 4864\n"
      "v_mfma_f32_16x16x32_bf16 v[164:167], v[224:227], a[100:103], v[164:167]\n"
      "v_pk_mul_f16 v32, v32, v210 op_sel_hi:[1,0]\n"
      "v_pk_mul_f16 v33, v33, v210 op_sel_hi:[1,0]\n"
      "v_mfma_f32_16x16x32_bf16 v[164:167], v[232:235], a[136:139], v[164:167]\n"
      "v_pk_mul_f16 v178, v178, v210 op_sel_hi:[1,0]\n"
      "v_pk_mul_f16 v155, v155, v169 op_sel_hi:[1,0]\n"
      "v_mfma_f32_16x16x32_bf16 v[172:175], v[224:227], a[104:107], v[172:175]\n"
      "v_cvt_pk_bf16_f32 v156, v156, v157\n"
      "v_cvt_pk_bf16_f32 v157, v158, v159\n"
      "v_mfma_f32_16x16x32_bf16 v[172:175], v[232:235], a[140:143], v[172:175]\n"
      "s_nop 0\n"
      "v_mfma_f32_16x16x32_bf16 v[224:227], v[206:209], a[244:247], v[216:219]\n"
      "s_nop 0\n"
      "v_pk_mul_f16 v179, v179, v210 op_sel_hi:[1,0]\n"
      "v_cvt_pk_bf16_f32 v159, v182, v183\n"
      "v_mfma_f32_16x16x32_bf16 v[232:235], v[204:207], a[244:247], v[216:219]\n"
      "s_waitcnt lgkmcnt(0)\n"
      "v_cvt_pk_bf16_f32 v158, v180, v181\n"
      "v_pk_mul_f16 v154, v154, v169 op_sel_hi:[1,0]\n"
      "v_mfma_f32_16x16x32_bf16 v[244:247], v[206:209], a[248:251], v[216:219]\n"
      "global_load_lds_dword v122, s[58:59]\n"
      "v_mfma_f32_16x16x32_bf16 v[136:139], a[144:147], v[240:243], v[136:139]\n"
      "s_nop 0\n"
      "v_cvt_pk_bf16_f32 v224, v224, v225\n"
      "v_pk_mul_f16 v177, v177, v210 op_sel_hi:[1,0]\n"
      "v_mfma_f32_16x16x32_bf16 v[136:139], a[180:183], v[248:251], v[136:139]\n"
      "ds_write_b128 v160, v[156:159] offset:6144\n"
      "v_mfma_f32_16x16x32_bf16 v[180:183], v[204:207], a[248:251], v[216:219]\n"
      "ds_read_b128 v[156:159], v114 offset:4096\n"
      "v_cvt_pk_bf16_f32 v225, v226, v227\n"
      "v_mfma_f32_16x16x32_bf16 v[196:199], a[148:151], v[240:243], v[196:199]\n"
      "v_cvt_pk_bf16_f32 v227, v234, v235\n"
      "v_cvt_pk_bf16_f32 v226, v232, v233\n"
      "v_mfma_f32_16x16x32_bf16 v[196:199], a[184:187], v[248:251], v[196:199]\n"
      "ds_write_b128 v160, v[224:227] offset:6272\n"
      "v_mfma_f32_16x16x32_bf16 v[232:235], v[252:255], a[252:255], 0\n"
      "v_cvt_pk_bf16_f32 v228, v228, v229\n"
      "v_cvt_pk_bf16_f32 v244, v244, v245\n"
      "v_mfma_f32_16x16x32_bf16 v[232:235], v[252:255], v[0:3], v[232:235]\n"
      "v_cvt_pk_bf16_f32 v245, v246, v247\n"
      "v_cvt_pk_bf16_f32 v246, v180, v181\n"
      "v_mfma_f32_16x16x32_bf16 v[232:235], v[252:255], v[4:7], v[232:235]\n"
      "v_cvt_pk_bf16_f32 v247, v182, v183\n"
      "v_cvt_pk_bf16_f32 v229, v230, v231\n"
      "v_mfma_f32_16x16x32_bf16 v[184:187], a[152:155], v[240:243], v[184:187]\n"
      "ds_read_b128 v[216:219], v115 offset:4096\n"
      "v_cvt_pk_bf16_f32 v149, v150, v151\n"
      "v_mfma_f32_16x16x32_bf16 v[184:187], a[188:191], v[248:251], v[184:187]\n"
      "ds_write_b128 v160, v[244:247] offset:6400\n"
      "v_mfma_f32_16x16x32_bf16 v[212:215], a[156:159], v[240:243], v[212:215]\n"
      "v_add_f32_dpp v232, v232, v232 quad_perm:[1,0,3,2] row_mask:0xf bank_mask:0xf\n"
      "v_mfma_f32_16x16x32_bf16 v[212:215], a[192:195], v[248:251], v[212:215]\n"
      "s_nop 0\n"
      "v_add_f32_dpp v232, v232, v232 quad_perm:[2,3,0,1] row_mask:0xf bank_mask:0xf\n"
      "v_mfma_f32_16x16x32_bf16 v[140:143], a[160:163], v[240:243], v[140:143]\n"
      "s_nop 0\n"
      "v_add_f32_dpp v232, v232, v232 row_half_mirror row_mask:0xf bank_mask:0xf\n"
      "v_mul_f32 v233, 0xbbaaaaab, v232\n"
      "v_mfma_f32_16x16x32_bf16 v[140:143], a[196:199], v[248:251], v[140:143]\n"
      "v_cvt_pk_bf16_f32 v190, v190, v191\n"
      "v_mul_f32 v235, 0xbbaaaaab, v232\n"
      "v_mfma_f32_16x16x32_bf16 v[236:239], a[164:167], v[240:243], v[236:239]\n"
      "v_mul_f32 v234, 0xbbaaaaab, v232\n"
      "v_mul_f32 v232, 0xbbaaaaab, v232\n"
      "v_mfma_f32_16x16x32_bf16 v[236:239], a[200:203], v[248:251], v[236:239]\n"
      "s_nop 0\n"
      "v_cvt_pk_f16_f32 v136, v136, v137\n"
      "v_cvt_pk_bf16_f32 v191, v192, v193\n"
      "v_mfma_f32_16x16x32_bf16 v[224:227], v[206:209], a[252:255], v[232:235]\n"
      "s_waitcnt lgkmcnt(1)\n"
      "v_cvt_pk_f16_f32 v212, v212, v213\n"
      "v_pk_mul_f16 v169, v136, v136\n"
      "v_mfma_f32_16x16x32_bf16 v[180:183], v[204:207], a[252:255], v[232:235]\n"
      "v_pk_mul_f16 v210, v212, v212\n"
      "v_cvt_pk_f16_f32 v137, v138, v139\n"
      "v_mfma_f32_16x16x32_bf16 v[144:147], v[240:243], a[168:171], v[144:147]\n"
      "v_cvt_pk_f16_f32 v213, v214, v215\n"
      "v_pk_fma_f16 v169, v137, v137, v169\n"
      "v_mfma_f32_16x16x32_bf16 v[144:147], v[248:251], a[204:207], v[144:147]\n"
      "v_cvt_pk_bf16_f32 v224, v224, v225\n"
      "v_cvt_pk_bf16_f32 v225, v226, v227\n"
      "v_mfma_f32_16x16x32_bf16 v[164:167], v[240:243], a[172:175], v[164:167]\n"
      "v_cvt_pk_bf16_f32 v227, v182, v183\n"
      "v_cvt_pk_bf16_f32 v226, v180, v181\n"
      "v_mfma_f32_16x16x32_bf16 v[164:167], v[248:251], a[208:211], v[164:167]\n"
      "ds_write_b128 v160, v[224:227] offset:9216\n"
      "v_mfma_f32_16x16x32_bf16 v[224:227], v[206:209], v[0:3], v[232:235]\n"
      "v_cvt_pk_bf16_f32 v231, v146, v147\n"
      "v_cvt_pk_bf16_f32 v230, v144, v145\n"
      "v_mfma_f32_16x16x32_bf16 v[180:183], v[204:207], v[0:3], v[232:235]\n"
      "v_pk_fma_f16 v210, v213, v213, v210\n"
      "v_cvt_pk_f16_f32 v138, v196, v197\n"
      "v_mfma_f32_16x16x32_bf16 v[194:197], v[206:209], v[4:7], v[232:235]\n"
      "v_cvt_pk_bf16_f32 v151, v166, v167\n"
      "v_cvt_pk_bf16_f32 v150, v164, v165\n"
      "v_mfma_f32_16x16x32_bf16 v[144:147], v[204:207], v[4:7], v[232:235]\n"
      "v_cvt_pk_bf16_f32 v224, v224, v225\n"
      "v_cvt_pk_bf16_f32 v225, v226, v227\n"
      "v_mfma_f32_16x16x32_bf16 v[172:175], v[240:243], a[176:179], v[172:175]\n"
      "ds_read_b128 v[164:167], v114 offset:10240\n"
      "v_cvt_pk_bf16_f32 v226, v180, v181\n"
      "v_mfma_f32_16x16x32_bf16 v[172:175], v[248:251], a[212:215], v[172:175]\n"
      "v_cvt_pk_bf16_f32 v227, v182, v183\n"
      "v_cvt_pk_bf16_f32 v194, v194, v195\n"
      "v_mfma_f32_16x16x32_bf16 v[180:183], a[0:3], v[156:159], 0\n"
      "ds_read_b128 v[240:243], v115 offset:10240\n"
      "v_cvt_pk_bf16_f32 v195, v196, v197\n"
      "v_mfma_f32_16x16x32_bf16 v[180:183], a[36:39], v[216:219], v[180:183]\n"
      "ds_write_b128 v160, v[224:227] offset:9344\n"
      "v_mfma_f32_16x16x32_bf16 v[224:227], a[4:7], v[156:159], 0\n"
      "v_cvt_pk_bf16_f32 v196, v144, v145\n"
      "v_cvt_pk_bf16_f32 v197, v146, v147\n"
      "v_mfma_f32_16x16x32_bf16 v[224:227], a[40:43], v[216:219], v[224:227]\n"
      "ds_write_b128 v160, v[194:197] offset:9472\n"
      "v_mfma_f32_16x16x32_bf16 v[232:235], a[8:11], v[156:159], 0\n"
      "v_cvt_pk_f16_f32 v41, v238, v239\n"
      "v_cvt_pk_f16_f32 v214, v140, v141\n"
      "v_mfma_f32_16x16x32_bf16 v[232:235], a[44:47], v[216:219], v[232:235]\n"
      "v_pk_fma_f16 v210, v214, v214, v210\n"
      "v_cvt_pk_f16_f32 v139, v198, v199\n"
      "v_mfma_f32_16x16x32_bf16 v[144:147], a[12:15], v[156:159], 0\n"
      "v_cvt_pk_bf16_f32 v193, v174, v175\n"
      "v_cvt_pk_f16_f32 v40, v236, v237\n"
      "v_mfma_f32_16x16x32_bf16 v[144:147], a[48:51], v[216:219], v[144:147]\n"
      "v_pk_fma_f16 v169, v138, v138, v169\n"
      "v_cvt_pk_f16_f32 v215, v142, v143\n"
      "v_mfma_f32_16x16x32_bf16 v[196:199], a[16:19], v[156:159], 0\n"
      "v_cvt_pk_f16_f32 v38, v184, v185\n"
      "v_pk_fma_f16 v169, v139, v139, v169\n"
      "v_mfma_f32_16x16x32_bf16 v[196:199], a[52:55], v[216:219], v[196:199]\n"
      "v_cvt_pk_f16_f32 v39, v186, v187\n"
      "v_pk_fma_f16 v210, v215, v215, v210\n"
      "v_mfma_f32_16x16x32_bf16 v[140:143], a[20:23], v[156:159], 0\n"
      "v_pk_fma_f16 v210, v40, v40, v210\n"
      "v_pk_fma_f16 v169, v38, v38, v169\n"
      "v_mfma_f32_16x16x32_bf16 v[140:143], a[56:59], v[216:219], v[140:143]\n"
      "v_cvt_pk_bf16_f32 v192, v172, v173\n"
      "s_waitcnt lgkmcnt(1)\n"
      "v_mfma_f32_16x16x32_bf16 v[184:187], v[156:159], a[24:27], 0\n"
      "v_pk_fma_f16 v169, v39, v39, v169\n"
      "v_pk_fma_f16 v210, v41, v41, v210\n"
      "v_mfma_f32_16x16x32_bf16 v[184:187], v[216:219], a[60:63], v[184:187]\n"
      "s_nop 0\n"
      "v_permlane16_swap_b32 v169, v210\n"
      "v_mfma_f32_16x16x32_bf16 v[236:239], v[156:159], a[28:31], 0\n"
      "v_pk_add_f16 v169, v169, v210\n"
      "v_mov_b32 v210, v169\n"
      "v_mfma_f32_16x16x32_bf16 v[236:239], v[216:219], a[64:67], v[236:239]\n"
      "s_nop 0\n"
      "v_permlane32_swap_b32 v169, v210\n"
      "v_mfma_f32_16x16x32_bf16 v[172:175], v[156:159], a[32:35], 0\n"
      "v_pk_add_f16 v169, v169, v210\n"
      "v_add_f16_sdwa v210, v169, v169 dst_sel:DWORD dst_unused:UNUSED_PAD src0_sel:WORD_0 src1_sel:WORD_1\n"
      "v_mfma_f32_16x16x32_bf16 v[172:175], v[216:219], a[68:71], v[172:175]\n"
      "v_rsq_f16 v169, v210\n"
      "v_mfma_f32_16x16x32_bf16 v[180:183], a[72:75], v[164:167], v[180:183]\n"
      "s_nop 0\n"
      "v_cmp_nle_f16 vcc, 0x1c00, v210\n"
      "v_mfma_f32_16x16x32_bf16 v[180:183], a[108:111], v[240:243], v[180:183]\n"
      "s_or_b64 s[34:35], s[34:35], vcc\n"
      "v_cmp_nge_f16 vcc, 0x7b00, v210\n"
      "v_mov_b32 v210, v169\n"
      "v_mfma_f32_16x16x32_bf16 v[224:227], a[76:79], v[164:167], v[224:227]\n"
      "v_mfma_f32_16x16x32_bf16 v[224:227], a[112:115], v[240:243], v[224:227]\n"
      "v_permlane16_swap_b32_e64 v169, v210\n"
      "s_waitcnt vmcnt(12)\n"
      "v_mfma_f32_16x16x32_bf16 v[216:219], v[252:255], v[8:11], 0\n"
      "s_waitcnt vmcnt(11)\n"
      "v_mfma_f32_16x16x32_bf16 v[216:219], v[252:255], v[12:15], v[216:219]\n"
      "v_pk_mul_f16 v214, v214, v210 op_sel_hi:[1,0]\n"
      "v_mul_f16 v169, s7, v169\n"
      "s_waitcnt vmcnt(10)\n"
      "v_mfma_f32_16x16x32_bf16 v[216:219], v[252:255], v[16:19], v[216:219]\n"
      "v_pk_mul_f16 v137, v137, v169 op_sel_hi:[1,0]\n"
      "v_pk_mul_f16 v215, v215, v210 op_sel_hi:[1,0]\n"
      "v_mfma_f32_16x16x32_bf16 v[232:235], a[80:83], v[164:167], v[232:235]\n"
      "v_pk_mul_f16 v213, v213, v210 op_sel_hi:[1,0]\n"
      "v_mfma_f32_16x16x32_bf16 v[232:235], a[116:119], v[240:243], v[232:235]\n"
      "s_nop 0\n"
      "v_mfma_f32_16x16x32_bf16 v[144:147], a[84:87], v[164:167], v[144:147]\n"
      "s_nop 1\n"
      "v_add_f32_dpp v216, v216, v216 quad_perm:[1,0,3,2] row_mask:0xf bank_mask:0xf\n"
      "v_mfma_f32_16x16x32_bf16 v[144:147], a[120:123], v[240:243], v[144:147]\n"
      "s_or_b64 s[34:35], s[34:35], vcc\n"
      "v_add_f32_dpp v216, v216, v216 quad_perm:[2,3,0,1] row_mask:0xf bank_mask:0xf\n"
      "v_mfma_f32_16x16x32_bf16 v[196:199], a[88:91], v[164:167], v[196:199]\n"
      "s_nop 0\n"
      "v_add_f32_dpp v216, v216, v216 row_half_mirror row_mask:0xf bank_mask:0xf\n"
      "v_mul_f32 v217, 0xbbaaaaab, v216\n"
      "v_mfma_f32_16x16x32_bf16 v[196:199], a[124:127], v[240:243], v[196:199]\n"
      "v_pk_mul_f16 v40, v40, v210 op_sel_hi:[1,0]\n"
      "v_mul_f32 v219, 0xbbaaaaab, v216\n"
      "v_mfma_f32_16x16x32_bf16 v[140:143], a[92:95], v[164:167], v[140:143]\n"
      "v_mul_f32 v218, 0xbbaaaaab, v216\n"
      "v_mul_f32 v216, 0xbbaaaaab, v216\n"
      "v_mfma_f32_16x16x32_bf16 v[140:143], a[128:131], v[240:243], v[140:143]\n"
      "v_pk_mul_f16 v212, v212, v210 op_sel_hi:[1,0]\n"
      "v_pk_mul_f16 v138, v138, v169 op_sel_hi:[1,0]\n"
      "v_mfma_f32_16x16x32_bf16 v[156:159], v[204:207], v[8:11], v[216:219]\n"
      "v_pk_mul_f16 v38, v38, v169 op_sel_hi:[1,0]\n"
      "v_mfma_f32_16x16x32_bf16 v[248:251], v[206:209], v[8:11], v[216:219]\n"
      "v_mfma_f32_16x16x32_bf16 v[184:187], v[164:167], a[96:99], v[184:187]\n"
      "v_mfma_f32_16x16x32_bf16 v[184:187], v[240:243], a[132:135], v[184:187]\n"
      "ds_read_b128 v[244:247], v114 offset:16384\n"
      "v_pk_mul_f16 v136, v136, v169 op_sel_hi:[1,0]\n"
      "v_mfma_f32_16x16x32_bf16 v[236:239], v[164:167], a[100:103], v[236:239]\n"
      "v_pk_mul_f16 v41, v41, v210 op_sel_hi:[1,0]\n"
      "v_mfma_f32_16x16x32_bf16 v[236:239], v[240:243], a[136:139], v[236:239]\n"
      "s_nop 0\n"
      "v_cvt_pk_bf16_f32 v248, v248, v249\n"
      "v_mfma_f32_16x16x32_bf16 v[172:175], v[164:167], a[104:107], v[172:175]\n"
      "s_nop 0\n"
      "v_cvt_pk_bf16_f32 v249, v250, v251\n"
      "v_cvt_pk_bf16_f32 v250, v156, v157\n"
      "v_mfma_f32_16x16x32_bf16 v[172:175], v[240:243], a[140:143], v[172:175]\n"
      "ds_read_b128 v[240:243], v115 offset:16384\n"
      "v_cvt_pk_bf16_f32 v251, v158, v159\n"
      "v_mfma_f32_16x16x32_bf16 v[164:167], v[206:209], v[12:15], v[216:219]\n"
      "ds_write_b128 v160, v[248:251] offset:12288\n"
      "v_mfma_f32_16x16x32_bf16 v[156:159], v[204:207], v[12:15], v[216:219]\n"
      "s_nop 5\n"
      "v_cvt_pk_bf16_f32 v164, v164, v165\n"
      "v_cvt_pk_bf16_f32 v165, v166, v167\n"
      "v_mfma_f32_16x16x32_bf16 v[248:251], v[206:209], v[16:19], v[216:219]\n"
      "v_cvt_pk_bf16_f32 v166, v156, v157\n"
      "v_cvt_pk_bf16_f32 v167, v158, v159\n"
      "s_waitcnt vmcnt(9)\n"
      "v_mfma_f32_16x16x32_bf16 v[156:159], v[252:255], v[20:23], 0\n"
      "ds_write_b128 v160, v[164:167] offset:12416\n"
      "v_mfma_f32_16x16x32_bf16 v[164:167], v[204:207], v[16:19], v[216:219]\n"
      "s_waitcnt lgkmcnt(2)\n"
      "v_mfma_f32_16x16x32_bf16 v[180:183], a[144:147], v[244:247], v[180:183]\n"
      "v_mfma_f32_16x16x32_bf16 v[180:183], a[180:183], v[240:243], v[180:183]\n"
      "s_nop 0\n"
      "v_cvt_pk_bf16_f32 v248, v248, v249\n"
      "v_pk_mul_f16 v139, v139, v169 op_sel_hi:[1,0]\n"
      "v_mfma_f32_16x16x32_bf16 v[236:239], v[244:247], a[172:175], v[236:239]\n"
      "v_pk_mul_f16 v39, v39, v169 op_sel_hi:[1,0]\n"
      "s_waitcnt vmcnt(8)\n"
      "v_mfma_f32_16x16x32_bf16 v[156:159], v[252:255], v[24:27], v[156:159]\n"
      "v_cvt_pk_bf16_f32 v249, v250, v251\n"
      "s_waitcnt vmcnt(7)\n"
      "v_mfma_f32_16x16x32_bf16 v[156:159], v[252:255], v[28:31], v[156:159]\n"
      "v_cvt_pk_bf16_f32 v251, v166, v167\n"
      "v_mfma_f32_16x16x32_bf16 v[224:227], a[148:151], v[244:247], v[224:227]\n"
      "ds_read_b128 v[252:255], v132 offset:32\n"
      "v_mfma_f32_16x16x32_bf16 v[224:227], a[184:187], v[240:243], v[224:227]\n"
      "v_mfma_f32_16x16x32_bf16 v[232:235], a[152:155], v[244:247], v[232:235]\n"
      "ds_read_u16 v56, v134 offset:32\n"
      "v_mfma_f32_16x16x32_bf16 v[232:235], a[188:191], v[240:243], v[232:235]\n"
      "s_nop 0\n"
      "v_add_f32_dpp v156, v156, v156 quad_perm:[1,0,3,2] row_mask:0xf bank_mask:0xf\n"
      "v_mfma_f32_16x16x32_bf16 v[144:147], a[156:159], v[244:247], v[144:147]\n"
      "v_cvt_pk_bf16_f32 v250, v164, v165\n"
      "v_add_f32_dpp v156, v156, v156 quad_perm:[2,3,0,1] row_mask:0xf bank_mask:0xf\n"
      "v_mfma_f32_16x16x32_bf16 v[144:147], a[192:195], v[240:243], v[144:147]\n"
      "s_nop 0\n"
      "v_add_f32_dpp v156, v156, v156 row_half_mirror row_mask:0xf bank_mask:0xf\n"
      "v_mfma_f32_16x16x32_bf16 v[196:199], a[160:163], v[244:247], v[196:199]\n"
      "ds_read_u16 v52, v134\n"
      "v_mfma_f32_16x16x32_bf16 v[196:199], a[196:199], v[240:243], v[196:199]\n"
      "v_mul_f32 v159, 0xbbaaaaab, v156\n"
      "v_mul_f32 v158, 0xbbaaaaab, v156\n"
      "v_mfma_f32_16x16x32_bf16 v[140:143], a[164:167], v[244:247], v[140:143]\n"
      "s_nop 0\n"
      "v_mul_f32 v157, 0xbbaaaaab, v156\n"
      "v_mul_f32 v156, 0xbbaaaaab, v156\n"
      "v_mfma_f32_16x16x32_bf16 v[140:143], a[200:203], v[240:243], v[140:143]\n"
      "s_nop 0\n"
      "v_mfma_f32_16x16x32_bf16 v[164:167], v[206:209], v[20:23], v[156:159]\n"
      "ds_write_b128 v160, v[248:251] offset:12544\n"
      "v_mfma_f32_16x16x32_bf16 v[172:175], v[244:247], a[176:179], v[172:175]\n"
      "ds_read_b64 v[216:217], v132 offset:24\n"
      "v_mfma_f32_16x16x32_bf16 v[172:175], v[240:243], a[212:215], v[172:175]\n"
      "v_mfma_f32_16x16x32_bf16 v[220:223], v[204:207], v[20:23], v[156:159]\n"
      "ds_read_u16 v60, v134 offset:64\n"
      "v_mfma_f32_16x16x32_bf16 v[200:203], v[206:209], v[24:27], v[156:159]\n"
      "s_nop 0\n"
      "v_cvt_pk_bf16_f32 v164, v164, v165\n"
      "v_cvt_pk_bf16_f32 v165, v166, v167\n"
      "v_mfma_f32_16x16x32_bf16 v[184:187], v[244:247], a[168:171], v[184:187]\n"
      "v_mfma_f32_16x16x32_bf16 v[184:187], v[240:243], a[204:207], v[184:187]\n"
      "s_nop 0\n"
      "v_cvt_pk_bf16_f32 v167, v222, v223\n"
      "v_cvt_pk_bf16_f32 v166, v220, v221\n"
      "v_mfma_f32_16x16x32_bf16 v[236:239], v[240:243], a[208:211], v[236:239]\n"
      "ds_write_b128 v160, v[164:167] offset:15360\n"
      "v_mfma_f32_16x16x32_bf16 v[164:167], v[204:207], v[24:27], v[156:159]\n"
      "s_nop 0\n"
      "v_cvt_pk_bf16_f32 v200, v200, v201\n"
      "v_cvt_pk_bf16_f32 v201, v202, v203\n"
      "v_mfma_f32_16x16x32_bf16 v[220:223], v[204:207], v[28:31], v[156:159]\n"
      "s_waitcnt vmcnt(0)\n"
      "ds_read_b32 v210, v122 offset:4864\n"
      "v_mfma_f32_16x16x32_bf16 v[240:243], v[206:209], v[28:31], v[156:159]\n"
      "ds_read_b128 v[206:209], v119 offset:41728\n"
      "s_nop 0\n"
      "v_cvt_pk_bf16_f32 v203, v166, v167\n"
      "v_cvt_pk_bf16_f32 v202, v164, v165\n"
      "s_nop 3\n"
      "v_cvt_pk_bf16_f32 v240, v240, v241\n"
      "v_cvt_pk_bf16_f32 v241, v242, v243\n"
      "v_cvt_pk_bf16_f32 v243, v222, v223\n"
      "ds_write_b128 v160, v[200:203] offset:15488\n"
      "v_cvt_pk_bf16_f32 v242, v220, v221\n"
      "ds_read_b128 v[246:249], v133\n"
      "ds_write_b128 v160, v[240:243] offset:15616\n"
      "ds_read_b64 v[218:219], v133 offset:16\n"
      "s_waitcnt lgkmcnt(5)\n"
      "s_waitcnt lgkmcnt(4)\n"
      "v_bitop3_b32 v210, v210, v209, s40 bitop3:0xa8\n"
      "v_or3_b32 v206, v206, v207, v208\n"
      "v_and_b32_e64 v206, s40, v206\n"
      "v_or3_b32 v206, v206, v210, s4\n"
      "ds_write_b32 v129, v206 offset:3072\n"
      "s_waitcnt lgkmcnt(0)\n"
      "s_barrier\n"
      "v_mfma_f32_16x16x32_f16 v[164:167], v[152:155], v[176:179], v[78:81]\n"
      "ds_read_b64_tr_b16 a[220:221], v170 offset:8\n"
      "v_cvt_pk_f16_f32 v180, v180, v181\n"
      "v_mfma_f32_16x16x32_f16 v[164:167], v[34:37], v[32:35], v[164:167]\n"
      "ds_read_b64_tr_b16 a[216:217], v170\n"
      "v_cvt_pk_f16_f32 v44, v232, v233\n"
      "v_mfma_f32_16x16x32_f16 v[206:209], v[212:215], v[176:179], v[82:85]\n"
      "ds_read_b64_tr_b16 a[218:219], v170 offset:6144\n"
      "v_cvt_pk_f16_f32 v144, v144, v145\n"
      "v_mfma_f32_16x16x32_f16 v[206:209], v[40:43], v[32:35], v[206:209]\n"
      "ds_read_b64_tr_b16 a[224:225], v171\n"
      "v_pk_mul_f16 v169, v180, v180\n"
      "v_cvt_pk_f16_f32 v145, v146, v147\n"
      "v_mfma_f32_16x16x32_f16 v[160:163], v[252:255], v[176:179], v[70:73]\n"
      "ds_read_b64_tr_b16 a[226:227], v171 offset:6144\n"
      "v_cvt_pk_f16_f32 v181, v182, v183\n"
      "v_mfma_f32_16x16x32_f16 v[160:163], v[246:249], v[152:155], v[160:163]\n"
      "ds_read_b64_tr_b16 a[222:223], v170 offset:6152\n"
      "v_pk_fma_f16 v169, v181, v181, v169\n"
      "v_mfma_f32_16x16x32_f16 v[160:163], v[216:219], v[32:35], v[160:163]\n"
      "ds_read_b64_tr_b16 a[228:229], v170 offset:18432\n"
      "v_cvt_pk_f16_f32 v182, v224, v225\n"
      "v_pk_mul_f16 v210, v144, v144\n"
      "v_cvt_pk_f16_f32 v183, v226, v227\n"
      "v_cvt_pk_f16_f32 v146, v196, v197\n"
      "v_pk_fma_f16 v210, v145, v145, v210\n"
      "v_cvt_pk_f16_f32 v47, v142, v143\n"
      "v_pk_fma_f16 v169, v182, v182, v169\n"
      "v_cvt_pk_f16_f32 v147, v198, v199\n"
      "v_pk_fma_f16 v210, v146, v146, v210\n"
      "v_pk_fma_f16 v210, v147, v147, v210\n"
      "v_cvt_pk_f16_f32 v46, v140, v141\n"
      "v_pk_fma_f16 v169, v183, v183, v169\n"
      "v_cvt_pk_f16_f32 v45, v234, v235\n"
      "v_pk_fma_f16 v169, v44, v44, v169\n"
      "v_pk_fma_f16 v210, v46, v46, v210\n"
      "v_pk_fma_f16 v210, v47, v47, v210\n"
      "v_pk_fma_f16 v169, v45, v45, v169\n"
      "v_cvt_pk_bf16_f32 v50, v184, v185\n"
      "v_cvt_pk_bf16_f32 v55, v238, v239\n"
      "v_permlane16_swap_b32_e64 v169, v210\n"
      "v_pk_add_f16 v169, v169, v210\n"
      "v_mov_b32 v210, v169\n"
      "v_cvt_pk_bf16_f32 v59, v174, v175\n"
      "v_cvt_pk_bf16_f32 v54, v236, v237\n"
      "v_permlane32_swap_b32 v169, v210\n"
      "v_pk_add_f16 v169, v169, v210\n"
      "v_add_f16_sdwa v210, v169, v169 dst_sel:DWORD dst_unused:UNUSED_PAD src0_sel:WORD_0 src1_sel:WORD_1\n"
      "v_cmp_nle_f16 vcc, 0x1c00, v210\n"
      "v_rsq_f16 v169, v210\n"
      "s_or_b64 s[34:35], s[34:35], vcc\n"
      "ds_read_b64_tr_b16 a[252:253], v211 offset:18432\n"
      "ds_read_b64_tr_b16 a[242:243], v211 offset:6144\n"
      "ds_read_b64_tr_b16 a[236:237], v171 offset:18432\n"
      "v_cmp_nge_f16 vcc, 0x7b00, v210\n"
      "v_mov_b32 v210, v169\n"
      "v_cvt_pk_bf16_f32 v58, v172, v173\n"
      "v_cvt_pk_bf16_f32 v51, v186, v187\n"
      "v_permlane16_swap_b32 v169, v210\n"
      "v_mul_f16 v169, s7, v169\n"
      "v_pk_mul_f16 v144, v144, v210 op_sel_hi:[1,0]\n"
      "v_pk_mul_f16 v180, v180, v169 op_sel_hi:[1,0]\n"
      "v_pk_mul_f16 v46, v46, v210 op_sel_hi:[1,0]\n"
      "v_pk_mul_f16 v147, v147, v210 op_sel_hi:[1,0]\n"
      "v_pk_mul_f16 v145, v145, v210 op_sel_hi:[1,0]\n"
      "v_pk_mul_f16 v146, v146, v210 op_sel_hi:[1,0]\n"
      "v_pk_mul_f16 v47, v47, v210 op_sel_hi:[1,0]\n"
      "s_or_b64 s[34:35], s[34:35], vcc\n"
      "ds_read_b64_tr_b16 v[18:19], v171 offset:30720\n"
      "ds_read_b64_tr_b16 v[12:13], v170 offset:12296\n"
      "ds_read_b64_tr_b16 v[8:9], v170 offset:12288\n"
      "ds_read_b64_tr_b16 a[248:249], v168\n"
      "ds_read_b64_tr_b16 v[6:7], v168 offset:24576\n"
      "ds_read_b64_tr_b16 a[230:231], v170 offset:24576\n"
      "ds_read_b64_tr_b16 a[244:245], v211 offset:8\n"
      "ds_read_b64_tr_b16 a[250:251], v168 offset:6144\n"
      "ds_read_b64_tr_b16 v[20:21], v211 offset:12288\n"
      "ds_read_b64_tr_b16 v[24:25], v211 offset:12296\n"
      "ds_read_b64_tr_b16 a[238:239], v171 offset:24576\n"
      "v_mfma_f32_16x16x32_f16 v[232:235], v[144:147], v[176:179], v[86:89]\n"
      "ds_read_b64_tr_b16 a[232:233], v170 offset:18440\n"
      "v_pk_mul_f16 v182, v182, v169 op_sel_hi:[1,0]\n"
      "v_mfma_f32_16x16x32_f16 v[232:235], v[46:49], v[32:35], v[232:235]\n"
      "ds_read_b64_tr_b16 v[28:29], v168 offset:12288\n"
      "ds_read_b64_tr_b16 a[234:235], v170 offset:24584\n"
      "ds_read_b64_tr_b16 a[240:241], v211\n"
      "ds_read_b64_tr_b16 v[26:27], v211 offset:30728\n"
      "ds_read_b64_tr_b16 v[16:17], v171 offset:12288\n"
      "ds_read_b64_tr_b16 v[30:31], v168 offset:30720\n"
      "ds_read_b64_tr_b16 a[254:255], v211 offset:24576\n"
      "ds_read_b64_tr_b16 v[14:15], v170 offset:30728\n"
      "ds_read_b64_tr_b16 v[22:23], v211 offset:30720\n"
      "ds_read_b64_tr_b16 v[4:5], v168 offset:18432\n"
      "ds_read_b64_tr_b16 v[10:11], v170 offset:30720\n"
      "ds_read_b64_tr_b16 v[2:3], v211 offset:24584\n"
      "ds_read_b64_tr_b16 a[246:247], v211 offset:6152\n"
      "ds_read_b64_tr_b16 v[0:1], v211 offset:18440\n"
      "v_pk_mul_f16 v45, v45, v169 op_sel_hi:[1,0]\n"
      "v_pk_mul_f16 v181, v181, v169 op_sel_hi:[1,0]\n"
      "v_pk_mul_f16 v183, v183, v169 op_sel_hi:[1,0]\n"
      "v_pk_mul_f16 v44, v44, v169 op_sel_hi:[1,0]\n"
      "s_waitcnt lgkmcnt(0)\n"
      "s_barrier\n"
      "s_or_b64 vcc, s[48:49], s[50:51]\n"
      "s_or_b64 vcc, vcc, s[34:35]\n"
      "s_cbranch_scc1 L_gen_8_%=\n"
      "L_fast_11_%=:\n"
      "v_mfma_f32_16x16x32_f16 v[168:171], v[152:155], v[136:139], v[90:93]\n"
      "v_exp_f32 v166, v166\n"
      "v_mfma_f32_16x16x32_f16 v[168:171], v[34:37], v[38:41], v[168:171]\n"
      "ds_read_b128 v[176:179], v128 offset:3072\n"
      "v_exp_f32 v164, v164\n"
      "v_mfma_f32_16x16x32_f16 v[196:199], v[212:215], v[136:139], v[94:97]\n"
      "v_exp_f32 v234, v234\n"
      "v_mfma_f32_16x16x32_f16 v[196:199], v[40:43], v[38:41], v[196:199]\n"
      "v_exp_f32 v165, v165\n"
      "v_mfma_f32_16x16x32_f16 v[184:187], v[252:255], v[136:139], v[72:75]\n"
      "v_exp_f32 v207, v207\n"
      "v_mfma_f32_16x16x32_f16 v[184:187], v[246:249], v[212:215], v[184:187]\n"
      "v_exp_f32_e64 v167, v167\n"
      "v_mfma_f32_16x16x32_f16 v[184:187], v[216:219], v[38:41], v[184:187]\n"
      "v_exp_f32_e64 v206, v206\n"
      "v_exp_f32_e64 v160, v160\n"
      "v_mfma_f32_16x16x32_f16 v[236:239], v[144:147], v[136:139], v[98:101]\n"
      "v_cvt_pk_bf16_f32 v164, v164, v165\n"
      "v_cvt_pk_bf16_f32 v165, v166, v167\n"
      "v_mfma_f32_16x16x32_f16 v[236:239], v[46:49], v[38:41], v[236:239]\n"
      "v_exp_f32 v199, v199\n"
      "v_mfma_f32_16x16x32_f16 v[136:139], v[152:155], v[180:183], v[102:105]\n"
      "s_waitcnt lgkmcnt(0)\n"
      "v_bitop3_b32 v176, v176, v177, s4 bitop3:0x7e\n"
      "v_bitop3_b32 v178, v178, v179, s4 bitop3:0x7e\n"
      "v_mfma_f32_16x16x32_f16 v[136:139], v[34:37], v[44:47], v[136:139]\n"
      "v_exp_f32 v209, v209\n"
      "v_mfma_f32_16x16x32_f16 v[152:155], v[212:215], v[180:183], v[106:109]\n"
      "v_or_b32 v176, v176, v178\n"
      "v_cvt_pk_bf16_f32 v166, v206, v207\n"
      "v_mfma_f32_16x16x32_f16 v[152:155], v[40:43], v[44:47], v[152:155]\n"
      "v_exp_f32 v208, v208\n"
      "v_mfma_f32_16x16x32_f16 v[204:207], v[252:255], v[180:183], v[74:77]\n"
      "v_exp_f32 v232, v232\n"
      "v_mfma_f32_16x16x32_f16 v[204:207], v[246:249], v[144:147], v[204:207]\n"
      "v_cmp_ne_u32 vcc, 0, v176\n"
      "v_cvt_pk_bf16_f32 v167, v208, v209\n"
      "v_mfma_f32_16x16x32_f16 v[204:207], v[216:219], v[44:47], v[204:207]\n"
      "v_exp_f32 v233, v233\n"
      "v_mfma_f32_16x16x32_f16 v[140:143], v[144:147], v[180:183], v[110:113]\n"
      "s_mov_b64 s[48:49], vcc\n"
      "v_exp_f32 v235, v235\n"
      "v_mfma_f32_16x16x32_f16 v[140:143], v[46:49], v[44:47], v[140:143]\n"
      "s_cbranch_vccnz L_flg_12_%=\n"
      "L_fb_13_%=:\n"
      "v_exp_f32 v239, v239\n"
      "v_exp_f32_e64 v170, v170\n"
      "v_mfma_f32_16x16x32_f16 v[176:179], v[246:249], v[252:255], v[76:79]\n"
      "v_cvt_pk_bf16_f32 v64, v160, 0\n"
      "v_exp_f32_e64 v198, v198\n"
      "v_cndmask_b32 v161, v161, v185, s[42:43]\n"
      "v_cvt_pk_bf16_f32 v63, v234, v235\n"
      "v_exp_f32 v237, v237\n"
      "v_cvt_pk_bf16_f32 v62, v232, v233\n"
      "v_mfma_f32_16x16x16_f16 v[176:179], v[218:219], v[216:217], v[176:179]\n"
      "s_add_u32 s28, s27, 2\n"
      "v_exp_f32 v169, v169\n"
      "v_mfma_f32_16x16x32_bf16 v[216:219], v[66:69], v[164:167], 0\n"
      "v_exp_f32 v168, v168\n"
      "v_mfma_f32_16x16x32_bf16 v[216:219], v[66:69], v[62:65], v[216:219]\n"
      "v_cvt_pk_bf16_f32 v168, v168, v169\n"
      "v_exp_f32 v238, v238\n"
      "v_mfma_f32_16x16x32_bf16 v[208:211], v[228:231], v[164:167], 0\n"
      "v_exp_f32 v171, v171\n"
      "v_mfma_f32_16x16x32_bf16 v[208:211], v[50:53], v[62:65], v[208:211]\n"
      "v_cvt_pk_bf16_f32 v169, v170, v171\n"
      "v_cndmask_b32 v161, v161, v205, s[44:45]\n"
      "v_mfma_f32_16x16x32_bf16 v[212:215], v[148:151], v[164:167], 0\n"
      "v_exp_f32 v196, v196\n"
      "v_mfma_f32_16x16x32_bf16 v[212:215], v[54:57], v[62:65], v[212:215]\n"
      "v_exp_f32 v236, v236\n"
      "v_exp_f32 v184, v184\n"
      "v_mfma_f32_16x16x32_bf16 v[224:227], v[190:193], v[164:167], 0\n"
      "v_exp_f32 v197, v197\n"
      "v_mfma_f32_16x16x32_bf16 v[224:227], v[58:61], v[62:65], v[224:227]\n"
      "v_cvt_pk_bf16_f32 v171, v198, v199\n"
      "v_cvt_pk_bf16_f32 v62, v236, v237\n"
      "v_cvt_pk_bf16_f32 v64, v184, 0\n"
      "v_cvt_pk_bf16_f32 v170, v196, v197\n"
      "v_rcp_f32_e64 v216, v216\n"
      "v_cvt_pk_bf16_f32 v63, v238, v239\n"
      "v_mfma_f32_16x16x32_bf16 v[236:239], v[66:69], v[168:171], 0\n"
      "ds_read_b128 v[232:235], v114 offset:18432\n"
      "v_mul_f32_e64 v208, v216, v208\n"
      "v_mfma_f32_16x16x32_bf16 v[236:239], v[66:69], v[62:65], v[236:239]\n"
      "ds_read_b128 v[144:147], v115 offset:18432\n"
      "v_mul_f32 v226, v216, v226\n"
      "v_mul_f32 v211, v216, v211\n"
      "v_mul_f32_e64 v209, v216, v209\n"
      "v_mfma_f32_16x16x32_bf16 v[242:245], v[228:231], v[168:171], 0\n"
      "s_min_u32 m0, s28, s3\n"
      "v_cvt_pk_bf16_f32 v208, v208, v209\n"
      "v_mul_f32 v210, v216, v210\n"
      "v_mfma_f32_16x16x32_bf16 v[242:245], v[50:53], v[62:65], v[242:245]\n"
      "s_movrels_b32 s9, s80\n"
      "v_mul_f32 v213, v216, v213\n"
      "v_cvt_pk_bf16_f32 v209, v210, v211\n"
      "v_mfma_f32_16x16x32_bf16 v[220:223], v[148:151], v[168:171], 0\n"
      "s_lshr_b32 s8, s9, 8\n"
      "v_mul_f32 v214, v216, v214\n"
      "v_mul_f32_e64 v212, v216, v212\n"
      "v_mfma_f32_16x16x32_bf16 v[220:223], v[54:57], v[62:65], v[220:223]\n"
      "s_and_b32 s9, s9, 0xff\n"
      "v_mul_f32_e64 v224, v216, v224\n"
      "v_mul_f32_e64 v225, v216, v225\n"
      "v_cvt_pk_bf16_f32 v210, v212, v213\n"
      "v_mfma_f32_16x16x32_bf16 v[238:241], v[190:193], v[168:171], 0\n"
      "s_mul_i32 s10, s8, 73\n"
      "v_mul_f32 v215, v216, v215\n"
      "v_cvt_pk_bf16_f32 v211, v214, v215\n"
      "v_mfma_f32_16x16x32_bf16 v[238:241], v[58:61], v[62:65], v[238:241]\n"
      "ds_write_b128 v124, v[208:211]\n"
      "s_waitcnt lgkmcnt(1)\n"
      "v_mfma_f32_16x16x32_bf16 v[208:211], a[0:3], v[232:235], 0\n"
      "s_add_u32 s10, s10, s9\n"
      "v_mul_f32 v227, v216, v227\n"
      "v_cvt_pk_bf16_f32 v224, v224, v225\n"
      "v_mfma_f32_16x16x32_bf16 v[208:211], a[36:39], v[144:147], v[208:211]\n"
      "s_mul_i32 s28, s10, 4802\n"
      "v_cvt_pk_bf16_f32 v225, v226, v227\n"
      "v_cndmask_b32 v161, v161, v177, s[46:47]\n"
      "v_mfma_f32_16x16x32_bf16 v[216:219], a[4:7], v[232:235], 0\n"
      "s_add_u32 s56, s18, s28\n"
      "v_exp_f32 v161, v161\n"
      "v_mfma_f32_16x16x32_bf16 v[216:219], a[40:43], v[144:147], v[216:219]\n"
      "s_addc_u32 s57, s19, 0\n"
      "v_mov_b32 v237, 0\n"
      "v_cvt_pk_bf16_f32 v161, v161, v161\n"
      "v_mfma_f32_16x16x32_bf16 v[176:179], a[8:11], v[232:235], 0\n"
      "s_and_b32 s56, s56, -16\n"
      "v_exp_f32 v136, v136\n"
      "v_mfma_f32_16x16x32_bf16 v[176:179], a[44:47], v[144:147], v[176:179]\n"
      "ds_write_b16 v130, v161\n"
      "v_mfma_f32_16x16x32_bf16 v[212:215], a[12:15], v[232:235], 0\n"
      "ds_read_b128 v[246:249], v131\n"
      "s_sub_u32 s58, s56, 110592\n"
      "v_mfma_f32_16x16x32_bf16 v[212:215], a[48:51], v[144:147], v[212:215]\n"
      "ds_read_b128 v[156:159], v131 offset:64\n"
      "v_exp_f32 v138, v138\n"
      "v_exp_f32 v153, v153\n"
      "v_mfma_f32_16x16x32_bf16 v[196:199], a[16:19], v[232:235], 0\n"
      "s_subb_u32 s59, s57, 0\n"
      "v_exp_f32 v137, v137\n"
      "v_mfma_f32_16x16x32_bf16 v[196:199], a[52:55], v[144:147], v[196:199]\n"
      "s_sub_u32 s56, s56, 73728\n"
      "v_exp_f32 v154, v154\n"
      "v_cvt_pk_bf16_f32 v136, v136, v137\n"
      "v_exp_f32 v140, v140\n"
      "v_mfma_f32_16x16x32_bf16 v[160:163], a[20:23], v[232:235], 0\n"
      "s_subb_u32 s57, s57, 0\n"
      "v_exp_f32 v139, v139\n"
      "v_mfma_f32_16x16x32_bf16 v[160:163], a[56:59], v[144:147], v[160:163]\n"
      "s_mul_i32 s28, s8, 1373568\n"
      "v_cvt_pk_bf16_f32 v137, v138, v139\n"
      "v_exp_f32 v141, v141\n"
      "v_exp_f32 v155, v155\n"
      "v_exp_f32 v152, v152\n"
      "v_exp_f32 v142, v142\n"
      "v_cvt_pk_bf16_f32 v138, v152, v153\n"
      "s_waitcnt lgkmcnt(0)\n"
      "v_cvt_pk_bf16_f32 v139, v154, v155\n"
      "v_mfma_f32_16x16x32_bf16 v[152:155], v[246:249], v[66:69], 0\n"
      "s_mul_i32 s29, s9, 2688\n"
      "v_exp_f32 v143, v143\n"
      "v_mfma_f32_16x16x32_bf16 v[152:155], v[156:159], v[66:69], v[152:155]\n"
      "s_add_u32 s28, s28, s29\n"
      "v_exp_f32 v204, v204\n"
      "v_mfma_f32_16x16x32_bf16 v[250:253], v[246:249], v[228:231], 0\n"
      "s_cmp_eq_u32 s8, 72\n"
      "v_cvt_pk_bf16_f32 v63, v142, v143\n"
      "v_cvt_pk_bf16_f32 v62, v140, v141\n"
      "v_mfma_f32_16x16x32_bf16 v[250:253], v[156:159], v[50:53], v[250:253]\n"
      "s_cselect_b32 s8, 0, 100270464\n"
      "v_cvt_pk_bf16_f32 v64, v204, 0\n"
      "v_mov_b32_dpp v152, v152 quad_perm:[0,0,0,0] row_mask:0xf bank_mask:0xf\n"
      "v_mfma_f32_16x16x32_bf16 v[140:143], v[246:249], v[148:151], 0\n"
      "ds_read_b128 v[168:171], v114 offset:24576\n"
      "s_cmp_eq_u32 s9, 72\n"
      "v_mfma_f32_16x16x32_bf16 v[140:143], v[156:159], v[54:57], v[140:143]\n"
      "s_cselect_b32 s9, 0, 196224\n"
      "v_rcp_f32_e64 v236, v236\n"
      "v_mfma_f32_16x16x32_bf16 v[180:183], v[66:69], v[136:139], 0\n"
      "ds_read_b128 v[252:255], v115 offset:24576\n"
      "v_mul_f32 v242, v236, v242\n"
      "v_mfma_f32_16x16x32_bf16 v[180:183], v[66:69], v[62:65], v[180:183]\n"
      "s_add_u32 s10, s8, s9\n"
      "v_mul_f32 v243, v236, v243\n"
      "v_cvt_pk_bf16_f32 v242, v242, v243\n"
      "v_mfma_f32_16x16x32_bf16 v[184:187], v[228:231], v[136:139], 0\n"
      "s_add_u32 s28, s28, s10\n"
      "v_mul_f32 v244, v236, v244\n"
      "v_mul_f32 v245, v236, v245\n"
      "v_mfma_f32_16x16x32_bf16 v[184:187], v[50:53], v[62:65], v[184:187]\n"
      "s_add_u32 s52, s16, s28\n"
      "v_cvt_pk_bf16_f32 v243, v244, v245\n"
      "v_mul_f32 v221, v236, v221\n"
      "v_mfma_f32_16x16x32_bf16 v[164:167], v[148:151], v[136:139], 0\n"
      "s_addc_u32 s53, s17, 0\n"
      "v_mul_f32 v220, v236, v220\n"
      "v_cvt_pk_bf16_f32 v244, v220, v221\n"
      "v_mfma_f32_16x16x32_bf16 v[164:167], v[54:57], v[62:65], v[164:167]\n"
      "s_cmp_lg_u32 s10, s11\n"
      "v_mul_f32 v223, v236, v223\n"
      "v_mul_f32 v222, v236, v222\n"
      "v_mfma_f32_16x16x32_bf16 v[204:207], v[190:193], v[136:139], 0\n"
      "s_cbranch_scc1 L_edge_15_%=\n"
      "L_eb_16_%=:\n"
      "v_mfma_f32_16x16x32_bf16 v[204:207], v[58:61], v[62:65], v[204:207]\n"
      "v_mul_f32_e64 v238, v236, v238\n"
      "v_cvt_pk_bf16_f32 v245, v222, v223\n"
      "v_mfma_f32_16x16x32_bf16 v[136:139], v[246:249], v[190:193], 0\n"
      "s_waitcnt lgkmcnt(0)\n"
      "v_mul_f32 v241, v236, v241\n"
      "v_mul_f32_e64 v239, v236, v239\n"
      "v_mfma_f32_16x16x32_bf16 v[136:139], v[156:159], v[58:61], v[136:139]\n"
      "ds_write_b128 v124, v[242:245] offset:1024\n"
      "v_mul_f32_e64 v240, v236, v240\n"
      "v_cvt_pk_bf16_f32 v238, v238, v239\n"
      "v_cndmask_b32 v250, v250, v140, s[42:43]\n"
      "v_cvt_pk_bf16_f32 v239, v240, v241\n"
      "ds_write2st64_b64 v125, v[224:225], v[238:239] offset0:0 offset1:2\n"
      "ds_write_b32 v129, v237\n"
      "v_rcp_f32 v152, v152\n"
      "v_cndmask_b32 v250, v250, v136, s[44:45]\n"
      "v_mul_f32 v250, v152, v250\n"
      "v_cvt_pk_bf16_f32 v250, v250, v250\n"
      "ds_write_b16 v135, v250\n"
      "L_join_10_%=:\n"
      "s_mov_b32 s27, 1\n"
      ".p2align 6\n"
      "L_loop_%=:\n"
      "v_mfma_f32_16x16x32_bf16 v[244:247], v[232:235], a[24:27], 0\n"
      "v_rcp_f32_e64 v180, v180\n"
      "v_mfma_f32_16x16x32_bf16 v[244:247], v[144:147], a[60:63], v[244:247]\n"
      "s_waitcnt lgkmcnt(0)\n"
      "s_barrier\n"
      "v_mfma_f32_16x16x32_bf16 v[156:159], v[232:235], a[28:31], 0\n"
      "s_add_u32 m0, s76, 0\n"
      "v_mul_f32 v186, v180, v186\n"
      "v_mul_f32 v184, v180, v184\n"
      "v_mfma_f32_16x16x32_bf16 v[156:159], v[144:147], a[64:67], v[156:159]\n"
      "v_mul_f32 v185, v180, v185\n"
      "v_cvt_pk_bf16_f32 v184, v184, v185\n"
      "v_mfma_f32_16x16x32_bf16 v[248:251], v[232:235], a[32:35], 0\n"
      "ds_read_b128 v[220:223], v128\n"
      "v_mul_f32_e64 v187, v180, v187\n"
      "v_mfma_f32_16x16x32_bf16 v[248:251], v[144:147], a[68:71], v[248:251]\n"
      "global_load_lds_dwordx4 v116, s[52:53]\n"
      "v_mfma_f32_16x16x32_bf16 v[208:211], a[72:75], v[168:171], v[208:211]\n"
      "ds_read_b128 v[232:235], v114 offset:30720\n"
      "v_cvt_pk_bf16_f32 v185, v186, v187\n"
      "v_mfma_f32_16x16x32_bf16 v[208:211], a[108:111], v[252:255], v[208:211]\n"
      "s_add_u32 m0, s76, 6016\n"
      "v_mul_f32 v164, v180, v164\n"
      "v_mul_f32 v165, v180, v165\n"
      "v_mfma_f32_16x16x32_bf16 v[216:219], a[76:79], v[168:171], v[216:219]\n"
      "v_cvt_pk_bf16_f32 v186, v164, v165\n"
      "v_mul_f32 v166, v180, v166\n"
      "v_mfma_f32_16x16x32_bf16 v[216:219], a[112:115], v[252:255], v[216:219]\n"
      "v_mul_f32 v167, v180, v167\n"
      "v_cvt_pk_bf16_f32 v187, v166, v167\n"
      "v_mfma_f32_16x16x32_bf16 v[176:179], a[80:83], v[168:171], v[176:179]\n"
      "ds_read_b128 v[144:147], v115 offset:30720\n"
      "v_mul_f32_e64 v204, v180, v204\n"
      "v_mfma_f32_16x16x32_bf16 v[176:179], a[116:119], v[252:255], v[176:179]\n"
      "ds_write_b128 v124, v[184:187] offset:2048\n"
      "v_mfma_f32_16x16x32_bf16 v[212:215], a[84:87], v[168:171], v[212:215]\n"
      "s_waitcnt lgkmcnt(3)\n"
      "v_mul_f32 v207, v180, v207\n"
      "v_mul_f32 v205, v180, v205\n"
      "v_mfma_f32_16x16x32_bf16 v[212:215], a[120:123], v[252:255], v[212:215]\n"
      "v_cvt_pk_bf16_f32 v204, v204, v205\n"
      "v_mul_f32 v206, v180, v206\n"
      "v_mfma_f32_16x16x32_bf16 v[196:199], a[88:91], v[168:171], v[196:199]\n"
      "v_cvt_pk_bf16_f32 v205, v206, v207\n"
      "v_add_f32 v220, |v220|, |v221|\n"
      "v_mfma_f32_16x16x32_bf16 v[196:199], a[124:127], v[252:255], v[196:199]\n"
      "ds_write_b64 v125, v[204:205] offset:2048\n"
      "v_mfma_f32_16x16x32_bf16 v[160:163], a[92:95], v[168:171], v[160:163]\n"
      "global_load_lds_dwordx4 v116, s[52:53] offset:128\n"
      "v_mfma_f32_16x16x32_bf16 v[160:163], a[128:131], v[252:255], v[160:163]\n"
      "v_add_f32 v222, |v222|, |v223|\n"
      "v_add_f32 v220, v220, v222\n"
      "v_mfma_f32_16x16x32_bf16 v[244:247], v[168:171], a[96:99], v[244:247]\n"
      "s_waitcnt lgkmcnt(2)\n"
      "v_rsq_f32 v220, v220\n"
      "v_mfma_f32_16x16x32_bf16 v[244:247], v[252:255], a[132:135], v[244:247]\n"
      "v_mul_f32 v220, s41, v220\n"
      "v_mfma_f32_16x16x32_bf16 v[156:159], v[168:171], a[100:103], v[156:159]\n"
      "ds_read_b128 v[164:167], v114 offset:20480\n"
      "v_mov_b32 v180, v220\n"
      "v_mfma_f32_16x16x32_bf16 v[156:159], v[252:255], a[136:139], v[156:159]\n"
      "s_nop 0\n"
      "v_permlane32_swap_b32 v220, v180\n"
      "v_mfma_f32_16x16x32_bf16 v[248:251], v[168:171], a[104:107], v[248:251]\n"
      "ds_read_b128 v[168:171], v115 offset:20480\n"
      "v_mov_b32 v181, v220\n"
      "v_mfma_f32_16x16x32_bf16 v[248:251], v[252:255], a[140:143], v[248:251]\n"
      "s_add_u32 m0, s76, 12032\n"
      "v_permlane16_swap_b32 v220, v181\n"
      "v_mfma_f32_16x16x32_bf16 v[208:211], a[144:147], v[232:235], v[208:211]\n"
      "v_mul_f32 v240, v220, v240\n"
      "v_mul_f32_e64 v241, v220, v241\n"
      "v_mfma_f32_16x16x32_bf16 v[208:211], a[180:183], v[144:147], v[208:211]\n"
      "v_cvt_pk_bf16_f32 v240, v240, v241\n"
      "v_mul_f32 v242, v220, v242\n"
      "v_mfma_f32_16x16x32_bf16 v[216:219], a[148:151], v[232:235], v[216:219]\n"
      "v_mul_f32 v243, v220, v243\n"
      "v_mul_f32_e64 v153, v220, v153\n"
      "v_mfma_f32_16x16x32_bf16 v[216:219], a[184:187], v[144:147], v[216:219]\n"
      "global_load_lds_dwordx4 v116, s[52:53] offset:256\n"
      "v_mfma_f32_16x16x32_bf16 v[176:179], a[152:155], v[232:235], v[176:179]\n"
      "v_cvt_pk_bf16_f32 v241, v242, v243\n"
      "v_mul_f32 v137, v220, v137\n"
      "v_mfma_f32_16x16x32_bf16 v[176:179], a[188:191], v[144:147], v[176:179]\n"
      "v_mul_f32 v139, v220, v139\n"
      "v_mfma_f32_16x16x32_bf16 v[212:215], a[156:159], v[232:235], v[212:215]\n"
      "s_waitcnt lgkmcnt(0)\n"
      "v_mul_f32 v138, v220, v138\n"
      "v_mul_f32 v152, v220, v152\n"
      "v_mfma_f32_16x16x32_bf16 v[212:215], a[192:195], v[144:147], v[212:215]\n"
      "v_mul_f32 v136, v220, v136\n"
      "v_cvt_pk_bf16_f32 v242, v136, v137\n"
      "v_mfma_f32_16x16x32_bf16 v[196:199], a[160:163], v[232:235], v[196:199]\n"
      "v_cvt_pk_bf16_f32 v243, v138, v139\n"
      "v_mul_f32_e64 v154, v220, v154\n"
      "v_mfma_f32_16x16x32_bf16 v[196:199], a[196:199], v[144:147], v[196:199]\n"
      "ds_write_b128 v126, v[240:243] offset:18432\n"
      "v_mfma_f32_16x16x32_bf16 v[160:163], a[164:167], v[232:235], v[160:163]\n"
      "v_mul_f32_e64 v155, v220, v155\n"
      "v_cvt_pk_bf16_f32 v152, v152, v153\n"
      "v_mfma_f32_16x16x32_bf16 v[160:163], a[200:203], v[144:147], v[160:163]\n"
      "v_cvt_pk_bf16_f32 v153, v154, v155\n"
      "v_mul_f32 v148, v181, v148\n"
      "v_mfma_f32_16x16x32_bf16 v[244:247], v[232:235], a[168:171], v[244:247]\n"
      "s_add_u32 m0, s77, 0\n"
      "v_mul_f32 v149, v181, v149\n"
      "v_mul_f32 v150, v181, v150\n"
      "v_mfma_f32_16x16x32_bf16 v[244:247], v[144:147], a[204:207], v[244:247]\n"
      "global_load_lds_dwordx4 v117, s[52:53]\n"
      "v_mfma_f32_16x16x32_bf16 v[156:159], v[232:235], a[172:175], v[156:159]\n"
      "v_cvt_pk_bf16_f32 v148, v148, v149\n"
      "v_mul_f32 v236, v181, v236\n"
      "v_mfma_f32_16x16x32_bf16 v[156:159], v[144:147], a[208:211], v[156:159]\n"
      "v_mul_f32 v151, v181, v151\n"
      "v_cvt_pk_bf16_f32 v149, v150, v151\n"
      "v_mfma_f32_16x16x32_bf16 v[248:251], v[232:235], a[176:179], v[248:251]\n"
      "ds_read_b128 v[240:243], v114 offset:26624\n"
      "v_mul_f32_e64 v237, v181, v237\n"
      "v_mfma_f32_16x16x32_bf16 v[248:251], v[144:147], a[212:215], v[248:251]\n"
      "ds_read_b128 v[136:139], v115 offset:26624\n"
      "v_cvt_pk_bf16_f32 v150, v236, v237\n"
      "v_mfma_f32_16x16x32_bf16 v[234:237], a[0:3], v[164:167], 0\n"
      "s_add_u32 m0, s78, 0\n"
      "v_mul_f32 v239, v181, v239\n"
      "v_mul_f32 v238, v181, v238\n"
      "v_mfma_f32_16x16x32_bf16 v[234:237], a[36:39], v[168:171], v[234:237]\n"
      "v_mul_f32 v140, v181, v140\n"
      "v_cvt_pk_bf16_f32 v151, v238, v239\n"
      "v_mfma_f32_16x16x32_bf16 v[144:147], a[4:7], v[164:167], 0\n"
      "v_mul_f32_e64 v141, v181, v141\n"
      "v_cvt_pk_bf16_f32 v140, v140, v141\n"
      "v_mfma_f32_16x16x32_bf16 v[144:147], a[40:43], v[168:171], v[144:147]\n"
      "ds_write_b128 v126, v[148:151] offset:20480\n"
      "v_mfma_f32_16x16x32_bf16 v[148:151], a[8:11], v[164:167], 0\n"
      "v_mul_f32 v142, v181, v142\n"
      "v_mul_f32_e64 v143, v181, v143\n"
      "v_mfma_f32_16x16x32_bf16 v[148:151], a[44:47], v[168:171], v[148:151]\n"
      "s_mov_b32 exec_hi, 0\n"
      "global_load_lds_dwordx4 v118, s[52:53]\n"
      "s_mov_b32 exec_hi, -1\n"
      "v_mfma_f32_16x16x32_bf16 v[252:255], a[12:15], v[164:167], 0\n"
      "v_mul_f32 v188, v180, v188\n"
      "v_cvt_pk_bf16_f32 v141, v142, v143\n"
      "v_mfma_f32_16x16x32_bf16 v[252:255], a[48:51], v[168:171], v[252:255]\n"
      "ds_write2st64_b64 v127, v[152:153], v[140:141] offset0:36 offset1:40\n"
      "v_mfma_f32_16x16x32_bf16 v[152:155], a[16:19], v[164:167], 0\n"
      "v_mul_f32_e64 v189, v180, v189\n"
      "v_cvt_pk_bf16_f32 v188, v188, v189\n"
      "v_mfma_f32_16x16x32_bf16 v[152:155], a[52:55], v[168:171], v[152:155]\n"
      "s_add_u32 m0, s76, 110592\n"
      "v_mul_f32 v190, v180, v190\n"
      "v_mul_f32 v191, v180, v191\n"
      "v_mfma_f32_16x16x32_bf16 v[184:187], a[20:23], v[164:167], 0\n"
      "s_waitcnt lgkmcnt(2)\n"
      "v_cvt_pk_bf16_f32 v189, v190, v191\n"
      "v_mul_f32 v224, v180, v224\n"
      "v_mfma_f32_16x16x32_bf16 v[184:187], a[56:59], v[168:171], v[184:187]\n"
      "v_mul_f32 v225, v180, v225\n"
      "v_cvt_pk_bf16_f32 v190, v224, v225\n"
      "v_mfma_f32_16x16x32_bf16 v[140:143], v[164:167], a[24:27], 0\n"
      "ds_read_b128 v[220:223], v114 offset:32768\n"
      "v_mul_f32 v226, v180, v226\n"
      "v_mfma_f32_16x16x32_bf16 v[140:143], v[168:171], a[60:63], v[140:143]\n"
      "v_mul_f32_e64 v227, v180, v227\n"
      "v_cvt_pk_bf16_f32 v191, v226, v227\n"
      "v_mfma_f32_16x16x32_bf16 v[204:207], v[164:167], a[28:31], 0\n"
      "global_load_lds_dwordx4 v119, s[56:57]\n"
      "v_cvt_pk_f16_f32 v212, v212, v213\n"
      "v_mfma_f32_16x16x32_bf16 v[204:207], v[168:171], a[64:67], v[204:207]\n"
      "ds_write_b128 v126, v[188:191] offset:22528\n"
      "v_mfma_f32_16x16x32_bf16 v[188:191], v[164:167], a[32:35], 0\n"
      "ds_read_b128 v[224:227], v115 offset:32768\n"
      "v_mul_f32 v228, v180, v228\n"
      "v_mfma_f32_16x16x32_bf16 v[188:191], v[168:171], a[68:71], v[188:191]\n"
      "s_add_u32 m0, s79, 0\n"
      "v_cvt_pk_f16_f32 v208, v208, v209\n"
      "v_mul_f32 v229, v180, v229\n"
      "v_mfma_f32_16x16x32_bf16 v[234:237], a[72:75], v[240:243], v[234:237]\n"
      "v_mul_f32 v230, v180, v230\n"
      "v_mul_f32_e64 v231, v180, v231\n"
      "v_mfma_f32_16x16x32_bf16 v[144:147], a[76:79], v[240:243], v[144:147]\n"
      "v_cvt_pk_bf16_f32 v228, v228, v229\n"
      "v_cvt_pk_bf16_f32 v229, v230, v231\n"
      "v_mfma_f32_16x16x32_bf16 v[234:237], a[108:111], v[136:139], v[234:237]\n"
      "global_load_lds_dword v122, s[58:59]\n"
      "v_mfma_f32_16x16x32_bf16 v[144:147], a[112:115], v[136:139], v[144:147]\n"
      "ds_write_b64 v127, v[228:229] offset:22528\n"
      "v_mfma_f32_16x16x32_bf16 v[148:151], a[80:83], v[240:243], v[148:151]\n"
      "v_cvt_pk_f16_f32 v213, v214, v215\n"
      "v_pk_mul_f16 v239, v208, v208\n"
      "v_mfma_f32_16x16x32_bf16 v[148:151], a[116:119], v[136:139], v[148:151]\n"
      "v_cvt_pk_f16_f32 v209, v210, v211\n"
      "v_pk_mul_f16 v238, v212, v212\n"
      "v_mfma_f32_16x16x32_bf16 v[252:255], a[84:87], v[240:243], v[252:255]\n"
      "ds_read_b128 v[168:171], v115 offset:22528\n"
      "v_pk_fma_f16 v239, v209, v209, v239\n"
      "v_mfma_f32_16x16x32_bf16 v[252:255], a[120:123], v[136:139], v[252:255]\n"
      "ds_read_b128 v[230:233], v114 offset:22528\n"
      "v_cvt_pk_f16_f32 v210, v216, v217\n"
      "v_mfma_f32_16x16x32_bf16 v[152:155], a[88:91], v[240:243], v[152:155]\n"
      "v_pk_fma_f16 v238, v213, v213, v238\n"
      "v_pk_fma_f16 v239, v210, v210, v239\n"
      "v_mfma_f32_16x16x32_bf16 v[152:155], a[124:127], v[136:139], v[152:155]\n"
      "v_cvt_pk_f16_f32 v214, v196, v197\n"
      "v_cvt_pk_f16_f32 v215, v198, v199\n"
      "v_mfma_f32_16x16x32_bf16 v[184:187], a[92:95], v[240:243], v[184:187]\n"
      "s_waitcnt lgkmcnt(3)\n"
      "v_pk_fma_f16 v238, v214, v214, v238\n"
      "v_pk_fma_f16 v238, v215, v215, v238\n"
      "v_mfma_f32_16x16x32_bf16 v[184:187], a[128:131], v[136:139], v[184:187]\n"
      "s_nop 0\n"
      "v_cvt_pk_f16_f32 v211, v218, v219\n"
      "v_cvt_pk_f16_f32 v33, v178, v179\n"
      "v_mfma_f32_16x16x32_bf16 v[140:143], v[240:243], a[96:99], v[140:143]\n"
      "v_pk_fma_f16 v239, v211, v211, v239\n"
      "v_cvt_pk_f16_f32 v32, v176, v177\n"
      "v_mfma_f32_16x16x32_bf16 v[140:143], v[136:139], a[132:135], v[140:143]\n"
      "v_pk_fma_f16 v239, v32, v32, v239\n"
      "v_cvt_pk_f16_f32 v34, v160, v161\n"
      "v_mfma_f32_16x16x32_bf16 v[204:207], v[240:243], a[100:103], v[204:207]\n"
      "v_pk_fma_f16 v238, v34, v34, v238\n"
      "v_mfma_f32_16x16x32_bf16 v[204:207], v[136:139], a[136:139], v[204:207]\n"
      "ds_read_b128 v[164:167], v119\n"
      "v_pk_fma_f16 v239, v33, v33, v239\n"
      "v_mfma_f32_16x16x32_bf16 v[188:191], v[240:243], a[104:107], v[188:191]\n"
      "v_cvt_pk_f16_f32 v35, v162, v163\n"
      "v_pk_fma_f16 v238, v35, v35, v238\n"
      "v_mfma_f32_16x16x32_bf16 v[188:191], v[136:139], a[140:143], v[188:191]\n"
      "v_cvt_pk_bf16_f32 v244, v244, v245\n"
      "v_cvt_pk_bf16_f32 v245, v246, v247\n"
      "v_mfma_f32_16x16x32_bf16 v[234:237], a[144:147], v[220:223], v[234:237]\n"
      "v_permlane16_swap_b32_e64 v239, v238\n"
      "v_mfma_f32_16x16x32_bf16 v[234:237], a[180:183], v[224:227], v[234:237]\n"
      "v_pk_add_f16 v239, v239, v238\n"
      "v_mov_b32 v238, v239\n"
      "v_mfma_f32_16x16x32_bf16 v[144:147], a[148:151], v[220:223], v[144:147]\n"
      "s_nop 0\n"
      "v_permlane32_swap_b32_e64 v239, v238\n"
      "v_mfma_f32_16x16x32_bf16 v[144:147], a[184:187], v[224:227], v[144:147]\n"
      "v_pk_add_f16 v239, v239, v238\n"
      "v_add_f16_sdwa v238, v239, v239 dst_sel:DWORD dst_unused:UNUSED_PAD src0_sel:WORD_0 src1_sel:WORD_1\n"
      "v_mfma_f32_16x16x32_bf16 v[148:151], a[152:155], v[220:223], v[148:151]\n"
      "ds_read_b128 v[136:139], v114 offset:28672\n"
      "v_cmp_nle_f16 vcc, 0x1c00, v238\n"
      "v_mfma_f32_16x16x32_bf16 v[148:151], a[188:191], v[224:227], v[148:151]\n"
      "s_mov_b64 s[34:35], vcc\n"
      "v_rsq_f16 v239, v238\n"
      "v_mfma_f32_16x16x32_bf16 v[252:255], a[156:159], v[220:223], v[252:255]\n"
      "s_waitcnt lgkmcnt(1)\n"
      "v_cmp_nge_f16 vcc, 0x7b00, v238\n"
      "v_mov_b32 v238, v239\n"
      "v_mfma_f32_16x16x32_bf16 v[252:255], a[192:195], v[224:227], v[252:255]\n"
      "ds_read_b128 v[240:243], v115 offset:28672\n"
      "v_cvt_pk_bf16_f32 v156, v156, v157\n"
      "v_mfma_f32_16x16x32_bf16 v[152:155], a[160:163], v[220:223], v[152:155]\n"
      "s_or_b64 s[34:35], s[34:35], vcc\n"
      "v_permlane16_swap_b32 v239, v238\n"
      "v_mfma_f32_16x16x32_bf16 v[152:155], a[196:199], v[224:227], v[152:155]\n"
      "s_sub_u32 s28, s27, 3\n"
      "v_pk_mul_f16 v213, v213, v238 op_sel_hi:[1,0]\n"
      "v_pk_mul_f16 v212, v212, v238 op_sel_hi:[1,0]\n"
      "v_mfma_f32_16x16x32_bf16 v[184:187], a[164:167], v[220:223], v[184:187]\n"
      "s_cselect_b64 s[54:55], s[12:13], s[22:23]\n"
      "v_pk_mul_f16 v34, v34, v238 op_sel_hi:[1,0]\n"
      "v_pk_mul_f16 v214, v214, v238 op_sel_hi:[1,0]\n"
      "v_mfma_f32_16x16x32_bf16 v[184:187], a[200:203], v[224:227], v[184:187]\n"
      "s_max_i32 m0, s28, 0\n"
      "v_pk_mul_f16 v215, v215, v238 op_sel_hi:[1,0]\n"
      "v_pk_mul_f16 v35, v35, v238 op_sel_hi:[1,0]\n"
      "v_mfma_f32_16x16x32_bf16 v[140:143], v[220:223], a[168:171], v[140:143]\n"
      "s_movrels_b32 s9, s80\n"
      "v_mul_f16 v239, s7, v239\n"
      "v_pk_mul_f16 v208, v208, v239 op_sel_hi:[1,0]\n"
      "v_mfma_f32_16x16x32_bf16 v[140:143], v[224:227], a[204:207], v[140:143]\n"
      "ds_read_b128 v[180:183], v119 offset:6144\n"
      "v_pk_mul_f16 v210, v210, v239 op_sel_hi:[1,0]\n"
      "v_mfma_f32_16x16x32_bf16 v[204:207], v[220:223], a[172:175], v[204:207]\n"
      "s_lshr_b32 s8, s9, 8\n"
      "v_pk_mul_f16 v209, v209, v239 op_sel_hi:[1,0]\n"
      "v_pk_mul_f16 v32, v32, v239 op_sel_hi:[1,0]\n"
      "v_mfma_f32_16x16x32_bf16 v[204:207], v[224:227], a[208:211], v[204:207]\n"
      "s_and_b32 s9, s9, 0xff\n"
      "v_pk_mul_f16 v33, v33, v239 op_sel_hi:[1,0]\n"
      "v_pk_mul_f16 v211, v211, v239 op_sel_hi:[1,0]\n"
      "v_mfma_f32_16x16x32_bf16 v[188:191], v[220:223], a[176:179], v[188:191]\n"
      "s_mul_i32 s28, s8, 1373568\n"
      "v_cvt_pk_bf16_f32 v247, v142, v143\n"
      "v_cvt_pk_bf16_f32 v246, v140, v141\n"
      "v_mfma_f32_16x16x32_bf16 v[188:191], v[224:227], a[212:215], v[188:191]\n"
      "s_mul_i32 s29, s9, 2688\n"
      "v_cvt_pk_bf16_f32 v157, v158, v159\n"
      "v_cvt_pk_bf16_f32 v158, v204, v205\n"
      "v_mfma_f32_16x16x32_bf16 v[224:227], a[0:3], v[230:233], 0\n"
      "s_add_u32 s28, s28, s29\n"
      "v_cvt_pk_bf16_f32 v248, v248, v249\n"
      "v_cvt_pk_bf16_f32 v159, v206, v207\n"
      "v_mfma_f32_16x16x32_bf16 v[224:227], a[36:39], v[168:171], v[224:227]\n"
      "s_waitcnt lgkmcnt(1)\n"
      "v_cvt_pk_bf16_f32 v249, v250, v251\n"
      "v_cvt_pk_bf16_f32 v250, v188, v189\n"
      "v_mfma_f32_16x16x32_bf16 v[204:207], a[4:7], v[230:233], 0\n"
      "s_cmp_eq_u32 s8, 72\n"
      "v_cvt_pk_bf16_f32 v251, v190, v191\n"
      "v_cvt_pk_f16_f32 v234, v234, v235\n"
      "v_mfma_f32_16x16x32_bf16 v[204:207], a[40:43], v[168:171], v[204:207]\n"
      "s_cselect_b32 s8, 0, 100270464\n"
      "v_pk_mul_f16 v238, v234, v234\n"
      "v_cvt_pk_f16_f32 v252, v252, v253\n"
      "v_mfma_f32_16x16x32_bf16 v[140:143], a[8:11], v[230:233], 0\n"
      "s_cmp_eq_u32 s9, 72\n"
      "v_pk_mul_f16 v239, v252, v252\n"
      "v_cvt_pk_f16_f32 v235, v236, v237\n"
      "v_mfma_f32_16x16x32_bf16 v[140:143], a[44:47], v[168:171], v[140:143]\n"
      "s_cselect_b32 s9, 0, 196224\n"
      "v_pk_fma_f16 v238, v235, v235, v238\n"
      "v_cvt_pk_f16_f32 v253, v254, v255\n"
      "v_mfma_f32_16x16x32_bf16 v[160:163], a[12:15], v[230:233], 0\n"
      "s_add_u32 s10, s8, s9\n"
      "v_pk_fma_f16 v239, v253, v253, v239\n"
      "v_cvt_pk_f16_f32 v236, v144, v145\n"
      "v_mfma_f32_16x16x32_bf16 v[160:163], a[48:51], v[168:171], v[160:163]\n"
      "s_add_u32 s28, s28, s10\n"
      "v_pk_fma_f16 v238, v236, v236, v238\n"
      "v_cvt_pk_f16_f32 v254, v152, v153\n"
      "v_mfma_f32_16x16x32_bf16 v[216:219], a[16:19], v[230:233], 0\n"
      "ds_read_b128 v[188:191], v114 offset:34816\n"
      "v_pk_fma_f16 v239, v254, v254, v239\n"
      "v_mfma_f32_16x16x32_bf16 v[216:219], a[52:55], v[168:171], v[216:219]\n"
      "s_add_u32 s54, s54, s28\n"
      "v_cvt_pk_f16_f32 v237, v146, v147\n"
      "v_pk_fma_f16 v238, v237, v237, v238\n"
      "v_mfma_f32_16x16x32_bf16 v[144:147], a[20:23], v[230:233], 0\n"
      "s_addc_u32 s55, s55, 0\n"
      "v_cvt_pk_f16_f32 v255, v154, v155\n"
      "v_pk_fma_f16 v239, v255, v255, v239\n"
      "v_mfma_f32_16x16x32_bf16 v[144:147], a[56:59], v[168:171], v[144:147]\n"
      "ds_read_b128 v[152:155], v115 offset:34816\n"
      "v_cvt_pk_f16_f32 v38, v148, v149\n"
      "v_mfma_f32_16x16x32_bf16 v[176:179], v[230:233], a[24:27], 0\n"
      "s_cmp_lg_u32 s10, s11\n"
      "v_cvt_pk_f16_f32 v40, v184, v185\n"
      "v_pk_fma_f16 v238, v38, v38, v238\n"
      "v_mfma_f32_16x16x32_bf16 v[176:179], v[168:171], a[60:63], v[176:179]\n"
      "s_cbranch_scc1 L_edge_17_%=\n"
      "L_eb_18_%=:\n"
      "v_mfma_f32_16x16x32_bf16 v[196:199], v[230:233], a[28:31], 0\n"
      "global_store_dwordx4 v116, v[164:167], s[54:55] sc1\n"
      "v_mfma_f32_16x16x32_bf16 v[196:199], v[168:171], a[64:67], v[196:199]\n"
      "ds_read_b128 v[200:203], v119 offset:12288\n"
      "v_pk_fma_f16 v239, v40, v40, v239\n"
      "v_mfma_f32_16x16x32_bf16 v[164:167], v[230:233], a[32:35], 0\n"
      "s_mov_b32 s5, s4\n"
      "v_cvt_pk_f16_f32 v39, v150, v151\n"
      "v_pk_fma_f16 v238, v39, v39, v238\n"
      "v_mfma_f32_16x16x32_bf16 v[164:167], v[168:171], a[68:71], v[164:167]\n"
      "s_add_u32 s4, s27, 1\n"
      "v_cvt_pk_f16_f32 v41, v186, v187\n"
      "v_pk_fma_f16 v239, v41, v41, v239\n"
      "v_mfma_f32_16x16x32_bf16 v[224:227], a[72:75], v[136:139], v[224:227]\n"
      "s_lshl_b32 s4, s4, 30\n"
      "v_permlane16_swap_b32_e64 v238, v239\n"
      "v_mfma_f32_16x16x32_bf16 v[224:227], a[108:111], v[240:243], v[224:227]\n"
      "s_waitcnt lgkmcnt(1)\n"
      "v_pk_add_f16 v238, v238, v239\n"
      "v_mov_b32 v239, v238\n"
      "v_mfma_f32_16x16x32_bf16 v[204:207], a[76:79], v[136:139], v[204:207]\n"
      "s_and_b32 s4, s4, 0x80000000\n"
      "v_permlane32_swap_b32 v238, v239\n"
      "v_mfma_f32_16x16x32_bf16 v[204:207], a[112:115], v[240:243], v[204:207]\n"
      "v_pk_add_f16 v238, v238, v239\n"
      "v_add_f16_sdwa v239, v238, v238 dst_sel:DWORD dst_unused:UNUSED_PAD src0_sel:WORD_0 src1_sel:WORD_1\n"
      "v_mfma_f32_16x16x32_bf16 v[140:143], a[80:83], v[136:139], v[140:143]\n"
      "s_nop 0\n"
      "v_rsq_f16 v238, v239\n"
      "v_mfma_f32_16x16x32_bf16 v[140:143], a[116:119], v[240:243], v[140:143]\n"
      "ds_read_b128 v[230:233], v132 offset:1200\n"
      "v_cmp_nle_f16 vcc, 0x1c00, v239\n"
      "v_mfma_f32_16x16x32_bf16 v[160:163], a[84:87], v[136:139], v[160:163]\n"
      "global_store_dwordx4 v116, v[180:183], s[54:55] offset:128 sc1\n"
      "v_mfma_f32_16x16x32_bf16 v[160:163], a[120:123], v[240:243], v[160:163]\n"
      "ds_read_b128 v[184:187], v133 offset:1168\n"
      "v_mfma_f32_16x16x32_bf16 v[216:219], a[88:91], v[136:139], v[216:219]\n"
      "ds_read_b64 v[168:169], v132 offset:1192\n"
      "s_or_b64 s[34:35], s[34:35], vcc\n"
      "v_mfma_f32_16x16x32_bf16 v[216:219], a[124:127], v[240:243], v[216:219]\n"
      "ds_read_b64 v[170:171], v133 offset:1184\n"
      "v_cmp_nge_f16 vcc, 0x7b00, v239\n"
      "v_mfma_f32_16x16x32_bf16 v[144:147], a[92:95], v[136:139], v[144:147]\n"
      "ds_read_u16 v52, v134 offset:1168\n"
      "v_mov_b32 v239, v238\n"
      "v_mfma_f32_16x16x32_bf16 v[144:147], a[128:131], v[240:243], v[144:147]\n"
      "ds_read_u16 v60, v134 offset:1232\n"
      "v_permlane16_swap_b32_e64 v238, v239\n"
      "v_mfma_f32_16x16x32_bf16 v[176:179], v[136:139], a[96:99], v[176:179]\n"
      "ds_read_u16 v56, v134 offset:1200\n"
      "v_pk_mul_f16 v252, v252, v239 op_sel_hi:[1,0]\n"
      "v_mfma_f32_16x16x32_bf16 v[176:179], v[240:243], a[132:135], v[176:179]\n"
      "ds_read_b128 v[148:151], v123\n"
      "v_pk_mul_f16 v254, v254, v239 op_sel_hi:[1,0]\n"
      "v_mfma_f32_16x16x32_bf16 v[196:199], v[136:139], a[100:103], v[196:199]\n"
      "v_pk_mul_f16 v255, v255, v239 op_sel_hi:[1,0]\n"
      "v_pk_mul_f16 v253, v253, v239 op_sel_hi:[1,0]\n"
      "v_mfma_f32_16x16x32_bf16 v[196:199], v[240:243], a[136:139], v[196:199]\n"
      "s_waitcnt lgkmcnt(7)\n"
      "v_pk_mul_f16 v41, v41, v239 op_sel_hi:[1,0]\n"
      "v_pk_mul_f16 v40, v40, v239 op_sel_hi:[1,0]\n"
      "v_mfma_f32_16x16x32_bf16 v[164:167], v[136:139], a[104:107], v[164:167]\n"
      "global_store_dwordx4 v116, v[200:203], s[54:55] offset:256 sc1\n"
      "v_mfma_f32_16x16x32_bf16 v[164:167], v[240:243], a[140:143], v[164:167]\n"
      "ds_read_b128 v[172:175], v123 offset:3072\n"
      "v_mul_f16 v238, s7, v238\n"
      "v_mfma_f32_16x16x32_bf16 v[224:227], a[144:147], v[188:191], v[224:227]\n"
      "ds_read_b128 v[180:183], v123 offset:9216\n"
      "v_pk_mul_f16 v234, v234, v238 op_sel_hi:[1,0]\n"
      "v_mfma_f32_16x16x32_bf16 v[224:227], a[180:183], v[152:155], v[224:227]\n"
      "ds_read_b128 v[200:203], v123 offset:6144\n"
      "v_pk_mul_f16 v235, v235, v238 op_sel_hi:[1,0]\n"
      "v_mfma_f32_16x16x32_bf16 v[204:207], a[148:151], v[188:191], v[204:207]\n"
      "v_pk_mul_f16 v237, v237, v238 op_sel_hi:[1,0]\n"
      "v_pk_mul_f16 v236, v236, v238 op_sel_hi:[1,0]\n"
      "v_mfma_f32_16x16x32_bf16 v[204:207], a[184:187], v[152:155], v[204:207]\n"
      "v_pk_mul_f16 v39, v39, v238 op_sel_hi:[1,0]\n"
      "v_pk_mul_f16 v38, v38, v238 op_sel_hi:[1,0]\n"
      "v_mfma_f32_16x16x32_bf16 v[140:143], a[152:155], v[188:191], v[140:143]\n"
      "s_or_b64 s[34:35], s[34:35], vcc\n"
      "v_cvt_pk_f16_f32 v224, v224, v225\n"
      "v_pk_mul_f16 v238, v224, v224\n"
      "v_mfma_f32_16x16x32_bf16 v[140:143], a[188:191], v[152:155], v[140:143]\n"
      "v_cvt_pk_f16_f32 v225, v226, v227\n"
      "v_pk_fma_f16 v238, v225, v225, v238\n"
      "v_mfma_f32_16x16x32_bf16 v[160:163], a[156:159], v[188:191], v[160:163]\n"
      "s_nop 0\n"
      "v_cvt_pk_f16_f32 v226, v204, v205\n"
      "v_pk_fma_f16 v238, v226, v226, v238\n"
      "v_mfma_f32_16x16x32_bf16 v[160:163], a[192:195], v[152:155], v[160:163]\n"
      "v_cvt_pk_f16_f32 v227, v206, v207\n"
      "v_pk_fma_f16 v238, v227, v227, v238\n"
      "v_mfma_f32_16x16x32_bf16 v[216:219], a[160:163], v[188:191], v[216:219]\n"
      "s_waitcnt lgkmcnt(2)\n"
      "v_cvt_pk_f16_f32 v44, v140, v141\n"
      "v_pk_fma_f16 v238, v44, v44, v238\n"
      "v_mfma_f32_16x16x32_bf16 v[216:219], a[196:199], v[152:155], v[216:219]\n"
      "s_nop 0\n"
      "v_cvt_pk_f16_f32 v160, v160, v161\n"
      "v_pk_mul_f16 v239, v160, v160\n"
      "v_mfma_f32_16x16x32_bf16 v[144:147], a[164:167], v[188:191], v[144:147]\n"
      "v_cvt_pk_f16_f32 v161, v162, v163\n"
      "v_pk_fma_f16 v239, v161, v161, v239\n"
      "v_mfma_f32_16x16x32_bf16 v[144:147], a[200:203], v[152:155], v[144:147]\n"
      "s_waitcnt lgkmcnt(0)\n"
      "v_cvt_pk_f16_f32 v162, v216, v217\n"
      "v_pk_fma_f16 v239, v162, v162, v239\n"
      "v_mfma_f32_16x16x32_bf16 v[176:179], v[188:191], a[168:171], v[176:179]\n"
      "ds_read_b128 v[204:207], v123 offset:12288\n"
      "v_cvt_pk_f16_f32 v163, v218, v219\n"
      "v_mfma_f32_16x16x32_bf16 v[176:179], v[152:155], a[204:207], v[176:179]\n"
      "s_nop 0\n"
      "v_cvt_pk_f16_f32 v46, v144, v145\n"
      "v_pk_fma_f16 v239, v163, v163, v239\n"
      "v_mfma_f32_16x16x32_bf16 v[196:199], v[188:191], a[172:175], v[196:199]\n"
      "ds_read_b128 v[216:219], v123 offset:15360\n"
      "v_pk_fma_f16 v239, v46, v46, v239\n"
      "v_mfma_f32_16x16x32_bf16 v[196:199], v[152:155], a[208:211], v[196:199]\n"
      "v_cvt_pk_f16_f32 v45, v142, v143\n"
      "v_pk_fma_f16 v238, v45, v45, v238\n"
      "v_mfma_f32_16x16x32_bf16 v[164:167], v[188:191], a[176:179], v[164:167]\n"
      "v_cvt_pk_f16_f32 v47, v146, v147\n"
      "v_pk_fma_f16 v239, v47, v47, v239\n"
      "v_mfma_f32_16x16x32_bf16 v[164:167], v[152:155], a[212:215], v[164:167]\n"
      "s_nop 0\n"
      "v_permlane16_swap_b32 v238, v239\n"
      "v_mfma_f32_16x16x32_bf16 v[136:139], a[216:219], v[148:151], 0\n"
      "v_pk_add_f16 v238, v238, v239\n"
      "v_mov_b32_e64 v239, v238\n"
      "v_mfma_f32_16x16x32_bf16 v[136:139], a[228:231], v[172:175], v[136:139]\n"
      "s_nop 0\n"
      "v_permlane32_swap_b32_e64 v238, v239\n"
      "v_mfma_f32_16x16x32_bf16 v[152:155], a[220:223], v[148:151], 0\n"
      "s_nop 0\n"
      "v_pk_add_f16 v238, v238, v239\n"
      "v_add_f16_sdwa v239, v238, v238 dst_sel:DWORD dst_unused:UNUSED_PAD src0_sel:WORD_0 src1_sel:WORD_1\n"
      "v_mfma_f32_16x16x32_bf16 v[152:155], a[232:235], v[172:175], v[152:155]\n"
      "v_rsq_f16 v238, v239\n"
      "v_mfma_f32_16x16x32_bf16 v[240:243], a[224:227], v[148:151], 0\n"
      "s_nop 0\n"
      "v_cmp_nle_f16 vcc, 0x1c00, v239\n"
      "s_waitcnt lgkmcnt(0)\n"
      "v_mfma_f32_16x16x32_bf16 v[240:243], a[236:239], v[172:175], v[240:243]\n"
      "s_or_b64 s[34:35], s[34:35], vcc\n"
      "v_cmp_nge_f16 vcc, 0x7b00, v239\n"
      "v_mov_b32 v239, v238\n"
      "v_mfma_f32_16x16x32_bf16 v[136:139], a[240:243], v[200:203], v[136:139]\n"
      "s_or_b64 s[34:35], s[34:35], vcc\n"
      "v_permlane16_swap_b32_e64 v238, v239\n"
      "v_mfma_f32_16x16x32_bf16 v[136:139], a[252:255], v[180:183], v[136:139]\n"
      "s_or_b64 vcc, s[48:49], s[50:51]\n"
      "v_pk_mul_f16 v161, v161, v239 op_sel_hi:[1,0]\n"
      "v_pk_mul_f16 v160, v160, v239 op_sel_hi:[1,0]\n"
      "v_mfma_f32_16x16x32_bf16 v[152:155], a[244:247], v[200:203], v[152:155]\n"
      "s_nop 0\n"
      "v_pk_mul_f16 v162, v162, v239 op_sel_hi:[1,0]\n"
      "v_pk_mul_f16 v46, v46, v239 op_sel_hi:[1,0]\n"
      "v_mfma_f32_16x16x32_bf16 v[152:155], v[0:3], v[180:183], v[152:155]\n"
      "v_pk_mul_f16 v163, v163, v239 op_sel_hi:[1,0]\n"
      "v_add_u32 v132, 2336, v132\n"
      "v_mfma_f32_16x16x32_bf16 v[240:243], a[248:251], v[200:203], v[240:243]\n"
      "v_pk_mul_f16 v47, v47, v239 op_sel_hi:[1,0]\n"
      "v_add_u32 v133, 2336, v133\n"
      "v_mfma_f32_16x16x32_bf16 v[240:243], v[4:7], v[180:183], v[240:243]\n"
      "s_nop 0\n"
      "v_add_u32 v134, 2336, v134\n"
      "v_mul_f16 v238, s7, v238\n"
      "v_mfma_f32_16x16x32_bf16 v[136:139], v[8:11], v[204:207], v[136:139]\n"
      "s_or_b64 vcc, vcc, s[34:35]\n"
      "v_pk_mul_f16 v226, v226, v238 op_sel_hi:[1,0]\n"
      "v_pk_mul_f16 v224, v224, v238 op_sel_hi:[1,0]\n"
      "v_mfma_f32_16x16x32_bf16 v[136:139], v[20:23], v[216:219], v[136:139]\n"
      "s_waitcnt vmcnt(3)\n"
      "v_pk_mul_f16 v225, v225, v238 op_sel_hi:[1,0]\n"
      "v_pk_mul_f16 v45, v45, v238 op_sel_hi:[1,0]\n"
      "v_mfma_f32_16x16x32_bf16 v[152:155], v[12:15], v[204:207], v[152:155]\n"
      "ds_read_b32 v239, v122\n"
      "v_pk_mul_f16 v227, v227, v238 op_sel_hi:[1,0]\n"
      "v_mfma_f32_16x16x32_bf16 v[152:155], v[24:27], v[216:219], v[152:155]\n"
      "ds_read_b128 v[140:143], v119 offset:36864\n"
      "v_pk_mul_f16 v44, v44, v238 op_sel_hi:[1,0]\n"
      "v_mfma_f32_16x16x32_bf16 v[240:243], v[16:19], v[204:207], v[240:243]\n"
      "v_cvt_pk_bf16_f32 v51, v178, v179\n"
      "v_cvt_pk_bf16_f32 v55, v198, v199\n"
      "v_mfma_f32_16x16x32_bf16 v[240:243], v[28:31], v[216:219], v[240:243]\n"
      "v_cvt_pk_bf16_f32 v50, v176, v177\n"
      "v_cvt_pk_bf16_f32 v54, v196, v197\n"
      "v_mfma_f32_16x16x32_f16 v[188:191], v[212:215], v[208:211], v[78:81]\n"
      "v_fma_f32 v180, v136, v136, s39\n"
      "v_cvt_pk_bf16_f32 v58, v164, v165\n"
      "v_mfma_f32_16x16x32_f16 v[188:191], v[34:37], v[32:35], v[188:191]\n"
      "v_fmac_f32 v180, v137, v137\n"
      "v_fmac_f32 v180, v138, v138\n"
      "v_mfma_f32_16x16x32_f16 v[148:151], v[252:255], v[208:211], v[82:85]\n"
      "v_fmac_f32 v180, v139, v139\n"
      "v_cvt_pk_bf16_f32 v59, v166, v167\n"
      "v_mfma_f32_16x16x32_f16 v[148:151], v[40:43], v[32:35], v[148:151]\n"
      "v_fmac_f32 v180, v152, v152\n"
      "v_fmac_f32 v180, v153, v153\n"
      "v_fmac_f32 v180, v154, v154\n"
      "v_mfma_f32_16x16x32_f16 v[144:147], v[230:233], v[208:211], v[70:73]\n"
      "v_fmac_f32 v180, v155, v155\n"
      "v_fmac_f32 v180, v240, v240\n"
      "v_mfma_f32_16x16x32_f16 v[144:147], v[184:187], v[212:215], v[144:147]\n"
      "s_waitcnt lgkmcnt(0)\n"
      "v_fmac_f32 v180, v241, v241\n"
      "v_fmac_f32 v180, v242, v242\n"
      "v_mfma_f32_16x16x32_f16 v[164:167], v[160:163], v[208:211], v[86:89]\n"
      "v_fmac_f32 v180, v243, v243\n"
      "v_or3_b32 v140, v140, v141, v142\n"
      "v_mfma_f32_16x16x32_f16 v[164:167], v[46:49], v[32:35], v[164:167]\n"
      "v_bitop3_b32 v239, v239, v143, s40 bitop3:0xa8\n"
      "v_and_b32 v140, s40, v140\n"
      "v_or3_b32 v140, v140, v239, s4\n"
      "v_mfma_f32_16x16x32_f16 v[144:147], v[168:171], v[32:35], v[144:147]\n"
      "ds_write_b32 v129, v140 offset:2048\n"
      "s_cbranch_scc1 L_gen_19_%=\n"
      "L_fast_22_%=:\n"
      "v_mfma_f32_16x16x32_f16 v[204:207], v[212:215], v[234:237], v[90:93]\n"
      "ds_read_b128 v[140:143], v120\n"
      "ds_read_b128 v[176:179], v123 offset:1024\n"
      "v_mfma_f32_16x16x32_f16 v[204:207], v[34:37], v[38:41], v[204:207]\n"
      "ds_read_b128 v[216:219], v123 offset:4096\n"
      "v_exp_f32 v188, v188\n"
      "v_exp_f32 v148, v148\n"
      "v_mfma_f32_16x16x32_f16 v[172:175], v[252:255], v[234:237], v[94:97]\n"
      "v_exp_f32 v150, v150\n"
      "v_mfma_f32_16x16x32_f16 v[172:175], v[40:43], v[38:41], v[172:175]\n"
      "ds_read_b128 v[208:211], v128 offset:2048\n"
      "v_exp_f32 v190, v190\n"
      "v_exp_f32_e64 v149, v149\n"
      "v_mfma_f32_16x16x32_f16 v[200:203], v[230:233], v[234:237], v[72:75]\n"
      "v_exp_f32 v189, v189\n"
      "v_mfma_f32_16x16x32_f16 v[200:203], v[184:187], v[252:255], v[200:203]\n"
      "v_exp_f32 v191, v191\n"
      "v_mfma_f32_16x16x32_f16 v[200:203], v[168:171], v[38:41], v[200:203]\n"
      "ds_read_b128 v[196:199], v123 offset:7168\n"
      "v_exp_f32 v151, v151\n"
      "v_cvt_pk_bf16_f32 v188, v188, v189\n"
      "v_cvt_pk_bf16_f32 v189, v190, v191\n"
      "v_cvt_pk_bf16_f32 v191, v150, v151\n"
      "v_mfma_f32_16x16x32_f16 v[192:195], v[160:163], v[234:237], v[98:101]\n"
      "s_waitcnt lgkmcnt(2)\n"
      "ds_read_b128 v[220:223], v123 offset:10240\n"
      "v_mfma_f32_16x16x32_f16 v[192:195], v[46:49], v[38:41], v[192:195]\n"
      "global_store_dwordx4 v117, v[140:143], s[54:55] sc1\n"
      "v_exp_f32 v164, v164\n"
      "v_mfma_f32_16x16x32_bf16 v[236:239], a[216:219], v[176:179], 0\n"
      "v_exp_f32 v166, v166\n"
      "v_mfma_f32_16x16x32_bf16 v[236:239], a[228:231], v[216:219], v[236:239]\n"
      "s_waitcnt lgkmcnt(2)\n"
      "v_exp_f32 v144, v144\n"
      "v_exp_f32 v165, v165\n"
      "v_bitop3_b32 v210, v210, v211, s4 bitop3:0x7e\n"
      "v_mfma_f32_16x16x32_bf16 v[140:143], a[220:223], v[176:179], 0\n"
      "v_exp_f32 v167, v167\n"
      "v_mfma_f32_16x16x32_bf16 v[140:143], a[232:235], v[216:219], v[140:143]\n"
      "v_cvt_pk_bf16_f32 v190, v148, v149\n"
      "v_bitop3_b32 v208, v208, v209, s4 bitop3:0x7e\n"
      "v_mfma_f32_16x16x32_bf16 v[148:151], a[224:227], v[176:179], 0\n"
      "v_cvt_pk_bf16_f32 v62, v164, v165\n"
      "v_cvt_pk_bf16_f32 v63, v166, v167\n"
      "v_mfma_f32_16x16x32_bf16 v[148:151], a[236:239], v[216:219], v[148:151]\n"
      "ds_read_b128 v[164:167], v123 offset:13312\n"
      "s_waitcnt lgkmcnt(1)\n"
      "v_mfma_f32_16x16x32_bf16 v[236:239], a[240:243], v[196:199], v[236:239]\n"
      "v_or_b32_e64 v208, v208, v210\n"
      "v_cmp_ne_u32 vcc, 0, v208\n"
      "v_mfma_f32_16x16x32_bf16 v[236:239], a[252:255], v[220:223], v[236:239]\n"
      "ds_read_b128 v[176:179], v123 offset:16384\n"
      "v_exp_f32 v204, v204\n"
      "v_mfma_f32_16x16x32_bf16 v[140:143], a[244:247], v[196:199], v[140:143]\n"
      "v_exp_f32 v205, v205\n"
      "v_mfma_f32_16x16x32_bf16 v[140:143], v[0:3], v[220:223], v[140:143]\n"
      "ds_read_b128 v[216:219], v121\n"
      "v_cvt_pk_bf16_f32 v204, v204, v205\n"
      "v_exp_f32 v206, v206\n"
      "v_exp_f32 v207, v207\n"
      "v_exp_f32_e64 v172, v172\n"
      "v_mfma_f32_16x16x32_bf16 v[148:151], a[248:251], v[196:199], v[148:151]\n"
      "s_mov_b64 s[48:49], vcc\n"
      "v_exp_f32 v174, v174\n"
      "v_mfma_f32_16x16x32_bf16 v[148:151], v[4:7], v[220:223], v[148:151]\n"
      "s_cbranch_vccnz L_flg_23_%=\n"
      "L_fb_24_%=:\n"
      "v_exp_f32 v173, v173\n"
      "v_cvt_pk_bf16_f32 v205, v206, v207\n"
      "v_exp_f32 v175, v175\n"
      "v_cvt_pk_bf16_f32 v206, v172, v173\n"
      "s_waitcnt lgkmcnt(1)\n"
      "v_mfma_f32_16x16x32_bf16 v[236:239], v[8:11], v[164:167], v[236:239]\n"
      "ds_read_b128 v[208:211], v123 offset:5120\n"
      "v_cvt_pk_bf16_f32 v207, v174, v175\n"
      "v_mfma_f32_16x16x32_bf16 v[236:239], v[20:23], v[176:179], v[236:239]\n"
      "ds_read_b128 v[196:199], v123 offset:2048\n"
      "v_exp_f32 v192, v192\n"
      "v_mfma_f32_16x16x32_bf16 v[140:143], v[12:15], v[164:167], v[140:143]\n"
      "s_waitcnt lgkmcnt(2)\n"
      "v_exp_f32 v193, v193\n"
      "v_mfma_f32_16x16x32_bf16 v[140:143], v[24:27], v[176:179], v[140:143]\n"
      "s_mov_b32 exec_hi, 0\n"
      "global_store_dwordx4 v118, v[216:219], s[54:55] sc1\n"
      "s_mov_b32 exec_hi, -1\n"
      "v_cvt_pk_bf16_f32 v64, v144, 0\n"
      "v_mfma_f32_16x16x32_bf16 v[148:151], v[16:19], v[164:167], v[148:151]\n"
      "v_exp_f32 v194, v194\n"
      "v_mfma_f32_16x16x32_bf16 v[148:151], v[28:31], v[176:179], v[148:151]\n"
      "v_exp_f32 v200, v200\n"
      "v_mfma_f32_16x16x32_bf16 v[176:179], v[66:69], v[188:191], 0\n"
      "v_exp_f32 v195, v195\n"
      "v_mfma_f32_16x16x32_bf16 v[176:179], v[66:69], v[62:65], v[176:179]\n"
      "v_fma_f32 v181, v236, v236, s39\n"
      "v_fmac_f32 v181, v237, v237\n"
      "v_mfma_f32_16x16x32_bf16 v[164:167], v[244:247], v[188:191], 0\n"
      "v_fmac_f32 v181, v238, v238\n"
      "v_fmac_f32_e64 v181, v239, v239\n"
      "v_mfma_f32_16x16x32_bf16 v[164:167], v[50:53], v[62:65], v[164:167]\n"
      "v_fmac_f32 v181, v140, v140\n"
      "v_fmac_f32 v181, v141, v141\n"
      "v_mfma_f32_16x16x32_bf16 v[220:223], v[156:159], v[188:191], 0\n"
      "v_fmac_f32 v181, v142, v142\n"
      "v_fmac_f32 v181, v143, v143\n"
      "v_mfma_f32_16x16x32_bf16 v[220:223], v[54:57], v[62:65], v[220:223]\n"
      "v_fmac_f32 v181, v148, v148\n"
      "v_fmac_f32 v181, v149, v149\n"
      "v_mfma_f32_16x16x32_bf16 v[216:219], v[248:251], v[188:191], 0\n"
      "v_fmac_f32 v181, v150, v150\n"
      "v_fmac_f32 v181, v151, v151\n"
      "v_mfma_f32_16x16x32_bf16 v[216:219], v[58:61], v[62:65], v[216:219]\n"
      "v_cvt_pk_bf16_f32 v64, v200, 0\n"
      "v_cvt_pk_bf16_f32 v62, v192, v193\n"
      "v_mfma_f32_16x16x32_f16 v[172:175], v[212:215], v[224:227], v[102:105]\n"
      "s_add_u32 s28, s27, 2\n"
      "v_cvt_pk_bf16_f32 v63, v194, v195\n"
      "v_cndmask_b32 v145, v145, v201, s[42:43]\n"
      "v_mfma_f32_16x16x32_f16 v[172:175], v[34:37], v[44:47], v[172:175]\n"
      "v_rcp_f32 v176, v176\n"
      "v_mfma_f32_16x16x32_f16 v[188:191], v[252:255], v[224:227], v[106:109]\n"
      "s_waitcnt lgkmcnt(0)\n"
      "v_mul_f32 v165, v176, v165\n"
      "v_mul_f32 v164, v176, v164\n"
      "v_mfma_f32_16x16x32_f16 v[188:191], v[40:43], v[44:47], v[188:191]\n"
      "v_mul_f32 v167, v176, v167\n"
      "v_cvt_pk_bf16_f32 v164, v164, v165\n"
      "v_mfma_f32_16x16x32_f16 v[212:215], v[230:233], v[224:227], v[74:77]\n"
      "ds_read_b128 v[252:255], v123 offset:8192\n"
      "v_mul_f32_e64 v166, v176, v166\n"
      "v_mfma_f32_16x16x32_f16 v[212:215], v[184:187], v[160:163], v[212:215]\n"
      "v_mul_f32 v220, v176, v220\n"
      "v_cvt_pk_bf16_f32 v165, v166, v167\n"
      "v_mfma_f32_16x16x32_f16 v[212:215], v[168:171], v[44:47], v[212:215]\n"
      "v_mul_f32 v223, v176, v223\n"
      "v_mul_f32 v222, v176, v222\n"
      "v_mfma_f32_16x16x32_f16 v[192:195], v[160:163], v[224:227], v[110:113]\n"
      "ds_read_b128 v[160:163], v123 offset:11264\n"
      "v_mul_f32 v221, v176, v221\n"
      "v_mfma_f32_16x16x32_f16 v[192:195], v[46:49], v[44:47], v[192:195]\n"
      "s_min_u32 m0, s28, s3\n"
      "v_mul_f32 v218, v176, v218\n"
      "v_cvt_pk_bf16_f32 v167, v222, v223\n"
      "v_cvt_pk_bf16_f32 v166, v220, v221\n"
      "v_mfma_f32_16x16x32_f16 v[224:227], v[184:187], v[230:233], v[76:79]\n"
      "ds_write_b128 v124, v[164:167] offset:18432\n"
      "v_mfma_f32_16x16x32_bf16 v[184:187], v[66:69], v[204:207], 0\n"
      "ds_read_b128 v[228:231], v123 offset:14336\n"
      "v_mul_f32_e64 v216, v176, v216\n"
      "v_mfma_f32_16x16x32_bf16 v[184:187], v[66:69], v[62:65], v[184:187]\n"
      "v_exp_f32 v188, v188\n"
      "v_mfma_f32_16x16x16_f16 v[224:227], v[170:171], v[168:169], v[224:227]\n"
      "s_movrels_b32 s9, s80\n"
      "v_mul_f32_e64 v217, v176, v217\n"
      "v_cvt_pk_bf16_f32 v216, v216, v217\n"
      "v_mul_f32 v219, v176, v219\n"
      "v_exp_f32 v192, v192\n"
      "v_mfma_f32_16x16x32_bf16 v[168:171], v[244:247], v[204:207], 0\n"
      "v_cvt_pk_bf16_f32 v217, v218, v219\n"
      "v_cndmask_b32 v145, v145, v213, s[44:45]\n"
      "v_mfma_f32_16x16x32_bf16 v[168:171], v[50:53], v[62:65], v[168:171]\n"
      "v_cndmask_b32 v145, v145, v225, s[46:47]\n"
      "v_exp_f32_e64 v145, v145\n"
      "v_mfma_f32_16x16x32_bf16 v[164:167], v[156:159], v[204:207], 0\n"
      "s_waitcnt lgkmcnt(2)\n"
      "s_lshr_b32 s8, s9, 8\n"
      "v_cvt_pk_bf16_f32 v145, v145, v145\n"
      "v_mfma_f32_16x16x32_bf16 v[164:167], v[54:57], v[62:65], v[164:167]\n"
      "ds_write_b16 v130, v145\n"
      "v_mfma_f32_16x16x32_bf16 v[144:147], v[248:251], v[204:207], 0\n"
      "s_and_b32 s9, s9, 0xff\n"
      "ds_read_b128 v[204:207], v131\n"
      "v_mfma_f32_16x16x32_bf16 v[144:147], v[58:61], v[62:65], v[144:147]\n"
      "ds_read_b128 v[176:179], v131 offset:64\n"
      "v_exp_f32_e64 v174, v174\n"
      "v_mfma_f32_16x16x32_bf16 v[232:235], a[216:219], v[196:199], 0\n"
      "s_mul_i32 s10, s8, 73\n"
      "v_exp_f32 v172, v172\n"
      "v_mfma_f32_16x16x32_bf16 v[232:235], a[228:231], v[208:211], v[232:235]\n"
      "s_add_u32 s10, s10, s9\n"
      "v_exp_f32_e64 v173, v173\n"
      "v_mfma_f32_16x16x32_bf16 v[224:227], a[220:223], v[196:199], 0\n"
      "s_mul_i32 s28, s10, 4802\n"
      "v_exp_f32 v175, v175\n"
      "v_mfma_f32_16x16x32_bf16 v[224:227], a[232:235], v[208:211], v[224:227]\n"
      "s_add_u32 s56, s18, s28\n"
      "v_exp_f32 v191, v191\n"
      "v_mfma_f32_16x16x32_bf16 v[232:235], a[240:243], v[252:255], v[232:235]\n"
      "s_addc_u32 s57, s19, 0\n"
      "v_cvt_pk_bf16_f32 v172, v172, v173\n"
      "v_cvt_pk_bf16_f32 v173, v174, v175\n"
      "v_mfma_f32_16x16x32_bf16 v[232:235], a[252:255], v[160:163], v[232:235]\n"
      "s_and_b32 s56, s56, -16\n"
      "v_exp_f32 v189, v189\n"
      "v_exp_f32_e64 v193, v193\n"
      "v_mfma_f32_16x16x32_bf16 v[224:227], a[244:247], v[252:255], v[224:227]\n"
      "s_sub_u32 s58, s56, 110592\n"
      "v_exp_f32 v190, v190\n"
      "v_mfma_f32_16x16x32_bf16 v[224:227], v[0:3], v[160:163], v[224:227]\n"
      "s_waitcnt lgkmcnt(0)\n"
      "v_cvt_pk_bf16_f32 v175, v190, v191\n"
      "v_cvt_pk_bf16_f32 v174, v188, v189\n"
      "v_mfma_f32_16x16x32_bf16 v[188:191], a[224:227], v[196:199], 0\n"
      "ds_read_b128 v[196:199], v123 offset:17408\n"
      "v_cvt_pk_bf16_f32 v62, v192, v193\n"
      "v_mfma_f32_16x16x32_bf16 v[188:191], a[236:239], v[208:211], v[188:191]\n"
      "ds_read_b128 v[208:211], v115\n"
      "v_exp_f32_e64 v194, v194\n"
      "v_mfma_f32_16x16x32_bf16 v[188:191], a[248:251], v[252:255], v[188:191]\n"
      "ds_read_b128 v[252:255], v114\n"
      "s_subb_u32 s59, s57, 0\n"
      "v_mfma_f32_16x16x32_bf16 v[188:191], v[4:7], v[160:163], v[188:191]\n"
      "s_sub_u32 s56, s56, 73728\n"
      "v_exp_f32 v195, v195\n"
      "v_mfma_f32_16x16x32_bf16 v[160:163], v[204:207], v[66:69], 0\n"
      "s_subb_u32 s57, s57, 0\n"
      "v_exp_f32 v212, v212\n"
      "v_mfma_f32_16x16x32_bf16 v[160:163], v[176:179], v[66:69], v[160:163]\n"
      "s_mul_i32 s28, s8, 1373568\n"
      "v_cvt_pk_bf16_f32 v64, v212, 0\n"
      "v_cvt_pk_bf16_f32 v63, v194, v195\n"
      "v_mfma_f32_16x16x32_bf16 v[218:221], v[204:207], v[244:247], 0\n"
      "s_mul_i32 s29, s9, 2688\n"
      "v_rcp_f32 v184, v184\n"
      "v_mfma_f32_16x16x32_bf16 v[218:221], v[176:179], v[50:53], v[218:221]\n"
      "s_add_u32 s28, s28, s29\n"
      "v_mul_f32 v169, v184, v169\n"
      "v_mul_f32 v168, v184, v168\n"
      "v_mfma_f32_16x16x32_bf16 v[212:215], v[66:69], v[172:175], 0\n"
      "s_waitcnt lgkmcnt(2)\n"
      "v_cvt_pk_bf16_f32 v168, v168, v169\n"
      "v_mul_f32 v171, v184, v171\n"
      "v_mfma_f32_16x16x32_bf16 v[212:215], v[66:69], v[62:65], v[212:215]\n"
      "s_cmp_eq_u32 s8, 72\n"
      "v_mul_f32 v170, v184, v170\n"
      "v_cvt_pk_bf16_f32 v169, v170, v171\n"
      "v_mfma_f32_16x16x32_bf16 v[232:235], v[8:11], v[228:231], v[232:235]\n"
      "v_mul_f32 v166, v184, v166\n"
      "v_mul_f32_e64 v165, v184, v165\n"
      "v_mfma_f32_16x16x32_bf16 v[232:235], v[20:23], v[196:199], v[232:235]\n"
      "s_cselect_b32 s8, 0, 100270464\n"
      "v_mul_f32 v164, v184, v164\n"
      "s_waitcnt lgkmcnt(0)\n"
      "v_mfma_f32_16x16x32_bf16 v[224:227], v[12:15], v[228:231], v[224:227]\n"
      "s_cmp_eq_u32 s9, 72\n"
      "v_cvt_pk_bf16_f32 v170, v164, v165\n"
      "v_mul_f32_e64 v167, v184, v167\n"
      "v_mfma_f32_16x16x32_bf16 v[224:227], v[24:27], v[196:199], v[224:227]\n"
      "s_cselect_b32 s9, 0, 196224\n"
      "v_cvt_pk_bf16_f32 v171, v166, v167\n"
      "v_mul_f32 v145, v184, v145\n"
      "v_mfma_f32_16x16x32_bf16 v[188:191], v[16:19], v[228:231], v[188:191]\n"
      "s_add_u32 s10, s8, s9\n"
      "v_mul_f32_e64 v144, v184, v144\n"
      "v_cvt_pk_bf16_f32 v144, v144, v145\n"
      "v_mfma_f32_16x16x32_bf16 v[188:191], v[28:31], v[196:199], v[188:191]\n"
      "ds_write_b128 v124, v[168:171] offset:19456\n"
      "v_mfma_f32_16x16x32_bf16 v[168:171], v[244:247], v[172:175], 0\n"
      "ds_read_b128 v[164:167], v114 offset:6144\n"
      "v_mul_f32_e64 v146, v184, v146\n"
      "v_mfma_f32_16x16x32_bf16 v[168:171], v[50:53], v[62:65], v[168:171]\n"
      "ds_read_b128 v[196:199], v115 offset:6144\n"
      "v_mul_f32 v147, v184, v147\n"
      "v_mfma_f32_16x16x32_bf16 v[228:231], v[156:159], v[172:175], 0\n"
      "s_add_u32 s28, s28, s10\n"
      "v_cvt_pk_bf16_f32 v145, v146, v147\n"
      "v_fma_f32 v219, v232, v232, s39\n"
      "v_mfma_f32_16x16x32_bf16 v[228:231], v[54:57], v[62:65], v[228:231]\n"
      "ds_write2st64_b64 v125, v[216:217], v[144:145] offset0:36 offset1:38\n"
      "v_mfma_f32_16x16x32_bf16 v[144:147], v[248:251], v[172:175], 0\n"
      "s_add_u32 s52, s16, s28\n"
      "v_fmac_f32 v219, v233, v233\n"
      "v_fmac_f32 v219, v234, v234\n"
      "v_mfma_f32_16x16x32_bf16 v[144:147], v[58:61], v[62:65], v[144:147]\n"
      "s_addc_u32 s53, s17, 0\n"
      "v_fmac_f32 v219, v235, v235\n"
      "v_fmac_f32 v219, v224, v224\n"
      "v_mfma_f32_16x16x32_bf16 v[184:187], v[204:207], v[156:159], 0\n"
      "s_add_u32 s27, s27, 1\n"
      "v_fmac_f32 v219, v225, v225\n"
      "v_fmac_f32 v219, v226, v226\n"
      "v_mfma_f32_16x16x32_bf16 v[184:187], v[176:179], v[54:57], v[184:187]\n"
      "s_cmp_ge_u32 s27, s26\n"
      "v_fmac_f32 v219, v227, v227\n"
      "v_fmac_f32 v219, v188, v188\n"
      "v_mfma_f32_16x16x32_bf16 v[244:247], v[204:207], v[248:251], 0\n"
      "s_cselect_b32 s28, 1, 0\n"
      "v_fmac_f32 v219, v189, v189\n"
      "v_fmac_f32_e64 v219, v190, v190\n"
      "v_mfma_f32_16x16x32_bf16 v[244:247], v[176:179], v[58:61], v[244:247]\n"
      "v_fmac_f32 v219, v191, v191\n"
      "v_mov_b32 v216, v219\n"
      "v_mfma_f32_16x16x32_bf16 v[176:179], a[0:3], v[252:255], 0\n"
      "s_waitcnt lgkmcnt(1)\n"
      "v_permlane16_swap_b32 v180, v181\n"
      "v_mfma_f32_16x16x32_bf16 v[176:179], a[36:39], v[208:211], v[176:179]\n"
      "v_mov_b32_dpp v160, v160 quad_perm:[0,0,0,0] row_mask:0xf bank_mask:0xf\n"
      "v_add_f32_e64 v180, v180, v181\n"
      "v_mfma_f32_16x16x32_bf16 v[204:207], a[4:7], v[252:255], 0\n"
      "v_permlane16_swap_b32_e64 v219, v216\n"
      "v_mfma_f32_16x16x32_bf16 v[204:207], a[40:43], v[208:211], v[204:207]\n"
      "v_cndmask_b32 v218, v218, v184, s[42:43]\n"
      "v_add_f32_e64 v219, v219, v216\n"
      "v_mfma_f32_16x16x32_bf16 v[172:175], a[8:11], v[252:255], 0\n"
      "s_xor_b32 s29, s10, s11\n"
      "v_permlane32_swap_b32 v180, v219\n"
      "v_mfma_f32_16x16x32_bf16 v[172:175], a[44:47], v[208:211], v[172:175]\n"
      "v_cndmask_b32 v218, v218, v244, s[44:45]\n"
      "v_add_f32 v180, v180, v219\n"
      "v_mfma_f32_16x16x32_bf16 v[184:187], a[12:15], v[252:255], 0\n"
      "s_or_b32 s28, s28, s29\n"
      "v_rcp_f32_e64 v160, v160\n"
      "v_mfma_f32_16x16x32_bf16 v[184:187], a[48:51], v[208:211], v[184:187]\n"
      "ds_write_b32 v129, v180 offset:1024\n"
      "v_mfma_f32_16x16x32_bf16 v[180:183], a[16:19], v[252:255], 0\n"
      "v_mul_f32_e64 v218, v160, v218\n"
      "v_cvt_pk_bf16_f32 v218, v218, v218\n"
      "v_mfma_f32_16x16x32_bf16 v[180:183], a[52:55], v[208:211], v[180:183]\n"
      "ds_write_b16 v135, v218 offset:16\n"
      "v_mfma_f32_16x16x32_bf16 v[160:163], a[20:23], v[252:255], 0\n"
      "v_mfma_f32_16x16x32_bf16 v[160:163], a[56:59], v[208:211], v[160:163]\n"
      "L_join_21_%=:\n"
      "s_cbranch_scc1 L_tail1_%=\n"
      "L_mid_%=:\n"
      "v_mfma_f32_16x16x32_bf16 v[216:219], v[252:255], a[24:27], 0\n"
      "s_add_u32 m0, s76, 18432\n"
      "v_rcp_f32 v212, v212\n"
      "v_mfma_f32_16x16x32_bf16 v[216:219], v[208:211], a[60:63], v[216:219]\n"
      "s_waitcnt lgkmcnt(0)\n"
      "s_barrier\n"
      "v_mfma_f32_16x16x32_bf16 v[248:251], v[252:255], a[28:31], 0\n"
      "v_mul_f32 v168, v212, v168\n"
      "v_mul_f32 v169, v212, v169\n"
      "v_mfma_f32_16x16x32_bf16 v[248:251], v[208:211], a[64:67], v[248:251]\n"
      "v_mul_f32 v170, v212, v170\n"
      "v_cvt_pk_bf16_f32 v168, v168, v169\n"
      "v_mfma_f32_16x16x32_bf16 v[192:195], v[252:255], a[32:35], 0\n"
      "ds_read_b128 v[252:255], v128 offset:1024\n"
      "v_mul_f32 v171, v212, v171\n"
      "v_mfma_f32_16x16x32_bf16 v[192:195], v[208:211], a[68:71], v[192:195]\n"
      "global_load_lds_dwordx4 v116, s[52:53]\n"
      "v_mfma_f32_16x16x32_bf16 v[176:179], a[72:75], v[164:167], v[176:179]\n"
      "ds_read_b128 v[208:211], v114 offset:12288\n"
      "v_mul_f32 v228, v212, v228\n"
      "v_mfma_f32_16x16x32_bf16 v[176:179], a[108:111], v[196:199], v[176:179]\n"
      "v_cvt_pk_bf16_f32 v169, v170, v171\n"
      "v_mul_f32 v229, v212, v229\n"
      "v_mfma_f32_16x16x32_bf16 v[204:207], a[76:79], v[164:167], v[204:207]\n"
      "v_mul_f32 v230, v212, v230\n"
      "v_cvt_pk_bf16_f32 v170, v228, v229\n"
      "v_mfma_f32_16x16x32_bf16 v[204:207], a[112:115], v[196:199], v[204:207]\n"
      "v_mul_f32 v231, v212, v231\n"
      "v_cvt_pk_bf16_f32 v171, v230, v231\n"
      "v_mfma_f32_16x16x32_bf16 v[172:175], a[80:83], v[164:167], v[172:175]\n"
      "ds_read_b128 v[228:231], v115 offset:12288\n"
      "v_mul_f32_e64 v144, v212, v144\n"
      "v_mfma_f32_16x16x32_bf16 v[172:175], a[116:119], v[196:199], v[172:175]\n"
      "ds_write_b128 v124, v[168:171] offset:20480\n"
      "v_mfma_f32_16x16x32_bf16 v[184:187], a[84:87], v[164:167], v[184:187]\n"
      "s_waitcnt lgkmcnt(3)\n"
      "v_mul_f32 v145, v212, v145\n"
      "v_cvt_pk_bf16_f32 v144, v144, v145\n"
      "v_mfma_f32_16x16x32_bf16 v[184:187], a[120:123], v[196:199], v[184:187]\n"
      "s_add_u32 m0, s76, 24448\n"
      "v_mul_f32 v147, v212, v147\n"
      "v_mul_f32 v146, v212, v146\n"
      "v_mfma_f32_16x16x32_bf16 v[180:183], a[88:91], v[164:167], v[180:183]\n"
      "v_cvt_pk_bf16_f32 v145, v146, v147\n"
      "v_add_f32 v252, |v252|, |v253|\n"
      "v_mfma_f32_16x16x32_bf16 v[180:183], a[124:127], v[196:199], v[180:183]\n"
      "ds_write_b64 v125, v[144:145] offset:20480\n"
      "v_mfma_f32_16x16x32_bf16 v[160:163], a[92:95], v[164:167], v[160:163]\n"
      "global_load_lds_dwordx4 v116, s[52:53] offset:128\n"
      "v_mfma_f32_16x16x32_bf16 v[160:163], a[128:131], v[196:199], v[160:163]\n"
      "v_add_f32 v254, |v254|, |v255|\n"
      "v_add_f32 v252, v252, v254\n"
      "v_mfma_f32_16x16x32_bf16 v[216:219], v[164:167], a[96:99], v[216:219]\n"
      "v_rsq_f32 v252, v252\n"
      "v_mfma_f32_16x16x32_bf16 v[216:219], v[196:199], a[132:135], v[216:219]\n"
      "s_waitcnt lgkmcnt(2)\n"
      "v_mul_f32 v252, s41, v252\n"
      "v_mov_b32 v255, v252\n"
      "v_mfma_f32_16x16x32_bf16 v[248:251], v[164:167], a[100:103], v[248:251]\n"
      "ds_read_b128 v[168:171], v114 offset:2048\n"
      "v_mfma_f32_16x16x32_bf16 v[248:251], v[196:199], a[136:139], v[248:251]\n"
      "v_permlane32_swap_b32 v252, v255\n"
      "v_mfma_f32_16x16x32_bf16 v[192:195], v[164:167], a[104:107], v[192:195]\n"
      "ds_read_b128 v[144:147], v115 offset:2048\n"
      "v_mov_b32 v253, v252\n"
      "v_mfma_f32_16x16x32_bf16 v[192:195], v[196:199], a[140:143], v[192:195]\n"
      "s_nop 0\n"
      "v_permlane16_swap_b32 v252, v253\n"
      "v_mfma_f32_16x16x32_bf16 v[176:179], a[144:147], v[208:211], v[176:179]\n"
      "v_mul_f32 v136, v252, v136\n"
      "v_mul_f32_e64 v137, v252, v137\n"
      "v_mfma_f32_16x16x32_bf16 v[176:179], a[180:183], v[228:231], v[176:179]\n"
      "s_add_u32 m0, s76, 30464\n"
      "v_mul_f32 v138, v252, v138\n"
      "v_cvt_pk_bf16_f32 v136, v136, v137\n"
      "v_mfma_f32_16x16x32_bf16 v[204:207], a[148:151], v[208:211], v[204:207]\n"
      "v_mul_f32 v139, v252, v139\n"
      "v_cvt_pk_bf16_f32 v137, v138, v139\n"
      "v_mfma_f32_16x16x32_bf16 v[204:207], a[184:187], v[228:231], v[204:207]\n"
      "global_load_lds_dwordx4 v116, s[52:53] offset:256\n"
      "v_mfma_f32_16x16x32_bf16 v[172:175], a[152:155], v[208:211], v[172:175]\n"
      "v_mul_f32 v152, v252, v152\n"
      "v_mul_f32 v153, v252, v153\n"
      "v_mfma_f32_16x16x32_bf16 v[172:175], a[188:191], v[228:231], v[172:175]\n"
      "v_mul_f32_e64 v154, v252, v154\n"
      "v_cvt_pk_bf16_f32 v138, v152, v153\n"
      "v_mfma_f32_16x16x32_bf16 v[184:187], a[156:159], v[208:211], v[184:187]\n"
      "v_mul_f32 v155, v252, v155\n"
      "v_mul_f32 v241, v252, v241\n"
      "v_mfma_f32_16x16x32_bf16 v[184:187], a[192:195], v[228:231], v[184:187]\n"
      "s_add_u32 m0, s77, 18432\n"
      "v_mul_f32 v240, v252, v240\n"
      "v_cvt_pk_bf16_f32 v139, v154, v155\n"
      "v_mfma_f32_16x16x32_bf16 v[180:183], a[160:163], v[208:211], v[180:183]\n"
      "v_mul_f32 v242, v252, v242\n"
      "v_cvt_pk_bf16_f32 v240, v240, v241\n"
      "v_mfma_f32_16x16x32_bf16 v[180:183], a[196:199], v[228:231], v[180:183]\n"
      "ds_write_b128 v126, v[136:139]\n"
      "v_mfma_f32_16x16x32_bf16 v[160:163], a[164:167], v[208:211], v[160:163]\n"
      "v_mul_f32 v243, v252, v243\n"
      "v_mul_f32 v236, v253, v236\n"
      "v_mfma_f32_16x16x32_bf16 v[160:163], a[200:203], v[228:231], v[160:163]\n"
      "v_mul_f32_e64 v237, v253, v237\n"
      "v_cvt_pk_bf16_f32 v241, v242, v243\n"
      "v_mfma_f32_16x16x32_bf16 v[216:219], v[208:211], a[168:171], v[216:219]\n"
      "v_cvt_pk_bf16_f32 v236, v236, v237\n"
      "v_mul_f32_e64 v239, v253, v239\n"
      "v_mfma_f32_16x16x32_bf16 v[216:219], v[228:231], a[204:207], v[216:219]\n"
      "global_load_lds_dwordx4 v117, s[52:53]\n"
      "v_mfma_f32_16x16x32_bf16 v[248:251], v[208:211], a[172:175], v[248:251]\n"
      "s_add_u32 m0, s78, 18432\n"
      "v_mul_f32 v238, v253, v238\n"
      "s_waitcnt lgkmcnt(1)\n"
      "v_mfma_f32_16x16x32_bf16 v[248:251], v[228:231], a[208:211], v[248:251]\n"
      "ds_read_b128 v[136:139], v114 offset:8192\n"
      "v_cvt_pk_bf16_f32 v237, v238, v239\n"
      "v_mfma_f32_16x16x32_bf16 v[192:195], v[208:211], a[176:179], v[192:195]\n"
      "v_mul_f32 v141, v253, v141\n"
      "v_mul_f32 v140, v253, v140\n"
      "v_mfma_f32_16x16x32_bf16 v[192:195], v[228:231], a[212:215], v[192:195]\n"
      "ds_read_b128 v[152:155], v115 offset:8192\n"
      "v_cvt_pk_bf16_f32 v238, v140, v141\n"
      "v_mfma_f32_16x16x32_bf16 v[228:231], a[4:7], v[168:171], 0\n"
      "v_mul_f32 v143, v253, v143\n"
      "v_mul_f32 v142, v253, v142\n"
      "v_mfma_f32_16x16x32_bf16 v[228:231], a[40:43], v[144:147], v[228:231]\n"
      "v_cvt_pk_bf16_f32 v239, v142, v143\n"
      "v_mul_f32_e64 v148, v253, v148\n"
      "v_mfma_f32_16x16x32_bf16 v[140:143], a[0:3], v[168:171], 0\n"
      "v_mul_f32_e64 v149, v253, v149\n"
      "v_cvt_pk_bf16_f32 v148, v148, v149\n"
      "v_mfma_f32_16x16x32_bf16 v[140:143], a[36:39], v[144:147], v[140:143]\n"
      "ds_write_b128 v126, v[236:239] offset:2048\n"
      "v_mfma_f32_16x16x32_bf16 v[236:239], a[8:11], v[168:171], 0\n"
      "v_mul_f32 v150, v253, v150\n"
      "v_mul_f32 v151, v253, v151\n"
      "v_mfma_f32_16x16x32_bf16 v[236:239], a[44:47], v[144:147], v[236:239]\n"
      "s_mov_b32 exec_hi, 0\n"
      "global_load_lds_dwordx4 v118, s[52:53]\n"
      "s_mov_b32 exec_hi, -1\n"
      "v_mfma_f32_16x16x32_bf16 v[164:167], a[12:15], v[168:171], 0\n"
      "s_waitcnt lgkmcnt(1)\n"
      "v_mul_f32 v233, v255, v233\n"
      "v_cvt_pk_bf16_f32 v149, v150, v151\n"
      "v_mfma_f32_16x16x32_bf16 v[164:167], a[48:51], v[144:147], v[164:167]\n"
      "ds_write2st64_b64 v127, v[240:241], v[148:149] offset0:0 offset1:4\n"
      "v_mfma_f32_16x16x32_bf16 v[148:151], a[16:19], v[168:171], 0\n"
      "v_mul_f32 v232, v255, v232\n"
      "v_cvt_pk_bf16_f32 v232, v232, v233\n"
      "v_mfma_f32_16x16x32_bf16 v[148:151], a[52:55], v[144:147], v[148:151]\n"
      "v_mul_f32 v234, v255, v234\n"
      "v_mul_f32_e64 v235, v255, v235\n"
      "v_mfma_f32_16x16x32_bf16 v[208:211], a[20:23], v[168:171], 0\n"
      "s_add_u32 m0, s76, 115456\n"
      "v_cvt_pk_bf16_f32 v233, v234, v235\n"
      "v_mul_f32_e64 v225, v255, v225\n"
      "v_mfma_f32_16x16x32_bf16 v[208:211], a[56:59], v[144:147], v[208:211]\n"
      "v_mul_f32_e64 v224, v255, v224\n"
      "v_cvt_pk_bf16_f32 v234, v224, v225\n"
      "v_mfma_f32_16x16x32_bf16 v[240:243], v[168:171], a[24:27], 0\n"
      "ds_read_b128 v[196:199], v114 offset:14336\n"
      "v_mul_f32 v226, v255, v226\n"
      "v_mfma_f32_16x16x32_bf16 v[240:243], v[144:147], a[60:63], v[240:243]\n"
      "v_mul_f32 v227, v255, v227\n"
      "v_cvt_pk_bf16_f32 v235, v226, v227\n"
      "v_mfma_f32_16x16x32_bf16 v[212:215], v[168:171], a[28:31], 0\n"
      "global_load_lds_dwordx4 v119, s[56:57]\n"
      "v_mfma_f32_16x16x32_bf16 v[212:215], v[144:147], a[64:67], v[212:215]\n"
      "ds_write_b128 v126, v[232:235] offset:4096\n"
      "v_mfma_f32_16x16x32_bf16 v[224:227], v[168:171], a[32:35], 0\n"
      "ds_read_b128 v[232:235], v115 offset:14336\n"
      "v_mul_f32 v188, v255, v188\n"
      "v_mfma_f32_16x16x32_bf16 v[224:227], v[144:147], a[68:71], v[224:227]\n"
      "v_mul_f32 v189, v255, v189\n"
      "v_cvt_pk_f16_f32 v176, v176, v177\n"
      "v_mfma_f32_16x16x32_bf16 v[228:231], a[76:79], v[136:139], v[228:231]\n"
      "s_add_u32 m0, s79, 4864\n"
      "v_mul_f32 v190, v255, v190\n"
      "v_mul_f32 v191, v255, v191\n"
      "v_mfma_f32_16x16x32_bf16 v[140:143], a[72:75], v[136:139], v[140:143]\n"
      "v_cvt_pk_bf16_f32 v188, v188, v189\n"
      "v_cvt_pk_bf16_f32 v189, v190, v191\n"
      "v_mfma_f32_16x16x32_bf16 v[140:143], a[108:111], v[152:155], v[140:143]\n"
      "global_load_lds_dword v122, s[58:59]\n"
      "v_mfma_f32_16x16x32_bf16 v[228:231], a[112:115], v[152:155], v[228:231]\n"
      "ds_write_b64 v127, v[188:189] offset:4096\n"
      "v_mfma_f32_16x16x32_bf16 v[236:239], a[80:83], v[136:139], v[236:239]\n"
      "v_cvt_pk_f16_f32 v184, v184, v185\n"
      "v_pk_mul_f16 v189, v176, v176\n"
      "v_mfma_f32_16x16x32_bf16 v[236:239], a[116:119], v[152:155], v[236:239]\n"
      "v_cvt_pk_f16_f32 v177, v178, v179\n"
      "v_pk_mul_f16 v191, v184, v184\n"
      "v_mfma_f32_16x16x32_bf16 v[164:167], a[84:87], v[136:139], v[164:167]\n"
      "v_pk_fma_f16 v189, v177, v177, v189\n"
      "v_cvt_pk_f16_f32 v185, v186, v187\n"
      "v_mfma_f32_16x16x32_bf16 v[164:167], a[120:123], v[152:155], v[164:167]\n"
      "ds_read_b128 v[144:147], v114 offset:4096\n"
      "v_cvt_pk_f16_f32 v179, v206, v207\n"
      "v_mfma_f32_16x16x32_bf16 v[148:151], a[88:91], v[136:139], v[148:151]\n"
      "ds_read_b128 v[168:171], v115 offset:4096\n"
      "v_cvt_pk_f16_f32 v178, v204, v205\n"
      "v_mfma_f32_16x16x32_bf16 v[148:151], a[124:127], v[152:155], v[148:151]\n"
      "v_pk_fma_f16 v191, v185, v185, v191\n"
      "v_cvt_pk_f16_f32 v186, v180, v181\n"
      "v_mfma_f32_16x16x32_bf16 v[208:211], a[92:95], v[136:139], v[208:211]\n"
      "s_nop 0\n"
      "v_pk_fma_f16 v191, v186, v186, v191\n"
      "s_waitcnt lgkmcnt(3)\n"
      "v_mfma_f32_16x16x32_bf16 v[208:211], a[128:131], v[152:155], v[208:211]\n"
      "v_cvt_pk_f16_f32 v187, v182, v183\n"
      "v_pk_fma_f16 v189, v178, v178, v189\n"
      "v_mfma_f32_16x16x32_bf16 v[240:243], v[136:139], a[96:99], v[240:243]\n"
      "ds_read_b128 v[180:183], v119 offset:18432\n"
      "v_cvt_pk_f16_f32 v32, v172, v173\n"
      "v_mfma_f32_16x16x32_bf16 v[240:243], v[152:155], a[132:135], v[240:243]\n"
      "v_pk_fma_f16 v189, v179, v179, v189\n"
      "v_pk_fma_f16 v191, v187, v187, v191\n"
      "v_mfma_f32_16x16x32_bf16 v[212:215], v[136:139], a[100:103], v[212:215]\n"
      "v_cvt_pk_f16_f32 v34, v160, v161\n"
      "v_cvt_pk_f16_f32 v33, v174, v175\n"
      "v_mfma_f32_16x16x32_bf16 v[212:215], v[152:155], a[136:139], v[212:215]\n"
      "v_pk_fma_f16 v189, v32, v32, v189\n"
      "v_pk_fma_f16 v189, v33, v33, v189\n"
      "v_mfma_f32_16x16x32_bf16 v[224:227], v[136:139], a[104:107], v[224:227]\n"
      "v_cvt_pk_f16_f32 v35, v162, v163\n"
      "v_pk_fma_f16 v191, v34, v34, v191\n"
      "v_mfma_f32_16x16x32_bf16 v[224:227], v[152:155], a[140:143], v[224:227]\n"
      "v_cvt_pk_bf16_f32 v216, v216, v217\n"
      "v_pk_fma_f16 v191, v35, v35, v191\n"
      "v_mfma_f32_16x16x32_bf16 v[140:143], a[144:147], v[196:199], v[140:143]\n"
      "s_nop 0\n"
      "v_permlane16_swap_b32 v189, v191\n"
      "v_mfma_f32_16x16x32_bf16 v[140:143], a[180:183], v[232:235], v[140:143]\n"
      "v_pk_add_f16 v189, v189, v191\n"
      "v_mov_b32 v191, v189\n"
      "v_mfma_f32_16x16x32_bf16 v[228:231], a[148:151], v[196:199], v[228:231]\n"
      "s_waitcnt lgkmcnt(1)\n"
      "v_permlane32_swap_b32_e64 v189, v191\n"
      "v_mfma_f32_16x16x32_bf16 v[228:231], a[184:187], v[232:235], v[228:231]\n"
      "v_pk_add_f16 v189, v189, v191\n"
      "v_add_f16_sdwa v191, v189, v189 dst_sel:DWORD dst_unused:UNUSED_PAD src0_sel:WORD_0 src1_sel:WORD_1\n"
      "v_mfma_f32_16x16x32_bf16 v[236:239], a[152:155], v[196:199], v[236:239]\n"
      "s_nop 0\n"
      "v_rsq_f16 v189, v191\n"
      "v_mfma_f32_16x16x32_bf16 v[236:239], a[188:191], v[232:235], v[236:239]\n"
      "ds_read_b128 v[136:139], v114 offset:10240\n"
      "v_cmp_nle_f16 vcc, 0x1c00, v191\n"
      "v_mfma_f32_16x16x32_bf16 v[164:167], a[156:159], v[196:199], v[164:167]\n"
      "s_mov_b64 s[34:35], vcc\n"
      "v_cmp_nge_f16 vcc, 0x7b00, v191\n"
      "v_mov_b32 v191, v189\n"
      "v_mfma_f32_16x16x32_bf16 v[164:167], a[192:195], v[232:235], v[164:167]\n"
      "ds_read_b128 v[152:155], v115 offset:10240\n"
      "v_cvt_pk_bf16_f32 v217, v218, v219\n"
      "v_mfma_f32_16x16x32_bf16 v[148:151], a[160:163], v[196:199], v[148:151]\n"
      "s_or_b64 s[34:35], s[34:35], vcc\n"
      "v_permlane16_swap_b32 v189, v191\n"
      "v_mfma_f32_16x16x32_bf16 v[148:151], a[196:199], v[232:235], v[148:151]\n"
      "s_sub_u32 s28, s27, 3\n"
      "v_pk_mul_f16 v185, v185, v191 op_sel_hi:[1,0]\n"
      "v_pk_mul_f16 v184, v184, v191 op_sel_hi:[1,0]\n"
      "v_mfma_f32_16x16x32_bf16 v[208:211], a[164:167], v[196:199], v[208:211]\n"
      "s_cselect_b64 s[54:55], s[12:13], s[22:23]\n"
      "v_pk_mul_f16 v186, v186, v191 op_sel_hi:[1,0]\n"
      "v_pk_mul_f16 v187, v187, v191 op_sel_hi:[1,0]\n"
      "v_mfma_f32_16x16x32_bf16 v[208:211], a[200:203], v[232:235], v[208:211]\n"
      "s_max_i32 m0, s28, 0\n"
      "v_pk_mul_f16 v34, v34, v191 op_sel_hi:[1,0]\n"
      "v_pk_mul_f16 v35, v35, v191 op_sel_hi:[1,0]\n"
      "v_mfma_f32_16x16x32_bf16 v[240:243], v[196:199], a[168:171], v[240:243]\n"
      "s_movrels_b32 s9, s80\n"
      "v_mul_f16 v189, s7, v189\n"
      "v_pk_mul_f16 v176, v176, v189 op_sel_hi:[1,0]\n"
      "v_mfma_f32_16x16x32_bf16 v[240:243], v[232:235], a[204:207], v[240:243]\n"
      "ds_read_b128 v[204:207], v119 offset:24576\n"
      "v_pk_mul_f16 v177, v177, v189 op_sel_hi:[1,0]\n"
      "v_mfma_f32_16x16x32_bf16 v[212:215], v[196:199], a[172:175], v[212:215]\n"
      "s_lshr_b32 s8, s9, 8\n"
      "v_pk_mul_f16 v179, v179, v189 op_sel_hi:[1,0]\n"
      "v_pk_mul_f16 v178, v178, v189 op_sel_hi:[1,0]\n"
      "v_mfma_f32_16x16x32_bf16 v[212:215], v[232:235], a[208:211], v[212:215]\n"
      "s_and_b32 s9, s9, 0xff\n"
      "v_cvt_pk_bf16_f32 v219, v242, v243\n"
      "v_pk_mul_f16 v33, v33, v189 op_sel_hi:[1,0]\n"
      "v_mfma_f32_16x16x32_bf16 v[224:227], v[196:199], a[176:179], v[224:227]\n"
      "s_mul_i32 s28, s8, 1373568\n"
      "v_cvt_pk_bf16_f32 v248, v248, v249\n"
      "v_cvt_pk_bf16_f32 v218, v240, v241\n"
      "v_mfma_f32_16x16x32_bf16 v[224:227], v[232:235], a[212:215], v[224:227]\n"
      "v_pk_mul_f16 v32, v32, v189 op_sel_hi:[1,0]\n"
      "v_cvt_pk_f16_f32 v164, v164, v165\n"
      "v_cvt_pk_bf16_f32 v249, v250, v251\n"
      "v_mfma_f32_16x16x32_bf16 v[188:191], a[0:3], v[144:147], 0\n"
      "s_mul_i32 s29, s9, 2688\n"
      "v_cvt_pk_bf16_f32 v251, v214, v215\n"
      "v_cvt_pk_bf16_f32 v250, v212, v213\n"
      "v_mfma_f32_16x16x32_bf16 v[188:191], a[36:39], v[168:171], v[188:191]\n"
      "s_add_u32 s28, s28, s29\n"
      "v_cvt_pk_bf16_f32 v192, v192, v193\n"
      "v_cvt_pk_bf16_f32 v193, v194, v195\n"
      "v_mfma_f32_16x16x32_bf16 v[240:243], a[4:7], v[144:147], 0\n"
      "s_cmp_eq_u32 s8, 72\n"
      "v_cvt_pk_bf16_f32 v194, v224, v225\n"
      "v_cvt_pk_f16_f32 v140, v140, v141\n"
      "v_mfma_f32_16x16x32_bf16 v[240:243], a[40:43], v[168:171], v[240:243]\n"
      "s_cselect_b32 s8, 0, 100270464\n"
      "v_pk_mul_f16 v232, v140, v140\n"
      "v_cvt_pk_bf16_f32 v195, v226, v227\n"
      "v_mfma_f32_16x16x32_bf16 v[224:227], a[8:11], v[144:147], 0\n"
      "s_cmp_eq_u32 s9, 72\n"
      "s_waitcnt lgkmcnt(0)\n"
      "v_pk_mul_f16 v233, v164, v164\n"
      "v_mfma_f32_16x16x32_bf16 v[224:227], a[44:47], v[168:171], v[224:227]\n"
      "s_cselect_b32 s9, 0, 196224\n"
      "v_cvt_pk_f16_f32 v141, v142, v143\n"
      "v_pk_fma_f16 v232, v141, v141, v232\n"
      "v_mfma_f32_16x16x32_bf16 v[212:215], a[12:15], v[144:147], 0\n"
      "s_add_u32 s10, s8, s9\n"
      "v_cvt_pk_f16_f32 v165, v166, v167\n"
      "v_pk_fma_f16 v233, v165, v165, v233\n"
      "v_mfma_f32_16x16x32_bf16 v[212:215], a[48:51], v[168:171], v[212:215]\n"
      "s_add_u32 s28, s28, s10\n"
      "v_cvt_pk_f16_f32 v142, v228, v229\n"
      "v_cvt_pk_f16_f32 v166, v148, v149\n"
      "v_mfma_f32_16x16x32_bf16 v[252:255], a[16:19], v[144:147], 0\n"
      "ds_read_b128 v[196:199], v114 offset:16384\n"
      "v_pk_fma_f16 v232, v142, v142, v232\n"
      "v_mfma_f32_16x16x32_bf16 v[252:255], a[52:55], v[168:171], v[252:255]\n"
      "s_add_u32 s54, s54, s28\n"
      "v_cvt_pk_f16_f32 v167, v150, v151\n"
      "v_pk_fma_f16 v233, v166, v166, v233\n"
      "v_mfma_f32_16x16x32_bf16 v[160:163], a[20:23], v[144:147], 0\n"
      "s_addc_u32 s55, s55, 0\n"
      "v_cvt_pk_f16_f32 v143, v230, v231\n"
      "v_pk_fma_f16 v232, v143, v143, v232\n"
      "v_mfma_f32_16x16x32_bf16 v[160:163], a[56:59], v[168:171], v[160:163]\n"
      "ds_read_b128 v[148:151], v115 offset:16384\n"
      "v_pk_fma_f16 v233, v167, v167, v233\n"
      "v_mfma_f32_16x16x32_bf16 v[228:231], v[144:147], a[24:27], 0\n"
      "s_cmp_lg_u32 s10, s11\n"
      "v_cvt_pk_f16_f32 v38, v236, v237\n"
      "v_pk_fma_f16 v232, v38, v38, v232\n"
      "v_mfma_f32_16x16x32_bf16 v[228:231], v[168:171], a[60:63], v[228:231]\n"
      "s_cbranch_scc1 L_edge_26_%=\n"
      "L_eb_27_%=:\n"
      "v_mfma_f32_16x16x32_bf16 v[172:175], v[144:147], a[28:31], 0\n"
      "s_mov_b32 s5, s4\n"
      "v_cvt_pk_f16_f32 v40, v208, v209\n"
      "v_pk_fma_f16 v233, v40, v40, v233\n"
      "v_mfma_f32_16x16x32_bf16 v[172:175], v[168:171], a[64:67], v[172:175]\n"
      "s_add_u32 s4, s27, 1\n"
      "v_cvt_pk_f16_f32 v39, v238, v239\n"
      "v_pk_fma_f16 v232, v39, v39, v232\n"
      "v_mfma_f32_16x16x32_bf16 v[236:239], v[144:147], a[32:35], 0\n"
      "s_lshl_b32 s4, s4, 30\n"
      "v_cvt_pk_f16_f32 v41, v210, v211\n"
      "v_pk_fma_f16 v233, v41, v41, v233\n"
      "v_mfma_f32_16x16x32_bf16 v[236:239], v[168:171], a[68:71], v[236:239]\n"
      "global_store_dwordx4 v116, v[180:183], s[54:55] sc1\n"
      "v_mfma_f32_16x16x32_bf16 v[224:227], a[80:83], v[136:139], v[224:227]\n"
      "s_and_b32 s4, s4, 0x80000000\n"
      "v_permlane16_swap_b32 v232, v233\n"
      "v_mfma_f32_16x16x32_bf16 v[188:191], a[72:75], v[136:139], v[188:191]\n"
      "s_nop 0\n"
      "v_pk_add_f16 v232, v232, v233\n"
      "v_mov_b32_e64 v233, v232\n"
      "v_mfma_f32_16x16x32_bf16 v[188:191], a[108:111], v[152:155], v[188:191]\n"
      "s_nop 0\n"
      "v_permlane32_swap_b32 v232, v233\n"
      "v_mfma_f32_16x16x32_bf16 v[240:243], a[76:79], v[136:139], v[240:243]\n"
      "ds_read_b128 v[144:147], v119 offset:30720\n"
      "v_mfma_f32_16x16x32_bf16 v[240:243], a[112:115], v[152:155], v[240:243]\n"
      "s_waitcnt lgkmcnt(1)\n"
      "v_pk_add_f16 v232, v232, v233\n"
      "v_add_f16_sdwa v233, v232, v232 dst_sel:DWORD dst_unused:UNUSED_PAD src0_sel:WORD_0 src1_sel:WORD_1\n"
      "v_cmp_nle_f16 vcc, 0x1c00, v233\n"
      "v_mfma_f32_16x16x32_bf16 v[224:227], a[116:119], v[152:155], v[224:227]\n"
      "s_or_b64 s[34:35], s[34:35], vcc\n"
      "v_rsq_f16 v232, v233\n"
      "v_mfma_f32_16x16x32_bf16 v[212:215], a[84:87], v[136:139], v[212:215]\n"
      "global_store_dwordx4 v116, v[204:207], s[54:55] offset:128 sc1\n"
      "v_mfma_f32_16x16x32_bf16 v[212:215], a[120:123], v[152:155], v[212:215]\n"
      "ds_read_b128 v[180:183], v132 offset:32\n"
      "v_cmp_nge_f16 vcc, 0x7b00, v233\n"
      "v_mfma_f32_16x16x32_bf16 v[252:255], a[88:91], v[136:139], v[252:255]\n"
      "ds_read_b128 v[168:171], v133\n"
      "v_mov_b32 v233, v232\n"
      "v_mfma_f32_16x16x32_bf16 v[252:255], a[124:127], v[152:155], v[252:255]\n"
      "ds_read_b64 v[204:205], v132 offset:24\n"
      "v_add_u32 v135, 32, v135\n"
      "v_mfma_f32_16x16x32_bf16 v[160:163], a[92:95], v[136:139], v[160:163]\n"
      "ds_read_u16 v52, v134\n"
      "s_or_b64 s[34:35], s[34:35], vcc\n"
      "v_mfma_f32_16x16x32_bf16 v[160:163], a[128:131], v[152:155], v[160:163]\n"
      "ds_read_b64 v[206:207], v133 offset:16\n"
      "v_permlane16_swap_b32 v232, v233\n"
      "v_mfma_f32_16x16x32_bf16 v[228:231], v[136:139], a[96:99], v[228:231]\n"
      "ds_read_u16 v56, v134 offset:32\n"
      "v_pk_mul_f16 v165, v165, v233 op_sel_hi:[1,0]\n"
      "v_mfma_f32_16x16x32_bf16 v[228:231], v[152:155], a[132:135], v[228:231]\n"
      "ds_read_u16 v60, v134 offset:64\n"
      "v_pk_mul_f16 v164, v164, v233 op_sel_hi:[1,0]\n"
      "v_mfma_f32_16x16x32_bf16 v[172:175], v[136:139], a[100:103], v[172:175]\n"
      "ds_read_b128 v[208:211], v123 offset:21504\n"
      "v_pk_mul_f16 v166, v166, v233 op_sel_hi:[1,0]\n"
      "v_mfma_f32_16x16x32_bf16 v[172:175], v[152:155], a[136:139], v[172:175]\n"
      "s_waitcnt lgkmcnt(8)\n"
      "v_pk_mul_f16 v40, v40, v233 op_sel_hi:[1,0]\n"
      "v_pk_mul_f16 v167, v167, v233 op_sel_hi:[1,0]\n"
      "v_mfma_f32_16x16x32_bf16 v[236:239], v[136:139], a[104:107], v[236:239]\n"
      "global_store_dwordx4 v116, v[144:147], s[54:55] offset:256 sc1\n"
      "v_mfma_f32_16x16x32_bf16 v[236:239], v[152:155], a[140:143], v[236:239]\n"
      "ds_read_b128 v[220:223], v123 offset:18432\n"
      "v_pk_mul_f16 v41, v41, v233 op_sel_hi:[1,0]\n"
      "v_mfma_f32_16x16x32_bf16 v[188:191], a[144:147], v[196:199], v[188:191]\n"
      "ds_read_b128 v[156:159], v123 offset:24576\n"
      "v_mul_f16 v232, s7, v232\n"
      "v_mfma_f32_16x16x32_bf16 v[188:191], a[180:183], v[148:151], v[188:191]\n"
      "ds_read_b128 v[144:147], v123 offset:27648\n"
      "v_pk_mul_f16 v140, v140, v232 op_sel_hi:[1,0]\n"
      "v_mfma_f32_16x16x32_bf16 v[240:243], a[148:151], v[196:199], v[240:243]\n"
      "v_pk_mul_f16 v141, v141, v232 op_sel_hi:[1,0]\n"
      "v_pk_mul_f16 v143, v143, v232 op_sel_hi:[1,0]\n"
      "v_mfma_f32_16x16x32_bf16 v[240:243], a[184:187], v[148:151], v[240:243]\n"
      "v_pk_mul_f16 v39, v39, v232 op_sel_hi:[1,0]\n"
      "v_pk_mul_f16 v142, v142, v232 op_sel_hi:[1,0]\n"
      "v_mfma_f32_16x16x32_bf16 v[224:227], a[152:155], v[196:199], v[224:227]\n"
      "v_cvt_pk_f16_f32 v188, v188, v189\n"
      "v_pk_mul_f16 v38, v38, v232 op_sel_hi:[1,0]\n"
      "v_mfma_f32_16x16x32_bf16 v[224:227], a[188:191], v[148:151], v[224:227]\n"
      "v_cvt_pk_f16_f32 v189, v190, v191\n"
      "v_pk_mul_f16 v233, v188, v188\n"
      "v_mfma_f32_16x16x32_bf16 v[212:215], a[156:159], v[196:199], v[212:215]\n"
      "v_cvt_pk_f16_f32 v190, v240, v241\n"
      "v_pk_fma_f16 v233, v189, v189, v233\n"
      "v_mfma_f32_16x16x32_bf16 v[212:215], a[192:195], v[148:151], v[212:215]\n"
      "v_cvt_pk_f16_f32 v191, v242, v243\n"
      "v_pk_fma_f16 v233, v190, v190, v233\n"
      "v_mfma_f32_16x16x32_bf16 v[252:255], a[160:163], v[196:199], v[252:255]\n"
      "s_waitcnt lgkmcnt(2)\n"
      "v_cvt_pk_f16_f32 v44, v224, v225\n"
      "v_pk_fma_f16 v233, v191, v191, v233\n"
      "v_mfma_f32_16x16x32_bf16 v[252:255], a[196:199], v[148:151], v[252:255]\n"
      "s_nop 0\n"
      "v_cvt_pk_f16_f32 v212, v212, v213\n"
      "v_pk_mul_f16 v224, v212, v212\n"
      "v_mfma_f32_16x16x32_bf16 v[160:163], a[164:167], v[196:199], v[160:163]\n"
      "v_cvt_pk_f16_f32 v213, v214, v215\n"
      "v_pk_fma_f16 v224, v213, v213, v224\n"
      "v_mfma_f32_16x16x32_bf16 v[160:163], a[200:203], v[148:151], v[160:163]\n"
      "s_nop 0\n"
      "v_cvt_pk_f16_f32 v214, v252, v253\n"
      "v_pk_fma_f16 v224, v214, v214, v224\n"
      "v_mfma_f32_16x16x32_bf16 v[228:231], v[196:199], a[168:171], v[228:231]\n"
      "s_nop 0\n"
      "v_cvt_pk_f16_f32 v215, v254, v255\n"
      "v_pk_fma_f16 v224, v215, v215, v224\n"
      "v_mfma_f32_16x16x32_bf16 v[228:231], v[148:151], a[204:207], v[228:231]\n"
      "ds_read_b128 v[200:203], v123 offset:30720\n"
      "v_mfma_f32_16x16x32_bf16 v[172:175], v[196:199], a[172:175], v[172:175]\n"
      "v_cvt_pk_f16_f32 v46, v160, v161\n"
      "v_cvt_pk_f16_f32 v45, v226, v227\n"
      "v_mfma_f32_16x16x32_bf16 v[172:175], v[148:151], a[208:211], v[172:175]\n"
      "v_pk_fma_f16 v233, v44, v44, v233\n"
      "v_pk_fma_f16 v224, v46, v46, v224\n"
      "v_mfma_f32_16x16x32_bf16 v[236:239], v[196:199], a[176:179], v[236:239]\n"
      "ds_read_b128 v[252:255], v123 offset:33792\n"
      "v_cvt_pk_f16_f32 v47, v162, v163\n"
      "v_mfma_f32_16x16x32_bf16 v[236:239], v[148:151], a[212:215], v[236:239]\n"
      "v_pk_fma_f16 v233, v45, v45, v233\n"
      "v_pk_fma_f16 v224, v47, v47, v224\n"
      "v_mfma_f32_16x16x32_bf16 v[240:243], a[216:219], v[220:223], 0\n"
      "s_nop 0\n"
      "v_permlane16_swap_b32 v233, v224\n"
      "v_mfma_f32_16x16x32_bf16 v[240:243], a[228:231], v[208:211], v[240:243]\n"
      "v_pk_add_f16 v233, v233, v224\n"
      "v_mov_b32 v224, v233\n"
      "v_mfma_f32_16x16x32_bf16 v[136:139], a[220:223], v[220:223], 0\n"
      "s_waitcnt lgkmcnt(2)\n"
      "v_permlane32_swap_b32_e64 v233, v224\n"
      "v_mfma_f32_16x16x32_bf16 v[136:139], a[232:235], v[208:211], v[136:139]\n"
      "v_pk_add_f16 v233, v233, v224\n"
      "v_add_f16_sdwa v224, v233, v233 dst_sel:DWORD dst_unused:UNUSED_PAD src0_sel:WORD_0 src1_sel:WORD_1\n"
      "v_mfma_f32_16x16x32_bf16 v[152:155], a[224:227], v[220:223], 0\n"
      "s_nop 0\n"
      "v_rsq_f16 v233, v224\n"
      "v_mfma_f32_16x16x32_bf16 v[152:155], a[236:239], v[208:211], v[152:155]\n"
      "v_cvt_pk_bf16_f32 v50, v228, v229\n"
      "v_cmp_nle_f16 vcc, 0x1c00, v224\n"
      "v_mfma_f32_16x16x32_bf16 v[240:243], a[240:243], v[156:159], v[240:243]\n"
      "s_or_b64 s[34:35], s[34:35], vcc\n"
      "v_cmp_nge_f16 vcc, 0x7b00, v224\n"
      "v_mov_b32_e64 v224, v233\n"
      "v_mfma_f32_16x16x32_bf16 v[240:243], a[252:255], v[144:147], v[240:243]\n"
      "s_nop 0\n"
      "v_permlane16_swap_b32 v233, v224\n"
      "v_mfma_f32_16x16x32_bf16 v[136:139], a[244:247], v[156:159], v[136:139]\n"
      "s_or_b64 s[34:35], s[34:35], vcc\n"
      "v_pk_mul_f16 v212, v212, v224 op_sel_hi:[1,0]\n"
      "v_pk_mul_f16 v213, v213, v224 op_sel_hi:[1,0]\n"
      "v_mfma_f32_16x16x32_bf16 v[136:139], v[0:3], v[144:147], v[136:139]\n"
      "v_pk_mul_f16 v214, v214, v224 op_sel_hi:[1,0]\n"
      "v_pk_mul_f16 v215, v215, v224 op_sel_hi:[1,0]\n"
      "v_mfma_f32_16x16x32_bf16 v[152:155], a[248:251], v[156:159], v[152:155]\n"
      "s_or_b64 vcc, s[48:49], s[50:51]\n"
      "v_pk_mul_f16 v47, v47, v224 op_sel_hi:[1,0]\n"
      "v_pk_mul_f16 v46, v46, v224 op_sel_hi:[1,0]\n"
      "v_mfma_f32_16x16x32_bf16 v[152:155], v[4:7], v[144:147], v[152:155]\n"
      "s_waitcnt lgkmcnt(0)\n"
      "v_mul_f16 v233, s7, v233\n"
      "v_pk_mul_f16 v188, v188, v233 op_sel_hi:[1,0]\n"
      "v_mfma_f32_16x16x32_bf16 v[240:243], v[8:11], v[200:203], v[240:243]\n"
      "s_nop 0\n"
      "v_pk_mul_f16 v190, v190, v233 op_sel_hi:[1,0]\n"
      "v_pk_mul_f16 v189, v189, v233 op_sel_hi:[1,0]\n"
      "v_mfma_f32_16x16x32_bf16 v[240:243], v[20:23], v[252:255], v[240:243]\n"
      "s_or_b64 vcc, vcc, s[34:35]\n"
      "v_pk_mul_f16 v44, v44, v233 op_sel_hi:[1,0]\n"
      "v_pk_mul_f16 v191, v191, v233 op_sel_hi:[1,0]\n"
      "v_mfma_f32_16x16x32_bf16 v[136:139], v[12:15], v[200:203], v[136:139]\n"
      "s_waitcnt vmcnt(3)\n"
      "v_pk_mul_f16 v45, v45, v233 op_sel_hi:[1,0]\n"
      "v_cvt_pk_bf16_f32 v51, v230, v231\n"
      "v_mfma_f32_16x16x32_bf16 v[136:139], v[24:27], v[252:255], v[136:139]\n"
      "ds_read_b32 v149, v122 offset:4864\n"
      "v_cvt_pk_bf16_f32 v54, v172, v173\n"
      "v_mfma_f32_16x16x32_bf16 v[152:155], v[16:19], v[200:203], v[152:155]\n"
      "ds_read_b128 v[224:227], v119 offset:41728\n"
      "v_cvt_pk_bf16_f32 v55, v174, v175\n"
      "v_mfma_f32_16x16x32_bf16 v[152:155], v[28:31], v[252:255], v[152:155]\n"
      "v_fma_f32 v197, v240, v240, s39\n"
      "v_cvt_pk_bf16_f32 v59, v238, v239\n"
      "v_mfma_f32_16x16x32_f16 v[228:231], v[184:187], v[176:179], v[78:81]\n"
      "v_fmac_f32_e64 v197, v241, v241\n"
      "v_cvt_pk_bf16_f32 v58, v236, v237\n"
      "v_mfma_f32_16x16x32_f16 v[228:231], v[34:37], v[32:35], v[228:231]\n"
      "v_fmac_f32 v197, v242, v242\n"
      "v_fmac_f32 v197, v243, v243\n"
      "v_mfma_f32_16x16x32_f16 v[144:147], v[164:167], v[176:179], v[82:85]\n"
      "v_fmac_f32 v197, v136, v136\n"
      "v_fmac_f32 v197, v137, v137\n"
      "v_mfma_f32_16x16x32_f16 v[144:147], v[40:43], v[32:35], v[144:147]\n"
      "v_fmac_f32 v197, v138, v138\n"
      "v_fmac_f32 v197, v139, v139\n"
      "v_mfma_f32_16x16x32_f16 v[234:237], v[180:183], v[176:179], v[70:73]\n"
      "v_fmac_f32 v197, v152, v152\n"
      "v_fmac_f32 v197, v153, v153\n"
      "v_mfma_f32_16x16x32_f16 v[234:237], v[168:171], v[184:187], v[234:237]\n"
      "v_fmac_f32_e64 v197, v154, v154\n"
      "v_fmac_f32_e64 v197, v155, v155\n"
      "v_mfma_f32_16x16x32_f16 v[234:237], v[204:207], v[32:35], v[234:237]\n"
      "s_waitcnt lgkmcnt(0)\n"
      "v_or3_b32 v224, v224, v225, v226\n"
      "v_bitop3_b32 v149, v149, v227, s40 bitop3:0xa8\n"
      "v_mfma_f32_16x16x32_f16 v[252:255], v[212:215], v[176:179], v[86:89]\n"
      "s_nop 0\n"
      "v_and_b32 v224, s40, v224\n"
      "v_or3_b32 v224, v224, v149, s4\n"
      "v_mfma_f32_16x16x32_f16 v[252:255], v[46:49], v[32:35], v[252:255]\n"
      "ds_write_b32 v129, v224 offset:3072\n"
      "s_cbranch_scc1 L_gen_28_%=\n"
      "L_fast_31_%=:\n"
      "ds_read_b128 v[160:163], v123 offset:19456\n"
      "v_mfma_f32_16x16x32_f16 v[224:227], v[184:187], v[140:143], v[90:93]\n"
      "ds_read_b128 v[236:239], v120 offset:18432\n"
      "v_exp_f32_e64 v146, v146\n"
      "v_mfma_f32_16x16x32_f16 v[224:227], v[34:37], v[38:41], v[224:227]\n"
      "ds_read_b128 v[198:201], v123 offset:22528\n"
      "v_exp_f32 v147, v147\n"
      "v_mfma_f32_16x16x32_f16 v[156:159], v[164:167], v[140:143], v[94:97]\n"
      "v_exp_f32 v230, v230\n"
      "v_mfma_f32_16x16x32_f16 v[156:159], v[40:43], v[38:41], v[156:159]\n"
      "ds_read_b128 v[208:211], v128 offset:3072\n"
      "v_exp_f32 v252, v252\n"
      "v_exp_f32 v144, v144\n"
      "v_exp_f32 v228, v228\n"
      "v_exp_f32 v145, v145\n"
      "v_mfma_f32_16x16x32_f16 v[176:179], v[180:183], v[140:143], v[72:75]\n"
      "v_exp_f32_e64 v229, v229\n"
      "v_mfma_f32_16x16x32_f16 v[176:179], v[168:171], v[164:167], v[176:179]\n"
      "v_exp_f32_e64 v231, v231\n"
      "v_mfma_f32_16x16x32_f16 v[176:179], v[204:207], v[38:41], v[176:179]\n"
      "ds_read_b128 v[220:223], v123 offset:25600\n"
      "v_cvt_pk_bf16_f32 v228, v228, v229\n"
      "v_cvt_pk_bf16_f32 v229, v230, v231\n"
      "v_cvt_pk_bf16_f32 v231, v146, v147\n"
      "v_cvt_pk_bf16_f32 v230, v144, v145\n"
      "v_mfma_f32_16x16x32_f16 v[144:147], v[212:215], v[140:143], v[98:101]\n"
      "ds_read_b128 v[172:175], v123 offset:28672\n"
      "s_waitcnt lgkmcnt(3)\n"
      "v_mfma_f32_16x16x32_f16 v[144:147], v[46:49], v[38:41], v[144:147]\n"
      "global_store_dwordx4 v117, v[236:239], s[54:55] sc1\n"
      "v_mfma_f32_16x16x32_bf16 v[148:151], a[216:219], v[160:163], 0\n"
      "v_exp_f32 v254, v254\n"
      "v_mfma_f32_16x16x32_bf16 v[148:151], a[228:231], v[198:201], v[148:151]\n"
      "s_waitcnt lgkmcnt(2)\n"
      "v_exp_f32 v234, v234\n"
      "v_bitop3_b32 v208, v208, v209, s4 bitop3:0x7e\n"
      "v_mfma_f32_16x16x32_bf16 v[236:239], a[220:223], v[160:163], 0\n"
      "v_exp_f32 v255, v255\n"
      "v_mfma_f32_16x16x32_bf16 v[236:239], a[232:235], v[198:201], v[236:239]\n"
      "v_exp_f32 v253, v253\n"
      "v_mfma_f32_16x16x32_bf16 v[140:143], a[224:227], v[160:163], 0\n"
      "v_cvt_pk_bf16_f32 v62, v252, v253\n"
      "v_cvt_pk_bf16_f32 v63, v254, v255\n"
      "v_mfma_f32_16x16x32_bf16 v[140:143], a[236:239], v[198:201], v[140:143]\n"
      "ds_read_b128 v[252:255], v123 offset:31744\n"
      "v_bitop3_b32 v210, v210, v211, s4 bitop3:0x7e\n"
      "s_waitcnt lgkmcnt(1)\n"
      "v_mfma_f32_16x16x32_bf16 v[148:151], a[240:243], v[220:223], v[148:151]\n"
      "v_or_b32_e64 v208, v208, v210\n"
      "v_cmp_ne_u32 vcc, 0, v208\n"
      "v_mfma_f32_16x16x32_bf16 v[148:151], a[252:255], v[172:175], v[148:151]\n"
      "ds_read_b128 v[198:201], v123 offset:34816\n"
      "v_cvt_pk_bf16_f32 v64, v234, 0\n"
      "v_mfma_f32_16x16x32_bf16 v[236:239], a[244:247], v[220:223], v[236:239]\n"
      "s_mov_b64 s[48:49], vcc\n"
      "v_exp_f32 v225, v225\n"
      "v_mfma_f32_16x16x32_bf16 v[236:239], v[0:3], v[172:175], v[236:239]\n"
      "ds_read_b128 v[160:163], v121 offset:18432\n"
      "v_exp_f32 v227, v227\n"
      "v_exp_f32 v224, v224\n"
      "v_exp_f32_e64 v226, v226\n"
      "v_cvt_pk_bf16_f32 v224, v224, v225\n"
      "v_exp_f32 v156, v156\n"
      "v_mfma_f32_16x16x32_bf16 v[140:143], a[248:251], v[220:223], v[140:143]\n"
      "v_exp_f32 v157, v157\n"
      "v_mfma_f32_16x16x32_bf16 v[140:143], v[4:7], v[172:175], v[140:143]\n"
      "s_cbranch_vccnz L_flg_32_%=\n"
      "L_fb_33_%=:\n"
      "v_cvt_pk_bf16_f32 v225, v226, v227\n"
      "v_exp_f32 v159, v159\n"
      "v_exp_f32 v158, v158\n"
      "s_waitcnt lgkmcnt(1)\n"
      "v_mfma_f32_16x16x32_bf16 v[148:151], v[8:11], v[252:255], v[148:151]\n"
      "ds_read_b128 v[220:223], v123 offset:23552\n"
      "v_cvt_pk_bf16_f32 v227, v158, v159\n"
      "v_mfma_f32_16x16x32_bf16 v[148:151], v[20:23], v[198:201], v[148:151]\n"
      "ds_read_b128 v[208:211], v123 offset:20480\n"
      "v_exp_f32 v144, v144\n"
      "v_mfma_f32_16x16x32_bf16 v[236:239], v[12:15], v[252:255], v[236:239]\n"
      "s_waitcnt lgkmcnt(2)\n"
      "v_exp_f32 v145, v145\n"
      "v_mfma_f32_16x16x32_bf16 v[236:239], v[24:27], v[198:201], v[236:239]\n"
      "s_mov_b32 exec_hi, 0\n"
      "global_store_dwordx4 v118, v[160:163], s[54:55] sc1\n"
      "s_mov_b32 exec_hi, -1\n"
      "v_mfma_f32_16x16x32_bf16 v[140:143], v[16:19], v[252:255], v[140:143]\n"
      "v_exp_f32 v146, v146\n"
      "v_mfma_f32_16x16x32_bf16 v[140:143], v[28:31], v[198:201], v[140:143]\n"
      "s_add_u32 s28, s27, 2\n"
      "v_fma_f32 v178, v148, v148, s39\n"
      "v_exp_f32 v176, v176\n"
      "v_mfma_f32_16x16x32_bf16 v[160:163], v[66:69], v[228:231], 0\n"
      "v_exp_f32_e64 v147, v147\n"
      "v_mfma_f32_16x16x32_bf16 v[160:163], v[66:69], v[62:65], v[160:163]\n"
      "v_cvt_pk_bf16_f32 v226, v156, v157\n"
      "v_fmac_f32 v178, v149, v149\n"
      "v_mfma_f32_16x16x32_bf16 v[252:255], v[216:219], v[228:231], 0\n"
      "v_fmac_f32 v178, v150, v150\n"
      "v_fmac_f32 v178, v151, v151\n"
      "v_mfma_f32_16x16x32_bf16 v[252:255], v[50:53], v[62:65], v[252:255]\n"
      "v_fmac_f32 v178, v236, v236\n"
      "v_fmac_f32 v178, v237, v237\n"
      "v_mfma_f32_16x16x32_bf16 v[156:159], v[248:251], v[228:231], 0\n"
      "v_fmac_f32 v178, v238, v238\n"
      "v_fmac_f32 v178, v239, v239\n"
      "v_mfma_f32_16x16x32_bf16 v[156:159], v[54:57], v[62:65], v[156:159]\n"
      "v_fmac_f32 v178, v140, v140\n"
      "v_fmac_f32 v178, v141, v141\n"
      "v_mfma_f32_16x16x32_bf16 v[172:175], v[192:195], v[228:231], 0\n"
      "s_waitcnt lgkmcnt(0)\n"
      "v_fmac_f32 v178, v142, v142\n"
      "v_fmac_f32 v178, v143, v143\n"
      "v_mfma_f32_16x16x32_bf16 v[172:175], v[58:61], v[62:65], v[172:175]\n"
      "v_cvt_pk_bf16_f32 v62, v144, v145\n"
      "v_cvt_pk_bf16_f32 v64, v176, 0\n"
      "v_mfma_f32_16x16x32_f16 v[198:201], v[184:187], v[188:191], v[102:105]\n"
      "v_cndmask_b32 v235, v235, v177, s[42:43]\n"
      "v_cvt_pk_bf16_f32 v63, v146, v147\n"
      "v_mfma_f32_16x16x32_f16 v[198:201], v[34:37], v[44:47], v[198:201]\n"
      "v_rcp_f32 v160, v160\n"
      "v_mfma_f32_16x16x32_f16 v[228:231], v[164:167], v[188:191], v[106:109]\n"
      "v_mul_f32 v253, v160, v253\n"
      "v_mul_f32 v252, v160, v252\n"
      "v_mfma_f32_16x16x32_f16 v[228:231], v[40:43], v[44:47], v[228:231]\n"
      "v_mul_f32 v255, v160, v255\n"
      "v_cvt_pk_bf16_f32 v252, v252, v253\n"
      "v_mfma_f32_16x16x32_f16 v[164:167], v[180:183], v[188:191], v[74:77]\n"
      "ds_read_b128 v[184:187], v123 offset:26624\n"
      "v_mul_f32_e64 v254, v160, v254\n"
      "v_mfma_f32_16x16x32_f16 v[164:167], v[168:171], v[212:215], v[164:167]\n"
      "v_cvt_pk_bf16_f32 v253, v254, v255\n"
      "v_mul_f32 v156, v160, v156\n"
      "v_mfma_f32_16x16x32_f16 v[164:167], v[204:207], v[44:47], v[164:167]\n"
      "v_mul_f32 v175, v160, v175\n"
      "v_mul_f32 v158, v160, v158\n"
      "v_mfma_f32_16x16x32_f16 v[244:247], v[212:215], v[188:191], v[110:113]\n"
      "ds_read_b128 v[212:215], v123 offset:29696\n"
      "v_mul_f32 v157, v160, v157\n"
      "v_mfma_f32_16x16x32_f16 v[244:247], v[46:49], v[44:47], v[244:247]\n"
      "v_mul_f32_e64 v159, v160, v159\n"
      "v_cvt_pk_bf16_f32 v255, v158, v159\n"
      "v_cvt_pk_bf16_f32 v254, v156, v157\n"
      "v_mfma_f32_16x16x32_f16 v[188:191], v[168:171], v[180:183], v[76:79]\n"
      "ds_write_b128 v124, v[252:255]\n"
      "v_mfma_f32_16x16x32_bf16 v[166:169], v[66:69], v[224:227], 0\n"
      "ds_read_b128 v[252:255], v123 offset:32768\n"
      "v_mul_f32_e64 v172, v160, v172\n"
      "v_mfma_f32_16x16x32_bf16 v[166:169], v[66:69], v[62:65], v[166:169]\n"
      "s_min_u32 m0, s28, s3\n"
      "v_mul_f32 v174, v160, v174\n"
      "v_exp_f32 v228, v228\n"
      "v_mfma_f32_16x16x16_f16 v[188:191], v[206:207], v[204:205], v[188:191]\n"
      "v_mul_f32 v173, v160, v173\n"
      "v_cvt_pk_bf16_f32 v172, v172, v173\n"
      "v_exp_f32 v245, v245\n"
      "v_mfma_f32_16x16x32_bf16 v[204:207], v[216:219], v[224:227], 0\n"
      "s_movrels_b32 s9, s80\n"
      "v_cvt_pk_bf16_f32 v173, v174, v175\n"
      "v_cndmask_b32 v235, v235, v165, s[44:45]\n"
      "v_mfma_f32_16x16x32_bf16 v[204:207], v[50:53], v[62:65], v[204:207]\n"
      "v_cndmask_b32 v235, v235, v189, s[46:47]\n"
      "v_exp_f32_e64 v231, v231\n"
      "v_exp_f32 v235, v235\n"
      "v_mfma_f32_16x16x32_bf16 v[168:171], v[248:251], v[224:227], 0\n"
      "s_lshr_b32 s8, s9, 8\n"
      "v_cvt_pk_bf16_f32 v235, v235, v235\n"
      "s_and_b32 s9, s9, 0xff\n"
      "v_mfma_f32_16x16x32_bf16 v[168:171], v[54:57], v[62:65], v[168:171]\n"
      "ds_write_b16 v130, v235\n"
      "v_mfma_f32_16x16x32_bf16 v[174:177], v[192:195], v[224:227], 0\n"
      "ds_read_b128 v[156:159], v131\n"
      "s_waitcnt lgkmcnt(2)\n"
      "v_mfma_f32_16x16x32_bf16 v[174:177], v[58:61], v[62:65], v[174:177]\n"
      "ds_read_b128 v[160:163], v131 offset:64\n"
      "v_exp_f32 v244, v244\n"
      "v_mfma_f32_16x16x32_bf16 v[188:191], a[216:219], v[208:211], 0\n"
      "s_mul_i32 s10, s8, 73\n"
      "v_exp_f32 v198, v198\n"
      "v_mfma_f32_16x16x32_bf16 v[188:191], a[228:231], v[220:223], v[188:191]\n"
      "s_add_u32 s10, s10, s9\n"
      "v_exp_f32_e64 v200, v200\n"
      "v_mfma_f32_16x16x32_bf16 v[224:227], a[220:223], v[208:211], 0\n"
      "s_mul_i32 s28, s10, 4802\n"
      "v_exp_f32 v201, v201\n"
      "v_mfma_f32_16x16x32_bf16 v[224:227], a[232:235], v[220:223], v[224:227]\n"
      "s_add_u32 s56, s18, s28\n"
      "v_exp_f32 v199, v199\n"
      "v_mfma_f32_16x16x32_bf16 v[188:191], a[240:243], v[184:187], v[188:191]\n"
      "s_addc_u32 s57, s19, 0\n"
      "v_cvt_pk_bf16_f32 v198, v198, v199\n"
      "v_cvt_pk_bf16_f32 v199, v200, v201\n"
      "v_mfma_f32_16x16x32_bf16 v[188:191], a[252:255], v[212:215], v[188:191]\n"
      "s_and_b32 s56, s56, -16\n"
      "v_exp_f32 v229, v229\n"
      "v_mfma_f32_16x16x32_bf16 v[224:227], a[244:247], v[184:187], v[224:227]\n"
      "s_sub_u32 s58, s56, 110592\n"
      "v_exp_f32 v230, v230\n"
      "v_mfma_f32_16x16x32_bf16 v[224:227], v[0:3], v[212:215], v[224:227]\n"
      "s_waitcnt lgkmcnt(0)\n"
      "v_cvt_pk_bf16_f32 v200, v228, v229\n"
      "v_cvt_pk_bf16_f32 v201, v230, v231\n"
      "v_mfma_f32_16x16x32_bf16 v[228:231], a[224:227], v[208:211], 0\n"
      "ds_read_b128 v[208:211], v123 offset:35840\n"
      "v_cvt_pk_bf16_f32 v62, v244, v245\n"
      "v_mfma_f32_16x16x32_bf16 v[228:231], a[236:239], v[220:223], v[228:231]\n"
      "ds_read_b128 v[144:147], v115 offset:18432\n"
      "v_exp_f32 v246, v246\n"
      "v_mfma_f32_16x16x32_bf16 v[228:231], a[248:251], v[184:187], v[228:231]\n"
      "s_subb_u32 s59, s57, 0\n"
      "ds_read_b128 v[232:235], v114 offset:18432\n"
      "v_mfma_f32_16x16x32_bf16 v[228:231], v[4:7], v[212:215], v[228:231]\n"
      "s_sub_u32 s56, s56, 73728\n"
      "v_exp_f32 v247, v247\n"
      "v_mfma_f32_16x16x32_bf16 v[220:223], v[156:159], v[66:69], 0\n"
      "s_subb_u32 s57, s57, 0\n"
      "v_exp_f32_e64 v164, v164\n"
      "v_mfma_f32_16x16x32_bf16 v[220:223], v[160:163], v[66:69], v[220:223]\n"
      "s_mul_i32 s28, s8, 1373568\n"
      "v_cvt_pk_bf16_f32 v63, v246, v247\n"
      "v_cvt_pk_bf16_f32 v64, v164, 0\n"
      "v_mfma_f32_16x16x32_bf16 v[244:247], v[156:159], v[216:219], 0\n"
      "s_mul_i32 s29, s9, 2688\n"
      "v_rcp_f32_e64 v166, v166\n"
      "v_mfma_f32_16x16x32_bf16 v[244:247], v[160:163], v[50:53], v[244:247]\n"
      "s_add_u32 s28, s28, s29\n"
      "v_mul_f32 v205, v166, v205\n"
      "v_mul_f32 v204, v166, v204\n"
      "v_mfma_f32_16x16x32_bf16 v[180:183], v[66:69], v[198:201], 0\n"
      "s_waitcnt lgkmcnt(2)\n"
      "v_mul_f32 v207, v166, v207\n"
      "v_cvt_pk_bf16_f32 v204, v204, v205\n"
      "v_mfma_f32_16x16x32_bf16 v[180:183], v[66:69], v[62:65], v[180:183]\n"
      "s_cmp_eq_u32 s8, 72\n"
      "v_mul_f32 v206, v166, v206\n"
      "v_cvt_pk_bf16_f32 v205, v206, v207\n"
      "v_mfma_f32_16x16x32_bf16 v[188:191], v[8:11], v[252:255], v[188:191]\n"
      "s_cselect_b32 s8, 0, 100270464\n"
      "v_mul_f32_e64 v168, v166, v168\n"
      "v_mfma_f32_16x16x32_bf16 v[188:191], v[20:23], v[208:211], v[188:191]\n"
      "s_waitcnt lgkmcnt(0)\n"
      "v_mul_f32 v169, v166, v169\n"
      "v_cvt_pk_bf16_f32 v206, v168, v169\n"
      "v_mfma_f32_16x16x32_bf16 v[224:227], v[12:15], v[252:255], v[224:227]\n"
      "s_cmp_eq_u32 s9, 72\n"
      "v_mul_f32 v171, v166, v171\n"
      "v_mul_f32 v170, v166, v170\n"
      "v_mfma_f32_16x16x32_bf16 v[224:227], v[24:27], v[208:211], v[224:227]\n"
      "s_cselect_b32 s9, 0, 196224\n"
      "v_cvt_pk_bf16_f32 v207, v170, v171\n"
      "v_mul_f32 v175, v166, v175\n"
      "v_mfma_f32_16x16x32_bf16 v[228:231], v[16:19], v[252:255], v[228:231]\n"
      "s_add_u32 s10, s8, s9\n"
      "v_mul_f32_e64 v174, v166, v174\n"
      "v_cvt_pk_bf16_f32 v174, v174, v175\n"
      "v_mfma_f32_16x16x32_bf16 v[228:231], v[28:31], v[208:211], v[228:231]\n"
      "ds_write_b128 v124, v[204:207] offset:1024\n"
      "v_mfma_f32_16x16x32_bf16 v[184:187], v[216:219], v[198:201], 0\n"
      "ds_read_b128 v[168:171], v114 offset:24576\n"
      "v_mul_f32_e64 v176, v166, v176\n"
      "v_mfma_f32_16x16x32_bf16 v[184:187], v[50:53], v[62:65], v[184:187]\n"
      "ds_read_b128 v[252:255], v115 offset:24576\n"
      "v_mul_f32 v177, v166, v177\n"
      "v_mfma_f32_16x16x32_bf16 v[164:167], v[248:251], v[198:201], 0\n"
      "s_add_u32 s28, s28, s10\n"
      "v_cvt_pk_bf16_f32 v175, v176, v177\n"
      "v_fma_f32 v196, v188, v188, s39\n"
      "v_mfma_f32_16x16x32_bf16 v[164:167], v[54:57], v[62:65], v[164:167]\n"
      "ds_write2st64_b64 v125, v[172:173], v[174:175] offset0:0 offset1:2\n"
      "v_mfma_f32_16x16x32_bf16 v[204:207], v[192:195], v[198:201], 0\n"
      "s_add_u32 s52, s16, s28\n"
      "v_fmac_f32 v196, v189, v189\n"
      "v_fmac_f32 v196, v190, v190\n"
      "v_mfma_f32_16x16x32_bf16 v[204:207], v[58:61], v[62:65], v[204:207]\n"
      "s_addc_u32 s53, s17, 0\n"
      "v_fmac_f32 v196, v191, v191\n"
      "v_fmac_f32 v196, v224, v224\n"
      "v_mfma_f32_16x16x32_bf16 v[212:215], v[156:159], v[248:251], 0\n"
      "s_add_u32 s27, s27, 1\n"
      "v_fmac_f32 v196, v225, v225\n"
      "v_fmac_f32 v196, v226, v226\n"
      "v_mfma_f32_16x16x32_bf16 v[212:215], v[160:163], v[54:57], v[212:215]\n"
      "s_cmp_ge_u32 s27, s26\n"
      "v_fmac_f32 v196, v227, v227\n"
      "v_fmac_f32 v196, v228, v228\n"
      "v_mfma_f32_16x16x32_bf16 v[198:201], v[156:159], v[192:195], 0\n"
      "s_cselect_b32 s28, 1, 0\n"
      "v_fmac_f32 v196, v229, v229\n"
      "v_fmac_f32 v196, v230, v230\n"
      "v_mfma_f32_16x16x32_bf16 v[198:201], v[160:163], v[58:61], v[198:201]\n"
      "v_fmac_f32 v196, v231, v231\n"
      "v_mov_b32 v215, v196\n"
      "v_mfma_f32_16x16x32_bf16 v[208:211], a[0:3], v[232:235], 0\n"
      "v_permlane16_swap_b32_e64 v197, v178\n"
      "v_mfma_f32_16x16x32_bf16 v[208:211], a[36:39], v[144:147], v[208:211]\n"
      "s_waitcnt lgkmcnt(1)\n"
      "v_mov_b32_dpp v220, v220 quad_perm:[0,0,0,0] row_mask:0xf bank_mask:0xf\n"
      "v_add_f32 v197, v197, v178\n"
      "v_mfma_f32_16x16x32_bf16 v[216:219], a[4:7], v[232:235], 0\n"
      "v_permlane16_swap_b32 v196, v215\n"
      "v_mfma_f32_16x16x32_bf16 v[216:219], a[40:43], v[144:147], v[216:219]\n"
      "v_add_f32 v196, v196, v215\n"
      "v_cndmask_b32 v244, v244, v212, s[42:43]\n"
      "v_mfma_f32_16x16x32_bf16 v[176:179], a[8:11], v[232:235], 0\n"
      "v_permlane32_swap_b32 v197, v196\n"
      "v_mfma_f32_16x16x32_bf16 v[176:179], a[44:47], v[144:147], v[176:179]\n"
      "v_add_f32_e64 v197, v197, v196\n"
      "v_cndmask_b32 v244, v244, v198, s[44:45]\n"
      "v_mfma_f32_16x16x32_bf16 v[212:215], a[12:15], v[232:235], 0\n"
      "v_rcp_f32_e64 v220, v220\n"
      "v_mfma_f32_16x16x32_bf16 v[212:215], a[48:51], v[144:147], v[212:215]\n"
      "ds_write_b32 v129, v197\n"
      "v_mfma_f32_16x16x32_bf16 v[196:199], a[16:19], v[232:235], 0\n"
      "v_mul_f32_e64 v244, v220, v244\n"
      "v_cvt_pk_bf16_f32 v244, v244, v244\n"
      "v_mfma_f32_16x16x32_bf16 v[196:199], a[52:55], v[144:147], v[196:199]\n"
      "ds_write_b16 v135, v244\n"
      "v_mfma_f32_16x16x32_bf16 v[160:163], a[20:23], v[232:235], 0\n"
      "s_xor_b32 s29, s10, s11\n"
      "v_mfma_f32_16x16x32_bf16 v[160:163], a[56:59], v[144:147], v[160:163]\n"
      "s_or_b32 s28, s28, s29\n"
      "L_join_30_%=:\n"
      "s_cbranch_scc0 L_loop_%=\n"
      "L_tail0_%=:\n"
      "s_cmp_eq_u32 s10, s11\n"
      "s_cbranch_scc1 L_sk_35_%=\n"
      "s_mov_b32 s11, s10\n"
      "s_sub_u32 s28, s60, s8\n"
      "s_sub_u32 s29, s61, s9\n"
      "s_mov_b32 s60, s8\n"
      "s_mov_b32 s61, s9\n"
      "s_mov_b64 exec, s[64:65]\n"
      "v_add_u32 v116, s28, v116\n"
      "s_mov_b64 exec, s[70:71]\n"
      "v_add_u32 v116, s29, v116\n"
      "s_mov_b64 exec, s[66:67]\n"
      "v_add_u32 v117, s28, v117\n"
      "s_mov_b64 exec, s[72:73]\n"
      "v_add_u32 v117, s29, v117\n"
      "s_mov_b64 exec, s[68:69]\n"
      "v_add_u32 v118, s28, v118\n"
      "s_mov_b64 exec, s[74:75]\n"
      "v_add_u32 v118, s29, v118\n"
      "s_mov_b64 exec, -1\n"
      "s_nop 4\n"
      "L_sk_35_%=:\n"
      "s_cmp_lt_u32 s27, s26\n"
      "s_cbranch_scc1 L_loop_%=\n"
      "s_nop 7\n"
      "v_rcp_f32 v180, v180\n"
      "v_mbcnt_lo_u32_b32 v234, -1, 0\n"
      "v_pk_mul_f32 v[166:167], v[166:167], v[180:181] op_sel_hi:[1,0]\n"
      "v_pk_mul_f32 v[204:205], v[204:205], v[180:181] op_sel_hi:[1,0]\n"
      "v_cvt_pk_bf16_f32 v204, v204, v205\n"
      "v_pk_mul_f32 v[184:185], v[184:185], v[180:181] op_sel_hi:[1,0]\n"
      "v_pk_mul_f32 v[164:165], v[164:165], v[180:181] op_sel_hi:[1,0]\n"
      "v_cvt_pk_bf16_f32 v184, v184, v185\n"
      "v_mbcnt_hi_u32_b32 v234, -1, v234\n"
      "v_pk_mul_f32 v[186:187], v[186:187], v[180:181] op_sel_hi:[1,0]\n"
      "v_lshrrev_b32 v235, 4, v234\n"
      "v_and_b32 v232, 15, v234\n"
      "v_cvt_pk_bf16_f32 v185, v186, v187\n"
      "v_pk_mul_f32 v[206:207], v[206:207], v[180:181] op_sel_hi:[1,0]\n"
      "v_mul_u32_u24 v235, 352, v235\n"
      "v_cvt_pk_bf16_f32 v186, v164, v165\n"
      "v_cvt_pk_bf16_f32 v187, v166, v167\n"
      "v_cvt_pk_bf16_f32 v205, v206, v207\n"
      "ds_write_b64 v125, v[204:205] offset:2048\n"
      "ds_write_b128 v124, v[184:187] offset:2048\n"
      "v_lshl_add_u32 v235, v232, 4, v235\n"
      "v_add_u32 v235, 150640, v235\n"
      "s_waitcnt lgkmcnt(0)\n"
      "s_barrier\n"
      "ds_read_b128 v[212:215], v128\n"
      "ds_read_b128 v[168:171], v123\n"
      "ds_read_b128 v[144:147], v123 offset:3072\n"
      "s_sub_u32 s28, s27, 3\n"
      "s_cselect_b64 s[54:55], s[12:13], s[22:23]\n"
      "s_max_i32 m0, s28, 0\n"
      "ds_read_b128 v[176:179], v121\n"
      "ds_read_b128 v[206:209], v119 offset:12288\n"
      "s_movrels_b32 s9, s80\n"
      "ds_read_b128 v[156:159], v120\n"
      "s_lshr_b32 s8, s9, 8\n"
      "s_and_b32 s9, s9, 0xff\n"
      "s_mul_i32 s28, s8, 1373568\n"
      "s_mul_i32 s29, s9, 2688\n"
      "s_add_u32 s28, s28, s29\n"
      "s_cmp_eq_u32 s8, 72\n"
      "ds_read_b128 v[244:247], v123 offset:6144\n"
      "ds_read_b128 v[252:255], v119 offset:6144\n"
      "s_cselect_b32 s8, 0, 100270464\n"
      "s_cmp_eq_u32 s9, 72\n"
      "s_cselect_b32 s9, 0, 196224\n"
      "ds_read_b128 v[184:187], v119\n"
      "s_add_u32 s10, s8, s9\n"
      "s_add_u32 s28, s28, s10\n"
      "s_waitcnt lgkmcnt(8)\n"
      "v_add_f32 v212, |v212|, |v213|\n"
      "v_add_f32 v214, |v214|, |v215|\n"
      "s_add_u32 s54, s54, s28\n"
      "s_waitcnt lgkmcnt(6)\n"
      "v_mfma_f32_16x16x32_bf16 v[172:175], a[216:219], v[168:171], 0\n"
      "ds_read_b128 v[216:219], v123 offset:9216\n"
      "v_add_f32 v212, v212, v214\n"
      "v_mfma_f32_16x16x32_bf16 v[172:175], a[228:231], v[144:147], v[172:175]\n"
      "ds_read_b128 v[202:205], v123 offset:1024\n"
      "v_rsq_f32 v212, v212\n"
      "v_mfma_f32_16x16x32_bf16 v[160:163], a[220:223], v[168:171], 0\n"
      "ds_read_b128 v[198:201], v123 offset:15360\n"
      "v_mul_f32 v212, s41, v212\n"
      "v_mfma_f32_16x16x32_bf16 v[160:163], a[232:235], v[144:147], v[160:163]\n"
      "ds_read_b128 v[164:167], v123 offset:12288\n"
      "v_mov_b32 v232, v212\n"
      "v_mfma_f32_16x16x32_bf16 v[192:195], a[224:227], v[168:171], 0\n"
      "s_addc_u32 s55, s55, 0\n"
      "v_permlane32_swap_b32 v212, v232\n"
      "v_mov_b32 v233, v212\n"
      "s_cmp_lg_u32 s10, s11\n"
      "v_mfma_f32_16x16x32_bf16 v[192:195], a[236:239], v[144:147], v[192:195]\n"
      "ds_read_b128 v[168:171], v123 offset:4096\n"
      "ds_read_b128 v[144:147], v123 offset:7168\n"
      "v_permlane16_swap_b32 v212, v233\n"
      "v_mul_f32 v243, v212, v243\n"
      "v_mul_f32 v241, v212, v241\n"
      "v_mul_f32_e64 v240, v212, v240\n"
      "s_waitcnt lgkmcnt(5)\n"
      "v_mfma_f32_16x16x32_bf16 v[172:175], a[240:243], v[244:247], v[172:175]\n"
      "s_cbranch_scc1 L_edge_36_%=\n"
      "L_eb_37_%=:\n"
      "v_mfma_f32_16x16x32_bf16 v[172:175], a[252:255], v[216:219], v[172:175]\n"
      "global_store_dwordx4 v116, v[184:187], s[54:55] sc1\n"
      "v_mfma_f32_16x16x32_bf16 v[160:163], a[244:247], v[244:247], v[160:163]\n"
      "ds_read_b128 v[220:223], v123 offset:10240\n"
      "v_mul_f32_e64 v242, v212, v242\n"
      "v_mfma_f32_16x16x32_bf16 v[160:163], v[0:3], v[216:219], v[160:163]\n"
      "ds_read_b128 v[184:187], v123 offset:13312\n"
      "v_cvt_pk_bf16_f32 v240, v240, v241\n"
      "v_mfma_f32_16x16x32_bf16 v[192:195], a[248:251], v[244:247], v[192:195]\n"
      "s_waitcnt lgkmcnt(4)\n"
      "v_mul_f32 v137, v212, v137\n"
      "v_mul_f32_e64 v136, v212, v136\n"
      "v_mfma_f32_16x16x32_bf16 v[192:195], v[4:7], v[216:219], v[192:195]\n"
      "ds_read_b128 v[214:217], v123 offset:16384\n"
      "v_cvt_pk_bf16_f32 v241, v242, v243\n"
      "v_mfma_f32_16x16x32_bf16 v[172:175], v[8:11], v[164:167], v[172:175]\n"
      "s_waitcnt lgkmcnt(3)\n"
      "v_mul_f32 v138, v212, v138\n"
      "v_mul_f32 v139, v212, v139\n"
      "v_mfma_f32_16x16x32_bf16 v[172:175], v[20:23], v[198:201], v[172:175]\n"
      "v_mul_f32 v153, v212, v153\n"
      "v_mul_f32_e64 v152, v212, v152\n"
      "v_mfma_f32_16x16x32_bf16 v[160:163], v[12:15], v[164:167], v[160:163]\n"
      "v_cvt_pk_bf16_f32 v242, v136, v137\n"
      "v_cvt_pk_bf16_f32 v243, v138, v139\n"
      "v_mfma_f32_16x16x32_bf16 v[160:163], v[24:27], v[198:201], v[160:163]\n"
      "ds_write_b128 v126, v[240:243] offset:18432\n"
      "v_mfma_f32_16x16x32_bf16 v[192:195], v[16:19], v[164:167], v[192:195]\n"
      "ds_read_b128 v[136:139], v123 offset:2048\n"
      "v_mfma_f32_16x16x32_bf16 v[192:195], v[28:31], v[198:201], v[192:195]\n"
      "global_store_dwordx4 v116, v[252:255], s[54:55] offset:128 sc1\n"
      "v_mfma_f32_16x16x32_bf16 v[240:243], a[216:219], v[202:205], 0\n"
      "ds_read_b128 v[244:247], v123 offset:5120\n"
      "v_cvt_pk_bf16_f32 v152, v152, v153\n"
      "v_mfma_f32_16x16x32_bf16 v[240:243], a[228:231], v[168:171], v[240:243]\n"
      "s_waitcnt lgkmcnt(3)\n"
      "v_mul_f32 v155, v212, v155\n"
      "v_mul_f32 v154, v212, v154\n"
      "v_mfma_f32_16x16x32_bf16 v[198:201], a[220:223], v[202:205], 0\n"
      "s_lshl_b32 s29, s24, 8\n"
      "v_cvt_pk_bf16_f32 v153, v154, v155\n"
      "v_mul_f32_e64 v148, v233, v148\n"
      "v_mfma_f32_16x16x32_bf16 v[198:201], a[232:235], v[168:171], v[198:201]\n"
      "ds_write_b64 v127, v[152:153] offset:18432\n"
      "v_mfma_f32_16x16x32_bf16 v[152:155], a[224:227], v[202:205], 0\n"
      "ds_read_b128 v[210:213], v123 offset:8192\n"
      "v_mul_f32_e64 v149, v233, v149\n"
      "v_mfma_f32_16x16x32_bf16 v[152:155], a[236:239], v[168:171], v[152:155]\n"
      "ds_read_b128 v[168:171], v123 offset:11264\n"
      "v_cvt_pk_bf16_f32 v148, v148, v149\n"
      "v_mfma_f32_16x16x32_bf16 v[240:243], a[240:243], v[144:147], v[240:243]\n"
      "v_mul_f32 v151, v233, v151\n"
      "v_mul_f32 v150, v233, v150\n"
      "v_mfma_f32_16x16x32_bf16 v[240:243], a[252:255], v[220:223], v[240:243]\n"
      "v_cvt_pk_bf16_f32 v149, v150, v151\n"
      "v_mul_f32 v236, v233, v236\n"
      "v_mfma_f32_16x16x32_bf16 v[198:201], a[244:247], v[144:147], v[198:201]\n"
      "v_mul_f32 v237, v233, v237\n"
      "v_cvt_pk_bf16_f32 v150, v236, v237\n"
      "v_mfma_f32_16x16x32_bf16 v[198:201], v[0:3], v[220:223], v[198:201]\n"
      "v_mul_f32 v239, v233, v239\n"
      "v_mul_f32 v238, v233, v238\n"
      "v_mfma_f32_16x16x32_bf16 v[152:155], a[248:251], v[144:147], v[152:155]\n"
      "s_add_u32 s29, s29, 159088\n"
      "v_mul_f32_e64 v140, v233, v140\n"
      "v_cvt_pk_bf16_f32 v151, v238, v239\n"
      "v_mfma_f32_16x16x32_bf16 v[152:155], v[4:7], v[220:223], v[152:155]\n"
      "ds_write_b128 v126, v[148:151] offset:20480\n"
      "v_mfma_f32_16x16x32_bf16 v[240:243], v[8:11], v[184:187], v[240:243]\n"
      "ds_read_b128 v[144:147], v123 offset:14336\n"
      "v_mul_f32_e64 v141, v233, v141\n"
      "v_mfma_f32_16x16x32_bf16 v[240:243], v[20:23], v[214:217], v[240:243]\n"
      "ds_read_b128 v[148:151], v123 offset:17408\n"
      "v_cvt_pk_bf16_f32 v140, v140, v141\n"
      "v_mfma_f32_16x16x32_bf16 v[198:201], v[12:15], v[184:187], v[198:201]\n"
      "s_waitcnt lgkmcnt(3)\n"
      "v_mul_f32 v143, v233, v143\n"
      "v_mul_f32_e64 v142, v233, v142\n"
      "v_mfma_f32_16x16x32_bf16 v[198:201], v[24:27], v[214:217], v[198:201]\n"
      "v_cvt_pk_bf16_f32 v141, v142, v143\n"
      "v_fma_f32 v233, v172, v172, s39\n"
      "v_mfma_f32_16x16x32_bf16 v[152:155], v[16:19], v[184:187], v[152:155]\n"
      "global_store_dwordx4 v116, v[206:209], s[54:55] offset:256 sc1\n"
      "v_mfma_f32_16x16x32_bf16 v[152:155], v[28:31], v[214:217], v[152:155]\n"
      "ds_write_b64 v127, v[140:141] offset:20480\n"
      "v_mfma_f32_16x16x32_bf16 v[236:239], a[216:219], v[136:139], 0\n"
      "ds_read_b128 v[184:187], v235\n"
      "v_fmac_f32_e64 v233, v173, v173\n"
      "v_mfma_f32_16x16x32_bf16 v[236:239], a[228:231], v[244:247], v[236:239]\n"
      "ds_read_b128 v[140:143], v235 offset:1408\n"
      "v_fmac_f32 v233, v174, v174\n"
      "v_mfma_f32_16x16x32_bf16 v[220:223], a[220:223], v[136:139], 0\n"
      "v_fmac_f32 v233, v175, v175\n"
      "v_fmac_f32_e64 v233, v160, v160\n"
      "v_mfma_f32_16x16x32_bf16 v[220:223], a[232:235], v[244:247], v[220:223]\n"
      "s_waitcnt lgkmcnt(3)\n"
      "v_fmac_f32 v233, v161, v161\n"
      "v_fmac_f32 v233, v162, v162\n"
      "v_mfma_f32_16x16x32_bf16 v[216:219], a[224:227], v[136:139], 0\n"
      "v_fmac_f32 v233, v163, v163\n"
      "v_fmac_f32 v233, v192, v192\n"
      "v_mfma_f32_16x16x32_bf16 v[216:219], a[236:239], v[244:247], v[216:219]\n"
      "v_fmac_f32 v233, v193, v193\n"
      "v_fmac_f32 v233, v194, v194\n"
      "v_mfma_f32_16x16x32_bf16 v[236:239], a[240:243], v[210:213], v[236:239]\n"
      "v_mul_f32 v189, v232, v189\n"
      "v_fmac_f32 v233, v195, v195\n"
      "v_mfma_f32_16x16x32_bf16 v[236:239], a[252:255], v[168:171], v[236:239]\n"
      "ds_read_b128 v[204:207], v235 offset:2816\n"
      "v_mul_f32 v188, v232, v188\n"
      "v_mfma_f32_16x16x32_bf16 v[220:223], a[244:247], v[210:213], v[220:223]\n"
      "ds_read_b128 v[136:139], v235 offset:4224\n"
      "v_mul_f32 v190, v232, v190\n"
      "v_mfma_f32_16x16x32_bf16 v[220:223], v[0:3], v[168:171], v[220:223]\n"
      "v_mul_f32 v191, v232, v191\n"
      "v_cvt_pk_bf16_f32 v188, v188, v189\n"
      "v_mfma_f32_16x16x32_bf16 v[216:219], a[248:251], v[210:213], v[216:219]\n"
      "global_store_dwordx4 v117, v[156:159], s[54:55] sc1\n"
      "v_mfma_f32_16x16x32_bf16 v[216:219], v[4:7], v[168:171], v[216:219]\n"
      "v_mul_f32_e64 v224, v232, v224\n"
      "v_cvt_pk_bf16_f32 v189, v190, v191\n"
      "v_mfma_f32_16x16x32_bf16 v[236:239], v[8:11], v[144:147], v[236:239]\n"
      "v_mul_f32_e64 v225, v232, v225\n"
      "v_cvt_pk_bf16_f32 v190, v224, v225\n"
      "v_mfma_f32_16x16x32_bf16 v[236:239], v[20:23], v[148:151], v[236:239]\n"
      "ds_read_b128 v[168:171], v235 offset:5632\n"
      "v_mul_f32 v227, v232, v227\n"
      "v_mfma_f32_16x16x32_bf16 v[220:223], v[12:15], v[144:147], v[220:223]\n"
      "s_waitcnt lgkmcnt(3)\n"
      "v_mul_f32_e64 v226, v232, v226\n"
      "v_cvt_pk_bf16_f32 v191, v226, v227\n"
      "v_mfma_f32_16x16x32_bf16 v[220:223], v[24:27], v[148:151], v[220:223]\n"
      "ds_write_b128 v126, v[188:191] offset:22528\n"
      "v_mfma_f32_16x16x32_bf16 v[216:219], v[16:19], v[144:147], v[216:219]\n"
      "ds_read_b128 v[144:147], v235 offset:7040\n"
      "v_mul_f32 v228, v232, v228\n"
      "v_mfma_f32_16x16x32_bf16 v[216:219], v[28:31], v[148:151], v[216:219]\n"
      "s_waitcnt lgkmcnt(3)\n"
      "v_mul_f32 v229, v232, v229\n"
      "v_cvt_pk_bf16_f32 v228, v228, v229\n"
      "v_mfma_f32_16x16x32_bf16 v[148:151], a[216:219], v[184:187], 0\n"
      "v_mul_f32 v231, v232, v231\n"
      "v_mul_f32_e64 v230, v232, v230\n"
      "v_mfma_f32_16x16x32_bf16 v[148:151], a[228:231], v[140:143], v[148:151]\n"
      "ds_read_b128 v[210:213], v235 offset:256\n"
      "v_cvt_pk_bf16_f32 v229, v230, v231\n"
      "v_mfma_f32_16x16x32_bf16 v[224:227], a[220:223], v[184:187], 0\n"
      "ds_read_b128 v[188:191], v235 offset:1664\n"
      "v_fma_f32 v232, v240, v240, s39\n"
      "v_mfma_f32_16x16x32_bf16 v[224:227], a[232:235], v[140:143], v[224:227]\n"
      "ds_write_b64 v127, v[228:229] offset:22528\n"
      "v_mfma_f32_16x16x32_bf16 v[164:167], a[224:227], v[184:187], 0\n"
      "v_fmac_f32 v232, v241, v241\n"
      "v_fmac_f32 v232, v242, v242\n"
      "v_mfma_f32_16x16x32_bf16 v[164:167], a[236:239], v[140:143], v[164:167]\n"
      "ds_read_b128 v[140:143], v235 offset:3072\n"
      "v_fmac_f32 v232, v243, v243\n"
      "v_mfma_f32_16x16x32_bf16 v[148:151], a[240:243], v[204:207], v[148:151]\n"
      "v_fmac_f32 v232, v198, v198\n"
      "v_fmac_f32_e64 v232, v199, v199\n"
      "v_mfma_f32_16x16x32_bf16 v[148:151], a[252:255], v[136:139], v[148:151]\n"
      "ds_read_b128 v[228:231], v235 offset:4480\n"
      "v_fmac_f32 v232, v200, v200\n"
      "v_mfma_f32_16x16x32_bf16 v[224:227], a[244:247], v[204:207], v[224:227]\n"
      "v_fmac_f32 v232, v201, v201\n"
      "v_fmac_f32_e64 v232, v152, v152\n"
      "v_mfma_f32_16x16x32_bf16 v[224:227], v[0:3], v[136:139], v[224:227]\n"
      "v_fmac_f32 v232, v153, v153\n"
      "v_fmac_f32 v232, v154, v154\n"
      "v_mfma_f32_16x16x32_bf16 v[164:167], a[248:251], v[204:207], v[164:167]\n"
      "s_waitcnt lgkmcnt(3)\n"
      "v_fmac_f32 v232, v155, v155\n"
      "v_lshl_add_u32 v203, v234, 2, s29\n"
      "v_mfma_f32_16x16x32_bf16 v[164:167], v[4:7], v[136:139], v[164:167]\n"
      "ds_read_b32 v203, v203\n"
      "v_lshrrev_b32_e64 v138, 3, v234\n"
      "v_mfma_f32_16x16x32_bf16 v[148:151], v[8:11], v[168:171], v[148:151]\n"
      "ds_read_b128 v[184:187], v235 offset:5888\n"
      "v_fma_f32 v234, v236, v236, s39\n"
      "v_mfma_f32_16x16x32_bf16 v[148:151], v[20:23], v[144:147], v[148:151]\n"
      "ds_read_b128 v[204:207], v235 offset:7296\n"
      "v_fmac_f32 v234, v237, v237\n"
      "v_mfma_f32_16x16x32_bf16 v[224:227], v[12:15], v[168:171], v[224:227]\n"
      "s_lshl_b32 s29, s24, 3\n"
      "v_add_u32_e64 v138, s29, v138\n"
      "v_cmp_gt_u32 s[48:49], s26, v138\n"
      "v_mfma_f32_16x16x32_bf16 v[224:227], v[24:27], v[144:147], v[224:227]\n"
      "v_fmac_f32 v234, v238, v238\n"
      "v_fmac_f32 v234, v239, v239\n"
      "v_mfma_f32_16x16x32_bf16 v[164:167], v[16:19], v[168:171], v[164:167]\n"
      "v_fmac_f32 v234, v220, v220\n"
      "v_fmac_f32 v234, v221, v221\n"
      "v_mfma_f32_16x16x32_bf16 v[164:167], v[28:31], v[144:147], v[164:167]\n"
      "s_mov_b32 exec_hi, 0\n"
      "global_store_dwordx4 v118, v[176:179], s[54:55] sc1\n"
      "s_mov_b32 exec_hi, -1\n"
      "v_mfma_f32_16x16x32_bf16 v[144:147], a[216:219], v[210:213], 0\n"
      "s_waitcnt lgkmcnt(3)\n"
      "v_fmac_f32 v234, v222, v222\n"
      "v_fmac_f32 v234, v223, v223\n"
      "v_mfma_f32_16x16x32_bf16 v[144:147], a[228:231], v[188:191], v[144:147]\n"
      "v_fmac_f32 v234, v216, v216\n"
      "v_fmac_f32 v234, v217, v217\n"
      "v_mfma_f32_16x16x32_bf16 v[136:139], a[220:223], v[210:213], 0\n"
      "v_fmac_f32 v234, v218, v218\n"
      "v_fmac_f32 v234, v219, v219\n"
      "v_mfma_f32_16x16x32_bf16 v[136:139], a[232:235], v[188:191], v[136:139]\n"
      "v_permlane16_swap_b32 v233, v232\n"
      "v_mfma_f32_16x16x32_bf16 v[168:171], a[224:227], v[210:213], 0\n"
      "v_mov_b32 v235, v234\n"
      "v_mfma_f32_16x16x32_bf16 v[168:171], a[236:239], v[188:191], v[168:171]\n"
      "v_mfma_f32_16x16x32_bf16 v[168:171], a[248:251], v[140:143], v[168:171]\n"
      "s_waitcnt lgkmcnt(0)\n"
      "v_mfma_f32_16x16x32_bf16 v[144:147], a[240:243], v[140:143], v[144:147]\n"
      "v_permlane16_swap_b32 v234, v235\n"
      "v_mfma_f32_16x16x32_bf16 v[144:147], a[252:255], v[228:231], v[144:147]\n"
      "v_add_f32 v234, v234, v235\n"
      "v_add_f32_e64 v233, v233, v232\n"
      "v_mfma_f32_16x16x32_bf16 v[136:139], a[244:247], v[140:143], v[136:139]\n"
      "s_nop 0\n"
      "v_permlane32_swap_b32 v233, v234\n"
      "v_mfma_f32_16x16x32_bf16 v[136:139], v[0:3], v[228:231], v[136:139]\n"
      "v_fma_f32 v235, v148, v148, s39\n"
      "v_fmac_f32 v235, v149, v149\n"
      "v_mfma_f32_16x16x32_bf16 v[136:139], v[12:15], v[184:187], v[136:139]\n"
      "v_add_f32 v233, v233, v234\n"
      "v_mfma_f32_16x16x32_bf16 v[136:139], v[24:27], v[204:207], v[136:139]\n"
      "ds_write_b32 v129, v233 offset:1024\n"
      "v_mfma_f32_16x16x32_bf16 v[168:171], v[4:7], v[228:231], v[168:171]\n"
      "v_fmac_f32 v235, v150, v150\n"
      "v_fmac_f32 v235, v151, v151\n"
      "v_mfma_f32_16x16x32_bf16 v[168:171], v[16:19], v[184:187], v[168:171]\n"
      "v_fmac_f32 v235, v224, v224\n"
      "v_fmac_f32 v235, v225, v225\n"
      "v_mfma_f32_16x16x32_bf16 v[144:147], v[8:11], v[184:187], v[144:147]\n"
      "v_fmac_f32 v235, v226, v226\n"
      "v_fmac_f32 v235, v227, v227\n"
      "v_mfma_f32_16x16x32_bf16 v[144:147], v[20:23], v[204:207], v[144:147]\n"
      "v_fmac_f32 v235, v164, v164\n"
      "v_fmac_f32 v235, v165, v165\n"
      "v_mfma_f32_16x16x32_bf16 v[168:171], v[28:31], v[204:207], v[168:171]\n"
      "v_fmac_f32 v235, v166, v166\n"
      "v_fmac_f32_e64 v235, v167, v167\n"
      "s_nop 2\n"
      "v_pk_mul_f32 v[232:233], v[144:145], v[144:145]\n"
      "v_pk_fma_f32 v[232:233], v[146:147], v[146:147], v[232:233]\n"
      "v_pk_fma_f32 v[232:233], v[136:137], v[136:137], v[232:233]\n"
      "v_pk_fma_f32 v[232:233], v[138:139], v[138:139], v[232:233]\n"
      "v_pk_fma_f32 v[232:233], v[168:169], v[168:169], v[232:233]\n"
      "v_pk_fma_f32 v[232:233], v[170:171], v[170:171], v[232:233]\n"
      "v_add_f32 v232, s39, v232\n"
      "v_add_f32 v229, v232, v233\n"
      "v_mov_b32 v234, v229\n"
      "v_mov_b32 v233, v234\n"
      "v_permlane16_swap_b32 v235, v229\n"
      "v_add_f32 v235, v235, v229\n"
      "v_permlane16_swap_b32 v234, v233\n"
      "v_add_f32 v234, v234, v233\n"
      "s_nop 1\n"
      "v_permlane32_swap_b32 v235, v234\n"
      "v_add_f32 v235, v235, v234\n"
      "ds_write_b32 v129, v235 offset:2048\n"
      "s_waitcnt lgkmcnt(0)\n"
      "s_barrier\n"
      "ds_read_b128 v[232:235], v128 offset:1024\n"
      "ds_read_b128 v[228:231], v128 offset:2048\n"
      "s_add_u32 s27, s27, 1\n"
      "ds_read_b128 v[140:143], v119 offset:18432\n"
      "s_sub_u32 s28, s27, 3\n"
      "s_cselect_b64 s[54:55], s[12:13], s[22:23]\n"
      "s_max_i32 m0, s28, 0\n"
      "ds_read_b128 v[176:179], v120 offset:18432\n"
      "s_movrels_b32 s9, s80\n"
      "s_lshr_b32 s8, s9, 8\n"
      "s_and_b32 s9, s9, 0xff\n"
      "s_mul_i32 s28, s8, 1373568\n"
      "s_mul_i32 s29, s9, 2688\n"
      "s_add_u32 s28, s28, s29\n"
      "s_cmp_eq_u32 s8, 72\n"
      "s_cselect_b32 s8, 0, 100270464\n"
      "s_cmp_eq_u32 s9, 72\n"
      "s_cselect_b32 s9, 0, 196224\n"
      "s_add_u32 s10, s8, s9\n"
      "s_add_u32 s28, s28, s10\n"
      "s_add_u32 s54, s54, s28\n"
      "s_addc_u32 s55, s55, 0\n"
      "ds_read_b128 v[244:247], v119 offset:24576\n"
      "ds_read_b128 v[212:215], v119 offset:30720\n"
      "ds_read_b128 v[188:191], v121 offset:18432\n"
      "s_cmp_lg_u32 s10, s11\n"
      "s_waitcnt lgkmcnt(6)\n"
      "v_add_f32 v234, |v234|, |v235|\n"
      "v_add_f32 v232, |v232|, |v233|\n"
      "v_add_f32 v232, v232, v234\n"
      "s_waitcnt lgkmcnt(5)\n"
      "v_rsq_f32 v232, v232\n"
      "s_cbranch_scc1 L_edge_38_%=\n"
      "L_eb_39_%=:\n"
      "v_mul_f32 v232, s41, v232\n"
      "v_mov_b32 v234, v232\n"
      "v_add_f32 v228, |v228|, |v229|\n"
      "v_add_f32 v230, |v230|, |v231|\n"
      "v_permlane32_swap_b32 v232, v234\n"
      "v_pk_mul_f32 v[220:221], v[220:221], v[234:235] op_sel_hi:[1,0]\n"
      "v_mov_b32 v231, v232\n"
      "v_pk_mul_f32 v[236:237], v[236:237], v[234:235] op_sel_hi:[1,0]\n"
      "v_add_f32 v228, v228, v230\n"
      "v_permlane16_swap_b32 v232, v231\n"
      "v_pk_mul_f32 v[152:153], v[152:153], v[230:231] op_sel:[0,1]\n"
      "v_cvt_pk_bf16_f32 v236, v236, v237\n"
      "v_pk_mul_f32 v[238:239], v[238:239], v[234:235] op_sel_hi:[1,0]\n"
      "v_cvt_pk_bf16_f32 v237, v238, v239\n"
      "v_rsq_f32_e64 v228, v228\n"
      "v_pk_mul_f32 v[192:193], v[192:193], v[232:233] op_sel_hi:[1,0]\n"
      "v_mul_f32_e64 v228, s41, v228\n"
      "v_pk_mul_f32 v[174:175], v[174:175], v[232:233] op_sel_hi:[1,0]\n"
      "v_mov_b32 v229, v228\n"
      "v_cvt_pk_bf16_f32 v192, v192, v193\n"
      "s_waitcnt lgkmcnt(0)\n"
      "global_store_dwordx4 v116, v[140:143], s[54:55] sc1\n"
      "v_permlane32_swap_b32_e64 v228, v229\n"
      "v_pk_mul_f32 v[240:241], v[240:241], v[230:231] op_sel:[0,1]\n"
      "v_pk_mul_f32 v[200:201], v[200:201], v[230:231] op_sel:[0,1]\n"
      "v_mov_b32_e64 v229, v228\n"
      "v_pk_mul_f32 v[194:195], v[194:195], v[232:233] op_sel_hi:[1,0]\n"
      "v_pk_mul_f32 v[242:243], v[242:243], v[230:231] op_sel:[0,1]\n"
      "v_permlane16_swap_b32_e64 v228, v229\n"
      "v_pk_mul_f32 v[216:217], v[216:217], v[234:235] op_sel_hi:[1,0]\n"
      "v_cvt_pk_bf16_f32 v240, v240, v241\n"
      "v_pk_mul_f32 v[148:149], v[148:149], v[228:229] op_sel_hi:[1,0]\n"
      "v_pk_mul_f32 v[172:173], v[172:173], v[232:233] op_sel_hi:[1,0]\n"
      "v_cvt_pk_bf16_f32 v238, v220, v221\n"
      "v_cvt_pk_bf16_f32 v241, v242, v243\n"
      "v_pk_mul_f32 v[224:225], v[224:225], v[228:229] op_sel_hi:[1,0]\n"
      "v_pk_mul_f32 v[154:155], v[154:155], v[230:231] op_sel:[0,1]\n"
      "v_pk_mul_f32 v[144:145], v[144:145], v[228:229] op_sel:[0,1]\n"
      "v_cvt_pk_bf16_f32 v193, v194, v195\n"
      "v_pk_mul_f32 v[222:223], v[222:223], v[234:235] op_sel_hi:[1,0]\n"
      "v_pk_mul_f32 v[168:169], v[168:169], v[228:229] op_sel:[0,1]\n"
      "v_cvt_pk_bf16_f32 v144, v144, v145\n"
      "v_pk_mul_f32 v[136:137], v[136:137], v[228:229] op_sel:[0,1]\n"
      "v_cvt_pk_bf16_f32 v148, v148, v149\n"
      "v_pk_mul_f32 v[160:161], v[160:161], v[232:233] op_sel_hi:[1,0]\n"
      "v_cvt_pk_bf16_f32 v152, v152, v153\n"
      "v_pk_mul_f32 v[226:227], v[226:227], v[228:229] op_sel_hi:[1,0]\n"
      "v_pk_mul_f32 v[170:171], v[170:171], v[228:229] op_sel:[0,1]\n"
      "v_pk_mul_f32 v[198:199], v[198:199], v[230:231] op_sel:[0,1]\n"
      "v_cvt_pk_bf16_f32 v168, v168, v169\n"
      "v_pk_mul_f32 v[150:151], v[150:151], v[228:229] op_sel_hi:[1,0]\n"
      "v_pk_mul_f32 v[162:163], v[162:163], v[232:233] op_sel_hi:[1,0]\n"
      "v_cvt_pk_bf16_f32 v172, v172, v173\n"
      "v_cvt_pk_bf16_f32 v149, v150, v151\n"
      "v_subrev_u32 v230, 73728, v127\n"
      "v_pk_mul_f32 v[146:147], v[146:147], v[228:229] op_sel:[0,1]\n"
      "v_pk_mul_f32 v[218:219], v[218:219], v[234:235] op_sel_hi:[1,0]\n"
      "v_cvt_pk_bf16_f32 v243, v200, v201\n"
      "v_cvt_pk_bf16_f32 v216, v216, v217\n"
      "v_cvt_pk_bf16_f32 v239, v222, v223\n"
      "v_cvt_pk_bf16_f32 v242, v198, v199\n"
      "v_pk_mul_f32 v[166:167], v[166:167], v[228:229] op_sel_hi:[1,0]\n"
      "v_cvt_pk_bf16_f32 v151, v226, v227\n"
      "v_cvt_pk_bf16_f32 v173, v174, v175\n"
      "global_store_dwordx4 v116, v[244:247], s[54:55] offset:128 sc1\n"
      "ds_write_b64 v127, v[192:193]\n"
      "ds_write_b128 v126, v[236:239] offset:4096\n"
      "v_cvt_pk_bf16_f32 v150, v224, v225\n"
      "v_cvt_pk_bf16_f32 v145, v146, v147\n"
      "v_cvt_pk_bf16_f32 v175, v162, v163\n"
      "v_pk_mul_f32 v[164:165], v[164:165], v[228:229] op_sel_hi:[1,0]\n"
      "v_pk_mul_f32 v[138:139], v[138:139], v[228:229] op_sel:[0,1]\n"
      "v_cvt_pk_bf16_f32 v174, v160, v161\n"
      "v_cvt_pk_bf16_f32 v153, v154, v155\n"
      "v_cvt_pk_bf16_f32 v164, v164, v165\n"
      "v_cvt_pk_bf16_f32 v169, v170, v171\n"
      "v_cvt_pk_bf16_f32 v146, v136, v137\n"
      "v_subrev_u32 v231, 73728, v126\n"
      "v_cvt_pk_bf16_f32 v165, v166, v167\n"
      "v_cvt_pk_bf16_f32 v147, v138, v139\n"
      "v_cvt_pk_bf16_f32 v217, v218, v219\n"
      "ds_write_b64 v230, v[168:169] offset:2048\n"
      "global_store_dwordx4 v116, v[212:215], s[54:55] offset:256 sc1\n"
      "ds_write_b128 v231, v[144:147] offset:2048\n"
      "ds_write_b64 v127, v[152:153] offset:2048\n"
      "ds_write_b128 v231, v[148:151]\n"
      "ds_write_b128 v126, v[172:175]\n"
      "ds_write_b64 v230, v[164:165]\n"
      "ds_write_b64 v127, v[216:217] offset:4096\n"
      "global_store_dwordx4 v117, v[176:179], s[54:55] sc1\n"
      "ds_write_b128 v126, v[240:243] offset:2048\n"
      "s_mov_b32 exec_hi, 0\n"
      "global_store_dwordx4 v118, v[188:191], s[54:55] sc1\n"
      "s_mov_b32 exec_hi, -1\n"
      "s_waitcnt lgkmcnt(0)\n"
      "s_barrier\n"
      "ds_read_b128 v[236:239], v119 offset:6144\n"
      "ds_read_b128 v[144:147], v121\n"
      "ds_read_b128 v[188:191], v119 offset:12288\n"
      "ds_read_b128 v[168:171], v119\n"
      "v_subrev_u32 v230, 73728, v119\n"
      "s_add_u32 s27, s27, 1\n"
      "ds_read_b128 v[152:155], v230 offset:6144\n"
      "s_sub_u32 s28, s27, 3\n"
      "s_cselect_b64 s[54:55], s[12:13], s[22:23]\n"
      "s_max_i32 m0, s28, 0\n"
      "ds_read_b128 v[224:227], v230 offset:12288\n"
      "s_movrels_b32 s9, s80\n"
      "s_lshr_b32 s8, s9, 8\n"
      "s_and_b32 s9, s9, 0xff\n"
      "s_mul_i32 s28, s8, 1373568\n"
      "s_mul_i32 s29, s9, 2688\n"
      "s_add_u32 s28, s28, s29\n"
      "s_cmp_eq_u32 s8, 72\n"
      "s_cselect_b32 s8, 0, 100270464\n"
      "ds_read_b128 v[136:139], v120\n"
      "ds_read_b128 v[148:151], v230\n"
      "s_cmp_eq_u32 s9, 72\n"
      "s_cselect_b32 s9, 0, 196224\n"
      "s_add_u32 s10, s8, s9\n"
      "s_add_u32 s28, s28, s10\n"
      "s_add_u32 s54, s54, s28\n"
      "s_addc_u32 s55, s55, 0\n"
      "s_cmp_lg_u32 s10, s11\n"
      "s_cbranch_scc1 L_edge_40_%=\n"
      "L_eb_41_%=:\n"
      "s_waitcnt lgkmcnt(4)\n"
      "global_store_dwordx4 v116, v[168:171], s[54:55] sc1\n"
      "global_store_dwordx4 v116, v[236:239], s[54:55] offset:128 sc1\n"
      "global_store_dwordx4 v116, v[188:191], s[54:55] offset:256 sc1\n"
      "s_waitcnt lgkmcnt(0)\n"
      "global_store_dwordx4 v117, v[136:139], s[54:55] sc1\n"
      "s_mov_b32 exec_hi, 0\n"
      "global_store_dwordx4 v118, v[144:147], s[54:55] sc1\n"
      "s_mov_b32 exec_hi, -1\n"
      "s_nop 0\n"
      "s_mov_b64 exec, s[48:49]\n"
      "s_cbranch_execz L_np_42_%=\n"
      "global_store_dwordx4 v203, v[148:151], s[22:23] sc1\n"
      "global_store_dwordx4 v203, v[152:155], s[22:23] offset:128 sc1\n"
      "global_store_dwordx4 v203, v[224:227], s[22:23] offset:256 sc1\n"
      "L_np_42_%=:\n"
      "s_mov_b64 exec, -1\n"
      "s_endpgm\n"
      "L_tail1_%=:\n"
      "s_cmp_eq_u32 s10, s11\n"
      "s_cbranch_scc1 L_sk_43_%=\n"
      "s_mov_b32 s11, s10\n"
      "s_sub_u32 s28, s60, s8\n"
      "s_sub_u32 s29, s61, s9\n"
      "s_mov_b32 s60, s8\n"
      "s_mov_b32 s61, s9\n"
      "s_mov_b64 exec, s[64:65]\n"
      "v_add_u32 v116, s28, v116\n"
      "s_mov_b64 exec, s[70:71]\n"
      "v_add_u32 v116, s29, v116\n"
      "s_mov_b64 exec, s[66:67]\n"
      "v_add_u32 v117, s28, v117\n"
      "s_mov_b64 exec, s[72:73]\n"
      "v_add_u32 v117, s29, v117\n"
      "s_mov_b64 exec, s[68:69]\n"
      "v_add_u32 v118, s28, v118\n"
      "s_mov_b64 exec, s[74:75]\n"
      "v_add_u32 v118, s29, v118\n"
      "s_mov_b64 exec, -1\n"
      "s_nop 4\n"
      "L_sk_43_%=:\n"
      "s_cmp_lt_u32 s27, s26\n"
      "s_cbranch_scc1 L_mid_%=\n"
      "s_nop 7\n"
      "v_rcp_f32_e64 v212, v212\n"
      "v_mbcnt_lo_u32_b32 v213, -1, 0\n"
      "v_pk_mul_f32 v[230:231], v[230:231], v[212:213] op_sel_hi:[1,0]\n"
      "v_pk_mul_f32 v[144:145], v[144:145], v[212:213] op_sel_hi:[1,0]\n"
      "v_cvt_pk_bf16_f32 v144, v144, v145\n"
      "v_pk_mul_f32 v[168:169], v[168:169], v[212:213] op_sel_hi:[1,0]\n"
      "v_pk_mul_f32 v[228:229], v[228:229], v[212:213] op_sel_hi:[1,0]\n"
      "v_cvt_pk_bf16_f32 v168, v168, v169\n"
      "v_mbcnt_hi_u32_b32 v213, -1, v213\n"
      "v_pk_mul_f32 v[170:171], v[170:171], v[212:213] op_sel_hi:[1,0]\n"
      "v_lshrrev_b32 v215, 4, v213\n"
      "v_and_b32 v214, 15, v213\n"
      "v_cvt_pk_bf16_f32 v169, v170, v171\n"
      "v_pk_mul_f32 v[146:147], v[146:147], v[212:213] op_sel_hi:[1,0]\n"
      "v_mul_u32_u24 v215, 352, v215\n"
      "v_cvt_pk_bf16_f32 v170, v228, v229\n"
      "v_cvt_pk_bf16_f32 v171, v230, v231\n"
      "v_cvt_pk_bf16_f32 v145, v146, v147\n"
      "ds_write_b64 v125, v[144:145] offset:20480\n"
      "ds_write_b128 v124, v[168:171] offset:20480\n"
      "v_lshl_add_u32 v215, v214, 4, v215\n"
      "v_add_u32 v215, 150640, v215\n"
      "s_waitcnt lgkmcnt(0)\n"
      "s_barrier\n"
      "ds_read_b128 v[176:179], v128 offset:1024\n"
      "ds_read_b128 v[220:223], v123 offset:18432\n"
      "ds_read_b128 v[168:171], v123 offset:21504\n"
      "ds_read_b128 v[216:219], v121 offset:18432\n"
      "s_sub_u32 s28, s27, 3\n"
      "s_cselect_b64 s[54:55], s[12:13], s[22:23]\n"
      "s_max_i32 m0, s28, 0\n"
      "ds_read_b128 v[228:231], v119 offset:30720\n"
      "s_movrels_b32 s9, s80\n"
      "ds_read_b128 v[192:195], v123 offset:24576\n"
      "s_lshr_b32 s8, s9, 8\n"
      "s_and_b32 s9, s9, 0xff\n"
      "s_mul_i32 s28, s8, 1373568\n"
      "s_mul_i32 s29, s9, 2688\n"
      "s_add_u32 s28, s28, s29\n"
      "ds_read_b128 v[248:251], v119 offset:24576\n"
      "s_cmp_eq_u32 s8, 72\n"
      "ds_read_b128 v[164:167], v119 offset:18432\n"
      "s_cselect_b32 s8, 0, 100270464\n"
      "s_cmp_eq_u32 s9, 72\n"
      "s_cselect_b32 s9, 0, 196224\n"
      "s_add_u32 s10, s8, s9\n"
      "ds_read_b128 v[144:147], v120 offset:18432\n"
      "s_waitcnt lgkmcnt(8)\n"
      "v_add_f32 v178, |v178|, |v179|\n"
      "v_add_f32 v176, |v176|, |v177|\n"
      "s_add_u32 s28, s28, s10\n"
      "s_waitcnt lgkmcnt(6)\n"
      "v_mfma_f32_16x16x32_bf16 v[244:247], a[216:219], v[220:223], 0\n"
      "ds_read_b128 v[172:175], v123 offset:27648\n"
      "v_add_f32 v176, v176, v178\n"
      "v_mfma_f32_16x16x32_bf16 v[244:247], a[228:231], v[168:171], v[244:247]\n"
      "ds_read_b128 v[160:163], v123 offset:19456\n"
      "s_add_u32 s54, s54, s28\n"
      "ds_read_b128 v[180:183], v123 offset:30720\n"
      "v_rsq_f32 v176, v176\n"
      "v_mfma_f32_16x16x32_bf16 v[156:159], a[220:223], v[220:223], 0\n"
      "ds_read_b128 v[196:199], v123 offset:33792\n"
      "v_mul_f32 v176, s41, v176\n"
      "v_mfma_f32_16x16x32_bf16 v[156:159], a[232:235], v[168:171], v[156:159]\n"
      "ds_read_b128 v[204:207], v123 offset:25600\n"
      "v_mov_b32_e64 v212, v176\n"
      "v_mfma_f32_16x16x32_bf16 v[200:203], a[224:227], v[220:223], 0\n"
      "ds_read_b128 v[220:223], v123 offset:22528\n"
      "v_permlane32_swap_b32 v176, v212\n"
      "v_mov_b32 v214, v176\n"
      "s_addc_u32 s55, s55, 0\n"
      "v_mfma_f32_16x16x32_bf16 v[200:203], a[236:239], v[168:171], v[200:203]\n"
      "s_cmp_lg_u32 s10, s11\n"
      "v_permlane16_swap_b32 v176, v214\n"
      "v_mul_f32 v137, v176, v137\n"
      "v_mul_f32 v138, v176, v138\n"
      "v_mul_f32 v136, v176, v136\n"
      "s_waitcnt lgkmcnt(5)\n"
      "v_mfma_f32_16x16x32_bf16 v[244:247], a[240:243], v[192:195], v[244:247]\n"
      "s_cbranch_scc1 L_edge_44_%=\n"
      "L_eb_45_%=:\n"
      "v_mfma_f32_16x16x32_bf16 v[244:247], a[252:255], v[172:175], v[244:247]\n"
      "global_store_dwordx4 v116, v[164:167], s[54:55] sc1\n"
      "v_mfma_f32_16x16x32_bf16 v[156:159], a[244:247], v[192:195], v[156:159]\n"
      "ds_read_b128 v[208:211], v123 offset:28672\n"
      "v_cvt_pk_bf16_f32 v136, v136, v137\n"
      "v_mfma_f32_16x16x32_bf16 v[156:159], v[0:3], v[172:175], v[156:159]\n"
      "v_mul_f32 v155, v176, v155\n"
      "v_mul_f32 v139, v176, v139\n"
      "v_mfma_f32_16x16x32_bf16 v[200:203], a[248:251], v[192:195], v[200:203]\n"
      "ds_read_b128 v[192:195], v123 offset:31744\n"
      "v_mul_f32 v153, v176, v153\n"
      "v_mfma_f32_16x16x32_bf16 v[200:203], v[4:7], v[172:175], v[200:203]\n"
      "s_waitcnt lgkmcnt(2)\n"
      "ds_read_b128 v[168:171], v123 offset:34816\n"
      "v_mfma_f32_16x16x32_bf16 v[244:247], v[8:11], v[180:183], v[244:247]\n"
      "v_mul_f32 v152, v176, v152\n"
      "v_mul_f32 v154, v176, v154\n"
      "v_mfma_f32_16x16x32_bf16 v[244:247], v[20:23], v[196:199], v[244:247]\n"
      "v_cvt_pk_bf16_f32 v137, v138, v139\n"
      "v_cvt_pk_bf16_f32 v138, v152, v153\n"
      "v_mfma_f32_16x16x32_bf16 v[156:159], v[12:15], v[180:183], v[156:159]\n"
      "v_cvt_pk_bf16_f32 v139, v154, v155\n"
      "v_mul_f32_e64 v240, v176, v240\n"
      "v_mfma_f32_16x16x32_bf16 v[156:159], v[24:27], v[196:199], v[156:159]\n"
      "ds_write_b128 v126, v[136:139]\n"
      "v_mfma_f32_16x16x32_bf16 v[200:203], v[16:19], v[180:183], v[200:203]\n"
      "ds_read_b128 v[136:139], v123 offset:20480\n"
      "v_mul_f32_e64 v241, v176, v241\n"
      "v_mfma_f32_16x16x32_bf16 v[200:203], v[28:31], v[196:199], v[200:203]\n"
      "global_store_dwordx4 v116, v[248:251], s[54:55] offset:128 sc1\n"
      "v_mfma_f32_16x16x32_bf16 v[180:183], a[216:219], v[160:163], 0\n"
      "ds_read_b128 v[164:167], v123 offset:23552\n"
      "v_cvt_pk_bf16_f32 v240, v240, v241\n"
      "v_mfma_f32_16x16x32_bf16 v[180:183], a[228:231], v[220:223], v[180:183]\n"
      "v_mul_f32 v242, v176, v242\n"
      "v_mul_f32 v243, v176, v243\n"
      "v_mfma_f32_16x16x32_bf16 v[248:251], a[220:223], v[160:163], 0\n"
      "s_waitcnt lgkmcnt(3)\n"
      "v_cvt_pk_bf16_f32 v241, v242, v243\n"
      "v_mul_f32 v236, v214, v236\n"
      "v_mfma_f32_16x16x32_bf16 v[248:251], a[232:235], v[220:223], v[248:251]\n"
      "ds_write_b64 v127, v[240:241]\n"
      "v_mfma_f32_16x16x32_bf16 v[152:155], a[224:227], v[160:163], 0\n"
      "ds_read_b128 v[196:199], v123 offset:26624\n"
      "v_mul_f32_e64 v237, v214, v237\n"
      "v_mfma_f32_16x16x32_bf16 v[152:155], a[236:239], v[220:223], v[152:155]\n"
      "ds_read_b128 v[220:223], v123 offset:29696\n"
      "v_cvt_pk_bf16_f32 v236, v236, v237\n"
      "v_mfma_f32_16x16x32_bf16 v[180:183], a[240:243], v[204:207], v[180:183]\n"
      "v_mul_f32 v239, v214, v239\n"
      "v_mul_f32 v238, v214, v238\n"
      "v_mfma_f32_16x16x32_bf16 v[180:183], a[252:255], v[208:211], v[180:183]\n"
      "v_mul_f32_e64 v140, v214, v140\n"
      "v_cvt_pk_bf16_f32 v237, v238, v239\n"
      "v_mfma_f32_16x16x32_bf16 v[248:251], a[244:247], v[204:207], v[248:251]\n"
      "s_lshl_b32 s29, s24, 8\n"
      "v_mul_f32 v141, v214, v141\n"
      "v_cvt_pk_bf16_f32 v238, v140, v141\n"
      "v_mfma_f32_16x16x32_bf16 v[248:251], v[0:3], v[208:211], v[248:251]\n"
      "s_add_u32 s29, s29, 159088\n"
      "v_mul_f32 v143, v214, v143\n"
      "v_mul_f32_e64 v142, v214, v142\n"
      "v_mfma_f32_16x16x32_bf16 v[152:155], a[248:251], v[204:207], v[152:155]\n"
      "s_waitcnt lgkmcnt(3)\n"
      "v_mul_f32_e64 v148, v214, v148\n"
      "v_cvt_pk_bf16_f32 v239, v142, v143\n"
      "v_mfma_f32_16x16x32_bf16 v[152:155], v[4:7], v[208:211], v[152:155]\n"
      "ds_write_b128 v126, v[236:239] offset:2048\n"
      "v_mfma_f32_16x16x32_bf16 v[180:183], v[8:11], v[192:195], v[180:183]\n"
      "ds_read_b128 v[204:207], v123 offset:32768\n"
      "v_mul_f32_e64 v149, v214, v149\n"
      "v_mfma_f32_16x16x32_bf16 v[180:183], v[20:23], v[168:171], v[180:183]\n"
      "ds_read_b128 v[240:243], v123 offset:35840\n"
      "v_cvt_pk_bf16_f32 v148, v148, v149\n"
      "v_mfma_f32_16x16x32_bf16 v[248:251], v[12:15], v[192:195], v[248:251]\n"
      "v_mul_f32_e64 v151, v214, v151\n"
      "v_mul_f32 v150, v214, v150\n"
      "v_mfma_f32_16x16x32_bf16 v[248:251], v[24:27], v[168:171], v[248:251]\n"
      "s_waitcnt lgkmcnt(3)\n"
      "v_fma_f32 v214, v244, v244, s39\n"
      "v_cvt_pk_bf16_f32 v149, v150, v151\n"
      "v_mfma_f32_16x16x32_bf16 v[152:155], v[16:19], v[192:195], v[152:155]\n"
      "global_store_dwordx4 v116, v[228:231], s[54:55] offset:256 sc1\n"
      "v_mfma_f32_16x16x32_bf16 v[152:155], v[28:31], v[168:171], v[152:155]\n"
      "ds_write_b64 v127, v[148:149] offset:2048\n"
      "v_mfma_f32_16x16x32_bf16 v[236:239], a[216:219], v[136:139], 0\n"
      "ds_read_b128 v[228:231], v215\n"
      "v_fmac_f32_e64 v214, v245, v245\n"
      "v_mfma_f32_16x16x32_bf16 v[236:239], a[228:231], v[164:167], v[236:239]\n"
      "ds_read_b128 v[140:143], v215 offset:1408\n"
      "v_fmac_f32 v214, v246, v246\n"
      "v_mfma_f32_16x16x32_bf16 v[168:171], a[220:223], v[136:139], 0\n"
      "v_fmac_f32 v214, v247, v247\n"
      "v_fmac_f32_e64 v214, v156, v156\n"
      "v_mfma_f32_16x16x32_bf16 v[168:171], a[232:235], v[164:167], v[168:171]\n"
      "v_fmac_f32 v214, v157, v157\n"
      "v_fmac_f32 v214, v158, v158\n"
      "v_mfma_f32_16x16x32_bf16 v[148:151], a[224:227], v[136:139], 0\n"
      "v_fmac_f32 v214, v159, v159\n"
      "v_fmac_f32 v214, v200, v200\n"
      "v_mfma_f32_16x16x32_bf16 v[148:151], a[236:239], v[164:167], v[148:151]\n"
      "s_waitcnt lgkmcnt(3)\n"
      "v_fmac_f32 v214, v201, v201\n"
      "v_fmac_f32 v214, v202, v202\n"
      "v_mfma_f32_16x16x32_bf16 v[236:239], a[240:243], v[196:199], v[236:239]\n"
      "v_mul_f32 v233, v212, v233\n"
      "v_fmac_f32 v214, v203, v203\n"
      "v_mfma_f32_16x16x32_bf16 v[236:239], a[252:255], v[220:223], v[236:239]\n"
      "ds_read_b128 v[164:167], v215 offset:2816\n"
      "v_mul_f32 v232, v212, v232\n"
      "v_mfma_f32_16x16x32_bf16 v[168:171], a[244:247], v[196:199], v[168:171]\n"
      "ds_read_b128 v[136:139], v215 offset:4224\n"
      "v_mul_f32 v234, v212, v234\n"
      "v_mfma_f32_16x16x32_bf16 v[168:171], v[0:3], v[220:223], v[168:171]\n"
      "v_mul_f32 v235, v212, v235\n"
      "v_cvt_pk_bf16_f32 v232, v232, v233\n"
      "v_mfma_f32_16x16x32_bf16 v[148:151], a[248:251], v[196:199], v[148:151]\n"
      "global_store_dwordx4 v117, v[144:147], s[54:55] sc1\n"
      "v_mfma_f32_16x16x32_bf16 v[148:151], v[4:7], v[220:223], v[148:151]\n"
      "s_waitcnt lgkmcnt(2)\n"
      "v_mul_f32 v224, v212, v224\n"
      "v_cvt_pk_bf16_f32 v233, v234, v235\n"
      "v_mfma_f32_16x16x32_bf16 v[236:239], v[8:11], v[204:207], v[236:239]\n"
      "v_mul_f32_e64 v225, v212, v225\n"
      "v_cvt_pk_bf16_f32 v234, v224, v225\n"
      "v_mfma_f32_16x16x32_bf16 v[236:239], v[20:23], v[240:243], v[236:239]\n"
      "ds_read_b128 v[192:195], v215 offset:5632\n"
      "v_mul_f32 v227, v212, v227\n"
      "v_mfma_f32_16x16x32_bf16 v[168:171], v[12:15], v[204:207], v[168:171]\n"
      "v_mul_f32 v226, v212, v226\n"
      "v_cvt_pk_bf16_f32 v235, v226, v227\n"
      "v_mfma_f32_16x16x32_bf16 v[168:171], v[24:27], v[240:243], v[168:171]\n"
      "ds_write_b128 v126, v[232:235] offset:4096\n"
      "v_mfma_f32_16x16x32_bf16 v[148:151], v[16:19], v[204:207], v[148:151]\n"
      "ds_read_b128 v[220:223], v215 offset:7040\n"
      "v_mul_f32 v188, v212, v188\n"
      "v_mfma_f32_16x16x32_bf16 v[148:151], v[28:31], v[240:243], v[148:151]\n"
      "s_waitcnt lgkmcnt(3)\n"
      "v_mul_f32_e64 v189, v212, v189\n"
      "v_cvt_pk_bf16_f32 v188, v188, v189\n"
      "v_mfma_f32_16x16x32_bf16 v[144:147], a[216:219], v[228:231], 0\n"
      "v_mul_f32 v191, v212, v191\n"
      "v_mul_f32 v190, v212, v190\n"
      "v_mfma_f32_16x16x32_bf16 v[144:147], a[228:231], v[140:143], v[144:147]\n"
      "ds_read_b128 v[196:199], v215 offset:256\n"
      "v_cvt_pk_bf16_f32 v189, v190, v191\n"
      "v_mfma_f32_16x16x32_bf16 v[240:243], a[220:223], v[228:231], 0\n"
      "ds_read_b128 v[206:209], v215 offset:1664\n"
      "v_fma_f32 v212, v180, v180, s39\n"
      "v_mfma_f32_16x16x32_bf16 v[240:243], a[232:235], v[140:143], v[240:243]\n"
      "ds_write_b64 v127, v[188:189] offset:4096\n"
      "v_mfma_f32_16x16x32_bf16 v[188:191], a[224:227], v[228:231], 0\n"
      "v_fmac_f32 v212, v181, v181\n"
      "v_fmac_f32 v212, v182, v182\n"
      "v_mfma_f32_16x16x32_bf16 v[188:191], a[236:239], v[140:143], v[188:191]\n"
      "ds_read_b128 v[160:163], v215 offset:3072\n"
      "v_fmac_f32_e64 v212, v183, v183\n"
      "v_mfma_f32_16x16x32_bf16 v[144:147], a[240:243], v[164:167], v[144:147]\n"
      "v_fmac_f32 v212, v248, v248\n"
      "v_fmac_f32 v212, v249, v249\n"
      "v_mfma_f32_16x16x32_bf16 v[144:147], a[252:255], v[136:139], v[144:147]\n"
      "ds_read_b128 v[228:231], v215 offset:4480\n"
      "v_fmac_f32 v212, v250, v250\n"
      "v_mfma_f32_16x16x32_bf16 v[240:243], a[244:247], v[164:167], v[240:243]\n"
      "v_fmac_f32 v212, v251, v251\n"
      "v_fmac_f32 v212, v152, v152\n"
      "v_mfma_f32_16x16x32_bf16 v[240:243], v[0:3], v[136:139], v[240:243]\n"
      "v_fmac_f32 v212, v153, v153\n"
      "v_fmac_f32 v212, v154, v154\n"
      "v_mfma_f32_16x16x32_bf16 v[188:191], a[248:251], v[164:167], v[188:191]\n"
      "s_waitcnt lgkmcnt(3)\n"
      "v_lshl_add_u32 v205, v213, 2, s29\n"
      "v_fmac_f32_e64 v212, v155, v155\n"
      "v_mfma_f32_16x16x32_bf16 v[188:191], v[4:7], v[136:139], v[188:191]\n"
      "ds_read_b32 v205, v205\n"
      "v_lshrrev_b32_e64 v211, 3, v213\n"
      "v_mfma_f32_16x16x32_bf16 v[144:147], v[8:11], v[192:195], v[144:147]\n"
      "ds_read_b128 v[164:167], v215 offset:5888\n"
      "v_fma_f32 v210, v236, v236, s39\n"
      "v_mfma_f32_16x16x32_bf16 v[144:147], v[20:23], v[220:223], v[144:147]\n"
      "ds_read_b128 v[224:227], v215 offset:7296\n"
      "v_fmac_f32 v210, v237, v237\n"
      "v_mfma_f32_16x16x32_bf16 v[240:243], v[12:15], v[192:195], v[240:243]\n"
      "s_lshl_b32 s29, s24, 3\n"
      "v_add_u32_e64 v211, s29, v211\n"
      "v_cmp_gt_u32 s[48:49], s26, v211\n"
      "v_mfma_f32_16x16x32_bf16 v[240:243], v[24:27], v[220:223], v[240:243]\n"
      "v_fmac_f32 v210, v238, v238\n"
      "v_fmac_f32 v210, v239, v239\n"
      "v_mfma_f32_16x16x32_bf16 v[188:191], v[16:19], v[192:195], v[188:191]\n"
      "s_waitcnt lgkmcnt(3)\n"
      "v_fmac_f32 v210, v168, v168\n"
      "v_fmac_f32_e64 v210, v169, v169\n"
      "v_mfma_f32_16x16x32_bf16 v[188:191], v[28:31], v[220:223], v[188:191]\n"
      "s_mov_b32 exec_hi, 0\n"
      "global_store_dwordx4 v118, v[216:219], s[54:55] sc1\n"
      "s_mov_b32 exec_hi, -1\n"
      "v_mfma_f32_16x16x32_bf16 v[140:143], a[216:219], v[196:199], 0\n"
      "v_fmac_f32 v210, v170, v170\n"
      "v_fmac_f32 v210, v171, v171\n"
      "v_mfma_f32_16x16x32_bf16 v[140:143], a[228:231], v[206:209], v[140:143]\n"
      "v_fmac_f32 v210, v148, v148\n"
      "v_fmac_f32 v210, v149, v149\n"
      "v_mfma_f32_16x16x32_bf16 v[192:195], a[220:223], v[196:199], 0\n"
      "v_fmac_f32 v210, v150, v150\n"
      "v_fmac_f32 v210, v151, v151\n"
      "v_mfma_f32_16x16x32_bf16 v[192:195], a[232:235], v[206:209], v[192:195]\n"
      "s_waitcnt lgkmcnt(0)\n"
      "v_permlane16_swap_b32 v214, v212\n"
      "v_mfma_f32_16x16x32_bf16 v[232:235], a[224:227], v[196:199], 0\n"
      "v_add_f32 v214, v214, v212\n"
      "v_mov_b32 v211, v210\n"
      "v_mfma_f32_16x16x32_bf16 v[232:235], a[236:239], v[206:209], v[232:235]\n"
      "s_nop 0\n"
      "v_permlane16_swap_b32 v210, v211\n"
      "v_mfma_f32_16x16x32_bf16 v[140:143], a[240:243], v[160:163], v[140:143]\n"
      "v_add_f32 v210, v210, v211\n"
      "v_fma_f32 v207, v144, v144, s39\n"
      "v_mfma_f32_16x16x32_bf16 v[140:143], a[252:255], v[228:231], v[140:143]\n"
      "v_permlane32_swap_b32 v214, v210\n"
      "v_mfma_f32_16x16x32_bf16 v[140:143], v[8:11], v[164:167], v[140:143]\n"
      "v_fmac_f32 v207, v145, v145\n"
      "v_fmac_f32 v207, v146, v146\n"
      "v_mfma_f32_16x16x32_bf16 v[140:143], v[20:23], v[224:227], v[140:143]\n"
      "v_mfma_f32_16x16x32_bf16 v[192:195], a[244:247], v[160:163], v[192:195]\n"
      "v_mfma_f32_16x16x32_bf16 v[192:195], v[0:3], v[228:231], v[192:195]\n"
      "v_fmac_f32 v207, v147, v147\n"
      "v_add_f32 v214, v214, v210\n"
      "v_mfma_f32_16x16x32_bf16 v[192:195], v[12:15], v[164:167], v[192:195]\n"
      "v_fmac_f32 v207, v240, v240\n"
      "v_fmac_f32 v207, v241, v241\n"
      "v_mfma_f32_16x16x32_bf16 v[192:195], v[24:27], v[224:227], v[192:195]\n"
      "v_mfma_f32_16x16x32_bf16 v[232:235], a[248:251], v[160:163], v[232:235]\n"
      "v_fmac_f32 v207, v242, v242\n"
      "v_mfma_f32_16x16x32_bf16 v[232:235], v[4:7], v[228:231], v[232:235]\n"
      "v_fmac_f32 v207, v243, v243\n"
      "v_fmac_f32 v207, v188, v188\n"
      "v_mfma_f32_16x16x32_bf16 v[232:235], v[16:19], v[164:167], v[232:235]\n"
      "v_mfma_f32_16x16x32_bf16 v[232:235], v[28:31], v[224:227], v[232:235]\n"
      "ds_write_b32 v129, v214\n"
      "v_fmac_f32 v207, v189, v189\n"
      "v_fmac_f32 v207, v190, v190\n"
      "v_fmac_f32 v207, v191, v191\n"
      "v_pk_mul_f32 v[228:229], v[140:141], v[140:141]\n"
      "v_pk_fma_f32 v[228:229], v[142:143], v[142:143], v[228:229]\n"
      "v_pk_fma_f32 v[228:229], v[192:193], v[192:193], v[228:229]\n"
      "v_pk_fma_f32 v[228:229], v[194:195], v[194:195], v[228:229]\n"
      "v_pk_fma_f32 v[228:229], v[232:233], v[232:233], v[228:229]\n"
      "v_pk_fma_f32 v[228:229], v[234:235], v[234:235], v[228:229]\n"
      "v_add_f32 v228, s39, v228\n"
      "v_add_f32 v206, v228, v229\n"
      "v_mov_b32 v204, v206\n"
      "v_mov_b32 v212, v204\n"
      "v_permlane16_swap_b32 v207, v206\n"
      "v_add_f32 v207, v207, v206\n"
      "v_permlane16_swap_b32 v204, v212\n"
      "v_add_f32 v204, v204, v212\n"
      "s_nop 1\n"
      "v_permlane32_swap_b32 v207, v204\n"
      "v_add_f32_e64 v207, v207, v204\n"
      "ds_write_b32 v129, v207 offset:2048\n"
      "s_waitcnt lgkmcnt(0)\n"
      "s_barrier\n"
      "ds_read_b128 v[216:219], v128\n"
      "s_add_u32 s27, s27, 1\n"
      "ds_read_b128 v[162:165], v128 offset:2048\n"
      "s_sub_u32 s28, s27, 3\n"
      "ds_read_b128 v[226:229], v120\n"
      "s_cselect_b64 s[54:55], s[12:13], s[22:23]\n"
      "s_max_i32 m0, s28, 0\n"
      "ds_read_b128 v[196:199], v119 offset:6144\n"
      "s_movrels_b32 s9, s80\n"
      "ds_read_b128 v[208:211], v119\n"
      "ds_read_b128 v[212:215], v121\n"
      "s_lshr_b32 s8, s9, 8\n"
      "s_and_b32 s9, s9, 0xff\n"
      "s_mul_i32 s28, s8, 1373568\n"
      "s_mul_i32 s29, s9, 2688\n"
      "ds_read_b128 v[222:225], v119 offset:12288\n"
      "s_add_u32 s28, s28, s29\n"
      "s_cmp_eq_u32 s8, 72\n"
      "s_cselect_b32 s8, 0, 100270464\n"
      "s_cmp_eq_u32 s9, 72\n"
      "s_cselect_b32 s9, 0, 196224\n"
      "s_add_u32 s10, s8, s9\n"
      "s_add_u32 s28, s28, s10\n"
      "s_add_u32 s54, s54, s28\n"
      "s_addc_u32 s55, s55, 0\n"
      "s_cmp_lg_u32 s10, s11\n"
      "s_cbranch_scc1 L_edge_46_%=\n"
      "L_eb_47_%=:\n"
      "s_waitcnt lgkmcnt(5)\n"
      "v_add_f32 v218, |v218|, |v219|\n"
      "v_add_f32 v216, |v216|, |v217|\n"
      "v_add_f32 v216, v216, v218\n"
      "v_rsq_f32 v216, v216\n"
      "v_add_f32 v162, |v162|, |v163|\n"
      "v_mul_f32 v216, s41, v216\n"
      "v_mov_b32 v230, v216\n"
      "v_add_f32 v164, |v164|, |v165|\n"
      "s_nop 0\n"
      "v_permlane32_swap_b32 v216, v230\n"
      "v_pk_mul_f32 v[170:171], v[170:171], v[230:231] op_sel_hi:[1,0]\n"
      "v_pk_mul_f32 v[236:237], v[236:237], v[230:231] op_sel_hi:[1,0]\n"
      "v_pk_mul_f32 v[168:169], v[168:169], v[230:231] op_sel_hi:[1,0]\n"
      "v_pk_mul_f32 v[148:149], v[148:149], v[230:231] op_sel_hi:[1,0]\n"
      "v_pk_mul_f32 v[238:239], v[238:239], v[230:231] op_sel_hi:[1,0]\n"
      "v_pk_mul_f32 v[150:151], v[150:151], v[230:231] op_sel_hi:[1,0]\n"
      "v_mov_b32 v230, v216\n"
      "v_add_f32 v162, v162, v164\n"
      "s_waitcnt lgkmcnt(0)\n"
      "v_rsq_f32 v162, v162\n"
      "v_permlane16_swap_b32 v216, v230\n"
      "v_mul_f32 v162, s41, v162\n"
      "v_mov_b32 v221, v162\n"
      "v_pk_mul_f32 v[244:245], v[244:245], v[216:217] op_sel_hi:[1,0]\n"
      "v_pk_mul_f32 v[200:201], v[200:201], v[216:217] op_sel_hi:[1,0]\n"
      "v_permlane32_swap_b32 v162, v221\n"
      "v_mov_b32_e64 v220, v162\n"
      "v_pk_mul_f32 v[182:183], v[182:183], v[230:231] op_sel_hi:[1,0]\n"
      "v_pk_mul_f32 v[246:247], v[246:247], v[216:217] op_sel_hi:[1,0]\n"
      "v_cvt_pk_bf16_f32 v244, v244, v245\n"
      "v_permlane16_swap_b32_e64 v162, v220\n"
      "v_pk_mul_f32 v[202:203], v[202:203], v[216:217] op_sel_hi:[1,0]\n"
      "v_pk_mul_f32 v[192:193], v[192:193], v[220:221] op_sel_hi:[1,0]\n"
      "v_cvt_pk_bf16_f32 v200, v200, v201\n"
      "v_pk_mul_f32 v[152:153], v[152:153], v[230:231] op_sel_hi:[1,0]\n"
      "v_pk_mul_f32 v[248:249], v[248:249], v[230:231] op_sel_hi:[1,0]\n"
      "v_pk_mul_f32 v[146:147], v[146:147], v[162:163] op_sel_hi:[1,0]\n"
      "v_cvt_pk_bf16_f32 v201, v202, v203\n"
      "v_pk_mul_f32 v[242:243], v[242:243], v[162:163] op_sel_hi:[1,0]\n"
      "v_pk_mul_f32 v[144:145], v[144:145], v[162:163] op_sel_hi:[1,0]\n"
      "v_pk_mul_f32 v[142:143], v[142:143], v[220:221] op_sel_hi:[1,0]\n"
      "v_pk_mul_f32 v[188:189], v[188:189], v[162:163] op_sel_hi:[1,0]\n"
      "v_pk_mul_f32 v[180:181], v[180:181], v[230:231] op_sel_hi:[1,0]\n"
      "v_cvt_pk_bf16_f32 v144, v144, v145\n"
      "v_pk_mul_f32 v[158:159], v[158:159], v[216:217] op_sel_hi:[1,0]\n"
      "v_pk_mul_f32 v[250:251], v[250:251], v[230:231] op_sel_hi:[1,0]\n"
      "v_cvt_pk_bf16_f32 v148, v148, v149\n"
      "v_pk_mul_f32 v[194:195], v[194:195], v[220:221] op_sel_hi:[1,0]\n"
      "ds_write_b64 v127, v[200:201] offset:18432\n"
      "global_store_dwordx4 v116, v[208:211], s[54:55] sc1\n"
      "v_pk_mul_f32 v[156:157], v[156:157], v[216:217] op_sel_hi:[1,0]\n"
      "v_pk_mul_f32 v[140:141], v[140:141], v[220:221] op_sel_hi:[1,0]\n"
      "v_pk_mul_f32 v[232:233], v[232:233], v[220:221] op_sel_hi:[1,0]\n"
      "v_pk_mul_f32 v[154:155], v[154:155], v[230:231] op_sel_hi:[1,0]\n"
      "v_cvt_pk_bf16_f32 v236, v236, v237\n"
      "v_cvt_pk_bf16_f32 v145, v146, v147\n"
      "v_cvt_pk_bf16_f32 v245, v246, v247\n"
      "v_cvt_pk_bf16_f32 v232, v232, v233\n"
      "v_cvt_pk_bf16_f32 v247, v158, v159\n"
      "v_cvt_pk_bf16_f32 v152, v152, v153\n"
      "v_cvt_pk_bf16_f32 v149, v150, v151\n"
      "v_cvt_pk_bf16_f32 v188, v188, v189\n"
      "v_cvt_pk_bf16_f32 v140, v140, v141\n"
      "v_cvt_pk_bf16_f32 v237, v238, v239\n"
      "v_cvt_pk_bf16_f32 v153, v154, v155\n"
      "v_pk_mul_f32 v[190:191], v[190:191], v[162:163] op_sel_hi:[1,0]\n"
      "v_pk_mul_f32 v[234:235], v[234:235], v[220:221] op_sel_hi:[1,0]\n"
      "v_pk_mul_f32 v[240:241], v[240:241], v[162:163] op_sel_hi:[1,0]\n"
      "v_cvt_pk_bf16_f32 v141, v142, v143\n"
      "v_subrev_u32 v230, 73728, v127\n"
      "v_cvt_pk_bf16_f32 v146, v240, v241\n"
      "v_cvt_pk_bf16_f32 v238, v168, v169\n"
      "v_cvt_pk_bf16_f32 v246, v156, v157\n"
      "v_cvt_pk_bf16_f32 v239, v170, v171\n"
      "v_cvt_pk_bf16_f32 v147, v242, v243\n"
      "global_store_dwordx4 v116, v[196:199], s[54:55] offset:128 sc1\n"
      "ds_write_b128 v126, v[244:247] offset:18432\n"
      "ds_write_b128 v126, v[236:239] offset:22528\n"
      "v_cvt_pk_bf16_f32 v180, v180, v181\n"
      "v_cvt_pk_bf16_f32 v142, v192, v193\n"
      "v_cvt_pk_bf16_f32 v181, v182, v183\n"
      "v_cvt_pk_bf16_f32 v189, v190, v191\n"
      "v_cvt_pk_bf16_f32 v183, v250, v251\n"
      "v_cvt_pk_bf16_f32 v182, v248, v249\n"
      "v_subrev_u32 v231, 73728, v126\n"
      "v_cvt_pk_bf16_f32 v233, v234, v235\n"
      "v_cvt_pk_bf16_f32 v143, v194, v195\n"
      "ds_write_b64 v230, v[188:189] offset:18432\n"
      "ds_write_b64 v127, v[152:153] offset:20480\n"
      "ds_write_b128 v126, v[180:183] offset:20480\n"
      "global_store_dwordx4 v116, v[222:225], s[54:55] offset:256 sc1\n"
      "global_store_dwordx4 v117, v[226:229], s[54:55] sc1\n"
      "ds_write_b128 v231, v[140:143] offset:20480\n"
      "ds_write_b128 v231, v[144:147] offset:18432\n"
      "ds_write_b64 v230, v[232:233] offset:20480\n"
      "ds_write_b64 v127, v[148:149] offset:22528\n"
      "s_mov_b32 exec_hi, 0\n"
      "global_store_dwordx4 v118, v[212:215], s[54:55] sc1\n"
      "s_mov_b32 exec_hi, -1\n"
      "s_waitcnt lgkmcnt(0)\n"
      "s_barrier\n"
      "ds_read_b128 v[192:195], v119 offset:24576\n"
      "ds_read_b128 v[140:143], v121 offset:18432\n"
      "ds_read_b128 v[156:159], v119 offset:30720\n"
      "ds_read_b128 v[212:215], v119 offset:18432\n"
      "v_subrev_u32 v206, 73728, v119\n"
      "s_add_u32 s27, s27, 1\n"
      "ds_read_b128 v[232:235], v206 offset:24576\n"
      "s_sub_u32 s28, s27, 3\n"
      "s_cselect_b64 s[54:55], s[12:13], s[22:23]\n"
      "s_max_i32 m0, s28, 0\n"
      "ds_read_b128 v[148:151], v206 offset:30720\n"
      "s_movrels_b32 s9, s80\n"
      "s_lshr_b32 s8, s9, 8\n"
      "s_and_b32 s9, s9, 0xff\n"
      "s_mul_i32 s28, s8, 1373568\n"
      "s_mul_i32 s29, s9, 2688\n"
      "s_add_u32 s28, s28, s29\n"
      "s_cmp_eq_u32 s8, 72\n"
      "s_cselect_b32 s8, 0, 100270464\n"
      "ds_read_b128 v[168:171], v120 offset:18432\n"
      "ds_read_b128 v[240:243], v206 offset:18432\n"
      "s_cmp_eq_u32 s9, 72\n"
      "s_cselect_b32 s9, 0, 196224\n"
      "s_add_u32 s10, s8, s9\n"
      "s_add_u32 s28, s28, s10\n"
      "s_add_u32 s54, s54, s28\n"
      "s_addc_u32 s55, s55, 0\n"
      "s_cmp_lg_u32 s10, s11\n"
      "s_cbranch_scc1 L_edge_48_%=\n"
      "L_eb_49_%=:\n"
      "s_waitcnt lgkmcnt(4)\n"
      "global_store_dwordx4 v116, v[212:215], s[54:55] sc1\n"
      "global_store_dwordx4 v116, v[192:195], s[54:55] offset:128 sc1\n"
      "global_store_dwordx4 v116, v[156:159], s[54:55] offset:256 sc1\n"
      "s_waitcnt lgkmcnt(0)\n"
      "global_store_dwordx4 v117, v[168:171], s[54:55] sc1\n"
      "s_mov_b32 exec_hi, 0\n"
      "global_store_dwordx4 v118, v[140:143], s[54:55] sc1\n"
      "s_mov_b32 exec_hi, -1\n"
      "s_nop 0\n"
      "s_mov_b64 exec, s[48:49]\n"
      "s_cbranch_execz L_np_50_%=\n"
      "global_store_dwordx4 v205, v[240:243], s[22:23] sc1\n"
      "global_store_dwordx4 v205, v[232:235], s[22:23] offset:128 sc1\n"
      "global_store_dwordx4 v205, v[148:151], s[22:23] offset:256 sc1\n"
      "L_np_50_%=:\n"
      "s_mov_b64 exec, -1\n"
      "s_endpgm\n"
      "L_gen_8_%=:\n"
      "s_waitcnt lgkmcnt(0)\n"
      "s_nop 5\n"
      "s_cmp_eq_u64 s[34:35], 0\n"
      "s_cbranch_scc1 L_ok_65_%=\n"
      "ds_read_b128 v[184:187], v115\n"
      "ds_read_b128 v[168:171], v114\n"
      "ds_read_b128 v[202:205], v114 offset:12288\n"
      "ds_read_b128 v[176:179], v114 offset:6144\n"
      "ds_read_b128 v[140:143], v115 offset:12288\n"
      "ds_read_b128 v[196:199], v115 offset:6144\n"
      "s_waitcnt lgkmcnt(4)\n"
      "v_mfma_f32_16x16x32_bf16 v[156:159], a[0:3], v[168:171], 0\n"
      "v_mfma_f32_16x16x32_bf16 v[156:159], a[36:39], v[184:187], v[156:159]\n"
      "v_mfma_f32_16x16x32_bf16 v[164:167], a[4:7], v[168:171], 0\n"
      "s_waitcnt lgkmcnt(0)\n"
      "v_mfma_f32_16x16x32_bf16 v[164:167], a[40:43], v[184:187], v[164:167]\n"
      "v_mfma_f32_16x16x32_bf16 v[206:209], a[8:11], v[168:171], 0\n"
      "v_mfma_f32_16x16x32_bf16 v[206:209], a[44:47], v[184:187], v[206:209]\n"
      "v_mfma_f32_16x16x32_bf16 v[152:155], a[12:15], v[168:171], 0\n"
      "v_mfma_f32_16x16x32_bf16 v[152:155], a[48:51], v[184:187], v[152:155]\n"
      "v_mfma_f32_16x16x32_bf16 v[232:235], a[16:19], v[168:171], 0\n"
      "v_mfma_f32_16x16x32_bf16 v[232:235], a[52:55], v[184:187], v[232:235]\n"
      "v_mfma_f32_16x16x32_bf16 v[172:175], a[20:23], v[168:171], 0\n"
      "ds_read_b128 v[168:171], v115 offset:2048\n"
      "v_mfma_f32_16x16x32_bf16 v[172:175], a[56:59], v[184:187], v[172:175]\n"
      "v_mfma_f32_16x16x32_bf16 v[156:159], a[72:75], v[176:179], v[156:159]\n"
      "v_mfma_f32_16x16x32_bf16 v[156:159], a[108:111], v[196:199], v[156:159]\n"
      "v_mfma_f32_16x16x32_bf16 v[206:209], a[80:83], v[176:179], v[206:209]\n"
      "v_mfma_f32_16x16x32_bf16 v[206:209], a[116:119], v[196:199], v[206:209]\n"
      "v_mfma_f32_16x16x32_bf16 v[164:167], a[76:79], v[176:179], v[164:167]\n"
      "v_mfma_f32_16x16x32_bf16 v[164:167], a[112:115], v[196:199], v[164:167]\n"
      "v_mfma_f32_16x16x32_bf16 v[232:235], a[88:91], v[176:179], v[232:235]\n"
      "v_mfma_f32_16x16x32_bf16 v[232:235], a[124:127], v[196:199], v[232:235]\n"
      "v_mfma_f32_16x16x32_bf16 v[152:155], a[84:87], v[176:179], v[152:155]\n"
      "v_mfma_f32_16x16x32_bf16 v[152:155], a[120:123], v[196:199], v[152:155]\n"
      "v_mfma_f32_16x16x32_bf16 v[156:159], a[144:147], v[202:205], v[156:159]\n"
      "v_mfma_f32_16x16x32_bf16 v[156:159], a[180:183], v[140:143], v[156:159]\n"
      "v_mfma_f32_16x16x32_bf16 v[172:175], a[92:95], v[176:179], v[172:175]\n"
      "v_mfma_f32_16x16x32_bf16 v[172:175], a[128:131], v[196:199], v[172:175]\n"
      "v_mfma_f32_16x16x32_bf16 v[206:209], a[152:155], v[202:205], v[206:209]\n"
      "v_mfma_f32_16x16x32_bf16 v[206:209], a[188:191], v[140:143], v[206:209]\n"
      "v_mfma_f32_16x16x32_bf16 v[164:167], a[148:151], v[202:205], v[164:167]\n"
      "s_nop 0\n"
      "v_mfma_f32_16x16x32_bf16 v[164:167], a[184:187], v[140:143], v[164:167]\n"
      "s_nop 1\n"
      "v_fma_f32 v210, v156, v156, s38\n"
      "v_mfma_f32_16x16x32_bf16 v[152:155], a[156:159], v[202:205], v[152:155]\n"
      "v_fmac_f32 v210, v157, v157\n"
      "v_fmac_f32 v210, v158, v158\n"
      "v_mfma_f32_16x16x32_bf16 v[152:155], a[192:195], v[140:143], v[152:155]\n"
      "v_fmac_f32 v210, v159, v159\n"
      "v_fmac_f32 v210, v164, v164\n"
      "v_mfma_f32_16x16x32_bf16 v[232:235], a[160:163], v[202:205], v[232:235]\n"
      "v_fmac_f32 v210, v165, v165\n"
      "v_fmac_f32 v210, v166, v166\n"
      "v_mfma_f32_16x16x32_bf16 v[232:235], a[196:199], v[140:143], v[232:235]\n"
      "v_fmac_f32 v210, v167, v167\n"
      "v_fmac_f32 v210, v206, v206\n"
      "v_mfma_f32_16x16x32_bf16 v[172:175], a[164:167], v[202:205], v[172:175]\n"
      "ds_read_b128 v[184:187], v114 offset:2048\n"
      "v_fma_f32 v211, v152, v152, s38\n"
      "v_mfma_f32_16x16x32_bf16 v[172:175], a[200:203], v[140:143], v[172:175]\n"
      "v_fmac_f32 v211, v153, v153\n"
      "v_fmac_f32 v211, v154, v154\n"
      "v_fmac_f32 v211, v155, v155\n"
      "v_fmac_f32 v211, v232, v232\n"
      "v_fmac_f32 v210, v207, v207\n"
      "v_fmac_f32 v211, v233, v233\n"
      "v_fmac_f32 v211, v234, v234\n"
      "v_fmac_f32 v211, v235, v235\n"
      "v_fmac_f32 v211, v172, v172\n"
      "v_fmac_f32 v211, v173, v173\n"
      "v_fmac_f32 v211, v174, v174\n"
      "v_fmac_f32 v210, v208, v208\n"
      "v_fmac_f32 v210, v209, v209\n"
      "v_fmac_f32 v211, v175, v175\n"
      "s_nop 1\n"
      "v_permlane16_swap_b32 v210, v211\n"
      "v_add_f32 v210, v210, v211\n"
      "v_mov_b32 v211, v210\n"
      "s_nop 1\n"
      "v_permlane32_swap_b32 v210, v211\n"
      "v_add_f32 v210, v210, v211\n"
      "v_rsq_f32 v210, v210\n"
      "s_waitcnt lgkmcnt(0)\n"
      "v_mov_b32 v211, v210\n"
      "v_mfma_f32_16x16x32_bf16 v[136:139], a[0:3], v[184:187], 0\n"
      "ds_read_b128 v[176:179], v114 offset:8192\n"
      "v_permlane16_swap_b32 v210, v211\n"
      "v_mul_f32 v175, v211, v175\n"
      "v_mfma_f32_16x16x32_bf16 v[136:139], a[36:39], v[168:171], v[136:139]\n"
      "ds_read_b128 v[196:199], v115 offset:8192\n"
      "v_mul_f32 v153, v211, v153\n"
      "v_mul_f32 v152, v211, v152\n"
      "v_mfma_f32_16x16x32_bf16 v[212:215], a[12:15], v[184:187], 0\n"
      "ds_read_b128 v[202:205], v114 offset:14336\n"
      "v_cvt_pk_f16_f32 v152, v152, v153\n"
      "v_mfma_f32_16x16x32_bf16 v[212:215], a[48:51], v[168:171], v[212:215]\n"
      "ds_read_b128 v[140:143], v115 offset:14336\n"
      "v_mul_f32 v154, v211, v154\n"
      "v_mfma_f32_16x16x32_bf16 v[220:223], a[20:23], v[184:187], 0\n"
      "v_mul_f32 v232, v211, v232\n"
      "v_mul_f32 v155, v211, v155\n"
      "v_mfma_f32_16x16x32_bf16 v[220:223], a[56:59], v[168:171], v[220:223]\n"
      "v_mul_f32 v234, v211, v234\n"
      "v_mul_f32 v235, v211, v235\n"
      "v_cvt_pk_f16_f32 v153, v154, v155\n"
      "v_mul_f32 v233, v211, v233\n"
      "v_cvt_pk_f16_f32 v154, v232, v233\n"
      "v_cvt_pk_f16_f32 v155, v234, v235\n"
      "v_mfma_f32_16x16x32_bf16 v[232:235], a[16:19], v[184:187], 0\n"
      "v_mul_f32 v172, v211, v172\n"
      "v_mul_f32 v173, v211, v173\n"
      "v_mfma_f32_16x16x32_bf16 v[232:235], a[52:55], v[168:171], v[232:235]\n"
      "v_mul_f32 v174, v211, v174\n"
      "v_cvt_pk_f16_f32 v34, v172, v173\n"
      "s_waitcnt lgkmcnt(2)\n"
      "v_mfma_f32_16x16x32_bf16 v[136:139], a[72:75], v[176:179], v[136:139]\n"
      "v_mul_f32 v210, s36, v210\n"
      "v_mul_f32 v156, v210, v156\n"
      "v_mfma_f32_16x16x32_bf16 v[136:139], a[108:111], v[196:199], v[136:139]\n"
      "v_mul_f32 v157, v210, v157\n"
      "v_mul_f32 v159, v210, v159\n"
      "v_mfma_f32_16x16x32_bf16 v[212:215], a[84:87], v[176:179], v[212:215]\n"
      "s_waitcnt lgkmcnt(0)\n"
      "v_cvt_pk_f16_f32 v156, v156, v157\n"
      "v_mul_f32 v158, v210, v158\n"
      "v_mfma_f32_16x16x32_bf16 v[212:215], a[120:123], v[196:199], v[212:215]\n"
      "v_mul_f32 v206, v210, v206\n"
      "v_mul_f32 v164, v210, v164\n"
      "v_mfma_f32_16x16x32_bf16 v[232:235], a[88:91], v[176:179], v[232:235]\n"
      "v_cvt_pk_f16_f32 v157, v158, v159\n"
      "v_mul_f32 v165, v210, v165\n"
      "v_mfma_f32_16x16x32_bf16 v[232:235], a[124:127], v[196:199], v[232:235]\n"
      "v_mul_f32 v167, v210, v167\n"
      "v_cvt_pk_f16_f32 v35, v174, v175\n"
      "v_mfma_f32_16x16x32_bf16 v[220:223], a[92:95], v[176:179], v[220:223]\n"
      "v_mul_f32_e64 v166, v210, v166\n"
      "v_cvt_pk_f16_f32 v159, v166, v167\n"
      "v_mfma_f32_16x16x32_bf16 v[220:223], a[128:131], v[196:199], v[220:223]\n"
      "v_cvt_pk_f16_f32 v158, v164, v165\n"
      "v_mfma_f32_16x16x32_bf16 v[164:167], a[4:7], v[184:187], 0\n"
      "v_mul_f32 v207, v210, v207\n"
      "v_mfma_f32_16x16x32_bf16 v[164:167], a[40:43], v[168:171], v[164:167]\n"
      "v_mul_f32 v208, v210, v208\n"
      "v_mul_f32_e64 v209, v210, v209\n"
      "v_mfma_f32_16x16x32_bf16 v[164:167], a[76:79], v[176:179], v[164:167]\n"
      "v_mfma_f32_16x16x32_bf16 v[164:167], a[112:115], v[196:199], v[164:167]\n"
      "v_cvt_pk_f16_f32 v33, v208, v209\n"
      "v_cvt_pk_f16_f32 v32, v206, v207\n"
      "v_mfma_f32_16x16x32_bf16 v[206:209], a[8:11], v[184:187], 0\n"
      "v_mfma_f32_16x16x32_bf16 v[206:209], a[44:47], v[168:171], v[206:209]\n"
      "v_mfma_f32_16x16x32_bf16 v[206:209], a[80:83], v[176:179], v[206:209]\n"
      "ds_read_b128 v[168:171], v115 offset:4096\n"
      "v_mfma_f32_16x16x32_bf16 v[206:209], a[116:119], v[196:199], v[206:209]\n"
      "v_mfma_f32_16x16x32_bf16 v[136:139], a[144:147], v[202:205], v[136:139]\n"
      "v_mfma_f32_16x16x32_bf16 v[136:139], a[180:183], v[140:143], v[136:139]\n"
      "v_mfma_f32_16x16x32_bf16 v[164:167], a[148:151], v[202:205], v[164:167]\n"
      "v_mfma_f32_16x16x32_bf16 v[164:167], a[184:187], v[140:143], v[164:167]\n"
      "v_mfma_f32_16x16x32_bf16 v[206:209], a[152:155], v[202:205], v[206:209]\n"
      "v_mfma_f32_16x16x32_bf16 v[206:209], a[188:191], v[140:143], v[206:209]\n"
      "v_mfma_f32_16x16x32_bf16 v[212:215], a[156:159], v[202:205], v[212:215]\n"
      "v_mfma_f32_16x16x32_bf16 v[212:215], a[192:195], v[140:143], v[212:215]\n"
      "s_nop 1\n"
      "v_fma_f32 v210, v136, v136, s38\n"
      "v_mfma_f32_16x16x32_bf16 v[232:235], a[160:163], v[202:205], v[232:235]\n"
      "v_fmac_f32 v210, v137, v137\n"
      "v_fmac_f32 v210, v138, v138\n"
      "v_mfma_f32_16x16x32_bf16 v[232:235], a[196:199], v[140:143], v[232:235]\n"
      "v_fmac_f32 v210, v139, v139\n"
      "v_fma_f32 v211, v212, v212, s38\n"
      "v_fmac_f32_e64 v211, v213, v213\n"
      "v_mfma_f32_16x16x32_bf16 v[220:223], a[164:167], v[202:205], v[220:223]\n"
      "ds_read_b128 v[184:187], v114 offset:4096\n"
      "v_fmac_f32 v211, v214, v214\n"
      "v_mfma_f32_16x16x32_bf16 v[220:223], a[200:203], v[140:143], v[220:223]\n"
      "v_fmac_f32 v211, v215, v215\n"
      "v_fmac_f32 v210, v164, v164\n"
      "v_fmac_f32 v210, v165, v165\n"
      "v_fmac_f32 v211, v232, v232\n"
      "v_fmac_f32 v210, v166, v166\n"
      "v_fmac_f32 v211, v233, v233\n"
      "v_fmac_f32 v210, v167, v167\n"
      "v_fmac_f32 v210, v206, v206\n"
      "v_fmac_f32 v210, v207, v207\n"
      "v_fmac_f32 v211, v234, v234\n"
      "v_fmac_f32 v211, v235, v235\n"
      "v_fmac_f32 v210, v208, v208\n"
      "v_fmac_f32 v211, v220, v220\n"
      "v_fmac_f32 v211, v221, v221\n"
      "v_fmac_f32 v211, v222, v222\n"
      "v_fmac_f32 v210, v209, v209\n"
      "v_fmac_f32 v211, v223, v223\n"
      "s_nop 1\n"
      "v_permlane16_swap_b32 v210, v211\n"
      "v_add_f32 v210, v210, v211\n"
      "v_mov_b32 v211, v210\n"
      "s_nop 1\n"
      "v_permlane32_swap_b32 v210, v211\n"
      "v_add_f32 v210, v210, v211\n"
      "v_rsq_f32 v210, v210\n"
      "s_waitcnt lgkmcnt(0)\n"
      "v_mfma_f32_16x16x32_bf16 v[180:183], a[0:3], v[184:187], 0\n"
      "ds_read_b128 v[176:179], v114 offset:10240\n"
      "v_mov_b32 v211, v210\n"
      "v_mfma_f32_16x16x32_bf16 v[180:183], a[36:39], v[168:171], v[180:183]\n"
      "ds_read_b128 v[196:199], v115 offset:10240\n"
      "v_permlane16_swap_b32 v210, v211\n"
      "v_mfma_f32_16x16x32_bf16 v[144:147], a[12:15], v[184:187], 0\n"
      "ds_read_b128 v[202:205], v114 offset:16384\n"
      "v_mul_f32 v212, v211, v212\n"
      "v_mfma_f32_16x16x32_bf16 v[144:147], a[48:51], v[168:171], v[144:147]\n"
      "ds_read_b128 v[140:143], v115 offset:16384\n"
      "v_mul_f32 v234, v211, v234\n"
      "v_mfma_f32_16x16x32_bf16 v[172:175], a[20:23], v[184:187], 0\n"
      "v_mul_f32 v214, v211, v214\n"
      "v_mul_f32 v215, v211, v215\n"
      "v_mfma_f32_16x16x32_bf16 v[172:175], a[56:59], v[168:171], v[172:175]\n"
      "v_mul_f32 v213, v211, v213\n"
      "v_cvt_pk_f16_f32 v212, v212, v213\n"
      "v_cvt_pk_f16_f32 v213, v214, v215\n"
      "v_mul_f32 v232, v211, v232\n"
      "v_mul_f32 v235, v211, v235\n"
      "v_mul_f32_e64 v233, v211, v233\n"
      "v_cvt_pk_f16_f32 v214, v232, v233\n"
      "v_cvt_pk_f16_f32 v215, v234, v235\n"
      "v_mfma_f32_16x16x32_bf16 v[232:235], a[16:19], v[184:187], 0\n"
      "v_mul_f32 v220, v211, v220\n"
      "v_mul_f32 v221, v211, v221\n"
      "v_mfma_f32_16x16x32_bf16 v[232:235], a[52:55], v[168:171], v[232:235]\n"
      "v_mul_f32 v223, v211, v223\n"
      "s_waitcnt lgkmcnt(2)\n"
      "v_mfma_f32_16x16x32_bf16 v[180:183], a[72:75], v[176:179], v[180:183]\n"
      "v_cvt_pk_f16_f32 v40, v220, v221\n"
      "v_mul_f32 v222, v211, v222\n"
      "v_mfma_f32_16x16x32_bf16 v[180:183], a[108:111], v[196:199], v[180:183]\n"
      "v_mul_f32 v210, s36, v210\n"
      "v_cvt_pk_f16_f32 v41, v222, v223\n"
      "v_mfma_f32_16x16x32_bf16 v[232:235], a[88:91], v[176:179], v[232:235]\n"
      "v_mul_f32 v136, v210, v136\n"
      "v_mul_f32 v137, v210, v137\n"
      "v_mfma_f32_16x16x32_bf16 v[232:235], a[124:127], v[196:199], v[232:235]\n"
      "v_mul_f32 v138, v210, v138\n"
      "v_cvt_pk_f16_f32 v136, v136, v137\n"
      "v_mfma_f32_16x16x32_bf16 v[144:147], a[84:87], v[176:179], v[144:147]\n"
      "v_mul_f32 v165, v210, v165\n"
      "v_mul_f32_e64 v139, v210, v139\n"
      "v_mfma_f32_16x16x32_bf16 v[144:147], a[120:123], v[196:199], v[144:147]\n"
      "v_cvt_pk_f16_f32 v137, v138, v139\n"
      "v_mul_f32 v164, v210, v164\n"
      "v_mfma_f32_16x16x32_bf16 v[172:175], a[92:95], v[176:179], v[172:175]\n"
      "s_waitcnt lgkmcnt(0)\n"
      "v_mul_f32 v166, v210, v166\n"
      "v_cvt_pk_f16_f32 v138, v164, v165\n"
      "v_mfma_f32_16x16x32_bf16 v[172:175], a[128:131], v[196:199], v[172:175]\n"
      "v_mul_f32 v167, v210, v167\n"
      "v_cvt_pk_f16_f32 v139, v166, v167\n"
      "v_mfma_f32_16x16x32_bf16 v[164:167], a[4:7], v[184:187], 0\n"
      "v_mul_f32 v206, v210, v206\n"
      "v_mul_f32 v207, v210, v207\n"
      "v_mfma_f32_16x16x32_bf16 v[164:167], a[40:43], v[168:171], v[164:167]\n"
      "v_mfma_f32_16x16x32_bf16 v[164:167], a[76:79], v[176:179], v[164:167]\n"
      "v_cvt_pk_f16_f32 v38, v206, v207\n"
      "v_mul_f32 v208, v210, v208\n"
      "v_mfma_f32_16x16x32_bf16 v[164:167], a[112:115], v[196:199], v[164:167]\n"
      "v_mul_f32 v209, v210, v209\n"
      "v_cvt_pk_f16_f32 v39, v208, v209\n"
      "v_mfma_f32_16x16x32_bf16 v[206:209], a[8:11], v[184:187], 0\n"
      "v_mfma_f32_16x16x32_bf16 v[206:209], a[44:47], v[168:171], v[206:209]\n"
      "v_mfma_f32_16x16x32_bf16 v[206:209], a[80:83], v[176:179], v[206:209]\n"
      "v_mfma_f32_16x16x32_bf16 v[206:209], a[116:119], v[196:199], v[206:209]\n"
      "v_mfma_f32_16x16x32_bf16 v[180:183], a[144:147], v[202:205], v[180:183]\n"
      "v_mfma_f32_16x16x32_bf16 v[180:183], a[180:183], v[140:143], v[180:183]\n"
      "v_mfma_f32_16x16x32_bf16 v[164:167], a[148:151], v[202:205], v[164:167]\n"
      "v_mfma_f32_16x16x32_bf16 v[164:167], a[184:187], v[140:143], v[164:167]\n"
      "v_mfma_f32_16x16x32_bf16 v[206:209], a[152:155], v[202:205], v[206:209]\n"
      "v_mfma_f32_16x16x32_bf16 v[206:209], a[188:191], v[140:143], v[206:209]\n"
      "v_mfma_f32_16x16x32_bf16 v[144:147], a[156:159], v[202:205], v[144:147]\n"
      "v_mfma_f32_16x16x32_bf16 v[144:147], a[192:195], v[140:143], v[144:147]\n"
      "s_nop 1\n"
      "v_fma_f32 v210, v180, v180, s38\n"
      "v_mfma_f32_16x16x32_bf16 v[232:235], a[160:163], v[202:205], v[232:235]\n"
      "v_fmac_f32 v210, v181, v181\n"
      "v_fmac_f32_e64 v210, v182, v182\n"
      "v_mfma_f32_16x16x32_bf16 v[232:235], a[196:199], v[140:143], v[232:235]\n"
      "s_nop 0\n"
      "v_fma_f32 v211, v144, v144, s38\n"
      "v_fmac_f32 v211, v145, v145\n"
      "v_mfma_f32_16x16x32_bf16 v[172:175], a[164:167], v[202:205], v[172:175]\n"
      "v_fmac_f32 v211, v146, v146\n"
      "v_fmac_f32 v210, v183, v183\n"
      "v_mfma_f32_16x16x32_bf16 v[172:175], a[200:203], v[140:143], v[172:175]\n"
      "v_fmac_f32 v210, v164, v164\n"
      "v_fmac_f32 v211, v147, v147\n"
      "v_mfma_f32_16x16x32_f16 v[184:187], v[252:255], v[156:159], v[70:73]\n"
      "v_fmac_f32 v210, v165, v165\n"
      "v_fmac_f32 v210, v166, v166\n"
      "v_mfma_f32_16x16x32_f16 v[184:187], v[246:249], v[152:155], v[184:187]\n"
      "v_fmac_f32 v211, v232, v232\n"
      "v_fmac_f32 v211, v233, v233\n"
      "v_mfma_f32_16x16x32_f16 v[184:187], v[216:219], v[32:35], v[184:187]\n"
      "v_fmac_f32 v211, v234, v234\n"
      "v_fmac_f32 v210, v167, v167\n"
      "v_fmac_f32 v210, v206, v206\n"
      "v_fmac_f32 v210, v207, v207\n"
      "v_fmac_f32 v211, v235, v235\n"
      "v_fmac_f32 v211, v172, v172\n"
      "v_fmac_f32 v210, v208, v208\n"
      "v_fmac_f32 v210, v209, v209\n"
      "v_fmac_f32 v211, v173, v173\n"
      "v_fmac_f32 v211, v174, v174\n"
      "v_fmac_f32 v211, v175, v175\n"
      "v_mov_b32 v161, v185\n"
      "v_mov_b32 v160, v184\n"
      "v_permlane16_swap_b32 v210, v211\n"
      "v_add_f32 v210, v210, v211\n"
      "v_mov_b32 v211, v210\n"
      "s_nop 1\n"
      "v_permlane32_swap_b32 v210, v211\n"
      "v_add_f32 v210, v210, v211\n"
      "v_rsq_f32 v210, v210\n"
      "s_nop 0\n"
      "v_mov_b32 v211, v210\n"
      "s_nop 1\n"
      "v_permlane16_swap_b32 v210, v211\n"
      "v_mul_f32 v210, s36, v210\n"
      "v_mul_f32 v147, v211, v147\n"
      "v_mul_f32 v145, v211, v145\n"
      "v_mul_f32 v144, v211, v144\n"
      "v_mul_f32 v232, v211, v232\n"
      "v_mul_f32 v146, v211, v146\n"
      "v_mul_f32 v235, v211, v235\n"
      "v_mul_f32 v167, v210, v167\n"
      "v_mul_f32 v183, v210, v183\n"
      "v_mul_f32 v233, v211, v233\n"
      "v_cvt_pk_f16_f32 v144, v144, v145\n"
      "v_cvt_pk_f16_f32 v145, v146, v147\n"
      "v_mul_f32 v166, v210, v166\n"
      "v_mul_f32 v172, v211, v172\n"
      "v_mul_f32 v165, v210, v165\n"
      "v_mul_f32 v173, v211, v173\n"
      "v_cvt_pk_f16_f32 v146, v232, v233\n"
      "v_mul_f32 v182, v210, v182\n"
      "v_mul_f32 v174, v211, v174\n"
      "v_mul_f32 v175, v211, v175\n"
      "v_mul_f32 v180, v210, v180\n"
      "v_mul_f32 v234, v211, v234\n"
      "v_cvt_pk_f16_f32 v147, v234, v235\n"
      "v_cvt_pk_f16_f32 v46, v172, v173\n"
      "v_cvt_pk_f16_f32 v47, v174, v175\n"
      "v_mul_f32 v164, v210, v164\n"
      "v_mfma_f32_16x16x32_f16 v[232:235], v[144:147], v[156:159], v[86:89]\n"
      "v_mul_f32_e64 v181, v210, v181\n"
      "v_cvt_pk_f16_f32 v180, v180, v181\n"
      "v_mfma_f32_16x16x32_f16 v[232:235], v[46:49], v[32:35], v[232:235]\n"
      "v_cvt_pk_f16_f32 v181, v182, v183\n"
      "v_cvt_pk_f16_f32 v182, v164, v165\n"
      "v_cvt_pk_f16_f32 v183, v166, v167\n"
      "v_mul_f32 v208, v210, v208\n"
      "v_mul_f32 v206, v210, v206\n"
      "v_mfma_f32_16x16x32_f16 v[164:167], v[152:155], v[156:159], v[78:81]\n"
      "v_mul_f32_e64 v207, v210, v207\n"
      "v_cvt_pk_f16_f32 v44, v206, v207\n"
      "v_mfma_f32_16x16x32_f16 v[164:167], v[34:37], v[32:35], v[164:167]\n"
      "v_mul_f32 v209, v210, v209\n"
      "v_cvt_pk_f16_f32 v45, v208, v209\n"
      "v_mfma_f32_16x16x32_f16 v[206:209], v[212:215], v[156:159], v[82:85]\n"
      "v_mfma_f32_16x16x32_f16 v[206:209], v[40:43], v[32:35], v[206:209]\n"
      "s_nop 7\n"
      "s_or_b64 vcc, s[48:49], s[50:51]\n"
      "s_cbranch_scc0 L_fast_11_%=\n"
      "L_ok_65_%=:\n"
      "s_cmp_eq_u64 s[48:49], 0\n"
      "s_cbranch_scc1 L_go_66_%=\n"
      "s_mov_b32 m0, s27\n"
      "s_nop 0\n"
      "s_movrels_b32 s9, s80\n"
      "s_lshr_b32 s8, s9, 8\n"
      "s_and_b32 s9, s9, 0xff\n"
      "s_mul_i32 s10, s8, 73\n"
      "s_add_u32 s10, s10, s9\n"
      "s_mul_i32 s30, s10, 4802\n"
      "s_add_u32 s30, s30, s18\n"
      "s_and_b32 s29, s30, 15\n"
      "s_add_u32 s30, s29, 110592\n"
      "s_lshl_b64 s[34:35], s[48:49], 4\n"
      "s_lshr_b64 s[34:35], s[34:35], 5\n"
      "s_cbranch_scc1 L_gen_9_%=\n"
      "v_mbcnt_lo_u32_b32 v210, -1, 0\n"
      "v_mbcnt_hi_u32_b32 v210, -1, v210\n"
      "v_lshlrev_b32 v211, 1, v210\n"
      "v_add_u32 v204, 4802, v211\n"
      "v_cmp_gt_u32 vcc, s29, v211\n"
      "s_nop 1\n"
      "v_cndmask_b32 v211, v204, v211, vcc\n"
      "v_mov_b32 v204, 0\n"
      "v_add_u32 v211, 110592, v211\n"
      "s_mov_b64 exec, 0x7f\n"
      "ds_write_b16 v211, v204\n"
      "s_mov_b64 exec, -1\n"
      "v_lshlrev_b32_e64 v211, 4, v210\n"
      "v_add_u32 v211, 110592, v211\n"
      "ds_read_b128 v[184:187], v211\n"
      "ds_read_b128 v[168:171], v211 offset:1024\n"
      "v_min_u32 v210, 111296, v211\n"
      "ds_read_b128 v[176:179], v211 offset:2048\n"
      "ds_read_b128 v[196:199], v210 offset:4096\n"
      "ds_read_b128 v[202:205], v211 offset:3072\n"
      "s_waitcnt lgkmcnt(4)\n"
      "v_or3_b32 v184, v184, v185, v186\n"
      "s_waitcnt lgkmcnt(3)\n"
      "v_or3_b32 v184, v184, v187, v168\n"
      "v_or3_b32 v184, v184, v169, v170\n"
      "s_waitcnt lgkmcnt(2)\n"
      "v_or3_b32 v184, v184, v171, v176\n"
      "v_or3_b32 v184, v184, v177, v178\n"
      "s_waitcnt lgkmcnt(0)\n"
      "v_or3_b32 v184, v184, v179, v202\n"
      "v_or3_b32 v184, v184, v203, v204\n"
      "v_or3_b32 v184, v184, v205, v196\n"
      "v_or3_b32 v184, v184, v197, v198\n"
      "v_bitop3_b32 v184, v184, v199, s40 bitop3:0xa8\n"
      "v_cmp_ne_u32 s[48:49], 0, v184\n"
      "s_cmp_lg_u64 s[48:49], 0\n"
      "s_cbranch_scc1 L_gen_9_%=\n"
      "s_cmp_eq_u64 s[50:51], 0\n"
      "s_cbranch_scc1 L_fast_11_%=\n"
      "L_go_66_%=:\n"
      "v_mfma_f32_16x16x32_f16 v[184:187], v[152:155], v[136:139], v[90:93]\n"
      "ds_read_b128 v[168:171], v128 offset:3072\n"
      "v_max3_f32 v205, v167, v206, v207\n"
      "v_mfma_f32_16x16x32_f16 v[184:187], v[34:37], v[38:41], v[184:187]\n"
      "v_max3_f32 v204, v164, v165, v166\n"
      "v_max3_f32 v204, v204, v208, v209\n"
      "v_mfma_f32_16x16x32_f16 v[176:179], v[212:215], v[136:139], v[94:97]\n"
      "v_max3_f32 v205, v205, v232, v233\n"
      "v_max3_f32 v204, v204, v234, v235\n"
      "v_mfma_f32_16x16x32_f16 v[176:179], v[40:43], v[38:41], v[176:179]\n"
      "v_max3_f32 v204, v204, v205, v160\n"
      "v_mov_b32 v205, v204\n"
      "v_mfma_f32_16x16x32_f16 v[196:199], v[252:255], v[136:139], v[72:75]\n"
      "s_nop 0\n"
      "v_permlane16_swap_b32 v204, v205\n"
      "v_mfma_f32_16x16x32_f16 v[196:199], v[246:249], v[212:215], v[196:199]\n"
      "v_max_f32 v204, v204, v205\n"
      "v_mov_b32 v205, v204\n"
      "v_mfma_f32_16x16x32_f16 v[196:199], v[216:219], v[38:41], v[196:199]\n"
      "s_nop 0\n"
      "v_permlane32_swap_b32 v204, v205\n"
      "v_mfma_f32_16x16x32_f16 v[156:159], v[144:147], v[136:139], v[98:101]\n"
      "v_max_f32 v204, v204, v205\n"
      "v_sub_f32 v164, v164, v204\n"
      "v_mfma_f32_16x16x32_f16 v[156:159], v[46:49], v[38:41], v[156:159]\n"
      "v_sub_f32 v206, v206, v204\n"
      "v_sub_f32 v165, v165, v204\n"
      "v_mfma_f32_16x16x32_f16 v[136:139], v[152:155], v[180:183], v[102:105]\n"
      "v_sub_f32 v167, v167, v204\n"
      "v_sub_f32 v207, v207, v204\n"
      "v_mfma_f32_16x16x32_f16 v[136:139], v[34:37], v[44:47], v[136:139]\n"
      "v_sub_f32 v208, v208, v204\n"
      "v_exp_f32 v165, v165\n"
      "v_sub_f32 v233, v233, v204\n"
      "v_mfma_f32_16x16x32_f16 v[198:201], v[212:215], v[180:183], v[106:109]\n"
      "v_sub_f32 v209, v209, v204\n"
      "v_sub_f32 v235, v235, v204\n"
      "v_mfma_f32_16x16x32_f16 v[198:201], v[40:43], v[44:47], v[198:201]\n"
      "v_sub_f32 v232, v232, v204\n"
      "v_sub_f32 v166, v166, v204\n"
      "v_mfma_f32_16x16x32_f16 v[212:215], v[252:255], v[180:183], v[74:77]\n"
      "v_sub_f32 v234, v234, v204\n"
      "v_sub_f32 v160, v160, v204\n"
      "v_mfma_f32_16x16x32_f16 v[212:215], v[246:249], v[144:147], v[212:215]\n"
      "v_exp_f32 v164, v164\n"
      "v_mfma_f32_16x16x32_f16 v[212:215], v[216:219], v[44:47], v[212:215]\n"
      "s_waitcnt lgkmcnt(0)\n"
      "v_exp_f32 v234, v234\n"
      "v_mfma_f32_16x16x32_f16 v[202:205], v[144:147], v[180:183], v[110:113]\n"
      "v_exp_f32 v166, v166\n"
      "v_mfma_f32_16x16x32_f16 v[202:205], v[46:49], v[44:47], v[202:205]\n"
      "v_exp_f32 v233, v233\n"
      "v_cvt_pk_bf16_f32 v164, v164, v165\n"
      "v_mfma_f32_16x16x32_f16 v[180:183], v[246:249], v[252:255], v[76:79]\n"
      "v_exp_f32 v167, v167\n"
      "v_exp_f32 v235, v235\n"
      "v_exp_f32 v206, v206\n"
      "v_bitop3_b32 v170, v170, v171, s4 bitop3:0x7e\n"
      "v_bitop3_b32 v168, v168, v169, s4 bitop3:0x7e\n"
      "v_cvt_pk_bf16_f32 v165, v166, v167\n"
      "v_mfma_f32_16x16x16_f16 v[180:183], v[218:219], v[216:217], v[180:183]\n"
      "v_exp_f32 v208, v208\n"
      "v_exp_f32 v209, v209\n"
      "v_exp_f32 v207, v207\n"
      "v_exp_f32 v160, v160\n"
      "v_cvt_pk_bf16_f32 v166, v206, v207\n"
      "v_or_b32 v168, v168, v170\n"
      "v_cvt_pk_bf16_f32 v167, v208, v209\n"
      "v_exp_f32 v232, v232\n"
      "v_cvt_pk_bf16_f32 v64, v160, 0\n"
      "v_cvt_pk_bf16_f32 v63, v234, v235\n"
      "v_cmp_ne_u32 vcc, 0, v168\n"
      "v_cvt_pk_bf16_f32 v62, v232, v233\n"
      "v_mfma_f32_16x16x32_bf16 v[232:235], v[66:69], v[164:167], 0\n"
      "s_mov_b64 s[48:49], vcc\n"
      "v_max3_f32 v206, v184, v185, v186\n"
      "v_max3_f32 v206, v206, v178, v179\n"
      "v_mfma_f32_16x16x32_bf16 v[232:235], v[66:69], v[62:65], v[232:235]\n"
      "s_cbranch_vccnz L_flg_67_%=\n"
      "L_fb_68_%=:\n"
      "v_mfma_f32_16x16x32_bf16 v[144:147], v[228:231], v[164:167], 0\n"
      "s_nop 0\n"
      "v_max3_f32 v207, v187, v176, v177\n"
      "v_max3_f32 v207, v207, v156, v157\n"
      "v_mfma_f32_16x16x32_bf16 v[144:147], v[50:53], v[62:65], v[144:147]\n"
      "v_max3_f32 v206, v206, v158, v159\n"
      "v_max3_f32 v206, v206, v207, v196\n"
      "v_mfma_f32_16x16x32_bf16 v[168:171], v[148:151], v[164:167], 0\n"
      "v_mov_b32_e64 v207, v206\n"
      "v_max3_f32 v214, v139, v198, v199\n"
      "v_mfma_f32_16x16x32_bf16 v[168:171], v[54:57], v[62:65], v[168:171]\n"
      "v_permlane16_swap_b32 v206, v207\n"
      "v_mfma_f32_16x16x32_bf16 v[250:253], v[190:193], v[164:167], 0\n"
      "v_max_f32 v206, v206, v207\n"
      "v_mov_b32_e64 v207, v206\n"
      "v_mfma_f32_16x16x32_bf16 v[250:253], v[58:61], v[62:65], v[250:253]\n"
      "s_nop 0\n"
      "v_permlane32_swap_b32 v206, v207\n"
      "v_max_f32 v206, v206, v207\n"
      "v_rcp_f32 v232, v232\n"
      "v_sub_f32 v187, v187, v206\n"
      "v_sub_f32 v176, v176, v206\n"
      "v_sub_f32 v184, v184, v206\n"
      "v_sub_f32 v177, v177, v206\n"
      "v_sub_f32 v158, v158, v206\n"
      "v_sub_f32 v179, v179, v206\n"
      "v_mul_f32 v144, v232, v144\n"
      "v_sub_f32 v186, v186, v206\n"
      "v_exp_f32 v186, v186\n"
      "v_sub_f32 v156, v156, v206\n"
      "v_sub_f32 v196, v196, v206\n"
      "v_sub_f32 v178, v178, v206\n"
      "v_exp_f32 v156, v156\n"
      "v_sub_f32 v157, v157, v206\n"
      "v_exp_f32 v178, v178\n"
      "v_exp_f32 v177, v177\n"
      "v_exp_f32 v184, v184\n"
      "v_exp_f32 v187, v187\n"
      "v_exp_f32 v157, v157\n"
      "v_exp_f32 v179, v179\n"
      "v_sub_f32 v185, v185, v206\n"
      "v_exp_f32 v176, v176\n"
      "v_exp_f32 v185, v185\n"
      "v_exp_f32 v196, v196\n"
      "v_cvt_pk_bf16_f32 v184, v184, v185\n"
      "v_sub_f32_e64 v159, v159, v206\n"
      "v_cvt_pk_bf16_f32 v185, v186, v187\n"
      "v_cvt_pk_bf16_f32 v186, v176, v177\n"
      "v_cvt_pk_bf16_f32 v62, v156, v157\n"
      "v_cvt_pk_bf16_f32 v64, v196, 0\n"
      "v_exp_f32 v159, v159\n"
      "v_exp_f32 v158, v158\n"
      "v_cvt_pk_bf16_f32 v187, v178, v179\n"
      "v_cvt_pk_bf16_f32 v63, v158, v159\n"
      "v_mul_f32 v145, v232, v145\n"
      "v_mfma_f32_16x16x32_bf16 v[162:165], v[66:69], v[184:187], 0\n"
      "s_add_u32 s28, s27, 2\n"
      "v_mul_f32_e64 v146, v232, v146\n"
      "v_cvt_pk_bf16_f32 v144, v144, v145\n"
      "v_mfma_f32_16x16x32_bf16 v[162:165], v[66:69], v[62:65], v[162:165]\n"
      "s_min_u32 m0, s28, s3\n"
      "v_mul_f32 v147, v232, v147\n"
      "v_cvt_pk_bf16_f32 v145, v146, v147\n"
      "v_mfma_f32_16x16x32_bf16 v[176:179], v[228:231], v[184:187], 0\n"
      "s_movrels_b32 s9, s80\n"
      "v_mul_f32 v169, v232, v169\n"
      "v_mul_f32 v168, v232, v168\n"
      "v_mfma_f32_16x16x32_bf16 v[176:179], v[50:53], v[62:65], v[176:179]\n"
      "s_lshr_b32 s8, s9, 8\n"
      "v_mul_f32_e64 v171, v232, v171\n"
      "v_cvt_pk_bf16_f32 v146, v168, v169\n"
      "v_mfma_f32_16x16x32_bf16 v[218:221], v[148:151], v[184:187], 0\n"
      "s_and_b32 s9, s9, 0xff\n"
      "v_mul_f32_e64 v170, v232, v170\n"
      "v_cvt_pk_bf16_f32 v147, v170, v171\n"
      "v_mfma_f32_16x16x32_bf16 v[218:221], v[54:57], v[62:65], v[218:221]\n"
      "ds_write_b128 v124, v[144:147]\n"
      "v_mul_f32_e64 v253, v232, v253\n"
      "v_mfma_f32_16x16x32_bf16 v[156:159], v[190:193], v[184:187], 0\n"
      "s_mul_i32 s10, s8, 73\n"
      "v_mul_f32 v252, v232, v252\n"
      "v_mul_f32 v250, v232, v250\n"
      "v_mfma_f32_16x16x32_bf16 v[156:159], v[58:61], v[62:65], v[156:159]\n"
      "s_add_u32 s10, s10, s9\n"
      "v_mul_f32 v251, v232, v251\n"
      "v_max3_f32 v196, v136, v137, v138\n"
      "v_cvt_pk_bf16_f32 v250, v250, v251\n"
      "v_max3_f32 v196, v196, v200, v201\n"
      "v_max3_f32 v214, v214, v202, v203\n"
      "v_max3_f32 v196, v196, v204, v205\n"
      "v_max3_f32 v196, v196, v214, v212\n"
      "v_mov_b32_e64 v206, v196\n"
      "v_cndmask_b32 v161, v161, v197, s[42:43]\n"
      "v_cndmask_b32 v161, v161, v213, s[44:45]\n"
      "v_cvt_pk_bf16_f32 v251, v252, v253\n"
      "v_permlane16_swap_b32 v196, v206\n"
      "v_max_f32 v196, v196, v206\n"
      "v_mov_b32 v197, v196\n"
      "v_cndmask_b32 v161, v161, v181, s[46:47]\n"
      "v_rcp_f32 v162, v162\n"
      "v_permlane32_swap_b32 v196, v197\n"
      "v_max_f32 v196, v196, v197\n"
      "v_sub_f32 v198, v198, v196\n"
      "v_sub_f32 v204, v204, v196\n"
      "v_sub_f32 v200, v200, v196\n"
      "v_exp_f32 v204, v204\n"
      "v_sub_f32 v202, v202, v196\n"
      "v_sub_f32 v138, v138, v196\n"
      "v_sub_f32 v199, v199, v196\n"
      "v_sub_f32 v136, v136, v196\n"
      "v_sub_f32 v212, v212, v196\n"
      "v_sub_f32 v203, v203, v196\n"
      "v_exp_f32 v138, v138\n"
      "v_sub_f32 v137, v137, v196\n"
      "v_exp_f32 v199, v199\n"
      "v_sub_f32 v139, v139, v196\n"
      "v_exp_f32 v200, v200\n"
      "v_exp_f32 v139, v139\n"
      "v_sub_f32 v201, v201, v196\n"
      "v_exp_f32 v136, v136\n"
      "v_exp_f32 v137, v137\n"
      "v_sub_f32 v205, v205, v196\n"
      "v_exp_f32 v205, v205\n"
      "v_cvt_pk_bf16_f32 v136, v136, v137\n"
      "v_max_f32_dpp v216, v161, v161 row_ror:8 row_mask:0xf bank_mask:0xf\n"
      "v_exp_f32 v201, v201\n"
      "v_cvt_pk_bf16_f32 v137, v138, v139\n"
      "v_exp_f32 v198, v198\n"
      "v_exp_f32 v203, v203\n"
      "v_cvt_pk_bf16_f32 v138, v198, v199\n"
      "v_cvt_pk_bf16_f32 v139, v200, v201\n"
      "v_cvt_pk_bf16_f32 v63, v204, v205\n"
      "v_exp_f32_e64 v202, v202\n"
      "v_exp_f32_e64 v212, v212\n"
      "v_cvt_pk_bf16_f32 v62, v202, v203\n"
      "v_mul_f32_e64 v176, v162, v176\n"
      "v_cvt_pk_bf16_f32 v64, v212, 0\n"
      "v_mfma_f32_16x16x32_bf16 v[180:183], v[66:69], v[136:139], 0\n"
      "ds_read_b128 v[144:147], v115 offset:18432\n"
      "v_max_f32_dpp v216, v216, v216 row_ror:4 row_mask:0xf bank_mask:0xf\n"
      "v_mfma_f32_16x16x32_bf16 v[180:183], v[66:69], v[62:65], v[180:183]\n"
      "ds_read_b128 v[232:235], v114 offset:18432\n"
      "v_max_f32_dpp v216, v216, v216 row_ror:2 row_mask:0xf bank_mask:0xf\n"
      "v_mfma_f32_16x16x32_bf16 v[184:187], v[228:231], v[136:139], 0\n"
      "s_mul_i32 s28, s10, 4802\n"
      "v_mul_f32 v177, v162, v177\n"
      "v_max_f32_dpp v216, v216, v216 row_ror:1 row_mask:0xf bank_mask:0xf\n"
      "v_mfma_f32_16x16x32_bf16 v[184:187], v[50:53], v[62:65], v[184:187]\n"
      "ds_read_b128 v[168:171], v114 offset:24576\n"
      "v_max_f32_dpp v216, v216, v216 row_bcast:15 row_mask:0xa bank_mask:0xf\n"
      "v_mfma_f32_16x16x32_bf16 v[164:167], v[148:151], v[136:139], 0\n"
      "s_add_u32 s56, s18, s28\n"
      "v_cvt_pk_bf16_f32 v176, v176, v177\n"
      "v_max_f32_dpp v216, v216, v216 row_bcast:31 row_mask:0xc bank_mask:0xf\n"
      "v_mfma_f32_16x16x32_bf16 v[164:167], v[54:57], v[62:65], v[164:167]\n"
      "ds_read_b128 v[252:255], v115 offset:24576\n"
      "v_mul_f32_e64 v218, v162, v218\n"
      "v_readlane_b32 s31, v216, 63\n"
      "v_mfma_f32_16x16x32_bf16 v[204:207], v[190:193], v[136:139], 0\n"
      "s_addc_u32 s57, s19, 0\n"
      "v_subrev_f32 v161, s31, v161\n"
      "v_mul_f32 v178, v162, v178\n"
      "v_mfma_f32_16x16x32_bf16 v[204:207], v[58:61], v[62:65], v[204:207]\n"
      "s_and_b32 s56, s56, -16\n"
      "v_exp_f32_e64 v161, v161\n"
      "v_mul_f32_e64 v219, v162, v219\n"
      "v_mul_f32 v179, v162, v179\n"
      "v_cvt_pk_bf16_f32 v177, v178, v179\n"
      "v_cvt_pk_bf16_f32 v178, v218, v219\n"
      "v_cvt_pk_bf16_f32 v161, v161, v161\n"
      "s_waitcnt lgkmcnt(2)\n"
      "v_mfma_f32_16x16x32_bf16 v[208:211], a[0:3], v[232:235], 0\n"
      "s_sub_u32 s58, s56, 110592\n"
      "v_mul_f32 v220, v162, v220\n"
      "v_mul_f32 v221, v162, v221\n"
      "v_mfma_f32_16x16x32_bf16 v[208:211], a[36:39], v[144:147], v[208:211]\n"
      "ds_write_b16 v130, v161\n"
      "v_mfma_f32_16x16x32_bf16 v[216:219], a[4:7], v[232:235], 0\n"
      "ds_read_b128 v[136:139], v131 offset:64\n"
      "v_cvt_pk_bf16_f32 v179, v220, v221\n"
      "v_mfma_f32_16x16x32_bf16 v[216:219], a[40:43], v[144:147], v[216:219]\n"
      "ds_read_b128 v[200:203], v131\n"
      "v_mul_f32 v157, v162, v157\n"
      "v_mfma_f32_16x16x32_bf16 v[212:215], a[12:15], v[232:235], 0\n"
      "s_subb_u32 s59, s57, 0\n"
      "v_mul_f32_e64 v156, v162, v156\n"
      "v_cvt_pk_bf16_f32 v156, v156, v157\n"
      "v_mfma_f32_16x16x32_bf16 v[212:215], a[48:51], v[144:147], v[212:215]\n"
      "ds_write_b128 v124, v[176:179] offset:1024\n"
      "v_mfma_f32_16x16x32_bf16 v[176:179], a[8:11], v[232:235], 0\n"
      "s_sub_u32 s56, s56, 73728\n"
      "v_mul_f32 v158, v162, v158\n"
      "v_mul_f32 v159, v162, v159\n"
      "v_mfma_f32_16x16x32_bf16 v[176:179], a[44:47], v[144:147], v[176:179]\n"
      "s_subb_u32 s57, s57, 0\n"
      "v_mov_b32 v162, 0\n"
      "v_cvt_pk_bf16_f32 v157, v158, v159\n"
      "v_mfma_f32_16x16x32_bf16 v[196:199], a[16:19], v[232:235], 0\n"
      "s_mul_i32 s28, s8, 1373568\n"
      "s_mul_i32 s29, s9, 2688\n"
      "s_add_u32 s28, s28, s29\n"
      "v_mfma_f32_16x16x32_bf16 v[196:199], a[52:55], v[144:147], v[196:199]\n"
      "ds_write2st64_b64 v125, v[250:251], v[156:157] offset0:0 offset1:2\n"
      "s_waitcnt lgkmcnt(2)\n"
      "v_mfma_f32_16x16x32_bf16 v[158:161], v[200:203], v[66:69], 0\n"
      "s_cmp_eq_u32 s8, 72\n"
      "s_cselect_b32 s8, 0, 100270464\n"
      "s_cmp_eq_u32 s9, 72\n"
      "v_mfma_f32_16x16x32_bf16 v[158:161], v[136:139], v[66:69], v[158:161]\n"
      "ds_write_b32 v129, v162\n"
      "v_mfma_f32_16x16x32_bf16 v[248:251], v[200:203], v[228:231], 0\n"
      "s_cselect_b32 s9, 0, 196224\n"
      "s_add_u32 s10, s8, s9\n"
      "s_add_u32 s28, s28, s10\n"
      "v_mfma_f32_16x16x32_bf16 v[248:251], v[136:139], v[50:53], v[248:251]\n"
      "s_add_u32 s52, s16, s28\n"
      "s_addc_u32 s53, s17, 0\n"
      "v_mfma_f32_16x16x32_bf16 v[228:231], v[200:203], v[148:151], 0\n"
      "v_mov_b32_dpp v158, v158 quad_perm:[0,0,0,0] row_mask:0xf bank_mask:0xf\n"
      "v_mfma_f32_16x16x32_bf16 v[228:231], v[136:139], v[54:57], v[228:231]\n"
      "s_cmp_lg_u32 s10, s11\n"
      "v_mfma_f32_16x16x32_bf16 v[148:151], v[200:203], v[190:193], 0\n"
      "v_rcp_f32 v158, v158\n"
      "v_mfma_f32_16x16x32_bf16 v[148:151], v[136:139], v[58:61], v[148:151]\n"
      "s_cbranch_scc1 L_edge_70_%=\n"
      "L_eb_71_%=:\n"
      "v_mfma_f32_16x16x32_bf16 v[160:163], a[20:23], v[232:235], 0\n"
      "s_nop 1\n"
      "v_cndmask_b32 v248, v248, v228, s[42:43]\n"
      "v_mfma_f32_16x16x32_bf16 v[160:163], a[56:59], v[144:147], v[160:163]\n"
      "s_nop 1\n"
      "v_cndmask_b32 v248, v248, v148, s[44:45]\n"
      "v_mul_f32 v248, v158, v248\n"
      "v_cvt_pk_bf16_f32 v248, v248, v248\n"
      "ds_write_b16 v135, v248\n"
      "s_branch L_join_10_%=\n"
      "L_flg_67_%=:\n"
      "v_cmp_gt_i32 vcc, 0, v168\n"
      "s_cbranch_vccz L_fb_68_%=\n"
      "s_sleep 1\n"
      "ds_read_b128 v[168:171], v128 offset:3072\n"
      "s_waitcnt lgkmcnt(0)\n"
      "v_bitop3_b32 v168, v168, v169, s4 bitop3:0x7e\n"
      "v_bitop3_b32 v170, v170, v171, s4 bitop3:0x7e\n"
      "v_or_b32 v168, v168, v170\n"
      "v_cmp_ne_u32 s[48:49], 0, v168\n"
      "s_branch L_flg_67_%=\n"
      "L_edge_70_%=:\n"
      "s_mov_b32 s11, s10\n"
      "s_sub_u32 s28, s60, s8\n"
      "s_sub_u32 s29, s61, s9\n"
      "s_mov_b32 s60, s8\n"
      "s_mov_b32 s61, s9\n"
      "s_mov_b64 exec, s[64:65]\n"
      "v_add_u32 v116, s28, v116\n"
      "s_mov_b64 exec, s[70:71]\n"
      "v_add_u32 v116, s29, v116\n"
      "s_mov_b64 exec, s[66:67]\n"
      "v_add_u32 v117, s28, v117\n"
      "s_mov_b64 exec, s[72:73]\n"
      "v_add_u32 v117, s29, v117\n"
      "s_mov_b64 exec, s[68:69]\n"
      "v_add_u32 v118, s28, v118\n"
      "s_mov_b64 exec, s[74:75]\n"
      "v_add_u32_e64 v118, s29, v118\n"
      "s_mov_b64 exec, -1\n"
      "s_nop 4\n"
      "s_branch L_eb_71_%=\n"
      "L_gen_9_%=:\n"
      "s_nop 0\n"
      "s_nop 0\n"
      "v_mfma_f32_16x16x32_f16 v[184:187], v[152:155], v[136:139], v[90:93]\n"
      "v_mbcnt_lo_u32_b32 v204, -1, 0\n"
      "v_mbcnt_hi_u32_b32 v204, -1, v204\n"
      "v_mfma_f32_16x16x32_f16 v[184:187], v[34:37], v[38:41], v[184:187]\n"
      "s_movk_i32 s31, 0x3fb9\n"
      "v_and_b32 v205, 15, v204\n"
      "v_lshrrev_b32 v210, 4, v204\n"
      "v_mul_u32_u24 v211, 0x62, v205\n"
      "v_lshl_add_u32 v211, v210, 3, v211\n"
      "v_add_u32 v211, s30, v211\n"
      "v_lshlrev_b32 v210, 2, v210\n"
      "v_sub_u32 v205, v205, v210\n"
      "v_and_b32 v210, 1, v205\n"
      "v_lshrrev_b32 v205, 1, v205\n"
      "v_lshlrev_b32 v210, 4, v210\n"
      "v_lshlrev_b32 v210, v210, s31\n"
      "v_cmp_eq_u32 vcc, 0, v205\n"
      "v_and_b32 v204, -4, v211\n"
      "v_and_b32_e64 v211, 2, v211\n"
      "v_cndmask_b32 v162, 0, v210, vcc\n"
      "ds_read2_b32 v[196:197], v204 offset0:0 offset1:1\n"
      "ds_read2_b32 v[168:169], v204 offset0:16 offset1:17\n"
      "ds_read2_b32 v[198:199], v204 offset0:8 offset1:9\n"
      "ds_read2_b32 v[170:171], v204 offset0:18 offset1:24\n"
      "ds_read2_b32 v[188:189], v204 offset0:2 offset1:10\n"
      "v_add_u32 v204, 1568, v204\n"
      "v_cmp_eq_u32 vcc, 1, v205\n"
      "v_mfma_f32_16x16x32_f16 v[176:179], v[212:215], v[136:139], v[94:97]\n"
      "ds_read2_b32 v[250:251], v204 offset0:2 offset1:10\n"
      "ds_read2_b32 v[140:141], v204 offset0:18 offset1:24\n"
      "ds_read2_b32 v[194:195], v204 offset0:8 offset1:9\n"
      "ds_read2_b32 v[244:245], v204 offset0:0 offset1:1\n"
      "ds_read2_b32 v[142:143], v204 offset0:16 offset1:17\n"
      "v_cndmask_b32 v163, 0, v210, vcc\n"
      "v_add_u32 v204, 1568, v204\n"
      "v_mfma_f32_16x16x32_f16 v[220:223], v[252:255], v[136:139], v[72:75]\n"
      "ds_read2_b32 v[156:157], v204 offset0:8 offset1:9\n"
      "v_mfma_f32_16x16x32_f16 v[220:223], v[246:249], v[212:215], v[220:223]\n"
      "ds_read2_b32 v[158:159], v204 offset0:16 offset1:17\n"
      "ds_read2_b32 v[172:173], v204 offset0:0 offset1:1\n"
      "ds_read2_b32 v[200:201], v204 offset0:18 offset1:24\n"
      "v_mfma_f32_16x16x32_f16 v[176:179], v[40:43], v[38:41], v[176:179]\n"
      "ds_read2_b32 v[174:175], v204 offset0:2 offset1:10\n"
      "s_waitcnt lgkmcnt(11)\n"
      "v_alignbyte_b32 v168, v169, v168, v211\n"
      "v_alignbyte_b32 v169, v170, v169, v211\n"
      "v_mfma_f32_16x16x32_f16 v[220:223], v[216:219], v[38:41], v[220:223]\n"
      "s_waitcnt lgkmcnt(10)\n"
      "v_alignbyte_b32 v171, v171, v171, v211\n"
      "v_lshlrev_b32 v171, 16, v171\n"
      "v_mfma_f32_16x16x16_bf16 v[232:235], v[162:163], v[168:169], v[232:235]\n"
      "s_waitcnt lgkmcnt(9)\n"
      "v_fmac_f32 v160, s37, v171\n"
      "v_alignbyte_b32 v196, v197, v196, v211\n"
      "v_alignbyte_b32 v197, v188, v197, v211\n"
      "s_waitcnt lgkmcnt(6)\n"
      "v_alignbyte_b32 v244, v245, v244, v211\n"
      "v_alignbyte_b32 v245, v250, v245, v211\n"
      "v_alignbyte_b32 v198, v199, v198, v211\n"
      "v_alignbyte_b32 v194, v195, v194, v211\n"
      "v_mfma_f32_16x16x32_f16 v[168:171], v[144:147], v[136:139], v[98:101]\n"
      "ds_read_b128 v[136:139], v128 offset:3072\n"
      "v_alignbyte_b32 v141, v141, v141, v211\n"
      "v_mfma_f32_16x16x32_f16 v[168:171], v[46:49], v[38:41], v[168:171]\n"
      "v_alignbyte_b32 v195, v251, v195, v211\n"
      "v_alignbyte_b32 v199, v189, v199, v211\n"
      "v_mfma_f32_16x16x16_bf16 v[164:167], v[162:163], v[196:197], v[164:167]\n"
      "s_waitcnt lgkmcnt(3)\n"
      "v_alignbyte_b32 v142, v143, v142, v211\n"
      "v_alignbyte_b32 v143, v140, v143, v211\n"
      "v_mfma_f32_16x16x16_bf16 v[206:209], v[162:163], v[198:199], v[206:209]\n"
      "s_waitcnt lgkmcnt(1)\n"
      "v_alignbyte_b32 v172, v173, v172, v211\n"
      "v_alignbyte_b32 v173, v174, v173, v211\n"
      "v_mfma_f32_16x16x32_f16 v[196:199], v[152:155], v[180:183], v[102:105]\n"
      "v_alignbyte_b32 v156, v157, v156, v211\n"
      "v_alignbyte_b32 v157, v175, v157, v211\n"
      "v_mfma_f32_16x16x32_f16 v[196:199], v[34:37], v[44:47], v[196:199]\n"
      "v_alignbyte_b32 v158, v159, v158, v211\n"
      "v_max3_f32 v205, v164, v165, v166\n"
      "v_mfma_f32_16x16x32_f16 v[152:155], v[212:215], v[180:183], v[106:109]\n"
      "v_alignbyte_b32 v201, v201, v201, v211\n"
      "v_alignbyte_b32 v159, v200, v159, v211\n"
      "v_mfma_f32_16x16x32_f16 v[152:155], v[40:43], v[44:47], v[152:155]\n"
      "v_lshlrev_b32 v201, 16, v201\n"
      "v_max3_f32 v205, v205, v208, v209\n"
      "v_mfma_f32_16x16x32_f16 v[212:215], v[252:255], v[180:183], v[74:77]\n"
      "v_lshlrev_b32 v141, 16, v141\n"
      "v_max3_f32 v211, v167, v206, v207\n"
      "v_mfma_f32_16x16x32_f16 v[212:215], v[246:249], v[144:147], v[212:215]\n"
      "v_max3_f32 v205, v205, v234, v235\n"
      "v_max3_f32 v211, v211, v232, v233\n"
      "v_mfma_f32_16x16x32_f16 v[212:215], v[216:219], v[44:47], v[212:215]\n"
      "s_waitcnt lgkmcnt(0)\n"
      "v_max3_f32 v205, v205, v211, v160\n"
      "v_mov_b32 v211, v205\n"
      "v_mfma_f32_16x16x32_f16 v[224:227], v[144:147], v[180:183], v[110:113]\n"
      "s_nop 0\n"
      "v_permlane16_swap_b32 v205, v211\n"
      "v_mfma_f32_16x16x32_f16 v[224:227], v[46:49], v[44:47], v[224:227]\n"
      "v_bitop3_b32 v138, v138, v139, s4 bitop3:0x7e\n"
      "v_max_f32 v205, v205, v211\n"
      "v_mov_b32 v211, v205\n"
      "v_mfma_f32_16x16x16_bf16 v[184:187], v[162:163], v[244:245], v[184:187]\n"
      "s_nop 0\n"
      "v_permlane32_swap_b32 v205, v211\n"
      "v_mfma_f32_16x16x16_bf16 v[176:179], v[162:163], v[194:195], v[176:179]\n"
      "v_max_f32 v205, v205, v211\n"
      "v_sub_f32 v165, v165, v205\n"
      "v_sub_f32 v206, v206, v205\n"
      "v_mfma_f32_16x16x32_f16 v[180:183], v[246:249], v[252:255], v[76:79]\n"
      "v_exp_f32 v165, v165\n"
      "v_mfma_f32_16x16x16_bf16 v[168:171], v[162:163], v[142:143], v[168:171]\n"
      "v_sub_f32 v233, v233, v205\n"
      "v_exp_f32 v233, v233\n"
      "v_sub_f32 v160, v160, v205\n"
      "v_sub_f32 v164, v164, v205\n"
      "v_exp_f32 v164, v164\n"
      "v_sub_f32 v167, v167, v205\n"
      "v_mfma_f32_16x16x16_f16 v[180:183], v[218:219], v[216:217], v[180:183]\n"
      "v_cvt_pk_bf16_f32 v164, v164, v165\n"
      "v_sub_f32 v208, v208, v205\n"
      "v_sub_f32 v232, v232, v205\n"
      "v_exp_f32 v206, v206\n"
      "v_sub_f32 v207, v207, v205\n"
      "v_exp_f32 v208, v208\n"
      "v_exp_f32 v232, v232\n"
      "v_exp_f32 v207, v207\n"
      "v_exp_f32 v160, v160\n"
      "v_sub_f32 v166, v166, v205\n"
      "v_exp_f32 v166, v166\n"
      "v_fmac_f32 v220, s37, v141\n"
      "v_exp_f32 v167, v167\n"
      "v_cvt_pk_bf16_f32 v62, v232, v233\n"
      "v_cvt_pk_bf16_f32 v165, v166, v167\n"
      "v_sub_f32 v235, v235, v205\n"
      "v_mfma_f32_16x16x16_bf16 v[152:155], v[162:163], v[156:157], v[152:155]\n"
      "v_sub_f32 v209, v209, v205\n"
      "v_exp_f32_e64 v209, v209\n"
      "v_cvt_pk_bf16_f32 v166, v206, v207\n"
      "v_mfma_f32_16x16x16_bf16 v[224:227], v[162:163], v[158:159], v[224:227]\n"
      "v_exp_f32 v235, v235\n"
      "v_cvt_pk_bf16_f32 v167, v208, v209\n"
      "v_sub_f32 v234, v234, v205\n"
      "v_cvt_pk_bf16_f32 v64, v160, 0\n"
      "v_mfma_f32_16x16x16_bf16 v[196:199], v[162:163], v[172:173], v[196:199]\n"
      "v_max3_f32 v214, v184, v185, v186\n"
      "v_exp_f32_e64 v234, v234\n"
      "v_bitop3_b32 v136, v136, v137, s4 bitop3:0x7e\n"
      "v_cvt_pk_bf16_f32 v63, v234, v235\n"
      "v_mfma_f32_16x16x32_bf16 v[232:235], v[66:69], v[164:167], 0\n"
      "v_or_b32 v136, v136, v138\n"
      "v_cmp_ne_u32 vcc, 0, v136\n"
      "v_mfma_f32_16x16x32_bf16 v[232:235], v[66:69], v[62:65], v[232:235]\n"
      "s_mov_b64 s[48:49], vcc\n"
      "v_max3_f32 v215, v187, v176, v177\n"
      "v_max3_f32 v214, v214, v178, v179\n"
      "v_mfma_f32_16x16x32_bf16 v[144:147], v[228:231], v[164:167], 0\n"
      "s_cbranch_vccnz L_flg_89_%=\n"
      "L_fb_90_%=:\n"
      "v_max3_f32 v215, v215, v168, v169\n"
      "v_mfma_f32_16x16x32_bf16 v[208:211], v[148:151], v[164:167], 0\n"
      "v_max3_f32 v214, v214, v170, v171\n"
      "v_max3_f32 v214, v214, v215, v220\n"
      "v_mfma_f32_16x16x32_bf16 v[208:211], v[54:57], v[62:65], v[208:211]\n"
      "v_fmac_f32 v212, s37, v201\n"
      "v_mov_b32 v215, v214\n"
      "v_mfma_f32_16x16x32_bf16 v[252:255], v[190:193], v[164:167], 0\n"
      "s_nop 0\n"
      "v_permlane16_swap_b32 v214, v215\n"
      "v_mfma_f32_16x16x32_bf16 v[252:255], v[58:61], v[62:65], v[252:255]\n"
      "v_max_f32 v214, v214, v215\n"
      "v_mov_b32 v215, v214\n"
      "v_max3_f32 v233, v199, v152, v153\n"
      "v_rcp_f32 v232, v232\n"
      "v_permlane32_swap_b32 v214, v215\n"
      "v_max_f32 v214, v214, v215\n"
      "v_sub_f32 v184, v184, v214\n"
      "v_sub_f32 v168, v168, v214\n"
      "v_sub_f32 v177, v177, v214\n"
      "v_sub_f32 v220, v220, v214\n"
      "v_sub_f32 v185, v185, v214\n"
      "v_exp_f32 v184, v184\n"
      "v_sub_f32 v186, v186, v214\n"
      "v_exp_f32 v177, v177\n"
      "v_mfma_f32_16x16x32_bf16 v[144:147], v[50:53], v[62:65], v[144:147]\n"
      "v_sub_f32 v170, v170, v214\n"
      "v_exp_f32 v185, v185\n"
      "v_sub_f32 v169, v169, v214\n"
      "v_sub_f32 v176, v176, v214\n"
      "v_sub_f32 v187, v187, v214\n"
      "v_sub_f32 v171, v171, v214\n"
      "v_sub_f32 v178, v178, v214\n"
      "v_exp_f32 v187, v187\n"
      "v_sub_f32 v179, v179, v214\n"
      "v_exp_f32 v176, v176\n"
      "v_exp_f32 v186, v186\n"
      "v_exp_f32 v178, v178\n"
      "v_cvt_pk_bf16_f32 v184, v184, v185\n"
      "v_exp_f32 v179, v179\n"
      "v_exp_f32 v170, v170\n"
      "v_exp_f32 v171, v171\n"
      "v_cvt_pk_bf16_f32 v185, v186, v187\n"
      "v_cvt_pk_bf16_f32 v187, v178, v179\n"
      "v_exp_f32 v220, v220\n"
      "v_cvt_pk_bf16_f32 v186, v176, v177\n"
      "v_mul_f32 v145, v232, v145\n"
      "v_mul_f32 v144, v232, v144\n"
      "v_exp_f32 v168, v168\n"
      "v_mul_f32 v147, v232, v147\n"
      "v_exp_f32 v169, v169\n"
      "v_max3_f32 v214, v196, v197, v198\n"
      "v_cvt_pk_bf16_f32 v63, v170, v171\n"
      "v_cvt_pk_bf16_f32 v62, v168, v169\n"
      "v_cvt_pk_bf16_f32 v64, v220, 0\n"
      "v_mfma_f32_16x16x32_bf16 v[176:179], v[66:69], v[184:187], 0\n"
      "v_cvt_pk_bf16_f32 v144, v144, v145\n"
      "v_mul_f32_e64 v146, v232, v146\n"
      "v_mfma_f32_16x16x32_bf16 v[176:179], v[66:69], v[62:65], v[176:179]\n"
      "v_cvt_pk_bf16_f32 v145, v146, v147\n"
      "v_max3_f32 v233, v233, v224, v225\n"
      "v_mul_f32 v254, v232, v254\n"
      "v_mfma_f32_16x16x32_bf16 v[168:171], v[228:231], v[184:187], 0\n"
      "v_mul_f32 v210, v232, v210\n"
      "v_mul_f32 v208, v232, v208\n"
      "v_mfma_f32_16x16x32_bf16 v[168:171], v[50:53], v[62:65], v[168:171]\n"
      "v_mul_f32 v209, v232, v209\n"
      "v_cvt_pk_bf16_f32 v146, v208, v209\n"
      "v_mfma_f32_16x16x32_bf16 v[216:219], v[148:151], v[184:187], 0\n"
      "v_mul_f32_e64 v211, v232, v211\n"
      "v_cvt_pk_bf16_f32 v147, v210, v211\n"
      "v_mfma_f32_16x16x32_bf16 v[216:219], v[54:57], v[62:65], v[216:219]\n"
      "ds_write_b128 v124, v[144:147]\n"
      "v_mfma_f32_16x16x32_bf16 v[136:139], v[190:193], v[184:187], 0\n"
      "s_add_u32 s28, s27, 2\n"
      "v_mul_f32 v253, v232, v253\n"
      "v_mul_f32 v252, v232, v252\n"
      "v_mfma_f32_16x16x32_bf16 v[136:139], v[58:61], v[62:65], v[136:139]\n"
      "s_min_u32 m0, s28, s3\n"
      "v_cvt_pk_bf16_f32 v252, v252, v253\n"
      "v_mul_f32_e64 v255, v232, v255\n"
      "v_max3_f32 v214, v214, v154, v155\n"
      "v_max3_f32 v214, v214, v226, v227\n"
      "v_max3_f32 v214, v214, v233, v212\n"
      "v_mov_b32 v215, v214\n"
      "v_cndmask_b32 v161, v161, v221, s[42:43]\n"
      "v_mbcnt_lo_u32_b32 v204, -1, 0\n"
      "v_permlane16_swap_b32 v214, v215\n"
      "v_max_f32 v214, v214, v215\n"
      "v_mov_b32 v215, v214\n"
      "v_cndmask_b32 v161, v161, v213, s[44:45]\n"
      "v_mbcnt_hi_u32_b32 v204, -1, v204\n"
      "v_permlane32_swap_b32 v214, v215\n"
      "v_max_f32 v214, v214, v215\n"
      "v_sub_f32 v199, v199, v214\n"
      "v_sub_f32 v152, v152, v214\n"
      "v_sub_f32 v224, v224, v214\n"
      "v_exp_f32 v199, v199\n"
      "v_sub_f32 v225, v225, v214\n"
      "v_sub_f32 v226, v226, v214\n"
      "v_sub_f32 v196, v196, v214\n"
      "v_min_u32 v204, 48, v204\n"
      "v_sub_f32 v197, v197, v214\n"
      "v_exp_f32 v225, v225\n"
      "v_exp_f32 v196, v196\n"
      "v_cvt_pk_bf16_f32 v253, v254, v255\n"
      "v_exp_f32 v224, v224\n"
      "v_sub_f32 v153, v153, v214\n"
      "v_sub_f32 v227, v227, v214\n"
      "v_exp_f32 v153, v153\n"
      "v_sub_f32 v155, v155, v214\n"
      "v_cvt_pk_bf16_f32 v62, v224, v225\n"
      "v_exp_f32 v227, v227\n"
      "v_exp_f32 v197, v197\n"
      "v_sub_f32_e64 v212, v212, v214\n"
      "v_cvt_pk_bf16_f32 v196, v196, v197\n"
      "v_exp_f32 v212, v212\n"
      "v_exp_f32 v226, v226\n"
      "v_exp_f32 v155, v155\n"
      "v_sub_f32 v154, v154, v214\n"
      "v_exp_f32 v152, v152\n"
      "v_sub_f32 v198, v198, v214\n"
      "v_exp_f32 v154, v154\n"
      "v_exp_f32 v198, v198\n"
      "v_cvt_pk_bf16_f32 v63, v226, v227\n"
      "v_cvt_pk_bf16_f32 v197, v198, v199\n"
      "v_cvt_pk_bf16_f32 v199, v154, v155\n"
      "v_cvt_pk_bf16_f32 v198, v152, v153\n"
      "v_cvt_pk_bf16_f32 v64, v212, 0\n"
      "v_cndmask_b32 v161, v161, v181, s[46:47]\n"
      "v_mfma_f32_16x16x32_bf16 v[180:183], v[66:69], v[196:199], 0\n"
      "ds_read_b128 v[232:235], v114 offset:18432\n"
      "v_lshlrev_b32 v204, 1, v204\n"
      "v_mfma_f32_16x16x32_bf16 v[180:183], v[66:69], v[62:65], v[180:183]\n"
      "ds_read_b128 v[144:147], v115 offset:18432\n"
      "v_add_u32 v204, s30, v204\n"
      "v_mfma_f32_16x16x32_bf16 v[184:187], v[228:231], v[196:199], 0\n"
      "ds_read_u16 v254, v204 offset:4704\n"
      "v_mov_b32 v255, 0\n"
      "v_mfma_f32_16x16x32_bf16 v[184:187], v[50:53], v[62:65], v[184:187]\n"
      "s_movrels_b32 s9, s80\n"
      "v_rcp_f32 v176, v176\n"
      "v_mfma_f32_16x16x32_bf16 v[164:167], v[148:151], v[196:199], 0\n"
      "s_lshr_b32 s8, s9, 8\n"
      "v_mul_f32 v169, v176, v169\n"
      "v_mul_f32 v168, v176, v168\n"
      "v_mfma_f32_16x16x32_bf16 v[164:167], v[54:57], v[62:65], v[164:167]\n"
      "s_and_b32 s9, s9, 0xff\n"
      "v_mul_f32 v218, v176, v218\n"
      "v_mul_f32 v137, v176, v137\n"
      "v_mul_f32 v170, v176, v170\n"
      "v_mul_f32 v217, v176, v217\n"
      "v_cvt_pk_bf16_f32 v168, v168, v169\n"
      "v_mul_f32 v219, v176, v219\n"
      "v_mfma_f32_16x16x32_bf16 v[204:207], v[190:193], v[196:199], 0\n"
      "s_mul_i32 s10, s8, 73\n"
      "v_mul_f32 v171, v176, v171\n"
      "v_mul_f32_e64 v216, v176, v216\n"
      "v_mfma_f32_16x16x32_bf16 v[204:207], v[58:61], v[62:65], v[204:207]\n"
      "s_add_u32 s10, s10, s9\n"
      "v_cvt_pk_bf16_f32 v169, v170, v171\n"
      "v_mul_f32 v136, v176, v136\n"
      "v_cvt_pk_bf16_f32 v170, v216, v217\n"
      "v_cvt_pk_bf16_f32 v171, v218, v219\n"
      "s_waitcnt lgkmcnt(1)\n"
      "v_mfma_f32_16x16x32_bf16 v[208:211], a[0:3], v[232:235], 0\n"
      "s_mul_i32 s28, s10, 4802\n"
      "v_cvt_pk_bf16_f32 v136, v136, v137\n"
      "v_mul_f32 v138, v176, v138\n"
      "v_mfma_f32_16x16x32_bf16 v[208:211], a[36:39], v[144:147], v[208:211]\n"
      "ds_write_b128 v124, v[168:171] offset:1024\n"
      "v_mfma_f32_16x16x32_bf16 v[216:219], a[4:7], v[232:235], 0\n"
      "s_waitcnt lgkmcnt(1)\n"
      "v_lshlrev_b32_e64 v254, 16, v254\n"
      "v_fmac_f32_e64 v161, s37, v254\n"
      "v_mfma_f32_16x16x32_bf16 v[216:219], a[40:43], v[144:147], v[216:219]\n"
      "s_add_u32 s56, s18, s28\n"
      "v_mul_f32 v139, v176, v139\n"
      "v_max_f32_dpp v254, v161, v161 row_ror:8 row_mask:0xf bank_mask:0xf\n"
      "v_mfma_f32_16x16x32_bf16 v[176:179], a[8:11], v[232:235], 0\n"
      "s_addc_u32 s57, s19, 0\n"
      "v_max_f32_dpp v254, v254, v254 row_ror:4 row_mask:0xf bank_mask:0xf\n"
      "v_cvt_pk_bf16_f32 v137, v138, v139\n"
      "v_mfma_f32_16x16x32_bf16 v[176:179], a[44:47], v[144:147], v[176:179]\n"
      "s_and_b32 s56, s56, -16\n"
      "v_max_f32_dpp v254, v254, v254 row_ror:2 row_mask:0xf bank_mask:0xf\n"
      "s_sub_u32 s58, s56, 110592\n"
      "s_subb_u32 s59, s57, 0\n"
      "v_max_f32_dpp v254, v254, v254 row_ror:1 row_mask:0xf bank_mask:0xf\n"
      "v_mfma_f32_16x16x32_bf16 v[196:199], a[16:19], v[232:235], 0\n"
      "ds_read_b128 v[168:171], v114 offset:24576\n"
      "v_max_f32_dpp v254, v254, v254 row_bcast:15 row_mask:0xa bank_mask:0xf\n"
      "v_mfma_f32_16x16x32_bf16 v[196:199], a[52:55], v[144:147], v[196:199]\n"
      "ds_write2st64_b64 v125, v[252:253], v[136:137] offset0:0 offset1:2\n"
      "v_max_f32_dpp v254, v254, v254 row_bcast:31 row_mask:0xc bank_mask:0xf\n"
      "s_nop 0\n"
      "v_readlane_b32 s31, v254, 63\n"
      "s_sub_u32 s56, s56, 73728\n"
      "s_subb_u32 s57, s57, 0\n"
      "v_subrev_f32 v161, s31, v161\n"
      "v_exp_f32 v161, v161\n"
      "s_nop 0\n"
      "v_cvt_pk_bf16_f32 v161, v161, v161\n"
      "s_mul_i32 s28, s8, 1373568\n"
      "ds_write_b16 v130, v161\n"
      "ds_read_b128 v[224:227], v131\n"
      "ds_read_b128 v[136:139], v131 offset:64\n"
      "ds_write_b32 v129, v255\n"
      "s_mul_i32 s29, s9, 2688\n"
      "s_add_u32 s28, s28, s29\n"
      "ds_read_b128 v[252:255], v115 offset:24576\n"
      "s_cmp_eq_u32 s8, 72\n"
      "v_mfma_f32_16x16x32_bf16 v[160:163], a[20:23], v[232:235], 0\n"
      "s_cselect_b32 s8, 0, 100270464\n"
      "s_cmp_eq_u32 s9, 72\n"
      "s_cselect_b32 s9, 0, 196224\n"
      "v_mfma_f32_16x16x32_bf16 v[160:163], a[56:59], v[144:147], v[160:163]\n"
      "v_mfma_f32_16x16x32_bf16 v[212:215], a[12:15], v[232:235], 0\n"
      "s_add_u32 s10, s8, s9\n"
      "v_mfma_f32_16x16x32_bf16 v[212:215], a[48:51], v[144:147], v[212:215]\n"
      "s_add_u32 s28, s28, s10\n"
      "s_add_u32 s52, s16, s28\n"
      "s_addc_u32 s53, s17, 0\n"
      "s_cmp_lg_u32 s10, s11\n"
      "s_cbranch_scc1 L_edge_92_%=\n"
      "L_eb_93_%=:\n"
      "s_waitcnt lgkmcnt(2)\n"
      "v_mfma_f32_16x16x32_bf16 v[152:155], v[224:227], v[66:69], 0\n"
      "v_mfma_f32_16x16x32_bf16 v[152:155], v[136:139], v[66:69], v[152:155]\n"
      "s_waitcnt lgkmcnt(0)\n"
      "v_mfma_f32_16x16x32_bf16 v[220:223], v[224:227], v[228:231], 0\n"
      "v_mfma_f32_16x16x32_bf16 v[220:223], v[136:139], v[50:53], v[220:223]\n"
      "v_mfma_f32_16x16x32_bf16 v[228:231], v[224:227], v[148:151], 0\n"
      "v_mfma_f32_16x16x32_bf16 v[228:231], v[136:139], v[54:57], v[228:231]\n"
      "v_mfma_f32_16x16x32_bf16 v[148:151], v[224:227], v[190:193], 0\n"
      "s_nop 1\n"
      "v_mov_b32_dpp v152, v152 quad_perm:[0,0,0,0] row_mask:0xf bank_mask:0xf\n"
      "v_mfma_f32_16x16x32_bf16 v[148:151], v[136:139], v[58:61], v[148:151]\n"
      "s_nop 2\n"
      "v_cndmask_b32 v220, v220, v228, s[42:43]\n"
      "v_rcp_f32 v152, v152\n"
      "s_nop 2\n"
      "v_cndmask_b32 v220, v220, v148, s[44:45]\n"
      "v_mul_f32_e64 v220, v152, v220\n"
      "v_cvt_pk_bf16_f32 v220, v220, v220\n"
      "ds_write_b16 v135, v220\n"
      "s_branch L_join_10_%=\n"
      "L_flg_89_%=:\n"
      "v_cmp_gt_i32 vcc, 0, v136\n"
      "s_cbranch_vccz L_fb_90_%=\n"
      "s_sleep 1\n"
      "ds_read_b128 v[136:139], v128 offset:3072\n"
      "s_waitcnt lgkmcnt(0)\n"
      "v_bitop3_b32 v136, v136, v137, s4 bitop3:0x7e\n"
      "v_bitop3_b32 v138, v138, v139, s4 bitop3:0x7e\n"
      "v_or_b32 v136, v136, v138\n"
      "v_cmp_ne_u32 s[48:49], 0, v136\n"
      "s_branch L_flg_89_%=\n"
      "L_edge_92_%=:\n"
      "s_mov_b32 s11, s10\n"
      "s_sub_u32 s28, s60, s8\n"
      "s_sub_u32 s29, s61, s9\n"
      "s_mov_b32 s60, s8\n"
      "s_mov_b32 s61, s9\n"
      "s_mov_b64 exec, s[64:65]\n"
      "v_add_u32 v116, s28, v116\n"
      "s_mov_b64 exec, s[70:71]\n"
      "v_add_u32 v116, s29, v116\n"
      "s_mov_b64 exec, s[66:67]\n"
      "v_add_u32 v117, s28, v117\n"
      "s_mov_b64 exec, s[72:73]\n"
      "v_add_u32 v117, s29, v117\n"
      "s_mov_b64 exec, s[68:69]\n"
      "v_add_u32 v118, s28, v118\n"
      "s_mov_b64 exec, s[74:75]\n"
      "v_add_u32_e64 v118, s29, v118\n"
      "s_mov_b64 exec, -1\n"
      "s_nop 4\n"
      "s_branch L_eb_93_%=\n"
      "L_gen_19_%=:\n"
      "s_waitcnt lgkmcnt(0)\n"
      "s_nop 5\n"
      "s_cmp_eq_u64 s[34:35], 0\n"
      "s_cbranch_scc1 L_ok_114_%=\n"
      "ds_read_b128 v[208:211], v114 offset:18432\n"
      "ds_read_b128 v[140:143], v115 offset:18432\n"
      "ds_read_b128 v[204:207], v114 offset:30720\n"
      "ds_read_b128 v[196:199], v114 offset:24576\n"
      "ds_read_b128 v[176:179], v115 offset:24576\n"
      "s_waitcnt lgkmcnt(3)\n"
      "v_mfma_f32_16x16x32_bf16 v[172:175], a[0:3], v[208:211], 0\n"
      "s_nop 0\n"
      "v_mfma_f32_16x16x32_bf16 v[172:175], a[36:39], v[140:143], v[172:175]\n"
      "v_mfma_f32_16x16x32_bf16 v[188:191], a[4:7], v[208:211], 0\n"
      "v_mfma_f32_16x16x32_bf16 v[188:191], a[40:43], v[140:143], v[188:191]\n"
      "v_mfma_f32_16x16x32_bf16 v[148:151], a[8:11], v[208:211], 0\n"
      "s_waitcnt lgkmcnt(0)\n"
      "v_mfma_f32_16x16x32_bf16 v[212:215], a[12:15], v[208:211], 0\n"
      "v_mfma_f32_16x16x32_bf16 v[212:215], a[48:51], v[140:143], v[212:215]\n"
      "v_mfma_f32_16x16x32_bf16 v[164:167], a[16:19], v[208:211], 0\n"
      "s_nop 0\n"
      "v_mfma_f32_16x16x32_bf16 v[164:167], a[52:55], v[140:143], v[164:167]\n"
      "v_mfma_f32_16x16x32_bf16 v[164:167], a[88:91], v[196:199], v[164:167]\n"
      "v_mfma_f32_16x16x32_bf16 v[212:215], a[84:87], v[196:199], v[212:215]\n"
      "v_mfma_f32_16x16x32_bf16 v[192:195], a[20:23], v[208:211], 0\n"
      "ds_read_b128 v[208:211], v115 offset:30720\n"
      "v_mfma_f32_16x16x32_bf16 v[192:195], a[56:59], v[140:143], v[192:195]\n"
      "v_mfma_f32_16x16x32_bf16 v[172:175], a[72:75], v[196:199], v[172:175]\n"
      "v_mfma_f32_16x16x32_bf16 v[172:175], a[108:111], v[176:179], v[172:175]\n"
      "v_mfma_f32_16x16x32_bf16 v[148:151], a[44:47], v[140:143], v[148:151]\n"
      "v_mfma_f32_16x16x32_bf16 v[148:151], a[80:83], v[196:199], v[148:151]\n"
      "v_mfma_f32_16x16x32_bf16 v[148:151], a[116:119], v[176:179], v[148:151]\n"
      "v_mfma_f32_16x16x32_bf16 v[188:191], a[76:79], v[196:199], v[188:191]\n"
      "s_nop 0\n"
      "v_mfma_f32_16x16x32_bf16 v[188:191], a[112:115], v[176:179], v[188:191]\n"
      "s_waitcnt lgkmcnt(0)\n"
      "v_mfma_f32_16x16x32_bf16 v[172:175], a[144:147], v[204:207], v[172:175]\n"
      "ds_read_b128 v[140:143], v114 offset:20480\n"
      "v_mfma_f32_16x16x32_bf16 v[172:175], a[180:183], v[208:211], v[172:175]\n"
      "v_mfma_f32_16x16x32_bf16 v[192:195], a[92:95], v[196:199], v[192:195]\n"
      "v_mfma_f32_16x16x32_bf16 v[192:195], a[128:131], v[176:179], v[192:195]\n"
      "v_mfma_f32_16x16x32_bf16 v[148:151], a[152:155], v[204:207], v[148:151]\n"
      "v_mfma_f32_16x16x32_bf16 v[148:151], a[188:191], v[208:211], v[148:151]\n"
      "v_mfma_f32_16x16x32_bf16 v[188:191], a[148:151], v[204:207], v[188:191]\n"
      "s_nop 0\n"
      "v_mfma_f32_16x16x32_bf16 v[188:191], a[184:187], v[208:211], v[188:191]\n"
      "s_nop 1\n"
      "v_fma_f32 v228, v172, v172, s38\n"
      "v_mfma_f32_16x16x32_bf16 v[164:167], a[124:127], v[176:179], v[164:167]\n"
      "v_mfma_f32_16x16x32_bf16 v[212:215], a[120:123], v[176:179], v[212:215]\n"
      "v_mfma_f32_16x16x32_bf16 v[212:215], a[156:159], v[204:207], v[212:215]\n"
      "v_fmac_f32 v228, v173, v173\n"
      "v_mfma_f32_16x16x32_bf16 v[212:215], a[192:195], v[208:211], v[212:215]\n"
      "v_fmac_f32 v228, v174, v174\n"
      "v_mfma_f32_16x16x32_bf16 v[164:167], a[160:163], v[204:207], v[164:167]\n"
      "v_fmac_f32 v228, v175, v175\n"
      "v_fmac_f32 v228, v188, v188\n"
      "v_mfma_f32_16x16x32_bf16 v[164:167], a[196:199], v[208:211], v[164:167]\n"
      "v_fmac_f32 v228, v189, v189\n"
      "v_fmac_f32 v228, v190, v190\n"
      "v_mfma_f32_16x16x32_bf16 v[192:195], a[164:167], v[204:207], v[192:195]\n"
      "v_fma_f32 v229, v212, v212, s38\n"
      "v_fmac_f32 v228, v191, v191\n"
      "v_mfma_f32_16x16x32_bf16 v[192:195], a[200:203], v[208:211], v[192:195]\n"
      "ds_read_b128 v[208:211], v115 offset:20480\n"
      "v_fmac_f32 v229, v213, v213\n"
      "v_fmac_f32 v229, v214, v214\n"
      "v_fmac_f32 v229, v215, v215\n"
      "v_fmac_f32 v229, v164, v164\n"
      "v_fmac_f32 v229, v165, v165\n"
      "v_fmac_f32 v229, v166, v166\n"
      "v_fmac_f32 v228, v148, v148\n"
      "v_fmac_f32 v229, v167, v167\n"
      "v_fmac_f32 v229, v192, v192\n"
      "v_fmac_f32 v229, v193, v193\n"
      "v_fmac_f32 v229, v194, v194\n"
      "v_fmac_f32 v228, v149, v149\n"
      "v_fmac_f32 v228, v150, v150\n"
      "v_fmac_f32 v229, v195, v195\n"
      "v_fmac_f32 v228, v151, v151\n"
      "s_nop 1\n"
      "v_permlane16_swap_b32 v228, v229\n"
      "v_add_f32 v228, v228, v229\n"
      "v_mov_b32 v229, v228\n"
      "s_nop 1\n"
      "v_permlane32_swap_b32 v228, v229\n"
      "v_add_f32 v228, v228, v229\n"
      "v_rsq_f32 v228, v228\n"
      "s_nop 0\n"
      "v_mov_b32 v229, v228\n"
      "s_waitcnt lgkmcnt(0)\n"
      "v_mfma_f32_16x16x32_bf16 v[234:237], a[0:3], v[140:143], 0\n"
      "ds_read_b128 v[196:199], v114 offset:26624\n"
      "v_permlane16_swap_b32 v228, v229\n"
      "v_mfma_f32_16x16x32_bf16 v[234:237], a[36:39], v[208:211], v[234:237]\n"
      "ds_read_b128 v[204:207], v115 offset:26624\n"
      "v_mul_f32 v214, v229, v214\n"
      "v_mul_f32 v213, v229, v213\n"
      "v_mul_f32 v215, v229, v215\n"
      "v_mul_f32 v212, v229, v212\n"
      "v_mfma_f32_16x16x32_bf16 v[252:255], a[12:15], v[140:143], 0\n"
      "ds_read_b128 v[176:179], v114 offset:32768\n"
      "v_cvt_pk_f16_f32 v212, v212, v213\n"
      "v_mfma_f32_16x16x32_bf16 v[252:255], a[48:51], v[208:211], v[252:255]\n"
      "v_mul_f32 v165, v229, v165\n"
      "v_cvt_pk_f16_f32 v213, v214, v215\n"
      "v_mul_f32 v166, v229, v166\n"
      "v_mul_f32 v167, v229, v167\n"
      "v_cvt_pk_f16_f32 v215, v166, v167\n"
      "v_mul_f32 v164, v229, v164\n"
      "v_cvt_pk_f16_f32 v214, v164, v165\n"
      "v_mfma_f32_16x16x32_bf16 v[164:167], a[16:19], v[140:143], 0\n"
      "v_mul_f32 v193, v229, v193\n"
      "v_mul_f32 v192, v229, v192\n"
      "v_mfma_f32_16x16x32_bf16 v[164:167], a[52:55], v[208:211], v[164:167]\n"
      "v_cvt_pk_f16_f32 v34, v192, v193\n"
      "v_mul_f32_e64 v195, v229, v195\n"
      "v_mfma_f32_16x16x32_bf16 v[200:203], a[20:23], v[140:143], 0\n"
      "ds_read_b128 v[216:219], v115 offset:32768\n"
      "v_mul_f32_e64 v194, v229, v194\n"
      "v_mfma_f32_16x16x32_bf16 v[200:203], a[56:59], v[208:211], v[200:203]\n"
      "v_cvt_pk_f16_f32 v35, v194, v195\n"
      "s_waitcnt lgkmcnt(2)\n"
      "v_mfma_f32_16x16x32_bf16 v[234:237], a[72:75], v[196:199], v[234:237]\n"
      "v_mul_f32 v228, s36, v228\n"
      "v_mul_f32_e64 v172, v228, v172\n"
      "v_mfma_f32_16x16x32_bf16 v[234:237], a[108:111], v[204:207], v[234:237]\n"
      "v_mul_f32 v173, v228, v173\n"
      "v_cvt_pk_f16_f32 v172, v172, v173\n"
      "v_mfma_f32_16x16x32_bf16 v[252:255], a[84:87], v[196:199], v[252:255]\n"
      "v_mul_f32 v174, v228, v174\n"
      "v_mul_f32 v175, v228, v175\n"
      "v_mfma_f32_16x16x32_bf16 v[252:255], a[120:123], v[204:207], v[252:255]\n"
      "v_cvt_pk_f16_f32 v173, v174, v175\n"
      "v_mul_f32 v188, v228, v188\n"
      "v_mfma_f32_16x16x32_bf16 v[164:167], a[88:91], v[196:199], v[164:167]\n"
      "v_mul_f32 v189, v228, v189\n"
      "v_cvt_pk_f16_f32 v174, v188, v189\n"
      "v_mfma_f32_16x16x32_bf16 v[164:167], a[124:127], v[204:207], v[164:167]\n"
      "v_mul_f32 v191, v228, v191\n"
      "v_mul_f32_e64 v190, v228, v190\n"
      "v_mfma_f32_16x16x32_bf16 v[200:203], a[92:95], v[196:199], v[200:203]\n"
      "s_waitcnt lgkmcnt(0)\n"
      "v_mul_f32 v148, v228, v148\n"
      "v_mfma_f32_16x16x32_bf16 v[200:203], a[128:131], v[204:207], v[200:203]\n"
      "v_cvt_pk_f16_f32 v175, v190, v191\n"
      "v_mul_f32 v149, v228, v149\n"
      "v_mfma_f32_16x16x32_bf16 v[188:191], a[4:7], v[140:143], 0\n"
      "v_mul_f32 v150, v228, v150\n"
      "v_mul_f32_e64 v151, v228, v151\n"
      "v_mfma_f32_16x16x32_bf16 v[188:191], a[40:43], v[208:211], v[188:191]\n"
      "v_cvt_pk_f16_f32 v32, v148, v149\n"
      "v_cvt_pk_f16_f32 v33, v150, v151\n"
      "v_mfma_f32_16x16x32_bf16 v[188:191], a[76:79], v[196:199], v[188:191]\n"
      "v_mfma_f32_16x16x32_bf16 v[188:191], a[112:115], v[204:207], v[188:191]\n"
      "v_mfma_f32_16x16x32_bf16 v[148:151], a[8:11], v[140:143], 0\n"
      "v_mfma_f32_16x16x32_bf16 v[148:151], a[44:47], v[208:211], v[148:151]\n"
      "v_mfma_f32_16x16x32_bf16 v[148:151], a[80:83], v[196:199], v[148:151]\n"
      "ds_read_b128 v[208:211], v115 offset:22528\n"
      "v_mfma_f32_16x16x32_bf16 v[148:151], a[116:119], v[204:207], v[148:151]\n"
      "v_mfma_f32_16x16x32_bf16 v[234:237], a[144:147], v[176:179], v[234:237]\n"
      "v_mfma_f32_16x16x32_bf16 v[234:237], a[180:183], v[216:219], v[234:237]\n"
      "v_mfma_f32_16x16x32_bf16 v[188:191], a[148:151], v[176:179], v[188:191]\n"
      "v_mfma_f32_16x16x32_bf16 v[188:191], a[184:187], v[216:219], v[188:191]\n"
      "v_mfma_f32_16x16x32_bf16 v[148:151], a[152:155], v[176:179], v[148:151]\n"
      "v_mfma_f32_16x16x32_bf16 v[148:151], a[188:191], v[216:219], v[148:151]\n"
      "v_mfma_f32_16x16x32_bf16 v[252:255], a[156:159], v[176:179], v[252:255]\n"
      "v_mfma_f32_16x16x32_bf16 v[252:255], a[192:195], v[216:219], v[252:255]\n"
      "s_nop 1\n"
      "v_fma_f32 v228, v234, v234, s38\n"
      "v_mfma_f32_16x16x32_bf16 v[164:167], a[160:163], v[176:179], v[164:167]\n"
      "v_fmac_f32 v228, v235, v235\n"
      "v_fmac_f32 v228, v236, v236\n"
      "v_mfma_f32_16x16x32_bf16 v[164:167], a[196:199], v[216:219], v[164:167]\n"
      "v_fmac_f32 v228, v237, v237\n"
      "v_fma_f32 v229, v252, v252, s38\n"
      "v_fmac_f32 v228, v188, v188\n"
      "v_fmac_f32 v229, v253, v253\n"
      "v_mfma_f32_16x16x32_bf16 v[200:203], a[164:167], v[176:179], v[200:203]\n"
      "ds_read_b128 v[140:143], v114 offset:22528\n"
      "v_fmac_f32 v229, v254, v254\n"
      "v_mfma_f32_16x16x32_bf16 v[200:203], a[200:203], v[216:219], v[200:203]\n"
      "v_fmac_f32 v229, v255, v255\n"
      "v_fmac_f32 v229, v164, v164\n"
      "v_fmac_f32 v229, v165, v165\n"
      "v_fmac_f32 v229, v166, v166\n"
      "v_fmac_f32 v228, v189, v189\n"
      "v_fmac_f32 v228, v190, v190\n"
      "v_fmac_f32 v228, v191, v191\n"
      "v_fmac_f32 v228, v148, v148\n"
      "v_fmac_f32 v228, v149, v149\n"
      "v_fmac_f32 v229, v167, v167\n"
      "v_fmac_f32 v229, v200, v200\n"
      "v_fmac_f32 v228, v150, v150\n"
      "v_fmac_f32 v229, v201, v201\n"
      "v_fmac_f32 v229, v202, v202\n"
      "v_fmac_f32 v228, v151, v151\n"
      "v_fmac_f32 v229, v203, v203\n"
      "s_nop 1\n"
      "v_permlane16_swap_b32 v228, v229\n"
      "v_add_f32 v228, v228, v229\n"
      "v_mov_b32 v229, v228\n"
      "s_nop 1\n"
      "v_permlane32_swap_b32 v228, v229\n"
      "v_add_f32 v228, v228, v229\n"
      "v_rsq_f32 v228, v228\n"
      "s_waitcnt lgkmcnt(0)\n"
      "v_mfma_f32_16x16x32_bf16 v[224:227], a[0:3], v[140:143], 0\n"
      "ds_read_b128 v[196:199], v114 offset:28672\n"
      "v_mov_b32_e64 v229, v228\n"
      "v_mfma_f32_16x16x32_bf16 v[224:227], a[36:39], v[208:211], v[224:227]\n"
      "ds_read_b128 v[176:179], v115 offset:28672\n"
      "v_permlane16_swap_b32 v228, v229\n"
      "v_mfma_f32_16x16x32_bf16 v[160:163], a[12:15], v[140:143], 0\n"
      "ds_read_b128 v[204:207], v114 offset:34816\n"
      "v_mul_f32 v252, v229, v252\n"
      "v_mfma_f32_16x16x32_bf16 v[160:163], a[48:51], v[208:211], v[160:163]\n"
      "v_mul_f32 v255, v229, v255\n"
      "v_mul_f32 v253, v229, v253\n"
      "v_mul_f32 v165, v229, v165\n"
      "v_mul_f32 v254, v229, v254\n"
      "v_cvt_pk_f16_f32 v252, v252, v253\n"
      "v_mul_f32 v166, v229, v166\n"
      "v_cvt_pk_f16_f32 v253, v254, v255\n"
      "v_mul_f32 v167, v229, v167\n"
      "v_mul_f32_e64 v164, v229, v164\n"
      "v_cvt_pk_f16_f32 v254, v164, v165\n"
      "v_cvt_pk_f16_f32 v255, v166, v167\n"
      "v_mfma_f32_16x16x32_bf16 v[164:167], a[16:19], v[140:143], 0\n"
      "v_mul_f32 v200, v229, v200\n"
      "v_mul_f32 v201, v229, v201\n"
      "v_mfma_f32_16x16x32_bf16 v[164:167], a[52:55], v[208:211], v[164:167]\n"
      "v_mul_f32 v203, v229, v203\n"
      "v_cvt_pk_f16_f32 v40, v200, v201\n"
      "v_mfma_f32_16x16x32_bf16 v[216:219], a[20:23], v[140:143], 0\n"
      "ds_read_b128 v[192:195], v115 offset:34816\n"
      "v_mul_f32 v202, v229, v202\n"
      "v_mfma_f32_16x16x32_bf16 v[216:219], a[56:59], v[208:211], v[216:219]\n"
      "s_waitcnt lgkmcnt(2)\n"
      "v_mul_f32 v228, s36, v228\n"
      "v_cvt_pk_f16_f32 v41, v202, v203\n"
      "v_mfma_f32_16x16x32_bf16 v[224:227], a[72:75], v[196:199], v[224:227]\n"
      "v_mul_f32 v234, v228, v234\n"
      "v_mul_f32 v235, v228, v235\n"
      "v_mfma_f32_16x16x32_bf16 v[224:227], a[108:111], v[176:179], v[224:227]\n"
      "v_mul_f32_e64 v236, v228, v236\n"
      "v_cvt_pk_f16_f32 v234, v234, v235\n"
      "v_mfma_f32_16x16x32_bf16 v[164:167], a[88:91], v[196:199], v[164:167]\n"
      "v_mul_f32 v237, v228, v237\n"
      "v_mul_f32 v189, v228, v189\n"
      "v_mfma_f32_16x16x32_bf16 v[164:167], a[124:127], v[176:179], v[164:167]\n"
      "v_cvt_pk_f16_f32 v235, v236, v237\n"
      "v_mul_f32 v188, v228, v188\n"
      "v_mfma_f32_16x16x32_bf16 v[160:163], a[84:87], v[196:199], v[160:163]\n"
      "v_cvt_pk_f16_f32 v236, v188, v189\n"
      "v_mul_f32 v190, v228, v190\n"
      "v_mfma_f32_16x16x32_bf16 v[160:163], a[120:123], v[176:179], v[160:163]\n"
      "v_mul_f32 v191, v228, v191\n"
      "v_cvt_pk_f16_f32 v237, v190, v191\n"
      "v_mfma_f32_16x16x32_bf16 v[188:191], a[4:7], v[140:143], 0\n"
      "v_mul_f32_e64 v149, v228, v149\n"
      "s_waitcnt lgkmcnt(0)\n"
      "v_mfma_f32_16x16x32_bf16 v[188:191], a[40:43], v[208:211], v[188:191]\n"
      "v_mul_f32 v151, v228, v151\n"
      "v_mul_f32 v150, v228, v150\n"
      "v_mfma_f32_16x16x32_bf16 v[188:191], a[76:79], v[196:199], v[188:191]\n"
      "v_mul_f32_e64 v148, v228, v148\n"
      "v_mfma_f32_16x16x32_bf16 v[188:191], a[112:115], v[176:179], v[188:191]\n"
      "v_cvt_pk_f16_f32 v39, v150, v151\n"
      "v_cvt_pk_f16_f32 v38, v148, v149\n"
      "v_mfma_f32_16x16x32_bf16 v[148:151], a[8:11], v[140:143], 0\n"
      "v_mfma_f32_16x16x32_bf16 v[148:151], a[44:47], v[208:211], v[148:151]\n"
      "v_mfma_f32_16x16x32_bf16 v[148:151], a[80:83], v[196:199], v[148:151]\n"
      "v_mfma_f32_16x16x32_bf16 v[148:151], a[116:119], v[176:179], v[148:151]\n"
      "v_mfma_f32_16x16x32_bf16 v[216:219], a[92:95], v[196:199], v[216:219]\n"
      "v_mfma_f32_16x16x32_bf16 v[216:219], a[128:131], v[176:179], v[216:219]\n"
      "v_mfma_f32_16x16x32_bf16 v[224:227], a[144:147], v[204:207], v[224:227]\n"
      "v_mfma_f32_16x16x32_bf16 v[224:227], a[180:183], v[192:195], v[224:227]\n"
      "v_mfma_f32_16x16x32_bf16 v[188:191], a[148:151], v[204:207], v[188:191]\n"
      "v_mfma_f32_16x16x32_bf16 v[188:191], a[184:187], v[192:195], v[188:191]\n"
      "v_mfma_f32_16x16x32_bf16 v[148:151], a[152:155], v[204:207], v[148:151]\n"
      "v_mfma_f32_16x16x32_bf16 v[148:151], a[188:191], v[192:195], v[148:151]\n"
      "v_mfma_f32_16x16x32_bf16 v[160:163], a[156:159], v[204:207], v[160:163]\n"
      "v_mfma_f32_16x16x32_bf16 v[160:163], a[192:195], v[192:195], v[160:163]\n"
      "s_nop 1\n"
      "v_fma_f32 v228, v224, v224, s38\n"
      "v_mfma_f32_16x16x32_bf16 v[164:167], a[160:163], v[204:207], v[164:167]\n"
      "v_fmac_f32 v228, v225, v225\n"
      "v_fmac_f32 v228, v226, v226\n"
      "v_mfma_f32_16x16x32_bf16 v[164:167], a[196:199], v[192:195], v[164:167]\n"
      "s_nop 0\n"
      "v_fma_f32 v229, v160, v160, s38\n"
      "v_fmac_f32_e64 v229, v161, v161\n"
      "v_mfma_f32_16x16x32_bf16 v[216:219], a[164:167], v[204:207], v[216:219]\n"
      "v_fmac_f32 v229, v162, v162\n"
      "v_fmac_f32 v228, v227, v227\n"
      "v_mfma_f32_16x16x32_bf16 v[216:219], a[200:203], v[192:195], v[216:219]\n"
      "v_fmac_f32 v228, v188, v188\n"
      "v_fmac_f32 v229, v163, v163\n"
      "v_mfma_f32_16x16x32_f16 v[140:143], v[230:233], v[172:175], v[70:73]\n"
      "v_fmac_f32 v228, v189, v189\n"
      "v_fmac_f32 v228, v190, v190\n"
      "v_mfma_f32_16x16x32_f16 v[140:143], v[184:187], v[212:215], v[140:143]\n"
      "v_fmac_f32 v229, v164, v164\n"
      "v_fmac_f32 v229, v165, v165\n"
      "v_mfma_f32_16x16x32_f16 v[140:143], v[168:171], v[32:35], v[140:143]\n"
      "v_fmac_f32 v228, v191, v191\n"
      "v_fmac_f32 v229, v166, v166\n"
      "v_fmac_f32 v228, v148, v148\n"
      "v_fmac_f32 v228, v149, v149\n"
      "v_fmac_f32 v229, v167, v167\n"
      "v_fmac_f32 v229, v216, v216\n"
      "v_fmac_f32 v229, v217, v217\n"
      "v_fmac_f32 v229, v218, v218\n"
      "v_fmac_f32 v228, v150, v150\n"
      "v_fmac_f32 v228, v151, v151\n"
      "v_fmac_f32 v229, v219, v219\n"
      "v_mov_b32 v145, v141\n"
      "v_mov_b32 v144, v140\n"
      "v_permlane16_swap_b32 v228, v229\n"
      "v_add_f32 v228, v228, v229\n"
      "v_mov_b32 v229, v228\n"
      "s_nop 1\n"
      "v_permlane32_swap_b32 v228, v229\n"
      "v_add_f32 v228, v228, v229\n"
      "v_rsq_f32 v228, v228\n"
      "s_nop 0\n"
      "v_mov_b32 v229, v228\n"
      "s_nop 1\n"
      "v_permlane16_swap_b32 v228, v229\n"
      "v_mul_f32 v160, v229, v160\n"
      "v_mul_f32 v163, v229, v163\n"
      "v_mul_f32 v161, v229, v161\n"
      "v_mul_f32 v165, v229, v165\n"
      "v_mul_f32 v162, v229, v162\n"
      "v_mul_f32 v217, v229, v217\n"
      "v_mul_f32 v216, v229, v216\n"
      "v_mul_f32 v164, v229, v164\n"
      "v_mul_f32_e64 v228, s36, v228\n"
      "v_cvt_pk_f16_f32 v160, v160, v161\n"
      "v_cvt_pk_f16_f32 v161, v162, v163\n"
      "v_cvt_pk_f16_f32 v46, v216, v217\n"
      "v_mul_f32 v188, v228, v188\n"
      "v_mul_f32 v166, v229, v166\n"
      "v_mul_f32 v218, v229, v218\n"
      "v_mul_f32 v224, v228, v224\n"
      "v_mul_f32 v191, v228, v191\n"
      "v_mul_f32 v219, v229, v219\n"
      "v_mul_f32 v167, v229, v167\n"
      "v_cvt_pk_f16_f32 v47, v218, v219\n"
      "v_cvt_pk_f16_f32 v163, v166, v167\n"
      "v_mul_f32 v148, v228, v148\n"
      "v_mul_f32_e64 v226, v228, v226\n"
      "v_cvt_pk_f16_f32 v162, v164, v165\n"
      "v_mul_f32 v190, v228, v190\n"
      "v_mul_f32 v189, v228, v189\n"
      "v_mul_f32 v227, v228, v227\n"
      "v_mfma_f32_16x16x32_f16 v[164:167], v[160:163], v[172:175], v[86:89]\n"
      "v_mul_f32 v225, v228, v225\n"
      "v_cvt_pk_f16_f32 v224, v224, v225\n"
      "v_mfma_f32_16x16x32_f16 v[164:167], v[46:49], v[32:35], v[164:167]\n"
      "v_cvt_pk_f16_f32 v225, v226, v227\n"
      "v_cvt_pk_f16_f32 v226, v188, v189\n"
      "v_mul_f32 v151, v228, v151\n"
      "v_cvt_pk_f16_f32 v227, v190, v191\n"
      "v_mfma_f32_16x16x32_f16 v[188:191], v[212:215], v[172:175], v[78:81]\n"
      "v_mul_f32 v149, v228, v149\n"
      "v_cvt_pk_f16_f32 v44, v148, v149\n"
      "v_mfma_f32_16x16x32_f16 v[188:191], v[34:37], v[32:35], v[188:191]\n"
      "v_mul_f32_e64 v150, v228, v150\n"
      "v_cvt_pk_f16_f32 v45, v150, v151\n"
      "v_mfma_f32_16x16x32_f16 v[148:151], v[252:255], v[172:175], v[82:85]\n"
      "v_mfma_f32_16x16x32_f16 v[148:151], v[40:43], v[32:35], v[148:151]\n"
      "s_nop 7\n"
      "s_or_b64 vcc, s[48:49], s[50:51]\n"
      "s_cbranch_scc0 L_fast_22_%=\n"
      "L_ok_114_%=:\n"
      "s_cmp_eq_u64 s[48:49], 0\n"
      "s_cbranch_scc1 L_go_115_%=\n"
      "s_mov_b32 m0, s27\n"
      "s_nop 0\n"
      "s_movrels_b32 s9, s80\n"
      "s_lshr_b32 s8, s9, 8\n"
      "s_and_b32 s9, s9, 0xff\n"
      "s_mul_i32 s10, s8, 73\n"
      "s_add_u32 s10, s10, s9\n"
      "s_mul_i32 s30, s10, 4802\n"
      "s_add_u32 s30, s30, s18\n"
      "s_and_b32 s29, s30, 15\n"
      "s_add_u32 s30, s29, 115456\n"
      "s_lshl_b64 s[34:35], s[48:49], 4\n"
      "s_lshr_b64 s[34:35], s[34:35], 5\n"
      "s_cbranch_scc1 L_gen_20_%=\n"
      "v_mbcnt_lo_u32_b32 v228, -1, 0\n"
      "v_mbcnt_hi_u32_b32 v228, -1, v228\n"
      "v_lshlrev_b32 v229, 1, v228\n"
      "v_add_u32 v146, 4802, v229\n"
      "v_cmp_gt_u32 vcc, s29, v229\n"
      "s_nop 1\n"
      "v_cndmask_b32 v229, v146, v229, vcc\n"
      "v_mov_b32 v146, 0\n"
      "v_add_u32 v229, 115456, v229\n"
      "s_mov_b64 exec, 0x7f\n"
      "ds_write_b16 v229, v146\n"
      "s_mov_b64 exec, -1\n"
      "v_lshlrev_b32 v229, 4, v228\n"
      "v_add_u32 v229, 115456, v229\n"
      "ds_read_b128 v[140:143], v229\n"
      "ds_read_b128 v[208:211], v229 offset:1024\n"
      "v_min_u32 v228, 116160, v229\n"
      "ds_read_b128 v[196:199], v229 offset:2048\n"
      "ds_read_b128 v[172:175], v228 offset:4096\n"
      "ds_read_b128 v[176:179], v229 offset:3072\n"
      "s_waitcnt lgkmcnt(4)\n"
      "v_or3_b32 v140, v140, v141, v142\n"
      "s_waitcnt lgkmcnt(3)\n"
      "v_or3_b32 v140, v140, v143, v208\n"
      "v_or3_b32 v140, v140, v209, v210\n"
      "s_waitcnt lgkmcnt(2)\n"
      "v_or3_b32 v140, v140, v211, v196\n"
      "v_or3_b32 v140, v140, v197, v198\n"
      "s_waitcnt lgkmcnt(0)\n"
      "v_or3_b32 v140, v140, v199, v176\n"
      "v_or3_b32 v140, v140, v177, v178\n"
      "v_or3_b32 v140, v140, v179, v172\n"
      "v_or3_b32 v140, v140, v173, v174\n"
      "v_bitop3_b32 v140, v140, v175, s40 bitop3:0xa8\n"
      "v_cmp_ne_u32 s[48:49], 0, v140\n"
      "s_cmp_lg_u64 s[48:49], 0\n"
      "s_cbranch_scc1 L_gen_20_%=\n"
      "s_cmp_eq_u64 s[50:51], 0\n"
      "s_cbranch_scc1 L_fast_22_%=\n"
      "L_go_115_%=:\n"
      "v_mfma_f32_16x16x32_f16 v[140:143], v[212:215], v[234:237], v[90:93]\n"
      "ds_read_b128 v[208:211], v128 offset:2048\n"
      "v_max3_f32 v229, v188, v189, v190\n"
      "v_mfma_f32_16x16x32_f16 v[140:143], v[34:37], v[38:41], v[140:143]\n"
      "v_max3_f32 v228, v191, v148, v149\n"
      "v_max3_f32 v228, v228, v164, v165\n"
      "v_mfma_f32_16x16x32_f16 v[196:199], v[252:255], v[234:237], v[94:97]\n"
      "v_max3_f32 v229, v229, v150, v151\n"
      "v_max3_f32 v229, v229, v166, v167\n"
      "v_mfma_f32_16x16x32_f16 v[196:199], v[40:43], v[38:41], v[196:199]\n"
      "v_max3_f32 v229, v229, v228, v144\n"
      "v_mov_b32_e64 v228, v229\n"
      "v_mfma_f32_16x16x32_f16 v[176:179], v[230:233], v[234:237], v[72:75]\n"
      "s_nop 0\n"
      "v_permlane16_swap_b32 v229, v228\n"
      "v_mfma_f32_16x16x32_f16 v[176:179], v[184:187], v[252:255], v[176:179]\n"
      "v_max_f32 v229, v229, v228\n"
      "v_mov_b32 v228, v229\n"
      "v_mfma_f32_16x16x32_f16 v[176:179], v[168:171], v[38:41], v[176:179]\n"
      "s_nop 0\n"
      "v_permlane32_swap_b32 v229, v228\n"
      "v_max_f32 v229, v229, v228\n"
      "v_sub_f32 v188, v188, v229\n"
      "v_exp_f32 v188, v188\n"
      "v_sub_f32 v190, v190, v229\n"
      "v_sub_f32 v149, v149, v229\n"
      "v_mfma_f32_16x16x32_f16 v[172:175], v[160:163], v[234:237], v[98:101]\n"
      "s_waitcnt lgkmcnt(0)\n"
      "v_exp_f32 v190, v190\n"
      "v_mfma_f32_16x16x32_f16 v[172:175], v[46:49], v[38:41], v[172:175]\n"
      "v_sub_f32 v148, v148, v229\n"
      "v_sub_f32 v167, v167, v229\n"
      "v_sub_f32 v150, v150, v229\n"
      "v_sub_f32 v191, v191, v229\n"
      "v_exp_f32 v149, v149\n"
      "v_sub_f32 v165, v165, v229\n"
      "v_sub_f32 v144, v144, v229\n"
      "v_sub_f32 v189, v189, v229\n"
      "v_exp_f32 v189, v189\n"
      "v_exp_f32 v191, v191\n"
      "v_cvt_pk_bf16_f32 v188, v188, v189\n"
      "v_exp_f32 v165, v165\n"
      "v_exp_f32_e64 v148, v148\n"
      "v_bitop3_b32 v210, v210, v211, s4 bitop3:0x7e\n"
      "v_cvt_pk_bf16_f32 v189, v190, v191\n"
      "v_sub_f32 v164, v164, v229\n"
      "v_exp_f32 v167, v167\n"
      "v_exp_f32 v150, v150\n"
      "v_sub_f32 v151, v151, v229\n"
      "v_sub_f32 v166, v166, v229\n"
      "v_exp_f32 v166, v166\n"
      "v_exp_f32_e64 v151, v151\n"
      "v_exp_f32_e64 v144, v144\n"
      "v_cvt_pk_bf16_f32 v63, v166, v167\n"
      "v_cvt_pk_bf16_f32 v191, v150, v151\n"
      "v_exp_f32_e64 v164, v164\n"
      "v_cvt_pk_bf16_f32 v190, v148, v149\n"
      "v_bitop3_b32 v208, v208, v209, s4 bitop3:0x7e\n"
      "v_cvt_pk_bf16_f32 v64, v144, 0\n"
      "v_cvt_pk_bf16_f32 v62, v164, v165\n"
      "v_mfma_f32_16x16x32_bf16 v[148:151], v[66:69], v[188:191], 0\n"
      "v_or_b32 v208, v208, v210\n"
      "v_cmp_ne_u32 vcc, 0, v208\n"
      "v_mfma_f32_16x16x32_bf16 v[148:151], v[66:69], v[62:65], v[148:151]\n"
      "s_mov_b64 s[48:49], vcc\n"
      "v_max3_f32 v228, v140, v141, v142\n"
      "v_max3_f32 v229, v143, v196, v197\n"
      "v_mfma_f32_16x16x32_bf16 v[236:239], v[244:247], v[188:191], 0\n"
      "s_cbranch_vccnz L_flg_116_%=\n"
      "L_fb_117_%=:\n"
      "v_mfma_f32_16x16x32_bf16 v[236:239], v[50:53], v[62:65], v[236:239]\n"
      "v_max3_f32 v229, v229, v172, v173\n"
      "v_max3_f32 v228, v228, v198, v199\n"
      "v_mfma_f32_16x16x32_bf16 v[164:167], v[156:159], v[188:191], 0\n"
      "v_max3_f32 v228, v228, v174, v175\n"
      "v_max3_f32 v228, v228, v229, v176\n"
      "v_mfma_f32_16x16x32_bf16 v[164:167], v[54:57], v[62:65], v[164:167]\n"
      "v_mov_b32 v229, v228\n"
      "v_rcp_f32 v148, v148\n"
      "v_mfma_f32_16x16x32_bf16 v[208:211], v[248:251], v[188:191], 0\n"
      "v_permlane16_swap_b32 v228, v229\n"
      "v_mfma_f32_16x16x32_bf16 v[208:211], v[58:61], v[62:65], v[208:211]\n"
      "v_max_f32 v228, v228, v229\n"
      "v_mov_b32 v229, v228\n"
      "v_mul_f32 v237, v148, v237\n"
      "v_mul_f32 v236, v148, v236\n"
      "v_permlane32_swap_b32 v228, v229\n"
      "v_max_f32 v228, v228, v229\n"
      "v_sub_f32 v141, v141, v228\n"
      "v_sub_f32 v140, v140, v228\n"
      "v_mfma_f32_16x16x32_f16 v[188:191], v[212:215], v[224:227], v[102:105]\n"
      "v_sub_f32 v174, v174, v228\n"
      "v_sub_f32 v175, v175, v228\n"
      "v_mfma_f32_16x16x32_f16 v[188:191], v[34:37], v[44:47], v[188:191]\n"
      "v_sub_f32 v197, v197, v228\n"
      "v_exp_f32 v140, v140\n"
      "v_sub_f32 v142, v142, v228\n"
      "v_exp_f32 v141, v141\n"
      "v_exp_f32 v174, v174\n"
      "v_sub_f32 v176, v176, v228\n"
      "v_sub_f32 v196, v196, v228\n"
      "v_sub_f32 v173, v173, v228\n"
      "v_mul_f32 v211, v148, v211\n"
      "v_exp_f32 v142, v142\n"
      "v_exp_f32 v196, v196\n"
      "v_exp_f32_e64 v173, v173\n"
      "v_mfma_f32_16x16x32_f16 v[212:215], v[252:255], v[224:227], v[106:109]\n"
      "ds_read_b128 v[252:255], v120\n"
      "v_cvt_pk_bf16_f32 v140, v140, v141\n"
      "v_mfma_f32_16x16x32_f16 v[212:215], v[40:43], v[44:47], v[212:215]\n"
      "v_sub_f32 v199, v199, v228\n"
      "v_sub_f32 v143, v143, v228\n"
      "v_exp_f32 v143, v143\n"
      "v_sub_f32 v198, v198, v228\n"
      "v_mfma_f32_16x16x32_f16 v[204:207], v[230:233], v[224:227], v[74:77]\n"
      "v_cvt_pk_bf16_f32 v141, v142, v143\n"
      "v_cvt_pk_bf16_f32 v236, v236, v237\n"
      "v_mfma_f32_16x16x32_f16 v[204:207], v[184:187], v[160:163], v[204:207]\n"
      "v_exp_f32 v197, v197\n"
      "v_mfma_f32_16x16x32_f16 v[204:207], v[168:171], v[44:47], v[204:207]\n"
      "v_exp_f32 v175, v175\n"
      "v_sub_f32 v172, v172, v228\n"
      "v_cvt_pk_bf16_f32 v142, v196, v197\n"
      "v_mfma_f32_16x16x32_f16 v[194:197], v[160:163], v[224:227], v[110:113]\n"
      "v_exp_f32 v172, v172\n"
      "v_mfma_f32_16x16x32_f16 v[194:197], v[46:49], v[44:47], v[194:197]\n"
      "v_mul_f32 v165, v148, v165\n"
      "v_exp_f32 v199, v199\n"
      "v_cvt_pk_bf16_f32 v62, v172, v173\n"
      "v_mfma_f32_16x16x32_f16 v[224:227], v[184:187], v[230:233], v[76:79]\n"
      "s_waitcnt lgkmcnt(0)\n"
      "v_mul_f32 v238, v148, v238\n"
      "v_exp_f32 v176, v176\n"
      "v_exp_f32 v198, v198\n"
      "v_cvt_pk_bf16_f32 v64, v176, 0\n"
      "v_cvt_pk_bf16_f32 v143, v198, v199\n"
      "v_mfma_f32_16x16x16_f16 v[224:227], v[170:171], v[168:169], v[224:227]\n"
      "global_store_dwordx4 v117, v[252:255], s[54:55] sc1\n"
      "v_cvt_pk_bf16_f32 v63, v174, v175\n"
      "v_mul_f32_e64 v239, v148, v239\n"
      "v_mfma_f32_16x16x32_bf16 v[252:255], v[66:69], v[140:143], 0\n"
      "v_cvt_pk_bf16_f32 v237, v238, v239\n"
      "v_mul_f32 v164, v148, v164\n"
      "v_mfma_f32_16x16x32_bf16 v[252:255], v[66:69], v[62:65], v[252:255]\n"
      "v_mul_f32 v210, v148, v210\n"
      "v_cvt_pk_bf16_f32 v238, v164, v165\n"
      "v_mfma_f32_16x16x32_bf16 v[232:235], v[244:247], v[140:143], 0\n"
      "v_mul_f32 v167, v148, v167\n"
      "v_mul_f32 v166, v148, v166\n"
      "v_mfma_f32_16x16x32_bf16 v[232:235], v[50:53], v[62:65], v[232:235]\n"
      "v_cvt_pk_bf16_f32 v239, v166, v167\n"
      "v_mul_f32 v208, v148, v208\n"
      "v_mfma_f32_16x16x32_bf16 v[164:167], v[156:159], v[140:143], 0\n"
      "v_mul_f32 v209, v148, v209\n"
      "v_cvt_pk_bf16_f32 v208, v208, v209\n"
      "v_mfma_f32_16x16x32_bf16 v[164:167], v[54:57], v[62:65], v[164:167]\n"
      "ds_write_b128 v124, v[236:239] offset:18432\n"
      "v_cvt_pk_bf16_f32 v209, v210, v211\n"
      "v_max3_f32 v211, v191, v212, v213\n"
      "v_max3_f32 v211, v211, v194, v195\n"
      "v_max3_f32 v210, v188, v189, v190\n"
      "v_max3_f32 v210, v210, v214, v215\n"
      "v_max3_f32 v210, v210, v196, v197\n"
      "v_mfma_f32_16x16x32_bf16 v[236:239], v[248:251], v[140:143], 0\n"
      "v_max3_f32 v210, v210, v211, v204\n"
      "v_mov_b32 v211, v210\n"
      "v_mfma_f32_16x16x32_bf16 v[236:239], v[58:61], v[62:65], v[236:239]\n"
      "s_nop 0\n"
      "v_permlane16_swap_b32 v210, v211\n"
      "v_max_f32 v210, v210, v211\n"
      "v_rcp_f32 v252, v252\n"
      "v_mov_b32 v211, v210\n"
      "v_mul_f32 v235, v252, v235\n"
      "v_mul_f32 v233, v252, v233\n"
      "v_permlane32_swap_b32 v210, v211\n"
      "v_max_f32 v210, v210, v211\n"
      "v_sub_f32 v190, v190, v210\n"
      "v_sub_f32 v194, v194, v210\n"
      "v_sub_f32 v212, v212, v210\n"
      "v_sub_f32 v189, v189, v210\n"
      "v_exp_f32 v189, v189\n"
      "v_sub_f32 v204, v204, v210\n"
      "v_sub_f32 v188, v188, v210\n"
      "v_sub_f32 v191, v191, v210\n"
      "v_exp_f32 v212, v212\n"
      "v_sub_f32 v197, v197, v210\n"
      "v_exp_f32 v188, v188\n"
      "v_exp_f32 v190, v190\n"
      "v_exp_f32 v191, v191\n"
      "v_cvt_pk_bf16_f32 v188, v188, v189\n"
      "v_sub_f32 v196, v196, v210\n"
      "v_mul_f32 v232, v252, v232\n"
      "v_mul_f32 v236, v252, v236\n"
      "v_sub_f32 v213, v213, v210\n"
      "v_exp_f32 v204, v204\n"
      "v_sub_f32 v215, v215, v210\n"
      "v_exp_f32 v196, v196\n"
      "v_exp_f32 v197, v197\n"
      "v_sub_f32 v214, v214, v210\n"
      "v_sub_f32 v195, v195, v210\n"
      "v_exp_f32 v213, v213\n"
      "v_cvt_pk_bf16_f32 v189, v190, v191\n"
      "v_exp_f32 v214, v214\n"
      "v_exp_f32 v194, v194\n"
      "v_exp_f32 v195, v195\n"
      "v_mul_f32 v165, v252, v165\n"
      "v_cvt_pk_bf16_f32 v190, v212, v213\n"
      "v_exp_f32 v215, v215\n"
      "v_cvt_pk_bf16_f32 v62, v194, v195\n"
      "v_mul_f32 v234, v252, v234\n"
      "v_cvt_pk_bf16_f32 v191, v214, v215\n"
      "v_cvt_pk_bf16_f32 v63, v196, v197\n"
      "v_cvt_pk_bf16_f32 v64, v204, 0\n"
      "v_mfma_f32_16x16x32_bf16 v[212:215], v[66:69], v[188:191], 0\n"
      "ds_read_b128 v[140:143], v121\n"
      "v_cvt_pk_bf16_f32 v232, v232, v233\n"
      "v_mfma_f32_16x16x32_bf16 v[212:215], v[66:69], v[62:65], v[212:215]\n"
      "ds_read_b128 v[196:199], v123 offset:4096\n"
      "v_mul_f32 v164, v252, v164\n"
      "v_mul_f32 v167, v252, v167\n"
      "v_mfma_f32_16x16x32_bf16 v[168:171], v[244:247], v[188:191], 0\n"
      "v_cvt_pk_bf16_f32 v233, v234, v235\n"
      "v_cvt_pk_bf16_f32 v234, v164, v165\n"
      "v_mul_f32 v237, v252, v237\n"
      "v_mfma_f32_16x16x32_bf16 v[228:231], v[156:159], v[188:191], 0\n"
      "v_mul_f32 v166, v252, v166\n"
      "v_cvt_pk_bf16_f32 v235, v166, v167\n"
      "v_mfma_f32_16x16x32_bf16 v[228:231], v[54:57], v[62:65], v[228:231]\n"
      "ds_write_b128 v124, v[232:235] offset:19456\n"
      "v_cndmask_b32 v145, v145, v177, s[42:43]\n"
      "v_cndmask_b32 v145, v145, v205, s[44:45]\n"
      "v_mul_f32 v239, v252, v239\n"
      "v_cvt_pk_bf16_f32 v236, v236, v237\n"
      "v_cndmask_b32 v145, v145, v225, s[46:47]\n"
      "v_mul_f32 v238, v252, v238\n"
      "v_cvt_pk_bf16_f32 v237, v238, v239\n"
      "v_max_f32_dpp v238, v145, v145 row_ror:8 row_mask:0xf bank_mask:0xf\n"
      "s_nop 1\n"
      "v_max_f32_dpp v238, v238, v238 row_ror:4 row_mask:0xf bank_mask:0xf\n"
      "s_waitcnt lgkmcnt(2)\n"
      "ds_read_b128 v[232:235], v123 offset:1024\n"
      "v_max_f32_dpp v238, v238, v238 row_ror:2 row_mask:0xf bank_mask:0xf\n"
      "s_nop 1\n"
      "v_max_f32_dpp v238, v238, v238 row_ror:1 row_mask:0xf bank_mask:0xf\n"
      "ds_read_b128 v[252:255], v123 offset:10240\n"
      "ds_read_b128 v[224:227], v123 offset:7168\n"
      "v_max_f32_dpp v238, v238, v238 row_bcast:15 row_mask:0xa bank_mask:0xf\n"
      "v_mfma_f32_16x16x32_bf16 v[168:171], v[50:53], v[62:65], v[168:171]\n"
      "s_nop 0\n"
      "v_max_f32_dpp v238, v238, v238 row_bcast:31 row_mask:0xc bank_mask:0xf\n"
      "s_nop 0\n"
      "v_readlane_b32 s31, v238, 63\n"
      "s_mov_b32 exec_hi, 0\n"
      "global_store_dwordx4 v118, v[140:143], s[54:55] sc1\n"
      "s_mov_b32 exec_hi, -1\n"
      "ds_write2st64_b64 v125, v[208:209], v[236:237] offset0:36 offset1:38\n"
      "v_subrev_f32 v145, s31, v145\n"
      "v_exp_f32_e64 v145, v145\n"
      "s_add_u32 s28, s27, 2\n"
      "s_waitcnt lgkmcnt(3)\n"
      "v_cvt_pk_bf16_f32 v145, v145, v145\n"
      "v_mfma_f32_16x16x32_bf16 v[236:239], a[216:219], v[232:235], 0\n"
      "ds_write_b16 v130, v145\n"
      "v_mfma_f32_16x16x32_bf16 v[144:147], v[248:251], v[188:191], 0\n"
      "ds_read_b128 v[188:191], v123 offset:13312\n"
      "s_min_u32 m0, s28, s3\n"
      "v_mfma_f32_16x16x32_bf16 v[144:147], v[58:61], v[62:65], v[144:147]\n"
      "s_movrels_b32 s9, s80\n"
      "s_lshr_b32 s8, s9, 8\n"
      "s_and_b32 s9, s9, 0xff\n"
      "v_mfma_f32_16x16x32_bf16 v[140:143], a[220:223], v[232:235], 0\n"
      "s_mul_i32 s10, s8, 73\n"
      "s_add_u32 s10, s10, s9\n"
      "s_mul_i32 s28, s10, 4802\n"
      "s_add_u32 s56, s18, s28\n"
      "v_mfma_f32_16x16x32_bf16 v[236:239], a[228:231], v[196:199], v[236:239]\n"
      "s_addc_u32 s57, s19, 0\n"
      "s_and_b32 s56, s56, -16\n"
      "s_sub_u32 s58, s56, 110592\n"
      "s_subb_u32 s59, s57, 0\n"
      "s_sub_u32 s56, s56, 73728\n"
      "v_mfma_f32_16x16x32_bf16 v[140:143], a[232:235], v[196:199], v[140:143]\n"
      "s_subb_u32 s57, s57, 0\n"
      "s_mul_i32 s28, s8, 1373568\n"
      "s_mul_i32 s29, s9, 2688\n"
      "v_mfma_f32_16x16x32_bf16 v[148:151], a[224:227], v[232:235], 0\n"
      "ds_read_b128 v[232:235], v123 offset:16384\n"
      "v_mfma_f32_16x16x32_bf16 v[148:151], a[236:239], v[196:199], v[148:151]\n"
      "s_waitcnt lgkmcnt(3)\n"
      "ds_read_b128 v[164:167], v123 offset:2048\n"
      "ds_read_b128 v[196:199], v123 offset:5120\n"
      "v_mfma_f32_16x16x32_bf16 v[236:239], a[240:243], v[224:227], v[236:239]\n"
      "s_add_u32 s28, s28, s29\n"
      "v_mfma_f32_16x16x32_bf16 v[236:239], a[252:255], v[252:255], v[236:239]\n"
      "s_cmp_eq_u32 s8, 72\n"
      "s_cselect_b32 s8, 0, 100270464\n"
      "s_cmp_eq_u32 s9, 72\n"
      "v_mfma_f32_16x16x32_bf16 v[140:143], a[244:247], v[224:227], v[140:143]\n"
      "s_cselect_b32 s9, 0, 196224\n"
      "s_add_u32 s10, s8, s9\n"
      "s_add_u32 s28, s28, s10\n"
      "v_mfma_f32_16x16x32_bf16 v[140:143], v[0:3], v[252:255], v[140:143]\n"
      "s_add_u32 s52, s16, s28\n"
      "s_addc_u32 s53, s17, 0\n"
      "s_add_u32 s27, s27, 1\n"
      "v_mfma_f32_16x16x32_bf16 v[148:151], a[248:251], v[224:227], v[148:151]\n"
      "v_mfma_f32_16x16x32_bf16 v[148:151], v[4:7], v[252:255], v[148:151]\n"
      "ds_read_b128 v[204:207], v131\n"
      "s_waitcnt lgkmcnt(3)\n"
      "v_mfma_f32_16x16x32_bf16 v[236:239], v[8:11], v[188:191], v[236:239]\n"
      "v_mfma_f32_16x16x32_bf16 v[236:239], v[20:23], v[232:235], v[236:239]\n"
      "v_mfma_f32_16x16x32_bf16 v[140:143], v[12:15], v[188:191], v[140:143]\n"
      "s_cmp_ge_u32 s27, s26\n"
      "s_waitcnt lgkmcnt(1)\n"
      "s_cselect_b32 s28, 1, 0\n"
      "v_mfma_f32_16x16x32_bf16 v[140:143], v[24:27], v[232:235], v[140:143]\n"
      "ds_read_b128 v[208:211], v123 offset:8192\n"
      "v_mfma_f32_16x16x32_bf16 v[148:151], v[16:19], v[188:191], v[148:151]\n"
      "s_xor_b32 s29, s10, s11\n"
      "ds_read_b128 v[176:179], v131 offset:64\n"
      "v_mfma_f32_16x16x32_bf16 v[148:151], v[28:31], v[232:235], v[148:151]\n"
      "s_or_b32 s28, s28, s29\n"
      "ds_read_b128 v[172:175], v123 offset:11264\n"
      "v_mfma_f32_16x16x32_bf16 v[232:235], a[216:219], v[164:167], 0\n"
      "v_mfma_f32_16x16x32_bf16 v[232:235], a[228:231], v[196:199], v[232:235]\n"
      "v_mfma_f32_16x16x32_bf16 v[224:227], a[220:223], v[164:167], 0\n"
      "v_fma_f32 v181, v236, v236, s39\n"
      "v_fmac_f32 v181, v237, v237\n"
      "v_mfma_f32_16x16x32_bf16 v[224:227], a[232:235], v[196:199], v[224:227]\n"
      "ds_read_b128 v[184:187], v123 offset:17408\n"
      "v_mfma_f32_16x16x32_bf16 v[188:191], a[224:227], v[164:167], 0\n"
      "v_fmac_f32 v181, v238, v238\n"
      "v_mfma_f32_16x16x32_bf16 v[188:191], a[236:239], v[196:199], v[188:191]\n"
      "ds_read_b128 v[196:199], v123 offset:14336\n"
      "v_fmac_f32 v181, v239, v239\n"
      "v_fmac_f32 v181, v140, v140\n"
      "v_fmac_f32 v181, v141, v141\n"
      "v_fmac_f32 v181, v142, v142\n"
      "v_fmac_f32 v181, v143, v143\n"
      "v_fmac_f32 v181, v148, v148\n"
      "v_fmac_f32 v181, v149, v149\n"
      "v_fmac_f32 v181, v150, v150\n"
      "s_waitcnt lgkmcnt(2)\n"
      "v_mfma_f32_16x16x32_bf16 v[188:191], a[248:251], v[208:211], v[188:191]\n"
      "v_fmac_f32 v181, v151, v151\n"
      "v_mfma_f32_16x16x32_bf16 v[232:235], a[240:243], v[208:211], v[232:235]\n"
      "s_nop 0\n"
      "v_permlane16_swap_b32 v180, v181\n"
      "v_mfma_f32_16x16x32_bf16 v[232:235], a[252:255], v[172:175], v[232:235]\n"
      "v_mfma_f32_16x16x32_bf16 v[224:227], a[244:247], v[208:211], v[224:227]\n"
      "ds_read_b128 v[208:211], v115\n"
      "v_mfma_f32_16x16x32_bf16 v[224:227], v[0:3], v[172:175], v[224:227]\n"
      "ds_read_b128 v[252:255], v114\n"
      "v_mfma_f32_16x16x32_bf16 v[188:191], v[4:7], v[172:175], v[188:191]\n"
      "s_waitcnt lgkmcnt(2)\n"
      "v_mfma_f32_16x16x32_bf16 v[232:235], v[8:11], v[196:199], v[232:235]\n"
      "v_mfma_f32_16x16x32_bf16 v[232:235], v[20:23], v[184:187], v[232:235]\n"
      "v_mfma_f32_16x16x32_bf16 v[224:227], v[12:15], v[196:199], v[224:227]\n"
      "s_nop 0\n"
      "v_mfma_f32_16x16x32_bf16 v[224:227], v[24:27], v[184:187], v[224:227]\n"
      "v_add_f32_e64 v180, v180, v181\n"
      "v_mfma_f32_16x16x32_bf16 v[188:191], v[16:19], v[196:199], v[188:191]\n"
      "ds_read_b128 v[164:167], v114 offset:6144\n"
      "v_mfma_f32_16x16x32_bf16 v[188:191], v[28:31], v[184:187], v[188:191]\n"
      "s_nop 1\n"
      "v_fma_f32 v181, v232, v232, s39\n"
      "v_mfma_f32_16x16x32_bf16 v[160:163], v[204:207], v[66:69], 0\n"
      "v_fmac_f32 v181, v233, v233\n"
      "v_mfma_f32_16x16x32_bf16 v[160:163], v[176:179], v[66:69], v[160:163]\n"
      "ds_read_b128 v[196:199], v115 offset:6144\n"
      "v_fmac_f32 v181, v234, v234\n"
      "v_mfma_f32_16x16x32_bf16 v[192:195], v[204:207], v[244:247], 0\n"
      "v_fmac_f32 v181, v235, v235\n"
      "s_waitcnt lgkmcnt(2)\n"
      "v_mfma_f32_16x16x32_bf16 v[192:195], v[176:179], v[50:53], v[192:195]\n"
      "v_fmac_f32 v181, v224, v224\n"
      "v_fmac_f32 v181, v225, v225\n"
      "v_mfma_f32_16x16x32_bf16 v[172:175], v[204:207], v[156:159], 0\n"
      "v_fmac_f32 v181, v226, v226\n"
      "v_fmac_f32 v181, v227, v227\n"
      "v_mfma_f32_16x16x32_bf16 v[172:175], v[176:179], v[54:57], v[172:175]\n"
      "v_fmac_f32 v181, v188, v188\n"
      "v_fmac_f32 v181, v189, v189\n"
      "v_mfma_f32_16x16x32_bf16 v[184:187], v[204:207], v[248:251], 0\n"
      "v_fmac_f32 v181, v190, v190\n"
      "v_fmac_f32 v181, v191, v191\n"
      "v_mfma_f32_16x16x32_bf16 v[184:187], v[176:179], v[58:61], v[184:187]\n"
      "v_mov_b32 v182, v181\n"
      "v_mov_b32_dpp v160, v160 quad_perm:[0,0,0,0] row_mask:0xf bank_mask:0xf\n"
      "v_mfma_f32_16x16x32_bf16 v[176:179], a[0:3], v[252:255], 0\n"
      "v_permlane16_swap_b32 v181, v182\n"
      "v_mfma_f32_16x16x32_bf16 v[176:179], a[36:39], v[208:211], v[176:179]\n"
      "v_mfma_f32_16x16x32_bf16 v[204:207], a[4:7], v[252:255], 0\n"
      "s_waitcnt lgkmcnt(0)\n"
      "v_add_f32_e64 v181, v181, v182\n"
      "v_cndmask_b32 v192, v192, v172, s[42:43]\n"
      "v_mfma_f32_16x16x32_bf16 v[204:207], a[40:43], v[208:211], v[204:207]\n"
      "v_permlane32_swap_b32_e64 v180, v181\n"
      "v_mfma_f32_16x16x32_bf16 v[172:175], a[8:11], v[252:255], 0\n"
      "v_cndmask_b32 v192, v192, v184, s[44:45]\n"
      "v_add_f32_e64 v180, v180, v181\n"
      "v_mfma_f32_16x16x32_bf16 v[172:175], a[44:47], v[208:211], v[172:175]\n"
      "ds_write_b32 v129, v180 offset:1024\n"
      "v_mfma_f32_16x16x32_bf16 v[184:187], a[12:15], v[252:255], 0\n"
      "v_rcp_f32 v160, v160\n"
      "v_mfma_f32_16x16x32_bf16 v[184:187], a[48:51], v[208:211], v[184:187]\n"
      "v_mul_f32 v192, v160, v192\n"
      "v_cvt_pk_bf16_f32 v192, v192, v192\n"
      "v_mfma_f32_16x16x32_bf16 v[180:183], a[16:19], v[252:255], 0\n"
      "v_mfma_f32_16x16x32_bf16 v[180:183], a[52:55], v[208:211], v[180:183]\n"
      "ds_write_b16 v135, v192 offset:16\n"
      "v_mfma_f32_16x16x32_bf16 v[160:163], a[20:23], v[252:255], 0\n"
      "v_mfma_f32_16x16x32_bf16 v[160:163], a[56:59], v[208:211], v[160:163]\n"
      "s_branch L_join_21_%=\n"
      "L_flg_116_%=:\n"
      "v_cmp_gt_i32 vcc, 0, v208\n"
      "s_cbranch_vccz L_fb_117_%=\n"
      "s_sleep 1\n"
      "ds_read_b128 v[208:211], v128 offset:2048\n"
      "s_waitcnt lgkmcnt(0)\n"
      "v_bitop3_b32 v208, v208, v209, s4 bitop3:0x7e\n"
      "v_bitop3_b32 v210, v210, v211, s4 bitop3:0x7e\n"
      "v_or_b32 v208, v208, v210\n"
      "v_cmp_ne_u32 s[48:49], 0, v208\n"
      "s_branch L_flg_116_%=\n"
      "L_gen_20_%=:\n"
      "s_nop 0\n"
      "s_nop 0\n"
      "v_mbcnt_lo_u32_b32 v181, -1, 0\n"
      "v_mbcnt_hi_u32_b32 v181, -1, v181\n"
      "v_and_b32 v193, 15, v181\n"
      "v_mul_u32_u24 v200, 0x62, v193\n"
      "v_lshrrev_b32 v192, 4, v181\n"
      "v_lshl_add_u32 v200, v192, 3, v200\n"
      "v_add_u32 v200, s30, v200\n"
      "v_lshlrev_b32 v192, 2, v192\n"
      "v_and_b32 v181, -4, v200\n"
      "v_sub_u32 v193, v193, v192\n"
      "v_and_b32 v192, 1, v193\n"
      "v_lshlrev_b32 v192, 4, v192\n"
      "v_lshrrev_b32_e64 v193, 1, v193\n"
      "s_movk_i32 s31, 0x3fb9\n"
      "ds_read2_b32 v[142:143], v181 offset0:18 offset1:24\n"
      "ds_read2_b32 v[228:229], v181 offset0:8 offset1:9\n"
      "ds_read2_b32 v[140:141], v181 offset0:2 offset1:10\n"
      "ds_read2_b32 v[146:147], v181 offset0:0 offset1:1\n"
      "ds_read2_b32 v[238:239], v181 offset0:16 offset1:17\n"
      "v_cmp_eq_u32 vcc, 0, v193\n"
      "v_lshlrev_b32 v192, v192, s31\n"
      "v_add_u32 v181, 1568, v181\n"
      "v_cndmask_b32 v174, 0, v192, vcc\n"
      "v_mfma_f32_16x16x32_f16 v[216:219], v[252:255], v[234:237], v[94:97]\n"
      "ds_read2_b32 v[182:183], v181 offset0:0 offset1:1\n"
      "ds_read2_b32 v[198:199], v181 offset0:18 offset1:24\n"
      "ds_read2_b32 v[196:197], v181 offset0:2 offset1:10\n"
      "ds_read2_b32 v[210:211], v181 offset0:16 offset1:17\n"
      "v_and_b32 v200, 2, v200\n"
      "ds_read2_b32 v[208:209], v181 offset0:8 offset1:9\n"
      "v_add_u32 v181, 1568, v181\n"
      "v_cmp_eq_u32 vcc, 1, v193\n"
      "v_mfma_f32_16x16x32_f16 v[216:219], v[40:43], v[38:41], v[216:219]\n"
      "ds_read2_b32 v[178:179], v181 offset0:8 offset1:9\n"
      "ds_read2_b32 v[204:205], v181 offset0:16 offset1:17\n"
      "ds_read2_b32 v[176:177], v181 offset0:0 offset1:1\n"
      "s_waitcnt lgkmcnt(11)\n"
      "v_cndmask_b32 v175, 0, v192, vcc\n"
      "v_mfma_f32_16x16x32_f16 v[192:195], v[212:215], v[234:237], v[90:93]\n"
      "ds_read2_b32 v[206:207], v181 offset0:2 offset1:10\n"
      "s_waitcnt lgkmcnt(9)\n"
      "v_mfma_f32_16x16x32_f16 v[192:195], v[34:37], v[38:41], v[192:195]\n"
      "ds_read2_b32 v[172:173], v181 offset0:18 offset1:24\n"
      "v_alignbyte_b32 v143, v143, v143, v200\n"
      "v_alignbyte_b32 v146, v147, v146, v200\n"
      "v_alignbyte_b32 v228, v229, v228, v200\n"
      "v_alignbyte_b32 v147, v140, v147, v200\n"
      "v_alignbyte_b32 v229, v141, v229, v200\n"
      "v_lshlrev_b32 v143, 16, v143\n"
      "v_alignbyte_b32 v238, v239, v238, v200\n"
      "v_alignbyte_b32 v239, v142, v239, v200\n"
      "v_mfma_f32_16x16x32_f16 v[220:223], v[230:233], v[234:237], v[72:75]\n"
      "s_waitcnt lgkmcnt(7)\n"
      "v_fmac_f32 v144, s37, v143\n"
      "v_alignbyte_b32 v182, v183, v182, v200\n"
      "v_mfma_f32_16x16x32_f16 v[220:223], v[184:187], v[252:255], v[220:223]\n"
      "s_waitcnt lgkmcnt(5)\n"
      "v_alignbyte_b32 v208, v209, v208, v200\n"
      "v_alignbyte_b32 v183, v196, v183, v200\n"
      "v_mfma_f32_16x16x32_f16 v[220:223], v[168:171], v[38:41], v[220:223]\n"
      "s_nop 0\n"
      "v_alignbyte_b32 v199, v199, v199, v200\n"
      "v_mfma_f32_16x16x16_bf16 v[148:151], v[174:175], v[228:229], v[148:151]\n"
      "v_alignbyte_b32 v209, v197, v209, v200\n"
      "v_alignbyte_b32 v210, v211, v210, v200\n"
      "v_mfma_f32_16x16x32_f16 v[140:143], v[160:163], v[234:237], v[98:101]\n"
      "v_alignbyte_b32 v211, v198, v211, v200\n"
      "v_mfma_f32_16x16x32_f16 v[140:143], v[46:49], v[38:41], v[140:143]\n"
      "s_waitcnt lgkmcnt(1)\n"
      "v_lshlrev_b32 v199, 16, v199\n"
      "v_alignbyte_b32 v176, v177, v176, v200\n"
      "v_mfma_f32_16x16x16_bf16 v[188:191], v[174:175], v[146:147], v[188:191]\n"
      "s_waitcnt lgkmcnt(0)\n"
      "v_fmac_f32 v220, s37, v199\n"
      "v_alignbyte_b32 v177, v206, v177, v200\n"
      "v_mfma_f32_16x16x16_bf16 v[164:167], v[174:175], v[238:239], v[164:167]\n"
      "ds_read_b128 v[236:239], v128 offset:2048\n"
      "v_alignbyte_b32 v178, v179, v178, v200\n"
      "v_mfma_f32_16x16x16_bf16 v[140:143], v[174:175], v[210:211], v[140:143]\n"
      "s_nop 0\n"
      "v_max3_f32 v228, v188, v189, v190\n"
      "v_max3_f32 v228, v228, v150, v151\n"
      "v_mfma_f32_16x16x16_bf16 v[216:219], v[174:175], v[208:209], v[216:219]\n"
      "s_nop 0\n"
      "v_alignbyte_b32 v204, v205, v204, v200\n"
      "v_max3_f32 v228, v228, v166, v167\n"
      "v_max3_f32 v229, v191, v148, v149\n"
      "v_max3_f32 v229, v229, v164, v165\n"
      "v_max3_f32 v228, v228, v229, v144\n"
      "v_mfma_f32_16x16x16_bf16 v[192:195], v[174:175], v[182:183], v[192:195]\n"
      "v_alignbyte_b32 v179, v207, v179, v200\n"
      "v_mov_b32_e64 v229, v228\n"
      "v_alignbyte_b32 v205, v172, v205, v200\n"
      "v_alignbyte_b32 v173, v173, v173, v200\n"
      "v_permlane16_swap_b32 v228, v229\n"
      "v_max_f32 v228, v228, v229\n"
      "v_mov_b32 v229, v228\n"
      "v_lshlrev_b32 v173, 16, v173\n"
      "v_max3_f32 v172, v195, v216, v217\n"
      "v_permlane32_swap_b32 v228, v229\n"
      "v_max_f32 v228, v228, v229\n"
      "v_sub_f32 v164, v164, v228\n"
      "v_sub_f32 v149, v149, v228\n"
      "v_sub_f32 v150, v150, v228\n"
      "v_sub_f32 v167, v167, v228\n"
      "v_sub_f32 v148, v148, v228\n"
      "v_sub_f32 v189, v189, v228\n"
      "v_sub_f32 v165, v165, v228\n"
      "v_exp_f32 v189, v189\n"
      "v_sub_f32 v151, v151, v228\n"
      "v_exp_f32 v151, v151\n"
      "v_sub_f32 v190, v190, v228\n"
      "v_sub_f32 v188, v188, v228\n"
      "v_exp_f32 v164, v164\n"
      "v_exp_f32 v150, v150\n"
      "v_exp_f32 v188, v188\n"
      "v_exp_f32 v165, v165\n"
      "v_exp_f32 v190, v190\n"
      "v_exp_f32 v149, v149\n"
      "v_sub_f32 v144, v144, v228\n"
      "v_sub_f32 v191, v191, v228\n"
      "v_exp_f32 v191, v191\n"
      "v_sub_f32 v166, v166, v228\n"
      "s_waitcnt lgkmcnt(0)\n"
      "v_max3_f32 v172, v172, v140, v141\n"
      "v_exp_f32 v144, v144\n"
      "v_max3_f32 v228, v192, v193, v194\n"
      "v_max3_f32 v228, v228, v218, v219\n"
      "v_cvt_pk_bf16_f32 v188, v188, v189\n"
      "v_cvt_pk_bf16_f32 v189, v190, v191\n"
      "v_bitop3_b32 v238, v238, v239, s4 bitop3:0x7e\n"
      "v_exp_f32_e64 v166, v166\n"
      "v_max3_f32 v228, v228, v142, v143\n"
      "v_bitop3_b32 v236, v236, v237, s4 bitop3:0x7e\n"
      "v_cvt_pk_bf16_f32 v62, v164, v165\n"
      "v_max3_f32 v228, v228, v172, v220\n"
      "v_mov_b32 v229, v228\n"
      "v_exp_f32 v167, v167\n"
      "v_cvt_pk_bf16_f32 v64, v144, 0\n"
      "v_permlane16_swap_b32_e64 v228, v229\n"
      "v_cvt_pk_bf16_f32 v191, v150, v151\n"
      "v_max_f32 v228, v228, v229\n"
      "v_mov_b32 v229, v228\n"
      "v_or_b32 v236, v236, v238\n"
      "v_cvt_pk_bf16_f32 v63, v166, v167\n"
      "v_cmp_ne_u32 vcc, 0, v236\n"
      "v_permlane32_swap_b32 v228, v229\n"
      "v_max_f32 v228, v228, v229\n"
      "v_exp_f32 v148, v148\n"
      "v_sub_f32 v194, v194, v228\n"
      "v_cvt_pk_bf16_f32 v190, v148, v149\n"
      "v_sub_f32 v216, v216, v228\n"
      "v_sub_f32 v140, v140, v228\n"
      "v_sub_f32 v195, v195, v228\n"
      "s_mov_b64 s[48:49], vcc\n"
      "v_exp_f32 v216, v216\n"
      "v_sub_f32 v219, v219, v228\n"
      "v_sub_f32 v192, v192, v228\n"
      "v_sub_f32 v220, v220, v228\n"
      "v_exp_f32 v195, v195\n"
      "v_sub_f32 v218, v218, v228\n"
      "v_exp_f32 v192, v192\n"
      "v_sub_f32 v193, v193, v228\n"
      "v_exp_f32 v219, v219\n"
      "v_exp_f32 v194, v194\n"
      "v_exp_f32 v193, v193\n"
      "v_exp_f32 v140, v140\n"
      "v_sub_f32_e64 v217, v217, v228\n"
      "v_cvt_pk_bf16_f32 v192, v192, v193\n"
      "v_sub_f32 v143, v143, v228\n"
      "v_sub_f32 v142, v142, v228\n"
      "v_exp_f32_e64 v217, v217\n"
      "v_sub_f32_e64 v141, v141, v228\n"
      "v_exp_f32 v218, v218\n"
      "v_cvt_pk_bf16_f32 v193, v194, v195\n"
      "v_mfma_f32_16x16x32_bf16 v[148:151], v[66:69], v[188:191], 0\n"
      "s_cbranch_vccnz L_flg_128_%=\n"
      "L_fb_129_%=:\n"
      "v_mfma_f32_16x16x32_bf16 v[148:151], v[66:69], v[62:65], v[148:151]\n"
      "v_cvt_pk_bf16_f32 v195, v218, v219\n"
      "v_cvt_pk_bf16_f32 v194, v216, v217\n"
      "v_mfma_f32_16x16x32_bf16 v[236:239], v[244:247], v[188:191], 0\n"
      "v_exp_f32_e64 v141, v141\n"
      "v_mfma_f32_16x16x32_bf16 v[236:239], v[50:53], v[62:65], v[236:239]\n"
      "ds_read_b128 v[208:211], v120\n"
      "v_exp_f32 v220, v220\n"
      "v_mfma_f32_16x16x32_bf16 v[164:167], v[156:159], v[188:191], 0\n"
      "v_exp_f32 v142, v142\n"
      "v_mfma_f32_16x16x32_bf16 v[164:167], v[54:57], v[62:65], v[164:167]\n"
      "v_exp_f32 v143, v143\n"
      "v_mfma_f32_16x16x32_bf16 v[196:199], v[248:251], v[188:191], 0\n"
      "v_rcp_f32 v148, v148\n"
      "v_mfma_f32_16x16x32_bf16 v[196:199], v[58:61], v[62:65], v[196:199]\n"
      "v_cvt_pk_bf16_f32 v62, v140, v141\n"
      "v_cvt_pk_bf16_f32 v63, v142, v143\n"
      "v_mfma_f32_16x16x32_f16 v[188:191], v[212:215], v[224:227], v[102:105]\n"
      "v_mul_f32_e64 v236, v148, v236\n"
      "v_cvt_pk_bf16_f32 v64, v220, 0\n"
      "v_mfma_f32_16x16x32_f16 v[188:191], v[34:37], v[44:47], v[188:191]\n"
      "v_mul_f32 v237, v148, v237\n"
      "v_mul_f32 v167, v148, v167\n"
      "v_mfma_f32_16x16x32_f16 v[212:215], v[252:255], v[224:227], v[106:109]\n"
      "v_mul_f32 v239, v148, v239\n"
      "v_mul_f32 v238, v148, v238\n"
      "v_mfma_f32_16x16x32_f16 v[212:215], v[40:43], v[44:47], v[212:215]\n"
      "v_cvt_pk_bf16_f32 v236, v236, v237\n"
      "v_mul_f32_e64 v196, v148, v196\n"
      "v_mfma_f32_16x16x16_bf16 v[188:191], v[174:175], v[176:177], v[188:191]\n"
      "v_mul_f32 v197, v148, v197\n"
      "v_mul_f32 v165, v148, v165\n"
      "v_cvt_pk_bf16_f32 v237, v238, v239\n"
      "v_mfma_f32_16x16x16_bf16 v[212:215], v[174:175], v[178:179], v[212:215]\n"
      "s_waitcnt lgkmcnt(0)\n"
      "v_mul_f32 v198, v148, v198\n"
      "v_mul_f32 v166, v148, v166\n"
      "v_mul_f32 v164, v148, v164\n"
      "v_cvt_pk_bf16_f32 v239, v166, v167\n"
      "v_mfma_f32_16x16x32_f16 v[140:143], v[230:233], v[224:227], v[74:77]\n"
      "v_mul_f32_e64 v199, v148, v199\n"
      "v_cvt_pk_bf16_f32 v238, v164, v165\n"
      "v_mfma_f32_16x16x32_f16 v[140:143], v[184:187], v[160:163], v[140:143]\n"
      "ds_write_b128 v124, v[236:239] offset:18432\n"
      "v_mfma_f32_16x16x32_f16 v[236:239], v[160:163], v[224:227], v[110:113]\n"
      "v_cvt_pk_bf16_f32 v196, v196, v197\n"
      "v_mfma_f32_16x16x32_f16 v[236:239], v[46:49], v[44:47], v[236:239]\n"
      "global_store_dwordx4 v117, v[208:211], s[54:55] sc1\n"
      "v_mfma_f32_16x16x32_f16 v[140:143], v[168:171], v[44:47], v[140:143]\n"
      "v_mfma_f32_16x16x32_f16 v[148:151], v[184:187], v[230:233], v[76:79]\n"
      "s_nop 2\n"
      "v_mfma_f32_16x16x16_bf16 v[236:239], v[174:175], v[204:205], v[236:239]\n"
      "s_nop 0\n"
      "v_cvt_pk_bf16_f32 v197, v198, v199\n"
      "s_nop 0\n"
      "v_mfma_f32_16x16x16_f16 v[148:151], v[170:171], v[168:169], v[148:151]\n"
      "v_mfma_f32_16x16x32_bf16 v[168:171], v[66:69], v[192:195], 0\n"
      "v_fmac_f32 v140, s37, v173\n"
      "v_mfma_f32_16x16x32_bf16 v[168:171], v[66:69], v[62:65], v[168:171]\n"
      "v_mfma_f32_16x16x32_bf16 v[228:231], v[244:247], v[192:195], 0\n"
      "v_mfma_f32_16x16x32_bf16 v[228:231], v[50:53], v[62:65], v[228:231]\n"
      "v_max3_f32 v143, v191, v212, v213\n"
      "v_mfma_f32_16x16x32_bf16 v[224:227], v[156:159], v[192:195], 0\n"
      "v_max3_f32 v142, v188, v189, v190\n"
      "v_max3_f32 v142, v142, v214, v215\n"
      "v_mfma_f32_16x16x32_bf16 v[224:227], v[54:57], v[62:65], v[224:227]\n"
      "s_nop 0\n"
      "v_rcp_f32 v168, v168\n"
      "v_max3_f32 v142, v142, v238, v239\n"
      "v_mul_f32 v231, v168, v231\n"
      "v_mul_f32 v230, v168, v230\n"
      "v_mul_f32 v228, v168, v228\n"
      "v_mul_f32 v229, v168, v229\n"
      "v_mfma_f32_16x16x32_bf16 v[252:255], v[248:251], v[192:195], 0\n"
      "v_mul_f32 v227, v168, v227\n"
      "v_max3_f32 v143, v143, v236, v237\n"
      "v_mfma_f32_16x16x32_bf16 v[252:255], v[58:61], v[62:65], v[252:255]\n"
      "v_mul_f32 v226, v168, v226\n"
      "v_max3_f32 v142, v142, v143, v140\n"
      "v_cvt_pk_bf16_f32 v228, v228, v229\n"
      "v_mul_f32 v224, v168, v224\n"
      "v_cvt_pk_bf16_f32 v229, v230, v231\n"
      "v_mul_f32 v225, v168, v225\n"
      "v_mov_b32 v143, v142\n"
      "v_cvt_pk_bf16_f32 v231, v226, v227\n"
      "v_mul_f32 v253, v168, v253\n"
      "v_permlane16_swap_b32 v142, v143\n"
      "v_max_f32 v142, v142, v143\n"
      "v_mul_f32 v254, v168, v254\n"
      "v_mov_b32 v143, v142\n"
      "v_mul_f32 v252, v168, v252\n"
      "v_cvt_pk_bf16_f32 v252, v252, v253\n"
      "v_mul_f32 v255, v168, v255\n"
      "v_permlane32_swap_b32 v142, v143\n"
      "v_max_f32 v142, v142, v143\n"
      "v_sub_f32_e64 v239, v239, v142\n"
      "v_cvt_pk_bf16_f32 v253, v254, v255\n"
      "v_sub_f32 v191, v191, v142\n"
      "v_sub_f32 v238, v238, v142\n"
      "v_exp_f32 v238, v238\n"
      "v_sub_f32 v189, v189, v142\n"
      "v_sub_f32 v140, v140, v142\n"
      "v_sub_f32 v188, v188, v142\n"
      "v_cndmask_b32 v145, v145, v221, s[42:43]\n"
      "v_sub_f32 v236, v236, v142\n"
      "v_sub_f32 v190, v190, v142\n"
      "v_exp_f32 v189, v189\n"
      "v_exp_f32 v188, v188\n"
      "v_exp_f32 v190, v190\n"
      "v_sub_f32 v212, v212, v142\n"
      "v_sub_f32 v237, v237, v142\n"
      "v_cvt_pk_bf16_f32 v230, v224, v225\n"
      "v_sub_f32 v214, v214, v142\n"
      "v_exp_f32 v239, v239\n"
      "v_cvt_pk_bf16_f32 v188, v188, v189\n"
      "v_exp_f32 v214, v214\n"
      "v_cvt_pk_bf16_f32 v63, v238, v239\n"
      "ds_read_b128 v[224:227], v121\n"
      "v_exp_f32 v191, v191\n"
      "v_sub_f32 v215, v215, v142\n"
      "v_exp_f32 v236, v236\n"
      "v_exp_f32 v215, v215\n"
      "v_sub_f32 v213, v213, v142\n"
      "v_cvt_pk_bf16_f32 v189, v190, v191\n"
      "v_exp_f32 v140, v140\n"
      "v_exp_f32 v212, v212\n"
      "v_exp_f32 v237, v237\n"
      "v_exp_f32_e64 v213, v213\n"
      "v_cvt_pk_bf16_f32 v191, v214, v215\n"
      "v_cvt_pk_bf16_f32 v190, v212, v213\n"
      "v_cvt_pk_bf16_f32 v64, v140, 0\n"
      "v_cvt_pk_bf16_f32 v62, v236, v237\n"
      "v_mfma_f32_16x16x32_bf16 v[212:215], v[66:69], v[188:191], 0\n"
      "ds_read_b128 v[164:167], v123 offset:1024\n"
      "ds_read_b128 v[232:235], v123 offset:4096\n"
      "v_mbcnt_lo_u32_b32 v181, -1, 0\n"
      "v_mfma_f32_16x16x32_bf16 v[212:215], v[66:69], v[62:65], v[212:215]\n"
      "v_mbcnt_hi_u32_b32 v181, -1, v181\n"
      "v_cndmask_b32 v145, v145, v141, s[44:45]\n"
      "v_mfma_f32_16x16x32_bf16 v[168:171], v[244:247], v[188:191], 0\n"
      "ds_read_b128 v[208:211], v123 offset:7168\n"
      "v_min_u32_e64 v181, 48, v181\n"
      "v_mfma_f32_16x16x32_bf16 v[168:171], v[50:53], v[62:65], v[168:171]\n"
      "ds_write_b128 v124, v[228:231] offset:19456\n"
      "v_mfma_f32_16x16x32_bf16 v[228:231], v[156:159], v[188:191], 0\n"
      "ds_read_b128 v[176:179], v123 offset:10240\n"
      "v_lshlrev_b32 v181, 1, v181\n"
      "v_mfma_f32_16x16x32_bf16 v[228:231], v[54:57], v[62:65], v[228:231]\n"
      "s_waitcnt lgkmcnt(5)\n"
      "ds_write2st64_b64 v125, v[196:197], v[252:253] offset0:36 offset1:38\n"
      "v_add_u32 v181, s30, v181\n"
      "ds_read_u16 v182, v181 offset:4704\n"
      "s_waitcnt lgkmcnt(5)\n"
      "v_mfma_f32_16x16x32_bf16 v[236:239], a[216:219], v[164:167], 0\n"
      "v_cndmask_b32 v145, v145, v149, s[46:47]\n"
      "v_mfma_f32_16x16x32_bf16 v[236:239], a[228:231], v[232:235], v[236:239]\n"
      "s_mov_b32 exec_hi, 0\n"
      "global_store_dwordx4 v118, v[224:227], s[54:55] sc1\n"
      "s_mov_b32 exec_hi, -1\n"
      "v_mfma_f32_16x16x32_bf16 v[140:143], a[220:223], v[164:167], 0\n"
      "s_add_u32 s28, s27, 2\n"
      "ds_read_b128 v[224:227], v123 offset:13312\n"
      "v_mfma_f32_16x16x32_bf16 v[140:143], a[232:235], v[232:235], v[140:143]\n"
      "s_min_u32 m0, s28, s3\n"
      "v_mfma_f32_16x16x32_bf16 v[148:151], a[224:227], v[164:167], 0\n"
      "s_movrels_b32 s9, s80\n"
      "v_mfma_f32_16x16x32_bf16 v[148:151], a[236:239], v[232:235], v[148:151]\n"
      "s_waitcnt lgkmcnt(3)\n"
      "ds_read_b128 v[232:235], v123 offset:16384\n"
      "s_lshr_b32 s8, s9, 8\n"
      "ds_read_b128 v[164:167], v123 offset:2048\n"
      "v_mfma_f32_16x16x32_bf16 v[236:239], a[240:243], v[208:211], v[236:239]\n"
      "s_waitcnt lgkmcnt(3)\n"
      "v_lshlrev_b32_e64 v182, 16, v182\n"
      "v_fmac_f32 v145, s37, v182\n"
      "v_mfma_f32_16x16x32_bf16 v[236:239], a[252:255], v[176:179], v[236:239]\n"
      "s_nop 0\n"
      "v_max_f32_dpp v144, v145, v145 row_ror:8 row_mask:0xf bank_mask:0xf\n"
      "s_and_b32 s9, s9, 0xff\n"
      "v_mfma_f32_16x16x32_bf16 v[148:151], a[248:251], v[208:211], v[148:151]\n"
      "s_mul_i32 s10, s8, 73\n"
      "v_max_f32_dpp v144, v144, v144 row_ror:4 row_mask:0xf bank_mask:0xf\n"
      "v_mfma_f32_16x16x32_bf16 v[140:143], a[244:247], v[208:211], v[140:143]\n"
      "s_add_u32 s10, s10, s9\n"
      "v_max_f32_dpp v144, v144, v144 row_ror:2 row_mask:0xf bank_mask:0xf\n"
      "s_mul_i32 s28, s10, 4802\n"
      "s_nop 0\n"
      "v_max_f32_dpp v144, v144, v144 row_ror:1 row_mask:0xf bank_mask:0xf\n"
      "v_mfma_f32_16x16x32_bf16 v[148:151], v[4:7], v[176:179], v[148:151]\n"
      "ds_read_b128 v[208:211], v123 offset:5120\n"
      "v_max_f32_dpp v144, v144, v144 row_bcast:15 row_mask:0xa bank_mask:0xf\n"
      "v_mfma_f32_16x16x32_bf16 v[140:143], v[0:3], v[176:179], v[140:143]\n"
      "s_add_u32 s56, s18, s28\n"
      "v_max_f32_dpp v144, v144, v144 row_bcast:31 row_mask:0xc bank_mask:0xf\n"
      "s_addc_u32 s57, s19, 0\n"
      "v_readlane_b32 s31, v144, 63\n"
      "s_waitcnt lgkmcnt(2)\n"
      "v_mfma_f32_16x16x32_bf16 v[236:239], v[8:11], v[224:227], v[236:239]\n"
      "s_and_b32 s56, s56, -16\n"
      "s_sub_u32 s58, s56, 110592\n"
      "v_subrev_f32 v145, s31, v145\n"
      "v_mfma_f32_16x16x32_bf16 v[236:239], v[20:23], v[232:235], v[236:239]\n"
      "s_subb_u32 s59, s57, 0\n"
      "v_exp_f32 v145, v145\n"
      "v_mfma_f32_16x16x32_bf16 v[140:143], v[12:15], v[224:227], v[140:143]\n"
      "s_sub_u32 s56, s56, 73728\n"
      "v_cvt_pk_bf16_f32 v145, v145, v145\n"
      "s_subb_u32 s57, s57, 0\n"
      "v_mfma_f32_16x16x32_bf16 v[140:143], v[24:27], v[232:235], v[140:143]\n"
      "ds_write_b16 v130, v145\n"
      "v_mfma_f32_16x16x32_bf16 v[144:147], v[248:251], v[188:191], 0\n"
      "s_mul_i32 s28, s8, 1373568\n"
      "ds_read_b128 v[176:179], v131\n"
      "v_mfma_f32_16x16x32_bf16 v[144:147], v[58:61], v[62:65], v[144:147]\n"
      "s_mul_i32 s29, s9, 2688\n"
      "ds_read_b128 v[172:175], v123 offset:11264\n"
      "s_add_u32 s28, s28, s29\n"
      "v_mfma_f32_16x16x32_bf16 v[148:151], v[16:19], v[224:227], v[148:151]\n"
      "ds_read_b128 v[184:187], v131 offset:64\n"
      "s_cmp_eq_u32 s8, 72\n"
      "v_mfma_f32_16x16x32_bf16 v[148:151], v[28:31], v[232:235], v[148:151]\n"
      "s_waitcnt lgkmcnt(4)\n"
      "ds_read_b128 v[196:199], v123 offset:8192\n"
      "v_mfma_f32_16x16x32_bf16 v[232:235], a[216:219], v[164:167], 0\n"
      "ds_read_b128 v[252:255], v114\n"
      "s_cselect_b32 s8, 0, 100270464\n"
      "v_mfma_f32_16x16x32_bf16 v[232:235], a[228:231], v[208:211], v[232:235]\n"
      "s_cmp_eq_u32 s9, 72\n"
      "s_cselect_b32 s9, 0, 196224\n"
      "s_add_u32 s10, s8, s9\n"
      "v_mfma_f32_16x16x32_bf16 v[224:227], a[220:223], v[164:167], 0\n"
      "ds_read_b128 v[204:207], v123 offset:14336\n"
      "v_fma_f32 v182, v236, v236, s39\n"
      "v_mfma_f32_16x16x32_bf16 v[224:227], a[232:235], v[208:211], v[224:227]\n"
      "s_add_u32 s28, s28, s10\n"
      "s_add_u32 s52, s16, s28\n"
      "ds_read_b128 v[160:163], v123 offset:17408\n"
      "v_mfma_f32_16x16x32_bf16 v[188:191], a[224:227], v[164:167], 0\n"
      "s_addc_u32 s53, s17, 0\n"
      "v_fmac_f32 v182, v237, v237\n"
      "v_fmac_f32 v182, v238, v238\n"
      "v_mfma_f32_16x16x32_bf16 v[188:191], a[236:239], v[208:211], v[188:191]\n"
      "v_fmac_f32 v182, v239, v239\n"
      "v_fmac_f32 v182, v140, v140\n"
      "v_fmac_f32 v182, v141, v141\n"
      "v_fmac_f32 v182, v142, v142\n"
      "v_fmac_f32 v182, v143, v143\n"
      "v_fmac_f32 v182, v148, v148\n"
      "v_fmac_f32 v182, v149, v149\n"
      "v_fmac_f32 v182, v150, v150\n"
      "s_waitcnt lgkmcnt(3)\n"
      "v_mfma_f32_16x16x32_bf16 v[232:235], a[240:243], v[196:199], v[232:235]\n"
      "v_fmac_f32 v182, v151, v151\n"
      "s_add_u32 s27, s27, 1\n"
      "v_mfma_f32_16x16x32_bf16 v[232:235], a[252:255], v[172:175], v[232:235]\n"
      "s_cmp_ge_u32 s27, s26\n"
      "v_mfma_f32_16x16x32_bf16 v[224:227], a[244:247], v[196:199], v[224:227]\n"
      "ds_read_b128 v[208:211], v115\n"
      "s_cselect_b32 s28, 1, 0\n"
      "v_mfma_f32_16x16x32_bf16 v[224:227], v[0:3], v[172:175], v[224:227]\n"
      "s_xor_b32 s29, s10, s11\n"
      "v_permlane16_swap_b32_e64 v180, v182\n"
      "v_mfma_f32_16x16x32_bf16 v[188:191], a[248:251], v[196:199], v[188:191]\n"
      "s_or_b32 s28, s28, s29\n"
      "v_mfma_f32_16x16x32_bf16 v[188:191], v[4:7], v[172:175], v[188:191]\n"
      "s_waitcnt lgkmcnt(1)\n"
      "v_mfma_f32_16x16x32_bf16 v[172:175], v[176:179], v[156:159], 0\n"
      "v_mfma_f32_16x16x32_bf16 v[232:235], v[8:11], v[204:207], v[232:235]\n"
      "ds_read_b128 v[196:199], v115 offset:6144\n"
      "v_mfma_f32_16x16x32_bf16 v[232:235], v[20:23], v[160:163], v[232:235]\n"
      "v_mfma_f32_16x16x32_bf16 v[224:227], v[12:15], v[204:207], v[224:227]\n"
      "v_add_f32_e64 v180, v180, v182\n"
      "v_mfma_f32_16x16x32_bf16 v[224:227], v[24:27], v[160:163], v[224:227]\n"
      "v_mfma_f32_16x16x32_bf16 v[188:191], v[16:19], v[204:207], v[188:191]\n"
      "v_mfma_f32_16x16x32_bf16 v[188:191], v[28:31], v[160:163], v[188:191]\n"
      "ds_read_b128 v[164:167], v114 offset:6144\n"
      "v_mfma_f32_16x16x32_bf16 v[160:163], v[176:179], v[66:69], 0\n"
      "s_nop 0\n"
      "v_fma_f32 v182, v232, v232, s39\n"
      "v_fmac_f32 v182, v233, v233\n"
      "v_mfma_f32_16x16x32_bf16 v[160:163], v[184:187], v[66:69], v[160:163]\n"
      "v_fmac_f32 v182, v234, v234\n"
      "s_waitcnt lgkmcnt(2)\n"
      "v_mfma_f32_16x16x32_bf16 v[202:205], v[176:179], v[244:247], 0\n"
      "v_fmac_f32 v182, v235, v235\n"
      "v_mfma_f32_16x16x32_bf16 v[202:205], v[184:187], v[50:53], v[202:205]\n"
      "v_fmac_f32 v182, v224, v224\n"
      "v_fmac_f32 v182, v225, v225\n"
      "v_fmac_f32 v182, v226, v226\n"
      "v_mfma_f32_16x16x32_bf16 v[172:175], v[184:187], v[54:57], v[172:175]\n"
      "v_fmac_f32 v182, v227, v227\n"
      "v_fmac_f32 v182, v188, v188\n"
      "v_fmac_f32_e64 v182, v189, v189\n"
      "v_mfma_f32_16x16x32_bf16 v[156:159], v[176:179], v[248:251], 0\n"
      "v_fmac_f32 v182, v190, v190\n"
      "v_fmac_f32 v182, v191, v191\n"
      "v_mfma_f32_16x16x32_bf16 v[156:159], v[184:187], v[58:61], v[156:159]\n"
      "v_mov_b32_e64 v181, v182\n"
      "v_mfma_f32_16x16x32_bf16 v[176:179], a[0:3], v[252:255], 0\n"
      "s_nop 0\n"
      "v_permlane16_swap_b32_e64 v182, v181\n"
      "v_mfma_f32_16x16x32_bf16 v[176:179], a[36:39], v[208:211], v[176:179]\n"
      "s_waitcnt lgkmcnt(0)\n"
      "v_mov_b32_dpp v160, v160 quad_perm:[0,0,0,0] row_mask:0xf bank_mask:0xf\n"
      "v_mfma_f32_16x16x32_bf16 v[204:207], a[4:7], v[252:255], 0\n"
      "v_cndmask_b32 v202, v202, v172, s[42:43]\n"
      "v_add_f32_e64 v182, v182, v181\n"
      "v_mfma_f32_16x16x32_bf16 v[204:207], a[40:43], v[208:211], v[204:207]\n"
      "s_nop 0\n"
      "v_permlane32_swap_b32 v180, v182\n"
      "v_mfma_f32_16x16x32_bf16 v[172:175], a[8:11], v[252:255], 0\n"
      "v_cndmask_b32 v202, v202, v156, s[44:45]\n"
      "v_add_f32_e64 v180, v180, v182\n"
      "v_mfma_f32_16x16x32_bf16 v[172:175], a[44:47], v[208:211], v[172:175]\n"
      "v_mfma_f32_16x16x32_bf16 v[184:187], a[12:15], v[252:255], 0\n"
      "v_rcp_f32_e64 v160, v160\n"
      "v_mfma_f32_16x16x32_bf16 v[184:187], a[48:51], v[208:211], v[184:187]\n"
      "ds_write_b32 v129, v180 offset:1024\n"
      "v_mfma_f32_16x16x32_bf16 v[180:183], a[16:19], v[252:255], 0\n"
      "v_mul_f32_e64 v202, v160, v202\n"
      "v_cvt_pk_bf16_f32 v202, v202, v202\n"
      "v_mfma_f32_16x16x32_bf16 v[180:183], a[52:55], v[208:211], v[180:183]\n"
      "ds_write_b16 v135, v202 offset:16\n"
      "v_mfma_f32_16x16x32_bf16 v[160:163], a[20:23], v[252:255], 0\n"
      "v_mfma_f32_16x16x32_bf16 v[160:163], a[56:59], v[208:211], v[160:163]\n"
      "s_branch L_join_21_%=\n"
      "L_flg_128_%=:\n"
      "v_cmp_gt_i32 vcc, 0, v236\n"
      "s_cbranch_vccz L_fb_129_%=\n"
      "s_sleep 1\n"
      "ds_read_b128 v[236:239], v128 offset:2048\n"
      "s_waitcnt lgkmcnt(0)\n"
      "v_bitop3_b32 v236, v236, v237, s4 bitop3:0x7e\n"
      "v_bitop3_b32 v238, v238, v239, s4 bitop3:0x7e\n"
      "v_or_b32 v236, v236, v238\n"
      "v_cmp_ne_u32 s[48:49], 0, v236\n"
      "s_branch L_flg_128_%=\n"
      "L_gen_28_%=:\n"
      "s_waitcnt lgkmcnt(0)\n"
      "s_nop 5\n"
      "s_cmp_eq_u64 s[34:35], 0\n"
      "s_cbranch_scc1 L_ok_146_%=\n"
      "ds_read_b128 v[236:239], v114\n"
      "ds_read_b128 v[148:151], v115\n"
      "ds_read_b128 v[176:179], v114 offset:12288\n"
      "ds_read_b128 v[224:227], v114 offset:6144\n"
      "ds_read_b128 v[208:211], v115 offset:6144\n"
      "s_waitcnt lgkmcnt(3)\n"
      "v_mfma_f32_16x16x32_bf16 v[160:163], a[0:3], v[236:239], 0\n"
      "v_mfma_f32_16x16x32_bf16 v[160:163], a[36:39], v[148:151], v[160:163]\n"
      "v_mfma_f32_16x16x32_bf16 v[228:231], a[4:7], v[236:239], 0\n"
      "v_mfma_f32_16x16x32_bf16 v[228:231], a[40:43], v[148:151], v[228:231]\n"
      "v_mfma_f32_16x16x32_bf16 v[144:147], a[8:11], v[236:239], 0\n"
      "s_waitcnt lgkmcnt(0)\n"
      "v_mfma_f32_16x16x32_bf16 v[184:187], a[12:15], v[236:239], 0\n"
      "v_mfma_f32_16x16x32_bf16 v[184:187], a[48:51], v[148:151], v[184:187]\n"
      "s_nop 0\n"
      "v_mfma_f32_16x16x32_bf16 v[252:255], a[16:19], v[236:239], 0\n"
      "v_mfma_f32_16x16x32_bf16 v[252:255], a[52:55], v[148:151], v[252:255]\n"
      "v_mfma_f32_16x16x32_bf16 v[252:255], a[88:91], v[224:227], v[252:255]\n"
      "v_mfma_f32_16x16x32_bf16 v[184:187], a[84:87], v[224:227], v[184:187]\n"
      "v_mfma_f32_16x16x32_bf16 v[198:201], a[20:23], v[236:239], 0\n"
      "ds_read_b128 v[236:239], v115 offset:12288\n"
      "v_mfma_f32_16x16x32_bf16 v[198:201], a[56:59], v[148:151], v[198:201]\n"
      "v_mfma_f32_16x16x32_bf16 v[160:163], a[72:75], v[224:227], v[160:163]\n"
      "v_mfma_f32_16x16x32_bf16 v[160:163], a[108:111], v[208:211], v[160:163]\n"
      "v_mfma_f32_16x16x32_bf16 v[144:147], a[44:47], v[148:151], v[144:147]\n"
      "v_mfma_f32_16x16x32_bf16 v[144:147], a[80:83], v[224:227], v[144:147]\n"
      "v_mfma_f32_16x16x32_bf16 v[144:147], a[116:119], v[208:211], v[144:147]\n"
      "v_mfma_f32_16x16x32_bf16 v[228:231], a[76:79], v[224:227], v[228:231]\n"
      "s_nop 0\n"
      "v_mfma_f32_16x16x32_bf16 v[228:231], a[112:115], v[208:211], v[228:231]\n"
      "s_waitcnt lgkmcnt(0)\n"
      "v_mfma_f32_16x16x32_bf16 v[160:163], a[144:147], v[176:179], v[160:163]\n"
      "ds_read_b128 v[148:151], v114 offset:2048\n"
      "v_mfma_f32_16x16x32_bf16 v[160:163], a[180:183], v[236:239], v[160:163]\n"
      "v_mfma_f32_16x16x32_bf16 v[198:201], a[92:95], v[224:227], v[198:201]\n"
      "v_mfma_f32_16x16x32_bf16 v[198:201], a[128:131], v[208:211], v[198:201]\n"
      "v_mfma_f32_16x16x32_bf16 v[144:147], a[152:155], v[176:179], v[144:147]\n"
      "v_mfma_f32_16x16x32_bf16 v[144:147], a[188:191], v[236:239], v[144:147]\n"
      "v_mfma_f32_16x16x32_bf16 v[228:231], a[148:151], v[176:179], v[228:231]\n"
      "v_mfma_f32_16x16x32_bf16 v[228:231], a[184:187], v[236:239], v[228:231]\n"
      "s_nop 1\n"
      "v_fma_f32 v196, v160, v160, s38\n"
      "v_mfma_f32_16x16x32_bf16 v[252:255], a[124:127], v[208:211], v[252:255]\n"
      "v_mfma_f32_16x16x32_bf16 v[184:187], a[120:123], v[208:211], v[184:187]\n"
      "s_nop 0\n"
      "v_mfma_f32_16x16x32_bf16 v[184:187], a[156:159], v[176:179], v[184:187]\n"
      "v_fmac_f32 v196, v161, v161\n"
      "v_mfma_f32_16x16x32_bf16 v[184:187], a[192:195], v[236:239], v[184:187]\n"
      "v_fmac_f32 v196, v162, v162\n"
      "v_mfma_f32_16x16x32_bf16 v[252:255], a[160:163], v[176:179], v[252:255]\n"
      "v_fmac_f32 v196, v163, v163\n"
      "v_fmac_f32 v196, v228, v228\n"
      "v_mfma_f32_16x16x32_bf16 v[252:255], a[196:199], v[236:239], v[252:255]\n"
      "v_fmac_f32 v196, v229, v229\n"
      "v_fmac_f32 v196, v230, v230\n"
      "v_mfma_f32_16x16x32_bf16 v[198:201], a[164:167], v[176:179], v[198:201]\n"
      "v_fma_f32 v232, v184, v184, s38\n"
      "v_fmac_f32 v196, v231, v231\n"
      "v_mfma_f32_16x16x32_bf16 v[198:201], a[200:203], v[236:239], v[198:201]\n"
      "ds_read_b128 v[236:239], v115 offset:2048\n"
      "v_fmac_f32 v232, v185, v185\n"
      "v_fmac_f32 v232, v186, v186\n"
      "v_fmac_f32 v232, v187, v187\n"
      "v_fmac_f32 v232, v252, v252\n"
      "v_fmac_f32 v232, v253, v253\n"
      "v_fmac_f32 v232, v254, v254\n"
      "v_fmac_f32 v196, v144, v144\n"
      "v_fmac_f32 v232, v255, v255\n"
      "v_fmac_f32 v232, v198, v198\n"
      "v_fmac_f32 v232, v199, v199\n"
      "v_fmac_f32 v232, v200, v200\n"
      "v_fmac_f32 v196, v145, v145\n"
      "v_fmac_f32 v196, v146, v146\n"
      "v_fmac_f32 v232, v201, v201\n"
      "v_fmac_f32 v196, v147, v147\n"
      "s_nop 1\n"
      "v_permlane16_swap_b32 v196, v232\n"
      "v_add_f32 v196, v196, v232\n"
      "v_mov_b32 v232, v196\n"
      "s_nop 1\n"
      "v_permlane32_swap_b32 v196, v232\n"
      "v_add_f32 v196, v196, v232\n"
      "v_rsq_f32 v196, v196\n"
      "s_nop 0\n"
      "v_mov_b32 v232, v196\n"
      "s_waitcnt lgkmcnt(0)\n"
      "v_mfma_f32_16x16x32_bf16 v[140:143], a[0:3], v[148:151], 0\n"
      "ds_read_b128 v[224:227], v114 offset:8192\n"
      "v_permlane16_swap_b32 v196, v232\n"
      "v_mfma_f32_16x16x32_bf16 v[140:143], a[36:39], v[236:239], v[140:143]\n"
      "ds_read_b128 v[208:211], v115 offset:8192\n"
      "v_mul_f32 v186, v232, v186\n"
      "v_mul_f32 v185, v232, v185\n"
      "v_mul_f32 v187, v232, v187\n"
      "v_mul_f32 v184, v232, v184\n"
      "v_mfma_f32_16x16x32_bf16 v[164:167], a[12:15], v[148:151], 0\n"
      "ds_read_b128 v[176:179], v114 offset:14336\n"
      "v_cvt_pk_f16_f32 v184, v184, v185\n"
      "v_mfma_f32_16x16x32_bf16 v[164:167], a[48:51], v[236:239], v[164:167]\n"
      "v_mul_f32 v253, v232, v253\n"
      "v_cvt_pk_f16_f32 v185, v186, v187\n"
      "v_mul_f32 v254, v232, v254\n"
      "v_mul_f32 v255, v232, v255\n"
      "v_cvt_pk_f16_f32 v187, v254, v255\n"
      "v_mul_f32 v252, v232, v252\n"
      "v_cvt_pk_f16_f32 v186, v252, v253\n"
      "v_mfma_f32_16x16x32_bf16 v[252:255], a[16:19], v[148:151], 0\n"
      "v_mul_f32 v199, v232, v199\n"
      "v_mul_f32 v198, v232, v198\n"
      "v_mfma_f32_16x16x32_bf16 v[252:255], a[52:55], v[236:239], v[252:255]\n"
      "v_cvt_pk_f16_f32 v34, v198, v199\n"
      "v_mul_f32 v201, v232, v201\n"
      "v_mfma_f32_16x16x32_bf16 v[156:159], a[20:23], v[148:151], 0\n"
      "ds_read_b128 v[172:175], v115 offset:14336\n"
      "v_mul_f32 v200, v232, v200\n"
      "v_mfma_f32_16x16x32_bf16 v[156:159], a[56:59], v[236:239], v[156:159]\n"
      "v_cvt_pk_f16_f32 v35, v200, v201\n"
      "s_waitcnt lgkmcnt(2)\n"
      "v_mfma_f32_16x16x32_bf16 v[140:143], a[72:75], v[224:227], v[140:143]\n"
      "v_mul_f32 v196, s36, v196\n"
      "v_mul_f32_e64 v160, v196, v160\n"
      "v_mfma_f32_16x16x32_bf16 v[140:143], a[108:111], v[208:211], v[140:143]\n"
      "v_mul_f32 v161, v196, v161\n"
      "v_cvt_pk_f16_f32 v160, v160, v161\n"
      "v_mfma_f32_16x16x32_bf16 v[164:167], a[84:87], v[224:227], v[164:167]\n"
      "v_mul_f32 v162, v196, v162\n"
      "v_mul_f32 v163, v196, v163\n"
      "v_mfma_f32_16x16x32_bf16 v[164:167], a[120:123], v[208:211], v[164:167]\n"
      "v_cvt_pk_f16_f32 v161, v162, v163\n"
      "v_mul_f32 v228, v196, v228\n"
      "v_mfma_f32_16x16x32_bf16 v[252:255], a[88:91], v[224:227], v[252:255]\n"
      "v_mul_f32 v229, v196, v229\n"
      "v_cvt_pk_f16_f32 v162, v228, v229\n"
      "v_mfma_f32_16x16x32_bf16 v[252:255], a[124:127], v[208:211], v[252:255]\n"
      "v_mul_f32 v231, v196, v231\n"
      "v_mul_f32_e64 v230, v196, v230\n"
      "v_mfma_f32_16x16x32_bf16 v[156:159], a[92:95], v[224:227], v[156:159]\n"
      "s_waitcnt lgkmcnt(0)\n"
      "v_mul_f32 v144, v196, v144\n"
      "v_mfma_f32_16x16x32_bf16 v[156:159], a[128:131], v[208:211], v[156:159]\n"
      "v_cvt_pk_f16_f32 v163, v230, v231\n"
      "v_mul_f32 v145, v196, v145\n"
      "v_mfma_f32_16x16x32_bf16 v[228:231], a[4:7], v[148:151], 0\n"
      "v_mul_f32 v146, v196, v146\n"
      "v_mul_f32_e64 v147, v196, v147\n"
      "v_mfma_f32_16x16x32_bf16 v[228:231], a[40:43], v[236:239], v[228:231]\n"
      "v_cvt_pk_f16_f32 v32, v144, v145\n"
      "v_cvt_pk_f16_f32 v33, v146, v147\n"
      "v_mfma_f32_16x16x32_bf16 v[228:231], a[76:79], v[224:227], v[228:231]\n"
      "v_mfma_f32_16x16x32_bf16 v[228:231], a[112:115], v[208:211], v[228:231]\n"
      "v_mfma_f32_16x16x32_bf16 v[144:147], a[8:11], v[148:151], 0\n"
      "v_mfma_f32_16x16x32_bf16 v[144:147], a[44:47], v[236:239], v[144:147]\n"
      "v_mfma_f32_16x16x32_bf16 v[144:147], a[80:83], v[224:227], v[144:147]\n"
      "ds_read_b128 v[236:239], v115 offset:4096\n"
      "v_mfma_f32_16x16x32_bf16 v[144:147], a[116:119], v[208:211], v[144:147]\n"
      "v_mfma_f32_16x16x32_bf16 v[140:143], a[144:147], v[176:179], v[140:143]\n"
      "v_mfma_f32_16x16x32_bf16 v[140:143], a[180:183], v[172:175], v[140:143]\n"
      "v_mfma_f32_16x16x32_bf16 v[228:231], a[148:151], v[176:179], v[228:231]\n"
      "v_mfma_f32_16x16x32_bf16 v[228:231], a[184:187], v[172:175], v[228:231]\n"
      "v_mfma_f32_16x16x32_bf16 v[144:147], a[152:155], v[176:179], v[144:147]\n"
      "v_mfma_f32_16x16x32_bf16 v[144:147], a[188:191], v[172:175], v[144:147]\n"
      "v_mfma_f32_16x16x32_bf16 v[164:167], a[156:159], v[176:179], v[164:167]\n"
      "v_mfma_f32_16x16x32_bf16 v[164:167], a[192:195], v[172:175], v[164:167]\n"
      "s_nop 1\n"
      "v_fma_f32 v232, v140, v140, s38\n"
      "v_mfma_f32_16x16x32_bf16 v[252:255], a[160:163], v[176:179], v[252:255]\n"
      "v_fmac_f32 v232, v141, v141\n"
      "v_fmac_f32 v232, v142, v142\n"
      "v_mfma_f32_16x16x32_bf16 v[252:255], a[196:199], v[172:175], v[252:255]\n"
      "v_fmac_f32 v232, v143, v143\n"
      "v_fma_f32 v233, v164, v164, s38\n"
      "v_fmac_f32 v232, v228, v228\n"
      "v_fmac_f32 v233, v165, v165\n"
      "v_mfma_f32_16x16x32_bf16 v[156:159], a[164:167], v[176:179], v[156:159]\n"
      "ds_read_b128 v[148:151], v114 offset:4096\n"
      "v_fmac_f32 v233, v166, v166\n"
      "v_mfma_f32_16x16x32_bf16 v[156:159], a[200:203], v[172:175], v[156:159]\n"
      "v_fmac_f32 v233, v167, v167\n"
      "v_fmac_f32 v233, v252, v252\n"
      "v_fmac_f32 v233, v253, v253\n"
      "v_fmac_f32 v233, v254, v254\n"
      "v_fmac_f32 v232, v229, v229\n"
      "v_fmac_f32 v232, v230, v230\n"
      "v_fmac_f32 v232, v231, v231\n"
      "v_fmac_f32 v232, v144, v144\n"
      "v_fmac_f32 v232, v145, v145\n"
      "v_fmac_f32 v233, v255, v255\n"
      "v_fmac_f32 v233, v156, v156\n"
      "v_fmac_f32 v232, v146, v146\n"
      "v_fmac_f32 v233, v157, v157\n"
      "v_fmac_f32 v233, v158, v158\n"
      "v_fmac_f32 v232, v147, v147\n"
      "v_fmac_f32 v233, v159, v159\n"
      "s_nop 1\n"
      "v_permlane16_swap_b32 v232, v233\n"
      "v_add_f32 v232, v232, v233\n"
      "v_mov_b32 v233, v232\n"
      "s_nop 1\n"
      "v_permlane32_swap_b32 v232, v233\n"
      "v_add_f32 v232, v232, v233\n"
      "v_rsq_f32 v232, v232\n"
      "s_waitcnt lgkmcnt(0)\n"
      "v_mfma_f32_16x16x32_bf16 v[188:191], a[0:3], v[148:151], 0\n"
      "ds_read_b128 v[224:227], v114 offset:10240\n"
      "v_mov_b32_e64 v233, v232\n"
      "v_mfma_f32_16x16x32_bf16 v[188:191], a[36:39], v[236:239], v[188:191]\n"
      "ds_read_b128 v[208:211], v115 offset:10240\n"
      "v_permlane16_swap_b32 v232, v233\n"
      "v_mfma_f32_16x16x32_bf16 v[212:215], a[12:15], v[148:151], 0\n"
      "ds_read_b128 v[176:179], v114 offset:16384\n"
      "v_mul_f32 v164, v233, v164\n"
      "v_mfma_f32_16x16x32_bf16 v[212:215], a[48:51], v[236:239], v[212:215]\n"
      "v_mul_f32 v167, v233, v167\n"
      "v_mul_f32 v165, v233, v165\n"
      "v_mul_f32 v253, v233, v253\n"
      "v_mul_f32 v166, v233, v166\n"
      "v_cvt_pk_f16_f32 v164, v164, v165\n"
      "v_mul_f32 v254, v233, v254\n"
      "v_cvt_pk_f16_f32 v165, v166, v167\n"
      "v_mul_f32 v255, v233, v255\n"
      "v_mul_f32_e64 v252, v233, v252\n"
      "v_cvt_pk_f16_f32 v166, v252, v253\n"
      "v_cvt_pk_f16_f32 v167, v254, v255\n"
      "v_mfma_f32_16x16x32_bf16 v[252:255], a[16:19], v[148:151], 0\n"
      "v_mul_f32 v156, v233, v156\n"
      "v_mul_f32 v157, v233, v157\n"
      "v_mfma_f32_16x16x32_bf16 v[252:255], a[52:55], v[236:239], v[252:255]\n"
      "v_mul_f32 v159, v233, v159\n"
      "v_cvt_pk_f16_f32 v40, v156, v157\n"
      "v_mfma_f32_16x16x32_bf16 v[198:201], a[20:23], v[148:151], 0\n"
      "ds_read_b128 v[172:175], v115 offset:16384\n"
      "v_mul_f32 v158, v233, v158\n"
      "v_mfma_f32_16x16x32_bf16 v[198:201], a[56:59], v[236:239], v[198:201]\n"
      "s_waitcnt lgkmcnt(2)\n"
      "v_mul_f32 v232, s36, v232\n"
      "v_cvt_pk_f16_f32 v41, v158, v159\n"
      "v_mfma_f32_16x16x32_bf16 v[188:191], a[72:75], v[224:227], v[188:191]\n"
      "v_mul_f32 v140, v232, v140\n"
      "v_mul_f32 v141, v232, v141\n"
      "v_mfma_f32_16x16x32_bf16 v[188:191], a[108:111], v[208:211], v[188:191]\n"
      "v_mul_f32_e64 v142, v232, v142\n"
      "v_cvt_pk_f16_f32 v140, v140, v141\n"
      "v_mfma_f32_16x16x32_bf16 v[252:255], a[88:91], v[224:227], v[252:255]\n"
      "v_mul_f32 v143, v232, v143\n"
      "v_mul_f32 v229, v232, v229\n"
      "v_mfma_f32_16x16x32_bf16 v[252:255], a[124:127], v[208:211], v[252:255]\n"
      "v_cvt_pk_f16_f32 v141, v142, v143\n"
      "v_mul_f32 v228, v232, v228\n"
      "v_mfma_f32_16x16x32_bf16 v[212:215], a[84:87], v[224:227], v[212:215]\n"
      "v_cvt_pk_f16_f32 v142, v228, v229\n"
      "v_mul_f32 v230, v232, v230\n"
      "v_mfma_f32_16x16x32_bf16 v[212:215], a[120:123], v[208:211], v[212:215]\n"
      "v_mul_f32 v231, v232, v231\n"
      "v_cvt_pk_f16_f32 v143, v230, v231\n"
      "v_mfma_f32_16x16x32_bf16 v[228:231], a[4:7], v[148:151], 0\n"
      "v_mul_f32_e64 v145, v232, v145\n"
      "s_waitcnt lgkmcnt(0)\n"
      "v_mfma_f32_16x16x32_bf16 v[228:231], a[40:43], v[236:239], v[228:231]\n"
      "v_mul_f32 v147, v232, v147\n"
      "v_mul_f32 v146, v232, v146\n"
      "v_mfma_f32_16x16x32_bf16 v[228:231], a[76:79], v[224:227], v[228:231]\n"
      "v_mul_f32_e64 v144, v232, v144\n"
      "v_mfma_f32_16x16x32_bf16 v[228:231], a[112:115], v[208:211], v[228:231]\n"
      "v_cvt_pk_f16_f32 v39, v146, v147\n"
      "v_cvt_pk_f16_f32 v38, v144, v145\n"
      "v_mfma_f32_16x16x32_bf16 v[144:147], a[8:11], v[148:151], 0\n"
      "v_mfma_f32_16x16x32_bf16 v[144:147], a[44:47], v[236:239], v[144:147]\n"
      "v_mfma_f32_16x16x32_bf16 v[144:147], a[80:83], v[224:227], v[144:147]\n"
      "v_mfma_f32_16x16x32_bf16 v[144:147], a[116:119], v[208:211], v[144:147]\n"
      "v_mfma_f32_16x16x32_bf16 v[198:201], a[92:95], v[224:227], v[198:201]\n"
      "v_mfma_f32_16x16x32_bf16 v[198:201], a[128:131], v[208:211], v[198:201]\n"
      "v_mfma_f32_16x16x32_bf16 v[188:191], a[144:147], v[176:179], v[188:191]\n"
      "v_mfma_f32_16x16x32_bf16 v[188:191], a[180:183], v[172:175], v[188:191]\n"
      "v_mfma_f32_16x16x32_bf16 v[228:231], a[148:151], v[176:179], v[228:231]\n"
      "v_mfma_f32_16x16x32_bf16 v[228:231], a[184:187], v[172:175], v[228:231]\n"
      "v_mfma_f32_16x16x32_bf16 v[144:147], a[152:155], v[176:179], v[144:147]\n"
      "v_mfma_f32_16x16x32_bf16 v[144:147], a[188:191], v[172:175], v[144:147]\n"
      "v_mfma_f32_16x16x32_bf16 v[212:215], a[156:159], v[176:179], v[212:215]\n"
      "v_mfma_f32_16x16x32_bf16 v[212:215], a[192:195], v[172:175], v[212:215]\n"
      "s_nop 1\n"
      "v_fma_f32 v196, v188, v188, s38\n"
      "v_mfma_f32_16x16x32_bf16 v[252:255], a[160:163], v[176:179], v[252:255]\n"
      "v_fmac_f32 v196, v189, v189\n"
      "v_fmac_f32 v196, v190, v190\n"
      "v_mfma_f32_16x16x32_bf16 v[252:255], a[196:199], v[172:175], v[252:255]\n"
      "s_nop 0\n"
      "v_fma_f32 v232, v212, v212, s38\n"
      "v_fmac_f32_e64 v232, v213, v213\n"
      "v_mfma_f32_16x16x32_bf16 v[198:201], a[164:167], v[176:179], v[198:201]\n"
      "v_fmac_f32 v232, v214, v214\n"
      "v_fmac_f32 v196, v191, v191\n"
      "v_mfma_f32_16x16x32_bf16 v[198:201], a[200:203], v[172:175], v[198:201]\n"
      "v_fmac_f32 v196, v228, v228\n"
      "v_fmac_f32 v232, v215, v215\n"
      "v_mfma_f32_16x16x32_f16 v[148:151], v[180:183], v[160:163], v[70:73]\n"
      "v_fmac_f32 v196, v229, v229\n"
      "v_fmac_f32 v196, v230, v230\n"
      "v_mfma_f32_16x16x32_f16 v[148:151], v[168:171], v[184:187], v[148:151]\n"
      "v_fmac_f32 v232, v252, v252\n"
      "v_fmac_f32 v232, v253, v253\n"
      "v_mfma_f32_16x16x32_f16 v[148:151], v[204:207], v[32:35], v[148:151]\n"
      "v_fmac_f32 v196, v231, v231\n"
      "v_fmac_f32 v232, v254, v254\n"
      "v_fmac_f32 v196, v144, v144\n"
      "v_fmac_f32 v196, v145, v145\n"
      "v_fmac_f32 v232, v255, v255\n"
      "v_fmac_f32 v232, v198, v198\n"
      "v_fmac_f32 v232, v199, v199\n"
      "v_fmac_f32 v232, v200, v200\n"
      "v_fmac_f32 v196, v146, v146\n"
      "v_fmac_f32 v196, v147, v147\n"
      "v_fmac_f32 v232, v201, v201\n"
      "v_mov_b32 v235, v149\n"
      "v_mov_b32 v234, v148\n"
      "v_permlane16_swap_b32 v196, v232\n"
      "v_add_f32 v196, v196, v232\n"
      "v_mov_b32 v232, v196\n"
      "s_nop 1\n"
      "v_permlane32_swap_b32 v196, v232\n"
      "v_add_f32 v196, v196, v232\n"
      "v_rsq_f32 v196, v196\n"
      "s_nop 0\n"
      "v_mov_b32 v232, v196\n"
      "s_nop 1\n"
      "v_permlane16_swap_b32 v196, v232\n"
      "v_mul_f32 v212, v232, v212\n"
      "v_mul_f32 v215, v232, v215\n"
      "v_mul_f32 v213, v232, v213\n"
      "v_mul_f32 v253, v232, v253\n"
      "v_mul_f32 v214, v232, v214\n"
      "v_mul_f32 v199, v232, v199\n"
      "v_mul_f32 v198, v232, v198\n"
      "v_mul_f32 v252, v232, v252\n"
      "v_mul_f32_e64 v196, s36, v196\n"
      "v_cvt_pk_f16_f32 v212, v212, v213\n"
      "v_cvt_pk_f16_f32 v213, v214, v215\n"
      "v_cvt_pk_f16_f32 v46, v198, v199\n"
      "v_mul_f32 v228, v196, v228\n"
      "v_mul_f32 v254, v232, v254\n"
      "v_mul_f32 v200, v232, v200\n"
      "v_mul_f32 v188, v196, v188\n"
      "v_mul_f32 v231, v196, v231\n"
      "v_mul_f32 v201, v232, v201\n"
      "v_mul_f32 v255, v232, v255\n"
      "v_cvt_pk_f16_f32 v47, v200, v201\n"
      "v_cvt_pk_f16_f32 v215, v254, v255\n"
      "v_mul_f32 v144, v196, v144\n"
      "v_mul_f32_e64 v190, v196, v190\n"
      "v_cvt_pk_f16_f32 v214, v252, v253\n"
      "v_mul_f32 v230, v196, v230\n"
      "v_mul_f32 v229, v196, v229\n"
      "v_mul_f32 v191, v196, v191\n"
      "v_mfma_f32_16x16x32_f16 v[252:255], v[212:215], v[160:163], v[86:89]\n"
      "v_mul_f32 v189, v196, v189\n"
      "v_cvt_pk_f16_f32 v188, v188, v189\n"
      "v_mfma_f32_16x16x32_f16 v[252:255], v[46:49], v[32:35], v[252:255]\n"
      "v_cvt_pk_f16_f32 v189, v190, v191\n"
      "v_cvt_pk_f16_f32 v190, v228, v229\n"
      "v_mul_f32 v147, v196, v147\n"
      "v_cvt_pk_f16_f32 v191, v230, v231\n"
      "v_mfma_f32_16x16x32_f16 v[228:231], v[184:187], v[160:163], v[78:81]\n"
      "v_mul_f32 v145, v196, v145\n"
      "v_cvt_pk_f16_f32 v44, v144, v145\n"
      "v_mfma_f32_16x16x32_f16 v[228:231], v[34:37], v[32:35], v[228:231]\n"
      "v_mul_f32_e64 v146, v196, v146\n"
      "v_cvt_pk_f16_f32 v45, v146, v147\n"
      "v_mfma_f32_16x16x32_f16 v[144:147], v[164:167], v[160:163], v[82:85]\n"
      "v_mfma_f32_16x16x32_f16 v[144:147], v[40:43], v[32:35], v[144:147]\n"
      "s_nop 7\n"
      "s_or_b64 vcc, s[48:49], s[50:51]\n"
      "s_cbranch_scc0 L_fast_31_%=\n"
      "L_ok_146_%=:\n"
      "s_cmp_eq_u64 s[48:49], 0\n"
      "s_cbranch_scc1 L_go_147_%=\n"
      "s_mov_b32 m0, s27\n"
      "s_nop 0\n"
      "s_movrels_b32 s9, s80\n"
      "s_lshr_b32 s8, s9, 8\n"
      "s_and_b32 s9, s9, 0xff\n"
      "s_mul_i32 s10, s8, 73\n"
      "s_add_u32 s10, s10, s9\n"
      "s_mul_i32 s30, s10, 4802\n"
      "s_add_u32 s30, s30, s18\n"
      "s_and_b32 s29, s30, 15\n"
      "s_add_u32 s30, s29, 110592\n"
      "s_lshl_b64 s[34:35], s[48:49], 4\n"
      "s_lshr_b64 s[34:35], s[34:35], 5\n"
      "s_cbranch_scc1 L_gen_29_%=\n"
      "v_mbcnt_lo_u32_b32 v232, -1, 0\n"
      "v_mbcnt_hi_u32_b32 v232, -1, v232\n"
      "v_lshlrev_b32 v233, 1, v232\n"
      "v_add_u32 v196, 4802, v233\n"
      "v_cmp_gt_u32 vcc, s29, v233\n"
      "s_nop 1\n"
      "v_cndmask_b32 v233, v196, v233, vcc\n"
      "v_mov_b32 v196, 0\n"
      "v_add_u32 v233, 110592, v233\n"
      "s_mov_b64 exec, 0x7f\n"
      "ds_write_b16 v233, v196\n"
      "s_mov_b64 exec, -1\n"
      "v_lshlrev_b32 v233, 4, v232\n"
      "v_add_u32 v233, 110592, v233\n"
      "ds_read_b128 v[148:151], v233\n"
      "ds_read_b128 v[236:239], v233 offset:1024\n"
      "v_min_u32 v232, 111296, v233\n"
      "ds_read_b128 v[224:227], v233 offset:2048\n"
      "ds_read_b128 v[160:163], v232 offset:4096\n"
      "ds_read_b128 v[208:211], v233 offset:3072\n"
      "s_waitcnt lgkmcnt(4)\n"
      "v_or3_b32 v148, v148, v149, v150\n"
      "s_waitcnt lgkmcnt(3)\n"
      "v_or3_b32 v148, v148, v151, v236\n"
      "v_or3_b32 v148, v148, v237, v238\n"
      "s_waitcnt lgkmcnt(2)\n"
      "v_or3_b32 v148, v148, v239, v224\n"
      "v_or3_b32 v148, v148, v225, v226\n"
      "s_waitcnt lgkmcnt(0)\n"
      "v_or3_b32 v148, v148, v227, v208\n"
      "v_or3_b32 v148, v148, v209, v210\n"
      "v_or3_b32 v148, v148, v211, v160\n"
      "v_or3_b32 v148, v148, v161, v162\n"
      "v_bitop3_b32 v148, v148, v163, s40 bitop3:0xa8\n"
      "v_cmp_ne_u32 s[48:49], 0, v148\n"
      "s_cmp_lg_u64 s[48:49], 0\n"
      "s_cbranch_scc1 L_gen_29_%=\n"
      "s_cmp_eq_u64 s[50:51], 0\n"
      "s_cbranch_scc1 L_fast_31_%=\n"
      "L_go_147_%=:\n"
      "v_mfma_f32_16x16x32_f16 v[148:151], v[184:187], v[140:143], v[90:93]\n"
      "ds_read_b128 v[236:239], v128 offset:3072\n"
      "v_max3_f32 v233, v228, v229, v230\n"
      "v_mfma_f32_16x16x32_f16 v[148:151], v[34:37], v[38:41], v[148:151]\n"
      "v_max3_f32 v232, v231, v144, v145\n"
      "v_max3_f32 v232, v232, v252, v253\n"
      "v_mfma_f32_16x16x32_f16 v[224:227], v[164:167], v[140:143], v[94:97]\n"
      "v_max3_f32 v233, v233, v146, v147\n"
      "v_max3_f32 v233, v233, v254, v255\n"
      "v_mfma_f32_16x16x32_f16 v[224:227], v[40:43], v[38:41], v[224:227]\n"
      "v_max3_f32 v233, v233, v232, v234\n"
      "v_mov_b32_e64 v232, v233\n"
      "v_mfma_f32_16x16x32_f16 v[208:211], v[180:183], v[140:143], v[72:75]\n"
      "s_nop 0\n"
      "v_permlane16_swap_b32 v233, v232\n"
      "v_mfma_f32_16x16x32_f16 v[208:211], v[168:171], v[164:167], v[208:211]\n"
      "v_max_f32 v233, v233, v232\n"
      "v_mov_b32 v232, v233\n"
      "v_mfma_f32_16x16x32_f16 v[208:211], v[204:207], v[38:41], v[208:211]\n"
      "s_nop 0\n"
      "v_permlane32_swap_b32 v233, v232\n"
      "v_max_f32 v233, v233, v232\n"
      "v_sub_f32 v231, v231, v233\n"
      "v_sub_f32 v229, v229, v233\n"
      "v_sub_f32 v230, v230, v233\n"
      "v_mfma_f32_16x16x32_f16 v[160:163], v[212:215], v[140:143], v[98:101]\n"
      "v_sub_f32 v228, v228, v233\n"
      "v_sub_f32 v254, v254, v233\n"
      "v_mfma_f32_16x16x32_f16 v[160:163], v[46:49], v[38:41], v[160:163]\n"
      "s_waitcnt lgkmcnt(0)\n"
      "v_sub_f32 v252, v252, v233\n"
      "v_sub_f32 v234, v234, v233\n"
      "v_sub_f32 v255, v255, v233\n"
      "v_exp_f32 v255, v255\n"
      "v_sub_f32 v144, v144, v233\n"
      "v_exp_f32 v228, v228\n"
      "v_sub_f32 v147, v147, v233\n"
      "v_sub_f32 v253, v253, v233\n"
      "v_exp_f32 v229, v229\n"
      "v_exp_f32 v252, v252\n"
      "v_exp_f32 v253, v253\n"
      "v_sub_f32 v145, v145, v233\n"
      "v_exp_f32 v144, v144\n"
      "v_exp_f32 v231, v231\n"
      "v_exp_f32 v230, v230\n"
      "v_exp_f32 v145, v145\n"
      "v_sub_f32 v146, v146, v233\n"
      "v_bitop3_b32 v236, v236, v237, s4 bitop3:0x7e\n"
      "v_exp_f32_e64 v146, v146\n"
      "v_exp_f32_e64 v147, v147\n"
      "v_exp_f32_e64 v234, v234\n"
      "v_cvt_pk_bf16_f32 v228, v228, v229\n"
      "v_exp_f32_e64 v254, v254\n"
      "v_cvt_pk_bf16_f32 v229, v230, v231\n"
      "v_cvt_pk_bf16_f32 v62, v252, v253\n"
      "v_cvt_pk_bf16_f32 v231, v146, v147\n"
      "v_cvt_pk_bf16_f32 v63, v254, v255\n"
      "v_cvt_pk_bf16_f32 v230, v144, v145\n"
      "v_cvt_pk_bf16_f32 v64, v234, 0\n"
      "v_bitop3_b32 v238, v238, v239, s4 bitop3:0x7e\n"
      "v_mfma_f32_16x16x32_bf16 v[140:143], v[66:69], v[228:231], 0\n"
      "v_or_b32 v236, v236, v238\n"
      "v_cmp_ne_u32 vcc, 0, v236\n"
      "v_mfma_f32_16x16x32_bf16 v[140:143], v[66:69], v[62:65], v[140:143]\n"
      "s_mov_b64 s[48:49], vcc\n"
      "v_max3_f32 v211, v148, v149, v150\n"
      "v_max3_f32 v210, v151, v224, v225\n"
      "v_mfma_f32_16x16x32_bf16 v[144:147], v[216:219], v[228:231], 0\n"
      "s_cbranch_vccnz L_flg_148_%=\n"
      "L_fb_149_%=:\n"
      "v_mfma_f32_16x16x32_bf16 v[144:147], v[50:53], v[62:65], v[144:147]\n"
      "v_max3_f32 v211, v211, v226, v227\n"
      "v_max3_f32 v210, v210, v160, v161\n"
      "v_mfma_f32_16x16x32_bf16 v[236:239], v[248:251], v[228:231], 0\n"
      "v_max3_f32 v211, v211, v162, v163\n"
      "v_max3_f32 v211, v211, v210, v208\n"
      "v_mfma_f32_16x16x32_bf16 v[236:239], v[54:57], v[62:65], v[236:239]\n"
      "v_mov_b32 v210, v211\n"
      "v_rcp_f32 v140, v140\n"
      "v_mfma_f32_16x16x32_bf16 v[252:255], v[192:195], v[228:231], 0\n"
      "v_permlane16_swap_b32 v211, v210\n"
      "v_mfma_f32_16x16x32_bf16 v[252:255], v[58:61], v[62:65], v[252:255]\n"
      "v_max_f32 v211, v211, v210\n"
      "v_mov_b32 v210, v211\n"
      "v_mul_f32 v145, v140, v145\n"
      "v_mul_f32 v144, v140, v144\n"
      "v_permlane32_swap_b32 v211, v210\n"
      "v_max_f32 v211, v211, v210\n"
      "v_sub_f32 v151, v151, v211\n"
      "v_sub_f32 v148, v148, v211\n"
      "v_sub_f32 v150, v150, v211\n"
      "v_sub_f32 v161, v161, v211\n"
      "v_sub_f32 v149, v149, v211\n"
      "v_sub_f32 v160, v160, v211\n"
      "v_exp_f32 v149, v149\n"
      "v_exp_f32 v151, v151\n"
      "v_exp_f32 v148, v148\n"
      "v_sub_f32 v224, v224, v211\n"
      "v_sub_f32 v226, v226, v211\n"
      "v_mfma_f32_16x16x32_f16 v[228:231], v[184:187], v[188:191], v[102:105]\n"
      "v_sub_f32 v162, v162, v211\n"
      "v_sub_f32 v163, v163, v211\n"
      "v_mfma_f32_16x16x32_f16 v[228:231], v[34:37], v[44:47], v[228:231]\n"
      "v_exp_f32 v150, v150\n"
      "v_exp_f32 v226, v226\n"
      "v_sub_f32 v225, v225, v211\n"
      "v_sub_f32 v227, v227, v211\n"
      "v_exp_f32 v224, v224\n"
      "v_sub_f32 v208, v208, v211\n"
      "v_mfma_f32_16x16x32_f16 v[184:187], v[164:167], v[188:191], v[106:109]\n"
      "ds_read_b128 v[164:167], v120 offset:18432\n"
      "v_cvt_pk_bf16_f32 v148, v148, v149\n"
      "v_mfma_f32_16x16x32_f16 v[184:187], v[40:43], v[44:47], v[184:187]\n"
      "v_exp_f32_e64 v161, v161\n"
      "v_mfma_f32_16x16x32_f16 v[176:179], v[180:183], v[188:191], v[74:77]\n"
      "v_cvt_pk_bf16_f32 v144, v144, v145\n"
      "v_cvt_pk_bf16_f32 v149, v150, v151\n"
      "v_mfma_f32_16x16x32_f16 v[176:179], v[168:171], v[212:215], v[176:179]\n"
      "v_exp_f32 v225, v225\n"
      "v_mfma_f32_16x16x32_f16 v[176:179], v[204:207], v[44:47], v[176:179]\n"
      "v_exp_f32 v162, v162\n"
      "v_cvt_pk_bf16_f32 v150, v224, v225\n"
      "v_mfma_f32_16x16x32_f16 v[222:225], v[212:215], v[188:191], v[110:113]\n"
      "v_exp_f32 v160, v160\n"
      "v_mfma_f32_16x16x32_f16 v[222:225], v[46:49], v[44:47], v[222:225]\n"
      "v_exp_f32 v227, v227\n"
      "v_cvt_pk_bf16_f32 v62, v160, v161\n"
      "v_exp_f32 v163, v163\n"
      "v_cvt_pk_bf16_f32 v151, v226, v227\n"
      "v_mfma_f32_16x16x32_f16 v[188:191], v[168:171], v[180:183], v[76:79]\n"
      "s_waitcnt lgkmcnt(0)\n"
      "v_exp_f32 v208, v208\n"
      "v_mul_f32 v147, v140, v147\n"
      "v_mul_f32 v146, v140, v146\n"
      "v_cvt_pk_bf16_f32 v63, v162, v163\n"
      "v_cvt_pk_bf16_f32 v64, v208, 0\n"
      "v_mul_f32 v255, v140, v255\n"
      "v_mfma_f32_16x16x16_f16 v[188:191], v[206:207], v[204:205], v[188:191]\n"
      "global_store_dwordx4 v117, v[164:167], s[54:55] sc1\n"
      "v_mfma_f32_16x16x32_bf16 v[168:171], v[66:69], v[148:151], 0\n"
      "v_mul_f32_e64 v236, v140, v236\n"
      "v_cvt_pk_bf16_f32 v145, v146, v147\n"
      "v_mfma_f32_16x16x32_bf16 v[168:171], v[66:69], v[62:65], v[168:171]\n"
      "v_mul_f32 v237, v140, v237\n"
      "v_max3_f32 v227, v231, v184, v185\n"
      "v_mul_f32 v252, v140, v252\n"
      "v_mfma_f32_16x16x32_bf16 v[212:215], v[216:219], v[148:151], 0\n"
      "v_mul_f32 v239, v140, v239\n"
      "v_mul_f32 v238, v140, v238\n"
      "v_mfma_f32_16x16x32_bf16 v[212:215], v[50:53], v[62:65], v[212:215]\n"
      "v_cvt_pk_bf16_f32 v147, v238, v239\n"
      "v_rcp_f32 v168, v168\n"
      "v_max3_f32 v226, v228, v229, v230\n"
      "v_mfma_f32_16x16x32_bf16 v[204:207], v[248:251], v[148:151], 0\n"
      "v_mul_f32 v253, v140, v253\n"
      "v_cvt_pk_bf16_f32 v252, v252, v253\n"
      "v_mfma_f32_16x16x32_bf16 v[204:207], v[54:57], v[62:65], v[204:207]\n"
      "v_cvt_pk_bf16_f32 v146, v236, v237\n"
      "v_max3_f32 v226, v226, v186, v187\n"
      "v_max3_f32 v226, v226, v224, v225\n"
      "v_max3_f32 v227, v227, v222, v223\n"
      "v_max3_f32 v226, v226, v227, v176\n"
      "v_mfma_f32_16x16x32_bf16 v[236:239], v[192:195], v[148:151], 0\n"
      "ds_write_b128 v124, v[144:147]\n"
      "v_mov_b32 v227, v226\n"
      "v_mfma_f32_16x16x32_bf16 v[236:239], v[58:61], v[62:65], v[236:239]\n"
      "v_mul_f32 v213, v168, v213\n"
      "v_permlane16_swap_b32 v226, v227\n"
      "v_max_f32 v226, v226, v227\n"
      "v_mov_b32 v227, v226\n"
      "v_mul_f32 v215, v168, v215\n"
      "v_mul_f32 v254, v140, v254\n"
      "v_permlane32_swap_b32 v226, v227\n"
      "v_max_f32 v226, v226, v227\n"
      "v_sub_f32 v224, v224, v226\n"
      "v_sub_f32 v185, v185, v226\n"
      "v_mul_f32 v204, v168, v204\n"
      "v_sub_f32 v186, v186, v226\n"
      "v_sub_f32 v187, v187, v226\n"
      "v_sub_f32 v228, v228, v226\n"
      "v_sub_f32 v184, v184, v226\n"
      "v_mul_f32 v214, v168, v214\n"
      "v_exp_f32 v228, v228\n"
      "v_sub_f32 v230, v230, v226\n"
      "v_sub_f32 v229, v229, v226\n"
      "v_exp_f32 v229, v229\n"
      "v_exp_f32 v185, v185\n"
      "v_sub_f32 v231, v231, v226\n"
      "v_sub_f32 v225, v225, v226\n"
      "v_sub_f32 v222, v222, v226\n"
      "v_exp_f32 v187, v187\n"
      "v_cvt_pk_bf16_f32 v253, v254, v255\n"
      "v_exp_f32 v230, v230\n"
      "v_mul_f32 v207, v168, v207\n"
      "v_exp_f32 v225, v225\n"
      "v_mul_f32 v212, v168, v212\n"
      "v_exp_f32 v222, v222\n"
      "v_exp_f32 v184, v184\n"
      "v_sub_f32 v223, v223, v226\n"
      "v_exp_f32 v186, v186\n"
      "v_sub_f32 v176, v176, v226\n"
      "v_exp_f32 v231, v231\n"
      "v_cvt_pk_bf16_f32 v212, v212, v213\n"
      "v_exp_f32_e64 v176, v176\n"
      "v_cvt_pk_bf16_f32 v228, v228, v229\n"
      "v_exp_f32_e64 v223, v223\n"
      "v_exp_f32_e64 v224, v224\n"
      "v_cvt_pk_bf16_f32 v229, v230, v231\n"
      "v_cvt_pk_bf16_f32 v230, v184, v185\n"
      "v_cvt_pk_bf16_f32 v62, v222, v223\n"
      "v_cvt_pk_bf16_f32 v231, v186, v187\n"
      "v_cvt_pk_bf16_f32 v63, v224, v225\n"
      "v_cvt_pk_bf16_f32 v64, v176, 0\n"
      "v_mfma_f32_16x16x32_bf16 v[180:183], v[66:69], v[228:231], 0\n"
      "ds_read_b128 v[160:163], v123 offset:19456\n"
      "ds_read_b128 v[224:227], v121 offset:18432\n"
      "v_mfma_f32_16x16x32_bf16 v[180:183], v[66:69], v[62:65], v[180:183]\n"
      "ds_read_b128 v[144:147], v123 offset:22528\n"
      "v_cvt_pk_bf16_f32 v213, v214, v215\n"
      "v_mfma_f32_16x16x32_bf16 v[184:187], v[216:219], v[228:231], 0\n"
      "v_mul_f32_e64 v205, v168, v205\n"
      "v_cvt_pk_bf16_f32 v214, v204, v205\n"
      "v_mfma_f32_16x16x32_bf16 v[164:167], v[248:251], v[228:231], 0\n"
      "v_mul_f32_e64 v206, v168, v206\n"
      "v_cvt_pk_bf16_f32 v215, v206, v207\n"
      "v_mfma_f32_16x16x32_bf16 v[164:167], v[54:57], v[62:65], v[164:167]\n"
      "ds_write_b128 v124, v[212:215] offset:1024\n"
      "v_mfma_f32_16x16x32_bf16 v[204:207], v[192:195], v[228:231], 0\n"
      "ds_read_b128 v[228:231], v123 offset:25600\n"
      "v_cndmask_b32 v235, v235, v209, s[42:43]\n"
      "v_mfma_f32_16x16x32_bf16 v[204:207], v[58:61], v[62:65], v[204:207]\n"
      "v_mul_f32_e64 v239, v168, v239\n"
      "v_mul_f32_e64 v236, v168, v236\n"
      "v_cndmask_b32 v235, v235, v177, s[44:45]\n"
      "v_cndmask_b32 v235, v235, v189, s[46:47]\n"
      "v_mul_f32 v238, v168, v238\n"
      "s_nop 0\n"
      "v_max_f32_dpp v254, v235, v235 row_ror:8 row_mask:0xf bank_mask:0xf\n"
      "s_waitcnt lgkmcnt(3)\n"
      "s_nop 0\n"
      "v_max_f32_dpp v254, v254, v254 row_ror:4 row_mask:0xf bank_mask:0xf\n"
      "v_mul_f32_e64 v237, v168, v237\n"
      "v_cvt_pk_bf16_f32 v236, v236, v237\n"
      "v_cvt_pk_bf16_f32 v237, v238, v239\n"
      "v_max_f32_dpp v254, v254, v254 row_ror:2 row_mask:0xf bank_mask:0xf\n"
      "v_mfma_f32_16x16x32_bf16 v[184:187], v[50:53], v[62:65], v[184:187]\n"
      "s_nop 0\n"
      "v_max_f32_dpp v254, v254, v254 row_ror:1 row_mask:0xf bank_mask:0xf\n"
      "s_waitcnt lgkmcnt(2)\n"
      "v_mfma_f32_16x16x32_bf16 v[148:151], a[216:219], v[160:163], 0\n"
      "ds_read_b128 v[188:191], v123 offset:28672\n"
      "v_max_f32_dpp v254, v254, v254 row_bcast:15 row_mask:0xa bank_mask:0xf\n"
      "v_mfma_f32_16x16x32_bf16 v[148:151], a[228:231], v[144:147], v[148:151]\n"
      "ds_write2st64_b64 v125, v[252:253], v[236:237] offset0:0 offset1:2\n"
      "v_mfma_f32_16x16x32_bf16 v[236:239], a[220:223], v[160:163], 0\n"
      "ds_read_b128 v[168:171], v123 offset:31744\n"
      "v_max_f32_dpp v254, v254, v254 row_bcast:31 row_mask:0xc bank_mask:0xf\n"
      "v_mfma_f32_16x16x32_bf16 v[236:239], a[232:235], v[144:147], v[236:239]\n"
      "s_mov_b32 exec_hi, 0\n"
      "global_store_dwordx4 v118, v[224:227], s[54:55] sc1\n"
      "s_mov_b32 exec_hi, -1\n"
      "v_mfma_f32_16x16x32_bf16 v[140:143], a[224:227], v[160:163], 0\n"
      "s_nop 0\n"
      "v_readlane_b32 s31, v254, 63\n"
      "v_mfma_f32_16x16x32_bf16 v[140:143], a[236:239], v[144:147], v[140:143]\n"
      "ds_read_b128 v[224:227], v123 offset:34816\n"
      "v_subrev_f32 v235, s31, v235\n"
      "v_exp_f32 v235, v235\n"
      "s_add_u32 s28, s27, 2\n"
      "v_cvt_pk_bf16_f32 v235, v235, v235\n"
      "s_waitcnt lgkmcnt(3)\n"
      "v_mfma_f32_16x16x32_bf16 v[148:151], a[240:243], v[228:231], v[148:151]\n"
      "s_min_u32 m0, s28, s3\n"
      "ds_read_b128 v[252:255], v123 offset:20480\n"
      "v_mfma_f32_16x16x32_bf16 v[148:151], a[252:255], v[188:191], v[148:151]\n"
      "ds_write_b16 v130, v235\n"
      "v_mfma_f32_16x16x32_bf16 v[236:239], a[244:247], v[228:231], v[236:239]\n"
      "s_movrels_b32 s9, s80\n"
      "s_lshr_b32 s8, s9, 8\n"
      "s_and_b32 s9, s9, 0xff\n"
      "v_mfma_f32_16x16x32_bf16 v[236:239], v[0:3], v[188:191], v[236:239]\n"
      "s_mul_i32 s10, s8, 73\n"
      "s_add_u32 s10, s10, s9\n"
      "v_mfma_f32_16x16x32_bf16 v[140:143], a[248:251], v[228:231], v[140:143]\n"
      "ds_read_b128 v[232:235], v123 offset:23552\n"
      "s_mul_i32 s28, s10, 4802\n"
      "v_mfma_f32_16x16x32_bf16 v[140:143], v[4:7], v[188:191], v[140:143]\n"
      "ds_read_b128 v[160:163], v131\n"
      "s_add_u32 s56, s18, s28\n"
      "s_waitcnt lgkmcnt(4)\n"
      "v_mfma_f32_16x16x32_bf16 v[148:151], v[8:11], v[168:171], v[148:151]\n"
      "s_addc_u32 s57, s19, 0\n"
      "s_and_b32 s56, s56, -16\n"
      "s_sub_u32 s58, s56, 110592\n"
      "v_mfma_f32_16x16x32_bf16 v[148:151], v[20:23], v[224:227], v[148:151]\n"
      "s_subb_u32 s59, s57, 0\n"
      "ds_read_b128 v[208:211], v123 offset:29696\n"
      "s_sub_u32 s56, s56, 73728\n"
      "v_mfma_f32_16x16x32_bf16 v[236:239], v[12:15], v[168:171], v[236:239]\n"
      "s_subb_u32 s57, s57, 0\n"
      "s_mul_i32 s28, s8, 1373568\n"
      "s_mul_i32 s29, s9, 2688\n"
      "v_mfma_f32_16x16x32_bf16 v[236:239], v[24:27], v[224:227], v[236:239]\n"
      "s_add_u32 s28, s28, s29\n"
      "ds_read_b128 v[176:179], v123 offset:26624\n"
      "s_cmp_eq_u32 s8, 72\n"
      "v_mfma_f32_16x16x32_bf16 v[140:143], v[16:19], v[168:171], v[140:143]\n"
      "ds_read_b128 v[212:215], v131 offset:64\n"
      "s_cselect_b32 s8, 0, 100270464\n"
      "v_mfma_f32_16x16x32_bf16 v[140:143], v[28:31], v[224:227], v[140:143]\n"
      "s_cmp_eq_u32 s9, 72\n"
      "s_cselect_b32 s9, 0, 196224\n"
      "s_waitcnt lgkmcnt(4)\n"
      "v_mfma_f32_16x16x32_bf16 v[188:191], a[216:219], v[252:255], 0\n"
      "s_add_u32 s10, s8, s9\n"
      "s_add_u32 s28, s28, s10\n"
      "s_add_u32 s52, s16, s28\n"
      "v_mfma_f32_16x16x32_bf16 v[188:191], a[228:231], v[232:235], v[188:191]\n"
      "ds_read_b128 v[144:147], v115 offset:18432\n"
      "v_fma_f32 v196, v148, v148, s39\n"
      "v_mfma_f32_16x16x32_bf16 v[224:227], a[220:223], v[252:255], 0\n"
      "s_addc_u32 s53, s17, 0\n"
      "s_add_u32 s27, s27, 1\n"
      "s_cmp_ge_u32 s27, s26\n"
      "v_fmac_f32 v196, v149, v149\n"
      "v_fmac_f32 v196, v150, v150\n"
      "v_fmac_f32 v196, v151, v151\n"
      "v_fmac_f32 v196, v236, v236\n"
      "s_cselect_b32 s28, 1, 0\n"
      "v_fmac_f32 v196, v237, v237\n"
      "v_fmac_f32 v196, v238, v238\n"
      "v_mfma_f32_16x16x32_bf16 v[228:231], a[224:227], v[252:255], 0\n"
      "ds_read_b128 v[220:223], v123 offset:35840\n"
      "v_fmac_f32 v196, v239, v239\n"
      "v_mfma_f32_16x16x32_bf16 v[224:227], a[232:235], v[232:235], v[224:227]\n"
      "s_waitcnt lgkmcnt(3)\n"
      "v_fmac_f32 v196, v140, v140\n"
      "v_fmac_f32 v196, v141, v141\n"
      "v_mfma_f32_16x16x32_bf16 v[188:191], a[240:243], v[176:179], v[188:191]\n"
      "ds_read_b128 v[198:201], v123 offset:32768\n"
      "v_fmac_f32 v196, v142, v142\n"
      "v_fmac_f32_e64 v196, v143, v143\n"
      "v_mfma_f32_16x16x32_bf16 v[224:227], a[244:247], v[176:179], v[224:227]\n"
      "s_nop 0\n"
      "v_permlane16_swap_b32 v197, v196\n"
      "ds_read_b128 v[252:255], v115 offset:24576\n"
      "v_mfma_f32_16x16x32_bf16 v[228:231], a[236:239], v[232:235], v[228:231]\n"
      "ds_read_b128 v[168:171], v114 offset:24576\n"
      "s_xor_b32 s29, s10, s11\n"
      "v_mfma_f32_16x16x32_bf16 v[228:231], a[248:251], v[176:179], v[228:231]\n"
      "ds_read_b128 v[232:235], v114 offset:18432\n"
      "v_mfma_f32_16x16x32_bf16 v[228:231], v[4:7], v[208:211], v[228:231]\n"
      "v_mfma_f32_16x16x32_bf16 v[224:227], v[0:3], v[208:211], v[224:227]\n"
      "v_mfma_f32_16x16x32_bf16 v[188:191], a[252:255], v[208:211], v[188:191]\n"
      "s_waitcnt lgkmcnt(3)\n"
      "v_mfma_f32_16x16x32_bf16 v[188:191], v[8:11], v[198:201], v[188:191]\n"
      "v_mfma_f32_16x16x32_bf16 v[188:191], v[20:23], v[220:223], v[188:191]\n"
      "v_add_f32 v197, v197, v196\n"
      "v_mfma_f32_16x16x32_bf16 v[224:227], v[12:15], v[198:201], v[224:227]\n"
      "s_nop 0\n"
      "v_mfma_f32_16x16x32_bf16 v[224:227], v[24:27], v[220:223], v[224:227]\n"
      "s_or_b32 s28, s28, s29\n"
      "s_waitcnt lgkmcnt(1)\n"
      "s_waitcnt lgkmcnt(0)\n"
      "v_mfma_f32_16x16x32_bf16 v[228:231], v[16:19], v[198:201], v[228:231]\n"
      "s_nop 0\n"
      "v_fma_f32 v198, v188, v188, s39\n"
      "v_mfma_f32_16x16x32_bf16 v[228:231], v[28:31], v[220:223], v[228:231]\n"
      "v_fmac_f32 v198, v189, v189\n"
      "v_fmac_f32 v198, v190, v190\n"
      "v_mfma_f32_16x16x32_bf16 v[174:177], v[160:163], v[66:69], 0\n"
      "v_fmac_f32 v198, v191, v191\n"
      "v_mfma_f32_16x16x32_bf16 v[174:177], v[212:215], v[66:69], v[174:177]\n"
      "v_fmac_f32_e64 v198, v224, v224\n"
      "v_mfma_f32_16x16x32_bf16 v[220:223], v[160:163], v[216:219], 0\n"
      "v_fmac_f32 v198, v225, v225\n"
      "v_fmac_f32 v198, v226, v226\n"
      "v_mfma_f32_16x16x32_bf16 v[220:223], v[212:215], v[50:53], v[220:223]\n"
      "v_fmac_f32 v198, v227, v227\n"
      "v_fmac_f32 v198, v228, v228\n"
      "v_mfma_f32_16x16x32_bf16 v[216:219], v[160:163], v[248:251], 0\n"
      "v_fmac_f32 v198, v229, v229\n"
      "v_fmac_f32 v198, v230, v230\n"
      "v_mfma_f32_16x16x32_bf16 v[216:219], v[212:215], v[54:57], v[216:219]\n"
      "v_fmac_f32 v198, v231, v231\n"
      "v_mov_b32 v199, v198\n"
      "v_mfma_f32_16x16x32_bf16 v[176:179], v[160:163], v[192:195], 0\n"
      "s_nop 0\n"
      "v_permlane16_swap_b32 v198, v199\n"
      "v_mfma_f32_16x16x32_bf16 v[176:179], v[212:215], v[58:61], v[176:179]\n"
      "v_mov_b32_dpp v174, v174 quad_perm:[0,0,0,0] row_mask:0xf bank_mask:0xf\n"
      "v_add_f32 v198, v198, v199\n"
      "v_mfma_f32_16x16x32_bf16 v[208:211], a[0:3], v[232:235], 0\n"
      "s_nop 0\n"
      "v_permlane32_swap_b32_e64 v197, v198\n"
      "v_mfma_f32_16x16x32_bf16 v[208:211], a[36:39], v[144:147], v[208:211]\n"
      "v_cndmask_b32 v220, v220, v216, s[42:43]\n"
      "v_mfma_f32_16x16x32_bf16 v[216:219], a[4:7], v[232:235], 0\n"
      "v_add_f32_e64 v197, v197, v198\n"
      "v_cndmask_b32 v220, v220, v176, s[44:45]\n"
      "v_mfma_f32_16x16x32_bf16 v[216:219], a[40:43], v[144:147], v[216:219]\n"
      "ds_write_b32 v129, v197\n"
      "v_mfma_f32_16x16x32_bf16 v[176:179], a[8:11], v[232:235], 0\n"
      "v_rcp_f32_e64 v174, v174\n"
      "v_mfma_f32_16x16x32_bf16 v[176:179], a[44:47], v[144:147], v[176:179]\n"
      "v_mul_f32_e64 v220, v174, v220\n"
      "v_cvt_pk_bf16_f32 v220, v220, v220\n"
      "v_mfma_f32_16x16x32_bf16 v[212:215], a[12:15], v[232:235], 0\n"
      "v_mfma_f32_16x16x32_bf16 v[212:215], a[48:51], v[144:147], v[212:215]\n"
      "ds_write_b16 v135, v220\n"
      "v_mfma_f32_16x16x32_bf16 v[196:199], a[16:19], v[232:235], 0\n"
      "v_mfma_f32_16x16x32_bf16 v[196:199], a[52:55], v[144:147], v[196:199]\n"
      "v_mfma_f32_16x16x32_bf16 v[160:163], a[20:23], v[232:235], 0\n"
      "s_nop 0\n"
      "v_mfma_f32_16x16x32_bf16 v[160:163], a[56:59], v[144:147], v[160:163]\n"
      "s_branch L_join_30_%=\n"
      "L_flg_148_%=:\n"
      "v_cmp_gt_i32 vcc, 0, v236\n"
      "s_cbranch_vccz L_fb_149_%=\n"
      "s_sleep 1\n"
      "ds_read_b128 v[236:239], v128 offset:3072\n"
      "s_waitcnt lgkmcnt(0)\n"
      "v_bitop3_b32 v236, v236, v237, s4 bitop3:0x7e\n"
      "v_bitop3_b32 v238, v238, v239, s4 bitop3:0x7e\n"
      "v_or_b32 v236, v236, v238\n"
      "v_cmp_ne_u32 s[48:49], 0, v236\n"
      "s_branch L_flg_148_%=\n"
      "L_gen_29_%=:\n"
      "s_nop 0\n"
      "s_nop 0\n"
      "v_mbcnt_lo_u32_b32 v196, -1, 0\n"
      "v_mbcnt_hi_u32_b32 v196, -1, v196\n"
      "v_lshrrev_b32 v172, 4, v196\n"
      "v_and_b32 v173, 15, v196\n"
      "v_mul_u32_u24 v200, 0x62, v173\n"
      "v_lshl_add_u32 v200, v172, 3, v200\n"
      "s_movk_i32 s31, 0x3fb9\n"
      "v_add_u32 v200, s30, v200\n"
      "v_and_b32 v196, -4, v200\n"
      "v_lshlrev_b32 v172, 2, v172\n"
      "v_sub_u32_e64 v173, v173, v172\n"
      "v_and_b32_e64 v172, 1, v173\n"
      "v_lshrrev_b32 v173, 1, v173\n"
      "v_cmp_eq_u32 vcc, 0, v173\n"
      "ds_read2_b32 v[232:233], v196 offset0:8 offset1:9\n"
      "ds_read2_b32 v[148:149], v196 offset0:16 offset1:17\n"
      "ds_read2_b32 v[238:239], v196 offset0:18 offset1:24\n"
      "ds_read2_b32 v[150:151], v196 offset0:0 offset1:1\n"
      "ds_read2_b32 v[236:237], v196 offset0:2 offset1:10\n"
      "v_lshlrev_b32_e64 v172, 4, v172\n"
      "v_add_u32 v196, 1568, v196\n"
      "v_mfma_f32_16x16x32_f16 v[244:247], v[180:183], v[140:143], v[72:75]\n"
      "ds_read2_b32 v[198:199], v196 offset0:0 offset1:1\n"
      "ds_read2_b32 v[210:211], v196 offset0:18 offset1:24\n"
      "ds_read2_b32 v[208:209], v196 offset0:2 offset1:10\n"
      "ds_read2_b32 v[224:225], v196 offset0:8 offset1:9\n"
      "ds_read2_b32 v[226:227], v196 offset0:16 offset1:17\n"
      "v_add_u32 v196, 1568, v196\n"
      "v_lshlrev_b32 v172, v172, s31\n"
      "v_cndmask_b32 v158, 0, v172, vcc\n"
      "ds_read2_b32 v[160:161], v196 offset0:16 offset1:17\n"
      "v_mfma_f32_16x16x32_f16 v[220:223], v[164:167], v[140:143], v[94:97]\n"
      "ds_read2_b32 v[178:179], v196 offset0:8 offset1:9\n"
      "v_cmp_eq_u32 vcc, 1, v173\n"
      "s_waitcnt lgkmcnt(11)\n"
      "ds_read2_b32 v[156:157], v196 offset0:18 offset1:24\n"
      "v_and_b32 v200, 2, v200\n"
      "v_cndmask_b32 v159, 0, v172, vcc\n"
      "v_mfma_f32_16x16x32_f16 v[172:175], v[184:187], v[140:143], v[90:93]\n"
      "ds_read2_b32 v[176:177], v196 offset0:0 offset1:1\n"
      "ds_read2_b32 v[162:163], v196 offset0:2 offset1:10\n"
      "v_mfma_f32_16x16x32_f16 v[172:175], v[34:37], v[38:41], v[172:175]\n"
      "s_waitcnt lgkmcnt(10)\n"
      "v_alignbyte_b32 v148, v149, v148, v200\n"
      "v_alignbyte_b32 v150, v151, v150, v200\n"
      "v_alignbyte_b32 v239, v239, v239, v200\n"
      "v_alignbyte_b32 v149, v238, v149, v200\n"
      "v_mfma_f32_16x16x32_f16 v[220:223], v[40:43], v[38:41], v[220:223]\n"
      "s_waitcnt lgkmcnt(7)\n"
      "v_alignbyte_b32 v232, v233, v232, v200\n"
      "v_alignbyte_b32 v151, v236, v151, v200\n"
      "v_lshlrev_b32 v239, 16, v239\n"
      "v_fmac_f32_e64 v234, s37, v239\n"
      "v_alignbyte_b32 v198, v199, v198, v200\n"
      "v_mfma_f32_16x16x32_f16 v[244:247], v[168:171], v[164:167], v[244:247]\n"
      "s_waitcnt lgkmcnt(5)\n"
      "v_alignbyte_b32 v224, v225, v224, v200\n"
      "v_alignbyte_b32 v233, v237, v233, v200\n"
      "v_mfma_f32_16x16x32_f16 v[244:247], v[204:207], v[38:41], v[244:247]\n"
      "s_waitcnt lgkmcnt(4)\n"
      "v_alignbyte_b32 v160, v161, v160, v200\n"
      "v_alignbyte_b32 v226, v227, v226, v200\n"
      "v_mfma_f32_16x16x32_f16 v[236:239], v[212:215], v[140:143], v[98:101]\n"
      "v_alignbyte_b32 v227, v210, v227, v200\n"
      "v_alignbyte_b32 v211, v211, v211, v200\n"
      "v_mfma_f32_16x16x32_f16 v[236:239], v[46:49], v[38:41], v[236:239]\n"
      "v_alignbyte_b32 v225, v209, v225, v200\n"
      "v_lshlrev_b32 v211, 16, v211\n"
      "v_mfma_f32_16x16x16_bf16 v[144:147], v[158:159], v[232:233], v[144:147]\n"
      "s_waitcnt lgkmcnt(0)\n"
      "v_alignbyte_b32 v176, v177, v176, v200\n"
      "v_alignbyte_b32 v177, v162, v177, v200\n"
      "v_mfma_f32_16x16x16_bf16 v[228:231], v[158:159], v[150:151], v[228:231]\n"
      "v_alignbyte_b32 v157, v157, v157, v200\n"
      "v_alignbyte_b32 v178, v179, v178, v200\n"
      "v_mfma_f32_16x16x16_bf16 v[252:255], v[158:159], v[148:149], v[252:255]\n"
      "v_fmac_f32 v244, s37, v211\n"
      "v_alignbyte_b32 v179, v163, v179, v200\n"
      "v_alignbyte_b32 v199, v208, v199, v200\n"
      "v_mfma_f32_16x16x16_bf16 v[220:223], v[158:159], v[224:225], v[220:223]\n"
      "s_nop 0\n"
      "v_max3_f32 v233, v231, v144, v145\n"
      "v_max3_f32 v232, v228, v229, v230\n"
      "v_mfma_f32_16x16x16_bf16 v[172:175], v[158:159], v[198:199], v[172:175]\n"
      "v_max3_f32 v233, v233, v252, v253\n"
      "v_max3_f32 v232, v232, v146, v147\n"
      "v_max3_f32 v232, v232, v254, v255\n"
      "v_max3_f32 v232, v232, v233, v234\n"
      "v_mov_b32 v233, v232\n"
      "v_mfma_f32_16x16x16_bf16 v[236:239], v[158:159], v[226:227], v[236:239]\n"
      "ds_read_b128 v[148:151], v128 offset:3072\n"
      "v_permlane16_swap_b32 v232, v233\n"
      "v_max_f32 v232, v232, v233\n"
      "v_mov_b32 v233, v232\n"
      "v_max3_f32 v198, v175, v220, v221\n"
      "v_lshlrev_b32 v157, 16, v157\n"
      "v_permlane32_swap_b32 v232, v233\n"
      "v_max_f32 v232, v232, v233\n"
      "v_sub_f32 v252, v252, v232\n"
      "v_sub_f32 v231, v231, v232\n"
      "v_sub_f32 v230, v230, v232\n"
      "v_exp_f32 v231, v231\n"
      "v_max3_f32 v199, v172, v173, v174\n"
      "v_sub_f32 v255, v255, v232\n"
      "v_sub_f32 v228, v228, v232\n"
      "v_sub_f32 v147, v147, v232\n"
      "v_exp_f32 v255, v255\n"
      "v_exp_f32 v228, v228\n"
      "v_sub_f32 v144, v144, v232\n"
      "v_sub_f32 v229, v229, v232\n"
      "v_exp_f32 v144, v144\n"
      "v_alignbyte_b32 v161, v156, v161, v200\n"
      "v_exp_f32 v229, v229\n"
      "v_exp_f32_e64 v147, v147\n"
      "v_cvt_pk_bf16_f32 v228, v228, v229\n"
      "v_sub_f32 v253, v253, v232\n"
      "v_sub_f32 v254, v254, v232\n"
      "v_exp_f32 v230, v230\n"
      "v_max3_f32 v199, v199, v222, v223\n"
      "v_sub_f32 v146, v146, v232\n"
      "s_waitcnt lgkmcnt(0)\n"
      "v_exp_f32 v146, v146\n"
      "v_sub_f32 v145, v145, v232\n"
      "v_bitop3_b32 v148, v148, v149, s4 bitop3:0x7e\n"
      "v_exp_f32 v252, v252\n"
      "v_max3_f32 v199, v199, v238, v239\n"
      "v_exp_f32 v253, v253\n"
      "v_cvt_pk_bf16_f32 v229, v230, v231\n"
      "v_sub_f32 v234, v234, v232\n"
      "v_exp_f32 v234, v234\n"
      "v_max3_f32 v198, v198, v236, v237\n"
      "v_exp_f32 v254, v254\n"
      "v_exp_f32 v145, v145\n"
      "v_max3_f32 v199, v199, v198, v244\n"
      "v_mov_b32 v198, v199\n"
      "v_bitop3_b32 v150, v150, v151, s4 bitop3:0x7e\n"
      "v_cvt_pk_bf16_f32 v230, v144, v145\n"
      "v_or_b32 v148, v148, v150\n"
      "v_cmp_ne_u32 vcc, 0, v148\n"
      "v_permlane16_swap_b32 v199, v198\n"
      "v_max_f32 v199, v199, v198\n"
      "v_mov_b32_e64 v198, v199\n"
      "v_cvt_pk_bf16_f32 v62, v252, v253\n"
      "v_cvt_pk_bf16_f32 v231, v146, v147\n"
      "v_permlane32_swap_b32 v199, v198\n"
      "v_max_f32 v199, v199, v198\n"
      "v_sub_f32 v221, v221, v199\n"
      "v_sub_f32 v220, v220, v199\n"
      "v_sub_f32 v174, v174, v199\n"
      "v_sub_f32 v222, v222, v199\n"
      "v_sub_f32 v175, v175, v199\n"
      "v_sub_f32 v173, v173, v199\n"
      "v_cvt_pk_bf16_f32 v64, v234, 0\n"
      "v_sub_f32 v172, v172, v199\n"
      "v_cvt_pk_bf16_f32 v63, v254, v255\n"
      "v_exp_f32 v175, v175\n"
      "v_exp_f32 v172, v172\n"
      "v_sub_f32 v223, v223, v199\n"
      "s_mov_b64 s[48:49], vcc\n"
      "v_sub_f32 v238, v238, v199\n"
      "v_exp_f32 v173, v173\n"
      "v_sub_f32 v236, v236, v199\n"
      "v_exp_f32_e64 v174, v174\n"
      "v_cvt_pk_bf16_f32 v172, v172, v173\n"
      "v_cvt_pk_bf16_f32 v173, v174, v175\n"
      "v_exp_f32 v236, v236\n"
      "v_sub_f32 v244, v244, v199\n"
      "v_sub_f32 v237, v237, v199\n"
      "v_exp_f32 v244, v244\n"
      "v_exp_f32 v220, v220\n"
      "v_sub_f32 v239, v239, v199\n"
      "v_exp_f32 v221, v221\n"
      "v_mfma_f32_16x16x32_bf16 v[140:143], v[66:69], v[228:231], 0\n"
      "v_exp_f32 v223, v223\n"
      "v_mfma_f32_16x16x32_bf16 v[140:143], v[66:69], v[62:65], v[140:143]\n"
      "s_cbranch_vccnz L_flg_160_%=\n"
      "L_fb_161_%=:\n"
      "v_exp_f32 v222, v222\n"
      "v_mfma_f32_16x16x32_bf16 v[148:151], v[216:219], v[228:231], 0\n"
      "v_exp_f32_e64 v237, v237\n"
      "v_mfma_f32_16x16x32_bf16 v[148:151], v[50:53], v[62:65], v[148:151]\n"
      "v_cvt_pk_bf16_f32 v175, v222, v223\n"
      "v_cvt_pk_bf16_f32 v174, v220, v221\n"
      "v_mfma_f32_16x16x32_bf16 v[144:147], v[248:251], v[228:231], 0\n"
      "v_exp_f32_e64 v238, v238\n"
      "v_mfma_f32_16x16x32_bf16 v[144:147], v[54:57], v[62:65], v[144:147]\n"
      "ds_read_b128 v[224:227], v120 offset:18432\n"
      "v_exp_f32 v239, v239\n"
      "v_mfma_f32_16x16x32_bf16 v[252:255], v[192:195], v[228:231], 0\n"
      "v_rcp_f32 v140, v140\n"
      "v_mfma_f32_16x16x32_bf16 v[252:255], v[58:61], v[62:65], v[252:255]\n"
      "v_cvt_pk_bf16_f32 v62, v236, v237\n"
      "v_cvt_pk_bf16_f32 v63, v238, v239\n"
      "v_mfma_f32_16x16x32_f16 v[236:239], v[184:187], v[188:191], v[102:105]\n"
      "v_cvt_pk_bf16_f32 v64, v244, 0\n"
      "v_mul_f32 v148, v140, v148\n"
      "v_mfma_f32_16x16x32_f16 v[236:239], v[34:37], v[44:47], v[236:239]\n"
      "v_mul_f32 v149, v140, v149\n"
      "v_cvt_pk_bf16_f32 v148, v148, v149\n"
      "v_mfma_f32_16x16x32_f16 v[184:187], v[164:167], v[188:191], v[106:109]\n"
      "v_mul_f32 v151, v140, v151\n"
      "v_mul_f32 v150, v140, v150\n"
      "v_mfma_f32_16x16x32_f16 v[184:187], v[40:43], v[44:47], v[184:187]\n"
      "v_mul_f32 v252, v140, v252\n"
      "v_mul_f32 v146, v140, v146\n"
      "v_mfma_f32_16x16x16_bf16 v[236:239], v[158:159], v[176:177], v[236:239]\n"
      "v_cvt_pk_bf16_f32 v149, v150, v151\n"
      "v_mul_f32 v147, v140, v147\n"
      "v_mul_f32 v253, v140, v253\n"
      "v_mfma_f32_16x16x16_bf16 v[184:187], v[158:159], v[178:179], v[184:187]\n"
      "v_mul_f32 v144, v140, v144\n"
      "v_mul_f32 v145, v140, v145\n"
      "v_mfma_f32_16x16x32_f16 v[164:167], v[180:183], v[188:191], v[74:77]\n"
      "v_cvt_pk_bf16_f32 v150, v144, v145\n"
      "v_cvt_pk_bf16_f32 v151, v146, v147\n"
      "v_mfma_f32_16x16x32_f16 v[164:167], v[168:171], v[212:215], v[164:167]\n"
      "ds_write_b128 v124, v[148:151]\n"
      "v_mfma_f32_16x16x32_f16 v[148:151], v[212:215], v[188:191], v[110:113]\n"
      "s_waitcnt lgkmcnt(1)\n"
      "v_mul_f32 v254, v140, v254\n"
      "v_cvt_pk_bf16_f32 v252, v252, v253\n"
      "v_mfma_f32_16x16x32_f16 v[148:151], v[46:49], v[44:47], v[148:151]\n"
      "global_store_dwordx4 v117, v[224:227], s[54:55] sc1\n"
      "v_mfma_f32_16x16x32_f16 v[164:167], v[204:207], v[44:47], v[164:167]\n"
      "v_mul_f32_e64 v255, v140, v255\n"
      "v_mfma_f32_16x16x32_f16 v[140:143], v[168:171], v[180:183], v[76:79]\n"
      "s_nop 1\n"
      "v_mfma_f32_16x16x16_bf16 v[148:151], v[158:159], v[160:161], v[148:151]\n"
      "s_nop 2\n"
      "v_mfma_f32_16x16x16_f16 v[140:143], v[206:207], v[204:205], v[140:143]\n"
      "v_cvt_pk_bf16_f32 v253, v254, v255\n"
      "v_mfma_f32_16x16x32_bf16 v[180:183], v[66:69], v[172:175], 0\n"
      "v_max3_f32 v167, v239, v184, v185\n"
      "v_mfma_f32_16x16x32_bf16 v[180:183], v[66:69], v[62:65], v[180:183]\n"
      "v_mfma_f32_16x16x32_bf16 v[204:207], v[216:219], v[172:175], 0\n"
      "v_max3_f32 v166, v236, v237, v238\n"
      "v_mfma_f32_16x16x32_bf16 v[204:207], v[50:53], v[62:65], v[204:207]\n"
      "v_mfma_f32_16x16x32_bf16 v[188:191], v[248:251], v[172:175], 0\n"
      "v_fmac_f32_e64 v164, s37, v157\n"
      "v_max3_f32 v166, v166, v186, v187\n"
      "v_mfma_f32_16x16x32_bf16 v[188:191], v[54:57], v[62:65], v[188:191]\n"
      "v_max3_f32 v167, v167, v148, v149\n"
      "v_max3_f32 v166, v166, v150, v151\n"
      "v_max3_f32 v166, v166, v167, v164\n"
      "v_mfma_f32_16x16x32_bf16 v[224:227], v[192:195], v[172:175], 0\n"
      "v_rcp_f32 v180, v180\n"
      "v_mfma_f32_16x16x32_bf16 v[224:227], v[58:61], v[62:65], v[224:227]\n"
      "v_mul_f32 v207, v180, v207\n"
      "v_mul_f32 v206, v180, v206\n"
      "v_mul_f32 v191, v180, v191\n"
      "v_mul_f32 v205, v180, v205\n"
      "v_mov_b32 v167, v166\n"
      "v_mul_f32 v189, v180, v189\n"
      "v_mul_f32 v190, v180, v190\n"
      "v_mul_f32 v204, v180, v204\n"
      "v_mul_f32 v224, v180, v224\n"
      "v_cvt_pk_bf16_f32 v204, v204, v205\n"
      "v_cvt_pk_bf16_f32 v205, v206, v207\n"
      "v_cvt_pk_bf16_f32 v207, v190, v191\n"
      "v_mul_f32 v188, v180, v188\n"
      "v_cvt_pk_bf16_f32 v206, v188, v189\n"
      "v_permlane16_swap_b32 v166, v167\n"
      "v_max_f32 v166, v166, v167\n"
      "v_mov_b32 v167, v166\n"
      "v_mbcnt_lo_u32_b32 v196, -1, 0\n"
      "v_mul_f32 v225, v180, v225\n"
      "v_permlane32_swap_b32 v166, v167\n"
      "v_max_f32 v166, v166, v167\n"
      "v_sub_f32 v239, v239, v166\n"
      "v_mul_f32 v227, v180, v227\n"
      "v_sub_f32 v236, v236, v166\n"
      "v_sub_f32 v149, v149, v166\n"
      "ds_read_b128 v[188:191], v121 offset:18432\n"
      "v_cvt_pk_bf16_f32 v224, v224, v225\n"
      "v_mul_f32 v226, v180, v226\n"
      "v_sub_f32 v184, v184, v166\n"
      "v_exp_f32 v239, v239\n"
      "v_sub_f32 v186, v186, v166\n"
      "v_sub_f32 v150, v150, v166\n"
      "v_sub_f32 v237, v237, v166\n"
      "v_sub_f32 v185, v185, v166\n"
      "v_sub_f32 v238, v238, v166\n"
      "v_sub_f32 v187, v187, v166\n"
      "v_exp_f32 v149, v149\n"
      "v_exp_f32 v185, v185\n"
      "v_exp_f32 v186, v186\n"
      "v_exp_f32 v237, v237\n"
      "v_exp_f32 v236, v236\n"
      "v_mbcnt_hi_u32_b32 v196, -1, v196\n"
      "v_exp_f32 v184, v184\n"
      "v_cvt_pk_bf16_f32 v236, v236, v237\n"
      "v_sub_f32 v164, v164, v166\n"
      "v_exp_f32 v238, v238\n"
      "v_exp_f32 v187, v187\n"
      "v_sub_f32 v148, v148, v166\n"
      "v_sub_f32 v151, v151, v166\n"
      "v_cvt_pk_bf16_f32 v237, v238, v239\n"
      "v_exp_f32 v150, v150\n"
      "v_exp_f32 v148, v148\n"
      "v_cndmask_b32 v235, v235, v245, s[42:43]\n"
      "v_cvt_pk_bf16_f32 v239, v186, v187\n"
      "v_exp_f32 v164, v164\n"
      "v_cvt_pk_bf16_f32 v238, v184, v185\n"
      "v_cvt_pk_bf16_f32 v225, v226, v227\n"
      "v_exp_f32 v151, v151\n"
      "v_cvt_pk_bf16_f32 v62, v148, v149\n"
      "v_cvt_pk_bf16_f32 v64, v164, 0\n"
      "v_cvt_pk_bf16_f32 v63, v150, v151\n"
      "v_mfma_f32_16x16x32_bf16 v[180:183], v[66:69], v[236:239], 0\n"
      "ds_read_b128 v[228:231], v123 offset:22528\n"
      "v_cndmask_b32 v235, v235, v165, s[44:45]\n"
      "s_waitcnt lgkmcnt(1)\n"
      "v_mfma_f32_16x16x32_bf16 v[180:183], v[66:69], v[62:65], v[180:183]\n"
      "ds_read_b128 v[144:147], v123 offset:19456\n"
      "v_mfma_f32_16x16x32_bf16 v[184:187], v[216:219], v[236:239], 0\n"
      "ds_read_b128 v[168:171], v123 offset:25600\n"
      "v_min_u32_e64 v196, 48, v196\n"
      "v_mfma_f32_16x16x32_bf16 v[184:187], v[50:53], v[62:65], v[184:187]\n"
      "ds_write_b128 v124, v[204:207] offset:1024\n"
      "v_mfma_f32_16x16x32_bf16 v[164:167], v[248:251], v[236:239], 0\n"
      "ds_read_b128 v[176:179], v123 offset:28672\n"
      "v_cndmask_b32 v235, v235, v141, s[46:47]\n"
      "v_mfma_f32_16x16x32_bf16 v[164:167], v[54:57], v[62:65], v[164:167]\n"
      "ds_write2st64_b64 v125, v[252:253], v[224:225] offset0:0 offset1:2\n"
      "v_mfma_f32_16x16x32_bf16 v[204:207], v[192:195], v[236:239], 0\n"
      "v_lshlrev_b32 v196, 1, v196\n"
      "v_add_u32 v196, s30, v196\n"
      "v_mfma_f32_16x16x32_bf16 v[204:207], v[58:61], v[62:65], v[204:207]\n"
      "s_mov_b32 exec_hi, 0\n"
      "global_store_dwordx4 v118, v[188:191], s[54:55] sc1\n"
      "s_mov_b32 exec_hi, -1\n"
      "s_waitcnt lgkmcnt(4)\n"
      "v_mfma_f32_16x16x32_bf16 v[148:151], a[216:219], v[144:147], 0\n"
      "ds_read_u16 v199, v196 offset:4704\n"
      "s_add_u32 s28, s27, 2\n"
      "v_mfma_f32_16x16x32_bf16 v[148:151], a[228:231], v[228:231], v[148:151]\n"
      "s_min_u32 m0, s28, s3\n"
      "ds_read_b128 v[188:191], v123 offset:31744\n"
      "v_mfma_f32_16x16x32_bf16 v[236:239], a[220:223], v[144:147], 0\n"
      "s_movrels_b32 s9, s80\n"
      "s_waitcnt lgkmcnt(3)\n"
      "v_mfma_f32_16x16x32_bf16 v[140:143], a[224:227], v[144:147], 0\n"
      "s_lshr_b32 s8, s9, 8\n"
      "ds_read_b128 v[144:147], v123 offset:34816\n"
      "v_mfma_f32_16x16x32_bf16 v[140:143], a[236:239], v[228:231], v[140:143]\n"
      "ds_read_b128 v[252:255], v123 offset:20480\n"
      "s_and_b32 s9, s9, 0xff\n"
      "v_mfma_f32_16x16x32_bf16 v[148:151], a[240:243], v[168:171], v[148:151]\n"
      "s_mul_i32 s10, s8, 73\n"
      "s_add_u32 s10, s10, s9\n"
      "s_mul_i32 s28, s10, 4802\n"
      "v_mfma_f32_16x16x32_bf16 v[148:151], a[252:255], v[176:179], v[148:151]\n"
      "v_mfma_f32_16x16x32_bf16 v[236:239], a[232:235], v[228:231], v[236:239]\n"
      "s_add_u32 s56, s18, s28\n"
      "v_mfma_f32_16x16x32_bf16 v[236:239], a[244:247], v[168:171], v[236:239]\n"
      "s_addc_u32 s57, s19, 0\n"
      "s_waitcnt lgkmcnt(3)\n"
      "v_lshlrev_b32_e64 v199, 16, v199\n"
      "v_mfma_f32_16x16x32_bf16 v[236:239], v[0:3], v[176:179], v[236:239]\n"
      "v_fmac_f32 v235, s37, v199\n"
      "s_and_b32 s56, s56, -16\n"
      "v_mfma_f32_16x16x32_bf16 v[140:143], a[248:251], v[168:171], v[140:143]\n"
      "ds_read_b128 v[168:171], v123 offset:23552\n"
      "v_max_f32_dpp v199, v235, v235 row_ror:8 row_mask:0xf bank_mask:0xf\n"
      "v_mfma_f32_16x16x32_bf16 v[140:143], v[4:7], v[176:179], v[140:143]\n"
      "ds_read_b128 v[176:179], v123 offset:26624\n"
      "v_max_f32_dpp v199, v199, v199 row_ror:4 row_mask:0xf bank_mask:0xf\n"
      "s_sub_u32 s58, s56, 110592\n"
      "s_waitcnt lgkmcnt(3)\n"
      "v_max_f32_dpp v199, v199, v199 row_ror:2 row_mask:0xf bank_mask:0xf\n"
      "v_mfma_f32_16x16x32_bf16 v[148:151], v[8:11], v[188:191], v[148:151]\n"
      "s_subb_u32 s59, s57, 0\n"
      "v_max_f32_dpp v199, v199, v199 row_ror:1 row_mask:0xf bank_mask:0xf\n"
      "s_sub_u32 s56, s56, 73728\n"
      "v_mfma_f32_16x16x32_bf16 v[148:151], v[20:23], v[144:147], v[148:151]\n"
      "s_subb_u32 s57, s57, 0\n"
      "s_mul_i32 s28, s8, 1373568\n"
      "v_max_f32_dpp v199, v199, v199 row_bcast:15 row_mask:0xa bank_mask:0xf\n"
      "v_mfma_f32_16x16x32_bf16 v[236:239], v[12:15], v[188:191], v[236:239]\n"
      "s_nop 0\n"
      "v_max_f32_dpp v199, v199, v199 row_bcast:31 row_mask:0xc bank_mask:0xf\n"
      "s_mul_i32 s29, s9, 2688\n"
      "v_mfma_f32_16x16x32_bf16 v[236:239], v[24:27], v[144:147], v[236:239]\n"
      "s_nop 0\n"
      "v_readlane_b32 s31, v199, 63\n"
      "s_add_u32 s28, s28, s29\n"
      "v_mfma_f32_16x16x32_bf16 v[140:143], v[16:19], v[188:191], v[140:143]\n"
      "s_cmp_eq_u32 s8, 72\n"
      "v_subrev_f32 v235, s31, v235\n"
      "s_cselect_b32 s8, 0, 100270464\n"
      "v_mfma_f32_16x16x32_bf16 v[140:143], v[28:31], v[144:147], v[140:143]\n"
      "s_nop 0\n"
      "v_exp_f32 v235, v235\n"
      "s_waitcnt lgkmcnt(1)\n"
      "v_mfma_f32_16x16x32_bf16 v[188:191], a[216:219], v[252:255], 0\n"
      "s_cmp_eq_u32 s9, 72\n"
      "v_cvt_pk_bf16_f32 v235, v235, v235\n"
      "s_cselect_b32 s9, 0, 196224\n"
      "v_mfma_f32_16x16x32_bf16 v[188:191], a[228:231], v[168:171], v[188:191]\n"
      "ds_write_b16 v130, v235\n"
      "v_mfma_f32_16x16x32_bf16 v[228:231], a[224:227], v[252:255], 0\n"
      "ds_read_b128 v[232:235], v123 offset:29696\n"
      "ds_read_b128 v[212:215], v131\n"
      "v_fma_f32 v199, v148, v148, s39\n"
      "v_mfma_f32_16x16x32_bf16 v[224:227], a[220:223], v[252:255], 0\n"
      "ds_read_b128 v[160:163], v131 offset:64\n"
      "v_fmac_f32 v199, v149, v149\n"
      "v_mfma_f32_16x16x32_bf16 v[224:227], a[232:235], v[168:171], v[224:227]\n"
      "ds_read_b128 v[144:147], v115 offset:18432\n"
      "v_fmac_f32 v199, v150, v150\n"
      "v_mfma_f32_16x16x32_bf16 v[228:231], a[236:239], v[168:171], v[228:231]\n"
      "s_add_u32 s10, s8, s9\n"
      "s_add_u32 s28, s28, s10\n"
      "v_fmac_f32 v199, v151, v151\n"
      "v_fmac_f32 v199, v236, v236\n"
      "v_fmac_f32 v199, v237, v237\n"
      "v_fmac_f32 v199, v238, v238\n"
      "v_fmac_f32 v199, v239, v239\n"
      "v_fmac_f32 v199, v140, v140\n"
      "v_fmac_f32 v199, v141, v141\n"
      "v_fmac_f32 v199, v142, v142\n"
      "v_fmac_f32 v199, v143, v143\n"
      "s_add_u32 s52, s16, s28\n"
      "s_addc_u32 s53, s17, 0\n"
      "v_permlane16_swap_b32 v197, v199\n"
      "s_add_u32 s27, s27, 1\n"
      "s_cmp_ge_u32 s27, s26\n"
      "s_cselect_b32 s28, 1, 0\n"
      "s_waitcnt lgkmcnt(3)\n"
      "v_mfma_f32_16x16x32_bf16 v[188:191], a[240:243], v[176:179], v[188:191]\n"
      "ds_read_b128 v[208:211], v123 offset:32768\n"
      "v_add_f32_e64 v197, v197, v199\n"
      "v_mfma_f32_16x16x32_bf16 v[188:191], a[252:255], v[232:235], v[188:191]\n"
      "ds_read_b128 v[198:201], v123 offset:35840\n"
      "v_mfma_f32_16x16x32_bf16 v[224:227], a[244:247], v[176:179], v[224:227]\n"
      "ds_read_b128 v[252:255], v115 offset:24576\n"
      "v_mfma_f32_16x16x32_bf16 v[224:227], v[0:3], v[232:235], v[224:227]\n"
      "ds_read_b128 v[168:171], v114 offset:24576\n"
      "v_mfma_f32_16x16x32_bf16 v[228:231], a[248:251], v[176:179], v[228:231]\n"
      "v_mfma_f32_16x16x32_bf16 v[228:231], v[4:7], v[232:235], v[228:231]\n"
      "ds_read_b128 v[232:235], v114 offset:18432\n"
      "s_waitcnt lgkmcnt(3)\n"
      "v_mfma_f32_16x16x32_bf16 v[188:191], v[8:11], v[208:211], v[188:191]\n"
      "s_xor_b32 s29, s10, s11\n"
      "s_or_b32 s28, s28, s29\n"
      "v_mfma_f32_16x16x32_bf16 v[188:191], v[20:23], v[198:201], v[188:191]\n"
      "v_mfma_f32_16x16x32_bf16 v[224:227], v[12:15], v[208:211], v[224:227]\n"
      "s_waitcnt lgkmcnt(1)\n"
      "v_mfma_f32_16x16x32_bf16 v[224:227], v[24:27], v[198:201], v[224:227]\n"
      "v_mfma_f32_16x16x32_bf16 v[228:231], v[16:19], v[208:211], v[228:231]\n"
      "s_waitcnt lgkmcnt(0)\n"
      "v_mfma_f32_16x16x32_bf16 v[228:231], v[28:31], v[198:201], v[228:231]\n"
      "s_nop 1\n"
      "v_fma_f32 v196, v188, v188, s39\n"
      "v_mfma_f32_16x16x32_bf16 v[198:201], v[212:215], v[66:69], 0\n"
      "v_fmac_f32 v196, v189, v189\n"
      "v_fmac_f32 v196, v190, v190\n"
      "v_mfma_f32_16x16x32_bf16 v[198:201], v[160:163], v[66:69], v[198:201]\n"
      "v_fmac_f32 v196, v191, v191\n"
      "v_fmac_f32 v196, v224, v224\n"
      "v_mfma_f32_16x16x32_bf16 v[174:177], v[212:215], v[216:219], 0\n"
      "v_fmac_f32 v196, v225, v225\n"
      "v_fmac_f32 v196, v226, v226\n"
      "v_mfma_f32_16x16x32_bf16 v[174:177], v[160:163], v[50:53], v[174:177]\n"
      "v_fmac_f32 v196, v227, v227\n"
      "v_fmac_f32 v196, v228, v228\n"
      "v_mfma_f32_16x16x32_bf16 v[216:219], v[212:215], v[248:251], 0\n"
      "v_fmac_f32 v196, v229, v229\n"
      "v_fmac_f32 v196, v230, v230\n"
      "v_mfma_f32_16x16x32_bf16 v[216:219], v[160:163], v[54:57], v[216:219]\n"
      "v_fmac_f32 v196, v231, v231\n"
      "v_mov_b32 v199, v196\n"
      "v_mfma_f32_16x16x32_bf16 v[248:251], v[212:215], v[192:195], 0\n"
      "s_nop 0\n"
      "v_permlane16_swap_b32 v196, v199\n"
      "v_mfma_f32_16x16x32_bf16 v[248:251], v[160:163], v[58:61], v[248:251]\n"
      "v_add_f32_e64 v196, v196, v199\n"
      "v_mov_b32_dpp v198, v198 quad_perm:[0,0,0,0] row_mask:0xf bank_mask:0xf\n"
      "v_mfma_f32_16x16x32_bf16 v[208:211], a[0:3], v[232:235], 0\n"
      "v_permlane32_swap_b32 v197, v196\n"
      "v_mfma_f32_16x16x32_bf16 v[208:211], a[36:39], v[144:147], v[208:211]\n"
      "v_add_f32 v197, v197, v196\n"
      "v_cndmask_b32 v174, v174, v216, s[42:43]\n"
      "v_mfma_f32_16x16x32_bf16 v[216:219], a[4:7], v[232:235], 0\n"
      "v_cndmask_b32 v174, v174, v248, s[44:45]\n"
      "v_mfma_f32_16x16x32_bf16 v[216:219], a[40:43], v[144:147], v[216:219]\n"
      "ds_write_b32 v129, v197\n"
      "v_mfma_f32_16x16x32_bf16 v[176:179], a[8:11], v[232:235], 0\n"
      "v_rcp_f32_e64 v198, v198\n"
      "v_mfma_f32_16x16x32_bf16 v[176:179], a[44:47], v[144:147], v[176:179]\n"
      "v_mul_f32_e64 v174, v198, v174\n"
      "v_mfma_f32_16x16x32_bf16 v[212:215], a[12:15], v[232:235], 0\n"
      "v_cvt_pk_bf16_f32 v174, v174, v174\n"
      "v_mfma_f32_16x16x32_bf16 v[212:215], a[48:51], v[144:147], v[212:215]\n"
      "ds_write_b16 v135, v174\n"
      "v_mfma_f32_16x16x32_bf16 v[196:199], a[16:19], v[232:235], 0\n"
      "v_mfma_f32_16x16x32_bf16 v[196:199], a[52:55], v[144:147], v[196:199]\n"
      "v_mfma_f32_16x16x32_bf16 v[160:163], a[20:23], v[232:235], 0\n"
      "v_mfma_f32_16x16x32_bf16 v[160:163], a[56:59], v[144:147], v[160:163]\n"
      "s_branch L_join_30_%=\n"
      "L_flg_160_%=:\n"
      "v_cmp_gt_i32 vcc, 0, v148\n"
      "s_cbranch_vccz L_fb_161_%=\n"
      "s_sleep 1\n"
      "ds_read_b128 v[148:151], v128 offset:3072\n"
      "s_waitcnt lgkmcnt(0)\n"
      "v_bitop3_b32 v148, v148, v149, s4 bitop3:0x7e\n"
      "v_bitop3_b32 v150, v150, v151, s4 bitop3:0x7e\n"
      "v_or_b32 v148, v148, v150\n"
      "v_cmp_ne_u32 s[48:49], 0, v148\n"
      "s_branch L_flg_160_%=\n"
      "L_edge_1_%=:\n"
      "s_mov_b32 s11, s10\n"
      "s_sub_u32 s28, s60, s8\n"
      "s_sub_u32 s29, s61, s9\n"
      "s_mov_b32 s60, s8\n"
      "s_mov_b32 s61, s9\n"
      "s_mov_b64 exec, s[64:65]\n"
      "v_add_u32 v116, s28, v116\n"
      "s_mov_b64 exec, s[70:71]\n"
      "v_add_u32 v116, s29, v116\n"
      "s_mov_b64 exec, s[66:67]\n"
      "v_add_u32 v117, s28, v117\n"
      "s_mov_b64 exec, s[72:73]\n"
      "v_add_u32 v117, s29, v117\n"
      "s_mov_b64 exec, s[68:69]\n"
      "v_add_u32 v118, s28, v118\n"
      "s_mov_b64 exec, s[74:75]\n"
      "v_add_u32 v118, s29, v118\n"
      "s_mov_b64 exec, -1\n"
      "s_nop 4\n"
      "s_branch L_eb_2_%=\n"
      "L_flg_3_%=:\n"
      "v_cmp_gt_i32 vcc, 0, v180\n"
      "s_cbranch_vccz L_fb_4_%=\n"
      "s_sleep 1\n"
      "ds_read_b128 v[180:183], v128 offset:2048\n"
      "s_waitcnt lgkmcnt(0)\n"
      "v_bitop3_b32 v180, v180, v181, s4 bitop3:0x7e\n"
      "v_bitop3_b32 v182, v182, v183, s4 bitop3:0x7e\n"
      "v_or_b32 v180, v180, v182\n"
      "v_cmp_ne_u32 s[48:49], 0, v180\n"
      "s_branch L_flg_3_%=\n"
      "L_edge_6_%=:\n"
      "s_mov_b32 s11, s10\n"
      "s_sub_u32 s28, s60, s8\n"
      "s_sub_u32 s29, s61, s9\n"
      "s_mov_b32 s60, s8\n"
      "s_mov_b32 s61, s9\n"
      "s_mov_b64 exec, s[64:65]\n"
      "v_add_u32 v116, s28, v116\n"
      "s_mov_b64 exec, s[70:71]\n"
      "v_add_u32 v116, s29, v116\n"
      "s_mov_b64 exec, s[66:67]\n"
      "v_add_u32 v117, s28, v117\n"
      "s_mov_b64 exec, s[72:73]\n"
      "v_add_u32 v117, s29, v117\n"
      "s_mov_b64 exec, s[68:69]\n"
      "v_add_u32 v118, s28, v118\n"
      "s_mov_b64 exec, s[74:75]\n"
      "v_add_u32_e64 v118, s29, v118\n"
      "s_mov_b64 exec, -1\n"
      "s_nop 4\n"
      "s_branch L_eb_7_%=\n"
      "L_flg_12_%=:\n"
      "v_cmp_gt_i32 vcc, 0, v176\n"
      "s_cbranch_vccz L_fb_13_%=\n"
      "s_sleep 1\n"
      "ds_read_b128 v[176:179], v128 offset:3072\n"
      "s_waitcnt lgkmcnt(0)\n"
      "v_bitop3_b32 v176, v176, v177, s4 bitop3:0x7e\n"
      "v_bitop3_b32 v178, v178, v179, s4 bitop3:0x7e\n"
      "v_or_b32 v176, v176, v178\n"
      "v_cmp_ne_u32 s[48:49], 0, v176\n"
      "s_branch L_flg_12_%=\n"
      "L_edge_15_%=:\n"
      "s_mov_b32 s11, s10\n"
      "s_sub_u32 s28, s60, s8\n"
      "s_sub_u32 s29, s61, s9\n"
      "s_mov_b32 s60, s8\n"
      "s_mov_b32 s61, s9\n"
      "s_mov_b64 exec, s[64:65]\n"
      "v_add_u32 v116, s28, v116\n"
      "s_mov_b64 exec, s[70:71]\n"
      "v_add_u32 v116, s29, v116\n"
      "s_mov_b64 exec, s[66:67]\n"
      "v_add_u32 v117, s28, v117\n"
      "s_mov_b64 exec, s[72:73]\n"
      "v_add_u32 v117, s29, v117\n"
      "s_mov_b64 exec, s[68:69]\n"
      "v_add_u32 v118, s28, v118\n"
      "s_mov_b64 exec, s[74:75]\n"
      "v_add_u32 v118, s29, v118\n"
      "s_mov_b64 exec, -1\n"
      "s_nop 4\n"
      "s_branch L_eb_16_%=\n"
      "L_edge_17_%=:\n"
      "s_mov_b32 s11, s10\n"
      "s_sub_u32 s28, s60, s8\n"
      "s_sub_u32 s29, s61, s9\n"
      "s_mov_b32 s60, s8\n"
      "s_mov_b32 s61, s9\n"
      "s_mov_b64 exec, s[64:65]\n"
      "v_add_u32 v116, s28, v116\n"
      "s_mov_b64 exec, s[70:71]\n"
      "v_add_u32 v116, s29, v116\n"
      "s_mov_b64 exec, s[66:67]\n"
      "v_add_u32 v117, s28, v117\n"
      "s_mov_b64 exec, s[72:73]\n"
      "v_add_u32 v117, s29, v117\n"
      "s_mov_b64 exec, s[68:69]\n"
      "v_add_u32 v118, s28, v118\n"
      "s_mov_b64 exec, s[74:75]\n"
      "v_add_u32 v118, s29, v118\n"
      "s_mov_b64 exec, -1\n"
      "s_nop 4\n"
      "s_branch L_eb_18_%=\n"
      "L_flg_23_%=:\n"
      "v_cmp_gt_i32 vcc, 0, v208\n"
      "s_cbranch_vccz L_fb_24_%=\n"
      "s_sleep 1\n"
      "ds_read_b128 v[208:211], v128 offset:2048\n"
      "s_waitcnt lgkmcnt(0)\n"
      "v_bitop3_b32 v208, v208, v209, s4 bitop3:0x7e\n"
      "v_bitop3_b32 v210, v210, v211, s4 bitop3:0x7e\n"
      "v_or_b32 v208, v208, v210\n"
      "v_cmp_ne_u32 s[48:49], 0, v208\n"
      "s_branch L_flg_23_%=\n"
      "L_edge_26_%=:\n"
      "s_mov_b32 s11, s10\n"
      "s_sub_u32 s28, s60, s8\n"
      "s_sub_u32 s29, s61, s9\n"
      "s_mov_b32 s60, s8\n"
      "s_mov_b32 s61, s9\n"
      "s_mov_b64 exec, s[64:65]\n"
      "v_add_u32 v116, s28, v116\n"
      "s_mov_b64 exec, s[70:71]\n"
      "v_add_u32 v116, s29, v116\n"
      "s_mov_b64 exec, s[66:67]\n"
      "v_add_u32 v117, s28, v117\n"
      "s_mov_b64 exec, s[72:73]\n"
      "v_add_u32 v117, s29, v117\n"
      "s_mov_b64 exec, s[68:69]\n"
      "v_add_u32 v118, s28, v118\n"
      "s_mov_b64 exec, s[74:75]\n"
      "v_add_u32 v118, s29, v118\n"
      "s_mov_b64 exec, -1\n"
      "s_nop 4\n"
      "s_branch L_eb_27_%=\n"
      "L_flg_32_%=:\n"
      "v_cmp_gt_i32 vcc, 0, v208\n"
      "s_cbranch_vccz L_fb_33_%=\n"
      "s_sleep 1\n"
      "ds_read_b128 v[208:211], v128 offset:3072\n"
      "s_waitcnt lgkmcnt(0)\n"
      "v_bitop3_b32 v208, v208, v209, s4 bitop3:0x7e\n"
      "v_bitop3_b32 v210, v210, v211, s4 bitop3:0x7e\n"
      "v_or_b32 v208, v208, v210\n"
      "v_cmp_ne_u32 s[48:49], 0, v208\n"
      "s_branch L_flg_32_%=\n"
      "L_edge_36_%=:\n"
      "s_mov_b32 s11, s10\n"
      "s_sub_u32 s28, s60, s8\n"
      "s_sub_u32 s29, s61, s9\n"
      "s_mov_b32 s60, s8\n"
      "s_mov_b32 s61, s9\n"
      "s_mov_b64 exec, s[64:65]\n"
      "v_add_u32 v116, s28, v116\n"
      "s_mov_b64 exec, s[70:71]\n"
      "v_add_u32 v116, s29, v116\n"
      "s_mov_b64 exec, s[66:67]\n"
      "v_add_u32 v117, s28, v117\n"
      "s_mov_b64 exec, s[72:73]\n"
      "v_add_u32 v117, s29, v117\n"
      "s_mov_b64 exec, s[68:69]\n"
      "v_add_u32 v118, s28, v118\n"
      "s_mov_b64 exec, s[74:75]\n"
      "v_add_u32 v118, s29, v118\n"
      "s_mov_b64 exec, -1\n"
      "s_nop 4\n"
      "s_branch L_eb_37_%=\n"
      "L_edge_38_%=:\n"
      "s_mov_b32 s11, s10\n"
      "s_sub_u32 s28, s60, s8\n"
      "s_sub_u32 s29, s61, s9\n"
      "s_mov_b32 s60, s8\n"
      "s_mov_b32 s61, s9\n"
      "s_mov_b64 exec, s[64:65]\n"
      "v_add_u32 v116, s28, v116\n"
      "s_mov_b64 exec, s[70:71]\n"
      "v_add_u32 v116, s29, v116\n"
      "s_mov_b64 exec, s[66:67]\n"
      "v_add_u32 v117, s28, v117\n"
      "s_mov_b64 exec, s[72:73]\n"
      "v_add_u32 v117, s29, v117\n"
      "s_mov_b64 exec, s[68:69]\n"
      "v_add_u32 v118, s28, v118\n"
      "s_mov_b64 exec, s[74:75]\n"
      "v_add_u32 v118, s29, v118\n"
      "s_mov_b64 exec, -1\n"
      "s_nop 4\n"
      "s_branch L_eb_39_%=\n"
      "L_edge_40_%=:\n"
      "s_mov_b32 s11, s10\n"
      "s_sub_u32 s28, s60, s8\n"
      "s_sub_u32 s29, s61, s9\n"
      "s_mov_b32 s60, s8\n"
      "s_mov_b32 s61, s9\n"
      "s_mov_b64 exec, s[64:65]\n"
      "v_add_u32 v116, s28, v116\n"
      "s_mov_b64 exec, s[70:71]\n"
      "v_add_u32 v116, s29, v116\n"
      "s_mov_b64 exec, s[66:67]\n"
      "v_add_u32 v117, s28, v117\n"
      "s_mov_b64 exec, s[72:73]\n"
      "v_add_u32 v117, s29, v117\n"
      "s_mov_b64 exec, s[68:69]\n"
      "v_add_u32 v118, s28, v118\n"
      "s_mov_b64 exec, s[74:75]\n"
      "v_add_u32 v118, s29, v118\n"
      "s_mov_b64 exec, -1\n"
      "s_nop 4\n"
      "s_branch L_eb_41_%=\n"
      "L_edge_44_%=:\n"
      "s_mov_b32 s11, s10\n"
      "s_sub_u32 s28, s60, s8\n"
      "s_sub_u32 s29, s61, s9\n"
      "s_mov_b32 s60, s8\n"
      "s_mov_b32 s61, s9\n"
      "s_mov_b64 exec, s[64:65]\n"
      "v_add_u32 v116, s28, v116\n"
      "s_mov_b64 exec, s[70:71]\n"
      "v_add_u32 v116, s29, v116\n"
      "s_mov_b64 exec, s[66:67]\n"
      "v_add_u32 v117, s28, v117\n"
      "s_mov_b64 exec, s[72:73]\n"
      "v_add_u32 v117, s29, v117\n"
      "s_mov_b64 exec, s[68:69]\n"
      "v_add_u32 v118, s28, v118\n"
      "s_mov_b64 exec, s[74:75]\n"
      "v_add_u32 v118, s29, v118\n"
      "s_mov_b64 exec, -1\n"
      "s_nop 4\n"
      "s_branch L_eb_45_%=\n"
      "L_edge_46_%=:\n"
      "s_mov_b32 s11, s10\n"
      "s_sub_u32 s28, s60, s8\n"
      "s_sub_u32 s29, s61, s9\n"
      "s_mov_b32 s60, s8\n"
      "s_mov_b32 s61, s9\n"
      "s_mov_b64 exec, s[64:65]\n"
      "v_add_u32 v116, s28, v116\n"
      "s_mov_b64 exec, s[70:71]\n"
      "v_add_u32 v116, s29, v116\n"
      "s_mov_b64 exec, s[66:67]\n"
      "v_add_u32 v117, s28, v117\n"
      "s_mov_b64 exec, s[72:73]\n"
      "v_add_u32 v117, s29, v117\n"
      "s_mov_b64 exec, s[68:69]\n"
      "v_add_u32 v118, s28, v118\n"
      "s_mov_b64 exec, s[74:75]\n"
      "v_add_u32 v118, s29, v118\n"
      "s_mov_b64 exec, -1\n"
      "s_nop 4\n"
      "s_branch L_eb_47_%=\n"
      "L_edge_48_%=:\n"
      "s_mov_b32 s11, s10\n"
      "s_sub_u32 s28, s60, s8\n"
      "s_sub_u32 s29, s61, s9\n"
      "s_mov_b32 s60, s8\n"
      "s_mov_b32 s61, s9\n"
      "s_mov_b64 exec, s[64:65]\n"
      "v_add_u32 v116, s28, v116\n"
      "s_mov_b64 exec, s[70:71]\n"
      "v_add_u32 v116, s29, v116\n"
      "s_mov_b64 exec, s[66:67]\n"
      "v_add_u32 v117, s28, v117\n"
      "s_mov_b64 exec, s[72:73]\n"
      "v_add_u32 v117, s29, v117\n"
      "s_mov_b64 exec, s[68:69]\n"
      "v_add_u32 v118, s28, v118\n"
      "s_mov_b64 exec, s[74:75]\n"
      "v_add_u32 v118, s29, v118\n"
      "s_mov_b64 exec, -1\n"
      "s_nop 4\n"
      "s_branch L_eb_49_%=\n"
      "L_cend_%=:\n"
      :
      : "s"(__builtin_amdgcn_kernarg_segment_ptr()), "s"(wave), "s"(blockIdx.x), "s"(CT_TAB), "s"(lds0)
      : "v0","v1","v2","v3","v4","v5","v6","v7","v8","v9","v10","v11","v12","v13","v14","v15","v16","v17","v18","v19","v20","v21","v22","v23","v24","v25","v26","v27","v28","v29","v30","v31","v32","v33","v34","v35","v36","v37","v38","v39","v40","v41","v42","v43","v44","v45","v46","v47","v48","v49","v50","v51","v52","v53","v54","v55","v56","v57","v58","v59","v60","v61","v62","v63","v64","v65","v66","v67","v68","v69","v70","v71","v72","v73","v74","v75","v76","v77","v78","v79","v80","v81","v82","v83","v84","v85","v86","v87","v88","v89","v90","v91","v92","v93","v94","v95","v96","v97","v98","v99","v100","v101","v102","v103","v104","v105","v106","v107","v108","v109","v110","v111","v112","v113","v114","v115","v116","v117","v118","v119","v120","v121","v122","v123","v124","v125","v126","v127","v128","v129","v130","v131","v132","v133","v134","v135","v136","v137","v138","v139","v140","v141","v142","v143","v144","v145","v146","v147","v148","v149","v150","v151","v152","v153","v154","v155","v156","v157","v158","v159","v160","v161","v162","v163","v164","v165","v166","v167","v168","v169","v170","v171","v172","v173","v174","v175","v176","v177","v178","v179","v180","v181","v182","v183","v184","v185","v186","v187","v188","v189","v190","v191","v192","v193","v194","v195","v196","v197","v198","v199","v200","v201","v202","v203","v204","v205","v206","v207","v208","v209","v210","v211","v212","v213","v214","v215","v216","v217","v218","v219","v220","v221","v222","v223","v224","v225","v226","v227","v228","v229","v230","v231","v232","v233","v234","v235","v236","v237","v238","v239","v240","v241","v242","v243","v244","v245","v246","v247","v248","v249","v250","v251","v252","v253","v254","v255","a0","a1","a2","a3","a4","a5","a6","a7","a8","a9","a10","a11","a12","a13","a14","a15","a16","a17","a18","a19","a20","a21","a22","a23","a24","a25","a26","a27","a28","a29","a30","a31","a32","a33","a34","a35","a36","a37","a38","a39","a40","a41","a42","a43","a44","a45","a46","a47","a48","a49","a50","a51","a52","a53","a54","a55","a56","a57","a58","a59","a60","a61","a62","a63","a64","a65","a66","a67","a68","a69","a70","a71","a72","a73","a74","a75","a76","a77","a78","a79","a80","a81","a82","a83","a84","a85","a86","a87","a88","a89","a90","a91","a92","a93","a94","a95","a96","a97","a98","a99","a100","a101","a102","a103","a104","a105","a106","a107","a108","a109","a110","a111","a112","a113","a114","a115","a116","a117","a118","a119","a120","a121","a122","a123","a124","a125","a126","a127","a128","a129","a130","a131","a132","a133","a134","a135","a136","a137","a138","a139","a140","a141","a142","a143","a144","a145","a146","a147","a148","a149","a150","a151","a152","a153","a154","a155","a156","a157","a158","a159","a160","a161","a162","a163","a164","a165","a166","a167","a168","a169","a170","a171","a172","a173","a174","a175","a176","a177","a178","a179","a180","a181","a182","a183","a184","a185","a186","a187","a188","a189","a190","a191","a192","a193","a194","a195","a196","a197","a198","a199","a200","a201","a202","a203","a204","a205","a206","a207","a208","a209","a210","a211","a212","a213","a214","a215","a216","a217","a218","a219","a220","a221","a222","a223","a224","a225","a226","a227","a228","a229","a230","a231","a232","a233","a234","a235","a236","a237","a238","a239","a240","a241","a242","a243","a244","a245","a246","a247","a248","a249","a250","a251","a252","a253","a254","a255","s2","s3","s4","s5","s6","s7","s8","s9","s10","s11","s12","s13","s16","s17","s18","s19","s20","s21","s22","s23","s24","s25","s26","s27","s28","s29","s30","s31","s34","s35","s36","s37","s38","s39","s40","s41","s42","s43","s44","s45","s46","s47","s48","s49","s50","s51","s52","s53","s54","s55","s60","s61","s62","s63","s64","s65","s66","s67","s68","s69","s70","s71","s72","s73","s74","s75","s76","s77","s78","s79","s80","s81","s82","s83","s84","s85","s86","s87","s88","s89","s90","s91","s92","s93","s94","s95","s96","s97","s98","s99","s100","s101", "vcc", "scc", "memory");
}

}

extern "C" void kernel_launch(void* const* d_in, const int* in_sizes, int n_in, void* d_out, int out_size, void* d_ws,
                              size_t ws_size, hipStream_t stream) {
  const uint16_t* x = (const uint16_t*)d_in[0];
  const uint16_t* wqkv = (const uint16_t*)d_in[1];
  const uint16_t* wo = (const uint16_t*)d_in[2];
  const uint16_t* ls = (const uint16_t*)d_in[3];
  const uint16_t* w1 = (const uint16_t*)d_in[4];
  const uint16_t* w2 = (const uint16_t*)d_in[5];
  const uint16_t* rc = (const uint16_t*)d_in[6];
  const int* ridx = (const int*)d_in[7];
  const uint16_t* mask = (const uint16_t*)d_in[8];
  swin::swin_main<<<256, 256, 0, stream>>>(x, mask, (char*)d_ws, (uint16_t*)d_out, wqkv, wo, ls, w1, w2, rc, ridx);
}
```

```cpp
#include <hip/hip_runtime.h>
#include <hip/hip_bf16.h>
#include <stdint.h>

namespace swin {

constexpr unsigned LDS_BYTES = 163840u;

__device__ __attribute__((aligned(256))) const unsigned CT_TAB[26704] = {0,64,0,0xc0000,144,208,16,0xc0010,288,352,32,0xc0020,432,496,48,0xc0030,576,512,64,0xc0040,720,656,80,0xc0050,864,800,96,0xc0060,1008,944,112,0xc0070,1024,1088,400,0xc0190,1168,1232,384,0xc0180,1312,1376,432,0xc01b0,1456,1520,416,0xc01a0,1600,1536,464,0xc01d0,1744,1680,448,0xc01c0,1888,1824,496,0xc01f0,2032,1968,480,0xc01e0,16,80,800,0xc0320,128,192,816,0xc0330,304,368,768,0xc0300,416,480,784,0xc0310,592,528,864,0xc0360,704,640,880,0xc0370,880,816,832,0xc0340,992,928,848,0xc0350,1040,1104,1200,0xef8b0,1152,1216,1184,0xef8a0,1328,1392,1168,0xef890,1440,1504,1152,0xef880,1616,1552,1264,0xef8f0,1728,1664,1248,0xef8e0,1904,1840,1232,0xef8d0,2016,1952,1216,0xef8c0,32,96,1600,0xefa40,176,240,1616,0xefa50,256,320,1632,0xefa60,400,464,1648,0xefa70,608,544,1536,0xefa00,752,688,1552,0xefa10,832,768,1568,0xefa20,976,912,1584,0xefa30,1056,1120,2000,0xefbd0,1200,1264,1984,0xefbc0,1280,1344,2032,0xefbf0,1424,1488,2016,0xefbe0,1632,1568,1936,0xefb90,1776,1712,1920,0xefb80,1856,1792,1968,0xefbb0,2000,1936,1952,0xefba0,48,112,2400,0xefd60,160,224,2416,0xefd70,272,336,2368,0xefd40,384,448,2384,0xefd50,624,560,2336,0xefd20,736,672,2352,0xefd30,848,784,2304,0xefd00,960,896,2320,0xefd10,1072,1136,0x2fef0,0xefef0,1184,1248,0x2fee0,0xefee0,1296,1360,0x2fed0,0xefed0,1408,1472,0x2fec0,0xefec0,1648,1584,0x2feb0,0xefeb0,1760,1696,0x2fea0,0xefea0,1872,1808,0x2fe90,0xefe90,1984,1920,0x2fe80,0xefe80,0xc0100,73728,77824,90112,0xc0110,73744,77840,90128,0xc0120,73760,77856,90144,0xc0130,73776,77872,90160,0xc0140,73792,77888,90176,0xc0150,73808,77904,90192,0xc0160,73824,77920,90208,0xc0170,73840,77936,90224,0xc0290,73856,77952,90240,0xc0280,73872,77968,90256,0xc02b0,73888,77984,90272,0xc02a0,73904,78000,90288,0xc02d0,73920,78016,90304,0xc02c0,73936,78032,90320,0xc02f0,73952,78048,90336,0xc02e0,73968,78064,90352,0xc0420,73984,78080,90368,0xc0430,74000,78096,90384,0xc0400,74016,78112,90400,0xc0410,74032,78128,90416,0xc0460,74048,78144,90432,0xc0470,74064,78160,90448,0xc0440,74080,78176,90464,0xc0450,74096,78192,90480,0xef9b0,74112,78208,90496,0xef9a0,74128,78224,90512,0xef990,74144,78240,90528,0xef980,74160,78256,90544,0xef9f0,74176,78272,90560,0xef9e0,74192,78288,90576,0xef9d0,74208,78304,90592,0xef9c0,74224,78320,90608,0xc0100,74240,78336,90624,0xc0110,74256,78352,90640,0xc0120,74272,78368,90656,0xc0130,74288,78384,90672,0xc0140,74304,78400,90688,0xc0150,74320,78416,90704,0xc0160,74336,78432,90720,0xc0170,74352,78448,90736,0xc0290,74368,78464,90752,0xc0280,74384,78480,90768,0xc02b0,74400,78496,90784,0xc02a0,74416,78512,90800,0xc02d0,74432,78528,90816,0xc02c0,74448,78544,90832,0xc02f0,74464,78560,90848,0xc02e0,74480,78576,90864,0xc0420,74496,78592,90880,0xc0430,74512,78608,90896,0xc0400,74528,78624,90912,0xc0410,74544,78640,90928,0xc0460,74560,78656,90944,0xc0470,74576,78672,90960,0xc0440,74592,78688,90976,0xc0450,74608,78704,90992,0xef9b0,74624,78720,91008,0xef9a0,74640,78736,91024,0xef990,74656,78752,91040,0xef980,74672,78768,91056,0xef9f0,74688,78784,91072,0xef9e0,74704,78800,91088,0xef9d0,74720,78816,91104,0xef9c0,74736,78832,91120,0x1c000,36864,36864,49152,0x1c004,36880,36880,49168,0x1c008,36896,36896,49184,0x1c00c,36912,36912,49200,0x1c010,36928,36928,49216,0x1c014,36944,36944,49232,0x1c018,36960,36960,49248,0x1c01c,36976,36976,49264,0x1c020,36992,36992,49280,0x1c024,37008,37008,49296,0x1c028,37024,37024,49312,0x1c02c,37040,37040,49328,0x1c030,37056,37056,49344,0x1c034,37072,37072,49360,0x1c038,37088,37088,49376,0x1c03c,37104,37104,49392,0x1c040,37120,37120,49408,0x1c044,37136,37136,49424,0x1c048,37152,37152,49440,0x1c04c,37168,37168,49456,0x1c050,37184,37184,49472,0x1c054,37200,37200,49488,0x1c058,37216,37216,49504,0x1c05c,37232,37232,49520,0x1c060,37248,37248,49536,0x1c064,37264,37264,49552,0x1c068,37280,37280,49568,0x1c06c,37296,37296,49584,0x1c070,37312,37312,49600,0x1c074,37328,37328,49616,0x1c078,37344,37344,49632,0x1c07c,37360,37360,49648,0x1c080,37376,37376,49664,0x1c084,37392,37392,49680,0x1c088,37408,37408,49696,0x1c08c,37424,37424,49712,0x1c090,37440,37440,49728,0x1c094,37456,37456,49744,0x1c098,37472,37472,49760,0x1c09c,37488,37488,49776,0x1c0a0,37504,37504,49792,0x1c0a4,37520,37520,49808,0x1c0a8,37536,37536,49824,0x1c0ac,37552,37552,49840,0x1c0b0,37568,37568,49856,0x1c0b4,37584,37584,49872,0x1c0b8,37600,37600,49888,0x1c0bc,37616,37616,49904,0x1c0c0,37632,37632,49920,0x1c0c4,37648,37648,49936,0x1c0c8,37664,37664,49952,0x1c0cc,37680,37680,49968,0x1c0d0,37696,37696,49984,0x1c0d4,37712,37712,50000,0x1c0d8,37728,37728,50016,0x1c0dc,37744,37744,50032,0x1c0e0,37760,37760,50048,0x1c0e4,37776,37776,50064,0x1c0e8,37792,37792,50080,0x1c0ec,37808,37808,50096,0x1c0f0,37824,37824,50112,0x1c0f4,37840,37840,50128,0x1c0f8,37856,37856,50144,0x1c0fc,37872,37872,50160,73728,73792,0x1d600,0x1d600,73872,73936,0x1d610,0x1d610,74016,74080,0x1d620,0x1d620,74160,74224,0x1d630,0x1d630,74304,74240,0x1d640,0x1d640,74448,74384,0x1d650,0x1d650,74592,74528,0x1d660,0x1d660,74736,74672,0x1d670,0x1d670,74752,74816,0x1d680,0x1d680,74896,74960,0x1d690,0x1d690,75040,75104,0x1d6a0,0x1d6a0,75184,75248,0x1d6b0,0x1d6b0,75328,75264,0x1d6c0,0x1d6c0,75472,75408,0x1d6d0,0x1d6d0,75616,75552,0x1d6e0,0x1d6e0,75760,75696,0x1d6f0,0x1d6f0,73744,73800,0x1d700,0x1d700,73856,73944,0x1d710,0x1d710,74032,74088,0x1d720,0x1d720,74144,74232,0x1d730,0x1d730,74320,74248,0x1d740,0x1d740,74432,74392,0x1d750,0x1d750,74608,74536,0x1d760,0x1d760,74720,74680,0x1d770,0x1d770,74768,74824,0x1d780,0x1d780,74880,74968,0x1d790,0x1d790,75056,75112,0x1d7a0,0x1d7a0,75168,75256,0x1d7b0,0x1d7b0,75344,75272,0x1d7c0,0x1d7c0,75456,75416,0x1d7d0,0x1d7d0,75632,75560,0x1d7e0,0x1d7e0,75744,75704,0x1d7f0,0x1d7f0,73760,73808,0x1d800,0x1d800,73904,73920,0x1d810,0x1d810,73984,74096,0x1d820,0x1d820,74128,74208,0x1d830,0x1d830,74336,74256,0x1d840,0x1d840,74480,74368,0x1d850,0x1d850,74560,74544,0x1d860,0x1d860,74704,74656,0x1d870,0x1d870,74784,74832,0x1d880,0x1d880,74928,74944,0x1d890,0x1d890,75008,75120,0x1d8a0,0x1d8a0,75152,75232,0x1d8b0,0x1d8b0,75360,75280,0x1d8c0,0x1d8c0,75504,75392,0x1d8d0,0x1d8d0,75584,75568,0x1d8e0,0x1d8e0,75728,75680,0x1d8f0,0x1d8f0,73776,73816,0x1d900,0x1d900,73888,73928,0x1d910,0x1d910,74000,74104,0x1d920,0x1d920,74112,74216,0x1d930,0x1d930,74352,74264,0x1d940,0x1d940,74464,74376,0x1d950,0x1d950,74576,74552,0x1d960,0x1d960,74688,74664,0x1d970,0x1d970,74800,74840,0x1d980,0x1d980,74912,74952,0x1d990,0x1d990,75024,75128,0x1d9a0,0x1d9a0,75136,75240,0x1d9b0,0x1d9b0,75376,75288,0x1d9c0,0x1d9c0,75488,75400,0x1d9d0,0x1d9d0,75600,75576,0x1d9e0,0x1d9e0,75712,75688,0x1d9f0,0x1d9f0,0x1e600,0x1e600,0x1e800,0x100000,0x1e602,0x100000,0x100000,0x1e800,0x1e604,0x100000,0x100000,0x100000,0x1e606,0x100000,0x100000,0x100000,0x1e610,0x1e600,0x100000,0x100000,0x1e612,0x100000,0x100000,0x1e800,0x1e614,0x100000,0x100000,0x100000,0x1e616,0x100000,0x100000,0x100000,0x1e620,0x1e600,0x100000,0x100000,0x1e622,0x100000,0x100000,0x1e800,0x1e624,0x100000,0x100000,0x100000,0x1e626,0x100000,0x100000,0x100000,0x1e630,0x1e600,0x100000,0x100000,0x1e632,0x100000,0x100000,0x1e800,0x1e634,0x100000,0x100000,0x100000,0x1e636,0x100000,0x100000,0x100000,0x1e608,0x1e610,0x1e830,0x100000,0x1e60a,0x100000,0x100000,0x1e830,0x1e60c,0x100000,0x100000,0x100000,0x1e60e,0x100000,0x100000,0x100000,0x1e618,0x1e610,0x100000,0x100000,0x1e61a,0x100000,0x100000,0x1e830,0x1e61c,0x100000,0x100000,0x100000,0x1e61e,0x100000,0x100000,0x100000,0x1e628,0x1e610,0x100000,0x100000,0x1e62a,0x100000,0x100000,0x1e830,0x1e62c,0x100000,0x100000,0x100000,0x1e62e,0x100000,0x100000,0x100000,0x1e638,0x1e610,0x100000,0x100000,0x1e63a,0x100000,0x100000,0x1e830,0x1e63c,0x100000,0x100000,0x100000,0x1e63e,0x100000,0x100000,0x100000,0x1e640,0x1e620,0x1e860,0x100000,0x1e642,0x100000,0x100000,0x1e860,0x1e644,0x100000,0x100000,0x100000,0x1e646,0x100000,0x100000,0x100000,0x1e650,0x1e620,0x100000,0x100000,0x1e652,0x100000,0x100000,0x1e860,0x1e654,0x100000,0x100000,0x100000,0x1e656,0x100000,0x100000,0x100000,0x1e660,0x1e620,0x100000,0x100000,0x1e662,0x100000,0x100000,0x1e860,0x1e664,0x100000,0x100000,0x100000,0x1e666,0x100000,0x100000,0x100000,0x1e670,0x1e620,0x100000,0x100000,0x1e672,0x100000,0x100000,0x1e860,0x1e674,0x100000,0x100000,0x100000,0x1e676,0x100000,0x100000,0x100000,0x1e648,0x1e630,0x1e890,0x100000,0x1e64a,0x100000,0x100000,0x1e890,0x1e64c,0x100000,0x100000,0x100000,0x1e64e,0x100000,0x100000,0x100000,0x1e658,0x1e630,0x100000,0x100000,0x1e65a,0x100000,0x100000,0x1e890,0x1e65c,0x100000,0x100000,0x100000,0x1e65e,0x100000,0x100000,0x100000,0x1e668,0x1e630,0x100000,0x100000,0x1e66a,0x100000,0x100000,0x1e890,0x1e66c,0x100000,0x100000,0x100000,0x1e66e,0x100000,0x100000,0x100000,0x1e678,0x1e630,0x100000,0x100000,0x1e67a,0x100000,0x100000,0x1e890,0x1e67c,0x100000,0x100000,0x100000,0x1e67e,0x100000,0x100000,0x100000,0x1eb00,0x24c70,36864,36928,0x1eb02,0x24c72,36880,36936,0x1eb04,0x24c74,36896,36944,0x1eb06,0x24c76,36912,36952,0x1eb08,0x24dd0,37248,37312,0x1eb0a,0x24dd2,37264,37320,0x1eb0c,0x24dd4,37280,37328,0x1eb0e,0x24dd6,37296,37336,0x1eb10,0x24f30,37664,37728,0x1eb12,0x24f32,37680,37736,0x1eb14,0x24f34,37632,37744,0x1eb16,0x24f36,37648,37752,0x1eb18,0x25090,38048,38112,0x1eb1a,0x25092,38064,38120,0x1eb1c,0x25094,38016,38128,0x1eb1e,0x25096,38032,38136,0x1eb00,0x24c78,38464,38400,0x1eb02,0x24c7a,38480,38408,0x1eb04,0x24c7c,38496,38416,0x1eb06,0x24c7e,38512,38424,0x1eb08,0x24dd8,38848,38784,0x1eb0a,0x24dda,38864,38792,0x1eb0c,0x24ddc,38880,38800,0x1eb0e,0x24dde,38896,38808,0x1eb10,0x24f38,39264,39200,0x1eb12,0x24f3a,39280,39208,0x1eb14,0x24f3c,39232,39216,0x1eb16,0x24f3e,39248,39224,0x1eb18,0x25098,39648,39584,0x1eb1a,0x2509a,39664,39592,0x1eb1c,0x2509c,39616,39600,0x1eb1e,0x2509e,39632,39608,0x1eb00,0x26270,39936,40000,0x1eb02,0x26272,39952,40008,0x1eb04,0x26274,39968,40016,0x1eb06,0x26276,39984,40024,0x1eb08,0x263d0,40320,40384,0x1eb0a,0x263d2,40336,40392,0x1eb0c,0x263d4,40352,40400,0x1eb0e,0x263d6,40368,40408,0x1eb10,0x26530,40736,40800,0x1eb12,0x26532,40752,40808,0x1eb14,0x26534,40704,40816,0x1eb16,0x26536,40720,40824,0x1eb18,0x26690,41120,41184,0x1eb1a,0x26692,41136,41192,0x1eb1c,0x26694,41088,41200,0x1eb1e,0x26696,41104,41208,0x1eb00,0x100000,41536,41472,0x1eb02,0x100000,41552,41480,0x1eb04,0x100000,41568,41488,0x1eb06,0x100000,41584,41496,0x1eb08,0x100000,41920,41856,0x1eb0a,0x100000,41936,41864,0x1eb0c,0x100000,41952,41872,0x1eb0e,0x100000,41968,41880,0x1eb10,0x100000,42336,42272,0x1eb12,0x100000,42352,42280,0x1eb14,0x100000,42304,42288,0x1eb16,0x100000,42320,42296,0x1eb18,0x100000,42720,42656,0x1eb1a,0x100000,42736,42664,0x1eb1c,0x100000,42688,42672,0x1eb1e,0x100000,42704,42680,36864,36896,36928,36864,36872,36904,36936,36880,36880,36912,36944,36896,36888,36920,36952,36912,38016,38048,38080,36928,38024,38056,38088,36944,38032,38064,38096,36960,38040,38072,38104,36976,39200,39168,39264,36992,39208,39176,39272,37008,39216,39184,39280,37024,39224,39192,39288,37040,40352,40320,40416,37056,40360,40328,40424,37072,40368,40336,40432,37088,40376,40344,40440,37104,46144,46176,46080,37120,46152,46184,46088,37136,46160,46192,46096,37152,46168,46200,46104,37168,47296,47328,47232,37184,47304,47336,47240,37200,47312,47344,47248,37216,47320,47352,47256,37232,48480,48448,48416,37248,48488,48456,48424,37264,48496,48464,48432,37280,48504,48472,48440,37296,49632,49600,49568,37312,49640,49608,49576,37328,49648,49616,49584,37344,49656,49624,49592,37360,55296,55328,55360,37376,55304,55336,55368,37392,55312,55344,55376,37408,55320,55352,55384,37424,56448,56480,56512,37440,56456,56488,56520,37456,56464,56496,56528,37472,56472,56504,56536,37488,57632,57600,57696,37504,57640,57608,57704,37520,57648,57616,57712,37536,57656,57624,57720,37552,58784,58752,58848,37568,58792,58760,58856,37584,58800,58768,58864,37600,58808,58776,58872,37616,64576,64608,64512,37632,64584,64616,64520,37648,64592,64624,64528,37664,64600,64632,64536,37680,65728,65760,65664,37696,65736,65768,65672,37712,65744,65776,65680,37728,65752,65784,65688,37744,66912,66880,66848,37760,66920,66888,66856,37776,66928,66896,66864,37792,66936,66904,66872,37808,68064,68032,68000,37824,68072,68040,68008,37840,68080,68048,68016,37856,68088,68056,68024,37872,0x1e800,0x1e800,0x20cc0,0,0x1e8c4,0x1e8c4,0x20cc4,16,0x1e988,0x1e988,0x20cc8,32,0x1ea4c,0x1ea4c,0x20ccc,48,0x1eb10,0x1eb10,0x20cd0,64,0x1ebd4,0x1ebd4,0x20cd4,80,0x1ec98,0x1ec98,0x20cd8,96,0x1ed5c,0x1ed5c,0x20cdc,112,0x1ee20,0x1ee20,0x20ce0,384,0x1eee4,0x1eee4,0x20ce4,400,0x1efa8,0x1efa8,0x20ce8,416,0x1f06c,0x1f06c,0x20cec,432,0x1f130,0x1f130,0x20cf0,448,0x1f1f4,0x1f1f4,0x20cf4,464,0x1f2b8,0x1f2b8,0x20cf8,480,0x1f37c,0x1f37c,0x20cfc,496,0x1e810,0x1e800,0x20cc0,768,0x1e8d4,0x1e8c4,0x20cc4,784,0x1e998,0x1e988,0x20cc8,800,0x1ea5c,0x1ea4c,0x20ccc,816,0x1eb20,0x1eb10,0x20cd0,832,0x1ebe4,0x1ebd4,0x20cd4,848,0x1eca8,0x1ec98,0x20cd8,864,0x1ed6c,0x1ed5c,0x20cdc,880,0x1ee30,0x1ee20,0x20ce0,1152,0x1eef4,0x1eee4,0x20ce4,1168,0x1efb8,0x1efa8,0x20ce8,1184,0x1f07c,0x1f06c,0x20cec,1200,0x1f140,0x1f130,0x20cf0,1216,0x1f204,0x1f1f4,0x20cf4,1232,0x1f2c8,0x1f2b8,0x20cf8,1248,0x1f38c,0x1f37c,0x20cfc,1264,0x1e820,0x1e800,0x20cc0,1536,0x1e8e4,0x1e8c4,0x20cc4,1552,0x1e9a8,0x1e988,0x20cc8,1568,0x1ea6c,0x1ea4c,0x20ccc,1584,0x1eb30,0x1eb10,0x20cd0,1600,0x1ebf4,0x1ebd4,0x20cd4,1616,0x1ecb8,0x1ec98,0x20cd8,1632,0x1ed7c,0x1ed5c,0x20cdc,1648,0x1ee40,0x1ee20,0x20ce0,1920,0x1ef04,0x1eee4,0x20ce4,1936,0x1efc8,0x1efa8,0x20ce8,1952,0x1f08c,0x1f06c,0x20cec,1968,0x1f150,0x1f130,0x20cf0,1984,0x1f214,0x1f1f4,0x20cf4,2000,0x1f2d8,0x1f2b8,0x20cf8,2016,0x1f39c,0x1f37c,0x20cfc,2032,0x1e830,0x1e800,0x20cc0,2304,0x1e8f4,0x1e8c4,0x20cc4,2320,0x1e9b8,0x1e988,0x20cc8,2336,0x1ea7c,0x1ea4c,0x20ccc,2352,0x1eb40,0x1eb10,0x20cd0,2368,0x1ec04,0x1ebd4,0x20cd4,2384,0x1ecc8,0x1ec98,0x20cd8,2400,0x1ed8c,0x1ed5c,0x20cdc,2416,0x1ee50,0x1ee20,0x20ce0,2688,0x1ef14,0x1eee4,0x20ce4,2704,0x1efd8,0x1efa8,0x20ce8,2720,0x1f09c,0x1f06c,0x20cec,2736,0x1f160,0x1f130,0x20cf0,2752,0x1f224,0x1f1f4,0x20cf4,2768,0x1f2e8,0x1f2b8,0x20cf8,2784,0x1f3ac,0x1f37c,0x20cfc,2800,36864,0x26d70,0x1e800,0x1eb00,36880,0x26d74,0x1ec90,0x1ef90,36896,0x26d78,0x1f120,0x1f420,36912,0x26d7c,0x1f5b0,0x1f8b0,36928,0x26d80,0x1fa40,0x1fd40,36944,0x26d84,0x1fed0,0x201d0,36960,0x26d88,0x20360,0x20660,36976,0x26d8c,0x207f0,0x20af0,37248,0x26d90,0x20c80,0x20f80,37264,0x26d94,0x21110,0x21410,37280,0x26d98,0x215a0,0x218a0,37296,0x26d9c,0x21a30,0x21d30,37312,0x26da0,0x21ec0,0x221c0,37328,0x26da4,0x22350,0x22650,37344,0x26da8,0x227e0,0x22ae0,37360,0x26dac,0x22c70,0x22f70,37664,0x26db0,0x1e830,0x1eb08,37680,0x26db4,0x1ecc0,0x1ef98,37632,0x26db8,0x1f150,0x1f428,37648,0x26dbc,0x1f5e0,0x1f8b8,37728,0x26dc0,0x1fa70,0x1fd48,37744,0x26dc4,0x1ff00,0x201d8,37696,0x26dc8,0x20390,0x20668,37712,0x26dcc,0x20820,0x20af8,38048,0x26dd0,0x20cb0,0x20f88,38064,0x26dd4,0x21140,0x21418,38016,0x26dd8,0x215d0,0x218a8,38032,0x26ddc,0x21a60,0x21d38,38112,0x26de0,0x21ef0,0x221c8,38128,0x26de4,0x22380,0x22658,38080,0x26de8,0x22810,0x22ae8,38096,0x26dec,0x22ca0,0x22f78,38464,0x26df0,0x1e860,0x1eb10,38480,0x26df4,0x1ecf0,0x1efa0,38496,0x26df8,0x1f180,0x1f430,38512,0x26dfc,0x1f610,0x1f8c0,38400,0x26e00,0x1faa0,0x1fd50,38416,0x26e04,0x1ff30,0x201e0,38432,0x26e08,0x203c0,0x20670,38448,0x26e0c,0x20850,0x20b00,38848,0x26e10,0x20ce0,0x20f90,38864,0x26e14,0x21170,0x21420,38880,0x26e18,0x21600,0x218b0,38896,0x26e1c,0x21a90,0x21d40,38784,0x26e20,0x21f20,0x221d0,38800,0x26e24,0x223b0,0x22660,38816,0x26e28,0x22840,0x22af0,38832,0x26e2c,0x22cd0,0x22f80,39264,0x26e30,0x1e890,0x1eb18,39280,0x26e34,0x1ed20,0x1efa8,39232,0x26e38,0x1f1b0,0x1f438,39248,0x26e3c,0x1f640,0x1f8c8,39200,0x26e40,0x1fad0,0x1fd58,39216,0x26e44,0x1ff60,0x201e8,39168,0x26e48,0x203f0,0x20678,39184,0x26e4c,0x20880,0x20b08,39648,0x26e50,0x20d10,0x20f98,39664,0x26e54,0x211a0,0x21428,39616,0x26e58,0x21630,0x218b8,39632,0x26e5c,0x21ac0,0x21d48,39584,0x26e60,0x21f50,0x221d8,39600,0x26e64,0x223e0,0x22668,39552,0x26e68,0x22870,0x22af8,39568,0x26e6c,0x22d00,0x22f88,0x3f803f80,0x3f803f80,16256,0,0,0x3f803f80,0x3f800000,0,0,0x3f803f80,0,16256,0,0x3f803f80,0,0x3f800000,0x3f803f80,0,0,0,0,0,0,0,0,0,0,0,0,0,0,0,0x3f803f80,0,0,0,0,0,0,0,0,0,0,0,0,0,0,0,0x3f803f80,0,0,0,0,0,0,0,0,0,0,0,0,0,0,0,0x3f803f80,0,0,0,0,0,0,0,0,0,0,0,0,0,0,0,0x3f803f80,0x3f803f80,16256,0,0,0x3f803f80,0x3f800000,0,0,0x3f803f80,0,16256,0,0x3f803f80,0,0x3f800000,0x3f803f80,0,0,0,0,0,0,0,0,0,0,0,0,0,0,0,0x3f803f80,0,0,0,0,0,0,0,0,0,0,0,0,0,0,0,0x3f803f80,0,0,0,0,0,0,0,0,0,0,0,0,0,0,0,0x3f803f80,0,0,0,0,0,0,0,0,0,0,0,0,0,0,0,0x3f803f80,0x3f803f80,16256,0,0,0x3f803f80,0x3f800000,0,0,0x3f803f80,0,16256,0,0x3f803f80,0,0x3f800000,0x3f803f80,0,0,0,0,0,0,0,0,0,0,0,0,0,0,0,0x3f803f80,0,0,0,0,0,0,0,0,0,0,0,0,0,0,0,0x3f803f80,0,0,0,0,0,0,0,0,0,0,0,0,0,0,0,0x3f803f80,0,0,0,0,0,0,0,0,0,0,0,0,0,0,0,0x3f803f80,0x3f803f80,16256,0,0,0x3f803f80,0x3f800000,0,0,0x3f803f80,0,16256,0,0x3f803f80,0,0x3f800000,0,64,0x30000,0xf0000,144,208,0x30010,0xf0010,288,352,0x30020,0xf0020,432,496,0x30030,0xf0030,576,512,0x30040,0xf0040,720,656,0x30050,0xf0050,864,800,0x30060,0xf0060,1008,944,0x30070,0xf0070,1024,1088,0x30190,0xf0190,1168,1232,0x30180,0xf0180,1312,1376,0x301b0,0xf01b0,1456,1520,0x301a0,0xf01a0,1600,1536,0x301d0,0xf01d0,1744,1680,0x301c0,0xf01c0,1888,1824,0x301f0,0xf01f0,2032,1968,0x301e0,0xf01e0,16,80,0x30320,0x11f720,128,192,0x30330,0x11f730,304,368,0x30300,0x11f700,416,480,0x30310,0x11f710,592,528,0x30360,0x11f760,704,640,0x30370,0x11f770,880,816,0x30340,0x11f740,992,928,0x30350,0x11f750,1040,1104,0x304b0,0x11f8b0,1152,1216,0x304a0,0x11f8a0,1328,1392,0x30490,0x11f890,1440,1504,0x30480,0x11f880,1616,1552,0x304f0,0x11f8f0,1728,1664,0x304e0,0x11f8e0,1904,1840,0x304d0,0x11f8d0,2016,1952,0x304c0,0x11f8c0,32,96,0x30640,0x11fa40,176,240,0x30650,0x11fa50,256,320,0x30660,0x11fa60,400,464,0x30670,0x11fa70,608,544,0x30600,0x11fa00,752,688,0x30610,0x11fa10,832,768,0x30620,0x11fa20,976,912,0x30630,0x11fa30,1056,1120,0x307d0,0x11fbd0,1200,1264,0x307c0,0x11fbc0,1280,1344,0x307f0,0x11fbf0,1424,1488,0x307e0,0x11fbe0,1632,1568,0x30790,0x11fb90,1776,1712,0x30780,0x11fb80,1856,1792,0x307b0,0x11fbb0,2000,1936,0x307a0,0x11fba0,48,112,0x5fd60,0x11fd60,160,224,0x5fd70,0x11fd70,272,336,0x5fd40,0x11fd40,384,448,0x5fd50,0x11fd50,624,560,0x5fd20,0x11fd20,736,672,0x5fd30,0x11fd30,848,784,0x5fd00,0x11fd00,960,896,0x5fd10,0x11fd10,1072,1136,0x5fef0,0x11fef0,1184,1248,0x5fee0,0x11fee0,1296,1360,0x5fed0,0x11fed0,1408,1472,0x5fec0,0x11fec0,1648,1584,0x5feb0,0x11feb0,1760,1696,0x5fea0,0x11fea0,1872,1808,0x5fe90,0x11fe90,1984,1920,0x5fe80,0x11fe80,0xefb40,74752,78848,90624,0xefb50,74768,78864,90640,0xefb60,74784,78880,90656,0xefb70,74800,78896,90672,0xefb00,74816,78912,90688,0xefb10,74832,78928,90704,0xefb20,74848,78944,90720,0xefb30,74864,78960,90736,0xefcd0,74880,78976,90752,0xefcc0,74896,78992,90768,0xefcf0,74912,79008,90784,0xefce0,74928,79024,90800,0xefc90,74944,79040,90816,0xefc80,74960,79056,90832,0xefcb0,74976,79072,90848,0xefca0,74992,79088,90864,0xefe60,75008,79104,90880,0xefe70,75024,79120,90896,0xefe40,75040,79136,90912,0xefe50,75056,79152,90928,0xefe20,75072,79168,90944,0xefe30,75088,79184,90960,0xefe00,75104,79200,90976,0xefe10,75120,79216,90992,0xefff0,75136,79232,91008,0xeffe0,75152,79248,91024,0xeffd0,75168,79264,91040,0xeffc0,75184,79280,91056,0xeffb0,75200,79296,91072,0xeffa0,75216,79312,91088,0xeff90,75232,79328,91104,0xeff80,75248,79344,91120,0xefb40,75264,79360,91136,0xefb50,75280,79376,91152,0xefb60,75296,79392,91168,0xefb70,75312,79408,91184,0xefb00,75328,79424,91200,0xefb10,75344,79440,91216,0xefb20,75360,79456,91232,0xefb30,75376,79472,91248,0xefcd0,75392,79488,91264,0xefcc0,75408,79504,91280,0xefcf0,75424,79520,91296,0xefce0,75440,79536,91312,0xefc90,75456,79552,91328,0xefc80,75472,79568,91344,0xefcb0,75488,79584,91360,0xefca0,75504,79600,91376,0xefe60,75520,79616,91392,0xefe70,75536,79632,91408,0xefe40,75552,79648,91424,0xefe50,75568,79664,91440,0xefe20,75584,79680,91456,0xefe30,75600,79696,91472,0xefe00,75616,79712,91488,0xefe10,75632,79728,91504,0xefff0,75648,79744,91520,0xeffe0,75664,79760,91536,0xeffd0,75680,79776,91552,0xeffc0,75696,79792,91568,0xeffb0,75712,79808,91584,0xeffa0,75728,79824,91600,0xeff90,75744,79840,91616,0xeff80,75760,79856,91632,0x1c0a0,36864,39936,49160,0x1c0a4,36880,39952,49176,0x1c0a8,36896,39968,49192,0x1c0ac,36912,39984,49208,0x1c0b0,36928,40000,49224,0x1c0b4,36944,40016,49240,0x1c0b8,36960,40032,49256,0x1c0bc,36976,40048,49272,0x1c0c0,36992,40064,49288,0x1c0c4,37008,40080,49304,0x1c0c8,37024,40096,49320,0x1c0cc,37040,40112,49336,0x1c0d0,37056,40128,49352,0x1c0d4,37072,40144,49368,0x1c0d8,37088,40160,49384,0x1c0dc,37104,40176,49400,0x1c0e0,37120,40192,49416,0x1c0e4,37136,40208,49432,0x1c0e8,37152,40224,49448,0x1c0ec,37168,40240,49464,0x1c0f0,37184,40256,49480,0x1c0f4,37200,40272,49496,0x1c0f8,37216,40288,49512,0x1c0fc,37232,40304,49528,0x1c100,37248,40320,49544,0x1c104,37264,40336,49560,0x1c108,37280,40352,49576,0x1c10c,37296,40368,49592,0x1c110,37312,40384,49608,0x1c114,37328,40400,49624,0x1c118,37344,40416,49640,0x1c11c,37360,40432,49656,0x1c120,37376,40448,49672,0x1c124,37392,40464,49688,0x1c128,37408,40480,49704,0x1c12c,37424,40496,49720,0x1c130,37440,40512,49736,0x1c134,37456,40528,49752,0x1c138,37472,40544,49768,0x1c13c,37488,40560,49784,0x1c140,37504,40576,49800,0x1c144,37520,40592,49816,0x1c148,37536,40608,49832,0x1c14c,37552,40624,49848,0x1c150,37568,40640,49864,0x1c154,37584,40656,49880,0x1c158,37600,40672,49896,0x1c15c,37616,40688,49912,0x1c160,37632,40704,49928,0x1c164,37648,40720,49944,0x1c168,37664,40736,49960,0x1c16c,37680,40752,49976,0x1c170,37696,40768,49992,0x1c174,37712,40784,50008,0x1c178,37728,40800,50024,0x1c17c,37744,40816,50040,0x1c180,37760,40832,50056,0x1c184,37776,40848,50072,0x1c188,37792,40864,50088,0x1c18c,37808,40880,50104,0x1c190,37824,40896,50120,0x1c194,37840,40912,50136,0x1c198,37856,40928,50152,0x1c19c,37872,40944,50168,73824,79904,0x1d600,0x1d604,73968,80048,0x1d610,0x1d614,74048,80128,0x1d620,0x1d624,74192,80272,0x1d630,0x1d634,74272,80480,0x1d640,0x1d644,74416,80624,0x1d650,0x1d654,74496,80704,0x1d660,0x1d664,74640,80848,0x1d670,0x1d674,74848,80928,0x1d680,0x1d684,74992,81072,0x1d690,0x1d694,75072,81152,0x1d6a0,0x1d6a4,75216,81296,0x1d6b0,0x1d6b4,75296,81504,0x1d6c0,0x1d6c4,75440,81648,0x1d6d0,0x1d6d4,75520,81728,0x1d6e0,0x1d6e4,75664,81872,0x1d6f0,0x1d6f4,73840,79912,0x1d700,0x1d704,73952,80056,0x1d710,0x1d714,74064,80136,0x1d720,0x1d724,74176,80280,0x1d730,0x1d734,74288,80488,0x1d740,0x1d744,74400,80632,0x1d750,0x1d754,74512,80712,0x1d760,0x1d764,74624,80856,0x1d770,0x1d774,74864,80936,0x1d780,0x1d784,74976,81080,0x1d790,0x1d794,75088,81160,0x1d7a0,0x1d7a4,75200,81304,0x1d7b0,0x1d7b4,75312,81512,0x1d7c0,0x1d7c4,75424,81656,0x1d7d0,0x1d7d4,75536,81736,0x1d7e0,0x1d7e4,75648,81880,0x1d7f0,0x1d7f4,79872,79920,0x1d800,0x1d804,80016,80032,0x1d810,0x1d814,80160,80144,0x1d820,0x1d824,80304,80256,0x1d830,0x1d834,80448,80496,0x1d840,0x1d844,80592,80608,0x1d850,0x1d854,80736,80720,0x1d860,0x1d864,80880,80832,0x1d870,0x1d874,80896,80944,0x1d880,0x1d884,81040,81056,0x1d890,0x1d894,81184,81168,0x1d8a0,0x1d8a4,81328,81280,0x1d8b0,0x1d8b4,81472,81520,0x1d8c0,0x1d8c4,81616,81632,0x1d8d0,0x1d8d4,81760,81744,0x1d8e0,0x1d8e4,81904,81856,0x1d8f0,0x1d8f4,79888,79928,0x1d900,0x1d904,80000,80040,0x1d910,0x1d914,80176,80152,0x1d920,0x1d924,80288,80264,0x1d930,0x1d934,80464,80504,0x1d940,0x1d944,80576,80616,0x1d950,0x1d954,80752,80728,0x1d960,0x1d964,80864,80840,0x1d970,0x1d974,80912,80952,0x1d980,0x1d984,81024,81064,0x1d990,0x1d994,81200,81176,0x1d9a0,0x1d9a4,81312,81288,0x1d9b0,0x1d9b4,81488,81528,0x1d9c0,0x1d9c4,81600,81640,0x1d9d0,0x1d9d4,81776,81752,0x1d9e0,0x1d9e4,81888,81864,0x1d9f0,0x1d9f4,0x1e680,0x1e680,0x1e8c0,0x100000,0x1e682,0x100000,0x100000,0x1e8c0,0x1e684,0x100000,0x100000,0x100000,0x1e686,0x100000,0x100000,0x100000,0x1e690,0x1e680,0x100000,0x100000,0x1e692,0x100000,0x100000,0x1e8c0,0x1e694,0x100000,0x100000,0x100000,0x1e696,0x100000,0x100000,0x100000,0x1e6a0,0x1e680,0x100000,0x100000,0x1e6a2,0x100000,0x100000,0x1e8c0,0x1e6a4,0x100000,0x100000,0x100000,0x1e6a6,0x100000,0x100000,0x100000,0x1e6b0,0x1e680,0x100000,0x100000,0x1e6b2,0x100000,0x100000,0x1e8c0,0x1e6b4,0x100000,0x100000,0x100000,0x1e6b6,0x100000,0x100000,0x100000,0x1e688,0x1e690,0x1e8f0,0x100000,0x1e68a,0x100000,0x100000,0x1e8f0,0x1e68c,0x100000,0x100000,0x100000,0x1e68e,0x100000,0x100000,0x100000,0x1e698,0x1e690,0x100000,0x100000,0x1e69a,0x100000,0x100000,0x1e8f0,0x1e69c,0x100000,0x100000,0x100000,0x1e69e,0x100000,0x100000,0x100000,0x1e6a8,0x1e690,0x100000,0x100000,0x1e6aa,0x100000,0x100000,0x1e8f0,0x1e6ac,0x100000,0x100000,0x100000,0x1e6ae,0x100000,0x100000,0x100000,0x1e6b8,0x1e690,0x100000,0x100000,0x1e6ba,0x100000,0x100000,0x1e8f0,0x1e6bc,0x100000,0x100000,0x100000,0x1e6be,0x100000,0x100000,0x100000,0x1e6c0,0x1e6a0,0x1e920,0x100000,0x1e6c2,0x100000,0x100000,0x1e920,0x1e6c4,0x100000,0x100000,0x100000,0x1e6c6,0x100000,0x100000,0x100000,0x1e6d0,0x1e6a0,0x100000,0x100000,0x1e6d2,0x100000,0x100000,0x1e920,0x1e6d4,0x100000,0x100000,0x100000,0x1e6d6,0x100000,0x100000,0x100000,0x1e6e0,0x1e6a0,0x100000,0x100000,0x1e6e2,0x100000,0x100000,0x1e920,0x1e6e4,0x100000,0x100000,0x100000,0x1e6e6,0x100000,0x100000,0x100000,0x1e6f0,0x1e6a0,0x100000,0x100000,0x1e6f2,0x100000,0x100000,0x1e920,0x1e6f4,0x100000,0x100000,0x100000,0x1e6f6,0x100000,0x100000,0x100000,0x1e6c8,0x1e6b0,0x1e950,0x100000,0x1e6ca,0x100000,0x100000,0x1e950,0x1e6cc,0x100000,0x100000,0x100000,0x1e6ce,0x100000,0x100000,0x100000,0x1e6d8,0x1e6b0,0x100000,0x100000,0x1e6da,0x100000,0x100000,0x1e950,0x1e6dc,0x100000,0x100000,0x100000,0x1e6de,0x100000,0x100000,0x100000,0x1e6e8,0x1e6b0,0x100000,0x100000,0x1e6ea,0x100000,0x100000,0x1e950,0x1e6ec,0x100000,0x100000,0x100000,0x1e6ee,0x100000,0x100000,0x100000,0x1e6f8,0x1e6b0,0x100000,0x100000,0x1e6fa,0x100000,0x100000,0x1e950,0x1e6fc,0x100000,0x100000,0x100000,0x1e6fe,0x100000,0x100000,0x100000,0x1eb60,0x251f0,36960,37024,0x1eb62,0x251f2,36976,37032,0x1eb64,0x251f4,36992,37040,0x1eb66,0x251f6,37008,37048,0x1eb68,0x25350,37344,37408,0x1eb6a,0x25352,37360,37416,0x1eb6c,0x25354,37376,37424,0x1eb6e,0x25356,37392,37432,0x1eb70,0x254b0,37696,37760,0x1eb72,0x254b2,37712,37768,0x1eb74,0x254b4,37792,37776,0x1eb76,0x254b6,37808,37784,0x1eb78,0x25610,38080,38144,0x1eb7a,0x25612,38096,38152,0x1eb7c,0x25614,38176,38160,0x1eb7e,0x25616,38192,38168,0x1eb60,0x251f8,38432,38624,0x1eb62,0x251fa,38448,38632,0x1eb64,0x251fc,38592,38640,0x1eb66,0x251fe,38608,38648,0x1eb68,0x25358,38816,39008,0x1eb6a,0x2535a,38832,39016,0x1eb6c,0x2535c,38976,39024,0x1eb6e,0x2535e,38992,39032,0x1eb70,0x254b8,39168,39360,0x1eb72,0x254ba,39184,39368,0x1eb74,0x254bc,39392,39376,0x1eb76,0x254be,39408,39384,0x1eb78,0x25618,39552,39744,0x1eb7a,0x2561a,39568,39752,0x1eb7c,0x2561c,39776,39760,0x1eb7e,0x2561e,39792,39768,0x1eb60,0x26278,40032,40096,0x1eb62,0x2627a,40048,40104,0x1eb64,0x2627c,40064,40112,0x1eb66,0x2627e,40080,40120,0x1eb68,0x263d8,40416,40480,0x1eb6a,0x263da,40432,40488,0x1eb6c,0x263dc,40448,40496,0x1eb6e,0x263de,40464,40504,0x1eb70,0x26538,40768,40832,0x1eb72,0x2653a,40784,40840,0x1eb74,0x2653c,40864,40848,0x1eb76,0x2653e,40880,40856,0x1eb78,0x26698,41152,41216,0x1eb7a,0x2669a,41168,41224,0x1eb7c,0x2669c,41248,41232,0x1eb7e,0x2669e,41264,41240,0x1eb60,0x100000,41504,41696,0x1eb62,0x100000,41520,41704,0x1eb64,0x100000,41664,41712,0x1eb66,0x100000,41680,41720,0x1eb68,0x100000,41888,42080,0x1eb6a,0x100000,41904,42088,0x1eb6c,0x100000,42048,42096,0x1eb6e,0x100000,42064,42104,0x1eb70,0x100000,42240,42432,0x1eb72,0x100000,42256,42440,0x1eb74,0x100000,42464,42448,0x1eb76,0x100000,42480,42456,0x1eb78,0x100000,42624,42816,0x1eb7a,0x100000,42640,42824,0x1eb7c,0x100000,42848,42832,0x1eb7e,0x100000,42864,42840,36960,36992,37024,46080,36968,37000,37032,46096,36976,37008,37040,46112,36984,37016,37048,46128,38112,38144,38176,46144,38120,38152,38184,46160,38128,38160,38192,46176,38136,38168,38200,46192,39232,39328,39296,46208,39240,39336,39304,46224,39248,39344,39312,46240,39256,39352,39320,46256,40384,40480,40448,46272,40392,40488,40456,46288,40400,40496,40464,46304,40408,40504,40472,46320,46112,46272,46304,46336,46120,46280,46312,46352,46128,46288,46320,46368,46136,46296,46328,46384,47264,47424,47456,46400,47272,47432,47464,46416,47280,47440,47472,46432,47288,47448,47480,46448,48384,48608,48576,46464,48392,48616,48584,46480,48400,48624,48592,46496,48408,48632,48600,46512,49536,49760,49728,46528,49544,49768,49736,46544,49552,49776,49744,46560,49560,49784,49752,46576,55392,55424,55456,46592,55400,55432,55464,46608,55408,55440,55472,46624,55416,55448,55480,46640,56544,56576,56608,46656,56552,56584,56616,46672,56560,56592,56624,46688,56568,56600,56632,46704,57664,57760,57728,46720,57672,57768,57736,46736,57680,57776,57744,46752,57688,57784,57752,46768,58816,58912,58880,46784,58824,58920,58888,46800,58832,58928,58896,46816,58840,58936,58904,46832,64544,64704,64736,46848,64552,64712,64744,46864,64560,64720,64752,46880,64568,64728,64760,46896,65696,65856,65888,46912,65704,65864,65896,46928,65712,65872,65904,46944,65720,65880,65912,46960,66816,67040,67008,46976,66824,67048,67016,46992,66832,67056,67024,47008,66840,67064,67032,47024,67968,68192,68160,47040,67976,68200,68168,47056,67984,68208,68176,47072,67992,68216,68184,47088,0x1e800,0x1e800,0x20cc0,18432,0x1e8c4,0x1e8c4,0x20cc4,18448,0x1e988,0x1e988,0x20cc8,18464,0x1ea4c,0x1ea4c,0x20ccc,18480,0x1eb10,0x1eb10,0x20cd0,18496,0x1ebd4,0x1ebd4,0x20cd4,18512,0x1ec98,0x1ec98,0x20cd8,18528,0x1ed5c,0x1ed5c,0x20cdc,18544,0x1ee20,0x1ee20,0x20ce0,18816,0x1eee4,0x1eee4,0x20ce4,18832,0x1efa8,0x1efa8,0x20ce8,18848,0x1f06c,0x1f06c,0x20cec,18864,0x1f130,0x1f130,0x20cf0,18880,0x1f1f4,0x1f1f4,0x20cf4,18896,0x1f2b8,0x1f2b8,0x20cf8,18912,0x1f37c,0x1f37c,0x20cfc,18928,0x1e810,0x1e800,0x20cc0,19200,0x1e8d4,0x1e8c4,0x20cc4,19216,0x1e998,0x1e988,0x20cc8,19232,0x1ea5c,0x1ea4c,0x20ccc,19248,0x1eb20,0x1eb10,0x20cd0,19264,0x1ebe4,0x1ebd4,0x20cd4,19280,0x1eca8,0x1ec98,0x20cd8,19296,0x1ed6c,0x1ed5c,0x20cdc,19312,0x1ee30,0x1ee20,0x20ce0,19584,0x1eef4,0x1eee4,0x20ce4,19600,0x1efb8,0x1efa8,0x20ce8,19616,0x1f07c,0x1f06c,0x20cec,19632,0x1f140,0x1f130,0x20cf0,19648,0x1f204,0x1f1f4,0x20cf4,19664,0x1f2c8,0x1f2b8,0x20cf8,19680,0x1f38c,0x1f37c,0x20cfc,19696,0x1e820,0x1e800,0x20cc0,19968,0x1e8e4,0x1e8c4,0x20cc4,19984,0x1e9a8,0x1e988,0x20cc8,20000,0x1ea6c,0x1ea4c,0x20ccc,20016,0x1eb30,0x1eb10,0x20cd0,20032,0x1ebf4,0x1ebd4,0x20cd4,20048,0x1ecb8,0x1ec98,0x20cd8,20064,0x1ed7c,0x1ed5c,0x20cdc,20080,0x1ee40,0x1ee20,0x20ce0,20352,0x1ef04,0x1eee4,0x20ce4,20368,0x1efc8,0x1efa8,0x20ce8,20384,0x1f08c,0x1f06c,0x20cec,20400,0x1f150,0x1f130,0x20cf0,20416,0x1f214,0x1f1f4,0x20cf4,20432,0x1f2d8,0x1f2b8,0x20cf8,20448,0x1f39c,0x1f37c,0x20cfc,20464,0x1e830,0x1e800,0x20cc0,20736,0x1e8f4,0x1e8c4,0x20cc4,20752,0x1e9b8,0x1e988,0x20cc8,20768,0x1ea7c,0x1ea4c,0x20ccc,20784,0x1eb40,0x1eb10,0x20cd0,20800,0x1ec04,0x1ebd4,0x20cd4,20816,0x1ecc8,0x1ec98,0x20cd8,20832,0x1ed8c,0x1ed5c,0x20cdc,20848,0x1ee50,0x1ee20,0x20ce0,21120,0x1ef14,0x1eee4,0x20ce4,21136,0x1efd8,0x1efa8,0x20ce8,21152,0x1f09c,0x1f06c,0x20cec,21168,0x1f160,0x1f130,0x20cf0,21184,0x1f224,0x1f1f4,0x20cf4,21200,0x1f2e8,0x1f2b8,0x20cf8,21216,0x1f3ac,0x1f37c,0x20cfc,21232,55296,0x26e70,0x1e8c0,0x1eb60,55312,0x26e74,0x1ed50,0x1eff0,55328,0x26e78,0x1f1e0,0x1f480,55344,0x26e7c,0x1f670,0x1f910,55360,0x26e80,0x1fb00,0x1fda0,55376,0x26e84,0x1ff90,0x20230,55392,0x26e88,0x20420,0x206c0,55408,0x26e8c,0x208b0,0x20b50,55680,0x26e90,0x20d40,0x20fe0,55696,0x26e94,0x211d0,0x21470,55712,0x26e98,0x21660,0x21900,55728,0x26e9c,0x21af0,0x21d90,55744,0x26ea0,0x21f80,0x22220,55760,0x26ea4,0x22410,0x226b0,55776,0x26ea8,0x228a0,0x22b40,55792,0x26eac,0x22d30,0x22fd0,56096,0x26eb0,0x1e8f0,0x1eb68,56112,0x26eb4,0x1ed80,0x1eff8,56064,0x26eb8,0x1f210,0x1f488,56080,0x26ebc,0x1f6a0,0x1f918,56160,0x26ec0,0x1fb30,0x1fda8,56176,0x26ec4,0x1ffc0,0x20238,56128,0x26ec8,0x20450,0x206c8,56144,0x26ecc,0x208e0,0x20b58,56480,0x26ed0,0x20d70,0x20fe8,56496,0x26ed4,0x21200,0x21478,56448,0x26ed8,0x21690,0x21908,56464,0x26edc,0x21b20,0x21d98,56544,0x26ee0,0x21fb0,0x22228,56560,0x26ee4,0x22440,0x226b8,56512,0x26ee8,0x228d0,0x22b48,56528,0x26eec,0x22d60,0x22fd8,56896,0x26ef0,0x1e920,0x1eb70,56912,0x26ef4,0x1edb0,0x1f000,56928,0x26ef8,0x1f240,0x1f490,56944,0x26efc,0x1f6d0,0x1f920,56832,0x26f00,0x1fb60,0x1fdb0,56848,0x26f04,0x1fff0,0x20240,56864,0x26f08,0x20480,0x206d0,56880,0x26f0c,0x20910,0x20b60,57280,0x26f10,0x20da0,0x20ff0,57296,0x26f14,0x21230,0x21480,57312,0x26f18,0x216c0,0x21910,57328,0x26f1c,0x21b50,0x21da0,57216,0x26f20,0x21fe0,0x22230,57232,0x26f24,0x22470,0x226c0,57248,0x26f28,0x22900,0x22b50,57264,0x26f2c,0x22d90,0x22fe0,57696,0x26f30,0x1e950,0x1eb78,57712,0x26f34,0x1ede0,0x1f008,57664,0x26f38,0x1f270,0x1f498,57680,0x26f3c,0x1f700,0x1f928,57632,0x26f40,0x1fb90,0x1fdb8,57648,0x26f44,0x20020,0x20248,57600,0x26f48,0x204b0,0x206d8,57616,0x26f4c,0x20940,0x20b68,58080,0x26f50,0x20dd0,0x20ff8,58096,0x26f54,0x21260,0x21488,58048,0x26f58,0x216f0,0x21918,58064,0x26f5c,0x21b80,0x21da8,58016,0x26f60,0x22010,0x22238,58032,0x26f64,0x224a0,0x226c8,57984,0x26f68,0x22930,0x22b58,58000,0x26f6c,0x22dc0,0x22fe8,0x3f803f80,0x3f803f80,16256,0,0,0x3f803f80,0x3f800000,0,0,0x3f803f80,0,16256,0,0x3f803f80,0,0x3f800000,0x3f803f80,0,0,0,0,0,0,0,0,0,0,0,0,0,0,0,0x3f803f80,0,0,0,0,0,0,0,0,0,0,0,0,0,0,0,0x3f803f80,0,0,0,0,0,0,0,0,0,0,0,0,0,0,0,0x3f803f80,0,0,0,0,0,0,0,0,0,0,0,0,0,0,0,0x3f803f80,0x3f803f80,16256,0,0,0x3f803f80,0x3f800000,0,0,0x3f803f80,0,16256,0,0x3f803f80,0,0x3f800000,0x3f803f80,0,0,0,0,0,0,0,0,0,0,0,0,0,0,0,0x3f803f80,0,0,0,0,0,0,0,0,0,0,0,0,0,0,0,0x3f803f80,0,0,0,0,0,0,0,0,0,0,0,0,0,0,0,0x3f803f80,0,0,0,0,0,0,0,0,0,0,0,0,0,0,0,0x3f803f80,0x3f803f80,16256,0,0,0x3f803f80,0x3f800000,0,0,0x3f803f80,0,16256,0,0x3f803f80,0,0x3f800000,0x3f803f80,0,0,0,0,0,0,0,0,0,0,0,0,0,0,0,0x3f803f80,0,0,0,0,0,0,0,0,0,0,0,0,0,0,0,0x3f803f80,0,0,0,0,0,0,0,0,0,0,0,0,0,0,0,0x3f803f80,0,0,0,0,0,0,0,0,0,0,0,0,0,0,0,0x3f803f80,0x3f803f80,16256,0,0,0x3f803f80,0x3f800000,0,0,0x3f803f80,0,16256,0,0x3f803f80,0,0x3f800000,0,64,0x60000,0xc0080,144,208,0x60010,0xc0090,288,352,0x60020,0xc00a0,432,496,0x60030,0xc00b0,576,512,0x60040,0xc00c0,720,656,0x60050,0xc00d0,864,800,0x60060,0xc00e0,1008,944,0x60070,0xc00f0,1024,1088,0x60190,0xc0210,1168,1232,0x60180,0xc0200,1312,1376,0x601b0,0xc0230,1456,1520,0x601a0,0xc0220,1600,1536,0x601d0,0xc0250,1744,1680,0x601c0,0xc0240,1888,1824,0x601f0,0xc0270,2032,1968,0x601e0,0xc0260,16,80,0x60320,0xc03a0,128,192,0x60330,0xc03b0,304,368,0x60300,0xc0380,416,480,0x60310,0xc0390,592,528,0x60360,0xc03e0,704,640,0x60370,0xc03f0,880,816,0x60340,0xc03c0,992,928,0x60350,0xc03d0,1040,1104,0x604b0,0xef930,1152,1216,0x604a0,0xef920,1328,1392,0x60490,0xef910,1440,1504,0x60480,0xef900,1616,1552,0x604f0,0xef970,1728,1664,0x604e0,0xef960,1904,1840,0x604d0,0xef950,2016,1952,0x604c0,0xef940,32,96,0x60640,0xefac0,176,240,0x60650,0xefad0,256,320,0x60660,0xefae0,400,464,0x60670,0xefaf0,608,544,0x60600,0xefa80,752,688,0x60610,0xefa90,832,768,0x60620,0xefaa0,976,912,0x60630,0xefab0,1056,1120,0x8fbd0,0xefc50,1200,1264,0x8fbc0,0xefc40,1280,1344,0x8fbf0,0xefc70,1424,1488,0x8fbe0,0xefc60,1632,1568,0x8fb90,0xefc10,1776,1712,0x8fb80,0xefc00,1856,1792,0x8fbb0,0xefc30,2000,1936,0x8fba0,0xefc20,48,112,0x8fd60,0xefde0,160,224,0x8fd70,0xefdf0,272,336,0x8fd40,0xefdc0,384,448,0x8fd50,0xefdd0,624,560,0x8fd20,0xefda0,736,672,0x8fd30,0xefdb0,848,784,0x8fd00,0xefd80,960,896,0x8fd10,0xefd90,1072,1136,0x8fef0,0xeff70,1184,1248,0x8fee0,0xeff60,1296,1360,0x8fed0,0xeff50,1408,1472,0x8fec0,0xeff40,1648,1584,0x8feb0,0xeff30,1760,1696,0x8fea0,0xeff20,1872,1808,0x8fe90,0xeff10,1984,1920,0x8fe80,0xeff00,0xf0100,75776,83968,91136,0xf0110,75792,83984,91152,0xf0120,75808,84000,91168,0xf0130,75824,84016,91184,0xf0140,75840,84032,91200,0xf0150,75856,84048,91216,0xf0160,75872,84064,91232,0xf0170,75888,84080,91248,0xf0290,75904,84096,91264,0xf0280,75920,84112,91280,0xf02b0,75936,84128,91296,0xf02a0,75952,84144,91312,0xf02d0,75968,84160,91328,0xf02c0,75984,84176,91344,0xf02f0,76000,84192,91360,0xf02e0,76016,84208,91376,0x11f820,76032,84224,91392,0x11f830,76048,84240,91408,0x11f800,76064,84256,91424,0x11f810,76080,84272,91440,0x11f860,76096,84288,91456,0x11f870,76112,84304,91472,0x11f840,76128,84320,91488,0x11f850,76144,84336,91504,0x11f9b0,76160,84352,91520,0x11f9a0,76176,84368,91536,0x11f990,76192,84384,91552,0x11f980,76208,84400,91568,0x11f9f0,76224,84416,91584,0x11f9e0,76240,84432,91600,0x11f9d0,76256,84448,91616,0x11f9c0,76272,84464,91632,0xf0100,76288,84480,91648,0xf0110,76304,84496,91664,0xf0120,76320,84512,91680,0xf0130,76336,84528,91696,0xf0140,76352,84544,91712,0xf0150,76368,84560,91728,0xf0160,76384,84576,91744,0xf0170,76400,84592,91760,0xf0290,76416,84608,91776,0xf0280,76432,84624,91792,0xf02b0,76448,84640,91808,0xf02a0,76464,84656,91824,0xf02d0,76480,84672,91840,0xf02c0,76496,84688,91856,0xf02f0,76512,84704,91872,0xf02e0,76528,84720,91888,0x11f820,76544,84736,91904,0x11f830,76560,84752,91920,0x11f800,76576,84768,91936,0x11f810,76592,84784,91952,0x11f860,76608,84800,91968,0x11f870,76624,84816,91984,0x11f840,76640,84832,92000,0x11f850,76656,84848,92016,0x11f9b0,76672,84864,92032,0x11f9a0,76688,84880,92048,0x11f990,76704,84896,92064,0x11f980,76720,84912,92080,0x11f9f0,76736,84928,92096,0x11f9e0,76752,84944,92112,0x11f9d0,76768,84960,92128,0x11f9c0,76784,84976,92144,0x1c140,36864,43008,52224,0x1c144,36880,43024,52240,0x1c148,36896,43040,52256,0x1c14c,36912,43056,52272,0x1c150,36928,43072,52288,0x1c154,36944,43088,52304,0x1c158,36960,43104,52320,0x1c15c,36976,43120,52336,0x1c160,36992,43136,52352,0x1c164,37008,43152,52368,0x1c168,37024,43168,52384,0x1c16c,37040,43184,52400,0x1c170,37056,43200,52416,0x1c174,37072,43216,52432,0x1c178,37088,43232,52448,0x1c17c,37104,43248,52464,0x1c180,37120,43264,52480,0x1c184,37136,43280,52496,0x1c188,37152,43296,52512,0x1c18c,37168,43312,52528,0x1c190,37184,43328,52544,0x1c194,37200,43344,52560,0x1c198,37216,43360,52576,0x1c19c,37232,43376,52592,0x1c1a0,37248,43392,52608,0x1c1a4,37264,43408,52624,0x1c1a8,37280,43424,52640,0x1c1ac,37296,43440,52656,0x1c1b0,37312,43456,52672,0x1c1b4,37328,43472,52688,0x1c1b8,37344,43488,52704,0x1c1bc,37360,43504,52720,0x1c1c0,37376,43520,52736,0x1c1c4,37392,43536,52752,0x1c1c8,37408,43552,52768,0x1c1cc,37424,43568,52784,0x1c1d0,37440,43584,52800,0x1c1d4,37456,43600,52816,0x1c1d8,37472,43616,52832,0x1c1dc,37488,43632,52848,0x1c1e0,37504,43648,52864,0x1c1e4,37520,43664,52880,0x1c1e8,37536,43680,52896,0x1c1ec,37552,43696,52912,0x1c1f0,37568,43712,52928,0x1c1f4,37584,43728,52944,0x1c1f8,37600,43744,52960,0x1c1fc,37616,43760,52976,0x1c200,37632,43776,52992,0x1c204,37648,43792,53008,0x1c208,37664,43808,53024,0x1c20c,37680,43824,53040,0x1c210,37696,43840,53056,0x1c214,37712,43856,53072,0x1c218,37728,43872,53088,0x1c21c,37744,43888,53104,0x1c220,37760,43904,53120,0x1c224,37776,43920,53136,0x1c228,37792,43936,53152,0x1c22c,37808,43952,53168,0x1c230,37824,43968,53184,0x1c234,37840,43984,53200,0x1c238,37856,44000,53216,0x1c23c,37872,44016,53232,79936,86016,0x1d600,0x1d608,80080,86160,0x1d610,0x1d618,80224,86304,0x1d620,0x1d628,80368,86448,0x1d630,0x1d638,80384,86592,0x1d640,0x1d648,80528,86736,0x1d650,0x1d658,80672,86880,0x1d660,0x1d668,80816,87024,0x1d670,0x1d678,80960,87040,0x1d680,0x1d688,81104,87184,0x1d690,0x1d698,81248,87328,0x1d6a0,0x1d6a8,81392,87472,0x1d6b0,0x1d6b8,81408,87616,0x1d6c0,0x1d6c8,81552,87760,0x1d6d0,0x1d6d8,81696,87904,0x1d6e0,0x1d6e8,81840,88048,0x1d6f0,0x1d6f8,79952,86024,0x1d700,0x1d708,80064,86168,0x1d710,0x1d718,80240,86312,0x1d720,0x1d728,80352,86456,0x1d730,0x1d738,80400,86600,0x1d740,0x1d748,80512,86744,0x1d750,0x1d758,80688,86888,0x1d760,0x1d768,80800,87032,0x1d770,0x1d778,80976,87048,0x1d780,0x1d788,81088,87192,0x1d790,0x1d798,81264,87336,0x1d7a0,0x1d7a8,81376,87480,0x1d7b0,0x1d7b8,81424,87624,0x1d7c0,0x1d7c8,81536,87768,0x1d7d0,0x1d7d8,81712,87912,0x1d7e0,0x1d7e8,81824,88056,0x1d7f0,0x1d7f8,79968,86032,0x1d800,0x1d808,80112,86144,0x1d810,0x1d818,80192,86320,0x1d820,0x1d828,80336,86432,0x1d830,0x1d838,80416,86608,0x1d840,0x1d848,80560,86720,0x1d850,0x1d858,80640,86896,0x1d860,0x1d868,80784,87008,0x1d870,0x1d878,80992,87056,0x1d880,0x1d888,81136,87168,0x1d890,0x1d898,81216,87344,0x1d8a0,0x1d8a8,81360,87456,0x1d8b0,0x1d8b8,81440,87632,0x1d8c0,0x1d8c8,81584,87744,0x1d8d0,0x1d8d8,81664,87920,0x1d8e0,0x1d8e8,81808,88032,0x1d8f0,0x1d8f8,79984,86040,0x1d900,0x1d908,80096,86152,0x1d910,0x1d918,80208,86328,0x1d920,0x1d928,80320,86440,0x1d930,0x1d938,80432,86616,0x1d940,0x1d948,80544,86728,0x1d950,0x1d958,80656,86904,0x1d960,0x1d968,80768,87016,0x1d970,0x1d978,81008,87064,0x1d980,0x1d988,81120,87176,0x1d990,0x1d998,81232,87352,0x1d9a0,0x1d9a8,81344,87464,0x1d9b0,0x1d9b8,81456,87640,0x1d9c0,0x1d9c8,81568,87752,0x1d9d0,0x1d9d8,81680,87928,0x1d9e0,0x1d9e8,81792,88040,0x1d9f0,0x1d9f8,0x1e700,0x1e700,0x1e980,0x100000,0x1e702,0x100000,0x100000,0x1e980,0x1e704,0x100000,0x100000,0x100000,0x1e706,0x100000,0x100000,0x100000,0x1e710,0x1e700,0x100000,0x100000,0x1e712,0x100000,0x100000,0x1e980,0x1e714,0x100000,0x100000,0x100000,0x1e716,0x100000,0x100000,0x100000,0x1e720,0x1e700,0x100000,0x100000,0x1e722,0x100000,0x100000,0x1e980,0x1e724,0x100000,0x100000,0x100000,0x1e726,0x100000,0x100000,0x100000,0x1e730,0x1e700,0x100000,0x100000,0x1e732,0x100000,0x100000,0x1e980,0x1e734,0x100000,0x100000,0x100000,0x1e736,0x100000,0x100000,0x100000,0x1e708,0x1e710,0x1e9b0,0x100000,0x1e70a,0x100000,0x100000,0x1e9b0,0x1e70c,0x100000,0x100000,0x100000,0x1e70e,0x100000,0x100000,0x100000,0x1e718,0x1e710,0x100000,0x100000,0x1e71a,0x100000,0x100000,0x1e9b0,0x1e71c,0x100000,0x100000,0x100000,0x1e71e,0x100000,0x100000,0x100000,0x1e728,0x1e710,0x100000,0x100000,0x1e72a,0x100000,0x100000,0x1e9b0,0x1e72c,0x100000,0x100000,0x100000,0x1e72e,0x100000,0x100000,0x100000,0x1e738,0x1e710,0x100000,0x100000,0x1e73a,0x100000,0x100000,0x1e9b0,0x1e73c,0x100000,0x100000,0x100000,0x1e73e,0x100000,0x100000,0x100000,0x1e740,0x1e720,0x1e9e0,0x100000,0x1e742,0x100000,0x100000,0x1e9e0,0x1e744,0x100000,0x100000,0x100000,0x1e746,0x100000,0x100000,0x100000,0x1e750,0x1e720,0x100000,0x100000,0x1e752,0x100000,0x100000,0x1e9e0,0x1e754,0x100000,0x100000,0x100000,0x1e756,0x100000,0x100000,0x100000,0x1e760,0x1e720,0x100000,0x100000,0x1e762,0x100000,0x100000,0x1e9e0,0x1e764,0x100000,0x100000,0x100000,0x1e766,0x100000,0x100000,0x100000,0x1e770,0x1e720,0x100000,0x100000,0x1e772,0x100000,0x100000,0x1e9e0,0x1e774,0x100000,0x100000,0x100000,0x1e776,0x100000,0x100000,0x100000,0x1e748,0x1e730,0x1ea10,0x100000,0x1e74a,0x100000,0x100000,0x1ea10,0x1e74c,0x100000,0x100000,0x100000,0x1e74e,0x100000,0x100000,0x100000,0x1e758,0x1e730,0x100000,0x100000,0x1e75a,0x100000,0x100000,0x1ea10,0x1e75c,0x100000,0x100000,0x100000,0x1e75e,0x100000,0x100000,0x100000,0x1e768,0x1e730,0x100000,0x100000,0x1e76a,0x100000,0x100000,0x1ea10,0x1e76c,0x100000,0x100000,0x100000,0x1e76e,0x100000,0x100000,0x100000,0x1e778,0x1e730,0x100000,0x100000,0x1e77a,0x100000,0x100000,0x1ea10,0x1e77c,0x100000,0x100000,0x100000,0x1e77e,0x100000,0x100000,0x100000,0x1ebc0,0x25770,37056,37120,0x1ebc2,0x25772,37072,37128,0x1ebc4,0x25774,37088,37136,0x1ebc6,0x25776,37104,37144,0x1ebc8,0x258d0,37440,37504,0x1ebca,0x258d2,37456,37512,0x1ebcc,0x258d4,37472,37520,0x1ebce,0x258d6,37488,37528,0x1ebd0,0x25a30,37856,37920,0x1ebd2,0x25a32,37872,37928,0x1ebd4,0x25a34,37824,37936,0x1ebd6,0x25a36,37840,37944,0x1ebd8,0x25b90,38240,38304,0x1ebda,0x25b92,38256,38312,0x1ebdc,0x25b94,38208,38320,0x1ebde,0x25b96,38224,38328,0x1ebc0,0x25778,38528,38720,0x1ebc2,0x2577a,38544,38728,0x1ebc4,0x2577c,38560,38736,0x1ebc6,0x2577e,38576,38744,0x1ebc8,0x258d8,38912,39104,0x1ebca,0x258da,38928,39112,0x1ebcc,0x258dc,38944,39120,0x1ebce,0x258de,38960,39128,0x1ebd0,0x25a38,39328,39520,0x1ebd2,0x25a3a,39344,39528,0x1ebd4,0x25a3c,39296,39536,0x1ebd6,0x25a3e,39312,39544,0x1ebd8,0x25b98,39712,39904,0x1ebda,0x25b9a,39728,39912,0x1ebdc,0x25b9c,39680,39920,0x1ebde,0x25b9e,39696,39928,0x1ebc0,0x267f0,40128,40192,0x1ebc2,0x267f2,40144,40200,0x1ebc4,0x267f4,40160,40208,0x1ebc6,0x267f6,40176,40216,0x1ebc8,0x26950,40512,40576,0x1ebca,0x26952,40528,40584,0x1ebcc,0x26954,40544,40592,0x1ebce,0x26956,40560,40600,0x1ebd0,0x26ab0,40928,40992,0x1ebd2,0x26ab2,40944,41000,0x1ebd4,0x26ab4,40896,41008,0x1ebd6,0x26ab6,40912,41016,0x1ebd8,0x26c10,41312,41376,0x1ebda,0x26c12,41328,41384,0x1ebdc,0x26c14,41280,41392,0x1ebde,0x26c16,41296,41400,0x1ebc0,0x100000,41600,41792,0x1ebc2,0x100000,41616,41800,0x1ebc4,0x100000,41632,41808,0x1ebc6,0x100000,41648,41816,0x1ebc8,0x100000,41984,42176,0x1ebca,0x100000,42000,42184,0x1ebcc,0x100000,42016,42192,0x1ebce,0x100000,42032,42200,0x1ebd0,0x100000,42400,42592,0x1ebd2,0x100000,42416,42600,0x1ebd4,0x100000,42368,42608,0x1ebd6,0x100000,42384,42616,0x1ebd8,0x100000,42784,42976,0x1ebda,0x100000,42800,42984,0x1ebdc,0x100000,42752,42992,0x1ebde,0x100000,42768,43000,37056,37088,37120,55296,37064,37096,37128,55312,37072,37104,37136,55328,37080,37112,37144,55344,38208,38240,38272,55360,38216,38248,38280,55376,38224,38256,38288,55392,38232,38264,38296,55408,39392,39360,39456,55424,39400,39368,39464,55440,39408,39376,39472,55456,39416,39384,39480,55472,40544,40512,40608,55488,40552,40520,40616,55504,40560,40528,40624,55520,40568,40536,40632,55536,46208,46240,46400,55552,46216,46248,46408,55568,46224,46256,46416,55584,46232,46264,46424,55600,47360,47392,47552,55616,47368,47400,47560,55632,47376,47408,47568,55648,47384,47416,47576,55664,48544,48512,48736,55680,48552,48520,48744,55696,48560,48528,48752,55712,48568,48536,48760,55728,49696,49664,49888,55744,49704,49672,49896,55760,49712,49680,49904,55776,49720,49688,49912,55792,55488,55520,55552,55808,55496,55528,55560,55824,55504,55536,55568,55840,55512,55544,55576,55856,56640,56672,56704,55872,56648,56680,56712,55888,56656,56688,56720,55904,56664,56696,56728,55920,57824,57792,57888,55936,57832,57800,57896,55952,57840,57808,57904,55968,57848,57816,57912,55984,58976,58944,59040,56000,58984,58952,59048,56016,58992,58960,59056,56032,59000,58968,59064,56048,64640,64672,64832,56064,64648,64680,64840,56080,64656,64688,64848,56096,64664,64696,64856,56112,65792,65824,65984,56128,65800,65832,65992,56144,65808,65840,66000,56160,65816,65848,66008,56176,66976,66944,67168,56192,66984,66952,67176,56208,66992,66960,67184,56224,67000,66968,67192,56240,68128,68096,68320,56256,68136,68104,68328,56272,68144,68112,68336,56288,68152,68120,68344,56304,0x1e800,0x1e800,0x20cc0,36864,0x1e8c4,0x1e8c4,0x20cc4,36880,0x1e988,0x1e988,0x20cc8,36896,0x1ea4c,0x1ea4c,0x20ccc,36912,0x1eb10,0x1eb10,0x20cd0,36928,0x1ebd4,0x1ebd4,0x20cd4,36944,0x1ec98,0x1ec98,0x20cd8,36960,0x1ed5c,0x1ed5c,0x20cdc,36976,0x1ee20,0x1ee20,0x20ce0,37248,0x1eee4,0x1eee4,0x20ce4,37264,0x1efa8,0x1efa8,0x20ce8,37280,0x1f06c,0x1f06c,0x20cec,37296,0x1f130,0x1f130,0x20cf0,37312,0x1f1f4,0x1f1f4,0x20cf4,37328,0x1f2b8,0x1f2b8,0x20cf8,37344,0x1f37c,0x1f37c,0x20cfc,37360,0x1e810,0x1e800,0x20cc0,37632,0x1e8d4,0x1e8c4,0x20cc4,37648,0x1e998,0x1e988,0x20cc8,37664,0x1ea5c,0x1ea4c,0x20ccc,37680,0x1eb20,0x1eb10,0x20cd0,37696,0x1ebe4,0x1ebd4,0x20cd4,37712,0x1eca8,0x1ec98,0x20cd8,37728,0x1ed6c,0x1ed5c,0x20cdc,37744,0x1ee30,0x1ee20,0x20ce0,38016,0x1eef4,0x1eee4,0x20ce4,38032,0x1efb8,0x1efa8,0x20ce8,38048,0x1f07c,0x1f06c,0x20cec,38064,0x1f140,0x1f130,0x20cf0,38080,0x1f204,0x1f1f4,0x20cf4,38096,0x1f2c8,0x1f2b8,0x20cf8,38112,0x1f38c,0x1f37c,0x20cfc,38128,0x1e820,0x1e800,0x20cc0,38400,0x1e8e4,0x1e8c4,0x20cc4,38416,0x1e9a8,0x1e988,0x20cc8,38432,0x1ea6c,0x1ea4c,0x20ccc,38448,0x1eb30,0x1eb10,0x20cd0,38464,0x1ebf4,0x1ebd4,0x20cd4,38480,0x1ecb8,0x1ec98,0x20cd8,38496,0x1ed7c,0x1ed5c,0x20cdc,38512,0x1ee40,0x1ee20,0x20ce0,38784,0x1ef04,0x1eee4,0x20ce4,38800,0x1efc8,0x1efa8,0x20ce8,38816,0x1f08c,0x1f06c,0x20cec,38832,0x1f150,0x1f130,0x20cf0,38848,0x1f214,0x1f1f4,0x20cf4,38864,0x1f2d8,0x1f2b8,0x20cf8,38880,0x1f39c,0x1f37c,0x20cfc,38896,0x1e830,0x1e800,0x20cc0,39168,0x1e8f4,0x1e8c4,0x20cc4,39184,0x1e9b8,0x1e988,0x20cc8,39200,0x1ea7c,0x1ea4c,0x20ccc,39216,0x1eb40,0x1eb10,0x20cd0,39232,0x1ec04,0x1ebd4,0x20cd4,39248,0x1ecc8,0x1ec98,0x20cd8,39264,0x1ed8c,0x1ed5c,0x20cdc,39280,0x1ee50,0x1ee20,0x20ce0,39552,0x1ef14,0x1eee4,0x20ce4,39568,0x1efd8,0x1efa8,0x20ce8,39584,0x1f09c,0x1f06c,0x20cec,39600,0x1f160,0x1f130,0x20cf0,39616,0x1f224,0x1f1f4,0x20cf4,39632,0x1f2e8,0x1f2b8,0x20cf8,39648,0x1f3ac,0x1f37c,0x20cfc,39664,73728,0x26f70,0x1e980,0x1ebc0,73744,0x26f74,0x1ee10,0x1f050,73760,0x26f78,0x1f2a0,0x1f4e0,73776,0x26f7c,0x1f730,0x1f970,73792,0x26f80,0x1fbc0,0x1fe00,73808,0x26f84,0x20050,0x20290,73824,0x26f88,0x204e0,0x20720,73840,0x26f8c,0x20970,0x20bb0,74112,0x26f90,0x20e00,0x21040,74128,0x26f94,0x21290,0x214d0,74144,0x26f98,0x21720,0x21960,74160,0x26f9c,0x21bb0,0x21df0,74176,0x26fa0,0x22040,0x22280,74192,0x26fa4,0x224d0,0x22710,74208,0x26fa8,0x22960,0x22ba0,74224,0x26fac,0x22df0,0x23030,74528,0x26fb0,0x1e9b0,0x1ebc8,74544,0x26fb4,0x1ee40,0x1f058,74496,0x26fb8,0x1f2d0,0x1f4e8,74512,0x26fbc,0x1f760,0x1f978,74592,0x26fc0,0x1fbf0,0x1fe08,74608,0x26fc4,0x20080,0x20298,74560,0x26fc8,0x20510,0x20728,74576,0x26fcc,0x209a0,0x20bb8,74912,0x26fd0,0x20e30,0x21048,74928,0x26fd4,0x212c0,0x214d8,74880,0x26fd8,0x21750,0x21968,74896,0x26fdc,0x21be0,0x21df8,74976,0x26fe0,0x22070,0x22288,74992,0x26fe4,0x22500,0x22718,74944,0x26fe8,0x22990,0x22ba8,74960,0x26fec,0x22e20,0x23038,75328,0x26ff0,0x1e9e0,0x1ebd0,75344,0x26ff4,0x1ee70,0x1f060,75360,0x26ff8,0x1f300,0x1f4f0,75376,0x26ffc,0x1f790,0x1f980,75264,0x27000,0x1fc20,0x1fe10,75280,0x27004,0x200b0,0x202a0,75296,0x27008,0x20540,0x20730,75312,0x2700c,0x209d0,0x20bc0,75712,0x27010,0x20e60,0x21050,75728,0x27014,0x212f0,0x214e0,75744,0x27018,0x21780,0x21970,75760,0x2701c,0x21c10,0x21e00,75648,0x27020,0x220a0,0x22290,75664,0x27024,0x22530,0x22720,75680,0x27028,0x229c0,0x22bb0,75696,0x2702c,0x22e50,0x23040,76128,0x27030,0x1ea10,0x1ebd8,76144,0x27034,0x1eea0,0x1f068,76096,0x27038,0x1f330,0x1f4f8,76112,0x2703c,0x1f7c0,0x1f988,76064,0x27040,0x1fc50,0x1fe18,76080,0x27044,0x200e0,0x202a8,76032,0x27048,0x20570,0x20738,76048,0x2704c,0x20a00,0x20bc8,76512,0x27050,0x20e90,0x21058,76528,0x27054,0x21320,0x214e8,76480,0x27058,0x217b0,0x21978,76496,0x2705c,0x21c40,0x21e08,76448,0x27060,0x220d0,0x22298,76464,0x27064,0x22560,0x22728,76416,0x27068,0x229f0,0x22bb8,76432,0x2706c,0x22e80,0x23048,0x3f803f80,0x3f803f80,16256,0,0,0x3f803f80,0x3f800000,0,0,0x3f803f80,0,16256,0,0x3f803f80,0,0x3f800000,0x3f803f80,0,0,0,0,0,0,0,0,0,0,0,0,0,0,0,0x3f803f80,0,0,0,0,0,0,0,0,0,0,0,0,0,0,0,0x3f803f80,0,0,0,0,0,0,0,0,0,0,0,0,0,0,0,0x3f803f80,0,0,0,0,0,0,0,0,0,0,0,0,0,0,0,0x3f803f80,0x3f803f80,16256,0,0,0x3f803f80,0x3f800000,0,0,0x3f803f80,0,16256,0,0x3f803f80,0,0x3f800000,0x3f803f80,0,0,0,0,0,0,0,0,0,0,0,0,0,0,0,0x3f803f80,0,0,0,0,0,0,0,0,0,0,0,0,0,0,0,0x3f803f80,0,0,0,0,0,0,0,0,0,0,0,0,0,0,0,0x3f803f80,0,0,0,0,0,0,0,0,0,0,0,0,0,0,0,0x3f803f80,0x3f803f80,16256,0,0,0x3f803f80,0x3f800000,0,0,0x3f803f80,0,16256,0,0x3f803f80,0,0x3f800000,0x3f803f80,0,0,0,0,0,0,0,0,0,0,0,0,0,0,0,0x3f803f80,0,0,0,0,0,0,0,0,0,0,0,0,0,0,0,0x3f803f80,0,0,0,0,0,0,0,0,0,0,0,0,0,0,0,0x3f803f80,0,0,0,0,0,0,0,0,0,0,0,0,0,0,0,0x3f803f80,0x3f803f80,16256,0,0,0x3f803f80,0x3f800000,0,0,0x3f803f80,0,16256,0,0x3f803f80,0,0x3f800000,0,64,0x90000,0xf0080,144,208,0x90010,0xf0090,288,352,0x90020,0xf00a0,432,496,0x90030,0xf00b0,576,512,0x90040,0xf00c0,720,656,0x90050,0xf00d0,864,800,0x90060,0xf00e0,1008,944,0x90070,0xf00f0,1024,1088,0x90190,0xf0210,1168,1232,0x90180,0xf0200,1312,1376,0x901b0,0xf0230,1456,1520,0x901a0,0xf0220,1600,1536,0x901d0,0xf0250,1744,1680,0x901c0,0xf0240,1888,1824,0x901f0,0xf0270,2032,1968,0x901e0,0xf0260,16,80,0x90320,0x11f7a0,128,192,0x90330,0x11f7b0,304,368,0x90300,0x11f780,416,480,0x90310,0x11f790,592,528,0x90360,0x11f7e0,704,640,0x90370,0x11f7f0,880,816,0x90340,0x11f7c0,992,928,0x90350,0x11f7d0,1040,1104,0x904b0,0x11f930,1152,1216,0x904a0,0x11f920,1328,1392,0x90490,0x11f910,1440,1504,0x90480,0x11f900,1616,1552,0x904f0,0x11f970,1728,1664,0x904e0,0x11f960,1904,1840,0x904d0,0x11f950,2016,1952,0x904c0,0x11f940,32,96,0xbfa40,0x11fac0,176,240,0xbfa50,0x11fad0,256,320,0xbfa60,0x11fae0,400,464,0xbfa70,0x11faf0,608,544,0xbfa00,0x11fa80,752,688,0xbfa10,0x11fa90,832,768,0xbfa20,0x11faa0,976,912,0xbfa30,0x11fab0,1056,1120,0xbfbd0,0x11fc50,1200,1264,0xbfbc0,0x11fc40,1280,1344,0xbfbf0,0x11fc70,1424,1488,0xbfbe0,0x11fc60,1632,1568,0xbfb90,0x11fc10,1776,1712,0xbfb80,0x11fc00,1856,1792,0xbfbb0,0x11fc30,2000,1936,0xbfba0,0x11fc20,48,112,0xbfd60,0x11fde0,160,224,0xbfd70,0x11fdf0,272,336,0xbfd40,0x11fdc0,384,448,0xbfd50,0x11fdd0,624,560,0xbfd20,0x11fda0,736,672,0xbfd30,0x11fdb0,848,784,0xbfd00,0x11fd80,960,896,0xbfd10,0x11fd90,1072,1136,0xbfef0,0x11ff70,1184,1248,0xbfee0,0x11ff60,1296,1360,0xbfed0,0x11ff50,1408,1472,0xbfec0,0x11ff40,1648,1584,0xbfeb0,0x11ff30,1760,1696,0xbfea0,0x11ff20,1872,1808,0xbfe90,0x11ff10,1984,1920,0xbfe80,0x11ff00,0x11fb40,76800,84992,91648,0x11fb50,76816,85008,91664,0x11fb60,76832,85024,91680,0x11fb70,76848,85040,91696,0x11fb00,76864,85056,91712,0x11fb10,76880,85072,91728,0x11fb20,76896,85088,91744,0x11fb30,76912,85104,91760,0x11fcd0,76928,85120,91776,0x11fcc0,76944,85136,91792,0x11fcf0,76960,85152,91808,0x11fce0,76976,85168,91824,0x11fc90,76992,85184,91840,0x11fc80,77008,85200,91856,0x11fcb0,77024,85216,91872,0x11fca0,77040,85232,91888,0x11fe60,77056,85248,91904,0x11fe70,77072,85264,91920,0x11fe40,77088,85280,91936,0x11fe50,77104,85296,91952,0x11fe20,77120,85312,91968,0x11fe30,77136,85328,91984,0x11fe00,77152,85344,92000,0x11fe10,77168,85360,92016,0x11fff0,77184,85376,92032,0x11ffe0,77200,85392,92048,0x11ffd0,77216,85408,92064,0x11ffc0,77232,85424,92080,0x11ffb0,77248,85440,92096,0x11ffa0,77264,85456,92112,0x11ff90,77280,85472,92128,0x11ff80,77296,85488,92144,0x11fb40,77312,85504,92160,0x11fb50,77328,85520,92176,0x11fb60,77344,85536,92192,0x11fb70,77360,85552,92208,0x11fb00,77376,85568,92224,0x11fb10,77392,85584,92240,0x11fb20,77408,85600,92256,0x11fb30,77424,85616,92272,0x11fcd0,77440,85632,92288,0x11fcc0,77456,85648,92304,0x11fcf0,77472,85664,92320,0x11fce0,77488,85680,92336,0x11fc90,77504,85696,92352,0x11fc80,77520,85712,92368,0x11fcb0,77536,85728,92384,0x11fca0,77552,85744,92400,0x11fe60,77568,85760,92416,0x11fe70,77584,85776,92432,0x11fe40,77600,85792,92448,0x11fe50,77616,85808,92464,0x11fe20,77632,85824,92480,0x11fe30,77648,85840,92496,0x11fe00,77664,85856,92512,0x11fe10,77680,85872,92528,0x11fff0,77696,85888,92544,0x11ffe0,77712,85904,92560,0x11ffd0,77728,85920,92576,0x11ffc0,77744,85936,92592,0x11ffb0,77760,85952,92608,0x11ffa0,77776,85968,92624,0x11ff90,77792,85984,92640,0x11ff80,77808,86000,92656,0x1c1d0,36864,46080,52232,0x1c1d4,36880,46096,52248,0x1c1d8,36896,46112,52264,0x1c1dc,36912,46128,52280,0x1c1e0,36928,46144,52296,0x1c1e4,36944,46160,52312,0x1c1e8,36960,46176,52328,0x1c1ec,36976,46192,52344,0x1c1f0,36992,46208,52360,0x1c1f4,37008,46224,52376,0x1c1f8,37024,46240,52392,0x1c1fc,37040,46256,52408,0x1c200,37056,46272,52424,0x1c204,37072,46288,52440,0x1c208,37088,46304,52456,0x1c20c,37104,46320,52472,0x1c210,37120,46336,52488,0x1c214,37136,46352,52504,0x1c218,37152,46368,52520,0x1c21c,37168,46384,52536,0x1c220,37184,46400,52552,0x1c224,37200,46416,52568,0x1c228,37216,46432,52584,0x1c22c,37232,46448,52600,0x1c230,37248,46464,52616,0x1c234,37264,46480,52632,0x1c238,37280,46496,52648,0x1c23c,37296,46512,52664,0x1c240,37312,46528,52680,0x1c244,37328,46544,52696,0x1c248,37344,46560,52712,0x1c24c,37360,46576,52728,0x1c250,37376,46592,52744,0x1c254,37392,46608,52760,0x1c258,37408,46624,52776,0x1c25c,37424,46640,52792,0x1c260,37440,46656,52808,0x1c264,37456,46672,52824,0x1c268,37472,46688,52840,0x1c26c,37488,46704,52856,0x1c270,37504,46720,52872,0x1c274,37520,46736,52888,0x1c278,37536,46752,52904,0x1c27c,37552,46768,52920,0x1c280,37568,46784,52936,0x1c284,37584,46800,52952,0x1c288,37600,46816,52968,0x1c28c,37616,46832,52984,0x1c290,37632,46848,53000,0x1c294,37648,46864,53016,0x1c298,37664,46880,53032,0x1c29c,37680,46896,53048,0x1c2a0,37696,46912,53064,0x1c2a4,37712,46928,53080,0x1c2a8,37728,46944,53096,0x1c2ac,37744,46960,53112,0x1c2b0,37760,46976,53128,0x1c2b4,37776,46992,53144,0x1c2b8,37792,47008,53160,0x1c2bc,37808,47024,53176,0x1c2c0,37824,47040,53192,0x1c2c4,37840,47056,53208,0x1c2c8,37856,47072,53224,0x1c2cc,37872,47088,53240,86048,86112,0x1d600,0x1d60c,86192,86256,0x1d610,0x1d61c,86272,86336,0x1d620,0x1d62c,86416,86480,0x1d630,0x1d63c,86624,86560,0x1d640,0x1d64c,86768,86704,0x1d650,0x1d65c,86848,86784,0x1d660,0x1d66c,86992,86928,0x1d670,0x1d67c,87072,87136,0x1d680,0x1d68c,87216,87280,0x1d690,0x1d69c,87296,87360,0x1d6a0,0x1d6ac,87440,87504,0x1d6b0,0x1d6bc,87648,87584,0x1d6c0,0x1d6cc,87792,87728,0x1d6d0,0x1d6dc,87872,87808,0x1d6e0,0x1d6ec,88016,87952,0x1d6f0,0x1d6fc,86064,86120,0x1d700,0x1d70c,86176,86264,0x1d710,0x1d71c,86288,86344,0x1d720,0x1d72c,86400,86488,0x1d730,0x1d73c,86640,86568,0x1d740,0x1d74c,86752,86712,0x1d750,0x1d75c,86864,86792,0x1d760,0x1d76c,86976,86936,0x1d770,0x1d77c,87088,87144,0x1d780,0x1d78c,87200,87288,0x1d790,0x1d79c,87312,87368,0x1d7a0,0x1d7ac,87424,87512,0x1d7b0,0x1d7bc,87664,87592,0x1d7c0,0x1d7cc,87776,87736,0x1d7d0,0x1d7dc,87888,87816,0x1d7e0,0x1d7ec,88000,87960,0x1d7f0,0x1d7fc,86080,86128,0x1d800,0x1d80c,86224,86240,0x1d810,0x1d81c,86368,86352,0x1d820,0x1d82c,86512,86464,0x1d830,0x1d83c,86528,86576,0x1d840,0x1d84c,86672,86688,0x1d850,0x1d85c,86816,86800,0x1d860,0x1d86c,86960,86912,0x1d870,0x1d87c,87104,87152,0x1d880,0x1d88c,87248,87264,0x1d890,0x1d89c,87392,87376,0x1d8a0,0x1d8ac,87536,87488,0x1d8b0,0x1d8bc,87552,87600,0x1d8c0,0x1d8cc,87696,87712,0x1d8d0,0x1d8dc,87840,87824,0x1d8e0,0x1d8ec,87984,87936,0x1d8f0,0x1d8fc,86096,86136,0x1d900,0x1d90c,86208,86248,0x1d910,0x1d91c,86384,86360,0x1d920,0x1d92c,86496,86472,0x1d930,0x1d93c,86544,86584,0x1d940,0x1d94c,86656,86696,0x1d950,0x1d95c,86832,86808,0x1d960,0x1d96c,86944,86920,0x1d970,0x1d97c,87120,87160,0x1d980,0x1d98c,87232,87272,0x1d990,0x1d99c,87408,87384,0x1d9a0,0x1d9ac,87520,87496,0x1d9b0,0x1d9bc,87568,87608,0x1d9c0,0x1d9cc,87680,87720,0x1d9d0,0x1d9dc,87856,87832,0x1d9e0,0x1d9ec,87968,87944,0x1d9f0,0x1d9fc,0x1e780,0x1e780,0x1ea40,0x100000,0x1e782,0x100000,0x100000,0x1ea40,0x1e784,0x100000,0x100000,0x100000,0x1e786,0x100000,0x100000,0x100000,0x1e790,0x1e780,0x100000,0x100000,0x1e792,0x100000,0x100000,0x1ea40,0x1e794,0x100000,0x100000,0x100000,0x1e796,0x100000,0x100000,0x100000,0x1e7a0,0x1e780,0x100000,0x100000,0x1e7a2,0x100000,0x100000,0x1ea40,0x1e7a4,0x100000,0x100000,0x100000,0x1e7a6,0x100000,0x100000,0x100000,0x1e7b0,0x1e780,0x100000,0x100000,0x1e7b2,0x100000,0x100000,0x1ea40,0x1e7b4,0x100000,0x100000,0x100000,0x1e7b6,0x100000,0x100000,0x100000,0x1e788,0x1e790,0x1ea70,0x100000,0x1e78a,0x100000,0x100000,0x1ea70,0x1e78c,0x100000,0x100000,0x100000,0x1e78e,0x100000,0x100000,0x100000,0x1e798,0x1e790,0x100000,0x100000,0x1e79a,0x100000,0x100000,0x1ea70,0x1e79c,0x100000,0x100000,0x100000,0x1e79e,0x100000,0x100000,0x100000,0x1e7a8,0x1e790,0x100000,0x100000,0x1e7aa,0x100000,0x100000,0x1ea70,0x1e7ac,0x100000,0x100000,0x100000,0x1e7ae,0x100000,0x100000,0x100000,0x1e7b8,0x1e790,0x100000,0x100000,0x1e7ba,0x100000,0x100000,0x1ea70,0x1e7bc,0x100000,0x100000,0x100000,0x1e7be,0x100000,0x100000,0x100000,0x1e7c0,0x1e7a0,0x1eaa0,0x100000,0x1e7c2,0x100000,0x100000,0x1eaa0,0x1e7c4,0x100000,0x100000,0x100000,0x1e7c6,0x100000,0x100000,0x100000,0x1e7d0,0x1e7a0,0x100000,0x100000,0x1e7d2,0x100000,0x100000,0x1eaa0,0x1e7d4,0x100000,0x100000,0x100000,0x1e7d6,0x100000,0x100000,0x100000,0x1e7e0,0x1e7a0,0x100000,0x100000,0x1e7e2,0x100000,0x100000,0x1eaa0,0x1e7e4,0x100000,0x100000,0x100000,0x1e7e6,0x100000,0x100000,0x100000,0x1e7f0,0x1e7a0,0x100000,0x100000,0x1e7f2,0x100000,0x100000,0x1eaa0,0x1e7f4,0x100000,0x100000,0x100000,0x1e7f6,0x100000,0x100000,0x100000,0x1e7c8,0x1e7b0,0x1ead0,0x100000,0x1e7ca,0x100000,0x100000,0x1ead0,0x1e7cc,0x100000,0x100000,0x100000,0x1e7ce,0x100000,0x100000,0x100000,0x1e7d8,0x1e7b0,0x100000,0x100000,0x1e7da,0x100000,0x100000,0x1ead0,0x1e7dc,0x100000,0x100000,0x100000,0x1e7de,0x100000,0x100000,0x100000,0x1e7e8,0x1e7b0,0x100000,0x100000,0x1e7ea,0x100000,0x100000,0x1ead0,0x1e7ec,0x100000,0x100000,0x100000,0x1e7ee,0x100000,0x100000,0x100000,0x1e7f8,0x1e7b0,0x100000,0x100000,0x1e7fa,0x100000,0x100000,0x1ead0,0x1e7fc,0x100000,0x100000,0x100000,0x1e7fe,0x100000,0x100000,0x100000,0x1ec20,0x25cf0,37152,37216,0x1ec22,0x25cf2,37168,37224,0x1ec24,0x25cf4,37184,37232,0x1ec26,0x25cf6,37200,37240,0x1ec28,0x25e50,37536,37600,0x1ec2a,0x25e52,37552,37608,0x1ec2c,0x25e54,37568,37616,0x1ec2e,0x25e56,37584,37624,0x1ec30,0x25fb0,37888,37952,0x1ec32,0x25fb2,37904,37960,0x1ec34,0x25fb4,37984,37968,0x1ec36,0x25fb6,38000,37976,0x1ec38,0x26110,38272,38336,0x1ec3a,0x26112,38288,38344,0x1ec3c,0x26114,38368,38352,0x1ec3e,0x26116,38384,38360,0x1ec20,0x25cf8,38752,38688,0x1ec22,0x25cfa,38768,38696,0x1ec24,0x25cfc,38656,38704,0x1ec26,0x25cfe,38672,38712,0x1ec28,0x25e58,39136,39072,0x1ec2a,0x25e5a,39152,39080,0x1ec2c,0x25e5c,39040,39088,0x1ec2e,0x25e5e,39056,39096,0x1ec30,0x25fb8,39488,39424,0x1ec32,0x25fba,39504,39432,0x1ec34,0x25fbc,39456,39440,0x1ec36,0x25fbe,39472,39448,0x1ec38,0x26118,39872,39808,0x1ec3a,0x2611a,39888,39816,0x1ec3c,0x2611c,39840,39824,0x1ec3e,0x2611e,39856,39832,0x1ec20,0x267f8,40224,40288,0x1ec22,0x267fa,40240,40296,0x1ec24,0x267fc,40256,40304,0x1ec26,0x267fe,40272,40312,0x1ec28,0x26958,40608,40672,0x1ec2a,0x2695a,40624,40680,0x1ec2c,0x2695c,40640,40688,0x1ec2e,0x2695e,40656,40696,0x1ec30,0x26ab8,40960,41024,0x1ec32,0x26aba,40976,41032,0x1ec34,0x26abc,41056,41040,0x1ec36,0x26abe,41072,41048,0x1ec38,0x26c18,41344,41408,0x1ec3a,0x26c1a,41360,41416,0x1ec3c,0x26c1c,41440,41424,0x1ec3e,0x26c1e,41456,41432,0x1ec20,0x100000,41824,41760,0x1ec22,0x100000,41840,41768,0x1ec24,0x100000,41728,41776,0x1ec26,0x100000,41744,41784,0x1ec28,0x100000,42208,42144,0x1ec2a,0x100000,42224,42152,0x1ec2c,0x100000,42112,42160,0x1ec2e,0x100000,42128,42168,0x1ec30,0x100000,42560,42496,0x1ec32,0x100000,42576,42504,0x1ec34,0x100000,42528,42512,0x1ec36,0x100000,42544,42520,0x1ec38,0x100000,42944,42880,0x1ec3a,0x100000,42960,42888,0x1ec3c,0x100000,42912,42896,0x1ec3e,0x100000,42928,42904,37152,37184,37216,64512,37160,37192,37224,64528,37168,37200,37232,64544,37176,37208,37240,64560,38304,38336,38368,64576,38312,38344,38376,64592,38320,38352,38384,64608,38328,38360,38392,64624,39424,39520,39488,64640,39432,39528,39496,64656,39440,39536,39504,64672,39448,39544,39512,64688,40576,40672,40640,64704,40584,40680,40648,64720,40592,40688,40656,64736,40600,40696,40664,64752,46432,46336,46368,64768,46440,46344,46376,64784,46448,46352,46384,64800,46456,46360,46392,64816,47584,47488,47520,64832,47592,47496,47528,64848,47600,47504,47536,64864,47608,47512,47544,64880,48704,48672,48640,64896,48712,48680,48648,64912,48720,48688,48656,64928,48728,48696,48664,64944,49856,49824,49792,64960,49864,49832,49800,64976,49872,49840,49808,64992,49880,49848,49816,65008,55584,55616,55648,65024,55592,55624,55656,65040,55600,55632,55664,65056,55608,55640,55672,65072,56736,56768,56800,65088,56744,56776,56808,65104,56752,56784,56816,65120,56760,56792,56824,65136,57856,57952,57920,65152,57864,57960,57928,65168,57872,57968,57936,65184,57880,57976,57944,65200,59008,59104,59072,65216,59016,59112,59080,65232,59024,59120,59088,65248,59032,59128,59096,65264,64864,64768,64800,65280,64872,64776,64808,65296,64880,64784,64816,65312,64888,64792,64824,65328,66016,65920,65952,65344,66024,65928,65960,65360,66032,65936,65968,65376,66040,65944,65976,65392,67136,67104,67072,65408,67144,67112,67080,65424,67152,67120,67088,65440,67160,67128,67096,65456,68288,68256,68224,65472,68296,68264,68232,65488,68304,68272,68240,65504,68312,68280,68248,65520,0x1e800,0x1e800,0x20cc0,55296,0x1e8c4,0x1e8c4,0x20cc4,55312,0x1e988,0x1e988,0x20cc8,55328,0x1ea4c,0x1ea4c,0x20ccc,55344,0x1eb10,0x1eb10,0x20cd0,55360,0x1ebd4,0x1ebd4,0x20cd4,55376,0x1ec98,0x1ec98,0x20cd8,55392,0x1ed5c,0x1ed5c,0x20cdc,55408,0x1ee20,0x1ee20,0x20ce0,55680,0x1eee4,0x1eee4,0x20ce4,55696,0x1efa8,0x1efa8,0x20ce8,55712,0x1f06c,0x1f06c,0x20cec,55728,0x1f130,0x1f130,0x20cf0,55744,0x1f1f4,0x1f1f4,0x20cf4,55760,0x1f2b8,0x1f2b8,0x20cf8,55776,0x1f37c,0x1f37c,0x20cfc,55792,0x1e810,0x1e800,0x20cc0,56064,0x1e8d4,0x1e8c4,0x20cc4,56080,0x1e998,0x1e988,0x20cc8,56096,0x1ea5c,0x1ea4c,0x20ccc,56112,0x1eb20,0x1eb10,0x20cd0,56128,0x1ebe4,0x1ebd4,0x20cd4,56144,0x1eca8,0x1ec98,0x20cd8,56160,0x1ed6c,0x1ed5c,0x20cdc,56176,0x1ee30,0x1ee20,0x20ce0,56448,0x1eef4,0x1eee4,0x20ce4,56464,0x1efb8,0x1efa8,0x20ce8,56480,0x1f07c,0x1f06c,0x20cec,56496,0x1f140,0x1f130,0x20cf0,56512,0x1f204,0x1f1f4,0x20cf4,56528,0x1f2c8,0x1f2b8,0x20cf8,56544,0x1f38c,0x1f37c,0x20cfc,56560,0x1e820,0x1e800,0x20cc0,56832,0x1e8e4,0x1e8c4,0x20cc4,56848,0x1e9a8,0x1e988,0x20cc8,56864,0x1ea6c,0x1ea4c,0x20ccc,56880,0x1eb30,0x1eb10,0x20cd0,56896,0x1ebf4,0x1ebd4,0x20cd4,56912,0x1ecb8,0x1ec98,0x20cd8,56928,0x1ed7c,0x1ed5c,0x20cdc,56944,0x1ee40,0x1ee20,0x20ce0,57216,0x1ef04,0x1eee4,0x20ce4,57232,0x1efc8,0x1efa8,0x20ce8,57248,0x1f08c,0x1f06c,0x20cec,57264,0x1f150,0x1f130,0x20cf0,57280,0x1f214,0x1f1f4,0x20cf4,57296,0x1f2d8,0x1f2b8,0x20cf8,57312,0x1f39c,0x1f37c,0x20cfc,57328,0x1e830,0x1e800,0x20cc0,57600,0x1e8f4,0x1e8c4,0x20cc4,57616,0x1e9b8,0x1e988,0x20cc8,57632,0x1ea7c,0x1ea4c,0x20ccc,57648,0x1eb40,0x1eb10,0x20cd0,57664,0x1ec04,0x1ebd4,0x20cd4,57680,0x1ecc8,0x1ec98,0x20cd8,57696,0x1ed8c,0x1ed5c,0x20cdc,57712,0x1ee50,0x1ee20,0x20ce0,57984,0x1ef14,0x1eee4,0x20ce4,58000,0x1efd8,0x1efa8,0x20ce8,58016,0x1f09c,0x1f06c,0x20cec,58032,0x1f160,0x1f130,0x20cf0,58048,0x1f224,0x1f1f4,0x20cf4,58064,0x1f2e8,0x1f2b8,0x20cf8,58080,0x1f3ac,0x1f37c,0x20cfc,58096,92160,0x27070,0x1ea40,0x1ec20,92176,0x27074,0x1eed0,0x1f0b0,92192,0x27078,0x1f360,0x1f540,92208,0x2707c,0x1f7f0,0x1f9d0,92224,0x27080,0x1fc80,0x1fe60,92240,0x27084,0x20110,0x202f0,92256,0x27088,0x205a0,0x20780,92272,0x2708c,0x20a30,0x20c10,92544,0x27090,0x20ec0,0x210a0,92560,0x27094,0x21350,0x21530,92576,0x27098,0x217e0,0x219c0,92592,0x2709c,0x21c70,0x21e50,92608,0x270a0,0x22100,0x222e0,92624,0x270a4,0x22590,0x22770,92640,0x270a8,0x22a20,0x22c00,92656,0x270ac,0x22eb0,0x23090,92960,0x270b0,0x1ea70,0x1ec28,92976,0x270b4,0x1ef00,0x1f0b8,92928,0x270b8,0x1f390,0x1f548,92944,0x270bc,0x1f820,0x1f9d8,93024,0x270c0,0x1fcb0,0x1fe68,93040,0x270c4,0x20140,0x202f8,92992,0x270c8,0x205d0,0x20788,93008,0x270cc,0x20a60,0x20c18,93344,0x270d0,0x20ef0,0x210a8,93360,0x270d4,0x21380,0x21538,93312,0x270d8,0x21810,0x219c8,93328,0x270dc,0x21ca0,0x21e58,93408,0x270e0,0x22130,0x222e8,93424,0x270e4,0x225c0,0x22778,93376,0x270e8,0x22a50,0x22c08,93392,0x270ec,0x22ee0,0x23098,93760,0x270f0,0x1eaa0,0x1ec30,93776,0x270f4,0x1ef30,0x1f0c0,93792,0x270f8,0x1f3c0,0x1f550,93808,0x270fc,0x1f850,0x1f9e0,93696,0x27100,0x1fce0,0x1fe70,93712,0x27104,0x20170,0x20300,93728,0x27108,0x20600,0x20790,93744,0x2710c,0x20a90,0x20c20,94144,0x27110,0x20f20,0x210b0,94160,0x27114,0x213b0,0x21540,94176,0x27118,0x21840,0x219d0,94192,0x2711c,0x21cd0,0x21e60,94080,0x27120,0x22160,0x222f0,94096,0x27124,0x225f0,0x22780,94112,0x27128,0x22a80,0x22c10,94128,0x2712c,0x22f10,0x230a0,94560,0x27130,0x1ead0,0x1ec38,94576,0x27134,0x1ef60,0x1f0c8,94528,0x27138,0x1f3f0,0x1f558,94544,0x2713c,0x1f880,0x1f9e8,94496,0x27140,0x1fd10,0x1fe78,94512,0x27144,0x201a0,0x20308,94464,0x27148,0x20630,0x20798,94480,0x2714c,0x20ac0,0x20c28,94944,0x27150,0x20f50,0x210b8,94960,0x27154,0x213e0,0x21548,94912,0x27158,0x21870,0x219d8,94928,0x2715c,0x21d00,0x21e68,94880,0x27160,0x22190,0x222f8,94896,0x27164,0x22620,0x22788,94848,0x27168,0x22ab0,0x22c18,94864,0x2716c,0x22f40,0x230a8,0x3f803f80,0x3f803f80,16256,0,0,0x3f803f80,0x3f800000,0,0,0x3f803f80,0,16256,0,0x3f803f80,0,0x3f800000,0x3f803f80,0,0,0,0,0,0,0,0,0,0,0,0,0,0,0,0x3f803f80,0,0,0,0,0,0,0,0,0,0,0,0,0,0,0,0x3f803f80,0,0,0,0,0,0,0,0,0,0,0,0,0,0,0,0x3f803f80,0,0,0,0,0,0,0,0,0,0,0,0,0,0,0,0x3f803f80,0x3f803f80,16256,0,0,0x3f803f80,0x3f800000,0,0,0x3f803f80,0,16256,0,0x3f803f80,0,0x3f800000,0x3f803f80,0,0,0,0,0,0,0,0,0,0,0,0,0,0,0,0x3f803f80,0,0,0,0,0,0,0,0,0,0,0,0,0,0,0,0x3f803f80,0,0,0,0,0,0,0,0,0,0,0,0,0,0,0,0x3f803f80,0,0,0,0,0,0,0,0,0,0,0,0,0,0,0,0x3f803f80,0x3f803f80,16256,0,0,0x3f803f80,0x3f800000,0,0,0x3f803f80,0,16256,0,0x3f803f80,0,0x3f800000,0x3f803f80,0,0,0,0,0,0,0,0,0,0,0,0,0,0,0,0x3f803f80,0,0,0,0,0,0,0,0,0,0,0,0,0,0,0,0x3f803f80,0,0,0,0,0,0,0,0,0,0,0,0,0,0,0,0x3f803f80,0,0,0,0,0,0,0,0,0,0,0,0,0,0,0,0x3f803f80,0x3f803f80,16256,0,0,0x3f803f80,0x3f800000,0,0,0x3f803f80,0,16256,0,0x3f803f80,0,0x3f800000,0xffffffff,0xffffffff,0,0,0,0,0xffffffff,0xff000000,0xff000000,0xffffff,0xff000000,0xff000000,0,4096,16384,0x1c000,0xffffffff,0xffffffff,0,0,0,0,0xffffff,0xffff0000,0xffff0000,65535,0xffffff,0xffffff,1024,5120,16896,0x1c0a0,0xffffffff,0xffffffff,0,0,0,0,65535,0xffffff00,0xff000000,0xffffff,0xffff0000,0xffff0000,2048,10240,17408,0x1c140,0xffffffff,0,0,0,0,0,255,0xffffffff,0xffff0000,65535,65535,65535,3072,11264,17920,0x1c1d0,0,0x3fb8aa3b,0x2a8cbccc,0x38fba882,0x7fff7fff,0x415db3d7,0xffff0000,0,0,65535,0,0xffff0000,0,0,0,0,0,808,1800,2608,3600,4408,5400,6208,7200,8192,9000,9992,10800,11792,12600,18432,14385,18447,16170,18462,18462,18462,18462,18462,20,19,0,0,0,0,0,0,0x1b0000,0x5b8480,0xae0a80,0xee8f00,0x1411500,0x1819980,0x1d41f80,0x214a400,0x2672a00,0x2b9b000,0x2fa3480,0x34cba80,0x38d3f00,0x3dfc500,0x4204980,0x60a80,0x4b2b680,0x6a800,0x5452380,0x74580,0x74580,0x74580,0x74580,0x74580,0x74580,0x74580,0x74580,0x74580,0x74580,0x74580,0x74580,0x74580,1,809,1801,2609,3601,4409,5401,6209,7201,8193,9001,9993,10801,11793,12601,13592,14386,15377,16171,17162,17671,17671,17671,17671,21,20,0,0,0,0,0,0,0x1b0a80,0x5b8f00,0xae1500,0xee9980,0x1411f80,0x181a400,0x1d42a00,0x214ae80,0x2673480,0x2b9ba80,0x2fa3f00,0x34cc500,0x38d4980,0x3dfcf80,0x4205400,0x472cf80,0x4b2c100,0x5053c80,0x5452e00,0x597a980,0x5c17500,0x5c17500,0x5c17500,0x5c17500,0x5c17500,0x5c17500,0x5c17500,0x5c17500,0x5c17500,0x5c17500,0x5c17500,0x5c17500,2,810,1802,2610,3602,4410,5402,6210,7202,8194,9002,9994,10802,11794,12602,13593,14387,15378,16172,17163,17672,18216,18216,18216,22,21,0,0,0,0,0,0,0x1b1500,0x5b9980,0xae1f80,0xeea400,0x1412a00,0x181ae80,0x1d43480,0x214b900,0x2673f00,0x2b9c500,0x2fa4980,0x34ccf80,0x38d5400,0x3dfda00,0x4205e80,0x472da00,0x4b2cb80,0x5054700,0x5453880,0x597b400,0x5c17f80,0x5ecba80,0x5ecba80,0x5ecba80,0x5ecba80,0x5ecba80,0x5ecba80,0x5ecba80,0x5ecba80,0x5ecba80,0x5ecba80,0x5ecba80,3,811,1803,2611,3603,4411,5403,6211,7203,8195,9003,9995,10803,11795,12603,13594,14388,15379,16173,17164,17673,17673,17673,17673,21,20,0,0,0,0,0,0,0x1b1f80,0x5ba400,0xae2a00,0xeeae80,0x1413480,0x181b900,0x1d43f00,0x214c380,0x2674980,0x2b9cf80,0x2fa5400,0x34cda00,0x38d5e80,0x3dfe480,0x4206900,0x472e480,0x4b2d600,0x5055180,0x5454300,0x597be80,0x5c18a00,0x5c18a00,0x5c18a00,0x5c18a00,0x5c18a00,0x5c18a00,0x5c18a00,0x5c18a00,0x5c18a00,0x5c18a00,0x5c18a00,0x5c18a00,4,812,1804,2612,3604,4412,5404,6212,7204,8196,9004,9996,10804,11796,12604,13595,14389,15380,16174,18463,17674,17674,17674,17674,21,20,0,0,0,0,0,0,0x1b2a00,0x5bae80,0xae3480,0xeeb900,0x1413f00,0x181c380,0x1d44980,0x214ce00,0x2675400,0x2b9da00,0x2fa5e80,0x34ce480,0x38d6900,0x3dfef00,0x4207380,0x472ef00,0x4b2e080,0x5055c00,0x5454d80,0x75000,0x5c19480,0x5c19480,0x5c19480,0x5c19480,0x5c19480,0x5c19480,0x5c19480,0x5c19480,0x5c19480,0x5c19480,0x5c19480,0x5c19480,5,813,1805,2613,3605,4413,5405,6213,7205,8197,9005,9997,10805,11797,12605,13596,14390,15381,16175,18464,18464,18464,18464,18464,20,19,0,0,0,0,0,0,0x1b3480,0x5bb900,0xae3f00,0xeec380,0x1414980,0x181ce00,0x1d45400,0x214d880,0x2675e80,0x2b9e480,0x2fa6900,0x34cef00,0x38d7380,0x3dff980,0x4207e00,0x472f980,0x4b2eb00,0x5056680,0x5455800,0x75a80,0x75a80,0x75a80,0x75a80,0x75a80,0x75a80,0x75a80,0x75a80,0x75a80,0x75a80,0x75a80,0x75a80,0x75a80,6,814,1806,2614,3606,4414,5406,6214,7206,8198,9006,9998,10806,11798,12606,13597,14391,15382,16176,18465,17675,17675,17675,17675,21,20,0,0,0,0,0,0,0x1b3f00,0x5bc380,0xae4980,0xeece00,0x1415400,0x181d880,0x1d45e80,0x214e300,0x2676900,0x2b9ef00,0x2fa7380,0x34cf980,0x38d7e00,0x3e00400,0x4208880,0x4730400,0x4b2f580,0x5057100,0x5456280,0x76500,0x5c19f00,0x5c19f00,0x5c19f00,0x5c19f00,0x5c19f00,0x5c19f00,0x5c19f00,0x5c19f00,0x5c19f00,0x5c19f00,0x5c19f00,0x5c19f00,7,815,1807,2615,3607,4415,5407,6215,7207,8199,9007,9999,10807,11799,12607,13598,14392,15383,16177,18466,18466,18466,18466,18466,20,19,0,0,0,0,0,0,0x1b4980,0x5bce00,0xae5400,0xeed880,0x1415e80,0x181e300,0x1d46900,0x214ed80,0x2677380,0x2b9f980,0x2fa7e00,0x34d0400,0x38d8880,0x3e00e80,0x4209300,0x4730e80,0x4b30000,0x5057b80,0x5456d00,0x76f80,0x76f80,0x76f80,0x76f80,0x76f80,0x76f80,0x76f80,0x76f80,0x76f80,0x76f80,0x76f80,0x76f80,0x76f80,8,816,1808,2616,3608,4416,5408,6400,7208,8200,9008,10000,10808,11800,12608,18433,14393,18448,16178,18467,18467,18467,18467,18467,20,19,0,0,0,0,0,0,0x1b5400,0x5bd880,0xae5e80,0xeee300,0x1416900,0x181ed80,0x1d47380,0x226f980,0x2677e00,0x2ba0400,0x2fa8880,0x34d0e80,0x38d9300,0x3e01900,0x4209d80,0x61500,0x4b30a80,0x6b280,0x5457780,0x77a00,0x77a00,0x77a00,0x77a00,0x77a00,0x77a00,0x77a00,0x77a00,0x77a00,0x77a00,0x77a00,0x77a00,0x77a00,9,817,1809,2617,3609,4417,5409,6401,7209,8201,9009,10001,10809,11801,12609,13599,14394,15384,16179,17165,17676,17676,17676,17676,21,20,0,0,0,0,0,0,0x1b5e80,0x5be300,0xae6900,0xeeed80,0x1417380,0x181f800,0x1d47e00,0x2270400,0x2678880,0x2ba0e80,0x2fa9300,0x34d1900,0x38d9d80,0x3e02380,0x420a800,0x4731900,0x4b31500,0x5058600,0x5458200,0x597c900,0x5c1a980,0x5c1a980,0x5c1a980,0x5c1a980,0x5c1a980,0x5c1a980,0x5c1a980,0x5c1a980,0x5c1a980,0x5c1a980,0x5c1a980,0x5c1a980,10,818,1810,2618,3610,4418,5410,6402,7210,8202,9010,10002,10810,11802,12610,13600,14395,15385,16180,17166,17677,18217,18217,18217,22,21,0,0,0,0,0,0,0x1b6900,0x5bed80,0xae7380,0xeef800,0x1417e00,0x1820280,0x1d48880,0x2270e80,0x2679300,0x2ba1900,0x2fa9d80,0x34d2380,0x38da800,0x3e02e00,0x420b280,0x4732380,0x4b31f80,0x5059080,0x5458c80,0x597d380,0x5c1b400,0x5ecc500,0x5ecc500,0x5ecc500,0x5ecc500,0x5ecc500,0x5ecc500,0x5ecc500,0x5ecc500,0x5ecc500,0x5ecc500,0x5ecc500,11,819,1811,2619,3611,4419,5411,6403,7211,8203,9011,10003,10811,11803,12611,13601,14396,15386,16181,17167,17678,17678,17678,17678,21,20,0,0,0,0,0,0,0x1b7380,0x5bf800,0xae7e00,0xef0280,0x1418880,0x1820d00,0x1d49300,0x2271900,0x2679d80,0x2ba2380,0x2faa800,0x34d2e00,0x38db280,0x3e03880,0x420bd00,0x4732e00,0x4b32a00,0x5059b00,0x5459700,0x597de00,0x5c1be80,0x5c1be80,0x5c1be80,0x5c1be80,0x5c1be80,0x5c1be80,0x5c1be80,0x5c1be80,0x5c1be80,0x5c1be80,0x5c1be80,0x5c1be80,12,820,1812,2620,3612,4420,5412,6404,7212,8204,9012,10004,10812,11804,12612,13602,14397,15387,16182,18468,17679,17679,17679,17679,21,20,0,0,0,0,0,0,0x1b7e00,0x5c0280,0xae8880,0xef0d00,0x1419300,0x1821780,0x1d49d80,0x2272380,0x267a800,0x2ba2e00,0x2fab280,0x34d3880,0x38dbd00,0x3e04300,0x420c780,0x4733880,0x4b33480,0x505a580,0x545a180,0x78480,0x5c1c900,0x5c1c900,0x5c1c900,0x5c1c900,0x5c1c900,0x5c1c900,0x5c1c900,0x5c1c900,0x5c1c900,0x5c1c900,0x5c1c900,0x5c1c900,13,821,1813,2621,3613,4421,5413,6405,7213,8205,9013,10005,10813,11805,12613,13603,14398,15388,16183,18469,18469,18469,18469,18469,20,19,0,0,0,0,0,0,0x1b8880,0x5c0d00,0xae9300,0xef1780,0x1419d80,0x1822200,0x1d4a800,0x2272e00,0x267b280,0x2ba3880,0x2fabd00,0x34d4300,0x38dc780,0x3e04d80,0x420d200,0x4734300,0x4b33f00,0x505b000,0x545ac00,0x78f00,0x78f00,0x78f00,0x78f00,0x78f00,0x78f00,0x78f00,0x78f00,0x78f00,0x78f00,0x78f00,0x78f00,0x78f00,14,822,1814,2622,3614,4422,5414,6406,7214,8206,9014,10006,10814,11806,12614,13604,14399,15389,16184,18470,17680,17680,17680,17680,21,20,0,0,0,0,0,0,0x1b9300,0x5c1780,0xae9d80,0xef2200,0x141a800,0x1822c80,0x1d4b280,0x2273880,0x267bd00,0x2ba4300,0x2fac780,0x34d4d80,0x38dd200,0x3e05800,0x420dc80,0x4734d80,0x4b34980,0x505ba80,0x545b680,0x79980,0x5c1d380,0x5c1d380,0x5c1d380,0x5c1d380,0x5c1d380,0x5c1d380,0x5c1d380,0x5c1d380,0x5c1d380,0x5c1d380,0x5c1d380,0x5c1d380,15,823,1815,2623,3615,4423,5415,6407,7215,8207,9015,10007,10815,11807,12615,13605,14400,15390,16185,18471,18471,18471,18471,18471,20,19,0,0,0,0,0,0,0x1b9d80,0x5c2200,0xaea800,0xef2c80,0x141b280,0x1823700,0x1d4bd00,0x2274300,0x267c780,0x2ba4d80,0x2fad200,0x34d5800,0x38ddc80,0x3e06280,0x420e700,0x4735800,0x4b35400,0x505c500,0x545c100,0x7a400,0x7a400,0x7a400,0x7a400,0x7a400,0x7a400,0x7a400,0x7a400,0x7a400,0x7a400,0x7a400,0x7a400,0x7a400,16,824,1816,2624,3616,4608,5416,6408,7216,8208,9016,10008,10816,11808,12800,18434,14401,18449,16186,18472,18472,18472,18472,18472,20,19,0,0,0,0,0,0,0x1ba800,0x5c2c80,0xaeb280,0xef3700,0x141bd00,0x1944300,0x1d4c780,0x2274d80,0x267d200,0x2ba5800,0x2fadc80,0x34d6280,0x38de700,0x3e06d00,0x432f300,0x61f80,0x4b35e80,0x6bd00,0x545cb80,0x7ae80,0x7ae80,0x7ae80,0x7ae80,0x7ae80,0x7ae80,0x7ae80,0x7ae80,0x7ae80,0x7ae80,0x7ae80,0x7ae80,0x7ae80,17,825,1817,2625,3617,4609,5417,6409,7217,8209,9017,10009,10817,11809,12801,13606,14402,15391,16187,17168,17681,17681,17681,17681,21,20,0,0,0,0,0,0,0x1bb280,0x5c3700,0xaebd00,0xef4180,0x141c780,0x1944d80,0x1d4d200,0x2275800,0x267dc80,0x2ba6280,0x2fae700,0x34d6d00,0x38df180,0x3e07780,0x432fd80,0x4736280,0x4b36900,0x505cf80,0x545d600,0x597e880,0x5c1de00,0x5c1de00,0x5c1de00,0x5c1de00,0x5c1de00,0x5c1de00,0x5c1de00,0x5c1de00,0x5c1de00,0x5c1de00,0x5c1de00,0x5c1de00,18,826,1818,2626,3618,4610,5418,6410,7218,8210,9018,10010,10818,11810,12802,13607,14403,15392,16188,17169,17682,18218,18218,18218,22,21,0,0,0,0,0,0,0x1bbd00,0x5c4180,0xaec780,0xef4c00,0x141d200,0x1945800,0x1d4dc80,0x2276280,0x267e700,0x2ba6d00,0x2faf180,0x34d7780,0x38dfc00,0x3e08200,0x4330800,0x4736d00,0x4b37380,0x505da00,0x545e080,0x597f300,0x5c1e880,0x5eccf80,0x5eccf80,0x5eccf80,0x5eccf80,0x5eccf80,0x5eccf80,0x5eccf80,0x5eccf80,0x5eccf80,0x5eccf80,0x5eccf80,19,827,1819,2627,3619,4611,5419,6411,7219,8211,9019,10011,10819,11811,12803,13608,14404,15393,16189,17170,17683,17683,17683,17683,21,20,0,0,0,0,0,0,0x1bc780,0x5c4c00,0xaed200,0xef5680,0x141dc80,0x1946280,0x1d4e700,0x2276d00,0x267f180,0x2ba7780,0x2fafc00,0x34d8200,0x38e0680,0x3e08c80,0x4331280,0x4737780,0x4b37e00,0x505e480,0x545eb00,0x597fd80,0x5c1f300,0x5c1f300,0x5c1f300,0x5c1f300,0x5c1f300,0x5c1f300,0x5c1f300,0x5c1f300,0x5c1f300,0x5c1f300,0x5c1f300,0x5c1f300,20,828,1820,2628,3620,4612,5420,6412,7220,8212,9020,10012,10820,11812,12804,13609,14405,15394,16190,18473,17684,17684,17684,17684,21,20,0,0,0,0,0,0,0x1bd200,0x5c5680,0xaedc80,0xef6100,0x141e700,0x1946d00,0x1d4f180,0x2277780,0x267fc00,0x2ba8200,0x2fb0680,0x34d8c80,0x38e1100,0x3e09700,0x4331d00,0x4738200,0x4b38880,0x505ef00,0x545f580,0x7b900,0x5c1fd80,0x5c1fd80,0x5c1fd80,0x5c1fd80,0x5c1fd80,0x5c1fd80,0x5c1fd80,0x5c1fd80,0x5c1fd80,0x5c1fd80,0x5c1fd80,0x5c1fd80,21,829,1821,2629,3621,4613,5421,6413,7221,8213,9021,10013,10821,11813,12805,13610,14406,15395,16191,18474,18474,18474,18474,18474,20,19,0,0,0,0,0,0,0x1bdc80,0x5c6100,0xaee700,0xef6b80,0x141f180,0x1947780,0x1d4fc00,0x2278200,0x2680680,0x2ba8c80,0x2fb1100,0x34d9700,0x38e1b80,0x3e0a180,0x4332780,0x4738c80,0x4b39300,0x505f980,0x5460000,0x7c380,0x7c380,0x7c380,0x7c380,0x7c380,0x7c380,0x7c380,0x7c380,0x7c380,0x7c380,0x7c380,0x7c380,0x7c380,22,830,1822,2630,3622,4614,5422,6414,7222,8214,9022,10014,10822,11814,12806,13611,14407,15396,16192,18475,17685,17685,17685,17685,21,20,0,0,0,0,0,0,0x1be700,0x5c6b80,0xaef180,0xef7600,0x141fc00,0x1948200,0x1d50680,0x2278c80,0x2681100,0x2ba9700,0x2fb1b80,0x34da180,0x38e2600,0x3e0ac00,0x4333200,0x4739700,0x4b39d80,0x5060400,0x5460a80,0x7ce00,0x5c20800,0x5c20800,0x5c20800,0x5c20800,0x5c20800,0x5c20800,0x5c20800,0x5c20800,0x5c20800,0x5c20800,0x5c20800,0x5c20800,23,831,1823,2631,3623,4615,5423,6415,7223,8215,9023,10015,10823,11815,12807,13612,14592,15397,16193,18476,18476,18476,18476,18476,20,19,0,0,0,0,0,0,0x1bf180,0x5c7600,0xaefc00,0xef8080,0x1420680,0x1948c80,0x1d51100,0x2279700,0x2681b80,0x2baa180,0x2fb2600,0x34dac00,0x38e3080,0x3e0b680,0x4333c80,0x473a180,0x4c5a980,0x5060e80,0x5461500,0x7d880,0x7d880,0x7d880,0x7d880,0x7d880,0x7d880,0x7d880,0x7d880,0x7d880,0x7d880,0x7d880,0x7d880,0x7d880,24,832,1824,2816,3624,4616,5424,6416,7224,8216,9024,10016,11008,11816,12808,18435,14593,18450,16194,18477,18477,18477,18477,18477,20,19,0,0,0,0,0,0,0x1bfc00,0x5c8080,0xaf0680,0x1018c80,0x1421100,0x1949700,0x1d51b80,0x227a180,0x2682600,0x2baac00,0x2fb3080,0x34db680,0x3a03c80,0x3e0c100,0x4334700,0x62a00,0x4c5b400,0x6c780,0x5461f80,0x7e300,0x7e300,0x7e300,0x7e300,0x7e300,0x7e300,0x7e300,0x7e300,0x7e300,0x7e300,0x7e300,0x7e300,0x7e300,25,833,1825,2817,3625,4617,5425,6417,7225,8217,9025,10017,11009,11817,12809,13613,14594,15398,16195,17171,17686,17686,17686,17686,21,20,0,0,0,0,0,0,0x1c0680,0x5c8b00,0xaf1100,0x1019700,0x1421b80,0x194a180,0x1d52600,0x227ac00,0x2683080,0x2bab680,0x2fb3b00,0x34dc100,0x3a04700,0x3e0cb80,0x4335180,0x473ac00,0x4c5be80,0x5061900,0x5462a00,0x5980800,0x5c21280,0x5c21280,0x5c21280,0x5c21280,0x5c21280,0x5c21280,0x5c21280,0x5c21280,0x5c21280,0x5c21280,0x5c21280,0x5c21280,26,834,1826,2818,3626,4618,5426,6418,7226,8218,9026,10018,11010,11818,12810,13614,14595,15399,16196,17172,17687,18219,18219,18219,22,21,0,0,0,0,0,0,0x1c1100,0x5c9580,0xaf1b80,0x101a180,0x1422600,0x194ac00,0x1d53080,0x227b680,0x2683b00,0x2bac100,0x2fb4580,0x34dcb80,0x3a05180,0x3e0d600,0x4335c00,0x473b680,0x4c5c900,0x5062380,0x5463480,0x5981280,0x5c21d00,0x5ecda00,0x5ecda00,0x5ecda00,0x5ecda00,0x5ecda00,0x5ecda00,0x5ecda00,0x5ecda00,0x5ecda00,0x5ecda00,0x5ecda00,27,835,1827,2819,3627,4619,5427,6419,7227,8219,9027,10019,11011,11819,12811,13615,14596,15400,16197,17173,17688,17688,17688,17688,21,20,0,0,0,0,0,0,0x1c1b80,0x5ca000,0xaf2600,0x101ac00,0x1423080,0x194b680,0x1d53b00,0x227c100,0x2684580,0x2bacb80,0x2fb5000,0x34dd600,0x3a05c00,0x3e0e080,0x4336680,0x473c100,0x4c5d380,0x5062e00,0x5463f00,0x5981d00,0x5c22780,0x5c22780,0x5c22780,0x5c22780,0x5c22780,0x5c22780,0x5c22780,0x5c22780,0x5c22780,0x5c22780,0x5c22780,0x5c22780,28,836,1828,2820,3628,4620,5428,6420,7228,8220,9028,10020,11012,11820,12812,13616,14597,15401,16198,18478,17689,17689,17689,17689,21,20,0,0,0,0,0,0,0x1c2600,0x5caa80,0xaf3080,0x101b680,0x1423b00,0x194c100,0x1d54580,0x227cb80,0x2685000,0x2bad600,0x2fb5a80,0x34de080,0x3a06680,0x3e0eb00,0x4337100,0x473cb80,0x4c5de00,0x5063880,0x5464980,0x7ed80,0x5c23200,0x5c23200,0x5c23200,0x5c23200,0x5c23200,0x5c23200,0x5c23200,0x5c23200,0x5c23200,0x5c23200,0x5c23200,0x5c23200,29,837,1829,2821,3629,4621,5429,6421,7229,8221,9029,10021,11013,11821,12813,13617,14598,15402,16199,18479,18479,18479,18479,18479,20,19,0,0,0,0,0,0,0x1c3080,0x5cb500,0xaf3b00,0x101c100,0x1424580,0x194cb80,0x1d55000,0x227d600,0x2685a80,0x2bae080,0x2fb6500,0x34deb00,0x3a07100,0x3e0f580,0x4337b80,0x473d600,0x4c5e880,0x5064300,0x5465400,0x7f800,0x7f800,0x7f800,0x7f800,0x7f800,0x7f800,0x7f800,0x7f800,0x7f800,0x7f800,0x7f800,0x7f800,0x7f800,30,838,1830,2822,3630,4622,5430,6422,7230,8222,9030,10022,11014,11822,12814,13618,14599,15403,16384,18480,17690,17690,17690,17690,21,20,0,0,0,0,0,0,0x1c3b00,0x5cbf80,0xaf4580,0x101cb80,0x1425000,0x194d600,0x1d55a80,0x227e080,0x2686500,0x2baeb00,0x2fb6f80,0x34df580,0x3a07b80,0x3e10000,0x4338600,0x473e080,0x4c5f300,0x5064d80,0x5586000,0x80280,0x5c23c80,0x5c23c80,0x5c23c80,0x5c23c80,0x5c23c80,0x5c23c80,0x5c23c80,0x5c23c80,0x5c23c80,0x5c23c80,0x5c23c80,0x5c23c80,31,839,1831,2823,3631,4623,5431,6423,7231,8223,9031,10023,11015,11823,12815,13619,14600,15404,16385,18481,18481,18481,18481,18481,20,19,0,0,0,0,0,0,0x1c4580,0x5cca00,0xaf5000,0x101d600,0x1425a80,0x194e080,0x1d56500,0x227eb00,0x2686f80,0x2baf580,0x2fb7a00,0x34e0000,0x3a08600,0x3e10a80,0x4339080,0x473eb00,0x4c5fd80,0x5065800,0x5586a80,0x80d00,0x80d00,0x80d00,0x80d00,0x80d00,0x80d00,0x80d00,0x80d00,0x80d00,0x80d00,0x80d00,0x80d00,0x80d00,32,1024,1832,2824,3632,4624,5432,6424,7232,8224,9216,10024,11016,11824,12816,18436,14601,18451,16386,18482,18482,18482,18482,18482,20,19,0,0,0,0,0,0,0x1c5000,0x6ed600,0xaf5a80,0x101e080,0x1426500,0x194eb00,0x1d56f80,0x227f580,0x2687a00,0x2bb0000,0x30d8600,0x34e0a80,0x3a09080,0x3e11500,0x4339b00,0x63480,0x4c60800,0x6d200,0x5587500,0x81780,0x81780,0x81780,0x81780,0x81780,0x81780,0x81780,0x81780,0x81780,0x81780,0x81780,0x81780,0x81780,33,1025,1833,2825,3633,4625,5433,6425,7233,8225,9217,10025,11017,11825,12817,13620,14602,15405,16387,17174,17691,17691,17691,17691,21,20,0,0,0,0,0,0,0x1c5a80,0x6ee080,0xaf6500,0x101eb00,0x1426f80,0x194f580,0x1d57a00,0x2280000,0x2688480,0x2bb0a80,0x30d9080,0x34e1500,0x3a09b00,0x3e11f80,0x433a580,0x473f580,0x4c61280,0x5066280,0x5587f80,0x5982780,0x5c24700,0x5c24700,0x5c24700,0x5c24700,0x5c24700,0x5c24700,0x5c24700,0x5c24700,0x5c24700,0x5c24700,0x5c24700,0x5c24700,34,1026,1834,2826,3634,4626,5434,6426,7234,8226,9218,10026,11018,11826,12818,13621,14603,15406,16388,17175,17692,18220,18220,18220,22,21,0,0,0,0,0,0,0x1c6500,0x6eeb00,0xaf6f80,0x101f580,0x1427a00,0x1950000,0x1d58480,0x2280a80,0x2688f00,0x2bb1500,0x30d9b00,0x34e1f80,0x3a0a580,0x3e12a00,0x433b000,0x4740000,0x4c61d00,0x5066d00,0x5588a00,0x5983200,0x5c25180,0x5ece480,0x5ece480,0x5ece480,0x5ece480,0x5ece480,0x5ece480,0x5ece480,0x5ece480,0x5ece480,0x5ece480,0x5ece480,35,1027,1835,2827,3635,4627,5435,6427,7235,8227,9219,10027,11019,11827,12819,13622,14604,15407,16389,17176,17693,17693,17693,17693,21,20,0,0,0,0,0,0,0x1c6f80,0x6ef580,0xaf7a00,0x1020000,0x1428480,0x1950a80,0x1d58f00,0x2281500,0x2689980,0x2bb1f80,0x30da580,0x34e2a00,0x3a0b000,0x3e13480,0x433ba80,0x4740a80,0x4c62780,0x5067780,0x5589480,0x5983c80,0x5c25c00,0x5c25c00,0x5c25c00,0x5c25c00,0x5c25c00,0x5c25c00,0x5c25c00,0x5c25c00,0x5c25c00,0x5c25c00,0x5c25c00,0x5c25c00,36,1028,1836,2828,3636,4628,5436,6428,7236,8228,9220,10028,11020,11828,12820,13623,14605,15408,16390,17177,17694,17694,17694,17694,21,20,0,0,0,0,0,0,0x1c7a00,0x6f0000,0xaf8480,0x1020a80,0x1428f00,0x1951500,0x1d59980,0x2281f80,0x268a400,0x2bb2a00,0x30db000,0x34e3480,0x3a0ba80,0x3e13f00,0x433c500,0x4741500,0x4c63200,0x5068200,0x5589f00,0x5984700,0x5c26680,0x5c26680,0x5c26680,0x5c26680,0x5c26680,0x5c26680,0x5c26680,0x5c26680,0x5c26680,0x5c26680,0x5c26680,0x5c26680,37,1029,1837,2829,3637,4629,5437,6429,7237,8229,9221,10029,11021,11829,12821,13624,14606,15409,16391,18483,18483,18483,18483,18483,20,19,0,0,0,0,0,0,0x1c8480,0x6f0a80,0xaf8f00,0x1021500,0x1429980,0x1951f80,0x1d5a400,0x2282a00,0x268ae80,0x2bb3480,0x30dba80,0x34e3f00,0x3a0c500,0x3e14980,0x433cf80,0x4741f80,0x4c63c80,0x5068c80,0x558a980,0x82200,0x82200,0x82200,0x82200,0x82200,0x82200,0x82200,0x82200,0x82200,0x82200,0x82200,0x82200,0x82200,38,1030,1838,2830,3638,4630,5438,6430,7238,8230,9222,10030,11022,11830,12822,13625,14607,15410,16392,18484,17695,17695,17695,17695,21,20,0,0,0,0,0,0,0x1c8f00,0x6f1500,0xaf9980,0x1021f80,0x142a400,0x1952a00,0x1d5ae80,0x2283480,0x268b900,0x2bb3f00,0x30dc500,0x34e4980,0x3a0cf80,0x3e15400,0x433da00,0x4742a00,0x4c64700,0x5069700,0x558b400,0x82c80,0x5c27100,0x5c27100,0x5c27100,0x5c27100,0x5c27100,0x5c27100,0x5c27100,0x5c27100,0x5c27100,0x5c27100,0x5c27100,0x5c27100,39,1031,1839,2831,3639,4631,5439,6431,7239,8231,9223,10031,11023,11831,12823,13626,14608,15411,16393,18485,18485,18485,18485,18485,20,19,0,0,0,0,0,0,0x1c9980,0x6f1f80,0xafa400,0x1022a00,0x142ae80,0x1953480,0x1d5b900,0x2283f00,0x268c380,0x2bb4980,0x30dcf80,0x34e5400,0x3a0da00,0x3e15e80,0x433e480,0x4743480,0x4c65180,0x506a180,0x558be80,0x83700,0x83700,0x83700,0x83700,0x83700,0x83700,0x83700,0x83700,0x83700,0x83700,0x83700,0x83700,0x83700,40,1032,1840,2832,3640,4632,5440,6432,7424,8232,9224,10032,11024,11832,12824,18437,14609,18452,16394,18486,18486,18486,18486,18486,20,19,0,0,0,0,0,0,0x1ca400,0x6f2a00,0xafae80,0x1023480,0x142b900,0x1953f00,0x1d5c380,0x2284980,0x27acf80,0x2bb5400,0x30dda00,0x34e5e80,0x3a0e480,0x3e16900,0x433ef00,0x63f00,0x4c65c00,0x6dc80,0x558c900,0x84180,0x84180,0x84180,0x84180,0x84180,0x84180,0x84180,0x84180,0x84180,0x84180,0x84180,0x84180,0x84180,41,1033,1841,2833,3641,4633,5441,6433,7425,8233,9225,10033,11025,11833,12825,13627,14610,15412,16395,17178,17696,17696,17696,17696,21,20,0,0,0,0,0,0,0x1cae80,0x6f3480,0xafb900,0x1023f00,0x142c380,0x1954980,0x1d5ce00,0x2285400,0x27ada00,0x2bb5e80,0x30de480,0x34e6900,0x3a0ef00,0x3e17380,0x433f980,0x4743f00,0x4c66680,0x506ac00,0x558d380,0x5985180,0x5c27b80,0x5c27b80,0x5c27b80,0x5c27b80,0x5c27b80,0x5c27b80,0x5c27b80,0x5c27b80,0x5c27b80,0x5c27b80,0x5c27b80,0x5c27b80,42,1034,1842,2834,3642,4634,5442,6434,7426,8234,9226,10034,11026,11834,12826,13628,14611,15413,16396,17179,17697,18221,18221,18221,22,21,0,0,0,0,0,0,0x1cb900,0x6f3f00,0xafc380,0x1024980,0x142ce00,0x1955400,0x1d5d880,0x2285e80,0x27ae480,0x2bb6900,0x30def00,0x34e7380,0x3a0f980,0x3e17e00,0x4340400,0x4744980,0x4c67100,0x506b680,0x558de00,0x5985c00,0x5c28600,0x5ecef00,0x5ecef00,0x5ecef00,0x5ecef00,0x5ecef00,0x5ecef00,0x5ecef00,0x5ecef00,0x5ecef00,0x5ecef00,0x5ecef00,43,1035,1843,2835,3643,4635,5443,6435,7427,8235,9227,10035,11027,11835,12827,13629,14612,15414,16397,17180,17698,17698,17698,17698,21,20,0,0,0,0,0,0,0x1cc380,0x6f4980,0xafce00,0x1025400,0x142d880,0x1955e80,0x1d5e300,0x2286900,0x27aef00,0x2bb7380,0x30df980,0x34e7e00,0x3a10400,0x3e18880,0x4340e80,0x4745400,0x4c67b80,0x506c100,0x558e880,0x5986680,0x5c29080,0x5c29080,0x5c29080,0x5c29080,0x5c29080,0x5c29080,0x5c29080,0x5c29080,0x5c29080,0x5c29080,0x5c29080,0x5c29080,44,1036,1844,2836,3644,4636,5444,6436,7428,8236,9228,10036,11028,11836,12828,13630,14613,15415,16398,17181,17699,17699,17699,17699,21,20,0,0,0,0,0,0,0x1cce00,0x6f5400,0xafd880,0x1025e80,0x142e300,0x1956900,0x1d5ed80,0x2287380,0x27af980,0x2bb7e00,0x30e0400,0x34e8880,0x3a10e80,0x3e19300,0x4341900,0x4745e80,0x4c68600,0x506cb80,0x558f300,0x5987100,0x5c29b00,0x5c29b00,0x5c29b00,0x5c29b00,0x5c29b00,0x5c29b00,0x5c29b00,0x5c29b00,0x5c29b00,0x5c29b00,0x5c29b00,0x5c29b00,45,1037,1845,2837,3645,4637,5445,6437,7429,8237,9229,10037,11029,11837,12829,13631,14614,15416,16399,18487,18487,18487,18487,18487,20,19,0,0,0,0,0,0,0x1cd880,0x6f5e80,0xafe300,0x1026900,0x142ed80,0x1957380,0x1d5f800,0x2287e00,0x27b0400,0x2bb8880,0x30e0e80,0x34e9300,0x3a11900,0x3e19d80,0x4342380,0x4746900,0x4c69080,0x506d600,0x558fd80,0x84c00,0x84c00,0x84c00,0x84c00,0x84c00,0x84c00,0x84c00,0x84c00,0x84c00,0x84c00,0x84c00,0x84c00,0x84c00,46,1038,1846,2838,3646,4638,5446,6438,7430,8238,9230,10038,11030,11838,12830,13632,14615,15417,16400,18488,17700,17700,17700,17700,21,20,0,0,0,0,0,0,0x1ce300,0x6f6900,0xafed80,0x1027380,0x142f800,0x1957e00,0x1d60280,0x2288880,0x27b0e80,0x2bb9300,0x30e1900,0x34e9d80,0x3a12380,0x3e1a800,0x4342e00,0x4747380,0x4c69b00,0x506e080,0x5590800,0x85680,0x5c2a580,0x5c2a580,0x5c2a580,0x5c2a580,0x5c2a580,0x5c2a580,0x5c2a580,0x5c2a580,0x5c2a580,0x5c2a580,0x5c2a580,0x5c2a580,47,1039,1847,2839,3647,4639,5447,6439,7431,8239,9231,10039,11031,11839,12831,13633,14616,15418,16401,18489,18489,18489,18489,18489,20,19,0,0,0,0,0,0,0x1ced80,0x6f7380,0xaff800,0x1027e00,0x1430280,0x1958880,0x1d60d00,0x2289300,0x27b1900,0x2bb9d80,0x30e2380,0x34ea800,0x3a12e00,0x3e1b280,0x4343880,0x4747e00,0x4c6a580,0x506eb00,0x5591280,0x86100,0x86100,0x86100,0x86100,0x86100,0x86100,0x86100,0x86100,0x86100,0x86100,0x86100,0x86100,0x86100,48,1040,1848,2840,3648,4640,5632,6440,7432,8240,9232,10040,11032,11840,12832,18438,14617,18453,16402,18490,18490,18490,18490,18490,20,19,0,0,0,0,0,0,0x1cf800,0x6f7e00,0xb00280,0x1028880,0x1430d00,0x1959300,0x1e81900,0x2289d80,0x27b2380,0x2bba800,0x30e2e00,0x34eb280,0x3a13880,0x3e1bd00,0x4344300,0x64980,0x4c6b000,0x6e700,0x5591d00,0x86b80,0x86b80,0x86b80,0x86b80,0x86b80,0x86b80,0x86b80,0x86b80,0x86b80,0x86b80,0x86b80,0x86b80,0x86b80,49,1041,1849,2841,3649,4641,5633,6441,7433,8241,9233,10041,11033,11841,12833,13634,14618,15419,16403,17182,17701,17701,17701,17701,21,20,0,0,0,0,0,0,0x1d0280,0x6f8880,0xb00d00,0x1029300,0x1431780,0x1959d80,0x1e82380,0x228a800,0x27b2e00,0x2bbb280,0x30e3880,0x34ebd00,0x3a14300,0x3e1c780,0x4344d80,0x4748880,0x4c6ba80,0x506f580,0x5592780,0x5987b80,0x5c2b000,0x5c2b000,0x5c2b000,0x5c2b000,0x5c2b000,0x5c2b000,0x5c2b000,0x5c2b000,0x5c2b000,0x5c2b000,0x5c2b000,0x5c2b000,50,1042,1850,2842,3650,4642,5634,6442,7434,8242,9234,10042,11034,11842,12834,13635,14619,15420,16404,17183,17702,18222,18222,18222,22,21,0,0,0,0,0,0,0x1d0d00,0x6f9300,0xb01780,0x1029d80,0x1432200,0x195a800,0x1e82e00,0x228b280,0x27b3880,0x2bbbd00,0x30e4300,0x34ec780,0x3a14d80,0x3e1d200,0x4345800,0x4749300,0x4c6c500,0x5070000,0x5593200,0x5988600,0x5c2ba80,0x5ecf980,0x5ecf980,0x5ecf980,0x5ecf980,0x5ecf980,0x5ecf980,0x5ecf980,0x5ecf980,0x5ecf980,0x5ecf980,0x5ecf980,51,1043,1851,2843,3651,4643,5635,6443,7435,8243,9235,10043,11035,11843,12835,13636,14620,15421,16405,17184,17703,17703,17703,17703,21,20,0,0,0,0,0,0,0x1d1780,0x6f9d80,0xb02200,0x102a800,0x1432c80,0x195b280,0x1e83880,0x228bd00,0x27b4300,0x2bbc780,0x30e4d80,0x34ed200,0x3a15800,0x3e1dc80,0x4346280,0x4749d80,0x4c6cf80,0x5070a80,0x5593c80,0x5989080,0x5c2c500,0x5c2c500,0x5c2c500,0x5c2c500,0x5c2c500,0x5c2c500,0x5c2c500,0x5c2c500,0x5c2c500,0x5c2c500,0x5c2c500,0x5c2c500,52,1044,1852,2844,3652,4644,5636,6444,7436,8244,9236,10044,11036,11844,12836,13637,14621,15422,16406,17185,17704,17704,17704,17704,21,20,0,0,0,0,0,0,0x1d2200,0x6fa800,0xb02c80,0x102b280,0x1433700,0x195bd00,0x1e84300,0x228c780,0x27b4d80,0x2bbd200,0x30e5800,0x34edc80,0x3a16280,0x3e1e700,0x4346d00,0x474a800,0x4c6da00,0x5071500,0x5594700,0x5989b00,0x5c2cf80,0x5c2cf80,0x5c2cf80,0x5c2cf80,0x5c2cf80,0x5c2cf80,0x5c2cf80,0x5c2cf80,0x5c2cf80,0x5c2cf80,0x5c2cf80,0x5c2cf80,53,1045,1853,2845,3653,4645,5637,6445,7437,8245,9237,10045,11037,11845,12837,13638,14622,15423,16407,18491,18491,18491,18491,18491,20,19,0,0,0,0,0,0,0x1d2c80,0x6fb280,0xb03700,0x102bd00,0x1434180,0x195c780,0x1e84d80,0x228d200,0x27b5800,0x2bbdc80,0x30e6280,0x34ee700,0x3a16d00,0x3e1f180,0x4347780,0x474b280,0x4c6e480,0x5071f80,0x5595180,0x87600,0x87600,0x87600,0x87600,0x87600,0x87600,0x87600,0x87600,0x87600,0x87600,0x87600,0x87600,0x87600,54,1046,1854,2846,3654,4646,5638,6446,7438,8246,9238,10046,11038,11846,12838,13639,14623,15424,16408,18492,17705,17705,17705,17705,21,20,0,0,0,0,0,0,0x1d3700,0x6fbd00,0xb04180,0x102c780,0x1434c00,0x195d200,0x1e85800,0x228dc80,0x27b6280,0x2bbe700,0x30e6d00,0x34ef180,0x3a17780,0x3e1fc00,0x4348200,0x474bd00,0x4c6ef00,0x5072a00,0x5595c00,0x88080,0x5c2da00,0x5c2da00,0x5c2da00,0x5c2da00,0x5c2da00,0x5c2da00,0x5c2da00,0x5c2da00,0x5c2da00,0x5c2da00,0x5c2da00,0x5c2da00,55,1047,1855,2847,3655,4647,5639,6447,7439,8247,9239,10047,11039,11847,12839,13824,14624,15425,16409,18493,18493,18493,18493,18493,20,19,0,0,0,0,0,0,0x1d4180,0x6fc780,0xb04c00,0x102d200,0x1435680,0x195dc80,0x1e86280,0x228e700,0x27b6d00,0x2bbf180,0x30e7780,0x34efc00,0x3a18200,0x3e20680,0x4348c80,0x486c900,0x4c6f980,0x5073480,0x5596680,0x88b00,0x88b00,0x88b00,0x88b00,0x88b00,0x88b00,0x88b00,0x88b00,0x88b00,0x88b00,0x88b00,0x88b00,0x88b00,56,1048,1856,2848,3840,4648,5640,6448,7440,8248,9240,10048,11040,12032,12840,18439,14625,18454,16410,18494,18494,18494,18494,18494,20,19,0,0,0,0,0,0,0x1d4c00,0x6fd200,0xb05680,0x102dc80,0x1556280,0x195e700,0x1e86d00,0x228f180,0x27b7780,0x2bbfc00,0x30e8200,0x34f0680,0x3a18c80,0x3f41280,0x4349700,0x65400,0x4c70400,0x6f180,0x5597100,0x89580,0x89580,0x89580,0x89580,0x89580,0x89580,0x89580,0x89580,0x89580,0x89580,0x89580,0x89580,0x89580,57,1049,1857,2849,3841,4649,5641,6449,7441,8249,9241,10049,11041,12033,12841,13825,14626,15426,16411,17186,17706,17706,17706,17706,21,20,0,0,0,0,0,0,0x1d5680,0x6fdc80,0xb06100,0x102e700,0x1556d00,0x195f180,0x1e87780,0x228fc00,0x27b8200,0x2bc0680,0x30e8c80,0x34f1100,0x3a19700,0x3f41d00,0x434a180,0x486d380,0x4c70e80,0x5073f00,0x5597b80,0x598a580,0x5c2e480,0x5c2e480,0x5c2e480,0x5c2e480,0x5c2e480,0x5c2e480,0x5c2e480,0x5c2e480,0x5c2e480,0x5c2e480,0x5c2e480,0x5c2e480,58,1050,1858,2850,3842,4650,5642,6450,7442,8250,9242,10050,11042,12034,12842,13826,14627,15427,16412,17187,17707,18223,18223,18223,22,21,0,0,0,0,0,0,0x1d6100,0x6fe700,0xb06b80,0x102f180,0x1557780,0x195fc00,0x1e88200,0x2290680,0x27b8c80,0x2bc1100,0x30e9700,0x34f1b80,0x3a1a180,0x3f42780,0x434ac00,0x486de00,0x4c71900,0x5074980,0x5598600,0x598b000,0x5c2ef00,0x5ed0400,0x5ed0400,0x5ed0400,0x5ed0400,0x5ed0400,0x5ed0400,0x5ed0400,0x5ed0400,0x5ed0400,0x5ed0400,0x5ed0400,59,1051,1859,2851,3843,4651,5643,6451,7443,8251,9243,10051,11043,12035,12843,13827,14628,15428,16413,17188,17708,17708,17708,17708,21,20,0,0,0,0,0,0,0x1d6b80,0x6ff180,0xb07600,0x102fc00,0x1558200,0x1960680,0x1e88c80,0x2291100,0x27b9700,0x2bc1b80,0x30ea180,0x34f2600,0x3a1ac00,0x3f43200,0x434b680,0x486e880,0x4c72380,0x5075400,0x5599080,0x598ba80,0x5c2f980,0x5c2f980,0x5c2f980,0x5c2f980,0x5c2f980,0x5c2f980,0x5c2f980,0x5c2f980,0x5c2f980,0x5c2f980,0x5c2f980,0x5c2f980,60,1052,1860,2852,3844,4652,5644,6452,7444,8252,9244,10052,11044,12036,12844,13828,14629,15429,16414,17189,17709,17709,17709,17709,21,20,0,0,0,0,0,0,0x1d7600,0x6ffc00,0xb08080,0x1030680,0x1558c80,0x1961100,0x1e89700,0x2291b80,0x27ba180,0x2bc2600,0x30eac00,0x34f3080,0x3a1b680,0x3f43c80,0x434c100,0x486f300,0x4c72e00,0x5075e80,0x5599b00,0x598c500,0x5c30400,0x5c30400,0x5c30400,0x5c30400,0x5c30400,0x5c30400,0x5c30400,0x5c30400,0x5c30400,0x5c30400,0x5c30400,0x5c30400,61,1053,1861,2853,3845,4653,5645,6453,7445,8253,9245,10053,11045,12037,12845,13829,14630,15430,16415,18495,18495,18495,18495,18495,20,19,0,0,0,0,0,0,0x1d8080,0x700680,0xb08b00,0x1031100,0x1559700,0x1961b80,0x1e8a180,0x2292600,0x27bac00,0x2bc3080,0x30eb680,0x34f3b00,0x3a1c100,0x3f44700,0x434cb80,0x486fd80,0x4c73880,0x5076900,0x559a580,0x8a000,0x8a000,0x8a000,0x8a000,0x8a000,0x8a000,0x8a000,0x8a000,0x8a000,0x8a000,0x8a000,0x8a000,0x8a000,62,1054,1862,2854,3846,4654,5646,6454,7446,8254,9246,10054,11046,12038,12846,13830,14631,15431,16416,18496,17710,17710,17710,17710,21,20,0,0,0,0,0,0,0x1d8b00,0x701100,0xb09580,0x1031b80,0x155a180,0x1962600,0x1e8ac00,0x2293080,0x27bb680,0x2bc3b00,0x30ec100,0x34f4580,0x3a1cb80,0x3f45180,0x434d600,0x4870800,0x4c74300,0x5077380,0x559b000,0x8aa80,0x5c30e80,0x5c30e80,0x5c30e80,0x5c30e80,0x5c30e80,0x5c30e80,0x5c30e80,0x5c30e80,0x5c30e80,0x5c30e80,0x5c30e80,0x5c30e80,63,1055,1863,2855,3847,4655,5647,6455,7447,8255,9247,10055,11047,12039,12847,13831,14632,15616,16417,18497,18497,18497,18497,18497,20,19,0,0,0,0,0,0,0x1d9580,0x701b80,0xb0a000,0x1032600,0x155ac00,0x1963080,0x1e8b680,0x2293b00,0x27bc100,0x2bc4580,0x30ecb80,0x34f5000,0x3a1d600,0x3f45c00,0x434e080,0x4871280,0x4c74d80,0x5197f80,0x559ba80,0x8b500,0x8b500,0x8b500,0x8b500,0x8b500,0x8b500,0x8b500,0x8b500,0x8b500,0x8b500,0x8b500,0x8b500,0x8b500,64,1056,2048,2856,3848,4656,5648,6456,7448,8256,9248,10240,11048,12040,12848,18440,14633,18455,16418,18498,18498,18498,18498,18498,20,19,0,0,0,0,0,0,0x1da000,0x702600,0xc2ac00,0x1033080,0x155b680,0x1963b00,0x1e8c100,0x2294580,0x27bcb80,0x2bc5000,0x30ed600,0x3615c00,0x3a1e080,0x3f46680,0x434eb00,0x65e80,0x4c75800,0x6fc00,0x559c500,0x8bf80,0x8bf80,0x8bf80,0x8bf80,0x8bf80,0x8bf80,0x8bf80,0x8bf80,0x8bf80,0x8bf80,0x8bf80,0x8bf80,0x8bf80,65,1057,2049,2857,3849,4657,5649,6457,7449,8257,9249,10241,11049,12041,12849,13832,14634,15617,16419,17190,17711,17711,17711,17711,21,20,0,0,0,0,0,0,0x1daa80,0x703080,0xc2b680,0x1033b00,0x155c100,0x1964580,0x1e8cb80,0x2295000,0x27bd600,0x2bc5a80,0x30ee080,0x3616680,0x3a1eb00,0x3f47100,0x434f580,0x4871d00,0x4c76280,0x5198a00,0x559cf80,0x598cf80,0x5c31900,0x5c31900,0x5c31900,0x5c31900,0x5c31900,0x5c31900,0x5c31900,0x5c31900,0x5c31900,0x5c31900,0x5c31900,0x5c31900,66,1058,2050,2858,3850,4658,5650,6458,7450,8258,9250,10242,11050,12042,12850,13833,14635,15618,16420,17191,17712,18224,18224,18224,22,21,0,0,0,0,0,0,0x1db500,0x703b00,0xc2c100,0x1034580,0x155cb80,0x1965000,0x1e8d600,0x2295a80,0x27be080,0x2bc6500,0x30eeb00,0x3617100,0x3a1f580,0x3f47b80,0x4350000,0x4872780,0x4c76d00,0x5199480,0x559da00,0x598da00,0x5c32380,0x5ed0e80,0x5ed0e80,0x5ed0e80,0x5ed0e80,0x5ed0e80,0x5ed0e80,0x5ed0e80,0x5ed0e80,0x5ed0e80,0x5ed0e80,0x5ed0e80,67,1059,2051,2859,3851,4659,5651,6459,7451,8259,9251,10243,11051,12043,12851,13834,14636,15619,16421,17192,17713,17713,17713,17713,21,20,0,0,0,0,0,0,0x1dbf80,0x704580,0xc2cb80,0x1035000,0x155d600,0x1965a80,0x1e8e080,0x2296500,0x27beb00,0x2bc6f80,0x30ef580,0x3617b80,0x3a20000,0x3f48600,0x4350a80,0x4873200,0x4c77780,0x5199f00,0x559e480,0x598e480,0x5c32e00,0x5c32e00,0x5c32e00,0x5c32e00,0x5c32e00,0x5c32e00,0x5c32e00,0x5c32e00,0x5c32e00,0x5c32e00,0x5c32e00,0x5c32e00,68,1060,2052,2860,3852,4660,5652,6460,7452,8260,9252,10244,11052,12044,12852,13835,14637,15620,16422,17193,17714,17714,17714,17714,21,20,0,0,0,0,0,0,0x1dca00,0x705000,0xc2d600,0x1035a80,0x155e080,0x1966500,0x1e8eb00,0x2296f80,0x27bf580,0x2bc7a00,0x30f0000,0x3618600,0x3a20a80,0x3f49080,0x4351500,0x4873c80,0x4c78200,0x519a980,0x559ef00,0x598ef00,0x5c33880,0x5c33880,0x5c33880,0x5c33880,0x5c33880,0x5c33880,0x5c33880,0x5c33880,0x5c33880,0x5c33880,0x5c33880,0x5c33880,69,1061,2053,2861,3853,4661,5653,6461,7453,8261,9253,10245,11053,12045,12853,13836,14638,15621,16423,18499,18499,18499,18499,18499,20,19,0,0,0,0,0,0,0x1dd480,0x705a80,0xc2e080,0x1036500,0x155eb00,0x1966f80,0x1e8f580,0x2297a00,0x27c0000,0x2bc8480,0x30f0a80,0x3619080,0x3a21500,0x3f49b00,0x4351f80,0x4874700,0x4c78c80,0x519b400,0x559f980,0x8ca00,0x8ca00,0x8ca00,0x8ca00,0x8ca00,0x8ca00,0x8ca00,0x8ca00,0x8ca00,0x8ca00,0x8ca00,0x8ca00,0x8ca00,70,1062,2054,2862,3854,4662,5654,6462,7454,8262,9254,10246,11054,12046,12854,13837,14639,15622,16424,18500,17715,17715,17715,17715,21,20,0,0,0,0,0,0,0x1ddf00,0x706500,0xc2eb00,0x1036f80,0x155f580,0x1967a00,0x1e90000,0x2298480,0x27c0a80,0x2bc8f00,0x30f1500,0x3619b00,0x3a21f80,0x3f4a580,0x4352a00,0x4875180,0x4c79700,0x519be80,0x55a0400,0x8d480,0x5c34300,0x5c34300,0x5c34300,0x5c34300,0x5c34300,0x5c34300,0x5c34300,0x5c34300,0x5c34300,0x5c34300,0x5c34300,0x5c34300,71,1063,2055,2863,3855,4663,5655,6463,7455,8263,9255,10247,11055,12047,12855,13838,14640,15623,16425,18501,18501,18501,18501,18501,20,19,0,0,0,0,0,0,0x1de980,0x706f80,0xc2f580,0x1037a00,0x1560000,0x1968480,0x1e90a80,0x2298f00,0x27c1500,0x2bc9980,0x30f1f80,0x361a580,0x3a22a00,0x3f4b000,0x4353480,0x4875c00,0x4c7a180,0x519c900,0x55a0e80,0x8df00,0x8df00,0x8df00,0x8df00,0x8df00,0x8df00,0x8df00,0x8df00,0x8df00,0x8df00,0x8df00,0x8df00,0x8df00,256,1064,2056,2864,3856,4664,5656,6464,7456,8448,9256,10248,11056,12048,12856,18441,14641,18456,16426,18502,18502,18502,18502,18502,20,19,0,0,0,0,0,0,0x2ff580,0x707a00,0xc30000,0x1038480,0x1560a80,0x1968f00,0x1e91500,0x2299980,0x27c1f80,0x2cea580,0x30f2a00,0x361b000,0x3a23480,0x3f4ba80,0x4353f00,0x66900,0x4c7ac00,0x70680,0x55a1900,0x8e980,0x8e980,0x8e980,0x8e980,0x8e980,0x8e980,0x8e980,0x8e980,0x8e980,0x8e980,0x8e980,0x8e980,0x8e980,257,1065,2057,2865,3857,4665,5657,6465,7457,8449,9257,10249,11057,12049,12857,13839,14642,15624,16427,17194,17716,17716,17716,17716,21,20,0,0,0,0,0,0,0x300000,0x708480,0xc30a80,0x1038f00,0x1561500,0x1969980,0x1e91f80,0x229a400,0x27c2a00,0x2ceb000,0x30f3480,0x361ba80,0x3a23f00,0x3f4c500,0x4354980,0x4876680,0x4c7b680,0x519d380,0x55a2380,0x598f980,0x5c34d80,0x5c34d80,0x5c34d80,0x5c34d80,0x5c34d80,0x5c34d80,0x5c34d80,0x5c34d80,0x5c34d80,0x5c34d80,0x5c34d80,0x5c34d80,258,1066,2058,2866,3858,4666,5658,6466,7458,8450,9258,10250,11058,12050,12858,13840,14643,15625,16428,17195,17717,18225,18225,18225,22,21,0,0,0,0,0,0,0x300a80,0x708f00,0xc31500,0x1039980,0x1561f80,0x196a400,0x1e92a00,0x229ae80,0x27c3480,0x2ceba80,0x30f3f00,0x361c500,0x3a24980,0x3f4cf80,0x4355400,0x4877100,0x4c7c100,0x519de00,0x55a2e00,0x5990400,0x5c35800,0x5ed1900,0x5ed1900,0x5ed1900,0x5ed1900,0x5ed1900,0x5ed1900,0x5ed1900,0x5ed1900,0x5ed1900,0x5ed1900,0x5ed1900,259,1067,2059,2867,3859,4667,5659,6467,7459,8451,9259,10251,11059,12051,12859,13841,14644,15626,16429,17196,17718,17718,17718,17718,21,20,0,0,0,0,0,0,0x301500,0x709980,0xc31f80,0x103a400,0x1562a00,0x196ae80,0x1e93480,0x229b900,0x27c3f00,0x2cec500,0x30f4980,0x361cf80,0x3a25400,0x3f4da00,0x4355e80,0x4877b80,0x4c7cb80,0x519e880,0x55a3880,0x5990e80,0x5c36280,0x5c36280,0x5c36280,0x5c36280,0x5c36280,0x5c36280,0x5c36280,0x5c36280,0x5c36280,0x5c36280,0x5c36280,0x5c36280,260,1068,2060,2868,3860,4668,5660,6468,7460,8452,9260,10252,11060,12052,12860,13842,14645,15627,16430,17197,17719,17719,17719,17719,21,20,0,0,0,0,0,0,0x301f80,0x70a400,0xc32a00,0x103ae80,0x1563480,0x196b900,0x1e93f00,0x229c380,0x27c4980,0x2cecf80,0x30f5400,0x361da00,0x3a25e80,0x3f4e480,0x4356900,0x4878600,0x4c7d600,0x519f300,0x55a4300,0x5991900,0x5c36d00,0x5c36d00,0x5c36d00,0x5c36d00,0x5c36d00,0x5c36d00,0x5c36d00,0x5c36d00,0x5c36d00,0x5c36d00,0x5c36d00,0x5c36d00,261,1069,2061,2869,3861,4669,5661,6469,7461,8453,9261,10253,11061,12053,12861,13843,14646,15628,16431,18503,18503,18503,18503,18503,20,19,0,0,0,0,0,0,0x302a00,0x70ae80,0xc33480,0x103b900,0x1563f00,0x196c380,0x1e94980,0x229ce00,0x27c5400,0x2ceda00,0x30f5e80,0x361e480,0x3a26900,0x3f4ef00,0x4357380,0x4879080,0x4c7e080,0x519fd80,0x55a4d80,0x8f400,0x8f400,0x8f400,0x8f400,0x8f400,0x8f400,0x8f400,0x8f400,0x8f400,0x8f400,0x8f400,0x8f400,0x8f400,262,1070,2062,2870,3862,4670,5662,6470,7462,8454,9262,10254,11062,12054,12862,13844,14647,15629,16432,18504,17720,17720,17720,17720,21,20,0,0,0,0,0,0,0x303480,0x70b900,0xc33f00,0x103c380,0x1564980,0x196ce00,0x1e95400,0x229d880,0x27c5e80,0x2cee480,0x30f6900,0x361ef00,0x3a27380,0x3f4f980,0x4357e00,0x4879b00,0x4c7eb00,0x51a0800,0x55a5800,0x60000,0x5c37780,0x5c37780,0x5c37780,0x5c37780,0x5c37780,0x5c37780,0x5c37780,0x5c37780,0x5c37780,0x5c37780,0x5c37780,0x5c37780,263,1071,2063,2871,3863,4671,5663,6471,7463,8455,9263,10255,11063,12055,12863,13845,14648,15630,16433,72,72,72,72,72,20,19,0,0,0,0,0,0,0x303f00,0x70c380,0xc34980,0x103ce00,0x1565400,0x196d880,0x1e95e80,0x229e300,0x27c6900,0x2ceef00,0x30f7380,0x361f980,0x3a27e00,0x3f50400,0x4358880,0x487a580,0x4c7f580,0x51a1280,0x55a6280,0x1af580,0x1af580,0x1af580,0x1af580,0x1af580,0x1af580,0x1af580,0x1af580,0x1af580,0x1af580,0x1af580,0x1af580,0x1af580,264,1072,2064,2872,3864,4672,5664,6656,7464,8456,9264,10256,11064,12056,12864,18442,14649,18457,16434,328,328,328,328,328,20,19,0,0,0,0,0,0,0x304980,0x70ce00,0xc35400,0x103d880,0x1565e80,0x196e300,0x1e96900,0x23bef00,0x27c7380,0x2cef980,0x30f7e00,0x3620400,0x3a28880,0x3f50e80,0x4359300,0x67380,0x4c80000,0x71100,0x55a6d00,0x2feb00,0x2feb00,0x2feb00,0x2feb00,0x2feb00,0x2feb00,0x2feb00,0x2feb00,0x2feb00,0x2feb00,0x2feb00,0x2feb00,0x2feb00,265,1073,2065,2873,3865,4673,5665,6657,7465,8457,9265,10257,11065,12057,12865,13846,14650,15631,16435,17198,17721,17721,17721,17721,21,20,0,0,0,0,0,0,0x305400,0x70d880,0xc35e80,0x103e300,0x1566900,0x196ed80,0x1e97380,0x23bf980,0x27c7e00,0x2cf0400,0x30f8880,0x3620e80,0x3a29300,0x3f51900,0x4359d80,0x487b000,0x4c80a80,0x51a1d00,0x55a7780,0x5992380,0x5c38200,0x5c38200,0x5c38200,0x5c38200,0x5c38200,0x5c38200,0x5c38200,0x5c38200,0x5c38200,0x5c38200,0x5c38200,0x5c38200,266,1074,2066,2874,3866,4674,5666,6658,7466,8458,9266,10258,11066,12058,12866,13847,14651,15632,16436,17199,17722,18226,18226,18226,22,21,0,0,0,0,0,0,0x305e80,0x70e300,0xc36900,0x103ed80,0x1567380,0x196f800,0x1e97e00,0x23c0400,0x27c8880,0x2cf0e80,0x30f9300,0x3621900,0x3a29d80,0x3f52380,0x435a800,0x487ba80,0x4c81500,0x51a2780,0x55a8200,0x5992e00,0x5c38c80,0x5ed2380,0x5ed2380,0x5ed2380,0x5ed2380,0x5ed2380,0x5ed2380,0x5ed2380,0x5ed2380,0x5ed2380,0x5ed2380,0x5ed2380,267,1075,2067,2875,3867,4675,5667,6659,7467,8459,9267,10259,11067,12059,12867,13848,14652,15633,16437,17200,17723,17723,17723,17723,21,20,0,0,0,0,0,0,0x306900,0x70ed80,0xc37380,0x103f800,0x1567e00,0x1970280,0x1e98880,0x23c0e80,0x27c9300,0x2cf1900,0x30f9d80,0x3622380,0x3a2a800,0x3f52e00,0x435b280,0x487c500,0x4c81f80,0x51a3200,0x55a8c80,0x5993880,0x5c39700,0x5c39700,0x5c39700,0x5c39700,0x5c39700,0x5c39700,0x5c39700,0x5c39700,0x5c39700,0x5c39700,0x5c39700,0x5c39700,268,1076,2068,2876,3868,4676,5668,6660,7468,8460,9268,10260,11068,12060,12868,13849,14653,15634,16438,17201,17724,17724,17724,17724,21,20,0,0,0,0,0,0,0x307380,0x70f800,0xc37e00,0x1040280,0x1568880,0x1970d00,0x1e99300,0x23c1900,0x27c9d80,0x2cf2380,0x30fa800,0x3622e00,0x3a2b280,0x3f53880,0x435bd00,0x487cf80,0x4c82a00,0x51a3c80,0x55a9700,0x5994300,0x5c3a180,0x5c3a180,0x5c3a180,0x5c3a180,0x5c3a180,0x5c3a180,0x5c3a180,0x5c3a180,0x5c3a180,0x5c3a180,0x5c3a180,0x5c3a180,269,1077,2069,2877,3869,4677,5669,6661,7469,8461,9269,10261,11069,12061,12869,13850,14654,15635,16439,584,584,584,584,584,20,19,0,0,0,0,0,0,0x307e00,0x710280,0xc38880,0x1040d00,0x1569300,0x1971780,0x1e99d80,0x23c2380,0x27ca800,0x2cf2e00,0x30fb280,0x3623880,0x3a2bd00,0x3f54300,0x435c780,0x487da00,0x4c83480,0x51a4700,0x55aa180,0x44e080,0x44e080,0x44e080,0x44e080,0x44e080,0x44e080,0x44e080,0x44e080,0x44e080,0x44e080,0x44e080,0x44e080,0x44e080,270,1078,2070,2878,3870,4678,5670,6662,7470,8462,9270,10262,11070,12062,12870,13851,14655,15636,16440,840,17725,17725,17725,17725,21,20,0,0,0,0,0,0,0x308880,0x710d00,0xc39300,0x1041780,0x1569d80,0x1972200,0x1e9a800,0x23c2e00,0x27cb280,0x2cf3880,0x30fbd00,0x3624300,0x3a2c780,0x3f54d80,0x435d200,0x487e480,0x4c83f00,0x51a5180,0x55aac00,0x59d600,0x5c3ac00,0x5c3ac00,0x5c3ac00,0x5c3ac00,0x5c3ac00,0x5c3ac00,0x5c3ac00,0x5c3ac00,0x5c3ac00,0x5c3ac00,0x5c3ac00,0x5c3ac00,271,1079,2071,2879,3871,4679,5671,6663,7471,8463,9271,10263,11071,12063,12871,13852,14656,15637,16441,1096,1096,1096,1096,1096,20,19,0,0,0,0,0,0,0x309300,0x711780,0xc39d80,0x1042200,0x156a800,0x1972c80,0x1e9b280,0x23c3880,0x27cbd00,0x2cf4300,0x30fc780,0x3624d80,0x3a2d200,0x3f55800,0x435dc80,0x487ef00,0x4c84980,0x51a5c00,0x55ab680,0x6ecb80,0x6ecb80,0x6ecb80,0x6ecb80,0x6ecb80,0x6ecb80,0x6ecb80,0x6ecb80,0x6ecb80,0x6ecb80,0x6ecb80,0x6ecb80,0x6ecb80,272,1080,2072,2880,3872,4864,5672,6664,7472,8464,9272,10264,11072,12064,13056,18443,14657,18458,16442,1352,1352,1352,1352,1352,20,19,0,0,0,0,0,0,0x309d80,0x712200,0xc3a800,0x1042c80,0x156b280,0x1a93880,0x1e9bd00,0x23c4300,0x27cc780,0x2cf4d80,0x30fd200,0x3625800,0x3a2dc80,0x3f56280,0x447e880,0x67e00,0x4c85400,0x71b80,0x55ac100,0x83c100,0x83c100,0x83c100,0x83c100,0x83c100,0x83c100,0x83c100,0x83c100,0x83c100,0x83c100,0x83c100,0x83c100,0x83c100,273,1081,2073,2881,3873,4865,5673,6665,7473,8465,9273,10265,11073,12065,13057,13853,14658,15638,16443,17202,17726,17726,17726,17726,21,20,0,0,0,0,0,0,0x30a800,0x712c80,0xc3b280,0x1043700,0x156bd00,0x1a94300,0x1e9c780,0x23c4d80,0x27cd200,0x2cf5800,0x30fdc80,0x3626280,0x3a2e700,0x3f56d00,0x447f300,0x487f980,0x4c85e80,0x51a6680,0x55acb80,0x5994d80,0x5c3b680,0x5c3b680,0x5c3b680,0x5c3b680,0x5c3b680,0x5c3b680,0x5c3b680,0x5c3b680,0x5c3b680,0x5c3b680,0x5c3b680,0x5c3b680,274,1082,2074,2882,3874,4866,5674,6666,7474,8466,9274,10266,11074,12066,13058,13854,14659,15639,16444,17203,17727,18227,18227,18227,22,21,0,0,0,0,0,0,0x30b280,0x713700,0xc3bd00,0x1044180,0x156c780,0x1a94d80,0x1e9d200,0x23c5800,0x27cdc80,0x2cf6280,0x30fe700,0x3626d00,0x3a2f180,0x3f57780,0x447fd80,0x4880400,0x4c86900,0x51a7100,0x55ad600,0x5995800,0x5c3c100,0x5ed2e00,0x5ed2e00,0x5ed2e00,0x5ed2e00,0x5ed2e00,0x5ed2e00,0x5ed2e00,0x5ed2e00,0x5ed2e00,0x5ed2e00,0x5ed2e00,275,1083,2075,2883,3875,4867,5675,6667,7475,8467,9275,10267,11075,12067,13059,13855,14660,15640,16445,17204,17728,17728,17728,17728,21,20,0,0,0,0,0,0,0x30bd00,0x714180,0xc3c780,0x1044c00,0x156d200,0x1a95800,0x1e9dc80,0x23c6280,0x27ce700,0x2cf6d00,0x30ff180,0x3627780,0x3a2fc00,0x3f58200,0x4480800,0x4880e80,0x4c87380,0x51a7b80,0x55ae080,0x5996280,0x5c3cb80,0x5c3cb80,0x5c3cb80,0x5c3cb80,0x5c3cb80,0x5c3cb80,0x5c3cb80,0x5c3cb80,0x5c3cb80,0x5c3cb80,0x5c3cb80,0x5c3cb80,276,1084,2076,2884,3876,4868,5676,6668,7476,8468,9276,10268,11076,12068,13060,13856,14661,15641,16446,17205,17729,17729,17729,17729,21,20,0,0,0,0,0,0,0x30c780,0x714c00,0xc3d200,0x1045680,0x156dc80,0x1a96280,0x1e9e700,0x23c6d00,0x27cf180,0x2cf7780,0x30ffc00,0x3628200,0x3a30680,0x3f58c80,0x4481280,0x4881900,0x4c87e00,0x51a8600,0x55aeb00,0x5996d00,0x5c3d600,0x5c3d600,0x5c3d600,0x5c3d600,0x5c3d600,0x5c3d600,0x5c3d600,0x5c3d600,0x5c3d600,0x5c3d600,0x5c3d600,0x5c3d600,277,1085,2077,2885,3877,4869,5677,6669,7477,8469,9277,10269,11077,12069,13061,13857,14662,15642,16447,1608,1608,1608,1608,1608,20,19,0,0,0,0,0,0,0x30d200,0x715680,0xc3dc80,0x1046100,0x156e700,0x1a96d00,0x1e9f180,0x23c7780,0x27cfc00,0x2cf8200,0x3100680,0x3628c80,0x3a31100,0x3f59700,0x4481d00,0x4882380,0x4c88880,0x51a9080,0x55af580,0x98b680,0x98b680,0x98b680,0x98b680,0x98b680,0x98b680,0x98b680,0x98b680,0x98b680,0x98b680,0x98b680,0x98b680,0x98b680,278,1086,2078,2886,3878,4870,5678,6670,7478,8470,9278,10270,11078,12070,13062,13858,14663,15643,16448,1864,17730,17730,17730,17730,21,20,0,0,0,0,0,0,0x30dc80,0x716100,0xc3e700,0x1046b80,0x156f180,0x1a97780,0x1e9fc00,0x23c8200,0x27d0680,0x2cf8c80,0x3101100,0x3629700,0x3a31b80,0x3f5a180,0x4482780,0x4882e00,0x4c89300,0x51a9b00,0x55b0000,0xadac00,0x5c3e080,0x5c3e080,0x5c3e080,0x5c3e080,0x5c3e080,0x5c3e080,0x5c3e080,0x5c3e080,0x5c3e080,0x5c3e080,0x5c3e080,0x5c3e080,279,1087,2079,2887,3879,4871,5679,6671,7479,8471,9279,10271,11079,12071,13063,13859,14848,15644,16449,2120,2120,2120,2120,2120,20,19,0,0,0,0,0,0,0x30e700,0x716b80,0xc3f180,0x1047600,0x156fc00,0x1a98200,0x1ea0680,0x23c8c80,0x27d1100,0x2cf9700,0x3101b80,0x362a180,0x3a32600,0x3f5ac00,0x4483200,0x4883880,0x4da9f00,0x51aa580,0x55b0a80,0xc2a180,0xc2a180,0xc2a180,0xc2a180,0xc2a180,0xc2a180,0xc2a180,0xc2a180,0xc2a180,0xc2a180,0xc2a180,0xc2a180,0xc2a180,280,1088,2080,3072,3880,4872,5680,6672,7480,8472,9280,10272,11264,12072,13064,18444,14849,18459,16450,2376,2376,2376,2376,2376,20,19,0,0,0,0,0,0,0x30f180,0x717600,0xc3fc00,0x1168200,0x1570680,0x1a98c80,0x1ea1100,0x23c9700,0x27d1b80,0x2cfa180,0x3102600,0x362ac00,0x3b53200,0x3f5b680,0x4483c80,0x68880,0x4daa980,0x72600,0x55b1500,0xd79700,0xd79700,0xd79700,0xd79700,0xd79700,0xd79700,0xd79700,0xd79700,0xd79700,0xd79700,0xd79700,0xd79700,0xd79700,281,1089,2081,3073,3881,4873,5681,6673,7481,8473,9281,10273,11265,12073,13065,13860,14850,15645,16451,17206,17731,17731,17731,17731,21,20,0,0,0,0,0,0,0x30fc00,0x718080,0xc40680,0x1168c80,0x1571100,0x1a99700,0x1ea1b80,0x23ca180,0x27d2600,0x2cfac00,0x3103080,0x362b680,0x3b53c80,0x3f5c100,0x4484700,0x4884300,0x4dab400,0x51ab000,0x55b1f80,0x5997780,0x5c3eb00,0x5c3eb00,0x5c3eb00,0x5c3eb00,0x5c3eb00,0x5c3eb00,0x5c3eb00,0x5c3eb00,0x5c3eb00,0x5c3eb00,0x5c3eb00,0x5c3eb00,282,1090,2082,3074,3882,4874,5682,6674,7482,8474,9282,10274,11266,12074,13066,13861,14851,15646,16452,17207,17732,18228,18228,18228,22,21,0,0,0,0,0,0,0x310680,0x718b00,0xc41100,0x1169700,0x1571b80,0x1a9a180,0x1ea2600,0x23cac00,0x27d3080,0x2cfb680,0x3103b00,0x362c100,0x3b54700,0x3f5cb80,0x4485180,0x4884d80,0x4dabe80,0x51aba80,0x55b2a00,0x5998200,0x5c3f580,0x5ed3880,0x5ed3880,0x5ed3880,0x5ed3880,0x5ed3880,0x5ed3880,0x5ed3880,0x5ed3880,0x5ed3880,0x5ed3880,0x5ed3880,283,1091,2083,3075,3883,4875,5683,6675,7483,8475,9283,10275,11267,12075,13067,13862,14852,15647,16453,17208,17733,17733,17733,17733,21,20,0,0,0,0,0,0,0x311100,0x719580,0xc41b80,0x116a180,0x1572600,0x1a9ac00,0x1ea3080,0x23cb680,0x27d3b00,0x2cfc100,0x3104580,0x362cb80,0x3b55180,0x3f5d600,0x4485c00,0x4885800,0x4dac900,0x51ac500,0x55b3480,0x5998c80,0x5c40000,0x5c40000,0x5c40000,0x5c40000,0x5c40000,0x5c40000,0x5c40000,0x5c40000,0x5c40000,0x5c40000,0x5c40000,0x5c40000,284,1092,2084,3076,3884,4876,5684,6676,7484,8476,9284,10276,11268,12076,13068,13863,14853,15648,16454,17209,17734,17734,17734,17734,21,20,0,0,0,0,0,0,0x311b80,0x71a000,0xc42600,0x116ac00,0x1573080,0x1a9b680,0x1ea3b00,0x23cc100,0x27d4580,0x2cfcb80,0x3105000,0x362d600,0x3b55c00,0x3f5e080,0x4486680,0x4886280,0x4dad380,0x51acf80,0x55b3f00,0x5999700,0x5c40a80,0x5c40a80,0x5c40a80,0x5c40a80,0x5c40a80,0x5c40a80,0x5c40a80,0x5c40a80,0x5c40a80,0x5c40a80,0x5c40a80,0x5c40a80,285,1093,2085,3077,3885,4877,5685,6677,7485,8477,9285,10277,11269,12077,13069,13864,14854,15649,16455,2632,2632,2632,2632,2632,20,19,0,0,0,0,0,0,0x312600,0x71aa80,0xc43080,0x116b680,0x1573b00,0x1a9c100,0x1ea4580,0x23ccb80,0x27d5000,0x2cfd600,0x3105a80,0x362e080,0x3b56680,0x3f5eb00,0x4487100,0x4886d00,0x4dade00,0x51ada00,0x55b4980,0xec8c80,0xec8c80,0xec8c80,0xec8c80,0xec8c80,0xec8c80,0xec8c80,0xec8c80,0xec8c80,0xec8c80,0xec8c80,0xec8c80,0xec8c80,286,1094,2086,3078,3886,4878,5686,6678,7486,8478,9286,10278,11270,12078,13070,13865,14855,15650,16640,2888,17735,17735,17735,17735,21,20,0,0,0,0,0,0,0x313080,0x71b500,0xc43b00,0x116c100,0x1574580,0x1a9cb80,0x1ea5000,0x23cd600,0x27d5a80,0x2cfe080,0x3106500,0x362eb00,0x3b57100,0x3f5f580,0x4487b80,0x4887780,0x4dae880,0x51ae480,0x56d5580,0x1018200,0x5c41500,0x5c41500,0x5c41500,0x5c41500,0x5c41500,0x5c41500,0x5c41500,0x5c41500,0x5c41500,0x5c41500,0x5c41500,0x5c41500,287,1095,2087,3079,3887,4879,5687,6679,7487,8479,9287,10279,11271,12079,13071,13866,14856,15651,16641,3144,3144,3144,3144,3144,20,19,0,0,0,0,0,0,0x313b00,0x71bf80,0xc44580,0x116cb80,0x1575000,0x1a9d600,0x1ea5a80,0x23ce080,0x27d6500,0x2cfeb00,0x3106f80,0x362f580,0x3b57b80,0x3f60000,0x4488600,0x4888200,0x4daf300,0x51aef00,0x56d6000,0x1167780,0x1167780,0x1167780,0x1167780,0x1167780,0x1167780,0x1167780,0x1167780,0x1167780,0x1167780,0x1167780,0x1167780,0x1167780,288,1280,2088,3080,3888,4880,5688,6680,7488,8480,9472,10280,11272,12080,13072,18445,14857,18460,16642,3400,3400,3400,3400,3400,20,19,0,0,0,0,0,0,0x314580,0x83cb80,0xc45000,0x116d600,0x1575a80,0x1a9e080,0x1ea6500,0x23ceb00,0x27d6f80,0x2cff580,0x3227b80,0x3630000,0x3b58600,0x3f60a80,0x4489080,0x69300,0x4dafd80,0x73080,0x56d6a80,0x12b6d00,0x12b6d00,0x12b6d00,0x12b6d00,0x12b6d00,0x12b6d00,0x12b6d00,0x12b6d00,0x12b6d00,0x12b6d00,0x12b6d00,0x12b6d00,0x12b6d00,289,1281,2089,3081,3889,4881,5689,6681,7489,8481,9473,10281,11273,12081,13073,13867,14858,15652,16643,17210,17920,17920,17920,17920,21,20,0,0,0,0,0,0,0x315000,0x83d600,0xc45a80,0x116e080,0x1576500,0x1a9eb00,0x1ea6f80,0x23cf580,0x27d7a00,0x2d00000,0x3228600,0x3630a80,0x3b59080,0x3f61500,0x4489b00,0x4888c80,0x4db0800,0x51af980,0x56d7500,0x599a180,0x5d62100,0x5d62100,0x5d62100,0x5d62100,0x5d62100,0x5d62100,0x5d62100,0x5d62100,0x5d62100,0x5d62100,0x5d62100,0x5d62100,290,1282,2090,3082,3890,4882,5690,6682,7490,8482,9474,10282,11274,12082,13074,13868,14859,15653,16644,17211,17921,18229,18229,18229,22,21,0,0,0,0,0,0,0x315a80,0x83e080,0xc46500,0x116eb00,0x1576f80,0x1a9f580,0x1ea7a00,0x23d0000,0x27d8480,0x2d00a80,0x3229080,0x3631500,0x3b59b00,0x3f61f80,0x448a580,0x4889700,0x4db1280,0x51b0400,0x56d7f80,0x599ac00,0x5d62b80,0x5ed4300,0x5ed4300,0x5ed4300,0x5ed4300,0x5ed4300,0x5ed4300,0x5ed4300,0x5ed4300,0x5ed4300,0x5ed4300,0x5ed4300,291,1283,2091,3083,3891,4883,5691,6683,7491,8483,9475,10283,11275,12083,13075,13869,14860,15654,16645,17212,17922,17922,17922,17922,21,20,0,0,0,0,0,0,0x316500,0x83eb00,0xc46f80,0x116f580,0x1577a00,0x1aa0000,0x1ea8480,0x23d0a80,0x27d8f00,0x2d01500,0x3229b00,0x3631f80,0x3b5a580,0x3f62a00,0x448b000,0x488a180,0x4db1d00,0x51b0e80,0x56d8a00,0x599b680,0x5d63600,0x5d63600,0x5d63600,0x5d63600,0x5d63600,0x5d63600,0x5d63600,0x5d63600,0x5d63600,0x5d63600,0x5d63600,0x5d63600,292,1284,2092,3084,3892,4884,5692,6684,7492,8484,9476,10284,11276,12084,13076,13870,14861,15655,16646,17213,17923,17923,17923,17923,21,20,0,0,0,0,0,0,0x316f80,0x83f580,0xc47a00,0x1170000,0x1578480,0x1aa0a80,0x1ea8f00,0x23d1500,0x27d9980,0x2d01f80,0x322a580,0x3632a00,0x3b5b000,0x3f63480,0x448ba80,0x488ac00,0x4db2780,0x51b1900,0x56d9480,0x599c100,0x5d64080,0x5d64080,0x5d64080,0x5d64080,0x5d64080,0x5d64080,0x5d64080,0x5d64080,0x5d64080,0x5d64080,0x5d64080,0x5d64080,293,1285,2093,3085,3893,4885,5693,6685,7493,8485,9477,10285,11277,12085,13077,13871,14862,15656,16647,3656,3656,3656,3656,3656,20,19,0,0,0,0,0,0,0x317a00,0x840000,0xc48480,0x1170a80,0x1578f00,0x1aa1500,0x1ea9980,0x23d1f80,0x27da400,0x2d02a00,0x322b000,0x3633480,0x3b5ba80,0x3f63f00,0x448c500,0x488b680,0x4db3200,0x51b2380,0x56d9f00,0x1406280,0x1406280,0x1406280,0x1406280,0x1406280,0x1406280,0x1406280,0x1406280,0x1406280,0x1406280,0x1406280,0x1406280,0x1406280,294,1286,2094,3086,3894,4886,5694,6686,7494,8486,9478,10286,11278,12086,13078,13872,14863,15657,16648,3912,17924,17924,17924,17924,21,20,0,0,0,0,0,0,0x318480,0x840a80,0xc48f00,0x1171500,0x1579980,0x1aa1f80,0x1eaa400,0x23d2a00,0x27dae80,0x2d03480,0x322ba80,0x3633f00,0x3b5c500,0x3f64980,0x448cf80,0x488c100,0x4db3c80,0x51b2e00,0x56da980,0x1555800,0x5d64b00,0x5d64b00,0x5d64b00,0x5d64b00,0x5d64b00,0x5d64b00,0x5d64b00,0x5d64b00,0x5d64b00,0x5d64b00,0x5d64b00,0x5d64b00,295,1287,2095,3087,3895,4887,5695,6687,7495,8487,9479,10287,11279,12087,13079,13873,14864,15658,16649,4168,4168,4168,4168,4168,20,19,0,0,0,0,0,0,0x318f00,0x841500,0xc49980,0x1171f80,0x157a400,0x1aa2a00,0x1eaae80,0x23d3480,0x27db900,0x2d03f00,0x322c500,0x3634980,0x3b5cf80,0x3f65400,0x448da00,0x488cb80,0x4db4700,0x51b3880,0x56db400,0x16a4d80,0x16a4d80,0x16a4d80,0x16a4d80,0x16a4d80,0x16a4d80,0x16a4d80,0x16a4d80,0x16a4d80,0x16a4d80,0x16a4d80,0x16a4d80,0x16a4d80,296,1288,2096,3088,3896,4888,5696,6688,7680,8488,9480,10288,11280,12088,13080,18446,14865,18461,16650,4424,4424,4424,4424,4424,20,19,0,0,0,0,0,0,0x319980,0x841f80,0xc4a400,0x1172a00,0x157ae80,0x1aa3480,0x1eab900,0x23d3f00,0x28fc500,0x2d04980,0x322cf80,0x3635400,0x3b5da00,0x3f65e80,0x448e480,0x69d80,0x4db5180,0x73b00,0x56dbe80,0x17f4300,0x17f4300,0x17f4300,0x17f4300,0x17f4300,0x17f4300,0x17f4300,0x17f4300,0x17f4300,0x17f4300,0x17f4300,0x17f4300,0x17f4300,297,1289,2097,3089,3897,4889,5697,6689,7681,8489,9481,10289,11281,12089,13081,13874,14866,15659,16651,17214,17925,17925,17925,17925,21,20,0,0,0,0,0,0,0x31a400,0x842a00,0xc4ae80,0x1173480,0x157b900,0x1aa3f00,0x1eac380,0x23d4980,0x28fcf80,0x2d05400,0x322da00,0x3635e80,0x3b5e480,0x3f66900,0x448ef00,0x488d600,0x4db5c00,0x51b4300,0x56dc900,0x599cb80,0x5d65580,0x5d65580,0x5d65580,0x5d65580,0x5d65580,0x5d65580,0x5d65580,0x5d65580,0x5d65580,0x5d65580,0x5d65580,0x5d65580,298,1290,2098,3090,3898,4890,5698,6690,7682,8490,9482,10290,11282,12090,13082,13875,14867,15660,16652,17215,17926,18230,18230,18230,22,21,0,0,0,0,0,0,0x31ae80,0x843480,0xc4b900,0x1173f00,0x157c380,0x1aa4980,0x1eace00,0x23d5400,0x28fda00,0x2d05e80,0x322e480,0x3636900,0x3b5ef00,0x3f67380,0x448f980,0x488e080,0x4db6680,0x51b4d80,0x56dd380,0x599d600,0x5d66000,0x5ed4d80,0x5ed4d80,0x5ed4d80,0x5ed4d80,0x5ed4d80,0x5ed4d80,0x5ed4d80,0x5ed4d80,0x5ed4d80,0x5ed4d80,0x5ed4d80,299,1291,2099,3091,3899,4891,5699,6691,7683,8491,9483,10291,11283,12091,13083,13876,14868,15661,16653,17216,17927,17927,17927,17927,21,20,0,0,0,0,0,0,0x31b900,0x843f00,0xc4c380,0x1174980,0x157ce00,0x1aa5400,0x1ead880,0x23d5e80,0x28fe480,0x2d06900,0x322ef00,0x3637380,0x3b5f980,0x3f67e00,0x4490400,0x488eb00,0x4db7100,0x51b5800,0x56dde00,0x599e080,0x5d66a80,0x5d66a80,0x5d66a80,0x5d66a80,0x5d66a80,0x5d66a80,0x5d66a80,0x5d66a80,0x5d66a80,0x5d66a80,0x5d66a80,0x5d66a80,300,1292,2100,3092,3900,4892,5700,6692,7684,8492,9484,10292,11284,12092,13084,13877,14869,15662,16654,17217,17928,17928,17928,17928,21,20,0,0,0,0,0,0,0x31c380,0x844980,0xc4ce00,0x1175400,0x157d880,0x1aa5e80,0x1eae300,0x23d6900,0x28fef00,0x2d07380,0x322f980,0x3637e00,0x3b60400,0x3f68880,0x4490e80,0x488f580,0x4db7b80,0x51b6280,0x56de880,0x599eb00,0x5d67500,0x5d67500,0x5d67500,0x5d67500,0x5d67500,0x5d67500,0x5d67500,0x5d67500,0x5d67500,0x5d67500,0x5d67500,0x5d67500,301,1293,2101,3093,3901,4893,5701,6693,7685,8493,9485,10293,11285,12093,13085,13878,14870,15663,16655,4680,4680,4680,4680,4680,20,19,0,0,0,0,0,0,0x31ce00,0x845400,0xc4d880,0x1175e80,0x157e300,0x1aa6900,0x1eaed80,0x23d7380,0x28ff980,0x2d07e00,0x3230400,0x3638880,0x3b60e80,0x3f69300,0x4491900,0x4890000,0x4db8600,0x51b6d00,0x56df300,0x1943880,0x1943880,0x1943880,0x1943880,0x1943880,0x1943880,0x1943880,0x1943880,0x1943880,0x1943880,0x1943880,0x1943880,0x1943880,302,1294,2102,3094,3902,4894,5702,6694,7686,8494,9486,10294,11286,12094,13086,13879,14871,15664,16656,4936,17929,17929,17929,17929,21,20,0,0,0,0,0,0,0x31d880,0x845e80,0xc4e300,0x1176900,0x157ed80,0x1aa7380,0x1eaf800,0x23d7e00,0x2900400,0x2d08880,0x3230e80,0x3639300,0x3b61900,0x3f69d80,0x4492380,0x4890a80,0x4db9080,0x51b7780,0x56dfd80,0x1a92e00,0x5d67f80,0x5d67f80,0x5d67f80,0x5d67f80,0x5d67f80,0x5d67f80,0x5d67f80,0x5d67f80,0x5d67f80,0x5d67f80,0x5d67f80,0x5d67f80,303,1295,2103,3095,3903,4895,5703,6695,7687,8495,9487,10295,11287,12095,13087,13880,14872,15665,16657,5192,5192,5192,5192,5192,20,19,0,0,0,0,0,0,0x31e300,0x846900,0xc4ed80,0x1177380,0x157f800,0x1aa7e00,0x1eb0280,0x23d8880,0x2900e80,0x2d09300,0x3231900,0x3639d80,0x3b62380,0x3f6a800,0x4492e00,0x4891500,0x4db9b00,0x51b8200,0x56e0800,0x1be2380,0x1be2380,0x1be2380,0x1be2380,0x1be2380,0x1be2380,0x1be2380,0x1be2380,0x1be2380,0x1be2380,0x1be2380,0x1be2380,0x1be2380,304,1296,2104,3096,3904,4896,5888,6696,7688,8496,9488,10296,11288,12096,13088,13881,14873,15666,16658,17218,17930,17930,17930,17930,21,20,0,0,0,0,0,0,0x31ed80,0x847380,0xc4f800,0x1177e00,0x1580280,0x1aa8880,0x1fd0e80,0x23d9300,0x2901900,0x2d09d80,0x3232380,0x363a800,0x3b62e00,0x3f6b280,0x4493880,0x4891f80,0x4dba580,0x51b8c80,0x56e1280,0x599f580,0x5d68a00,0x5d68a00,0x5d68a00,0x5d68a00,0x5d68a00,0x5d68a00,0x5d68a00,0x5d68a00,0x5d68a00,0x5d68a00,0x5d68a00,0x5d68a00,305,1297,2105,3097,3905,4897,5889,6697,7689,8497,9489,10297,11289,12097,13089,13882,14874,15667,16659,17219,17931,17931,17931,17931,21,20,0,0,0,0,0,0,0x31f800,0x847e00,0xc50280,0x1178880,0x1580d00,0x1aa9300,0x1fd1900,0x23d9d80,0x2902380,0x2d0a800,0x3232e00,0x363b280,0x3b63880,0x3f6bd00,0x4494300,0x4892a00,0x4dbb000,0x51b9700,0x56e1d00,0x59a0000,0x5d69480,0x5d69480,0x5d69480,0x5d69480,0x5d69480,0x5d69480,0x5d69480,0x5d69480,0x5d69480,0x5d69480,0x5d69480,0x5d69480,306,1298,2106,3098,3906,4898,5890,6698,7690,8498,9490,10298,11290,12098,13090,13883,14875,15668,16660,17220,17932,18231,18231,18231,22,21,0,0,0,0,0,0,0x320280,0x848880,0xc50d00,0x1179300,0x1581780,0x1aa9d80,0x1fd2380,0x23da800,0x2902e00,0x2d0b280,0x3233880,0x363bd00,0x3b64300,0x3f6c780,0x4494d80,0x4893480,0x4dbba80,0x51ba180,0x56e2780,0x59a0a80,0x5d69f00,0x5ed5800,0x5ed5800,0x5ed5800,0x5ed5800,0x5ed5800,0x5ed5800,0x5ed5800,0x5ed5800,0x5ed5800,0x5ed5800,0x5ed5800,307,1299,2107,3099,3907,4899,5891,6699,7691,8499,9491,10299,11291,12099,13091,13884,14876,15669,16661,17221,17933,17933,17933,17933,21,20,0,0,0,0,0,0,0x320d00,0x849300,0xc51780,0x1179d80,0x1582200,0x1aaa800,0x1fd2e00,0x23db280,0x2903880,0x2d0bd00,0x3234300,0x363c780,0x3b64d80,0x3f6d200,0x4495800,0x4893f00,0x4dbc500,0x51bac00,0x56e3200,0x59a1500,0x5d6a980,0x5d6a980,0x5d6a980,0x5d6a980,0x5d6a980,0x5d6a980,0x5d6a980,0x5d6a980,0x5d6a980,0x5d6a980,0x5d6a980,0x5d6a980,308,1300,2108,3100,3908,4900,5892,6700,7692,8500,9492,10300,11292,12100,13092,13885,14877,15670,16662,17222,17934,17934,17934,17934,21,20,0,0,0,0,0,0,0x321780,0x849d80,0xc52200,0x117a800,0x1582c80,0x1aab280,0x1fd3880,0x23dbd00,0x2904300,0x2d0c780,0x3234d80,0x363d200,0x3b65800,0x3f6dc80,0x4496280,0x4894980,0x4dbcf80,0x51bb680,0x56e3c80,0x59a1f80,0x5d6b400,0x5d6b400,0x5d6b400,0x5d6b400,0x5d6b400,0x5d6b400,0x5d6b400,0x5d6b400,0x5d6b400,0x5d6b400,0x5d6b400,0x5d6b400,309,1301,2109,3101,3909,4901,5893,6701,7693,8501,9493,10301,11293,12101,13093,13886,14878,15671,16663,5448,5448,5448,5448,5448,20,19,0,0,0,0,0,0,0x322200,0x84a800,0xc52c80,0x117b280,0x1583700,0x1aabd00,0x1fd4300,0x23dc780,0x2904d80,0x2d0d200,0x3235800,0x363dc80,0x3b66280,0x3f6e700,0x4496d00,0x4895400,0x4dbda00,0x51bc100,0x56e4700,0x1d31900,0x1d31900,0x1d31900,0x1d31900,0x1d31900,0x1d31900,0x1d31900,0x1d31900,0x1d31900,0x1d31900,0x1d31900,0x1d31900,0x1d31900,310,1302,2110,3102,3910,4902,5894,6702,7694,8502,9494,10302,11294,12102,13094,13887,14879,15672,16664,5704,17935,17935,17935,17935,21,20,0,0,0,0,0,0,0x322c80,0x84b280,0xc53700,0x117bd00,0x1584180,0x1aac780,0x1fd4d80,0x23dd200,0x2905800,0x2d0dc80,0x3236280,0x363e700,0x3b66d00,0x3f6f180,0x4497780,0x4895e80,0x4dbe480,0x51bcb80,0x56e5180,0x1e80e80,0x5d6be80,0x5d6be80,0x5d6be80,0x5d6be80,0x5d6be80,0x5d6be80,0x5d6be80,0x5d6be80,0x5d6be80,0x5d6be80,0x5d6be80,0x5d6be80,311,1303,2111,3103,3911,4903,5895,6703,7695,8503,9495,10303,11295,12103,13095,13888,14880,15673,16665,5960,5960,5960,5960,5960,20,19,0,0,0,0,0,0,0x323700,0x84bd00,0xc54180,0x117c780,0x1584c00,0x1aad200,0x1fd5800,0x23ddc80,0x2906280,0x2d0e700,0x3236d00,0x363f180,0x3b67780,0x3f6fc00,0x4498200,0x4896900,0x4dbef00,0x51bd600,0x56e5c00,0x1fd0400,0x1fd0400,0x1fd0400,0x1fd0400,0x1fd0400,0x1fd0400,0x1fd0400,0x1fd0400,0x1fd0400,0x1fd0400,0x1fd0400,0x1fd0400,0x1fd0400,312,1304,2112,3104,4096,4904,5896,6704,7696,8504,9496,10304,11296,12288,13096,13889,14881,15674,16666,17223,17936,17936,17936,17936,21,20,0,0,0,0,0,0,0x324180,0x84c780,0xc54c00,0x117d200,0x16a5800,0x1aadc80,0x1fd6280,0x23de700,0x2906d00,0x2d0f180,0x3237780,0x363fc00,0x3b68200,0x4090800,0x4498c80,0x4897380,0x4dbf980,0x51be080,0x56e6680,0x59a2a00,0x5d6c900,0x5d6c900,0x5d6c900,0x5d6c900,0x5d6c900,0x5d6c900,0x5d6c900,0x5d6c900,0x5d6c900,0x5d6c900,0x5d6c900,0x5d6c900,313,1305,2113,3105,4097,4905,5897,6705,7697,8505,9497,10305,11297,12289,13097,13890,14882,15675,16667,17408,17937,17937,17937,17937,21,20,0,0,0,0,0,0,0x324c00,0x84d200,0xc55680,0x117dc80,0x16a6280,0x1aae700,0x1fd6d00,0x23df180,0x2907780,0x2d0fc00,0x3238200,0x3640680,0x3b68c80,0x4091280,0x4499700,0x4897e00,0x4dc0400,0x51beb00,0x56e7100,0x5ac3600,0x5d6d380,0x5d6d380,0x5d6d380,0x5d6d380,0x5d6d380,0x5d6d380,0x5d6d380,0x5d6d380,0x5d6d380,0x5d6d380,0x5d6d380,0x5d6d380,314,1306,2114,3106,4098,4906,5898,6706,7698,8506,9498,10306,11298,12290,13098,13891,14883,15676,16668,17409,17938,18232,18232,18232,22,21,0,0,0,0,0,0,0x325680,0x84dc80,0xc56100,0x117e700,0x16a6d00,0x1aaf180,0x1fd7780,0x23dfc00,0x2908200,0x2d10680,0x3238c80,0x3641100,0x3b69700,0x4091d00,0x449a180,0x4898880,0x4dc0e80,0x51bf580,0x56e7b80,0x5ac4080,0x5d6de00,0x5ed6280,0x5ed6280,0x5ed6280,0x5ed6280,0x5ed6280,0x5ed6280,0x5ed6280,0x5ed6280,0x5ed6280,0x5ed6280,0x5ed6280,315,1307,2115,3107,4099,4907,5899,6707,7699,8507,9499,10307,11299,12291,13099,13892,14884,15677,16669,17410,17939,17939,17939,17939,21,20,0,0,0,0,0,0,0x326100,0x84e700,0xc56b80,0x117f180,0x16a7780,0x1aafc00,0x1fd8200,0x23e0680,0x2908c80,0x2d11100,0x3239700,0x3641b80,0x3b6a180,0x4092780,0x449ac00,0x4899300,0x4dc1900,0x51c0000,0x56e8600,0x5ac4b00,0x5d6e880,0x5d6e880,0x5d6e880,0x5d6e880,0x5d6e880,0x5d6e880,0x5d6e880,0x5d6e880,0x5d6e880,0x5d6e880,0x5d6e880,0x5d6e880,316,1308,2116,3108,4100,4908,5900,6708,7700,8508,9500,10308,11300,12292,13100,13893,14885,15678,16670,17411,17940,17940,17940,17940,21,20,0,0,0,0,0,0,0x326b80,0x84f180,0xc57600,0x117fc00,0x16a8200,0x1ab0680,0x1fd8c80,0x23e1100,0x2909700,0x2d11b80,0x323a180,0x3642600,0x3b6ac00,0x4093200,0x449b680,0x4899d80,0x4dc2380,0x51c0a80,0x56e9080,0x5ac5580,0x5d6f300,0x5d6f300,0x5d6f300,0x5d6f300,0x5d6f300,0x5d6f300,0x5d6f300,0x5d6f300,0x5d6f300,0x5d6f300,0x5d6f300,0x5d6f300,317,1309,2117,3109,4101,4909,5901,6709,7701,8509,9501,10309,11301,12293,13101,13894,14886,15679,16671,6216,6216,6216,6216,6216,20,19,0,0,0,0,0,0,0x327600,0x84fc00,0xc58080,0x1180680,0x16a8c80,0x1ab1100,0x1fd9700,0x23e1b80,0x290a180,0x2d12600,0x323ac00,0x3643080,0x3b6b680,0x4093c80,0x449c100,0x489a800,0x4dc2e00,0x51c1500,0x56e9b00,0x211f980,0x211f980,0x211f980,0x211f980,0x211f980,0x211f980,0x211f980,0x211f980,0x211f980,0x211f980,0x211f980,0x211f980,0x211f980,318,1310,2118,3110,4102,4910,5902,6710,7702,8510,9502,10310,11302,12294,13102,13895,14887,15680,16672,6472,17941,17941,17941,17941,21,20,0,0,0,0,0,0,0x328080,0x850680,0xc58b00,0x1181100,0x16a9700,0x1ab1b80,0x1fda180,0x23e2600,0x290ac00,0x2d13080,0x323b680,0x3643b00,0x3b6c100,0x4094700,0x449cb80,0x489b280,0x4dc3880,0x51c1f80,0x56ea580,0x226ef00,0x5d6fd80,0x5d6fd80,0x5d6fd80,0x5d6fd80,0x5d6fd80,0x5d6fd80,0x5d6fd80,0x5d6fd80,0x5d6fd80,0x5d6fd80,0x5d6fd80,0x5d6fd80,319,1311,2119,3111,4103,4911,5903,6711,7703,8511,9503,10311,11303,12295,13103,14080,14888,15681,16673,6728,6728,6728,6728,6728,20,19,0,0,0,0,0,0,0x328b00,0x851100,0xc59580,0x1181b80,0x16aa180,0x1ab2600,0x1fdac00,0x23e3080,0x290b680,0x2d13b00,0x323c100,0x3644580,0x3b6cb80,0x4095180,0x449d600,0x49bbe80,0x4dc4300,0x51c2a00,0x56eb000,0x23be480,0x23be480,0x23be480,0x23be480,0x23be480,0x23be480,0x23be480,0x23be480,0x23be480,0x23be480,0x23be480,0x23be480,0x23be480,320,1312,2304,3112,4104,4912,5904,6712,7704,8512,9504,10496,11304,12296,13104,14081,14889,15682,16674,17412,17942,17942,17942,17942,21,20,0,0,0,0,0,0,0x329580,0x851b80,0xd7a180,0x1182600,0x16aac00,0x1ab3080,0x1fdb680,0x23e3b00,0x290c100,0x2d14580,0x323cb80,0x3765180,0x3b6d600,0x4095c00,0x449e080,0x49bc900,0x4dc4d80,0x51c3480,0x56eba80,0x5ac6000,0x5d70800,0x5d70800,0x5d70800,0x5d70800,0x5d70800,0x5d70800,0x5d70800,0x5d70800,0x5d70800,0x5d70800,0x5d70800,0x5d70800,321,1313,2305,3113,4105,4913,5905,6713,7705,8513,9505,10497,11305,12297,13105,14082,14890,15683,16675,17413,17943,17943,17943,17943,21,20,0,0,0,0,0,0,0x32a000,0x852600,0xd7ac00,0x1183080,0x16ab680,0x1ab3b00,0x1fdc100,0x23e4580,0x290cb80,0x2d15000,0x323d600,0x3765c00,0x3b6e080,0x4096680,0x449eb00,0x49bd380,0x4dc5800,0x51c3f00,0x56ec500,0x5ac6a80,0x5d71280,0x5d71280,0x5d71280,0x5d71280,0x5d71280,0x5d71280,0x5d71280,0x5d71280,0x5d71280,0x5d71280,0x5d71280,0x5d71280,322,1314,2306,3114,4106,4914,5906,6714,7706,8514,9506,10498,11306,12298,13106,14083,14891,15684,16676,17414,17944,18233,18233,18233,22,21,0,0,0,0,0,0,0x32aa80,0x853080,0xd7b680,0x1183b00,0x16ac100,0x1ab4580,0x1fdcb80,0x23e5000,0x290d600,0x2d15a80,0x323e080,0x3766680,0x3b6eb00,0x4097100,0x449f580,0x49bde00,0x4dc6280,0x51c4980,0x56ecf80,0x5ac7500,0x5d71d00,0x5ed6d00,0x5ed6d00,0x5ed6d00,0x5ed6d00,0x5ed6d00,0x5ed6d00,0x5ed6d00,0x5ed6d00,0x5ed6d00,0x5ed6d00,0x5ed6d00,323,1315,2307,3115,4107,4915,5907,6715,7707,8515,9507,10499,11307,12299,13107,14084,14892,15685,16677,17415,17945,17945,17945,17945,21,20,0,0,0,0,0,0,0x32b500,0x853b00,0xd7c100,0x1184580,0x16acb80,0x1ab5000,0x1fdd600,0x23e5a80,0x290e080,0x2d16500,0x323eb00,0x3767100,0x3b6f580,0x4097b80,0x44a0000,0x49be880,0x4dc6d00,0x51c5400,0x56eda00,0x5ac7f80,0x5d72780,0x5d72780,0x5d72780,0x5d72780,0x5d72780,0x5d72780,0x5d72780,0x5d72780,0x5d72780,0x5d72780,0x5d72780,0x5d72780,324,1316,2308,3116,4108,4916,5908,6716,7708,8516,9508,10500,11308,12300,13108,14085,14893,15686,16678,17416,17946,17946,17946,17946,21,20,0,0,0,0,0,0,0x32bf80,0x854580,0xd7cb80,0x1185000,0x16ad600,0x1ab5a80,0x1fde080,0x23e6500,0x290eb00,0x2d16f80,0x323f580,0x3767b80,0x3b70000,0x4098600,0x44a0a80,0x49bf300,0x4dc7780,0x51c5e80,0x56ee480,0x5ac8a00,0x5d73200,0x5d73200,0x5d73200,0x5d73200,0x5d73200,0x5d73200,0x5d73200,0x5d73200,0x5d73200,0x5d73200,0x5d73200,0x5d73200,325,1317,2309,3117,4109,4917,5909,6717,7709,8517,9509,10501,11309,12301,13109,14086,14894,15687,16679,6984,6984,6984,6984,6984,20,19,0,0,0,0,0,0,0x32ca00,0x855000,0xd7d600,0x1185a80,0x16ae080,0x1ab6500,0x1fdeb00,0x23e6f80,0x290f580,0x2d17a00,0x3240000,0x3768600,0x3b70a80,0x4099080,0x44a1500,0x49bfd80,0x4dc8200,0x51c6900,0x56eef00,0x250da00,0x250da00,0x250da00,0x250da00,0x250da00,0x250da00,0x250da00,0x250da00,0x250da00,0x250da00,0x250da00,0x250da00,0x250da00,326,1318,2310,3118,4110,4918,5910,6718,7710,8518,9510,10502,11310,12302,13110,14087,14895,15872,16680,7240,17947,17947,17947,17947,21,20,0,0,0,0,0,0,0x32d480,0x855a80,0xd7e080,0x1186500,0x16aeb00,0x1ab6f80,0x1fdf580,0x23e7a00,0x2910000,0x2d18480,0x3240a80,0x3769080,0x3b71500,0x4099b00,0x44a1f80,0x49c0800,0x4dc8c80,0x52e7500,0x56ef980,0x265cf80,0x5d73c80,0x5d73c80,0x5d73c80,0x5d73c80,0x5d73c80,0x5d73c80,0x5d73c80,0x5d73c80,0x5d73c80,0x5d73c80,0x5d73c80,0x5d73c80,327,1319,2311,3119,4111,4919,5911,6719,7711,8519,9511,10503,11311,12303,13111,14088,14896,15873,16681,7496,7496,7496,7496,7496,20,19,0,0,0,0,0,0,0x32df00,0x856500,0xd7eb00,0x1186f80,0x16af580,0x1ab7a00,0x1fe0000,0x23e8480,0x2910a80,0x2d18f00,0x3241500,0x3769b00,0x3b71f80,0x409a580,0x44a2a00,0x49c1280,0x4dc9700,0x52e7f80,0x56f0400,0x27ac500,0x27ac500,0x27ac500,0x27ac500,0x27ac500,0x27ac500,0x27ac500,0x27ac500,0x27ac500,0x27ac500,0x27ac500,0x27ac500,0x27ac500,512,1320,2312,3120,4112,4920,5912,6720,7712,8704,9512,10504,11312,12304,13112,14089,14897,15874,16682,17417,17948,17948,17948,17948,21,20,0,0,0,0,0,0,0x44eb00,0x856f80,0xd7f580,0x1187a00,0x16b0000,0x1ab8480,0x1fe0a80,0x23e8f00,0x2911500,0x2e39b00,0x3241f80,0x376a580,0x3b72a00,0x409b000,0x44a3480,0x49c1d00,0x4dca180,0x52e8a00,0x56f0e80,0x5ac9480,0x5d74700,0x5d74700,0x5d74700,0x5d74700,0x5d74700,0x5d74700,0x5d74700,0x5d74700,0x5d74700,0x5d74700,0x5d74700,0x5d74700,513,1321,2313,3121,4113,4921,5913,6721,7713,8705,9513,10505,11313,12305,13113,14090,14898,15875,16683,17418,17949,17949,17949,17949,21,20,0,0,0,0,0,0,0x44f580,0x857a00,0xd80000,0x1188480,0x16b0a80,0x1ab8f00,0x1fe1500,0x23e9980,0x2911f80,0x2e3a580,0x3242a00,0x376b000,0x3b73480,0x409ba80,0x44a3f00,0x49c2780,0x4dcac00,0x52e9480,0x56f1900,0x5ac9f00,0x5d75180,0x5d75180,0x5d75180,0x5d75180,0x5d75180,0x5d75180,0x5d75180,0x5d75180,0x5d75180,0x5d75180,0x5d75180,0x5d75180,514,1322,2314,3122,4114,4922,5914,6722,7714,8706,9514,10506,11314,12306,13114,14091,14899,15876,16684,17419,17950,18234,18234,18234,22,21,0,0,0,0,0,0,0x450000,0x858480,0xd80a80,0x1188f00,0x16b1500,0x1ab9980,0x1fe1f80,0x23ea400,0x2912a00,0x2e3b000,0x3243480,0x376ba80,0x3b73f00,0x409c500,0x44a4980,0x49c3200,0x4dcb680,0x52e9f00,0x56f2380,0x5aca980,0x5d75c00,0x5ed7780,0x5ed7780,0x5ed7780,0x5ed7780,0x5ed7780,0x5ed7780,0x5ed7780,0x5ed7780,0x5ed7780,0x5ed7780,0x5ed7780,515,1323,2315,3123,4115,4923,5915,6723,7715,8707,9515,10507,11315,12307,13115,14092,14900,15877,16685,17420,17951,17951,17951,17951,21,20,0,0,0,0,0,0,0x450a80,0x858f00,0xd81500,0x1189980,0x16b1f80,0x1aba400,0x1fe2a00,0x23eae80,0x2913480,0x2e3ba80,0x3243f00,0x376c500,0x3b74980,0x409cf80,0x44a5400,0x49c3c80,0x4dcc100,0x52ea980,0x56f2e00,0x5acb400,0x5d76680,0x5d76680,0x5d76680,0x5d76680,0x5d76680,0x5d76680,0x5d76680,0x5d76680,0x5d76680,0x5d76680,0x5d76680,0x5d76680,516,1324,2316,3124,4116,4924,5916,6724,7716,8708,9516,10508,11316,12308,13116,14093,14901,15878,16686,17421,17952,17952,17952,17952,21,20,0,0,0,0,0,0,0x451500,0x859980,0xd81f80,0x118a400,0x16b2a00,0x1abae80,0x1fe3480,0x23eb900,0x2913f00,0x2e3c500,0x3244980,0x376cf80,0x3b75400,0x409da00,0x44a5e80,0x49c4700,0x4dccb80,0x52eb400,0x56f3880,0x5acbe80,0x5d77100,0x5d77100,0x5d77100,0x5d77100,0x5d77100,0x5d77100,0x5d77100,0x5d77100,0x5d77100,0x5d77100,0x5d77100,0x5d77100,517,1325,2317,3125,4117,4925,5917,6725,7717,8709,9517,10509,11317,12309,13117,14094,14902,15879,16687,7752,7752,7752,7752,7752,20,19,0,0,0,0,0,0,0x451f80,0x85a400,0xd82a00,0x118ae80,0x16b3480,0x1abb900,0x1fe3f00,0x23ec380,0x2914980,0x2e3cf80,0x3245400,0x376da00,0x3b75e80,0x409e480,0x44a6900,0x49c5180,0x4dcd600,0x52ebe80,0x56f4300,0x28fba80,0x28fba80,0x28fba80,0x28fba80,0x28fba80,0x28fba80,0x28fba80,0x28fba80,0x28fba80,0x28fba80,0x28fba80,0x28fba80,0x28fba80,518,1326,2318,3126,4118,4926,5918,6726,7718,8710,9518,10510,11318,12310,13118,14095,14903,15880,16688,8008,17953,17953,17953,17953,21,20,0,0,0,0,0,0,0x452a00,0x85ae80,0xd83480,0x118b900,0x16b3f00,0x1abc380,0x1fe4980,0x23ece00,0x2915400,0x2e3da00,0x3245e80,0x376e480,0x3b76900,0x409ef00,0x44a7380,0x49c5c00,0x4dce080,0x52ec900,0x56f4d80,0x2a4b000,0x5d77b80,0x5d77b80,0x5d77b80,0x5d77b80,0x5d77b80,0x5d77b80,0x5d77b80,0x5d77b80,0x5d77b80,0x5d77b80,0x5d77b80,0x5d77b80,519,1327,2319,3127,4119,4927,5919,6727,7719,8711,9519,10511,11319,12311,13119,14096,14904,15881,16689,8264,8264,8264,8264,8264,20,19,0,0,0,0,0,0,0x453480,0x85b900,0xd83f00,0x118c380,0x16b4980,0x1abce00,0x1fe5400,0x23ed880,0x2915e80,0x2e3e480,0x3246900,0x376ef00,0x3b77380,0x409f980,0x44a7e00,0x49c6680,0x4dceb00,0x52ed380,0x56f5800,0x2b9a580,0x2b9a580,0x2b9a580,0x2b9a580,0x2b9a580,0x2b9a580,0x2b9a580,0x2b9a580,0x2b9a580,0x2b9a580,0x2b9a580,0x2b9a580,0x2b9a580,520,1328,2320,3128,4120,4928,5920,6912,7720,8712,9520,10512,11320,12312,13120,14097,14905,15882,16690,17422,17954,17954,17954,17954,21,20,0,0,0,0,0,0,0x453f00,0x85c380,0xd84980,0x118ce00,0x16b5400,0x1abd880,0x1fe5e80,0x250e480,0x2916900,0x2e3ef00,0x3247380,0x376f980,0x3b77e00,0x40a0400,0x44a8880,0x49c7100,0x4dcf580,0x52ede00,0x56f6280,0x5acc900,0x5d78600,0x5d78600,0x5d78600,0x5d78600,0x5d78600,0x5d78600,0x5d78600,0x5d78600,0x5d78600,0x5d78600,0x5d78600,0x5d78600,521,1329,2321,3129,4121,4929,5921,6913,7721,8713,9521,10513,11321,12313,13121,14098,14906,15883,16691,17423,17955,17955,17955,17955,21,20,0,0,0,0,0,0,0x454980,0x85ce00,0xd85400,0x118d880,0x16b5e80,0x1abe300,0x1fe6900,0x250ef00,0x2917380,0x2e3f980,0x3247e00,0x3770400,0x3b78880,0x40a0e80,0x44a9300,0x49c7b80,0x4dd0000,0x52ee880,0x56f6d00,0x5acd380,0x5d79080,0x5d79080,0x5d79080,0x5d79080,0x5d79080,0x5d79080,0x5d79080,0x5d79080,0x5d79080,0x5d79080,0x5d79080,0x5d79080,522,1330,2322,3130,4122,4930,5922,6914,7722,8714,9522,10514,11322,12314,13122,14099,14907,15884,16692,17424,17956,18235,18235,18235,22,21,0,0,0,0,0,0,0x455400,0x85d880,0xd85e80,0x118e300,0x16b6900,0x1abed80,0x1fe7380,0x250f980,0x2917e00,0x2e40400,0x3248880,0x3770e80,0x3b79300,0x40a1900,0x44a9d80,0x49c8600,0x4dd0a80,0x52ef300,0x56f7780,0x5acde00,0x5d79b00,0x5ed8200,0x5ed8200,0x5ed8200,0x5ed8200,0x5ed8200,0x5ed8200,0x5ed8200,0x5ed8200,0x5ed8200,0x5ed8200,0x5ed8200,523,1331,2323,3131,4123,4931,5923,6915,7723,8715,9523,10515,11323,12315,13123,14100,14908,15885,16693,17425,17957,17957,17957,17957,21,20,0,0,0,0,0,0,0x455e80,0x85e300,0xd86900,0x118ed80,0x16b7380,0x1abf800,0x1fe7e00,0x2510400,0x2918880,0x2e40e80,0x3249300,0x3771900,0x3b79d80,0x40a2380,0x44aa800,0x49c9080,0x4dd1500,0x52efd80,0x56f8200,0x5ace880,0x5d7a580,0x5d7a580,0x5d7a580,0x5d7a580,0x5d7a580,0x5d7a580,0x5d7a580,0x5d7a580,0x5d7a580,0x5d7a580,0x5d7a580,0x5d7a580,524,1332,2324,3132,4124,4932,5924,6916,7724,8716,9524,10516,11324,12316,13124,14101,14909,15886,16694,17426,17958,17958,17958,17958,21,20,0,0,0,0,0,0,0x456900,0x85ed80,0xd87380,0x118f800,0x16b7e00,0x1ac0280,0x1fe8880,0x2510e80,0x2919300,0x2e41900,0x3249d80,0x3772380,0x3b7a800,0x40a2e00,0x44ab280,0x49c9b00,0x4dd1f80,0x52f0800,0x56f8c80,0x5acf300,0x5d7b000,0x5d7b000,0x5d7b000,0x5d7b000,0x5d7b000,0x5d7b000,0x5d7b000,0x5d7b000,0x5d7b000,0x5d7b000,0x5d7b000,0x5d7b000,525,1333,2325,3133,4125,4933,5925,6917,7725,8717,9525,10517,11325,12317,13125,14102,14910,15887,16695,8520,8520,8520,8520,8520,20,19,0,0,0,0,0,0,0x457380,0x85f800,0xd87e00,0x1190280,0x16b8880,0x1ac0d00,0x1fe9300,0x2511900,0x2919d80,0x2e42380,0x324a800,0x3772e00,0x3b7b280,0x40a3880,0x44abd00,0x49ca580,0x4dd2a00,0x52f1280,0x56f9700,0x2ce9b00,0x2ce9b00,0x2ce9b00,0x2ce9b00,0x2ce9b00,0x2ce9b00,0x2ce9b00,0x2ce9b00,0x2ce9b00,0x2ce9b00,0x2ce9b00,0x2ce9b00,0x2ce9b00,526,1334,2326,3134,4126,4934,5926,6918,7726,8718,9526,10518,11326,12318,13126,14103,14911,15888,16696,8776,17959,17959,17959,17959,21,20,0,0,0,0,0,0,0x457e00,0x860280,0xd88880,0x1190d00,0x16b9300,0x1ac1780,0x1fe9d80,0x2512380,0x291a800,0x2e42e00,0x324b280,0x3773880,0x3b7bd00,0x40a4300,0x44ac780,0x49cb000,0x4dd3480,0x52f1d00,0x56fa180,0x2e39080,0x5d7ba80,0x5d7ba80,0x5d7ba80,0x5d7ba80,0x5d7ba80,0x5d7ba80,0x5d7ba80,0x5d7ba80,0x5d7ba80,0x5d7ba80,0x5d7ba80,0x5d7ba80,527,1335,2327,3135,4127,4935,5927,6919,7727,8719,9527,10519,11327,12319,13127,14104,14912,15889,16697,9032,9032,9032,9032,9032,20,19,0,0,0,0,0,0,0x458880,0x860d00,0xd89300,0x1191780,0x16b9d80,0x1ac2200,0x1fea800,0x2512e00,0x291b280,0x2e43880,0x324bd00,0x3774300,0x3b7c780,0x40a4d80,0x44ad200,0x49cba80,0x4dd3f00,0x52f2780,0x56fac00,0x2f88600,0x2f88600,0x2f88600,0x2f88600,0x2f88600,0x2f88600,0x2f88600,0x2f88600,0x2f88600,0x2f88600,0x2f88600,0x2f88600,0x2f88600,528,1336,2328,3136,4128,5120,5928,6920,7728,8720,9528,10520,11328,12320,13312,14105,14913,15890,16698,17427,17960,17960,17960,17960,21,20,0,0,0,0,0,0,0x459300,0x861780,0xd89d80,0x1192200,0x16ba800,0x1be2e00,0x1feb280,0x2513880,0x291bd00,0x2e44300,0x324c780,0x3774d80,0x3b7d200,0x40a5800,0x45cde00,0x49cc500,0x4dd4980,0x52f3200,0x56fb680,0x5acfd80,0x5d7c500,0x5d7c500,0x5d7c500,0x5d7c500,0x5d7c500,0x5d7c500,0x5d7c500,0x5d7c500,0x5d7c500,0x5d7c500,0x5d7c500,0x5d7c500,529,1337,2329,3137,4129,5121,5929,6921,7729,8721,9529,10521,11329,12321,13313,14106,14914,15891,16699,17428,17961,17961,17961,17961,21,20,0,0,0,0,0,0,0x459d80,0x862200,0xd8a800,0x1192c80,0x16bb280,0x1be3880,0x1febd00,0x2514300,0x291c780,0x2e44d80,0x324d200,0x3775800,0x3b7dc80,0x40a6280,0x45ce880,0x49ccf80,0x4dd5400,0x52f3c80,0x56fc100,0x5ad0800,0x5d7cf80,0x5d7cf80,0x5d7cf80,0x5d7cf80,0x5d7cf80,0x5d7cf80,0x5d7cf80,0x5d7cf80,0x5d7cf80,0x5d7cf80,0x5d7cf80,0x5d7cf80,530,1338,2330,3138,4130,5122,5930,6922,7730,8722,9530,10522,11330,12322,13314,14107,14915,15892,16700,17429,17962,18236,18236,18236,22,21,0,0,0,0,0,0,0x45a800,0x862c80,0xd8b280,0x1193700,0x16bbd00,0x1be4300,0x1fec780,0x2514d80,0x291d200,0x2e45800,0x324dc80,0x3776280,0x3b7e700,0x40a6d00,0x45cf300,0x49cda00,0x4dd5e80,0x52f4700,0x56fcb80,0x5ad1280,0x5d7da00,0x5ed8c80,0x5ed8c80,0x5ed8c80,0x5ed8c80,0x5ed8c80,0x5ed8c80,0x5ed8c80,0x5ed8c80,0x5ed8c80,0x5ed8c80,0x5ed8c80,531,1339,2331,3139,4131,5123,5931,6923,7731,8723,9531,10523,11331,12323,13315,14108,14916,15893,16701,17430,17963,17963,17963,17963,21,20,0,0,0,0,0,0,0x45b280,0x863700,0xd8bd00,0x1194180,0x16bc780,0x1be4d80,0x1fed200,0x2515800,0x291dc80,0x2e46280,0x324e700,0x3776d00,0x3b7f180,0x40a7780,0x45cfd80,0x49ce480,0x4dd6900,0x52f5180,0x56fd600,0x5ad1d00,0x5d7e480,0x5d7e480,0x5d7e480,0x5d7e480,0x5d7e480,0x5d7e480,0x5d7e480,0x5d7e480,0x5d7e480,0x5d7e480,0x5d7e480,0x5d7e480,532,1340,2332,3140,4132,5124,5932,6924,7732,8724,9532,10524,11332,12324,13316,14109,14917,15894,16702,17431,17964,17964,17964,17964,21,20,0,0,0,0,0,0,0x45bd00,0x864180,0xd8c780,0x1194c00,0x16bd200,0x1be5800,0x1fedc80,0x2516280,0x291e700,0x2e46d00,0x324f180,0x3777780,0x3b7fc00,0x40a8200,0x45d0800,0x49cef00,0x4dd7380,0x52f5c00,0x56fe080,0x5ad2780,0x5d7ef00,0x5d7ef00,0x5d7ef00,0x5d7ef00,0x5d7ef00,0x5d7ef00,0x5d7ef00,0x5d7ef00,0x5d7ef00,0x5d7ef00,0x5d7ef00,0x5d7ef00,533,1341,2333,3141,4133,5125,5933,6925,7733,8725,9533,10525,11333,12325,13317,14110,14918,15895,16703,9288,9288,9288,9288,9288,20,19,0,0,0,0,0,0,0x45c780,0x864c00,0xd8d200,0x1195680,0x16bdc80,0x1be6280,0x1fee700,0x2516d00,0x291f180,0x2e47780,0x324fc00,0x3778200,0x3b80680,0x40a8c80,0x45d1280,0x49cf980,0x4dd7e00,0x52f6680,0x56feb00,0x30d7b80,0x30d7b80,0x30d7b80,0x30d7b80,0x30d7b80,0x30d7b80,0x30d7b80,0x30d7b80,0x30d7b80,0x30d7b80,0x30d7b80,0x30d7b80,0x30d7b80,534,1342,2334,3142,4134,5126,5934,6926,7734,8726,9534,10526,11334,12326,13318,14111,14919,15896,16704,9544,17965,17965,17965,17965,21,20,0,0,0,0,0,0,0x45d200,0x865680,0xd8dc80,0x1196100,0x16be700,0x1be6d00,0x1fef180,0x2517780,0x291fc00,0x2e48200,0x3250680,0x3778c80,0x3b81100,0x40a9700,0x45d1d00,0x49d0400,0x4dd8880,0x52f7100,0x56ff580,0x3227100,0x5d7f980,0x5d7f980,0x5d7f980,0x5d7f980,0x5d7f980,0x5d7f980,0x5d7f980,0x5d7f980,0x5d7f980,0x5d7f980,0x5d7f980,0x5d7f980,535,1343,2335,3143,4135,5127,5935,6927,7735,8727,9535,10527,11335,12327,13319,14112,15104,15897,16705,9800,9800,9800,9800,9800,20,19,0,0,0,0,0,0,0x45dc80,0x866100,0xd8e700,0x1196b80,0x16bf180,0x1be7780,0x1fefc00,0x2518200,0x2920680,0x2e48c80,0x3251100,0x3779700,0x3b81b80,0x40aa180,0x45d2780,0x49d0e80,0x4ef9480,0x52f7b80,0x5700000,0x3376680,0x3376680,0x3376680,0x3376680,0x3376680,0x3376680,0x3376680,0x3376680,0x3376680,0x3376680,0x3376680,0x3376680,0x3376680,536,1344,2336,3328,4136,5128,5936,6928,7736,8728,9536,10528,11520,12328,13320,14113,15105,15898,16706,17432,17966,17966,17966,17966,21,20,0,0,0,0,0,0,0x45e700,0x866b80,0xd8f180,0x12b7780,0x16bfc00,0x1be8200,0x1ff0680,0x2518c80,0x2921100,0x2e49700,0x3251b80,0x377a180,0x3ca2780,0x40aac00,0x45d3200,0x49d1900,0x4ef9f00,0x52f8600,0x5700a80,0x5ad3200,0x5d80400,0x5d80400,0x5d80400,0x5d80400,0x5d80400,0x5d80400,0x5d80400,0x5d80400,0x5d80400,0x5d80400,0x5d80400,0x5d80400,537,1345,2337,3329,4137,5129,5937,6929,7737,8729,9537,10529,11521,12329,13321,14114,15106,15899,16707,17433,17967,17967,17967,17967,21,20,0,0,0,0,0,0,0x45f180,0x867600,0xd8fc00,0x12b8200,0x16c0680,0x1be8c80,0x1ff1100,0x2519700,0x2921b80,0x2e4a180,0x3252600,0x377ac00,0x3ca3200,0x40ab680,0x45d3c80,0x49d2380,0x4efa980,0x52f9080,0x5701500,0x5ad3c80,0x5d80e80,0x5d80e80,0x5d80e80,0x5d80e80,0x5d80e80,0x5d80e80,0x5d80e80,0x5d80e80,0x5d80e80,0x5d80e80,0x5d80e80,0x5d80e80,538,1346,2338,3330,4138,5130,5938,6930,7738,8730,9538,10530,11522,12330,13322,14115,15107,15900,16708,17434,17968,18237,18237,18237,22,21,0,0,0,0,0,0,0x45fc00,0x868080,0xd90680,0x12b8c80,0x16c1100,0x1be9700,0x1ff1b80,0x251a180,0x2922600,0x2e4ac00,0x3253080,0x377b680,0x3ca3c80,0x40ac100,0x45d4700,0x49d2e00,0x4efb400,0x52f9b00,0x5701f80,0x5ad4700,0x5d81900,0x5ed9700,0x5ed9700,0x5ed9700,0x5ed9700,0x5ed9700,0x5ed9700,0x5ed9700,0x5ed9700,0x5ed9700,0x5ed9700,0x5ed9700,539,1347,2339,3331,4139,5131,5939,6931,7739,8731,9539,10531,11523,12331,13323,14116,15108,15901,16709,17435,17969,17969,17969,17969,21,20,0,0,0,0,0,0,0x460680,0x868b00,0xd91100,0x12b9700,0x16c1b80,0x1bea180,0x1ff2600,0x251ac00,0x2923080,0x2e4b680,0x3253b00,0x377c100,0x3ca4700,0x40acb80,0x45d5180,0x49d3880,0x4efbe80,0x52fa580,0x5702a00,0x5ad5180,0x5d82380,0x5d82380,0x5d82380,0x5d82380,0x5d82380,0x5d82380,0x5d82380,0x5d82380,0x5d82380,0x5d82380,0x5d82380,0x5d82380,540,1348,2340,3332,4140,5132,5940,6932,7740,8732,9540,10532,11524,12332,13324,14117,15109,15902,16710,17436,17970,17970,17970,17970,21,20,0,0,0,0,0,0,0x461100,0x869580,0xd91b80,0x12ba180,0x16c2600,0x1beac00,0x1ff3080,0x251b680,0x2923b00,0x2e4c100,0x3254580,0x377cb80,0x3ca5180,0x40ad600,0x45d5c00,0x49d4300,0x4efc900,0x52fb000,0x5703480,0x5ad5c00,0x5d82e00,0x5d82e00,0x5d82e00,0x5d82e00,0x5d82e00,0x5d82e00,0x5d82e00,0x5d82e00,0x5d82e00,0x5d82e00,0x5d82e00,0x5d82e00,541,1349,2341,3333,4141,5133,5941,6933,7741,8733,9541,10533,11525,12333,13325,14118,15110,15903,16711,10056,10056,10056,10056,10056,20,19,0,0,0,0,0,0,0x461b80,0x86a000,0xd92600,0x12bac00,0x16c3080,0x1beb680,0x1ff3b00,0x251c100,0x2924580,0x2e4cb80,0x3255000,0x377d600,0x3ca5c00,0x40ae080,0x45d6680,0x49d4d80,0x4efd380,0x52fba80,0x5703f00,0x34c5c00,0x34c5c00,0x34c5c00,0x34c5c00,0x34c5c00,0x34c5c00,0x34c5c00,0x34c5c00,0x34c5c00,0x34c5c00,0x34c5c00,0x34c5c00,0x34c5c00,542,1350,2342,3334,4142,5134,5942,6934,7742,8734,9542,10534,11526,12334,13326,14119,15111,15904,16896,10312,17971,17971,17971,17971,21,20,0,0,0,0,0,0,0x462600,0x86aa80,0xd93080,0x12bb680,0x16c3b00,0x1bec100,0x1ff4580,0x251cb80,0x2925000,0x2e4d600,0x3255a80,0x377e080,0x3ca6680,0x40aeb00,0x45d7100,0x49d5800,0x4efde00,0x52fc500,0x5824b00,0x3615180,0x5d83880,0x5d83880,0x5d83880,0x5d83880,0x5d83880,0x5d83880,0x5d83880,0x5d83880,0x5d83880,0x5d83880,0x5d83880,0x5d83880,543,1351,2343,3335,4143,5135,5943,6935,7743,8735,9543,10535,11527,12335,13327,14120,15112,15905,16897,10568,10568,10568,10568,10568,20,19,0,0,0,0,0,0,0x463080,0x86b500,0xd93b00,0x12bc100,0x16c4580,0x1becb80,0x1ff5000,0x251d600,0x2925a80,0x2e4e080,0x3256500,0x377eb00,0x3ca7100,0x40af580,0x45d7b80,0x49d6280,0x4efe880,0x52fcf80,0x5825580,0x3764700,0x3764700,0x3764700,0x3764700,0x3764700,0x3764700,0x3764700,0x3764700,0x3764700,0x3764700,0x3764700,0x3764700,0x3764700,544,1536,2344,3336,4144,5136,5944,6936,7744,8736,9728,10536,11528,12336,13328,14121,15113,15906,16898,17437,17972,17972,17972,17972,21,20,0,0,0,0,0,0,0x463b00,0x98c100,0xd94580,0x12bcb80,0x16c5000,0x1bed600,0x1ff5a80,0x251e080,0x2926500,0x2e4eb00,0x3377100,0x377f580,0x3ca7b80,0x40b0000,0x45d8600,0x49d6d00,0x4eff300,0x52fda00,0x5826000,0x5ad6680,0x5d84300,0x5d84300,0x5d84300,0x5d84300,0x5d84300,0x5d84300,0x5d84300,0x5d84300,0x5d84300,0x5d84300,0x5d84300,0x5d84300,545,1537,2345,3337,4145,5137,5945,6937,7745,8737,9729,10537,11529,12337,13329,14122,15114,15907,16899,17438,17973,17973,17973,17973,21,20,0,0,0,0,0,0,0x464580,0x98cb80,0xd95000,0x12bd600,0x16c5a80,0x1bee080,0x1ff6500,0x251eb00,0x2926f80,0x2e4f580,0x3377b80,0x3780000,0x3ca8600,0x40b0a80,0x45d9080,0x49d7780,0x4effd80,0x52fe480,0x5826a80,0x5ad7100,0x5d84d80,0x5d84d80,0x5d84d80,0x5d84d80,0x5d84d80,0x5d84d80,0x5d84d80,0x5d84d80,0x5d84d80,0x5d84d80,0x5d84d80,0x5d84d80,546,1538,2346,3338,4146,5138,5946,6938,7746,8738,9730,10538,11530,12338,13330,14123,15115,15908,16900,17439,17974,18238,18238,18238,22,21,0,0,0,0,0,0,0x465000,0x98d600,0xd95a80,0x12be080,0x16c6500,0x1beeb00,0x1ff6f80,0x251f580,0x2927a00,0x2e50000,0x3378600,0x3780a80,0x3ca9080,0x40b1500,0x45d9b00,0x49d8200,0x4f00800,0x52fef00,0x5827500,0x5ad7b80,0x5d85800,0x5eda180,0x5eda180,0x5eda180,0x5eda180,0x5eda180,0x5eda180,0x5eda180,0x5eda180,0x5eda180,0x5eda180,0x5eda180,547,1539,2347,3339,4147,5139,5947,6939,7747,8739,9731,10539,11531,12339,13331,14124,15116,15909,16901,17440,17975,17975,17975,17975,21,20,0,0,0,0,0,0,0x465a80,0x98e080,0xd96500,0x12beb00,0x16c6f80,0x1bef580,0x1ff7a00,0x2520000,0x2928480,0x2e50a80,0x3379080,0x3781500,0x3ca9b00,0x40b1f80,0x45da580,0x49d8c80,0x4f01280,0x52ff980,0x5827f80,0x5ad8600,0x5d86280,0x5d86280,0x5d86280,0x5d86280,0x5d86280,0x5d86280,0x5d86280,0x5d86280,0x5d86280,0x5d86280,0x5d86280,0x5d86280,548,1540,2348,3340,4148,5140,5948,6940,7748,8740,9732,10540,11532,12340,13332,14125,15117,15910,16902,17441,17976,17976,17976,17976,21,20,0,0,0,0,0,0,0x466500,0x98eb00,0xd96f80,0x12bf580,0x16c7a00,0x1bf0000,0x1ff8480,0x2520a80,0x2928f00,0x2e51500,0x3379b00,0x3781f80,0x3caa580,0x40b2a00,0x45db000,0x49d9700,0x4f01d00,0x5300400,0x5828a00,0x5ad9080,0x5d86d00,0x5d86d00,0x5d86d00,0x5d86d00,0x5d86d00,0x5d86d00,0x5d86d00,0x5d86d00,0x5d86d00,0x5d86d00,0x5d86d00,0x5d86d00,549,1541,2349,3341,4149,5141,5949,6941,7749,8741,9733,10541,11533,12341,13333,14126,15118,15911,16903,10824,10824,10824,10824,10824,20,19,0,0,0,0,0,0,0x466f80,0x98f580,0xd97a00,0x12c0000,0x16c8480,0x1bf0a80,0x1ff8f00,0x2521500,0x2929980,0x2e51f80,0x337a580,0x3782a00,0x3cab000,0x40b3480,0x45dba80,0x49da180,0x4f02780,0x5300e80,0x5829480,0x38b3c80,0x38b3c80,0x38b3c80,0x38b3c80,0x38b3c80,0x38b3c80,0x38b3c80,0x38b3c80,0x38b3c80,0x38b3c80,0x38b3c80,0x38b3c80,0x38b3c80,550,1542,2350,3342,4150,5142,5950,6942,7750,8742,9734,10542,11534,12342,13334,14127,15119,15912,16904,11080,17977,17977,17977,17977,21,20,0,0,0,0,0,0,0x467a00,0x990000,0xd98480,0x12c0a80,0x16c8f00,0x1bf1500,0x1ff9980,0x2521f80,0x292a400,0x2e52a00,0x337b000,0x3783480,0x3caba80,0x40b3f00,0x45dc500,0x49dac00,0x4f03200,0x5301900,0x5829f00,0x3a03200,0x5d87780,0x5d87780,0x5d87780,0x5d87780,0x5d87780,0x5d87780,0x5d87780,0x5d87780,0x5d87780,0x5d87780,0x5d87780,0x5d87780,551,1543,2351,3343,4151,5143,5951,6943,7751,8743,9735,10543,11535,12343,13335,14128,15120,15913,16905,11336,11336,11336,11336,11336,20,19,0,0,0,0,0,0,0x468480,0x990a80,0xd98f00,0x12c1500,0x16c9980,0x1bf1f80,0x1ffa400,0x2522a00,0x292ae80,0x2e53480,0x337ba80,0x3783f00,0x3cac500,0x40b4980,0x45dcf80,0x49db680,0x4f03c80,0x5302380,0x582a980,0x3b52780,0x3b52780,0x3b52780,0x3b52780,0x3b52780,0x3b52780,0x3b52780,0x3b52780,0x3b52780,0x3b52780,0x3b52780,0x3b52780,0x3b52780,552,1544,2352,3344,4152,5144,5952,6944,7936,8744,9736,10544,11536,12344,13336,14129,15121,15914,16906,17442,17978,17978,17978,17978,21,20,0,0,0,0,0,0,0x468f00,0x991500,0xd99980,0x12c1f80,0x16ca400,0x1bf2a00,0x1ffae80,0x2523480,0x2a4ba80,0x2e53f00,0x337c500,0x3784980,0x3cacf80,0x40b5400,0x45dda00,0x49dc100,0x4f04700,0x5302e00,0x582b400,0x5ad9b00,0x5d88200,0x5d88200,0x5d88200,0x5d88200,0x5d88200,0x5d88200,0x5d88200,0x5d88200,0x5d88200,0x5d88200,0x5d88200,0x5d88200,553,1545,2353,3345,4153,5145,5953,6945,7937,8745,9737,10545,11537,12345,13337,14130,15122,15915,16907,17443,17979,17979,17979,17979,21,20,0,0,0,0,0,0,0x469980,0x991f80,0xd9a400,0x12c2a00,0x16cae80,0x1bf3480,0x1ffb900,0x2523f00,0x2a4c500,0x2e54980,0x337cf80,0x3785400,0x3cada00,0x40b5e80,0x45de480,0x49dcb80,0x4f05180,0x5303880,0x582be80,0x5ada580,0x5d88c80,0x5d88c80,0x5d88c80,0x5d88c80,0x5d88c80,0x5d88c80,0x5d88c80,0x5d88c80,0x5d88c80,0x5d88c80,0x5d88c80,0x5d88c80,554,1546,2354,3346,4154,5146,5954,6946,7938,8746,9738,10546,11538,12346,13338,14131,15123,15916,16908,17444,17980,18239,18239,18239,22,21,0,0,0,0,0,0,0x46a400,0x992a00,0xd9ae80,0x12c3480,0x16cb900,0x1bf3f00,0x1ffc380,0x2524980,0x2a4cf80,0x2e55400,0x337da00,0x3785e80,0x3cae480,0x40b6900,0x45def00,0x49dd600,0x4f05c00,0x5304300,0x582c900,0x5adb000,0x5d89700,0x5edac00,0x5edac00,0x5edac00,0x5edac00,0x5edac00,0x5edac00,0x5edac00,0x5edac00,0x5edac00,0x5edac00,0x5edac00,555,1547,2355,3347,4155,5147,5955,6947,7939,8747,9739,10547,11539,12347,13339,14132,15124,15917,16909,17445,17981,17981,17981,17981,21,20,0,0,0,0,0,0,0x46ae80,0x993480,0xd9b900,0x12c3f00,0x16cc380,0x1bf4980,0x1ffce00,0x2525400,0x2a4da00,0x2e55e80,0x337e480,0x3786900,0x3caef00,0x40b7380,0x45df980,0x49de080,0x4f06680,0x5304d80,0x582d380,0x5adba80,0x5d8a180,0x5d8a180,0x5d8a180,0x5d8a180,0x5d8a180,0x5d8a180,0x5d8a180,0x5d8a180,0x5d8a180,0x5d8a180,0x5d8a180,0x5d8a180,556,1548,2356,3348,4156,5148,5956,6948,7940,8748,9740,10548,11540,12348,13340,14133,15125,15918,16910,17446,17982,17982,17982,17982,21,20,0,0,0,0,0,0,0x46b900,0x993f00,0xd9c380,0x12c4980,0x16cce00,0x1bf5400,0x1ffd880,0x2525e80,0x2a4e480,0x2e56900,0x337ef00,0x3787380,0x3caf980,0x40b7e00,0x45e0400,0x49deb00,0x4f07100,0x5305800,0x582de00,0x5adc500,0x5d8ac00,0x5d8ac00,0x5d8ac00,0x5d8ac00,0x5d8ac00,0x5d8ac00,0x5d8ac00,0x5d8ac00,0x5d8ac00,0x5d8ac00,0x5d8ac00,0x5d8ac00,557,1549,2357,3349,4157,5149,5957,6949,7941,8749,9741,10549,11541,12349,13341,14134,15126,15919,16911,11592,11592,11592,11592,11592,20,19,0,0,0,0,0,0,0x46c380,0x994980,0xd9ce00,0x12c5400,0x16cd880,0x1bf5e80,0x1ffe300,0x2526900,0x2a4ef00,0x2e57380,0x337f980,0x3787e00,0x3cb0400,0x40b8880,0x45e0e80,0x49df580,0x4f07b80,0x5306280,0x582e880,0x3ca1d00,0x3ca1d00,0x3ca1d00,0x3ca1d00,0x3ca1d00,0x3ca1d00,0x3ca1d00,0x3ca1d00,0x3ca1d00,0x3ca1d00,0x3ca1d00,0x3ca1d00,0x3ca1d00,558,1550,2358,3350,4158,5150,5958,6950,7942,8750,9742,10550,11542,12350,13342,14135,15127,15920,16912,11848,17983,17983,17983,17983,21,20,0,0,0,0,0,0,0x46ce00,0x995400,0xd9d880,0x12c5e80,0x16ce300,0x1bf6900,0x1ffed80,0x2527380,0x2a4f980,0x2e57e00,0x3380400,0x3788880,0x3cb0e80,0x40b9300,0x45e1900,0x49e0000,0x4f08600,0x5306d00,0x582f300,0x3df1280,0x5d8b680,0x5d8b680,0x5d8b680,0x5d8b680,0x5d8b680,0x5d8b680,0x5d8b680,0x5d8b680,0x5d8b680,0x5d8b680,0x5d8b680,0x5d8b680,559,1551,2359,3351,4159,5151,5959,6951,7943,8751,9743,10551,11543,12351,13343,14136,15128,15921,16913,12104,12104,12104,12104,12104,20,19,0,0,0,0,0,0,0x46d880,0x995e80,0xd9e300,0x12c6900,0x16ced80,0x1bf7380,0x1fff800,0x2527e00,0x2a50400,0x2e58880,0x3380e80,0x3789300,0x3cb1900,0x40b9d80,0x45e2380,0x49e0a80,0x4f09080,0x5307780,0x582fd80,0x3f40800,0x3f40800,0x3f40800,0x3f40800,0x3f40800,0x3f40800,0x3f40800,0x3f40800,0x3f40800,0x3f40800,0x3f40800,0x3f40800,0x3f40800,560,1552,2360,3352,4160,5152,6144,6952,7944,8752,9744,10552,11544,12352,13344,14137,15129,15922,16914,17447,17984,17984,17984,17984,21,20,0,0,0,0,0,0,0x46e300,0x996900,0xd9ed80,0x12c7380,0x16cf800,0x1bf7e00,0x2120400,0x2528880,0x2a50e80,0x2e59300,0x3381900,0x3789d80,0x3cb2380,0x40ba800,0x45e2e00,0x49e1500,0x4f09b00,0x5308200,0x5830800,0x5adcf80,0x5d8c100,0x5d8c100,0x5d8c100,0x5d8c100,0x5d8c100,0x5d8c100,0x5d8c100,0x5d8c100,0x5d8c100,0x5d8c100,0x5d8c100,0x5d8c100,561,1553,2361,3353,4161,5153,6145,6953,7945,8753,9745,10553,11545,12353,13345,14138,15130,15923,16915,17448,17985,17985,17985,17985,21,20,0,0,0,0,0,0,0x46ed80,0x997380,0xd9f800,0x12c7e00,0x16d0280,0x1bf8880,0x2120e80,0x2529300,0x2a51900,0x2e59d80,0x3382380,0x378a800,0x3cb2e00,0x40bb280,0x45e3880,0x49e1f80,0x4f0a580,0x5308c80,0x5831280,0x5adda00,0x5d8cb80,0x5d8cb80,0x5d8cb80,0x5d8cb80,0x5d8cb80,0x5d8cb80,0x5d8cb80,0x5d8cb80,0x5d8cb80,0x5d8cb80,0x5d8cb80,0x5d8cb80,562,1554,2362,3354,4162,5154,6146,6954,7946,8754,9746,10554,11546,12354,13346,14139,15131,15924,16916,17449,17986,18240,18240,18240,22,21,0,0,0,0,0,0,0x46f800,0x997e00,0xda0280,0x12c8880,0x16d0d00,0x1bf9300,0x2121900,0x2529d80,0x2a52380,0x2e5a800,0x3382e00,0x378b280,0x3cb3880,0x40bbd00,0x45e4300,0x49e2a00,0x4f0b000,0x5309700,0x5831d00,0x5ade480,0x5d8d600,0x5edb680,0x5edb680,0x5edb680,0x5edb680,0x5edb680,0x5edb680,0x5edb680,0x5edb680,0x5edb680,0x5edb680,0x5edb680,563,1555,2363,3355,4163,5155,6147,6955,7947,8755,9747,10555,11547,12355,13347,14140,15132,15925,16917,17450,17987,17987,17987,17987,21,20,0,0,0,0,0,0,0x470280,0x998880,0xda0d00,0x12c9300,0x16d1780,0x1bf9d80,0x2122380,0x252a800,0x2a52e00,0x2e5b280,0x3383880,0x378bd00,0x3cb4300,0x40bc780,0x45e4d80,0x49e3480,0x4f0ba80,0x530a180,0x5832780,0x5adef00,0x5d8e080,0x5d8e080,0x5d8e080,0x5d8e080,0x5d8e080,0x5d8e080,0x5d8e080,0x5d8e080,0x5d8e080,0x5d8e080,0x5d8e080,0x5d8e080,564,1556,2364,3356,4164,5156,6148,6956,7948,8756,9748,10556,11548,12356,13348,14141,15133,15926,16918,17451,17988,17988,17988,17988,21,20,0,0,0,0,0,0,0x470d00,0x999300,0xda1780,0x12c9d80,0x16d2200,0x1bfa800,0x2122e00,0x252b280,0x2a53880,0x2e5bd00,0x3384300,0x378c780,0x3cb4d80,0x40bd200,0x45e5800,0x49e3f00,0x4f0c500,0x530ac00,0x5833200,0x5adf980,0x5d8eb00,0x5d8eb00,0x5d8eb00,0x5d8eb00,0x5d8eb00,0x5d8eb00,0x5d8eb00,0x5d8eb00,0x5d8eb00,0x5d8eb00,0x5d8eb00,0x5d8eb00,565,1557,2365,3357,4165,5157,6149,6957,7949,8757,9749,10557,11549,12357,13349,14142,15134,15927,16919,12360,12360,12360,12360,12360,20,19,0,0,0,0,0,0,0x471780,0x999d80,0xda2200,0x12ca800,0x16d2c80,0x1bfb280,0x2123880,0x252bd00,0x2a54300,0x2e5c780,0x3384d80,0x378d200,0x3cb5800,0x40bdc80,0x45e6280,0x49e4980,0x4f0cf80,0x530b680,0x5833c80,0x408fd80,0x408fd80,0x408fd80,0x408fd80,0x408fd80,0x408fd80,0x408fd80,0x408fd80,0x408fd80,0x408fd80,0x408fd80,0x408fd80,0x408fd80,566,1558,2366,3358,4166,5158,6150,6958,7950,8758,9750,10558,11550,12358,13350,14143,15135,15928,16920,12616,17989,17989,17989,17989,21,20,0,0,0,0,0,0,0x472200,0x99a800,0xda2c80,0x12cb280,0x16d3700,0x1bfbd00,0x2124300,0x252c780,0x2a54d80,0x2e5d200,0x3385800,0x378dc80,0x3cb6280,0x40be700,0x45e6d00,0x49e5400,0x4f0da00,0x530c100,0x5834700,0x41df300,0x5d8f580,0x5d8f580,0x5d8f580,0x5d8f580,0x5d8f580,0x5d8f580,0x5d8f580,0x5d8f580,0x5d8f580,0x5d8f580,0x5d8f580,0x5d8f580,567,1559,2367,3359,4167,5159,6151,6959,7951,8759,9751,10559,11551,12359,13351,14144,15136,15929,16921,12872,12872,12872,12872,12872,20,19,0,0,0,0,0,0,0x472c80,0x99b280,0xda3700,0x12cbd00,0x16d4180,0x1bfc780,0x2124d80,0x252d200,0x2a55800,0x2e5dc80,0x3386280,0x378e700,0x3cb6d00,0x40bf180,0x45e7780,0x49e5e80,0x4f0e480,0x530cb80,0x5835180,0x432e880,0x432e880,0x432e880,0x432e880,0x432e880,0x432e880,0x432e880,0x432e880,0x432e880,0x432e880,0x432e880,0x432e880,0x432e880,568,1560,2368,3360,4352,5160,6152,6960,7952,8760,9752,10560,11552,12544,13352,14145,15137,15930,16922,17452,17990,17990,17990,17990,21,20,0,0,0,0,0,0,0x473700,0x99bd00,0xda4180,0x12cc780,0x17f4d80,0x1bfd200,0x2125800,0x252dc80,0x2a56280,0x2e5e700,0x3386d00,0x378f180,0x3cb7780,0x41dfd80,0x45e8200,0x49e6900,0x4f0ef00,0x530d600,0x5835c00,0x5ae0400,0x5d90000,0x5d90000,0x5d90000,0x5d90000,0x5d90000,0x5d90000,0x5d90000,0x5d90000,0x5d90000,0x5d90000,0x5d90000,0x5d90000,569,1561,2369,3361,4353,5161,6153,6961,7953,8761,9753,10561,11553,12545,13353,14146,15138,15931,16923,17453,17991,17991,17991,17991,21,20,0,0,0,0,0,0,0x474180,0x99c780,0xda4c00,0x12cd200,0x17f5800,0x1bfdc80,0x2126280,0x252e700,0x2a56d00,0x2e5f180,0x3387780,0x378fc00,0x3cb8200,0x41e0800,0x45e8c80,0x49e7380,0x4f0f980,0x530e080,0x5836680,0x5ae0e80,0x5d90a80,0x5d90a80,0x5d90a80,0x5d90a80,0x5d90a80,0x5d90a80,0x5d90a80,0x5d90a80,0x5d90a80,0x5d90a80,0x5d90a80,0x5d90a80,570,1562,2370,3362,4354,5162,6154,6962,7954,8762,9754,10562,11554,12546,13354,14147,15139,15932,16924,17454,18176,18241,18241,18241,22,21,0,0,0,0,0,0,0x474c00,0x99d200,0xda5680,0x12cdc80,0x17f6280,0x1bfe700,0x2126d00,0x252f180,0x2a57780,0x2e5fc00,0x3388200,0x3790680,0x3cb8c80,0x41e1280,0x45e9700,0x49e7e00,0x4f10400,0x530eb00,0x5837100,0x5ae1900,0x5eb1680,0x5edc100,0x5edc100,0x5edc100,0x5edc100,0x5edc100,0x5edc100,0x5edc100,0x5edc100,0x5edc100,0x5edc100,0x5edc100,571,1563,2371,3363,4355,5163,6155,6963,7955,8763,9755,10563,11555,12547,13355,14148,15140,15933,16925,17455,18177,18177,18177,18177,21,20,0,0,0,0,0,0,0x475680,0x99dc80,0xda6100,0x12ce700,0x17f6d00,0x1bff180,0x2127780,0x252fc00,0x2a58200,0x2e60680,0x3388c80,0x3791100,0x3cb9700,0x41e1d00,0x45ea180,0x49e8880,0x4f10e80,0x530f580,0x5837b80,0x5ae2380,0x5eb2100,0x5eb2100,0x5eb2100,0x5eb2100,0x5eb2100,0x5eb2100,0x5eb2100,0x5eb2100,0x5eb2100,0x5eb2100,0x5eb2100,0x5eb2100,572,1564,2372,3364,4356,5164,6156,6964,7956,8764,9756,10564,11556,12548,13356,14149,15141,15934,16926,17456,18178,18178,18178,18178,21,20,0,0,0,0,0,0,0x476100,0x99e700,0xda6b80,0x12cf180,0x17f7780,0x1bffc00,0x2128200,0x2530680,0x2a58c80,0x2e61100,0x3389700,0x3791b80,0x3cba180,0x41e2780,0x45eac00,0x49e9300,0x4f11900,0x5310000,0x5838600,0x5ae2e00,0x5eb2b80,0x5eb2b80,0x5eb2b80,0x5eb2b80,0x5eb2b80,0x5eb2b80,0x5eb2b80,0x5eb2b80,0x5eb2b80,0x5eb2b80,0x5eb2b80,0x5eb2b80,573,1565,2373,3365,4357,5165,6157,6965,7957,8765,9757,10565,11557,12549,13357,14150,15142,15935,16927,13128,13128,13128,13128,13128,20,19,0,0,0,0,0,0,0x476b80,0x99f180,0xda7600,0x12cfc00,0x17f8200,0x1c00680,0x2128c80,0x2531100,0x2a59700,0x2e61b80,0x338a180,0x3792600,0x3cbac00,0x41e3200,0x45eb680,0x49e9d80,0x4f12380,0x5310a80,0x5839080,0x447de00,0x447de00,0x447de00,0x447de00,0x447de00,0x447de00,0x447de00,0x447de00,0x447de00,0x447de00,0x447de00,0x447de00,0x447de00,574,1566,2374,3366,4358,5166,6158,6966,7958,8766,9758,10566,11558,12550,13358,14151,15143,15936,16928,13384,18179,18179,18179,18179,21,20,0,0,0,0,0,0,0x477600,0x99fc00,0xda8080,0x12d0680,0x17f8c80,0x1c01100,0x2129700,0x2531b80,0x2a5a180,0x2e62600,0x338ac00,0x3793080,0x3cbb680,0x41e3c80,0x45ec100,0x49ea800,0x4f12e00,0x5311500,0x5839b00,0x45cd380,0x5eb3600,0x5eb3600,0x5eb3600,0x5eb3600,0x5eb3600,0x5eb3600,0x5eb3600,0x5eb3600,0x5eb3600,0x5eb3600,0x5eb3600,0x5eb3600,575,1567,2375,3367,4359,5167,6159,6967,7959,8767,9759,10567,11559,12551,13359,14336,15144,15937,16929,13640,13640,13640,13640,13640,20,19,0,0,0,0,0,0,0x478080,0x9a0680,0xda8b00,0x12d1100,0x17f9700,0x1c01b80,0x212a180,0x2532600,0x2a5ac00,0x2e63080,0x338b680,0x3793b00,0x3cbc100,0x41e4700,0x45ecb80,0x4b0b400,0x4f13880,0x5311f80,0x583a580,0x471c900,0x471c900,0x471c900,0x471c900,0x471c900,0x471c900,0x471c900,0x471c900,0x471c900,0x471c900,0x471c900,0x471c900,0x471c900,576,1568,2560,3368,4360,5168,6160,6968,7960,8768,9760,10752,11560,12552,13360,14337,15145,15938,16930,17457,18180,18180,18180,18180,21,20,0,0,0,0,0,0,0x478b00,0x9a1100,0xec9700,0x12d1b80,0x17fa180,0x1c02600,0x212ac00,0x2533080,0x2a5b680,0x2e63b00,0x338c100,0x38b4700,0x3cbcb80,0x41e5180,0x45ed600,0x4b0be80,0x4f14300,0x5312a00,0x583b000,0x5ae3880,0x5eb4080,0x5eb4080,0x5eb4080,0x5eb4080,0x5eb4080,0x5eb4080,0x5eb4080,0x5eb4080,0x5eb4080,0x5eb4080,0x5eb4080,0x5eb4080,577,1569,2561,3369,4361,5169,6161,6969,7961,8769,9761,10753,11561,12553,13361,14338,15146,15939,16931,17458,18181,18181,18181,18181,21,20,0,0,0,0,0,0,0x479580,0x9a1b80,0xeca180,0x12d2600,0x17fac00,0x1c03080,0x212b680,0x2533b00,0x2a5c100,0x2e64580,0x338cb80,0x38b5180,0x3cbd600,0x41e5c00,0x45ee080,0x4b0c900,0x4f14d80,0x5313480,0x583ba80,0x5ae4300,0x5eb4b00,0x5eb4b00,0x5eb4b00,0x5eb4b00,0x5eb4b00,0x5eb4b00,0x5eb4b00,0x5eb4b00,0x5eb4b00,0x5eb4b00,0x5eb4b00,0x5eb4b00,578,1570,2562,3370,4362,5170,6162,6970,7962,8770,9762,10754,11562,12554,13362,14339,15147,15940,16932,17459,18182,18242,18242,18242,22,21,0,0,0,0,0,0,0x47a000,0x9a2600,0xecac00,0x12d3080,0x17fb680,0x1c03b00,0x212c100,0x2534580,0x2a5cb80,0x2e65000,0x338d600,0x38b5c00,0x3cbe080,0x41e6680,0x45eeb00,0x4b0d380,0x4f15800,0x5313f00,0x583c500,0x5ae4d80,0x5eb5580,0x5edcb80,0x5edcb80,0x5edcb80,0x5edcb80,0x5edcb80,0x5edcb80,0x5edcb80,0x5edcb80,0x5edcb80,0x5edcb80,0x5edcb80,579,1571,2563,3371,4363,5171,6163,6971,7963,8771,9763,10755,11563,12555,13363,14340,15148,15941,16933,17460,18183,18183,18183,18183,21,20,0,0,0,0,0,0,0x47aa80,0x9a3080,0xecb680,0x12d3b00,0x17fc100,0x1c04580,0x212cb80,0x2535000,0x2a5d600,0x2e65a80,0x338e080,0x38b6680,0x3cbeb00,0x41e7100,0x45ef580,0x4b0de00,0x4f16280,0x5314980,0x583cf80,0x5ae5800,0x5eb6000,0x5eb6000,0x5eb6000,0x5eb6000,0x5eb6000,0x5eb6000,0x5eb6000,0x5eb6000,0x5eb6000,0x5eb6000,0x5eb6000,0x5eb6000,580,1572,2564,3372,4364,5172,6164,6972,7964,8772,9764,10756,11564,12556,13364,14341,15149,15942,16934,17461,18184,18184,18184,18184,21,20,0,0,0,0,0,0,0x47b500,0x9a3b00,0xecc100,0x12d4580,0x17fcb80,0x1c05000,0x212d600,0x2535a80,0x2a5e080,0x2e66500,0x338eb00,0x38b7100,0x3cbf580,0x41e7b80,0x45f0000,0x4b0e880,0x4f16d00,0x5315400,0x583da00,0x5ae6280,0x5eb6a80,0x5eb6a80,0x5eb6a80,0x5eb6a80,0x5eb6a80,0x5eb6a80,0x5eb6a80,0x5eb6a80,0x5eb6a80,0x5eb6a80,0x5eb6a80,0x5eb6a80,581,1573,2565,3373,4365,5173,6165,6973,7965,8773,9765,10757,11565,12557,13365,14342,15150,15943,16935,13896,13896,13896,13896,13896,20,19,0,0,0,0,0,0,0x47bf80,0x9a4580,0xeccb80,0x12d5000,0x17fd600,0x1c05a80,0x212e080,0x2536500,0x2a5eb00,0x2e66f80,0x338f580,0x38b7b80,0x3cc0000,0x41e8600,0x45f0a80,0x4b0f300,0x4f17780,0x5315e80,0x583e480,0x486be80,0x486be80,0x486be80,0x486be80,0x486be80,0x486be80,0x486be80,0x486be80,0x486be80,0x486be80,0x486be80,0x486be80,0x486be80,582,1574,2566,3374,4366,5174,6166,6974,7966,8774,9766,10758,11566,12558,13366,14343,15151,16128,16936,14152,18185,18185,18185,18185,21,20,0,0,0,0,0,0,0x47ca00,0x9a5000,0xecd600,0x12d5a80,0x17fe080,0x1c06500,0x212eb00,0x2536f80,0x2a5f580,0x2e67a00,0x3390000,0x38b8600,0x3cc0a80,0x41e9080,0x45f1500,0x4b0fd80,0x4f18200,0x5436a80,0x583ef00,0x49bb400,0x5eb7500,0x5eb7500,0x5eb7500,0x5eb7500,0x5eb7500,0x5eb7500,0x5eb7500,0x5eb7500,0x5eb7500,0x5eb7500,0x5eb7500,0x5eb7500,583,1575,2567,3375,4367,5175,6167,6975,7967,8775,9767,10759,11567,12559,13367,14344,15152,16129,16937,14408,14408,14408,14408,14408,20,19,0,0,0,0,0,0,0x47d480,0x9a5a80,0xece080,0x12d6500,0x17feb00,0x1c06f80,0x212f580,0x2537a00,0x2a60000,0x2e68480,0x3390a80,0x38b9080,0x3cc1500,0x41e9b00,0x45f1f80,0x4b10800,0x4f18c80,0x5437500,0x583f980,0x4b0a980,0x4b0a980,0x4b0a980,0x4b0a980,0x4b0a980,0x4b0a980,0x4b0a980,0x4b0a980,0x4b0a980,0x4b0a980,0x4b0a980,0x4b0a980,0x4b0a980,768,1576,2568,3376,4368,5176,6168,6976,7968,8960,9768,10760,11568,12560,13368,14345,15153,16130,16938,17462,18186,18186,18186,18186,21,20,0,0,0,0,0,0,0x59e080,0x9a6500,0xeceb00,0x12d6f80,0x17ff580,0x1c07a00,0x2130000,0x2538480,0x2a60a80,0x2f89080,0x3391500,0x38b9b00,0x3cc1f80,0x41ea580,0x45f2a00,0x4b11280,0x4f19700,0x5437f80,0x5840400,0x5ae6d00,0x5eb7f80,0x5eb7f80,0x5eb7f80,0x5eb7f80,0x5eb7f80,0x5eb7f80,0x5eb7f80,0x5eb7f80,0x5eb7f80,0x5eb7f80,0x5eb7f80,0x5eb7f80,769,1577,2569,3377,4369,5177,6169,6977,7969,8961,9769,10761,11569,12561,13369,14346,15154,16131,16939,17463,18187,18187,18187,18187,21,20,0,0,0,0,0,0,0x59eb00,0x9a6f80,0xecf580,0x12d7a00,0x1800000,0x1c08480,0x2130a80,0x2538f00,0x2a61500,0x2f89b00,0x3391f80,0x38ba580,0x3cc2a00,0x41eb000,0x45f3480,0x4b11d00,0x4f1a180,0x5438a00,0x5840e80,0x5ae7780,0x5eb8a00,0x5eb8a00,0x5eb8a00,0x5eb8a00,0x5eb8a00,0x5eb8a00,0x5eb8a00,0x5eb8a00,0x5eb8a00,0x5eb8a00,0x5eb8a00,0x5eb8a00,770,1578,2570,3378,4370,5178,6170,6978,7970,8962,9770,10762,11570,12562,13370,14347,15155,16132,16940,17464,18188,18243,18243,18243,22,21,0,0,0,0,0,0,0x59f580,0x9a7a00,0xed0000,0x12d8480,0x1800a80,0x1c08f00,0x2131500,0x2539980,0x2a61f80,0x2f8a580,0x3392a00,0x38bb000,0x3cc3480,0x41eba80,0x45f3f00,0x4b12780,0x4f1ac00,0x5439480,0x5841900,0x5ae8200,0x5eb9480,0x5edd600,0x5edd600,0x5edd600,0x5edd600,0x5edd600,0x5edd600,0x5edd600,0x5edd600,0x5edd600,0x5edd600,0x5edd600,771,1579,2571,3379,4371,5179,6171,6979,7971,8963,9771,10763,11571,12563,13371,14348,15156,16133,16941,17465,18189,18189,18189,18189,21,20,0,0,0,0,0,0,0x5a0000,0x9a8480,0xed0a80,0x12d8f00,0x1801500,0x1c09980,0x2131f80,0x253a400,0x2a62a00,0x2f8b000,0x3393480,0x38bba80,0x3cc3f00,0x41ec500,0x45f4980,0x4b13200,0x4f1b680,0x5439f00,0x5842380,0x5ae8c80,0x5eb9f00,0x5eb9f00,0x5eb9f00,0x5eb9f00,0x5eb9f00,0x5eb9f00,0x5eb9f00,0x5eb9f00,0x5eb9f00,0x5eb9f00,0x5eb9f00,0x5eb9f00,772,1580,2572,3380,4372,5180,6172,6980,7972,8964,9772,10764,11572,12564,13372,14349,15157,16134,16942,17466,18190,18190,18190,18190,21,20,0,0,0,0,0,0,0x5a0a80,0x9a8f00,0xed1500,0x12d9980,0x1801f80,0x1c0a400,0x2132a00,0x253ae80,0x2a63480,0x2f8ba80,0x3393f00,0x38bc500,0x3cc4980,0x41ecf80,0x45f5400,0x4b13c80,0x4f1c100,0x543a980,0x5842e00,0x5ae9700,0x5eba980,0x5eba980,0x5eba980,0x5eba980,0x5eba980,0x5eba980,0x5eba980,0x5eba980,0x5eba980,0x5eba980,0x5eba980,0x5eba980,773,1581,2573,3381,4373,5181,6173,6981,7973,8965,9773,10765,11573,12565,13373,14350,15158,16135,16943,14664,14664,14664,14664,14664,20,19,0,0,0,0,0,0,0x5a1500,0x9a9980,0xed1f80,0x12da400,0x1802a00,0x1c0ae80,0x2133480,0x253b900,0x2a63f00,0x2f8c500,0x3394980,0x38bcf80,0x3cc5400,0x41eda00,0x45f5e80,0x4b14700,0x4f1cb80,0x543b400,0x5843880,0x4c59f00,0x4c59f00,0x4c59f00,0x4c59f00,0x4c59f00,0x4c59f00,0x4c59f00,0x4c59f00,0x4c59f00,0x4c59f00,0x4c59f00,0x4c59f00,0x4c59f00,774,1582,2574,3382,4374,5182,6174,6982,7974,8966,9774,10766,11574,12566,13374,14351,15159,16136,16944,14920,18191,18191,18191,18191,21,20,0,0,0,0,0,0,0x5a1f80,0x9aa400,0xed2a00,0x12dae80,0x1803480,0x1c0b900,0x2133f00,0x253c380,0x2a64980,0x2f8cf80,0x3395400,0x38bda00,0x3cc5e80,0x41ee480,0x45f6900,0x4b15180,0x4f1d600,0x543be80,0x5844300,0x4da9480,0x5ebb400,0x5ebb400,0x5ebb400,0x5ebb400,0x5ebb400,0x5ebb400,0x5ebb400,0x5ebb400,0x5ebb400,0x5ebb400,0x5ebb400,0x5ebb400,775,1583,2575,3383,4375,5183,6175,6983,7975,8967,9775,10767,11575,12567,13375,14352,15160,16137,16945,15176,15176,15176,15176,15176,20,19,0,0,0,0,0,0,0x5a2a00,0x9aae80,0xed3480,0x12db900,0x1803f00,0x1c0c380,0x2134980,0x253ce00,0x2a65400,0x2f8da00,0x3395e80,0x38be480,0x3cc6900,0x41eef00,0x45f7380,0x4b15c00,0x4f1e080,0x543c900,0x5844d80,0x4ef8a00,0x4ef8a00,0x4ef8a00,0x4ef8a00,0x4ef8a00,0x4ef8a00,0x4ef8a00,0x4ef8a00,0x4ef8a00,0x4ef8a00,0x4ef8a00,0x4ef8a00,0x4ef8a00,776,1584,2576,3384,4376,5184,6176,7168,7976,8968,9776,10768,11576,12568,13376,14353,15161,16138,16946,17467,18192,18192,18192,18192,21,20,0,0,0,0,0,0,0x5a3480,0x9ab900,0xed3f00,0x12dc380,0x1804980,0x1c0ce00,0x2135400,0x265da00,0x2a65e80,0x2f8e480,0x3396900,0x38bef00,0x3cc7380,0x41ef980,0x45f7e00,0x4b16680,0x4f1eb00,0x543d380,0x5845800,0x5aea180,0x5ebbe80,0x5ebbe80,0x5ebbe80,0x5ebbe80,0x5ebbe80,0x5ebbe80,0x5ebbe80,0x5ebbe80,0x5ebbe80,0x5ebbe80,0x5ebbe80,0x5ebbe80,777,1585,2577,3385,4377,5185,6177,7169,7977,8969,9777,10769,11577,12569,13377,14354,15162,16139,16947,17468,18193,18193,18193,18193,21,20,0,0,0,0,0,0,0x5a3f00,0x9ac380,0xed4980,0x12dce00,0x1805400,0x1c0d880,0x2135e80,0x265e480,0x2a66900,0x2f8ef00,0x3397380,0x38bf980,0x3cc7e00,0x41f0400,0x45f8880,0x4b17100,0x4f1f580,0x543de00,0x5846280,0x5aeac00,0x5ebc900,0x5ebc900,0x5ebc900,0x5ebc900,0x5ebc900,0x5ebc900,0x5ebc900,0x5ebc900,0x5ebc900,0x5ebc900,0x5ebc900,0x5ebc900,778,1586,2578,3386,4378,5186,6178,7170,7978,8970,9778,10770,11578,12570,13378,14355,15163,16140,16948,17469,18194,18244,18244,18244,22,21,0,0,0,0,0,0,0x5a4980,0x9ace00,0xed5400,0x12dd880,0x1805e80,0x1c0e300,0x2136900,0x265ef00,0x2a67380,0x2f8f980,0x3397e00,0x38c0400,0x3cc8880,0x41f0e80,0x45f9300,0x4b17b80,0x4f20000,0x543e880,0x5846d00,0x5aeb680,0x5ebd380,0x5ede080,0x5ede080,0x5ede080,0x5ede080,0x5ede080,0x5ede080,0x5ede080,0x5ede080,0x5ede080,0x5ede080,0x5ede080,779,1587,2579,3387,4379,5187,6179,7171,7979,8971,9779,10771,11579,12571,13379,14356,15164,16141,16949,17470,18195,18195,18195,18195,21,20,0,0,0,0,0,0,0x5a5400,0x9ad880,0xed5e80,0x12de300,0x1806900,0x1c0ed80,0x2137380,0x265f980,0x2a67e00,0x2f90400,0x3398880,0x38c0e80,0x3cc9300,0x41f1900,0x45f9d80,0x4b18600,0x4f20a80,0x543f300,0x5847780,0x5aec100,0x5ebde00,0x5ebde00,0x5ebde00,0x5ebde00,0x5ebde00,0x5ebde00,0x5ebde00,0x5ebde00,0x5ebde00,0x5ebde00,0x5ebde00,0x5ebde00,780,1588,2580,3388,4380,5188,6180,7172,7980,8972,9780,10772,11580,12572,13380,14357,15165,16142,16950,17471,18196,18196,18196,18196,21,20,0,0,0,0,0,0,0x5a5e80,0x9ae300,0xed6900,0x12ded80,0x1807380,0x1c0f800,0x2137e00,0x2660400,0x2a68880,0x2f90e80,0x3399300,0x38c1900,0x3cc9d80,0x41f2380,0x45fa800,0x4b19080,0x4f21500,0x543fd80,0x5848200,0x5aecb80,0x5ebe880,0x5ebe880,0x5ebe880,0x5ebe880,0x5ebe880,0x5ebe880,0x5ebe880,0x5ebe880,0x5ebe880,0x5ebe880,0x5ebe880,0x5ebe880,781,1589,2581,3389,4381,5189,6181,7173,7981,8973,9781,10773,11581,12573,13381,14358,15166,16143,16951,15432,15432,15432,15432,15432,20,19,0,0,0,0,0,0,0x5a6900,0x9aed80,0xed7380,0x12df800,0x1807e00,0x1c10280,0x2138880,0x2660e80,0x2a69300,0x2f91900,0x3399d80,0x38c2380,0x3cca800,0x41f2e00,0x45fb280,0x4b19b00,0x4f21f80,0x5440800,0x5848c80,0x5047f80,0x5047f80,0x5047f80,0x5047f80,0x5047f80,0x5047f80,0x5047f80,0x5047f80,0x5047f80,0x5047f80,0x5047f80,0x5047f80,0x5047f80,782,1590,2582,3390,4382,5190,6182,7174,7982,8974,9782,10774,11582,12574,13382,14359,15167,16144,16952,15688,18197,18197,18197,18197,21,20,0,0,0,0,0,0,0x5a7380,0x9af800,0xed7e00,0x12e0280,0x1808880,0x1c10d00,0x2139300,0x2661900,0x2a69d80,0x2f92380,0x339a800,0x38c2e00,0x3ccb280,0x41f3880,0x45fbd00,0x4b1a580,0x4f22a00,0x5441280,0x5849700,0x5197500,0x5ebf300,0x5ebf300,0x5ebf300,0x5ebf300,0x5ebf300,0x5ebf300,0x5ebf300,0x5ebf300,0x5ebf300,0x5ebf300,0x5ebf300,0x5ebf300,783,1591,2583,3391,4383,5191,6183,7175,7983,8975,9783,10775,11583,12575,13383,14360,15168,16145,16953,15944,15944,15944,15944,15944,20,19,0,0,0,0,0,0,0x5a7e00,0x9b0280,0xed8880,0x12e0d00,0x1809300,0x1c11780,0x2139d80,0x2662380,0x2a6a800,0x2f92e00,0x339b280,0x38c3880,0x3ccbd00,0x41f4300,0x45fc780,0x4b1b000,0x4f23480,0x5441d00,0x584a180,0x52e6a80,0x52e6a80,0x52e6a80,0x52e6a80,0x52e6a80,0x52e6a80,0x52e6a80,0x52e6a80,0x52e6a80,0x52e6a80,0x52e6a80,0x52e6a80,0x52e6a80,784,1592,2584,3392,4384,5376,6184,7176,7984,8976,9784,10776,11584,12576,13568,14361,15169,16146,16954,17472,18198,18198,18198,18198,21,20,0,0,0,0,0,0,0x5a8880,0x9b0d00,0xed9300,0x12e1780,0x1809d80,0x1d32380,0x213a800,0x2662e00,0x2a6b280,0x2f93880,0x339bd00,0x38c4300,0x3ccc780,0x41f4d80,0x471d380,0x4b1ba80,0x4f23f00,0x5442780,0x584ac00,0x5aed600,0x5ebfd80,0x5ebfd80,0x5ebfd80,0x5ebfd80,0x5ebfd80,0x5ebfd80,0x5ebfd80,0x5ebfd80,0x5ebfd80,0x5ebfd80,0x5ebfd80,0x5ebfd80,785,1593,2585,3393,4385,5377,6185,7177,7985,8977,9785,10777,11585,12577,13569,14362,15170,16147,16955,17473,18199,18199,18199,18199,21,20,0,0,0,0,0,0,0x5a9300,0x9b1780,0xed9d80,0x12e2200,0x180a800,0x1d32e00,0x213b280,0x2663880,0x2a6bd00,0x2f94300,0x339c780,0x38c4d80,0x3ccd200,0x41f5800,0x471de00,0x4b1c500,0x4f24980,0x5443200,0x584b680,0x5aee080,0x5ec0800,0x5ec0800,0x5ec0800,0x5ec0800,0x5ec0800,0x5ec0800,0x5ec0800,0x5ec0800,0x5ec0800,0x5ec0800,0x5ec0800,0x5ec0800,786,1594,2586,3394,4386,5378,6186,7178,7986,8978,9786,10778,11586,12578,13570,14363,15171,16148,16956,17474,18200,18245,18245,18245,22,21,0,0,0,0,0,0,0x5a9d80,0x9b2200,0xeda800,0x12e2c80,0x180b280,0x1d33880,0x213bd00,0x2664300,0x2a6c780,0x2f94d80,0x339d200,0x38c5800,0x3ccdc80,0x41f6280,0x471e880,0x4b1cf80,0x4f25400,0x5443c80,0x584c100,0x5aeeb00,0x5ec1280,0x5edeb00,0x5edeb00,0x5edeb00,0x5edeb00,0x5edeb00,0x5edeb00,0x5edeb00,0x5edeb00,0x5edeb00,0x5edeb00,0x5edeb00,787,1595,2587,3395,4387,5379,6187,7179,7987,8979,9787,10779,11587,12579,13571,14364,15172,16149,16957,17475,18201,18201,18201,18201,21,20,0,0,0,0,0,0,0x5aa800,0x9b2c80,0xedb280,0x12e3700,0x180bd00,0x1d34300,0x213c780,0x2664d80,0x2a6d200,0x2f95800,0x339dc80,0x38c6280,0x3cce700,0x41f6d00,0x471f300,0x4b1da00,0x4f25e80,0x5444700,0x584cb80,0x5aef580,0x5ec1d00,0x5ec1d00,0x5ec1d00,0x5ec1d00,0x5ec1d00,0x5ec1d00,0x5ec1d00,0x5ec1d00,0x5ec1d00,0x5ec1d00,0x5ec1d00,0x5ec1d00,788,1596,2588,3396,4388,5380,6188,7180,7988,8980,9788,10780,11588,12580,13572,14365,15173,16150,16958,17476,18202,18202,18202,18202,21,20,0,0,0,0,0,0,0x5ab280,0x9b3700,0xedbd00,0x12e4180,0x180c780,0x1d34d80,0x213d200,0x2665800,0x2a6dc80,0x2f96280,0x339e700,0x38c6d00,0x3ccf180,0x41f7780,0x471fd80,0x4b1e480,0x4f26900,0x5445180,0x584d600,0x5af0000,0x5ec2780,0x5ec2780,0x5ec2780,0x5ec2780,0x5ec2780,0x5ec2780,0x5ec2780,0x5ec2780,0x5ec2780,0x5ec2780,0x5ec2780,0x5ec2780,789,1597,2589,3397,4389,5381,6189,7181,7989,8981,9789,10781,11589,12581,13573,14366,15174,16151,16959,16200,16200,16200,16200,16200,20,19,0,0,0,0,0,0,0x5abd00,0x9b4180,0xedc780,0x12e4c00,0x180d200,0x1d35800,0x213dc80,0x2666280,0x2a6e700,0x2f96d00,0x339f180,0x38c7780,0x3ccfc00,0x41f8200,0x4720800,0x4b1ef00,0x4f27380,0x5445c00,0x584e080,0x5436000,0x5436000,0x5436000,0x5436000,0x5436000,0x5436000,0x5436000,0x5436000,0x5436000,0x5436000,0x5436000,0x5436000,0x5436000,790,1598,2590,3398,4390,5382,6190,7182,7990,8982,9790,10782,11590,12582,13574,14367,15175,16152,16960,16456,18203,18203,18203,18203,21,20,0,0,0,0,0,0,0x5ac780,0x9b4c00,0xedd200,0x12e5680,0x180dc80,0x1d36280,0x213e700,0x2666d00,0x2a6f180,0x2f97780,0x339fc00,0x38c8200,0x3cd0680,0x41f8c80,0x4721280,0x4b1f980,0x4f27e00,0x5446680,0x584eb00,0x5585580,0x5ec3200,0x5ec3200,0x5ec3200,0x5ec3200,0x5ec3200,0x5ec3200,0x5ec3200,0x5ec3200,0x5ec3200,0x5ec3200,0x5ec3200,0x5ec3200,791,1599,2591,3399,4391,5383,6191,7183,7991,8983,9791,10783,11591,12583,13575,14368,15360,16153,16961,16712,16712,16712,16712,16712,20,19,0,0,0,0,0,0,0x5ad200,0x9b5680,0xeddc80,0x12e6100,0x180e700,0x1d36d00,0x213f180,0x2667780,0x2a6fc00,0x2f98200,0x33a0680,0x38c8c80,0x3cd1100,0x41f9700,0x4721d00,0x4b20400,0x5048a00,0x5447100,0x584f580,0x56d4b00,0x56d4b00,0x56d4b00,0x56d4b00,0x56d4b00,0x56d4b00,0x56d4b00,0x56d4b00,0x56d4b00,0x56d4b00,0x56d4b00,0x56d4b00,0x56d4b00,792,1600,2592,3584,4392,5384,6192,7184,7992,8984,9792,10784,11776,12584,13576,14369,15361,16154,16962,17477,18204,18204,18204,18204,21,20,0,0,0,0,0,0,0x5adc80,0x9b6100,0xede700,0x1406d00,0x180f180,0x1d37780,0x213fc00,0x2668200,0x2a70680,0x2f98c80,0x33a1100,0x38c9700,0x3df1d00,0x41fa180,0x4722780,0x4b20e80,0x5049480,0x5447b80,0x5850000,0x5af0a80,0x5ec3c80,0x5ec3c80,0x5ec3c80,0x5ec3c80,0x5ec3c80,0x5ec3c80,0x5ec3c80,0x5ec3c80,0x5ec3c80,0x5ec3c80,0x5ec3c80,0x5ec3c80,793,1601,2593,3585,4393,5385,6193,7185,7993,8985,9793,10785,11777,12585,13577,14370,15362,16155,16963,17478,18205,18205,18205,18205,21,20,0,0,0,0,0,0,0x5ae700,0x9b6b80,0xedf180,0x1407780,0x180fc00,0x1d38200,0x2140680,0x2668c80,0x2a71100,0x2f99700,0x33a1b80,0x38ca180,0x3df2780,0x41fac00,0x4723200,0x4b21900,0x5049f00,0x5448600,0x5850a80,0x5af1500,0x5ec4700,0x5ec4700,0x5ec4700,0x5ec4700,0x5ec4700,0x5ec4700,0x5ec4700,0x5ec4700,0x5ec4700,0x5ec4700,0x5ec4700,0x5ec4700,794,1602,2594,3586,4394,5386,6194,7186,7994,8986,9794,10786,11778,12586,13578,14371,15363,16156,16964,17479,18206,18246,18246,18246,22,21,0,0,0,0,0,0,0x5af180,0x9b7600,0xedfc00,0x1408200,0x1810680,0x1d38c80,0x2141100,0x2669700,0x2a71b80,0x2f9a180,0x33a2600,0x38cac00,0x3df3200,0x41fb680,0x4723c80,0x4b22380,0x504a980,0x5449080,0x5851500,0x5af1f80,0x5ec5180,0x5edf580,0x5edf580,0x5edf580,0x5edf580,0x5edf580,0x5edf580,0x5edf580,0x5edf580,0x5edf580,0x5edf580,0x5edf580,795,1603,2595,3587,4395,5387,6195,7187,7995,8987,9795,10787,11779,12587,13579,14372,15364,16157,16965,17664,18207,18207,18207,18207,21,20,0,0,0,0,0,0,0x5afc00,0x9b8080,0xee0680,0x1408c80,0x1811100,0x1d39700,0x2141b80,0x266a180,0x2a72600,0x2f9ac00,0x33a3080,0x38cb680,0x3df3c80,0x41fc100,0x4724700,0x4b22e00,0x504b400,0x5449b00,0x5851f80,0x5c12b80,0x5ec5c00,0x5ec5c00,0x5ec5c00,0x5ec5c00,0x5ec5c00,0x5ec5c00,0x5ec5c00,0x5ec5c00,0x5ec5c00,0x5ec5c00,0x5ec5c00,0x5ec5c00,796,1604,2596,3588,4396,5388,6196,7188,7996,8988,9796,10788,11780,12588,13580,14373,15365,16158,16966,17665,18208,18208,18208,18208,21,20,0,0,0,0,0,0,0x5b0680,0x9b8b00,0xee1100,0x1409700,0x1811b80,0x1d3a180,0x2142600,0x266ac00,0x2a73080,0x2f9b680,0x33a3b00,0x38cc100,0x3df4700,0x41fcb80,0x4725180,0x4b23880,0x504be80,0x544a580,0x5852a00,0x5c13600,0x5ec6680,0x5ec6680,0x5ec6680,0x5ec6680,0x5ec6680,0x5ec6680,0x5ec6680,0x5ec6680,0x5ec6680,0x5ec6680,0x5ec6680,0x5ec6680,797,1605,2597,3589,4397,5389,6197,7189,7997,8989,9797,10789,11781,12589,13581,14374,15366,16159,16967,16968,16968,16968,16968,16968,20,19,0,0,0,0,0,0,0x5b1100,0x9b9580,0xee1b80,0x140a180,0x1812600,0x1d3ac00,0x2143080,0x266b680,0x2a73b00,0x2f9c100,0x33a4580,0x38ccb80,0x3df5180,0x41fd600,0x4725c00,0x4b24300,0x504c900,0x544b000,0x5853480,0x5824080,0x5824080,0x5824080,0x5824080,0x5824080,0x5824080,0x5824080,0x5824080,0x5824080,0x5824080,0x5824080,0x5824080,0x5824080,798,1606,2598,3590,4398,5390,6198,7190,7998,8990,9798,10790,11782,12590,13582,14375,15367,16160,17152,17224,18209,18209,18209,18209,21,20,0,0,0,0,0,0,0x5b1b80,0x9ba000,0xee2600,0x140ac00,0x1813080,0x1d3b680,0x2143b00,0x266c100,0x2a74580,0x2f9cb80,0x33a5000,0x38cd600,0x3df5c00,0x41fe080,0x4726680,0x4b24d80,0x504d380,0x544ba80,0x5974080,0x5973600,0x5ec7100,0x5ec7100,0x5ec7100,0x5ec7100,0x5ec7100,0x5ec7100,0x5ec7100,0x5ec7100,0x5ec7100,0x5ec7100,0x5ec7100,0x5ec7100,799,1607,2599,3591,4399,5391,6199,7191,7999,8991,9799,10791,11783,12591,13583,14376,15368,16161,17153,17480,17480,17480,17480,17480,20,19,0,0,0,0,0,0,0x5b2600,0x9baa80,0xee3080,0x140b680,0x1813b00,0x1d3c100,0x2144580,0x266cb80,0x2a75000,0x2f9d600,0x33a5a80,0x38ce080,0x3df6680,0x41feb00,0x4727100,0x4b25800,0x504de00,0x544c500,0x5974b00,0x5ac2b80,0x5ac2b80,0x5ac2b80,0x5ac2b80,0x5ac2b80,0x5ac2b80,0x5ac2b80,0x5ac2b80,0x5ac2b80,0x5ac2b80,0x5ac2b80,0x5ac2b80,0x5ac2b80,800,1792,2600,3592,4400,5392,6200,7192,8000,8992,9984,10792,11784,12592,13584,14377,15369,16162,17154,17666,18210,18210,18210,18210,21,20,0,0,0,0,0,0,0x5b3080,0xadb680,0xee3b00,0x140c100,0x1814580,0x1d3cb80,0x2145000,0x266d600,0x2a75a80,0x2f9e080,0x34c6680,0x38ceb00,0x3df7100,0x41ff580,0x4727b80,0x4b26280,0x504e880,0x544cf80,0x5975580,0x5c14080,0x5ec7b80,0x5ec7b80,0x5ec7b80,0x5ec7b80,0x5ec7b80,0x5ec7b80,0x5ec7b80,0x5ec7b80,0x5ec7b80,0x5ec7b80,0x5ec7b80,0x5ec7b80,801,1793,2601,3593,4401,5393,6201,7193,8001,8993,9985,10793,11785,12593,13585,14378,15370,16163,17155,17667,18211,18211,18211,18211,21,20,0,0,0,0,0,0,0x5b3b00,0xadc100,0xee4580,0x140cb80,0x1815000,0x1d3d600,0x2145a80,0x266e080,0x2a76500,0x2f9eb00,0x34c7100,0x38cf580,0x3df7b80,0x4200000,0x4728600,0x4b26d00,0x504f300,0x544da00,0x5976000,0x5c14b00,0x5ec8600,0x5ec8600,0x5ec8600,0x5ec8600,0x5ec8600,0x5ec8600,0x5ec8600,0x5ec8600,0x5ec8600,0x5ec8600,0x5ec8600,0x5ec8600,802,1794,2602,3594,4402,5394,6202,7194,8002,8994,9986,10794,11786,12594,13586,14379,15371,16164,17156,17668,18212,18247,18247,18247,22,21,0,0,0,0,0,0,0x5b4580,0xadcb80,0xee5000,0x140d600,0x1815a80,0x1d3e080,0x2146500,0x266eb00,0x2a76f80,0x2f9f580,0x34c7b80,0x38d0000,0x3df8600,0x4200a80,0x4729080,0x4b27780,0x504fd80,0x544e480,0x5976a80,0x5c15580,0x5ec9080,0x5ee0000,0x5ee0000,0x5ee0000,0x5ee0000,0x5ee0000,0x5ee0000,0x5ee0000,0x5ee0000,0x5ee0000,0x5ee0000,0x5ee0000,803,1795,2603,3595,4403,5395,6203,7195,8003,8995,9987,10795,11787,12595,13587,14380,15372,16165,17157,17669,18213,18213,18213,18213,21,20,0,0,0,0,0,0,0x5b5000,0xadd600,0xee5a80,0x140e080,0x1816500,0x1d3eb00,0x2146f80,0x266f580,0x2a77a00,0x2fa0000,0x34c8600,0x38d0a80,0x3df9080,0x4201500,0x4729b00,0x4b28200,0x5050800,0x544ef00,0x5977500,0x5c16000,0x5ec9b00,0x5ec9b00,0x5ec9b00,0x5ec9b00,0x5ec9b00,0x5ec9b00,0x5ec9b00,0x5ec9b00,0x5ec9b00,0x5ec9b00,0x5ec9b00,0x5ec9b00,804,1796,2604,3596,4404,5396,6204,7196,8004,8996,9988,10796,11788,12596,13588,14381,15373,16166,17158,17670,18214,18214,18214,18214,21,20,0,0,0,0,0,0,0x5b5a80,0xade080,0xee6500,0x140eb00,0x1816f80,0x1d3f580,0x2147a00,0x2670000,0x2a78480,0x2fa0a80,0x34c9080,0x38d1500,0x3df9b00,0x4201f80,0x472a580,0x4b28c80,0x5051280,0x544f980,0x5977f80,0x5c16a80,0x5eca580,0x5eca580,0x5eca580,0x5eca580,0x5eca580,0x5eca580,0x5eca580,0x5eca580,0x5eca580,0x5eca580,0x5eca580,0x5eca580,805,1797,2605,3597,4405,5397,6205,7197,8005,8997,9989,10797,11789,12597,13589,14382,15374,16167,17159,17736,17736,17736,17736,17736,20,19,0,0,0,0,0,0,0x5b6500,0xadeb00,0xee6f80,0x140f580,0x1817a00,0x1d40000,0x2148480,0x2670a80,0x2a78f00,0x2fa1500,0x34c9b00,0x38d1f80,0x3dfa580,0x4202a00,0x472b000,0x4b29700,0x5051d00,0x5450400,0x5978a00,0x5c12100,0x5c12100,0x5c12100,0x5c12100,0x5c12100,0x5c12100,0x5c12100,0x5c12100,0x5c12100,0x5c12100,0x5c12100,0x5c12100,0x5c12100,806,1798,2606,3598,4406,5398,6206,7198,8006,8998,9990,10798,11790,12598,13590,14383,15375,16168,17160,17992,18215,18215,18215,18215,21,20,0,0,0,0,0,0,0x5b6f80,0xadf580,0xee7a00,0x1410000,0x1818480,0x1d40a80,0x2148f00,0x2671500,0x2a79980,0x2fa1f80,0x34ca580,0x38d2a00,0x3dfb000,0x4203480,0x472ba80,0x4b2a180,0x5052780,0x5450e80,0x5979480,0x5d61680,0x5ecb000,0x5ecb000,0x5ecb000,0x5ecb000,0x5ecb000,0x5ecb000,0x5ecb000,0x5ecb000,0x5ecb000,0x5ecb000,0x5ecb000,0x5ecb000,807,1799,2607,3599,4407,5399,6207,7199,8007,8999,9991,10799,11791,12599,13591,14384,15376,16169,17161,18248,18248,18248,18248,18248,20,19,0,0,0,0,0,0,0x5b7a00,0xae0000,0xee8480,0x1410a80,0x1818f00,0x1d41500,0x2149980,0x2671f80,0x2a7a400,0x2fa2a00,0x34cb000,0x38d3480,0x3dfba80,0x4203f00,0x472c500,0x4b2ac00,0x5053200,0x5451900,0x5979f00,0x5eb0c00,0x5eb0c00,0x5eb0c00,0x5eb0c00,0x5eb0c00,0x5eb0c00,0x5eb0c00,0x5eb0c00,0x5eb0c00,0x5eb0c00,0x5eb0c00,0x5eb0c00,0x5eb0c00};

__global__ __launch_bounds__(256, 1) void swin_main(const uint16_t* x, const uint16_t* mask, char* ws, uint16_t* out,
                                                    const uint16_t* wqkv, const uint16_t* wo, const uint16_t* ls,
                                                    const uint16_t* w1, const uint16_t* w2, const uint16_t* rc,
                                                    const int* ridx) {
  __shared__ __attribute__((aligned(16))) char smem[LDS_BYTES];
  const int wave = __builtin_amdgcn_readfirstlane(threadIdx.x >> 6);
  const unsigned lds0 = (unsigned)(size_t)smem;
  asm volatile(
      ".p2align 5\n"
      "s_load_dwordx8 s[16:23], %0, 0\n"
      "s_load_dwordx4 s[4:7], %0, 32\n"
      "s_load_dwordx8 s[80:87], %0, 48\n"
      "s_load_dwordx2 s[90:91], %0, 80\n"
      "s_mov_b32 s24, %1\n"
      "s_mov_b32 s25, %2\n"
      "s_mov_b64 s[60:61], %3\n"
      "s_getpc_b64 s[12:13]\n"
      "L_ipf0_%=:\n"
      "v_mbcnt_lo_u32_b32 v203, -1, 0\n"
      "v_mbcnt_hi_u32_b32 v203, -1, v203\n"
      "s_lshr_b32 s28, s25, 3\n"
      "s_lshl_b32 s29, s28, 2\n"
      "s_add_u32 s29, s29, s24\n"
      "s_mul_i32 s29, s29, 14\n"
      "v_min_u32_e64 v207, 13, v203\n"
      "v_add_lshl_u32 v207, v207, s29, 7\n"
      "v_min_u32 v207, 221056, v207\n"
      "v_lshlrev_b32 v243, 4, v203\n"
      "s_lshl_b32 s29, s24, 6\n"
      "s_add_u32 s54, s60, s29\n"
      "s_addc_u32 s55, s61, 0\n"
      "s_and_b32 s29, s29, 64\n"
      "v_xor_b32 v242, s29, v243\n"
      "s_mul_i32 s30, s24, 9216\n"
      "v_add_u32 v242, s30, v242\n"
      "s_add_u32 s2, s30, 0\n"
      "v_xor_b32 v240, 32, v242\n"
      "s_mov_b64 s[34:35], 0xffff\n"
      "v_cndmask_b32 v205, v240, v242, s[34:35]\n"
      "s_lshl_b64 s[34:35], s[34:35], 48\n"
      "v_cndmask_b32 v201, v242, v240, s[34:35]\n"
      "s_lshl_b64 s[34:35], -1, 32\n"
      "v_cndmask_b32 v204, v240, v242, s[34:35]\n"
      "s_waitcnt lgkmcnt(0)\n"
      "global_load_dword v31, v207, s[4:5]\n"
      "s_lshl_b32 s29, s25, 8\n"
      "s_lshl_b32 s30, s24, 5\n"
      "s_add_u32 s29, s29, s30\n"
      "s_add_u32 s29, s29, 41408\n"
      "s_load_dwordx8 s[92:99], s[60:61], s29\n"
      "s_add_u32 m0, s2, 0\n"
      "s_nop 0\n"
      "global_load_lds_dwordx4 v242, s[4:5]\n"
      "global_load_lds_dwordx4 v242, s[4:5] offset:1024\n"
      "global_load_lds_dwordx4 v205, s[4:5] offset:2048\n"
      "global_load_lds_dwordx4 v240, s[4:5] offset:3072\n"
      "s_add_u32 s4, s4, 4096\n"
      "s_addc_u32 s5, s5, 0\n"
      "s_add_u32 m0, s2, 4096\n"
      "s_nop 0\n"
      "global_load_lds_dwordx4 v204, s[4:5]\n"
      "global_load_lds_dwordx4 v242, s[4:5] offset:1024\n"
      "global_load_lds_dwordx4 v201, s[4:5] offset:2048\n"
      "global_load_lds_dwordx4 v240, s[4:5] offset:3072\n"
      "s_add_u32 s4, s4, 4096\n"
      "s_addc_u32 s5, s5, 0\n"
      "s_add_u32 m0, s2, 8192\n"
      "s_nop 0\n"
      "global_load_lds_dwordx4 v240, s[4:5]\n"
      "s_add_u32 s4, s4, 28672\n"
      "s_addc_u32 s5, s5, 0\n"
      "s_load_dwordx16 s[36:51], s[82:83], 0\n"
      "s_load_dwordx2 s[8:9], s[80:81], 0\n"
      "s_load_dwordx16 s[64:79], s[84:85], 0\n"
      "s_lshl_b32 s29, s24, 8\n"
      "v_lshl_add_u32 v200, v203, 2, s29\n"
      "v_min_u32 v200, 672, v200\n"
      "v_add_u32 v206, 150640, v200\n"
      "s_lshl_b32 s29, s28, 2\n"
      "s_add_u32 s29, s29, s24\n"
      "s_mov_b64 s[34:35], s[16:17]\n"
      "s_mov_b32 s30, s29\n"
      "s_sub_u32 s31, s29, 48\n"
      "s_cmp_ge_u32 s29, 48\n"
      "s_cselect_b64 s[34:35], s[22:23], s[34:35]\n"
      "s_cselect_b32 s30, s31, s30\n"
      "s_sub_u32 s31, s29, 96\n"
      "s_cmp_ge_u32 s29, 96\n"
      "s_cselect_b64 s[34:35], s[18:19], s[34:35]\n"
      "s_cselect_b32 s30, s31, s30\n"
      "s_sub_u32 s31, s29, 109\n"
      "s_cmp_ge_u32 s29, 109\n"
      "s_cselect_b64 s[34:35], s[16:17], s[34:35]\n"
      "s_cselect_b32 s30, s31, s30\n"
      "s_lshl_b32 s30, s30, 21\n"
      "s_load_dword s100, s[34:35], s30\n"
      "s_and_b32 s29, s29, 7\n"
      "s_cmp_eq_u32 s29, 0\n"
      "s_cselect_b64 s[34:35], s[6:7], s[34:35]\n"
      "s_cmp_eq_u32 s29, 1\n"
      "s_cselect_b64 s[34:35], s[90:91], s[34:35]\n"
      "s_cmp_eq_u32 s29, 2\n"
      "s_cselect_b64 s[34:35], s[86:87], s[34:35]\n"
      "s_cmp_eq_u32 s29, 3\n"
      "s_cselect_b64 s[34:35], s[60:61], s[34:35]\n"
      "s_cmp_eq_u32 s29, 4\n"
      "s_cselect_b64 s[34:35], s[12:13], s[34:35]\n"
      "s_load_dword s100, s[34:35], 0\n"
      "s_add_u32 m0, s2, 36864\n"
      "s_nop 0\n"
      "global_load_lds_dwordx4 v242, s[4:5]\n"
      "global_load_lds_dwordx4 v242, s[4:5] offset:1024\n"
      "global_load_lds_dwordx4 v205, s[4:5] offset:2048\n"
      "global_load_lds_dwordx4 v240, s[4:5] offset:3072\n"
      "s_add_u32 s4, s4, 4096\n"
      "s_addc_u32 s5, s5, 0\n"
      "s_add_u32 m0, s2, 40960\n"
      "s_nop 0\n"
      "global_load_lds_dwordx4 v204, s[4:5]\n"
      "global_load_lds_dwordx4 v242, s[4:5] offset:1024\n"
      "global_load_lds_dwordx4 v201, s[4:5] offset:2048\n"
      "global_load_lds_dwordx4 v240, s[4:5] offset:3072\n"
      "s_add_u32 s4, s4, 4096\n"
      "s_addc_u32 s5, s5, 0\n"
      "s_add_u32 m0, s2, 45056\n"
      "s_nop 0\n"
      "global_load_lds_dwordx4 v240, s[4:5]\n"
      "s_add_u32 s4, s4, 28672\n"
      "s_addc_u32 s5, s5, 0\n"
      "s_add_u32 m0, s2, 73728\n"
      "s_nop 0\n"
      "global_load_lds_dwordx4 v242, s[4:5]\n"
      "global_load_lds_dwordx4 v242, s[4:5] offset:1024\n"
      "global_load_lds_dwordx4 v205, s[4:5] offset:2048\n"
      "global_load_lds_dwordx4 v240, s[4:5] offset:3072\n"
      "s_add_u32 s4, s4, 4096\n"
      "s_addc_u32 s5, s5, 0\n"
      "s_add_u32 m0, s2, 77824\n"
      "s_nop 0\n"
      "global_load_lds_dwordx4 v204, s[4:5]\n"
      "global_load_lds_dwordx4 v242, s[4:5] offset:1024\n"
      "global_load_lds_dwordx4 v201, s[4:5] offset:2048\n"
      "global_load_lds_dwordx4 v240, s[4:5] offset:3072\n"
      "s_add_u32 s4, s4, 4096\n"
      "s_addc_u32 s5, s5, 0\n"
      "s_add_u32 m0, s2, 81920\n"
      "s_nop 0\n"
      "global_load_lds_dwordx4 v240, s[4:5]\n"
      "s_add_u32 s4, s4, 32768\n"
      "s_addc_u32 s5, s5, 0\n"
      "s_waitcnt lgkmcnt(0)\n"
      "s_load_dword s100, s[16:17], s92\n"
      "s_load_dword s100, s[16:17], s93\n"
      "s_load_dword s100, s[16:17], s94\n"
      "s_load_dword s100, s[16:17], s95\n"
      "s_load_dword s100, s[16:17], s96\n"
      "s_load_dword s100, s[16:17], s97\n"
      "s_load_dword s100, s[16:17], s98\n"
      "s_load_dword s100, s[16:17], s99\n"
      "v_add_u32 v241, 4096, v240\n"
      "v_add_u32 v202, s2, v243\n"
      "v_lshrrev_b32 v208, 2, v203\n"
      "global_load_dwordx4 a[108:111], v242, s[4:5] offset:-4096\n"
      "v_and_b32 v161, 3, v208\n"
      "v_lshrrev_b32 v160, 4, v203\n"
      "global_load_dwordx4 a[112:115], v242, s[4:5] offset:-3072\n"
      "v_lshl_add_u32 v161, v160, 3, v161\n"
      "v_mul_u32_u24 v161, 1152, v161\n"
      "global_load_dwordx4 a[116:119], v205, s[4:5] offset:-2048\n"
      "v_and_b32_e64 v160, 1, v203\n"
      "v_lshl_add_u32 v161, v160, 3, v161\n"
      "global_load_dwordx4 a[120:123], v240, s[4:5] offset:-1024\n"
      "v_and_b32 v208, 6, v208\n"
      "v_bfe_u32 v160, v203, 1, 1\n"
      "s_mul_i32 s29, s24, 6\n"
      "global_load_dwordx4 a[124:127], v204, s[4:5]\n"
      "v_add_u32 v160, s29, v160\n"
      "v_xor_b32 v211, v160, v208\n"
      "global_load_dwordx4 a[128:131], v242, s[4:5] offset:1024\n"
      "v_lshl_add_u32 v211, v211, 4, v161\n"
      "v_add_u32 v160, 2, v160\n"
      "global_load_dwordx4 a[132:135], v201, s[4:5] offset:2048\n"
      "v_xor_b32 v162, v160, v208\n"
      "v_lshl_add_u32 v162, v162, 4, v161\n"
      "v_add_u32_e64 v160, 2, v160\n"
      "global_load_dwordx4 a[136:139], v240, s[4:5] offset:3072\n"
      "v_xor_b32 v210, v160, v208\n"
      "v_lshl_add_u32 v210, v210, 4, v161\n"
      "global_load_dwordx4 a[140:143], v241, s[4:5]\n"
      "v_add_u32 v158, 73728, v211\n"
      "v_add_u32 v208, 73728, v162\n"
      "v_add_u32 v209, 73728, v210\n"
      "v_add_u32 v249, 73728, v202\n"
      "s_movk_i32 s29, 18\n"
      "s_mov_b32 s30, 73600\n"
      "v_add_u32 v242, 36864, v242\n"
      "v_add_u32 v240, 36864, v240\n"
      "v_add_u32 v205, 36864, v205\n"
      "v_add_u32 v204, 36864, v204\n"
      "v_add_u32 v201, 36864, v201\n"
      "v_add_u32 v241, 36864, v241\n"
      "global_load_dwordx4 a[144:147], v242, s[4:5] offset:-4096\n"
      "s_cmp_eq_u32 s24, 1\n"
      "s_cselect_b64 s[12:13], s[6:7], s[12:13]\n"
      "s_cmp_eq_u32 s24, 2\n"
      "s_cselect_b32 s29, 16, s29\n"
      "s_cselect_b32 s30, L_cend_%=-L_ipf0_%=-128, s30\n"
      "global_load_dwordx4 a[148:151], v242, s[4:5] offset:-3072\n"
      "s_cmp_eq_u32 s24, 3\n"
      "s_cselect_b64 s[12:13], s[60:61], s[12:13]\n"
      "s_cselect_b32 s29, 10, s29\n"
      "global_load_dwordx4 a[152:155], v205, s[4:5] offset:-2048\n"
      "s_cselect_b32 s30, 40832, s30\n"
      "s_cmp_eq_u32 s24, 0\n"
      "global_load_dwordx4 a[156:159], v240, s[4:5] offset:-1024\n"
      "s_cselect_b64 s[12:13], s[90:91], s[12:13]\n"
      "s_cselect_b32 s29, 3, s29\n"
      "global_load_dwordx4 a[160:163], v204, s[4:5]\n"
      "s_cselect_b32 s30, 9476, s30\n"
      "s_mul_i32 s28, s28, s29\n"
      "global_load_dwordx4 a[164:167], v242, s[4:5] offset:1024\n"
      "s_sub_u32 s29, s29, 1\n"
      "v_min_u32_e64 v207, s29, v203\n"
      "v_add_lshl_u32 v207, v207, s28, 7\n"
      "global_load_dwordx4 a[168:171], v201, s[4:5] offset:2048\n"
      "v_min_u32_e64 v207, s30, v207\n"
      "s_mul_i32 s28, s24, 10240\n"
      "global_load_dwordx4 a[172:175], v240, s[4:5] offset:3072\n"
      "s_add_u32 s28, s28, 4096\n"
      "v_add_u32_e64 v159, s28, v243\n"
      "global_load_dwordx4 a[176:179], v241, s[4:5]\n"
      "v_add_u32 v251, 8192, v159\n"
      "global_load_dword v200, v200, s[86:87]\n"
      "s_mov_b64 s[56:57], s[90:91]\n"
      "s_load_dwordx16 s[84:99], s[84:85], 64\n"
      "s_lshl_b32 s29, s25, 8\n"
      "s_add_u32 s52, s60, s29\n"
      "s_addc_u32 s53, s61, 0\n"
      "s_lshl_b32 s29, s24, 5\n"
      "v_lshrrev_b32 v156, 3, v203\n"
      "v_lshl_add_u32 v248, v156, 2, s29\n"
      "v_add_u32 v248, 41408, v248\n"
      "global_load_dword v248, v248, s[52:53]\n"
      "global_load_dword v30, v207, s[12:13]\n"
      "v_xor_b32_e64 v156, v156, v203\n"
      "v_and_b32_e64 v156, 7, v156\n"
      "s_waitcnt vmcnt(21)\n"
      "s_barrier\n"
      "ds_read_b64_tr_b16 a[0:1], v211\n"
      "ds_read_b64_tr_b16 a[2:3], v211 offset:4608\n"
      "ds_read_b64_tr_b16 a[4:5], v162\n"
      "ds_read_b64_tr_b16 a[6:7], v162 offset:4608\n"
      "ds_read_b64_tr_b16 a[8:9], v210\n"
      "ds_read_b64_tr_b16 a[10:11], v210 offset:4608\n"
      "ds_read_b64_tr_b16 a[12:13], v211 offset:384\n"
      "ds_read_b64_tr_b16 a[14:15], v211 offset:4992\n"
      "ds_read_b64_tr_b16 a[16:17], v162 offset:384\n"
      "ds_read_b64_tr_b16 a[18:19], v162 offset:4992\n"
      "ds_read_b64_tr_b16 a[20:21], v210 offset:384\n"
      "ds_read_b64_tr_b16 a[22:23], v210 offset:4992\n"
      "ds_read_b64_tr_b16 a[24:25], v211 offset:768\n"
      "ds_read_b64_tr_b16 a[26:27], v211 offset:5376\n"
      "v_add_u32 v242, 36864, v242\n"
      "v_add_u32 v240, 36864, v240\n"
      "v_add_u32 v205, 36864, v205\n"
      "v_add_u32 v204, 36864, v204\n"
      "v_add_u32 v201, 36864, v201\n"
      "v_add_u32 v241, 36864, v241\n"
      "global_load_dwordx4 a[180:183], v242, s[4:5] offset:-4096\n"
      "ds_read_b64_tr_b16 a[28:29], v162 offset:768\n"
      "ds_read_b64_tr_b16 a[30:31], v162 offset:5376\n"
      "ds_read_b64_tr_b16 a[32:33], v210 offset:768\n"
      "ds_read_b64_tr_b16 a[34:35], v210 offset:5376\n"
      "ds_read_b64_tr_b16 a[36:37], v211 offset:36864\n"
      "ds_read_b64_tr_b16 a[38:39], v211 offset:41472\n"
      "ds_read_b64_tr_b16 a[40:41], v162 offset:36864\n"
      "ds_read_b64_tr_b16 a[42:43], v162 offset:41472\n"
      "ds_read_b64_tr_b16 a[44:45], v210 offset:36864\n"
      "ds_read_b64_tr_b16 a[46:47], v210 offset:41472\n"
      "ds_read_b64_tr_b16 a[48:49], v211 offset:37248\n"
      "ds_read_b64_tr_b16 a[50:51], v211 offset:41856\n"
      "ds_read_b64_tr_b16 a[52:53], v162 offset:37248\n"
      "ds_read_b64_tr_b16 a[54:55], v162 offset:41856\n"
      "ds_read_b64_tr_b16 a[56:57], v210 offset:37248\n"
      "ds_read_b64_tr_b16 a[58:59], v210 offset:41856\n"
      "global_load_dwordx4 a[184:187], v242, s[4:5] offset:-3072\n"
      "ds_read_b64_tr_b16 a[60:61], v211 offset:37632\n"
      "ds_read_b64_tr_b16 a[62:63], v211 offset:42240\n"
      "ds_read_b64_tr_b16 a[64:65], v162 offset:37632\n"
      "ds_read_b64_tr_b16 a[66:67], v162 offset:42240\n"
      "ds_read_b64_tr_b16 a[68:69], v210 offset:37632\n"
      "ds_read_b64_tr_b16 a[70:71], v210 offset:42240\n"
      "ds_read_b64_tr_b16 a[72:73], v158\n"
      "global_load_dwordx4 a[188:191], v205, s[4:5] offset:-2048\n"
      "ds_read_b64_tr_b16 a[74:75], v158 offset:4608\n"
      "ds_read_b64_tr_b16 a[76:77], v208\n"
      "ds_read_b64_tr_b16 a[78:79], v208 offset:4608\n"
      "ds_read_b64_tr_b16 a[80:81], v209\n"
      "ds_read_b64_tr_b16 a[82:83], v209 offset:4608\n"
      "ds_read_b64_tr_b16 a[84:85], v158 offset:384\n"
      "ds_read_b64_tr_b16 a[86:87], v158 offset:4992\n"
      "global_load_dwordx4 a[192:195], v240, s[4:5] offset:-1024\n"
      "ds_read_b64_tr_b16 a[88:89], v208 offset:384\n"
      "ds_read_b64_tr_b16 a[90:91], v208 offset:4992\n"
      "ds_read_b64_tr_b16 a[92:93], v209 offset:384\n"
      "ds_read_b64_tr_b16 a[94:95], v209 offset:4992\n"
      "ds_read_b64_tr_b16 a[96:97], v158 offset:768\n"
      "ds_read_b64_tr_b16 a[98:99], v158 offset:5376\n"
      "global_load_dwordx4 a[196:199], v204, s[4:5]\n"
      "ds_read_b64_tr_b16 a[100:101], v208 offset:768\n"
      "ds_read_b64_tr_b16 a[102:103], v208 offset:5376\n"
      "ds_read_b64_tr_b16 a[104:105], v209 offset:768\n"
      "ds_read_b64_tr_b16 a[106:107], v209 offset:5376\n"
      "s_waitcnt lgkmcnt(0)\n"
      "s_pack_ll_b32_b16 s28, s36, s44\n"
      "s_waitcnt vmcnt(7)\n"
      "v_dot2_f32_bf16 v250, s28, v200, 0\n"
      "global_load_dwordx4 a[200:203], v242, s[4:5] offset:1024\n"
      "s_pack_hh_b32_b16 s29, s36, s44\n"
      "v_dot2_f32_bf16 v157, s29, v200, 0\n"
      "s_pack_ll_b32_b16 s30, s37, s45\n"
      "v_dot2_f32_bf16 v242, s30, v200, 0\n"
      "s_pack_hh_b32_b16 s31, s37, s45\n"
      "v_dot2_f32_bf16 v203, s31, v200, 0\n"
      "s_pack_ll_b32_b16 s28, s38, s46\n"
      "global_load_dwordx4 a[204:207], v201, s[4:5] offset:2048\n"
      "v_dot2_f32_bf16 v201, s28, v200, 0\n"
      "s_pack_hh_b32_b16 s29, s38, s46\n"
      "v_dot2_f32_bf16 v205, s29, v200, 0\n"
      "s_pack_ll_b32_b16 s30, s39, s47\n"
      "v_dot2_f32_bf16 v207, s30, v200, 0\n"
      "s_pack_hh_b32_b16 s31, s39, s47\n"
      "global_load_dwordx4 a[208:211], v240, s[4:5] offset:3072\n"
      "v_dot2_f32_bf16 v204, s31, v200, 0\n"
      "s_pack_ll_b32_b16 s28, s40, s48\n"
      "v_dot2_f32_bf16 v160, s28, v200, 0\n"
      "s_pack_hh_b32_b16 s29, s40, s48\n"
      "v_dot2_f32_bf16 v240, s29, v200, 0\n"
      "s_pack_ll_b32_b16 s30, s41, s49\n"
      "v_dot2_f32_bf16 v197, s30, v200, 0\n"
      "global_load_dwordx4 a[212:215], v241, s[4:5]\n"
      "s_pack_hh_b32_b16 s31, s41, s49\n"
      "v_dot2_f32_bf16 v163, s31, v200, 0\n"
      "s_pack_ll_b32_b16 s28, s42, s50\n"
      "v_dot2_f32_bf16 v198, s28, v200, 0\n"
      "s_pack_hh_b32_b16 s29, s42, s50\n"
      "v_dot2_f32_bf16 v241, s29, v200, 0\n"
      "s_pack_ll_b32_b16 s30, s43, s51\n"
      "global_load_dwordx4 v[114:117], v159, s[60:61] offset:-4096\n"
      "v_dot2_f32_bf16 v161, s30, v200, 0\n"
      "s_pack_hh_b32_b16 s31, s43, s51\n"
      "v_dot2_f32_bf16 v164, s31, v200, 0\n"
      "s_load_dwordx8 s[36:43], s[60:61], 41216\n"
      "s_load_dwordx4 s[44:47], s[60:61], 41248\n"
      "s_mov_b64 s[48:49], s[60:61]\n"
      "global_load_dwordx4 v[136:139], v159, s[60:61] offset:3072\n"
      "v_max_f32 v250, 0, v250\n"
      "v_max_f32_e64 v157, 0, v157\n"
      "v_cvt_pk_bf16_f32 v250, v250, v157\n"
      "v_max_f32 v242, 0, v242\n"
      "v_max_f32 v203, 0, v203\n"
      "v_cvt_pk_bf16_f32 v242, v242, v203\n"
      "v_max_f32 v201, 0, v201\n"
      "v_max_f32 v205, 0, v205\n"
      "v_cvt_pk_bf16_f32 v201, v201, v205\n"
      "v_max_f32 v207, 0, v207\n"
      "v_max_f32 v204, 0, v204\n"
      "v_cvt_pk_bf16_f32 v207, v207, v204\n"
      "v_max_f32 v160, 0, v160\n"
      "v_max_f32 v240, 0, v240\n"
      "v_cvt_pk_bf16_f32 v160, v160, v240\n"
      "v_max_f32 v197, 0, v197\n"
      "v_max_f32 v163, 0, v163\n"
      "v_cvt_pk_bf16_f32 v197, v197, v163\n"
      "v_max_f32 v198, 0, v198\n"
      "v_max_f32 v241, 0, v241\n"
      "v_cvt_pk_bf16_f32 v198, v198, v241\n"
      "v_max_f32_e64 v161, 0, v161\n"
      "v_max_f32_e64 v164, 0, v164\n"
      "v_cvt_pk_bf16_f32 v161, v161, v164\n"
      "s_pack_ll_b32_b16 s28, s64, s66\n"
      "v_dot2_f32_bf16 v244, s28, v250, 0\n"
      "s_pack_hh_b32_b16 s29, s64, s66\n"
      "v_dot2_f32_bf16 v245, s29, v250, 0\n"
      "s_pack_ll_b32_b16 s30, s65, s67\n"
      "v_dot2_f32_bf16 v246, s30, v250, 0\n"
      "s_pack_hh_b32_b16 s31, s65, s67\n"
      "v_dot2_f32_bf16 v247, s31, v250, 0\n"
      "s_pack_ll_b32_b16 s28, s68, s70\n"
      "v_dot2_f32_bf16 v244, s28, v242, v244\n"
      "s_pack_hh_b32_b16 s29, s68, s70\n"
      "v_dot2_f32_bf16 v245, s29, v242, v245\n"
      "s_waitcnt vmcnt(12)\n"
      "v_lshl_add_u32 v248, v156, 4, v248\n"
      "s_lshl_b32 s10, s24, 10\n"
      "s_add_u32 m0, s10, 137216\n"
      "s_nop 0\n"
      "global_load_lds_dwordx4 v248, s[16:17]\n"
      "s_add_u32 m0, s10, 141184\n"
      "s_nop 0\n"
      "global_load_lds_dwordx4 v248, s[16:17] offset:128\n"
      "s_add_u32 m0, s10, 145152\n"
      "s_nop 0\n"
      "global_load_lds_dwordx4 v248, s[16:17] offset:256\n"
      "v_add_u32 v163, s10, v243\n"
      "s_add_u32 m0, s10, 124928\n"
      "s_nop 0\n"
      "global_load_lds_dwordx4 v163, s[56:57]\n"
      "v_add_u32 v163, 4096, v163\n"
      "s_add_u32 m0, s10, 129024\n"
      "s_nop 0\n"
      "global_load_lds_dwordx4 v163, s[56:57]\n"
      "v_add_u32 v163, 4096, v163\n"
      "v_min_u32 v163, 9600, v163\n"
      "s_add_u32 m0, s10, 133120\n"
      "s_nop 0\n"
      "global_load_lds_dwordx4 v163, s[56:57]\n"
      "s_pack_ll_b32_b16 s30, s69, s71\n"
      "v_dot2_f32_bf16 v246, s30, v242, v246\n"
      "s_pack_hh_b32_b16 s31, s69, s71\n"
      "v_dot2_f32_bf16 v247, s31, v242, v247\n"
      "s_pack_ll_b32_b16 s28, s72, s74\n"
      "v_dot2_f32_bf16 v244, s28, v201, v244\n"
      "s_pack_hh_b32_b16 s29, s72, s74\n"
      "v_dot2_f32_bf16 v245, s29, v201, v245\n"
      "s_pack_ll_b32_b16 s30, s73, s75\n"
      "v_dot2_f32_bf16 v246, s30, v201, v246\n"
      "s_pack_hh_b32_b16 s31, s73, s75\n"
      "v_dot2_f32_bf16 v247, s31, v201, v247\n"
      "s_pack_ll_b32_b16 s28, s76, s78\n"
      "v_dot2_f32_bf16 v244, s28, v207, v244\n"
      "s_pack_hh_b32_b16 s29, s76, s78\n"
      "v_dot2_f32_bf16 v245, s29, v207, v245\n"
      "s_pack_ll_b32_b16 s30, s77, s79\n"
      "v_dot2_f32_bf16 v246, s30, v207, v246\n"
      "s_pack_hh_b32_b16 s31, s77, s79\n"
      "v_dot2_f32_bf16 v247, s31, v207, v247\n"
      "s_load_dwordx16 s[64:79], s[54:55], 40960\n"
      "s_lshl_b32 s28, s24, 4\n"
      "s_lshr_b64 s[8:9], s[8:9], s28\n"
      "s_lshl_b32 s8, s8, 16\n"
      "v_mov_b32_e64 v250, s8\n"
      "v_max_f32 v250, 0xc2c80000, v250\n"
      "v_min_f32 v250, 0x40930000, v250\n"
      "v_mul_f32 v250, 0x3fb8aa3b, v250\n"
      "v_exp_f32 v250, v250\n"
      "s_nop 0\n"
      "v_cvt_pk_bf16_f32 v250, v250, v250\n"
      "v_lshlrev_b32_e64 v250, 16, v250\n"
      "s_waitcnt lgkmcnt(0)\n"
      "s_barrier\n"
      "s_pack_ll_b32_b16 s28, s84, s86\n"
      "v_dot2_f32_bf16 v244, s28, v160, v244\n"
      "ds_write_b128 v202, a[108:111]\n"
      "s_pack_hh_b32_b16 s29, s84, s86\n"
      "v_dot2_f32_bf16 v245, s29, v160, v245\n"
      "s_pack_ll_b32_b16 s30, s85, s87\n"
      "ds_write_b128 v202, a[112:115] offset:1024\n"
      "v_dot2_f32_bf16 v246, s30, v160, v246\n"
      "s_pack_hh_b32_b16 s31, s85, s87\n"
      "v_dot2_f32_bf16 v247, s31, v160, v247\n"
      "ds_write_b128 v202, a[116:119] offset:2048\n"
      "s_pack_ll_b32_b16 s28, s88, s90\n"
      "v_dot2_f32_bf16 v244, s28, v197, v244\n"
      "s_pack_hh_b32_b16 s29, s88, s90\n"
      "ds_write_b128 v202, a[120:123] offset:3072\n"
      "v_dot2_f32_bf16 v245, s29, v197, v245\n"
      "s_pack_ll_b32_b16 s30, s89, s91\n"
      "v_dot2_f32_bf16 v246, s30, v197, v246\n"
      "ds_write_b128 v202, a[124:127] offset:4096\n"
      "s_pack_hh_b32_b16 s31, s89, s91\n"
      "v_dot2_f32_bf16 v247, s31, v197, v247\n"
      "s_pack_ll_b32_b16 s28, s92, s94\n"
      "ds_write_b128 v202, a[128:131] offset:5120\n"
      "v_dot2_f32_bf16 v244, s28, v198, v244\n"
      "s_pack_hh_b32_b16 s29, s92, s94\n"
      "v_dot2_f32_bf16 v245, s29, v198, v245\n"
      "ds_write_b128 v202, a[132:135] offset:6144\n"
      "s_pack_ll_b32_b16 s30, s93, s95\n"
      "v_dot2_f32_bf16 v246, s30, v198, v246\n"
      "s_pack_hh_b32_b16 s31, s93, s95\n"
      "ds_write_b128 v202, a[136:139] offset:7168\n"
      "v_dot2_f32_bf16 v247, s31, v198, v247\n"
      "s_pack_ll_b32_b16 s28, s96, s98\n"
      "v_dot2_f32_bf16 v244, s28, v161, v244\n"
      "ds_write_b128 v202, a[140:143] offset:8192\n"
      "s_pack_hh_b32_b16 s29, s96, s98\n"
      "v_dot2_f32_bf16 v245, s29, v161, v245\n"
      "s_pack_ll_b32_b16 s30, s97, s99\n"
      "ds_write_b128 v202, a[144:147] offset:36864\n"
      "v_dot2_f32_bf16 v246, s30, v161, v246\n"
      "s_pack_hh_b32_b16 s31, s97, s99\n"
      "v_dot2_f32_bf16 v247, s31, v161, v247\n"
      "ds_write_b128 v202, a[148:151] offset:37888\n"
      "global_load_dwordx4 v[118:121], v159, s[48:49] offset:-3072\n"
      "s_load_dwordx16 s[80:95], s[52:53], 41280\n"
      "s_load_dwordx4 s[96:99], s[52:53], 41344\n"
      "s_load_dwordx2 s[100:101], s[52:53], 41360\n"
      "ds_write_b128 v202, a[152:155] offset:38912\n"
      "global_load_dwordx4 v[122:125], v159, s[48:49] offset:-2048\n"
      "s_load_dword s26, s[52:53], 41376\n"
      "s_load_dword s3, s[52:53], 41380\n"
      "v_cmp_nge_f32 vcc, 0x42480000, v250\n"
      "ds_write_b128 v202, a[156:159] offset:39936\n"
      "v_mul_f32 v250, 0x3fb8aa3b, v250\n"
      "s_mov_b64 s[50:51], vcc\n"
      "s_waitcnt vmcnt(8)\n"
      "v_add_u32 v156, 3072, v139\n"
      "global_load_dwordx4 a[216:219], v156, s[6:7] offset:-3072\n"
      "ds_write_b128 v202, a[160:163] offset:40960\n"
      "v_cvt_pk_bf16_f32 v245, v244, v245\n"
      "v_lshlrev_b32 v244, 16, v245\n"
      "v_and_b32 v245, 0xffff0000, v245\n"
      "ds_write_b128 v202, a[164:167] offset:41984\n"
      "global_load_dwordx4 a[220:223], v156, s[6:7] offset:-2944\n"
      "v_cvt_pk_bf16_f32 v247, v246, v247\n"
      "v_lshlrev_b32 v246, 16, v247\n"
      "v_and_b32 v247, 0xffff0000, v247\n"
      "ds_write_b128 v202, a[168:171] offset:43008\n"
      "v_mul_f32 v244, 0xbfb8aa3b, v244\n"
      "global_load_dwordx4 a[224:227], v156, s[6:7] offset:-2816\n"
      "v_mul_f32 v245, 0xbfb8aa3b, v245\n"
      "v_mul_f32 v246, 0xbfb8aa3b, v246\n"
      "ds_write_b128 v202, a[172:175] offset:44032\n"
      "v_mul_f32 v247, 0xbfb8aa3b, v247\n"
      "global_load_dwordx4 a[228:231], v156, s[6:7]\n"
      "v_exp_f32 v244, v244\n"
      "v_exp_f32 v245, v245\n"
      "ds_write_b128 v202, a[176:179] offset:45056\n"
      "v_exp_f32 v246, v246\n"
      "v_exp_f32 v247, v247\n"
      "global_load_dwordx4 a[232:235], v156, s[6:7] offset:128\n"
      "v_add_f32 v244, 1.0, v244\n"
      "ds_write_b128 v249, a[180:183]\n"
      "v_add_f32 v245, 1.0, v245\n"
      "v_add_f32 v246, 1.0, v246\n"
      "global_load_dwordx4 a[236:239], v156, s[6:7] offset:256\n"
      "v_add_f32 v247, 1.0, v247\n"
      "ds_write_b128 v249, a[184:187] offset:1024\n"
      "v_rcp_f32 v244, v244\n"
      "v_rcp_f32 v245, v245\n"
      "v_rcp_f32_e64 v246, v246\n"
      "ds_write_b128 v249, a[188:191] offset:2048\n"
      "v_add_u32 v156, 9216, v139\n"
      "global_load_dwordx4 a[240:243], v156, s[6:7] offset:-3072\n"
      "v_rcp_f32_e64 v247, v247\n"
      "v_cvt_pk_bf16_f32 v245, v244, v245\n"
      "v_lshlrev_b32_e64 v244, 16, v245\n"
      "ds_write_b128 v249, a[192:195] offset:3072\n"
      "v_and_b32 v245, 0xffff0000, v245\n"
      "global_load_dwordx4 a[244:247], v156, s[6:7] offset:-2944\n"
      "v_cvt_pk_bf16_f32 v247, v246, v247\n"
      "v_lshlrev_b32_e64 v246, 16, v247\n"
      "ds_write_b128 v249, a[196:199] offset:4096\n"
      "v_and_b32 v247, 0xffff0000, v247\n"
      "v_add_f32_e64 v244, -0.5, v244\n"
      "global_load_dwordx4 a[248:251], v156, s[6:7] offset:-2816\n"
      "v_mul_f32 v244, 0x41b8aa3b, v244\n"
      "ds_write_b128 v249, a[200:203] offset:5120\n"
      "ds_write_b32 v206, v244\n"
      "v_add_f32_e64 v245, -0.5, v245\n"
      "global_load_dwordx4 a[252:255], v156, s[6:7]\n"
      "v_mul_f32 v245, 0x41b8aa3b, v245\n"
      "ds_write_b128 v249, a[204:207] offset:6144\n"
      "ds_write_b32 v206, v245 offset:768\n"
      "v_add_f32_e64 v246, -0.5, v246\n"
      "v_mul_f32 v246, 0x41b8aa3b, v246\n"
      "global_load_dwordx4 v[0:3], v156, s[6:7] offset:128\n"
      "ds_write_b128 v249, a[208:211] offset:7168\n"
      "ds_write_b32 v206, v246 offset:1536\n"
      "v_add_f32_e64 v247, -0.5, v247\n"
      "v_mul_f32 v247, 0x41b8aa3b, v247\n"
      "global_load_dwordx4 v[4:7], v156, s[6:7] offset:256\n"
      "ds_write_b128 v249, a[212:215] offset:8192\n"
      "ds_write_b32 v206, v247 offset:2304\n"
      "s_waitcnt vmcnt(14)\n"
      "s_waitcnt lgkmcnt(0)\n"
      "s_barrier\n"
      "v_readfirstlane_b32 s36, v250\n"
      "L_sched_%=:\n"
      "v_add_u32 v157, 137216, v115\n"
      "v_add_u32 v160, 137216, v114\n"
      "s_mov_b32 s11, 100466688\n"
      "ds_read_b128 v[152:155], v160\n"
      "ds_read_b128 v[200:203], v157\n"
      "ds_read_b64_tr_b16 a[108:109], v211\n"
      "ds_read_b64_tr_b16 a[122:123], v211 offset:4992\n"
      "ds_read_b64_tr_b16 a[132:133], v211 offset:768\n"
      "ds_read_b64_tr_b16 a[116:117], v210\n"
      "ds_read_b64_tr_b16 a[118:119], v210 offset:4608\n"
      "ds_read_b64_tr_b16 a[128:129], v210 offset:384\n"
      "ds_read_b64_tr_b16 a[112:113], v162\n"
      "ds_read_b64_tr_b16 a[110:111], v211 offset:4608\n"
      "ds_read_b64_tr_b16 a[120:121], v211 offset:384\n"
      "ds_read_b64_tr_b16 a[124:125], v162 offset:384\n"
      "ds_read_b64_tr_b16 a[134:135], v211 offset:5376\n"
      "ds_read_b64_tr_b16 a[126:127], v162 offset:4992\n"
      "ds_read_b64_tr_b16 a[136:137], v162 offset:768\n"
      "ds_read_b64_tr_b16 a[130:131], v210 offset:4992\n"
      "ds_read_b64_tr_b16 a[114:115], v162 offset:4608\n"
      "s_waitcnt lgkmcnt(15)\n"
      "v_mfma_f32_16x16x32_bf16 v[204:207], a[0:3], v[152:155], 0\n"
      "ds_read_b128 v[188:191], v160 offset:2048\n"
      "v_mfma_f32_16x16x32_bf16 v[204:207], a[36:39], v[200:203], v[204:207]\n"
      "ds_read_b64_tr_b16 a[140:141], v210 offset:768\n"
      "ds_read_b64_tr_b16 a[138:139], v162 offset:5376\n"
      "v_mfma_f32_16x16x32_bf16 v[196:199], a[4:7], v[152:155], 0\n"
      "s_sub_u32 s16, s16, 99876864\n"
      "ds_read_b128 v[212:215], v157 offset:2048\n"
      "v_mfma_f32_16x16x32_bf16 v[196:199], a[40:43], v[200:203], v[196:199]\n"
      "global_load_dwordx4 v[148:151], v251, s[48:49] offset:-3072\n"
      "v_mfma_f32_16x16x32_bf16 v[172:175], a[8:11], v[152:155], 0\n"
      "ds_read_b64_tr_b16 a[142:143], v210 offset:5376\n"
      "v_mov_b32 v36, 0\n"
      "v_mfma_f32_16x16x32_bf16 v[172:175], a[44:47], v[200:203], v[172:175]\n"
      "ds_read_b64_tr_b16 a[144:145], v211 offset:36864\n"
      "v_mov_b32_e64 v37, 0\n"
      "v_mfma_f32_16x16x32_bf16 v[164:167], a[12:15], v[152:155], 0\n"
      "ds_read_b64_tr_b16 a[146:147], v211 offset:41472\n"
      "v_mov_b32 v57, 0\n"
      "v_mfma_f32_16x16x32_bf16 v[164:167], a[48:51], v[200:203], v[164:167]\n"
      "ds_read_b64_tr_b16 a[148:149], v162 offset:36864\n"
      "s_subb_u32 s17, s17, 0\n"
      "v_mfma_f32_16x16x32_bf16 v[180:183], a[16:19], v[152:155], 0\n"
      "ds_read_b64_tr_b16 a[150:151], v162 offset:41472\n"
      "s_sub_u32 s12, s22, 99876864\n"
      "v_mfma_f32_16x16x32_bf16 v[180:183], a[52:55], v[200:203], v[180:183]\n"
      "ds_read_b64_tr_b16 a[152:153], v210 offset:36864\n"
      "v_mfma_f32_16x16x32_bf16 v[240:243], a[20:23], v[152:155], 0\n"
      "ds_read_b64_tr_b16 a[156:157], v211 offset:37248\n"
      "v_mov_b32_e64 v48, 0\n"
      "v_mfma_f32_16x16x32_bf16 v[240:243], a[56:59], v[200:203], v[240:243]\n"
      "ds_read_b64_tr_b16 a[158:159], v211 offset:41856\n"
      "v_mov_b32_e64 v49, 0\n"
      "v_mfma_f32_16x16x32_bf16 v[144:147], a[24:27], v[152:155], 0\n"
      "ds_read_b64_tr_b16 a[154:155], v210 offset:41472\n"
      "v_mov_b32 v61, 0\n"
      "v_mfma_f32_16x16x32_bf16 v[144:147], a[60:63], v[200:203], v[144:147]\n"
      "ds_read_b64_tr_b16 a[164:165], v210 offset:37248\n"
      "s_waitcnt lgkmcnt(8)\n"
      "v_mfma_f32_16x16x32_bf16 v[176:179], a[28:31], v[152:155], 0\n"
      "ds_read_b64_tr_b16 a[160:161], v162 offset:37248\n"
      "s_subb_u32 s13, s23, 0\n"
      "v_mfma_f32_16x16x32_bf16 v[176:179], a[64:67], v[200:203], v[176:179]\n"
      "ds_read_b64_tr_b16 a[182:183], v158 offset:4608\n"
      "v_mov_b32 v43, 0\n"
      "v_mfma_f32_16x16x32_bf16 v[244:247], a[32:35], v[152:155], 0\n"
      "ds_read_b64_tr_b16 a[162:163], v162 offset:41856\n"
      "v_mfma_f32_16x16x32_bf16 v[244:247], a[68:71], v[200:203], v[244:247]\n"
      "ds_read_b64_tr_b16 a[166:167], v210 offset:41856\n"
      "ds_read_b64_tr_b16 a[206:207], v158 offset:5376\n"
      "v_mfma_f32_16x16x32_bf16 v[252:255], a[0:3], v[188:191], 0\n"
      "ds_read_b64_tr_b16 a[170:171], v211 offset:42240\n"
      "v_mov_b32_e64 v42, 0\n"
      "v_mfma_f32_16x16x32_bf16 v[252:255], a[36:39], v[212:215], v[252:255]\n"
      "ds_read_b64_tr_b16 a[168:169], v211 offset:37632\n"
      "ds_read_b64_tr_b16 a[176:177], v210 offset:37632\n"
      "v_mfma_f32_16x16x32_bf16 v[200:203], a[4:7], v[188:191], 0\n"
      "ds_read_b64_tr_b16 a[172:173], v162 offset:37632\n"
      "v_mov_b32_e64 v53, 0\n"
      "v_mfma_f32_16x16x32_bf16 v[200:203], a[40:43], v[212:215], v[200:203]\n"
      "ds_read_b64_tr_b16 a[196:197], v208 offset:384\n"
      "ds_read_b64_tr_b16 a[194:195], v158 offset:4992\n"
      "v_mfma_f32_16x16x32_bf16 v[232:235], a[8:11], v[188:191], 0\n"
      "ds_read_b64_tr_b16 a[174:175], v162 offset:42240\n"
      "v_mfma_f32_16x16x32_bf16 v[232:235], a[44:47], v[212:215], v[232:235]\n"
      "s_mov_b64 s[54:55], s[20:21]\n"
      "ds_read_b64_tr_b16 a[212:213], v209 offset:768\n"
      "v_mfma_f32_16x16x32_bf16 v[140:143], a[12:15], v[188:191], 0\n"
      "ds_read_b64_tr_b16 a[178:179], v210 offset:42240\n"
      "v_mfma_f32_16x16x32_bf16 v[140:143], a[48:51], v[212:215], v[140:143]\n"
      "ds_read_b64_tr_b16 a[190:191], v209 offset:4608\n"
      "ds_read_b64_tr_b16 a[200:201], v209 offset:384\n"
      "v_mfma_f32_16x16x32_bf16 v[224:227], a[16:19], v[188:191], 0\n"
      "s_mov_b32 s22, 0\n"
      "ds_read_b64_tr_b16 a[184:185], v208\n"
      "v_mfma_f32_16x16x32_bf16 v[224:227], a[52:55], v[212:215], v[224:227]\n"
      "ds_read_b64_tr_b16 a[204:205], v158 offset:768\n"
      "ds_read_b64_tr_b16 a[214:215], v209 offset:5376\n"
      "v_mfma_f32_16x16x32_bf16 v[184:187], a[20:23], v[188:191], 0\n"
      "s_mov_b32 s23, 0x20000\n"
      "ds_read_b64_tr_b16 a[186:187], v208 offset:4608\n"
      "v_mfma_f32_16x16x32_bf16 v[184:187], a[56:59], v[212:215], v[184:187]\n"
      "ds_read_b64_tr_b16 a[198:199], v208 offset:4992\n"
      "v_mov_b32_e64 v65, 0\n"
      "v_mfma_f32_16x16x32_bf16 v[228:231], a[24:27], v[188:191], 0\n"
      "ds_read_b64_tr_b16 a[188:189], v209\n"
      "v_mfma_f32_16x16x32_bf16 v[228:231], a[60:63], v[212:215], v[228:231]\n"
      "ds_read_b64_tr_b16 a[180:181], v158\n"
      "ds_read_b64_tr_b16 a[208:209], v208 offset:768\n"
      "v_mfma_f32_16x16x32_bf16 v[236:239], a[28:31], v[188:191], 0\n"
      "global_load_dwordx4 v[216:219], v251, s[48:49] offset:-4096\n"
      "v_mfma_f32_16x16x32_bf16 v[236:239], a[64:67], v[212:215], v[236:239]\n"
      "ds_read_b64_tr_b16 a[210:211], v208 offset:5376\n"
      "ds_read_b64_tr_b16 a[192:193], v158 offset:384\n"
      "v_mfma_f32_16x16x32_bf16 v[220:223], a[32:35], v[188:191], 0\n"
      "ds_read_b64_tr_b16 a[202:203], v209 offset:4992\n"
      "v_mfma_f32_16x16x32_bf16 v[220:223], a[68:71], v[212:215], v[220:223]\n"
      "s_waitcnt lgkmcnt(0)\n"
      "s_barrier\n"
      "ds_read_b128 v[192:195], v157 offset:4096\n"
      "ds_read_b128 v[152:155], v160 offset:4096\n"
      "s_mov_b32 s60, 100270464\n"
      "s_mov_b32 s61, 196224\n"
      "s_lshr_b32 s8, s80, 8\n"
      "s_and_b32 s9, s80, 0xff\n"
      "s_mul_i32 s10, s8, 73\n"
      "s_add_u32 s10, s10, s9\n"
      "s_mul_i32 s28, s10, 4802\n"
      "s_add_u32 s56, s18, s28\n"
      "s_addc_u32 s57, s19, 0\n"
      "s_and_b32 s56, s56, -16\n"
      "s_sub_u32 s58, s56, 110592\n"
      "ds_read2_b32 v[78:79], v136 offset0:0 offset1:1\n"
      "s_subb_u32 s59, s57, 0\n"
      "s_sub_u32 s56, s56, 73728\n"
      "s_waitcnt vmcnt(1)\n"
      "v_mov_b32_e64 v68, v148\n"
      "s_subb_u32 s57, s57, 0\n"
      "s_mul_i32 s28, s8, 1373568\n"
      "s_mul_i32 s29, s9, 2688\n"
      "s_add_u32 s28, s28, s29\n"
      "s_cmp_eq_u32 s8, 72\n"
      "v_add_u32 v248, 99876864, v248\n"
      "v_mov_b32 v67, v148\n"
      "s_cselect_b32 s8, 0, 100270464\n"
      "ds_read2_b32 v[80:81], v136 offset0:2 offset1:3\n"
      "v_mov_b32_e64 v66, v148\n"
      "s_cmp_eq_u32 s9, 72\n"
      "s_cselect_b32 s9, 0, 196224\n"
      "s_add_u32 s10, s8, s9\n"
      "s_waitcnt lgkmcnt(2)\n"
      "v_mfma_f32_16x16x32_bf16 v[204:207], a[72:75], v[152:155], v[204:207]\n"
      "ds_read2_b32 v[84:85], v136 offset0:18 offset1:19\n"
      "v_mov_b32 v69, v148\n"
      "v_mfma_f32_16x16x32_bf16 v[204:207], a[108:111], v[192:195], v[204:207]\n"
      "ds_read2_b32 v[82:83], v136 offset0:16 offset1:17\n"
      "s_add_u32 s28, s28, s10\n"
      "v_mfma_f32_16x16x32_bf16 v[196:199], a[76:79], v[152:155], v[196:199]\n"
      "ds_read2_b32 v[88:89], v136 offset0:34 offset1:35\n"
      "v_mov_b32 v148, 0\n"
      "v_mfma_f32_16x16x32_bf16 v[196:199], a[112:115], v[192:195], v[196:199]\n"
      "ds_read_b32 v71, v138\n"
      "ds_read2_b32 v[86:87], v136 offset0:32 offset1:33\n"
      "v_mfma_f32_16x16x32_bf16 v[172:175], a[80:83], v[152:155], v[172:175]\n"
      "s_add_u32 s52, s16, s28\n"
      "ds_read_b32 v70, v137 offset:192\n"
      "v_mfma_f32_16x16x32_bf16 v[172:175], a[116:119], v[192:195], v[172:175]\n"
      "ds_read_b128 v[210:213], v160 offset:6144\n"
      "s_addc_u32 s53, s17, 0\n"
      "v_mfma_f32_16x16x32_bf16 v[164:167], a[84:87], v[152:155], v[164:167]\n"
      "s_cmp_lg_u32 s10, s11\n"
      "ds_read_b32 v72, v137 offset:3328\n"
      "v_mfma_f32_16x16x32_bf16 v[164:167], a[120:123], v[192:195], v[164:167]\n"
      "ds_read_b32 v74, v137 offset:6464\n"
      "ds_read_b32 v73, v138 offset:64\n"
      "v_mfma_f32_16x16x32_bf16 v[180:183], a[88:91], v[152:155], v[180:183]\n"
      "s_cbranch_scc1 L_edge_1_%=\n"
      "L_eb_2_%=:\n"
      "v_mfma_f32_16x16x32_bf16 v[180:183], a[124:127], v[192:195], v[180:183]\n"
      "ds_read_b32 v75, v138 offset:128\n"
      "v_mov_b32_e64 v189, v149\n"
      "v_mfma_f32_16x16x32_bf16 v[240:243], a[92:95], v[152:155], v[240:243]\n"
      "s_add_u32 m0, s76, 0\n"
      "v_mov_b32_e64 v188, v149\n"
      "v_mov_b32 v137, 134528\n"
      "v_mfma_f32_16x16x32_bf16 v[240:243], a[128:131], v[192:195], v[240:243]\n"
      "s_waitcnt vmcnt(0)\n"
      "ds_write_b32 v217, v248\n"
      "v_mfma_f32_16x16x32_bf16 v[144:147], a[96:99], v[152:155], v[144:147]\n"
      "ds_read_b128 v[168:171], v157 offset:6144\n"
      "s_waitcnt lgkmcnt(7)\n"
      "v_mfma_f32_16x16x32_bf16 v[144:147], a[132:135], v[192:195], v[144:147]\n"
      "ds_read_b32 v77, v137\n"
      "v_mov_b32_e64 v191, v149\n"
      "v_mov_b32_e64 v190, v149\n"
      "v_add_u32 v137, 3136, v136\n"
      "v_mfma_f32_16x16x32_bf16 v[176:179], a[100:103], v[152:155], v[176:179]\n"
      "global_load_lds_dwordx4 v116, s[52:53]\n"
      "ds_read2_b32 v[90:91], v137 offset0:0 offset1:1\n"
      "v_mfma_f32_16x16x32_bf16 v[244:247], a[104:107], v[152:155], v[244:247]\n"
      "ds_read2_b32 v[94:95], v137 offset0:16 offset1:17\n"
      "v_add_u32 v158, 6272, v136\n"
      "v_mfma_f32_16x16x32_bf16 v[244:247], a[140:143], v[192:195], v[244:247]\n"
      "ds_read2_b32 v[92:93], v137 offset0:2 offset1:3\n"
      "s_add_u32 m0, s76, 6016\n"
      "v_mfma_f32_16x16x32_bf16 v[176:179], a[136:139], v[192:195], v[176:179]\n"
      "ds_read2_b32 v[96:97], v137 offset0:18 offset1:19\n"
      "global_load_lds_dwordx4 v116, s[52:53] offset:128\n"
      "s_waitcnt lgkmcnt(5)\n"
      "v_mfma_f32_16x16x32_bf16 v[252:255], a[72:75], v[210:213], v[252:255]\n"
      "ds_read2_b32 v[98:99], v137 offset0:32 offset1:33\n"
      "s_add_u32 m0, s76, 12032\n"
      "v_mfma_f32_16x16x32_bf16 v[252:255], a[108:111], v[168:171], v[252:255]\n"
      "global_load_lds_dwordx4 v116, s[52:53] offset:256\n"
      "v_mfma_f32_16x16x32_bf16 v[200:203], a[76:79], v[210:213], v[200:203]\n"
      "s_add_u32 m0, s77, 0\n"
      "ds_read2_b32 v[100:101], v137 offset0:34 offset1:35\n"
      "v_mfma_f32_16x16x32_bf16 v[200:203], a[112:115], v[168:171], v[200:203]\n"
      "global_load_lds_dwordx4 v117, s[52:53]\n"
      "v_mfma_f32_16x16x32_bf16 v[232:235], a[80:83], v[210:213], v[232:235]\n"
      "s_waitcnt lgkmcnt(2)\n"
      "ds_read2_b32 v[102:103], v158 offset0:0 offset1:1\n"
      "v_mfma_f32_16x16x32_bf16 v[232:235], a[116:119], v[168:171], v[232:235]\n"
      "ds_read2_b32 v[104:105], v158 offset0:2 offset1:3\n"
      "v_mov_b32 v149, 0\n"
      "v_mfma_f32_16x16x32_bf16 v[224:227], a[88:91], v[210:213], v[224:227]\n"
      "ds_read2_b32 v[110:111], v158 offset0:32 offset1:33\n"
      "v_mfma_f32_16x16x32_bf16 v[140:143], a[84:87], v[210:213], v[140:143]\n"
      "ds_read2_b32 v[106:107], v158 offset0:16 offset1:17\n"
      "s_add_u32 m0, s78, 0\n"
      "v_mfma_f32_16x16x32_bf16 v[140:143], a[120:123], v[168:171], v[140:143]\n"
      "ds_read2_b32 v[108:109], v158 offset0:18 offset1:19\n"
      "ds_read_b128 v[192:195], v160 offset:8192\n"
      "v_mfma_f32_16x16x32_bf16 v[224:227], a[124:127], v[168:171], v[224:227]\n"
      "ds_read_b128 v[248:251], v157 offset:8192\n"
      "v_mov_b32 v153, 0\n"
      "v_mfma_f32_16x16x32_bf16 v[184:187], a[92:95], v[210:213], v[184:187]\n"
      "ds_read2_b32 v[112:113], v158 offset0:34 offset1:35\n"
      "v_mov_b32_e64 v152, 0\n"
      "v_mfma_f32_16x16x32_bf16 v[184:187], a[128:131], v[168:171], v[184:187]\n"
      "s_mov_b32 exec_hi, 0\n"
      "global_load_lds_dwordx4 v118, s[52:53]\n"
      "s_mov_b32 exec_hi, -1\n"
      "v_mfma_f32_16x16x32_bf16 v[228:231], a[96:99], v[210:213], v[228:231]\n"
      "s_waitcnt lgkmcnt(7)\n"
      "s_add_u32 m0, s76, 110592\n"
      "v_add_u32 v156, 15360, v139\n"
      "v_mfma_f32_16x16x32_bf16 v[228:231], a[132:135], v[168:171], v[228:231]\n"
      "global_load_lds_dwordx4 v119, s[56:57]\n"
      "s_add_u32 m0, s79, 0\n"
      "s_mul_i32 s2, s24, 768\n"
      "v_mfma_f32_16x16x32_bf16 v[236:239], a[100:103], v[210:213], v[236:239]\n"
      "s_add_u32 s2, s2, 150640\n"
      "v_lshl_add_u32 v81, v81, 2, s2\n"
      "v_lshl_add_u32 v88, v88, 2, s2\n"
      "v_lshl_add_u32 v91, v91, 2, s2\n"
      "v_lshl_add_u32 v78, v78, 2, s2\n"
      "v_lshl_add_u32 v80, v80, 2, s2\n"
      "v_lshl_add_u32 v92, v92, 2, s2\n"
      "v_mfma_f32_16x16x32_bf16 v[220:223], a[104:107], v[210:213], v[220:223]\n"
      "v_lshl_add_u32 v89, v89, 2, s2\n"
      "v_lshl_add_u32 v82, v82, 2, s2\n"
      "v_mfma_f32_16x16x32_bf16 v[220:223], a[140:143], v[168:171], v[220:223]\n"
      "s_waitcnt lgkmcnt(1)\n"
      "v_lshl_add_u32 v84, v84, 2, s2\n"
      "v_lshl_add_u32 v86, v86, 2, s2\n"
      "s_waitcnt lgkmcnt(0)\n"
      "v_mfma_f32_16x16x32_bf16 v[236:239], a[136:139], v[168:171], v[236:239]\n"
      "global_load_lds_dword v122, s[58:59]\n"
      "ds_read_b32 v80, v80\n"
      "v_lshl_add_u32 v87, v87, 2, s2\n"
      "v_lshl_add_u32 v100, v100, 2, s2\n"
      "v_lshl_add_u32 v79, v79, 2, s2\n"
      "v_mfma_f32_16x16x32_bf16 v[204:207], a[144:147], v[192:195], v[204:207]\n"
      "ds_read_b32 v78, v78\n"
      "v_lshl_add_u32 v94, v94, 2, s2\n"
      "v_mfma_f32_16x16x32_bf16 v[204:207], a[180:183], v[248:251], v[204:207]\n"
      "v_lshl_add_u32 v93, v93, 2, s2\n"
      "v_lshl_add_u32 v85, v85, 2, s2\n"
      "v_mfma_f32_16x16x32_bf16 v[196:199], a[148:151], v[192:195], v[196:199]\n"
      "ds_read_b32 v79, v79\n"
      "v_lshl_add_u32 v97, v97, 2, s2\n"
      "v_mfma_f32_16x16x32_bf16 v[196:199], a[184:187], v[248:251], v[196:199]\n"
      "v_lshl_add_u32 v99, v99, 2, s2\n"
      "v_lshl_add_u32 v101, v101, 2, s2\n"
      "v_lshl_add_u32 v95, v95, 2, s2\n"
      "v_lshl_add_u32 v96, v96, 2, s2\n"
      "v_mfma_f32_16x16x32_bf16 v[240:243], a[164:167], v[192:195], v[240:243]\n"
      "global_load_dwordx4 v[126:129], v159, s[48:49] offset:-1024\n"
      "v_mfma_f32_16x16x32_bf16 v[172:175], a[152:155], v[192:195], v[172:175]\n"
      "ds_read_b32 v81, v81\n"
      "v_lshl_add_u32 v90, v90, 2, s2\n"
      "v_mfma_f32_16x16x32_bf16 v[172:175], a[188:191], v[248:251], v[172:175]\n"
      "ds_read_b32 v82, v82\n"
      "v_lshl_add_u32 v83, v83, 2, s2\n"
      "v_mfma_f32_16x16x32_bf16 v[164:167], a[156:159], v[192:195], v[164:167]\n"
      "ds_read_b32 v83, v83\n"
      "v_lshl_add_u32 v103, v103, 2, s2\n"
      "v_mfma_f32_16x16x32_bf16 v[164:167], a[192:195], v[248:251], v[164:167]\n"
      "ds_read_b32 v84, v84\n"
      "v_lshl_add_u32 v102, v102, 2, s2\n"
      "v_mfma_f32_16x16x32_bf16 v[180:183], a[160:163], v[192:195], v[180:183]\n"
      "global_load_dwordx4 v[130:133], v159, s[48:49]\n"
      "ds_read_b32 v85, v85\n"
      "v_lshl_add_u32 v109, v109, 2, s2\n"
      "v_mfma_f32_16x16x32_bf16 v[240:243], a[200:203], v[248:251], v[240:243]\n"
      "ds_read_b32 v87, v87\n"
      "v_lshl_add_u32 v77, v77, 2, s2\n"
      "v_fma_f32 v154, v164, v164, s38\n"
      "v_lshl_add_u32 v108, v108, 2, s2\n"
      "v_mfma_f32_16x16x32_bf16 v[144:147], a[168:171], v[192:195], v[144:147]\n"
      "ds_read_b32 v88, v88\n"
      "v_lshl_add_u32 v105, v105, 2, s2\n"
      "v_mfma_f32_16x16x32_bf16 v[144:147], a[204:207], v[248:251], v[144:147]\n"
      "ds_read_b32 v93, v93\n"
      "v_lshl_add_u32 v71, v71, 2, s2\n"
      "v_lshl_add_u32 v98, v98, 2, s2\n"
      "v_mfma_f32_16x16x32_bf16 v[176:179], a[172:175], v[192:195], v[176:179]\n"
      "global_load_dwordx4 v[212:215], v159, s[48:49] offset:1024\n"
      "ds_read_b32 v86, v86\n"
      "ds_read_b32 v89, v89\n"
      "ds_read_b32 v90, v90\n"
      "v_lshl_add_u32 v113, v113, 2, s2\n"
      "v_mfma_f32_16x16x32_bf16 v[244:247], a[176:179], v[192:195], v[244:247]\n"
      "ds_read_b32 v94, v94\n"
      "v_lshl_add_u32 v110, v110, 2, s2\n"
      "v_mfma_f32_16x16x32_bf16 v[244:247], a[212:215], v[248:251], v[244:247]\n"
      "ds_read_b32 v91, v91\n"
      "ds_read_b32 v92, v92\n"
      "v_lshl_add_u32 v73, v73, 2, s2\n"
      "v_lshl_add_u32 v111, v111, 2, s2\n"
      "v_fma_f32 v217, v204, v204, s38\n"
      "v_lshl_add_u32 v106, v106, 2, s2\n"
      "v_fmac_f32_e64 v154, v165, v165\n"
      "ds_read_b128 v[168:171], v160 offset:10240\n"
      "global_load_dwordx4 v[8:11], v156, s[6:7] offset:-3072\n"
      "ds_read_b128 v[192:195], v157 offset:10240\n"
      "v_mfma_f32_16x16x32_bf16 v[176:179], a[208:211], v[248:251], v[176:179]\n"
      "ds_read_b32 v96, v96\n"
      "ds_read_b32 v98, v98\n"
      "v_mfma_f32_16x16x32_bf16 v[180:183], a[196:199], v[248:251], v[180:183]\n"
      "ds_read_b32 v95, v95\n"
      "ds_read_b32 v101, v101\n"
      "v_fmac_f32 v154, v166, v166\n"
      "v_lshl_add_u32 v75, v75, 2, s2\n"
      "v_fmac_f32 v154, v167, v167\n"
      "v_fmac_f32_e64 v217, v205, v205\n"
      "v_lshl_add_u32 v70, v70, 2, s2\n"
      "v_lshl_add_u32 v112, v112, 2, s2\n"
      "v_lshl_add_u32 v74, v74, 2, s2\n"
      "v_lshl_add_u32 v104, v104, 2, s2\n"
      "ds_read_b32 v97, v97\n"
      "ds_read_b32 v100, v100\n"
      "global_load_dwordx4 v[12:15], v156, s[6:7] offset:-2944\n"
      "ds_read_b32 v99, v99\n"
      "v_fmac_f32 v217, v206, v206\n"
      "v_fmac_f32 v154, v180, v180\n"
      "v_lshl_add_u32 v107, v107, 2, s2\n"
      "v_fmac_f32 v217, v207, v207\n"
      "s_waitcnt lgkmcnt(7)\n"
      "v_fmac_f32 v154, v181, v181\n"
      "v_fmac_f32 v217, v196, v196\n"
      "v_fmac_f32 v217, v197, v197\n"
      "v_mfma_f32_16x16x32_bf16 v[252:255], a[144:147], v[168:171], v[252:255]\n"
      "ds_read_b32 v103, v103\n"
      "v_fmac_f32 v217, v198, v198\n"
      "v_mfma_f32_16x16x32_bf16 v[252:255], a[180:183], v[192:195], v[252:255]\n"
      "ds_read_b32 v102, v102\n"
      "ds_read_b32 v104, v104\n"
      "v_fmac_f32_e64 v154, v182, v182\n"
      "v_mfma_f32_16x16x32_bf16 v[200:203], a[148:151], v[168:171], v[200:203]\n"
      "ds_read_b32 v105, v105\n"
      "v_fmac_f32_e64 v154, v183, v183\n"
      "v_mfma_f32_16x16x32_bf16 v[200:203], a[184:187], v[192:195], v[200:203]\n"
      "global_load_dwordx4 v[16:19], v156, s[6:7] offset:-2816\n"
      "v_lshl_add_u32 v72, v72, 2, s2\n"
      "v_mfma_f32_16x16x32_bf16 v[232:235], a[152:155], v[168:171], v[232:235]\n"
      "ds_read_b32 v106, v106\n"
      "v_fmac_f32_e64 v217, v199, v199\n"
      "v_mfma_f32_16x16x32_bf16 v[232:235], a[188:191], v[192:195], v[232:235]\n"
      "ds_read_b32 v111, v111\n"
      "ds_read_b32 v107, v107\n"
      "v_mfma_f32_16x16x32_bf16 v[140:143], a[156:159], v[168:171], v[140:143]\n"
      "ds_read_b32 v108, v108\n"
      "v_fmac_f32_e64 v154, v240, v240\n"
      "v_mfma_f32_16x16x32_bf16 v[140:143], a[192:195], v[192:195], v[140:143]\n"
      "ds_read_b32 v74, v74\n"
      "v_fmac_f32 v154, v241, v241\n"
      "v_fmac_f32 v217, v172, v172\n"
      "v_fmac_f32 v217, v173, v173\n"
      "v_fmac_f32 v217, v174, v174\n"
      "v_fmac_f32 v217, v175, v175\n"
      "v_fmac_f32 v154, v242, v242\n"
      "v_fmac_f32_e64 v154, v243, v243\n"
      "v_mfma_f32_16x16x32_bf16 v[224:227], a[160:163], v[168:171], v[224:227]\n"
      "ds_read_b32 v109, v109\n"
      "ds_read_b32 v72, v72\n"
      "ds_read_b32 v70, v70\n"
      "global_load_dwordx4 v[20:23], v156, s[6:7]\n"
      "v_permlane16_swap_b32 v217, v154\n"
      "s_waitcnt vmcnt(4)\n"
      "v_mov_b32 v135, v213\n"
      "v_add_f32 v217, v217, v154\n"
      "v_mov_b32 v154, v217\n"
      "ds_read_b32 v77, v77\n"
      "ds_read_b32 v113, v113\n"
      "v_permlane32_swap_b32 v217, v154\n"
      "v_add_f32 v217, v217, v154\n"
      "v_rsq_f32 v217, v217\n"
      "s_nop 0\n"
      "v_mov_b32 v154, v217\n"
      "v_mfma_f32_16x16x32_bf16 v[224:227], a[196:199], v[192:195], v[224:227]\n"
      "ds_read_b32 v112, v112\n"
      "v_permlane16_swap_b32 v217, v154\n"
      "v_mul_f32 v164, v154, v164\n"
      "v_mul_f32 v167, v154, v167\n"
      "v_mul_f32 v165, v154, v165\n"
      "v_mul_f32 v166, v154, v166\n"
      "v_mul_f32 v181, v154, v181\n"
      "v_mul_f32 v182, v154, v182\n"
      "v_cvt_pk_f16_f32 v164, v164, v165\n"
      "v_mul_f32 v183, v154, v183\n"
      "v_mul_f32 v242, v154, v242\n"
      "v_cvt_pk_f16_f32 v165, v166, v167\n"
      "v_cvt_pk_f16_f32 v167, v182, v183\n"
      "v_mul_f32 v241, v154, v241\n"
      "v_mfma_f32_16x16x32_bf16 v[184:187], a[164:167], v[168:171], v[184:187]\n"
      "ds_read_b32 v71, v71\n"
      "v_mul_f32 v217, s36, v217\n"
      "v_mfma_f32_16x16x32_bf16 v[184:187], a[200:203], v[192:195], v[184:187]\n"
      "global_load_dwordx4 v[24:27], v156, s[6:7] offset:128\n"
      "ds_read_b32 v110, v110\n"
      "v_mul_f32 v204, v217, v204\n"
      "v_mul_f32 v174, v217, v174\n"
      "s_waitcnt lgkmcnt(3)\n"
      "v_mul_f32 v199, v217, v199\n"
      "v_mul_f32 v173, v217, v173\n"
      "v_mul_f32 v198, v217, v198\n"
      "v_mul_f32 v206, v217, v206\n"
      "v_mul_f32 v172, v217, v172\n"
      "v_mul_f32 v243, v154, v243\n"
      "v_mul_f32 v180, v154, v180\n"
      "v_mfma_f32_16x16x32_bf16 v[228:231], a[168:171], v[168:171], v[228:231]\n"
      "ds_read_b32 v75, v75\n"
      "v_mul_f32 v205, v217, v205\n"
      "v_mfma_f32_16x16x32_bf16 v[228:231], a[204:207], v[192:195], v[228:231]\n"
      "ds_read_b32 v73, v73\n"
      "v_mul_f32 v197, v217, v197\n"
      "v_mul_f32 v207, v217, v207\n"
      "v_mul_f32 v240, v154, v240\n"
      "v_mov_b32 v134, v212\n"
      "v_cvt_pk_f16_f32 v204, v204, v205\n"
      "v_mfma_f32_16x16x32_bf16 v[236:239], a[172:175], v[168:171], v[236:239]\n"
      "global_load_dwordx4 v[28:31], v156, s[6:7] offset:256\n"
      "v_mfma_f32_16x16x32_bf16 v[220:223], a[176:179], v[168:171], v[220:223]\n"
      "ds_read_b128 v[168:171], v119 offset:36864\n"
      "v_mfma_f32_16x16x32_bf16 v[220:223], a[212:215], v[192:195], v[220:223]\n"
      "ds_read_b32 v213, v122\n"
      "s_mov_b32 exec_lo, 0xffff0000\n"
      "s_mov_b32 exec_hi, 0xffffffff\n"
      "v_mov_b32 v70, 0xf149f2ca\n"
      "v_mov_b32 v72, 0xf149f2ca\n"
      "v_mov_b32 v74, 0xf149f2ca\n"
      "s_mov_b64 exec, -1\n"
      "v_add_u32 v212, 36864, v215\n"
      "v_mul_f32 v196, v217, v196\n"
      "v_cvt_pk_f16_f32 v35, v242, v243\n"
      "v_mul_f32 v175, v217, v175\n"
      "v_cvt_pk_f16_f32 v33, v174, v175\n"
      "v_cvt_pk_f16_f32 v205, v206, v207\n"
      "v_mov_b32 v174, -1\n"
      "v_cvt_pk_f16_f32 v206, v196, v197\n"
      "v_cvt_pk_f16_f32 v207, v198, v199\n"
      "v_cvt_pk_f16_f32 v34, v240, v241\n"
      "v_add_u32 v217, 36864, v214\n"
      "s_mov_b32 exec_lo, 0x0\n"
      "s_mov_b32 exec_hi, 0xfffe0000\n"
      "v_mov_b32 v77, 0xf149f2ca\n"
      "s_mov_b64 exec, -1\n"
      "s_mov_b32 s4, 0\n"
      "ds_write_b32 v129, v174 offset:1024\n"
      "ds_write_b32 v129, v174 offset:3072\n"
      "ds_write_b32 v129, v174\n"
      "s_waitcnt lgkmcnt(3)\n"
      "v_cvt_pk_f16_f32 v32, v172, v173\n"
      "v_or3_b32 v168, v168, v169, v170\n"
      "s_mov_b32 s5, 0\n"
      "v_bitop3_b32 v213, v213, v171, s40 bitop3:0xa8\n"
      "v_and_b32 v168, s40, v168\n"
      "v_or3_b32 v168, v168, v213, s4\n"
      "v_cvt_pk_f16_f32 v166, v180, v181\n"
      "v_mfma_f32_16x16x32_bf16 v[236:239], a[208:211], v[192:195], v[236:239]\n"
      "ds_write_b32 v129, v168 offset:2048\n"
      "s_waitcnt lgkmcnt(0)\n"
      "s_barrier\n"
      "v_cvt_pk_bf16_f32 v144, v144, v145\n"
      "v_cvt_pk_bf16_f32 v145, v146, v147\n"
      "v_cvt_f16_f32 v213, s36\n"
      "v_fma_f32 v147, v252, v252, s38\n"
      "v_readfirstlane_b32 s7, v213\n"
      "v_fma_f32 v213, v140, v140, s38\n"
      "v_fmac_f32 v213, v141, v141\n"
      "v_fmac_f32 v213, v142, v142\n"
      "v_fmac_f32 v147, v253, v253\n"
      "v_fmac_f32 v147, v254, v254\n"
      "v_cvt_pk_bf16_f32 v176, v176, v177\n"
      "v_fmac_f32 v213, v143, v143\n"
      "v_fmac_f32 v147, v255, v255\n"
      "v_fmac_f32 v147, v200, v200\n"
      "v_fmac_f32 v213, v224, v224\n"
      "v_fmac_f32 v147, v201, v201\n"
      "v_fmac_f32 v147, v202, v202\n"
      "v_fmac_f32 v213, v225, v225\n"
      "v_fmac_f32 v213, v226, v226\n"
      "v_fmac_f32 v213, v227, v227\n"
      "v_fmac_f32 v147, v203, v203\n"
      "v_fmac_f32 v147, v232, v232\n"
      "v_fmac_f32 v213, v184, v184\n"
      "v_fmac_f32 v213, v185, v185\n"
      "v_fmac_f32 v147, v233, v233\n"
      "v_fmac_f32 v147, v234, v234\n"
      "v_fmac_f32 v147, v235, v235\n"
      "v_fmac_f32 v213, v186, v186\n"
      "v_fmac_f32 v213, v187, v187\n"
      "s_mov_b32 s27, 0\n"
      "v_cvt_pk_bf16_f32 v177, v178, v179\n"
      "v_permlane16_swap_b32 v147, v213\n"
      "v_add_f32 v147, v147, v213\n"
      "v_mov_b32 v213, v147\n"
      "s_add_u32 s28, s27, 1\n"
      "ds_read_b128 v[208:211], v115\n"
      "v_permlane32_swap_b32 v147, v213\n"
      "v_add_f32 v147, v147, v213\n"
      "v_rsq_f32 v147, v147\n"
      "v_cvt_pk_bf16_f32 v244, v244, v245\n"
      "v_mov_b32 v213, v147\n"
      "s_min_u32 m0, s28, s3\n"
      "v_cvt_pk_bf16_f32 v245, v246, v247\n"
      "s_movrels_b32 s9, s80\n"
      "v_permlane16_swap_b32 v147, v213\n"
      "v_mul_f32 v187, v213, v187\n"
      "v_mul_f32 v226, v213, v226\n"
      "v_mul_f32 v147, s36, v147\n"
      "v_mul_f32 v143, v213, v143\n"
      "v_mul_f32 v141, v213, v141\n"
      "v_mul_f32 v185, v213, v185\n"
      "v_mul_f32 v252, v147, v252\n"
      "s_lshr_b32 s8, s9, 8\n"
      "ds_read_b128 v[172:175], v114\n"
      "ds_write_b128 v218, v[204:207]\n"
      "ds_write_b128 v218, v[32:35] offset:16\n"
      "v_mul_f32 v140, v213, v140\n"
      "v_mul_f32 v184, v213, v184\n"
      "v_mul_f32 v225, v213, v225\n"
      "v_cvt_pk_f16_f32 v140, v140, v141\n"
      "v_mul_f32 v253, v147, v253\n"
      "v_mul_f32 v186, v213, v186\n"
      "v_mul_f32 v255, v147, v255\n"
      "v_mul_f32 v142, v213, v142\n"
      "s_and_b32 s9, s9, 0xff\n"
      "s_mul_i32 s10, s8, 73\n"
      "s_add_u32 s10, s10, s9\n"
      "ds_write_b128 v218, v[164:167] offset:32\n"
      "v_mul_f32 v254, v147, v254\n"
      "v_mul_f32 v200, v147, v200\n"
      "v_cvt_pk_f16_f32 v40, v184, v185\n"
      "v_cvt_pk_f16_f32 v141, v142, v143\n"
      "v_cvt_pk_f16_f32 v252, v252, v253\n"
      "v_mul_f32 v224, v213, v224\n"
      "v_cvt_pk_f16_f32 v142, v224, v225\n"
      "s_mul_i32 s28, s10, 4802\n"
      "v_cvt_pk_f16_f32 v253, v254, v255\n"
      "s_waitcnt lgkmcnt(2)\n"
      "v_mul_f32 v227, v213, v227\n"
      "v_cvt_pk_f16_f32 v41, v186, v187\n"
      "v_mfma_f32_16x16x32_bf16 v[180:183], a[0:3], v[172:175], 0\n"
      "ds_read_b128 v[192:195], v114 offset:6144\n"
      "v_mul_f32 v201, v147, v201\n"
      "v_mfma_f32_16x16x32_bf16 v[180:183], a[36:39], v[208:211], v[180:183]\n"
      "ds_write_b64 v219, v[144:145]\n"
      "v_mfma_f32_16x16x32_bf16 v[184:187], a[4:7], v[172:175], 0\n"
      "ds_read_b128 v[166:169], v115 offset:6144\n"
      "s_add_u32 s56, s18, s28\n"
      "v_mfma_f32_16x16x32_bf16 v[184:187], a[40:43], v[208:211], v[184:187]\n"
      "ds_write_b64 v219, v[176:177] offset:32\n"
      "v_cvt_pk_f16_f32 v143, v226, v227\n"
      "v_mfma_f32_16x16x32_bf16 v[224:227], a[8:11], v[172:175], 0\n"
      "s_addc_u32 s57, s19, 0\n"
      "s_and_b32 s56, s56, -16\n"
      "s_sub_u32 s58, s56, 110592\n"
      "v_mfma_f32_16x16x32_bf16 v[224:227], a[44:47], v[208:211], v[224:227]\n"
      "ds_write_b64 v219, v[244:245] offset:64\n"
      "v_mfma_f32_16x16x32_bf16 v[156:159], a[12:15], v[172:175], 0\n"
      "s_subb_u32 s59, s57, 0\n"
      "v_cvt_pk_f16_f32 v254, v200, v201\n"
      "v_mul_f32 v203, v147, v203\n"
      "v_mfma_f32_16x16x32_bf16 v[156:159], a[48:51], v[208:211], v[156:159]\n"
      "s_sub_u32 s56, s56, 73728\n"
      "v_mul_f32 v202, v147, v202\n"
      "v_cvt_pk_f16_f32 v255, v202, v203\n"
      "v_mfma_f32_16x16x32_bf16 v[244:247], a[16:19], v[172:175], 0\n"
      "s_subb_u32 s57, s57, 0\n"
      "v_mul_f32 v233, v147, v233\n"
      "v_mfma_f32_16x16x32_bf16 v[244:247], a[52:55], v[208:211], v[244:247]\n"
      "ds_read_b128 v[196:199], v128 offset:2048\n"
      "v_mul_f32 v234, v147, v234\n"
      "v_mfma_f32_16x16x32_bf16 v[200:203], a[20:23], v[172:175], 0\n"
      "s_mul_i32 s28, s8, 1373568\n"
      "s_mul_i32 s29, s9, 2688\n"
      "v_mul_f32 v235, v147, v235\n"
      "v_mfma_f32_16x16x32_bf16 v[200:203], a[56:59], v[208:211], v[200:203]\n"
      "ds_read_b128 v[204:207], v114 offset:12288\n"
      "v_cvt_pk_bf16_f32 v228, v228, v229\n"
      "v_mfma_f32_16x16x32_bf16 v[240:243], v[172:175], a[24:27], 0\n"
      "s_add_u32 s28, s28, s29\n"
      "v_cvt_pk_bf16_f32 v220, v220, v221\n"
      "v_cvt_pk_bf16_f32 v229, v230, v231\n"
      "v_mfma_f32_16x16x32_bf16 v[240:243], v[208:211], a[60:63], v[240:243]\n"
      "ds_read_b128 v[176:179], v115 offset:12288\n"
      "v_cvt_pk_bf16_f32 v236, v236, v237\n"
      "v_mfma_f32_16x16x32_bf16 v[160:163], v[172:175], a[28:31], 0\n"
      "s_waitcnt lgkmcnt(3)\n"
      "v_cvt_pk_bf16_f32 v237, v238, v239\n"
      "v_mul_f32 v232, v147, v232\n"
      "v_mfma_f32_16x16x32_bf16 v[160:163], v[208:211], a[64:67], v[160:163]\n"
      "s_cmp_eq_u32 s8, 72\n"
      "v_cvt_pk_bf16_f32 v221, v222, v223\n"
      "v_cvt_pk_f16_f32 v38, v232, v233\n"
      "v_mfma_f32_16x16x32_bf16 v[144:147], v[172:175], a[32:35], 0\n"
      "s_cselect_b32 s8, 0, 100270464\n"
      "s_cmp_eq_u32 s9, 72\n"
      "v_cvt_pk_f16_f32 v39, v234, v235\n"
      "v_mfma_f32_16x16x32_bf16 v[144:147], v[208:211], a[68:71], v[144:147]\n"
      "s_cselect_b32 s9, 0, 196224\n"
      "s_add_u32 s10, s8, s9\n"
      "s_waitcnt lgkmcnt(2)\n"
      "s_add_u32 s28, s28, s10\n"
      "v_mfma_f32_16x16x32_bf16 v[180:183], a[72:75], v[192:195], v[180:183]\n"
      "s_add_u32 s52, s16, s28\n"
      "s_addc_u32 s53, s17, 0\n"
      "s_cmp_lg_u32 s10, s11\n"
      "v_mfma_f32_16x16x32_bf16 v[180:183], a[108:111], v[166:169], v[180:183]\n"
      "s_cbranch_scc1 L_edge_6_%=\n"
      "L_eb_7_%=:\n"
      "v_mfma_f32_16x16x32_bf16 v[184:187], a[76:79], v[192:195], v[184:187]\n"
      "s_add_u32 m0, s76, 18432\n"
      "v_bitop3_b32 v196, v196, v197, s4 bitop3:0x7e\n"
      "v_bitop3_b32 v198, v198, v199, s4 bitop3:0x7e\n"
      "v_mfma_f32_16x16x32_bf16 v[184:187], a[112:115], v[166:169], v[184:187]\n"
      "s_nop 0\n"
      "v_or_b32 v196, v196, v198\n"
      "v_cmp_ne_u32 vcc, 0, v196\n"
      "v_mfma_f32_16x16x32_bf16 v[224:227], a[80:83], v[192:195], v[224:227]\n"
      "global_load_lds_dwordx4 v116, s[52:53]\n"
      "v_mfma_f32_16x16x32_bf16 v[224:227], a[116:119], v[166:169], v[224:227]\n"
      "s_mov_b32 exec_lo, 0x003f003f\n"
      "s_mov_b32 exec_hi, 0x003f003f\n"
      "ds_write_b128 v218, v[252:255] offset:18688\n"
      "ds_write_b128 v218, v[38:41] offset:18704\n"
      "ds_write_b128 v218, v[140:143] offset:18720\n"
      "ds_write_b64 v219, v[228:229] offset:18688\n"
      "ds_write_b64 v219, v[236:237] offset:18720\n"
      "ds_write_b64 v219, v[220:221] offset:18752\n"
      "s_mov_b64 exec, -1\n"
      "v_mfma_f32_16x16x32_bf16 v[156:159], a[84:87], v[192:195], v[156:159]\n"
      "s_mov_b64 s[48:49], vcc\n"
      "s_add_u32 m0, s76, 24448\n"
      "v_mfma_f32_16x16x32_bf16 v[156:159], a[120:123], v[166:169], v[156:159]\n"
      "v_mfma_f32_16x16x32_bf16 v[244:247], a[88:91], v[192:195], v[244:247]\n"
      "v_mfma_f32_16x16x32_bf16 v[244:247], a[124:127], v[166:169], v[244:247]\n"
      "global_load_lds_dwordx4 v116, s[52:53] offset:128\n"
      "v_mfma_f32_16x16x32_bf16 v[200:203], a[92:95], v[192:195], v[200:203]\n"
      "v_mfma_f32_16x16x32_bf16 v[200:203], a[128:131], v[166:169], v[200:203]\n"
      "s_cbranch_vccnz L_flg_3_%=\n"
      "L_fb_4_%=:\n"
      "v_mfma_f32_16x16x32_bf16 v[196:199], v[188:191], a[216:219], 0\n"
      "v_mfma_f32_16x16x32_bf16 v[196:199], v[188:191], a[220:223], v[196:199]\n"
      "s_nop 0\n"
      "v_mfma_f32_16x16x32_bf16 v[196:199], v[188:191], a[224:227], v[196:199]\n"
      "v_mfma_f32_16x16x32_bf16 v[240:243], v[192:195], a[96:99], v[240:243]\n"
      "s_add_u32 m0, s76, 30464\n"
      "v_mfma_f32_16x16x32_bf16 v[240:243], v[166:169], a[132:135], v[240:243]\n"
      "global_load_lds_dwordx4 v116, s[52:53] offset:256\n"
      "v_mfma_f32_16x16x32_bf16 v[160:163], v[192:195], a[100:103], v[160:163]\n"
      "s_nop 0\n"
      "v_mfma_f32_16x16x32_bf16 v[160:163], v[166:169], a[136:139], v[160:163]\n"
      "s_waitcnt lgkmcnt(0)\n"
      "ds_read_b128 v[232:235], v114 offset:2048\n"
      "v_mfma_f32_16x16x32_bf16 v[144:147], v[192:195], a[104:107], v[144:147]\n"
      "v_add_f32_dpp v196, v196, v196 quad_perm:[1,0,3,2] row_mask:0xf bank_mask:0xf\n"
      "v_mfma_f32_16x16x32_bf16 v[144:147], v[166:169], a[140:143], v[144:147]\n"
      "s_nop 0\n"
      "v_add_f32_dpp v196, v196, v196 quad_perm:[2,3,0,1] row_mask:0xf bank_mask:0xf\n"
      "v_mfma_f32_16x16x32_bf16 v[180:183], a[144:147], v[204:207], v[180:183]\n"
      "s_nop 0\n"
      "v_add_f32_dpp v196, v196, v196 row_half_mirror row_mask:0xf bank_mask:0xf\n"
      "v_mul_f32 v197, 0xbbaaaaab, v196\n"
      "v_mfma_f32_16x16x32_bf16 v[180:183], a[180:183], v[176:179], v[180:183]\n"
      "v_mfma_f32_16x16x32_bf16 v[184:187], a[148:151], v[204:207], v[184:187]\n"
      "s_add_u32 m0, s77, 18432\n"
      "v_mul_f32 v198, 0xbbaaaaab, v196\n"
      "v_mfma_f32_16x16x32_bf16 v[184:187], a[184:187], v[176:179], v[184:187]\n"
      "v_mfma_f32_16x16x32_bf16 v[224:227], a[152:155], v[204:207], v[224:227]\n"
      "v_mul_f32 v199, 0xbbaaaaab, v196\n"
      "v_mul_f32 v196, 0xbbaaaaab, v196\n"
      "v_mfma_f32_16x16x32_bf16 v[224:227], a[188:191], v[176:179], v[224:227]\n"
      "ds_read_b128 v[164:167], v115 offset:2048\n"
      "v_mfma_f32_16x16x32_bf16 v[220:223], v[150:153], a[220:223], v[196:199]\n"
      "v_mfma_f32_16x16x32_bf16 v[236:239], v[148:151], a[224:227], v[196:199]\n"
      "v_cvt_pk_f16_f32 v180, v180, v181\n"
      "v_mfma_f32_16x16x32_bf16 v[172:175], v[150:153], a[216:219], v[196:199]\n"
      "v_pk_mul_f16 v219, v180, v180\n"
      "v_mfma_f32_16x16x32_bf16 v[192:195], v[150:153], a[224:227], v[196:199]\n"
      "global_load_lds_dwordx4 v117, s[52:53]\n"
      "v_mfma_f32_16x16x32_bf16 v[208:211], v[148:151], a[220:223], v[196:199]\n"
      "s_nop 0\n"
      "v_cvt_pk_bf16_f32 v220, v220, v221\n"
      "v_mfma_f32_16x16x32_bf16 v[252:255], v[148:151], a[216:219], v[196:199]\n"
      "s_nop 0\n"
      "v_cvt_pk_bf16_f32 v172, v172, v173\n"
      "v_cvt_pk_bf16_f32 v221, v222, v223\n"
      "v_mfma_f32_16x16x32_bf16 v[168:171], v[188:191], a[228:231], 0\n"
      "v_cvt_pk_bf16_f32 v192, v192, v193\n"
      "v_cvt_pk_bf16_f32 v173, v174, v175\n"
      "v_mfma_f32_16x16x32_bf16 v[168:171], v[188:191], a[232:235], v[168:171]\n"
      "s_nop 0\n"
      "v_cvt_pk_bf16_f32 v175, v254, v255\n"
      "v_cvt_pk_bf16_f32 v174, v252, v253\n"
      "v_mfma_f32_16x16x32_bf16 v[168:171], v[188:191], a[236:239], v[168:171]\n"
      "ds_write_b128 v216, v[172:175]\n"
      "v_mfma_f32_16x16x32_bf16 v[156:159], a[156:159], v[204:207], v[156:159]\n"
      "v_mfma_f32_16x16x32_bf16 v[156:159], a[192:195], v[176:179], v[156:159]\n"
      "s_waitcnt lgkmcnt(1)\n"
      "v_cvt_pk_bf16_f32 v193, v194, v195\n"
      "v_cvt_pk_bf16_f32 v195, v238, v239\n"
      "v_mfma_f32_16x16x32_bf16 v[244:247], a[160:163], v[204:207], v[244:247]\n"
      "v_cvt_pk_bf16_f32 v222, v208, v209\n"
      "v_cvt_pk_bf16_f32 v223, v210, v211\n"
      "v_mfma_f32_16x16x32_bf16 v[244:247], a[196:199], v[176:179], v[244:247]\n"
      "ds_write_b128 v216, v[220:223] offset:128\n"
      "v_mfma_f32_16x16x32_bf16 v[200:203], a[164:167], v[204:207], v[200:203]\n"
      "v_add_f32_dpp v168, v168, v168 quad_perm:[1,0,3,2] row_mask:0xf bank_mask:0xf\n"
      "v_cvt_pk_bf16_f32 v194, v236, v237\n"
      "v_mfma_f32_16x16x32_bf16 v[200:203], a[200:203], v[176:179], v[200:203]\n"
      "ds_write_b128 v216, v[192:195] offset:256\n"
      "v_mfma_f32_16x16x32_bf16 v[240:243], v[204:207], a[168:171], v[240:243]\n"
      "v_add_f32_dpp v168, v168, v168 quad_perm:[2,3,0,1] row_mask:0xf bank_mask:0xf\n"
      "v_cvt_pk_f16_f32 v156, v156, v157\n"
      "v_mfma_f32_16x16x32_bf16 v[240:243], v[176:179], a[204:207], v[240:243]\n"
      "v_add_f32_dpp v168, v168, v168 row_half_mirror row_mask:0xf bank_mask:0xf\n"
      "v_mul_f32 v169, 0xbbaaaaab, v168\n"
      "v_mfma_f32_16x16x32_bf16 v[160:163], v[204:207], a[172:175], v[160:163]\n"
      "v_mul_f32 v170, 0xbbaaaaab, v168\n"
      "v_mul_f32 v171, 0xbbaaaaab, v168\n"
      "v_mfma_f32_16x16x32_bf16 v[160:163], v[176:179], a[208:211], v[160:163]\n"
      "v_mul_f32 v168, 0xbbaaaaab, v168\n"
      "v_pk_mul_f16 v154, v156, v156\n"
      "v_mfma_f32_16x16x32_bf16 v[144:147], v[204:207], a[176:179], v[144:147]\n"
      "v_cvt_pk_f16_f32 v181, v182, v183\n"
      "v_pk_fma_f16 v219, v181, v181, v219\n"
      "v_mfma_f32_16x16x32_bf16 v[144:147], v[176:179], a[212:215], v[144:147]\n"
      "v_cvt_pk_f16_f32 v182, v184, v185\n"
      "v_cvt_pk_f16_f32 v157, v158, v159\n"
      "v_mfma_f32_16x16x32_bf16 v[176:179], v[148:151], a[228:231], v[168:171]\n"
      "v_pk_fma_f16 v154, v157, v157, v154\n"
      "v_pk_fma_f16 v219, v182, v182, v219\n"
      "v_mfma_f32_16x16x32_bf16 v[204:207], v[150:153], a[228:231], v[168:171]\n"
      "v_cvt_pk_f16_f32 v158, v244, v245\n"
      "v_pk_fma_f16 v154, v158, v158, v154\n"
      "v_mfma_f32_16x16x32_bf16 v[252:255], v[150:153], a[232:235], v[168:171]\n"
      "v_cvt_pk_f16_f32 v159, v246, v247\n"
      "v_cvt_pk_f16_f32 v183, v186, v187\n"
      "v_mfma_f32_16x16x32_bf16 v[228:231], v[148:151], a[232:235], v[168:171]\n"
      "v_pk_fma_f16 v219, v183, v183, v219\n"
      "v_pk_fma_f16 v154, v159, v159, v154\n"
      "v_mfma_f32_16x16x32_bf16 v[192:195], v[150:153], a[236:239], v[168:171]\n"
      "v_cvt_pk_bf16_f32 v204, v204, v205\n"
      "v_cvt_pk_bf16_f32 v205, v206, v207\n"
      "v_mfma_f32_16x16x32_bf16 v[136:139], v[148:151], a[236:239], v[168:171]\n"
      "v_cvt_pk_bf16_f32 v206, v176, v177\n"
      "v_cvt_pk_bf16_f32 v207, v178, v179\n"
      "v_mfma_f32_16x16x32_bf16 v[176:179], a[0:3], v[232:235], 0\n"
      "ds_read_b128 v[196:199], v114 offset:8192\n"
      "v_cvt_pk_bf16_f32 v252, v252, v253\n"
      "v_mfma_f32_16x16x32_bf16 v[176:179], a[36:39], v[164:167], v[176:179]\n"
      "ds_write_b128 v216, v[204:207] offset:3072\n"
      "v_mfma_f32_16x16x32_bf16 v[184:187], a[4:7], v[232:235], 0\n"
      "ds_read_b128 v[204:207], v115 offset:8192\n"
      "v_cvt_pk_bf16_f32 v253, v254, v255\n"
      "v_mfma_f32_16x16x32_bf16 v[184:187], a[40:43], v[164:167], v[184:187]\n"
      "v_cvt_pk_bf16_f32 v255, v230, v231\n"
      "v_cvt_pk_bf16_f32 v254, v228, v229\n"
      "v_mfma_f32_16x16x32_bf16 v[140:143], a[8:11], v[232:235], 0\n"
      "v_cvt_pk_bf16_f32 v192, v192, v193\n"
      "v_cvt_pk_bf16_f32 v193, v194, v195\n"
      "v_mfma_f32_16x16x32_bf16 v[140:143], a[44:47], v[164:167], v[140:143]\n"
      "ds_write_b128 v216, v[252:255] offset:3200\n"
      "v_mfma_f32_16x16x32_bf16 v[228:231], a[12:15], v[232:235], 0\n"
      "v_cvt_pk_bf16_f32 v195, v138, v139\n"
      "v_cvt_pk_bf16_f32 v194, v136, v137\n"
      "v_mfma_f32_16x16x32_bf16 v[228:231], a[48:51], v[164:167], v[228:231]\n"
      "ds_write_b128 v216, v[192:195] offset:3328\n"
      "v_mfma_f32_16x16x32_bf16 v[244:247], a[16:19], v[232:235], 0\n"
      "v_cvt_pk_f16_f32 v32, v224, v225\n"
      "v_pk_fma_f16 v219, v32, v32, v219\n"
      "v_mfma_f32_16x16x32_bf16 v[244:247], a[52:55], v[164:167], v[244:247]\n"
      "v_cvt_pk_f16_f32 v34, v200, v201\n"
      "v_pk_fma_f16 v154, v34, v34, v154\n"
      "v_mfma_f32_16x16x32_bf16 v[136:139], a[20:23], v[232:235], 0\n"
      "v_cvt_pk_f16_f32 v33, v226, v227\n"
      "v_pk_fma_f16 v219, v33, v33, v219\n"
      "v_mfma_f32_16x16x32_bf16 v[136:139], a[56:59], v[164:167], v[136:139]\n"
      "v_cvt_pk_f16_f32 v35, v202, v203\n"
      "v_pk_fma_f16 v154, v35, v35, v154\n"
      "v_mfma_f32_16x16x32_bf16 v[220:223], v[232:235], a[24:27], 0\n"
      "s_nop 0\n"
      "v_permlane16_swap_b32 v219, v154\n"
      "v_mfma_f32_16x16x32_bf16 v[220:223], v[164:167], a[60:63], v[220:223]\n"
      "ds_read_b128 v[208:211], v114 offset:14336\n"
      "v_pk_add_f16 v219, v219, v154\n"
      "v_mfma_f32_16x16x32_bf16 v[224:227], v[232:235], a[28:31], 0\n"
      "s_waitcnt lgkmcnt(2)\n"
      "v_mov_b32 v154, v219\n"
      "v_mfma_f32_16x16x32_bf16 v[224:227], v[164:167], a[64:67], v[224:227]\n"
      "s_nop 0\n"
      "v_permlane32_swap_b32 v219, v154\n"
      "v_mfma_f32_16x16x32_bf16 v[172:175], v[232:235], a[32:35], 0\n"
      "v_pk_add_f16 v219, v219, v154\n"
      "v_add_f16_sdwa v154, v219, v219 dst_sel:DWORD dst_unused:UNUSED_PAD src0_sel:WORD_0 src1_sel:WORD_1\n"
      "v_mfma_f32_16x16x32_bf16 v[172:175], v[164:167], a[68:71], v[172:175]\n"
      "s_nop 0\n"
      "v_rsq_f16 v219, v154\n"
      "v_mfma_f32_16x16x32_bf16 v[176:179], a[72:75], v[196:199], v[176:179]\n"
      "ds_read_b128 v[236:239], v115 offset:14336\n"
      "v_cmp_nle_f16 vcc, 0x1c00, v154\n"
      "v_mfma_f32_16x16x32_bf16 v[176:179], a[108:111], v[204:207], v[176:179]\n"
      "s_mov_b64 s[34:35], vcc\n"
      "v_cmp_nge_f16 vcc, 0x7b00, v154\n"
      "v_mov_b32 v154, v219\n"
      "v_mfma_f32_16x16x32_bf16 v[200:203], v[188:191], a[240:243], 0\n"
      "v_mfma_f32_16x16x32_bf16 v[200:203], v[188:191], a[244:247], v[200:203]\n"
      "v_permlane16_swap_b32_e64 v219, v154\n"
      "v_mfma_f32_16x16x32_bf16 v[200:203], v[188:191], a[248:251], v[200:203]\n"
      "s_nop 0\n"
      "v_pk_mul_f16 v156, v156, v154 op_sel_hi:[1,0]\n"
      "v_mfma_f32_16x16x32_bf16 v[184:187], a[76:79], v[196:199], v[184:187]\n"
      "s_or_b64 s[34:35], s[34:35], vcc\n"
      "s_add_u32 m0, s78, 18432\n"
      "v_pk_mul_f16 v158, v158, v154 op_sel_hi:[1,0]\n"
      "v_mfma_f32_16x16x32_bf16 v[184:187], a[112:115], v[204:207], v[184:187]\n"
      "s_mov_b32 exec_hi, 0\n"
      "global_load_lds_dwordx4 v118, s[52:53]\n"
      "s_mov_b32 exec_hi, -1\n"
      "v_mfma_f32_16x16x32_bf16 v[140:143], a[80:83], v[196:199], v[140:143]\n"
      "v_mfma_f32_16x16x32_bf16 v[140:143], a[116:119], v[204:207], v[140:143]\n"
      "v_mfma_f32_16x16x32_bf16 v[228:231], a[84:87], v[196:199], v[228:231]\n"
      "v_add_f32_dpp v200, v200, v200 quad_perm:[1,0,3,2] row_mask:0xf bank_mask:0xf\n"
      "v_mfma_f32_16x16x32_bf16 v[228:231], a[120:123], v[204:207], v[228:231]\n"
      "s_nop 0\n"
      "v_add_f32_dpp v200, v200, v200 quad_perm:[2,3,0,1] row_mask:0xf bank_mask:0xf\n"
      "v_mul_f16 v219, s7, v219\n"
      "v_mfma_f32_16x16x32_bf16 v[244:247], a[88:91], v[196:199], v[244:247]\n"
      "s_nop 0\n"
      "v_add_f32_dpp v200, v200, v200 row_half_mirror row_mask:0xf bank_mask:0xf\n"
      "v_pk_mul_f16 v35, v35, v154 op_sel_hi:[1,0]\n"
      "v_mfma_f32_16x16x32_bf16 v[244:247], a[124:127], v[204:207], v[244:247]\n"
      "s_waitcnt lgkmcnt(0)\n"
      "v_mul_f32 v203, 0xbbaaaaab, v200\n"
      "v_mfma_f32_16x16x32_bf16 v[136:139], a[92:95], v[196:199], v[136:139]\n"
      "s_add_u32 m0, s76, 115456\n"
      "v_mul_f32 v202, 0xbbaaaaab, v200\n"
      "v_mul_f32 v201, 0xbbaaaaab, v200\n"
      "v_mfma_f32_16x16x32_bf16 v[136:139], a[128:131], v[204:207], v[136:139]\n"
      "v_pk_mul_f16 v159, v159, v154 op_sel_hi:[1,0]\n"
      "v_mul_f32 v200, 0xbbaaaaab, v200\n"
      "v_mfma_f32_16x16x32_bf16 v[220:223], v[196:199], a[96:99], v[220:223]\n"
      "v_pk_mul_f16 v157, v157, v154 op_sel_hi:[1,0]\n"
      "v_pk_mul_f16 v183, v183, v219 op_sel_hi:[1,0]\n"
      "v_mfma_f32_16x16x32_bf16 v[220:223], v[204:207], a[132:135], v[220:223]\n"
      "global_load_lds_dwordx4 v119, s[56:57]\n"
      "v_mfma_f32_16x16x32_bf16 v[192:195], v[150:153], a[240:243], v[200:203]\n"
      "v_mfma_f32_16x16x32_bf16 v[224:227], v[196:199], a[100:103], v[224:227]\n"
      "v_pk_mul_f16 v33, v33, v219 op_sel_hi:[1,0]\n"
      "v_pk_mul_f16 v32, v32, v219 op_sel_hi:[1,0]\n"
      "v_mfma_f32_16x16x32_bf16 v[224:227], v[204:207], a[136:139], v[224:227]\n"
      "v_pk_mul_f16 v34, v34, v154 op_sel_hi:[1,0]\n"
      "v_pk_mul_f16 v180, v180, v219 op_sel_hi:[1,0]\n"
      "v_mfma_f32_16x16x32_bf16 v[252:255], v[148:151], a[240:243], v[200:203]\n"
      "s_nop 0\n"
      "v_cvt_pk_bf16_f32 v192, v192, v193\n"
      "v_mfma_f32_16x16x32_bf16 v[168:171], v[150:153], a[244:247], v[200:203]\n"
      "v_cvt_pk_bf16_f32 v193, v194, v195\n"
      "v_pk_mul_f16 v182, v182, v219 op_sel_hi:[1,0]\n"
      "v_mfma_f32_16x16x32_bf16 v[172:175], v[196:199], a[104:107], v[172:175]\n"
      "s_nop 1\n"
      "v_cvt_pk_bf16_f32 v195, v254, v255\n"
      "v_mfma_f32_16x16x32_bf16 v[172:175], v[204:207], a[140:143], v[172:175]\n"
      "s_add_u32 m0, s79, 4864\n"
      "v_mfma_f32_16x16x32_bf16 v[204:207], v[150:153], a[248:251], v[200:203]\n"
      "v_pk_mul_f16 v181, v181, v219 op_sel_hi:[1,0]\n"
      "v_cvt_pk_bf16_f32 v194, v252, v253\n"
      "v_mfma_f32_16x16x32_bf16 v[248:251], v[148:151], a[244:247], v[200:203]\n"
      "global_load_lds_dword v122, s[58:59]\n"
      "v_mfma_f32_16x16x32_bf16 v[176:179], a[144:147], v[208:211], v[176:179]\n"
      "ds_read_b128 v[164:167], v114 offset:4096\n"
      "v_cvt_pk_bf16_f32 v168, v168, v169\n"
      "v_mfma_f32_16x16x32_bf16 v[176:179], a[180:183], v[236:239], v[176:179]\n"
      "ds_write_b128 v216, v[192:195] offset:6144\n"
      "v_mfma_f32_16x16x32_bf16 v[252:255], v[148:151], a[248:251], v[200:203]\n"
      "v_cvt_pk_bf16_f32 v204, v204, v205\n"
      "v_cvt_pk_bf16_f32 v169, v170, v171\n"
      "v_mfma_f32_16x16x32_bf16 v[184:187], a[148:151], v[208:211], v[184:187]\n"
      "v_cvt_pk_bf16_f32 v171, v250, v251\n"
      "v_cvt_pk_bf16_f32 v170, v248, v249\n"
      "v_mfma_f32_16x16x32_bf16 v[184:187], a[184:187], v[236:239], v[184:187]\n"
      "ds_write_b128 v216, v[168:171] offset:6272\n"
      "v_mfma_f32_16x16x32_bf16 v[248:251], v[188:191], a[252:255], 0\n"
      "ds_read_b128 v[200:203], v115 offset:4096\n"
      "v_mfma_f32_16x16x32_bf16 v[248:251], v[188:191], v[0:3], v[248:251]\n"
      "v_cvt_pk_bf16_f32 v205, v206, v207\n"
      "v_cvt_pk_bf16_f32 v206, v252, v253\n"
      "v_mfma_f32_16x16x32_bf16 v[248:251], v[188:191], v[4:7], v[248:251]\n"
      "v_cvt_pk_bf16_f32 v240, v240, v241\n"
      "v_cvt_pk_bf16_f32 v160, v160, v161\n"
      "v_mfma_f32_16x16x32_bf16 v[140:143], a[152:155], v[208:211], v[140:143]\n"
      "v_cvt_pk_bf16_f32 v207, v254, v255\n"
      "v_cvt_pk_bf16_f32 v144, v144, v145\n"
      "v_mfma_f32_16x16x32_bf16 v[140:143], a[188:191], v[236:239], v[140:143]\n"
      "ds_write_b128 v216, v[204:207] offset:6400\n"
      "v_mfma_f32_16x16x32_bf16 v[228:231], a[156:159], v[208:211], v[228:231]\n"
      "v_cvt_pk_bf16_f32 v161, v162, v163\n"
      "v_add_f32_dpp v248, v248, v248 quad_perm:[1,0,3,2] row_mask:0xf bank_mask:0xf\n"
      "v_mfma_f32_16x16x32_bf16 v[228:231], a[192:195], v[236:239], v[228:231]\n"
      "v_cvt_pk_bf16_f32 v241, v242, v243\n"
      "v_add_f32_dpp v248, v248, v248 quad_perm:[2,3,0,1] row_mask:0xf bank_mask:0xf\n"
      "v_mfma_f32_16x16x32_bf16 v[244:247], a[160:163], v[208:211], v[244:247]\n"
      "s_nop 0\n"
      "v_add_f32_dpp v248, v248, v248 row_half_mirror row_mask:0xf bank_mask:0xf\n"
      "v_cvt_pk_bf16_f32 v145, v146, v147\n"
      "v_mfma_f32_16x16x32_bf16 v[244:247], a[196:199], v[236:239], v[244:247]\n"
      "s_nop 0\n"
      "v_mul_f32 v251, 0xbbaaaaab, v248\n"
      "v_mul_f32 v249, 0xbbaaaaab, v248\n"
      "v_mfma_f32_16x16x32_bf16 v[136:139], a[164:167], v[208:211], v[136:139]\n"
      "v_mul_f32 v250, 0xbbaaaaab, v248\n"
      "v_mul_f32 v248, 0xbbaaaaab, v248\n"
      "v_mfma_f32_16x16x32_bf16 v[136:139], a[200:203], v[236:239], v[136:139]\n"
      "s_nop 0\n"
      "v_cvt_pk_f16_f32 v176, v176, v177\n"
      "s_waitcnt lgkmcnt(1)\n"
      "v_mfma_f32_16x16x32_bf16 v[232:235], v[150:153], a[252:255], v[248:251]\n"
      "v_cvt_pk_f16_f32 v228, v228, v229\n"
      "v_pk_mul_f16 v218, v228, v228\n"
      "v_mfma_f32_16x16x32_bf16 v[168:171], v[148:151], a[252:255], v[248:251]\n"
      "v_pk_mul_f16 v213, v176, v176\n"
      "v_cvt_pk_f16_f32 v177, v178, v179\n"
      "v_mfma_f32_16x16x32_bf16 v[220:223], v[208:211], a[168:171], v[220:223]\n"
      "v_pk_fma_f16 v213, v177, v177, v213\n"
      "v_cvt_pk_f16_f32 v229, v230, v231\n"
      "v_mfma_f32_16x16x32_bf16 v[220:223], v[236:239], a[204:207], v[220:223]\n"
      "v_cvt_pk_bf16_f32 v232, v232, v233\n"
      "v_cvt_pk_bf16_f32 v233, v234, v235\n"
      "v_mfma_f32_16x16x32_bf16 v[224:227], v[208:211], a[172:175], v[224:227]\n"
      "v_cvt_pk_bf16_f32 v235, v170, v171\n"
      "v_cvt_pk_bf16_f32 v234, v168, v169\n"
      "v_mfma_f32_16x16x32_bf16 v[224:227], v[236:239], a[208:211], v[224:227]\n"
      "ds_write_b128 v216, v[232:235] offset:9216\n"
      "v_mfma_f32_16x16x32_bf16 v[252:255], v[150:153], v[0:3], v[248:251]\n"
      "v_cvt_pk_bf16_f32 v242, v220, v221\n"
      "v_cvt_pk_bf16_f32 v243, v222, v223\n"
      "v_mfma_f32_16x16x32_bf16 v[196:199], v[148:151], v[0:3], v[248:251]\n"
      "v_pk_fma_f16 v218, v229, v229, v218\n"
      "v_cvt_pk_f16_f32 v178, v184, v185\n"
      "v_mfma_f32_16x16x32_bf16 v[220:223], v[150:153], v[4:7], v[248:251]\n"
      "v_cvt_pk_bf16_f32 v163, v226, v227\n"
      "v_cvt_pk_bf16_f32 v162, v224, v225\n"
      "v_mfma_f32_16x16x32_bf16 v[224:227], v[148:151], v[4:7], v[248:251]\n"
      "v_cvt_pk_bf16_f32 v252, v252, v253\n"
      "v_cvt_pk_bf16_f32 v253, v254, v255\n"
      "v_mfma_f32_16x16x32_bf16 v[172:175], v[208:211], a[176:179], v[172:175]\n"
      "ds_read_b128 v[232:235], v114 offset:10240\n"
      "v_cvt_pk_bf16_f32 v254, v196, v197\n"
      "v_mfma_f32_16x16x32_bf16 v[172:175], v[236:239], a[212:215], v[172:175]\n"
      "v_cvt_pk_bf16_f32 v255, v198, v199\n"
      "v_cvt_pk_bf16_f32 v220, v220, v221\n"
      "v_mfma_f32_16x16x32_bf16 v[168:171], a[0:3], v[164:167], 0\n"
      "ds_read_b128 v[204:207], v115 offset:10240\n"
      "v_cvt_pk_bf16_f32 v221, v222, v223\n"
      "v_mfma_f32_16x16x32_bf16 v[168:171], a[36:39], v[200:203], v[168:171]\n"
      "ds_write_b128 v216, v[252:255] offset:9344\n"
      "v_mfma_f32_16x16x32_bf16 v[196:199], a[4:7], v[164:167], 0\n"
      "v_cvt_pk_bf16_f32 v223, v226, v227\n"
      "v_cvt_pk_bf16_f32 v222, v224, v225\n"
      "v_mfma_f32_16x16x32_bf16 v[196:199], a[40:43], v[200:203], v[196:199]\n"
      "ds_write_b128 v216, v[220:223] offset:9472\n"
      "v_mfma_f32_16x16x32_bf16 v[220:223], a[8:11], v[164:167], 0\n"
      "v_cvt_pk_bf16_f32 v147, v174, v175\n"
      "v_cvt_pk_f16_f32 v230, v244, v245\n"
      "v_mfma_f32_16x16x32_bf16 v[220:223], a[44:47], v[200:203], v[220:223]\n"
      "v_pk_fma_f16 v213, v178, v178, v213\n"
      "v_cvt_pk_f16_f32 v179, v186, v187\n"
      "v_mfma_f32_16x16x32_bf16 v[224:227], a[12:15], v[164:167], 0\n"
      "v_pk_fma_f16 v218, v230, v230, v218\n"
      "v_pk_fma_f16 v213, v179, v179, v213\n"
      "v_mfma_f32_16x16x32_bf16 v[224:227], a[48:51], v[200:203], v[224:227]\n"
      "v_cvt_pk_f16_f32 v39, v142, v143\n"
      "v_cvt_pk_f16_f32 v231, v246, v247\n"
      "v_mfma_f32_16x16x32_bf16 v[236:239], a[16:19], v[164:167], 0\n"
      "v_pk_fma_f16 v218, v231, v231, v218\n"
      "v_cvt_pk_f16_f32 v38, v140, v141\n"
      "v_mfma_f32_16x16x32_bf16 v[236:239], a[52:55], v[200:203], v[236:239]\n"
      "v_cvt_pk_f16_f32 v40, v136, v137\n"
      "v_pk_fma_f16 v213, v38, v38, v213\n"
      "v_mfma_f32_16x16x32_bf16 v[248:251], a[20:23], v[164:167], 0\n"
      "v_pk_fma_f16 v218, v40, v40, v218\n"
      "v_pk_fma_f16 v213, v39, v39, v213\n"
      "v_mfma_f32_16x16x32_bf16 v[248:251], a[56:59], v[200:203], v[248:251]\n"
      "v_cvt_pk_f16_f32 v41, v138, v139\n"
      "v_cvt_pk_bf16_f32 v146, v172, v173\n"
      "v_mfma_f32_16x16x32_bf16 v[136:139], v[164:167], a[24:27], 0\n"
      "v_pk_fma_f16 v218, v41, v41, v218\n"
      "v_mfma_f32_16x16x32_bf16 v[136:139], v[200:203], a[60:63], v[136:139]\n"
      "s_nop 0\n"
      "v_permlane16_swap_b32_e64 v213, v218\n"
      "v_mfma_f32_16x16x32_bf16 v[192:195], v[164:167], a[28:31], 0\n"
      "s_waitcnt lgkmcnt(0)\n"
      "v_pk_add_f16 v213, v213, v218\n"
      "v_mov_b32 v218, v213\n"
      "v_mfma_f32_16x16x32_bf16 v[192:195], v[200:203], a[64:67], v[192:195]\n"
      "s_nop 0\n"
      "v_permlane32_swap_b32_e64 v213, v218\n"
      "v_mfma_f32_16x16x32_bf16 v[140:143], v[164:167], a[32:35], 0\n"
      "v_pk_add_f16 v213, v213, v218\n"
      "v_add_f16_sdwa v218, v213, v213 dst_sel:DWORD dst_unused:UNUSED_PAD src0_sel:WORD_0 src1_sel:WORD_1\n"
      "v_mfma_f32_16x16x32_bf16 v[140:143], v[200:203], a[68:71], v[140:143]\n"
      "s_nop 0\n"
      "v_rsq_f16 v213, v218\n"
      "v_mfma_f32_16x16x32_bf16 v[168:171], a[72:75], v[232:235], v[168:171]\n"
      "s_nop 0\n"
      "v_cmp_nle_f16 vcc, 0x1c00, v218\n"
      "v_mfma_f32_16x16x32_bf16 v[168:171], a[108:111], v[204:207], v[168:171]\n"
      "s_or_b64 s[34:35], s[34:35], vcc\n"
      "v_cmp_nge_f16 vcc, 0x7b00, v218\n"
      "v_mov_b32 v218, v213\n"
      "v_mfma_f32_16x16x32_bf16 v[196:199], a[76:79], v[232:235], v[196:199]\n"
      "s_nop 0\n"
      "v_permlane16_swap_b32_e64 v213, v218\n"
      "v_mfma_f32_16x16x32_bf16 v[196:199], a[112:115], v[204:207], v[196:199]\n"
      "s_waitcnt vmcnt(12)\n"
      "v_mfma_f32_16x16x32_bf16 v[208:211], v[188:191], v[8:11], 0\n"
      "v_pk_mul_f16 v229, v229, v218 op_sel_hi:[1,0]\n"
      "v_mul_f16 v213, s7, v213\n"
      "s_waitcnt vmcnt(11)\n"
      "v_mfma_f32_16x16x32_bf16 v[208:211], v[188:191], v[12:15], v[208:211]\n"
      "s_waitcnt vmcnt(10)\n"
      "v_mfma_f32_16x16x32_bf16 v[208:211], v[188:191], v[16:19], v[208:211]\n"
      "v_pk_mul_f16 v177, v177, v213 op_sel_hi:[1,0]\n"
      "v_pk_mul_f16 v230, v230, v218 op_sel_hi:[1,0]\n"
      "v_mfma_f32_16x16x32_bf16 v[220:223], a[80:83], v[232:235], v[220:223]\n"
      "v_pk_mul_f16 v39, v39, v213 op_sel_hi:[1,0]\n"
      "v_pk_mul_f16 v179, v179, v213 op_sel_hi:[1,0]\n"
      "v_mfma_f32_16x16x32_bf16 v[220:223], a[116:119], v[204:207], v[220:223]\n"
      "s_nop 1\n"
      "v_add_f32_dpp v208, v208, v208 quad_perm:[1,0,3,2] row_mask:0xf bank_mask:0xf\n"
      "v_mfma_f32_16x16x32_bf16 v[224:227], a[84:87], v[232:235], v[224:227]\n"
      "v_pk_mul_f16 v40, v40, v218 op_sel_hi:[1,0]\n"
      "v_add_f32_dpp v208, v208, v208 quad_perm:[2,3,0,1] row_mask:0xf bank_mask:0xf\n"
      "v_mfma_f32_16x16x32_bf16 v[224:227], a[120:123], v[204:207], v[224:227]\n"
      "s_nop 0\n"
      "v_add_f32_dpp v208, v208, v208 row_half_mirror row_mask:0xf bank_mask:0xf\n"
      "v_mfma_f32_16x16x32_bf16 v[236:239], a[88:91], v[232:235], v[236:239]\n"
      "v_mfma_f32_16x16x32_bf16 v[236:239], a[124:127], v[204:207], v[236:239]\n"
      "v_mul_f32 v210, 0xbbaaaaab, v208\n"
      "v_mfma_f32_16x16x32_bf16 v[248:251], a[92:95], v[232:235], v[248:251]\n"
      "v_mul_f32 v211, 0xbbaaaaab, v208\n"
      "v_mfma_f32_16x16x32_bf16 v[248:251], a[128:131], v[204:207], v[248:251]\n"
      "v_pk_mul_f16 v231, v231, v218 op_sel_hi:[1,0]\n"
      "v_mul_f32 v209, 0xbbaaaaab, v208\n"
      "v_mfma_f32_16x16x32_bf16 v[136:139], v[232:235], a[96:99], v[136:139]\n"
      "s_nop 0\n"
      "v_mfma_f32_16x16x32_bf16 v[136:139], v[204:207], a[132:135], v[136:139]\n"
      "s_or_b64 s[34:35], s[34:35], vcc\n"
      "v_pk_mul_f16 v41, v41, v218 op_sel_hi:[1,0]\n"
      "v_mfma_f32_16x16x32_bf16 v[192:195], v[232:235], a[100:103], v[192:195]\n"
      "v_mul_f32 v208, 0xbbaaaaab, v208\n"
      "v_pk_mul_f16 v176, v176, v213 op_sel_hi:[1,0]\n"
      "v_mfma_f32_16x16x32_bf16 v[192:195], v[204:207], a[136:139], v[192:195]\n"
      "v_mfma_f32_16x16x32_bf16 v[200:203], v[150:153], v[8:11], v[208:211]\n"
      "v_pk_mul_f16 v178, v178, v213 op_sel_hi:[1,0]\n"
      "v_mfma_f32_16x16x32_bf16 v[140:143], v[232:235], a[104:107], v[140:143]\n"
      "ds_read_b128 v[232:235], v114 offset:16384\n"
      "v_pk_mul_f16 v38, v38, v213 op_sel_hi:[1,0]\n"
      "v_mfma_f32_16x16x32_bf16 v[140:143], v[204:207], a[140:143], v[140:143]\n"
      "v_mfma_f32_16x16x32_bf16 v[164:167], v[148:151], v[8:11], v[208:211]\n"
      "ds_read_b128 v[204:207], v115 offset:16384\n"
      "v_pk_mul_f16 v228, v228, v218 op_sel_hi:[1,0]\n"
      "v_mfma_f32_16x16x32_bf16 v[252:255], v[150:153], v[12:15], v[208:211]\n"
      "s_nop 0\n"
      "v_cvt_pk_bf16_f32 v200, v200, v201\n"
      "v_cvt_pk_bf16_f32 v201, v202, v203\n"
      "v_mfma_f32_16x16x32_bf16 v[184:187], v[148:151], v[12:15], v[208:211]\n"
      "s_nop 1\n"
      "v_cvt_pk_bf16_f32 v203, v166, v167\n"
      "v_cvt_pk_bf16_f32 v202, v164, v165\n"
      "v_mfma_f32_16x16x32_bf16 v[172:175], v[150:153], v[16:19], v[208:211]\n"
      "ds_write_b128 v216, v[200:203] offset:12288\n"
      "v_cvt_pk_bf16_f32 v252, v252, v253\n"
      "v_cvt_pk_bf16_f32 v253, v254, v255\n"
      "v_cvt_pk_bf16_f32 v255, v186, v187\n"
      "v_cvt_pk_bf16_f32 v254, v184, v185\n"
      "v_mfma_f32_16x16x32_bf16 v[164:167], v[148:151], v[16:19], v[208:211]\n"
      "ds_write_b128 v216, v[252:255] offset:12416\n"
      "s_waitcnt vmcnt(9)\n"
      "v_mfma_f32_16x16x32_bf16 v[252:255], v[188:191], v[20:23], 0\n"
      "s_waitcnt vmcnt(8)\n"
      "v_mfma_f32_16x16x32_bf16 v[252:255], v[188:191], v[24:27], v[252:255]\n"
      "v_cvt_pk_bf16_f32 v172, v172, v173\n"
      "v_cvt_pk_bf16_f32 v173, v174, v175\n"
      "s_waitcnt vmcnt(7)\n"
      "v_mfma_f32_16x16x32_bf16 v[252:255], v[188:191], v[28:31], v[252:255]\n"
      "s_waitcnt lgkmcnt(2)\n"
      "v_mfma_f32_16x16x32_bf16 v[168:171], a[144:147], v[232:235], v[168:171]\n"
      "v_cvt_pk_bf16_f32 v174, v164, v165\n"
      "v_mfma_f32_16x16x32_bf16 v[168:171], a[180:183], v[204:207], v[168:171]\n"
      "ds_read_b128 v[188:191], v132 offset:32\n"
      "v_cvt_pk_bf16_f32 v175, v166, v167\n"
      "v_mfma_f32_16x16x32_bf16 v[196:199], a[148:151], v[232:235], v[196:199]\n"
      "s_nop 0\n"
      "v_add_f32_dpp v252, v252, v252 quad_perm:[1,0,3,2] row_mask:0xf bank_mask:0xf\n"
      "v_mfma_f32_16x16x32_bf16 v[196:199], a[184:187], v[204:207], v[196:199]\n"
      "ds_write_b128 v216, v[172:175] offset:12544\n"
      "v_mfma_f32_16x16x32_bf16 v[220:223], a[152:155], v[232:235], v[220:223]\n"
      "v_add_f32_dpp v252, v252, v252 quad_perm:[2,3,0,1] row_mask:0xf bank_mask:0xf\n"
      "v_mfma_f32_16x16x32_bf16 v[220:223], a[188:191], v[204:207], v[220:223]\n"
      "s_nop 0\n"
      "v_mfma_f32_16x16x32_bf16 v[224:227], a[156:159], v[232:235], v[224:227]\n"
      "ds_read_b64 v[200:201], v132 offset:24\n"
      "v_mfma_f32_16x16x32_bf16 v[224:227], a[192:195], v[204:207], v[224:227]\n"
      "v_add_f32_dpp v252, v252, v252 row_half_mirror row_mask:0xf bank_mask:0xf\n"
      "v_mfma_f32_16x16x32_bf16 v[136:139], v[232:235], a[168:171], v[136:139]\n"
      "v_mul_f32 v255, 0xbbaaaaab, v252\n"
      "v_mfma_f32_16x16x32_bf16 v[136:139], v[204:207], a[204:207], v[136:139]\n"
      "v_mul_f32 v254, 0xbbaaaaab, v252\n"
      "v_mul_f32 v253, 0xbbaaaaab, v252\n"
      "v_mfma_f32_16x16x32_bf16 v[236:239], a[160:163], v[232:235], v[236:239]\n"
      "v_mfma_f32_16x16x32_bf16 v[236:239], a[196:199], v[204:207], v[236:239]\n"
      "v_mul_f32 v252, 0xbbaaaaab, v252\n"
      "v_mfma_f32_16x16x32_bf16 v[248:251], a[164:167], v[232:235], v[248:251]\n"
      "v_mfma_f32_16x16x32_bf16 v[248:251], a[200:203], v[204:207], v[248:251]\n"
      "v_mfma_f32_16x16x32_bf16 v[192:195], v[232:235], a[172:175], v[192:195]\n"
      "v_mfma_f32_16x16x32_bf16 v[192:195], v[204:207], a[208:211], v[192:195]\n"
      "v_mfma_f32_16x16x32_bf16 v[172:175], v[150:153], v[20:23], v[252:255]\n"
      "s_nop 0\n"
      "v_mfma_f32_16x16x32_bf16 v[140:143], v[232:235], a[176:179], v[140:143]\n"
      "s_nop 0\n"
      "v_mfma_f32_16x16x32_bf16 v[140:143], v[204:207], a[212:215], v[140:143]\n"
      "ds_read_u16 v56, v134 offset:32\n"
      "v_mfma_f32_16x16x32_bf16 v[204:207], v[150:153], v[24:27], v[252:255]\n"
      "ds_read_b128 v[184:187], v133\n"
      "v_mfma_f32_16x16x32_bf16 v[232:235], v[148:151], v[24:27], v[252:255]\n"
      "s_nop 1\n"
      "v_cvt_pk_bf16_f32 v172, v172, v173\n"
      "v_cvt_pk_bf16_f32 v173, v174, v175\n"
      "v_mfma_f32_16x16x32_bf16 v[244:247], v[150:153], v[28:31], v[252:255]\n"
      "s_nop 0\n"
      "v_cvt_pk_bf16_f32 v204, v204, v205\n"
      "v_cvt_pk_bf16_f32 v205, v206, v207\n"
      "v_cvt_pk_bf16_f32 v207, v234, v235\n"
      "v_cvt_pk_bf16_f32 v206, v232, v233\n"
      "s_waitcnt vmcnt(0)\n"
      "v_mfma_f32_16x16x32_bf16 v[208:211], v[148:151], v[20:23], v[252:255]\n"
      "ds_write_b128 v216, v[204:207] offset:15488\n"
      "ds_read_b128 v[204:207], v119 offset:41728\n"
      "v_mfma_f32_16x16x32_bf16 v[152:155], v[148:151], v[28:31], v[252:255]\n"
      "ds_read_b32 v218, v122 offset:4864\n"
      "ds_read_u16 v52, v134\n"
      "v_cvt_pk_bf16_f32 v244, v244, v245\n"
      "ds_read_u16 v60, v134 offset:64\n"
      "ds_read_b64 v[202:203], v133 offset:16\n"
      "v_cvt_pk_bf16_f32 v245, v246, v247\n"
      "v_cvt_pk_bf16_f32 v174, v208, v209\n"
      "s_nop 0\n"
      "v_cvt_pk_bf16_f32 v246, v152, v153\n"
      "v_cvt_pk_bf16_f32 v247, v154, v155\n"
      "v_cvt_pk_bf16_f32 v175, v210, v211\n"
      "ds_write_b128 v216, v[244:247] offset:15616\n"
      "ds_write_b128 v216, v[172:175] offset:15360\n"
      "s_waitcnt lgkmcnt(6)\n"
      "v_or3_b32 v204, v204, v205, v206\n"
      "v_and_b32_e64 v204, s40, v204\n"
      "s_waitcnt lgkmcnt(5)\n"
      "v_bitop3_b32 v218, v218, v207, s40 bitop3:0xa8\n"
      "v_or3_b32 v204, v204, v218, s4\n"
      "ds_write_b32 v129, v204 offset:3072\n"
      "s_waitcnt lgkmcnt(0)\n"
      "s_barrier\n"
      "v_mfma_f32_16x16x32_f16 v[208:211], v[156:159], v[180:183], v[78:81]\n"
      "ds_read_b64_tr_b16 a[220:221], v214 offset:8\n"
      "v_cvt_pk_f16_f32 v168, v168, v169\n"
      "v_mfma_f32_16x16x32_f16 v[208:211], v[34:37], v[32:35], v[208:211]\n"
      "ds_read_b64_tr_b16 a[216:217], v214\n"
      "v_cvt_pk_f16_f32 v44, v220, v221\n"
      "v_mfma_f32_16x16x32_f16 v[172:175], v[228:231], v[180:183], v[82:85]\n"
      "ds_read_b64_tr_b16 a[218:219], v214 offset:6144\n"
      "v_cvt_pk_f16_f32 v224, v224, v225\n"
      "v_mfma_f32_16x16x32_f16 v[172:175], v[40:43], v[32:35], v[172:175]\n"
      "ds_read_b64_tr_b16 a[224:225], v215\n"
      "v_pk_mul_f16 v213, v168, v168\n"
      "v_cvt_pk_f16_f32 v225, v226, v227\n"
      "v_mfma_f32_16x16x32_f16 v[204:207], v[188:191], v[180:183], v[70:73]\n"
      "ds_read_b64_tr_b16 a[226:227], v215 offset:6144\n"
      "v_cvt_pk_f16_f32 v169, v170, v171\n"
      "v_mfma_f32_16x16x32_f16 v[204:207], v[184:187], v[156:159], v[204:207]\n"
      "ds_read_b64_tr_b16 a[222:223], v214 offset:6152\n"
      "v_pk_fma_f16 v213, v169, v169, v213\n"
      "v_mfma_f32_16x16x32_f16 v[204:207], v[200:203], v[32:35], v[204:207]\n"
      "ds_read_b64_tr_b16 a[228:229], v214 offset:18432\n"
      "v_cvt_pk_f16_f32 v170, v196, v197\n"
      "v_pk_mul_f16 v216, v224, v224\n"
      "v_cvt_pk_f16_f32 v171, v198, v199\n"
      "v_cvt_pk_f16_f32 v226, v236, v237\n"
      "v_pk_fma_f16 v216, v225, v225, v216\n"
      "v_cvt_pk_f16_f32 v47, v250, v251\n"
      "v_pk_fma_f16 v213, v170, v170, v213\n"
      "v_cvt_pk_f16_f32 v227, v238, v239\n"
      "v_pk_fma_f16 v216, v226, v226, v216\n"
      "v_pk_fma_f16 v216, v227, v227, v216\n"
      "v_cvt_pk_f16_f32 v46, v248, v249\n"
      "v_pk_fma_f16 v213, v171, v171, v213\n"
      "v_cvt_pk_f16_f32 v45, v222, v223\n"
      "v_pk_fma_f16 v213, v44, v44, v213\n"
      "v_pk_fma_f16 v216, v46, v46, v216\n"
      "v_pk_fma_f16 v216, v47, v47, v216\n"
      "v_pk_fma_f16 v213, v45, v45, v213\n"
      "v_cvt_pk_bf16_f32 v50, v136, v137\n"
      "v_cvt_pk_bf16_f32 v55, v194, v195\n"
      "v_permlane16_swap_b32 v213, v216\n"
      "v_pk_add_f16 v213, v213, v216\n"
      "v_mov_b32 v216, v213\n"
      "v_cvt_pk_bf16_f32 v59, v142, v143\n"
      "v_cvt_pk_bf16_f32 v54, v192, v193\n"
      "v_permlane32_swap_b32_e64 v213, v216\n"
      "v_pk_add_f16 v213, v213, v216\n"
      "v_add_f16_sdwa v216, v213, v213 dst_sel:DWORD dst_unused:UNUSED_PAD src0_sel:WORD_0 src1_sel:WORD_1\n"
      "v_cmp_nle_f16 vcc, 0x1c00, v216\n"
      "v_rsq_f16 v213, v216\n"
      "s_or_b64 s[34:35], s[34:35], vcc\n"
      "ds_read_b64_tr_b16 a[252:253], v217 offset:18432\n"
      "ds_read_b64_tr_b16 a[242:243], v217 offset:6144\n"
      "ds_read_b64_tr_b16 a[236:237], v215 offset:18432\n"
      "v_cmp_nge_f16 vcc, 0x7b00, v216\n"
      "v_mov_b32 v216, v213\n"
      "v_cvt_pk_bf16_f32 v58, v140, v141\n"
      "v_cvt_pk_bf16_f32 v51, v138, v139\n"
      "v_permlane16_swap_b32 v213, v216\n"
      "v_mul_f16 v213, s7, v213\n"
      "v_pk_mul_f16 v224, v224, v216 op_sel_hi:[1,0]\n"
      "v_pk_mul_f16 v168, v168, v213 op_sel_hi:[1,0]\n"
      "v_pk_mul_f16 v46, v46, v216 op_sel_hi:[1,0]\n"
      "v_pk_mul_f16 v227, v227, v216 op_sel_hi:[1,0]\n"
      "v_pk_mul_f16 v225, v225, v216 op_sel_hi:[1,0]\n"
      "v_pk_mul_f16 v226, v226, v216 op_sel_hi:[1,0]\n"
      "v_pk_mul_f16 v47, v47, v216 op_sel_hi:[1,0]\n"
      "s_or_b64 s[34:35], s[34:35], vcc\n"
      "ds_read_b64_tr_b16 v[18:19], v215 offset:30720\n"
      "ds_read_b64_tr_b16 v[12:13], v214 offset:12296\n"
      "ds_read_b64_tr_b16 v[8:9], v214 offset:12288\n"
      "ds_read_b64_tr_b16 a[248:249], v212\n"
      "ds_read_b64_tr_b16 v[6:7], v212 offset:24576\n"
      "ds_read_b64_tr_b16 a[230:231], v214 offset:24576\n"
      "ds_read_b64_tr_b16 a[244:245], v217 offset:8\n"
      "ds_read_b64_tr_b16 a[250:251], v212 offset:6144\n"
      "ds_read_b64_tr_b16 v[20:21], v217 offset:12288\n"
      "ds_read_b64_tr_b16 v[24:25], v217 offset:12296\n"
      "ds_read_b64_tr_b16 a[238:239], v215 offset:24576\n"
      "v_mfma_f32_16x16x32_f16 v[196:199], v[224:227], v[180:183], v[86:89]\n"
      "ds_read_b64_tr_b16 a[232:233], v214 offset:18440\n"
      "v_pk_mul_f16 v170, v170, v213 op_sel_hi:[1,0]\n"
      "v_mfma_f32_16x16x32_f16 v[196:199], v[46:49], v[32:35], v[196:199]\n"
      "ds_read_b64_tr_b16 v[28:29], v212 offset:12288\n"
      "ds_read_b64_tr_b16 a[234:235], v214 offset:24584\n"
      "ds_read_b64_tr_b16 a[240:241], v217\n"
      "ds_read_b64_tr_b16 v[26:27], v217 offset:30728\n"
      "ds_read_b64_tr_b16 v[16:17], v215 offset:12288\n"
      "ds_read_b64_tr_b16 v[30:31], v212 offset:30720\n"
      "ds_read_b64_tr_b16 a[254:255], v217 offset:24576\n"
      "ds_read_b64_tr_b16 v[14:15], v214 offset:30728\n"
      "ds_read_b64_tr_b16 v[22:23], v217 offset:30720\n"
      "ds_read_b64_tr_b16 v[4:5], v212 offset:18432\n"
      "ds_read_b64_tr_b16 v[10:11], v214 offset:30720\n"
      "ds_read_b64_tr_b16 v[2:3], v217 offset:24584\n"
      "ds_read_b64_tr_b16 a[246:247], v217 offset:6152\n"
      "ds_read_b64_tr_b16 v[0:1], v217 offset:18440\n"
      "v_pk_mul_f16 v45, v45, v213 op_sel_hi:[1,0]\n"
      "v_pk_mul_f16 v169, v169, v213 op_sel_hi:[1,0]\n"
      "v_pk_mul_f16 v171, v171, v213 op_sel_hi:[1,0]\n"
      "v_pk_mul_f16 v44, v44, v213 op_sel_hi:[1,0]\n"
      "s_waitcnt lgkmcnt(0)\n"
      "s_barrier\n"
      "s_or_b64 vcc, s[48:49], s[50:51]\n"
      "s_or_b64 vcc, vcc, s[34:35]\n"
      "s_cbranch_scc1 L_gen_8_%=\n"
      "L_fast_11_%=:\n"
      "v_exp_f32 v173, v173\n"
      "v_mfma_f32_16x16x32_f16 v[244:247], v[156:159], v[176:179], v[90:93]\n"
      "v_exp_f32 v204, v204\n"
      "v_mfma_f32_16x16x32_f16 v[244:247], v[34:37], v[38:41], v[244:247]\n"
      "ds_read_b128 v[212:215], v128 offset:3072\n"
      "v_exp_f32 v209, v209\n"
      "v_cvt_pk_bf16_f32 v64, v204, 0\n"
      "v_mfma_f32_16x16x32_f16 v[236:239], v[228:231], v[176:179], v[94:97]\n"
      "v_exp_f32 v208, v208\n"
      "v_mfma_f32_16x16x32_f16 v[236:239], v[40:43], v[38:41], v[236:239]\n"
      "v_exp_f32 v210, v210\n"
      "v_mfma_f32_16x16x32_f16 v[148:151], v[188:191], v[176:179], v[72:75]\n"
      "v_exp_f32 v211, v211\n"
      "v_mfma_f32_16x16x32_f16 v[148:151], v[184:187], v[228:231], v[148:151]\n"
      "v_exp_f32 v175, v175\n"
      "v_mfma_f32_16x16x32_f16 v[148:151], v[200:203], v[38:41], v[148:151]\n"
      "v_exp_f32 v245, v245\n"
      "v_exp_f32 v246, v246\n"
      "v_mfma_f32_16x16x32_f16 v[232:235], v[224:227], v[176:179], v[98:101]\n"
      "v_cvt_pk_bf16_f32 v208, v208, v209\n"
      "v_cvt_pk_bf16_f32 v209, v210, v211\n"
      "v_mfma_f32_16x16x32_f16 v[232:235], v[46:49], v[38:41], v[232:235]\n"
      "v_exp_f32 v172, v172\n"
      "v_mfma_f32_16x16x32_f16 v[152:155], v[156:159], v[168:171], v[102:105]\n"
      "s_waitcnt lgkmcnt(0)\n"
      "v_bitop3_b32 v214, v214, v215, s4 bitop3:0x7e\n"
      "v_bitop3_b32 v212, v212, v213, s4 bitop3:0x7e\n"
      "v_mfma_f32_16x16x32_f16 v[152:155], v[34:37], v[44:47], v[152:155]\n"
      "v_exp_f32_e64 v174, v174\n"
      "v_mfma_f32_16x16x32_f16 v[252:255], v[228:231], v[168:171], v[106:109]\n"
      "v_cvt_pk_bf16_f32 v210, v172, v173\n"
      "v_or_b32 v212, v212, v214\n"
      "v_mfma_f32_16x16x32_f16 v[252:255], v[40:43], v[44:47], v[252:255]\n"
      "v_exp_f32 v196, v196\n"
      "v_mfma_f32_16x16x32_f16 v[216:219], v[188:191], v[168:171], v[74:77]\n"
      "v_exp_f32 v199, v199\n"
      "v_mfma_f32_16x16x32_f16 v[216:219], v[184:187], v[224:227], v[216:219]\n"
      "v_cmp_ne_u32 vcc, 0, v212\n"
      "v_cvt_pk_bf16_f32 v211, v174, v175\n"
      "v_mfma_f32_16x16x32_f16 v[216:219], v[200:203], v[44:47], v[216:219]\n"
      "v_cndmask_b32 v205, v205, v149, s[42:43]\n"
      "v_exp_f32 v235, v235\n"
      "v_mfma_f32_16x16x32_f16 v[220:223], v[184:187], v[188:191], v[76:79]\n"
      "v_exp_f32 v198, v198\n"
      "v_exp_f32_e64 v234, v234\n"
      "v_cvt_pk_bf16_f32 v63, v198, v199\n"
      "v_exp_f32 v233, v233\n"
      "v_mfma_f32_16x16x32_f16 v[172:175], v[224:227], v[168:171], v[110:113]\n"
      "s_mov_b64 s[48:49], vcc\n"
      "v_exp_f32_e64 v197, v197\n"
      "v_mfma_f32_16x16x32_f16 v[172:175], v[46:49], v[44:47], v[172:175]\n"
      "v_cvt_pk_bf16_f32 v62, v196, v197\n"
      "v_cndmask_b32 v205, v205, v217, s[44:45]\n"
      "v_mfma_f32_16x16x16_f16 v[220:223], v[202:203], v[200:201], v[220:223]\n"
      "v_exp_f32 v148, v148\n"
      "v_mfma_f32_16x16x32_bf16 v[192:195], v[66:69], v[208:211], 0\n"
      "v_exp_f32 v244, v244\n"
      "v_mfma_f32_16x16x32_bf16 v[192:195], v[66:69], v[62:65], v[192:195]\n"
      "v_cvt_pk_bf16_f32 v244, v244, v245\n"
      "v_exp_f32 v239, v239\n"
      "v_mfma_f32_16x16x32_bf16 v[200:203], v[240:243], v[208:211], 0\n"
      "v_exp_f32_e64 v247, v247\n"
      "v_mfma_f32_16x16x32_bf16 v[200:203], v[50:53], v[62:65], v[200:203]\n"
      "s_add_u32 s28, s27, 2\n"
      "v_cvt_pk_bf16_f32 v245, v246, v247\n"
      "v_exp_f32 v232, v232\n"
      "v_mfma_f32_16x16x32_bf16 v[228:231], v[160:163], v[208:211], 0\n"
      "v_exp_f32 v236, v236\n"
      "v_mfma_f32_16x16x32_bf16 v[228:231], v[54:57], v[62:65], v[228:231]\n"
      "v_exp_f32 v238, v238\n"
      "v_mfma_f32_16x16x32_bf16 v[248:251], v[144:147], v[208:211], 0\n"
      "s_min_u32 m0, s28, s3\n"
      "v_exp_f32 v237, v237\n"
      "v_mfma_f32_16x16x32_bf16 v[248:251], v[58:61], v[62:65], v[248:251]\n"
      "s_cbranch_vccnz L_flg_12_%=\n"
      "L_fb_13_%=:\n"
      "v_cvt_pk_bf16_f32 v63, v234, v235\n"
      "v_cvt_pk_bf16_f32 v247, v238, v239\n"
      "v_cvt_pk_bf16_f32 v246, v236, v237\n"
      "v_cvt_pk_bf16_f32 v62, v232, v233\n"
      "v_cvt_pk_bf16_f32 v64, v148, 0\n"
      "v_rcp_f32 v192, v192\n"
      "v_mfma_f32_16x16x32_bf16 v[212:215], v[66:69], v[244:247], 0\n"
      "ds_read_b128 v[148:151], v114 offset:18432\n"
      "v_mul_f32 v200, v192, v200\n"
      "v_mfma_f32_16x16x32_bf16 v[212:215], v[66:69], v[62:65], v[212:215]\n"
      "ds_read_b128 v[140:143], v115 offset:18432\n"
      "v_mul_f32 v203, v192, v203\n"
      "v_mul_f32 v201, v192, v201\n"
      "v_mul_f32_e64 v250, v192, v250\n"
      "v_mfma_f32_16x16x32_bf16 v[180:183], v[240:243], v[244:247], 0\n"
      "s_movrels_b32 s9, s80\n"
      "v_cvt_pk_bf16_f32 v200, v200, v201\n"
      "v_mul_f32 v202, v192, v202\n"
      "v_mfma_f32_16x16x32_bf16 v[180:183], v[50:53], v[62:65], v[180:183]\n"
      "s_lshr_b32 s8, s9, 8\n"
      "v_mul_f32 v249, v192, v249\n"
      "v_mul_f32_e64 v248, v192, v248\n"
      "v_mfma_f32_16x16x32_bf16 v[236:239], v[160:163], v[244:247], 0\n"
      "v_mul_f32_e64 v228, v192, v228\n"
      "v_mul_f32_e64 v230, v192, v230\n"
      "v_mfma_f32_16x16x32_bf16 v[236:239], v[54:57], v[62:65], v[236:239]\n"
      "s_and_b32 s9, s9, 0xff\n"
      "v_mul_f32_e64 v229, v192, v229\n"
      "v_cvt_pk_bf16_f32 v201, v202, v203\n"
      "v_cvt_pk_bf16_f32 v202, v228, v229\n"
      "v_mfma_f32_16x16x32_bf16 v[188:191], v[144:147], v[244:247], 0\n"
      "s_mul_i32 s10, s8, 73\n"
      "v_mul_f32 v231, v192, v231\n"
      "v_cvt_pk_bf16_f32 v203, v230, v231\n"
      "v_mfma_f32_16x16x32_bf16 v[188:191], v[58:61], v[62:65], v[188:191]\n"
      "ds_write_b128 v124, v[200:203]\n"
      "s_waitcnt lgkmcnt(1)\n"
      "v_mfma_f32_16x16x32_bf16 v[184:187], a[0:3], v[148:151], 0\n"
      "s_add_u32 s10, s10, s9\n"
      "v_mul_f32 v251, v192, v251\n"
      "v_cvt_pk_bf16_f32 v248, v248, v249\n"
      "v_mfma_f32_16x16x32_bf16 v[184:187], a[36:39], v[140:143], v[184:187]\n"
      "s_mul_i32 s28, s10, 4802\n"
      "v_cndmask_b32 v205, v205, v221, s[46:47]\n"
      "v_cvt_pk_bf16_f32 v249, v250, v251\n"
      "v_mfma_f32_16x16x32_bf16 v[208:211], a[4:7], v[148:151], 0\n"
      "s_add_u32 s56, s18, s28\n"
      "v_exp_f32 v205, v205\n"
      "v_mfma_f32_16x16x32_bf16 v[208:211], a[40:43], v[140:143], v[208:211]\n"
      "s_addc_u32 s57, s19, 0\n"
      "v_mov_b32 v250, 0\n"
      "v_cvt_pk_bf16_f32 v205, v205, v205\n"
      "v_mfma_f32_16x16x32_bf16 v[176:179], a[8:11], v[148:151], 0\n"
      "s_and_b32 s56, s56, -16\n"
      "v_exp_f32 v152, v152\n"
      "v_mfma_f32_16x16x32_bf16 v[176:179], a[44:47], v[140:143], v[176:179]\n"
      "ds_write_b16 v130, v205\n"
      "v_mfma_f32_16x16x32_bf16 v[224:227], a[12:15], v[148:151], 0\n"
      "s_sub_u32 s58, s56, 110592\n"
      "ds_read_b128 v[244:247], v131\n"
      "v_mfma_f32_16x16x32_bf16 v[224:227], a[48:51], v[140:143], v[224:227]\n"
      "ds_read_b128 v[192:195], v131 offset:64\n"
      "v_exp_f32 v255, v255\n"
      "v_exp_f32 v252, v252\n"
      "v_mfma_f32_16x16x32_bf16 v[228:231], a[16:19], v[148:151], 0\n"
      "s_subb_u32 s59, s57, 0\n"
      "v_exp_f32 v153, v153\n"
      "v_mfma_f32_16x16x32_bf16 v[228:231], a[52:55], v[140:143], v[228:231]\n"
      "s_sub_u32 s56, s56, 73728\n"
      "v_exp_f32 v254, v254\n"
      "v_cvt_pk_bf16_f32 v152, v152, v153\n"
      "v_exp_f32 v174, v174\n"
      "v_mfma_f32_16x16x32_bf16 v[168:171], a[20:23], v[148:151], 0\n"
      "s_subb_u32 s57, s57, 0\n"
      "v_exp_f32 v155, v155\n"
      "v_mfma_f32_16x16x32_bf16 v[168:171], a[56:59], v[140:143], v[168:171]\n"
      "s_mul_i32 s28, s8, 1373568\n"
      "v_exp_f32 v253, v253\n"
      "v_exp_f32 v154, v154\n"
      "v_exp_f32 v172, v172\n"
      "v_cvt_pk_bf16_f32 v153, v154, v155\n"
      "v_exp_f32 v173, v173\n"
      "v_cvt_pk_bf16_f32 v155, v254, v255\n"
      "s_waitcnt lgkmcnt(0)\n"
      "v_mfma_f32_16x16x32_bf16 v[200:203], v[244:247], v[66:69], 0\n"
      "s_mul_i32 s29, s9, 2688\n"
      "v_exp_f32 v175, v175\n"
      "v_mfma_f32_16x16x32_bf16 v[200:203], v[192:195], v[66:69], v[200:203]\n"
      "s_add_u32 s28, s28, s29\n"
      "v_exp_f32 v216, v216\n"
      "v_mfma_f32_16x16x32_bf16 v[232:235], v[244:247], v[240:243], 0\n"
      "s_cmp_eq_u32 s8, 72\n"
      "v_cvt_pk_bf16_f32 v62, v172, v173\n"
      "v_cvt_pk_bf16_f32 v63, v174, v175\n"
      "v_mfma_f32_16x16x32_bf16 v[232:235], v[192:195], v[50:53], v[232:235]\n"
      "s_cselect_b32 s8, 0, 100270464\n"
      "v_cvt_pk_bf16_f32 v64, v216, 0\n"
      "v_cvt_pk_bf16_f32 v154, v252, v253\n"
      "v_mfma_f32_16x16x32_bf16 v[252:255], v[244:247], v[160:163], 0\n"
      "ds_read_b128 v[204:207], v114 offset:24576\n"
      "s_cmp_eq_u32 s9, 72\n"
      "v_mfma_f32_16x16x32_bf16 v[252:255], v[192:195], v[54:57], v[252:255]\n"
      "s_cselect_b32 s9, 0, 196224\n"
      "v_rcp_f32_e64 v212, v212\n"
      "v_mov_b32_dpp v200, v200 quad_perm:[0,0,0,0] row_mask:0xf bank_mask:0xf\n"
      "v_mfma_f32_16x16x32_bf16 v[164:167], v[66:69], v[152:155], 0\n"
      "ds_read_b128 v[216:219], v115 offset:24576\n"
      "v_mul_f32 v180, v212, v180\n"
      "v_mfma_f32_16x16x32_bf16 v[164:167], v[66:69], v[62:65], v[164:167]\n"
      "s_add_u32 s10, s8, s9\n"
      "v_mul_f32 v181, v212, v181\n"
      "v_cvt_pk_bf16_f32 v180, v180, v181\n"
      "v_mfma_f32_16x16x32_bf16 v[196:199], v[240:243], v[152:155], 0\n"
      "s_add_u32 s28, s28, s10\n"
      "v_mul_f32 v182, v212, v182\n"
      "v_mul_f32 v183, v212, v183\n"
      "v_mfma_f32_16x16x32_bf16 v[196:199], v[50:53], v[62:65], v[196:199]\n"
      "s_add_u32 s52, s16, s28\n"
      "v_cvt_pk_bf16_f32 v181, v182, v183\n"
      "v_mul_f32 v237, v212, v237\n"
      "v_mfma_f32_16x16x32_bf16 v[172:175], v[160:163], v[152:155], 0\n"
      "s_addc_u32 s53, s17, 0\n"
      "v_mul_f32 v236, v212, v236\n"
      "v_cvt_pk_bf16_f32 v182, v236, v237\n"
      "v_mfma_f32_16x16x32_bf16 v[172:175], v[54:57], v[62:65], v[172:175]\n"
      "s_cmp_lg_u32 s10, s11\n"
      "v_mul_f32 v238, v212, v238\n"
      "v_mul_f32 v239, v212, v239\n"
      "v_mfma_f32_16x16x32_bf16 v[136:139], v[144:147], v[152:155], 0\n"
      "s_cbranch_scc1 L_edge_15_%=\n"
      "L_eb_16_%=:\n"
      "v_mfma_f32_16x16x32_bf16 v[136:139], v[58:61], v[62:65], v[136:139]\n"
      "v_mul_f32_e64 v188, v212, v188\n"
      "v_cvt_pk_bf16_f32 v183, v238, v239\n"
      "v_mfma_f32_16x16x32_bf16 v[160:163], v[244:247], v[144:147], 0\n"
      "s_waitcnt lgkmcnt(0)\n"
      "v_mul_f32 v191, v212, v191\n"
      "v_mul_f32_e64 v189, v212, v189\n"
      "v_mfma_f32_16x16x32_bf16 v[160:163], v[192:195], v[58:61], v[160:163]\n"
      "ds_write_b128 v124, v[180:183] offset:1024\n"
      "v_mul_f32_e64 v190, v212, v190\n"
      "v_cvt_pk_bf16_f32 v188, v188, v189\n"
      "v_cvt_pk_bf16_f32 v189, v190, v191\n"
      "v_rcp_f32_e64 v200, v200\n"
      "v_cndmask_b32 v232, v232, v252, s[42:43]\n"
      "ds_write2st64_b64 v125, v[248:249], v[188:189] offset0:0 offset1:2\n"
      "ds_write_b32 v129, v250\n"
      "v_cndmask_b32 v232, v232, v160, s[44:45]\n"
      "v_mul_f32_e64 v232, v200, v232\n"
      "v_cvt_pk_bf16_f32 v232, v232, v232\n"
      "ds_write_b16 v135, v232\n"
      "L_join_10_%=:\n"
      "s_mov_b32 s27, 1\n"
      ".p2align 6\n"
      "L_loop_%=:\n"
      "v_mfma_f32_16x16x32_bf16 v[192:195], v[148:151], a[24:27], 0\n"
      "s_add_u32 m0, s76, 0\n"
      "v_rcp_f32 v164, v164\n"
      "v_mfma_f32_16x16x32_bf16 v[192:195], v[140:143], a[60:63], v[192:195]\n"
      "s_waitcnt lgkmcnt(0)\n"
      "s_barrier\n"
      "v_mfma_f32_16x16x32_bf16 v[244:247], v[148:151], a[28:31], 0\n"
      "v_mul_f32 v198, v164, v198\n"
      "v_mul_f32 v196, v164, v196\n"
      "v_mfma_f32_16x16x32_bf16 v[244:247], v[140:143], a[64:67], v[244:247]\n"
      "v_mul_f32_e64 v197, v164, v197\n"
      "v_cvt_pk_bf16_f32 v196, v196, v197\n"
      "v_mfma_f32_16x16x32_bf16 v[160:163], v[148:151], a[32:35], 0\n"
      "ds_read_b128 v[148:151], v128\n"
      "v_mul_f32_e64 v199, v164, v199\n"
      "v_mfma_f32_16x16x32_bf16 v[160:163], v[140:143], a[68:71], v[160:163]\n"
      "global_load_lds_dwordx4 v116, s[52:53]\n"
      "v_mfma_f32_16x16x32_bf16 v[184:187], a[72:75], v[204:207], v[184:187]\n"
      "ds_read_b128 v[140:143], v114 offset:30720\n"
      "v_cvt_pk_bf16_f32 v197, v198, v199\n"
      "v_mfma_f32_16x16x32_bf16 v[184:187], a[108:111], v[216:219], v[184:187]\n"
      "v_mul_f32 v172, v164, v172\n"
      "v_mul_f32 v173, v164, v173\n"
      "v_mfma_f32_16x16x32_bf16 v[208:211], a[76:79], v[204:207], v[208:211]\n"
      "v_cvt_pk_bf16_f32 v198, v172, v173\n"
      "v_mul_f32 v174, v164, v174\n"
      "v_mfma_f32_16x16x32_bf16 v[208:211], a[112:115], v[216:219], v[208:211]\n"
      "v_mul_f32 v175, v164, v175\n"
      "v_cvt_pk_bf16_f32 v199, v174, v175\n"
      "v_mfma_f32_16x16x32_bf16 v[176:179], a[80:83], v[204:207], v[176:179]\n"
      "ds_read_b128 v[172:175], v115 offset:30720\n"
      "v_mul_f32_e64 v136, v164, v136\n"
      "v_mfma_f32_16x16x32_bf16 v[176:179], a[116:119], v[216:219], v[176:179]\n"
      "ds_write_b128 v124, v[196:199] offset:2048\n"
      "v_mfma_f32_16x16x32_bf16 v[224:227], a[84:87], v[204:207], v[224:227]\n"
      "s_waitcnt lgkmcnt(3)\n"
      "v_mul_f32 v139, v164, v139\n"
      "v_mul_f32_e64 v137, v164, v137\n"
      "v_mfma_f32_16x16x32_bf16 v[224:227], a[120:123], v[216:219], v[224:227]\n"
      "s_add_u32 m0, s76, 6016\n"
      "v_cvt_pk_bf16_f32 v136, v136, v137\n"
      "v_mul_f32_e64 v138, v164, v138\n"
      "v_mfma_f32_16x16x32_bf16 v[228:231], a[88:91], v[204:207], v[228:231]\n"
      "v_cvt_pk_bf16_f32 v137, v138, v139\n"
      "v_add_f32 v148, |v148|, |v149|\n"
      "v_mfma_f32_16x16x32_bf16 v[228:231], a[124:127], v[216:219], v[228:231]\n"
      "ds_write_b64 v125, v[136:137] offset:2048\n"
      "v_mfma_f32_16x16x32_bf16 v[168:171], a[92:95], v[204:207], v[168:171]\n"
      "global_load_lds_dwordx4 v116, s[52:53] offset:128\n"
      "v_mfma_f32_16x16x32_bf16 v[168:171], a[128:131], v[216:219], v[168:171]\n"
      "v_add_f32 v150, |v150|, |v151|\n"
      "v_add_f32 v148, v148, v150\n"
      "v_mfma_f32_16x16x32_bf16 v[192:195], v[204:207], a[96:99], v[192:195]\n"
      "v_rsq_f32 v148, v148\n"
      "v_mfma_f32_16x16x32_bf16 v[192:195], v[216:219], a[132:135], v[192:195]\n"
      "s_waitcnt lgkmcnt(2)\n"
      "v_mul_f32 v148, s41, v148\n"
      "v_mfma_f32_16x16x32_bf16 v[244:247], v[204:207], a[100:103], v[244:247]\n"
      "ds_read_b128 v[196:199], v114 offset:20480\n"
      "v_mov_b32_e64 v164, v148\n"
      "v_mfma_f32_16x16x32_bf16 v[244:247], v[216:219], a[136:139], v[244:247]\n"
      "s_nop 0\n"
      "v_permlane32_swap_b32 v148, v164\n"
      "v_mfma_f32_16x16x32_bf16 v[160:163], v[204:207], a[104:107], v[160:163]\n"
      "ds_read_b128 v[204:207], v115 offset:20480\n"
      "v_mov_b32 v165, v148\n"
      "v_mfma_f32_16x16x32_bf16 v[160:163], v[216:219], a[140:143], v[160:163]\n"
      "s_add_u32 m0, s76, 12032\n"
      "v_permlane16_swap_b32 v148, v165\n"
      "v_mfma_f32_16x16x32_bf16 v[184:187], a[144:147], v[140:143], v[184:187]\n"
      "v_mul_f32 v212, v148, v212\n"
      "v_mul_f32 v213, v148, v213\n"
      "v_mfma_f32_16x16x32_bf16 v[184:187], a[180:183], v[172:175], v[184:187]\n"
      "v_cvt_pk_bf16_f32 v212, v212, v213\n"
      "v_mul_f32_e64 v214, v148, v214\n"
      "v_mfma_f32_16x16x32_bf16 v[208:211], a[148:151], v[140:143], v[208:211]\n"
      "v_mul_f32_e64 v215, v148, v215\n"
      "v_cvt_pk_bf16_f32 v213, v214, v215\n"
      "v_mfma_f32_16x16x32_bf16 v[208:211], a[184:187], v[172:175], v[208:211]\n"
      "global_load_lds_dwordx4 v116, s[52:53] offset:256\n"
      "v_mfma_f32_16x16x32_bf16 v[176:179], a[152:155], v[140:143], v[176:179]\n"
      "v_mul_f32 v237, v148, v237\n"
      "v_mul_f32 v236, v148, v236\n"
      "v_mfma_f32_16x16x32_bf16 v[176:179], a[188:191], v[172:175], v[176:179]\n"
      "v_mul_f32 v238, v148, v238\n"
      "v_mul_f32_e64 v239, v148, v239\n"
      "v_mfma_f32_16x16x32_bf16 v[224:227], a[156:159], v[140:143], v[224:227]\n"
      "v_cvt_pk_bf16_f32 v215, v238, v239\n"
      "s_waitcnt lgkmcnt(0)\n"
      "v_mfma_f32_16x16x32_bf16 v[224:227], a[192:195], v[172:175], v[224:227]\n"
      "v_cvt_pk_bf16_f32 v214, v236, v237\n"
      "v_mul_f32 v152, v148, v152\n"
      "v_mfma_f32_16x16x32_bf16 v[228:231], a[160:163], v[140:143], v[228:231]\n"
      "s_add_u32 m0, s77, 0\n"
      "v_mul_f32 v154, v148, v154\n"
      "v_mul_f32 v153, v148, v153\n"
      "v_mfma_f32_16x16x32_bf16 v[228:231], a[196:199], v[172:175], v[228:231]\n"
      "ds_write_b128 v126, v[212:215] offset:18432\n"
      "v_mfma_f32_16x16x32_bf16 v[168:171], a[164:167], v[140:143], v[168:171]\n"
      "v_cvt_pk_bf16_f32 v152, v152, v153\n"
      "v_mul_f32 v155, v148, v155\n"
      "v_mfma_f32_16x16x32_bf16 v[168:171], a[200:203], v[172:175], v[168:171]\n"
      "v_mul_f32 v156, v165, v156\n"
      "v_cvt_pk_bf16_f32 v153, v154, v155\n"
      "v_mfma_f32_16x16x32_bf16 v[192:195], v[140:143], a[168:171], v[192:195]\n"
      "v_mul_f32 v158, v165, v158\n"
      "v_mul_f32 v157, v165, v157\n"
      "v_mfma_f32_16x16x32_bf16 v[192:195], v[172:175], a[204:207], v[192:195]\n"
      "global_load_lds_dwordx4 v117, s[52:53]\n"
      "v_mfma_f32_16x16x32_bf16 v[244:247], v[140:143], a[172:175], v[244:247]\n"
      "v_mul_f32 v188, v165, v188\n"
      "v_cvt_pk_bf16_f32 v156, v156, v157\n"
      "v_mfma_f32_16x16x32_bf16 v[244:247], v[172:175], a[208:211], v[244:247]\n"
      "v_mul_f32 v159, v165, v159\n"
      "v_cvt_pk_bf16_f32 v157, v158, v159\n"
      "v_mfma_f32_16x16x32_bf16 v[160:163], v[140:143], a[176:179], v[160:163]\n"
      "ds_read_b128 v[216:219], v114 offset:26624\n"
      "v_mul_f32_e64 v189, v165, v189\n"
      "v_mfma_f32_16x16x32_bf16 v[160:163], v[172:175], a[212:215], v[160:163]\n"
      "ds_read_b128 v[140:143], v115 offset:26624\n"
      "v_cvt_pk_bf16_f32 v158, v188, v189\n"
      "v_mfma_f32_16x16x32_bf16 v[212:215], a[0:3], v[196:199], 0\n"
      "s_add_u32 m0, s78, 0\n"
      "v_mul_f32 v190, v165, v190\n"
      "v_mul_f32 v191, v165, v191\n"
      "v_mfma_f32_16x16x32_bf16 v[212:215], a[36:39], v[204:207], v[212:215]\n"
      "v_cvt_pk_bf16_f32 v159, v190, v191\n"
      "v_mul_f32 v144, v165, v144\n"
      "v_mfma_f32_16x16x32_bf16 v[188:191], a[4:7], v[196:199], 0\n"
      "v_mul_f32_e64 v145, v165, v145\n"
      "v_cvt_pk_bf16_f32 v144, v144, v145\n"
      "v_mfma_f32_16x16x32_bf16 v[188:191], a[40:43], v[204:207], v[188:191]\n"
      "ds_write_b128 v126, v[156:159] offset:20480\n"
      "v_mfma_f32_16x16x32_bf16 v[236:239], a[8:11], v[196:199], 0\n"
      "v_mul_f32 v146, v165, v146\n"
      "v_mul_f32 v147, v165, v147\n"
      "v_mfma_f32_16x16x32_bf16 v[236:239], a[44:47], v[204:207], v[236:239]\n"
      "s_mov_b32 exec_hi, 0\n"
      "global_load_lds_dwordx4 v118, s[52:53]\n"
      "s_mov_b32 exec_hi, -1\n"
      "v_mfma_f32_16x16x32_bf16 v[156:159], a[12:15], v[196:199], 0\n"
      "v_cvt_pk_bf16_f32 v145, v146, v147\n"
      "v_mul_f32 v180, v164, v180\n"
      "v_mfma_f32_16x16x32_bf16 v[156:159], a[48:51], v[204:207], v[156:159]\n"
      "ds_write2st64_b64 v127, v[152:153], v[144:145] offset0:36 offset1:40\n"
      "v_mfma_f32_16x16x32_bf16 v[152:155], a[16:19], v[196:199], 0\n"
      "v_mul_f32 v181, v164, v181\n"
      "v_cvt_pk_bf16_f32 v180, v180, v181\n"
      "v_mfma_f32_16x16x32_bf16 v[152:155], a[52:55], v[204:207], v[152:155]\n"
      "s_waitcnt lgkmcnt(2)\n"
      "v_mul_f32 v182, v164, v182\n"
      "v_mul_f32_e64 v183, v164, v183\n"
      "v_mfma_f32_16x16x32_bf16 v[136:139], a[20:23], v[196:199], 0\n"
      "s_add_u32 m0, s76, 110592\n"
      "v_cvt_pk_bf16_f32 v181, v182, v183\n"
      "v_mul_f32_e64 v220, v164, v220\n"
      "v_mfma_f32_16x16x32_bf16 v[136:139], a[56:59], v[204:207], v[136:139]\n"
      "v_mul_f32_e64 v221, v164, v221\n"
      "v_cvt_pk_bf16_f32 v182, v220, v221\n"
      "v_mfma_f32_16x16x32_bf16 v[144:147], v[196:199], a[24:27], 0\n"
      "ds_read_b128 v[148:151], v114 offset:32768\n"
      "v_mul_f32 v222, v164, v222\n"
      "v_mfma_f32_16x16x32_bf16 v[144:147], v[204:207], a[60:63], v[144:147]\n"
      "v_mul_f32 v223, v164, v223\n"
      "v_cvt_pk_bf16_f32 v183, v222, v223\n"
      "v_mfma_f32_16x16x32_bf16 v[220:223], v[196:199], a[28:31], 0\n"
      "global_load_lds_dwordx4 v119, s[56:57]\n"
      "v_mfma_f32_16x16x32_bf16 v[220:223], v[204:207], a[64:67], v[220:223]\n"
      "ds_write_b128 v126, v[180:183] offset:22528\n"
      "v_mfma_f32_16x16x32_bf16 v[172:175], v[196:199], a[32:35], 0\n"
      "ds_read_b128 v[180:183], v115 offset:32768\n"
      "v_mul_f32 v240, v164, v240\n"
      "v_mfma_f32_16x16x32_bf16 v[172:175], v[204:207], a[68:71], v[172:175]\n"
      "v_mul_f32 v243, v164, v243\n"
      "v_mul_f32 v241, v164, v241\n"
      "v_mfma_f32_16x16x32_bf16 v[212:215], a[72:75], v[216:219], v[212:215]\n"
      "s_add_u32 m0, s79, 0\n"
      "v_cvt_pk_bf16_f32 v240, v240, v241\n"
      "v_mul_f32_e64 v242, v164, v242\n"
      "v_mfma_f32_16x16x32_bf16 v[188:191], a[76:79], v[216:219], v[188:191]\n"
      "v_cvt_pk_f16_f32 v184, v184, v185\n"
      "v_cvt_pk_f16_f32 v224, v224, v225\n"
      "v_cvt_pk_bf16_f32 v241, v242, v243\n"
      "v_mfma_f32_16x16x32_bf16 v[212:215], a[108:111], v[140:143], v[212:215]\n"
      "global_load_lds_dword v122, s[58:59]\n"
      "v_mfma_f32_16x16x32_bf16 v[188:191], a[112:115], v[140:143], v[188:191]\n"
      "ds_write_b64 v127, v[240:241] offset:22528\n"
      "v_mfma_f32_16x16x32_bf16 v[236:239], a[80:83], v[216:219], v[236:239]\n"
      "v_cvt_pk_f16_f32 v185, v186, v187\n"
      "v_pk_mul_f16 v240, v184, v184\n"
      "v_mfma_f32_16x16x32_bf16 v[236:239], a[116:119], v[140:143], v[236:239]\n"
      "v_pk_mul_f16 v243, v224, v224\n"
      "v_pk_fma_f16 v240, v185, v185, v240\n"
      "v_mfma_f32_16x16x32_bf16 v[156:159], a[84:87], v[216:219], v[156:159]\n"
      "v_cvt_pk_f16_f32 v225, v226, v227\n"
      "v_cvt_pk_f16_f32 v34, v168, v169\n"
      "v_mfma_f32_16x16x32_bf16 v[156:159], a[120:123], v[140:143], v[156:159]\n"
      "ds_read_b128 v[164:167], v114 offset:22528\n"
      "v_mfma_f32_16x16x32_bf16 v[152:155], a[88:91], v[216:219], v[152:155]\n"
      "v_cvt_pk_f16_f32 v186, v208, v209\n"
      "v_pk_fma_f16 v243, v225, v225, v243\n"
      "v_mfma_f32_16x16x32_bf16 v[152:155], a[124:127], v[140:143], v[152:155]\n"
      "ds_read_b128 v[204:207], v115 offset:22528\n"
      "v_cvt_pk_f16_f32 v226, v228, v229\n"
      "v_mfma_f32_16x16x32_bf16 v[136:139], a[92:95], v[216:219], v[136:139]\n"
      "v_pk_fma_f16 v243, v226, v226, v243\n"
      "v_cvt_pk_f16_f32 v187, v210, v211\n"
      "v_mfma_f32_16x16x32_bf16 v[136:139], a[128:131], v[140:143], v[136:139]\n"
      "v_pk_fma_f16 v240, v186, v186, v240\n"
      "v_cvt_pk_f16_f32 v227, v230, v231\n"
      "v_mfma_f32_16x16x32_bf16 v[144:147], v[216:219], a[96:99], v[144:147]\n"
      "v_pk_fma_f16 v243, v227, v227, v243\n"
      "v_cvt_pk_f16_f32 v32, v176, v177\n"
      "v_mfma_f32_16x16x32_bf16 v[144:147], v[140:143], a[132:135], v[144:147]\n"
      "v_pk_fma_f16 v240, v187, v187, v240\n"
      "v_pk_fma_f16 v240, v32, v32, v240\n"
      "v_mfma_f32_16x16x32_bf16 v[220:223], v[216:219], a[100:103], v[220:223]\n"
      "v_pk_fma_f16 v243, v34, v34, v243\n"
      "v_cvt_pk_f16_f32 v33, v178, v179\n"
      "v_mfma_f32_16x16x32_bf16 v[220:223], v[140:143], a[136:139], v[220:223]\n"
      "ds_read_b128 v[176:179], v119\n"
      "v_pk_fma_f16 v240, v33, v33, v240\n"
      "v_mfma_f32_16x16x32_bf16 v[172:175], v[216:219], a[104:107], v[172:175]\n"
      "v_cvt_pk_f16_f32 v35, v170, v171\n"
      "v_pk_fma_f16 v243, v35, v35, v243\n"
      "v_mfma_f32_16x16x32_bf16 v[172:175], v[140:143], a[140:143], v[172:175]\n"
      "s_waitcnt lgkmcnt(2)\n"
      "v_cvt_pk_bf16_f32 v192, v192, v193\n"
      "v_cvt_pk_bf16_f32 v193, v194, v195\n"
      "v_mfma_f32_16x16x32_bf16 v[212:215], a[144:147], v[148:151], v[212:215]\n"
      "v_permlane16_swap_b32 v240, v243\n"
      "v_mfma_f32_16x16x32_bf16 v[212:215], a[180:183], v[180:183], v[212:215]\n"
      "v_pk_add_f16 v240, v240, v243\n"
      "v_mov_b32 v243, v240\n"
      "v_mfma_f32_16x16x32_bf16 v[188:191], a[148:151], v[148:151], v[188:191]\n"
      "s_nop 0\n"
      "v_permlane32_swap_b32_e64 v240, v243\n"
      "v_mfma_f32_16x16x32_bf16 v[188:191], a[184:187], v[180:183], v[188:191]\n"
      "v_pk_add_f16 v240, v240, v243\n"
      "v_add_f16_sdwa v243, v240, v240 dst_sel:DWORD dst_unused:UNUSED_PAD src0_sel:WORD_0 src1_sel:WORD_1\n"
      "v_mfma_f32_16x16x32_bf16 v[236:239], a[152:155], v[148:151], v[236:239]\n"
      "ds_read_b128 v[140:143], v114 offset:28672\n"
      "v_cmp_nle_f16 vcc, 0x1c00, v243\n"
      "v_mfma_f32_16x16x32_bf16 v[236:239], a[188:191], v[180:183], v[236:239]\n"
      "s_mov_b64 s[34:35], vcc\n"
      "v_rsq_f16 v240, v243\n"
      "v_mfma_f32_16x16x32_bf16 v[156:159], a[156:159], v[148:151], v[156:159]\n"
      "s_waitcnt lgkmcnt(1)\n"
      "v_cmp_nge_f16 vcc, 0x7b00, v243\n"
      "v_mov_b32 v243, v240\n"
      "v_mfma_f32_16x16x32_bf16 v[156:159], a[192:195], v[180:183], v[156:159]\n"
      "ds_read_b128 v[216:219], v115 offset:28672\n"
      "v_cvt_pk_bf16_f32 v244, v244, v245\n"
      "v_mfma_f32_16x16x32_bf16 v[152:155], a[160:163], v[148:151], v[152:155]\n"
      "s_or_b64 s[34:35], s[34:35], vcc\n"
      "v_permlane16_swap_b32 v240, v243\n"
      "v_mfma_f32_16x16x32_bf16 v[152:155], a[196:199], v[180:183], v[152:155]\n"
      "s_sub_u32 s28, s27, 3\n"
      "v_pk_mul_f16 v225, v225, v243 op_sel_hi:[1,0]\n"
      "v_pk_mul_f16 v224, v224, v243 op_sel_hi:[1,0]\n"
      "v_mfma_f32_16x16x32_bf16 v[136:139], a[164:167], v[148:151], v[136:139]\n"
      "s_cselect_b32 s22, 0, -1\n"
      "v_pk_mul_f16 v226, v226, v243 op_sel_hi:[1,0]\n"
      "v_pk_mul_f16 v34, v34, v243 op_sel_hi:[1,0]\n"
      "v_mfma_f32_16x16x32_bf16 v[136:139], a[200:203], v[180:183], v[136:139]\n"
      "s_max_i32 m0, s28, 0\n"
      "v_pk_mul_f16 v227, v227, v243 op_sel_hi:[1,0]\n"
      "v_pk_mul_f16 v35, v35, v243 op_sel_hi:[1,0]\n"
      "v_mfma_f32_16x16x32_bf16 v[144:147], v[148:151], a[168:171], v[144:147]\n"
      "s_movrels_b32 s9, s80\n"
      "v_mul_f16 v240, s7, v240\n"
      "v_pk_mul_f16 v184, v184, v240 op_sel_hi:[1,0]\n"
      "v_mfma_f32_16x16x32_bf16 v[144:147], v[180:183], a[204:207], v[144:147]\n"
      "ds_read_b128 v[168:171], v119 offset:6144\n"
      "v_pk_mul_f16 v186, v186, v240 op_sel_hi:[1,0]\n"
      "v_mfma_f32_16x16x32_bf16 v[220:223], v[148:151], a[172:175], v[220:223]\n"
      "s_lshr_b32 s8, s9, 8\n"
      "v_pk_mul_f16 v32, v32, v240 op_sel_hi:[1,0]\n"
      "v_pk_mul_f16 v185, v185, v240 op_sel_hi:[1,0]\n"
      "v_mfma_f32_16x16x32_bf16 v[220:223], v[180:183], a[208:211], v[220:223]\n"
      "s_and_b32 s9, s9, 0xff\n"
      "v_pk_mul_f16 v187, v187, v240 op_sel_hi:[1,0]\n"
      "v_pk_mul_f16 v33, v33, v240 op_sel_hi:[1,0]\n"
      "v_mfma_f32_16x16x32_bf16 v[172:175], v[148:151], a[176:179], v[172:175]\n"
      "s_mul_i32 s28, s8, 1373568\n"
      "v_cvt_pk_bf16_f32 v194, v144, v145\n"
      "v_cvt_pk_bf16_f32 v195, v146, v147\n"
      "v_mfma_f32_16x16x32_bf16 v[172:175], v[180:183], a[212:215], v[172:175]\n"
      "s_mul_i32 s29, s9, 2688\n"
      "v_cvt_pk_bf16_f32 v245, v246, v247\n"
      "v_cvt_pk_bf16_f32 v246, v220, v221\n"
      "v_mfma_f32_16x16x32_bf16 v[148:151], a[0:3], v[164:167], 0\n"
      "s_add_u32 s28, s28, s29\n"
      "v_cvt_pk_bf16_f32 v247, v222, v223\n"
      "v_cvt_pk_bf16_f32 v160, v160, v161\n"
      "v_mfma_f32_16x16x32_bf16 v[148:151], a[36:39], v[204:207], v[148:151]\n"
      "s_waitcnt lgkmcnt(1)\n"
      "v_cvt_pk_bf16_f32 v161, v162, v163\n"
      "v_cvt_pk_bf16_f32 v162, v172, v173\n"
      "v_mfma_f32_16x16x32_bf16 v[144:147], a[4:7], v[164:167], 0\n"
      "s_cmp_eq_u32 s8, 72\n"
      "v_cvt_pk_bf16_f32 v163, v174, v175\n"
      "v_cvt_pk_f16_f32 v212, v212, v213\n"
      "v_mfma_f32_16x16x32_bf16 v[144:147], a[40:43], v[204:207], v[144:147]\n"
      "s_cselect_b32 s8, 0, 100270464\n"
      "v_cvt_pk_f16_f32 v156, v156, v157\n"
      "v_pk_mul_f16 v182, v212, v212\n"
      "v_mfma_f32_16x16x32_bf16 v[220:223], a[8:11], v[164:167], 0\n"
      "s_cmp_eq_u32 s9, 72\n"
      "v_pk_mul_f16 v181, v156, v156\n"
      "v_cvt_pk_f16_f32 v213, v214, v215\n"
      "v_mfma_f32_16x16x32_bf16 v[220:223], a[44:47], v[204:207], v[220:223]\n"
      "s_cselect_b32 s9, 0, 196224\n"
      "v_pk_fma_f16 v182, v213, v213, v182\n"
      "v_cvt_pk_f16_f32 v157, v158, v159\n"
      "v_mfma_f32_16x16x32_bf16 v[172:175], a[12:15], v[164:167], 0\n"
      "s_add_u32 s10, s8, s9\n"
      "v_pk_fma_f16 v181, v157, v157, v181\n"
      "v_cvt_pk_f16_f32 v214, v188, v189\n"
      "v_mfma_f32_16x16x32_bf16 v[172:175], a[48:51], v[204:207], v[172:175]\n"
      "s_add_u32 s28, s28, s10\n"
      "v_cvt_pk_f16_f32 v158, v152, v153\n"
      "v_pk_fma_f16 v182, v214, v214, v182\n"
      "v_mfma_f32_16x16x32_bf16 v[208:211], a[16:19], v[164:167], 0\n"
      "ds_read_b128 v[240:243], v114 offset:34816\n"
      "v_pk_fma_f16 v181, v158, v158, v181\n"
      "v_mfma_f32_16x16x32_bf16 v[208:211], a[52:55], v[204:207], v[208:211]\n"
      "s_add_u32 s20, s12, s28\n"
      "v_cvt_pk_f16_f32 v215, v190, v191\n"
      "v_pk_fma_f16 v182, v215, v215, v182\n"
      "v_mfma_f32_16x16x32_bf16 v[188:191], a[20:23], v[164:167], 0\n"
      "s_addc_u32 s21, s13, 0\n"
      "v_cvt_pk_f16_f32 v159, v154, v155\n"
      "v_pk_fma_f16 v181, v159, v159, v181\n"
      "v_mfma_f32_16x16x32_bf16 v[188:191], a[56:59], v[204:207], v[188:191]\n"
      "ds_read_b128 v[152:155], v115 offset:34816\n"
      "v_cvt_pk_f16_f32 v38, v236, v237\n"
      "v_mfma_f32_16x16x32_bf16 v[228:231], v[164:167], a[24:27], 0\n"
      "s_cmp_lg_u32 s10, s11\n"
      "v_pk_fma_f16 v182, v38, v38, v182\n"
      "v_cvt_pk_f16_f32 v40, v136, v137\n"
      "v_mfma_f32_16x16x32_bf16 v[228:231], v[204:207], a[60:63], v[228:231]\n"
      "s_cbranch_scc1 L_edge_17_%=\n"
      "L_eb_18_%=:\n"
      "v_mfma_f32_16x16x32_bf16 v[196:199], v[164:167], a[28:31], 0\n"
      "buffer_store_dwordx4 v[176:179], v116, s[20:23], 0 offen offset:0 sc1\n"
      "v_mfma_f32_16x16x32_bf16 v[196:199], v[204:207], a[64:67], v[196:199]\n"
      "ds_read_b128 v[234:237], v119 offset:12288\n"
      "v_pk_fma_f16 v181, v40, v40, v181\n"
      "v_mfma_f32_16x16x32_bf16 v[176:179], v[164:167], a[32:35], 0\n"
      "s_mov_b32 s5, s4\n"
      "v_cvt_pk_f16_f32 v39, v238, v239\n"
      "v_pk_fma_f16 v182, v39, v39, v182\n"
      "v_mfma_f32_16x16x32_bf16 v[176:179], v[204:207], a[68:71], v[176:179]\n"
      "s_add_u32 s4, s27, 1\n"
      "v_cvt_pk_f16_f32 v41, v138, v139\n"
      "v_pk_fma_f16 v181, v41, v41, v181\n"
      "v_mfma_f32_16x16x32_bf16 v[148:151], a[72:75], v[140:143], v[148:151]\n"
      "s_lshl_b32 s4, s4, 30\n"
      "v_permlane16_swap_b32 v182, v181\n"
      "v_mfma_f32_16x16x32_bf16 v[148:151], a[108:111], v[216:219], v[148:151]\n"
      "v_pk_add_f16 v182, v182, v181\n"
      "v_mov_b32 v181, v182\n"
      "v_mfma_f32_16x16x32_bf16 v[144:147], a[76:79], v[140:143], v[144:147]\n"
      "s_waitcnt lgkmcnt(1)\n"
      "v_permlane32_swap_b32_e64 v182, v181\n"
      "v_mfma_f32_16x16x32_bf16 v[144:147], a[112:115], v[216:219], v[144:147]\n"
      "v_pk_add_f16 v182, v182, v181\n"
      "v_add_f16_sdwa v181, v182, v182 dst_sel:DWORD dst_unused:UNUSED_PAD src0_sel:WORD_0 src1_sel:WORD_1\n"
      "v_mfma_f32_16x16x32_bf16 v[220:223], a[80:83], v[140:143], v[220:223]\n"
      "s_nop 0\n"
      "v_rsq_f16 v182, v181\n"
      "v_mfma_f32_16x16x32_bf16 v[220:223], a[116:119], v[216:219], v[220:223]\n"
      "ds_read_b128 v[136:139], v132 offset:1200\n"
      "v_cmp_nle_f16 vcc, 0x1c00, v181\n"
      "v_mfma_f32_16x16x32_bf16 v[172:175], a[84:87], v[140:143], v[172:175]\n"
      "buffer_store_dwordx4 v[168:171], v116, s[20:23], 0 offen offset:128 sc1\n"
      "v_mfma_f32_16x16x32_bf16 v[172:175], a[120:123], v[216:219], v[172:175]\n"
      "ds_read_b128 v[164:167], v133 offset:1168\n"
      "s_and_b32 s4, s4, 0x80000000\n"
      "v_mfma_f32_16x16x32_bf16 v[208:211], a[88:91], v[140:143], v[208:211]\n"
      "s_or_b64 s[34:35], s[34:35], vcc\n"
      "ds_read_b64 v[204:205], v132 offset:1192\n"
      "v_mfma_f32_16x16x32_bf16 v[208:211], a[124:127], v[216:219], v[208:211]\n"
      "ds_read_b64 v[206:207], v133 offset:1184\n"
      "v_cmp_nge_f16 vcc, 0x7b00, v181\n"
      "v_mfma_f32_16x16x32_bf16 v[188:191], a[92:95], v[140:143], v[188:191]\n"
      "ds_read_u16 v52, v134 offset:1168\n"
      "v_mov_b32 v181, v182\n"
      "v_mfma_f32_16x16x32_bf16 v[188:191], a[128:131], v[216:219], v[188:191]\n"
      "ds_read_u16 v60, v134 offset:1232\n"
      "v_permlane16_swap_b32_e64 v182, v181\n"
      "v_mfma_f32_16x16x32_bf16 v[228:231], v[140:143], a[96:99], v[228:231]\n"
      "ds_read_u16 v56, v134 offset:1200\n"
      "v_pk_mul_f16 v156, v156, v181 op_sel_hi:[1,0]\n"
      "v_mfma_f32_16x16x32_bf16 v[228:231], v[216:219], a[132:135], v[228:231]\n"
      "ds_read_b128 v[168:171], v123\n"
      "v_pk_mul_f16 v158, v158, v181 op_sel_hi:[1,0]\n"
      "v_mfma_f32_16x16x32_bf16 v[196:199], v[140:143], a[100:103], v[196:199]\n"
      "s_or_b64 s[34:35], s[34:35], vcc\n"
      "v_pk_mul_f16 v159, v159, v181 op_sel_hi:[1,0]\n"
      "v_pk_mul_f16 v157, v157, v181 op_sel_hi:[1,0]\n"
      "v_mfma_f32_16x16x32_bf16 v[196:199], v[216:219], a[136:139], v[196:199]\n"
      "s_waitcnt lgkmcnt(7)\n"
      "v_pk_mul_f16 v41, v41, v181 op_sel_hi:[1,0]\n"
      "v_pk_mul_f16 v40, v40, v181 op_sel_hi:[1,0]\n"
      "v_mfma_f32_16x16x32_bf16 v[176:179], v[140:143], a[104:107], v[176:179]\n"
      "buffer_store_dwordx4 v[234:237], v116, s[20:23], 0 offen offset:256 sc1\n"
      "v_mfma_f32_16x16x32_bf16 v[176:179], v[216:219], a[140:143], v[176:179]\n"
      "ds_read_b128 v[140:143], v123 offset:3072\n"
      "v_mul_f16 v182, s7, v182\n"
      "v_mfma_f32_16x16x32_bf16 v[148:151], a[144:147], v[240:243], v[148:151]\n"
      "ds_read_b128 v[236:239], v123 offset:9216\n"
      "v_pk_mul_f16 v212, v212, v182 op_sel_hi:[1,0]\n"
      "v_mfma_f32_16x16x32_bf16 v[148:151], a[180:183], v[152:155], v[148:151]\n"
      "ds_read_b128 v[216:219], v123 offset:6144\n"
      "v_pk_mul_f16 v213, v213, v182 op_sel_hi:[1,0]\n"
      "v_mfma_f32_16x16x32_bf16 v[144:147], a[148:151], v[240:243], v[144:147]\n"
      "s_nop 0\n"
      "v_pk_mul_f16 v215, v215, v182 op_sel_hi:[1,0]\n"
      "v_pk_mul_f16 v214, v214, v182 op_sel_hi:[1,0]\n"
      "v_mfma_f32_16x16x32_bf16 v[144:147], a[184:187], v[152:155], v[144:147]\n"
      "v_pk_mul_f16 v39, v39, v182 op_sel_hi:[1,0]\n"
      "v_pk_mul_f16 v38, v38, v182 op_sel_hi:[1,0]\n"
      "v_mfma_f32_16x16x32_bf16 v[220:223], a[152:155], v[240:243], v[220:223]\n"
      "v_cvt_pk_f16_f32 v148, v148, v149\n"
      "v_pk_mul_f16 v180, v148, v148\n"
      "v_mfma_f32_16x16x32_bf16 v[220:223], a[188:191], v[152:155], v[220:223]\n"
      "v_cvt_pk_f16_f32 v149, v150, v151\n"
      "v_pk_fma_f16 v180, v149, v149, v180\n"
      "v_mfma_f32_16x16x32_bf16 v[172:175], a[156:159], v[240:243], v[172:175]\n"
      "v_cvt_pk_f16_f32 v150, v144, v145\n"
      "v_pk_fma_f16 v180, v150, v150, v180\n"
      "v_mfma_f32_16x16x32_bf16 v[172:175], a[192:195], v[152:155], v[172:175]\n"
      "v_cvt_pk_f16_f32 v151, v146, v147\n"
      "v_pk_fma_f16 v180, v151, v151, v180\n"
      "v_mfma_f32_16x16x32_bf16 v[208:211], a[160:163], v[240:243], v[208:211]\n"
      "s_waitcnt lgkmcnt(2)\n"
      "v_cvt_pk_f16_f32 v44, v220, v221\n"
      "v_pk_fma_f16 v180, v44, v44, v180\n"
      "v_mfma_f32_16x16x32_bf16 v[208:211], a[196:199], v[152:155], v[208:211]\n"
      "s_nop 0\n"
      "v_cvt_pk_f16_f32 v172, v172, v173\n"
      "v_pk_mul_f16 v221, v172, v172\n"
      "v_mfma_f32_16x16x32_bf16 v[188:191], a[164:167], v[240:243], v[188:191]\n"
      "v_cvt_pk_f16_f32 v173, v174, v175\n"
      "v_pk_fma_f16 v221, v173, v173, v221\n"
      "v_mfma_f32_16x16x32_bf16 v[188:191], a[200:203], v[152:155], v[188:191]\n"
      "s_nop 0\n"
      "v_cvt_pk_f16_f32 v174, v208, v209\n"
      "v_pk_fma_f16 v221, v174, v174, v221\n"
      "v_mfma_f32_16x16x32_bf16 v[228:231], v[240:243], a[168:171], v[228:231]\n"
      "ds_read_b128 v[144:147], v123 offset:12288\n"
      "v_cvt_pk_f16_f32 v175, v210, v211\n"
      "v_mfma_f32_16x16x32_bf16 v[228:231], v[152:155], a[204:207], v[228:231]\n"
      "s_nop 0\n"
      "v_cvt_pk_f16_f32 v46, v188, v189\n"
      "v_pk_fma_f16 v221, v175, v175, v221\n"
      "v_mfma_f32_16x16x32_bf16 v[196:199], v[240:243], a[172:175], v[196:199]\n"
      "ds_read_b128 v[208:211], v123 offset:15360\n"
      "v_pk_fma_f16 v221, v46, v46, v221\n"
      "v_mfma_f32_16x16x32_bf16 v[196:199], v[152:155], a[208:211], v[196:199]\n"
      "s_waitcnt lgkmcnt(2)\n"
      "v_cvt_pk_f16_f32 v45, v222, v223\n"
      "v_pk_fma_f16 v180, v45, v45, v180\n"
      "v_mfma_f32_16x16x32_bf16 v[176:179], v[240:243], a[176:179], v[176:179]\n"
      "v_cvt_pk_f16_f32 v47, v190, v191\n"
      "v_pk_fma_f16 v221, v47, v47, v221\n"
      "v_mfma_f32_16x16x32_bf16 v[176:179], v[152:155], a[212:215], v[176:179]\n"
      "s_nop 0\n"
      "v_permlane16_swap_b32_e64 v180, v221\n"
      "v_mfma_f32_16x16x32_bf16 v[152:155], a[216:219], v[168:171], 0\n"
      "v_pk_add_f16 v180, v180, v221\n"
      "v_mov_b32_e64 v221, v180\n"
      "v_mfma_f32_16x16x32_bf16 v[152:155], a[228:231], v[140:143], v[152:155]\n"
      "s_nop 0\n"
      "v_permlane32_swap_b32 v180, v221\n"
      "v_mfma_f32_16x16x32_bf16 v[188:191], a[220:223], v[168:171], 0\n"
      "v_pk_add_f16 v180, v180, v221\n"
      "v_add_f16_sdwa v221, v180, v180 dst_sel:DWORD dst_unused:UNUSED_PAD src0_sel:WORD_0 src1_sel:WORD_1\n"
      "v_mfma_f32_16x16x32_bf16 v[188:191], a[232:235], v[140:143], v[188:191]\n"
      "s_nop 0\n"
      "v_rsq_f16 v180, v221\n"
      "v_mfma_f32_16x16x32_bf16 v[240:243], a[224:227], v[168:171], 0\n"
      "s_waitcnt lgkmcnt(0)\n"
      "v_cmp_nle_f16 vcc, 0x1c00, v221\n"
      "v_mfma_f32_16x16x32_bf16 v[240:243], a[236:239], v[140:143], v[240:243]\n"
      "s_or_b64 s[34:35], s[34:35], vcc\n"
      "v_cmp_nge_f16 vcc, 0x7b00, v221\n"
      "v_mov_b32 v221, v180\n"
      "v_mfma_f32_16x16x32_bf16 v[152:155], a[240:243], v[216:219], v[152:155]\n"
      "s_or_b64 s[34:35], s[34:35], vcc\n"
      "v_permlane16_swap_b32_e64 v180, v221\n"
      "v_mfma_f32_16x16x32_bf16 v[152:155], a[252:255], v[236:239], v[152:155]\n"
      "s_nop 0\n"
      "v_pk_mul_f16 v173, v173, v221 op_sel_hi:[1,0]\n"
      "v_pk_mul_f16 v172, v172, v221 op_sel_hi:[1,0]\n"
      "v_mfma_f32_16x16x32_bf16 v[188:191], a[244:247], v[216:219], v[188:191]\n"
      "s_or_b64 vcc, s[48:49], s[50:51]\n"
      "v_pk_mul_f16 v46, v46, v221 op_sel_hi:[1,0]\n"
      "v_pk_mul_f16 v174, v174, v221 op_sel_hi:[1,0]\n"
      "v_mfma_f32_16x16x32_bf16 v[188:191], v[0:3], v[236:239], v[188:191]\n"
      "v_pk_mul_f16 v175, v175, v221 op_sel_hi:[1,0]\n"
      "v_add_u32 v132, 2336, v132\n"
      "v_mfma_f32_16x16x32_bf16 v[240:243], a[248:251], v[216:219], v[240:243]\n"
      "v_add_u32 v133, 2336, v133\n"
      "v_pk_mul_f16 v47, v47, v221 op_sel_hi:[1,0]\n"
      "v_mfma_f32_16x16x32_bf16 v[240:243], v[4:7], v[236:239], v[240:243]\n"
      "s_nop 0\n"
      "v_mul_f16 v180, s7, v180\n"
      "v_add_u32 v134, 2336, v134\n"
      "v_mfma_f32_16x16x32_bf16 v[152:155], v[8:11], v[144:147], v[152:155]\n"
      "s_or_b64 vcc, vcc, s[34:35]\n"
      "v_pk_mul_f16 v148, v148, v180 op_sel_hi:[1,0]\n"
      "v_pk_mul_f16 v150, v150, v180 op_sel_hi:[1,0]\n"
      "v_mfma_f32_16x16x32_bf16 v[152:155], v[20:23], v[208:211], v[152:155]\n"
      "s_waitcnt vmcnt(3)\n"
      "v_pk_mul_f16 v149, v149, v180 op_sel_hi:[1,0]\n"
      "v_cvt_pk_bf16_f32 v51, v230, v231\n"
      "v_mfma_f32_16x16x32_bf16 v[188:191], v[12:15], v[144:147], v[188:191]\n"
      "ds_read_b32 v181, v122\n"
      "v_pk_mul_f16 v151, v151, v180 op_sel_hi:[1,0]\n"
      "v_mfma_f32_16x16x32_bf16 v[188:191], v[24:27], v[208:211], v[188:191]\n"
      "ds_read_b128 v[236:239], v119 offset:36864\n"
      "v_pk_mul_f16 v44, v44, v180 op_sel_hi:[1,0]\n"
      "v_mfma_f32_16x16x32_bf16 v[240:243], v[16:19], v[144:147], v[240:243]\n"
      "v_pk_mul_f16 v45, v45, v180 op_sel_hi:[1,0]\n"
      "v_fma_f32 v180, v152, v152, s39\n"
      "v_mfma_f32_16x16x32_bf16 v[240:243], v[28:31], v[208:211], v[240:243]\n"
      "v_cvt_pk_bf16_f32 v50, v228, v229\n"
      "v_mfma_f32_16x16x32_f16 v[220:223], v[224:227], v[184:187], v[78:81]\n"
      "v_cvt_pk_bf16_f32 v54, v196, v197\n"
      "v_cvt_pk_bf16_f32 v59, v178, v179\n"
      "v_mfma_f32_16x16x32_f16 v[220:223], v[34:37], v[32:35], v[220:223]\n"
      "v_cvt_pk_bf16_f32 v55, v198, v199\n"
      "v_fmac_f32 v180, v153, v153\n"
      "v_mfma_f32_16x16x32_f16 v[144:147], v[156:159], v[184:187], v[82:85]\n"
      "v_fmac_f32 v180, v154, v154\n"
      "v_fmac_f32 v180, v155, v155\n"
      "v_mfma_f32_16x16x32_f16 v[144:147], v[40:43], v[32:35], v[144:147]\n"
      "v_cvt_pk_bf16_f32 v58, v176, v177\n"
      "v_fmac_f32 v180, v188, v188\n"
      "v_fmac_f32_e64 v180, v189, v189\n"
      "v_mfma_f32_16x16x32_f16 v[216:219], v[136:139], v[184:187], v[70:73]\n"
      "s_waitcnt lgkmcnt(1)\n"
      "v_fmac_f32 v180, v190, v190\n"
      "v_fmac_f32 v180, v191, v191\n"
      "v_mfma_f32_16x16x32_f16 v[216:219], v[164:167], v[224:227], v[216:219]\n"
      "s_waitcnt lgkmcnt(0)\n"
      "v_fmac_f32_e64 v180, v240, v240\n"
      "v_bitop3_b32 v181, v181, v239, s40 bitop3:0xa8\n"
      "v_mfma_f32_16x16x32_f16 v[216:219], v[204:207], v[32:35], v[216:219]\n"
      "v_fmac_f32_e64 v180, v241, v241\n"
      "v_fmac_f32_e64 v180, v242, v242\n"
      "v_fmac_f32_e64 v180, v243, v243\n"
      "v_or3_b32 v236, v236, v237, v238\n"
      "v_mfma_f32_16x16x32_f16 v[196:199], v[172:175], v[184:187], v[86:89]\n"
      "v_and_b32 v236, s40, v236\n"
      "v_or3_b32 v236, v236, v181, s4\n"
      "v_mfma_f32_16x16x32_f16 v[196:199], v[46:49], v[32:35], v[196:199]\n"
      "ds_write_b32 v129, v236 offset:2048\n"
      "s_cbranch_scc1 L_gen_19_%=\n"
      "L_fast_22_%=:\n"
      "v_mfma_f32_16x16x32_f16 v[236:239], v[224:227], v[212:215], v[90:93]\n"
      "ds_read_b128 v[228:231], v123 offset:1024\n"
      "v_exp_f32_e64 v144, v144\n"
      "v_mfma_f32_16x16x32_f16 v[236:239], v[34:37], v[38:41], v[236:239]\n"
      "ds_read_b128 v[176:179], v123 offset:4096\n"
      "v_exp_f32 v147, v147\n"
      "v_mfma_f32_16x16x32_f16 v[168:171], v[156:159], v[212:215], v[94:97]\n"
      "ds_read_b128 v[184:187], v120\n"
      "v_exp_f32 v145, v145\n"
      "v_exp_f32 v222, v222\n"
      "v_mfma_f32_16x16x32_f16 v[168:171], v[40:43], v[38:41], v[168:171]\n"
      "ds_read_b128 v[208:211], v128 offset:2048\n"
      "v_exp_f32 v220, v220\n"
      "v_exp_f32 v196, v196\n"
      "v_exp_f32 v146, v146\n"
      "v_exp_f32 v197, v197\n"
      "v_mfma_f32_16x16x32_f16 v[140:143], v[136:139], v[212:215], v[72:75]\n"
      "v_exp_f32 v221, v221\n"
      "v_mfma_f32_16x16x32_f16 v[140:143], v[164:167], v[156:159], v[140:143]\n"
      "v_exp_f32_e64 v223, v223\n"
      "v_mfma_f32_16x16x32_f16 v[140:143], v[204:207], v[38:41], v[140:143]\n"
      "ds_read_b128 v[252:255], v123 offset:7168\n"
      "v_cvt_pk_bf16_f32 v220, v220, v221\n"
      "v_cvt_pk_bf16_f32 v221, v222, v223\n"
      "v_mfma_f32_16x16x32_f16 v[232:235], v[172:175], v[212:215], v[98:101]\n"
      "ds_read_b128 v[212:215], v123 offset:10240\n"
      "s_waitcnt lgkmcnt(3)\n"
      "v_mfma_f32_16x16x32_f16 v[232:235], v[46:49], v[38:41], v[232:235]\n"
      "buffer_store_dwordx4 v[184:187], v117, s[20:23], 0 offen offset:0 sc1\n"
      "v_mfma_f32_16x16x32_bf16 v[248:251], a[216:219], v[228:231], 0\n"
      "v_exp_f32 v198, v198\n"
      "v_mfma_f32_16x16x32_bf16 v[248:251], a[228:231], v[176:179], v[248:251]\n"
      "ds_read_b128 v[184:187], v123 offset:16384\n"
      "v_cvt_pk_bf16_f32 v222, v144, v145\n"
      "v_exp_f32_e64 v236, v236\n"
      "v_cvt_pk_bf16_f32 v223, v146, v147\n"
      "v_exp_f32_e64 v238, v238\n"
      "v_mfma_f32_16x16x32_bf16 v[144:147], a[220:223], v[228:231], 0\n"
      "v_exp_f32 v199, v199\n"
      "v_mfma_f32_16x16x32_bf16 v[144:147], a[232:235], v[176:179], v[144:147]\n"
      "s_waitcnt lgkmcnt(3)\n"
      "v_bitop3_b32 v210, v210, v211, s4 bitop3:0x7e\n"
      "v_exp_f32 v216, v216\n"
      "v_bitop3_b32 v208, v208, v209, s4 bitop3:0x7e\n"
      "v_cvt_pk_bf16_f32 v64, v216, 0\n"
      "v_mfma_f32_16x16x32_bf16 v[200:203], a[224:227], v[228:231], 0\n"
      "s_waitcnt lgkmcnt(1)\n"
      "v_cvt_pk_bf16_f32 v62, v196, v197\n"
      "v_cvt_pk_bf16_f32 v63, v198, v199\n"
      "v_mfma_f32_16x16x32_bf16 v[200:203], a[236:239], v[176:179], v[200:203]\n"
      "ds_read_b128 v[176:179], v123 offset:13312\n"
      "v_or_b32 v208, v208, v210\n"
      "v_exp_f32_e64 v171, v171\n"
      "v_mfma_f32_16x16x32_bf16 v[248:251], a[240:243], v[252:255], v[248:251]\n"
      "ds_read_b128 v[196:199], v121\n"
      "v_cmp_ne_u32 vcc, 0, v208\n"
      "v_mfma_f32_16x16x32_bf16 v[248:251], a[252:255], v[212:215], v[248:251]\n"
      "s_mov_b64 s[48:49], vcc\n"
      "v_exp_f32 v170, v170\n"
      "v_mfma_f32_16x16x32_bf16 v[144:147], a[244:247], v[252:255], v[144:147]\n"
      "v_exp_f32_e64 v237, v237\n"
      "v_mfma_f32_16x16x32_bf16 v[144:147], v[0:3], v[212:215], v[144:147]\n"
      "s_cbranch_vccnz L_flg_23_%=\n"
      "L_fb_24_%=:\n"
      "v_exp_f32 v239, v239\n"
      "v_cvt_pk_bf16_f32 v236, v236, v237\n"
      "v_cvt_pk_bf16_f32 v237, v238, v239\n"
      "v_exp_f32 v168, v168\n"
      "v_mfma_f32_16x16x32_bf16 v[200:203], a[248:251], v[252:255], v[200:203]\n"
      "v_exp_f32 v169, v169\n"
      "v_mfma_f32_16x16x32_bf16 v[200:203], v[4:7], v[212:215], v[200:203]\n"
      "s_waitcnt lgkmcnt(1)\n"
      "v_exp_f32 v232, v232\n"
      "v_mfma_f32_16x16x32_bf16 v[248:251], v[8:11], v[176:179], v[248:251]\n"
      "ds_read_b128 v[208:211], v123 offset:5120\n"
      "v_cvt_pk_bf16_f32 v239, v170, v171\n"
      "v_mfma_f32_16x16x32_bf16 v[248:251], v[20:23], v[184:187], v[248:251]\n"
      "ds_read_b128 v[212:215], v123 offset:2048\n"
      "v_cvt_pk_bf16_f32 v238, v168, v169\n"
      "v_mfma_f32_16x16x32_bf16 v[144:147], v[12:15], v[176:179], v[144:147]\n"
      "s_waitcnt lgkmcnt(2)\n"
      "v_exp_f32 v233, v233\n"
      "v_mfma_f32_16x16x32_bf16 v[144:147], v[24:27], v[184:187], v[144:147]\n"
      "s_mov_b32 exec_hi, 0\n"
      "buffer_store_dwordx4 v[196:199], v118, s[20:23], 0 offen offset:0 sc1\n"
      "s_mov_b32 exec_hi, -1\n"
      "v_mfma_f32_16x16x32_bf16 v[200:203], v[16:19], v[176:179], v[200:203]\n"
      "v_exp_f32 v234, v234\n"
      "v_mfma_f32_16x16x32_bf16 v[200:203], v[28:31], v[184:187], v[200:203]\n"
      "v_exp_f32 v140, v140\n"
      "v_mfma_f32_16x16x32_bf16 v[196:199], v[66:69], v[220:223], 0\n"
      "v_exp_f32_e64 v235, v235\n"
      "v_mfma_f32_16x16x32_bf16 v[196:199], v[66:69], v[62:65], v[196:199]\n"
      "v_fma_f32 v142, v248, v248, s39\n"
      "v_fmac_f32 v142, v249, v249\n"
      "v_mfma_f32_16x16x32_bf16 v[168:171], v[192:195], v[220:223], 0\n"
      "v_fmac_f32 v142, v250, v250\n"
      "v_fmac_f32 v142, v251, v251\n"
      "v_mfma_f32_16x16x32_bf16 v[168:171], v[50:53], v[62:65], v[168:171]\n"
      "v_fmac_f32 v142, v144, v144\n"
      "v_fmac_f32 v142, v145, v145\n"
      "v_mfma_f32_16x16x32_bf16 v[228:231], v[244:247], v[220:223], 0\n"
      "v_fmac_f32 v142, v146, v146\n"
      "v_fmac_f32 v142, v147, v147\n"
      "v_mfma_f32_16x16x32_bf16 v[228:231], v[54:57], v[62:65], v[228:231]\n"
      "v_fmac_f32 v142, v200, v200\n"
      "v_fmac_f32 v142, v201, v201\n"
      "v_mfma_f32_16x16x32_bf16 v[176:179], v[160:163], v[220:223], 0\n"
      "s_waitcnt lgkmcnt(0)\n"
      "v_fmac_f32 v142, v202, v202\n"
      "v_fmac_f32 v142, v203, v203\n"
      "v_mfma_f32_16x16x32_bf16 v[176:179], v[58:61], v[62:65], v[176:179]\n"
      "v_cvt_pk_bf16_f32 v64, v140, 0\n"
      "v_cvt_pk_bf16_f32 v62, v232, v233\n"
      "v_mfma_f32_16x16x32_f16 v[220:223], v[224:227], v[148:151], v[102:105]\n"
      "v_cvt_pk_bf16_f32 v63, v234, v235\n"
      "v_cndmask_b32 v217, v217, v141, s[42:43]\n"
      "v_mfma_f32_16x16x32_f16 v[220:223], v[34:37], v[44:47], v[220:223]\n"
      "v_rcp_f32 v196, v196\n"
      "v_mfma_f32_16x16x32_f16 v[224:227], v[156:159], v[148:151], v[106:109]\n"
      "v_mul_f32 v168, v196, v168\n"
      "v_mul_f32 v169, v196, v169\n"
      "v_mfma_f32_16x16x32_f16 v[224:227], v[40:43], v[44:47], v[224:227]\n"
      "v_mul_f32 v171, v196, v171\n"
      "v_mul_f32 v177, v196, v177\n"
      "v_cvt_pk_bf16_f32 v168, v168, v169\n"
      "v_mfma_f32_16x16x32_f16 v[156:159], v[136:139], v[148:151], v[74:77]\n"
      "ds_read_b128 v[184:187], v123 offset:8192\n"
      "v_mul_f32 v170, v196, v170\n"
      "v_mfma_f32_16x16x32_f16 v[156:159], v[164:167], v[172:175], v[156:159]\n"
      "v_cvt_pk_bf16_f32 v169, v170, v171\n"
      "v_mul_f32 v228, v196, v228\n"
      "v_mfma_f32_16x16x32_f16 v[156:159], v[204:207], v[44:47], v[156:159]\n"
      "s_add_u32 s28, s27, 2\n"
      "v_mul_f32 v179, v196, v179\n"
      "v_mul_f32 v231, v196, v231\n"
      "v_mfma_f32_16x16x32_f16 v[232:235], v[172:175], v[148:151], v[110:113]\n"
      "ds_read_b128 v[172:175], v123 offset:11264\n"
      "v_mul_f32_e64 v229, v196, v229\n"
      "v_mfma_f32_16x16x32_f16 v[232:235], v[46:49], v[44:47], v[232:235]\n"
      "v_mul_f32_e64 v230, v196, v230\n"
      "v_cvt_pk_bf16_f32 v171, v230, v231\n"
      "v_cvt_pk_bf16_f32 v170, v228, v229\n"
      "v_mfma_f32_16x16x32_f16 v[148:151], v[164:167], v[136:139], v[76:79]\n"
      "ds_write_b128 v124, v[168:171] offset:18432\n"
      "v_mfma_f32_16x16x32_bf16 v[164:167], v[66:69], v[236:239], 0\n"
      "ds_read_b128 v[136:139], v123 offset:14336\n"
      "v_mul_f32 v176, v196, v176\n"
      "v_mfma_f32_16x16x32_bf16 v[164:167], v[66:69], v[62:65], v[164:167]\n"
      "v_cvt_pk_bf16_f32 v176, v176, v177\n"
      "v_exp_f32 v227, v227\n"
      "v_mfma_f32_16x16x16_f16 v[148:151], v[206:207], v[204:205], v[148:151]\n"
      "s_min_u32 m0, s28, s3\n"
      "v_mul_f32 v178, v196, v178\n"
      "v_exp_f32_e64 v222, v222\n"
      "v_mfma_f32_16x16x32_bf16 v[204:207], v[192:195], v[236:239], 0\n"
      "v_cvt_pk_bf16_f32 v177, v178, v179\n"
      "v_cndmask_b32 v217, v217, v157, s[44:45]\n"
      "v_mfma_f32_16x16x32_bf16 v[204:207], v[50:53], v[62:65], v[204:207]\n"
      "s_movrels_b32 s9, s80\n"
      "v_cndmask_b32 v217, v217, v149, s[46:47]\n"
      "v_exp_f32 v217, v217\n"
      "v_mfma_f32_16x16x32_bf16 v[148:151], v[244:247], v[236:239], 0\n"
      "s_waitcnt lgkmcnt(2)\n"
      "s_lshr_b32 s8, s9, 8\n"
      "v_cvt_pk_bf16_f32 v217, v217, v217\n"
      "v_mfma_f32_16x16x32_bf16 v[148:151], v[54:57], v[62:65], v[148:151]\n"
      "ds_write_b16 v130, v217\n"
      "v_mfma_f32_16x16x32_bf16 v[196:199], v[160:163], v[236:239], 0\n"
      "ds_read_b128 v[236:239], v131\n"
      "s_and_b32 s9, s9, 0xff\n"
      "v_mfma_f32_16x16x32_bf16 v[196:199], v[58:61], v[62:65], v[196:199]\n"
      "ds_read_b128 v[216:219], v131 offset:64\n"
      "v_exp_f32 v232, v232\n"
      "v_exp_f32 v221, v221\n"
      "v_mfma_f32_16x16x32_bf16 v[228:231], a[216:219], v[212:215], 0\n"
      "s_mul_i32 s10, s8, 73\n"
      "v_exp_f32 v220, v220\n"
      "v_mfma_f32_16x16x32_bf16 v[228:231], a[228:231], v[208:211], v[228:231]\n"
      "s_add_u32 s10, s10, s9\n"
      "v_exp_f32_e64 v233, v233\n"
      "v_mfma_f32_16x16x32_bf16 v[168:171], a[220:223], v[212:215], 0\n"
      "s_mul_i32 s28, s10, 4802\n"
      "v_exp_f32 v223, v223\n"
      "v_mfma_f32_16x16x32_bf16 v[168:171], a[232:235], v[208:211], v[168:171]\n"
      "s_add_u32 s56, s18, s28\n"
      "v_exp_f32 v224, v224\n"
      "v_mfma_f32_16x16x32_bf16 v[228:231], a[240:243], v[184:187], v[228:231]\n"
      "s_addc_u32 s57, s19, 0\n"
      "v_cvt_pk_bf16_f32 v220, v220, v221\n"
      "v_cvt_pk_bf16_f32 v221, v222, v223\n"
      "v_mfma_f32_16x16x32_bf16 v[228:231], a[252:255], v[172:175], v[228:231]\n"
      "s_and_b32 s56, s56, -16\n"
      "v_exp_f32 v225, v225\n"
      "v_mfma_f32_16x16x32_bf16 v[168:171], a[244:247], v[184:187], v[168:171]\n"
      "s_sub_u32 s58, s56, 110592\n"
      "v_exp_f32 v226, v226\n"
      "v_mfma_f32_16x16x32_bf16 v[168:171], v[0:3], v[172:175], v[168:171]\n"
      "s_waitcnt lgkmcnt(0)\n"
      "v_cvt_pk_bf16_f32 v222, v224, v225\n"
      "v_cvt_pk_bf16_f32 v223, v226, v227\n"
      "v_mfma_f32_16x16x32_bf16 v[252:255], a[224:227], v[212:215], 0\n"
      "ds_read_b128 v[224:227], v123 offset:17408\n"
      "v_cvt_pk_bf16_f32 v62, v232, v233\n"
      "v_mfma_f32_16x16x32_bf16 v[252:255], a[236:239], v[208:211], v[252:255]\n"
      "ds_read_b128 v[208:211], v115\n"
      "v_exp_f32 v234, v234\n"
      "v_mfma_f32_16x16x32_bf16 v[252:255], a[248:251], v[184:187], v[252:255]\n"
      "ds_read_b128 v[212:215], v114\n"
      "s_subb_u32 s59, s57, 0\n"
      "v_mfma_f32_16x16x32_bf16 v[252:255], v[4:7], v[172:175], v[252:255]\n"
      "s_sub_u32 s56, s56, 73728\n"
      "v_exp_f32 v235, v235\n"
      "v_mfma_f32_16x16x32_bf16 v[172:175], v[236:239], v[66:69], 0\n"
      "s_subb_u32 s57, s57, 0\n"
      "v_exp_f32_e64 v156, v156\n"
      "v_mfma_f32_16x16x32_bf16 v[172:175], v[216:219], v[66:69], v[172:175]\n"
      "s_mul_i32 s28, s8, 1373568\n"
      "v_cvt_pk_bf16_f32 v64, v156, 0\n"
      "v_cvt_pk_bf16_f32 v63, v234, v235\n"
      "v_mfma_f32_16x16x32_bf16 v[184:187], v[236:239], v[192:195], 0\n"
      "s_mul_i32 s29, s9, 2688\n"
      "v_rcp_f32_e64 v164, v164\n"
      "v_mfma_f32_16x16x32_bf16 v[184:187], v[216:219], v[50:53], v[184:187]\n"
      "s_add_u32 s28, s28, s29\n"
      "v_mul_f32 v205, v164, v205\n"
      "v_mul_f32 v204, v164, v204\n"
      "v_mfma_f32_16x16x32_bf16 v[156:159], v[66:69], v[220:223], 0\n"
      "s_waitcnt lgkmcnt(2)\n"
      "v_mul_f32 v207, v164, v207\n"
      "v_cvt_pk_bf16_f32 v204, v204, v205\n"
      "v_mfma_f32_16x16x32_bf16 v[156:159], v[66:69], v[62:65], v[156:159]\n"
      "s_cmp_eq_u32 s8, 72\n"
      "v_mul_f32 v206, v164, v206\n"
      "v_cvt_pk_bf16_f32 v205, v206, v207\n"
      "v_mfma_f32_16x16x32_bf16 v[228:231], v[8:11], v[136:139], v[228:231]\n"
      "s_waitcnt lgkmcnt(0)\n"
      "v_mul_f32 v149, v164, v149\n"
      "v_mfma_f32_16x16x32_bf16 v[228:231], v[20:23], v[224:227], v[228:231]\n"
      "s_cselect_b32 s8, 0, 100270464\n"
      "v_mul_f32_e64 v148, v164, v148\n"
      "v_cvt_pk_bf16_f32 v206, v148, v149\n"
      "v_mfma_f32_16x16x32_bf16 v[168:171], v[12:15], v[136:139], v[168:171]\n"
      "s_cmp_eq_u32 s9, 72\n"
      "v_mul_f32 v150, v164, v150\n"
      "v_mul_f32 v151, v164, v151\n"
      "v_mfma_f32_16x16x32_bf16 v[168:171], v[24:27], v[224:227], v[168:171]\n"
      "s_cselect_b32 s9, 0, 196224\n"
      "v_mul_f32 v197, v164, v197\n"
      "v_cvt_pk_bf16_f32 v207, v150, v151\n"
      "v_mfma_f32_16x16x32_bf16 v[252:255], v[16:19], v[136:139], v[252:255]\n"
      "s_add_u32 s10, s8, s9\n"
      "v_mul_f32_e64 v196, v164, v196\n"
      "v_cvt_pk_bf16_f32 v196, v196, v197\n"
      "v_mfma_f32_16x16x32_bf16 v[252:255], v[28:31], v[224:227], v[252:255]\n"
      "ds_write_b128 v124, v[204:207] offset:19456\n"
      "v_mfma_f32_16x16x32_bf16 v[224:227], v[192:195], v[220:223], 0\n"
      "ds_read_b128 v[204:207], v114 offset:6144\n"
      "v_mul_f32_e64 v198, v164, v198\n"
      "v_mfma_f32_16x16x32_bf16 v[224:227], v[50:53], v[62:65], v[224:227]\n"
      "ds_read_b128 v[136:139], v115 offset:6144\n"
      "v_mul_f32 v199, v164, v199\n"
      "v_mfma_f32_16x16x32_bf16 v[164:167], v[244:247], v[220:223], 0\n"
      "s_add_u32 s28, s28, s10\n"
      "v_cvt_pk_bf16_f32 v197, v198, v199\n"
      "v_fma_f32 v199, v228, v228, s39\n"
      "v_mfma_f32_16x16x32_bf16 v[164:167], v[54:57], v[62:65], v[164:167]\n"
      "ds_write2st64_b64 v125, v[176:177], v[196:197] offset0:36 offset1:38\n"
      "v_mfma_f32_16x16x32_bf16 v[148:151], v[160:163], v[220:223], 0\n"
      "s_add_u32 s52, s16, s28\n"
      "v_fmac_f32 v199, v229, v229\n"
      "v_fmac_f32 v199, v230, v230\n"
      "v_mfma_f32_16x16x32_bf16 v[148:151], v[58:61], v[62:65], v[148:151]\n"
      "s_addc_u32 s53, s17, 0\n"
      "v_fmac_f32 v199, v231, v231\n"
      "v_fmac_f32 v199, v168, v168\n"
      "v_mfma_f32_16x16x32_bf16 v[220:223], v[236:239], v[244:247], 0\n"
      "v_fmac_f32 v199, v169, v169\n"
      "v_fmac_f32 v199, v170, v170\n"
      "v_mfma_f32_16x16x32_bf16 v[220:223], v[216:219], v[54:57], v[220:223]\n"
      "s_add_u32 s27, s27, 1\n"
      "v_fmac_f32 v199, v171, v171\n"
      "v_fmac_f32_e64 v199, v252, v252\n"
      "v_mfma_f32_16x16x32_bf16 v[176:179], v[236:239], v[160:163], 0\n"
      "s_cmp_ge_u32 s27, s26\n"
      "v_fmac_f32 v199, v253, v253\n"
      "v_fmac_f32 v199, v254, v254\n"
      "v_mfma_f32_16x16x32_bf16 v[176:179], v[216:219], v[58:61], v[176:179]\n"
      "s_waitcnt lgkmcnt(1)\n"
      "v_fmac_f32 v199, v255, v255\n"
      "v_mov_b32 v236, v199\n"
      "v_mfma_f32_16x16x32_bf16 v[216:219], a[0:3], v[212:215], 0\n"
      "s_cselect_b32 s28, 1, 0\n"
      "v_permlane16_swap_b32 v180, v142\n"
      "v_mfma_f32_16x16x32_bf16 v[216:219], a[36:39], v[208:211], v[216:219]\n"
      "s_xor_b32 s29, s10, s11\n"
      "v_mov_b32_dpp v172, v172 quad_perm:[0,0,0,0] row_mask:0xf bank_mask:0xf\n"
      "v_add_f32 v180, v180, v142\n"
      "v_mfma_f32_16x16x32_bf16 v[140:143], a[4:7], v[212:215], 0\n"
      "v_permlane16_swap_b32_e64 v199, v236\n"
      "v_mfma_f32_16x16x32_bf16 v[140:143], a[40:43], v[208:211], v[140:143]\n"
      "v_cndmask_b32 v184, v184, v220, s[42:43]\n"
      "v_add_f32 v199, v199, v236\n"
      "v_mfma_f32_16x16x32_bf16 v[220:223], a[8:11], v[212:215], 0\n"
      "s_nop 0\n"
      "v_permlane32_swap_b32 v180, v199\n"
      "v_mfma_f32_16x16x32_bf16 v[220:223], a[44:47], v[208:211], v[220:223]\n"
      "v_add_f32 v180, v180, v199\n"
      "v_cndmask_b32 v184, v184, v176, s[44:45]\n"
      "v_mfma_f32_16x16x32_bf16 v[196:199], a[12:15], v[212:215], 0\n"
      "v_rcp_f32 v172, v172\n"
      "v_mfma_f32_16x16x32_bf16 v[196:199], a[48:51], v[208:211], v[196:199]\n"
      "ds_write_b32 v129, v180 offset:1024\n"
      "v_mfma_f32_16x16x32_bf16 v[236:239], a[16:19], v[212:215], 0\n"
      "v_mul_f32 v184, v172, v184\n"
      "v_cvt_pk_bf16_f32 v184, v184, v184\n"
      "v_mfma_f32_16x16x32_bf16 v[236:239], a[52:55], v[208:211], v[236:239]\n"
      "ds_write_b16 v135, v184 offset:16\n"
      "v_mfma_f32_16x16x32_bf16 v[172:175], a[20:23], v[212:215], 0\n"
      "s_or_b32 s28, s28, s29\n"
      "v_mfma_f32_16x16x32_bf16 v[172:175], a[56:59], v[208:211], v[172:175]\n"
      "L_join_21_%=:\n"
      "s_cbranch_scc1 L_tail1_%=\n"
      "L_mid_%=:\n"
      "v_mfma_f32_16x16x32_bf16 v[184:187], v[212:215], a[24:27], 0\n"
      "v_rcp_f32 v156, v156\n"
      "v_mfma_f32_16x16x32_bf16 v[184:187], v[208:211], a[60:63], v[184:187]\n"
      "s_waitcnt lgkmcnt(0)\n"
      "s_barrier\n"
      "v_mfma_f32_16x16x32_bf16 v[176:179], v[212:215], a[28:31], 0\n"
      "s_add_u32 m0, s76, 18432\n"
      "v_mul_f32 v224, v156, v224\n"
      "v_mul_f32 v225, v156, v225\n"
      "v_mfma_f32_16x16x32_bf16 v[176:179], v[208:211], a[64:67], v[176:179]\n"
      "v_cvt_pk_bf16_f32 v224, v224, v225\n"
      "v_mul_f32 v226, v156, v226\n"
      "v_mfma_f32_16x16x32_bf16 v[244:247], v[212:215], a[32:35], 0\n"
      "ds_read_b128 v[180:183], v128 offset:1024\n"
      "v_mul_f32_e64 v227, v156, v227\n"
      "v_mfma_f32_16x16x32_bf16 v[244:247], v[208:211], a[68:71], v[244:247]\n"
      "global_load_lds_dwordx4 v116, s[52:53]\n"
      "v_mfma_f32_16x16x32_bf16 v[216:219], a[72:75], v[204:207], v[216:219]\n"
      "ds_read_b128 v[212:215], v114 offset:12288\n"
      "v_mul_f32_e64 v164, v156, v164\n"
      "v_mfma_f32_16x16x32_bf16 v[216:219], a[108:111], v[136:139], v[216:219]\n"
      "v_cvt_pk_bf16_f32 v225, v226, v227\n"
      "v_mul_f32 v165, v156, v165\n"
      "v_mfma_f32_16x16x32_bf16 v[140:143], a[76:79], v[204:207], v[140:143]\n"
      "v_cvt_pk_bf16_f32 v226, v164, v165\n"
      "v_mul_f32 v166, v156, v166\n"
      "v_mfma_f32_16x16x32_bf16 v[140:143], a[112:115], v[136:139], v[140:143]\n"
      "v_mul_f32_e64 v167, v156, v167\n"
      "v_cvt_pk_bf16_f32 v227, v166, v167\n"
      "v_mfma_f32_16x16x32_bf16 v[220:223], a[80:83], v[204:207], v[220:223]\n"
      "ds_read_b128 v[164:167], v115 offset:12288\n"
      "v_mul_f32_e64 v148, v156, v148\n"
      "v_mfma_f32_16x16x32_bf16 v[220:223], a[116:119], v[136:139], v[220:223]\n"
      "ds_write_b128 v124, v[224:227] offset:20480\n"
      "v_mfma_f32_16x16x32_bf16 v[196:199], a[84:87], v[204:207], v[196:199]\n"
      "s_waitcnt lgkmcnt(3)\n"
      "v_mul_f32 v149, v156, v149\n"
      "v_cvt_pk_bf16_f32 v148, v148, v149\n"
      "v_mfma_f32_16x16x32_bf16 v[196:199], a[120:123], v[136:139], v[196:199]\n"
      "v_mul_f32 v150, v156, v150\n"
      "v_mul_f32 v151, v156, v151\n"
      "v_mfma_f32_16x16x32_bf16 v[236:239], a[88:91], v[204:207], v[236:239]\n"
      "s_add_u32 m0, s76, 24448\n"
      "v_cvt_pk_bf16_f32 v149, v150, v151\n"
      "v_add_f32 v180, |v180|, |v181|\n"
      "v_mfma_f32_16x16x32_bf16 v[236:239], a[124:127], v[136:139], v[236:239]\n"
      "ds_write_b64 v125, v[148:149] offset:20480\n"
      "v_mfma_f32_16x16x32_bf16 v[172:175], a[92:95], v[204:207], v[172:175]\n"
      "global_load_lds_dwordx4 v116, s[52:53] offset:128\n"
      "v_mfma_f32_16x16x32_bf16 v[172:175], a[128:131], v[136:139], v[172:175]\n"
      "s_waitcnt lgkmcnt(2)\n"
      "v_add_f32 v182, |v182|, |v183|\n"
      "v_add_f32 v180, v180, v182\n"
      "v_mfma_f32_16x16x32_bf16 v[184:187], v[204:207], a[96:99], v[184:187]\n"
      "v_rsq_f32 v180, v180\n"
      "v_mfma_f32_16x16x32_bf16 v[184:187], v[136:139], a[132:135], v[184:187]\n"
      "v_mul_f32 v180, s41, v180\n"
      "v_mfma_f32_16x16x32_bf16 v[176:179], v[204:207], a[100:103], v[176:179]\n"
      "ds_read_b128 v[156:159], v114 offset:2048\n"
      "v_mov_b32 v182, v180\n"
      "v_mfma_f32_16x16x32_bf16 v[176:179], v[136:139], a[136:139], v[176:179]\n"
      "s_add_u32 m0, s76, 30464\n"
      "v_permlane32_swap_b32 v180, v182\n"
      "v_mfma_f32_16x16x32_bf16 v[244:247], v[204:207], a[104:107], v[244:247]\n"
      "ds_read_b128 v[148:151], v115 offset:2048\n"
      "v_mov_b32 v183, v180\n"
      "v_mfma_f32_16x16x32_bf16 v[244:247], v[136:139], a[140:143], v[244:247]\n"
      "s_nop 0\n"
      "v_permlane16_swap_b32 v180, v183\n"
      "v_mfma_f32_16x16x32_bf16 v[216:219], a[144:147], v[212:215], v[216:219]\n"
      "v_mul_f32 v152, v180, v152\n"
      "v_mul_f32 v153, v180, v153\n"
      "v_mfma_f32_16x16x32_bf16 v[216:219], a[180:183], v[164:167], v[216:219]\n"
      "v_mul_f32 v154, v180, v154\n"
      "v_cvt_pk_bf16_f32 v152, v152, v153\n"
      "v_mfma_f32_16x16x32_bf16 v[140:143], a[148:151], v[212:215], v[140:143]\n"
      "v_mul_f32_e64 v155, v180, v155\n"
      "v_cvt_pk_bf16_f32 v153, v154, v155\n"
      "v_mfma_f32_16x16x32_bf16 v[140:143], a[184:187], v[164:167], v[140:143]\n"
      "global_load_lds_dwordx4 v116, s[52:53] offset:256\n"
      "v_mfma_f32_16x16x32_bf16 v[220:223], a[152:155], v[212:215], v[220:223]\n"
      "s_add_u32 m0, s77, 18432\n"
      "v_mul_f32 v189, v180, v189\n"
      "v_mul_f32 v188, v180, v188\n"
      "v_mfma_f32_16x16x32_bf16 v[220:223], a[188:191], v[164:167], v[220:223]\n"
      "v_cvt_pk_bf16_f32 v154, v188, v189\n"
      "v_mul_f32_e64 v190, v180, v190\n"
      "v_mfma_f32_16x16x32_bf16 v[196:199], a[156:159], v[212:215], v[196:199]\n"
      "s_waitcnt lgkmcnt(0)\n"
      "v_mul_f32 v191, v180, v191\n"
      "v_mul_f32 v241, v180, v241\n"
      "v_mfma_f32_16x16x32_bf16 v[196:199], a[192:195], v[164:167], v[196:199]\n"
      "v_mul_f32 v240, v180, v240\n"
      "v_cvt_pk_bf16_f32 v155, v190, v191\n"
      "v_mfma_f32_16x16x32_bf16 v[236:239], a[160:163], v[212:215], v[236:239]\n"
      "v_mul_f32_e64 v242, v180, v242\n"
      "v_cvt_pk_bf16_f32 v240, v240, v241\n"
      "v_mfma_f32_16x16x32_bf16 v[236:239], a[196:199], v[164:167], v[236:239]\n"
      "ds_write_b128 v126, v[152:155]\n"
      "v_mfma_f32_16x16x32_bf16 v[172:175], a[164:167], v[212:215], v[172:175]\n"
      "v_mul_f32 v243, v180, v243\n"
      "v_mul_f32 v248, v183, v248\n"
      "v_mfma_f32_16x16x32_bf16 v[172:175], a[200:203], v[164:167], v[172:175]\n"
      "v_mul_f32 v249, v183, v249\n"
      "v_cvt_pk_bf16_f32 v241, v242, v243\n"
      "v_mfma_f32_16x16x32_bf16 v[184:187], v[212:215], a[168:171], v[184:187]\n"
      "v_mul_f32 v250, v183, v250\n"
      "v_cvt_pk_bf16_f32 v248, v248, v249\n"
      "v_mfma_f32_16x16x32_bf16 v[184:187], v[164:167], a[204:207], v[184:187]\n"
      "global_load_lds_dwordx4 v117, s[52:53]\n"
      "v_mfma_f32_16x16x32_bf16 v[176:179], v[212:215], a[172:175], v[176:179]\n"
      "v_mul_f32 v144, v183, v144\n"
      "v_mul_f32 v251, v183, v251\n"
      "v_mfma_f32_16x16x32_bf16 v[176:179], v[164:167], a[208:211], v[176:179]\n"
      "ds_read_b128 v[188:191], v114 offset:8192\n"
      "v_cvt_pk_bf16_f32 v249, v250, v251\n"
      "v_mfma_f32_16x16x32_bf16 v[244:247], v[212:215], a[176:179], v[244:247]\n"
      "v_mul_f32 v147, v183, v147\n"
      "v_mul_f32 v145, v183, v145\n"
      "v_mfma_f32_16x16x32_bf16 v[244:247], v[164:167], a[212:215], v[244:247]\n"
      "ds_read_b128 v[212:215], v115 offset:8192\n"
      "v_cvt_pk_bf16_f32 v250, v144, v145\n"
      "v_mfma_f32_16x16x32_bf16 v[152:155], a[4:7], v[156:159], 0\n"
      "v_mul_f32_e64 v146, v183, v146\n"
      "v_cvt_pk_bf16_f32 v251, v146, v147\n"
      "v_mfma_f32_16x16x32_bf16 v[152:155], a[40:43], v[148:151], v[152:155]\n"
      "s_add_u32 m0, s78, 18432\n"
      "v_mul_f32 v200, v183, v200\n"
      "v_mfma_f32_16x16x32_bf16 v[144:147], a[0:3], v[156:159], 0\n"
      "v_mul_f32 v201, v183, v201\n"
      "v_cvt_pk_bf16_f32 v200, v200, v201\n"
      "v_mfma_f32_16x16x32_bf16 v[144:147], a[36:39], v[148:151], v[144:147]\n"
      "ds_write_b128 v126, v[248:251] offset:2048\n"
      "v_mfma_f32_16x16x32_bf16 v[204:207], a[8:11], v[156:159], 0\n"
      "v_mul_f32 v202, v183, v202\n"
      "v_mul_f32 v203, v183, v203\n"
      "v_mfma_f32_16x16x32_bf16 v[204:207], a[44:47], v[148:151], v[204:207]\n"
      "s_mov_b32 exec_hi, 0\n"
      "global_load_lds_dwordx4 v118, s[52:53]\n"
      "s_mov_b32 exec_hi, -1\n"
      "v_mfma_f32_16x16x32_bf16 v[164:167], a[12:15], v[156:159], 0\n"
      "s_waitcnt lgkmcnt(1)\n"
      "v_cvt_pk_bf16_f32 v201, v202, v203\n"
      "v_mul_f32 v229, v182, v229\n"
      "v_mfma_f32_16x16x32_bf16 v[164:167], a[48:51], v[148:151], v[164:167]\n"
      "ds_write2st64_b64 v127, v[240:241], v[200:201] offset0:0 offset1:4\n"
      "v_mfma_f32_16x16x32_bf16 v[240:243], a[16:19], v[156:159], 0\n"
      "v_mul_f32_e64 v228, v182, v228\n"
      "v_cvt_pk_bf16_f32 v228, v228, v229\n"
      "v_mfma_f32_16x16x32_bf16 v[240:243], a[52:55], v[148:151], v[240:243]\n"
      "s_add_u32 m0, s76, 115456\n"
      "v_mul_f32 v230, v182, v230\n"
      "v_mul_f32 v231, v182, v231\n"
      "v_mfma_f32_16x16x32_bf16 v[224:227], a[20:23], v[156:159], 0\n"
      "v_cvt_pk_bf16_f32 v229, v230, v231\n"
      "v_mul_f32 v169, v182, v169\n"
      "v_mfma_f32_16x16x32_bf16 v[224:227], a[56:59], v[148:151], v[224:227]\n"
      "v_mul_f32 v168, v182, v168\n"
      "v_cvt_pk_bf16_f32 v230, v168, v169\n"
      "v_mfma_f32_16x16x32_bf16 v[208:211], v[156:159], a[24:27], 0\n"
      "ds_read_b128 v[136:139], v114 offset:14336\n"
      "v_mul_f32 v170, v182, v170\n"
      "v_mfma_f32_16x16x32_bf16 v[208:211], v[148:151], a[60:63], v[208:211]\n"
      "v_mul_f32 v171, v182, v171\n"
      "v_cvt_pk_bf16_f32 v231, v170, v171\n"
      "v_mfma_f32_16x16x32_bf16 v[168:171], v[156:159], a[28:31], 0\n"
      "global_load_lds_dwordx4 v119, s[56:57]\n"
      "v_mfma_f32_16x16x32_bf16 v[168:171], v[148:151], a[64:67], v[168:171]\n"
      "ds_write_b128 v126, v[228:231] offset:4096\n"
      "v_mfma_f32_16x16x32_bf16 v[228:231], v[156:159], a[32:35], 0\n"
      "ds_read_b128 v[156:159], v115 offset:14336\n"
      "v_mul_f32_e64 v252, v182, v252\n"
      "v_mfma_f32_16x16x32_bf16 v[228:231], v[148:151], a[68:71], v[228:231]\n"
      "s_add_u32 m0, s79, 4864\n"
      "v_mul_f32_e64 v253, v182, v253\n"
      "v_cvt_pk_f16_f32 v216, v216, v217\n"
      "v_mfma_f32_16x16x32_bf16 v[152:155], a[76:79], v[188:191], v[152:155]\n"
      "v_mul_f32_e64 v254, v182, v254\n"
      "v_mul_f32_e64 v255, v182, v255\n"
      "v_mfma_f32_16x16x32_bf16 v[144:147], a[72:75], v[188:191], v[144:147]\n"
      "v_cvt_pk_bf16_f32 v252, v252, v253\n"
      "v_cvt_pk_bf16_f32 v253, v254, v255\n"
      "v_mfma_f32_16x16x32_bf16 v[144:147], a[108:111], v[212:215], v[144:147]\n"
      "global_load_lds_dword v122, s[58:59]\n"
      "v_mfma_f32_16x16x32_bf16 v[152:155], a[112:115], v[212:215], v[152:155]\n"
      "ds_write_b64 v127, v[252:253] offset:4096\n"
      "v_mfma_f32_16x16x32_bf16 v[204:207], a[80:83], v[188:191], v[204:207]\n"
      "v_pk_mul_f16 v183, v216, v216\n"
      "v_mfma_f32_16x16x32_bf16 v[204:207], a[116:119], v[212:215], v[204:207]\n"
      "v_cvt_pk_f16_f32 v196, v196, v197\n"
      "v_cvt_pk_f16_f32 v217, v218, v219\n"
      "v_mfma_f32_16x16x32_bf16 v[164:167], a[84:87], v[188:191], v[164:167]\n"
      "v_pk_fma_f16 v183, v217, v217, v183\n"
      "v_cvt_pk_f16_f32 v197, v198, v199\n"
      "v_mfma_f32_16x16x32_bf16 v[164:167], a[120:123], v[212:215], v[164:167]\n"
      "ds_read_b128 v[148:151], v114 offset:4096\n"
      "v_pk_mul_f16 v180, v196, v196\n"
      "v_mfma_f32_16x16x32_bf16 v[240:243], a[88:91], v[188:191], v[240:243]\n"
      "ds_read_b128 v[252:255], v115 offset:4096\n"
      "v_cvt_pk_f16_f32 v218, v140, v141\n"
      "v_mfma_f32_16x16x32_bf16 v[240:243], a[124:127], v[212:215], v[240:243]\n"
      "v_cvt_pk_f16_f32 v198, v236, v237\n"
      "v_pk_fma_f16 v180, v197, v197, v180\n"
      "v_mfma_f32_16x16x32_bf16 v[224:227], a[92:95], v[188:191], v[224:227]\n"
      "v_pk_fma_f16 v180, v198, v198, v180\n"
      "v_pk_fma_f16 v183, v218, v218, v183\n"
      "v_mfma_f32_16x16x32_bf16 v[224:227], a[128:131], v[212:215], v[224:227]\n"
      "v_cvt_pk_f16_f32 v219, v142, v143\n"
      "v_cvt_pk_f16_f32 v199, v238, v239\n"
      "v_mfma_f32_16x16x32_bf16 v[208:211], v[188:191], a[96:99], v[208:211]\n"
      "ds_read_b128 v[236:239], v119 offset:18432\n"
      "v_cvt_pk_f16_f32 v32, v220, v221\n"
      "v_mfma_f32_16x16x32_bf16 v[208:211], v[212:215], a[132:135], v[208:211]\n"
      "v_pk_fma_f16 v180, v199, v199, v180\n"
      "v_pk_fma_f16 v183, v219, v219, v183\n"
      "v_mfma_f32_16x16x32_bf16 v[168:171], v[188:191], a[100:103], v[168:171]\n"
      "v_cvt_pk_f16_f32 v33, v222, v223\n"
      "v_cvt_pk_f16_f32 v34, v172, v173\n"
      "v_mfma_f32_16x16x32_bf16 v[168:171], v[212:215], a[136:139], v[168:171]\n"
      "v_pk_fma_f16 v183, v32, v32, v183\n"
      "v_pk_fma_f16 v183, v33, v33, v183\n"
      "v_mfma_f32_16x16x32_bf16 v[228:231], v[188:191], a[104:107], v[228:231]\n"
      "s_waitcnt lgkmcnt(2)\n"
      "v_pk_fma_f16 v180, v34, v34, v180\n"
      "v_cvt_pk_f16_f32 v35, v174, v175\n"
      "v_mfma_f32_16x16x32_bf16 v[228:231], v[212:215], a[140:143], v[228:231]\n"
      "s_nop 0\n"
      "v_pk_fma_f16 v180, v35, v35, v180\n"
      "v_cvt_pk_bf16_f32 v184, v184, v185\n"
      "v_mfma_f32_16x16x32_bf16 v[144:147], a[144:147], v[136:139], v[144:147]\n"
      "v_permlane16_swap_b32_e64 v183, v180\n"
      "v_mfma_f32_16x16x32_bf16 v[144:147], a[180:183], v[156:159], v[144:147]\n"
      "v_pk_add_f16 v183, v183, v180\n"
      "v_mov_b32 v180, v183\n"
      "v_mfma_f32_16x16x32_bf16 v[152:155], a[148:151], v[136:139], v[152:155]\n"
      "s_nop 0\n"
      "v_permlane32_swap_b32_e64 v183, v180\n"
      "v_mfma_f32_16x16x32_bf16 v[152:155], a[184:187], v[156:159], v[152:155]\n"
      "s_waitcnt lgkmcnt(0)\n"
      "v_pk_add_f16 v183, v183, v180\n"
      "v_add_f16_sdwa v180, v183, v183 dst_sel:DWORD dst_unused:UNUSED_PAD src0_sel:WORD_0 src1_sel:WORD_1\n"
      "v_mfma_f32_16x16x32_bf16 v[204:207], a[152:155], v[136:139], v[204:207]\n"
      "v_rsq_f16 v183, v180\n"
      "v_mfma_f32_16x16x32_bf16 v[204:207], a[188:191], v[156:159], v[204:207]\n"
      "ds_read_b128 v[188:191], v114 offset:10240\n"
      "v_cmp_nle_f16 vcc, 0x1c00, v180\n"
      "v_mfma_f32_16x16x32_bf16 v[164:167], a[156:159], v[136:139], v[164:167]\n"
      "s_mov_b64 s[34:35], vcc\n"
      "v_cmp_nge_f16 vcc, 0x7b00, v180\n"
      "v_mov_b32 v180, v183\n"
      "v_mfma_f32_16x16x32_bf16 v[164:167], a[192:195], v[156:159], v[164:167]\n"
      "ds_read_b128 v[212:215], v115 offset:10240\n"
      "v_cvt_pk_bf16_f32 v185, v186, v187\n"
      "v_mfma_f32_16x16x32_bf16 v[240:243], a[160:163], v[136:139], v[240:243]\n"
      "s_or_b64 s[34:35], s[34:35], vcc\n"
      "v_permlane16_swap_b32 v183, v180\n"
      "v_mfma_f32_16x16x32_bf16 v[240:243], a[196:199], v[156:159], v[240:243]\n"
      "s_sub_u32 s28, s27, 3\n"
      "v_pk_mul_f16 v196, v196, v180 op_sel_hi:[1,0]\n"
      "v_pk_mul_f16 v197, v197, v180 op_sel_hi:[1,0]\n"
      "v_mfma_f32_16x16x32_bf16 v[224:227], a[164:167], v[136:139], v[224:227]\n"
      "s_cselect_b32 s22, 0, -1\n"
      "v_pk_mul_f16 v198, v198, v180 op_sel_hi:[1,0]\n"
      "v_pk_mul_f16 v199, v199, v180 op_sel_hi:[1,0]\n"
      "v_mfma_f32_16x16x32_bf16 v[224:227], a[200:203], v[156:159], v[224:227]\n"
      "s_max_i32 m0, s28, 0\n"
      "v_pk_mul_f16 v35, v35, v180 op_sel_hi:[1,0]\n"
      "v_pk_mul_f16 v34, v34, v180 op_sel_hi:[1,0]\n"
      "v_mfma_f32_16x16x32_bf16 v[208:211], v[136:139], a[168:171], v[208:211]\n"
      "v_mul_f16 v183, s7, v183\n"
      "v_pk_mul_f16 v216, v216, v183 op_sel_hi:[1,0]\n"
      "v_mfma_f32_16x16x32_bf16 v[208:211], v[156:159], a[204:207], v[208:211]\n"
      "ds_read_b128 v[220:223], v119 offset:24576\n"
      "v_pk_mul_f16 v217, v217, v183 op_sel_hi:[1,0]\n"
      "v_mfma_f32_16x16x32_bf16 v[168:171], v[136:139], a[172:175], v[168:171]\n"
      "s_movrels_b32 s9, s80\n"
      "v_pk_mul_f16 v219, v219, v183 op_sel_hi:[1,0]\n"
      "v_pk_mul_f16 v218, v218, v183 op_sel_hi:[1,0]\n"
      "v_mfma_f32_16x16x32_bf16 v[168:171], v[156:159], a[208:211], v[168:171]\n"
      "s_lshr_b32 s8, s9, 8\n"
      "v_pk_mul_f16 v33, v33, v183 op_sel_hi:[1,0]\n"
      "v_pk_mul_f16 v32, v32, v183 op_sel_hi:[1,0]\n"
      "v_mfma_f32_16x16x32_bf16 v[228:231], v[136:139], a[176:179], v[228:231]\n"
      "s_and_b32 s9, s9, 0xff\n"
      "v_cvt_pk_bf16_f32 v186, v208, v209\n"
      "v_cvt_pk_bf16_f32 v176, v176, v177\n"
      "v_mfma_f32_16x16x32_bf16 v[228:231], v[156:159], a[212:215], v[228:231]\n"
      "s_mul_i32 s28, s8, 1373568\n"
      "v_cvt_pk_bf16_f32 v177, v178, v179\n"
      "v_cvt_pk_bf16_f32 v187, v210, v211\n"
      "v_cvt_pk_bf16_f32 v244, v244, v245\n"
      "v_mfma_f32_16x16x32_bf16 v[180:183], a[0:3], v[148:151], 0\n"
      "s_mul_i32 s29, s9, 2688\n"
      "v_cvt_pk_bf16_f32 v178, v168, v169\n"
      "v_cvt_pk_bf16_f32 v179, v170, v171\n"
      "v_mfma_f32_16x16x32_bf16 v[180:183], a[36:39], v[252:255], v[180:183]\n"
      "s_add_u32 s28, s28, s29\n"
      "v_cvt_pk_bf16_f32 v245, v246, v247\n"
      "v_cvt_pk_bf16_f32 v247, v230, v231\n"
      "v_mfma_f32_16x16x32_bf16 v[156:159], a[4:7], v[148:151], 0\n"
      "s_cmp_eq_u32 s8, 72\n"
      "v_cvt_pk_f16_f32 v144, v144, v145\n"
      "v_cvt_pk_bf16_f32 v246, v228, v229\n"
      "v_mfma_f32_16x16x32_bf16 v[156:159], a[40:43], v[252:255], v[156:159]\n"
      "s_waitcnt lgkmcnt(0)\n"
      "s_cselect_b32 s8, 0, 100270464\n"
      "v_pk_mul_f16 v140, v144, v144\n"
      "v_mfma_f32_16x16x32_bf16 v[172:175], a[8:11], v[148:151], 0\n"
      "s_cmp_eq_u32 s9, 72\n"
      "v_cvt_pk_f16_f32 v164, v164, v165\n"
      "v_pk_mul_f16 v143, v164, v164\n"
      "v_mfma_f32_16x16x32_bf16 v[172:175], a[44:47], v[252:255], v[172:175]\n"
      "s_cselect_b32 s9, 0, 196224\n"
      "v_cvt_pk_f16_f32 v145, v146, v147\n"
      "v_pk_fma_f16 v140, v145, v145, v140\n"
      "v_mfma_f32_16x16x32_bf16 v[136:139], a[12:15], v[148:151], 0\n"
      "s_add_u32 s10, s8, s9\n"
      "v_cvt_pk_f16_f32 v165, v166, v167\n"
      "v_pk_fma_f16 v143, v165, v165, v143\n"
      "v_mfma_f32_16x16x32_bf16 v[136:139], a[48:51], v[252:255], v[136:139]\n"
      "s_add_u32 s28, s28, s10\n"
      "v_cvt_pk_f16_f32 v146, v152, v153\n"
      "v_cvt_pk_f16_f32 v166, v240, v241\n"
      "v_mfma_f32_16x16x32_bf16 v[208:211], a[16:19], v[148:151], 0\n"
      "ds_read_b128 v[168:171], v114 offset:16384\n"
      "v_pk_fma_f16 v140, v146, v146, v140\n"
      "v_mfma_f32_16x16x32_bf16 v[208:211], a[52:55], v[252:255], v[208:211]\n"
      "s_add_u32 s20, s12, s28\n"
      "v_pk_fma_f16 v143, v166, v166, v143\n"
      "v_cvt_pk_f16_f32 v167, v242, v243\n"
      "v_mfma_f32_16x16x32_bf16 v[240:243], a[20:23], v[148:151], 0\n"
      "s_addc_u32 s21, s13, 0\n"
      "v_cvt_pk_f16_f32 v147, v154, v155\n"
      "v_pk_fma_f16 v140, v147, v147, v140\n"
      "v_mfma_f32_16x16x32_bf16 v[240:243], a[56:59], v[252:255], v[240:243]\n"
      "ds_read_b128 v[152:155], v115 offset:16384\n"
      "v_pk_fma_f16 v143, v167, v167, v143\n"
      "v_mfma_f32_16x16x32_bf16 v[228:231], v[148:151], a[24:27], 0\n"
      "s_cmp_lg_u32 s10, s11\n"
      "v_cvt_pk_f16_f32 v38, v204, v205\n"
      "v_pk_fma_f16 v140, v38, v38, v140\n"
      "v_mfma_f32_16x16x32_bf16 v[228:231], v[252:255], a[60:63], v[228:231]\n"
      "s_cbranch_scc1 L_edge_26_%=\n"
      "L_eb_27_%=:\n"
      "v_mfma_f32_16x16x32_bf16 v[202:205], v[148:151], a[28:31], 0\n"
      "s_mov_b32 s5, s4\n"
      "v_cvt_pk_f16_f32 v40, v224, v225\n"
      "v_pk_fma_f16 v143, v40, v40, v143\n"
      "v_mfma_f32_16x16x32_bf16 v[202:205], v[252:255], a[64:67], v[202:205]\n"
      "s_add_u32 s4, s27, 1\n"
      "v_cvt_pk_f16_f32 v39, v206, v207\n"
      "v_pk_fma_f16 v140, v39, v39, v140\n"
      "v_mfma_f32_16x16x32_bf16 v[248:251], v[148:151], a[32:35], 0\n"
      "v_cvt_pk_f16_f32 v41, v226, v227\n"
      "v_pk_fma_f16 v143, v41, v41, v143\n"
      "v_mfma_f32_16x16x32_bf16 v[248:251], v[252:255], a[68:71], v[248:251]\n"
      "buffer_store_dwordx4 v[236:239], v116, s[20:23], 0 offen offset:0 sc1\n"
      "v_mfma_f32_16x16x32_bf16 v[172:175], a[80:83], v[188:191], v[172:175]\n"
      "s_lshl_b32 s4, s4, 30\n"
      "v_permlane16_swap_b32 v140, v143\n"
      "v_mfma_f32_16x16x32_bf16 v[180:183], a[72:75], v[188:191], v[180:183]\n"
      "s_and_b32 s4, s4, 0x80000000\n"
      "v_pk_add_f16 v140, v140, v143\n"
      "v_mov_b32 v143, v140\n"
      "v_mfma_f32_16x16x32_bf16 v[180:183], a[108:111], v[212:215], v[180:183]\n"
      "s_waitcnt lgkmcnt(0)\n"
      "v_permlane32_swap_b32_e64 v140, v143\n"
      "v_mfma_f32_16x16x32_bf16 v[156:159], a[76:79], v[188:191], v[156:159]\n"
      "ds_read_b128 v[148:151], v119 offset:30720\n"
      "v_pk_add_f16 v140, v140, v143\n"
      "v_mfma_f32_16x16x32_bf16 v[156:159], a[112:115], v[212:215], v[156:159]\n"
      "v_add_f16_sdwa v143, v140, v140 dst_sel:DWORD dst_unused:UNUSED_PAD src0_sel:WORD_0 src1_sel:WORD_1\n"
      "v_cmp_nle_f16 vcc, 0x1c00, v143\n"
      "v_mfma_f32_16x16x32_bf16 v[172:175], a[116:119], v[212:215], v[172:175]\n"
      "s_or_b64 s[34:35], s[34:35], vcc\n"
      "v_rsq_f16 v140, v143\n"
      "v_mfma_f32_16x16x32_bf16 v[136:139], a[84:87], v[188:191], v[136:139]\n"
      "buffer_store_dwordx4 v[220:223], v116, s[20:23], 0 offen offset:128 sc1\n"
      "v_mfma_f32_16x16x32_bf16 v[136:139], a[120:123], v[212:215], v[136:139]\n"
      "ds_read_b128 v[224:227], v132 offset:32\n"
      "v_cmp_nge_f16 vcc, 0x7b00, v143\n"
      "v_mfma_f32_16x16x32_bf16 v[208:211], a[88:91], v[188:191], v[208:211]\n"
      "ds_read_b128 v[252:255], v133\n"
      "v_mov_b32_e64 v143, v140\n"
      "v_mfma_f32_16x16x32_bf16 v[208:211], a[124:127], v[212:215], v[208:211]\n"
      "ds_read_b64 v[220:221], v132 offset:24\n"
      "v_add_u32 v135, 32, v135\n"
      "v_mfma_f32_16x16x32_bf16 v[240:243], a[92:95], v[188:191], v[240:243]\n"
      "s_or_b64 s[34:35], s[34:35], vcc\n"
      "ds_read_u16 v52, v134\n"
      "v_mfma_f32_16x16x32_bf16 v[240:243], a[128:131], v[212:215], v[240:243]\n"
      "ds_read_b64 v[222:223], v133 offset:16\n"
      "v_permlane16_swap_b32_e64 v140, v143\n"
      "v_mfma_f32_16x16x32_bf16 v[228:231], v[188:191], a[96:99], v[228:231]\n"
      "ds_read_u16 v56, v134 offset:32\n"
      "v_pk_mul_f16 v165, v165, v143 op_sel_hi:[1,0]\n"
      "v_mfma_f32_16x16x32_bf16 v[228:231], v[212:215], a[132:135], v[228:231]\n"
      "ds_read_u16 v60, v134 offset:64\n"
      "v_pk_mul_f16 v164, v164, v143 op_sel_hi:[1,0]\n"
      "v_mfma_f32_16x16x32_bf16 v[202:205], v[188:191], a[100:103], v[202:205]\n"
      "ds_read_b128 v[160:163], v123 offset:21504\n"
      "v_pk_mul_f16 v166, v166, v143 op_sel_hi:[1,0]\n"
      "v_mfma_f32_16x16x32_bf16 v[202:205], v[212:215], a[136:139], v[202:205]\n"
      "s_waitcnt lgkmcnt(8)\n"
      "v_pk_mul_f16 v167, v167, v143 op_sel_hi:[1,0]\n"
      "v_pk_mul_f16 v40, v40, v143 op_sel_hi:[1,0]\n"
      "v_mfma_f32_16x16x32_bf16 v[248:251], v[188:191], a[104:107], v[248:251]\n"
      "buffer_store_dwordx4 v[148:151], v116, s[20:23], 0 offen offset:256 sc1\n"
      "v_mfma_f32_16x16x32_bf16 v[248:251], v[212:215], a[140:143], v[248:251]\n"
      "ds_read_b128 v[192:195], v123 offset:18432\n"
      "v_pk_mul_f16 v41, v41, v143 op_sel_hi:[1,0]\n"
      "v_mfma_f32_16x16x32_bf16 v[180:183], a[144:147], v[168:171], v[180:183]\n"
      "ds_read_b128 v[148:151], v123 offset:24576\n"
      "v_mul_f16 v140, s7, v140\n"
      "v_mfma_f32_16x16x32_bf16 v[180:183], a[180:183], v[152:155], v[180:183]\n"
      "ds_read_b128 v[188:191], v123 offset:27648\n"
      "v_pk_mul_f16 v144, v144, v140 op_sel_hi:[1,0]\n"
      "v_mfma_f32_16x16x32_bf16 v[156:159], a[148:151], v[168:171], v[156:159]\n"
      "v_pk_mul_f16 v145, v145, v140 op_sel_hi:[1,0]\n"
      "v_pk_mul_f16 v147, v147, v140 op_sel_hi:[1,0]\n"
      "v_mfma_f32_16x16x32_bf16 v[156:159], a[184:187], v[152:155], v[156:159]\n"
      "v_pk_mul_f16 v146, v146, v140 op_sel_hi:[1,0]\n"
      "v_pk_mul_f16 v39, v39, v140 op_sel_hi:[1,0]\n"
      "v_mfma_f32_16x16x32_bf16 v[172:175], a[152:155], v[168:171], v[172:175]\n"
      "v_pk_mul_f16 v38, v38, v140 op_sel_hi:[1,0]\n"
      "v_cvt_pk_f16_f32 v180, v180, v181\n"
      "v_mfma_f32_16x16x32_bf16 v[172:175], a[188:191], v[152:155], v[172:175]\n"
      "v_pk_mul_f16 v206, v180, v180\n"
      "v_cvt_pk_f16_f32 v181, v182, v183\n"
      "v_mfma_f32_16x16x32_bf16 v[136:139], a[156:159], v[168:171], v[136:139]\n"
      "v_pk_fma_f16 v206, v181, v181, v206\n"
      "v_cvt_pk_f16_f32 v182, v156, v157\n"
      "v_mfma_f32_16x16x32_bf16 v[136:139], a[192:195], v[152:155], v[136:139]\n"
      "v_cvt_pk_f16_f32 v183, v158, v159\n"
      "v_pk_fma_f16 v206, v182, v182, v206\n"
      "v_mfma_f32_16x16x32_bf16 v[208:211], a[160:163], v[168:171], v[208:211]\n"
      "s_waitcnt lgkmcnt(2)\n"
      "v_cvt_pk_f16_f32 v44, v172, v173\n"
      "v_pk_fma_f16 v206, v183, v183, v206\n"
      "v_mfma_f32_16x16x32_bf16 v[208:211], a[196:199], v[152:155], v[208:211]\n"
      "s_nop 0\n"
      "v_cvt_pk_f16_f32 v136, v136, v137\n"
      "v_pk_mul_f16 v207, v136, v136\n"
      "v_mfma_f32_16x16x32_bf16 v[240:243], a[164:167], v[168:171], v[240:243]\n"
      "s_nop 0\n"
      "v_cvt_pk_f16_f32 v137, v138, v139\n"
      "v_pk_fma_f16 v207, v137, v137, v207\n"
      "v_mfma_f32_16x16x32_bf16 v[240:243], a[200:203], v[152:155], v[240:243]\n"
      "s_waitcnt lgkmcnt(0)\n"
      "v_cvt_pk_f16_f32 v138, v208, v209\n"
      "v_pk_fma_f16 v207, v138, v138, v207\n"
      "v_mfma_f32_16x16x32_bf16 v[228:231], v[168:171], a[168:171], v[228:231]\n"
      "v_cvt_pk_f16_f32 v139, v210, v211\n"
      "v_pk_fma_f16 v207, v139, v139, v207\n"
      "v_mfma_f32_16x16x32_bf16 v[228:231], v[152:155], a[204:207], v[228:231]\n"
      "ds_read_b128 v[156:159], v123 offset:30720\n"
      "v_mfma_f32_16x16x32_bf16 v[202:205], v[168:171], a[172:175], v[202:205]\n"
      "v_cvt_pk_f16_f32 v45, v174, v175\n"
      "v_pk_fma_f16 v206, v44, v44, v206\n"
      "v_mfma_f32_16x16x32_bf16 v[202:205], v[152:155], a[208:211], v[202:205]\n"
      "ds_read_b128 v[140:143], v123 offset:33792\n"
      "v_cvt_pk_f16_f32 v46, v240, v241\n"
      "v_mfma_f32_16x16x32_bf16 v[248:251], v[168:171], a[176:179], v[248:251]\n"
      "v_pk_fma_f16 v207, v46, v46, v207\n"
      "v_cvt_pk_f16_f32 v47, v242, v243\n"
      "v_mfma_f32_16x16x32_bf16 v[248:251], v[152:155], a[212:215], v[248:251]\n"
      "v_pk_fma_f16 v207, v47, v47, v207\n"
      "v_pk_fma_f16 v206, v45, v45, v206\n"
      "v_mfma_f32_16x16x32_bf16 v[212:215], a[216:219], v[192:195], 0\n"
      "s_nop 0\n"
      "v_permlane16_swap_b32 v206, v207\n"
      "v_mfma_f32_16x16x32_bf16 v[212:215], a[228:231], v[160:163], v[212:215]\n"
      "v_pk_add_f16 v206, v206, v207\n"
      "v_mov_b32_e64 v207, v206\n"
      "v_mfma_f32_16x16x32_bf16 v[236:239], a[220:223], v[192:195], 0\n"
      "s_nop 0\n"
      "v_permlane32_swap_b32_e64 v206, v207\n"
      "v_mfma_f32_16x16x32_bf16 v[236:239], a[232:235], v[160:163], v[236:239]\n"
      "s_nop 0\n"
      "v_pk_add_f16 v206, v206, v207\n"
      "v_add_f16_sdwa v207, v206, v206 dst_sel:DWORD dst_unused:UNUSED_PAD src0_sel:WORD_0 src1_sel:WORD_1\n"
      "v_mfma_f32_16x16x32_bf16 v[152:155], a[224:227], v[192:195], 0\n"
      "s_nop 0\n"
      "v_rsq_f16 v206, v207\n"
      "v_mfma_f32_16x16x32_bf16 v[152:155], a[236:239], v[160:163], v[152:155]\n"
      "v_cmp_nle_f16 vcc, 0x1c00, v207\n"
      "v_cvt_pk_bf16_f32 v50, v228, v229\n"
      "v_mfma_f32_16x16x32_bf16 v[212:215], a[240:243], v[148:151], v[212:215]\n"
      "s_or_b64 s[34:35], s[34:35], vcc\n"
      "v_cmp_nge_f16 vcc, 0x7b00, v207\n"
      "v_mov_b32_e64 v207, v206\n"
      "v_mfma_f32_16x16x32_bf16 v[212:215], a[252:255], v[188:191], v[212:215]\n"
      "s_nop 0\n"
      "v_permlane16_swap_b32 v206, v207\n"
      "v_mfma_f32_16x16x32_bf16 v[236:239], a[244:247], v[148:151], v[236:239]\n"
      "s_or_b64 s[34:35], s[34:35], vcc\n"
      "v_pk_mul_f16 v137, v137, v207 op_sel_hi:[1,0]\n"
      "v_pk_mul_f16 v136, v136, v207 op_sel_hi:[1,0]\n"
      "v_mfma_f32_16x16x32_bf16 v[236:239], v[0:3], v[188:191], v[236:239]\n"
      "v_pk_mul_f16 v138, v138, v207 op_sel_hi:[1,0]\n"
      "v_pk_mul_f16 v139, v139, v207 op_sel_hi:[1,0]\n"
      "v_mfma_f32_16x16x32_bf16 v[152:155], a[248:251], v[148:151], v[152:155]\n"
      "s_or_b64 vcc, s[48:49], s[50:51]\n"
      "v_pk_mul_f16 v47, v47, v207 op_sel_hi:[1,0]\n"
      "v_pk_mul_f16 v46, v46, v207 op_sel_hi:[1,0]\n"
      "v_mfma_f32_16x16x32_bf16 v[152:155], v[4:7], v[188:191], v[152:155]\n"
      "s_waitcnt lgkmcnt(0)\n"
      "v_mul_f16 v206, s7, v206\n"
      "v_pk_mul_f16 v180, v180, v206 op_sel_hi:[1,0]\n"
      "v_mfma_f32_16x16x32_bf16 v[212:215], v[8:11], v[156:159], v[212:215]\n"
      "s_nop 0\n"
      "v_pk_mul_f16 v181, v181, v206 op_sel_hi:[1,0]\n"
      "v_pk_mul_f16 v182, v182, v206 op_sel_hi:[1,0]\n"
      "v_mfma_f32_16x16x32_bf16 v[212:215], v[20:23], v[140:143], v[212:215]\n"
      "s_or_b64 vcc, vcc, s[34:35]\n"
      "v_pk_mul_f16 v183, v183, v206 op_sel_hi:[1,0]\n"
      "v_pk_mul_f16 v44, v44, v206 op_sel_hi:[1,0]\n"
      "v_mfma_f32_16x16x32_bf16 v[236:239], v[12:15], v[156:159], v[236:239]\n"
      "s_waitcnt vmcnt(3)\n"
      "v_pk_mul_f16 v45, v45, v206 op_sel_hi:[1,0]\n"
      "v_cvt_pk_bf16_f32 v51, v230, v231\n"
      "v_mfma_f32_16x16x32_bf16 v[236:239], v[24:27], v[140:143], v[236:239]\n"
      "ds_read_b32 v207, v122 offset:4864\n"
      "v_cvt_pk_bf16_f32 v54, v202, v203\n"
      "v_mfma_f32_16x16x32_bf16 v[152:155], v[16:19], v[156:159], v[152:155]\n"
      "ds_read_b128 v[156:159], v119 offset:41728\n"
      "v_cvt_pk_bf16_f32 v55, v204, v205\n"
      "v_mfma_f32_16x16x32_bf16 v[152:155], v[28:31], v[140:143], v[152:155]\n"
      "v_fma_f32 v230, v212, v212, s39\n"
      "v_cvt_pk_bf16_f32 v59, v250, v251\n"
      "v_mfma_f32_16x16x32_f16 v[140:143], v[196:199], v[216:219], v[78:81]\n"
      "v_fmac_f32_e64 v230, v213, v213\n"
      "v_cvt_pk_bf16_f32 v58, v248, v249\n"
      "v_mfma_f32_16x16x32_f16 v[140:143], v[34:37], v[32:35], v[140:143]\n"
      "v_fmac_f32 v230, v214, v214\n"
      "v_fmac_f32 v230, v215, v215\n"
      "v_mfma_f32_16x16x32_f16 v[188:191], v[164:167], v[216:219], v[82:85]\n"
      "v_fmac_f32 v230, v236, v236\n"
      "v_fmac_f32 v230, v237, v237\n"
      "v_mfma_f32_16x16x32_f16 v[188:191], v[40:43], v[32:35], v[188:191]\n"
      "v_fmac_f32 v230, v238, v238\n"
      "v_fmac_f32 v230, v239, v239\n"
      "v_mfma_f32_16x16x32_f16 v[148:151], v[224:227], v[216:219], v[70:73]\n"
      "v_fmac_f32 v230, v152, v152\n"
      "v_fmac_f32 v230, v153, v153\n"
      "v_mfma_f32_16x16x32_f16 v[148:151], v[252:255], v[196:199], v[148:151]\n"
      "v_fmac_f32 v230, v154, v154\n"
      "v_fmac_f32_e64 v230, v155, v155\n"
      "v_mfma_f32_16x16x32_f16 v[148:151], v[220:223], v[32:35], v[148:151]\n"
      "s_waitcnt lgkmcnt(0)\n"
      "v_bitop3_b32 v207, v207, v159, s40 bitop3:0xa8\n"
      "v_or3_b32 v156, v156, v157, v158\n"
      "v_mfma_f32_16x16x32_f16 v[240:243], v[136:139], v[216:219], v[86:89]\n"
      "v_and_b32_e64 v156, s40, v156\n"
      "v_or3_b32 v156, v156, v207, s4\n"
      "v_mfma_f32_16x16x32_f16 v[240:243], v[46:49], v[32:35], v[240:243]\n"
      "ds_write_b32 v129, v156 offset:3072\n"
      "s_cbranch_scc1 L_gen_28_%=\n"
      "L_fast_31_%=:\n"
      "v_exp_f32 v140, v140\n"
      "v_mfma_f32_16x16x32_f16 v[208:211], v[196:199], v[144:147], v[90:93]\n"
      "ds_read_b128 v[200:203], v123 offset:19456\n"
      "v_exp_f32_e64 v148, v148\n"
      "v_mfma_f32_16x16x32_f16 v[208:211], v[34:37], v[38:41], v[208:211]\n"
      "ds_read_b128 v[172:175], v123 offset:22528\n"
      "v_exp_f32 v142, v142\n"
      "v_mfma_f32_16x16x32_f16 v[168:171], v[164:167], v[144:147], v[94:97]\n"
      "ds_read_b128 v[216:219], v120 offset:18432\n"
      "v_exp_f32 v240, v240\n"
      "v_exp_f32 v189, v189\n"
      "v_exp_f32 v241, v241\n"
      "v_mfma_f32_16x16x32_f16 v[168:171], v[40:43], v[38:41], v[168:171]\n"
      "ds_read_b128 v[204:207], v128 offset:3072\n"
      "v_exp_f32 v188, v188\n"
      "v_exp_f32 v190, v190\n"
      "v_mfma_f32_16x16x32_f16 v[248:251], v[224:227], v[144:147], v[72:75]\n"
      "v_exp_f32_e64 v141, v141\n"
      "v_mfma_f32_16x16x32_f16 v[248:251], v[252:255], v[164:167], v[248:251]\n"
      "v_exp_f32_e64 v143, v143\n"
      "v_mfma_f32_16x16x32_f16 v[248:251], v[220:223], v[38:41], v[248:251]\n"
      "ds_read_b128 v[192:195], v123 offset:25600\n"
      "v_cvt_pk_bf16_f32 v140, v140, v141\n"
      "v_cvt_pk_bf16_f32 v141, v142, v143\n"
      "v_exp_f32 v191, v191\n"
      "v_mfma_f32_16x16x32_f16 v[232:235], v[136:139], v[144:147], v[98:101]\n"
      "ds_read_b128 v[160:163], v123 offset:28672\n"
      "s_waitcnt lgkmcnt(3)\n"
      "v_mfma_f32_16x16x32_f16 v[232:235], v[46:49], v[38:41], v[232:235]\n"
      "buffer_store_dwordx4 v[216:219], v117, s[20:23], 0 offen offset:0 sc1\n"
      "v_cvt_pk_bf16_f32 v142, v188, v189\n"
      "v_exp_f32 v210, v210\n"
      "v_mfma_f32_16x16x32_bf16 v[156:159], a[216:219], v[200:203], 0\n"
      "v_exp_f32 v242, v242\n"
      "v_mfma_f32_16x16x32_bf16 v[156:159], a[228:231], v[172:175], v[156:159]\n"
      "ds_read_b128 v[216:219], v123 offset:34816\n"
      "v_cvt_pk_bf16_f32 v143, v190, v191\n"
      "v_mfma_f32_16x16x32_bf16 v[188:191], a[220:223], v[200:203], 0\n"
      "v_exp_f32 v243, v243\n"
      "v_mfma_f32_16x16x32_bf16 v[188:191], a[232:235], v[172:175], v[188:191]\n"
      "s_waitcnt lgkmcnt(3)\n"
      "v_bitop3_b32 v206, v206, v207, s4 bitop3:0x7e\n"
      "v_exp_f32_e64 v208, v208\n"
      "v_mfma_f32_16x16x32_bf16 v[144:147], a[224:227], v[200:203], 0\n"
      "v_cvt_pk_bf16_f32 v62, v240, v241\n"
      "v_cvt_pk_bf16_f32 v63, v242, v243\n"
      "v_mfma_f32_16x16x32_bf16 v[144:147], a[236:239], v[172:175], v[144:147]\n"
      "ds_read_b128 v[240:243], v123 offset:31744\n"
      "v_bitop3_b32 v204, v204, v205, s4 bitop3:0x7e\n"
      "s_waitcnt lgkmcnt(2)\n"
      "v_or_b32 v204, v204, v206\n"
      "v_exp_f32 v211, v211\n"
      "v_cmp_ne_u32 vcc, 0, v204\n"
      "v_mfma_f32_16x16x32_bf16 v[188:191], a[244:247], v[192:195], v[188:191]\n"
      "s_mov_b64 s[48:49], vcc\n"
      "v_exp_f32 v209, v209\n"
      "v_mfma_f32_16x16x32_bf16 v[188:191], v[0:3], v[160:163], v[188:191]\n"
      "ds_read_b128 v[172:175], v121 offset:18432\n"
      "v_cvt_pk_bf16_f32 v208, v208, v209\n"
      "v_exp_f32 v169, v169\n"
      "v_mfma_f32_16x16x32_bf16 v[156:159], a[240:243], v[192:195], v[156:159]\n"
      "v_exp_f32 v168, v168\n"
      "v_mfma_f32_16x16x32_bf16 v[156:159], a[252:255], v[160:163], v[156:159]\n"
      "s_cbranch_vccnz L_flg_32_%=\n"
      "L_fb_33_%=:\n"
      "v_cvt_pk_bf16_f32 v209, v210, v211\n"
      "v_cvt_pk_bf16_f32 v210, v168, v169\n"
      "v_mfma_f32_16x16x32_bf16 v[144:147], a[248:251], v[192:195], v[144:147]\n"
      "v_exp_f32 v171, v171\n"
      "v_mfma_f32_16x16x32_bf16 v[144:147], v[4:7], v[160:163], v[144:147]\n"
      "s_waitcnt lgkmcnt(1)\n"
      "v_exp_f32 v170, v170\n"
      "v_mfma_f32_16x16x32_bf16 v[156:159], v[8:11], v[240:243], v[156:159]\n"
      "ds_read_b128 v[200:203], v123 offset:23552\n"
      "v_cvt_pk_bf16_f32 v211, v170, v171\n"
      "v_mfma_f32_16x16x32_bf16 v[156:159], v[20:23], v[216:219], v[156:159]\n"
      "ds_read_b128 v[204:207], v123 offset:20480\n"
      "v_exp_f32_e64 v232, v232\n"
      "v_mfma_f32_16x16x32_bf16 v[188:191], v[12:15], v[240:243], v[188:191]\n"
      "s_waitcnt lgkmcnt(2)\n"
      "v_exp_f32_e64 v233, v233\n"
      "v_mfma_f32_16x16x32_bf16 v[188:191], v[24:27], v[216:219], v[188:191]\n"
      "s_mov_b32 exec_hi, 0\n"
      "buffer_store_dwordx4 v[172:175], v118, s[20:23], 0 offen offset:0 sc1\n"
      "s_mov_b32 exec_hi, -1\n"
      "v_mfma_f32_16x16x32_bf16 v[144:147], v[16:19], v[240:243], v[144:147]\n"
      "v_exp_f32 v234, v234\n"
      "v_mfma_f32_16x16x32_bf16 v[144:147], v[28:31], v[216:219], v[144:147]\n"
      "v_cvt_pk_bf16_f32 v64, v148, 0\n"
      "v_exp_f32 v248, v248\n"
      "v_mfma_f32_16x16x32_bf16 v[240:243], v[66:69], v[140:143], 0\n"
      "v_exp_f32 v235, v235\n"
      "v_mfma_f32_16x16x32_bf16 v[240:243], v[66:69], v[62:65], v[240:243]\n"
      "v_fma_f32 v231, v156, v156, s39\n"
      "v_fmac_f32 v231, v157, v157\n"
      "v_mfma_f32_16x16x32_bf16 v[216:219], v[184:187], v[140:143], 0\n"
      "s_waitcnt lgkmcnt(0)\n"
      "v_fmac_f32 v231, v158, v158\n"
      "v_fmac_f32 v231, v159, v159\n"
      "v_mfma_f32_16x16x32_bf16 v[216:219], v[50:53], v[62:65], v[216:219]\n"
      "v_fmac_f32 v231, v188, v188\n"
      "v_fmac_f32 v231, v189, v189\n"
      "v_mfma_f32_16x16x32_bf16 v[172:175], v[176:179], v[140:143], 0\n"
      "v_fmac_f32 v231, v190, v190\n"
      "v_fmac_f32 v231, v191, v191\n"
      "v_mfma_f32_16x16x32_bf16 v[172:175], v[54:57], v[62:65], v[172:175]\n"
      "v_fmac_f32 v231, v144, v144\n"
      "v_fmac_f32 v231, v145, v145\n"
      "v_mfma_f32_16x16x32_bf16 v[168:171], v[244:247], v[140:143], 0\n"
      "v_fmac_f32_e64 v231, v146, v146\n"
      "v_fmac_f32 v231, v147, v147\n"
      "v_mfma_f32_16x16x32_bf16 v[168:171], v[58:61], v[62:65], v[168:171]\n"
      "s_add_u32 s28, s27, 2\n"
      "v_cvt_pk_bf16_f32 v64, v248, 0\n"
      "v_cvt_pk_bf16_f32 v62, v232, v233\n"
      "v_mfma_f32_16x16x32_f16 v[160:163], v[196:199], v[180:183], v[102:105]\n"
      "v_cvt_pk_bf16_f32 v63, v234, v235\n"
      "v_cndmask_b32 v149, v149, v249, s[42:43]\n"
      "v_mfma_f32_16x16x32_f16 v[160:163], v[34:37], v[44:47], v[160:163]\n"
      "v_rcp_f32 v240, v240\n"
      "v_mfma_f32_16x16x32_f16 v[140:143], v[164:167], v[180:183], v[106:109]\n"
      "v_mul_f32 v216, v240, v216\n"
      "v_mul_f32_e64 v217, v240, v217\n"
      "v_mfma_f32_16x16x32_f16 v[140:143], v[40:43], v[44:47], v[140:143]\n"
      "v_cvt_pk_bf16_f32 v216, v216, v217\n"
      "v_mul_f32 v219, v240, v219\n"
      "v_mfma_f32_16x16x32_f16 v[164:167], v[224:227], v[180:183], v[74:77]\n"
      "ds_read_b128 v[196:199], v123 offset:26624\n"
      "v_mul_f32 v218, v240, v218\n"
      "v_mfma_f32_16x16x32_f16 v[164:167], v[252:255], v[136:139], v[164:167]\n"
      "v_cvt_pk_bf16_f32 v217, v218, v219\n"
      "v_mul_f32 v172, v240, v172\n"
      "v_mfma_f32_16x16x32_f16 v[164:167], v[220:223], v[44:47], v[164:167]\n"
      "v_mul_f32 v174, v240, v174\n"
      "v_mul_f32 v170, v240, v170\n"
      "v_mfma_f32_16x16x32_f16 v[248:251], v[136:139], v[180:183], v[110:113]\n"
      "ds_read_b128 v[136:139], v123 offset:29696\n"
      "v_mul_f32 v173, v240, v173\n"
      "v_mfma_f32_16x16x32_f16 v[248:251], v[46:49], v[44:47], v[248:251]\n"
      "v_mul_f32_e64 v175, v240, v175\n"
      "v_cvt_pk_bf16_f32 v218, v172, v173\n"
      "v_cvt_pk_bf16_f32 v219, v174, v175\n"
      "v_mfma_f32_16x16x32_f16 v[180:183], v[252:255], v[224:227], v[76:79]\n"
      "ds_write_b128 v124, v[216:219]\n"
      "v_mfma_f32_16x16x32_bf16 v[224:227], v[66:69], v[208:211], 0\n"
      "ds_read_b128 v[216:219], v123 offset:32768\n"
      "v_mul_f32_e64 v168, v240, v168\n"
      "v_mfma_f32_16x16x32_bf16 v[224:227], v[66:69], v[62:65], v[224:227]\n"
      "v_exp_f32 v162, v162\n"
      "v_mul_f32 v169, v240, v169\n"
      "v_cvt_pk_bf16_f32 v168, v168, v169\n"
      "v_mfma_f32_16x16x16_f16 v[180:183], v[222:223], v[220:221], v[180:183]\n"
      "s_min_u32 m0, s28, s3\n"
      "v_mul_f32 v171, v240, v171\n"
      "v_exp_f32 v143, v143\n"
      "v_mfma_f32_16x16x32_bf16 v[172:175], v[184:187], v[208:211], 0\n"
      "s_movrels_b32 s9, s80\n"
      "v_cvt_pk_bf16_f32 v169, v170, v171\n"
      "v_cndmask_b32 v149, v149, v165, s[44:45]\n"
      "v_mfma_f32_16x16x32_bf16 v[172:175], v[50:53], v[62:65], v[172:175]\n"
      "s_lshr_b32 s8, s9, 8\n"
      "v_cndmask_b32 v149, v149, v181, s[46:47]\n"
      "v_exp_f32 v149, v149\n"
      "v_mfma_f32_16x16x32_bf16 v[226:229], v[176:179], v[208:211], 0\n"
      "s_waitcnt lgkmcnt(2)\n"
      "v_cvt_pk_bf16_f32 v149, v149, v149\n"
      "v_exp_f32 v248, v248\n"
      "v_mfma_f32_16x16x32_bf16 v[226:229], v[54:57], v[62:65], v[226:229]\n"
      "ds_write_b16 v130, v149\n"
      "v_mfma_f32_16x16x32_bf16 v[252:255], v[244:247], v[208:211], 0\n"
      "s_and_b32 s9, s9, 0xff\n"
      "ds_read_b128 v[208:211], v131\n"
      "v_mfma_f32_16x16x32_bf16 v[252:255], v[58:61], v[62:65], v[252:255]\n"
      "ds_read_b128 v[192:195], v131 offset:64\n"
      "v_exp_f32_e64 v140, v140\n"
      "v_mfma_f32_16x16x32_bf16 v[180:183], a[216:219], v[204:207], 0\n"
      "s_mul_i32 s10, s8, 73\n"
      "v_exp_f32 v160, v160\n"
      "v_mfma_f32_16x16x32_bf16 v[180:183], a[228:231], v[200:203], v[180:183]\n"
      "s_add_u32 s10, s10, s9\n"
      "v_exp_f32_e64 v249, v249\n"
      "v_mfma_f32_16x16x32_bf16 v[220:223], a[220:223], v[204:207], 0\n"
      "s_mul_i32 s28, s10, 4802\n"
      "v_exp_f32 v163, v163\n"
      "v_mfma_f32_16x16x32_bf16 v[220:223], a[232:235], v[200:203], v[220:223]\n"
      "s_add_u32 s56, s18, s28\n"
      "v_exp_f32 v161, v161\n"
      "v_mfma_f32_16x16x32_bf16 v[180:183], a[240:243], v[196:199], v[180:183]\n"
      "s_addc_u32 s57, s19, 0\n"
      "v_cvt_pk_bf16_f32 v160, v160, v161\n"
      "v_cvt_pk_bf16_f32 v161, v162, v163\n"
      "v_mfma_f32_16x16x32_bf16 v[180:183], a[252:255], v[136:139], v[180:183]\n"
      "s_and_b32 s56, s56, -16\n"
      "v_exp_f32 v141, v141\n"
      "v_mfma_f32_16x16x32_bf16 v[220:223], a[244:247], v[196:199], v[220:223]\n"
      "s_sub_u32 s58, s56, 110592\n"
      "v_exp_f32 v142, v142\n"
      "v_mfma_f32_16x16x32_bf16 v[220:223], v[0:3], v[136:139], v[220:223]\n"
      "s_waitcnt lgkmcnt(0)\n"
      "v_cvt_pk_bf16_f32 v162, v140, v141\n"
      "v_cvt_pk_bf16_f32 v163, v142, v143\n"
      "v_mfma_f32_16x16x32_bf16 v[240:243], a[224:227], v[204:207], 0\n"
      "ds_read_b128 v[204:207], v123 offset:35840\n"
      "v_cvt_pk_bf16_f32 v62, v248, v249\n"
      "v_mfma_f32_16x16x32_bf16 v[240:243], a[236:239], v[200:203], v[240:243]\n"
      "ds_read_b128 v[140:143], v115 offset:18432\n"
      "v_exp_f32 v250, v250\n"
      "v_mfma_f32_16x16x32_bf16 v[240:243], a[248:251], v[196:199], v[240:243]\n"
      "ds_read_b128 v[148:151], v114 offset:18432\n"
      "s_subb_u32 s59, s57, 0\n"
      "v_mfma_f32_16x16x32_bf16 v[240:243], v[4:7], v[136:139], v[240:243]\n"
      "s_sub_u32 s56, s56, 73728\n"
      "v_exp_f32 v251, v251\n"
      "v_mfma_f32_16x16x32_bf16 v[200:203], v[208:211], v[66:69], 0\n"
      "s_subb_u32 s57, s57, 0\n"
      "v_exp_f32_e64 v164, v164\n"
      "v_mfma_f32_16x16x32_bf16 v[200:203], v[192:195], v[66:69], v[200:203]\n"
      "s_mul_i32 s28, s8, 1373568\n"
      "v_cvt_pk_bf16_f32 v64, v164, 0\n"
      "v_cvt_pk_bf16_f32 v63, v250, v251\n"
      "v_mfma_f32_16x16x32_bf16 v[248:251], v[208:211], v[184:187], 0\n"
      "s_mul_i32 s29, s9, 2688\n"
      "v_rcp_f32_e64 v224, v224\n"
      "v_mfma_f32_16x16x32_bf16 v[248:251], v[192:195], v[50:53], v[248:251]\n"
      "s_add_u32 s28, s28, s29\n"
      "v_mul_f32 v172, v224, v172\n"
      "v_mul_f32 v173, v224, v173\n"
      "v_mfma_f32_16x16x32_bf16 v[164:167], v[66:69], v[160:163], 0\n"
      "s_waitcnt lgkmcnt(2)\n"
      "v_mul_f32 v175, v224, v175\n"
      "v_cvt_pk_bf16_f32 v172, v172, v173\n"
      "v_mfma_f32_16x16x32_bf16 v[164:167], v[66:69], v[62:65], v[164:167]\n"
      "s_cmp_eq_u32 s8, 72\n"
      "v_mul_f32 v174, v224, v174\n"
      "v_cvt_pk_bf16_f32 v173, v174, v175\n"
      "v_mfma_f32_16x16x32_bf16 v[180:183], v[8:11], v[216:219], v[180:183]\n"
      "s_cselect_b32 s8, 0, 100270464\n"
      "v_mul_f32 v227, v224, v227\n"
      "v_mul_f32 v229, v224, v229\n"
      "v_mfma_f32_16x16x32_bf16 v[180:183], v[20:23], v[204:207], v[180:183]\n"
      "s_waitcnt lgkmcnt(0)\n"
      "v_mul_f32 v226, v224, v226\n"
      "v_cvt_pk_bf16_f32 v174, v226, v227\n"
      "v_mfma_f32_16x16x32_bf16 v[220:223], v[12:15], v[216:219], v[220:223]\n"
      "s_cmp_eq_u32 s9, 72\n"
      "v_mul_f32_e64 v228, v224, v228\n"
      "v_cvt_pk_bf16_f32 v175, v228, v229\n"
      "v_mfma_f32_16x16x32_bf16 v[220:223], v[24:27], v[204:207], v[220:223]\n"
      "s_cselect_b32 s9, 0, 196224\n"
      "v_mul_f32 v255, v224, v255\n"
      "v_mul_f32 v253, v224, v253\n"
      "v_mfma_f32_16x16x32_bf16 v[240:243], v[16:19], v[216:219], v[240:243]\n"
      "s_add_u32 s10, s8, s9\n"
      "v_mul_f32 v252, v224, v252\n"
      "v_cvt_pk_bf16_f32 v252, v252, v253\n"
      "v_mfma_f32_16x16x32_bf16 v[240:243], v[28:31], v[204:207], v[240:243]\n"
      "ds_write_b128 v124, v[172:175] offset:1024\n"
      "v_mfma_f32_16x16x32_bf16 v[196:199], v[184:187], v[160:163], 0\n"
      "ds_read_b128 v[204:207], v114 offset:24576\n"
      "v_mul_f32 v254, v224, v254\n"
      "v_mfma_f32_16x16x32_bf16 v[196:199], v[50:53], v[62:65], v[196:199]\n"
      "ds_read_b128 v[216:219], v115 offset:24576\n"
      "s_add_u32 s28, s28, s10\n"
      "v_mfma_f32_16x16x32_bf16 v[172:175], v[176:179], v[160:163], 0\n"
      "v_cvt_pk_bf16_f32 v253, v254, v255\n"
      "v_fma_f32 v228, v180, v180, s39\n"
      "v_mfma_f32_16x16x32_bf16 v[172:175], v[54:57], v[62:65], v[172:175]\n"
      "ds_write2st64_b64 v125, v[168:169], v[252:253] offset0:0 offset1:2\n"
      "v_mfma_f32_16x16x32_bf16 v[136:139], v[244:247], v[160:163], 0\n"
      "s_add_u32 s52, s16, s28\n"
      "v_fmac_f32 v228, v181, v181\n"
      "v_fmac_f32_e64 v228, v182, v182\n"
      "v_mfma_f32_16x16x32_bf16 v[136:139], v[58:61], v[62:65], v[136:139]\n"
      "s_addc_u32 s53, s17, 0\n"
      "v_fmac_f32 v228, v183, v183\n"
      "v_fmac_f32 v228, v220, v220\n"
      "v_mfma_f32_16x16x32_bf16 v[224:227], v[208:211], v[176:179], 0\n"
      "s_add_u32 s27, s27, 1\n"
      "v_fmac_f32 v228, v221, v221\n"
      "v_fmac_f32 v228, v222, v222\n"
      "v_mfma_f32_16x16x32_bf16 v[224:227], v[192:195], v[54:57], v[224:227]\n"
      "s_cmp_ge_u32 s27, s26\n"
      "v_fmac_f32 v228, v223, v223\n"
      "v_fmac_f32_e64 v228, v240, v240\n"
      "v_mfma_f32_16x16x32_bf16 v[168:171], v[208:211], v[244:247], 0\n"
      "s_cselect_b32 s28, 1, 0\n"
      "v_fmac_f32 v228, v241, v241\n"
      "v_fmac_f32 v228, v242, v242\n"
      "v_mfma_f32_16x16x32_bf16 v[168:171], v[192:195], v[58:61], v[168:171]\n"
      "v_fmac_f32 v228, v243, v243\n"
      "v_mov_b32 v229, v228\n"
      "v_mfma_f32_16x16x32_bf16 v[184:187], a[0:3], v[148:151], 0\n"
      "s_xor_b32 s29, s10, s11\n"
      "v_permlane16_swap_b32 v230, v231\n"
      "v_mfma_f32_16x16x32_bf16 v[184:187], a[36:39], v[140:143], v[184:187]\n"
      "s_waitcnt lgkmcnt(1)\n"
      "v_mov_b32_dpp v200, v200 quad_perm:[0,0,0,0] row_mask:0xf bank_mask:0xf\n"
      "v_add_f32 v230, v230, v231\n"
      "v_mfma_f32_16x16x32_bf16 v[208:211], a[4:7], v[148:151], 0\n"
      "v_permlane16_swap_b32 v228, v229\n"
      "v_mfma_f32_16x16x32_bf16 v[208:211], a[40:43], v[140:143], v[208:211]\n"
      "v_add_f32 v228, v228, v229\n"
      "v_cndmask_b32 v248, v248, v224, s[42:43]\n"
      "v_mfma_f32_16x16x32_bf16 v[176:179], a[8:11], v[148:151], 0\n"
      "v_permlane32_swap_b32 v230, v228\n"
      "v_mfma_f32_16x16x32_bf16 v[176:179], a[44:47], v[140:143], v[176:179]\n"
      "v_cndmask_b32 v248, v248, v168, s[44:45]\n"
      "v_add_f32 v230, v230, v228\n"
      "v_mfma_f32_16x16x32_bf16 v[224:227], a[12:15], v[148:151], 0\n"
      "s_or_b32 s28, s28, s29\n"
      "v_rcp_f32_e64 v200, v200\n"
      "v_mfma_f32_16x16x32_bf16 v[224:227], a[48:51], v[140:143], v[224:227]\n"
      "ds_write_b32 v129, v230\n"
      "v_mfma_f32_16x16x32_bf16 v[228:231], a[16:19], v[148:151], 0\n"
      "v_mul_f32_e64 v248, v200, v248\n"
      "v_cvt_pk_bf16_f32 v248, v248, v248\n"
      "v_mfma_f32_16x16x32_bf16 v[228:231], a[52:55], v[140:143], v[228:231]\n"
      "ds_write_b16 v135, v248\n"
      "v_mfma_f32_16x16x32_bf16 v[168:171], a[20:23], v[148:151], 0\n"
      "v_mfma_f32_16x16x32_bf16 v[168:171], a[56:59], v[140:143], v[168:171]\n"
      "L_join_30_%=:\n"
      "s_cbranch_scc0 L_loop_%=\n"
      "L_tail0_%=:\n"
      "s_cmp_eq_u32 s10, s11\n"
      "s_cbranch_scc1 L_sk_35_%=\n"
      "s_mov_b32 s11, s10\n"
      "s_sub_u32 s28, s60, s8\n"
      "s_sub_u32 s29, s61, s9\n"
      "s_mov_b32 s60, s8\n"
      "s_mov_b32 s61, s9\n"
      "s_mov_b64 exec, s[64:65]\n"
      "v_add_u32 v116, s28, v116\n"
      "s_mov_b64 exec, s[70:71]\n"
      "v_add_u32 v116, s29, v116\n"
      "s_mov_b64 exec, s[66:67]\n"
      "v_add_u32 v117, s28, v117\n"
      "s_mov_b64 exec, s[72:73]\n"
      "v_add_u32 v117, s29, v117\n"
      "s_mov_b64 exec, s[68:69]\n"
      "v_add_u32 v118, s28, v118\n"
      "s_mov_b64 exec, s[74:75]\n"
      "v_add_u32 v118, s29, v118\n"
      "s_mov_b64 exec, -1\n"
      "s_nop 4\n"
      "L_sk_35_%=:\n"
      "s_cmp_lt_u32 s27, s26\n"
      "s_cbranch_scc1 L_loop_%=\n"
      "s_nop 7\n"
      "v_rcp_f32 v164, v164\n"
      "v_mbcnt_lo_u32_b32 v166, -1, 0\n"
      "v_pk_mul_f32 v[174:175], v[174:175], v[164:165] op_sel_hi:[1,0]\n"
      "v_pk_mul_f32 v[136:137], v[136:137], v[164:165] op_sel_hi:[1,0]\n"
      "v_cvt_pk_bf16_f32 v136, v136, v137\n"
      "v_pk_mul_f32 v[196:197], v[196:197], v[164:165] op_sel_hi:[1,0]\n"
      "v_pk_mul_f32 v[172:173], v[172:173], v[164:165] op_sel_hi:[1,0]\n"
      "v_cvt_pk_bf16_f32 v196, v196, v197\n"
      "v_mbcnt_hi_u32_b32 v166, -1, v166\n"
      "v_pk_mul_f32 v[198:199], v[198:199], v[164:165] op_sel_hi:[1,0]\n"
      "v_lshrrev_b32 v167, 4, v166\n"
      "v_and_b32 v165, 15, v166\n"
      "v_cvt_pk_bf16_f32 v197, v198, v199\n"
      "v_pk_mul_f32 v[138:139], v[138:139], v[164:165] op_sel_hi:[1,0]\n"
      "v_mul_u32_u24 v167, 352, v167\n"
      "v_cvt_pk_bf16_f32 v198, v172, v173\n"
      "v_cvt_pk_bf16_f32 v199, v174, v175\n"
      "v_cvt_pk_bf16_f32 v137, v138, v139\n"
      "ds_write_b64 v125, v[136:137] offset:2048\n"
      "ds_write_b128 v124, v[196:199] offset:2048\n"
      "v_lshl_add_u32 v167, v165, 4, v167\n"
      "v_add_u32 v167, 150640, v167\n"
      "s_waitcnt lgkmcnt(0)\n"
      "s_barrier\n"
      "ds_read_b128 v[248:251], v123\n"
      "ds_read_b128 v[228:231], v123 offset:3072\n"
      "ds_read_b128 v[148:151], v128\n"
      "s_sub_u32 s28, s27, 3\n"
      "ds_read_b128 v[224:227], v120\n"
      "s_cselect_b32 s22, 0, -1\n"
      "s_max_i32 m0, s28, 0\n"
      "ds_read_b128 v[140:143], v123 offset:9216\n"
      "s_movrels_b32 s9, s80\n"
      "ds_read_b128 v[168:171], v119\n"
      "s_lshr_b32 s8, s9, 8\n"
      "s_and_b32 s9, s9, 0xff\n"
      "s_mul_i32 s28, s8, 1373568\n"
      "s_mul_i32 s29, s9, 2688\n"
      "ds_read_b128 v[162:165], v123 offset:6144\n"
      "s_add_u32 s28, s28, s29\n"
      "ds_read_b128 v[192:195], v123 offset:4096\n"
      "s_cmp_eq_u32 s8, 72\n"
      "ds_read_b128 v[208:211], v119 offset:6144\n"
      "s_cselect_b32 s8, 0, 100270464\n"
      "ds_read_b128 v[252:255], v121\n"
      "s_cmp_eq_u32 s9, 72\n"
      "s_cselect_b32 s9, 0, 196224\n"
      "s_add_u32 s10, s8, s9\n"
      "s_add_u32 s28, s28, s10\n"
      "s_add_u32 s20, s12, s28\n"
      "s_addc_u32 s21, s13, 0\n"
      "s_waitcnt lgkmcnt(7)\n"
      "v_add_f32 v150, |v150|, |v151|\n"
      "v_add_f32 v148, |v148|, |v149|\n"
      "v_mfma_f32_16x16x32_bf16 v[196:199], a[216:219], v[248:251], 0\n"
      "ds_read_b128 v[200:203], v119 offset:12288\n"
      "v_add_f32 v148, v148, v150\n"
      "v_mfma_f32_16x16x32_bf16 v[196:199], a[228:231], v[228:231], v[196:199]\n"
      "ds_read_b128 v[216:219], v123 offset:12288\n"
      "v_rsq_f32 v148, v148\n"
      "v_mfma_f32_16x16x32_bf16 v[172:175], a[220:223], v[248:251], 0\n"
      "ds_read_b128 v[204:207], v123 offset:15360\n"
      "v_mul_f32 v148, s41, v148\n"
      "v_mfma_f32_16x16x32_bf16 v[172:175], a[232:235], v[228:231], v[172:175]\n"
      "ds_read_b128 v[136:139], v123 offset:1024\n"
      "v_mov_b32 v150, v148\n"
      "v_mfma_f32_16x16x32_bf16 v[176:179], a[224:227], v[248:251], 0\n"
      "ds_read_b128 v[248:251], v123 offset:7168\n"
      "s_cmp_lg_u32 s10, s11\n"
      "v_permlane32_swap_b32 v148, v150\n"
      "v_mov_b32 v149, v148\n"
      "v_mfma_f32_16x16x32_bf16 v[176:179], a[236:239], v[228:231], v[176:179]\n"
      "s_waitcnt lgkmcnt(5)\n"
      "v_permlane16_swap_b32 v148, v149\n"
      "v_mul_f32 v214, v148, v214\n"
      "v_mul_f32 v213, v148, v213\n"
      "v_mul_f32 v212, v148, v212\n"
      "v_mfma_f32_16x16x32_bf16 v[196:199], a[240:243], v[162:165], v[196:199]\n"
      "s_cbranch_scc1 L_edge_36_%=\n"
      "L_eb_37_%=:\n"
      "v_mfma_f32_16x16x32_bf16 v[196:199], a[252:255], v[140:143], v[196:199]\n"
      "buffer_store_dwordx4 v[168:171], v116, s[20:23], 0 offen offset:0 sc1\n"
      "v_mfma_f32_16x16x32_bf16 v[172:175], a[244:247], v[162:165], v[172:175]\n"
      "ds_read_b128 v[228:231], v123 offset:10240\n"
      "v_cvt_pk_bf16_f32 v212, v212, v213\n"
      "v_mfma_f32_16x16x32_bf16 v[172:175], v[0:3], v[140:143], v[172:175]\n"
      "v_mul_f32 v215, v148, v215\n"
      "s_waitcnt lgkmcnt(2)\n"
      "v_mfma_f32_16x16x32_bf16 v[176:179], a[248:251], v[162:165], v[176:179]\n"
      "ds_read_b128 v[168:171], v123 offset:13312\n"
      "v_mul_f32 v237, v148, v237\n"
      "v_mfma_f32_16x16x32_bf16 v[176:179], v[4:7], v[140:143], v[176:179]\n"
      "ds_read_b128 v[162:165], v123 offset:16384\n"
      "v_cvt_pk_bf16_f32 v213, v214, v215\n"
      "v_mfma_f32_16x16x32_bf16 v[196:199], v[8:11], v[216:219], v[196:199]\n"
      "v_mul_f32 v236, v148, v236\n"
      "v_mul_f32 v238, v148, v238\n"
      "v_mfma_f32_16x16x32_bf16 v[196:199], v[20:23], v[204:207], v[196:199]\n"
      "v_cvt_pk_bf16_f32 v214, v236, v237\n"
      "v_mul_f32 v239, v148, v239\n"
      "v_mfma_f32_16x16x32_bf16 v[172:175], v[12:15], v[216:219], v[172:175]\n"
      "v_cvt_pk_bf16_f32 v215, v238, v239\n"
      "v_mul_f32_e64 v152, v148, v152\n"
      "v_mfma_f32_16x16x32_bf16 v[172:175], v[24:27], v[204:207], v[172:175]\n"
      "ds_write_b128 v126, v[212:215] offset:18432\n"
      "v_mfma_f32_16x16x32_bf16 v[176:179], v[16:19], v[216:219], v[176:179]\n"
      "ds_read_b128 v[216:219], v123 offset:2048\n"
      "v_mul_f32_e64 v153, v148, v153\n"
      "v_mfma_f32_16x16x32_bf16 v[176:179], v[28:31], v[204:207], v[176:179]\n"
      "buffer_store_dwordx4 v[208:211], v116, s[20:23], 0 offen offset:128 sc1\n"
      "v_mfma_f32_16x16x32_bf16 v[204:207], a[216:219], v[136:139], 0\n"
      "ds_read_b128 v[212:215], v123 offset:5120\n"
      "v_cvt_pk_bf16_f32 v152, v152, v153\n"
      "v_mfma_f32_16x16x32_bf16 v[204:207], a[228:231], v[192:195], v[204:207]\n"
      "s_waitcnt lgkmcnt(3)\n"
      "v_mul_f32 v155, v148, v155\n"
      "v_mul_f32 v154, v148, v154\n"
      "v_mfma_f32_16x16x32_bf16 v[236:239], a[220:223], v[136:139], 0\n"
      "v_mul_f32 v156, v149, v156\n"
      "v_cvt_pk_bf16_f32 v153, v154, v155\n"
      "v_mfma_f32_16x16x32_bf16 v[236:239], a[232:235], v[192:195], v[236:239]\n"
      "ds_write_b64 v127, v[152:153] offset:18432\n"
      "v_mfma_f32_16x16x32_bf16 v[152:155], a[224:227], v[136:139], 0\n"
      "ds_read_b128 v[232:235], v123 offset:8192\n"
      "v_mul_f32_e64 v157, v149, v157\n"
      "v_mfma_f32_16x16x32_bf16 v[152:155], a[236:239], v[192:195], v[152:155]\n"
      "ds_read_b128 v[140:143], v123 offset:11264\n"
      "v_cvt_pk_bf16_f32 v156, v156, v157\n"
      "v_mfma_f32_16x16x32_bf16 v[204:207], a[240:243], v[248:251], v[204:207]\n"
      "s_lshl_b32 s29, s24, 8\n"
      "v_mul_f32 v159, v149, v159\n"
      "v_mul_f32_e64 v158, v149, v158\n"
      "v_mfma_f32_16x16x32_bf16 v[204:207], a[252:255], v[228:231], v[204:207]\n"
      "s_add_u32 s29, s29, 159088\n"
      "v_mul_f32_e64 v188, v149, v188\n"
      "v_cvt_pk_bf16_f32 v157, v158, v159\n"
      "v_mfma_f32_16x16x32_bf16 v[236:239], a[244:247], v[248:251], v[236:239]\n"
      "v_mul_f32 v189, v149, v189\n"
      "v_cvt_pk_bf16_f32 v158, v188, v189\n"
      "v_mfma_f32_16x16x32_bf16 v[236:239], v[0:3], v[228:231], v[236:239]\n"
      "s_waitcnt lgkmcnt(3)\n"
      "v_mul_f32 v191, v149, v191\n"
      "v_mul_f32 v190, v149, v190\n"
      "v_mfma_f32_16x16x32_bf16 v[152:155], a[248:251], v[248:251], v[152:155]\n"
      "v_cvt_pk_bf16_f32 v159, v190, v191\n"
      "v_mul_f32_e64 v144, v149, v144\n"
      "v_mfma_f32_16x16x32_bf16 v[152:155], v[4:7], v[228:231], v[152:155]\n"
      "ds_write_b128 v126, v[156:159] offset:20480\n"
      "v_mfma_f32_16x16x32_bf16 v[204:207], v[8:11], v[168:171], v[204:207]\n"
      "ds_read_b128 v[248:251], v123 offset:14336\n"
      "v_mul_f32_e64 v145, v149, v145\n"
      "v_mfma_f32_16x16x32_bf16 v[204:207], v[20:23], v[162:165], v[204:207]\n"
      "ds_read_b128 v[188:191], v123 offset:17408\n"
      "v_cvt_pk_bf16_f32 v144, v144, v145\n"
      "v_mfma_f32_16x16x32_bf16 v[236:239], v[12:15], v[168:171], v[236:239]\n"
      "v_mul_f32 v146, v149, v146\n"
      "v_mul_f32_e64 v147, v149, v147\n"
      "v_mfma_f32_16x16x32_bf16 v[236:239], v[24:27], v[162:165], v[236:239]\n"
      "s_waitcnt lgkmcnt(3)\n"
      "v_cvt_pk_bf16_f32 v145, v146, v147\n"
      "v_fma_f32 v146, v196, v196, s39\n"
      "v_mfma_f32_16x16x32_bf16 v[152:155], v[16:19], v[168:171], v[152:155]\n"
      "buffer_store_dwordx4 v[200:203], v116, s[20:23], 0 offen offset:256 sc1\n"
      "v_mfma_f32_16x16x32_bf16 v[152:155], v[28:31], v[162:165], v[152:155]\n"
      "ds_write_b64 v127, v[144:145] offset:20480\n"
      "v_mfma_f32_16x16x32_bf16 v[228:231], a[216:219], v[216:219], 0\n"
      "ds_read_b128 v[200:203], v167\n"
      "v_fmac_f32 v146, v197, v197\n"
      "v_mfma_f32_16x16x32_bf16 v[228:231], a[228:231], v[212:215], v[228:231]\n"
      "ds_read_b128 v[168:171], v167 offset:1408\n"
      "v_fmac_f32 v146, v198, v198\n"
      "v_mfma_f32_16x16x32_bf16 v[162:165], a[220:223], v[216:219], 0\n"
      "v_fmac_f32 v146, v199, v199\n"
      "v_fmac_f32 v146, v172, v172\n"
      "v_mfma_f32_16x16x32_bf16 v[162:165], a[232:235], v[212:215], v[162:165]\n"
      "s_waitcnt lgkmcnt(3)\n"
      "v_fmac_f32 v146, v173, v173\n"
      "v_fmac_f32_e64 v146, v174, v174\n"
      "v_mfma_f32_16x16x32_bf16 v[156:159], a[224:227], v[216:219], 0\n"
      "v_fmac_f32 v146, v175, v175\n"
      "v_fmac_f32 v146, v176, v176\n"
      "v_mfma_f32_16x16x32_bf16 v[156:159], a[236:239], v[212:215], v[156:159]\n"
      "v_fmac_f32 v146, v177, v177\n"
      "v_fmac_f32 v146, v178, v178\n"
      "v_mfma_f32_16x16x32_bf16 v[228:231], a[240:243], v[232:235], v[228:231]\n"
      "v_fmac_f32 v146, v179, v179\n"
      "v_mul_f32 v181, v150, v181\n"
      "v_mfma_f32_16x16x32_bf16 v[228:231], a[252:255], v[140:143], v[228:231]\n"
      "ds_read_b128 v[212:215], v167 offset:2816\n"
      "v_mul_f32 v180, v150, v180\n"
      "v_mfma_f32_16x16x32_bf16 v[162:165], a[244:247], v[232:235], v[162:165]\n"
      "ds_read_b128 v[136:139], v167 offset:4224\n"
      "v_mul_f32 v182, v150, v182\n"
      "v_mfma_f32_16x16x32_bf16 v[162:165], v[0:3], v[140:143], v[162:165]\n"
      "v_cvt_pk_bf16_f32 v180, v180, v181\n"
      "v_mul_f32_e64 v183, v150, v183\n"
      "v_mfma_f32_16x16x32_bf16 v[156:159], a[248:251], v[232:235], v[156:159]\n"
      "buffer_store_dwordx4 v[224:227], v117, s[20:23], 0 offen offset:0 sc1\n"
      "v_mfma_f32_16x16x32_bf16 v[156:159], v[4:7], v[140:143], v[156:159]\n"
      "s_waitcnt lgkmcnt(2)\n"
      "v_mul_f32 v220, v150, v220\n"
      "v_cvt_pk_bf16_f32 v181, v182, v183\n"
      "v_mfma_f32_16x16x32_bf16 v[228:231], v[8:11], v[248:251], v[228:231]\n"
      "v_mul_f32 v221, v150, v221\n"
      "v_cvt_pk_bf16_f32 v182, v220, v221\n"
      "v_mfma_f32_16x16x32_bf16 v[228:231], v[20:23], v[188:191], v[228:231]\n"
      "ds_read_b128 v[224:227], v167 offset:5632\n"
      "v_mul_f32 v223, v150, v223\n"
      "v_mfma_f32_16x16x32_bf16 v[162:165], v[12:15], v[248:251], v[162:165]\n"
      "v_mul_f32_e64 v222, v150, v222\n"
      "v_cvt_pk_bf16_f32 v183, v222, v223\n"
      "v_mfma_f32_16x16x32_bf16 v[162:165], v[24:27], v[188:191], v[162:165]\n"
      "ds_write_b128 v126, v[180:183] offset:22528\n"
      "v_mfma_f32_16x16x32_bf16 v[156:159], v[16:19], v[248:251], v[156:159]\n"
      "ds_read_b128 v[142:145], v167 offset:7040\n"
      "v_mul_f32_e64 v240, v150, v240\n"
      "v_mfma_f32_16x16x32_bf16 v[156:159], v[28:31], v[188:191], v[156:159]\n"
      "s_waitcnt lgkmcnt(3)\n"
      "v_mul_f32 v241, v150, v241\n"
      "v_cvt_pk_bf16_f32 v240, v240, v241\n"
      "v_mfma_f32_16x16x32_bf16 v[244:247], a[216:219], v[200:203], 0\n"
      "v_mul_f32 v243, v150, v243\n"
      "v_mul_f32 v242, v150, v242\n"
      "v_mfma_f32_16x16x32_bf16 v[244:247], a[228:231], v[168:171], v[244:247]\n"
      "ds_read_b128 v[188:191], v167 offset:256\n"
      "v_cvt_pk_bf16_f32 v241, v242, v243\n"
      "v_mfma_f32_16x16x32_bf16 v[248:251], a[220:223], v[200:203], 0\n"
      "ds_read_b128 v[148:151], v167 offset:1664\n"
      "v_fma_f32 v147, v204, v204, s39\n"
      "v_mfma_f32_16x16x32_bf16 v[248:251], a[232:235], v[168:171], v[248:251]\n"
      "ds_write_b64 v127, v[240:241] offset:22528\n"
      "v_mfma_f32_16x16x32_bf16 v[240:243], a[224:227], v[200:203], 0\n"
      "v_fmac_f32 v147, v205, v205\n"
      "v_fmac_f32 v147, v206, v206\n"
      "v_mfma_f32_16x16x32_bf16 v[240:243], a[236:239], v[168:171], v[240:243]\n"
      "ds_read_b128 v[168:171], v167 offset:3072\n"
      "v_fmac_f32 v147, v207, v207\n"
      "v_mfma_f32_16x16x32_bf16 v[244:247], a[240:243], v[212:215], v[244:247]\n"
      "v_fmac_f32 v147, v236, v236\n"
      "v_fmac_f32_e64 v147, v237, v237\n"
      "v_mfma_f32_16x16x32_bf16 v[244:247], a[252:255], v[136:139], v[244:247]\n"
      "ds_read_b128 v[200:203], v167 offset:4480\n"
      "v_fmac_f32 v147, v238, v238\n"
      "v_mfma_f32_16x16x32_bf16 v[248:251], a[244:247], v[212:215], v[248:251]\n"
      "v_fmac_f32 v147, v239, v239\n"
      "v_fmac_f32 v147, v152, v152\n"
      "v_mfma_f32_16x16x32_bf16 v[248:251], v[0:3], v[136:139], v[248:251]\n"
      "v_fmac_f32 v147, v153, v153\n"
      "v_fmac_f32 v147, v154, v154\n"
      "v_mfma_f32_16x16x32_bf16 v[240:243], a[248:251], v[212:215], v[240:243]\n"
      "s_waitcnt lgkmcnt(3)\n"
      "v_fmac_f32 v147, v155, v155\n"
      "v_lshl_add_u32 v140, v166, 2, s29\n"
      "v_mfma_f32_16x16x32_bf16 v[240:243], v[4:7], v[136:139], v[240:243]\n"
      "ds_read_b32 v140, v140\n"
      "v_lshrrev_b32 v141, 3, v166\n"
      "v_mfma_f32_16x16x32_bf16 v[244:247], v[8:11], v[224:227], v[244:247]\n"
      "ds_read_b128 v[192:195], v167 offset:5888\n"
      "v_fma_f32 v166, v228, v228, s39\n"
      "v_mfma_f32_16x16x32_bf16 v[244:247], v[20:23], v[142:145], v[244:247]\n"
      "ds_read_b128 v[180:183], v167 offset:7296\n"
      "v_fmac_f32 v166, v229, v229\n"
      "v_mfma_f32_16x16x32_bf16 v[248:251], v[12:15], v[224:227], v[248:251]\n"
      "s_lshl_b32 s29, s24, 3\n"
      "v_add_u32_e64 v141, s29, v141\n"
      "v_cmp_gt_u32 s[48:49], s26, v141\n"
      "v_mfma_f32_16x16x32_bf16 v[248:251], v[24:27], v[142:145], v[248:251]\n"
      "v_fmac_f32 v166, v230, v230\n"
      "v_fmac_f32 v166, v231, v231\n"
      "v_mfma_f32_16x16x32_bf16 v[240:243], v[16:19], v[224:227], v[240:243]\n"
      "v_fmac_f32 v166, v162, v162\n"
      "v_fmac_f32 v166, v163, v163\n"
      "v_mfma_f32_16x16x32_bf16 v[240:243], v[28:31], v[142:145], v[240:243]\n"
      "s_mov_b32 exec_hi, 0\n"
      "buffer_store_dwordx4 v[252:255], v118, s[20:23], 0 offen offset:0 sc1\n"
      "s_mov_b32 exec_hi, -1\n"
      "v_mfma_f32_16x16x32_bf16 v[224:227], a[216:219], v[188:191], 0\n"
      "v_fmac_f32 v166, v164, v164\n"
      "v_fmac_f32 v166, v165, v165\n"
      "v_mfma_f32_16x16x32_bf16 v[224:227], a[228:231], v[148:151], v[224:227]\n"
      "v_fmac_f32 v166, v156, v156\n"
      "v_fmac_f32 v166, v157, v157\n"
      "v_mfma_f32_16x16x32_bf16 v[252:255], a[220:223], v[188:191], 0\n"
      "v_fmac_f32 v166, v158, v158\n"
      "v_mfma_f32_16x16x32_bf16 v[252:255], a[232:235], v[148:151], v[252:255]\n"
      "v_permlane16_swap_b32 v146, v147\n"
      "v_mfma_f32_16x16x32_bf16 v[142:145], a[224:227], v[188:191], 0\n"
      "s_waitcnt lgkmcnt(0)\n"
      "v_add_f32 v146, v146, v147\n"
      "v_mfma_f32_16x16x32_bf16 v[142:145], a[236:239], v[148:151], v[142:145]\n"
      "v_fmac_f32 v166, v159, v159\n"
      "v_mov_b32 v167, v166\n"
      "v_mfma_f32_16x16x32_bf16 v[142:145], a[248:251], v[168:171], v[142:145]\n"
      "s_nop 0\n"
      "v_permlane16_swap_b32 v166, v167\n"
      "v_mfma_f32_16x16x32_bf16 v[224:227], a[240:243], v[168:171], v[224:227]\n"
      "v_add_f32_e64 v166, v166, v167\n"
      "v_fma_f32 v167, v244, v244, s39\n"
      "v_mfma_f32_16x16x32_bf16 v[224:227], a[252:255], v[200:203], v[224:227]\n"
      "v_permlane32_swap_b32 v146, v166\n"
      "v_mfma_f32_16x16x32_bf16 v[252:255], a[244:247], v[168:171], v[252:255]\n"
      "v_add_f32 v146, v146, v166\n"
      "v_fmac_f32_e64 v167, v245, v245\n"
      "v_mfma_f32_16x16x32_bf16 v[252:255], v[0:3], v[200:203], v[252:255]\n"
      "v_fmac_f32_e64 v167, v246, v246\n"
      "v_mfma_f32_16x16x32_bf16 v[252:255], v[12:15], v[192:195], v[252:255]\n"
      "v_mfma_f32_16x16x32_bf16 v[252:255], v[24:27], v[180:183], v[252:255]\n"
      "ds_write_b32 v129, v146 offset:1024\n"
      "v_mfma_f32_16x16x32_bf16 v[142:145], v[4:7], v[200:203], v[142:145]\n"
      "v_fmac_f32 v167, v247, v247\n"
      "v_fmac_f32 v167, v248, v248\n"
      "v_mfma_f32_16x16x32_bf16 v[142:145], v[16:19], v[192:195], v[142:145]\n"
      "v_fmac_f32 v167, v249, v249\n"
      "v_fmac_f32 v167, v250, v250\n"
      "v_mfma_f32_16x16x32_bf16 v[224:227], v[8:11], v[192:195], v[224:227]\n"
      "v_fmac_f32 v167, v251, v251\n"
      "v_mfma_f32_16x16x32_bf16 v[224:227], v[20:23], v[180:183], v[224:227]\n"
      "v_fmac_f32 v167, v240, v240\n"
      "v_fmac_f32 v167, v241, v241\n"
      "v_mfma_f32_16x16x32_bf16 v[142:145], v[28:31], v[180:183], v[142:145]\n"
      "v_fmac_f32 v167, v242, v242\n"
      "v_fmac_f32 v167, v243, v243\n"
      "s_nop 2\n"
      "v_pk_mul_f32 v[146:147], v[224:225], v[224:225]\n"
      "v_pk_fma_f32 v[146:147], v[226:227], v[226:227], v[146:147]\n"
      "v_pk_fma_f32 v[146:147], v[252:253], v[252:253], v[146:147]\n"
      "v_pk_fma_f32 v[146:147], v[254:255], v[254:255], v[146:147]\n"
      "v_pk_fma_f32 v[146:147], v[142:143], v[142:143], v[146:147]\n"
      "v_pk_fma_f32 v[146:147], v[144:145], v[144:145], v[146:147]\n"
      "v_add_f32 v146, s39, v146\n"
      "v_add_f32 v166, v146, v147\n"
      "v_mov_b32 v147, v166\n"
      "v_mov_b32 v146, v147\n"
      "v_permlane16_swap_b32 v167, v166\n"
      "v_add_f32 v167, v167, v166\n"
      "v_permlane16_swap_b32 v147, v146\n"
      "v_add_f32_e64 v147, v147, v146\n"
      "s_nop 1\n"
      "v_permlane32_swap_b32_e64 v167, v147\n"
      "v_add_f32_e64 v167, v167, v147\n"
      "ds_write_b32 v129, v167 offset:2048\n"
      "s_waitcnt lgkmcnt(0)\n"
      "s_barrier\n"
      "ds_read_b128 v[168:171], v119 offset:18432\n"
      "ds_read_b128 v[200:203], v128 offset:2048\n"
      "s_add_u32 s27, s27, 1\n"
      "ds_read_b128 v[148:151], v128 offset:1024\n"
      "s_sub_u32 s28, s27, 3\n"
      "v_subrev_u32 v146, 73728, v126\n"
      "s_cselect_b32 s22, 0, -1\n"
      "s_max_i32 m0, s28, 0\n"
      "ds_read_b128 v[180:183], v120 offset:18432\n"
      "s_movrels_b32 s9, s80\n"
      "ds_read_b128 v[136:139], v119 offset:30720\n"
      "s_lshr_b32 s8, s9, 8\n"
      "s_and_b32 s9, s9, 0xff\n"
      "s_mul_i32 s28, s8, 1373568\n"
      "ds_read_b128 v[216:219], v119 offset:24576\n"
      "s_mul_i32 s29, s9, 2688\n"
      "s_add_u32 s28, s28, s29\n"
      "s_cmp_eq_u32 s8, 72\n"
      "s_cselect_b32 s8, 0, 100270464\n"
      "s_cmp_eq_u32 s9, 72\n"
      "s_cselect_b32 s9, 0, 196224\n"
      "s_add_u32 s10, s8, s9\n"
      "s_add_u32 s28, s28, s10\n"
      "s_add_u32 s20, s12, s28\n"
      "s_addc_u32 s21, s13, 0\n"
      "s_cmp_lg_u32 s10, s11\n"
      "ds_read_b128 v[188:191], v121 offset:18432\n"
      "s_cbranch_scc1 L_edge_38_%=\n"
      "L_eb_39_%=:\n"
      "s_waitcnt lgkmcnt(4)\n"
      "v_add_f32 v150, |v150|, |v151|\n"
      "v_add_f32 v148, |v148|, |v149|\n"
      "v_add_f32 v148, v148, v150\n"
      "v_rsq_f32 v148, v148\n"
      "v_subrev_u32 v147, 73728, v127\n"
      "v_mul_f32 v148, s41, v148\n"
      "v_mov_b32 v167, v148\n"
      "v_add_f32 v202, |v202|, |v203|\n"
      "v_add_f32 v200, |v200|, |v201|\n"
      "v_permlane32_swap_b32 v148, v167\n"
      "v_mov_b32 v166, v148\n"
      "v_pk_mul_f32 v[230:231], v[230:231], v[166:167] op_sel:[0,1]\n"
      "v_pk_mul_f32 v[156:157], v[156:157], v[166:167] op_sel:[0,1]\n"
      "v_pk_mul_f32 v[162:163], v[162:163], v[166:167] op_sel:[0,1]\n"
      "v_permlane16_swap_b32 v148, v166\n"
      "v_add_f32 v200, v200, v202\n"
      "v_pk_mul_f32 v[238:239], v[238:239], v[166:167] op_sel_hi:[1,0]\n"
      "v_pk_mul_f32 v[206:207], v[206:207], v[166:167] op_sel_hi:[1,0]\n"
      "v_pk_mul_f32 v[228:229], v[228:229], v[166:167] op_sel:[0,1]\n"
      "v_pk_mul_f32 v[154:155], v[154:155], v[166:167] op_sel_hi:[1,0]\n"
      "v_pk_mul_f32 v[204:205], v[204:205], v[166:167] op_sel_hi:[1,0]\n"
      "v_pk_mul_f32 v[152:153], v[152:153], v[166:167] op_sel_hi:[1,0]\n"
      "v_rsq_f32_e64 v200, v200\n"
      "v_cvt_pk_bf16_f32 v152, v152, v153\n"
      "v_pk_mul_f32 v[236:237], v[236:237], v[166:167] op_sel_hi:[1,0]\n"
      "v_mul_f32 v200, s41, v200\n"
      "v_pk_mul_f32 v[176:177], v[176:177], v[148:149] op_sel_hi:[1,0]\n"
      "v_mov_b32 v166, v200\n"
      "v_cvt_pk_bf16_f32 v204, v204, v205\n"
      "v_pk_mul_f32 v[196:197], v[196:197], v[148:149] op_sel_hi:[1,0]\n"
      "v_cvt_pk_bf16_f32 v196, v196, v197\n"
      "v_permlane32_swap_b32_e64 v200, v166\n"
      "v_pk_mul_f32 v[178:179], v[178:179], v[148:149] op_sel_hi:[1,0]\n"
      "v_pk_mul_f32 v[174:175], v[174:175], v[148:149] op_sel_hi:[1,0]\n"
      "v_pk_mul_f32 v[164:165], v[164:165], v[166:167] op_sel:[0,1]\n"
      "v_cvt_pk_bf16_f32 v156, v156, v157\n"
      "v_cvt_pk_bf16_f32 v176, v176, v177\n"
      "v_cvt_pk_bf16_f32 v228, v228, v229\n"
      "v_pk_mul_f32 v[158:159], v[158:159], v[166:167] op_sel:[0,1]\n"
      "v_pk_mul_f32 v[198:199], v[198:199], v[148:149] op_sel_hi:[1,0]\n"
      "v_mov_b32_e64 v167, v200\n"
      "v_cvt_pk_bf16_f32 v229, v230, v231\n"
      "v_cvt_pk_bf16_f32 v197, v198, v199\n"
      "v_cvt_pk_bf16_f32 v177, v178, v179\n"
      "v_cvt_pk_bf16_f32 v199, v174, v175\n"
      "v_permlane16_swap_b32 v200, v167\n"
      "v_pk_mul_f32 v[244:245], v[244:245], v[200:201] op_sel_hi:[1,0]\n"
      "v_pk_mul_f32 v[254:255], v[254:255], v[166:167] op_sel:[0,1]\n"
      "v_cvt_pk_bf16_f32 v244, v244, v245\n"
      "v_pk_mul_f32 v[250:251], v[250:251], v[200:201] op_sel_hi:[1,0]\n"
      "v_pk_mul_f32 v[172:173], v[172:173], v[148:149] op_sel_hi:[1,0]\n"
      "v_cvt_pk_bf16_f32 v231, v164, v165\n"
      "v_cvt_pk_bf16_f32 v205, v206, v207\n"
      "v_pk_mul_f32 v[224:225], v[224:225], v[166:167] op_sel:[0,1]\n"
      "v_pk_mul_f32 v[240:241], v[240:241], v[200:201] op_sel_hi:[1,0]\n"
      "v_cvt_pk_bf16_f32 v240, v240, v241\n"
      "v_pk_mul_f32 v[226:227], v[226:227], v[166:167] op_sel:[0,1]\n"
      "v_cvt_pk_bf16_f32 v198, v172, v173\n"
      "v_cvt_pk_bf16_f32 v157, v158, v159\n"
      "v_cvt_pk_bf16_f32 v207, v238, v239\n"
      "v_cvt_pk_bf16_f32 v153, v154, v155\n"
      "v_cvt_pk_bf16_f32 v206, v236, v237\n"
      "v_pk_mul_f32 v[142:143], v[142:143], v[166:167] op_sel:[0,1]\n"
      "v_cvt_pk_bf16_f32 v224, v224, v225\n"
      "v_cvt_pk_bf16_f32 v225, v226, v227\n"
      "s_waitcnt lgkmcnt(0)\n"
      "v_cvt_pk_bf16_f32 v142, v142, v143\n"
      "v_pk_mul_f32 v[246:247], v[246:247], v[200:201] op_sel_hi:[1,0]\n"
      "v_pk_mul_f32 v[252:253], v[252:253], v[166:167] op_sel:[0,1]\n"
      "ds_write_b64 v127, v[156:157] offset:4096\n"
      "ds_write_b128 v126, v[196:199]\n"
      "buffer_store_dwordx4 v[168:171], v116, s[20:23], 0 offen offset:0 sc1\n"
      "v_cvt_pk_bf16_f32 v245, v246, v247\n"
      "v_cvt_pk_bf16_f32 v230, v162, v163\n"
      "v_pk_mul_f32 v[144:145], v[144:145], v[166:167] op_sel:[0,1]\n"
      "v_cvt_pk_bf16_f32 v143, v144, v145\n"
      "v_cvt_pk_bf16_f32 v226, v252, v253\n"
      "v_pk_mul_f32 v[242:243], v[242:243], v[200:201] op_sel_hi:[1,0]\n"
      "v_cvt_pk_bf16_f32 v227, v254, v255\n"
      "v_pk_mul_f32 v[248:249], v[248:249], v[200:201] op_sel_hi:[1,0]\n"
      "v_cvt_pk_bf16_f32 v247, v250, v251\n"
      "v_cvt_pk_bf16_f32 v241, v242, v243\n"
      "v_cvt_pk_bf16_f32 v246, v248, v249\n"
      "ds_write_b128 v126, v[204:207] offset:2048\n"
      "ds_write_b128 v146, v[224:227] offset:2048\n"
      "buffer_store_dwordx4 v[216:219], v116, s[20:23], 0 offen offset:128 sc1\n"
      "buffer_store_dwordx4 v[136:139], v116, s[20:23], 0 offen offset:256 sc1\n"
      "ds_write_b64 v127, v[152:153] offset:2048\n"
      "ds_write_b128 v146, v[244:247]\n"
      "ds_write_b64 v147, v[142:143] offset:2048\n"
      "ds_write_b64 v147, v[240:241]\n"
      "ds_write_b128 v126, v[228:231] offset:4096\n"
      "ds_write_b64 v127, v[176:177]\n"
      "buffer_store_dwordx4 v[180:183], v117, s[20:23], 0 offen offset:0 sc1\n"
      "s_mov_b32 exec_hi, 0\n"
      "buffer_store_dwordx4 v[188:191], v118, s[20:23], 0 offen offset:0 sc1\n"
      "s_mov_b32 exec_hi, -1\n"
      "s_waitcnt lgkmcnt(0)\n"
      "s_barrier\n"
      "ds_read_b128 v[248:251], v119 offset:6144\n"
      "ds_read_b128 v[240:243], v121\n"
      "ds_read_b128 v[228:231], v119 offset:12288\n"
      "ds_read_b128 v[188:191], v119\n"
      "v_subrev_u32 v164, 73728, v119\n"
      "s_add_u32 s27, s27, 1\n"
      "ds_read_b128 v[144:147], v164 offset:6144\n"
      "s_sub_u32 s28, s27, 3\n"
      "s_cselect_b32 s22, 0, -1\n"
      "s_max_i32 m0, s28, 0\n"
      "ds_read_b128 v[252:255], v164 offset:12288\n"
      "s_movrels_b32 s9, s80\n"
      "s_lshr_b32 s8, s9, 8\n"
      "s_and_b32 s9, s9, 0xff\n"
      "s_mul_i32 s28, s8, 1373568\n"
      "s_mul_i32 s29, s9, 2688\n"
      "s_add_u32 s28, s28, s29\n"
      "s_cmp_eq_u32 s8, 72\n"
      "s_cselect_b32 s8, 0, 100270464\n"
      "ds_read_b128 v[168:171], v120\n"
      "ds_read_b128 v[224:227], v164\n"
      "s_cmp_eq_u32 s9, 72\n"
      "s_cselect_b32 s9, 0, 196224\n"
      "s_add_u32 s10, s8, s9\n"
      "s_add_u32 s28, s28, s10\n"
      "s_add_u32 s20, s12, s28\n"
      "s_addc_u32 s21, s13, 0\n"
      "s_cmp_lg_u32 s10, s11\n"
      "s_cbranch_scc1 L_edge_40_%=\n"
      "L_eb_41_%=:\n"
      "s_waitcnt lgkmcnt(4)\n"
      "buffer_store_dwordx4 v[188:191], v116, s[20:23], 0 offen offset:0 sc1\n"
      "buffer_store_dwordx4 v[248:251], v116, s[20:23], 0 offen offset:128 sc1\n"
      "buffer_store_dwordx4 v[228:231], v116, s[20:23], 0 offen offset:256 sc1\n"
      "s_waitcnt lgkmcnt(0)\n"
      "buffer_store_dwordx4 v[168:171], v117, s[20:23], 0 offen offset:0 sc1\n"
      "s_mov_b32 exec_hi, 0\n"
      "buffer_store_dwordx4 v[240:243], v118, s[20:23], 0 offen offset:0 sc1\n"
      "s_mov_b32 exec_hi, -1\n"
      "s_nop 0\n"
      "s_mov_b64 exec, s[48:49]\n"
      "s_cbranch_execz L_np_42_%=\n"
      "global_store_dwordx4 v140, v[224:227], s[12:13] sc1\n"
      "global_store_dwordx4 v140, v[144:147], s[12:13] offset:128 sc1\n"
      "global_store_dwordx4 v140, v[252:255], s[12:13] offset:256 sc1\n"
      "L_np_42_%=:\n"
      "s_mov_b64 exec, -1\n"
      "s_endpgm\n"
      "L_tail1_%=:\n"
      "s_cmp_eq_u32 s10, s11\n"
      "s_cbranch_scc1 L_sk_43_%=\n"
      "s_mov_b32 s11, s10\n"
      "s_sub_u32 s28, s60, s8\n"
      "s_sub_u32 s29, s61, s9\n"
      "s_mov_b32 s60, s8\n"
      "s_mov_b32 s61, s9\n"
      "s_mov_b64 exec, s[64:65]\n"
      "v_add_u32 v116, s28, v116\n"
      "s_mov_b64 exec, s[70:71]\n"
      "v_add_u32 v116, s29, v116\n"
      "s_mov_b64 exec, s[66:67]\n"
      "v_add_u32 v117, s28, v117\n"
      "s_mov_b64 exec, s[72:73]\n"
      "v_add_u32 v117, s29, v117\n"
      "s_mov_b64 exec, s[68:69]\n"
      "v_add_u32 v118, s28, v118\n"
      "s_mov_b64 exec, s[74:75]\n"
      "v_add_u32 v118, s29, v118\n"
      "s_mov_b64 exec, -1\n"
      "s_nop 4\n"
      "L_sk_43_%=:\n"
      "s_cmp_lt_u32 s27, s26\n"
      "s_cbranch_scc1 L_mid_%=\n"
      "s_nop 7\n"
      "v_rcp_f32_e64 v156, v156\n"
      "v_mbcnt_lo_u32_b32 v246, -1, 0\n"
      "v_pk_mul_f32 v[166:167], v[166:167], v[156:157] op_sel_hi:[1,0]\n"
      "v_pk_mul_f32 v[148:149], v[148:149], v[156:157] op_sel_hi:[1,0]\n"
      "v_cvt_pk_bf16_f32 v148, v148, v149\n"
      "v_pk_mul_f32 v[224:225], v[224:225], v[156:157] op_sel_hi:[1,0]\n"
      "v_pk_mul_f32 v[164:165], v[164:165], v[156:157] op_sel_hi:[1,0]\n"
      "v_cvt_pk_bf16_f32 v224, v224, v225\n"
      "v_mbcnt_hi_u32_b32 v246, -1, v246\n"
      "v_pk_mul_f32 v[226:227], v[226:227], v[156:157] op_sel_hi:[1,0]\n"
      "v_lshrrev_b32 v245, 4, v246\n"
      "v_and_b32 v158, 15, v246\n"
      "v_cvt_pk_bf16_f32 v225, v226, v227\n"
      "v_pk_mul_f32 v[150:151], v[150:151], v[156:157] op_sel_hi:[1,0]\n"
      "v_mul_u32_u24 v245, 352, v245\n"
      "v_cvt_pk_bf16_f32 v226, v164, v165\n"
      "v_cvt_pk_bf16_f32 v227, v166, v167\n"
      "v_cvt_pk_bf16_f32 v149, v150, v151\n"
      "ds_write_b64 v125, v[148:149] offset:20480\n"
      "ds_write_b128 v124, v[224:227] offset:20480\n"
      "v_lshl_add_u32 v245, v158, 4, v245\n"
      "v_add_u32 v245, 150640, v245\n"
      "s_waitcnt lgkmcnt(0)\n"
      "s_barrier\n"
      "ds_read_b128 v[180:183], v128 offset:1024\n"
      "ds_read_b128 v[148:151], v123 offset:18432\n"
      "s_sub_u32 s28, s27, 3\n"
      "ds_read_b128 v[140:143], v123 offset:21504\n"
      "s_cselect_b32 s22, 0, -1\n"
      "ds_read_b128 v[216:219], v120 offset:18432\n"
      "s_max_i32 m0, s28, 0\n"
      "ds_read_b128 v[192:195], v119 offset:18432\n"
      "ds_read_b128 v[172:175], v121 offset:18432\n"
      "s_movrels_b32 s9, s80\n"
      "s_lshr_b32 s8, s9, 8\n"
      "s_and_b32 s9, s9, 0xff\n"
      "s_mul_i32 s28, s8, 1373568\n"
      "s_mul_i32 s29, s9, 2688\n"
      "s_add_u32 s28, s28, s29\n"
      "s_cmp_eq_u32 s8, 72\n"
      "ds_read_b128 v[156:159], v119 offset:24576\n"
      "ds_read_b128 v[212:215], v123 offset:24576\n"
      "s_cselect_b32 s8, 0, 100270464\n"
      "s_cmp_eq_u32 s9, 72\n"
      "s_cselect_b32 s9, 0, 196224\n"
      "ds_read_b128 v[164:167], v119 offset:30720\n"
      "s_add_u32 s10, s8, s9\n"
      "s_add_u32 s28, s28, s10\n"
      "s_waitcnt lgkmcnt(8)\n"
      "s_add_u32 s20, s12, s28\n"
      "v_add_f32 v182, |v182|, |v183|\n"
      "v_add_f32 v180, |v180|, |v181|\n"
      "s_addc_u32 s21, s13, 0\n"
      "s_waitcnt lgkmcnt(6)\n"
      "v_mfma_f32_16x16x32_bf16 v[160:163], a[216:219], v[148:151], 0\n"
      "ds_read_b128 v[220:223], v123 offset:27648\n"
      "v_add_f32_e64 v180, v180, v182\n"
      "v_mfma_f32_16x16x32_bf16 v[160:163], a[228:231], v[140:143], v[160:163]\n"
      "ds_read_b128 v[224:227], v123 offset:30720\n"
      "ds_read_b128 v[176:179], v123 offset:22528\n"
      "v_rsq_f32_e64 v180, v180\n"
      "v_mfma_f32_16x16x32_bf16 v[236:239], a[220:223], v[148:151], 0\n"
      "ds_read_b128 v[232:235], v123 offset:33792\n"
      "v_mul_f32 v180, s41, v180\n"
      "v_mfma_f32_16x16x32_bf16 v[236:239], a[232:235], v[140:143], v[236:239]\n"
      "ds_read_b128 v[204:207], v123 offset:19456\n"
      "v_mov_b32 v244, v180\n"
      "v_mfma_f32_16x16x32_bf16 v[184:187], a[224:227], v[148:151], 0\n"
      "ds_read_b128 v[136:139], v123 offset:25600\n"
      "s_cmp_lg_u32 s10, s11\n"
      "v_permlane32_swap_b32 v180, v244\n"
      "v_mov_b32_e64 v181, v180\n"
      "v_mfma_f32_16x16x32_bf16 v[184:187], a[236:239], v[140:143], v[184:187]\n"
      "ds_read_b128 v[148:151], v123 offset:34816\n"
      "v_permlane16_swap_b32 v180, v181\n"
      "v_mul_f32 v153, v180, v153\n"
      "v_mul_f32_e64 v152, v180, v152\n"
      "s_waitcnt lgkmcnt(6)\n"
      "v_mfma_f32_16x16x32_bf16 v[160:163], a[240:243], v[212:215], v[160:163]\n"
      "s_cbranch_scc1 L_edge_44_%=\n"
      "L_eb_45_%=:\n"
      "v_mfma_f32_16x16x32_bf16 v[160:163], a[252:255], v[220:223], v[160:163]\n"
      "buffer_store_dwordx4 v[192:195], v116, s[20:23], 0 offen offset:0 sc1\n"
      "v_mfma_f32_16x16x32_bf16 v[236:239], a[244:247], v[212:215], v[236:239]\n"
      "ds_read_b128 v[140:143], v123 offset:28672\n"
      "v_cvt_pk_bf16_f32 v152, v152, v153\n"
      "v_mfma_f32_16x16x32_bf16 v[236:239], v[0:3], v[220:223], v[236:239]\n"
      "s_waitcnt lgkmcnt(3)\n"
      "v_mul_f32 v155, v180, v155\n"
      "v_mul_f32 v154, v180, v154\n"
      "v_mfma_f32_16x16x32_bf16 v[184:187], a[248:251], v[212:215], v[184:187]\n"
      "ds_read_b128 v[192:195], v123 offset:31744\n"
      "v_mul_f32 v189, v180, v189\n"
      "v_mfma_f32_16x16x32_bf16 v[184:187], v[4:7], v[220:223], v[184:187]\n"
      "v_cvt_pk_bf16_f32 v153, v154, v155\n"
      "v_mul_f32 v190, v180, v190\n"
      "v_mfma_f32_16x16x32_bf16 v[160:163], v[8:11], v[224:227], v[160:163]\n"
      "v_mul_f32 v188, v180, v188\n"
      "v_mul_f32 v243, v180, v243\n"
      "v_mfma_f32_16x16x32_bf16 v[160:163], v[20:23], v[232:235], v[160:163]\n"
      "v_cvt_pk_bf16_f32 v154, v188, v189\n"
      "v_mul_f32 v191, v180, v191\n"
      "v_mfma_f32_16x16x32_bf16 v[236:239], v[12:15], v[224:227], v[236:239]\n"
      "v_cvt_pk_bf16_f32 v155, v190, v191\n"
      "v_mul_f32 v240, v180, v240\n"
      "v_mfma_f32_16x16x32_bf16 v[236:239], v[24:27], v[232:235], v[236:239]\n"
      "ds_write_b128 v126, v[152:155]\n"
      "v_mfma_f32_16x16x32_bf16 v[184:187], v[16:19], v[224:227], v[184:187]\n"
      "ds_read_b128 v[188:191], v123 offset:20480\n"
      "v_mul_f32 v241, v180, v241\n"
      "v_mfma_f32_16x16x32_bf16 v[184:187], v[28:31], v[232:235], v[184:187]\n"
      "buffer_store_dwordx4 v[156:159], v116, s[20:23], 0 offen offset:128 sc1\n"
      "v_mfma_f32_16x16x32_bf16 v[224:227], a[216:219], v[204:207], 0\n"
      "ds_read_b128 v[152:155], v123 offset:23552\n"
      "v_cvt_pk_bf16_f32 v240, v240, v241\n"
      "v_mfma_f32_16x16x32_bf16 v[224:227], a[228:231], v[176:179], v[224:227]\n"
      "s_waitcnt lgkmcnt(3)\n"
      "v_mul_f32 v242, v180, v242\n"
      "v_mfma_f32_16x16x32_bf16 v[232:235], a[220:223], v[204:207], 0\n"
      "v_cvt_pk_bf16_f32 v241, v242, v243\n"
      "v_mul_f32_e64 v248, v181, v248\n"
      "v_mfma_f32_16x16x32_bf16 v[232:235], a[232:235], v[176:179], v[232:235]\n"
      "ds_write_b64 v127, v[240:241]\n"
      "v_mfma_f32_16x16x32_bf16 v[220:223], a[224:227], v[204:207], 0\n"
      "ds_read_b128 v[212:215], v123 offset:26624\n"
      "v_mul_f32_e64 v249, v181, v249\n"
      "v_mfma_f32_16x16x32_bf16 v[220:223], a[236:239], v[176:179], v[220:223]\n"
      "ds_read_b128 v[176:179], v123 offset:29696\n"
      "v_cvt_pk_bf16_f32 v248, v248, v249\n"
      "v_mfma_f32_16x16x32_bf16 v[224:227], a[240:243], v[136:139], v[224:227]\n"
      "s_lshl_b32 s29, s24, 8\n"
      "v_mul_f32 v251, v181, v251\n"
      "v_mul_f32 v250, v181, v250\n"
      "v_mfma_f32_16x16x32_bf16 v[224:227], a[252:255], v[140:143], v[224:227]\n"
      "v_mul_f32 v144, v181, v144\n"
      "v_cvt_pk_bf16_f32 v249, v250, v251\n"
      "v_mfma_f32_16x16x32_bf16 v[232:235], a[244:247], v[136:139], v[232:235]\n"
      "s_add_u32 s29, s29, 159088\n"
      "v_mul_f32_e64 v145, v181, v145\n"
      "v_cvt_pk_bf16_f32 v250, v144, v145\n"
      "v_mfma_f32_16x16x32_bf16 v[232:235], v[0:3], v[140:143], v[232:235]\n"
      "v_mul_f32 v146, v181, v146\n"
      "v_mul_f32 v147, v181, v147\n"
      "v_mfma_f32_16x16x32_bf16 v[220:223], a[248:251], v[136:139], v[220:223]\n"
      "v_cvt_pk_bf16_f32 v251, v146, v147\n"
      "v_mul_f32_e64 v200, v181, v200\n"
      "v_mfma_f32_16x16x32_bf16 v[220:223], v[4:7], v[140:143], v[220:223]\n"
      "ds_write_b128 v126, v[248:251] offset:2048\n"
      "v_mfma_f32_16x16x32_bf16 v[224:227], v[8:11], v[192:195], v[224:227]\n"
      "ds_read_b128 v[156:159], v123 offset:32768\n"
      "v_mul_f32_e64 v201, v181, v201\n"
      "v_mfma_f32_16x16x32_bf16 v[224:227], v[20:23], v[148:151], v[224:227]\n"
      "ds_read_b128 v[144:147], v123 offset:35840\n"
      "v_cvt_pk_bf16_f32 v200, v200, v201\n"
      "v_mfma_f32_16x16x32_bf16 v[232:235], v[12:15], v[192:195], v[232:235]\n"
      "v_mul_f32_e64 v203, v181, v203\n"
      "v_mul_f32 v202, v181, v202\n"
      "v_mfma_f32_16x16x32_bf16 v[232:235], v[24:27], v[148:151], v[232:235]\n"
      "s_waitcnt lgkmcnt(3)\n"
      "v_cvt_pk_bf16_f32 v201, v202, v203\n"
      "v_fma_f32 v202, v160, v160, s39\n"
      "v_mfma_f32_16x16x32_bf16 v[220:223], v[16:19], v[192:195], v[220:223]\n"
      "buffer_store_dwordx4 v[164:167], v116, s[20:23], 0 offen offset:256 sc1\n"
      "v_mfma_f32_16x16x32_bf16 v[220:223], v[28:31], v[148:151], v[220:223]\n"
      "ds_write_b64 v127, v[200:201] offset:2048\n"
      "v_mfma_f32_16x16x32_bf16 v[248:251], a[216:219], v[188:191], 0\n"
      "ds_read_b128 v[192:195], v245\n"
      "v_fmac_f32_e64 v202, v161, v161\n"
      "v_mfma_f32_16x16x32_bf16 v[248:251], a[228:231], v[152:155], v[248:251]\n"
      "ds_read_b128 v[204:207], v245 offset:1408\n"
      "v_fmac_f32 v202, v162, v162\n"
      "v_mfma_f32_16x16x32_bf16 v[164:167], a[220:223], v[188:191], 0\n"
      "v_fmac_f32 v202, v163, v163\n"
      "v_fmac_f32_e64 v202, v236, v236\n"
      "v_mfma_f32_16x16x32_bf16 v[164:167], a[232:235], v[152:155], v[164:167]\n"
      "s_waitcnt lgkmcnt(3)\n"
      "v_fmac_f32 v202, v237, v237\n"
      "v_fmac_f32 v202, v238, v238\n"
      "v_mfma_f32_16x16x32_bf16 v[180:183], a[224:227], v[188:191], 0\n"
      "v_fmac_f32 v202, v239, v239\n"
      "v_fmac_f32 v202, v184, v184\n"
      "v_mfma_f32_16x16x32_bf16 v[180:183], a[236:239], v[152:155], v[180:183]\n"
      "v_fmac_f32 v202, v185, v185\n"
      "v_fmac_f32 v202, v186, v186\n"
      "v_mfma_f32_16x16x32_bf16 v[248:251], a[240:243], v[212:215], v[248:251]\n"
      "v_fmac_f32 v202, v187, v187\n"
      "v_mul_f32 v229, v244, v229\n"
      "v_mfma_f32_16x16x32_bf16 v[248:251], a[252:255], v[176:179], v[248:251]\n"
      "ds_read_b128 v[148:151], v245 offset:2816\n"
      "v_mul_f32 v228, v244, v228\n"
      "v_mfma_f32_16x16x32_bf16 v[164:167], a[244:247], v[212:215], v[164:167]\n"
      "ds_read_b128 v[208:211], v245 offset:4224\n"
      "v_mul_f32 v230, v244, v230\n"
      "v_mfma_f32_16x16x32_bf16 v[164:167], v[0:3], v[176:179], v[164:167]\n"
      "v_cvt_pk_bf16_f32 v228, v228, v229\n"
      "v_mul_f32 v231, v244, v231\n"
      "v_mfma_f32_16x16x32_bf16 v[180:183], a[248:251], v[212:215], v[180:183]\n"
      "buffer_store_dwordx4 v[216:219], v117, s[20:23], 0 offen offset:0 sc1\n"
      "v_mfma_f32_16x16x32_bf16 v[180:183], v[4:7], v[176:179], v[180:183]\n"
      "v_cvt_pk_bf16_f32 v229, v230, v231\n"
      "v_mul_f32 v168, v244, v168\n"
      "v_mfma_f32_16x16x32_bf16 v[248:251], v[8:11], v[156:159], v[248:251]\n"
      "v_mul_f32 v169, v244, v169\n"
      "v_cvt_pk_bf16_f32 v230, v168, v169\n"
      "v_mfma_f32_16x16x32_bf16 v[248:251], v[20:23], v[144:147], v[248:251]\n"
      "ds_read_b128 v[216:219], v245 offset:5632\n"
      "v_mul_f32 v171, v244, v171\n"
      "v_mfma_f32_16x16x32_bf16 v[164:167], v[12:15], v[156:159], v[164:167]\n"
      "v_mul_f32 v170, v244, v170\n"
      "v_cvt_pk_bf16_f32 v231, v170, v171\n"
      "v_mfma_f32_16x16x32_bf16 v[164:167], v[24:27], v[144:147], v[164:167]\n"
      "ds_write_b128 v126, v[228:231] offset:4096\n"
      "v_mfma_f32_16x16x32_bf16 v[180:183], v[16:19], v[156:159], v[180:183]\n"
      "ds_read_b128 v[240:243], v245 offset:7040\n"
      "v_mul_f32 v252, v244, v252\n"
      "v_mfma_f32_16x16x32_bf16 v[180:183], v[28:31], v[144:147], v[180:183]\n"
      "s_waitcnt lgkmcnt(3)\n"
      "v_mul_f32_e64 v253, v244, v253\n"
      "v_cvt_pk_bf16_f32 v252, v252, v253\n"
      "v_mfma_f32_16x16x32_bf16 v[152:155], a[216:219], v[192:195], 0\n"
      "v_mul_f32 v255, v244, v255\n"
      "v_mul_f32 v254, v244, v254\n"
      "v_mfma_f32_16x16x32_bf16 v[152:155], a[228:231], v[204:207], v[152:155]\n"
      "ds_read_b128 v[136:139], v245 offset:256\n"
      "v_cvt_pk_bf16_f32 v253, v254, v255\n"
      "v_mfma_f32_16x16x32_bf16 v[228:231], a[220:223], v[192:195], 0\n"
      "ds_read_b128 v[140:143], v245 offset:1664\n"
      "v_fma_f32 v200, v224, v224, s39\n"
      "v_mfma_f32_16x16x32_bf16 v[228:231], a[232:235], v[204:207], v[228:231]\n"
      "ds_write_b64 v127, v[252:253] offset:4096\n"
      "v_mfma_f32_16x16x32_bf16 v[212:215], a[224:227], v[192:195], 0\n"
      "v_fmac_f32 v200, v225, v225\n"
      "v_fmac_f32 v200, v226, v226\n"
      "v_mfma_f32_16x16x32_bf16 v[212:215], a[236:239], v[204:207], v[212:215]\n"
      "ds_read_b128 v[204:207], v245 offset:3072\n"
      "v_fmac_f32 v200, v227, v227\n"
      "v_mfma_f32_16x16x32_bf16 v[152:155], a[240:243], v[148:151], v[152:155]\n"
      "v_fmac_f32 v200, v232, v232\n"
      "v_fmac_f32 v200, v233, v233\n"
      "v_mfma_f32_16x16x32_bf16 v[152:155], a[252:255], v[208:211], v[152:155]\n"
      "ds_read_b128 v[192:195], v245 offset:4480\n"
      "v_fmac_f32 v200, v234, v234\n"
      "v_mfma_f32_16x16x32_bf16 v[228:231], a[244:247], v[148:151], v[228:231]\n"
      "v_fmac_f32 v200, v235, v235\n"
      "v_fmac_f32 v200, v220, v220\n"
      "v_mfma_f32_16x16x32_bf16 v[228:231], v[0:3], v[208:211], v[228:231]\n"
      "v_fmac_f32 v200, v221, v221\n"
      "v_fmac_f32 v200, v222, v222\n"
      "v_mfma_f32_16x16x32_bf16 v[212:215], a[248:251], v[148:151], v[212:215]\n"
      "s_waitcnt lgkmcnt(3)\n"
      "v_lshl_add_u32 v203, v246, 2, s29\n"
      "v_fmac_f32 v200, v223, v223\n"
      "v_mfma_f32_16x16x32_bf16 v[212:215], v[4:7], v[208:211], v[212:215]\n"
      "ds_read_b32 v203, v203\n"
      "v_lshrrev_b32_e64 v247, 3, v246\n"
      "v_mfma_f32_16x16x32_bf16 v[152:155], v[8:11], v[216:219], v[152:155]\n"
      "ds_read_b128 v[208:211], v245 offset:5888\n"
      "v_fma_f32 v244, v248, v248, s39\n"
      "v_mfma_f32_16x16x32_bf16 v[152:155], v[20:23], v[240:243], v[152:155]\n"
      "ds_read_b128 v[148:151], v245 offset:7296\n"
      "v_fmac_f32 v244, v249, v249\n"
      "v_mfma_f32_16x16x32_bf16 v[228:231], v[12:15], v[216:219], v[228:231]\n"
      "s_lshl_b32 s29, s24, 3\n"
      "v_add_u32 v247, s29, v247\n"
      "v_cmp_gt_u32 s[48:49], s26, v247\n"
      "v_mfma_f32_16x16x32_bf16 v[228:231], v[24:27], v[240:243], v[228:231]\n"
      "v_fmac_f32 v244, v250, v250\n"
      "v_fmac_f32_e64 v244, v251, v251\n"
      "v_mfma_f32_16x16x32_bf16 v[212:215], v[16:19], v[216:219], v[212:215]\n"
      "s_waitcnt lgkmcnt(3)\n"
      "v_fmac_f32 v244, v164, v164\n"
      "v_fmac_f32 v244, v165, v165\n"
      "v_mfma_f32_16x16x32_bf16 v[212:215], v[28:31], v[240:243], v[212:215]\n"
      "s_mov_b32 exec_hi, 0\n"
      "buffer_store_dwordx4 v[172:175], v118, s[20:23], 0 offen offset:0 sc1\n"
      "s_mov_b32 exec_hi, -1\n"
      "v_mfma_f32_16x16x32_bf16 v[168:171], a[216:219], v[136:139], 0\n"
      "v_fmac_f32 v244, v166, v166\n"
      "v_fmac_f32_e64 v244, v167, v167\n"
      "v_mfma_f32_16x16x32_bf16 v[168:171], a[228:231], v[140:143], v[168:171]\n"
      "v_fmac_f32 v244, v180, v180\n"
      "v_fmac_f32 v244, v181, v181\n"
      "v_mfma_f32_16x16x32_bf16 v[216:219], a[220:223], v[136:139], 0\n"
      "v_fmac_f32 v244, v182, v182\n"
      "v_fmac_f32 v244, v183, v183\n"
      "v_mfma_f32_16x16x32_bf16 v[216:219], a[232:235], v[140:143], v[216:219]\n"
      "v_permlane16_swap_b32 v202, v200\n"
      "v_mfma_f32_16x16x32_bf16 v[252:255], a[224:227], v[136:139], 0\n"
      "s_waitcnt lgkmcnt(0)\n"
      "v_mov_b32_e64 v201, v244\n"
      "v_mfma_f32_16x16x32_bf16 v[252:255], a[236:239], v[140:143], v[252:255]\n"
      "s_nop 0\n"
      "v_permlane16_swap_b32 v244, v201\n"
      "v_mfma_f32_16x16x32_bf16 v[168:171], a[240:243], v[204:207], v[168:171]\n"
      "v_add_f32 v202, v202, v200\n"
      "v_add_f32 v244, v244, v201\n"
      "v_mfma_f32_16x16x32_bf16 v[168:171], a[252:255], v[192:195], v[168:171]\n"
      "s_nop 0\n"
      "v_permlane32_swap_b32 v202, v244\n"
      "v_mfma_f32_16x16x32_bf16 v[216:219], a[244:247], v[204:207], v[216:219]\n"
      "v_fma_f32 v201, v152, v152, s39\n"
      "v_add_f32 v202, v202, v244\n"
      "v_mfma_f32_16x16x32_bf16 v[216:219], v[0:3], v[192:195], v[216:219]\n"
      "v_fmac_f32 v201, v153, v153\n"
      "v_mfma_f32_16x16x32_bf16 v[216:219], v[12:15], v[208:211], v[216:219]\n"
      "v_fmac_f32_e64 v201, v154, v154\n"
      "v_mfma_f32_16x16x32_bf16 v[216:219], v[24:27], v[148:151], v[216:219]\n"
      "ds_write_b32 v129, v202\n"
      "v_mfma_f32_16x16x32_bf16 v[252:255], a[248:251], v[204:207], v[252:255]\n"
      "v_fmac_f32 v201, v155, v155\n"
      "v_fmac_f32 v201, v228, v228\n"
      "v_mfma_f32_16x16x32_bf16 v[252:255], v[4:7], v[192:195], v[252:255]\n"
      "v_fmac_f32 v201, v229, v229\n"
      "v_mfma_f32_16x16x32_bf16 v[168:171], v[8:11], v[208:211], v[168:171]\n"
      "v_fmac_f32 v201, v230, v230\n"
      "v_fmac_f32_e64 v201, v231, v231\n"
      "v_mfma_f32_16x16x32_bf16 v[168:171], v[20:23], v[148:151], v[168:171]\n"
      "v_mfma_f32_16x16x32_bf16 v[252:255], v[16:19], v[208:211], v[252:255]\n"
      "v_fmac_f32 v201, v212, v212\n"
      "v_fmac_f32 v201, v213, v213\n"
      "v_mfma_f32_16x16x32_bf16 v[252:255], v[28:31], v[148:151], v[252:255]\n"
      "v_fmac_f32 v201, v214, v214\n"
      "v_fmac_f32_e64 v201, v215, v215\n"
      "s_nop 1\n"
      "v_pk_mul_f32 v[244:245], v[168:169], v[168:169]\n"
      "v_pk_fma_f32 v[244:245], v[170:171], v[170:171], v[244:245]\n"
      "v_pk_fma_f32 v[244:245], v[216:217], v[216:217], v[244:245]\n"
      "v_pk_fma_f32 v[244:245], v[218:219], v[218:219], v[244:245]\n"
      "v_pk_fma_f32 v[244:245], v[252:253], v[252:253], v[244:245]\n"
      "v_pk_fma_f32 v[244:245], v[254:255], v[254:255], v[244:245]\n"
      "v_add_f32 v244, s39, v244\n"
      "v_add_f32 v207, v244, v245\n"
      "v_mov_b32 v202, v207\n"
      "v_mov_b32 v200, v202\n"
      "v_permlane16_swap_b32 v201, v207\n"
      "v_add_f32 v201, v201, v207\n"
      "v_permlane16_swap_b32 v202, v200\n"
      "v_add_f32 v202, v202, v200\n"
      "s_nop 1\n"
      "v_permlane32_swap_b32_e64 v201, v202\n"
      "v_add_f32_e64 v201, v201, v202\n"
      "ds_write_b32 v129, v201 offset:2048\n"
      "s_waitcnt lgkmcnt(0)\n"
      "s_barrier\n"
      "ds_read_b128 v[140:143], v128\n"
      "ds_read_b128 v[240:243], v119 offset:6144\n"
      "s_add_u32 s27, s27, 1\n"
      "ds_read_b128 v[136:139], v128 offset:2048\n"
      "s_sub_u32 s28, s27, 3\n"
      "s_cselect_b32 s22, 0, -1\n"
      "s_max_i32 m0, s28, 0\n"
      "ds_read_b128 v[172:175], v119\n"
      "s_movrels_b32 s9, s80\n"
      "ds_read_b128 v[208:211], v120\n"
      "s_lshr_b32 s8, s9, 8\n"
      "s_and_b32 s9, s9, 0xff\n"
      "s_mul_i32 s28, s8, 1373568\n"
      "s_mul_i32 s29, s9, 2688\n"
      "s_add_u32 s28, s28, s29\n"
      "s_cmp_eq_u32 s8, 72\n"
      "s_cselect_b32 s8, 0, 100270464\n"
      "s_cmp_eq_u32 s9, 72\n"
      "ds_read_b128 v[192:195], v121\n"
      "s_cselect_b32 s9, 0, 196224\n"
      "s_add_u32 s10, s8, s9\n"
      "s_add_u32 s28, s28, s10\n"
      "s_add_u32 s20, s12, s28\n"
      "s_addc_u32 s21, s13, 0\n"
      "ds_read_b128 v[148:151], v119 offset:12288\n"
      "s_waitcnt lgkmcnt(6)\n"
      "v_add_f32 v140, |v140|, |v141|\n"
      "v_add_f32 v142, |v142|, |v143|\n"
      "v_add_f32 v140, v140, v142\n"
      "v_rsq_f32 v140, v140\n"
      "s_waitcnt lgkmcnt(4)\n"
      "v_mul_f32 v140, s41, v140\n"
      "v_add_f32 v136, |v136|, |v137|\n"
      "v_mov_b32 v143, v140\n"
      "v_add_f32 v138, |v138|, |v139|\n"
      "v_add_f32 v136, v136, v138\n"
      "v_permlane32_swap_b32 v140, v143\n"
      "v_mov_b32 v141, v140\n"
      "v_rsq_f32 v136, v136\n"
      "s_cmp_lg_u32 s10, s11\n"
      "v_mul_f32 v136, s41, v136\n"
      "v_permlane16_swap_b32 v140, v141\n"
      "v_mov_b32 v139, v136\n"
      "v_pk_mul_f32 v[162:163], v[162:163], v[140:141] op_sel_hi:[1,0]\n"
      "v_pk_mul_f32 v[184:185], v[184:185], v[140:141] op_sel_hi:[1,0]\n"
      "v_permlane32_swap_b32 v136, v139\n"
      "v_cvt_pk_bf16_f32 v184, v184, v185\n"
      "v_subrev_u32 v137, 73728, v126\n"
      "v_mov_b32 v139, v136\n"
      "v_pk_mul_f32 v[164:165], v[164:165], v[142:143] op_sel:[0,1]\n"
      "v_pk_mul_f32 v[236:237], v[236:237], v[140:141] op_sel_hi:[1,0]\n"
      "v_pk_mul_f32 v[238:239], v[238:239], v[140:141] op_sel_hi:[1,0]\n"
      "v_pk_mul_f32 v[232:233], v[232:233], v[140:141] op_sel:[0,1]\n"
      "v_pk_mul_f32 v[220:221], v[220:221], v[140:141] op_sel:[0,1]\n"
      "v_permlane16_swap_b32_e64 v136, v139\n"
      "v_pk_mul_f32 v[160:161], v[160:161], v[140:141] op_sel_hi:[1,0]\n"
      "v_pk_mul_f32 v[166:167], v[166:167], v[142:143] op_sel:[0,1]\n"
      "v_cvt_pk_bf16_f32 v160, v160, v161\n"
      "v_pk_mul_f32 v[182:183], v[182:183], v[142:143] op_sel:[0,1]\n"
      "v_pk_mul_f32 v[154:155], v[154:155], v[136:137] op_sel_hi:[1,0]\n"
      "v_pk_mul_f32 v[248:249], v[248:249], v[142:143] op_sel:[0,1]\n"
      "v_cvt_pk_bf16_f32 v161, v162, v163\n"
      "v_pk_mul_f32 v[228:229], v[228:229], v[136:137] op_sel_hi:[1,0]\n"
      "s_cbranch_scc1 L_edge_46_%=\n"
      "L_eb_47_%=:\n"
      "v_cvt_pk_bf16_f32 v162, v236, v237\n"
      "v_subrev_u32 v201, 73728, v127\n"
      "v_pk_mul_f32 v[212:213], v[212:213], v[136:137] op_sel_hi:[1,0]\n"
      "v_cvt_pk_bf16_f32 v248, v248, v249\n"
      "v_pk_mul_f32 v[186:187], v[186:187], v[140:141] op_sel_hi:[1,0]\n"
      "v_cvt_pk_bf16_f32 v163, v238, v239\n"
      "v_pk_mul_f32 v[250:251], v[250:251], v[142:143] op_sel:[0,1]\n"
      "v_pk_mul_f32 v[214:215], v[214:215], v[136:137] op_sel_hi:[1,0]\n"
      "v_cvt_pk_bf16_f32 v185, v186, v187\n"
      "s_waitcnt lgkmcnt(0)\n"
      "ds_write_b64 v127, v[184:185] offset:18432\n"
      "ds_write_b128 v126, v[160:163] offset:18432\n"
      "buffer_store_dwordx4 v[172:175], v116, s[20:23], 0 offen offset:0 sc1\n"
      "v_pk_mul_f32 v[168:169], v[168:169], v[138:139] op_sel:[0,1]\n"
      "v_pk_mul_f32 v[252:253], v[252:253], v[138:139] op_sel:[0,1]\n"
      "v_cvt_pk_bf16_f32 v168, v168, v169\n"
      "v_pk_mul_f32 v[152:153], v[152:153], v[136:137] op_sel_hi:[1,0]\n"
      "v_cvt_pk_bf16_f32 v212, v212, v213\n"
      "v_pk_mul_f32 v[254:255], v[254:255], v[138:139] op_sel:[0,1]\n"
      "v_pk_mul_f32 v[170:171], v[170:171], v[138:139] op_sel:[0,1]\n"
      "v_pk_mul_f32 v[224:225], v[224:225], v[140:141] op_sel:[0,1]\n"
      "v_cvt_pk_bf16_f32 v249, v250, v251\n"
      "v_pk_mul_f32 v[230:231], v[230:231], v[136:137] op_sel_hi:[1,0]\n"
      "v_pk_mul_f32 v[226:227], v[226:227], v[140:141] op_sel:[0,1]\n"
      "v_cvt_pk_bf16_f32 v250, v164, v165\n"
      "v_cvt_pk_bf16_f32 v224, v224, v225\n"
      "v_pk_mul_f32 v[234:235], v[234:235], v[140:141] op_sel:[0,1]\n"
      "v_cvt_pk_bf16_f32 v220, v220, v221\n"
      "v_pk_mul_f32 v[180:181], v[180:181], v[142:143] op_sel:[0,1]\n"
      "v_cvt_pk_bf16_f32 v180, v180, v181\n"
      "v_cvt_pk_bf16_f32 v225, v226, v227\n"
      "v_pk_mul_f32 v[222:223], v[222:223], v[140:141] op_sel:[0,1]\n"
      "v_cvt_pk_bf16_f32 v226, v232, v233\n"
      "v_cvt_pk_bf16_f32 v152, v152, v153\n"
      "v_cvt_pk_bf16_f32 v181, v182, v183\n"
      "v_cvt_pk_bf16_f32 v213, v214, v215\n"
      "v_pk_mul_f32 v[216:217], v[216:217], v[138:139] op_sel:[0,1]\n"
      "v_cvt_pk_bf16_f32 v221, v222, v223\n"
      "v_cvt_pk_bf16_f32 v251, v166, v167\n"
      "v_cvt_pk_bf16_f32 v153, v154, v155\n"
      "buffer_store_dwordx4 v[240:243], v116, s[20:23], 0 offen offset:128 sc1\n"
      "ds_write_b128 v126, v[248:251] offset:22528\n"
      "ds_write_b64 v127, v[180:181] offset:22528\n"
      "v_cvt_pk_bf16_f32 v154, v228, v229\n"
      "v_cvt_pk_bf16_f32 v227, v234, v235\n"
      "v_cvt_pk_bf16_f32 v169, v170, v171\n"
      "ds_write_b64 v201, v[212:213] offset:18432\n"
      "ds_write_b128 v126, v[224:227] offset:20480\n"
      "buffer_store_dwordx4 v[148:151], v116, s[20:23], 0 offen offset:256 sc1\n"
      "buffer_store_dwordx4 v[208:211], v117, s[20:23], 0 offen offset:0 sc1\n"
      "v_pk_mul_f32 v[218:219], v[218:219], v[138:139] op_sel:[0,1]\n"
      "v_cvt_pk_bf16_f32 v155, v230, v231\n"
      "v_cvt_pk_bf16_f32 v252, v252, v253\n"
      "v_cvt_pk_bf16_f32 v171, v218, v219\n"
      "v_cvt_pk_bf16_f32 v170, v216, v217\n"
      "ds_write_b64 v127, v[220:221] offset:20480\n"
      "ds_write_b128 v137, v[152:155] offset:18432\n"
      "ds_write_b128 v137, v[168:171] offset:20480\n"
      "v_cvt_pk_bf16_f32 v253, v254, v255\n"
      "s_mov_b32 exec_hi, 0\n"
      "buffer_store_dwordx4 v[192:195], v118, s[20:23], 0 offen offset:0 sc1\n"
      "s_mov_b32 exec_hi, -1\n"
      "ds_write_b64 v201, v[252:253] offset:20480\n"
      "s_waitcnt lgkmcnt(0)\n"
      "s_barrier\n"
      "ds_read_b128 v[240:243], v119 offset:24576\n"
      "ds_read_b128 v[232:235], v121 offset:18432\n"
      "ds_read_b128 v[216:219], v119 offset:30720\n"
      "ds_read_b128 v[220:223], v119 offset:18432\n"
      "v_subrev_u32 v202, 73728, v119\n"
      "s_add_u32 s27, s27, 1\n"
      "ds_read_b128 v[136:139], v202 offset:24576\n"
      "s_sub_u32 s28, s27, 3\n"
      "s_cselect_b32 s22, 0, -1\n"
      "s_max_i32 m0, s28, 0\n"
      "ds_read_b128 v[192:195], v202 offset:30720\n"
      "s_movrels_b32 s9, s80\n"
      "s_lshr_b32 s8, s9, 8\n"
      "s_and_b32 s9, s9, 0xff\n"
      "s_mul_i32 s28, s8, 1373568\n"
      "s_mul_i32 s29, s9, 2688\n"
      "s_add_u32 s28, s28, s29\n"
      "s_cmp_eq_u32 s8, 72\n"
      "s_cselect_b32 s8, 0, 100270464\n"
      "ds_read_b128 v[152:155], v120 offset:18432\n"
      "ds_read_b128 v[204:207], v202 offset:18432\n"
      "s_cmp_eq_u32 s9, 72\n"
      "s_cselect_b32 s9, 0, 196224\n"
      "s_add_u32 s10, s8, s9\n"
      "s_add_u32 s28, s28, s10\n"
      "s_add_u32 s20, s12, s28\n"
      "s_addc_u32 s21, s13, 0\n"
      "s_cmp_lg_u32 s10, s11\n"
      "s_cbranch_scc1 L_edge_48_%=\n"
      "L_eb_49_%=:\n"
      "s_waitcnt lgkmcnt(4)\n"
      "buffer_store_dwordx4 v[220:223], v116, s[20:23], 0 offen offset:0 sc1\n"
      "buffer_store_dwordx4 v[240:243], v116, s[20:23], 0 offen offset:128 sc1\n"
      "buffer_store_dwordx4 v[216:219], v116, s[20:23], 0 offen offset:256 sc1\n"
      "s_waitcnt lgkmcnt(0)\n"
      "buffer_store_dwordx4 v[152:155], v117, s[20:23], 0 offen offset:0 sc1\n"
      "s_mov_b32 exec_hi, 0\n"
      "buffer_store_dwordx4 v[232:235], v118, s[20:23], 0 offen offset:0 sc1\n"
      "s_mov_b32 exec_hi, -1\n"
      "s_nop 0\n"
      "s_mov_b64 exec, s[48:49]\n"
      "s_cbranch_execz L_np_50_%=\n"
      "global_store_dwordx4 v203, v[204:207], s[12:13] sc1\n"
      "global_store_dwordx4 v203, v[136:139], s[12:13] offset:128 sc1\n"
      "global_store_dwordx4 v203, v[192:195], s[12:13] offset:256 sc1\n"
      "L_np_50_%=:\n"
      "s_mov_b64 exec, -1\n"
      "s_endpgm\n"
      "L_gen_8_%=:\n"
      "s_waitcnt lgkmcnt(0)\n"
      "s_nop 5\n"
      "s_cmp_eq_u64 s[34:35], 0\n"
      "s_cbranch_scc1 L_ok_65_%=\n"
      "ds_read_b128 v[164:167], v115\n"
      "ds_read_b128 v[140:143], v114\n"
      "ds_read_b128 v[136:139], v114 offset:12288\n"
      "ds_read_b128 v[148:151], v114 offset:6144\n"
      "ds_read_b128 v[152:155], v115 offset:12288\n"
      "ds_read_b128 v[216:219], v115 offset:6144\n"
      "s_waitcnt lgkmcnt(4)\n"
      "v_mfma_f32_16x16x32_bf16 v[180:183], a[0:3], v[140:143], 0\n"
      "v_mfma_f32_16x16x32_bf16 v[180:183], a[36:39], v[164:167], v[180:183]\n"
      "v_mfma_f32_16x16x32_bf16 v[208:211], a[4:7], v[140:143], 0\n"
      "s_nop 0\n"
      "v_mfma_f32_16x16x32_bf16 v[208:211], a[40:43], v[164:167], v[208:211]\n"
      "v_mfma_f32_16x16x32_bf16 v[172:175], a[8:11], v[140:143], 0\n"
      "v_mfma_f32_16x16x32_bf16 v[172:175], a[44:47], v[164:167], v[172:175]\n"
      "v_mfma_f32_16x16x32_bf16 v[156:159], a[12:15], v[140:143], 0\n"
      "v_mfma_f32_16x16x32_bf16 v[156:159], a[48:51], v[164:167], v[156:159]\n"
      "v_mfma_f32_16x16x32_bf16 v[196:199], a[16:19], v[140:143], 0\n"
      "s_waitcnt lgkmcnt(0)\n"
      "v_mfma_f32_16x16x32_bf16 v[196:199], a[52:55], v[164:167], v[196:199]\n"
      "s_nop 0\n"
      "v_mfma_f32_16x16x32_bf16 v[192:195], a[20:23], v[140:143], 0\n"
      "v_mfma_f32_16x16x32_bf16 v[192:195], a[56:59], v[164:167], v[192:195]\n"
      "v_mfma_f32_16x16x32_bf16 v[180:183], a[72:75], v[148:151], v[180:183]\n"
      "v_mfma_f32_16x16x32_bf16 v[180:183], a[108:111], v[216:219], v[180:183]\n"
      "v_mfma_f32_16x16x32_bf16 v[172:175], a[80:83], v[148:151], v[172:175]\n"
      "v_mfma_f32_16x16x32_bf16 v[172:175], a[116:119], v[216:219], v[172:175]\n"
      "v_mfma_f32_16x16x32_bf16 v[208:211], a[76:79], v[148:151], v[208:211]\n"
      "v_mfma_f32_16x16x32_bf16 v[208:211], a[112:115], v[216:219], v[208:211]\n"
      "v_mfma_f32_16x16x32_bf16 v[196:199], a[88:91], v[148:151], v[196:199]\n"
      "v_mfma_f32_16x16x32_bf16 v[196:199], a[124:127], v[216:219], v[196:199]\n"
      "v_mfma_f32_16x16x32_bf16 v[156:159], a[84:87], v[148:151], v[156:159]\n"
      "v_mfma_f32_16x16x32_bf16 v[156:159], a[120:123], v[216:219], v[156:159]\n"
      "v_mfma_f32_16x16x32_bf16 v[180:183], a[144:147], v[136:139], v[180:183]\n"
      "v_mfma_f32_16x16x32_bf16 v[180:183], a[180:183], v[152:155], v[180:183]\n"
      "v_mfma_f32_16x16x32_bf16 v[192:195], a[92:95], v[148:151], v[192:195]\n"
      "v_mfma_f32_16x16x32_bf16 v[192:195], a[128:131], v[216:219], v[192:195]\n"
      "ds_read_b128 v[148:151], v115 offset:2048\n"
      "v_mfma_f32_16x16x32_bf16 v[172:175], a[152:155], v[136:139], v[172:175]\n"
      "v_mfma_f32_16x16x32_bf16 v[208:211], a[148:151], v[136:139], v[208:211]\n"
      "v_mfma_f32_16x16x32_bf16 v[208:211], a[184:187], v[152:155], v[208:211]\n"
      "v_mfma_f32_16x16x32_bf16 v[172:175], a[188:191], v[152:155], v[172:175]\n"
      "s_nop 0\n"
      "v_fma_f32 v206, v180, v180, s38\n"
      "v_mfma_f32_16x16x32_bf16 v[156:159], a[156:159], v[136:139], v[156:159]\n"
      "v_fmac_f32 v206, v181, v181\n"
      "v_fmac_f32 v206, v182, v182\n"
      "v_mfma_f32_16x16x32_bf16 v[156:159], a[192:195], v[152:155], v[156:159]\n"
      "v_fmac_f32 v206, v183, v183\n"
      "v_fmac_f32 v206, v208, v208\n"
      "v_mfma_f32_16x16x32_bf16 v[196:199], a[160:163], v[136:139], v[196:199]\n"
      "v_fmac_f32 v206, v209, v209\n"
      "v_fmac_f32 v206, v210, v210\n"
      "v_mfma_f32_16x16x32_bf16 v[196:199], a[196:199], v[152:155], v[196:199]\n"
      "v_fmac_f32 v206, v211, v211\n"
      "v_fmac_f32_e64 v206, v172, v172\n"
      "v_mfma_f32_16x16x32_bf16 v[192:195], a[164:167], v[136:139], v[192:195]\n"
      "ds_read_b128 v[164:167], v114 offset:2048\n"
      "v_fma_f32 v207, v156, v156, s38\n"
      "v_mfma_f32_16x16x32_bf16 v[192:195], a[200:203], v[152:155], v[192:195]\n"
      "v_fmac_f32 v207, v157, v157\n"
      "v_fmac_f32 v207, v158, v158\n"
      "v_fmac_f32 v207, v159, v159\n"
      "v_fmac_f32 v207, v196, v196\n"
      "v_fmac_f32 v206, v173, v173\n"
      "v_fmac_f32 v207, v197, v197\n"
      "v_fmac_f32 v207, v198, v198\n"
      "v_fmac_f32 v207, v199, v199\n"
      "v_fmac_f32 v207, v192, v192\n"
      "v_fmac_f32 v207, v193, v193\n"
      "v_fmac_f32 v206, v174, v174\n"
      "v_fmac_f32 v206, v175, v175\n"
      "v_fmac_f32 v207, v194, v194\n"
      "v_fmac_f32 v207, v195, v195\n"
      "s_nop 1\n"
      "v_permlane16_swap_b32 v206, v207\n"
      "v_add_f32 v206, v206, v207\n"
      "v_mov_b32 v207, v206\n"
      "s_nop 1\n"
      "v_permlane32_swap_b32 v206, v207\n"
      "v_add_f32 v206, v206, v207\n"
      "v_rsq_f32 v206, v206\n"
      "s_waitcnt lgkmcnt(0)\n"
      "v_mov_b32 v207, v206\n"
      "v_mfma_f32_16x16x32_bf16 v[176:179], a[0:3], v[164:167], 0\n"
      "ds_read_b128 v[140:143], v114 offset:8192\n"
      "v_permlane16_swap_b32 v206, v207\n"
      "v_mfma_f32_16x16x32_bf16 v[176:179], a[36:39], v[148:151], v[176:179]\n"
      "ds_read_b128 v[136:139], v115 offset:8192\n"
      "v_mul_f32 v156, v207, v156\n"
      "v_mul_f32_e64 v157, v207, v157\n"
      "v_mfma_f32_16x16x32_bf16 v[228:231], a[12:15], v[164:167], 0\n"
      "ds_read_b128 v[216:219], v114 offset:14336\n"
      "v_cvt_pk_f16_f32 v156, v156, v157\n"
      "v_mfma_f32_16x16x32_bf16 v[228:231], a[48:51], v[148:151], v[228:231]\n"
      "ds_read_b128 v[152:155], v115 offset:14336\n"
      "v_mul_f32 v158, v207, v158\n"
      "v_mfma_f32_16x16x32_bf16 v[212:215], a[20:23], v[164:167], 0\n"
      "v_mul_f32 v196, v207, v196\n"
      "v_mul_f32 v159, v207, v159\n"
      "v_mfma_f32_16x16x32_bf16 v[212:215], a[56:59], v[148:151], v[212:215]\n"
      "v_cvt_pk_f16_f32 v157, v158, v159\n"
      "v_mul_f32 v197, v207, v197\n"
      "v_mul_f32 v198, v207, v198\n"
      "v_mul_f32 v199, v207, v199\n"
      "v_cvt_pk_f16_f32 v159, v198, v199\n"
      "v_cvt_pk_f16_f32 v158, v196, v197\n"
      "v_mul_f32 v195, v207, v195\n"
      "v_mfma_f32_16x16x32_bf16 v[196:199], a[16:19], v[164:167], 0\n"
      "v_mul_f32 v192, v207, v192\n"
      "v_mul_f32 v193, v207, v193\n"
      "v_mfma_f32_16x16x32_bf16 v[196:199], a[52:55], v[148:151], v[196:199]\n"
      "v_cvt_pk_f16_f32 v34, v192, v193\n"
      "v_mul_f32_e64 v194, v207, v194\n"
      "s_waitcnt lgkmcnt(2)\n"
      "v_mfma_f32_16x16x32_bf16 v[176:179], a[72:75], v[140:143], v[176:179]\n"
      "v_mul_f32 v206, s36, v206\n"
      "v_mul_f32 v180, v206, v180\n"
      "v_mfma_f32_16x16x32_bf16 v[176:179], a[108:111], v[136:139], v[176:179]\n"
      "v_mul_f32_e64 v181, v206, v181\n"
      "v_cvt_pk_f16_f32 v35, v194, v195\n"
      "v_mul_f32 v183, v206, v183\n"
      "v_mfma_f32_16x16x32_bf16 v[228:231], a[84:87], v[140:143], v[228:231]\n"
      "s_waitcnt lgkmcnt(0)\n"
      "v_cvt_pk_f16_f32 v180, v180, v181\n"
      "v_mul_f32 v182, v206, v182\n"
      "v_mfma_f32_16x16x32_bf16 v[228:231], a[120:123], v[136:139], v[228:231]\n"
      "v_mul_f32 v172, v206, v172\n"
      "v_mul_f32_e64 v208, v206, v208\n"
      "v_mfma_f32_16x16x32_bf16 v[196:199], a[88:91], v[140:143], v[196:199]\n"
      "v_cvt_pk_f16_f32 v181, v182, v183\n"
      "v_mul_f32 v209, v206, v209\n"
      "v_mfma_f32_16x16x32_bf16 v[196:199], a[124:127], v[136:139], v[196:199]\n"
      "v_mul_f32 v211, v206, v211\n"
      "v_cvt_pk_f16_f32 v182, v208, v209\n"
      "v_mfma_f32_16x16x32_bf16 v[212:215], a[92:95], v[140:143], v[212:215]\n"
      "v_mul_f32 v210, v206, v210\n"
      "v_cvt_pk_f16_f32 v183, v210, v211\n"
      "v_mfma_f32_16x16x32_bf16 v[212:215], a[128:131], v[136:139], v[212:215]\n"
      "v_mul_f32 v173, v206, v173\n"
      "v_mfma_f32_16x16x32_bf16 v[208:211], a[4:7], v[164:167], 0\n"
      "v_mfma_f32_16x16x32_bf16 v[208:211], a[40:43], v[148:151], v[208:211]\n"
      "v_mul_f32 v174, v206, v174\n"
      "v_mul_f32 v175, v206, v175\n"
      "v_mfma_f32_16x16x32_bf16 v[208:211], a[76:79], v[140:143], v[208:211]\n"
      "v_mfma_f32_16x16x32_bf16 v[208:211], a[112:115], v[136:139], v[208:211]\n"
      "v_cvt_pk_f16_f32 v33, v174, v175\n"
      "v_cvt_pk_f16_f32 v32, v172, v173\n"
      "v_mfma_f32_16x16x32_bf16 v[172:175], a[8:11], v[164:167], 0\n"
      "v_mfma_f32_16x16x32_bf16 v[172:175], a[44:47], v[148:151], v[172:175]\n"
      "v_mfma_f32_16x16x32_bf16 v[172:175], a[80:83], v[140:143], v[172:175]\n"
      "v_mfma_f32_16x16x32_bf16 v[172:175], a[116:119], v[136:139], v[172:175]\n"
      "ds_read_b128 v[164:167], v115 offset:4096\n"
      "v_mfma_f32_16x16x32_bf16 v[176:179], a[144:147], v[216:219], v[176:179]\n"
      "v_mfma_f32_16x16x32_bf16 v[176:179], a[180:183], v[152:155], v[176:179]\n"
      "v_mfma_f32_16x16x32_bf16 v[208:211], a[148:151], v[216:219], v[208:211]\n"
      "v_mfma_f32_16x16x32_bf16 v[208:211], a[184:187], v[152:155], v[208:211]\n"
      "v_mfma_f32_16x16x32_bf16 v[172:175], a[152:155], v[216:219], v[172:175]\n"
      "v_mfma_f32_16x16x32_bf16 v[172:175], a[188:191], v[152:155], v[172:175]\n"
      "v_mfma_f32_16x16x32_bf16 v[228:231], a[156:159], v[216:219], v[228:231]\n"
      "v_mfma_f32_16x16x32_bf16 v[228:231], a[192:195], v[152:155], v[228:231]\n"
      "s_nop 1\n"
      "v_fma_f32 v206, v176, v176, s38\n"
      "v_mfma_f32_16x16x32_bf16 v[196:199], a[160:163], v[216:219], v[196:199]\n"
      "v_fmac_f32 v206, v177, v177\n"
      "v_fmac_f32_e64 v206, v178, v178\n"
      "v_mfma_f32_16x16x32_bf16 v[196:199], a[196:199], v[152:155], v[196:199]\n"
      "v_fmac_f32 v206, v179, v179\n"
      "v_fma_f32 v207, v228, v228, s38\n"
      "v_fmac_f32 v207, v229, v229\n"
      "v_mfma_f32_16x16x32_bf16 v[212:215], a[164:167], v[216:219], v[212:215]\n"
      "ds_read_b128 v[140:143], v114 offset:4096\n"
      "v_fmac_f32 v207, v230, v230\n"
      "v_mfma_f32_16x16x32_bf16 v[212:215], a[200:203], v[152:155], v[212:215]\n"
      "v_fmac_f32 v207, v231, v231\n"
      "v_fmac_f32 v206, v208, v208\n"
      "v_fmac_f32 v206, v209, v209\n"
      "v_fmac_f32 v207, v196, v196\n"
      "v_fmac_f32 v206, v210, v210\n"
      "v_fmac_f32 v206, v211, v211\n"
      "v_fmac_f32 v207, v197, v197\n"
      "v_fmac_f32 v207, v198, v198\n"
      "v_fmac_f32 v206, v172, v172\n"
      "v_fmac_f32 v207, v199, v199\n"
      "v_fmac_f32 v206, v173, v173\n"
      "v_fmac_f32 v206, v174, v174\n"
      "v_fmac_f32 v207, v212, v212\n"
      "v_fmac_f32 v207, v213, v213\n"
      "v_fmac_f32 v207, v214, v214\n"
      "v_fmac_f32 v206, v175, v175\n"
      "v_fmac_f32 v207, v215, v215\n"
      "s_nop 1\n"
      "v_permlane16_swap_b32 v206, v207\n"
      "v_add_f32 v206, v206, v207\n"
      "v_mov_b32 v207, v206\n"
      "s_nop 1\n"
      "v_permlane32_swap_b32 v206, v207\n"
      "v_add_f32 v206, v206, v207\n"
      "v_rsq_f32_e64 v206, v206\n"
      "s_waitcnt lgkmcnt(0)\n"
      "v_mfma_f32_16x16x32_bf16 v[168:171], a[0:3], v[140:143], 0\n"
      "ds_read_b128 v[148:151], v114 offset:10240\n"
      "v_mov_b32_e64 v207, v206\n"
      "v_mfma_f32_16x16x32_bf16 v[168:171], a[36:39], v[164:167], v[168:171]\n"
      "ds_read_b128 v[216:219], v115 offset:10240\n"
      "v_permlane16_swap_b32 v206, v207\n"
      "v_mfma_f32_16x16x32_bf16 v[224:227], a[12:15], v[140:143], 0\n"
      "ds_read_b128 v[136:139], v114 offset:16384\n"
      "v_mul_f32 v228, v207, v228\n"
      "v_mfma_f32_16x16x32_bf16 v[224:227], a[48:51], v[164:167], v[224:227]\n"
      "ds_read_b128 v[152:155], v115 offset:16384\n"
      "v_mul_f32 v198, v207, v198\n"
      "v_mfma_f32_16x16x32_bf16 v[192:195], a[20:23], v[140:143], 0\n"
      "v_mul_f32 v231, v207, v231\n"
      "v_mul_f32 v230, v207, v230\n"
      "v_mfma_f32_16x16x32_bf16 v[192:195], a[56:59], v[164:167], v[192:195]\n"
      "v_mul_f32 v229, v207, v229\n"
      "v_mul_f32 v196, v207, v196\n"
      "v_mul_f32 v199, v207, v199\n"
      "v_cvt_pk_f16_f32 v228, v228, v229\n"
      "v_cvt_pk_f16_f32 v229, v230, v231\n"
      "v_mul_f32_e64 v197, v207, v197\n"
      "v_cvt_pk_f16_f32 v230, v196, v197\n"
      "v_cvt_pk_f16_f32 v231, v198, v199\n"
      "v_mfma_f32_16x16x32_bf16 v[196:199], a[16:19], v[140:143], 0\n"
      "v_mul_f32 v212, v207, v212\n"
      "v_mul_f32 v213, v207, v213\n"
      "v_mfma_f32_16x16x32_bf16 v[196:199], a[52:55], v[164:167], v[196:199]\n"
      "v_mul_f32 v215, v207, v215\n"
      "s_waitcnt lgkmcnt(2)\n"
      "v_mfma_f32_16x16x32_bf16 v[168:171], a[72:75], v[148:151], v[168:171]\n"
      "v_cvt_pk_f16_f32 v40, v212, v213\n"
      "v_mul_f32 v214, v207, v214\n"
      "v_mfma_f32_16x16x32_bf16 v[168:171], a[108:111], v[216:219], v[168:171]\n"
      "v_mul_f32 v206, s36, v206\n"
      "v_cvt_pk_f16_f32 v41, v214, v215\n"
      "v_mfma_f32_16x16x32_bf16 v[196:199], a[88:91], v[148:151], v[196:199]\n"
      "v_mul_f32 v176, v206, v176\n"
      "v_mul_f32 v177, v206, v177\n"
      "v_mfma_f32_16x16x32_bf16 v[196:199], a[124:127], v[216:219], v[196:199]\n"
      "v_mul_f32 v178, v206, v178\n"
      "v_cvt_pk_f16_f32 v176, v176, v177\n"
      "v_mfma_f32_16x16x32_bf16 v[224:227], a[84:87], v[148:151], v[224:227]\n"
      "v_mul_f32 v209, v206, v209\n"
      "v_mul_f32_e64 v179, v206, v179\n"
      "v_mfma_f32_16x16x32_bf16 v[224:227], a[120:123], v[216:219], v[224:227]\n"
      "v_cvt_pk_f16_f32 v177, v178, v179\n"
      "v_mul_f32 v208, v206, v208\n"
      "v_mfma_f32_16x16x32_bf16 v[192:195], a[92:95], v[148:151], v[192:195]\n"
      "s_waitcnt lgkmcnt(0)\n"
      "v_mul_f32 v210, v206, v210\n"
      "v_cvt_pk_f16_f32 v178, v208, v209\n"
      "v_mfma_f32_16x16x32_bf16 v[192:195], a[128:131], v[216:219], v[192:195]\n"
      "v_mul_f32 v211, v206, v211\n"
      "v_cvt_pk_f16_f32 v179, v210, v211\n"
      "v_mfma_f32_16x16x32_bf16 v[208:211], a[4:7], v[140:143], 0\n"
      "v_mul_f32 v172, v206, v172\n"
      "v_mul_f32 v173, v206, v173\n"
      "v_mfma_f32_16x16x32_bf16 v[208:211], a[40:43], v[164:167], v[208:211]\n"
      "v_mfma_f32_16x16x32_bf16 v[208:211], a[76:79], v[148:151], v[208:211]\n"
      "v_cvt_pk_f16_f32 v38, v172, v173\n"
      "v_mul_f32 v174, v206, v174\n"
      "v_mfma_f32_16x16x32_bf16 v[208:211], a[112:115], v[216:219], v[208:211]\n"
      "v_mul_f32 v175, v206, v175\n"
      "v_cvt_pk_f16_f32 v39, v174, v175\n"
      "v_mfma_f32_16x16x32_bf16 v[172:175], a[8:11], v[140:143], 0\n"
      "v_mfma_f32_16x16x32_bf16 v[172:175], a[44:47], v[164:167], v[172:175]\n"
      "v_mfma_f32_16x16x32_bf16 v[172:175], a[80:83], v[148:151], v[172:175]\n"
      "v_mfma_f32_16x16x32_bf16 v[172:175], a[116:119], v[216:219], v[172:175]\n"
      "v_mfma_f32_16x16x32_bf16 v[168:171], a[144:147], v[136:139], v[168:171]\n"
      "v_mfma_f32_16x16x32_bf16 v[168:171], a[180:183], v[152:155], v[168:171]\n"
      "v_mfma_f32_16x16x32_bf16 v[208:211], a[148:151], v[136:139], v[208:211]\n"
      "v_mfma_f32_16x16x32_bf16 v[208:211], a[184:187], v[152:155], v[208:211]\n"
      "v_mfma_f32_16x16x32_bf16 v[172:175], a[152:155], v[136:139], v[172:175]\n"
      "v_mfma_f32_16x16x32_bf16 v[172:175], a[188:191], v[152:155], v[172:175]\n"
      "v_mfma_f32_16x16x32_bf16 v[224:227], a[156:159], v[136:139], v[224:227]\n"
      "v_mfma_f32_16x16x32_bf16 v[224:227], a[192:195], v[152:155], v[224:227]\n"
      "s_nop 1\n"
      "v_fma_f32 v206, v168, v168, s38\n"
      "v_mfma_f32_16x16x32_bf16 v[196:199], a[160:163], v[136:139], v[196:199]\n"
      "v_fmac_f32 v206, v169, v169\n"
      "v_fmac_f32 v206, v170, v170\n"
      "v_mfma_f32_16x16x32_bf16 v[196:199], a[196:199], v[152:155], v[196:199]\n"
      "s_nop 0\n"
      "v_fma_f32 v207, v224, v224, s38\n"
      "v_fmac_f32 v207, v225, v225\n"
      "v_mfma_f32_16x16x32_bf16 v[192:195], a[164:167], v[136:139], v[192:195]\n"
      "v_fmac_f32 v207, v226, v226\n"
      "v_fmac_f32 v206, v171, v171\n"
      "v_mfma_f32_16x16x32_bf16 v[192:195], a[200:203], v[152:155], v[192:195]\n"
      "v_fmac_f32 v206, v208, v208\n"
      "v_fmac_f32 v207, v227, v227\n"
      "v_mfma_f32_16x16x32_f16 v[164:167], v[188:191], v[180:183], v[70:73]\n"
      "v_fmac_f32 v206, v209, v209\n"
      "v_fmac_f32 v206, v210, v210\n"
      "v_mfma_f32_16x16x32_f16 v[164:167], v[184:187], v[156:159], v[164:167]\n"
      "v_fmac_f32 v207, v196, v196\n"
      "v_fmac_f32 v207, v197, v197\n"
      "v_mfma_f32_16x16x32_f16 v[164:167], v[200:203], v[32:35], v[164:167]\n"
      "v_fmac_f32 v206, v211, v211\n"
      "v_fmac_f32 v207, v198, v198\n"
      "v_fmac_f32 v207, v199, v199\n"
      "v_fmac_f32 v206, v172, v172\n"
      "v_fmac_f32 v207, v192, v192\n"
      "v_fmac_f32 v207, v193, v193\n"
      "v_fmac_f32 v206, v173, v173\n"
      "v_fmac_f32 v206, v174, v174\n"
      "v_fmac_f32 v207, v194, v194\n"
      "v_fmac_f32 v207, v195, v195\n"
      "v_fmac_f32 v206, v175, v175\n"
      "v_mov_b32 v204, v164\n"
      "v_mov_b32 v205, v165\n"
      "v_permlane16_swap_b32 v206, v207\n"
      "v_add_f32 v206, v206, v207\n"
      "v_mov_b32 v207, v206\n"
      "s_nop 1\n"
      "v_permlane32_swap_b32 v206, v207\n"
      "v_add_f32 v206, v206, v207\n"
      "v_rsq_f32 v206, v206\n"
      "s_nop 0\n"
      "v_mov_b32 v207, v206\n"
      "s_nop 1\n"
      "v_permlane16_swap_b32 v206, v207\n"
      "v_mul_f32 v198, v207, v198\n"
      "v_mul_f32 v227, v207, v227\n"
      "v_mul_f32 v192, v207, v192\n"
      "v_mul_f32 v194, v207, v194\n"
      "v_mul_f32 v226, v207, v226\n"
      "v_mul_f32 v224, v207, v224\n"
      "v_mul_f32 v225, v207, v225\n"
      "v_cvt_pk_f16_f32 v224, v224, v225\n"
      "v_mul_f32 v206, s36, v206\n"
      "v_mul_f32 v197, v207, v197\n"
      "v_cvt_pk_f16_f32 v225, v226, v227\n"
      "v_mul_f32 v196, v207, v196\n"
      "v_mul_f32 v171, v206, v171\n"
      "v_mul_f32 v199, v207, v199\n"
      "v_mul_f32 v193, v207, v193\n"
      "v_mul_f32 v211, v206, v211\n"
      "v_mul_f32 v210, v206, v210\n"
      "v_mul_f32_e64 v209, v206, v209\n"
      "v_cvt_pk_f16_f32 v46, v192, v193\n"
      "v_cvt_pk_f16_f32 v227, v198, v199\n"
      "v_cvt_pk_f16_f32 v226, v196, v197\n"
      "v_mul_f32 v195, v207, v195\n"
      "v_cvt_pk_f16_f32 v47, v194, v195\n"
      "v_mul_f32 v172, v206, v172\n"
      "v_mul_f32 v208, v206, v208\n"
      "v_mul_f32 v168, v206, v168\n"
      "v_mul_f32 v170, v206, v170\n"
      "v_mfma_f32_16x16x32_f16 v[196:199], v[224:227], v[180:183], v[86:89]\n"
      "v_mul_f32 v169, v206, v169\n"
      "v_cvt_pk_f16_f32 v168, v168, v169\n"
      "v_mfma_f32_16x16x32_f16 v[196:199], v[46:49], v[32:35], v[196:199]\n"
      "v_cvt_pk_f16_f32 v169, v170, v171\n"
      "v_cvt_pk_f16_f32 v170, v208, v209\n"
      "v_mul_f32_e64 v174, v206, v174\n"
      "v_cvt_pk_f16_f32 v171, v210, v211\n"
      "v_mfma_f32_16x16x32_f16 v[208:211], v[156:159], v[180:183], v[78:81]\n"
      "v_mul_f32_e64 v173, v206, v173\n"
      "v_cvt_pk_f16_f32 v44, v172, v173\n"
      "v_mfma_f32_16x16x32_f16 v[208:211], v[34:37], v[32:35], v[208:211]\n"
      "v_mul_f32 v175, v206, v175\n"
      "v_cvt_pk_f16_f32 v45, v174, v175\n"
      "v_mfma_f32_16x16x32_f16 v[172:175], v[228:231], v[180:183], v[82:85]\n"
      "v_mfma_f32_16x16x32_f16 v[172:175], v[40:43], v[32:35], v[172:175]\n"
      "s_nop 7\n"
      "s_or_b64 vcc, s[48:49], s[50:51]\n"
      "s_cbranch_scc0 L_fast_11_%=\n"
      "L_ok_65_%=:\n"
      "s_cmp_eq_u64 s[48:49], 0\n"
      "s_cbranch_scc1 L_go_66_%=\n"
      "s_mov_b32 m0, s27\n"
      "s_nop 0\n"
      "s_movrels_b32 s9, s80\n"
      "s_lshr_b32 s8, s9, 8\n"
      "s_and_b32 s9, s9, 0xff\n"
      "s_mul_i32 s10, s8, 73\n"
      "s_add_u32 s10, s10, s9\n"
      "s_mul_i32 s30, s10, 4802\n"
      "s_add_u32 s30, s30, s18\n"
      "s_and_b32 s29, s30, 15\n"
      "s_add_u32 s30, s29, 110592\n"
      "s_lshl_b64 s[34:35], s[48:49], 4\n"
      "s_lshr_b64 s[34:35], s[34:35], 5\n"
      "s_cbranch_scc1 L_gen_9_%=\n"
      "v_mbcnt_lo_u32_b32 v206, -1, 0\n"
      "v_mbcnt_hi_u32_b32 v206, -1, v206\n"
      "v_lshlrev_b32 v207, 1, v206\n"
      "v_add_u32 v165, 4802, v207\n"
      "v_cmp_gt_u32 vcc, s29, v207\n"
      "s_nop 1\n"
      "v_cndmask_b32 v207, v165, v207, vcc\n"
      "v_mov_b32 v165, 0\n"
      "v_add_u32 v207, 110592, v207\n"
      "s_mov_b64 exec, 0x7f\n"
      "ds_write_b16 v207, v165\n"
      "s_mov_b64 exec, -1\n"
      "v_lshlrev_b32_e64 v207, 4, v206\n"
      "v_add_u32 v207, 110592, v207\n"
      "ds_read_b128 v[164:167], v207\n"
      "ds_read_b128 v[140:143], v207 offset:1024\n"
      "v_min_u32 v206, 111296, v207\n"
      "ds_read_b128 v[148:151], v207 offset:2048\n"
      "ds_read_b128 v[136:139], v206 offset:4096\n"
      "ds_read_b128 v[216:219], v207 offset:3072\n"
      "s_waitcnt lgkmcnt(4)\n"
      "v_or3_b32 v164, v164, v165, v166\n"
      "s_waitcnt lgkmcnt(3)\n"
      "v_or3_b32 v164, v164, v167, v140\n"
      "v_or3_b32 v164, v164, v141, v142\n"
      "s_waitcnt lgkmcnt(2)\n"
      "v_or3_b32 v164, v164, v143, v148\n"
      "v_or3_b32 v164, v164, v149, v150\n"
      "s_waitcnt lgkmcnt(0)\n"
      "v_or3_b32 v164, v164, v151, v216\n"
      "v_or3_b32 v164, v164, v217, v218\n"
      "v_or3_b32 v164, v164, v219, v136\n"
      "v_or3_b32 v164, v164, v137, v138\n"
      "v_bitop3_b32 v164, v164, v139, s40 bitop3:0xa8\n"
      "v_cmp_ne_u32 s[48:49], 0, v164\n"
      "s_cmp_lg_u64 s[48:49], 0\n"
      "s_cbranch_scc1 L_gen_9_%=\n"
      "s_cmp_eq_u64 s[50:51], 0\n"
      "s_cbranch_scc1 L_fast_11_%=\n"
      "L_go_66_%=:\n"
      "v_mfma_f32_16x16x32_f16 v[164:167], v[156:159], v[176:179], v[90:93]\n"
      "ds_read_b128 v[140:143], v128 offset:3072\n"
      "v_max3_f32 v207, v211, v172, v173\n"
      "v_mfma_f32_16x16x32_f16 v[164:167], v[34:37], v[38:41], v[164:167]\n"
      "v_max3_f32 v206, v208, v209, v210\n"
      "v_max3_f32 v206, v206, v174, v175\n"
      "v_mfma_f32_16x16x32_f16 v[148:151], v[228:231], v[176:179], v[94:97]\n"
      "v_max3_f32 v206, v206, v198, v199\n"
      "v_max3_f32 v207, v207, v196, v197\n"
      "v_mfma_f32_16x16x32_f16 v[148:151], v[40:43], v[38:41], v[148:151]\n"
      "v_max3_f32 v206, v206, v207, v204\n"
      "v_mov_b32 v207, v206\n"
      "v_mfma_f32_16x16x32_f16 v[216:219], v[188:191], v[176:179], v[72:75]\n"
      "s_nop 0\n"
      "v_permlane16_swap_b32 v206, v207\n"
      "v_mfma_f32_16x16x32_f16 v[216:219], v[184:187], v[228:231], v[216:219]\n"
      "v_max_f32 v206, v206, v207\n"
      "v_mov_b32 v207, v206\n"
      "v_mfma_f32_16x16x32_f16 v[216:219], v[200:203], v[38:41], v[216:219]\n"
      "s_nop 0\n"
      "v_permlane32_swap_b32 v206, v207\n"
      "v_mfma_f32_16x16x32_f16 v[136:139], v[224:227], v[176:179], v[98:101]\n"
      "v_max_f32 v206, v206, v207\n"
      "v_sub_f32 v208, v208, v206\n"
      "v_mfma_f32_16x16x32_f16 v[136:139], v[46:49], v[38:41], v[136:139]\n"
      "v_sub_f32 v210, v210, v206\n"
      "v_sub_f32 v174, v174, v206\n"
      "v_mfma_f32_16x16x32_f16 v[176:179], v[156:159], v[168:171], v[102:105]\n"
      "s_waitcnt lgkmcnt(0)\n"
      "v_sub_f32 v211, v211, v206\n"
      "v_sub_f32 v173, v173, v206\n"
      "v_mfma_f32_16x16x32_f16 v[176:179], v[34:37], v[44:47], v[176:179]\n"
      "v_sub_f32 v209, v209, v206\n"
      "v_sub_f32 v172, v172, v206\n"
      "v_mfma_f32_16x16x32_f16 v[218:221], v[228:231], v[168:171], v[106:109]\n"
      "v_sub_f32 v175, v175, v206\n"
      "v_sub_f32 v199, v199, v206\n"
      "v_mfma_f32_16x16x32_f16 v[218:221], v[40:43], v[44:47], v[218:221]\n"
      "v_exp_f32 v172, v172\n"
      "v_mfma_f32_16x16x32_f16 v[228:231], v[188:191], v[168:171], v[74:77]\n"
      "v_sub_f32 v198, v198, v206\n"
      "v_sub_f32 v204, v204, v206\n"
      "v_mfma_f32_16x16x32_f16 v[228:231], v[184:187], v[224:227], v[228:231]\n"
      "v_exp_f32 v208, v208\n"
      "v_mfma_f32_16x16x32_f16 v[228:231], v[200:203], v[44:47], v[228:231]\n"
      "v_sub_f32 v197, v197, v206\n"
      "v_exp_f32 v198, v198\n"
      "v_mfma_f32_16x16x32_f16 v[156:159], v[224:227], v[168:171], v[110:113]\n"
      "v_exp_f32_e64 v210, v210\n"
      "v_mfma_f32_16x16x32_f16 v[156:159], v[46:49], v[44:47], v[156:159]\n"
      "v_exp_f32 v211, v211\n"
      "v_exp_f32 v209, v209\n"
      "v_exp_f32 v174, v174\n"
      "v_exp_f32 v173, v173\n"
      "v_exp_f32 v199, v199\n"
      "v_sub_f32 v196, v196, v206\n"
      "v_mfma_f32_16x16x32_f16 v[224:227], v[184:187], v[188:191], v[76:79]\n"
      "v_cvt_pk_bf16_f32 v208, v208, v209\n"
      "v_bitop3_b32 v142, v142, v143, s4 bitop3:0x7e\n"
      "v_exp_f32_e64 v175, v175\n"
      "v_cvt_pk_bf16_f32 v209, v210, v211\n"
      "v_exp_f32 v196, v196\n"
      "v_bitop3_b32 v140, v140, v141, s4 bitop3:0x7e\n"
      "v_exp_f32 v197, v197\n"
      "v_mfma_f32_16x16x16_f16 v[224:227], v[202:203], v[200:201], v[224:227]\n"
      "v_or_b32_e64 v140, v140, v142\n"
      "v_cvt_pk_bf16_f32 v210, v172, v173\n"
      "v_cvt_pk_bf16_f32 v62, v196, v197\n"
      "v_cvt_pk_bf16_f32 v211, v174, v175\n"
      "v_exp_f32 v204, v204\n"
      "v_cvt_pk_bf16_f32 v63, v198, v199\n"
      "v_cmp_ne_u32 vcc, 0, v140\n"
      "v_cvt_pk_bf16_f32 v64, v204, 0\n"
      "v_mfma_f32_16x16x32_bf16 v[196:199], v[66:69], v[208:211], 0\n"
      "s_mov_b64 s[48:49], vcc\n"
      "v_max3_f32 v230, v164, v165, v166\n"
      "v_max3_f32 v230, v230, v150, v151\n"
      "v_mfma_f32_16x16x32_bf16 v[196:199], v[66:69], v[62:65], v[196:199]\n"
      "s_cbranch_vccnz L_flg_67_%=\n"
      "L_fb_68_%=:\n"
      "v_mfma_f32_16x16x32_bf16 v[140:143], v[240:243], v[208:211], 0\n"
      "v_max3_f32 v231, v167, v148, v149\n"
      "v_max3_f32 v231, v231, v136, v137\n"
      "v_mfma_f32_16x16x32_bf16 v[140:143], v[50:53], v[62:65], v[140:143]\n"
      "v_max3_f32 v230, v230, v138, v139\n"
      "v_max3_f32 v230, v230, v231, v216\n"
      "v_mfma_f32_16x16x32_bf16 v[172:175], v[160:163], v[208:211], 0\n"
      "v_mov_b32_e64 v231, v230\n"
      "v_max3_f32 v197, v179, v218, v219\n"
      "v_mfma_f32_16x16x32_bf16 v[172:175], v[54:57], v[62:65], v[172:175]\n"
      "v_permlane16_swap_b32 v230, v231\n"
      "v_mfma_f32_16x16x32_bf16 v[168:171], v[144:147], v[208:211], 0\n"
      "v_max_f32 v230, v230, v231\n"
      "v_mov_b32 v231, v230\n"
      "v_mfma_f32_16x16x32_bf16 v[168:171], v[58:61], v[62:65], v[168:171]\n"
      "s_nop 0\n"
      "v_permlane32_swap_b32 v230, v231\n"
      "v_max_f32 v230, v230, v231\n"
      "v_sub_f32 v165, v165, v230\n"
      "v_sub_f32 v164, v164, v230\n"
      "v_exp_f32 v164, v164\n"
      "v_sub_f32 v136, v136, v230\n"
      "v_sub_f32 v167, v167, v230\n"
      "v_exp_f32 v165, v165\n"
      "v_sub_f32 v137, v137, v230\n"
      "v_sub_f32 v138, v138, v230\n"
      "v_sub_f32 v139, v139, v230\n"
      "v_rcp_f32 v196, v196\n"
      "v_exp_f32 v136, v136\n"
      "v_sub_f32 v216, v216, v230\n"
      "v_cvt_pk_bf16_f32 v164, v164, v165\n"
      "v_sub_f32 v151, v151, v230\n"
      "v_sub_f32 v149, v149, v230\n"
      "v_sub_f32 v166, v166, v230\n"
      "v_mul_f32 v141, v196, v141\n"
      "v_exp_f32 v149, v149\n"
      "v_sub_f32 v148, v148, v230\n"
      "v_exp_f32 v166, v166\n"
      "v_sub_f32 v150, v150, v230\n"
      "v_exp_f32 v139, v139\n"
      "v_exp_f32 v150, v150\n"
      "v_exp_f32 v167, v167\n"
      "v_exp_f32 v148, v148\n"
      "v_cvt_pk_bf16_f32 v165, v166, v167\n"
      "v_exp_f32 v138, v138\n"
      "v_exp_f32 v137, v137\n"
      "v_exp_f32 v151, v151\n"
      "v_exp_f32 v216, v216\n"
      "v_cvt_pk_bf16_f32 v166, v148, v149\n"
      "v_cvt_pk_bf16_f32 v167, v150, v151\n"
      "v_cvt_pk_bf16_f32 v62, v136, v137\n"
      "v_mul_f32_e64 v140, v196, v140\n"
      "v_cvt_pk_bf16_f32 v64, v216, 0\n"
      "v_cvt_pk_bf16_f32 v63, v138, v139\n"
      "v_mfma_f32_16x16x32_bf16 v[230:233], v[66:69], v[164:167], 0\n"
      "s_add_u32 s28, s27, 2\n"
      "v_mul_f32 v142, v196, v142\n"
      "v_cvt_pk_bf16_f32 v140, v140, v141\n"
      "v_mfma_f32_16x16x32_bf16 v[230:233], v[66:69], v[62:65], v[230:233]\n"
      "s_min_u32 m0, s28, s3\n"
      "v_mul_f32 v143, v196, v143\n"
      "v_cvt_pk_bf16_f32 v141, v142, v143\n"
      "v_mfma_f32_16x16x32_bf16 v[200:203], v[240:243], v[164:167], 0\n"
      "s_movrels_b32 s9, s80\n"
      "v_mul_f32 v173, v196, v173\n"
      "v_mul_f32 v172, v196, v172\n"
      "v_mfma_f32_16x16x32_bf16 v[200:203], v[50:53], v[62:65], v[200:203]\n"
      "s_lshr_b32 s8, s9, 8\n"
      "v_cvt_pk_bf16_f32 v142, v172, v173\n"
      "v_mul_f32 v175, v196, v175\n"
      "v_mfma_f32_16x16x32_bf16 v[208:211], v[160:163], v[164:167], 0\n"
      "s_and_b32 s9, s9, 0xff\n"
      "v_mul_f32 v174, v196, v174\n"
      "v_cvt_pk_bf16_f32 v143, v174, v175\n"
      "v_mfma_f32_16x16x32_bf16 v[208:211], v[54:57], v[62:65], v[208:211]\n"
      "ds_write_b128 v124, v[140:143]\n"
      "v_max3_f32 v216, v176, v177, v178\n"
      "v_mfma_f32_16x16x32_bf16 v[188:191], v[144:147], v[164:167], 0\n"
      "s_mul_i32 s10, s8, 73\n"
      "v_mul_f32 v168, v196, v168\n"
      "v_mul_f32 v170, v196, v170\n"
      "v_mfma_f32_16x16x32_bf16 v[188:191], v[58:61], v[62:65], v[188:191]\n"
      "s_add_u32 s10, s10, s9\n"
      "v_mul_f32 v169, v196, v169\n"
      "v_max3_f32 v197, v197, v156, v157\n"
      "v_cndmask_b32 v205, v205, v217, s[42:43]\n"
      "v_max3_f32 v216, v216, v220, v221\n"
      "v_max3_f32 v216, v216, v158, v159\n"
      "v_max3_f32 v216, v216, v197, v228\n"
      "v_mov_b32 v231, v216\n"
      "v_rcp_f32 v230, v230\n"
      "v_mul_f32_e64 v171, v196, v171\n"
      "v_cvt_pk_bf16_f32 v168, v168, v169\n"
      "v_cvt_pk_bf16_f32 v169, v170, v171\n"
      "v_permlane16_swap_b32 v216, v231\n"
      "v_max_f32 v216, v216, v231\n"
      "v_mov_b32_e64 v217, v216\n"
      "v_cndmask_b32 v205, v205, v229, s[44:45]\n"
      "v_cndmask_b32 v205, v205, v225, s[46:47]\n"
      "v_permlane32_swap_b32 v216, v217\n"
      "v_max_f32 v216, v216, v217\n"
      "v_sub_f32 v176, v176, v216\n"
      "v_sub_f32 v218, v218, v216\n"
      "v_sub_f32 v177, v177, v216\n"
      "v_exp_f32 v177, v177\n"
      "v_sub_f32 v159, v159, v216\n"
      "v_sub_f32 v178, v178, v216\n"
      "v_exp_f32 v178, v178\n"
      "v_sub_f32 v228, v228, v216\n"
      "v_sub_f32 v219, v219, v216\n"
      "v_exp_f32 v218, v218\n"
      "v_sub_f32 v156, v156, v216\n"
      "v_sub_f32 v220, v220, v216\n"
      "v_sub_f32 v221, v221, v216\n"
      "v_sub_f32 v179, v179, v216\n"
      "v_exp_f32 v221, v221\n"
      "v_exp_f32 v156, v156\n"
      "v_exp_f32 v179, v179\n"
      "v_sub_f32 v157, v157, v216\n"
      "v_exp_f32 v176, v176\n"
      "v_exp_f32 v157, v157\n"
      "v_sub_f32 v158, v158, v216\n"
      "v_cvt_pk_bf16_f32 v176, v176, v177\n"
      "v_cvt_pk_bf16_f32 v177, v178, v179\n"
      "v_exp_f32 v219, v219\n"
      "v_exp_f32 v220, v220\n"
      "v_exp_f32 v228, v228\n"
      "v_exp_f32_e64 v158, v158\n"
      "v_exp_f32_e64 v159, v159\n"
      "v_cvt_pk_bf16_f32 v179, v220, v221\n"
      "v_cvt_pk_bf16_f32 v178, v218, v219\n"
      "v_cvt_pk_bf16_f32 v63, v158, v159\n"
      "v_cvt_pk_bf16_f32 v64, v228, 0\n"
      "v_mul_f32_e64 v200, v230, v200\n"
      "v_cvt_pk_bf16_f32 v62, v156, v157\n"
      "v_max_f32_dpp v170, v205, v205 row_ror:8 row_mask:0xf bank_mask:0xf\n"
      "v_mfma_f32_16x16x32_bf16 v[164:167], v[66:69], v[176:179], 0\n"
      "ds_read_b128 v[140:143], v115 offset:18432\n"
      "v_max_f32_dpp v170, v170, v170 row_ror:4 row_mask:0xf bank_mask:0xf\n"
      "v_mfma_f32_16x16x32_bf16 v[164:167], v[66:69], v[62:65], v[164:167]\n"
      "ds_read_b128 v[148:151], v114 offset:18432\n"
      "v_max_f32_dpp v170, v170, v170 row_ror:2 row_mask:0xf bank_mask:0xf\n"
      "v_mfma_f32_16x16x32_bf16 v[196:199], v[240:243], v[176:179], 0\n"
      "s_mul_i32 s28, s10, 4802\n"
      "v_max_f32_dpp v170, v170, v170 row_ror:1 row_mask:0xf bank_mask:0xf\n"
      "v_mul_f32 v201, v230, v201\n"
      "v_mfma_f32_16x16x32_bf16 v[196:199], v[50:53], v[62:65], v[196:199]\n"
      "s_add_u32 s56, s18, s28\n"
      "v_mul_f32 v210, v230, v210\n"
      "v_max_f32_dpp v170, v170, v170 row_bcast:15 row_mask:0xa bank_mask:0xf\n"
      "v_cvt_pk_bf16_f32 v200, v200, v201\n"
      "v_mfma_f32_16x16x32_bf16 v[172:175], v[160:163], v[176:179], 0\n"
      "s_addc_u32 s57, s19, 0\n"
      "v_max_f32_dpp v170, v170, v170 row_bcast:31 row_mask:0xc bank_mask:0xf\n"
      "v_mul_f32_e64 v202, v230, v202\n"
      "v_mfma_f32_16x16x32_bf16 v[172:175], v[54:57], v[62:65], v[172:175]\n"
      "ds_read_b128 v[216:219], v115 offset:24576\n"
      "v_readlane_b32 s31, v170, 63\n"
      "v_mfma_f32_16x16x32_bf16 v[136:139], v[144:147], v[176:179], 0\n"
      "s_and_b32 s56, s56, -16\n"
      "v_subrev_f32 v205, s31, v205\n"
      "v_mul_f32_e64 v208, v230, v208\n"
      "v_mfma_f32_16x16x32_bf16 v[136:139], v[58:61], v[62:65], v[136:139]\n"
      "s_sub_u32 s58, s56, 110592\n"
      "v_mul_f32 v203, v230, v203\n"
      "v_cvt_pk_bf16_f32 v201, v202, v203\n"
      "v_exp_f32 v205, v205\n"
      "v_mul_f32 v209, v230, v209\n"
      "v_cvt_pk_bf16_f32 v202, v208, v209\n"
      "v_cvt_pk_bf16_f32 v205, v205, v205\n"
      "s_waitcnt lgkmcnt(1)\n"
      "v_mfma_f32_16x16x32_bf16 v[184:187], a[0:3], v[148:151], 0\n"
      "s_subb_u32 s59, s57, 0\n"
      "v_mul_f32 v211, v230, v211\n"
      "v_cvt_pk_bf16_f32 v203, v210, v211\n"
      "v_mfma_f32_16x16x32_bf16 v[184:187], a[36:39], v[140:143], v[184:187]\n"
      "ds_write_b16 v130, v205\n"
      "v_mfma_f32_16x16x32_bf16 v[208:211], a[4:7], v[148:151], 0\n"
      "ds_read_b128 v[156:159], v131 offset:64\n"
      "v_mul_f32_e64 v189, v230, v189\n"
      "v_mfma_f32_16x16x32_bf16 v[208:211], a[40:43], v[140:143], v[208:211]\n"
      "ds_read_b128 v[220:223], v131\n"
      "v_mul_f32_e64 v188, v230, v188\n"
      "v_mfma_f32_16x16x32_bf16 v[224:227], a[12:15], v[148:151], 0\n"
      "ds_read_b128 v[204:207], v114 offset:24576\n"
      "v_cvt_pk_bf16_f32 v188, v188, v189\n"
      "v_mfma_f32_16x16x32_bf16 v[224:227], a[48:51], v[140:143], v[224:227]\n"
      "ds_write_b128 v124, v[200:203] offset:1024\n"
      "v_mfma_f32_16x16x32_bf16 v[176:179], a[8:11], v[148:151], 0\n"
      "s_sub_u32 s56, s56, 73728\n"
      "v_mul_f32 v190, v230, v190\n"
      "v_mul_f32 v191, v230, v191\n"
      "v_mfma_f32_16x16x32_bf16 v[176:179], a[44:47], v[140:143], v[176:179]\n"
      "s_subb_u32 s57, s57, 0\n"
      "v_mov_b32 v170, 0\n"
      "v_cvt_pk_bf16_f32 v189, v190, v191\n"
      "v_mfma_f32_16x16x32_bf16 v[228:231], a[16:19], v[148:151], 0\n"
      "s_mul_i32 s28, s8, 1373568\n"
      "s_mul_i32 s29, s9, 2688\n"
      "s_add_u32 s28, s28, s29\n"
      "v_mfma_f32_16x16x32_bf16 v[228:231], a[52:55], v[140:143], v[228:231]\n"
      "ds_write2st64_b64 v125, v[168:169], v[188:189] offset0:0 offset1:2\n"
      "s_waitcnt lgkmcnt(3)\n"
      "v_mfma_f32_16x16x32_bf16 v[188:191], v[220:223], v[66:69], 0\n"
      "s_cmp_eq_u32 s8, 72\n"
      "s_cselect_b32 s8, 0, 100270464\n"
      "s_cmp_eq_u32 s9, 72\n"
      "v_mfma_f32_16x16x32_bf16 v[188:191], v[156:159], v[66:69], v[188:191]\n"
      "ds_write_b32 v129, v170\n"
      "v_mfma_f32_16x16x32_bf16 v[200:203], v[220:223], v[240:243], 0\n"
      "s_cselect_b32 s9, 0, 196224\n"
      "s_add_u32 s10, s8, s9\n"
      "s_add_u32 s28, s28, s10\n"
      "v_mfma_f32_16x16x32_bf16 v[200:203], v[156:159], v[50:53], v[200:203]\n"
      "s_add_u32 s52, s16, s28\n"
      "s_nop 0\n"
      "v_mov_b32_dpp v188, v188 quad_perm:[0,0,0,0] row_mask:0xf bank_mask:0xf\n"
      "v_mfma_f32_16x16x32_bf16 v[240:243], v[220:223], v[160:163], 0\n"
      "s_addc_u32 s53, s17, 0\n"
      "v_rcp_f32 v188, v188\n"
      "v_mfma_f32_16x16x32_bf16 v[240:243], v[156:159], v[54:57], v[240:243]\n"
      "s_cmp_lg_u32 s10, s11\n"
      "v_mfma_f32_16x16x32_bf16 v[160:163], v[220:223], v[144:147], 0\n"
      "s_waitcnt lgkmcnt(2)\n"
      "v_mfma_f32_16x16x32_bf16 v[160:163], v[156:159], v[58:61], v[160:163]\n"
      "s_cbranch_scc1 L_edge_70_%=\n"
      "L_eb_71_%=:\n"
      "v_mfma_f32_16x16x32_bf16 v[168:171], a[20:23], v[148:151], 0\n"
      "s_nop 1\n"
      "v_cndmask_b32 v200, v200, v240, s[42:43]\n"
      "v_mfma_f32_16x16x32_bf16 v[168:171], a[56:59], v[140:143], v[168:171]\n"
      "s_nop 1\n"
      "v_cndmask_b32 v200, v200, v160, s[44:45]\n"
      "v_mul_f32 v200, v188, v200\n"
      "v_cvt_pk_bf16_f32 v200, v200, v200\n"
      "ds_write_b16 v135, v200\n"
      "s_branch L_join_10_%=\n"
      "L_flg_67_%=:\n"
      "v_cmp_gt_i32 vcc, 0, v140\n"
      "s_cbranch_vccz L_fb_68_%=\n"
      "s_sleep 1\n"
      "ds_read_b128 v[140:143], v128 offset:3072\n"
      "s_waitcnt lgkmcnt(0)\n"
      "v_bitop3_b32 v140, v140, v141, s4 bitop3:0x7e\n"
      "v_bitop3_b32 v142, v142, v143, s4 bitop3:0x7e\n"
      "v_or_b32 v140, v140, v142\n"
      "v_cmp_ne_u32 s[48:49], 0, v140\n"
      "s_branch L_flg_67_%=\n"
      "L_edge_70_%=:\n"
      "s_mov_b32 s11, s10\n"
      "s_sub_u32 s28, s60, s8\n"
      "s_sub_u32 s29, s61, s9\n"
      "s_mov_b32 s60, s8\n"
      "s_mov_b32 s61, s9\n"
      "s_mov_b64 exec, s[64:65]\n"
      "v_add_u32 v116, s28, v116\n"
      "s_mov_b64 exec, s[70:71]\n"
      "v_add_u32 v116, s29, v116\n"
      "s_mov_b64 exec, s[66:67]\n"
      "v_add_u32 v117, s28, v117\n"
      "s_mov_b64 exec, s[72:73]\n"
      "v_add_u32 v117, s29, v117\n"
      "s_mov_b64 exec, s[68:69]\n"
      "v_add_u32 v118, s28, v118\n"
      "s_mov_b64 exec, s[74:75]\n"
      "v_add_u32 v118, s29, v118\n"
      "s_mov_b64 exec, -1\n"
      "s_nop 4\n"
      "s_branch L_eb_71_%=\n"
      "L_gen_9_%=:\n"
      "s_nop 0\n"
      "s_nop 0\n"
      "v_mfma_f32_16x16x32_f16 v[164:167], v[156:159], v[176:179], v[90:93]\n"
      "s_movk_i32 s31, 0x3fb9\n"
      "v_mbcnt_lo_u32_b32 v206, -1, 0\n"
      "v_mbcnt_hi_u32_b32 v206, -1, v206\n"
      "v_mfma_f32_16x16x32_f16 v[164:167], v[34:37], v[38:41], v[164:167]\n"
      "v_and_b32 v207, 15, v206\n"
      "v_lshrrev_b32 v148, 4, v206\n"
      "v_mul_u32_u24 v149, 0x62, v207\n"
      "v_lshl_add_u32 v149, v148, 3, v149\n"
      "v_lshlrev_b32 v148, 2, v148\n"
      "v_add_u32 v149, s30, v149\n"
      "v_and_b32 v206, -4, v149\n"
      "v_sub_u32 v207, v207, v148\n"
      "v_and_b32 v148, 1, v207\n"
      "v_lshrrev_b32 v207, 1, v207\n"
      "v_and_b32 v149, 2, v149\n"
      "v_lshlrev_b32_e64 v148, 4, v148\n"
      "v_cmp_eq_u32 vcc, 0, v207\n"
      "v_lshlrev_b32 v148, v148, s31\n"
      "v_mfma_f32_16x16x32_f16 v[140:143], v[228:231], v[176:179], v[94:97]\n"
      "ds_read2_b32 v[216:217], v206 offset0:16 offset1:17\n"
      "v_cndmask_b32 v150, 0, v148, vcc\n"
      "v_cmp_eq_u32 vcc, 1, v207\n"
      "ds_read2_b32 v[136:137], v206 offset0:0 offset1:1\n"
      "ds_read2_b32 v[138:139], v206 offset0:8 offset1:9\n"
      "ds_read2_b32 v[218:219], v206 offset0:18 offset1:24\n"
      "ds_read2_b32 v[152:153], v206 offset0:2 offset1:10\n"
      "v_add_u32 v206, 1568, v206\n"
      "v_mfma_f32_16x16x32_f16 v[236:239], v[188:191], v[176:179], v[72:75]\n"
      "ds_read2_b32 v[182:183], v206 offset0:2 offset1:10\n"
      "ds_read2_b32 v[180:181], v206 offset0:0 offset1:1\n"
      "ds_read2_b32 v[154:155], v206 offset0:8 offset1:9\n"
      "ds_read2_b32 v[192:193], v206 offset0:18 offset1:24\n"
      "ds_read2_b32 v[194:195], v206 offset0:16 offset1:17\n"
      "v_add_u32 v206, 1568, v206\n"
      "v_mfma_f32_16x16x32_f16 v[236:239], v[184:187], v[228:231], v[236:239]\n"
      "ds_read2_b32 v[232:233], v206 offset0:18 offset1:24\n"
      "ds_read2_b32 v[212:213], v206 offset0:8 offset1:9\n"
      "ds_read2_b32 v[220:221], v206 offset0:0 offset1:1\n"
      "v_mfma_f32_16x16x32_f16 v[236:239], v[200:203], v[38:41], v[236:239]\n"
      "ds_read2_b32 v[222:223], v206 offset0:2 offset1:10\n"
      "ds_read2_b32 v[214:215], v206 offset0:16 offset1:17\n"
      "v_mfma_f32_16x16x32_f16 v[140:143], v[40:43], v[38:41], v[140:143]\n"
      "s_waitcnt lgkmcnt(13)\n"
      "v_cndmask_b32 v151, 0, v148, vcc\n"
      "s_waitcnt lgkmcnt(11)\n"
      "v_alignbyte_b32 v216, v217, v216, v149\n"
      "v_alignbyte_b32 v217, v218, v217, v149\n"
      "v_alignbyte_b32 v138, v139, v138, v149\n"
      "v_alignbyte_b32 v136, v137, v136, v149\n"
      "v_mfma_f32_16x16x16_bf16 v[196:199], v[150:151], v[216:217], v[196:199]\n"
      "s_waitcnt lgkmcnt(10)\n"
      "v_alignbyte_b32 v219, v219, v219, v149\n"
      "v_alignbyte_b32 v137, v152, v137, v149\n"
      "v_lshlrev_b32 v219, 16, v219\n"
      "s_waitcnt lgkmcnt(7)\n"
      "v_alignbyte_b32 v154, v155, v154, v149\n"
      "v_alignbyte_b32 v180, v181, v180, v149\n"
      "v_fmac_f32 v204, s37, v219\n"
      "s_waitcnt lgkmcnt(5)\n"
      "v_mfma_f32_16x16x32_f16 v[216:219], v[224:227], v[176:179], v[98:101]\n"
      "ds_read_b128 v[176:179], v128 offset:3072\n"
      "v_alignbyte_b32 v193, v193, v193, v149\n"
      "v_mfma_f32_16x16x32_f16 v[216:219], v[46:49], v[38:41], v[216:219]\n"
      "v_alignbyte_b32 v139, v153, v139, v149\n"
      "v_alignbyte_b32 v194, v195, v194, v149\n"
      "v_mfma_f32_16x16x16_bf16 v[208:211], v[150:151], v[136:137], v[208:211]\n"
      "s_waitcnt lgkmcnt(3)\n"
      "v_alignbyte_b32 v181, v182, v181, v149\n"
      "v_alignbyte_b32 v195, v192, v195, v149\n"
      "v_mfma_f32_16x16x16_bf16 v[172:175], v[150:151], v[138:139], v[172:175]\n"
      "s_waitcnt lgkmcnt(1)\n"
      "v_lshlrev_b32_e64 v193, 16, v193\n"
      "v_alignbyte_b32 v220, v221, v220, v149\n"
      "v_alignbyte_b32 v221, v222, v221, v149\n"
      "v_mfma_f32_16x16x32_f16 v[244:247], v[156:159], v[168:171], v[102:105]\n"
      "v_alignbyte_b32 v212, v213, v212, v149\n"
      "v_alignbyte_b32 v213, v223, v213, v149\n"
      "v_mfma_f32_16x16x32_f16 v[244:247], v[34:37], v[44:47], v[244:247]\n"
      "v_max3_f32 v148, v208, v209, v210\n"
      "v_alignbyte_b32 v214, v215, v214, v149\n"
      "v_max3_f32 v148, v148, v174, v175\n"
      "v_mfma_f32_16x16x32_f16 v[136:139], v[228:231], v[168:171], v[106:109]\n"
      "v_alignbyte_b32 v233, v233, v233, v149\n"
      "v_alignbyte_b32 v215, v232, v215, v149\n"
      "v_mfma_f32_16x16x32_f16 v[136:139], v[40:43], v[44:47], v[136:139]\n"
      "v_alignbyte_b32 v155, v183, v155, v149\n"
      "v_max3_f32 v149, v211, v172, v173\n"
      "v_max3_f32 v149, v149, v196, v197\n"
      "v_mfma_f32_16x16x32_f16 v[228:231], v[188:191], v[168:171], v[74:77]\n"
      "v_max3_f32 v148, v148, v198, v199\n"
      "v_max3_f32 v148, v148, v149, v204\n"
      "v_mfma_f32_16x16x32_f16 v[228:231], v[184:187], v[224:227], v[228:231]\n"
      "v_mov_b32 v149, v148\n"
      "v_fmac_f32 v236, s37, v193\n"
      "v_mfma_f32_16x16x32_f16 v[228:231], v[200:203], v[44:47], v[228:231]\n"
      "s_waitcnt lgkmcnt(0)\n"
      "v_permlane16_swap_b32 v148, v149\n"
      "v_mfma_f32_16x16x32_f16 v[156:159], v[224:227], v[168:171], v[110:113]\n"
      "v_max_f32 v148, v148, v149\n"
      "v_mov_b32 v149, v148\n"
      "v_mfma_f32_16x16x32_f16 v[156:159], v[46:49], v[44:47], v[156:159]\n"
      "s_nop 0\n"
      "v_permlane32_swap_b32 v148, v149\n"
      "v_max_f32 v148, v148, v149\n"
      "v_sub_f32 v210, v210, v148\n"
      "v_sub_f32 v208, v208, v148\n"
      "v_sub_f32 v209, v209, v148\n"
      "v_mfma_f32_16x16x32_f16 v[224:227], v[184:187], v[188:191], v[76:79]\n"
      "v_sub_f32 v172, v172, v148\n"
      "v_sub_f32 v175, v175, v148\n"
      "v_sub_f32 v199, v199, v148\n"
      "v_sub_f32 v196, v196, v148\n"
      "v_exp_f32 v196, v196\n"
      "v_exp_f32 v209, v209\n"
      "v_exp_f32 v199, v199\n"
      "v_lshlrev_b32 v233, 16, v233\n"
      "v_sub_f32 v204, v204, v148\n"
      "v_exp_f32 v210, v210\n"
      "v_sub_f32 v211, v211, v148\n"
      "v_exp_f32 v172, v172\n"
      "v_sub_f32 v174, v174, v148\n"
      "v_sub_f32 v198, v198, v148\n"
      "v_exp_f32 v198, v198\n"
      "v_mfma_f32_16x16x16_bf16 v[140:143], v[150:151], v[154:155], v[140:143]\n"
      "v_exp_f32 v208, v208\n"
      "v_exp_f32 v175, v175\n"
      "v_mfma_f32_16x16x16_bf16 v[156:159], v[150:151], v[214:215], v[156:159]\n"
      "v_bitop3_b32 v176, v176, v177, s4 bitop3:0x7e\n"
      "v_exp_f32 v204, v204\n"
      "v_mfma_f32_16x16x16_bf16 v[164:167], v[150:151], v[180:181], v[164:167]\n"
      "v_sub_f32 v197, v197, v148\n"
      "v_bitop3_b32 v178, v178, v179, s4 bitop3:0x7e\n"
      "v_mfma_f32_16x16x16_f16 v[224:227], v[202:203], v[200:201], v[224:227]\n"
      "v_sub_f32 v173, v173, v148\n"
      "v_exp_f32_e64 v211, v211\n"
      "v_cvt_pk_bf16_f32 v208, v208, v209\n"
      "v_mfma_f32_16x16x16_bf16 v[244:247], v[150:151], v[220:221], v[244:247]\n"
      "v_cvt_pk_bf16_f32 v209, v210, v211\n"
      "v_exp_f32_e64 v174, v174\n"
      "v_max3_f32 v230, v164, v165, v166\n"
      "v_exp_f32_e64 v173, v173\n"
      "v_mfma_f32_16x16x16_bf16 v[216:219], v[150:151], v[194:195], v[216:219]\n"
      "v_cvt_pk_bf16_f32 v210, v172, v173\n"
      "v_cvt_pk_bf16_f32 v211, v174, v175\n"
      "v_exp_f32_e64 v197, v197\n"
      "v_mfma_f32_16x16x16_bf16 v[136:139], v[150:151], v[212:213], v[136:139]\n"
      "v_cvt_pk_bf16_f32 v63, v198, v199\n"
      "v_cvt_pk_bf16_f32 v64, v204, 0\n"
      "v_cvt_pk_bf16_f32 v62, v196, v197\n"
      "v_mfma_f32_16x16x32_bf16 v[196:199], v[66:69], v[208:211], 0\n"
      "v_or_b32_e64 v176, v176, v178\n"
      "v_cmp_ne_u32 vcc, 0, v176\n"
      "v_mfma_f32_16x16x32_bf16 v[196:199], v[66:69], v[62:65], v[196:199]\n"
      "s_mov_b64 s[48:49], vcc\n"
      "v_max3_f32 v231, v167, v140, v141\n"
      "v_max3_f32 v230, v230, v142, v143\n"
      "v_mfma_f32_16x16x32_bf16 v[148:151], v[240:243], v[208:211], 0\n"
      "s_cbranch_vccnz L_flg_89_%=\n"
      "L_fb_90_%=:\n"
      "v_max3_f32 v231, v231, v216, v217\n"
      "v_mfma_f32_16x16x32_bf16 v[172:175], v[160:163], v[208:211], 0\n"
      "s_nop 0\n"
      "v_max3_f32 v230, v230, v218, v219\n"
      "v_max3_f32 v230, v230, v231, v236\n"
      "v_mfma_f32_16x16x32_bf16 v[172:175], v[54:57], v[62:65], v[172:175]\n"
      "v_mov_b32 v231, v230\n"
      "v_fmac_f32 v228, s37, v233\n"
      "v_mfma_f32_16x16x32_bf16 v[168:171], v[144:147], v[208:211], 0\n"
      "v_permlane16_swap_b32 v230, v231\n"
      "v_mfma_f32_16x16x32_bf16 v[168:171], v[58:61], v[62:65], v[168:171]\n"
      "v_max_f32 v230, v230, v231\n"
      "v_mov_b32_e64 v231, v230\n"
      "v_max3_f32 v204, v247, v136, v137\n"
      "v_rcp_f32 v196, v196\n"
      "v_permlane32_swap_b32 v230, v231\n"
      "v_max_f32 v230, v230, v231\n"
      "v_sub_f32 v164, v164, v230\n"
      "v_sub_f32 v217, v217, v230\n"
      "v_sub_f32 v216, v216, v230\n"
      "v_exp_f32 v164, v164\n"
      "v_sub_f32 v219, v219, v230\n"
      "v_sub_f32 v143, v143, v230\n"
      "v_mul_f32 v171, v196, v171\n"
      "v_sub_f32 v165, v165, v230\n"
      "v_mfma_f32_16x16x32_bf16 v[148:151], v[50:53], v[62:65], v[148:151]\n"
      "v_sub_f32 v142, v142, v230\n"
      "v_exp_f32 v165, v165\n"
      "v_sub_f32 v167, v167, v230\n"
      "v_sub_f32 v140, v140, v230\n"
      "v_exp_f32 v140, v140\n"
      "v_exp_f32 v216, v216\n"
      "v_sub_f32 v166, v166, v230\n"
      "v_exp_f32_e64 v143, v143\n"
      "v_cvt_pk_bf16_f32 v164, v164, v165\n"
      "v_sub_f32 v218, v218, v230\n"
      "v_sub_f32 v141, v141, v230\n"
      "v_exp_f32 v167, v167\n"
      "v_exp_f32 v166, v166\n"
      "v_mul_f32 v148, v196, v148\n"
      "v_sub_f32 v236, v236, v230\n"
      "v_exp_f32 v141, v141\n"
      "v_exp_f32 v236, v236\n"
      "v_exp_f32 v142, v142\n"
      "v_exp_f32 v219, v219\n"
      "v_cvt_pk_bf16_f32 v165, v166, v167\n"
      "v_exp_f32_e64 v217, v217\n"
      "v_cvt_pk_bf16_f32 v166, v140, v141\n"
      "v_exp_f32 v218, v218\n"
      "v_mul_f32 v173, v196, v173\n"
      "v_cvt_pk_bf16_f32 v167, v142, v143\n"
      "v_cvt_pk_bf16_f32 v62, v216, v217\n"
      "v_cvt_pk_bf16_f32 v63, v218, v219\n"
      "v_cvt_pk_bf16_f32 v64, v236, 0\n"
      "v_mul_f32 v149, v196, v149\n"
      "v_mfma_f32_16x16x32_bf16 v[176:179], v[66:69], v[164:167], 0\n"
      "v_mul_f32 v150, v196, v150\n"
      "v_cvt_pk_bf16_f32 v148, v148, v149\n"
      "v_mfma_f32_16x16x32_bf16 v[176:179], v[66:69], v[62:65], v[176:179]\n"
      "v_mul_f32_e64 v151, v196, v151\n"
      "v_max3_f32 v207, v244, v245, v246\n"
      "v_mfma_f32_16x16x32_bf16 v[208:211], v[240:243], v[164:167], 0\n"
      "v_mul_f32 v172, v196, v172\n"
      "v_mul_f32 v174, v196, v174\n"
      "v_mfma_f32_16x16x32_bf16 v[208:211], v[50:53], v[62:65], v[208:211]\n"
      "v_cvt_pk_bf16_f32 v149, v150, v151\n"
      "v_cvt_pk_bf16_f32 v150, v172, v173\n"
      "v_mfma_f32_16x16x32_bf16 v[184:187], v[160:163], v[164:167], 0\n"
      "s_add_u32 s28, s27, 2\n"
      "v_mul_f32 v175, v196, v175\n"
      "v_cvt_pk_bf16_f32 v151, v174, v175\n"
      "v_mfma_f32_16x16x32_bf16 v[184:187], v[54:57], v[62:65], v[184:187]\n"
      "ds_write_b128 v124, v[148:151]\n"
      "v_mfma_f32_16x16x32_bf16 v[216:219], v[144:147], v[164:167], 0\n"
      "v_mul_f32_e64 v169, v196, v169\n"
      "v_mul_f32 v168, v196, v168\n"
      "v_mfma_f32_16x16x32_bf16 v[216:219], v[58:61], v[62:65], v[216:219]\n"
      "s_min_u32 m0, s28, s3\n"
      "v_cvt_pk_bf16_f32 v168, v168, v169\n"
      "v_max3_f32 v207, v207, v138, v139\n"
      "v_max3_f32 v204, v204, v156, v157\n"
      "v_max3_f32 v207, v207, v158, v159\n"
      "v_max3_f32 v207, v207, v204, v228\n"
      "v_mov_b32 v204, v207\n"
      "v_mul_f32 v170, v196, v170\n"
      "v_mbcnt_lo_u32_b32 v206, -1, 0\n"
      "v_permlane16_swap_b32 v207, v204\n"
      "v_max_f32 v207, v207, v204\n"
      "v_mov_b32_e64 v204, v207\n"
      "v_cndmask_b32 v205, v205, v237, s[42:43]\n"
      "v_mbcnt_hi_u32_b32 v206, -1, v206\n"
      "v_permlane32_swap_b32 v207, v204\n"
      "v_max_f32 v207, v207, v204\n"
      "v_sub_f32 v136, v136, v207\n"
      "v_sub_f32 v244, v244, v207\n"
      "v_cndmask_b32 v205, v205, v229, s[44:45]\n"
      "v_sub_f32 v246, v246, v207\n"
      "v_sub_f32 v158, v158, v207\n"
      "v_cvt_pk_bf16_f32 v169, v170, v171\n"
      "v_sub_f32 v157, v157, v207\n"
      "v_sub_f32 v156, v156, v207\n"
      "v_sub_f32 v245, v245, v207\n"
      "v_sub_f32 v137, v137, v207\n"
      "v_exp_f32 v244, v244\n"
      "v_sub_f32 v139, v139, v207\n"
      "v_exp_f32 v245, v245\n"
      "v_sub_f32 v247, v247, v207\n"
      "v_sub_f32 v138, v138, v207\n"
      "v_exp_f32 v157, v157\n"
      "v_sub_f32 v159, v159, v207\n"
      "v_exp_f32 v246, v246\n"
      "v_exp_f32 v247, v247\n"
      "v_cndmask_b32 v205, v205, v225, s[46:47]\n"
      "v_exp_f32 v138, v138\n"
      "v_sub_f32 v228, v228, v207\n"
      "v_min_u32 v206, 48, v206\n"
      "v_exp_f32_e64 v136, v136\n"
      "v_cvt_pk_bf16_f32 v244, v244, v245\n"
      "v_exp_f32 v137, v137\n"
      "v_cvt_pk_bf16_f32 v245, v246, v247\n"
      "v_cvt_pk_bf16_f32 v246, v136, v137\n"
      "v_exp_f32 v228, v228\n"
      "v_exp_f32 v159, v159\n"
      "v_exp_f32 v139, v139\n"
      "v_exp_f32 v158, v158\n"
      "v_cvt_pk_bf16_f32 v247, v138, v139\n"
      "v_cvt_pk_bf16_f32 v63, v158, v159\n"
      "v_exp_f32 v156, v156\n"
      "v_cvt_pk_bf16_f32 v64, v228, 0\n"
      "v_cvt_pk_bf16_f32 v62, v156, v157\n"
      "v_mfma_f32_16x16x32_bf16 v[164:167], v[66:69], v[244:247], 0\n"
      "ds_read_b128 v[148:151], v114 offset:18432\n"
      "v_lshlrev_b32 v206, 1, v206\n"
      "v_mfma_f32_16x16x32_bf16 v[164:167], v[66:69], v[62:65], v[164:167]\n"
      "ds_read_b128 v[140:143], v115 offset:18432\n"
      "v_add_u32 v206, s30, v206\n"
      "v_mfma_f32_16x16x32_bf16 v[196:199], v[240:243], v[244:247], 0\n"
      "ds_read_u16 v170, v206 offset:4704\n"
      "v_mov_b32 v206, 0\n"
      "v_mfma_f32_16x16x32_bf16 v[196:199], v[50:53], v[62:65], v[196:199]\n"
      "s_movrels_b32 s9, s80\n"
      "v_rcp_f32_e64 v176, v176\n"
      "v_mfma_f32_16x16x32_bf16 v[172:175], v[160:163], v[244:247], 0\n"
      "s_lshr_b32 s8, s9, 8\n"
      "v_mul_f32 v209, v176, v209\n"
      "v_mul_f32_e64 v208, v176, v208\n"
      "v_mfma_f32_16x16x32_bf16 v[172:175], v[54:57], v[62:65], v[172:175]\n"
      "s_and_b32 s9, s9, 0xff\n"
      "v_mul_f32 v186, v176, v186\n"
      "v_mul_f32 v217, v176, v217\n"
      "v_mul_f32 v185, v176, v185\n"
      "v_mul_f32 v210, v176, v210\n"
      "v_cvt_pk_bf16_f32 v208, v208, v209\n"
      "v_mfma_f32_16x16x32_bf16 v[136:139], v[144:147], v[244:247], 0\n"
      "s_mul_i32 s10, s8, 73\n"
      "v_mul_f32 v211, v176, v211\n"
      "v_mul_f32 v184, v176, v184\n"
      "v_mfma_f32_16x16x32_bf16 v[136:139], v[58:61], v[62:65], v[136:139]\n"
      "s_add_u32 s10, s10, s9\n"
      "v_mul_f32 v187, v176, v187\n"
      "v_cvt_pk_bf16_f32 v209, v210, v211\n"
      "v_cvt_pk_bf16_f32 v210, v184, v185\n"
      "v_cvt_pk_bf16_f32 v211, v186, v187\n"
      "v_mul_f32 v216, v176, v216\n"
      "s_waitcnt lgkmcnt(1)\n"
      "v_mfma_f32_16x16x32_bf16 v[184:187], a[0:3], v[148:151], 0\n"
      "s_mul_i32 s28, s10, 4802\n"
      "v_cvt_pk_bf16_f32 v216, v216, v217\n"
      "v_mul_f32 v218, v176, v218\n"
      "v_mfma_f32_16x16x32_bf16 v[184:187], a[36:39], v[140:143], v[184:187]\n"
      "ds_write_b128 v124, v[208:211] offset:1024\n"
      "v_mfma_f32_16x16x32_bf16 v[208:211], a[4:7], v[148:151], 0\n"
      "s_waitcnt lgkmcnt(1)\n"
      "v_lshlrev_b32 v170, 16, v170\n"
      "v_fmac_f32_e64 v205, s37, v170\n"
      "v_mfma_f32_16x16x32_bf16 v[208:211], a[40:43], v[140:143], v[208:211]\n"
      "s_add_u32 s56, s18, s28\n"
      "v_max_f32_dpp v207, v205, v205 row_ror:8 row_mask:0xf bank_mask:0xf\n"
      "v_mul_f32 v219, v176, v219\n"
      "v_mfma_f32_16x16x32_bf16 v[176:179], a[8:11], v[148:151], 0\n"
      "s_addc_u32 s57, s19, 0\n"
      "v_cvt_pk_bf16_f32 v217, v218, v219\n"
      "v_max_f32_dpp v207, v207, v207 row_ror:4 row_mask:0xf bank_mask:0xf\n"
      "v_mfma_f32_16x16x32_bf16 v[176:179], a[44:47], v[140:143], v[176:179]\n"
      "ds_write2st64_b64 v125, v[168:169], v[216:217] offset0:0 offset1:2\n"
      "v_mfma_f32_16x16x32_bf16 v[224:227], a[12:15], v[148:151], 0\n"
      "s_and_b32 s56, s56, -16\n"
      "s_sub_u32 s58, s56, 110592\n"
      "v_max_f32_dpp v207, v207, v207 row_ror:2 row_mask:0xf bank_mask:0xf\n"
      "v_mfma_f32_16x16x32_bf16 v[224:227], a[48:51], v[140:143], v[224:227]\n"
      "s_subb_u32 s59, s57, 0\n"
      "v_max_f32_dpp v207, v207, v207 row_ror:1 row_mask:0xf bank_mask:0xf\n"
      "s_sub_u32 s56, s56, 73728\n"
      "s_nop 0\n"
      "v_max_f32_dpp v207, v207, v207 row_bcast:15 row_mask:0xa bank_mask:0xf\n"
      "v_mfma_f32_16x16x32_bf16 v[228:231], a[16:19], v[148:151], 0\n"
      "s_subb_u32 s57, s57, 0\n"
      "s_mul_i32 s28, s8, 1373568\n"
      "v_max_f32_dpp v207, v207, v207 row_bcast:31 row_mask:0xc bank_mask:0xf\n"
      "v_mfma_f32_16x16x32_bf16 v[168:171], a[20:23], v[148:151], 0\n"
      "ds_read_b128 v[216:219], v115 offset:24576\n"
      "v_readlane_b32 s31, v207, 63\n"
      "v_mfma_f32_16x16x32_bf16 v[168:171], a[56:59], v[140:143], v[168:171]\n"
      "s_mul_i32 s29, s9, 2688\n"
      "v_subrev_f32 v205, s31, v205\n"
      "v_exp_f32 v205, v205\n"
      "s_nop 0\n"
      "v_cvt_pk_bf16_f32 v205, v205, v205\n"
      "ds_write_b16 v130, v205\n"
      "ds_read_b128 v[244:247], v131 offset:64\n"
      "ds_read_b128 v[156:159], v131\n"
      "ds_write_b32 v129, v206\n"
      "s_add_u32 s28, s28, s29\n"
      "s_cmp_eq_u32 s8, 72\n"
      "ds_read_b128 v[204:207], v114 offset:24576\n"
      "s_cselect_b32 s8, 0, 100270464\n"
      "v_mfma_f32_16x16x32_bf16 v[228:231], a[52:55], v[140:143], v[228:231]\n"
      "s_cmp_eq_u32 s9, 72\n"
      "s_cselect_b32 s9, 0, 196224\n"
      "s_add_u32 s10, s8, s9\n"
      "s_add_u32 s28, s28, s10\n"
      "s_add_u32 s52, s16, s28\n"
      "s_addc_u32 s53, s17, 0\n"
      "s_cmp_lg_u32 s10, s11\n"
      "s_cbranch_scc1 L_edge_92_%=\n"
      "L_eb_93_%=:\n"
      "s_waitcnt lgkmcnt(2)\n"
      "v_mfma_f32_16x16x32_bf16 v[236:239], v[156:159], v[66:69], 0\n"
      "v_mfma_f32_16x16x32_bf16 v[236:239], v[244:247], v[66:69], v[236:239]\n"
      "s_waitcnt lgkmcnt(0)\n"
      "v_mfma_f32_16x16x32_bf16 v[200:203], v[156:159], v[240:243], 0\n"
      "v_mfma_f32_16x16x32_bf16 v[200:203], v[244:247], v[50:53], v[200:203]\n"
      "v_mfma_f32_16x16x32_bf16 v[240:243], v[156:159], v[160:163], 0\n"
      "v_mfma_f32_16x16x32_bf16 v[240:243], v[244:247], v[54:57], v[240:243]\n"
      "v_mfma_f32_16x16x32_bf16 v[160:163], v[156:159], v[144:147], 0\n"
      "v_mfma_f32_16x16x32_bf16 v[160:163], v[244:247], v[58:61], v[160:163]\n"
      "s_nop 0\n"
      "v_mov_b32_dpp v236, v236 quad_perm:[0,0,0,0] row_mask:0xf bank_mask:0xf\n"
      "s_nop 3\n"
      "v_cndmask_b32 v200, v200, v240, s[42:43]\n"
      "v_rcp_f32_e64 v236, v236\n"
      "v_cndmask_b32 v200, v200, v160, s[44:45]\n"
      "v_mul_f32 v200, v236, v200\n"
      "v_cvt_pk_bf16_f32 v200, v200, v200\n"
      "ds_write_b16 v135, v200\n"
      "s_branch L_join_10_%=\n"
      "L_flg_89_%=:\n"
      "v_cmp_gt_i32 vcc, 0, v176\n"
      "s_cbranch_vccz L_fb_90_%=\n"
      "s_sleep 1\n"
      "ds_read_b128 v[176:179], v128 offset:3072\n"
      "s_waitcnt lgkmcnt(0)\n"
      "v_bitop3_b32 v176, v176, v177, s4 bitop3:0x7e\n"
      "v_bitop3_b32 v178, v178, v179, s4 bitop3:0x7e\n"
      "v_or_b32 v176, v176, v178\n"
      "v_cmp_ne_u32 s[48:49], 0, v176\n"
      "s_branch L_flg_89_%=\n"
      "L_edge_92_%=:\n"
      "s_mov_b32 s11, s10\n"
      "s_sub_u32 s28, s60, s8\n"
      "s_sub_u32 s29, s61, s9\n"
      "s_mov_b32 s60, s8\n"
      "s_mov_b32 s61, s9\n"
      "s_mov_b64 exec, s[64:65]\n"
      "v_add_u32 v116, s28, v116\n"
      "s_mov_b64 exec, s[70:71]\n"
      "v_add_u32 v116, s29, v116\n"
      "s_mov_b64 exec, s[66:67]\n"
      "v_add_u32 v117, s28, v117\n"
      "s_mov_b64 exec, s[72:73]\n"
      "v_add_u32 v117, s29, v117\n"
      "s_mov_b64 exec, s[68:69]\n"
      "v_add_u32 v118, s28, v118\n"
      "s_mov_b64 exec, s[74:75]\n"
      "v_add_u32 v118, s29, v118\n"
      "s_mov_b64 exec, -1\n"
      "s_nop 4\n"
      "s_branch L_eb_93_%=\n"
      "L_gen_19_%=:\n"
      "s_waitcnt lgkmcnt(0)\n"
      "s_nop 5\n"
      "s_cmp_eq_u64 s[34:35], 0\n"
      "s_cbranch_scc1 L_ok_114_%=\n"
      "ds_read_b128 v[200:203], v114 offset:18432\n"
      "ds_read_b128 v[248:251], v115 offset:18432\n"
      "ds_read_b128 v[252:255], v114 offset:30720\n"
      "ds_read_b128 v[228:231], v114 offset:24576\n"
      "ds_read_b128 v[168:171], v115 offset:24576\n"
      "s_waitcnt lgkmcnt(3)\n"
      "v_mfma_f32_16x16x32_bf16 v[208:211], a[0:3], v[200:203], 0\n"
      "v_mfma_f32_16x16x32_bf16 v[208:211], a[36:39], v[248:251], v[208:211]\n"
      "v_mfma_f32_16x16x32_bf16 v[220:223], a[4:7], v[200:203], 0\n"
      "s_nop 0\n"
      "v_mfma_f32_16x16x32_bf16 v[220:223], a[40:43], v[248:251], v[220:223]\n"
      "v_mfma_f32_16x16x32_bf16 v[144:147], a[8:11], v[200:203], 0\n"
      "s_waitcnt lgkmcnt(0)\n"
      "v_mfma_f32_16x16x32_bf16 v[224:227], a[12:15], v[200:203], 0\n"
      "s_nop 0\n"
      "v_mfma_f32_16x16x32_bf16 v[224:227], a[48:51], v[248:251], v[224:227]\n"
      "v_mfma_f32_16x16x32_bf16 v[196:199], a[16:19], v[200:203], 0\n"
      "v_mfma_f32_16x16x32_bf16 v[196:199], a[52:55], v[248:251], v[196:199]\n"
      "v_mfma_f32_16x16x32_bf16 v[196:199], a[88:91], v[228:231], v[196:199]\n"
      "v_mfma_f32_16x16x32_bf16 v[224:227], a[84:87], v[228:231], v[224:227]\n"
      "v_mfma_f32_16x16x32_bf16 v[140:143], a[20:23], v[200:203], 0\n"
      "ds_read_b128 v[200:203], v115 offset:30720\n"
      "v_mfma_f32_16x16x32_bf16 v[140:143], a[56:59], v[248:251], v[140:143]\n"
      "v_mfma_f32_16x16x32_bf16 v[208:211], a[72:75], v[228:231], v[208:211]\n"
      "v_mfma_f32_16x16x32_bf16 v[208:211], a[108:111], v[168:171], v[208:211]\n"
      "v_mfma_f32_16x16x32_bf16 v[144:147], a[44:47], v[248:251], v[144:147]\n"
      "v_mfma_f32_16x16x32_bf16 v[144:147], a[80:83], v[228:231], v[144:147]\n"
      "v_mfma_f32_16x16x32_bf16 v[144:147], a[116:119], v[168:171], v[144:147]\n"
      "v_mfma_f32_16x16x32_bf16 v[220:223], a[76:79], v[228:231], v[220:223]\n"
      "s_nop 0\n"
      "v_mfma_f32_16x16x32_bf16 v[220:223], a[112:115], v[168:171], v[220:223]\n"
      "s_waitcnt lgkmcnt(0)\n"
      "v_mfma_f32_16x16x32_bf16 v[208:211], a[144:147], v[252:255], v[208:211]\n"
      "ds_read_b128 v[248:251], v114 offset:20480\n"
      "v_mfma_f32_16x16x32_bf16 v[208:211], a[180:183], v[200:203], v[208:211]\n"
      "v_mfma_f32_16x16x32_bf16 v[140:143], a[92:95], v[228:231], v[140:143]\n"
      "v_mfma_f32_16x16x32_bf16 v[140:143], a[128:131], v[168:171], v[140:143]\n"
      "v_mfma_f32_16x16x32_bf16 v[144:147], a[152:155], v[252:255], v[144:147]\n"
      "v_mfma_f32_16x16x32_bf16 v[144:147], a[188:191], v[200:203], v[144:147]\n"
      "v_mfma_f32_16x16x32_bf16 v[220:223], a[148:151], v[252:255], v[220:223]\n"
      "v_mfma_f32_16x16x32_bf16 v[220:223], a[184:187], v[200:203], v[220:223]\n"
      "s_nop 1\n"
      "v_fma_f32 v218, v208, v208, s38\n"
      "v_mfma_f32_16x16x32_bf16 v[196:199], a[124:127], v[168:171], v[196:199]\n"
      "s_nop 0\n"
      "v_mfma_f32_16x16x32_bf16 v[224:227], a[120:123], v[168:171], v[224:227]\n"
      "v_mfma_f32_16x16x32_bf16 v[224:227], a[156:159], v[252:255], v[224:227]\n"
      "v_fmac_f32 v218, v209, v209\n"
      "v_mfma_f32_16x16x32_bf16 v[224:227], a[192:195], v[200:203], v[224:227]\n"
      "v_fmac_f32 v218, v210, v210\n"
      "v_mfma_f32_16x16x32_bf16 v[196:199], a[160:163], v[252:255], v[196:199]\n"
      "v_fmac_f32 v218, v211, v211\n"
      "v_fmac_f32 v218, v220, v220\n"
      "v_mfma_f32_16x16x32_bf16 v[196:199], a[196:199], v[200:203], v[196:199]\n"
      "v_fmac_f32 v218, v221, v221\n"
      "v_fmac_f32 v218, v222, v222\n"
      "v_mfma_f32_16x16x32_bf16 v[140:143], a[164:167], v[252:255], v[140:143]\n"
      "v_fma_f32 v219, v224, v224, s38\n"
      "v_fmac_f32_e64 v218, v223, v223\n"
      "v_mfma_f32_16x16x32_bf16 v[140:143], a[200:203], v[200:203], v[140:143]\n"
      "ds_read_b128 v[200:203], v115 offset:20480\n"
      "v_fmac_f32 v219, v225, v225\n"
      "v_fmac_f32 v219, v226, v226\n"
      "v_fmac_f32 v219, v227, v227\n"
      "v_fmac_f32 v219, v196, v196\n"
      "v_fmac_f32 v219, v197, v197\n"
      "v_fmac_f32 v219, v198, v198\n"
      "v_fmac_f32 v218, v144, v144\n"
      "v_fmac_f32 v219, v199, v199\n"
      "v_fmac_f32 v219, v140, v140\n"
      "v_fmac_f32 v219, v141, v141\n"
      "v_fmac_f32 v219, v142, v142\n"
      "v_fmac_f32 v218, v145, v145\n"
      "v_fmac_f32 v218, v146, v146\n"
      "v_fmac_f32 v219, v143, v143\n"
      "v_fmac_f32 v218, v147, v147\n"
      "s_nop 1\n"
      "v_permlane16_swap_b32 v218, v219\n"
      "v_add_f32 v218, v218, v219\n"
      "v_mov_b32 v219, v218\n"
      "s_nop 1\n"
      "v_permlane32_swap_b32 v218, v219\n"
      "v_add_f32 v218, v218, v219\n"
      "v_rsq_f32 v218, v218\n"
      "s_nop 0\n"
      "v_mov_b32 v219, v218\n"
      "s_waitcnt lgkmcnt(0)\n"
      "v_mfma_f32_16x16x32_bf16 v[212:215], a[0:3], v[248:251], 0\n"
      "ds_read_b128 v[228:231], v114 offset:26624\n"
      "v_permlane16_swap_b32 v218, v219\n"
      "v_mfma_f32_16x16x32_bf16 v[212:215], a[36:39], v[200:203], v[212:215]\n"
      "ds_read_b128 v[168:171], v115 offset:26624\n"
      "v_mul_f32 v226, v219, v226\n"
      "v_mul_f32 v225, v219, v225\n"
      "v_mul_f32 v227, v219, v227\n"
      "v_mul_f32_e64 v224, v219, v224\n"
      "v_mfma_f32_16x16x32_bf16 v[156:159], a[12:15], v[248:251], 0\n"
      "ds_read_b128 v[252:255], v114 offset:32768\n"
      "v_cvt_pk_f16_f32 v224, v224, v225\n"
      "v_mfma_f32_16x16x32_bf16 v[156:159], a[48:51], v[200:203], v[156:159]\n"
      "v_mul_f32 v197, v219, v197\n"
      "v_cvt_pk_f16_f32 v225, v226, v227\n"
      "v_mul_f32 v198, v219, v198\n"
      "v_mul_f32_e64 v199, v219, v199\n"
      "v_cvt_pk_f16_f32 v227, v198, v199\n"
      "v_mul_f32 v196, v219, v196\n"
      "v_cvt_pk_f16_f32 v226, v196, v197\n"
      "v_mfma_f32_16x16x32_bf16 v[196:199], a[16:19], v[248:251], 0\n"
      "v_mul_f32 v141, v219, v141\n"
      "v_mul_f32 v140, v219, v140\n"
      "v_mfma_f32_16x16x32_bf16 v[196:199], a[52:55], v[200:203], v[196:199]\n"
      "v_cvt_pk_f16_f32 v34, v140, v141\n"
      "v_mul_f32 v143, v219, v143\n"
      "v_mfma_f32_16x16x32_bf16 v[236:239], a[20:23], v[248:251], 0\n"
      "ds_read_b128 v[176:179], v115 offset:32768\n"
      "v_mul_f32 v142, v219, v142\n"
      "v_mfma_f32_16x16x32_bf16 v[236:239], a[56:59], v[200:203], v[236:239]\n"
      "v_cvt_pk_f16_f32 v35, v142, v143\n"
      "s_waitcnt lgkmcnt(2)\n"
      "v_mfma_f32_16x16x32_bf16 v[212:215], a[72:75], v[228:231], v[212:215]\n"
      "v_mul_f32 v218, s36, v218\n"
      "v_mul_f32 v208, v218, v208\n"
      "v_mfma_f32_16x16x32_bf16 v[212:215], a[108:111], v[168:171], v[212:215]\n"
      "v_mul_f32_e64 v209, v218, v209\n"
      "v_cvt_pk_f16_f32 v208, v208, v209\n"
      "v_mfma_f32_16x16x32_bf16 v[156:159], a[84:87], v[228:231], v[156:159]\n"
      "v_mul_f32 v210, v218, v210\n"
      "v_mul_f32 v211, v218, v211\n"
      "v_mfma_f32_16x16x32_bf16 v[156:159], a[120:123], v[168:171], v[156:159]\n"
      "v_cvt_pk_f16_f32 v209, v210, v211\n"
      "v_mul_f32 v220, v218, v220\n"
      "v_mfma_f32_16x16x32_bf16 v[196:199], a[88:91], v[228:231], v[196:199]\n"
      "v_mul_f32 v221, v218, v221\n"
      "v_cvt_pk_f16_f32 v210, v220, v221\n"
      "v_mfma_f32_16x16x32_bf16 v[196:199], a[124:127], v[168:171], v[196:199]\n"
      "v_mul_f32 v223, v218, v223\n"
      "v_mul_f32 v222, v218, v222\n"
      "v_mfma_f32_16x16x32_bf16 v[236:239], a[92:95], v[228:231], v[236:239]\n"
      "s_waitcnt lgkmcnt(0)\n"
      "v_mul_f32 v144, v218, v144\n"
      "v_mfma_f32_16x16x32_bf16 v[236:239], a[128:131], v[168:171], v[236:239]\n"
      "v_cvt_pk_f16_f32 v211, v222, v223\n"
      "v_mul_f32 v145, v218, v145\n"
      "v_mfma_f32_16x16x32_bf16 v[220:223], a[4:7], v[248:251], 0\n"
      "v_mul_f32 v146, v218, v146\n"
      "v_mul_f32_e64 v147, v218, v147\n"
      "v_mfma_f32_16x16x32_bf16 v[220:223], a[40:43], v[200:203], v[220:223]\n"
      "v_cvt_pk_f16_f32 v32, v144, v145\n"
      "v_cvt_pk_f16_f32 v33, v146, v147\n"
      "v_mfma_f32_16x16x32_bf16 v[220:223], a[76:79], v[228:231], v[220:223]\n"
      "v_mfma_f32_16x16x32_bf16 v[220:223], a[112:115], v[168:171], v[220:223]\n"
      "v_mfma_f32_16x16x32_bf16 v[144:147], a[8:11], v[248:251], 0\n"
      "v_mfma_f32_16x16x32_bf16 v[144:147], a[44:47], v[200:203], v[144:147]\n"
      "v_mfma_f32_16x16x32_bf16 v[144:147], a[80:83], v[228:231], v[144:147]\n"
      "ds_read_b128 v[200:203], v115 offset:22528\n"
      "v_mfma_f32_16x16x32_bf16 v[144:147], a[116:119], v[168:171], v[144:147]\n"
      "v_mfma_f32_16x16x32_bf16 v[212:215], a[144:147], v[252:255], v[212:215]\n"
      "v_mfma_f32_16x16x32_bf16 v[212:215], a[180:183], v[176:179], v[212:215]\n"
      "v_mfma_f32_16x16x32_bf16 v[220:223], a[148:151], v[252:255], v[220:223]\n"
      "v_mfma_f32_16x16x32_bf16 v[220:223], a[184:187], v[176:179], v[220:223]\n"
      "v_mfma_f32_16x16x32_bf16 v[144:147], a[152:155], v[252:255], v[144:147]\n"
      "v_mfma_f32_16x16x32_bf16 v[144:147], a[188:191], v[176:179], v[144:147]\n"
      "v_mfma_f32_16x16x32_bf16 v[156:159], a[156:159], v[252:255], v[156:159]\n"
      "s_nop 0\n"
      "v_mfma_f32_16x16x32_bf16 v[156:159], a[192:195], v[176:179], v[156:159]\n"
      "s_nop 1\n"
      "v_fma_f32 v218, v212, v212, s38\n"
      "v_mfma_f32_16x16x32_bf16 v[196:199], a[160:163], v[252:255], v[196:199]\n"
      "v_fmac_f32 v218, v213, v213\n"
      "v_fmac_f32 v218, v214, v214\n"
      "v_mfma_f32_16x16x32_bf16 v[196:199], a[196:199], v[176:179], v[196:199]\n"
      "v_fmac_f32 v218, v215, v215\n"
      "v_fma_f32 v219, v156, v156, s38\n"
      "v_fmac_f32 v218, v220, v220\n"
      "v_fmac_f32_e64 v219, v157, v157\n"
      "v_mfma_f32_16x16x32_bf16 v[236:239], a[164:167], v[252:255], v[236:239]\n"
      "ds_read_b128 v[248:251], v114 offset:22528\n"
      "v_fmac_f32 v219, v158, v158\n"
      "v_mfma_f32_16x16x32_bf16 v[236:239], a[200:203], v[176:179], v[236:239]\n"
      "v_fmac_f32 v219, v159, v159\n"
      "v_fmac_f32 v219, v196, v196\n"
      "v_fmac_f32 v219, v197, v197\n"
      "v_fmac_f32 v219, v198, v198\n"
      "v_fmac_f32 v218, v221, v221\n"
      "v_fmac_f32 v218, v222, v222\n"
      "v_fmac_f32 v218, v223, v223\n"
      "v_fmac_f32 v218, v144, v144\n"
      "v_fmac_f32 v218, v145, v145\n"
      "v_fmac_f32 v219, v199, v199\n"
      "v_fmac_f32 v219, v236, v236\n"
      "v_fmac_f32 v218, v146, v146\n"
      "v_fmac_f32 v219, v237, v237\n"
      "v_fmac_f32 v219, v238, v238\n"
      "v_fmac_f32 v218, v147, v147\n"
      "v_fmac_f32 v219, v239, v239\n"
      "s_nop 1\n"
      "v_permlane16_swap_b32 v218, v219\n"
      "v_add_f32 v218, v218, v219\n"
      "v_mov_b32 v219, v218\n"
      "s_nop 1\n"
      "v_permlane32_swap_b32 v218, v219\n"
      "v_add_f32 v218, v218, v219\n"
      "v_rsq_f32 v218, v218\n"
      "s_waitcnt lgkmcnt(0)\n"
      "v_mfma_f32_16x16x32_bf16 v[148:151], a[0:3], v[248:251], 0\n"
      "ds_read_b128 v[228:231], v114 offset:28672\n"
      "v_mov_b32 v219, v218\n"
      "v_mfma_f32_16x16x32_bf16 v[148:151], a[36:39], v[200:203], v[148:151]\n"
      "ds_read_b128 v[168:171], v115 offset:28672\n"
      "v_permlane16_swap_b32 v218, v219\n"
      "v_mfma_f32_16x16x32_bf16 v[172:175], a[12:15], v[248:251], 0\n"
      "ds_read_b128 v[252:255], v114 offset:34816\n"
      "v_mul_f32 v156, v219, v156\n"
      "v_mfma_f32_16x16x32_bf16 v[172:175], a[48:51], v[200:203], v[172:175]\n"
      "v_mul_f32 v159, v219, v159\n"
      "v_mul_f32 v157, v219, v157\n"
      "v_mul_f32 v197, v219, v197\n"
      "v_mul_f32 v158, v219, v158\n"
      "v_cvt_pk_f16_f32 v156, v156, v157\n"
      "v_mul_f32 v198, v219, v198\n"
      "v_cvt_pk_f16_f32 v157, v158, v159\n"
      "v_mul_f32 v199, v219, v199\n"
      "v_mul_f32 v196, v219, v196\n"
      "v_cvt_pk_f16_f32 v158, v196, v197\n"
      "v_cvt_pk_f16_f32 v159, v198, v199\n"
      "v_mfma_f32_16x16x32_bf16 v[196:199], a[16:19], v[248:251], 0\n"
      "v_mul_f32 v236, v219, v236\n"
      "v_mul_f32 v237, v219, v237\n"
      "v_mfma_f32_16x16x32_bf16 v[196:199], a[52:55], v[200:203], v[196:199]\n"
      "v_mul_f32_e64 v239, v219, v239\n"
      "v_cvt_pk_f16_f32 v40, v236, v237\n"
      "v_mfma_f32_16x16x32_bf16 v[140:143], a[20:23], v[248:251], 0\n"
      "ds_read_b128 v[234:237], v115 offset:34816\n"
      "v_mul_f32 v238, v219, v238\n"
      "v_mfma_f32_16x16x32_bf16 v[140:143], a[56:59], v[200:203], v[140:143]\n"
      "s_waitcnt lgkmcnt(2)\n"
      "v_mul_f32 v218, s36, v218\n"
      "v_cvt_pk_f16_f32 v41, v238, v239\n"
      "v_mfma_f32_16x16x32_bf16 v[148:151], a[72:75], v[228:231], v[148:151]\n"
      "v_mul_f32 v212, v218, v212\n"
      "v_mul_f32_e64 v213, v218, v213\n"
      "v_mfma_f32_16x16x32_bf16 v[148:151], a[108:111], v[168:171], v[148:151]\n"
      "v_mul_f32 v214, v218, v214\n"
      "v_cvt_pk_f16_f32 v212, v212, v213\n"
      "v_mfma_f32_16x16x32_bf16 v[196:199], a[88:91], v[228:231], v[196:199]\n"
      "v_mul_f32 v215, v218, v215\n"
      "v_mul_f32 v221, v218, v221\n"
      "v_mfma_f32_16x16x32_bf16 v[196:199], a[124:127], v[168:171], v[196:199]\n"
      "v_cvt_pk_f16_f32 v213, v214, v215\n"
      "v_mul_f32 v220, v218, v220\n"
      "v_mfma_f32_16x16x32_bf16 v[172:175], a[84:87], v[228:231], v[172:175]\n"
      "v_cvt_pk_f16_f32 v214, v220, v221\n"
      "v_mul_f32 v222, v218, v222\n"
      "v_mfma_f32_16x16x32_bf16 v[172:175], a[120:123], v[168:171], v[172:175]\n"
      "v_mul_f32 v223, v218, v223\n"
      "v_cvt_pk_f16_f32 v215, v222, v223\n"
      "v_mfma_f32_16x16x32_bf16 v[220:223], a[4:7], v[248:251], 0\n"
      "v_mul_f32 v145, v218, v145\n"
      "s_waitcnt lgkmcnt(0)\n"
      "v_mfma_f32_16x16x32_bf16 v[220:223], a[40:43], v[200:203], v[220:223]\n"
      "v_mul_f32_e64 v147, v218, v147\n"
      "v_mul_f32_e64 v146, v218, v146\n"
      "v_mfma_f32_16x16x32_bf16 v[220:223], a[76:79], v[228:231], v[220:223]\n"
      "v_mul_f32_e64 v144, v218, v144\n"
      "v_mfma_f32_16x16x32_bf16 v[220:223], a[112:115], v[168:171], v[220:223]\n"
      "v_cvt_pk_f16_f32 v39, v146, v147\n"
      "v_cvt_pk_f16_f32 v38, v144, v145\n"
      "v_mfma_f32_16x16x32_bf16 v[144:147], a[8:11], v[248:251], 0\n"
      "v_mfma_f32_16x16x32_bf16 v[144:147], a[44:47], v[200:203], v[144:147]\n"
      "v_mfma_f32_16x16x32_bf16 v[144:147], a[80:83], v[228:231], v[144:147]\n"
      "v_mfma_f32_16x16x32_bf16 v[144:147], a[116:119], v[168:171], v[144:147]\n"
      "v_mfma_f32_16x16x32_bf16 v[140:143], a[92:95], v[228:231], v[140:143]\n"
      "v_mfma_f32_16x16x32_bf16 v[140:143], a[128:131], v[168:171], v[140:143]\n"
      "v_mfma_f32_16x16x32_bf16 v[148:151], a[144:147], v[252:255], v[148:151]\n"
      "v_mfma_f32_16x16x32_bf16 v[148:151], a[180:183], v[234:237], v[148:151]\n"
      "v_mfma_f32_16x16x32_bf16 v[220:223], a[148:151], v[252:255], v[220:223]\n"
      "v_mfma_f32_16x16x32_bf16 v[220:223], a[184:187], v[234:237], v[220:223]\n"
      "v_mfma_f32_16x16x32_bf16 v[144:147], a[152:155], v[252:255], v[144:147]\n"
      "v_mfma_f32_16x16x32_bf16 v[144:147], a[188:191], v[234:237], v[144:147]\n"
      "v_mfma_f32_16x16x32_bf16 v[172:175], a[156:159], v[252:255], v[172:175]\n"
      "v_mfma_f32_16x16x32_bf16 v[172:175], a[192:195], v[234:237], v[172:175]\n"
      "s_nop 1\n"
      "v_fma_f32 v218, v148, v148, s38\n"
      "v_mfma_f32_16x16x32_bf16 v[196:199], a[160:163], v[252:255], v[196:199]\n"
      "v_fmac_f32 v218, v149, v149\n"
      "v_fmac_f32_e64 v218, v150, v150\n"
      "v_mfma_f32_16x16x32_bf16 v[196:199], a[196:199], v[234:237], v[196:199]\n"
      "s_nop 0\n"
      "v_fma_f32 v219, v172, v172, s38\n"
      "v_fmac_f32 v219, v173, v173\n"
      "v_mfma_f32_16x16x32_bf16 v[140:143], a[164:167], v[252:255], v[140:143]\n"
      "v_fmac_f32 v219, v174, v174\n"
      "v_fmac_f32 v218, v151, v151\n"
      "v_mfma_f32_16x16x32_bf16 v[140:143], a[200:203], v[234:237], v[140:143]\n"
      "v_fmac_f32 v218, v220, v220\n"
      "v_fmac_f32 v219, v175, v175\n"
      "v_mfma_f32_16x16x32_f16 v[248:251], v[136:139], v[208:211], v[70:73]\n"
      "v_fmac_f32 v218, v221, v221\n"
      "v_fmac_f32 v218, v222, v222\n"
      "v_mfma_f32_16x16x32_f16 v[248:251], v[164:167], v[224:227], v[248:251]\n"
      "v_fmac_f32 v219, v196, v196\n"
      "v_fmac_f32 v219, v197, v197\n"
      "v_mfma_f32_16x16x32_f16 v[248:251], v[204:207], v[32:35], v[248:251]\n"
      "v_fmac_f32 v218, v223, v223\n"
      "v_fmac_f32 v219, v198, v198\n"
      "v_fmac_f32 v218, v144, v144\n"
      "v_fmac_f32 v218, v145, v145\n"
      "v_fmac_f32 v219, v199, v199\n"
      "v_fmac_f32 v219, v140, v140\n"
      "v_fmac_f32 v219, v141, v141\n"
      "v_fmac_f32 v219, v142, v142\n"
      "v_fmac_f32 v218, v146, v146\n"
      "v_fmac_f32 v218, v147, v147\n"
      "v_fmac_f32 v219, v143, v143\n"
      "v_mov_b32 v217, v249\n"
      "v_mov_b32 v216, v248\n"
      "v_permlane16_swap_b32 v218, v219\n"
      "v_add_f32 v218, v218, v219\n"
      "v_mov_b32 v219, v218\n"
      "s_nop 1\n"
      "v_permlane32_swap_b32 v218, v219\n"
      "v_add_f32 v218, v218, v219\n"
      "v_rsq_f32 v218, v218\n"
      "s_nop 0\n"
      "v_mov_b32 v219, v218\n"
      "s_nop 1\n"
      "v_permlane16_swap_b32 v218, v219\n"
      "v_mul_f32 v172, v219, v172\n"
      "v_mul_f32 v175, v219, v175\n"
      "v_mul_f32 v173, v219, v173\n"
      "v_mul_f32 v197, v219, v197\n"
      "v_mul_f32 v174, v219, v174\n"
      "v_mul_f32 v141, v219, v141\n"
      "v_mul_f32 v140, v219, v140\n"
      "v_mul_f32 v196, v219, v196\n"
      "v_mul_f32 v218, s36, v218\n"
      "v_cvt_pk_f16_f32 v172, v172, v173\n"
      "v_cvt_pk_f16_f32 v173, v174, v175\n"
      "v_cvt_pk_f16_f32 v46, v140, v141\n"
      "v_mul_f32 v220, v218, v220\n"
      "v_mul_f32 v198, v219, v198\n"
      "v_mul_f32 v142, v219, v142\n"
      "v_mul_f32 v148, v218, v148\n"
      "v_mul_f32 v223, v218, v223\n"
      "v_mul_f32 v143, v219, v143\n"
      "v_mul_f32 v199, v219, v199\n"
      "v_cvt_pk_f16_f32 v47, v142, v143\n"
      "v_cvt_pk_f16_f32 v175, v198, v199\n"
      "v_mul_f32 v144, v218, v144\n"
      "v_mul_f32 v150, v218, v150\n"
      "v_cvt_pk_f16_f32 v174, v196, v197\n"
      "v_mul_f32 v222, v218, v222\n"
      "v_mul_f32 v221, v218, v221\n"
      "v_mul_f32 v151, v218, v151\n"
      "v_mfma_f32_16x16x32_f16 v[196:199], v[172:175], v[208:211], v[86:89]\n"
      "v_mul_f32 v149, v218, v149\n"
      "v_cvt_pk_f16_f32 v148, v148, v149\n"
      "v_mfma_f32_16x16x32_f16 v[196:199], v[46:49], v[32:35], v[196:199]\n"
      "v_cvt_pk_f16_f32 v149, v150, v151\n"
      "v_cvt_pk_f16_f32 v150, v220, v221\n"
      "v_mul_f32_e64 v147, v218, v147\n"
      "v_cvt_pk_f16_f32 v151, v222, v223\n"
      "v_mfma_f32_16x16x32_f16 v[220:223], v[224:227], v[208:211], v[78:81]\n"
      "v_mul_f32_e64 v145, v218, v145\n"
      "v_cvt_pk_f16_f32 v44, v144, v145\n"
      "v_mfma_f32_16x16x32_f16 v[220:223], v[34:37], v[32:35], v[220:223]\n"
      "v_mul_f32 v146, v218, v146\n"
      "v_cvt_pk_f16_f32 v45, v146, v147\n"
      "v_mfma_f32_16x16x32_f16 v[144:147], v[156:159], v[208:211], v[82:85]\n"
      "v_mfma_f32_16x16x32_f16 v[144:147], v[40:43], v[32:35], v[144:147]\n"
      "s_nop 7\n"
      "s_or_b64 vcc, s[48:49], s[50:51]\n"
      "s_cbranch_scc0 L_fast_22_%=\n"
      "L_ok_114_%=:\n"
      "s_cmp_eq_u64 s[48:49], 0\n"
      "s_cbranch_scc1 L_go_115_%=\n"
      "s_mov_b32 m0, s27\n"
      "s_nop 0\n"
      "s_movrels_b32 s9, s80\n"
      "s_lshr_b32 s8, s9, 8\n"
      "s_and_b32 s9, s9, 0xff\n"
      "s_mul_i32 s10, s8, 73\n"
      "s_add_u32 s10, s10, s9\n"
      "s_mul_i32 s30, s10, 4802\n"
      "s_add_u32 s30, s30, s18\n"
      "s_and_b32 s29, s30, 15\n"
      "s_add_u32 s30, s29, 115456\n"
      "s_lshl_b64 s[34:35], s[48:49], 4\n"
      "s_lshr_b64 s[34:35], s[34:35], 5\n"
      "s_cbranch_scc1 L_gen_20_%=\n"
      "v_mbcnt_lo_u32_b32 v218, -1, 0\n"
      "v_mbcnt_hi_u32_b32 v218, -1, v218\n"
      "v_lshlrev_b32 v219, 1, v218\n"
      "v_add_u32 v249, 4802, v219\n"
      "v_cmp_gt_u32 vcc, s29, v219\n"
      "s_nop 1\n"
      "v_cndmask_b32 v219, v249, v219, vcc\n"
      "v_mov_b32 v249, 0\n"
      "v_add_u32 v219, 115456, v219\n"
      "s_mov_b64 exec, 0x7f\n"
      "ds_write_b16 v219, v249\n"
      "s_mov_b64 exec, -1\n"
      "v_lshlrev_b32_e64 v219, 4, v218\n"
      "v_add_u32 v219, 115456, v219\n"
      "ds_read_b128 v[248:251], v219\n"
      "ds_read_b128 v[200:203], v219 offset:1024\n"
      "v_min_u32 v218, 116160, v219\n"
      "ds_read_b128 v[228:231], v219 offset:2048\n"
      "ds_read_b128 v[168:171], v218 offset:4096\n"
      "ds_read_b128 v[252:255], v219 offset:3072\n"
      "s_waitcnt lgkmcnt(4)\n"
      "v_or3_b32 v248, v248, v249, v250\n"
      "s_waitcnt lgkmcnt(3)\n"
      "v_or3_b32 v248, v248, v251, v200\n"
      "v_or3_b32 v248, v248, v201, v202\n"
      "s_waitcnt lgkmcnt(2)\n"
      "v_or3_b32 v248, v248, v203, v228\n"
      "v_or3_b32 v248, v248, v229, v230\n"
      "s_waitcnt lgkmcnt(0)\n"
      "v_or3_b32 v248, v248, v231, v252\n"
      "v_or3_b32 v248, v248, v253, v254\n"
      "v_or3_b32 v248, v248, v255, v168\n"
      "v_or3_b32 v248, v248, v169, v170\n"
      "v_bitop3_b32 v248, v248, v171, s40 bitop3:0xa8\n"
      "v_cmp_ne_u32 s[48:49], 0, v248\n"
      "s_cmp_lg_u64 s[48:49], 0\n"
      "s_cbranch_scc1 L_gen_20_%=\n"
      "s_cmp_eq_u64 s[50:51], 0\n"
      "s_cbranch_scc1 L_fast_22_%=\n"
      "L_go_115_%=:\n"
      "v_mfma_f32_16x16x32_f16 v[248:251], v[224:227], v[212:215], v[90:93]\n"
      "ds_read_b128 v[200:203], v128 offset:2048\n"
      "v_max3_f32 v219, v220, v221, v222\n"
      "v_mfma_f32_16x16x32_f16 v[248:251], v[34:37], v[38:41], v[248:251]\n"
      "v_max3_f32 v218, v223, v144, v145\n"
      "v_max3_f32 v218, v218, v196, v197\n"
      "v_mfma_f32_16x16x32_f16 v[228:231], v[156:159], v[212:215], v[94:97]\n"
      "v_max3_f32 v219, v219, v146, v147\n"
      "v_max3_f32 v219, v219, v198, v199\n"
      "v_mfma_f32_16x16x32_f16 v[228:231], v[40:43], v[38:41], v[228:231]\n"
      "v_max3_f32 v219, v219, v218, v216\n"
      "v_mov_b32 v218, v219\n"
      "v_mfma_f32_16x16x32_f16 v[168:171], v[136:139], v[212:215], v[72:75]\n"
      "s_nop 0\n"
      "v_permlane16_swap_b32 v219, v218\n"
      "v_mfma_f32_16x16x32_f16 v[168:171], v[164:167], v[156:159], v[168:171]\n"
      "v_max_f32 v219, v219, v218\n"
      "v_mov_b32 v218, v219\n"
      "v_mfma_f32_16x16x32_f16 v[168:171], v[204:207], v[38:41], v[168:171]\n"
      "s_nop 0\n"
      "v_permlane32_swap_b32 v219, v218\n"
      "v_max_f32 v219, v219, v218\n"
      "v_sub_f32 v222, v222, v219\n"
      "v_sub_f32 v220, v220, v219\n"
      "v_sub_f32 v221, v221, v219\n"
      "v_exp_f32 v222, v222\n"
      "v_sub_f32 v147, v147, v219\n"
      "v_sub_f32 v197, v197, v219\n"
      "v_sub_f32 v146, v146, v219\n"
      "v_sub_f32 v223, v223, v219\n"
      "v_exp_f32 v147, v147\n"
      "v_mfma_f32_16x16x32_f16 v[252:255], v[172:175], v[212:215], v[98:101]\n"
      "v_sub_f32 v216, v216, v219\n"
      "v_sub_f32_e64 v198, v198, v219\n"
      "v_mfma_f32_16x16x32_f16 v[252:255], v[46:49], v[38:41], v[252:255]\n"
      "s_waitcnt lgkmcnt(0)\n"
      "v_sub_f32 v145, v145, v219\n"
      "v_exp_f32 v221, v221\n"
      "v_exp_f32 v146, v146\n"
      "v_exp_f32 v220, v220\n"
      "v_exp_f32 v145, v145\n"
      "v_sub_f32 v199, v199, v219\n"
      "v_exp_f32 v216, v216\n"
      "v_exp_f32 v199, v199\n"
      "v_cvt_pk_bf16_f32 v220, v220, v221\n"
      "v_exp_f32 v223, v223\n"
      "v_exp_f32 v198, v198\n"
      "v_sub_f32 v144, v144, v219\n"
      "v_cvt_pk_bf16_f32 v221, v222, v223\n"
      "v_cvt_pk_bf16_f32 v63, v198, v199\n"
      "v_bitop3_b32 v202, v202, v203, s4 bitop3:0x7e\n"
      "v_sub_f32 v196, v196, v219\n"
      "v_exp_f32 v197, v197\n"
      "v_exp_f32 v196, v196\n"
      "v_exp_f32 v144, v144\n"
      "v_cvt_pk_bf16_f32 v64, v216, 0\n"
      "v_cvt_pk_bf16_f32 v222, v144, v145\n"
      "v_cvt_pk_bf16_f32 v223, v146, v147\n"
      "v_cvt_pk_bf16_f32 v62, v196, v197\n"
      "v_bitop3_b32 v200, v200, v201, s4 bitop3:0x7e\n"
      "v_mfma_f32_16x16x32_bf16 v[144:147], v[66:69], v[220:223], 0\n"
      "v_or_b32 v200, v200, v202\n"
      "v_cmp_ne_u32 vcc, 0, v200\n"
      "v_mfma_f32_16x16x32_bf16 v[144:147], v[66:69], v[62:65], v[144:147]\n"
      "s_mov_b64 s[48:49], vcc\n"
      "v_max3_f32 v171, v248, v249, v250\n"
      "v_max3_f32 v170, v251, v228, v229\n"
      "v_mfma_f32_16x16x32_bf16 v[212:215], v[192:195], v[220:223], 0\n"
      "s_cbranch_vccnz L_flg_116_%=\n"
      "L_fb_117_%=:\n"
      "v_mfma_f32_16x16x32_bf16 v[212:215], v[50:53], v[62:65], v[212:215]\n"
      "v_max3_f32 v171, v171, v230, v231\n"
      "v_max3_f32 v170, v170, v252, v253\n"
      "v_mfma_f32_16x16x32_bf16 v[200:203], v[244:247], v[220:223], 0\n"
      "v_max3_f32 v171, v171, v254, v255\n"
      "v_max3_f32 v171, v171, v170, v168\n"
      "v_mfma_f32_16x16x32_bf16 v[200:203], v[54:57], v[62:65], v[200:203]\n"
      "v_mov_b32 v170, v171\n"
      "v_rcp_f32 v144, v144\n"
      "v_mfma_f32_16x16x32_bf16 v[196:199], v[160:163], v[220:223], 0\n"
      "v_permlane16_swap_b32 v171, v170\n"
      "v_mfma_f32_16x16x32_bf16 v[196:199], v[58:61], v[62:65], v[196:199]\n"
      "v_max_f32 v171, v171, v170\n"
      "v_mov_b32 v170, v171\n"
      "v_mul_f32 v212, v144, v212\n"
      "v_mul_f32 v213, v144, v213\n"
      "v_permlane32_swap_b32 v171, v170\n"
      "v_max_f32 v171, v171, v170\n"
      "v_sub_f32 v250, v250, v171\n"
      "v_sub_f32 v251, v251, v171\n"
      "v_sub_f32 v248, v248, v171\n"
      "v_mfma_f32_16x16x32_f16 v[220:223], v[224:227], v[148:151], v[102:105]\n"
      "v_sub_f32 v254, v254, v171\n"
      "v_sub_f32 v255, v255, v171\n"
      "v_mfma_f32_16x16x32_f16 v[220:223], v[34:37], v[44:47], v[220:223]\n"
      "v_exp_f32 v248, v248\n"
      "v_exp_f32 v250, v250\n"
      "v_sub_f32 v249, v249, v171\n"
      "v_sub_f32 v252, v252, v171\n"
      "v_exp_f32 v254, v254\n"
      "v_sub_f32 v230, v230, v171\n"
      "v_sub_f32 v253, v253, v171\n"
      "v_sub_f32 v168, v168, v171\n"
      "v_exp_f32 v249, v249\n"
      "v_sub_f32 v229, v229, v171\n"
      "v_exp_f32 v230, v230\n"
      "v_exp_f32 v255, v255\n"
      "v_sub_f32_e64 v228, v228, v171\n"
      "v_mfma_f32_16x16x32_f16 v[224:227], v[156:159], v[148:151], v[106:109]\n"
      "ds_read_b128 v[156:159], v120\n"
      "v_cvt_pk_bf16_f32 v248, v248, v249\n"
      "v_mfma_f32_16x16x32_f16 v[224:227], v[40:43], v[44:47], v[224:227]\n"
      "v_exp_f32 v251, v251\n"
      "v_sub_f32 v231, v231, v171\n"
      "v_exp_f32_e64 v253, v253\n"
      "v_mfma_f32_16x16x32_f16 v[208:211], v[136:139], v[148:151], v[74:77]\n"
      "v_cvt_pk_bf16_f32 v212, v212, v213\n"
      "v_cvt_pk_bf16_f32 v249, v250, v251\n"
      "v_mfma_f32_16x16x32_f16 v[208:211], v[164:167], v[172:175], v[208:211]\n"
      "v_exp_f32 v229, v229\n"
      "v_mfma_f32_16x16x32_f16 v[208:211], v[204:207], v[44:47], v[208:211]\n"
      "v_mul_f32 v199, v144, v199\n"
      "v_exp_f32 v228, v228\n"
      "v_mfma_f32_16x16x32_f16 v[140:143], v[172:175], v[148:151], v[110:113]\n"
      "v_exp_f32 v252, v252\n"
      "v_mfma_f32_16x16x32_f16 v[140:143], v[46:49], v[44:47], v[140:143]\n"
      "v_exp_f32 v231, v231\n"
      "v_mul_f32 v214, v144, v214\n"
      "v_cvt_pk_bf16_f32 v251, v230, v231\n"
      "v_cvt_pk_bf16_f32 v250, v228, v229\n"
      "v_mfma_f32_16x16x32_f16 v[228:231], v[164:167], v[136:139], v[76:79]\n"
      "s_waitcnt lgkmcnt(0)\n"
      "v_cvt_pk_bf16_f32 v62, v252, v253\n"
      "v_mul_f32 v215, v144, v215\n"
      "v_max3_f32 v211, v223, v224, v225\n"
      "v_cvt_pk_bf16_f32 v63, v254, v255\n"
      "v_mul_f32 v201, v144, v201\n"
      "v_exp_f32 v168, v168\n"
      "v_mfma_f32_16x16x16_f16 v[228:231], v[206:207], v[204:205], v[228:231]\n"
      "buffer_store_dwordx4 v[156:159], v117, s[20:23], 0 offen offset:0 sc1\n"
      "v_cvt_pk_bf16_f32 v64, v168, 0\n"
      "v_mfma_f32_16x16x32_bf16 v[252:255], v[66:69], v[248:251], 0\n"
      "v_mul_f32_e64 v200, v144, v200\n"
      "v_cvt_pk_bf16_f32 v213, v214, v215\n"
      "v_mfma_f32_16x16x32_bf16 v[252:255], v[66:69], v[62:65], v[252:255]\n"
      "v_mul_f32 v196, v144, v196\n"
      "v_max3_f32 v211, v211, v140, v141\n"
      "v_mfma_f32_16x16x32_bf16 v[204:207], v[192:195], v[248:251], 0\n"
      "v_mul_f32 v203, v144, v203\n"
      "v_mul_f32_e64 v202, v144, v202\n"
      "v_mfma_f32_16x16x32_bf16 v[204:207], v[50:53], v[62:65], v[204:207]\n"
      "v_cvt_pk_bf16_f32 v215, v202, v203\n"
      "v_cvt_pk_bf16_f32 v214, v200, v201\n"
      "v_max3_f32 v210, v220, v221, v222\n"
      "v_max3_f32 v210, v210, v226, v227\n"
      "v_mfma_f32_16x16x32_bf16 v[148:151], v[244:247], v[248:251], 0\n"
      "v_mul_f32_e64 v197, v144, v197\n"
      "v_cvt_pk_bf16_f32 v196, v196, v197\n"
      "v_mfma_f32_16x16x32_bf16 v[148:151], v[54:57], v[62:65], v[148:151]\n"
      "v_max3_f32 v210, v210, v142, v143\n"
      "v_max3_f32 v210, v210, v211, v208\n"
      "v_mov_b32 v211, v210\n"
      "v_mfma_f32_16x16x32_bf16 v[200:203], v[160:163], v[248:251], 0\n"
      "ds_write_b128 v124, v[212:215] offset:18432\n"
      "v_mfma_f32_16x16x32_bf16 v[200:203], v[58:61], v[62:65], v[200:203]\n"
      "v_rcp_f32 v252, v252\n"
      "v_permlane16_swap_b32 v210, v211\n"
      "v_max_f32 v210, v210, v211\n"
      "v_mov_b32 v211, v210\n"
      "v_mul_f32 v205, v252, v205\n"
      "v_mul_f32 v151, v252, v151\n"
      "v_permlane32_swap_b32 v210, v211\n"
      "v_max_f32 v210, v210, v211\n"
      "v_sub_f32 v225, v225, v210\n"
      "v_sub_f32 v221, v221, v210\n"
      "v_mul_f32 v204, v252, v204\n"
      "v_sub_f32 v222, v222, v210\n"
      "v_sub_f32 v143, v143, v210\n"
      "v_sub_f32 v220, v220, v210\n"
      "v_sub_f32 v208, v208, v210\n"
      "v_sub_f32 v141, v141, v210\n"
      "v_sub_f32 v223, v223, v210\n"
      "v_mul_f32 v207, v252, v207\n"
      "v_exp_f32 v221, v221\n"
      "v_exp_f32 v141, v141\n"
      "v_exp_f32 v220, v220\n"
      "v_sub_f32 v224, v224, v210\n"
      "v_exp_f32 v222, v222\n"
      "v_sub_f32 v226, v226, v210\n"
      "v_exp_f32 v223, v223\n"
      "v_exp_f32 v225, v225\n"
      "v_sub_f32 v140, v140, v210\n"
      "v_exp_f32 v226, v226\n"
      "v_mul_f32 v206, v252, v206\n"
      "v_cvt_pk_bf16_f32 v220, v220, v221\n"
      "v_sub_f32 v142, v142, v210\n"
      "v_exp_f32 v140, v140\n"
      "v_sub_f32 v227, v227, v210\n"
      "v_mul_f32 v149, v252, v149\n"
      "v_exp_f32 v227, v227\n"
      "v_exp_f32 v143, v143\n"
      "v_cvt_pk_bf16_f32 v221, v222, v223\n"
      "v_exp_f32 v224, v224\n"
      "v_exp_f32 v142, v142\n"
      "v_exp_f32_e64 v208, v208\n"
      "v_cvt_pk_bf16_f32 v62, v140, v141\n"
      "v_cvt_pk_bf16_f32 v222, v224, v225\n"
      "v_cvt_pk_bf16_f32 v223, v226, v227\n"
      "v_cvt_pk_bf16_f32 v64, v208, 0\n"
      "v_mul_f32 v198, v144, v198\n"
      "v_cvt_pk_bf16_f32 v63, v142, v143\n"
      "v_mul_f32 v203, v252, v203\n"
      "v_mfma_f32_16x16x32_bf16 v[156:159], v[66:69], v[220:223], 0\n"
      "ds_read_b128 v[212:215], v121\n"
      "v_cvt_pk_bf16_f32 v204, v204, v205\n"
      "v_mfma_f32_16x16x32_bf16 v[156:159], v[66:69], v[62:65], v[156:159]\n"
      "ds_read_b128 v[136:139], v123 offset:4096\n"
      "v_mul_f32_e64 v148, v252, v148\n"
      "v_cvt_pk_bf16_f32 v205, v206, v207\n"
      "v_cvt_pk_bf16_f32 v206, v148, v149\n"
      "v_mfma_f32_16x16x32_bf16 v[224:227], v[192:195], v[220:223], 0\n"
      "ds_read_b128 v[140:143], v123 offset:1024\n"
      "v_cvt_pk_bf16_f32 v197, v198, v199\n"
      "v_mfma_f32_16x16x32_bf16 v[164:167], v[244:247], v[220:223], 0\n"
      "v_mul_f32_e64 v150, v252, v150\n"
      "v_cvt_pk_bf16_f32 v207, v150, v151\n"
      "v_mfma_f32_16x16x32_bf16 v[164:167], v[54:57], v[62:65], v[164:167]\n"
      "v_cndmask_b32 v217, v217, v169, s[42:43]\n"
      "v_mul_f32_e64 v200, v252, v200\n"
      "v_mfma_f32_16x16x32_bf16 v[148:151], v[160:163], v[220:223], 0\n"
      "ds_read_b128 v[220:223], v123 offset:7168\n"
      "v_mul_f32_e64 v202, v252, v202\n"
      "v_cndmask_b32 v217, v217, v209, s[44:45]\n"
      "v_mul_f32_e64 v201, v252, v201\n"
      "v_cndmask_b32 v217, v217, v229, s[46:47]\n"
      "v_mfma_f32_16x16x32_bf16 v[224:227], v[50:53], v[62:65], v[224:227]\n"
      "ds_write_b128 v124, v[204:207] offset:19456\n"
      "v_max_f32_dpp v198, v217, v217 row_ror:8 row_mask:0xf bank_mask:0xf\n"
      "v_mfma_f32_16x16x32_bf16 v[148:151], v[58:61], v[62:65], v[148:151]\n"
      "v_cvt_pk_bf16_f32 v200, v200, v201\n"
      "v_max_f32_dpp v198, v198, v198 row_ror:4 row_mask:0xf bank_mask:0xf\n"
      "v_cvt_pk_bf16_f32 v201, v202, v203\n"
      "s_nop 0\n"
      "v_max_f32_dpp v198, v198, v198 row_ror:2 row_mask:0xf bank_mask:0xf\n"
      "s_waitcnt lgkmcnt(2)\n"
      "s_nop 0\n"
      "v_max_f32_dpp v198, v198, v198 row_ror:1 row_mask:0xf bank_mask:0xf\n"
      "v_mfma_f32_16x16x32_bf16 v[248:251], a[216:219], v[140:143], 0\n"
      "ds_read_b128 v[228:231], v123 offset:10240\n"
      "v_max_f32_dpp v198, v198, v198 row_bcast:15 row_mask:0xa bank_mask:0xf\n"
      "v_mfma_f32_16x16x32_bf16 v[248:251], a[228:231], v[136:139], v[248:251]\n"
      "ds_write2st64_b64 v125, v[196:197], v[200:201] offset0:36 offset1:38\n"
      "v_mfma_f32_16x16x32_bf16 v[144:147], a[220:223], v[140:143], 0\n"
      "ds_read_b128 v[252:255], v123 offset:13312\n"
      "v_max_f32_dpp v198, v198, v198 row_bcast:31 row_mask:0xc bank_mask:0xf\n"
      "v_mfma_f32_16x16x32_bf16 v[144:147], a[232:235], v[136:139], v[144:147]\n"
      "s_mov_b32 exec_hi, 0\n"
      "buffer_store_dwordx4 v[212:215], v118, s[20:23], 0 offen offset:0 sc1\n"
      "s_mov_b32 exec_hi, -1\n"
      "s_add_u32 s28, s27, 2\n"
      "v_mfma_f32_16x16x32_bf16 v[200:203], a[224:227], v[140:143], 0\n"
      "ds_read_b128 v[168:171], v123 offset:16384\n"
      "v_readlane_b32 s31, v198, 63\n"
      "v_mfma_f32_16x16x32_bf16 v[200:203], a[236:239], v[136:139], v[200:203]\n"
      "ds_read_b128 v[136:139], v123 offset:2048\n"
      "v_subrev_f32 v217, s31, v217\n"
      "s_min_u32 m0, s28, s3\n"
      "v_exp_f32 v217, v217\n"
      "s_waitcnt lgkmcnt(4)\n"
      "v_mfma_f32_16x16x32_bf16 v[248:251], a[240:243], v[220:223], v[248:251]\n"
      "v_cvt_pk_bf16_f32 v217, v217, v217\n"
      "s_movrels_b32 s9, s80\n"
      "v_mfma_f32_16x16x32_bf16 v[248:251], a[252:255], v[228:231], v[248:251]\n"
      "ds_write_b16 v130, v217\n"
      "v_mfma_f32_16x16x32_bf16 v[144:147], a[244:247], v[220:223], v[144:147]\n"
      "s_lshr_b32 s8, s9, 8\n"
      "s_and_b32 s9, s9, 0xff\n"
      "s_mul_i32 s10, s8, 73\n"
      "v_mfma_f32_16x16x32_bf16 v[144:147], v[0:3], v[228:231], v[144:147]\n"
      "s_add_u32 s10, s10, s9\n"
      "s_mul_i32 s28, s10, 4802\n"
      "v_mfma_f32_16x16x32_bf16 v[200:203], a[248:251], v[220:223], v[200:203]\n"
      "ds_read_b128 v[204:207], v123 offset:5120\n"
      "s_add_u32 s56, s18, s28\n"
      "v_mfma_f32_16x16x32_bf16 v[200:203], v[4:7], v[228:231], v[200:203]\n"
      "s_waitcnt lgkmcnt(3)\n"
      "ds_read_b128 v[220:223], v131\n"
      "v_mfma_f32_16x16x32_bf16 v[248:251], v[8:11], v[252:255], v[248:251]\n"
      "s_addc_u32 s57, s19, 0\n"
      "s_and_b32 s56, s56, -16\n"
      "s_sub_u32 s58, s56, 110592\n"
      "v_mfma_f32_16x16x32_bf16 v[248:251], v[20:23], v[168:171], v[248:251]\n"
      "s_subb_u32 s59, s57, 0\n"
      "s_sub_u32 s56, s56, 73728\n"
      "s_subb_u32 s57, s57, 0\n"
      "v_mfma_f32_16x16x32_bf16 v[144:147], v[12:15], v[252:255], v[144:147]\n"
      "s_mul_i32 s28, s8, 1373568\n"
      "s_mul_i32 s29, s9, 2688\n"
      "s_add_u32 s28, s28, s29\n"
      "s_cmp_eq_u32 s8, 72\n"
      "v_mfma_f32_16x16x32_bf16 v[200:203], v[16:19], v[252:255], v[200:203]\n"
      "ds_read_b128 v[140:143], v131 offset:64\n"
      "s_cselect_b32 s8, 0, 100270464\n"
      "v_mfma_f32_16x16x32_bf16 v[200:203], v[28:31], v[168:171], v[200:203]\n"
      "s_cmp_eq_u32 s9, 72\n"
      "ds_read_b128 v[208:211], v123 offset:8192\n"
      "v_mfma_f32_16x16x32_bf16 v[144:147], v[24:27], v[168:171], v[144:147]\n"
      "s_cselect_b32 s9, 0, 196224\n"
      "s_add_u32 s10, s8, s9\n"
      "s_add_u32 s28, s28, s10\n"
      "s_add_u32 s52, s16, s28\n"
      "s_waitcnt lgkmcnt(3)\n"
      "v_mfma_f32_16x16x32_bf16 v[228:231], a[216:219], v[136:139], 0\n"
      "ds_read_b128 v[196:199], v123 offset:11264\n"
      "s_addc_u32 s53, s17, 0\n"
      "s_add_u32 s27, s27, 1\n"
      "v_mfma_f32_16x16x32_bf16 v[168:171], a[220:223], v[136:139], 0\n"
      "s_cmp_ge_u32 s27, s26\n"
      "ds_read_b128 v[212:215], v114\n"
      "v_mfma_f32_16x16x32_bf16 v[168:171], a[232:235], v[204:207], v[168:171]\n"
      "ds_read_b128 v[172:175], v123 offset:17408\n"
      "s_cselect_b32 s28, 1, 0\n"
      "v_mfma_f32_16x16x32_bf16 v[252:255], a[224:227], v[136:139], 0\n"
      "ds_read_b128 v[216:219], v123 offset:14336\n"
      "v_mfma_f32_16x16x32_bf16 v[252:255], a[236:239], v[204:207], v[252:255]\n"
      "s_xor_b32 s29, s10, s11\n"
      "v_fma_f32 v136, v248, v248, s39\n"
      "v_fmac_f32 v136, v249, v249\n"
      "v_fmac_f32 v136, v250, v250\n"
      "v_fmac_f32 v136, v251, v251\n"
      "v_fmac_f32 v136, v144, v144\n"
      "v_fmac_f32 v136, v145, v145\n"
      "v_fmac_f32 v136, v146, v146\n"
      "v_mfma_f32_16x16x32_bf16 v[228:231], a[228:231], v[204:207], v[228:231]\n"
      "s_or_b32 s28, s28, s29\n"
      "v_fmac_f32 v136, v147, v147\n"
      "v_fmac_f32 v136, v200, v200\n"
      "v_fmac_f32 v136, v201, v201\n"
      "v_fmac_f32 v136, v202, v202\n"
      "v_fmac_f32 v136, v203, v203\n"
      "s_waitcnt lgkmcnt(3)\n"
      "v_mfma_f32_16x16x32_bf16 v[252:255], a[248:251], v[208:211], v[252:255]\n"
      "ds_read_b128 v[204:207], v114 offset:6144\n"
      "v_mfma_f32_16x16x32_bf16 v[252:255], v[4:7], v[196:199], v[252:255]\n"
      "v_permlane16_swap_b32_e64 v180, v136\n"
      "v_mfma_f32_16x16x32_bf16 v[228:231], a[240:243], v[208:211], v[228:231]\n"
      "v_mfma_f32_16x16x32_bf16 v[228:231], a[252:255], v[196:199], v[228:231]\n"
      "v_add_f32 v180, v180, v136\n"
      "v_mfma_f32_16x16x32_bf16 v[168:171], a[244:247], v[208:211], v[168:171]\n"
      "s_waitcnt lgkmcnt(1)\n"
      "v_mfma_f32_16x16x32_bf16 v[228:231], v[8:11], v[216:219], v[228:231]\n"
      "ds_read_b128 v[136:139], v115 offset:6144\n"
      "v_mfma_f32_16x16x32_bf16 v[228:231], v[20:23], v[172:175], v[228:231]\n"
      "v_mfma_f32_16x16x32_bf16 v[168:171], v[0:3], v[196:199], v[168:171]\n"
      "ds_read_b128 v[208:211], v115\n"
      "v_mfma_f32_16x16x32_bf16 v[168:171], v[12:15], v[216:219], v[168:171]\n"
      "v_mfma_f32_16x16x32_bf16 v[168:171], v[24:27], v[172:175], v[168:171]\n"
      "v_mfma_f32_16x16x32_bf16 v[252:255], v[16:19], v[216:219], v[252:255]\n"
      "v_mfma_f32_16x16x32_bf16 v[252:255], v[28:31], v[172:175], v[252:255]\n"
      "v_mfma_f32_16x16x32_bf16 v[216:219], v[220:223], v[66:69], 0\n"
      "s_nop 0\n"
      "v_fma_f32 v196, v228, v228, s39\n"
      "v_mfma_f32_16x16x32_bf16 v[216:219], v[140:143], v[66:69], v[216:219]\n"
      "v_fmac_f32 v196, v229, v229\n"
      "v_fmac_f32 v196, v230, v230\n"
      "v_mfma_f32_16x16x32_bf16 v[172:175], v[220:223], v[192:195], 0\n"
      "v_fmac_f32 v196, v231, v231\n"
      "v_fmac_f32 v196, v168, v168\n"
      "v_mfma_f32_16x16x32_bf16 v[172:175], v[140:143], v[50:53], v[172:175]\n"
      "v_fmac_f32 v196, v169, v169\n"
      "v_fmac_f32 v196, v170, v170\n"
      "v_mfma_f32_16x16x32_bf16 v[236:239], v[220:223], v[244:247], 0\n"
      "v_fmac_f32 v196, v171, v171\n"
      "v_fmac_f32 v196, v252, v252\n"
      "v_mfma_f32_16x16x32_bf16 v[236:239], v[140:143], v[54:57], v[236:239]\n"
      "s_waitcnt lgkmcnt(0)\n"
      "v_fmac_f32 v196, v253, v253\n"
      "v_fmac_f32 v196, v254, v254\n"
      "v_mfma_f32_16x16x32_bf16 v[244:247], v[220:223], v[160:163], 0\n"
      "v_fmac_f32 v196, v255, v255\n"
      "v_mov_b32 v217, v196\n"
      "v_mfma_f32_16x16x32_bf16 v[244:247], v[140:143], v[58:61], v[244:247]\n"
      "v_mov_b32_dpp v216, v216 quad_perm:[0,0,0,0] row_mask:0xf bank_mask:0xf\n"
      "v_cndmask_b32 v172, v172, v236, s[42:43]\n"
      "v_permlane16_swap_b32 v196, v217\n"
      "v_mfma_f32_16x16x32_bf16 v[220:223], a[8:11], v[212:215], 0\n"
      "v_add_f32 v196, v196, v217\n"
      "v_rcp_f32 v216, v216\n"
      "v_mfma_f32_16x16x32_bf16 v[140:143], a[4:7], v[212:215], 0\n"
      "s_nop 0\n"
      "v_cndmask_b32 v172, v172, v244, s[44:45]\n"
      "v_mul_f32_e64 v172, v216, v172\n"
      "v_mfma_f32_16x16x32_bf16 v[216:219], a[0:3], v[212:215], 0\n"
      "v_permlane32_swap_b32 v180, v196\n"
      "v_mfma_f32_16x16x32_bf16 v[216:219], a[36:39], v[208:211], v[216:219]\n"
      "v_mfma_f32_16x16x32_bf16 v[140:143], a[40:43], v[208:211], v[140:143]\n"
      "v_add_f32 v180, v180, v196\n"
      "v_cvt_pk_bf16_f32 v172, v172, v172\n"
      "v_mfma_f32_16x16x32_bf16 v[196:199], a[12:15], v[212:215], 0\n"
      "v_mfma_f32_16x16x32_bf16 v[196:199], a[48:51], v[208:211], v[196:199]\n"
      "ds_write_b32 v129, v180 offset:1024\n"
      "v_mfma_f32_16x16x32_bf16 v[236:239], a[16:19], v[212:215], 0\n"
      "v_mfma_f32_16x16x32_bf16 v[236:239], a[52:55], v[208:211], v[236:239]\n"
      "ds_write_b16 v135, v172 offset:16\n"
      "v_mfma_f32_16x16x32_bf16 v[220:223], a[44:47], v[208:211], v[220:223]\n"
      "v_mfma_f32_16x16x32_bf16 v[172:175], a[20:23], v[212:215], 0\n"
      "s_nop 0\n"
      "v_mfma_f32_16x16x32_bf16 v[172:175], a[56:59], v[208:211], v[172:175]\n"
      "s_branch L_join_21_%=\n"
      "L_flg_116_%=:\n"
      "v_cmp_gt_i32 vcc, 0, v200\n"
      "s_cbranch_vccz L_fb_117_%=\n"
      "s_sleep 1\n"
      "ds_read_b128 v[200:203], v128 offset:2048\n"
      "s_waitcnt lgkmcnt(0)\n"
      "v_bitop3_b32 v200, v200, v201, s4 bitop3:0x7e\n"
      "v_bitop3_b32 v202, v202, v203, s4 bitop3:0x7e\n"
      "v_or_b32 v200, v200, v202\n"
      "v_cmp_ne_u32 s[48:49], 0, v200\n"
      "s_branch L_flg_116_%=\n"
      "L_gen_20_%=:\n"
      "s_nop 0\n"
      "s_nop 0\n"
      "v_mbcnt_lo_u32_b32 v218, -1, 0\n"
      "v_mbcnt_hi_u32_b32 v218, -1, v218\n"
      "v_and_b32 v248, 15, v218\n"
      "v_lshrrev_b32 v219, 4, v218\n"
      "v_mul_u32_u24 v249, 0x62, v248\n"
      "v_lshl_add_u32 v249, v219, 3, v249\n"
      "v_lshlrev_b32 v219, 2, v219\n"
      "s_movk_i32 s31, 0x3fb9\n"
      "v_sub_u32 v248, v248, v219\n"
      "v_add_u32 v249, s30, v249\n"
      "v_and_b32 v218, -4, v249\n"
      "v_and_b32 v219, 1, v248\n"
      "v_lshrrev_b32 v248, 1, v248\n"
      "v_cmp_eq_u32 vcc, 0, v248\n"
      "v_lshlrev_b32_e64 v219, 4, v219\n"
      "ds_read2_b32 v[228:229], v218 offset0:2 offset1:10\n"
      "ds_read2_b32 v[250:251], v218 offset0:8 offset1:9\n"
      "ds_read2_b32 v[202:203], v218 offset0:0 offset1:1\n"
      "ds_read2_b32 v[230:231], v218 offset0:18 offset1:24\n"
      "ds_read2_b32 v[200:201], v218 offset0:16 offset1:17\n"
      "v_add_u32 v218, 1568, v218\n"
      "v_lshlrev_b32 v219, v219, s31\n"
      "ds_read2_b32 v[170:171], v218 offset0:8 offset1:9\n"
      "ds_read2_b32 v[208:209], v218 offset0:18 offset1:24\n"
      "ds_read2_b32 v[168:169], v218 offset0:0 offset1:1\n"
      "ds_read2_b32 v[254:255], v218 offset0:2 offset1:10\n"
      "ds_read2_b32 v[252:253], v218 offset0:16 offset1:17\n"
      "v_and_b32_e64 v249, 2, v249\n"
      "v_add_u32 v218, 1568, v218\n"
      "ds_read2_b32 v[210:211], v218 offset0:0 offset1:1\n"
      "ds_read2_b32 v[140:141], v218 offset0:8 offset1:9\n"
      "ds_read2_b32 v[236:237], v218 offset0:2 offset1:10\n"
      "ds_read2_b32 v[238:239], v218 offset0:18 offset1:24\n"
      "v_cndmask_b32 v182, 0, v219, vcc\n"
      "v_cmp_eq_u32 vcc, 1, v248\n"
      "s_waitcnt lgkmcnt(12)\n"
      "ds_read2_b32 v[142:143], v218 offset0:16 offset1:17\n"
      "v_alignbyte_b32 v250, v251, v250, v249\n"
      "v_mfma_f32_16x16x32_f16 v[176:179], v[224:227], v[212:215], v[90:93]\n"
      "s_waitcnt lgkmcnt(10)\n"
      "v_cndmask_b32 v183, 0, v219, vcc\n"
      "v_alignbyte_b32 v202, v203, v202, v249\n"
      "v_mfma_f32_16x16x32_f16 v[176:179], v[34:37], v[38:41], v[176:179]\n"
      "s_nop 0\n"
      "v_alignbyte_b32 v251, v229, v251, v249\n"
      "v_alignbyte_b32 v203, v228, v203, v249\n"
      "v_mfma_f32_16x16x32_f16 v[184:187], v[156:159], v[212:215], v[94:97]\n"
      "v_alignbyte_b32 v200, v201, v200, v249\n"
      "v_alignbyte_b32 v201, v230, v201, v249\n"
      "v_mfma_f32_16x16x32_f16 v[184:187], v[40:43], v[38:41], v[184:187]\n"
      "s_waitcnt lgkmcnt(6)\n"
      "v_alignbyte_b32 v231, v231, v231, v249\n"
      "v_lshlrev_b32 v231, 16, v231\n"
      "v_mfma_f32_16x16x32_f16 v[232:235], v[136:139], v[212:215], v[72:75]\n"
      "v_fmac_f32 v216, s37, v231\n"
      "v_alignbyte_b32 v168, v169, v168, v249\n"
      "v_mfma_f32_16x16x32_f16 v[232:235], v[164:167], v[156:159], v[232:235]\n"
      "s_waitcnt lgkmcnt(4)\n"
      "v_alignbyte_b32 v170, v171, v170, v249\n"
      "v_alignbyte_b32 v209, v209, v209, v249\n"
      "v_mfma_f32_16x16x32_f16 v[232:235], v[204:207], v[38:41], v[232:235]\n"
      "s_waitcnt lgkmcnt(2)\n"
      "v_alignbyte_b32 v252, v253, v252, v249\n"
      "v_alignbyte_b32 v171, v255, v171, v249\n"
      "v_mfma_f32_16x16x32_f16 v[228:231], v[172:175], v[212:215], v[98:101]\n"
      "v_alignbyte_b32 v253, v208, v253, v249\n"
      "v_lshlrev_b32 v209, 16, v209\n"
      "v_mfma_f32_16x16x32_f16 v[228:231], v[46:49], v[38:41], v[228:231]\n"
      "v_alignbyte_b32 v140, v141, v140, v249\n"
      "v_alignbyte_b32 v210, v211, v210, v249\n"
      "v_mfma_f32_16x16x16_bf16 v[220:223], v[182:183], v[202:203], v[220:223]\n"
      "v_fmac_f32 v232, s37, v209\n"
      "s_waitcnt lgkmcnt(0)\n"
      "v_alignbyte_b32 v142, v143, v142, v249\n"
      "v_mfma_f32_16x16x16_bf16 v[144:147], v[182:183], v[250:251], v[144:147]\n"
      "v_alignbyte_b32 v169, v254, v169, v249\n"
      "v_alignbyte_b32 v211, v236, v211, v249\n"
      "v_mfma_f32_16x16x16_bf16 v[196:199], v[182:183], v[200:201], v[196:199]\n"
      "ds_read_b128 v[200:203], v128 offset:2048\n"
      "v_max3_f32 v219, v220, v221, v222\n"
      "v_mfma_f32_16x16x16_bf16 v[176:179], v[182:183], v[168:169], v[176:179]\n"
      "s_nop 1\n"
      "v_max3_f32 v219, v219, v146, v147\n"
      "v_max3_f32 v209, v223, v144, v145\n"
      "v_mfma_f32_16x16x16_bf16 v[184:187], v[182:183], v[170:171], v[184:187]\n"
      "s_nop 0\n"
      "v_max3_f32 v209, v209, v196, v197\n"
      "v_max3_f32 v219, v219, v198, v199\n"
      "v_max3_f32 v219, v219, v209, v216\n"
      "v_alignbyte_b32 v239, v239, v239, v249\n"
      "v_mov_b32 v209, v219\n"
      "v_alignbyte_b32 v143, v238, v143, v249\n"
      "v_alignbyte_b32 v141, v237, v141, v249\n"
      "v_permlane16_swap_b32 v219, v209\n"
      "v_max_f32 v219, v219, v209\n"
      "v_mov_b32 v209, v219\n"
      "v_lshlrev_b32 v239, 16, v239\n"
      "v_max3_f32 v208, v179, v184, v185\n"
      "v_permlane32_swap_b32 v219, v209\n"
      "v_max_f32 v219, v219, v209\n"
      "v_sub_f32 v223, v223, v219\n"
      "v_mfma_f32_16x16x16_bf16 v[228:231], v[182:183], v[252:253], v[228:231]\n"
      "v_sub_f32 v196, v196, v219\n"
      "v_sub_f32 v146, v146, v219\n"
      "v_sub_f32 v197, v197, v219\n"
      "v_sub_f32 v221, v221, v219\n"
      "v_sub_f32 v220, v220, v219\n"
      "v_exp_f32 v220, v220\n"
      "v_exp_f32 v221, v221\n"
      "v_sub_f32 v145, v145, v219\n"
      "v_sub_f32 v222, v222, v219\n"
      "s_waitcnt lgkmcnt(0)\n"
      "v_exp_f32 v223, v223\n"
      "v_sub_f32 v147, v147, v219\n"
      "v_sub_f32 v199, v199, v219\n"
      "v_cvt_pk_bf16_f32 v220, v220, v221\n"
      "v_sub_f32 v216, v216, v219\n"
      "v_sub_f32 v198, v198, v219\n"
      "v_max3_f32 v208, v208, v228, v229\n"
      "v_exp_f32 v197, v197\n"
      "v_max3_f32 v209, v176, v177, v178\n"
      "v_exp_f32 v222, v222\n"
      "v_exp_f32 v216, v216\n"
      "v_cvt_pk_bf16_f32 v221, v222, v223\n"
      "v_sub_f32 v144, v144, v219\n"
      "v_exp_f32 v144, v144\n"
      "v_bitop3_b32 v200, v200, v201, s4 bitop3:0x7e\n"
      "v_exp_f32 v199, v199\n"
      "v_exp_f32 v147, v147\n"
      "v_exp_f32 v146, v146\n"
      "v_bitop3_b32 v202, v202, v203, s4 bitop3:0x7e\n"
      "v_max3_f32 v209, v209, v186, v187\n"
      "v_exp_f32 v145, v145\n"
      "v_exp_f32_e64 v196, v196\n"
      "v_max3_f32 v209, v209, v230, v231\n"
      "v_cvt_pk_bf16_f32 v64, v216, 0\n"
      "v_max3_f32 v209, v209, v208, v232\n"
      "v_mov_b32 v208, v209\n"
      "v_exp_f32 v198, v198\n"
      "v_or_b32 v200, v200, v202\n"
      "v_permlane16_swap_b32 v209, v208\n"
      "v_cmp_ne_u32 vcc, 0, v200\n"
      "v_max_f32 v209, v209, v208\n"
      "v_mov_b32 v208, v209\n"
      "v_cvt_pk_bf16_f32 v62, v196, v197\n"
      "s_mov_b64 s[48:49], vcc\n"
      "v_permlane32_swap_b32 v209, v208\n"
      "v_max_f32 v209, v209, v208\n"
      "v_sub_f32 v231, v231, v209\n"
      "v_sub_f32 v178, v178, v209\n"
      "v_sub_f32 v176, v176, v209\n"
      "v_sub_f32 v229, v229, v209\n"
      "v_sub_f32 v184, v184, v209\n"
      "v_sub_f32 v177, v177, v209\n"
      "v_cvt_pk_bf16_f32 v223, v146, v147\n"
      "v_exp_f32 v177, v177\n"
      "v_exp_f32 v178, v178\n"
      "v_sub_f32 v185, v185, v209\n"
      "v_sub_f32 v179, v179, v209\n"
      "v_sub_f32 v187, v187, v209\n"
      "v_exp_f32 v184, v184\n"
      "v_sub_f32 v232, v232, v209\n"
      "v_exp_f32 v176, v176\n"
      "v_exp_f32 v179, v179\n"
      "v_sub_f32 v230, v230, v209\n"
      "v_cvt_pk_bf16_f32 v222, v144, v145\n"
      "v_cvt_pk_bf16_f32 v63, v198, v199\n"
      "v_sub_f32 v228, v228, v209\n"
      "v_cvt_pk_bf16_f32 v176, v176, v177\n"
      "v_exp_f32 v232, v232\n"
      "v_sub_f32 v186, v186, v209\n"
      "v_cvt_pk_bf16_f32 v177, v178, v179\n"
      "v_mfma_f32_16x16x32_bf16 v[144:147], v[66:69], v[220:223], 0\n"
      "s_cbranch_vccnz L_flg_128_%=\n"
      "L_fb_129_%=:\n"
      "v_exp_f32 v185, v185\n"
      "v_mfma_f32_16x16x32_bf16 v[144:147], v[66:69], v[62:65], v[144:147]\n"
      "v_exp_f32 v228, v228\n"
      "v_mfma_f32_16x16x32_bf16 v[200:203], v[192:195], v[220:223], 0\n"
      "v_exp_f32 v229, v229\n"
      "v_mfma_f32_16x16x32_bf16 v[200:203], v[50:53], v[62:65], v[200:203]\n"
      "ds_read_b128 v[168:171], v120\n"
      "v_cvt_pk_bf16_f32 v178, v184, v185\n"
      "v_exp_f32 v187, v187\n"
      "v_mfma_f32_16x16x32_bf16 v[248:251], v[244:247], v[220:223], 0\n"
      "v_exp_f32 v230, v230\n"
      "v_mfma_f32_16x16x32_bf16 v[248:251], v[54:57], v[62:65], v[248:251]\n"
      "v_exp_f32 v231, v231\n"
      "v_mfma_f32_16x16x32_bf16 v[212:215], v[160:163], v[220:223], 0\n"
      "v_rcp_f32_e64 v144, v144\n"
      "v_mfma_f32_16x16x32_bf16 v[212:215], v[58:61], v[62:65], v[212:215]\n"
      "v_cvt_pk_bf16_f32 v63, v230, v231\n"
      "v_cvt_pk_bf16_f32 v62, v228, v229\n"
      "v_mfma_f32_16x16x32_f16 v[228:231], v[224:227], v[148:151], v[102:105]\n"
      "v_cvt_pk_bf16_f32 v64, v232, 0\n"
      "v_mul_f32 v200, v144, v200\n"
      "v_mfma_f32_16x16x32_f16 v[228:231], v[34:37], v[44:47], v[228:231]\n"
      "v_exp_f32 v186, v186\n"
      "v_mul_f32 v201, v144, v201\n"
      "v_mul_f32 v250, v144, v250\n"
      "v_mul_f32 v202, v144, v202\n"
      "v_mfma_f32_16x16x32_f16 v[224:227], v[156:159], v[148:151], v[106:109]\n"
      "s_waitcnt lgkmcnt(0)\n"
      "v_mul_f32 v203, v144, v203\n"
      "v_mul_f32 v212, v144, v212\n"
      "v_mfma_f32_16x16x32_f16 v[224:227], v[40:43], v[44:47], v[224:227]\n"
      "v_cvt_pk_bf16_f32 v200, v200, v201\n"
      "v_cvt_pk_bf16_f32 v201, v202, v203\n"
      "v_mfma_f32_16x16x16_bf16 v[228:231], v[182:183], v[210:211], v[228:231]\n"
      "v_mul_f32 v248, v144, v248\n"
      "v_mul_f32 v249, v144, v249\n"
      "v_mul_f32_e64 v213, v144, v213\n"
      "v_mfma_f32_16x16x16_bf16 v[224:227], v[182:183], v[140:141], v[224:227]\n"
      "v_cvt_pk_bf16_f32 v202, v248, v249\n"
      "v_mfma_f32_16x16x32_f16 v[156:159], v[172:175], v[148:151], v[110:113]\n"
      "v_cvt_pk_bf16_f32 v179, v186, v187\n"
      "v_mul_f32 v214, v144, v214\n"
      "v_mfma_f32_16x16x32_f16 v[156:159], v[46:49], v[44:47], v[156:159]\n"
      "v_mul_f32 v251, v144, v251\n"
      "v_cvt_pk_bf16_f32 v203, v250, v251\n"
      "v_mfma_f32_16x16x32_f16 v[248:251], v[136:139], v[148:151], v[74:77]\n"
      "v_mul_f32_e64 v215, v144, v215\n"
      "v_cvt_pk_bf16_f32 v212, v212, v213\n"
      "v_mfma_f32_16x16x32_f16 v[248:251], v[164:167], v[172:175], v[248:251]\n"
      "v_mfma_f32_16x16x32_f16 v[248:251], v[204:207], v[44:47], v[248:251]\n"
      "buffer_store_dwordx4 v[168:171], v117, s[20:23], 0 offen offset:0 sc1\n"
      "v_cvt_pk_bf16_f32 v213, v214, v215\n"
      "s_nop 0\n"
      "v_mfma_f32_16x16x32_bf16 v[168:171], v[160:163], v[176:179], 0\n"
      "v_mfma_f32_16x16x32_f16 v[148:151], v[164:167], v[136:139], v[76:79]\n"
      "v_mfma_f32_16x16x16_bf16 v[156:159], v[182:183], v[142:143], v[156:159]\n"
      "s_nop 1\n"
      "v_max3_f32 v251, v231, v224, v225\n"
      "v_mfma_f32_16x16x32_bf16 v[164:167], v[66:69], v[176:179], 0\n"
      "v_mfma_f32_16x16x32_bf16 v[164:167], v[66:69], v[62:65], v[164:167]\n"
      "ds_write_b128 v124, v[200:203] offset:18432\n"
      "v_mfma_f32_16x16x32_bf16 v[200:203], v[192:195], v[176:179], 0\n"
      "v_max3_f32 v250, v228, v229, v230\n"
      "v_fmac_f32_e64 v248, s37, v239\n"
      "v_mfma_f32_16x16x32_bf16 v[200:203], v[50:53], v[62:65], v[200:203]\n"
      "v_max3_f32 v251, v251, v156, v157\n"
      "v_max3_f32 v250, v250, v226, v227\n"
      "v_mfma_f32_16x16x32_bf16 v[144:147], v[244:247], v[176:179], 0\n"
      "v_rcp_f32_e64 v164, v164\n"
      "v_mfma_f32_16x16x32_bf16 v[144:147], v[54:57], v[62:65], v[144:147]\n"
      "v_max3_f32 v250, v250, v158, v159\n"
      "v_mfma_f32_16x16x32_bf16 v[168:171], v[58:61], v[62:65], v[168:171]\n"
      "s_nop 0\n"
      "v_mul_f32 v200, v164, v200\n"
      "v_mul_f32 v201, v164, v201\n"
      "v_cvt_pk_bf16_f32 v200, v200, v201\n"
      "v_mul_f32 v203, v164, v203\n"
      "v_mul_f32 v202, v164, v202\n"
      "v_mul_f32 v146, v164, v146\n"
      "v_mul_f32_e64 v145, v164, v145\n"
      "v_cvt_pk_bf16_f32 v201, v202, v203\n"
      "v_mul_f32 v168, v164, v168\n"
      "v_mul_f32 v147, v164, v147\n"
      "v_mul_f32 v169, v164, v169\n"
      "v_mul_f32 v144, v164, v144\n"
      "v_max3_f32 v250, v250, v251, v248\n"
      "v_cvt_pk_bf16_f32 v168, v168, v169\n"
      "v_mov_b32 v251, v250\n"
      "v_cvt_pk_bf16_f32 v203, v146, v147\n"
      "v_cvt_pk_bf16_f32 v202, v144, v145\n"
      "v_permlane16_swap_b32 v250, v251\n"
      "v_mul_f32 v171, v164, v171\n"
      "v_mfma_f32_16x16x16_f16 v[148:151], v[206:207], v[204:205], v[148:151]\n"
      "ds_read_b128 v[144:147], v121\n"
      "v_max_f32 v250, v250, v251\n"
      "v_mov_b32_e64 v251, v250\n"
      "v_mbcnt_lo_u32_b32 v218, -1, 0\n"
      "v_mul_f32 v170, v164, v170\n"
      "v_permlane32_swap_b32 v250, v251\n"
      "v_max_f32 v250, v250, v251\n"
      "v_sub_f32 v231, v231, v250\n"
      "v_sub_f32 v226, v226, v250\n"
      "v_sub_f32 v225, v225, v250\n"
      "v_sub_f32 v159, v159, v250\n"
      "v_sub_f32 v227, v227, v250\n"
      "v_sub_f32 v248, v248, v250\n"
      "v_sub_f32 v230, v230, v250\n"
      "v_sub_f32 v156, v156, v250\n"
      "v_exp_f32 v225, v225\n"
      "v_sub_f32 v229, v229, v250\n"
      "v_exp_f32 v229, v229\n"
      "v_sub_f32 v228, v228, v250\n"
      "v_exp_f32 v248, v248\n"
      "v_exp_f32 v231, v231\n"
      "v_exp_f32 v230, v230\n"
      "v_cndmask_b32 v217, v217, v233, s[42:43]\n"
      "v_exp_f32 v228, v228\n"
      "v_exp_f32 v156, v156\n"
      "v_sub_f32 v157, v157, v250\n"
      "v_sub_f32 v158, v158, v250\n"
      "v_exp_f32_e64 v227, v227\n"
      "v_exp_f32_e64 v226, v226\n"
      "v_sub_f32_e64 v224, v224, v250\n"
      "v_cvt_pk_bf16_f32 v228, v228, v229\n"
      "v_cvt_pk_bf16_f32 v229, v230, v231\n"
      "v_cvt_pk_bf16_f32 v169, v170, v171\n"
      "v_exp_f32_e64 v157, v157\n"
      "v_exp_f32_e64 v159, v159\n"
      "v_cvt_pk_bf16_f32 v62, v156, v157\n"
      "v_exp_f32_e64 v224, v224\n"
      "v_cvt_pk_bf16_f32 v231, v226, v227\n"
      "v_exp_f32_e64 v158, v158\n"
      "v_cvt_pk_bf16_f32 v230, v224, v225\n"
      "v_cvt_pk_bf16_f32 v64, v248, 0\n"
      "v_cvt_pk_bf16_f32 v63, v158, v159\n"
      "v_mfma_f32_16x16x32_bf16 v[156:159], v[66:69], v[228:231], 0\n"
      "ds_read_b128 v[136:139], v123 offset:4096\n"
      "v_cndmask_b32 v217, v217, v249, s[44:45]\n"
      "v_mfma_f32_16x16x32_bf16 v[156:159], v[66:69], v[62:65], v[156:159]\n"
      "ds_read_b128 v[204:207], v123 offset:1024\n"
      "v_cndmask_b32 v217, v217, v149, s[46:47]\n"
      "v_mfma_f32_16x16x32_bf16 v[224:227], v[192:195], v[228:231], 0\n"
      "ds_read_b128 v[252:255], v123 offset:7168\n"
      "v_mbcnt_hi_u32_b32 v218, -1, v218\n"
      "v_mfma_f32_16x16x32_bf16 v[224:227], v[50:53], v[62:65], v[224:227]\n"
      "ds_write_b128 v124, v[200:203] offset:19456\n"
      "v_mfma_f32_16x16x32_bf16 v[164:167], v[244:247], v[228:231], 0\n"
      "ds_read_b128 v[208:211], v123 offset:10240\n"
      "v_min_u32 v218, 48, v218\n"
      "v_mfma_f32_16x16x32_bf16 v[164:167], v[54:57], v[62:65], v[164:167]\n"
      "ds_write2st64_b64 v125, v[212:213], v[168:169] offset0:36 offset1:38\n"
      "s_waitcnt lgkmcnt(6)\n"
      "v_mfma_f32_16x16x32_bf16 v[148:151], v[160:163], v[228:231], 0\n"
      "v_lshlrev_b32 v218, 1, v218\n"
      "v_add_u32_e64 v218, s30, v218\n"
      "v_mfma_f32_16x16x32_bf16 v[148:151], v[58:61], v[62:65], v[148:151]\n"
      "s_mov_b32 exec_hi, 0\n"
      "buffer_store_dwordx4 v[144:147], v118, s[20:23], 0 offen offset:0 sc1\n"
      "s_mov_b32 exec_hi, -1\n"
      "s_waitcnt lgkmcnt(4)\n"
      "v_mfma_f32_16x16x32_bf16 v[248:251], a[216:219], v[204:207], 0\n"
      "ds_read_u16 v216, v218 offset:4704\n"
      "s_add_u32 s28, s27, 2\n"
      "v_mfma_f32_16x16x32_bf16 v[248:251], a[228:231], v[136:139], v[248:251]\n"
      "s_min_u32 m0, s28, s3\n"
      "v_mfma_f32_16x16x32_bf16 v[144:147], a[220:223], v[204:207], 0\n"
      "ds_read_b128 v[168:171], v123 offset:16384\n"
      "v_mfma_f32_16x16x32_bf16 v[200:203], a[224:227], v[204:207], 0\n"
      "ds_read_b128 v[228:231], v123 offset:13312\n"
      "s_movrels_b32 s9, s80\n"
      "v_mfma_f32_16x16x32_bf16 v[200:203], a[236:239], v[136:139], v[200:203]\n"
      "s_lshr_b32 s8, s9, 8\n"
      "s_and_b32 s9, s9, 0xff\n"
      "v_mfma_f32_16x16x32_bf16 v[144:147], a[232:235], v[136:139], v[144:147]\n"
      "s_waitcnt lgkmcnt(3)\n"
      "s_mul_i32 s10, s8, 73\n"
      "v_mfma_f32_16x16x32_bf16 v[248:251], a[240:243], v[252:255], v[248:251]\n"
      "s_add_u32 s10, s10, s9\n"
      "ds_read_b128 v[136:139], v123 offset:2048\n"
      "v_mfma_f32_16x16x32_bf16 v[248:251], a[252:255], v[208:211], v[248:251]\n"
      "s_mul_i32 s28, s10, 4802\n"
      "s_add_u32 s56, s18, s28\n"
      "s_addc_u32 s57, s19, 0\n"
      "v_mfma_f32_16x16x32_bf16 v[144:147], a[244:247], v[252:255], v[144:147]\n"
      "s_waitcnt lgkmcnt(3)\n"
      "v_lshlrev_b32 v216, 16, v216\n"
      "v_fmac_f32_e64 v217, s37, v216\n"
      "v_mfma_f32_16x16x32_bf16 v[200:203], a[248:251], v[252:255], v[200:203]\n"
      "ds_read_b128 v[204:207], v123 offset:5120\n"
      "v_max_f32_dpp v216, v217, v217 row_ror:8 row_mask:0xf bank_mask:0xf\n"
      "v_mfma_f32_16x16x32_bf16 v[200:203], v[4:7], v[208:211], v[200:203]\n"
      "s_and_b32 s56, s56, -16\n"
      "v_max_f32_dpp v216, v216, v216 row_ror:4 row_mask:0xf bank_mask:0xf\n"
      "s_sub_u32 s58, s56, 110592\n"
      "v_mfma_f32_16x16x32_bf16 v[144:147], v[0:3], v[208:211], v[144:147]\n"
      "s_subb_u32 s59, s57, 0\n"
      "v_max_f32_dpp v216, v216, v216 row_ror:2 row_mask:0xf bank_mask:0xf\n"
      "s_waitcnt lgkmcnt(2)\n"
      "v_mfma_f32_16x16x32_bf16 v[248:251], v[8:11], v[228:231], v[248:251]\n"
      "s_sub_u32 s56, s56, 73728\n"
      "s_subb_u32 s57, s57, 0\n"
      "v_max_f32_dpp v216, v216, v216 row_ror:1 row_mask:0xf bank_mask:0xf\n"
      "v_mfma_f32_16x16x32_bf16 v[248:251], v[20:23], v[168:171], v[248:251]\n"
      "s_mul_i32 s28, s8, 1373568\n"
      "v_max_f32_dpp v216, v216, v216 row_bcast:15 row_mask:0xa bank_mask:0xf\n"
      "s_mul_i32 s29, s9, 2688\n"
      "v_mfma_f32_16x16x32_bf16 v[144:147], v[12:15], v[228:231], v[144:147]\n"
      "ds_read_b128 v[172:175], v123 offset:11264\n"
      "v_max_f32_dpp v216, v216, v216 row_bcast:31 row_mask:0xc bank_mask:0xf\n"
      "v_mfma_f32_16x16x32_bf16 v[144:147], v[24:27], v[168:171], v[144:147]\n"
      "s_add_u32 s28, s28, s29\n"
      "v_readlane_b32 s31, v216, 63\n"
      "s_cmp_eq_u32 s8, 72\n"
      "ds_read_b128 v[140:143], v123 offset:8192\n"
      "v_mfma_f32_16x16x32_bf16 v[200:203], v[16:19], v[228:231], v[200:203]\n"
      "s_cselect_b32 s8, 0, 100270464\n"
      "v_subrev_f32 v217, s31, v217\n"
      "s_cmp_eq_u32 s9, 72\n"
      "v_mfma_f32_16x16x32_bf16 v[200:203], v[28:31], v[168:171], v[200:203]\n"
      "s_cselect_b32 s9, 0, 196224\n"
      "ds_read_b128 v[212:215], v114\n"
      "v_exp_f32_e64 v217, v217\n"
      "s_waitcnt lgkmcnt(3)\n"
      "v_mfma_f32_16x16x32_bf16 v[228:231], a[216:219], v[136:139], 0\n"
      "s_add_u32 s10, s8, s9\n"
      "v_cvt_pk_bf16_f32 v217, v217, v217\n"
      "s_add_u32 s28, s28, s10\n"
      "v_mfma_f32_16x16x32_bf16 v[228:231], a[228:231], v[204:207], v[228:231]\n"
      "ds_write_b16 v130, v217\n"
      "v_mfma_f32_16x16x32_bf16 v[168:171], a[220:223], v[136:139], 0\n"
      "ds_read_b128 v[208:211], v131\n"
      "s_add_u32 s52, s16, s28\n"
      "v_mfma_f32_16x16x32_bf16 v[168:171], a[232:235], v[204:207], v[168:171]\n"
      "ds_read_b128 v[216:219], v131 offset:64\n"
      "s_addc_u32 s53, s17, 0\n"
      "s_add_u32 s27, s27, 1\n"
      "v_mfma_f32_16x16x32_bf16 v[252:255], a[224:227], v[136:139], 0\n"
      "ds_read_b128 v[220:223], v123 offset:14336\n"
      "s_cmp_ge_u32 s27, s26\n"
      "v_fma_f32 v136, v248, v248, s39\n"
      "v_fmac_f32 v136, v249, v249\n"
      "s_waitcnt lgkmcnt(5)\n"
      "v_fmac_f32 v136, v250, v250\n"
      "v_fmac_f32 v136, v251, v251\n"
      "v_fmac_f32 v136, v144, v144\n"
      "v_fmac_f32 v136, v145, v145\n"
      "v_fmac_f32 v136, v146, v146\n"
      "v_fmac_f32 v136, v147, v147\n"
      "v_fmac_f32 v136, v200, v200\n"
      "s_cselect_b32 s28, 1, 0\n"
      "v_fmac_f32 v136, v201, v201\n"
      "v_fmac_f32 v136, v202, v202\n"
      "v_mfma_f32_16x16x32_bf16 v[228:231], a[240:243], v[140:143], v[228:231]\n"
      "ds_read_b128 v[236:239], v123 offset:17408\n"
      "v_fmac_f32 v136, v203, v203\n"
      "v_mfma_f32_16x16x32_bf16 v[168:171], a[244:247], v[140:143], v[168:171]\n"
      "v_mfma_f32_16x16x32_bf16 v[228:231], a[252:255], v[172:175], v[228:231]\n"
      "v_permlane16_swap_b32 v180, v136\n"
      "v_add_f32 v180, v180, v136\n"
      "v_mfma_f32_16x16x32_bf16 v[168:171], v[0:3], v[172:175], v[168:171]\n"
      "ds_read_b128 v[136:139], v115 offset:6144\n"
      "v_mfma_f32_16x16x32_bf16 v[252:255], a[236:239], v[204:207], v[252:255]\n"
      "ds_read_b128 v[204:207], v114 offset:6144\n"
      "v_mfma_f32_16x16x32_bf16 v[252:255], a[248:251], v[140:143], v[252:255]\n"
      "v_mfma_f32_16x16x32_bf16 v[252:255], v[4:7], v[172:175], v[252:255]\n"
      "s_waitcnt lgkmcnt(3)\n"
      "s_xor_b32 s29, s10, s11\n"
      "v_mfma_f32_16x16x32_bf16 v[172:175], v[208:211], v[244:247], 0\n"
      "s_waitcnt lgkmcnt(2)\n"
      "v_mfma_f32_16x16x32_bf16 v[228:231], v[8:11], v[220:223], v[228:231]\n"
      "s_or_b32 s28, s28, s29\n"
      "v_mfma_f32_16x16x32_bf16 v[228:231], v[20:23], v[236:239], v[228:231]\n"
      "v_mfma_f32_16x16x32_bf16 v[168:171], v[12:15], v[220:223], v[168:171]\n"
      "v_mfma_f32_16x16x32_bf16 v[168:171], v[24:27], v[236:239], v[168:171]\n"
      "v_mfma_f32_16x16x32_bf16 v[252:255], v[16:19], v[220:223], v[252:255]\n"
      "v_mfma_f32_16x16x32_bf16 v[252:255], v[28:31], v[236:239], v[252:255]\n"
      "s_waitcnt lgkmcnt(0)\n"
      "v_mfma_f32_16x16x32_bf16 v[220:223], v[208:211], v[66:69], 0\n"
      "s_nop 1\n"
      "v_fma_f32 v140, v228, v228, s39\n"
      "v_mfma_f32_16x16x32_bf16 v[220:223], v[216:219], v[66:69], v[220:223]\n"
      "v_fmac_f32 v140, v229, v229\n"
      "v_fmac_f32 v140, v230, v230\n"
      "v_mfma_f32_16x16x32_bf16 v[236:239], v[208:211], v[192:195], 0\n"
      "v_fmac_f32 v140, v231, v231\n"
      "v_fmac_f32 v140, v168, v168\n"
      "v_mfma_f32_16x16x32_bf16 v[236:239], v[216:219], v[50:53], v[236:239]\n"
      "v_fmac_f32 v140, v169, v169\n"
      "v_fmac_f32 v140, v170, v170\n"
      "v_fmac_f32 v140, v171, v171\n"
      "v_fmac_f32 v140, v252, v252\n"
      "v_fmac_f32 v140, v253, v253\n"
      "v_fmac_f32 v140, v254, v254\n"
      "v_mfma_f32_16x16x32_bf16 v[196:199], v[208:211], v[160:163], 0\n"
      "ds_read_b128 v[208:211], v115\n"
      "v_fmac_f32 v140, v255, v255\n"
      "v_mfma_f32_16x16x32_bf16 v[196:199], v[216:219], v[58:61], v[196:199]\n"
      "v_mov_b32 v221, v140\n"
      "v_mov_b32_dpp v220, v220 quad_perm:[0,0,0,0] row_mask:0xf bank_mask:0xf\n"
      "v_mfma_f32_16x16x32_bf16 v[172:175], v[216:219], v[54:57], v[172:175]\n"
      "v_permlane16_swap_b32 v140, v221\n"
      "v_add_f32 v140, v140, v221\n"
      "s_nop 1\n"
      "v_permlane32_swap_b32 v180, v140\n"
      "v_rcp_f32 v220, v220\n"
      "s_nop 1\n"
      "v_cndmask_b32 v236, v236, v172, s[42:43]\n"
      "v_cndmask_b32 v236, v236, v196, s[44:45]\n"
      "v_add_f32_e64 v180, v180, v140\n"
      "v_mul_f32_e64 v236, v220, v236\n"
      "v_cvt_pk_bf16_f32 v236, v236, v236\n"
      "ds_write_b32 v129, v180 offset:1024\n"
      "ds_write_b16 v135, v236 offset:16\n"
      "s_waitcnt lgkmcnt(2)\n"
      "v_mfma_f32_16x16x32_bf16 v[216:219], a[0:3], v[212:215], 0\n"
      "v_mfma_f32_16x16x32_bf16 v[216:219], a[36:39], v[208:211], v[216:219]\n"
      "v_mfma_f32_16x16x32_bf16 v[140:143], a[4:7], v[212:215], 0\n"
      "s_nop 0\n"
      "v_mfma_f32_16x16x32_bf16 v[140:143], a[40:43], v[208:211], v[140:143]\n"
      "v_mfma_f32_16x16x32_bf16 v[220:223], a[8:11], v[212:215], 0\n"
      "v_mfma_f32_16x16x32_bf16 v[220:223], a[44:47], v[208:211], v[220:223]\n"
      "v_mfma_f32_16x16x32_bf16 v[196:199], a[12:15], v[212:215], 0\n"
      "v_mfma_f32_16x16x32_bf16 v[196:199], a[48:51], v[208:211], v[196:199]\n"
      "v_mfma_f32_16x16x32_bf16 v[236:239], a[16:19], v[212:215], 0\n"
      "v_mfma_f32_16x16x32_bf16 v[236:239], a[52:55], v[208:211], v[236:239]\n"
      "v_mfma_f32_16x16x32_bf16 v[172:175], a[20:23], v[212:215], 0\n"
      "v_mfma_f32_16x16x32_bf16 v[172:175], a[56:59], v[208:211], v[172:175]\n"
      "s_branch L_join_21_%=\n"
      "L_flg_128_%=:\n"
      "v_cmp_gt_i32 vcc, 0, v200\n"
      "s_cbranch_vccz L_fb_129_%=\n"
      "s_sleep 1\n"
      "ds_read_b128 v[200:203], v128 offset:2048\n"
      "s_waitcnt lgkmcnt(0)\n"
      "v_bitop3_b32 v200, v200, v201, s4 bitop3:0x7e\n"
      "v_bitop3_b32 v202, v202, v203, s4 bitop3:0x7e\n"
      "v_or_b32 v200, v200, v202\n"
      "v_cmp_ne_u32 s[48:49], 0, v200\n"
      "s_branch L_flg_128_%=\n"
      "L_gen_28_%=:\n"
      "s_waitcnt lgkmcnt(0)\n"
      "s_nop 5\n"
      "s_cmp_eq_u64 s[34:35], 0\n"
      "s_cbranch_scc1 L_ok_146_%=\n"
      "ds_read_b128 v[156:159], v114\n"
      "ds_read_b128 v[172:175], v115\n"
      "ds_read_b128 v[208:211], v114 offset:12288\n"
      "ds_read_b128 v[204:207], v114 offset:6144\n"
      "ds_read_b128 v[216:219], v115 offset:6144\n"
      "s_waitcnt lgkmcnt(3)\n"
      "v_mfma_f32_16x16x32_bf16 v[168:171], a[0:3], v[156:159], 0\n"
      "v_mfma_f32_16x16x32_bf16 v[168:171], a[36:39], v[172:175], v[168:171]\n"
      "v_mfma_f32_16x16x32_bf16 v[140:143], a[4:7], v[156:159], 0\n"
      "v_mfma_f32_16x16x32_bf16 v[140:143], a[40:43], v[172:175], v[140:143]\n"
      "v_mfma_f32_16x16x32_bf16 v[188:191], a[8:11], v[156:159], 0\n"
      "s_waitcnt lgkmcnt(0)\n"
      "v_mfma_f32_16x16x32_bf16 v[196:199], a[12:15], v[156:159], 0\n"
      "v_mfma_f32_16x16x32_bf16 v[196:199], a[48:51], v[172:175], v[196:199]\n"
      "s_nop 0\n"
      "v_mfma_f32_16x16x32_bf16 v[240:243], a[16:19], v[156:159], 0\n"
      "v_mfma_f32_16x16x32_bf16 v[240:243], a[52:55], v[172:175], v[240:243]\n"
      "v_mfma_f32_16x16x32_bf16 v[240:243], a[88:91], v[204:207], v[240:243]\n"
      "v_mfma_f32_16x16x32_bf16 v[196:199], a[84:87], v[204:207], v[196:199]\n"
      "v_mfma_f32_16x16x32_bf16 v[160:163], a[20:23], v[156:159], 0\n"
      "ds_read_b128 v[156:159], v115 offset:12288\n"
      "v_mfma_f32_16x16x32_bf16 v[160:163], a[56:59], v[172:175], v[160:163]\n"
      "v_mfma_f32_16x16x32_bf16 v[168:171], a[72:75], v[204:207], v[168:171]\n"
      "v_mfma_f32_16x16x32_bf16 v[168:171], a[108:111], v[216:219], v[168:171]\n"
      "v_mfma_f32_16x16x32_bf16 v[188:191], a[44:47], v[172:175], v[188:191]\n"
      "v_mfma_f32_16x16x32_bf16 v[188:191], a[80:83], v[204:207], v[188:191]\n"
      "v_mfma_f32_16x16x32_bf16 v[188:191], a[116:119], v[216:219], v[188:191]\n"
      "v_mfma_f32_16x16x32_bf16 v[140:143], a[76:79], v[204:207], v[140:143]\n"
      "s_nop 0\n"
      "v_mfma_f32_16x16x32_bf16 v[140:143], a[112:115], v[216:219], v[140:143]\n"
      "s_waitcnt lgkmcnt(0)\n"
      "v_mfma_f32_16x16x32_bf16 v[168:171], a[144:147], v[208:211], v[168:171]\n"
      "ds_read_b128 v[172:175], v114 offset:2048\n"
      "v_mfma_f32_16x16x32_bf16 v[168:171], a[180:183], v[156:159], v[168:171]\n"
      "v_mfma_f32_16x16x32_bf16 v[160:163], a[92:95], v[204:207], v[160:163]\n"
      "v_mfma_f32_16x16x32_bf16 v[160:163], a[128:131], v[216:219], v[160:163]\n"
      "v_mfma_f32_16x16x32_bf16 v[188:191], a[152:155], v[208:211], v[188:191]\n"
      "v_mfma_f32_16x16x32_bf16 v[188:191], a[188:191], v[156:159], v[188:191]\n"
      "v_mfma_f32_16x16x32_bf16 v[140:143], a[148:151], v[208:211], v[140:143]\n"
      "v_mfma_f32_16x16x32_bf16 v[140:143], a[184:187], v[156:159], v[140:143]\n"
      "s_nop 1\n"
      "v_fma_f32 v150, v168, v168, s38\n"
      "v_mfma_f32_16x16x32_bf16 v[240:243], a[124:127], v[216:219], v[240:243]\n"
      "v_mfma_f32_16x16x32_bf16 v[196:199], a[120:123], v[216:219], v[196:199]\n"
      "s_nop 0\n"
      "v_mfma_f32_16x16x32_bf16 v[196:199], a[156:159], v[208:211], v[196:199]\n"
      "v_fmac_f32 v150, v169, v169\n"
      "v_mfma_f32_16x16x32_bf16 v[196:199], a[192:195], v[156:159], v[196:199]\n"
      "v_fmac_f32 v150, v170, v170\n"
      "v_mfma_f32_16x16x32_bf16 v[240:243], a[160:163], v[208:211], v[240:243]\n"
      "v_fmac_f32 v150, v171, v171\n"
      "v_fmac_f32 v150, v140, v140\n"
      "v_mfma_f32_16x16x32_bf16 v[240:243], a[196:199], v[156:159], v[240:243]\n"
      "v_fmac_f32 v150, v141, v141\n"
      "v_fmac_f32 v150, v142, v142\n"
      "v_mfma_f32_16x16x32_bf16 v[160:163], a[164:167], v[208:211], v[160:163]\n"
      "v_fma_f32 v151, v196, v196, s38\n"
      "v_fmac_f32 v150, v143, v143\n"
      "v_mfma_f32_16x16x32_bf16 v[160:163], a[200:203], v[156:159], v[160:163]\n"
      "ds_read_b128 v[156:159], v115 offset:2048\n"
      "v_fmac_f32 v151, v197, v197\n"
      "v_fmac_f32 v151, v198, v198\n"
      "v_fmac_f32 v151, v199, v199\n"
      "v_fmac_f32 v151, v240, v240\n"
      "v_fmac_f32 v151, v241, v241\n"
      "v_fmac_f32 v151, v242, v242\n"
      "v_fmac_f32 v150, v188, v188\n"
      "v_fmac_f32 v151, v243, v243\n"
      "v_fmac_f32 v151, v160, v160\n"
      "v_fmac_f32 v151, v161, v161\n"
      "v_fmac_f32 v151, v162, v162\n"
      "v_fmac_f32 v150, v189, v189\n"
      "v_fmac_f32 v150, v190, v190\n"
      "v_fmac_f32 v151, v163, v163\n"
      "v_fmac_f32 v150, v191, v191\n"
      "s_nop 1\n"
      "v_permlane16_swap_b32 v150, v151\n"
      "v_add_f32 v150, v150, v151\n"
      "v_mov_b32 v151, v150\n"
      "s_nop 1\n"
      "v_permlane32_swap_b32 v150, v151\n"
      "v_add_f32 v150, v150, v151\n"
      "v_rsq_f32 v150, v150\n"
      "s_nop 0\n"
      "v_mov_b32 v151, v150\n"
      "s_waitcnt lgkmcnt(0)\n"
      "v_mfma_f32_16x16x32_bf16 v[144:147], a[0:3], v[172:175], 0\n"
      "ds_read_b128 v[204:207], v114 offset:8192\n"
      "v_permlane16_swap_b32 v150, v151\n"
      "v_mfma_f32_16x16x32_bf16 v[144:147], a[36:39], v[156:159], v[144:147]\n"
      "ds_read_b128 v[216:219], v115 offset:8192\n"
      "v_mul_f32 v198, v151, v198\n"
      "v_mul_f32 v197, v151, v197\n"
      "v_mul_f32 v199, v151, v199\n"
      "v_mul_f32 v196, v151, v196\n"
      "v_mfma_f32_16x16x32_bf16 v[164:167], a[12:15], v[172:175], 0\n"
      "ds_read_b128 v[208:211], v114 offset:14336\n"
      "v_cvt_pk_f16_f32 v196, v196, v197\n"
      "v_mfma_f32_16x16x32_bf16 v[164:167], a[48:51], v[156:159], v[164:167]\n"
      "v_mul_f32_e64 v241, v151, v241\n"
      "v_cvt_pk_f16_f32 v197, v198, v199\n"
      "v_mul_f32 v242, v151, v242\n"
      "v_mul_f32 v243, v151, v243\n"
      "v_cvt_pk_f16_f32 v199, v242, v243\n"
      "v_mul_f32_e64 v240, v151, v240\n"
      "v_cvt_pk_f16_f32 v198, v240, v241\n"
      "v_mfma_f32_16x16x32_bf16 v[240:243], a[16:19], v[172:175], 0\n"
      "v_mul_f32 v161, v151, v161\n"
      "v_mul_f32 v160, v151, v160\n"
      "v_mfma_f32_16x16x32_bf16 v[240:243], a[52:55], v[156:159], v[240:243]\n"
      "v_cvt_pk_f16_f32 v34, v160, v161\n"
      "v_mul_f32 v163, v151, v163\n"
      "v_mfma_f32_16x16x32_bf16 v[192:195], a[20:23], v[172:175], 0\n"
      "ds_read_b128 v[200:203], v115 offset:14336\n"
      "v_mul_f32 v162, v151, v162\n"
      "v_mfma_f32_16x16x32_bf16 v[192:195], a[56:59], v[156:159], v[192:195]\n"
      "v_cvt_pk_f16_f32 v35, v162, v163\n"
      "s_waitcnt lgkmcnt(2)\n"
      "v_mfma_f32_16x16x32_bf16 v[144:147], a[72:75], v[204:207], v[144:147]\n"
      "v_mul_f32 v150, s36, v150\n"
      "v_mul_f32 v168, v150, v168\n"
      "v_mfma_f32_16x16x32_bf16 v[144:147], a[108:111], v[216:219], v[144:147]\n"
      "v_mul_f32 v169, v150, v169\n"
      "v_cvt_pk_f16_f32 v168, v168, v169\n"
      "v_mfma_f32_16x16x32_bf16 v[164:167], a[84:87], v[204:207], v[164:167]\n"
      "v_mul_f32 v170, v150, v170\n"
      "v_mul_f32 v171, v150, v171\n"
      "v_mfma_f32_16x16x32_bf16 v[164:167], a[120:123], v[216:219], v[164:167]\n"
      "v_cvt_pk_f16_f32 v169, v170, v171\n"
      "v_mul_f32_e64 v140, v150, v140\n"
      "v_mfma_f32_16x16x32_bf16 v[240:243], a[88:91], v[204:207], v[240:243]\n"
      "v_mul_f32 v141, v150, v141\n"
      "v_cvt_pk_f16_f32 v170, v140, v141\n"
      "v_mfma_f32_16x16x32_bf16 v[240:243], a[124:127], v[216:219], v[240:243]\n"
      "v_mul_f32 v143, v150, v143\n"
      "v_mul_f32 v142, v150, v142\n"
      "v_mfma_f32_16x16x32_bf16 v[192:195], a[92:95], v[204:207], v[192:195]\n"
      "s_waitcnt lgkmcnt(0)\n"
      "v_mul_f32_e64 v188, v150, v188\n"
      "v_mfma_f32_16x16x32_bf16 v[192:195], a[128:131], v[216:219], v[192:195]\n"
      "v_cvt_pk_f16_f32 v171, v142, v143\n"
      "v_mul_f32 v189, v150, v189\n"
      "v_mfma_f32_16x16x32_bf16 v[140:143], a[4:7], v[172:175], 0\n"
      "v_mul_f32 v190, v150, v190\n"
      "v_mul_f32_e64 v191, v150, v191\n"
      "v_mfma_f32_16x16x32_bf16 v[140:143], a[40:43], v[156:159], v[140:143]\n"
      "v_cvt_pk_f16_f32 v32, v188, v189\n"
      "v_cvt_pk_f16_f32 v33, v190, v191\n"
      "v_mfma_f32_16x16x32_bf16 v[140:143], a[76:79], v[204:207], v[140:143]\n"
      "v_mfma_f32_16x16x32_bf16 v[140:143], a[112:115], v[216:219], v[140:143]\n"
      "v_mfma_f32_16x16x32_bf16 v[188:191], a[8:11], v[172:175], 0\n"
      "v_mfma_f32_16x16x32_bf16 v[188:191], a[44:47], v[156:159], v[188:191]\n"
      "v_mfma_f32_16x16x32_bf16 v[188:191], a[80:83], v[204:207], v[188:191]\n"
      "ds_read_b128 v[156:159], v115 offset:4096\n"
      "v_mfma_f32_16x16x32_bf16 v[188:191], a[116:119], v[216:219], v[188:191]\n"
      "v_mfma_f32_16x16x32_bf16 v[144:147], a[144:147], v[208:211], v[144:147]\n"
      "v_mfma_f32_16x16x32_bf16 v[144:147], a[180:183], v[200:203], v[144:147]\n"
      "v_mfma_f32_16x16x32_bf16 v[140:143], a[148:151], v[208:211], v[140:143]\n"
      "v_mfma_f32_16x16x32_bf16 v[140:143], a[184:187], v[200:203], v[140:143]\n"
      "v_mfma_f32_16x16x32_bf16 v[188:191], a[152:155], v[208:211], v[188:191]\n"
      "v_mfma_f32_16x16x32_bf16 v[188:191], a[188:191], v[200:203], v[188:191]\n"
      "v_mfma_f32_16x16x32_bf16 v[164:167], a[156:159], v[208:211], v[164:167]\n"
      "s_nop 0\n"
      "v_mfma_f32_16x16x32_bf16 v[164:167], a[192:195], v[200:203], v[164:167]\n"
      "s_nop 1\n"
      "v_fma_f32 v150, v144, v144, s38\n"
      "v_mfma_f32_16x16x32_bf16 v[240:243], a[160:163], v[208:211], v[240:243]\n"
      "v_fmac_f32 v150, v145, v145\n"
      "v_fmac_f32 v150, v146, v146\n"
      "v_mfma_f32_16x16x32_bf16 v[240:243], a[196:199], v[200:203], v[240:243]\n"
      "v_fmac_f32_e64 v150, v147, v147\n"
      "v_fma_f32 v151, v164, v164, s38\n"
      "v_fmac_f32 v150, v140, v140\n"
      "v_fmac_f32 v151, v165, v165\n"
      "v_mfma_f32_16x16x32_bf16 v[192:195], a[164:167], v[208:211], v[192:195]\n"
      "ds_read_b128 v[172:175], v114 offset:4096\n"
      "v_fmac_f32 v151, v166, v166\n"
      "v_mfma_f32_16x16x32_bf16 v[192:195], a[200:203], v[200:203], v[192:195]\n"
      "v_fmac_f32 v151, v167, v167\n"
      "v_fmac_f32 v151, v240, v240\n"
      "v_fmac_f32 v151, v241, v241\n"
      "v_fmac_f32 v151, v242, v242\n"
      "v_fmac_f32 v150, v141, v141\n"
      "v_fmac_f32 v150, v142, v142\n"
      "v_fmac_f32 v150, v143, v143\n"
      "v_fmac_f32 v150, v188, v188\n"
      "v_fmac_f32 v150, v189, v189\n"
      "v_fmac_f32 v151, v243, v243\n"
      "v_fmac_f32 v151, v192, v192\n"
      "v_fmac_f32 v150, v190, v190\n"
      "v_fmac_f32 v151, v193, v193\n"
      "v_fmac_f32 v151, v194, v194\n"
      "v_fmac_f32 v150, v191, v191\n"
      "v_fmac_f32 v151, v195, v195\n"
      "s_nop 1\n"
      "v_permlane16_swap_b32 v150, v151\n"
      "v_add_f32 v150, v150, v151\n"
      "v_mov_b32 v151, v150\n"
      "s_nop 1\n"
      "v_permlane32_swap_b32 v150, v151\n"
      "v_add_f32 v150, v150, v151\n"
      "v_rsq_f32 v150, v150\n"
      "s_waitcnt lgkmcnt(0)\n"
      "v_mfma_f32_16x16x32_bf16 v[180:183], a[0:3], v[172:175], 0\n"
      "ds_read_b128 v[204:207], v114 offset:10240\n"
      "v_mov_b32 v151, v150\n"
      "v_mfma_f32_16x16x32_bf16 v[180:183], a[36:39], v[156:159], v[180:183]\n"
      "ds_read_b128 v[216:219], v115 offset:10240\n"
      "v_permlane16_swap_b32 v150, v151\n"
      "v_mfma_f32_16x16x32_bf16 v[136:139], a[12:15], v[172:175], 0\n"
      "ds_read_b128 v[208:211], v114 offset:16384\n"
      "v_mul_f32 v164, v151, v164\n"
      "v_mfma_f32_16x16x32_bf16 v[136:139], a[48:51], v[156:159], v[136:139]\n"
      "v_mul_f32 v167, v151, v167\n"
      "v_mul_f32 v165, v151, v165\n"
      "v_mul_f32 v241, v151, v241\n"
      "v_mul_f32 v166, v151, v166\n"
      "v_cvt_pk_f16_f32 v164, v164, v165\n"
      "v_mul_f32 v242, v151, v242\n"
      "v_cvt_pk_f16_f32 v165, v166, v167\n"
      "v_mul_f32 v243, v151, v243\n"
      "v_mul_f32 v240, v151, v240\n"
      "v_cvt_pk_f16_f32 v166, v240, v241\n"
      "v_cvt_pk_f16_f32 v167, v242, v243\n"
      "v_mfma_f32_16x16x32_bf16 v[240:243], a[16:19], v[172:175], 0\n"
      "v_mul_f32 v192, v151, v192\n"
      "v_mul_f32 v193, v151, v193\n"
      "v_mfma_f32_16x16x32_bf16 v[240:243], a[52:55], v[156:159], v[240:243]\n"
      "v_mul_f32_e64 v195, v151, v195\n"
      "v_cvt_pk_f16_f32 v40, v192, v193\n"
      "v_mfma_f32_16x16x32_bf16 v[200:203], a[20:23], v[172:175], 0\n"
      "ds_read_b128 v[160:163], v115 offset:16384\n"
      "v_mul_f32 v194, v151, v194\n"
      "v_mfma_f32_16x16x32_bf16 v[200:203], a[56:59], v[156:159], v[200:203]\n"
      "s_waitcnt lgkmcnt(2)\n"
      "v_mul_f32_e64 v150, s36, v150\n"
      "v_cvt_pk_f16_f32 v41, v194, v195\n"
      "v_mfma_f32_16x16x32_bf16 v[180:183], a[72:75], v[204:207], v[180:183]\n"
      "v_mul_f32 v144, v150, v144\n"
      "v_mul_f32 v145, v150, v145\n"
      "v_mfma_f32_16x16x32_bf16 v[180:183], a[108:111], v[216:219], v[180:183]\n"
      "v_mul_f32 v146, v150, v146\n"
      "v_cvt_pk_f16_f32 v144, v144, v145\n"
      "v_mfma_f32_16x16x32_bf16 v[240:243], a[88:91], v[204:207], v[240:243]\n"
      "v_mul_f32 v147, v150, v147\n"
      "v_mul_f32_e64 v141, v150, v141\n"
      "v_mfma_f32_16x16x32_bf16 v[240:243], a[124:127], v[216:219], v[240:243]\n"
      "v_cvt_pk_f16_f32 v145, v146, v147\n"
      "v_mul_f32_e64 v140, v150, v140\n"
      "v_mfma_f32_16x16x32_bf16 v[136:139], a[84:87], v[204:207], v[136:139]\n"
      "v_cvt_pk_f16_f32 v146, v140, v141\n"
      "v_mul_f32 v142, v150, v142\n"
      "v_mfma_f32_16x16x32_bf16 v[136:139], a[120:123], v[216:219], v[136:139]\n"
      "v_mul_f32 v143, v150, v143\n"
      "v_cvt_pk_f16_f32 v147, v142, v143\n"
      "v_mfma_f32_16x16x32_bf16 v[140:143], a[4:7], v[172:175], 0\n"
      "v_mul_f32 v189, v150, v189\n"
      "s_waitcnt lgkmcnt(0)\n"
      "v_mfma_f32_16x16x32_bf16 v[140:143], a[40:43], v[156:159], v[140:143]\n"
      "v_mul_f32_e64 v191, v150, v191\n"
      "v_mul_f32_e64 v190, v150, v190\n"
      "v_mfma_f32_16x16x32_bf16 v[140:143], a[76:79], v[204:207], v[140:143]\n"
      "v_mul_f32_e64 v188, v150, v188\n"
      "v_mfma_f32_16x16x32_bf16 v[140:143], a[112:115], v[216:219], v[140:143]\n"
      "v_cvt_pk_f16_f32 v39, v190, v191\n"
      "v_cvt_pk_f16_f32 v38, v188, v189\n"
      "v_mfma_f32_16x16x32_bf16 v[188:191], a[8:11], v[172:175], 0\n"
      "v_mfma_f32_16x16x32_bf16 v[188:191], a[44:47], v[156:159], v[188:191]\n"
      "v_mfma_f32_16x16x32_bf16 v[188:191], a[80:83], v[204:207], v[188:191]\n"
      "v_mfma_f32_16x16x32_bf16 v[188:191], a[116:119], v[216:219], v[188:191]\n"
      "v_mfma_f32_16x16x32_bf16 v[200:203], a[92:95], v[204:207], v[200:203]\n"
      "v_mfma_f32_16x16x32_bf16 v[200:203], a[128:131], v[216:219], v[200:203]\n"
      "v_mfma_f32_16x16x32_bf16 v[180:183], a[144:147], v[208:211], v[180:183]\n"
      "v_mfma_f32_16x16x32_bf16 v[180:183], a[180:183], v[160:163], v[180:183]\n"
      "v_mfma_f32_16x16x32_bf16 v[140:143], a[148:151], v[208:211], v[140:143]\n"
      "v_mfma_f32_16x16x32_bf16 v[140:143], a[184:187], v[160:163], v[140:143]\n"
      "v_mfma_f32_16x16x32_bf16 v[188:191], a[152:155], v[208:211], v[188:191]\n"
      "v_mfma_f32_16x16x32_bf16 v[188:191], a[188:191], v[160:163], v[188:191]\n"
      "v_mfma_f32_16x16x32_bf16 v[136:139], a[156:159], v[208:211], v[136:139]\n"
      "v_mfma_f32_16x16x32_bf16 v[136:139], a[192:195], v[160:163], v[136:139]\n"
      "s_nop 1\n"
      "v_fma_f32 v150, v180, v180, s38\n"
      "v_mfma_f32_16x16x32_bf16 v[240:243], a[160:163], v[208:211], v[240:243]\n"
      "v_fmac_f32 v150, v181, v181\n"
      "v_fmac_f32_e64 v150, v182, v182\n"
      "v_mfma_f32_16x16x32_bf16 v[240:243], a[196:199], v[160:163], v[240:243]\n"
      "s_nop 0\n"
      "v_fma_f32 v151, v136, v136, s38\n"
      "v_fmac_f32 v151, v137, v137\n"
      "v_mfma_f32_16x16x32_bf16 v[200:203], a[164:167], v[208:211], v[200:203]\n"
      "v_fmac_f32 v151, v138, v138\n"
      "v_fmac_f32 v150, v183, v183\n"
      "v_mfma_f32_16x16x32_bf16 v[200:203], a[200:203], v[160:163], v[200:203]\n"
      "v_fmac_f32 v150, v140, v140\n"
      "v_fmac_f32 v151, v139, v139\n"
      "v_mfma_f32_16x16x32_f16 v[172:175], v[224:227], v[168:171], v[70:73]\n"
      "v_fmac_f32 v150, v141, v141\n"
      "v_fmac_f32 v150, v142, v142\n"
      "v_mfma_f32_16x16x32_f16 v[172:175], v[252:255], v[196:199], v[172:175]\n"
      "v_fmac_f32 v151, v240, v240\n"
      "v_fmac_f32 v151, v241, v241\n"
      "v_mfma_f32_16x16x32_f16 v[172:175], v[220:223], v[32:35], v[172:175]\n"
      "v_fmac_f32 v150, v143, v143\n"
      "v_fmac_f32 v151, v242, v242\n"
      "v_fmac_f32 v150, v188, v188\n"
      "v_fmac_f32 v150, v189, v189\n"
      "v_fmac_f32 v151, v243, v243\n"
      "v_fmac_f32 v151, v200, v200\n"
      "v_fmac_f32 v151, v201, v201\n"
      "v_fmac_f32 v151, v202, v202\n"
      "v_fmac_f32 v150, v190, v190\n"
      "v_fmac_f32 v150, v191, v191\n"
      "v_fmac_f32 v151, v203, v203\n"
      "v_mov_b32 v149, v173\n"
      "v_mov_b32 v148, v172\n"
      "v_permlane16_swap_b32 v150, v151\n"
      "v_add_f32 v150, v150, v151\n"
      "v_mov_b32 v151, v150\n"
      "s_nop 1\n"
      "v_permlane32_swap_b32 v150, v151\n"
      "v_add_f32 v150, v150, v151\n"
      "v_rsq_f32 v150, v150\n"
      "s_nop 0\n"
      "v_mov_b32 v151, v150\n"
      "s_nop 1\n"
      "v_permlane16_swap_b32 v150, v151\n"
      "v_mul_f32 v136, v151, v136\n"
      "v_mul_f32 v139, v151, v139\n"
      "v_mul_f32 v137, v151, v137\n"
      "v_mul_f32 v241, v151, v241\n"
      "v_mul_f32 v138, v151, v138\n"
      "v_mul_f32 v201, v151, v201\n"
      "v_mul_f32 v200, v151, v200\n"
      "v_mul_f32 v240, v151, v240\n"
      "v_mul_f32 v150, s36, v150\n"
      "v_cvt_pk_f16_f32 v136, v136, v137\n"
      "v_cvt_pk_f16_f32 v137, v138, v139\n"
      "v_cvt_pk_f16_f32 v46, v200, v201\n"
      "v_mul_f32 v140, v150, v140\n"
      "v_mul_f32 v242, v151, v242\n"
      "v_mul_f32 v202, v151, v202\n"
      "v_mul_f32 v180, v150, v180\n"
      "v_mul_f32 v143, v150, v143\n"
      "v_mul_f32 v203, v151, v203\n"
      "v_mul_f32 v243, v151, v243\n"
      "v_cvt_pk_f16_f32 v47, v202, v203\n"
      "v_cvt_pk_f16_f32 v139, v242, v243\n"
      "v_mul_f32 v188, v150, v188\n"
      "v_mul_f32 v182, v150, v182\n"
      "v_cvt_pk_f16_f32 v138, v240, v241\n"
      "v_mul_f32 v142, v150, v142\n"
      "v_mul_f32 v141, v150, v141\n"
      "v_mul_f32 v183, v150, v183\n"
      "v_mfma_f32_16x16x32_f16 v[240:243], v[136:139], v[168:171], v[86:89]\n"
      "v_mul_f32 v181, v150, v181\n"
      "v_cvt_pk_f16_f32 v180, v180, v181\n"
      "v_mfma_f32_16x16x32_f16 v[240:243], v[46:49], v[32:35], v[240:243]\n"
      "v_cvt_pk_f16_f32 v181, v182, v183\n"
      "v_cvt_pk_f16_f32 v182, v140, v141\n"
      "v_mul_f32_e64 v191, v150, v191\n"
      "v_cvt_pk_f16_f32 v183, v142, v143\n"
      "v_mfma_f32_16x16x32_f16 v[140:143], v[196:199], v[168:171], v[78:81]\n"
      "v_mul_f32_e64 v189, v150, v189\n"
      "v_cvt_pk_f16_f32 v44, v188, v189\n"
      "v_mfma_f32_16x16x32_f16 v[140:143], v[34:37], v[32:35], v[140:143]\n"
      "v_mul_f32 v190, v150, v190\n"
      "v_cvt_pk_f16_f32 v45, v190, v191\n"
      "v_mfma_f32_16x16x32_f16 v[188:191], v[164:167], v[168:171], v[82:85]\n"
      "v_mfma_f32_16x16x32_f16 v[188:191], v[40:43], v[32:35], v[188:191]\n"
      "s_nop 7\n"
      "s_or_b64 vcc, s[48:49], s[50:51]\n"
      "s_cbranch_scc0 L_fast_31_%=\n"
      "L_ok_146_%=:\n"
      "s_cmp_eq_u64 s[48:49], 0\n"
      "s_cbranch_scc1 L_go_147_%=\n"
      "s_mov_b32 m0, s27\n"
      "s_nop 0\n"
      "s_movrels_b32 s9, s80\n"
      "s_lshr_b32 s8, s9, 8\n"
      "s_and_b32 s9, s9, 0xff\n"
      "s_mul_i32 s10, s8, 73\n"
      "s_add_u32 s10, s10, s9\n"
      "s_mul_i32 s30, s10, 4802\n"
      "s_add_u32 s30, s30, s18\n"
      "s_and_b32 s29, s30, 15\n"
      "s_add_u32 s30, s29, 110592\n"
      "s_lshl_b64 s[34:35], s[48:49], 4\n"
      "s_lshr_b64 s[34:35], s[34:35], 5\n"
      "s_cbranch_scc1 L_gen_29_%=\n"
      "v_mbcnt_lo_u32_b32 v150, -1, 0\n"
      "v_mbcnt_hi_u32_b32 v150, -1, v150\n"
      "v_lshlrev_b32 v151, 1, v150\n"
      "v_add_u32 v173, 4802, v151\n"
      "v_cmp_gt_u32 vcc, s29, v151\n"
      "s_nop 1\n"
      "v_cndmask_b32 v151, v173, v151, vcc\n"
      "v_mov_b32 v173, 0\n"
      "v_add_u32 v151, 110592, v151\n"
      "s_mov_b64 exec, 0x7f\n"
      "ds_write_b16 v151, v173\n"
      "s_mov_b64 exec, -1\n"
      "v_lshlrev_b32_e64 v151, 4, v150\n"
      "v_add_u32 v151, 110592, v151\n"
      "ds_read_b128 v[172:175], v151\n"
      "ds_read_b128 v[156:159], v151 offset:1024\n"
      "v_min_u32 v150, 111296, v151\n"
      "ds_read_b128 v[204:207], v151 offset:2048\n"
      "ds_read_b128 v[216:219], v150 offset:4096\n"
      "ds_read_b128 v[168:171], v151 offset:3072\n"
      "s_waitcnt lgkmcnt(4)\n"
      "v_or3_b32 v172, v172, v173, v174\n"
      "s_waitcnt lgkmcnt(3)\n"
      "v_or3_b32 v172, v172, v175, v156\n"
      "v_or3_b32 v172, v172, v157, v158\n"
      "s_waitcnt lgkmcnt(2)\n"
      "v_or3_b32 v172, v172, v159, v204\n"
      "v_or3_b32 v172, v172, v205, v206\n"
      "s_waitcnt lgkmcnt(0)\n"
      "v_or3_b32 v172, v172, v207, v168\n"
      "v_or3_b32 v172, v172, v169, v170\n"
      "v_or3_b32 v172, v172, v171, v216\n"
      "v_or3_b32 v172, v172, v217, v218\n"
      "v_bitop3_b32 v172, v172, v219, s40 bitop3:0xa8\n"
      "v_cmp_ne_u32 s[48:49], 0, v172\n"
      "s_cmp_lg_u64 s[48:49], 0\n"
      "s_cbranch_scc1 L_gen_29_%=\n"
      "s_cmp_eq_u64 s[50:51], 0\n"
      "s_cbranch_scc1 L_fast_31_%=\n"
      "L_go_147_%=:\n"
      "v_mfma_f32_16x16x32_f16 v[172:175], v[196:199], v[144:147], v[90:93]\n"
      "ds_read_b128 v[156:159], v128 offset:3072\n"
      "v_max3_f32 v151, v140, v141, v142\n"
      "v_mfma_f32_16x16x32_f16 v[172:175], v[34:37], v[38:41], v[172:175]\n"
      "v_max3_f32 v150, v143, v188, v189\n"
      "v_max3_f32 v150, v150, v240, v241\n"
      "v_mfma_f32_16x16x32_f16 v[204:207], v[164:167], v[144:147], v[94:97]\n"
      "v_max3_f32 v151, v151, v190, v191\n"
      "v_max3_f32 v151, v151, v242, v243\n"
      "v_mfma_f32_16x16x32_f16 v[204:207], v[40:43], v[38:41], v[204:207]\n"
      "v_max3_f32 v151, v151, v150, v148\n"
      "v_mov_b32 v150, v151\n"
      "v_mfma_f32_16x16x32_f16 v[216:219], v[224:227], v[144:147], v[72:75]\n"
      "s_nop 0\n"
      "v_permlane16_swap_b32 v151, v150\n"
      "v_mfma_f32_16x16x32_f16 v[216:219], v[252:255], v[164:167], v[216:219]\n"
      "v_max_f32 v151, v151, v150\n"
      "v_mov_b32 v150, v151\n"
      "v_mfma_f32_16x16x32_f16 v[216:219], v[220:223], v[38:41], v[216:219]\n"
      "s_nop 0\n"
      "v_permlane32_swap_b32 v151, v150\n"
      "v_max_f32 v151, v151, v150\n"
      "v_sub_f32 v191, v191, v151\n"
      "v_sub_f32 v243, v243, v151\n"
      "v_mfma_f32_16x16x32_f16 v[168:171], v[136:139], v[144:147], v[98:101]\n"
      "v_sub_f32 v142, v142, v151\n"
      "v_sub_f32 v141, v141, v151\n"
      "v_mfma_f32_16x16x32_f16 v[168:171], v[46:49], v[38:41], v[168:171]\n"
      "s_waitcnt lgkmcnt(0)\n"
      "v_bitop3_b32 v156, v156, v157, s4 bitop3:0x7e\n"
      "v_sub_f32 v148, v148, v151\n"
      "v_sub_f32 v143, v143, v151\n"
      "v_sub_f32 v190, v190, v151\n"
      "v_exp_f32 v142, v142\n"
      "v_exp_f32 v143, v143\n"
      "v_sub_f32 v140, v140, v151\n"
      "v_exp_f32 v148, v148\n"
      "v_sub_f32 v188, v188, v151\n"
      "v_exp_f32 v141, v141\n"
      "v_exp_f32 v140, v140\n"
      "v_sub_f32 v241, v241, v151\n"
      "v_exp_f32 v190, v190\n"
      "v_exp_f32 v241, v241\n"
      "v_sub_f32 v240, v240, v151\n"
      "v_exp_f32 v240, v240\n"
      "v_sub_f32 v189, v189, v151\n"
      "v_cvt_pk_bf16_f32 v140, v140, v141\n"
      "v_exp_f32 v191, v191\n"
      "v_cvt_pk_bf16_f32 v62, v240, v241\n"
      "v_exp_f32 v189, v189\n"
      "v_cvt_pk_bf16_f32 v141, v142, v143\n"
      "v_exp_f32 v188, v188\n"
      "v_cvt_pk_bf16_f32 v143, v190, v191\n"
      "v_sub_f32 v242, v242, v151\n"
      "v_exp_f32 v242, v242\n"
      "v_cvt_pk_bf16_f32 v142, v188, v189\n"
      "v_exp_f32_e64 v243, v243\n"
      "v_cvt_pk_bf16_f32 v64, v148, 0\n"
      "v_bitop3_b32 v158, v158, v159, s4 bitop3:0x7e\n"
      "v_cvt_pk_bf16_f32 v63, v242, v243\n"
      "v_mfma_f32_16x16x32_bf16 v[188:191], v[66:69], v[140:143], 0\n"
      "v_or_b32_e64 v156, v156, v158\n"
      "v_cmp_ne_u32 vcc, 0, v156\n"
      "v_mfma_f32_16x16x32_bf16 v[188:191], v[66:69], v[62:65], v[188:191]\n"
      "s_mov_b64 s[48:49], vcc\n"
      "v_max3_f32 v219, v172, v173, v174\n"
      "v_max3_f32 v218, v175, v204, v205\n"
      "v_mfma_f32_16x16x32_bf16 v[144:147], v[184:187], v[140:143], 0\n"
      "s_cbranch_vccnz L_flg_148_%=\n"
      "L_fb_149_%=:\n"
      "v_mfma_f32_16x16x32_bf16 v[144:147], v[50:53], v[62:65], v[144:147]\n"
      "s_nop 0\n"
      "v_max3_f32 v218, v218, v168, v169\n"
      "v_max3_f32 v219, v219, v206, v207\n"
      "v_mfma_f32_16x16x32_bf16 v[156:159], v[176:179], v[140:143], 0\n"
      "v_max3_f32 v219, v219, v170, v171\n"
      "v_max3_f32 v219, v219, v218, v216\n"
      "v_mfma_f32_16x16x32_bf16 v[156:159], v[54:57], v[62:65], v[156:159]\n"
      "v_mov_b32 v218, v219\n"
      "v_rcp_f32 v188, v188\n"
      "v_mfma_f32_16x16x32_bf16 v[240:243], v[244:247], v[140:143], 0\n"
      "v_permlane16_swap_b32_e64 v219, v218\n"
      "v_mfma_f32_16x16x32_bf16 v[240:243], v[58:61], v[62:65], v[240:243]\n"
      "v_max_f32 v219, v219, v218\n"
      "v_mov_b32 v218, v219\n"
      "v_mul_f32 v144, v188, v144\n"
      "v_mul_f32 v145, v188, v145\n"
      "v_permlane32_swap_b32 v219, v218\n"
      "v_max_f32 v219, v219, v218\n"
      "v_sub_f32 v173, v173, v219\n"
      "v_sub_f32 v204, v204, v219\n"
      "v_sub_f32 v207, v207, v219\n"
      "v_sub_f32 v172, v172, v219\n"
      "v_sub_f32 v174, v174, v219\n"
      "v_sub_f32 v206, v206, v219\n"
      "v_exp_f32 v204, v204\n"
      "v_sub_f32 v175, v175, v219\n"
      "v_sub_f32 v216, v216, v219\n"
      "v_sub_f32 v205, v205, v219\n"
      "v_exp_f32 v172, v172\n"
      "v_exp_f32 v174, v174\n"
      "v_exp_f32 v175, v175\n"
      "v_sub_f32 v169, v169, v219\n"
      "v_mfma_f32_16x16x32_f16 v[140:143], v[196:199], v[180:183], v[102:105]\n"
      "v_sub_f32 v170, v170, v219\n"
      "v_sub_f32 v171, v171, v219\n"
      "v_mfma_f32_16x16x32_f16 v[140:143], v[34:37], v[44:47], v[140:143]\n"
      "v_exp_f32 v170, v170\n"
      "v_exp_f32 v173, v173\n"
      "v_mfma_f32_16x16x32_f16 v[196:199], v[164:167], v[180:183], v[106:109]\n"
      "ds_read_b128 v[164:167], v120 offset:18432\n"
      "v_cvt_pk_bf16_f32 v172, v172, v173\n"
      "v_mfma_f32_16x16x32_f16 v[196:199], v[40:43], v[44:47], v[196:199]\n"
      "v_exp_f32 v206, v206\n"
      "v_exp_f32 v169, v169\n"
      "v_sub_f32 v168, v168, v219\n"
      "v_mul_f32 v146, v188, v146\n"
      "v_mfma_f32_16x16x32_f16 v[208:211], v[224:227], v[180:183], v[74:77]\n"
      "v_cvt_pk_bf16_f32 v144, v144, v145\n"
      "v_cvt_pk_bf16_f32 v173, v174, v175\n"
      "v_mfma_f32_16x16x32_f16 v[208:211], v[252:255], v[136:139], v[208:211]\n"
      "v_exp_f32 v205, v205\n"
      "v_mfma_f32_16x16x32_f16 v[208:211], v[220:223], v[44:47], v[208:211]\n"
      "v_exp_f32 v171, v171\n"
      "v_mfma_f32_16x16x32_f16 v[190:193], v[136:139], v[180:183], v[110:113]\n"
      "v_exp_f32 v168, v168\n"
      "v_mfma_f32_16x16x32_f16 v[190:193], v[46:49], v[44:47], v[190:193]\n"
      "v_cvt_pk_bf16_f32 v174, v204, v205\n"
      "v_exp_f32 v207, v207\n"
      "v_mfma_f32_16x16x32_f16 v[180:183], v[252:255], v[224:227], v[76:79]\n"
      "s_waitcnt lgkmcnt(0)\n"
      "v_cvt_pk_bf16_f32 v63, v170, v171\n"
      "v_mul_f32 v147, v188, v147\n"
      "v_cvt_pk_bf16_f32 v62, v168, v169\n"
      "v_cvt_pk_bf16_f32 v175, v206, v207\n"
      "v_exp_f32_e64 v216, v216\n"
      "v_mfma_f32_16x16x16_f16 v[180:183], v[222:223], v[220:221], v[180:183]\n"
      "buffer_store_dwordx4 v[164:167], v117, s[20:23], 0 offen offset:0 sc1\n"
      "v_cvt_pk_bf16_f32 v64, v216, 0\n"
      "v_mfma_f32_16x16x32_bf16 v[220:223], v[66:69], v[172:175], 0\n"
      "v_mul_f32 v156, v188, v156\n"
      "v_cvt_pk_bf16_f32 v145, v146, v147\n"
      "v_mfma_f32_16x16x32_bf16 v[220:223], v[66:69], v[62:65], v[220:223]\n"
      "v_mul_f32 v157, v188, v157\n"
      "v_cvt_pk_bf16_f32 v146, v156, v157\n"
      "v_mfma_f32_16x16x32_bf16 v[204:207], v[184:187], v[172:175], 0\n"
      "v_mul_f32 v159, v188, v159\n"
      "v_mul_f32 v158, v188, v158\n"
      "v_mfma_f32_16x16x32_bf16 v[204:207], v[50:53], v[62:65], v[204:207]\n"
      "v_cvt_pk_bf16_f32 v147, v158, v159\n"
      "v_mul_f32 v240, v188, v240\n"
      "v_mfma_f32_16x16x32_bf16 v[156:159], v[176:179], v[172:175], 0\n"
      "v_mul_f32 v241, v188, v241\n"
      "v_cvt_pk_bf16_f32 v240, v240, v241\n"
      "v_mfma_f32_16x16x32_bf16 v[156:159], v[54:57], v[62:65], v[156:159]\n"
      "ds_write_b128 v124, v[144:147]\n"
      "v_mfma_f32_16x16x32_bf16 v[144:147], v[244:247], v[172:175], 0\n"
      "v_mul_f32 v242, v188, v242\n"
      "v_mul_f32 v243, v188, v243\n"
      "v_mfma_f32_16x16x32_bf16 v[144:147], v[58:61], v[62:65], v[144:147]\n"
      "v_max3_f32 v188, v140, v141, v142\n"
      "v_max3_f32 v189, v143, v196, v197\n"
      "v_max3_f32 v189, v189, v190, v191\n"
      "v_max3_f32 v188, v188, v198, v199\n"
      "v_max3_f32 v188, v188, v192, v193\n"
      "v_max3_f32 v188, v188, v189, v208\n"
      "v_mov_b32_e64 v189, v188\n"
      "v_cvt_pk_bf16_f32 v241, v242, v243\n"
      "v_rcp_f32 v220, v220\n"
      "v_permlane16_swap_b32 v188, v189\n"
      "v_max_f32 v188, v188, v189\n"
      "v_mov_b32 v189, v188\n"
      "v_mul_f32 v205, v220, v205\n"
      "v_mul_f32 v204, v220, v204\n"
      "v_permlane32_swap_b32 v188, v189\n"
      "v_max_f32 v188, v188, v189\n"
      "v_sub_f32 v191, v191, v188\n"
      "v_sub_f32 v142, v142, v188\n"
      "v_sub_f32 v141, v141, v188\n"
      "v_sub_f32 v140, v140, v188\n"
      "v_sub_f32 v198, v198, v188\n"
      "v_sub_f32 v208, v208, v188\n"
      "v_sub_f32 v197, v197, v188\n"
      "v_exp_f32 v141, v141\n"
      "v_sub_f32 v192, v192, v188\n"
      "v_exp_f32 v198, v198\n"
      "v_exp_f32 v191, v191\n"
      "v_sub_f32 v199, v199, v188\n"
      "v_exp_f32 v192, v192\n"
      "v_exp_f32 v208, v208\n"
      "v_exp_f32 v197, v197\n"
      "v_sub_f32 v196, v196, v188\n"
      "v_sub_f32 v143, v143, v188\n"
      "v_sub_f32 v190, v190, v188\n"
      "v_exp_f32 v199, v199\n"
      "v_exp_f32 v140, v140\n"
      "v_exp_f32 v190, v190\n"
      "v_sub_f32 v193, v193, v188\n"
      "v_exp_f32 v142, v142\n"
      "v_exp_f32 v143, v143\n"
      "v_cvt_pk_bf16_f32 v64, v208, 0\n"
      "v_exp_f32_e64 v196, v196\n"
      "v_cvt_pk_bf16_f32 v140, v140, v141\n"
      "v_cvt_pk_bf16_f32 v141, v142, v143\n"
      "v_cvt_pk_bf16_f32 v142, v196, v197\n"
      "v_mul_f32 v206, v220, v206\n"
      "v_exp_f32 v193, v193\n"
      "v_cvt_pk_bf16_f32 v143, v198, v199\n"
      "v_mul_f32_e64 v207, v220, v207\n"
      "v_cvt_pk_bf16_f32 v62, v190, v191\n"
      "v_cvt_pk_bf16_f32 v63, v192, v193\n"
      "v_mfma_f32_16x16x32_bf16 v[164:167], v[66:69], v[140:143], 0\n"
      "ds_read_b128 v[224:227], v121 offset:18432\n"
      "v_cvt_pk_bf16_f32 v204, v204, v205\n"
      "v_mfma_f32_16x16x32_bf16 v[164:167], v[66:69], v[62:65], v[164:167]\n"
      "ds_read_b128 v[168:171], v123 offset:22528\n"
      "v_cvt_pk_bf16_f32 v205, v206, v207\n"
      "v_mfma_f32_16x16x32_bf16 v[196:199], v[184:187], v[140:143], 0\n"
      "ds_read_b128 v[252:255], v123 offset:19456\n"
      "v_mul_f32_e64 v156, v220, v156\n"
      "v_mfma_f32_16x16x32_bf16 v[196:199], v[50:53], v[62:65], v[196:199]\n"
      "v_mul_f32 v145, v220, v145\n"
      "v_mul_f32 v158, v220, v158\n"
      "v_mul_f32_e64 v157, v220, v157\n"
      "v_mfma_f32_16x16x32_bf16 v[136:139], v[244:247], v[140:143], 0\n"
      "ds_read_b128 v[192:195], v123 offset:25600\n"
      "v_mul_f32_e64 v159, v220, v159\n"
      "v_mfma_f32_16x16x32_bf16 v[136:139], v[58:61], v[62:65], v[136:139]\n"
      "v_cvt_pk_bf16_f32 v206, v156, v157\n"
      "v_cvt_pk_bf16_f32 v207, v158, v159\n"
      "v_mfma_f32_16x16x32_bf16 v[172:175], v[176:179], v[140:143], 0\n"
      "v_mul_f32_e64 v146, v220, v146\n"
      "v_mul_f32_e64 v144, v220, v144\n"
      "v_mfma_f32_16x16x32_bf16 v[172:175], v[54:57], v[62:65], v[172:175]\n"
      "ds_write_b128 v124, v[204:207] offset:1024\n"
      "v_cndmask_b32 v149, v149, v217, s[42:43]\n"
      "v_mul_f32_e64 v147, v220, v147\n"
      "v_cvt_pk_bf16_f32 v144, v144, v145\n"
      "v_cvt_pk_bf16_f32 v145, v146, v147\n"
      "v_cndmask_b32 v149, v149, v209, s[44:45]\n"
      "v_cndmask_b32 v149, v149, v181, s[46:47]\n"
      "s_waitcnt lgkmcnt(2)\n"
      "v_mfma_f32_16x16x32_bf16 v[156:159], a[216:219], v[252:255], 0\n"
      "ds_read_b128 v[140:143], v123 offset:28672\n"
      "v_max_f32_dpp v242, v149, v149 row_ror:8 row_mask:0xf bank_mask:0xf\n"
      "v_mfma_f32_16x16x32_bf16 v[156:159], a[228:231], v[168:171], v[156:159]\n"
      "ds_write2st64_b64 v125, v[240:241], v[144:145] offset0:0 offset1:2\n"
      "v_max_f32_dpp v242, v242, v242 row_ror:4 row_mask:0xf bank_mask:0xf\n"
      "s_mov_b32 exec_hi, 0\n"
      "buffer_store_dwordx4 v[224:227], v118, s[20:23], 0 offen offset:0 sc1\n"
      "s_mov_b32 exec_hi, -1\n"
      "v_mfma_f32_16x16x32_bf16 v[188:191], a[220:223], v[252:255], 0\n"
      "s_nop 0\n"
      "v_max_f32_dpp v242, v242, v242 row_ror:2 row_mask:0xf bank_mask:0xf\n"
      "v_mfma_f32_16x16x32_bf16 v[144:147], a[224:227], v[252:255], 0\n"
      "ds_read_b128 v[220:223], v123 offset:34816\n"
      "v_max_f32_dpp v242, v242, v242 row_ror:1 row_mask:0xf bank_mask:0xf\n"
      "v_mfma_f32_16x16x32_bf16 v[144:147], a[236:239], v[168:171], v[144:147]\n"
      "ds_read_b128 v[180:183], v123 offset:31744\n"
      "v_max_f32_dpp v242, v242, v242 row_bcast:15 row_mask:0xa bank_mask:0xf\n"
      "s_add_u32 s28, s27, 2\n"
      "v_mfma_f32_16x16x32_bf16 v[188:191], a[232:235], v[168:171], v[188:191]\n"
      "s_nop 0\n"
      "v_max_f32_dpp v242, v242, v242 row_bcast:31 row_mask:0xc bank_mask:0xf\n"
      "s_waitcnt lgkmcnt(3)\n"
      "v_readlane_b32 s31, v242, 63\n"
      "v_mfma_f32_16x16x32_bf16 v[156:159], a[240:243], v[192:195], v[156:159]\n"
      "ds_read_b128 v[224:227], v123 offset:20480\n"
      "v_subrev_f32 v149, s31, v149\n"
      "v_mfma_f32_16x16x32_bf16 v[156:159], a[252:255], v[140:143], v[156:159]\n"
      "ds_read_b128 v[204:207], v123 offset:23552\n"
      "v_exp_f32 v149, v149\n"
      "v_mfma_f32_16x16x32_bf16 v[188:191], a[244:247], v[192:195], v[188:191]\n"
      "s_min_u32 m0, s28, s3\n"
      "v_cvt_pk_bf16_f32 v149, v149, v149\n"
      "s_movrels_b32 s9, s80\n"
      "v_mfma_f32_16x16x32_bf16 v[188:191], v[0:3], v[140:143], v[188:191]\n"
      "ds_write_b16 v130, v149\n"
      "v_mfma_f32_16x16x32_bf16 v[144:147], a[248:251], v[192:195], v[144:147]\n"
      "ds_read_b128 v[168:171], v131\n"
      "s_lshr_b32 s8, s9, 8\n"
      "v_mfma_f32_16x16x32_bf16 v[144:147], v[4:7], v[140:143], v[144:147]\n"
      "ds_read_b128 v[208:211], v131 offset:64\n"
      "s_waitcnt lgkmcnt(5)\n"
      "v_mfma_f32_16x16x32_bf16 v[156:159], v[8:11], v[180:183], v[156:159]\n"
      "s_and_b32 s9, s9, 0xff\n"
      "s_mul_i32 s10, s8, 73\n"
      "s_add_u32 s10, s10, s9\n"
      "v_mfma_f32_16x16x32_bf16 v[156:159], v[20:23], v[220:223], v[156:159]\n"
      "s_mul_i32 s28, s10, 4802\n"
      "s_add_u32 s56, s18, s28\n"
      "v_mfma_f32_16x16x32_bf16 v[188:191], v[12:15], v[180:183], v[188:191]\n"
      "s_addc_u32 s57, s19, 0\n"
      "s_and_b32 s56, s56, -16\n"
      "s_sub_u32 s58, s56, 110592\n"
      "v_mfma_f32_16x16x32_bf16 v[188:191], v[24:27], v[220:223], v[188:191]\n"
      "s_waitcnt lgkmcnt(3)\n"
      "v_fma_f32 v228, v156, v156, s39\n"
      "v_fmac_f32 v228, v157, v157\n"
      "v_mfma_f32_16x16x32_bf16 v[144:147], v[16:19], v[180:183], v[144:147]\n"
      "ds_read_b128 v[216:219], v123 offset:26624\n"
      "v_fmac_f32 v228, v158, v158\n"
      "v_mfma_f32_16x16x32_bf16 v[144:147], v[28:31], v[220:223], v[144:147]\n"
      "ds_read_b128 v[192:195], v123 offset:29696\n"
      "v_fmac_f32 v228, v159, v159\n"
      "v_mfma_f32_16x16x32_bf16 v[180:183], a[216:219], v[224:227], 0\n"
      "ds_read_b128 v[148:151], v114 offset:18432\n"
      "v_fmac_f32 v228, v188, v188\n"
      "v_mfma_f32_16x16x32_bf16 v[180:183], a[228:231], v[204:207], v[180:183]\n"
      "ds_read_b128 v[140:143], v115 offset:18432\n"
      "v_fmac_f32 v228, v189, v189\n"
      "v_fmac_f32 v228, v190, v190\n"
      "v_mfma_f32_16x16x32_bf16 v[220:223], a[220:223], v[224:227], 0\n"
      "ds_read_b128 v[252:255], v123 offset:32768\n"
      "v_fmac_f32 v228, v191, v191\n"
      "v_mfma_f32_16x16x32_bf16 v[220:223], a[232:235], v[204:207], v[220:223]\n"
      "s_subb_u32 s59, s57, 0\n"
      "v_fmac_f32 v228, v144, v144\n"
      "v_fmac_f32 v228, v145, v145\n"
      "v_mfma_f32_16x16x32_bf16 v[240:243], a[224:227], v[224:227], 0\n"
      "v_fmac_f32 v228, v146, v146\n"
      "v_mfma_f32_16x16x32_bf16 v[240:243], a[236:239], v[204:207], v[240:243]\n"
      "ds_read_b128 v[160:163], v123 offset:35840\n"
      "v_fmac_f32 v228, v147, v147\n"
      "s_sub_u32 s56, s56, 73728\n"
      "s_nop 0\n"
      "v_permlane16_swap_b32 v230, v228\n"
      "s_waitcnt lgkmcnt(4)\n"
      "v_mfma_f32_16x16x32_bf16 v[180:183], a[240:243], v[216:219], v[180:183]\n"
      "s_subb_u32 s57, s57, 0\n"
      "s_mul_i32 s28, s8, 1373568\n"
      "s_mul_i32 s29, s9, 2688\n"
      "v_mfma_f32_16x16x32_bf16 v[180:183], a[252:255], v[192:195], v[180:183]\n"
      "s_add_u32 s28, s28, s29\n"
      "s_cmp_eq_u32 s8, 72\n"
      "v_mfma_f32_16x16x32_bf16 v[220:223], a[244:247], v[216:219], v[220:223]\n"
      "s_cselect_b32 s8, 0, 100270464\n"
      "s_cmp_eq_u32 s9, 72\n"
      "v_add_f32 v230, v230, v228\n"
      "v_mfma_f32_16x16x32_bf16 v[220:223], v[0:3], v[192:195], v[220:223]\n"
      "s_waitcnt lgkmcnt(1)\n"
      "s_cselect_b32 s9, 0, 196224\n"
      "v_mfma_f32_16x16x32_bf16 v[240:243], a[248:251], v[216:219], v[240:243]\n"
      "s_add_u32 s10, s8, s9\n"
      "ds_read_b128 v[204:207], v114 offset:24576\n"
      "v_mfma_f32_16x16x32_bf16 v[240:243], v[4:7], v[192:195], v[240:243]\n"
      "ds_read_b128 v[216:219], v115 offset:24576\n"
      "v_mfma_f32_16x16x32_bf16 v[224:227], a[12:15], v[148:151], 0\n"
      "s_add_u32 s28, s28, s10\n"
      "s_add_u32 s52, s16, s28\n"
      "v_mfma_f32_16x16x32_bf16 v[224:227], a[48:51], v[140:143], v[224:227]\n"
      "s_waitcnt lgkmcnt(2)\n"
      "s_addc_u32 s53, s17, 0\n"
      "v_mfma_f32_16x16x32_bf16 v[180:183], v[8:11], v[252:255], v[180:183]\n"
      "s_add_u32 s27, s27, 1\n"
      "v_mfma_f32_16x16x32_bf16 v[180:183], v[20:23], v[160:163], v[180:183]\n"
      "s_cmp_ge_u32 s27, s26\n"
      "v_mfma_f32_16x16x32_bf16 v[220:223], v[12:15], v[252:255], v[220:223]\n"
      "s_cselect_b32 s28, 1, 0\n"
      "s_xor_b32 s29, s10, s11\n"
      "v_mfma_f32_16x16x32_bf16 v[220:223], v[24:27], v[160:163], v[220:223]\n"
      "v_mfma_f32_16x16x32_bf16 v[240:243], v[16:19], v[252:255], v[240:243]\n"
      "v_mfma_f32_16x16x32_bf16 v[240:243], v[28:31], v[160:163], v[240:243]\n"
      "v_mfma_f32_16x16x32_bf16 v[160:163], v[168:171], v[66:69], 0\n"
      "s_waitcnt lgkmcnt(0)\n"
      "v_fma_f32 v228, v180, v180, s39\n"
      "s_or_b32 s28, s28, s29\n"
      "v_mfma_f32_16x16x32_bf16 v[160:163], v[208:211], v[66:69], v[160:163]\n"
      "v_fmac_f32 v228, v181, v181\n"
      "v_fmac_f32 v228, v182, v182\n"
      "v_mfma_f32_16x16x32_bf16 v[252:255], v[168:171], v[184:187], 0\n"
      "v_fmac_f32 v228, v183, v183\n"
      "v_fmac_f32 v228, v220, v220\n"
      "v_mfma_f32_16x16x32_bf16 v[252:255], v[208:211], v[50:53], v[252:255]\n"
      "v_fmac_f32 v228, v221, v221\n"
      "v_fmac_f32 v228, v222, v222\n"
      "v_mfma_f32_16x16x32_bf16 v[192:195], v[168:171], v[176:179], 0\n"
      "v_fmac_f32 v228, v223, v223\n"
      "v_fmac_f32 v228, v240, v240\n"
      "v_mfma_f32_16x16x32_bf16 v[192:195], v[208:211], v[54:57], v[192:195]\n"
      "v_fmac_f32 v228, v241, v241\n"
      "v_fmac_f32 v228, v242, v242\n"
      "v_mfma_f32_16x16x32_bf16 v[200:203], v[168:171], v[244:247], 0\n"
      "v_fmac_f32 v228, v243, v243\n"
      "v_mov_b32 v229, v228\n"
      "v_mfma_f32_16x16x32_bf16 v[200:203], v[208:211], v[58:61], v[200:203]\n"
      "s_nop 0\n"
      "v_permlane16_swap_b32 v228, v229\n"
      "v_mfma_f32_16x16x32_bf16 v[184:187], a[0:3], v[148:151], 0\n"
      "v_mov_b32_dpp v160, v160 quad_perm:[0,0,0,0] row_mask:0xf bank_mask:0xf\n"
      "v_add_f32 v228, v228, v229\n"
      "v_mfma_f32_16x16x32_bf16 v[184:187], a[36:39], v[140:143], v[184:187]\n"
      "s_nop 0\n"
      "v_permlane32_swap_b32 v230, v228\n"
      "v_mfma_f32_16x16x32_bf16 v[208:211], a[4:7], v[148:151], 0\n"
      "v_add_f32 v230, v230, v228\n"
      "v_mfma_f32_16x16x32_bf16 v[208:211], a[40:43], v[140:143], v[208:211]\n"
      "ds_write_b32 v129, v230\n"
      "v_mfma_f32_16x16x32_bf16 v[176:179], a[8:11], v[148:151], 0\n"
      "v_cndmask_b32 v252, v252, v192, s[42:43]\n"
      "v_cndmask_b32 v252, v252, v200, s[44:45]\n"
      "v_mfma_f32_16x16x32_bf16 v[176:179], a[44:47], v[140:143], v[176:179]\n"
      "v_rcp_f32 v160, v160\n"
      "v_mfma_f32_16x16x32_bf16 v[228:231], a[16:19], v[148:151], 0\n"
      "v_mul_f32 v252, v160, v252\n"
      "v_cvt_pk_bf16_f32 v252, v252, v252\n"
      "v_mfma_f32_16x16x32_bf16 v[228:231], a[52:55], v[140:143], v[228:231]\n"
      "ds_write_b16 v135, v252\n"
      "v_mfma_f32_16x16x32_bf16 v[168:171], a[20:23], v[148:151], 0\n"
      "s_nop 0\n"
      "v_mfma_f32_16x16x32_bf16 v[168:171], a[56:59], v[140:143], v[168:171]\n"
      "s_branch L_join_30_%=\n"
      "L_flg_148_%=:\n"
      "v_cmp_gt_i32 vcc, 0, v156\n"
      "s_cbranch_vccz L_fb_149_%=\n"
      "s_sleep 1\n"
      "ds_read_b128 v[156:159], v128 offset:3072\n"
      "s_waitcnt lgkmcnt(0)\n"
      "v_bitop3_b32 v156, v156, v157, s4 bitop3:0x7e\n"
      "v_bitop3_b32 v158, v158, v159, s4 bitop3:0x7e\n"
      "v_or_b32 v156, v156, v158\n"
      "v_cmp_ne_u32 s[48:49], 0, v156\n"
      "s_branch L_flg_148_%=\n"
      "L_gen_29_%=:\n"
      "s_nop 0\n"
      "s_nop 0\n"
      "v_mbcnt_lo_u32_b32 v231, -1, 0\n"
      "v_mbcnt_hi_u32_b32 v231, -1, v231\n"
      "v_lshrrev_b32 v192, 4, v231\n"
      "v_and_b32 v193, 15, v231\n"
      "v_mul_u32_u24 v200, 0x62, v193\n"
      "v_lshl_add_u32 v200, v192, 3, v200\n"
      "v_add_u32 v200, s30, v200\n"
      "v_lshlrev_b32 v192, 2, v192\n"
      "v_sub_u32 v193, v193, v192\n"
      "v_and_b32 v192, 1, v193\n"
      "v_and_b32_e64 v231, -4, v200\n"
      "v_lshrrev_b32 v193, 1, v193\n"
      "s_movk_i32 s31, 0x3fb9\n"
      "ds_read2_b32 v[158:159], v231 offset0:18 offset1:24\n"
      "ds_read2_b32 v[172:173], v231 offset0:16 offset1:17\n"
      "ds_read2_b32 v[150:151], v231 offset0:8 offset1:9\n"
      "ds_read2_b32 v[174:175], v231 offset0:0 offset1:1\n"
      "ds_read2_b32 v[156:157], v231 offset0:2 offset1:10\n"
      "v_lshlrev_b32 v192, 4, v192\n"
      "v_cmp_eq_u32 vcc, 0, v193\n"
      "v_lshlrev_b32 v192, v192, s31\n"
      "v_add_u32 v231, 1568, v231\n"
      "ds_read2_b32 v[206:207], v231 offset0:16 offset1:17\n"
      "ds_read2_b32 v[218:219], v231 offset0:18 offset1:24\n"
      "ds_read2_b32 v[216:217], v231 offset0:2 offset1:10\n"
      "ds_read2_b32 v[228:229], v231 offset0:0 offset1:1\n"
      "ds_read2_b32 v[204:205], v231 offset0:8 offset1:9\n"
      "v_cndmask_b32 v162, 0, v192, vcc\n"
      "v_add_u32 v231, 1568, v231\n"
      "v_cmp_eq_u32 vcc, 1, v193\n"
      "v_and_b32_e64 v200, 2, v200\n"
      "v_mfma_f32_16x16x32_f16 v[232:235], v[164:167], v[144:147], v[94:97]\n"
      "ds_read2_b32 v[168:169], v231 offset0:16 offset1:17\n"
      "ds_read2_b32 v[160:161], v231 offset0:18 offset1:24\n"
      "ds_read2_b32 v[210:211], v231 offset0:8 offset1:9\n"
      "ds_read2_b32 v[208:209], v231 offset0:0 offset1:1\n"
      "s_waitcnt lgkmcnt(10)\n"
      "v_cndmask_b32 v163, 0, v192, vcc\n"
      "v_mfma_f32_16x16x32_f16 v[192:195], v[196:199], v[144:147], v[90:93]\n"
      "s_nop 0\n"
      "v_alignbyte_b32 v174, v175, v174, v200\n"
      "v_mfma_f32_16x16x32_f16 v[192:195], v[34:37], v[38:41], v[192:195]\n"
      "s_waitcnt lgkmcnt(9)\n"
      "v_alignbyte_b32 v172, v173, v172, v200\n"
      "v_alignbyte_b32 v175, v156, v175, v200\n"
      "v_mfma_f32_16x16x32_f16 v[232:235], v[40:43], v[38:41], v[232:235]\n"
      "v_alignbyte_b32 v173, v158, v173, v200\n"
      "v_alignbyte_b32 v159, v159, v159, v200\n"
      "v_mfma_f32_16x16x32_f16 v[248:251], v[224:227], v[144:147], v[72:75]\n"
      "s_waitcnt lgkmcnt(5)\n"
      "ds_read2_b32 v[170:171], v231 offset0:2 offset1:10\n"
      "v_mfma_f32_16x16x32_f16 v[248:251], v[252:255], v[164:167], v[248:251]\n"
      "s_nop 0\n"
      "v_alignbyte_b32 v150, v151, v150, v200\n"
      "v_lshlrev_b32_e64 v159, 16, v159\n"
      "v_alignbyte_b32 v151, v157, v151, v200\n"
      "v_mfma_f32_16x16x16_bf16 v[140:143], v[162:163], v[174:175], v[140:143]\n"
      "s_waitcnt lgkmcnt(5)\n"
      "v_alignbyte_b32 v206, v207, v206, v200\n"
      "v_fmac_f32_e64 v148, s37, v159\n"
      "v_mfma_f32_16x16x32_f16 v[156:159], v[136:139], v[144:147], v[98:101]\n"
      "s_nop 0\n"
      "v_alignbyte_b32 v219, v219, v219, v200\n"
      "v_alignbyte_b32 v204, v205, v204, v200\n"
      "v_mfma_f32_16x16x32_f16 v[156:159], v[46:49], v[38:41], v[156:159]\n"
      "v_alignbyte_b32 v228, v229, v228, v200\n"
      "v_alignbyte_b32 v205, v217, v205, v200\n"
      "v_mfma_f32_16x16x16_bf16 v[188:191], v[162:163], v[150:151], v[188:191]\n"
      "s_waitcnt lgkmcnt(1)\n"
      "v_lshlrev_b32 v219, 16, v219\n"
      "v_alignbyte_b32 v229, v216, v229, v200\n"
      "v_mfma_f32_16x16x16_bf16 v[240:243], v[162:163], v[172:173], v[240:243]\n"
      "ds_read_b128 v[172:175], v128 offset:3072\n"
      "v_max3_f32 v150, v140, v141, v142\n"
      "v_mfma_f32_16x16x32_f16 v[248:251], v[220:223], v[38:41], v[248:251]\n"
      "s_nop 0\n"
      "v_max3_f32 v150, v150, v190, v191\n"
      "v_max3_f32 v151, v143, v188, v189\n"
      "v_mfma_f32_16x16x16_bf16 v[232:235], v[162:163], v[204:205], v[232:235]\n"
      "s_nop 0\n"
      "v_max3_f32 v150, v150, v242, v243\n"
      "v_max3_f32 v151, v151, v240, v241\n"
      "v_max3_f32 v150, v150, v151, v148\n"
      "v_mov_b32_e64 v151, v150\n"
      "v_mfma_f32_16x16x16_bf16 v[192:195], v[162:163], v[228:229], v[192:195]\n"
      "s_nop 0\n"
      "v_permlane16_swap_b32 v150, v151\n"
      "v_max_f32 v150, v150, v151\n"
      "v_mov_b32 v151, v150\n"
      "v_fmac_f32 v248, s37, v219\n"
      "s_nop 0\n"
      "v_permlane32_swap_b32 v150, v151\n"
      "v_max_f32 v150, v150, v151\n"
      "v_sub_f32 v188, v188, v150\n"
      "v_sub_f32 v140, v140, v150\n"
      "v_sub_f32 v190, v190, v150\n"
      "v_exp_f32 v140, v140\n"
      "v_sub_f32 v241, v241, v150\n"
      "v_sub_f32 v148, v148, v150\n"
      "v_sub_f32 v141, v141, v150\n"
      "v_sub_f32 v142, v142, v150\n"
      "v_exp_f32 v141, v141\n"
      "v_sub_f32 v243, v243, v150\n"
      "v_sub_f32 v143, v143, v150\n"
      "s_waitcnt lgkmcnt(0)\n"
      "v_alignbyte_b32 v207, v218, v207, v200\n"
      "v_cvt_pk_bf16_f32 v140, v140, v141\n"
      "v_sub_f32 v191, v191, v150\n"
      "v_exp_f32 v142, v142\n"
      "v_exp_f32 v143, v143\n"
      "v_sub_f32 v189, v189, v150\n"
      "v_cvt_pk_bf16_f32 v141, v142, v143\n"
      "v_sub_f32 v240, v240, v150\n"
      "v_exp_f32 v190, v190\n"
      "v_exp_f32 v188, v188\n"
      "v_alignbyte_b32 v208, v209, v208, v200\n"
      "v_alignbyte_b32 v210, v211, v210, v200\n"
      "v_exp_f32 v189, v189\n"
      "v_exp_f32 v148, v148\n"
      "v_alignbyte_b32 v211, v171, v211, v200\n"
      "v_exp_f32 v243, v243\n"
      "v_sub_f32_e64 v242, v242, v150\n"
      "v_alignbyte_b32 v209, v170, v209, v200\n"
      "v_cvt_pk_bf16_f32 v64, v148, 0\n"
      "v_max3_f32 v170, v192, v193, v194\n"
      "v_mfma_f32_16x16x16_bf16 v[156:159], v[162:163], v[206:207], v[156:159]\n"
      "v_bitop3_b32 v172, v172, v173, s4 bitop3:0x7e\n"
      "v_exp_f32_e64 v191, v191\n"
      "v_bitop3_b32 v174, v174, v175, s4 bitop3:0x7e\n"
      "v_alignbyte_b32 v161, v161, v161, v200\n"
      "v_cvt_pk_bf16_f32 v143, v190, v191\n"
      "v_max3_f32 v171, v195, v232, v233\n"
      "v_exp_f32 v241, v241\n"
      "v_max3_f32 v170, v170, v234, v235\n"
      "v_exp_f32 v240, v240\n"
      "v_max3_f32 v170, v170, v158, v159\n"
      "v_alignbyte_b32 v168, v169, v168, v200\n"
      "v_max3_f32 v171, v171, v156, v157\n"
      "v_max3_f32 v170, v170, v171, v248\n"
      "v_cvt_pk_bf16_f32 v142, v188, v189\n"
      "v_mov_b32 v171, v170\n"
      "v_or_b32 v172, v172, v174\n"
      "v_cmp_ne_u32 vcc, 0, v172\n"
      "v_permlane16_swap_b32 v170, v171\n"
      "v_max_f32 v170, v170, v171\n"
      "v_mov_b32_e64 v171, v170\n"
      "s_mov_b64 s[48:49], vcc\n"
      "v_alignbyte_b32 v169, v160, v169, v200\n"
      "v_permlane32_swap_b32 v170, v171\n"
      "v_max_f32 v170, v170, v171\n"
      "v_sub_f32 v192, v192, v170\n"
      "v_sub_f32 v232, v232, v170\n"
      "v_sub_f32 v195, v195, v170\n"
      "v_sub_f32 v157, v157, v170\n"
      "v_exp_f32 v195, v195\n"
      "v_exp_f32 v242, v242\n"
      "v_sub_f32 v156, v156, v170\n"
      "v_cvt_pk_bf16_f32 v62, v240, v241\n"
      "v_sub_f32 v193, v193, v170\n"
      "v_sub_f32 v233, v233, v170\n"
      "s_cbranch_vccnz L_flg_154_%=\n"
      "L_fb_155_%=:\n"
      "v_exp_f32 v193, v193\n"
      "v_lshlrev_b32 v161, 16, v161\n"
      "v_exp_f32 v192, v192\n"
      "v_cvt_pk_bf16_f32 v63, v242, v243\n"
      "v_sub_f32 v194, v194, v170\n"
      "v_exp_f32_e64 v194, v194\n"
      "v_mfma_f32_16x16x32_bf16 v[172:175], v[66:69], v[140:143], 0\n"
      "v_cvt_pk_bf16_f32 v192, v192, v193\n"
      "v_sub_f32 v159, v159, v170\n"
      "v_mfma_f32_16x16x32_bf16 v[172:175], v[66:69], v[62:65], v[172:175]\n"
      "v_exp_f32 v233, v233\n"
      "v_sub_f32 v248, v248, v170\n"
      "v_mfma_f32_16x16x32_bf16 v[188:191], v[184:187], v[140:143], 0\n"
      "v_exp_f32 v232, v232\n"
      "v_mfma_f32_16x16x32_bf16 v[188:191], v[50:53], v[62:65], v[188:191]\n"
      "ds_read_b128 v[240:243], v120 offset:18432\n"
      "v_sub_f32 v234, v234, v170\n"
      "v_mfma_f32_16x16x32_bf16 v[144:147], v[176:179], v[140:143], 0\n"
      "v_exp_f32 v234, v234\n"
      "v_mfma_f32_16x16x32_bf16 v[144:147], v[54:57], v[62:65], v[144:147]\n"
      "v_cvt_pk_bf16_f32 v193, v194, v195\n"
      "v_exp_f32 v156, v156\n"
      "v_sub_f32 v158, v158, v170\n"
      "v_sub_f32 v235, v235, v170\n"
      "v_mfma_f32_16x16x32_bf16 v[204:207], v[244:247], v[140:143], 0\n"
      "v_exp_f32 v235, v235\n"
      "v_mfma_f32_16x16x32_bf16 v[204:207], v[58:61], v[62:65], v[204:207]\n"
      "v_cvt_pk_bf16_f32 v195, v234, v235\n"
      "v_cvt_pk_bf16_f32 v194, v232, v233\n"
      "v_mfma_f32_16x16x32_f16 v[140:143], v[196:199], v[180:183], v[102:105]\n"
      "v_exp_f32_e64 v157, v157\n"
      "v_mfma_f32_16x16x32_f16 v[140:143], v[34:37], v[44:47], v[140:143]\n"
      "v_exp_f32 v248, v248\n"
      "v_mfma_f32_16x16x32_f16 v[196:199], v[164:167], v[180:183], v[106:109]\n"
      "v_exp_f32 v158, v158\n"
      "v_mfma_f32_16x16x32_f16 v[196:199], v[40:43], v[44:47], v[196:199]\n"
      "v_cvt_pk_bf16_f32 v64, v248, 0\n"
      "v_cvt_pk_bf16_f32 v62, v156, v157\n"
      "v_exp_f32_e64 v159, v159\n"
      "v_mfma_f32_16x16x16_bf16 v[140:143], v[162:163], v[208:209], v[140:143]\n"
      "v_cvt_pk_bf16_f32 v63, v158, v159\n"
      "v_rcp_f32 v172, v172\n"
      "v_mfma_f32_16x16x16_bf16 v[196:199], v[162:163], v[210:211], v[196:199]\n"
      "v_mul_f32 v145, v172, v145\n"
      "v_mul_f32 v188, v172, v188\n"
      "v_mul_f32 v189, v172, v189\n"
      "v_mul_f32 v191, v172, v191\n"
      "v_cvt_pk_bf16_f32 v188, v188, v189\n"
      "v_mul_f32 v190, v172, v190\n"
      "v_mul_f32_e64 v144, v172, v144\n"
      "v_cvt_pk_bf16_f32 v189, v190, v191\n"
      "v_mfma_f32_16x16x32_f16 v[208:211], v[224:227], v[180:183], v[74:77]\n"
      "v_mul_f32 v146, v172, v146\n"
      "v_cvt_pk_bf16_f32 v190, v144, v145\n"
      "v_mfma_f32_16x16x32_f16 v[208:211], v[252:255], v[136:139], v[208:211]\n"
      "s_waitcnt lgkmcnt(0)\n"
      "v_mul_f32_e64 v147, v172, v147\n"
      "v_mfma_f32_16x16x32_f16 v[208:211], v[220:223], v[44:47], v[208:211]\n"
      "buffer_store_dwordx4 v[240:243], v117, s[20:23], 0 offen offset:0 sc1\n"
      "v_mfma_f32_16x16x32_f16 v[164:167], v[136:139], v[180:183], v[110:113]\n"
      "v_cvt_pk_bf16_f32 v191, v146, v147\n"
      "v_mul_f32_e64 v207, v172, v207\n"
      "v_mfma_f32_16x16x32_f16 v[164:167], v[46:49], v[44:47], v[164:167]\n"
      "ds_write_b128 v124, v[188:191]\n"
      "v_mfma_f32_16x16x32_f16 v[180:183], v[252:255], v[224:227], v[76:79]\n"
      "v_mul_f32 v204, v172, v204\n"
      "v_mfma_f32_16x16x32_bf16 v[188:191], v[66:69], v[192:195], 0\n"
      "v_mfma_f32_16x16x32_bf16 v[188:191], v[66:69], v[62:65], v[188:191]\n"
      "v_mul_f32 v206, v172, v206\n"
      "v_mul_f32 v205, v172, v205\n"
      "v_mfma_f32_16x16x16_bf16 v[164:167], v[162:163], v[168:169], v[164:167]\n"
      "v_fmac_f32 v208, s37, v161\n"
      "v_cvt_pk_bf16_f32 v204, v204, v205\n"
      "v_mfma_f32_16x16x16_f16 v[180:183], v[222:223], v[220:221], v[180:183]\n"
      "v_cvt_pk_bf16_f32 v205, v206, v207\n"
      "v_max3_f32 v207, v143, v196, v197\n"
      "v_max3_f32 v206, v140, v141, v142\n"
      "v_mfma_f32_16x16x32_bf16 v[156:159], v[176:179], v[192:195], 0\n"
      "v_rcp_f32_e64 v188, v188\n"
      "v_max3_f32 v206, v206, v198, v199\n"
      "v_max3_f32 v207, v207, v164, v165\n"
      "v_mfma_f32_16x16x32_bf16 v[220:223], v[244:247], v[192:195], 0\n"
      "v_max3_f32 v206, v206, v166, v167\n"
      "v_max3_f32 v206, v206, v207, v208\n"
      "v_mov_b32 v207, v206\n"
      "v_mfma_f32_16x16x32_bf16 v[220:223], v[58:61], v[62:65], v[220:223]\n"
      "s_nop 0\n"
      "v_permlane16_swap_b32 v206, v207\n"
      "v_max_f32 v206, v206, v207\n"
      "v_mov_b32 v207, v206\n"
      "v_mfma_f32_16x16x32_bf16 v[156:159], v[54:57], v[62:65], v[156:159]\n"
      "ds_read_b128 v[136:139], v121 offset:18432\n"
      "v_permlane32_swap_b32 v206, v207\n"
      "v_max_f32 v206, v206, v207\n"
      "v_sub_f32 v142, v142, v206\n"
      "v_sub_f32 v167, v167, v206\n"
      "v_sub_f32 v199, v199, v206\n"
      "v_sub_f32 v164, v164, v206\n"
      "v_exp_f32 v167, v167\n"
      "v_sub_f32 v143, v143, v206\n"
      "v_sub_f32 v141, v141, v206\n"
      "v_mfma_f32_16x16x32_bf16 v[144:147], v[184:187], v[192:195], 0\n"
      "v_sub_f32 v166, v166, v206\n"
      "v_sub_f32 v197, v197, v206\n"
      "v_mfma_f32_16x16x32_bf16 v[144:147], v[50:53], v[62:65], v[144:147]\n"
      "v_sub_f32 v196, v196, v206\n"
      "v_sub_f32 v140, v140, v206\n"
      "v_exp_f32 v166, v166\n"
      "v_exp_f32 v142, v142\n"
      "v_sub_f32 v165, v165, v206\n"
      "v_exp_f32 v197, v197\n"
      "v_exp_f32 v140, v140\n"
      "v_exp_f32 v143, v143\n"
      "v_exp_f32 v196, v196\n"
      "v_exp_f32 v141, v141\n"
      "v_mul_f32 v147, v188, v147\n"
      "v_sub_f32 v198, v198, v206\n"
      "v_exp_f32 v198, v198\n"
      "v_mul_f32 v145, v188, v145\n"
      "v_mul_f32 v144, v188, v144\n"
      "v_sub_f32 v208, v208, v206\n"
      "v_cvt_pk_bf16_f32 v63, v166, v167\n"
      "v_cvt_pk_bf16_f32 v140, v140, v141\n"
      "v_exp_f32 v208, v208\n"
      "v_exp_f32 v199, v199\n"
      "v_cvt_pk_bf16_f32 v141, v142, v143\n"
      "v_exp_f32_e64 v164, v164\n"
      "v_exp_f32 v165, v165\n"
      "v_cvt_pk_bf16_f32 v142, v196, v197\n"
      "v_cvt_pk_bf16_f32 v143, v198, v199\n"
      "v_cvt_pk_bf16_f32 v64, v208, 0\n"
      "s_waitcnt lgkmcnt(0)\n"
      "v_cvt_pk_bf16_f32 v62, v164, v165\n"
      "v_mfma_f32_16x16x32_bf16 v[164:167], v[66:69], v[140:143], 0\n"
      "ds_read_b128 v[240:243], v123 offset:22528\n"
      "v_cvt_pk_bf16_f32 v144, v144, v145\n"
      "v_mfma_f32_16x16x32_bf16 v[164:167], v[66:69], v[62:65], v[164:167]\n"
      "ds_read_b128 v[224:227], v123 offset:19456\n"
      "v_mul_f32 v146, v188, v146\n"
      "v_mul_f32 v223, v188, v223\n"
      "v_mfma_f32_16x16x32_bf16 v[196:199], v[184:187], v[140:143], 0\n"
      "v_cvt_pk_bf16_f32 v145, v146, v147\n"
      "v_mul_f32 v157, v188, v157\n"
      "v_mfma_f32_16x16x32_bf16 v[196:199], v[50:53], v[62:65], v[196:199]\n"
      "s_mov_b32 exec_hi, 0\n"
      "buffer_store_dwordx4 v[136:139], v118, s[20:23], 0 offen offset:0 sc1\n"
      "s_mov_b32 exec_hi, -1\n"
      "v_mfma_f32_16x16x32_bf16 v[172:175], v[176:179], v[140:143], 0\n"
      "v_mul_f32 v156, v188, v156\n"
      "v_mul_f32_e64 v158, v188, v158\n"
      "v_mfma_f32_16x16x32_bf16 v[172:175], v[54:57], v[62:65], v[172:175]\n"
      "v_cvt_pk_bf16_f32 v146, v156, v157\n"
      "v_mul_f32 v221, v188, v221\n"
      "v_mul_f32 v220, v188, v220\n"
      "v_mfma_f32_16x16x32_bf16 v[136:139], v[244:247], v[140:143], 0\n"
      "ds_read_b128 v[140:143], v123 offset:25600\n"
      "v_mul_f32 v159, v188, v159\n"
      "v_mfma_f32_16x16x32_bf16 v[136:139], v[58:61], v[62:65], v[136:139]\n"
      "v_mul_f32 v222, v188, v222\n"
      "v_cvt_pk_bf16_f32 v220, v220, v221\n"
      "v_cvt_pk_bf16_f32 v147, v158, v159\n"
      "s_waitcnt lgkmcnt(1)\n"
      "v_mfma_f32_16x16x32_bf16 v[156:159], a[216:219], v[224:227], 0\n"
      "s_add_u32 s28, s27, 2\n"
      "v_cvt_pk_bf16_f32 v221, v222, v223\n"
      "v_cndmask_b32 v149, v149, v249, s[42:43]\n"
      "v_mfma_f32_16x16x32_bf16 v[156:159], a[228:231], v[240:243], v[156:159]\n"
      "ds_write_b128 v124, v[144:147] offset:1024\n"
      "v_mfma_f32_16x16x32_bf16 v[188:191], a[220:223], v[224:227], 0\n"
      "ds_read_b128 v[168:171], v123 offset:28672\n"
      "v_mbcnt_lo_u32_b32 v231, -1, 0\n"
      "v_mfma_f32_16x16x32_bf16 v[188:191], a[232:235], v[240:243], v[188:191]\n"
      "ds_write2st64_b64 v125, v[204:205], v[220:221] offset0:0 offset1:2\n"
      "ds_read_b128 v[204:207], v123 offset:20480\n"
      "v_mfma_f32_16x16x32_bf16 v[144:147], a[224:227], v[224:227], 0\n"
      "ds_read_b128 v[220:223], v123 offset:31744\n"
      "v_cndmask_b32 v149, v149, v209, s[44:45]\n"
      "v_mfma_f32_16x16x32_bf16 v[144:147], a[236:239], v[240:243], v[144:147]\n"
      "ds_read_b128 v[240:243], v123 offset:34816\n"
      "ds_read_b128 v[224:227], v123 offset:23552\n"
      "v_mbcnt_hi_u32_b32 v231, -1, v231\n"
      "v_cndmask_b32 v149, v149, v181, s[46:47]\n"
      "v_min_u32 v231, 48, v231\n"
      "s_min_u32 m0, s28, s3\n"
      "v_lshlrev_b32 v231, 1, v231\n"
      "v_add_u32 v231, s30, v231\n"
      "ds_read_u16 v228, v231 offset:4704\n"
      "s_movrels_b32 s9, s80\n"
      "s_lshr_b32 s8, s9, 8\n"
      "s_waitcnt lgkmcnt(6)\n"
      "v_mfma_f32_16x16x32_bf16 v[156:159], a[240:243], v[140:143], v[156:159]\n"
      "s_and_b32 s9, s9, 0xff\n"
      "v_mfma_f32_16x16x32_bf16 v[156:159], a[252:255], v[168:171], v[156:159]\n"
      "s_mul_i32 s10, s8, 73\n"
      "s_add_u32 s10, s10, s9\n"
      "s_mul_i32 s28, s10, 4802\n"
      "v_mfma_f32_16x16x32_bf16 v[188:191], a[244:247], v[140:143], v[188:191]\n"
      "s_add_u32 s56, s18, s28\n"
      "s_addc_u32 s57, s19, 0\n"
      "v_mfma_f32_16x16x32_bf16 v[188:191], v[0:3], v[168:171], v[188:191]\n"
      "s_and_b32 s56, s56, -16\n"
      "s_sub_u32 s58, s56, 110592\n"
      "s_subb_u32 s59, s57, 0\n"
      "v_mfma_f32_16x16x32_bf16 v[144:147], a[248:251], v[140:143], v[144:147]\n"
      "ds_read_b128 v[140:143], v123 offset:26624\n"
      "s_sub_u32 s56, s56, 73728\n"
      "v_mfma_f32_16x16x32_bf16 v[144:147], v[4:7], v[168:171], v[144:147]\n"
      "ds_read_b128 v[168:171], v123 offset:29696\n"
      "s_waitcnt lgkmcnt(3)\n"
      "v_mfma_f32_16x16x32_bf16 v[156:159], v[8:11], v[220:223], v[156:159]\n"
      "s_subb_u32 s57, s57, 0\n"
      "v_mfma_f32_16x16x32_bf16 v[156:159], v[20:23], v[240:243], v[156:159]\n"
      "s_waitcnt lgkmcnt(2)\n"
      "v_lshlrev_b32 v228, 16, v228\n"
      "v_fmac_f32 v149, s37, v228\n"
      "v_mfma_f32_16x16x32_bf16 v[188:191], v[12:15], v[220:223], v[188:191]\n"
      "s_mul_i32 s28, s8, 1373568\n"
      "s_mul_i32 s29, s9, 2688\n"
      "v_max_f32_dpp v228, v149, v149 row_ror:8 row_mask:0xf bank_mask:0xf\n"
      "v_mfma_f32_16x16x32_bf16 v[188:191], v[24:27], v[240:243], v[188:191]\n"
      "s_add_u32 s28, s28, s29\n"
      "v_max_f32_dpp v228, v228, v228 row_ror:4 row_mask:0xf bank_mask:0xf\n"
      "v_fma_f32 v231, v156, v156, s39\n"
      "v_mfma_f32_16x16x32_bf16 v[144:147], v[16:19], v[220:223], v[144:147]\n"
      "s_cmp_eq_u32 s8, 72\n"
      "v_fmac_f32_e64 v231, v157, v157\n"
      "v_max_f32_dpp v228, v228, v228 row_ror:2 row_mask:0xf bank_mask:0xf\n"
      "v_mfma_f32_16x16x32_bf16 v[144:147], v[28:31], v[240:243], v[144:147]\n"
      "s_cselect_b32 s8, 0, 100270464\n"
      "v_max_f32_dpp v228, v228, v228 row_ror:1 row_mask:0xf bank_mask:0xf\n"
      "v_fmac_f32_e64 v231, v158, v158\n"
      "v_mfma_f32_16x16x32_bf16 v[180:183], a[216:219], v[204:207], 0\n"
      "s_cmp_eq_u32 s9, 72\n"
      "v_fmac_f32_e64 v231, v159, v159\n"
      "v_max_f32_dpp v228, v228, v228 row_bcast:15 row_mask:0xa bank_mask:0xf\n"
      "v_mfma_f32_16x16x32_bf16 v[180:183], a[228:231], v[224:227], v[180:183]\n"
      "s_waitcnt lgkmcnt(0)\n"
      "v_fmac_f32 v231, v188, v188\n"
      "v_max_f32_dpp v228, v228, v228 row_bcast:31 row_mask:0xc bank_mask:0xf\n"
      "v_mfma_f32_16x16x32_bf16 v[220:223], a[220:223], v[204:207], 0\n"
      "s_cselect_b32 s9, 0, 196224\n"
      "v_fmac_f32 v231, v189, v189\n"
      "v_readlane_b32 s31, v228, 63\n"
      "v_mfma_f32_16x16x32_bf16 v[220:223], a[232:235], v[224:227], v[220:223]\n"
      "s_add_u32 s10, s8, s9\n"
      "v_subrev_f32 v149, s31, v149\n"
      "v_fmac_f32 v231, v190, v190\n"
      "v_mfma_f32_16x16x32_bf16 v[240:243], a[224:227], v[204:207], 0\n"
      "ds_read_b128 v[204:207], v123 offset:32768\n"
      "v_fmac_f32_e64 v231, v191, v191\n"
      "v_mfma_f32_16x16x32_bf16 v[240:243], a[236:239], v[224:227], v[240:243]\n"
      "ds_read_b128 v[224:227], v123 offset:35840\n"
      "v_fmac_f32 v231, v144, v144\n"
      "v_mfma_f32_16x16x32_bf16 v[180:183], a[240:243], v[140:143], v[180:183]\n"
      "s_add_u32 s28, s28, s10\n"
      "v_exp_f32 v149, v149\n"
      "v_mfma_f32_16x16x32_bf16 v[180:183], a[252:255], v[168:171], v[180:183]\n"
      "v_fmac_f32 v231, v145, v145\n"
      "s_add_u32 s52, s16, s28\n"
      "v_mfma_f32_16x16x32_bf16 v[220:223], a[244:247], v[140:143], v[220:223]\n"
      "s_addc_u32 s53, s17, 0\n"
      "v_fmac_f32_e64 v231, v146, v146\n"
      "v_cvt_pk_bf16_f32 v149, v149, v149\n"
      "v_mfma_f32_16x16x32_bf16 v[220:223], v[0:3], v[168:171], v[220:223]\n"
      "ds_write_b16 v130, v149\n"
      "v_mfma_f32_16x16x32_bf16 v[240:243], a[248:251], v[140:143], v[240:243]\n"
      "ds_read_b128 v[208:211], v131\n"
      "v_mfma_f32_16x16x32_bf16 v[240:243], v[4:7], v[168:171], v[240:243]\n"
      "ds_read_b128 v[168:171], v131 offset:64\n"
      "v_fmac_f32 v231, v147, v147\n"
      "s_add_u32 s27, s27, 1\n"
      "s_nop 0\n"
      "v_permlane16_swap_b32 v230, v231\n"
      "s_cmp_ge_u32 s27, s26\n"
      "s_waitcnt lgkmcnt(3)\n"
      "v_mfma_f32_16x16x32_bf16 v[180:183], v[8:11], v[204:207], v[180:183]\n"
      "v_mfma_f32_16x16x32_bf16 v[180:183], v[20:23], v[224:227], v[180:183]\n"
      "v_add_f32_e64 v230, v230, v231\n"
      "v_mfma_f32_16x16x32_bf16 v[220:223], v[12:15], v[204:207], v[220:223]\n"
      "ds_read_b128 v[140:143], v115 offset:18432\n"
      "v_mfma_f32_16x16x32_bf16 v[220:223], v[24:27], v[224:227], v[220:223]\n"
      "s_cselect_b32 s28, 1, 0\n"
      "v_mfma_f32_16x16x32_bf16 v[240:243], v[16:19], v[204:207], v[240:243]\n"
      "ds_read_b128 v[148:151], v114 offset:18432\n"
      "s_xor_b32 s29, s10, s11\n"
      "v_mfma_f32_16x16x32_bf16 v[240:243], v[28:31], v[224:227], v[240:243]\n"
      "ds_read_b128 v[204:207], v114 offset:24576\n"
      "s_waitcnt lgkmcnt(3)\n"
      "v_mfma_f32_16x16x32_bf16 v[192:195], v[208:211], v[66:69], 0\n"
      "s_nop 0\n"
      "v_fma_f32 v231, v180, v180, s39\n"
      "v_fmac_f32 v231, v181, v181\n"
      "v_mfma_f32_16x16x32_bf16 v[192:195], v[168:171], v[66:69], v[192:195]\n"
      "v_fmac_f32 v231, v182, v182\n"
      "v_fmac_f32_e64 v231, v183, v183\n"
      "v_mfma_f32_16x16x32_bf16 v[252:255], v[208:211], v[184:187], 0\n"
      "s_or_b32 s28, s28, s29\n"
      "v_fmac_f32 v231, v220, v220\n"
      "v_fmac_f32 v231, v221, v221\n"
      "v_mfma_f32_16x16x32_bf16 v[252:255], v[168:171], v[50:53], v[252:255]\n"
      "v_fmac_f32 v231, v222, v222\n"
      "v_fmac_f32 v231, v223, v223\n"
      "v_mfma_f32_16x16x32_bf16 v[224:227], v[208:211], v[176:179], 0\n"
      "ds_read_b128 v[216:219], v115 offset:24576\n"
      "v_mfma_f32_16x16x32_bf16 v[224:227], v[168:171], v[54:57], v[224:227]\n"
      "v_fmac_f32 v231, v240, v240\n"
      "v_fmac_f32 v231, v241, v241\n"
      "v_mfma_f32_16x16x32_bf16 v[176:179], v[208:211], v[244:247], 0\n"
      "v_fmac_f32 v231, v242, v242\n"
      "v_mfma_f32_16x16x32_bf16 v[176:179], v[168:171], v[58:61], v[176:179]\n"
      "s_waitcnt lgkmcnt(2)\n"
      "v_fmac_f32 v231, v243, v243\n"
      "v_mov_b32 v228, v231\n"
      "v_mfma_f32_16x16x32_bf16 v[184:187], a[0:3], v[148:151], 0\n"
      "s_nop 0\n"
      "v_permlane16_swap_b32_e64 v231, v228\n"
      "v_mfma_f32_16x16x32_bf16 v[184:187], a[36:39], v[140:143], v[184:187]\n"
      "v_cndmask_b32 v252, v252, v224, s[42:43]\n"
      "v_add_f32 v231, v231, v228\n"
      "v_mfma_f32_16x16x32_bf16 v[208:211], a[4:7], v[148:151], 0\n"
      "s_nop 0\n"
      "v_permlane32_swap_b32_e64 v230, v231\n"
      "v_mfma_f32_16x16x32_bf16 v[208:211], a[40:43], v[140:143], v[208:211]\n"
      "v_cndmask_b32 v252, v252, v176, s[44:45]\n"
      "v_add_f32 v230, v230, v231\n"
      "v_mfma_f32_16x16x32_bf16 v[176:179], a[8:11], v[148:151], 0\n"
      "s_waitcnt lgkmcnt(0)\n"
      "v_mov_b32_dpp v192, v192 quad_perm:[0,0,0,0] row_mask:0xf bank_mask:0xf\n"
      "v_mfma_f32_16x16x32_bf16 v[176:179], a[44:47], v[140:143], v[176:179]\n"
      "v_mfma_f32_16x16x32_bf16 v[224:227], a[12:15], v[148:151], 0\n"
      "v_rcp_f32_e64 v192, v192\n"
      "v_mfma_f32_16x16x32_bf16 v[224:227], a[48:51], v[140:143], v[224:227]\n"
      "ds_write_b32 v129, v230\n"
      "v_mfma_f32_16x16x32_bf16 v[228:231], a[16:19], v[148:151], 0\n"
      "v_mul_f32_e64 v252, v192, v252\n"
      "v_cvt_pk_bf16_f32 v252, v252, v252\n"
      "v_mfma_f32_16x16x32_bf16 v[228:231], a[52:55], v[140:143], v[228:231]\n"
      "ds_write_b16 v135, v252\n"
      "v_mfma_f32_16x16x32_bf16 v[168:171], a[20:23], v[148:151], 0\n"
      "v_mfma_f32_16x16x32_bf16 v[168:171], a[56:59], v[140:143], v[168:171]\n"
      "s_branch L_join_30_%=\n"
      "L_flg_154_%=:\n"
      "v_cmp_gt_i32 vcc, 0, v172\n"
      "s_cbranch_vccz L_fb_155_%=\n"
      "s_sleep 1\n"
      "ds_read_b128 v[172:175], v128 offset:3072\n"
      "s_waitcnt lgkmcnt(0)\n"
      "v_bitop3_b32 v172, v172, v173, s4 bitop3:0x7e\n"
      "v_bitop3_b32 v174, v174, v175, s4 bitop3:0x7e\n"
      "v_or_b32 v172, v172, v174\n"
      "v_cmp_ne_u32 s[48:49], 0, v172\n"
      "s_branch L_flg_154_%=\n"
      "L_edge_1_%=:\n"
      "s_mov_b32 s11, s10\n"
      "s_sub_u32 s28, s60, s8\n"
      "s_sub_u32 s29, s61, s9\n"
      "s_mov_b32 s60, s8\n"
      "s_mov_b32 s61, s9\n"
      "s_mov_b64 exec, s[64:65]\n"
      "v_add_u32 v116, s28, v116\n"
      "s_mov_b64 exec, s[70:71]\n"
      "v_add_u32 v116, s29, v116\n"
      "s_mov_b64 exec, s[66:67]\n"
      "v_add_u32 v117, s28, v117\n"
      "s_mov_b64 exec, s[72:73]\n"
      "v_add_u32 v117, s29, v117\n"
      "s_mov_b64 exec, s[68:69]\n"
      "v_add_u32 v118, s28, v118\n"
      "s_mov_b64 exec, s[74:75]\n"
      "v_add_u32 v118, s29, v118\n"
      "s_mov_b64 exec, -1\n"
      "s_nop 4\n"
      "s_branch L_eb_2_%=\n"
      "L_flg_3_%=:\n"
      "v_cmp_gt_i32 vcc, 0, v196\n"
      "s_cbranch_vccz L_fb_4_%=\n"
      "s_sleep 1\n"
      "ds_read_b128 v[196:199], v128 offset:2048\n"
      "s_waitcnt lgkmcnt(0)\n"
      "v_bitop3_b32 v196, v196, v197, s4 bitop3:0x7e\n"
      "v_bitop3_b32 v198, v198, v199, s4 bitop3:0x7e\n"
      "v_or_b32 v196, v196, v198\n"
      "v_cmp_ne_u32 s[48:49], 0, v196\n"
      "s_branch L_flg_3_%=\n"
      "L_edge_6_%=:\n"
      "s_mov_b32 s11, s10\n"
      "s_sub_u32 s28, s60, s8\n"
      "s_sub_u32 s29, s61, s9\n"
      "s_mov_b32 s60, s8\n"
      "s_mov_b32 s61, s9\n"
      "s_mov_b64 exec, s[64:65]\n"
      "v_add_u32 v116, s28, v116\n"
      "s_mov_b64 exec, s[70:71]\n"
      "v_add_u32 v116, s29, v116\n"
      "s_mov_b64 exec, s[66:67]\n"
      "v_add_u32 v117, s28, v117\n"
      "s_mov_b64 exec, s[72:73]\n"
      "v_add_u32 v117, s29, v117\n"
      "s_mov_b64 exec, s[68:69]\n"
      "v_add_u32 v118, s28, v118\n"
      "s_mov_b64 exec, s[74:75]\n"
      "v_add_u32_e64 v118, s29, v118\n"
      "s_mov_b64 exec, -1\n"
      "s_nop 4\n"
      "s_branch L_eb_7_%=\n"
      "L_flg_12_%=:\n"
      "v_cmp_gt_i32 vcc, 0, v212\n"
      "s_cbranch_vccz L_fb_13_%=\n"
      "s_sleep 1\n"
      "ds_read_b128 v[212:215], v128 offset:3072\n"
      "s_waitcnt lgkmcnt(0)\n"
      "v_bitop3_b32 v212, v212, v213, s4 bitop3:0x7e\n"
      "v_bitop3_b32 v214, v214, v215, s4 bitop3:0x7e\n"
      "v_or_b32 v212, v212, v214\n"
      "v_cmp_ne_u32 s[48:49], 0, v212\n"
      "s_branch L_flg_12_%=\n"
      "L_edge_15_%=:\n"
      "s_mov_b32 s11, s10\n"
      "s_sub_u32 s28, s60, s8\n"
      "s_sub_u32 s29, s61, s9\n"
      "s_mov_b32 s60, s8\n"
      "s_mov_b32 s61, s9\n"
      "s_mov_b64 exec, s[64:65]\n"
      "v_add_u32 v116, s28, v116\n"
      "s_mov_b64 exec, s[70:71]\n"
      "v_add_u32 v116, s29, v116\n"
      "s_mov_b64 exec, s[66:67]\n"
      "v_add_u32 v117, s28, v117\n"
      "s_mov_b64 exec, s[72:73]\n"
      "v_add_u32 v117, s29, v117\n"
      "s_mov_b64 exec, s[68:69]\n"
      "v_add_u32 v118, s28, v118\n"
      "s_mov_b64 exec, s[74:75]\n"
      "v_add_u32 v118, s29, v118\n"
      "s_mov_b64 exec, -1\n"
      "s_nop 4\n"
      "s_branch L_eb_16_%=\n"
      "L_edge_17_%=:\n"
      "s_mov_b32 s11, s10\n"
      "s_sub_u32 s28, s60, s8\n"
      "s_sub_u32 s29, s61, s9\n"
      "s_mov_b32 s60, s8\n"
      "s_mov_b32 s61, s9\n"
      "s_mov_b64 exec, s[64:65]\n"
      "v_add_u32 v116, s28, v116\n"
      "s_mov_b64 exec, s[70:71]\n"
      "v_add_u32 v116, s29, v116\n"
      "s_mov_b64 exec, s[66:67]\n"
      "v_add_u32 v117, s28, v117\n"
      "s_mov_b64 exec, s[72:73]\n"
      "v_add_u32 v117, s29, v117\n"
      "s_mov_b64 exec, s[68:69]\n"
      "v_add_u32 v118, s28, v118\n"
      "s_mov_b64 exec, s[74:75]\n"
      "v_add_u32 v118, s29, v118\n"
      "s_mov_b64 exec, -1\n"
      "s_nop 4\n"
      "s_branch L_eb_18_%=\n"
      "L_flg_23_%=:\n"
      "v_cmp_gt_i32 vcc, 0, v208\n"
      "s_cbranch_vccz L_fb_24_%=\n"
      "s_sleep 1\n"
      "ds_read_b128 v[208:211], v128 offset:2048\n"
      "s_waitcnt lgkmcnt(0)\n"
      "v_bitop3_b32 v208, v208, v209, s4 bitop3:0x7e\n"
      "v_bitop3_b32 v210, v210, v211, s4 bitop3:0x7e\n"
      "v_or_b32 v208, v208, v210\n"
      "v_cmp_ne_u32 s[48:49], 0, v208\n"
      "s_branch L_flg_23_%=\n"
      "L_edge_26_%=:\n"
      "s_mov_b32 s11, s10\n"
      "s_sub_u32 s28, s60, s8\n"
      "s_sub_u32 s29, s61, s9\n"
      "s_mov_b32 s60, s8\n"
      "s_mov_b32 s61, s9\n"
      "s_mov_b64 exec, s[64:65]\n"
      "v_add_u32 v116, s28, v116\n"
      "s_mov_b64 exec, s[70:71]\n"
      "v_add_u32 v116, s29, v116\n"
      "s_mov_b64 exec, s[66:67]\n"
      "v_add_u32 v117, s28, v117\n"
      "s_mov_b64 exec, s[72:73]\n"
      "v_add_u32 v117, s29, v117\n"
      "s_mov_b64 exec, s[68:69]\n"
      "v_add_u32 v118, s28, v118\n"
      "s_mov_b64 exec, s[74:75]\n"
      "v_add_u32 v118, s29, v118\n"
      "s_mov_b64 exec, -1\n"
      "s_nop 4\n"
      "s_branch L_eb_27_%=\n"
      "L_flg_32_%=:\n"
      "v_cmp_gt_i32 vcc, 0, v204\n"
      "s_cbranch_vccz L_fb_33_%=\n"
      "s_sleep 1\n"
      "ds_read_b128 v[204:207], v128 offset:3072\n"
      "s_waitcnt lgkmcnt(0)\n"
      "v_bitop3_b32 v204, v204, v205, s4 bitop3:0x7e\n"
      "v_bitop3_b32 v206, v206, v207, s4 bitop3:0x7e\n"
      "v_or_b32 v204, v204, v206\n"
      "v_cmp_ne_u32 s[48:49], 0, v204\n"
      "s_branch L_flg_32_%=\n"
      "L_edge_36_%=:\n"
      "s_mov_b32 s11, s10\n"
      "s_sub_u32 s28, s60, s8\n"
      "s_sub_u32 s29, s61, s9\n"
      "s_mov_b32 s60, s8\n"
      "s_mov_b32 s61, s9\n"
      "s_mov_b64 exec, s[64:65]\n"
      "v_add_u32 v116, s28, v116\n"
      "s_mov_b64 exec, s[70:71]\n"
      "v_add_u32 v116, s29, v116\n"
      "s_mov_b64 exec, s[66:67]\n"
      "v_add_u32 v117, s28, v117\n"
      "s_mov_b64 exec, s[72:73]\n"
      "v_add_u32 v117, s29, v117\n"
      "s_mov_b64 exec, s[68:69]\n"
      "v_add_u32 v118, s28, v118\n"
      "s_mov_b64 exec, s[74:75]\n"
      "v_add_u32 v118, s29, v118\n"
      "s_mov_b64 exec, -1\n"
      "s_nop 4\n"
      "s_branch L_eb_37_%=\n"
      "L_edge_38_%=:\n"
      "s_mov_b32 s11, s10\n"
      "s_sub_u32 s28, s60, s8\n"
      "s_sub_u32 s29, s61, s9\n"
      "s_mov_b32 s60, s8\n"
      "s_mov_b32 s61, s9\n"
      "s_mov_b64 exec, s[64:65]\n"
      "v_add_u32 v116, s28, v116\n"
      "s_mov_b64 exec, s[70:71]\n"
      "v_add_u32 v116, s29, v116\n"
      "s_mov_b64 exec, s[66:67]\n"
      "v_add_u32 v117, s28, v117\n"
      "s_mov_b64 exec, s[72:73]\n"
      "v_add_u32 v117, s29, v117\n"
      "s_mov_b64 exec, s[68:69]\n"
      "v_add_u32 v118, s28, v118\n"
      "s_mov_b64 exec, s[74:75]\n"
      "v_add_u32 v118, s29, v118\n"
      "s_mov_b64 exec, -1\n"
      "s_nop 4\n"
      "s_branch L_eb_39_%=\n"
      "L_edge_40_%=:\n"
      "s_mov_b32 s11, s10\n"
      "s_sub_u32 s28, s60, s8\n"
      "s_sub_u32 s29, s61, s9\n"
      "s_mov_b32 s60, s8\n"
      "s_mov_b32 s61, s9\n"
      "s_mov_b64 exec, s[64:65]\n"
      "v_add_u32 v116, s28, v116\n"
      "s_mov_b64 exec, s[70:71]\n"
      "v_add_u32 v116, s29, v116\n"
      "s_mov_b64 exec, s[66:67]\n"
      "v_add_u32 v117, s28, v117\n"
      "s_mov_b64 exec, s[72:73]\n"
      "v_add_u32 v117, s29, v117\n"
      "s_mov_b64 exec, s[68:69]\n"
      "v_add_u32 v118, s28, v118\n"
      "s_mov_b64 exec, s[74:75]\n"
      "v_add_u32 v118, s29, v118\n"
      "s_mov_b64 exec, -1\n"
      "s_nop 4\n"
      "s_branch L_eb_41_%=\n"
      "L_edge_44_%=:\n"
      "s_mov_b32 s11, s10\n"
      "s_sub_u32 s28, s60, s8\n"
      "s_sub_u32 s29, s61, s9\n"
      "s_mov_b32 s60, s8\n"
      "s_mov_b32 s61, s9\n"
      "s_mov_b64 exec, s[64:65]\n"
      "v_add_u32 v116, s28, v116\n"
      "s_mov_b64 exec, s[70:71]\n"
      "v_add_u32 v116, s29, v116\n"
      "s_mov_b64 exec, s[66:67]\n"
      "v_add_u32 v117, s28, v117\n"
      "s_mov_b64 exec, s[72:73]\n"
      "v_add_u32 v117, s29, v117\n"
      "s_mov_b64 exec, s[68:69]\n"
      "v_add_u32 v118, s28, v118\n"
      "s_mov_b64 exec, s[74:75]\n"
      "v_add_u32 v118, s29, v118\n"
      "s_mov_b64 exec, -1\n"
      "s_nop 4\n"
      "s_branch L_eb_45_%=\n"
      "L_edge_46_%=:\n"
      "s_mov_b32 s11, s10\n"
      "s_sub_u32 s28, s60, s8\n"
      "s_sub_u32 s29, s61, s9\n"
      "s_mov_b32 s60, s8\n"
      "s_mov_b32 s61, s9\n"
      "s_mov_b64 exec, s[64:65]\n"
      "v_add_u32 v116, s28, v116\n"
      "s_mov_b64 exec, s[70:71]\n"
      "v_add_u32 v116, s29, v116\n"
      "s_mov_b64 exec, s[66:67]\n"
      "v_add_u32 v117, s28, v117\n"
      "s_mov_b64 exec, s[72:73]\n"
      "v_add_u32 v117, s29, v117\n"
      "s_mov_b64 exec, s[68:69]\n"
      "v_add_u32 v118, s28, v118\n"
      "s_mov_b64 exec, s[74:75]\n"
      "v_add_u32 v118, s29, v118\n"
      "s_mov_b64 exec, -1\n"
      "s_nop 4\n"
      "s_branch L_eb_47_%=\n"
      "L_edge_48_%=:\n"
      "s_mov_b32 s11, s10\n"
      "s_sub_u32 s28, s60, s8\n"
      "s_sub_u32 s29, s61, s9\n"
      "s_mov_b32 s60, s8\n"
      "s_mov_b32 s61, s9\n"
      "s_mov_b64 exec, s[64:65]\n"
      "v_add_u32 v116, s28, v116\n"
      "s_mov_b64 exec, s[70:71]\n"
      "v_add_u32 v116, s29, v116\n"
      "s_mov_b64 exec, s[66:67]\n"
      "v_add_u32 v117, s28, v117\n"
      "s_mov_b64 exec, s[72:73]\n"
      "v_add_u32 v117, s29, v117\n"
      "s_mov_b64 exec, s[68:69]\n"
      "v_add_u32 v118, s28, v118\n"
      "s_mov_b64 exec, s[74:75]\n"
      "v_add_u32 v118, s29, v118\n"
      "s_mov_b64 exec, -1\n"
      "s_nop 4\n"
      "s_branch L_eb_49_%=\n"
      "L_cend_%=:\n"
      :
      : "s"(__builtin_amdgcn_kernarg_segment_ptr()), "s"(wave), "s"(blockIdx.x), "s"(CT_TAB), "s"(lds0)
      : "v0","v1","v2","v3","v4","v5","v6","v7","v8","v9","v10","v11","v12","v13","v14","v15","v16","v17","v18","v19","v20","v21","v22","v23","v24","v25","v26","v27","v28","v29","v30","v31","v32","v33","v34","v35","v36","v37","v38","v39","v40","v41","v42","v43","v44","v45","v46","v47","v48","v49","v50","v51","v52","v53","v54","v55","v56","v57","v58","v59","v60","v61","v62","v63","v64","v65","v66","v67","v68","v69","v70","v71","v72","v73","v74","v75","v76","v77","v78","v79","v80","v81","v82","v83","v84","v85","v86","v87","v88","v89","v90","v91","v92","v93","v94","v95","v96","v97","v98","v99","v100","v101","v102","v103","v104","v105","v106","v107","v108","v109","v110","v111","v112","v113","v114","v115","v116","v117","v118","v119","v120","v121","v122","v123","v124","v125","v126","v127","v128","v129","v130","v131","v132","v133","v134","v135","v136","v137","v138","v139","v140","v141","v142","v143","v144","v145","v146","v147","v148","v149","v150","v151","v152","v153","v154","v155","v156","v157","v158","v159","v160","v161","v162","v163","v164","v165","v166","v167","v168","v169","v170","v171","v172","v173","v174","v175","v176","v177","v178","v179","v180","v181","v182","v183","v184","v185","v186","v187","v188","v189","v190","v191","v192","v193","v194","v195","v196","v197","v198","v199","v200","v201","v202","v203","v204","v205","v206","v207","v208","v209","v210","v211","v212","v213","v214","v215","v216","v217","v218","v219","v220","v221","v222","v223","v224","v225","v226","v227","v228","v229","v230","v231","v232","v233","v234","v235","v236","v237","v238","v239","v240","v241","v242","v243","v244","v245","v246","v247","v248","v249","v250","v251","v252","v253","v254","v255","a0","a1","a2","a3","a4","a5","a6","a7","a8","a9","a10","a11","a12","a13","a14","a15","a16","a17","a18","a19","a20","a21","a22","a23","a24","a25","a26","a27","a28","a29","a30","a31","a32","a33","a34","a35","a36","a37","a38","a39","a40","a41","a42","a43","a44","a45","a46","a47","a48","a49","a50","a51","a52","a53","a54","a55","a56","a57","a58","a59","a60","a61","a62","a63","a64","a65","a66","a67","a68","a69","a70","a71","a72","a73","a74","a75","a76","a77","a78","a79","a80","a81","a82","a83","a84","a85","a86","a87","a88","a89","a90","a91","a92","a93","a94","a95","a96","a97","a98","a99","a100","a101","a102","a103","a104","a105","a106","a107","a108","a109","a110","a111","a112","a113","a114","a115","a116","a117","a118","a119","a120","a121","a122","a123","a124","a125","a126","a127","a128","a129","a130","a131","a132","a133","a134","a135","a136","a137","a138","a139","a140","a141","a142","a143","a144","a145","a146","a147","a148","a149","a150","a151","a152","a153","a154","a155","a156","a157","a158","a159","a160","a161","a162","a163","a164","a165","a166","a167","a168","a169","a170","a171","a172","a173","a174","a175","a176","a177","a178","a179","a180","a181","a182","a183","a184","a185","a186","a187","a188","a189","a190","a191","a192","a193","a194","a195","a196","a197","a198","a199","a200","a201","a202","a203","a204","a205","a206","a207","a208","a209","a210","a211","a212","a213","a214","a215","a216","a217","a218","a219","a220","a221","a222","a223","a224","a225","a226","a227","a228","a229","a230","a231","a232","a233","a234","a235","a236","a237","a238","a239","a240","a241","a242","a243","a244","a245","a246","a247","a248","a249","a250","a251","a252","a253","a254","a255","s2","s3","s4","s5","s6","s7","s8","s9","s10","s11","s12","s13","s16","s17","s18","s19","s20","s21","s22","s23","s24","s25","s26","s27","s28","s29","s30","s31","s34","s35","s36","s37","s38","s39","s40","s41","s42","s43","s44","s45","s46","s47","s48","s49","s50","s51","s52","s53","s54","s55","s60","s61","s62","s63","s64","s65","s66","s67","s68","s69","s70","s71","s72","s73","s74","s75","s76","s77","s78","s79","s80","s81","s82","s83","s84","s85","s86","s87","s88","s89","s90","s91","s92","s93","s94","s95","s96","s97","s98","s99","s100","s101", "vcc", "scc", "memory");
}

}

extern "C" void kernel_launch(void* const* d_in, const int* in_sizes, int n_in, void* d_out, int out_size, void* d_ws,
                              size_t ws_size, hipStream_t stream) {
  const uint16_t* x = (const uint16_t*)d_in[0];
  const uint16_t* wqkv = (const uint16_t*)d_in[1];
  const uint16_t* wo = (const uint16_t*)d_in[2];
  const uint16_t* ls = (const uint16_t*)d_in[3];
  const uint16_t* w1 = (const uint16_t*)d_in[4];
  const uint16_t* w2 = (const uint16_t*)d_in[5];
  const uint16_t* rc = (const uint16_t*)d_in[6];
  const int* ridx = (const int*)d_in[7];
  const uint16_t* mask = (const uint16_t*)d_in[8];
  swin::swin_main<<<256, 256, 0, stream>>>(x, mask, (char*)d_ws, (uint16_t*)d_out, wqkv, wo, ls, w1, w2, rc, ridx);
}
```
